```python
import torch

def _softmax(x, axis=-1):
    return torch.softmax(x, dim=axis)

def _gelu(x):
    return torch.nn.functional.gelu(x, approximate='tanh')

def _silu(x):
    return torch.nn.functional.silu(x)

def _sigmoid(x):
    return torch.sigmoid(x)

def _layernorm(x, axis=-1, eps=1e-05):
    m = x.mean(dim=axis, keepdim=True)
    v = x.var(dim=axis, keepdim=True, unbiased=False)
    return (x - m) / torch.sqrt(v + eps)

def _scatter_add(tgt, idx, src, axis=0):
    return tgt.clone().index_add_(axis, idx, src)

def _segment_sum(src, seg, n):
    out = torch.zeros((n,) + src.shape[1:], dtype=src.dtype)
    return out.index_add_(0, seg, src)

def _pad(x, pw):
    flat = tuple((v for pair in reversed(pw) for v in pair))
    return torch.nn.functional.pad(x, flat)
kernel_name = 'concat_heads__memory_aug_recurrent_mem_tr__ssm_conv_h3__prompt'

def setup_inputs():

    _w = lambda a: (lambda t: t.to(torch.bfloat16) if t.is_floating_point() else t)(torch.as_tensor(a))

    def _A_setup():
        B, S, Dm = (1, 2048, 1024)
        H, Dh = (32, 128)
        M, seg = (8, 32)
        seg = min(seg, S)
        S = S // seg * seg
        return {'x': _w((lambda *s: torch.randn(*s, dtype=torch.bfloat16) * 0.05)(B, S, Dm)), 'mem0': _w((lambda *s: torch.randn(*s, dtype=torch.bfloat16) * 0.4)(B, M, Dm)), 'Wq': _w((lambda *s: torch.randn(*s, dtype=torch.bfloat16) * 6.4)(Dm, H, Dh) * Dm ** (-0.5)), 'Wk': _w((lambda *s: torch.randn(*s, dtype=torch.bfloat16) * 6.4)(Dm, H, Dh) * Dm ** (-0.5)), 'Wv': _w((lambda *s: torch.randn(*s, dtype=torch.bfloat16) * 0.4)(Dm, H, Dh) * Dm ** (-0.5)), 'Wo': _w((lambda *s: torch.randn(*s, dtype=torch.bfloat16) * 0.4)(H, Dh, Dm) * (H * Dh) ** (-0.5)), 'seg_len': seg}
    _A_in = _A_setup()

    def _B_setup():
        B, S, D = (1, 2048, 1024)
        ks = 4
        return {'q': _w((lambda *s: torch.randn(*s, dtype=torch.bfloat16) * 0.05)(B, S, D)), 'k': _w((lambda *s: torch.randn(*s, dtype=torch.bfloat16) * 0.4)(B, S, D)), 'v': _w((lambda *s: torch.randn(*s, dtype=torch.bfloat16) * 0.4)(B, S, D)), 'shift_k': _w((lambda *s: torch.randn(*s, dtype=torch.bfloat16) * 0.4)(D, ks) * 0.5), 'ssm_k': _w((lambda *s: torch.randn(*s, dtype=torch.bfloat16) * 0.4)(D, S) * (1.0 / S ** 0.5)), 'ssm_D': _w((lambda *s: torch.randn(*s, dtype=torch.bfloat16) * 0.4)(D))}
    _B_in = _B_setup()
    return {'A_x': _A_in['x'], 'A_mem0': _A_in['mem0'], 'A_Wq': _A_in['Wq'], 'A_Wk': _A_in['Wk'], 'A_Wv': _A_in['Wv'], 'A_Wo': _A_in['Wo'], 'A_seg_len': _A_in['seg_len'], 'B_q': _B_in['q'], 'B_k': _B_in['k'], 'B_v': _B_in['v'], 'B_shift_k': _B_in['shift_k'], 'B_ssm_k': _B_in['ssm_k'], 'B_ssm_D': _B_in['ssm_D']}

def reference(A_x, A_mem0, A_Wq, A_Wk, A_Wv, A_Wo, A_seg_len, B_q, B_k, B_v, B_shift_k, B_ssm_k, B_ssm_D):

    def _A_ref(x, mem0, Wq, Wk, Wv, Wo, seg_len):
        B, S, Dm = x.shape
        H, Dh = (Wq.shape[1], Wq.shape[2])
        M = mem0.shape[1]
        scale = Dh ** (-0.5)
        n_seg = S // seg_len
        L = M + seg_len
        idx = torch.arange(L)
        mask = (idx[:, None] < M) | (idx[:, None] >= idx[None, :])
        mem = mem0
        outs = []
        for s in range(n_seg):
            xs = x[:, s * seg_len:(s + 1) * seg_len, :].clone()
            h = torch.concatenate([mem, xs], axis=1)
            q = torch.einsum('btd,dhe->bhte', h, Wq)
            k = torch.einsum('btd,dhe->bhte', h, Wk)
            v = torch.einsum('btd,dhe->bhte', h, Wv)
            scores = torch.einsum('bhqe,bhke->bhqk', q, k) * scale
            scores = torch.where(mask[None, None], scores, -1000000000.0)
            p = _softmax(scores, axis=-1)
            o = torch.einsum('bhqk,bhke->bhqe', p, v)
            o = torch.einsum('bhte,hed->btd', o, Wo)
            mem = o[:, :M, :].clone()
            outs.append(o[:, M:, :])
        return torch.concatenate(outs, axis=1)

    def _B_ref(q, k, v, shift_k, ssm_k, ssm_D):
        B, S, D = q.shape
        ks = shift_k.shape[-1]
        k_pad = _pad(k, ((0, 0), (ks - 1, 0), (0, 0)))
        k_shift = torch.zeros_like(k)
        for t in range(ks):
            k_shift = k_shift + k_pad[:, t:t + S, :] * shift_k[:, ks - 1 - t][None, None, :]
        kv = k_shift * v
        L = 2 * S
        kv_pad = _pad(kv, ((0, 0), (0, S), (0, 0)))
        kf_pad = _pad(ssm_k, ((0, 0), (0, S)))
        kv_f = (lambda x, axes=(-2, -1): torch.fft.rfft2(x.float(), dim=axes))(kv_pad[:, None, :, :], axes=(1, 2))
        kf_f = (lambda x, axes=(-2, -1): torch.fft.rfft2(x.float(), dim=axes))(kf_pad[:, None, :], axes=(1, 2))
        kf_f = (lambda x, *d: x.permute(*d))(kf_f, 1, 2, 0)
        y_f = kv_f * kf_f[None, :, :, :]
        y = (lambda x, s, axes=(-2, -1): torch.fft.irfft2(x, s=s, dim=axes).to(torch.bfloat16))(y_f, (1, L), axes=(1, 2))[:, 0, :S, :]
        y = y + kv * ssm_D[None, None, :]
        return q * y
    a = _A_ref(A_x, A_mem0, A_Wq, A_Wk, A_Wv, A_Wo, A_seg_len)
    b = _B_ref(B_q, B_k, B_v, B_shift_k, B_ssm_k, B_ssm_D)
    return torch.concatenate([a, b], axis=-1)
```

```cpp
#include <hip/hip_runtime.h>
#include <hip/hip_bf16.h>

namespace mk {

#define GAS __attribute__((address_space(1)))
#define LAS __attribute__((address_space(3)))
#define DEV __device__ __forceinline__
#define RLX_AGENT __ATOMIC_RELAXED, __HIP_MEMORY_SCOPE_AGENT
typedef unsigned short u16;
typedef unsigned v2u __attribute__((ext_vector_type(2)));
typedef unsigned v4u __attribute__((ext_vector_type(4)));
typedef unsigned v8u __attribute__((ext_vector_type(8)));
typedef unsigned v16u __attribute__((ext_vector_type(16)));
typedef unsigned v32u __attribute__((ext_vector_type(32)));
typedef float f32x2 __attribute__((ext_vector_type(2)));
typedef float f32x4 __attribute__((ext_vector_type(4)));
typedef float f32x16 __attribute__((ext_vector_type(16)));
typedef __bf16 bf16x2 __attribute__((ext_vector_type(2)));
typedef __bf16 bf16x8 __attribute__((ext_vector_type(8)));
typedef __bf16 bf16x16 __attribute__((ext_vector_type(16)));
typedef GAS unsigned gu32;
typedef __amdgpu_buffer_rsrc_t rsrc_t;
typedef LAS unsigned char* lds_t;

#define XCNT(r) ""
#define XT(a, b) ""
#define UCNT ""
#define GSUM ""
#define ZSUM ""
#define GTRACE
#define MIXED 1
#define NOFLAG 0
#define TOK_KICK 1
#define ZRING 16
#define Z_AT 16
#define Z_GAP 10
#define STR_(x) #x
#define STR(x) STR_(x)
#define GQ_SLEEP 127
#define PULL_AT 8
constexpr int NSEG = 64;
constexpr unsigned SENT = 0xFFFFFFFFu;
constexpr int AUX_SC1 = 16, AUX_NT = 2;
constexpr int T_LAG = 61;
constexpr int G_AHEAD = 4;
constexpr unsigned SPIN_CAP = 1u << 21;
constexpr int LDS_BYTES = 163840;

constexpr int C_ECHO = 0, C_CLAIM = 1024, C_VERDICT = 3072, C_TMO = 3328, C_CONV = 3456, C_FLAG = 4096, C_ASK = C_FLAG + 256 * 128;
constexpr int F_Q = 0, F_K = 32, F_V = 64, F_S = 96, F_Z = 128;
constexpr int CTRL_BYTES = C_ASK + 256 * 64;
constexpr int MB = 1 << 20;
constexpr int R_U = 0, R_U2 = 3 * MB;
constexpr int R_MEM = 2 * MB, MEM_SLOT = 32768;
constexpr int R_P = R_MEM + 4 * MEM_SLOT, P_SLOT = 65536;
constexpr int R_PT = 4 * MB, PT_SLOT = 131072;
constexpr int R_QX = 8 * MB, R_KX = 40 * MB, R_Z = 72 * MB, Z_SLOT = 4 * MB, R_KVT = R_KX, R_YT = R_KX + 8 * MB, R_END = 208 * MB;

struct Params {
  const u16 *x, *mem0, *Wq, *Wk, *Wv, *Wo, *bq, *bk, *bv, *shift, *ssmk, *ssmD;
  u16* out;
  unsigned char* ctrl;
  unsigned char* data;
};

DEV rsrc_t mkrs(const void* p, unsigned bytes) { return __builtin_amdgcn_make_buffer_rsrc((void*)p, 0, bytes, 0x00020000); }
DEV v4u ld16s(rsrc_t rs, int v, int s) { return __builtin_amdgcn_raw_buffer_load_b128(rs, v, s, AUX_SC1); }
DEV v2u ld8s(rsrc_t rs, int v, int s) { return __builtin_amdgcn_raw_buffer_load_b64(rs, v, s, AUX_SC1); }
DEV v4u ld16(rsrc_t rs, int v, int s) { return __builtin_amdgcn_raw_buffer_load_b128(rs, v, s, 0); }
DEV v2u ld8(rsrc_t rs, int v, int s) { return __builtin_amdgcn_raw_buffer_load_b64(rs, v, s, 0); }
DEV void dma16(rsrc_t rs, lds_t l, int v) {
#if __HIP_DEVICE_COMPILE__
  __builtin_amdgcn_raw_ptr_buffer_load_lds(rs, (LAS void*)l, 16, v, 0, 0, 0);
#endif
}
DEV void st16s(rsrc_t rs, int v, int s, v4u d) { __builtin_amdgcn_raw_buffer_store_b128(d, rs, v, s, AUX_SC1); }
DEV void st8s(rsrc_t rs, int v, int s, v2u d) { __builtin_amdgcn_raw_buffer_store_b64(d, rs, v, s, AUX_SC1); }
DEV void st4s(rsrc_t rs, int v, int s, unsigned d) { __builtin_amdgcn_raw_buffer_store_b32(d, rs, v, s, AUX_SC1); }
DEV int amap(int o, int par) { return ((o >> 13) << 14) | (par << 13) | (o & 8191); }
DEV int xblk(int s, int h, int par) { return (s * 32 + h) * 16384 + ((par ^ (s >> 2)) & 1) * 8192; }
DEV unsigned umax(unsigned a, unsigned b) { return a > b ? a : b; }
DEV unsigned top(v4u v) { return umax(umax(v.x, v.y), umax(v.z, v.w)); }
DEV unsigned pk(float a, float b) { bf16x2 v = {(__bf16)a, (__bf16)b}; return __builtin_bit_cast(unsigned, v); }
DEV unsigned pks(float a, float b) { unsigned u = pk(a, b); return u < SENT - 1u ? u : SENT - 1u; }
DEV float blo(unsigned u) { return __builtin_bit_cast(float, u << 16); }
DEV float bhi(unsigned u) { return __builtin_bit_cast(float, u & 0xFFFF0000u); }
DEV float rb(float a) { return blo(pk(a, a)); }
DEV unsigned fu(float f) { return __builtin_bit_cast(unsigned, f); }
DEV float uf(unsigned u) { return __builtin_bit_cast(float, u); }
DEV f32x4 mfma16(v4u a, v4u b, f32x4 c) {
  return __builtin_amdgcn_mfma_f32_16x16x32_bf16(__builtin_bit_cast(bf16x8, a), __builtin_bit_cast(bf16x8, b), c, 0, 0, 0);
}
DEV f32x16 mfma32(v4u a, v4u b, f32x16 c) {
  return __builtin_amdgcn_mfma_f32_32x32x16_bf16(__builtin_bit_cast(bf16x8, a), __builtin_bit_cast(bf16x8, b), c, 0, 0, 0);
}
DEV f32x4 smf(v4u a, v8u b, f32x4 c, int idx) {
  return __builtin_amdgcn_smfmac_f32_16x16x64_bf16(__builtin_bit_cast(bf16x8, a), __builtin_bit_cast(bf16x16, b), c, idx, 0, 0);
}
DEV v8u cat(v4u a, v4u b) { return __builtin_shufflevector(a, b, 0, 1, 2, 3, 4, 5, 6, 7); }
DEV v4u lo4(v8u a) { return __builtin_shufflevector(a, a, 0, 1, 2, 3); }
DEV v4u hi4(v8u a) { return __builtin_shufflevector(a, a, 4, 5, 6, 7); }
DEV v4u lds16(lds_t L, int o) { return *(LAS v4u*)(L + o); }
DEV void sts16(lds_t L, int o, v4u v) { *(LAS v4u*)(L + o) = v; }
DEV void sts8(lds_t L, int o, v2u v) { *(LAS v2u*)(L + o) = v; }
#define SCHED() __builtin_amdgcn_sched_barrier(0)
#define PIN_A(x) asm volatile("" : "+a"(x))
#define PIN_V(x) asm volatile("" : "+v"(x))
DEV int opq(int v) { asm volatile("" : "+v"(v)); return v; }
DEV int fresh_lane() {
  int l; asm volatile("v_mbcnt_lo_u32_b32 %0, -1, 0\n\tv_mbcnt_hi_u32_b32 %0, -1, %0" : "=v"(l)); return l;
}
DEV f32x4 fresh_zero() {
  f32x2 a, b;
  asm volatile("v_mov_b64 %0, 0" : "=v"(a));
  asm volatile("v_mov_b64 %0, 0" : "=v"(b));
  return __builtin_shufflevector(a, b, 0, 1, 2, 3);
}
DEV unsigned fresh_sent() { unsigned z; asm volatile("v_mov_b32 %0, -1" : "=v"(z)); return z; }
#define SMF "v_smfmac_f32_16x16x64_bf16 "
#define MF16 "v_mfma_f32_16x16x32_bf16 "
#define SMF1(cls, d, a, b) asm volatile(SMF "%0, %1, %2, %3" : "+v"(d) : "v"(a), cls(b), "v"(idx))
#define SMF1F(cls, lo, hi, d, a, b) asm volatile(SMF "v[" #lo ":" #hi "], %1, %2, %3" : "+{v[" #lo ":" #hi "]}"(d) : "v"(a), cls(b), "v"(idx))
#define AT_HOME(M, j) do { const int j_ = (j); if (j_ == 0) M("{v[32:39]}"); if (j_ == 1) M("{v[40:47]}"); if (j_ == 2) M("{v[48:55]}"); if (j_ == 3) M("{v[56:63]}"); if (j_ == 4) M("{v[64:71]}"); if (j_ == 5) M("{v[72:79]}"); if (j_ == 6) M("{v[80:87]}"); if (j_ == 7) M("{v[88:95]}"); if (j_ == 8) M("{v[96:103]}"); if (j_ == 9) M("{v[104:111]}"); if (j_ == 10) M("{v[112:119]}"); if (j_ == 11) M("{v[120:127]}"); if (j_ == 12) M("{v[128:135]}"); if (j_ == 13) M("{v[136:143]}"); if (j_ == 14) M("{v[144:151]}"); if (j_ == 15) M("{v[152:159]}"); } while (0)
#define MF1(d, a, b) asm volatile(MF16 "%0, %1, %2, %0" : "+v"(d) : "v"(a), "v"(b))
#define MF1Z(d, a, b) asm volatile(MF16 "%0, %1, %2, 0" : "=&v"(d) : "v"(a), "v"(b))
#define LGKM(n) asm volatile("s_waitcnt lgkmcnt(" #n ")" ::: "memory")
#define RAWBAR() do { __builtin_amdgcn_s_barrier(); asm volatile("" ::: "memory"); } while (0)
DEV void settle3(f32x4& s0, f32x4& s1, f32x4& s2) { asm volatile("s_nop 7" : "+v"(s0), "+v"(s1), "+v"(s2)); }
DEV void settle4(f32x4 (&d)[4]) { asm volatile("s_nop 7" : "+v"(d[0]), "+v"(d[1]), "+v"(d[2]), "+v"(d[3])); }
#define BAR() do { asm volatile("s_waitcnt lgkmcnt(0)" ::: "memory"); __builtin_amdgcn_s_barrier(); asm volatile("" ::: "memory"); } while (0)
#define VMWAIT(n) asm volatile("s_waitcnt vmcnt(" #n ")" ::: "memory")
DEV float add1(float a, float b) {
  float o; asm("v_add_f32 %0, %1, %2" : "=v"(o) : "v"(a), "v"(b)); return o;
}
DEV float xmax(float v) {
  auto r = __builtin_amdgcn_permlane32_swap(fu(v), fu(v), false, false); v = fmaxf(uf(r[0]), uf(r[1]));
  auto q = __builtin_amdgcn_permlane16_swap(fu(v), fu(v), false, false); return fmaxf(uf(q[0]), uf(q[1]));
}
DEV float xsum(float v) {
  auto r = __builtin_amdgcn_permlane32_swap(fu(v), fu(v), false, false); v = uf(r[0]) + uf(r[1]);
  auto q = __builtin_amdgcn_permlane16_swap(fu(v), fu(v), false, false); return uf(q[0]) + uf(q[1]);
}

DEV float ror8(float v) { return uf(__builtin_amdgcn_update_dpp(0u, fu(v), 0x128, 0xf, 0xf, true)); }
DEV float fmax2(float a, float b) { return __builtin_amdgcn_fmed3f(a, b, __builtin_inff()); }
DEV float fmax3(float a, float b, float c) { float r; asm("v_max3_f32 %0, %1, %2, %3" : "=v"(r) : "v"(a), "v"(b), "v"(c)); return r; }
DEV bool allok(unsigned t) { return __all(t != SENT); }

template <int N> struct IC { static constexpr int value = N; };
struct Ctx {
  gu32* ctrl;
  rsrc_t rs;
  lds_t L;
  int w, lane;
  int b, slot, att;
  unsigned ask;
  unsigned budget;
};
DEV bool spend(Ctx& c) {
  const unsigned old = c.budget;
  asm volatile("s_cmp_lg_u32 %0, 0\n\ts_subb_u32 %0, %0, 0" : "+s"(c.budget) :: "scc");
  return old == 0u;
}
DEV unsigned wait_flag(Ctx& c, int flag, unsigned need) {
  gu32* f = c.ctrl + (C_FLAG + flag * 128) / 4;
  unsigned v;
#pragma unroll 1
  for (;;) {
    v = __builtin_amdgcn_readfirstlane(__hip_atomic_load(f, RLX_AGENT));
    if (v >= need || spend(c)) break;
    __builtin_amdgcn_s_sleep(2);
  }
  __builtin_amdgcn_fence(__ATOMIC_ACQUIRE, "wavefront");
  return v;
}
DEV void post_flag(Ctx& c, int flag, unsigned v, int off = 0) {
  st4s(mkrs((const void*)c.ctrl, CTRL_BYTES), (c.w | off | fresh_lane()) ? 1 << 30 : 0, C_FLAG + flag * 128, v);
}
DEV void claim(Ctx& c, unsigned votes) {
  if (c.w != 0) return;
  if (c.att == 0) {
#pragma unroll 1
    while (__builtin_amdgcn_readfirstlane(__hip_atomic_load(c.ctrl + C_ECHO / 4 + c.b, RLX_AGENT)) != c.ask) {
      if (spend(c)) break;
      __builtin_amdgcn_s_sleep(2);
    }
  }
  if (fresh_lane() == 0) __hip_atomic_fetch_add(c.ctrl + (C_CLAIM + c.att * 1024) / 4 + c.slot, 1u + votes, RLX_AGENT);
}
DEV unsigned wait_verdict(Ctx& c) {
  unsigned v;
#pragma unroll 1
  for (;;) {
    v = __builtin_amdgcn_readfirstlane(__hip_atomic_load(c.ctrl + (C_VERDICT + c.att * 128) / 4, RLX_AGENT));
    if ((v & 1u) || spend(c)) break;
    __builtin_amdgcn_s_sleep(2);
  }
  __builtin_amdgcn_fence(__ATOMIC_ACQUIRE, "wavefront");
  if ((v & 3u) == 3u && c.att == 0) {
    asm volatile("s_waitcnt vmcnt(0) lgkmcnt(0)\n\ts_barrier\n\ts_mov_b64 s[0:1], %0\n\ts_mov_b32 s2, %1\n\tv_mov_b32 v0, %2\n\t"
                 "s_getpc_b64 s[4:5]\n1:\n\ts_sub_u32 s4, s4, 1b-mk_mega\n\ts_subb_u32 s5, s5, 0\n\ts_setpc_b64 s[4:5]"
                 :: "s"(__builtin_amdgcn_kernarg_segment_ptr()), "s"(c.b + 256), "v"(c.w * 64 + fresh_lane()) : "s0", "s1", "s2", "s4", "s5", "v0", "scc", "memory");
    __builtin_unreachable();
  }
  return v;
}
DEV void serve(Ctx& c) {
  c.budget = SPIN_CAP;
  const int lane = fresh_lane();
  const rsrc_t rc = mkrs((const void*)c.ctrl, CTRL_BYTES);
  if (c.att == 0) {
#pragma unroll
    for (int i = C_CLAIM / 1024; i < C_ASK / 1024; ++i) st16s(rc, lane * 16, i * 1024, v4u{0, 0, 0, 0});
    VMWAIT(0);
  }
#pragma unroll 1
  for (;;) {
    asm volatile("" ::: "memory");
    const v4u cl = ld16s(rc, lane * 16, C_CLAIM + c.att * 1024);
    if (c.att == 0) {
      v4u e;
#pragma unroll
      for (int i = 0; i < 4; ++i) e[i] = __builtin_amdgcn_raw_buffer_load_b32(rc, (lane * 4 + i) * 64, C_ASK, AUX_SC1);
      st16s(rc, lane * 16, C_ECHO, e);
    }
    unsigned n = 0, one = 1, va = 0, vb = 0;
#pragma unroll
    for (int i = 0; i < 4; ++i) { n += cl[i] & 0xFFFFu; one &= (cl[i] & 0xFFFFu) == 1u; va += (cl[i] >> 16) & 1u; vb += (cl[i] >> 24) & 1u; }
    unsigned sum = n | (va << 12) | (vb << 22);
#pragma unroll
    for (int o = 1; o < 64; o <<= 1) sum += (unsigned)__shfl_xor((int)sum, o);
    sum = __builtin_amdgcn_readfirstlane(sum);
    if ((sum & 0xFFFu) >= 256u) {
      const unsigned v = 1u | (__all(one) ? 0u : 2u) | (((sum >> 12) & 0x3FFu) > 32u ? 4u : 0u) | ((sum >> 22) > 32u ? 8u : 0u);
      if (lane == 0) __hip_atomic_store(c.ctrl + (C_VERDICT + c.att * 128) / 4, v, RLX_AGENT);
      break;
    }
    if (spend(c)) break;
  }
}
DEV int vote_a(unsigned v) { return (int)((v >> 2) & 1u); }
DEV int vote_b(unsigned v) { return (int)((v >> 3) & 1u); }

#define STAMP(slot) do {} while (0)
#define TICK_DECL
#define TICK(i) do {} while (0)
#define TICK_SUM(n) do {} while (0)
#define TICK_LDS(b) do {} while (0)
#define TICK_DUMP(b) do {} while (0)
#define TICK_OUT() do {} while (0)

DEV constexpr int krow(int rho8, int i) { return 32 * (rho8 >> 2) + 8 * (rho8 & 1) + 2 * ((rho8 >> 1) & 1) + i; }
DEV int krow_lane(int g) { return 16 * (g >> 1) + 4 * (g & 1); }
DEV unsigned prm(unsigned hi, unsigned lo, int odd) { return __builtin_amdgcn_perm(hi, lo, odd ? 0x07060302u : 0x05040100u); }

#define AT_ACC(M, n) do { const int n_ = (n); if (n_ == 0) M("{a[0:7]}"); if (n_ == 1) M("{a[8:15]}"); if (n_ == 2) M("{a[16:23]}"); if (n_ == 3) M("{a[24:31]}"); if (n_ == 4) M("{a[32:39]}"); if (n_ == 5) M("{a[40:47]}"); if (n_ == 6) M("{a[48:55]}"); if (n_ == 7) M("{a[56:63]}"); if (n_ == 8) M("{a[64:71]}"); if (n_ == 9) M("{a[72:79]}"); if (n_ == 10) M("{a[80:87]}"); if (n_ == 11) M("{a[88:95]}"); if (n_ == 12) M("{a[96:103]}"); if (n_ == 13) M("{a[104:111]}"); if (n_ == 14) M("{a[112:119]}"); if (n_ == 15) M("{a[120:127]}"); if (n_ == 16) M("{a[128:135]}"); if (n_ == 17) M("{a[136:143]}"); if (n_ == 18) M("{a[144:151]}"); if (n_ == 19) M("{a[152:159]}"); if (n_ == 20) M("{a[160:167]}"); if (n_ == 21) M("{a[168:175]}"); if (n_ == 22) M("{a[176:183]}"); if (n_ == 23) M("{a[184:191]}"); if (n_ == 24) M("{a[192:199]}"); if (n_ == 25) M("{a[200:207]}"); if (n_ == 26) M("{a[208:215]}"); if (n_ == 27) M("{a[216:223]}"); if (n_ == 28) M("{a[224:231]}"); if (n_ == 29) M("{a[232:239]}"); if (n_ == 30) M("{a[240:247]}"); if (n_ == 31) M("{a[248:255]}"); } while (0)
#define PIN_ACC0(x) asm volatile("" :: "{a[0:7]}"(x[0]), "{a[8:15]}"(x[1]), "{a[16:23]}"(x[2]), "{a[24:31]}"(x[3]), "{a[32:39]}"(x[4]), "{a[40:47]}"(x[5]), "{a[48:55]}"(x[6]), "{a[56:63]}"(x[7]), "{a[64:71]}"(x[8]), "{a[72:79]}"(x[9]), "{a[80:87]}"(x[10]), "{a[88:95]}"(x[11]), "{a[96:103]}"(x[12]), "{a[104:111]}"(x[13]), "{a[112:119]}"(x[14]), "{a[120:127]}"(x[15]))
#define PIN_ACC1(x) asm volatile("" :: "{a[128:135]}"(x[16]), "{a[136:143]}"(x[17]), "{a[144:151]}"(x[18]), "{a[152:159]}"(x[19]), "{a[160:167]}"(x[20]), "{a[168:175]}"(x[21]), "{a[176:183]}"(x[22]), "{a[184:191]}"(x[23]), "{a[192:199]}"(x[24]), "{a[200:207]}"(x[25]), "{a[208:215]}"(x[26]), "{a[216:223]}"(x[27]), "{a[224:231]}"(x[28]), "{a[232:239]}"(x[29]), "{a[240:247]}"(x[30]), "{a[248:255]}"(x[31]))
#define PIN_HOME(x) asm volatile("" :: "{v[32:39]}"(x[0]), "{v[40:47]}"(x[1]), "{v[48:55]}"(x[2]), "{v[56:63]}"(x[3]), "{v[64:71]}"(x[4]), "{v[72:79]}"(x[5]), "{v[80:87]}"(x[6]), "{v[88:95]}"(x[7]), "{v[96:103]}"(x[8]), "{v[104:111]}"(x[9]), "{v[112:119]}"(x[10]), "{v[120:127]}"(x[11]), "{v[128:135]}"(x[12]), "{v[136:143]}"(x[13]), "{v[144:151]}"(x[14]), "{v[152:159]}"(x[15]))
#define CL_32_100 "v32", "v33", "v34", "v35", "v36", "v37", "v38", "v39", "v40", "v41", "v42", "v43", "v44", "v45", "v46", "v47", "v48", "v49", "v50", "v51", "v52", "v53", "v54", "v55", "v56", "v57", "v58", "v59", "v60", "v61", "v62", "v63", "v64", "v65", "v66", "v67", "v68", "v69", "v70", "v71", "v72", "v73", "v74", "v75", "v76", "v77", "v78", "v79", "v80", "v81", "v82", "v83", "v84", "v85", "v86", "v87", "v88", "v89", "v90", "v91", "v92", "v93", "v94", "v95", "v96", "v97", "v98", "v99", "v100", "v101", "v102", "v103", "v104", "v105", "v106", "v107", "v108", "v109", "v110", "v111", "v112", "v113", "v114", "v115", "v116", "v117", "v118", "v119", "v120", "v121", "v122", "v123", "v124", "v125", "v126", "v127", "v128", "v129", "v130", "v131"
#define ACC_IN(x) "{a[0:31]}"(x[0]), "{a[32:63]}"(x[1]), "{a[64:95]}"(x[2]), "{a[96:127]}"(x[3]), "{a[128:159]}"(x[4]), "{a[160:191]}"(x[5]), "{a[192:223]}"(x[6]), "{a[224:255]}"(x[7])
#define CL_16_32 "v16", "v17", "v18", "v19", "v20", "v21", "v22", "v23", "v24", "v25", "v26", "v27", "v28", "v29", "v30", "v31"
#define CL_120_128 "v120", "v121", "v122", "v123", "v124", "v125", "v126", "v127"
#define CL_160_200 "v160", "v161", "v162", "v163", "v164", "v165", "v166", "v167", "v168", "v169", "v170", "v171", "v172", "v173", "v174", "v175", "v176", "v177", "v178", "v179", "v180", "v181", "v182", "v183", "v184", "v185", "v186", "v187", "v188", "v189", "v190", "v191", "v192", "v193", "v194", "v195", "v196", "v197", "v198", "v199"
#define CL_176_200 "v176", "v177", "v178", "v179", "v180", "v181", "v182", "v183", "v184", "v185", "v186", "v187", "v188", "v189", "v190", "v191", "v192", "v193", "v194", "v195", "v196", "v197", "v198", "v199"
#define CL_200_216 "v200", "v201", "v202", "v203", "v204", "v205", "v206", "v207", "v208", "v209", "v210", "v211", "v212", "v213", "v214", "v215"
#define CL_216_256 "v216", "v217", "v218", "v219", "v220", "v221", "v222", "v223", "v224", "v225", "v226", "v227", "v228", "v229", "v230", "v231", "v232", "v233", "v234", "v235", "v236", "v237", "v238", "v239", "v240", "v241", "v242", "v243", "v244", "v245", "v246", "v247", "v248", "v249", "v250", "v251", "v252", "v253", "v254", "v255"
#define CL_S "memory", "vcc", "scc", "s60", "s61", "s62", "s63", "s64", "s65", "s66", "s67"
#define TIMED_WAIT \
    "s_getpc_b64 s[62:63]\n\t" "1:\n\t" "s_memtime s[64:65]\n\t" "s_waitcnt lgkmcnt(0)\n\t" "s_sub_u32 s61, %0, s64\n\t" "s_cmp_gt_i32 s61, 124\n\t" "s_cbranch_scc1 1b\n\t" \
    "s_max_i32 s61, s61, 0\n\t" "s_and_b32 s61, s61, 124\n\t" "s_sub_u32 s61, 2f-1b, s61\n\t" "s_add_u32 s62, s62, s61\n\t" "s_addc_u32 s63, s63, 0\n\t" \
    "s_setpc_b64 s[62:63]\n\t" ".rept 31\n\t" "s_nop 0\n\t" ".endr\n\t" "2:\n\t"
#define QK_STEP0 \
    "v_xor_b32 v216, 0x400, %[mo]\n\t" "s_mov_b32 s60, 0\n\t" "s_mov_b32 s66, 0x3e0293ee\n\t" "s_mov_b32 s67, 0x3e0293ee\n\t" XT(80, 81) "s_add_u32 s61, %[kb], 0x1000\n\t" \
    "buffer_load_dwordx4 v[16:19], %[l1], %[rs], %[kb] offen offset:0 sc1\n\t" "buffer_load_dwordx4 v[20:23], %[l1], %[rs], %[kb] offen offset:1024 sc1\n\t" \
    "buffer_load_dwordx4 v[24:27], %[l1], %[rs], %[kb] offen offset:2048 sc1\n\t" "buffer_load_dwordx4 v[28:31], %[l1], %[rs], %[kb] offen offset:3072 sc1\n\t" \
    "buffer_load_dwordx4 v[200:203], %[l1], %[rs], s61 offen offset:0 sc1\n\t" "buffer_load_dwordx4 v[204:207], %[l1], %[rs], s61 offen offset:1024 sc1\n\t" \
    "buffer_load_dwordx4 v[208:211], %[l1], %[rs], s61 offen offset:2048 sc1\n\t" "buffer_load_dwordx4 v[212:215], %[l1], %[rs], s61 offen offset:3072 sc1\n\t" \
    "v_mov_b64 v[218:219], -1\n\t" "buffer_store_dwordx2 v[218:219], %[px], %[rs], %[pr] offen sc1\n\t" "buffer_store_dword v218, %[pm], %[rs], %[pr] offen sc1\n\t" \
    "s_waitcnt vmcnt(2)\n\t" XT(98, 99) "s_barrier\n\t" XT(82, 83) "10:\n\t" "buffer_load_dwordx4 v[240:243], %[mo], %[rs], %[sb] offen offset:0 sc1\n\t" \
    "v_mov_b64 v[224:225], 0\n\t" "v_mov_b64 v[226:227], 0\n\t" "v_mov_b64 v[228:229], 0\n\t" "v_mov_b64 v[230:231], 0\n\t" "v_mov_b64 v[232:233], 0\n\t" \
    "v_mov_b64 v[234:235], 0\n\t" "v_mov_b64 v[236:237], 0\n\t" "v_mov_b64 v[238:239], 0\n\t" "buffer_load_dwordx4 v[244:247], v216, %[rs], %[sb] offen offset:0 sc1\n\t" \
    "ds_read_b128 v[160:163], %[l0] offset:0\n\t" "ds_read_b128 v[164:167], %[l0] offset:1024\n\t" "ds_read_b128 v[168:171], %[l0] offset:2048\n\t" \
    "ds_read_b128 v[172:175], %[l0] offset:3072\n\t" "s_nop 3\n\t" "buffer_load_dwordx4 v[248:251], %[mo], %[rs], %[sb] offen offset:2048 sc1\n\t" \
    "ds_read_b128 v[176:179], %[l0] offset:4096\n\t" "ds_read_b128 v[180:183], %[l0] offset:5120\n\t" "ds_read_b128 v[184:187], %[l0] offset:6144\n\t" \
    "ds_read_b128 v[188:191], %[l0] offset:7168\n\t" "s_nop 3\n\t" "buffer_load_dwordx4 v[252:255], v216, %[rs], %[sb] offen offset:2048 sc1\n\t" "s_cmp_lg_u32 s61, 0\n\t" \
    "s_cbranch_scc0 177f\n\t" "ds_write_b128 %[l1], v[16:19] offset:16384\n\t" "ds_write_b128 %[l1], v[20:23] offset:17408\n\t" \
    "ds_write_b128 %[l1], v[24:27] offset:18432\n\t" "ds_write_b128 %[l1], v[28:31] offset:19456\n\t" "ds_write_b128 %[l1], v[200:203] offset:20480\n\t" \
    "ds_write_b128 %[l1], v[204:207] offset:21504\n\t" "ds_write_b128 %[l1], v[208:211] offset:22528\n\t" "ds_write_b128 %[l1], v[212:215] offset:23552\n\t" \
    "s_mov_b32 s61, 0\n\t" "s_waitcnt lgkmcnt(0)\n\t" "177:\n\t" "v_mov_b64 v[208:209], 0\n\t" "v_mov_b64 v[210:211], 0\n\t" "v_mov_b64 v[212:213], 0\n\t" \
    "v_mov_b64 v[214:215], 0\n\t" "s_waitcnt vmcnt(3)\n\t" XT(84, 85) "v_smfmac_f32_16x16x64_bf16 v[224:227], v[240:243], a[0:7], %[ix]\n\t" \
    "ds_write_b128 %[l1], v[240:243] offset:0\n\t" "v_smfmac_f32_16x16x64_bf16 v[228:231], v[240:243], a[8:15], %[ix]\n\t" "v_max3_u32 v217, v240, v241, v242\n\t" \
    "v_smfmac_f32_16x16x64_bf16 v[232:235], v[240:243], v[32:39], %[ix]\n\t" "s_waitcnt lgkmcnt(7)\n\t" \
    "v_smfmac_f32_16x16x64_bf16 v[236:239], v[240:243], v[160:167], %[ix]\n\t" "ds_read_b128 v[160:163], %[l0] offset:8192\n\t" \
    "ds_read_b128 v[164:167], %[l0] offset:9216\n\t" "s_waitcnt vmcnt(2)\n\t" "v_smfmac_f32_16x16x64_bf16 v[224:227], v[244:247], a[16:23], %[ix]\n\t" \
    "ds_write_b128 %[l1], v[244:247] offset:1024\n\t" "v_max3_u32 v217, v217, v243, v244\n\t" "v_smfmac_f32_16x16x64_bf16 v[228:231], v[244:247], a[24:31], %[ix]\n\t" \
    "v_max3_u32 v217, v217, v245, v246\n\t" "v_max_u32 v217, v217, v247\n\t" "v_smfmac_f32_16x16x64_bf16 v[232:235], v[244:247], v[40:47], %[ix]\n\t" \
    "v_cmp_eq_u32 vcc, -1, v217\n\t" "s_cmp_lg_u32 s60, 0\n\t" "s_waitcnt lgkmcnt(8)\n\t" "v_smfmac_f32_16x16x64_bf16 v[236:239], v[244:247], v[168:175], %[ix]\n\t" \
    "s_cbranch_vccnz 11f\n\t" "12:\n\t" "ds_read_b128 v[168:171], %[l0] offset:10240\n\t" "s_waitcnt vmcnt(1)\n\t" \
    "v_smfmac_f32_16x16x64_bf16 v[224:227], v[248:251], a[32:39], %[ix]\n\t" "ds_write_b128 %[l1], v[248:251] offset:2048\n\t" "s_waitcnt lgkmcnt(2)\n\t" \
    "v_smfmac_f32_16x16x64_bf16 v[228:231], v[248:251], a[40:47], %[ix]\n\t" "s_cbranch_scc1 13f\n\t" "s_barrier\n\t" "13:\n\t" XT(86, 87) \
    "v_smfmac_f32_16x16x64_bf16 v[232:235], v[248:251], v[48:55], %[ix]\n\t" "ds_read_b128 v[192:195], %[l1] offset:4096\n\t" "ds_read_b128 v[196:199], %[l1] offset:8192\n\t" \
    "v_smfmac_f32_16x16x64_bf16 v[236:239], v[248:251], v[176:183], %[ix]\n\t" "ds_read_b128 v[172:175], %[l0] offset:11264\n\t" \
    "ds_read_b128 v[176:179], %[l0] offset:12288\n\t" "s_waitcnt vmcnt(0)\n\t" "v_smfmac_f32_16x16x64_bf16 v[224:227], v[252:255], a[48:55], %[ix]\n\t" \
    "ds_write_b128 %[l1], v[252:255] offset:3072\n\t" "v_max3_u32 v217, v248, v249, v250\n\t" "v_smfmac_f32_16x16x64_bf16 v[228:231], v[252:255], a[56:63], %[ix]\n\t" \
    "ds_read_b128 v[180:183], %[l0] offset:13312\n\t" "v_max3_u32 v217, v217, v251, v252\n\t" "v_max3_u32 v217, v217, v253, v254\n\t" \
    "v_smfmac_f32_16x16x64_bf16 v[232:235], v[252:255], v[56:63], %[ix]\n\t" "v_max_u32 v217, v217, v255\n\t" "v_cmp_eq_u32 vcc, -1, v217\n\t" \
    "v_smfmac_f32_16x16x64_bf16 v[236:239], v[252:255], v[184:191], %[ix]\n\t" "s_cbranch_vccnz 14f\n\t" "15:\n\t" "ds_read_b128 v[240:243], %[l1] offset:12288\n\t" \
    "s_waitcnt lgkmcnt(6)\n\t" "v_smfmac_f32_16x16x64_bf16 v[224:227], v[192:195], a[64:71], %[ix]\n\t" "ds_read_b128 v[184:187], %[l0] offset:14336\n\t" \
    "ds_read_b128 v[188:191], %[l0] offset:15360\n\t" "v_smfmac_f32_16x16x64_bf16 v[228:231], v[192:195], a[72:79], %[ix]\n\t" \
    "ds_read_b128 v[244:247], %[l1] offset:5120\n\t" "s_waitcnt lgkmcnt(5)\n\t" "v_smfmac_f32_16x16x64_bf16 v[232:235], v[192:195], v[64:71], %[ix]\n\t" "s_barrier\n\t" \
    XT(88, 89) "v_smfmac_f32_16x16x64_bf16 v[236:239], v[192:195], v[160:167], %[ix]\n\t" "ds_read_b128 v[160:163], %[l0] offset:16384\n\t" \
    "ds_read_b128 v[164:167], %[l0] offset:17408\n\t" "v_smfmac_f32_16x16x64_bf16 v[224:227], v[196:199], a[80:87], %[ix]\n\t" \
    "ds_read_b128 v[248:251], %[l1] offset:9216\n\t" "ds_read_b128 v[252:255], %[l1] offset:13312\n\t" \
    "v_smfmac_f32_16x16x64_bf16 v[228:231], v[196:199], a[88:95], %[ix]\n\t" "ds_read_b128 v[192:195], %[l1] offset:6144\n\t" \
    "v_smfmac_f32_16x16x64_bf16 v[232:235], v[196:199], v[72:79], %[ix]\n\t" "ds_read_b128 v[16:19], %[kx] offset:0\n\t" \
    "v_smfmac_f32_16x16x64_bf16 v[236:239], v[196:199], v[168:175], %[ix]\n\t" "ds_read_b128 v[168:171], %[l0] offset:18432\n\t" \
    "ds_read_b128 v[172:175], %[l0] offset:19456\n\t" "s_waitcnt lgkmcnt(11)\n\t" "v_smfmac_f32_16x16x64_bf16 v[224:227], v[240:243], a[96:103], %[ix]\n\t" \
    "ds_read_b128 v[196:199], %[l1] offset:10240\n\t" "v_smfmac_f32_16x16x64_bf16 v[228:231], v[240:243], a[104:111], %[ix]\n\t" \
    "ds_read_b128 v[20:23], %[kx] offset:2048\n\t" "v_smfmac_f32_16x16x64_bf16 v[232:235], v[240:243], v[80:87], %[ix]\n\t" \
    "v_smfmac_f32_16x16x64_bf16 v[236:239], v[240:243], v[176:183], %[ix]\n\t" "ds_read_b128 v[176:179], %[l0] offset:20480\n\t" \
    "ds_read_b128 v[180:183], %[l0] offset:21504\n\t" "s_waitcnt lgkmcnt(12)\n\t" "v_smfmac_f32_16x16x64_bf16 v[224:227], v[244:247], a[112:119], %[ix]\n\t" \
    "ds_read_b128 v[240:243], %[l1] offset:14336\n\t" "ds_read_b128 v[24:27], %[kx] offset:4096\n\t" \
    "v_smfmac_f32_16x16x64_bf16 v[228:231], v[244:247], a[120:127], %[ix]\n\t" "v_smfmac_f32_16x16x64_bf16 v[232:235], v[244:247], v[88:95], %[ix]\n\t" \
    "v_smfmac_f32_16x16x64_bf16 v[236:239], v[244:247], v[184:191], %[ix]\n\t" "ds_read_b128 v[184:187], %[l0] offset:22528\n\t" \
    "ds_read_b128 v[188:191], %[l0] offset:23552\n\t" "s_waitcnt lgkmcnt(13)\n\t" "v_smfmac_f32_16x16x64_bf16 v[224:227], v[248:251], a[128:135], %[ix]\n\t" \
    "ds_read_b128 v[244:247], %[l1] offset:7168\n\t" "ds_read_b128 v[28:31], %[kx] offset:6144\n\t" "v_smfmac_f32_16x16x64_bf16 v[228:231], v[248:251], a[136:143], %[ix]\n\t" \
    "v_smfmac_f32_16x16x64_bf16 v[232:235], v[248:251], v[96:103], %[ix]\n\t" "ds_read_b128 v[200:203], %[kx] offset:256\n\t" \
    "v_smfmac_f32_16x16x64_bf16 v[236:239], v[248:251], v[160:167], %[ix]\n\t" "ds_read_b128 v[160:163], %[l0] offset:24576\n\t" \
    "ds_read_b128 v[164:167], %[l0] offset:25600\n\t" "s_waitcnt lgkmcnt(14)\n\t" "v_smfmac_f32_16x16x64_bf16 v[224:227], v[252:255], a[144:151], %[ix]\n\t" \
    "ds_read_b128 v[248:251], %[l1] offset:11264\n\t" "v_smfmac_f32_16x16x64_bf16 v[228:231], v[252:255], a[152:159], %[ix]\n\t" \
    "ds_read_b128 v[204:207], %[kx] offset:2304\n\t" "v_smfmac_f32_16x16x64_bf16 v[232:235], v[252:255], v[104:111], %[ix]\n\t" "s_waitcnt lgkmcnt(14)\n\t" \
    "v_smfmac_f32_16x16x64_bf16 v[236:239], v[252:255], v[168:175], %[ix]\n\t" "ds_read_b128 v[168:171], %[l0] offset:26624\n\t" \
    "ds_read_b128 v[172:175], %[l0] offset:27648\n\t" "v_smfmac_f32_16x16x64_bf16 v[224:227], v[192:195], a[160:167], %[ix]\n\t" \
    "ds_read_b128 v[252:255], %[l1] offset:15360\n\t" "v_smfmac_f32_16x16x64_bf16 v[228:231], v[192:195], a[168:175], %[ix]\n\t" \
    "v_smfmac_f32_16x16x64_bf16 v[224:227], v[196:199], a[176:183], %[ix]\n\t" "v_smfmac_f32_16x16x64_bf16 v[228:231], v[196:199], a[184:191], %[ix]\n\t" \
    "s_waitcnt lgkmcnt(13)\n\t" "v_smfmac_f32_16x16x64_bf16 v[224:227], v[240:243], a[192:199], %[ix]\n\t" \
    "v_smfmac_f32_16x16x64_bf16 v[228:231], v[240:243], a[200:207], %[ix]\n\t" "s_waitcnt lgkmcnt(9)\n\t" \
    "v_smfmac_f32_16x16x64_bf16 v[224:227], v[244:247], a[208:215], %[ix]\n\t" "v_smfmac_f32_16x16x64_bf16 v[228:231], v[244:247], a[216:223], %[ix]\n\t" \
    "s_waitcnt lgkmcnt(4)\n\t" "v_smfmac_f32_16x16x64_bf16 v[224:227], v[248:251], a[224:231], %[ix]\n\t" \
    "v_smfmac_f32_16x16x64_bf16 v[228:231], v[248:251], a[232:239], %[ix]\n\t" "s_waitcnt lgkmcnt(0)\n\t" \
    "v_smfmac_f32_16x16x64_bf16 v[224:227], v[252:255], a[240:247], %[ix]\n\t" "s_nop 1\n\t" "v_smfmac_f32_16x16x64_bf16 v[228:231], v[252:255], a[248:255], %[ix]\n\t" \
    XT(90, 91) "s_nop 1\n\t" "v_smfmac_f32_16x16x64_bf16 v[232:235], v[192:195], v[112:119], %[ix]\n\t" "s_nop 1\n\t" \
    "v_smfmac_f32_16x16x64_bf16 v[236:239], v[192:195], v[176:183], %[ix]\n\t" "ds_read_b128 v[176:179], %[l0] offset:28672\n\t" "v_add_f32 v224, v224, v225\n\t" \
    "v_add_f32 v226, v226, v227\n\t" "v_smfmac_f32_16x16x64_bf16 v[232:235], v[196:199], v[120:127], %[ix]\n\t" "ds_read_b128 v[180:183], %[l0] offset:29696\n\t" \
    "v_add_f32 v228, v228, v229\n\t" "v_add_f32 v230, v230, v231\n\t" "v_smfmac_f32_16x16x64_bf16 v[236:239], v[196:199], v[184:191], %[ix]\n\t" \
    "ds_read_b128 v[184:187], %[l0] offset:30720\n\t" "v_cvt_pk_bf16_f32 v218, v224, v228\n\t" "v_cvt_pk_bf16_f32 v219, v226, v230\n\t" \
    "v_smfmac_f32_16x16x64_bf16 v[232:235], v[240:243], v[128:135], %[ix]\n\t" "ds_write2_b32 %[qw], v218, v219 offset0:0 offset1:8\n\t" \
    "ds_read_b128 v[188:191], %[l0] offset:31744\n\t" "v_smfmac_f32_16x16x64_bf16 v[236:239], v[240:243], v[160:167], %[ix]\n\t" \
    "ds_read_b128 v[192:195], %[kx] offset:4352\n\t" "ds_read_b128 v[196:199], %[kx] offset:6400\n\t" \
    "v_smfmac_f32_16x16x64_bf16 v[232:235], v[244:247], v[136:143], %[ix]\n\t" "v_smfmac_f32_16x16x64_bf16 v[236:239], v[244:247], v[168:175], %[ix]\n\t" \
    "s_waitcnt lgkmcnt(3)\n\t" "s_barrier\n\t" "v_smfmac_f32_16x16x64_bf16 v[232:235], v[248:251], v[144:151], %[ix]\n\t" "ds_read_b128 v[216:219], %[l1] offset:24576\n\t" \
    "ds_read_b128 v[220:223], %[l1] offset:25600\n\t" "v_smfmac_f32_16x16x64_bf16 v[236:239], v[248:251], v[176:183], %[ix]\n\t" \
    "v_smfmac_f32_16x16x64_bf16 v[232:235], v[252:255], v[152:159], %[ix]\n\t" "s_waitcnt lgkmcnt(4)\n\t" \
    "v_smfmac_f32_16x16x64_bf16 v[236:239], v[252:255], v[184:191], %[ix]\n\t" XT(92, 93) "s_waitcnt lgkmcnt(1)\n\t" \
    "v_smfmac_f32_16x16x64_bf16 v[208:211], v[216:219], v[16:23], %[ix]\n\t" "s_nop 1\n\t" "v_smfmac_f32_16x16x64_bf16 v[212:215], v[216:219], v[200:207], %[ix]\n\t" \
    "s_waitcnt lgkmcnt(0)\n\t" "s_nop 0\n\t" "v_add_f32 v232, v232, v233\n\t" "v_add_f32 v234, v234, v235\n\t" "v_add_f32 v236, v236, v237\n\t" \
    "v_add_f32 v238, v238, v239\n\t" "v_cvt_pk_bf16_f32 v232, v232, v236\n\t" "v_cvt_pk_bf16_f32 v233, v234, v238\n\t" \
    "ds_write2_b32 %[kw], v232, v233 offset0:1 offset1:69\n\t" "v_smfmac_f32_16x16x64_bf16 v[208:211], v[220:223], v[24:31], %[ix]\n\t" \
    "v_smfmac_f32_16x16x64_bf16 v[212:215], v[220:223], v[192:199], %[ix]\n\t" "v_mov_b64 v[224:225], 0\n\t" "v_mov_b64 v[226:227], 0\n\t" "s_waitcnt lgkmcnt(0)\n\t" \
    "s_barrier\n\t" XT(94, 95) "ds_read_b128 v[160:163], %[km] offset:0\n\t" "ds_read_b128 v[164:167], %[km] offset:64\n\t" "ds_read_b128 v[168:171], %[km] offset:128\n\t" \
    "ds_read_b128 v[172:175], %[km] offset:192\n\t" "v_add_f32 v178, v208, v209\n\t" "v_add_f32 v179, v210, v211\n\t" "v_add_f32 v180, v212, v213\n\t" \
    "v_add_f32 v181, v214, v215\n\t" "v_max_f32 v182, v178, v180\n\t" "v_max_f32 v183, v179, v181\n\t" "s_nop 0\n\t" \
    "v_max_f32_dpp v182, v182, v182 row_ror:8 row_mask:0xf bank_mask:0xf\n\t" "v_max_f32_dpp v183, v183, v183 row_ror:8 row_mask:0xf bank_mask:0xf\n\t" "s_nop 0\n\t" \
    "v_max_f32_dpp v182, v182, v182 row_ror:4 row_mask:0xf bank_mask:0xf\n\t" "v_max_f32_dpp v183, v183, v183 row_ror:4 row_mask:0xf bank_mask:0xf\n\t" "s_nop 0\n\t" \
    "v_max_f32_dpp v182, v182, v182 row_ror:2 row_mask:0xf bank_mask:0xf\n\t" "v_max_f32_dpp v183, v183, v183 row_ror:2 row_mask:0xf bank_mask:0xf\n\t" "s_nop 0\n\t" \
    "v_max_f32_dpp v182, v182, v182 row_ror:1 row_mask:0xf bank_mask:0xf\n\t" "v_max_f32_dpp v183, v183, v183 row_ror:1 row_mask:0xf bank_mask:0xf\n\t" \
    "s_waitcnt lgkmcnt(2)\n\t" "v_smfmac_f32_16x16x64_bf16 v[224:227], v[216:219], v[160:167], %[ix]\n\t" "s_waitcnt lgkmcnt(0)\n\t" \
    "v_smfmac_f32_16x16x64_bf16 v[224:227], v[220:223], v[168:175], %[ix]\n\t" "s_nop 7\n\t" "v_add_f32 v176, v224, v225\n\t" "v_add_f32 v177, v226, v227\n\t" \
    "v_max_f32 v182, v176, v182\n\t" "v_max_f32 v183, v177, v183\n\t" "s_nop 0\n\t" "v_max_f32_dpp v182, v182, v182 row_ror:4 row_mask:0xf bank_mask:0xf\n\t" \
    "v_max_f32_dpp v183, v183, v183 row_ror:4 row_mask:0xf bank_mask:0xf\n\t" "s_nop 0\n\t" "v_max_f32_dpp v182, v182, v182 row_ror:2 row_mask:0xf bank_mask:0xf\n\t" \
    "v_max_f32_dpp v183, v183, v183 row_ror:2 row_mask:0xf bank_mask:0xf\n\t" "s_nop 0\n\t" "v_max_f32_dpp v182, v182, v182 row_ror:1 row_mask:0xf bank_mask:0xf\n\t" \
    "v_max_f32_dpp v183, v183, v183 row_ror:1 row_mask:0xf bank_mask:0xf\n\t" "v_pk_add_f32 v[178:179], v[178:179], v[182:183] neg_lo:[0,1] neg_hi:[0,1]\n\t" \
    "v_pk_add_f32 v[180:181], v[180:181], v[182:183] neg_lo:[0,1] neg_hi:[0,1]\n\t" "v_pk_add_f32 v[176:177], v[176:177], v[182:183] neg_lo:[0,1] neg_hi:[0,1]\n\t" \
    "v_pk_mul_f32 v[178:179], v[178:179], s[66:67]\n\t" "v_pk_mul_f32 v[180:181], v[180:181], s[66:67]\n\t" "v_pk_mul_f32 v[176:177], v[176:177], s[66:67]\n\t" \
    "v_exp_f32 v178, v178\n\t" "v_exp_f32 v179, v179\n\t" "v_exp_f32 v180, v180\n\t" "v_exp_f32 v181, v181\n\t" "v_exp_f32 v176, v176\n\t" \
    "v_pk_add_f32 v[182:183], v[178:179], v[180:181]\n\t" "v_exp_f32 v177, v177\n\t" "s_nop 0\n\t" "v_add_f32_dpp v182, v182, v182 row_ror:8 row_mask:0xf bank_mask:0xf\n\t" \
    "v_add_f32_dpp v183, v183, v183 row_ror:8 row_mask:0xf bank_mask:0xf\n\t" "v_add_f32 v182, v182, v176\n\t" "v_add_f32 v183, v183, v177\n\t" "s_nop 0\n\t" \
    "v_add_f32_dpp v182, v182, v182 row_ror:4 row_mask:0xf bank_mask:0xf\n\t" "v_add_f32_dpp v183, v183, v183 row_ror:4 row_mask:0xf bank_mask:0xf\n\t" "s_nop 0\n\t" \
    "v_add_f32_dpp v182, v182, v182 row_ror:2 row_mask:0xf bank_mask:0xf\n\t" "v_add_f32_dpp v183, v183, v183 row_ror:2 row_mask:0xf bank_mask:0xf\n\t" "s_nop 0\n\t" \
    "v_add_f32_dpp v182, v182, v182 row_ror:1 row_mask:0xf bank_mask:0xf\n\t" "v_add_f32_dpp v183, v183, v183 row_ror:1 row_mask:0xf bank_mask:0xf\n\t" \
    "v_rcp_f32 v182, v182\n\t" "v_rcp_f32 v183, v183\n\t" "s_nop 0\n\t" "v_pk_mul_f32 v[176:177], v[176:177], v[182:183]\n\t" \
    "v_pk_mul_f32 v[178:179], v[178:179], v[182:183]\n\t" "v_pk_mul_f32 v[180:181], v[180:181], v[182:183]\n\t" "v_cvt_pk_bf16_f32 v176, v176, v177\n\t" \
    "v_cvt_pk_bf16_f32 v178, v178, v179\n\t" "v_cvt_pk_bf16_f32 v180, v180, v181\n\t" "v_mov_b32_dpp v177, v176 quad_perm:[1,0,3,2] row_mask:0xf bank_mask:0xf\n\t" \
    "v_mov_b32_dpp v179, v178 quad_perm:[1,0,3,2] row_mask:0xf bank_mask:0xf\n\t" "v_mov_b32_dpp v181, v180 quad_perm:[1,0,3,2] row_mask:0xf bank_mask:0xf\n\t" \
    "v_perm_b32 v184, v179, v178, %[ps]\n\t" "v_perm_b32 v185, v181, v180, %[ps]\n\t" "buffer_store_dwordx2 v[184:185], %[px], %[rs], %[pb] offen sc1\n\t" \
    "v_perm_b32 v186, v177, v176, %[ps]\n\t" "buffer_store_dword v186, %[pm], %[rs], %[pb] offen sc1\n\t" XT(96, 97) "s_branch 99f\n\t" "11:\n\t" "s_cmp_eq_u32 %[bd], 0\n\t" \
    "s_cbranch_scc1 16f\n\t" "s_sub_u32 %[bd], %[bd], 1\n\t" XCNT(100) "s_waitcnt lgkmcnt(0)\n\t" "s_nop 7\n\t" "s_branch 10b\n\t" "16:\n\t" "s_cmp_lg_u32 s60, 0\n\t" \
    "s_branch 12b\n\t" "14:\n\t" "s_cmp_eq_u32 %[bd], 0\n\t" "s_cbranch_scc1 15b\n\t" "s_sub_u32 %[bd], %[bd], 1\n\t" XCNT(101) "s_mov_b32 s60, 1\n\t" \
    "s_waitcnt lgkmcnt(0)\n\t" "s_nop 7\n\t" "s_branch 10b\n\t" "99:\n\t"
#define QK_STEP1 \
    "v_xor_b32 v216, 0x400, %[mo]\n\t" "s_mov_b32 s60, 0\n\t" "s_add_u32 s61, %[qb], 0x1000\n\t" "s_mov_b32 s66, 0x3e0293ee\n\t" "s_mov_b32 s67, 0x3e0293ee\n\t" XT(80, 81) \
    XT(98, 99) "s_barrier\n\t" XT(82, 83) "20:\n\t" "buffer_load_dwordx4 v[240:243], %[mo], %[rs], %[sb] offen offset:0 sc1\n\t" "v_mov_b64 v[224:225], 0\n\t" \
    "v_mov_b64 v[226:227], 0\n\t" "v_mov_b64 v[228:229], 0\n\t" "v_mov_b64 v[230:231], 0\n\t" "v_mov_b64 v[232:233], 0\n\t" "v_mov_b64 v[234:235], 0\n\t" \
    "v_mov_b64 v[236:237], 0\n\t" "v_mov_b64 v[238:239], 0\n\t" "buffer_load_dwordx4 v[244:247], v216, %[rs], %[sb] offen offset:0 sc1\n\t" \
    "ds_read_b128 v[160:163], %[l0] offset:0\n\t" "ds_read_b128 v[164:167], %[l0] offset:1024\n\t" "ds_read_b128 v[168:171], %[l0] offset:2048\n\t" \
    "ds_read_b128 v[172:175], %[l0] offset:3072\n\t" "s_nop 3\n\t" "buffer_load_dwordx4 v[248:251], %[mo], %[rs], %[sb] offen offset:2048 sc1\n\t" \
    "ds_read_b128 v[176:179], %[l0] offset:4096\n\t" "ds_read_b128 v[180:183], %[l0] offset:5120\n\t" "ds_read_b128 v[184:187], %[l0] offset:6144\n\t" \
    "ds_read_b128 v[188:191], %[l0] offset:7168\n\t" "s_nop 3\n\t" "buffer_load_dwordx4 v[252:255], v216, %[rs], %[sb] offen offset:2048 sc1\n\t" \
    "v_mov_b32 v208, 0xd028965f\n\t" "v_mov_b32 v209, 0xff800000\n\t" "s_mov_b32 s62, 0\n\t" "s_mov_b32 s63, -1\n\t" "v_mov_b32 v210, 0\n\t" "v_cmp_gt_i32 vcc, 0, %[ps]\n\t" \
    "v_cndmask_b32 v204, 0, v209, s[62:63]\n\t" "s_nop 0\n\t" "v_cndmask_b32 v200, 0, v208, vcc\n\t" "v_cmp_gt_i32 vcc, 1, %[ps]\n\t" \
    "v_cndmask_b32 v205, 0, v209, s[62:63]\n\t" "s_nop 0\n\t" "v_cndmask_b32 v201, 0, v208, vcc\n\t" "v_cmp_gt_i32 vcc, 2, %[ps]\n\t" \
    "v_cndmask_b32 v206, 0, v209, s[62:63]\n\t" "s_nop 0\n\t" "v_cndmask_b32 v202, 0, v208, vcc\n\t" "v_cmp_gt_i32 vcc, 3, %[ps]\n\t" \
    "v_cndmask_b32 v207, 0, v209, s[62:63]\n\t" "s_nop 0\n\t" "v_cndmask_b32 v203, 0, v208, vcc\n\t" "s_waitcnt vmcnt(3)\n\t" XT(84, 85) \
    "v_smfmac_f32_16x16x64_bf16 v[224:227], v[240:243], a[0:7], %[ix]\n\t" "ds_write_b128 %[l1], v[240:243] offset:4096\n\t" \
    "v_smfmac_f32_16x16x64_bf16 v[228:231], v[240:243], a[8:15], %[ix]\n\t" "v_max3_u32 v217, v240, v241, v242\n\t" \
    "v_smfmac_f32_16x16x64_bf16 v[232:235], v[240:243], v[32:39], %[ix]\n\t" "s_waitcnt lgkmcnt(7)\n\t" \
    "v_smfmac_f32_16x16x64_bf16 v[236:239], v[240:243], v[160:167], %[ix]\n\t" "ds_read_b128 v[160:163], %[l0] offset:8192\n\t" \
    "ds_read_b128 v[164:167], %[l0] offset:9216\n\t" "s_waitcnt vmcnt(2)\n\t" "v_smfmac_f32_16x16x64_bf16 v[224:227], v[244:247], a[16:23], %[ix]\n\t" \
    "ds_write_b128 %[l1], v[244:247] offset:5120\n\t" "v_max3_u32 v217, v217, v243, v244\n\t" "v_smfmac_f32_16x16x64_bf16 v[228:231], v[244:247], a[24:31], %[ix]\n\t" \
    "v_max3_u32 v217, v217, v245, v246\n\t" "v_max_u32 v217, v217, v247\n\t" "v_smfmac_f32_16x16x64_bf16 v[232:235], v[244:247], v[40:47], %[ix]\n\t" \
    "v_cmp_eq_u32 vcc, -1, v217\n\t" "s_cmp_lg_u32 s60, 0\n\t" "s_waitcnt lgkmcnt(8)\n\t" "v_smfmac_f32_16x16x64_bf16 v[236:239], v[244:247], v[168:175], %[ix]\n\t" \
    "s_cbranch_vccnz 21f\n\t" "22:\n\t" "ds_read_b128 v[168:171], %[l0] offset:10240\n\t" "s_waitcnt vmcnt(1)\n\t" \
    "v_smfmac_f32_16x16x64_bf16 v[224:227], v[248:251], a[32:39], %[ix]\n\t" "ds_write_b128 %[l1], v[248:251] offset:6144\n\t" "s_waitcnt lgkmcnt(2)\n\t" \
    "v_smfmac_f32_16x16x64_bf16 v[228:231], v[248:251], a[40:47], %[ix]\n\t" "s_cbranch_scc1 23f\n\t" "s_barrier\n\t" "23:\n\t" XT(86, 87) \
    "v_smfmac_f32_16x16x64_bf16 v[232:235], v[248:251], v[48:55], %[ix]\n\t" "ds_read_b128 v[192:195], %[l1] offset:8192\n\t" \
    "ds_read_b128 v[196:199], %[l1] offset:12288\n\t" "v_smfmac_f32_16x16x64_bf16 v[236:239], v[248:251], v[176:183], %[ix]\n\t" \
    "ds_read_b128 v[172:175], %[l0] offset:11264\n\t" "ds_read_b128 v[176:179], %[l0] offset:12288\n\t" "s_waitcnt vmcnt(0)\n\t" \
    "v_smfmac_f32_16x16x64_bf16 v[224:227], v[252:255], a[48:55], %[ix]\n\t" "ds_write_b128 %[l1], v[252:255] offset:7168\n\t" "v_max3_u32 v217, v248, v249, v250\n\t" \
    "v_smfmac_f32_16x16x64_bf16 v[228:231], v[252:255], a[56:63], %[ix]\n\t" "ds_read_b128 v[180:183], %[l0] offset:13312\n\t" "v_max3_u32 v217, v217, v251, v252\n\t" \
    "v_max3_u32 v217, v217, v253, v254\n\t" "v_smfmac_f32_16x16x64_bf16 v[232:235], v[252:255], v[56:63], %[ix]\n\t" "v_max_u32 v217, v217, v255\n\t" \
    "v_cmp_eq_u32 vcc, -1, v217\n\t" "v_smfmac_f32_16x16x64_bf16 v[236:239], v[252:255], v[184:191], %[ix]\n\t" "s_cbranch_vccnz 24f\n\t" "25:\n\t" \
    "ds_read_b128 v[240:243], %[l1] offset:0\n\t" "s_waitcnt lgkmcnt(6)\n\t" "v_smfmac_f32_16x16x64_bf16 v[224:227], v[192:195], a[64:71], %[ix]\n\t" \
    "ds_read_b128 v[184:187], %[l0] offset:14336\n\t" "ds_read_b128 v[188:191], %[l0] offset:15360\n\t" \
    "v_smfmac_f32_16x16x64_bf16 v[228:231], v[192:195], a[72:79], %[ix]\n\t" "ds_read_b128 v[244:247], %[l1] offset:9216\n\t" "s_waitcnt lgkmcnt(5)\n\t" \
    "v_smfmac_f32_16x16x64_bf16 v[232:235], v[192:195], v[64:71], %[ix]\n\t" "s_barrier\n\t" XT(88, 89) \
    "v_smfmac_f32_16x16x64_bf16 v[236:239], v[192:195], v[160:167], %[ix]\n\t" "ds_read_b128 v[160:163], %[l0] offset:16384\n\t" \
    "ds_read_b128 v[164:167], %[l0] offset:17408\n\t" "v_smfmac_f32_16x16x64_bf16 v[224:227], v[196:199], a[80:87], %[ix]\n\t" \
    "ds_read_b128 v[248:251], %[l1] offset:13312\n\t" "ds_read_b128 v[252:255], %[l1] offset:1024\n\t" \
    "v_smfmac_f32_16x16x64_bf16 v[228:231], v[196:199], a[88:95], %[ix]\n\t" "ds_read_b128 v[192:195], %[l1] offset:10240\n\t" \
    "v_smfmac_f32_16x16x64_bf16 v[232:235], v[196:199], v[72:79], %[ix]\n\t" "buffer_load_dwordx4 v[16:19], %[hq], %[rs], %[qb] offen offset:0 sc1\n\t" \
    "v_smfmac_f32_16x16x64_bf16 v[236:239], v[196:199], v[168:175], %[ix]\n\t" "ds_read_b128 v[168:171], %[l0] offset:18432\n\t" \
    "ds_read_b128 v[172:175], %[l0] offset:19456\n\t" "s_waitcnt lgkmcnt(10)\n\t" "v_smfmac_f32_16x16x64_bf16 v[224:227], v[240:243], a[96:103], %[ix]\n\t" \
    "ds_read_b128 v[196:199], %[l1] offset:14336\n\t" "v_smfmac_f32_16x16x64_bf16 v[228:231], v[240:243], a[104:111], %[ix]\n\t" \
    "v_smfmac_f32_16x16x64_bf16 v[232:235], v[240:243], v[80:87], %[ix]\n\t" "buffer_load_dwordx4 v[20:23], %[hq], %[rs], %[qb] offen offset:2048 sc1\n\t" \
    "v_smfmac_f32_16x16x64_bf16 v[236:239], v[240:243], v[176:183], %[ix]\n\t" "ds_read_b128 v[176:179], %[l0] offset:20480\n\t" \
    "ds_read_b128 v[180:183], %[l0] offset:21504\n\t" "s_waitcnt lgkmcnt(10)\n\t" "v_smfmac_f32_16x16x64_bf16 v[224:227], v[244:247], a[112:119], %[ix]\n\t" \
    "ds_read_b128 v[240:243], %[l1] offset:2048\n\t" "v_smfmac_f32_16x16x64_bf16 v[228:231], v[244:247], a[120:127], %[ix]\n\t" \
    "v_smfmac_f32_16x16x64_bf16 v[232:235], v[244:247], v[88:95], %[ix]\n\t" "buffer_load_dwordx4 v[24:27], %[hq], %[rs], s61 offen offset:0 sc1\n\t" \
    "v_smfmac_f32_16x16x64_bf16 v[236:239], v[244:247], v[184:191], %[ix]\n\t" "ds_read_b128 v[184:187], %[l0] offset:22528\n\t" \
    "ds_read_b128 v[188:191], %[l0] offset:23552\n\t" "s_waitcnt lgkmcnt(10)\n\t" "v_smfmac_f32_16x16x64_bf16 v[224:227], v[248:251], a[128:135], %[ix]\n\t" \
    "ds_read_b128 v[244:247], %[l1] offset:11264\n\t" "v_smfmac_f32_16x16x64_bf16 v[228:231], v[248:251], a[136:143], %[ix]\n\t" \
    "v_smfmac_f32_16x16x64_bf16 v[232:235], v[248:251], v[96:103], %[ix]\n\t" "buffer_load_dwordx4 v[28:31], %[hq], %[rs], s61 offen offset:2048 sc1\n\t" \
    "v_smfmac_f32_16x16x64_bf16 v[236:239], v[248:251], v[160:167], %[ix]\n\t" "ds_read_b128 v[160:163], %[l0] offset:24576\n\t" \
    "ds_read_b128 v[164:167], %[l0] offset:25600\n\t" "s_waitcnt lgkmcnt(12)\n\t" "v_smfmac_f32_16x16x64_bf16 v[224:227], v[252:255], a[144:151], %[ix]\n\t" \
    "ds_read_b128 v[248:251], %[l1] offset:15360\n\t" "v_smfmac_f32_16x16x64_bf16 v[228:231], v[252:255], a[152:159], %[ix]\n\t" \
    "v_smfmac_f32_16x16x64_bf16 v[232:235], v[252:255], v[104:111], %[ix]\n\t" "s_waitcnt lgkmcnt(10)\n\t" \
    "v_smfmac_f32_16x16x64_bf16 v[236:239], v[252:255], v[168:175], %[ix]\n\t" "ds_read_b128 v[168:171], %[l0] offset:26624\n\t" \
    "ds_read_b128 v[172:175], %[l0] offset:27648\n\t" "v_smfmac_f32_16x16x64_bf16 v[224:227], v[192:195], a[160:167], %[ix]\n\t" \
    "ds_read_b128 v[252:255], %[l1] offset:3072\n\t" "v_smfmac_f32_16x16x64_bf16 v[228:231], v[192:195], a[168:175], %[ix]\n\t" "s_waitcnt lgkmcnt(12)\n\t" \
    "v_smfmac_f32_16x16x64_bf16 v[224:227], v[196:199], a[176:183], %[ix]\n\t" "v_smfmac_f32_16x16x64_bf16 v[228:231], v[196:199], a[184:191], %[ix]\n\t" \
    "s_waitcnt lgkmcnt(9)\n\t" "v_smfmac_f32_16x16x64_bf16 v[224:227], v[240:243], a[192:199], %[ix]\n\t" \
    "v_smfmac_f32_16x16x64_bf16 v[228:231], v[240:243], a[200:207], %[ix]\n\t" "s_waitcnt lgkmcnt(6)\n\t" \
    "v_smfmac_f32_16x16x64_bf16 v[224:227], v[244:247], a[208:215], %[ix]\n\t" "v_smfmac_f32_16x16x64_bf16 v[228:231], v[244:247], a[216:223], %[ix]\n\t" \
    "s_waitcnt lgkmcnt(3)\n\t" "v_smfmac_f32_16x16x64_bf16 v[224:227], v[248:251], a[224:231], %[ix]\n\t" \
    "v_smfmac_f32_16x16x64_bf16 v[228:231], v[248:251], a[232:239], %[ix]\n\t" "s_waitcnt lgkmcnt(0)\n\t" \
    "v_smfmac_f32_16x16x64_bf16 v[224:227], v[252:255], a[240:247], %[ix]\n\t" "s_nop 1\n\t" "v_smfmac_f32_16x16x64_bf16 v[228:231], v[252:255], a[248:255], %[ix]\n\t" \
    XT(90, 91) "s_nop 1\n\t" "v_smfmac_f32_16x16x64_bf16 v[232:235], v[192:195], v[112:119], %[ix]\n\t" "s_nop 1\n\t" \
    "v_smfmac_f32_16x16x64_bf16 v[236:239], v[192:195], v[176:183], %[ix]\n\t" "ds_read_b128 v[176:179], %[l0] offset:28672\n\t" "v_add_f32 v224, v224, v225\n\t" \
    "v_add_f32 v226, v226, v227\n\t" "v_smfmac_f32_16x16x64_bf16 v[232:235], v[196:199], v[120:127], %[ix]\n\t" "ds_read_b128 v[180:183], %[l0] offset:29696\n\t" \
    "v_add_f32 v228, v228, v229\n\t" "v_add_f32 v230, v230, v231\n\t" "v_smfmac_f32_16x16x64_bf16 v[236:239], v[196:199], v[184:191], %[ix]\n\t" \
    "ds_read_b128 v[184:187], %[l0] offset:30720\n\t" "v_cvt_pk_bf16_f32 v218, v224, v228\n\t" "v_cvt_pk_bf16_f32 v219, v226, v230\n\t" \
    "v_smfmac_f32_16x16x64_bf16 v[232:235], v[240:243], v[128:135], %[ix]\n\t" "ds_write2_b32 %[qw], v218, v219 offset0:0 offset1:8\n\t" \
    "ds_read_b128 v[188:191], %[l0] offset:31744\n\t" "v_smfmac_f32_16x16x64_bf16 v[236:239], v[240:243], v[160:167], %[ix]\n\t" \
    "v_smfmac_f32_16x16x64_bf16 v[232:235], v[244:247], v[136:143], %[ix]\n\t" "v_smfmac_f32_16x16x64_bf16 v[236:239], v[244:247], v[168:175], %[ix]\n\t" \
    "s_waitcnt lgkmcnt(1)\n\t" "s_barrier\n\t" "v_smfmac_f32_16x16x64_bf16 v[232:235], v[248:251], v[144:151], %[ix]\n\t" \
    "v_smfmac_f32_16x16x64_bf16 v[236:239], v[248:251], v[176:183], %[ix]\n\t" "v_smfmac_f32_16x16x64_bf16 v[232:235], v[252:255], v[152:159], %[ix]\n\t" \
    "s_waitcnt lgkmcnt(0)\n\t" "v_smfmac_f32_16x16x64_bf16 v[236:239], v[252:255], v[184:191], %[ix]\n\t" XT(92, 93) "s_nop 6\n\t" "v_add_f32 v232, v232, v233\n\t" \
    "v_add_f32 v234, v234, v235\n\t" "v_add_f32 v236, v236, v237\n\t" "v_add_f32 v238, v238, v239\n\t" "v_cvt_pk_bf16_f32 v232, v232, v236\n\t" \
    "v_cvt_pk_bf16_f32 v233, v234, v238\n\t" "ds_write2_b32 %[kw], v232, v233 offset0:1 offset1:69\n\t" "s_waitcnt lgkmcnt(0)\n\t" "s_barrier\n\t" XT(94, 95) \
    "s_cmp_ge_u32 %[sq], %[sn]\n\t" "s_cbranch_scc1 277f\n\t" "275:\n\t" "global_load_dword v240, v210, %[fq] sc1\n\t" "s_waitcnt vmcnt(0)\n\t" \
    "v_readfirstlane_b32 %[sq], v240\n\t" "s_nop 3\n\t" "s_cmp_ge_u32 %[sq], %[sn]\n\t" "s_cbranch_scc1 276f\n\t" "s_cmp_eq_u32 %[bd], 0\n\t" "s_cbranch_scc1 276f\n\t" \
    "s_sub_u32 %[bd], %[bd], 1\n\t" "s_sleep 2\n\t" "s_branch 275b\n\t" "276:\n\t" "buffer_load_dwordx4 v[16:19], %[hq], %[rs], %[qb] offen offset:0 sc1\n\t" \
    "buffer_load_dwordx4 v[20:23], %[hq], %[rs], %[qb] offen offset:2048 sc1\n\t" "buffer_load_dwordx4 v[24:27], %[hq], %[rs], s61 offen offset:0 sc1\n\t" \
    "buffer_load_dwordx4 v[28:31], %[hq], %[rs], s61 offen offset:2048 sc1\n\t" "277:\n\t" "s_nop 3\n\t" "ds_read_b128 v[160:163], %[kx] offset:0\n\t" \
    "ds_read_b128 v[164:167], %[kx] offset:2048\n\t" "ds_read_b128 v[168:171], %[kx] offset:4096\n\t" "ds_read_b128 v[172:175], %[kx] offset:6144\n\t" \
    "ds_read_b128 v[224:227], %[km] offset:0\n\t" "ds_read_b128 v[228:231], %[km] offset:64\n\t" "ds_read_b128 v[232:235], %[km] offset:128\n\t" \
    "ds_read_b128 v[236:239], %[km] offset:192\n\t" "s_waitcnt vmcnt(0)\n\t" "global_load_dword v240, v210, %[fq] sc1\n\t" "s_waitcnt lgkmcnt(7)\n\t" \
    "v_mfma_f32_16x16x32_bf16 v[248:251], v[160:163], v[16:19], v[200:203]\n\t" "s_waitcnt lgkmcnt(6)\n\t" \
    "v_mfma_f32_16x16x32_bf16 v[248:251], v[164:167], v[20:23], v[248:251]\n\t" "s_waitcnt lgkmcnt(5)\n\t" \
    "v_mfma_f32_16x16x32_bf16 v[248:251], v[168:171], v[24:27], v[248:251]\n\t" "s_waitcnt lgkmcnt(4)\n\t" \
    "v_mfma_f32_16x16x32_bf16 v[248:251], v[172:175], v[28:31], v[248:251]\n\t" "s_waitcnt lgkmcnt(3)\n\t" \
    "v_mfma_f32_16x16x32_bf16 v[244:247], v[224:227], v[16:19], v[204:207]\n\t" "s_waitcnt lgkmcnt(2)\n\t" \
    "v_mfma_f32_16x16x32_bf16 v[244:247], v[228:231], v[20:23], v[244:247]\n\t" "s_waitcnt lgkmcnt(1)\n\t" \
    "v_mfma_f32_16x16x32_bf16 v[244:247], v[232:235], v[24:27], v[244:247]\n\t" "s_waitcnt lgkmcnt(0)\n\t" \
    "v_mfma_f32_16x16x32_bf16 v[244:247], v[236:239], v[28:31], v[244:247]\n\t" "v_mov_b32 v192, -1\n\t" "v_mov_b32 v193, -1\n\t" "v_mov_b32 v194, -1\n\t" \
    "v_mov_b32 v195, -1\n\t" "v_max3_f32 v242, v248, v249, v250\n\t" "v_max_f32 v242, v242, v251\n\t" "s_nop 1\n\t" "v_max3_f32 v242, v242, v244, v245\n\t" \
    "v_max3_f32 v242, v242, v246, v247\n\t" "v_mov_b32 v243, v242\n\t" "s_nop 1\n\t" "v_permlane32_swap_b32 v242, v243\n\t" "v_max_f32 v242, v242, v243\n\t" \
    "v_mov_b32 v243, v242\n\t" "s_nop 1\n\t" "v_permlane16_swap_b32 v242, v243\n\t" "v_max_f32 v242, v242, v243\n\t" \
    "v_pk_add_f32 v[244:245], v[244:245], v[242:243] op_sel_hi:[1,0] neg_lo:[0,1] neg_hi:[0,1]\n\t" \
    "v_pk_add_f32 v[246:247], v[246:247], v[242:243] op_sel_hi:[1,0] neg_lo:[0,1] neg_hi:[0,1]\n\t" \
    "v_pk_add_f32 v[248:249], v[248:249], v[242:243] op_sel_hi:[1,0] neg_lo:[0,1] neg_hi:[0,1]\n\t" \
    "v_pk_add_f32 v[250:251], v[250:251], v[242:243] op_sel_hi:[1,0] neg_lo:[0,1] neg_hi:[0,1]\n\t" "v_pk_mul_f32 v[244:245], v[244:245], s[66:67]\n\t" \
    "v_pk_mul_f32 v[246:247], v[246:247], s[66:67]\n\t" "v_pk_mul_f32 v[248:249], v[248:249], s[66:67]\n\t" "v_pk_mul_f32 v[250:251], v[250:251], s[66:67]\n\t" \
    "v_exp_f32 v244, v244\n\t" "v_exp_f32 v245, v245\n\t" "v_exp_f32 v246, v246\n\t" "v_exp_f32 v247, v247\n\t" "v_exp_f32 v248, v248\n\t" "v_exp_f32 v249, v249\n\t" \
    "v_exp_f32 v250, v250\n\t" "v_exp_f32 v251, v251\n\t" "v_pk_add_f32 v[242:243], v[244:245], v[246:247]\n\t" "v_pk_add_f32 v[242:243], v[242:243], v[248:249]\n\t" \
    "v_pk_add_f32 v[242:243], v[242:243], v[250:251]\n\t" "v_add_f32 v242, v242, v243\n\t" "v_mov_b32 v243, v242\n\t" "s_nop 1\n\t" "v_permlane32_swap_b32 v242, v243\n\t" \
    "v_add_f32 v242, v242, v243\n\t" "v_mov_b32 v243, v242\n\t" "s_nop 1\n\t" "v_permlane16_swap_b32 v242, v243\n\t" "v_add_f32 v242, v242, v243\n\t" \
    "v_rcp_f32 v242, v242\n\t" "s_nop 0\n\t" "v_pk_mul_f32 v[244:245], v[244:245], v[242:243] op_sel_hi:[1,0]\n\t" \
    "v_pk_mul_f32 v[246:247], v[246:247], v[242:243] op_sel_hi:[1,0]\n\t" "v_pk_mul_f32 v[248:249], v[248:249], v[242:243] op_sel_hi:[1,0]\n\t" \
    "v_pk_mul_f32 v[250:251], v[250:251], v[242:243] op_sel_hi:[1,0]\n\t" "v_cvt_pk_bf16_f32 v162, v244, v245\n\t" "v_cvt_pk_bf16_f32 v163, v246, v247\n\t" \
    "v_cvt_pk_bf16_f32 v164, v248, v249\n\t" "v_cvt_pk_bf16_f32 v165, v250, v251\n\t" "buffer_store_dwordx2 v[164:165], %[px], %[rs], %[tb] offen sc1\n\t" \
    "buffer_store_dwordx2 v[162:163], %[pm], %[rs], %[tb] offen sc1\n\t" "buffer_store_dwordx2 v[192:193], %[px], %[rs], %[tr] offen sc1\n\t" \
    "buffer_store_dwordx2 v[192:193], %[pm], %[rs], %[tr] offen sc1\n\t" "s_waitcnt vmcnt(4)\n\t" "v_readfirstlane_b32 s62, v240\n\t" "s_nop 3\n\t" \
    "s_max_u32 %[sq], %[sq], s62\n\t" XT(96, 97) "s_branch 99f\n\t" "21:\n\t" "s_cmp_eq_u32 %[bd], 0\n\t" "s_cbranch_scc1 26f\n\t" "s_sub_u32 %[bd], %[bd], 1\n\t" XCNT(100) \
    "s_waitcnt lgkmcnt(0)\n\t" "s_nop 7\n\t" "s_branch 20b\n\t" "26:\n\t" "s_cmp_lg_u32 s60, 0\n\t" "s_branch 22b\n\t" "24:\n\t" "s_cmp_eq_u32 %[bd], 0\n\t" \
    "s_cbranch_scc1 25b\n\t" "s_sub_u32 %[bd], %[bd], 1\n\t" XCNT(101) "s_mov_b32 s60, 1\n\t" "s_waitcnt lgkmcnt(0)\n\t" "s_nop 7\n\t" "s_branch 20b\n\t" "99:\n\t"
#define QK_STEP2 \
    "v_xor_b32 v216, 0x400, %[mo]\n\t" "s_mov_b32 s60, 0\n\t" "s_add_u32 s61, %[qb], 0x1000\n\t" "s_mov_b32 s66, 0x3e0293ee\n\t" "s_mov_b32 s67, 0x3e0293ee\n\t" XT(80, 81) \
    XT(98, 99) "s_barrier\n\t" XT(82, 83) "30:\n\t" "buffer_load_dwordx4 v[240:243], %[mo], %[rs], %[sb] offen offset:0 sc1\n\t" "v_mov_b64 v[224:225], 0\n\t" \
    "v_mov_b64 v[226:227], 0\n\t" "v_mov_b64 v[228:229], 0\n\t" "v_mov_b64 v[230:231], 0\n\t" "v_mov_b64 v[232:233], 0\n\t" "v_mov_b64 v[234:235], 0\n\t" \
    "v_mov_b64 v[236:237], 0\n\t" "v_mov_b64 v[238:239], 0\n\t" "buffer_load_dwordx4 v[244:247], v216, %[rs], %[sb] offen offset:0 sc1\n\t" \
    "ds_read_b128 v[160:163], %[l0] offset:0\n\t" "ds_read_b128 v[164:167], %[l0] offset:1024\n\t" "ds_read_b128 v[168:171], %[l0] offset:2048\n\t" \
    "ds_read_b128 v[172:175], %[l0] offset:3072\n\t" "s_nop 3\n\t" "buffer_load_dwordx4 v[248:251], %[mo], %[rs], %[sb] offen offset:2048 sc1\n\t" \
    "ds_read_b128 v[176:179], %[l0] offset:4096\n\t" "ds_read_b128 v[180:183], %[l0] offset:5120\n\t" "ds_read_b128 v[184:187], %[l0] offset:6144\n\t" \
    "ds_read_b128 v[188:191], %[l0] offset:7168\n\t" "s_nop 3\n\t" "buffer_load_dwordx4 v[252:255], v216, %[rs], %[sb] offen offset:2048 sc1\n\t" \
    "v_mov_b32 v208, 0xd028965f\n\t" "v_mov_b32 v209, 0xff800000\n\t" "s_mov_b32 s62, 0\n\t" "s_mov_b32 s63, -1\n\t" "v_mov_b32 v210, 0\n\t" "v_cmp_gt_i32 vcc, 0, %[ps]\n\t" \
    "v_cndmask_b32 v204, 0, v209, s[62:63]\n\t" "s_nop 0\n\t" "v_cndmask_b32 v200, 0, v208, vcc\n\t" "v_cmp_gt_i32 vcc, 1, %[ps]\n\t" \
    "v_cndmask_b32 v205, 0, v209, s[62:63]\n\t" "s_nop 0\n\t" "v_cndmask_b32 v201, 0, v208, vcc\n\t" "v_cmp_gt_i32 vcc, 2, %[ps]\n\t" \
    "v_cndmask_b32 v206, 0, v209, s[62:63]\n\t" "s_nop 0\n\t" "v_cndmask_b32 v202, 0, v208, vcc\n\t" "v_cmp_gt_i32 vcc, 3, %[ps]\n\t" \
    "v_cndmask_b32 v207, 0, v209, s[62:63]\n\t" "s_nop 0\n\t" "v_cndmask_b32 v203, 0, v208, vcc\n\t" "s_waitcnt vmcnt(3)\n\t" XT(84, 85) \
    "v_smfmac_f32_16x16x64_bf16 v[232:235], v[240:243], a[0:7], %[ix]\n\t" "ds_write_b128 %[l1], v[240:243] offset:8192\n\t" \
    "v_smfmac_f32_16x16x64_bf16 v[236:239], v[240:243], a[8:15], %[ix]\n\t" "v_max3_u32 v217, v240, v241, v242\n\t" \
    "v_smfmac_f32_16x16x64_bf16 v[224:227], v[240:243], v[32:39], %[ix]\n\t" "s_waitcnt lgkmcnt(7)\n\t" \
    "v_smfmac_f32_16x16x64_bf16 v[228:231], v[240:243], v[160:167], %[ix]\n\t" "ds_read_b128 v[160:163], %[l0] offset:8192\n\t" \
    "ds_read_b128 v[164:167], %[l0] offset:9216\n\t" "s_waitcnt vmcnt(2)\n\t" "v_smfmac_f32_16x16x64_bf16 v[232:235], v[244:247], a[16:23], %[ix]\n\t" \
    "ds_write_b128 %[l1], v[244:247] offset:9216\n\t" "v_max3_u32 v217, v217, v243, v244\n\t" "v_smfmac_f32_16x16x64_bf16 v[236:239], v[244:247], a[24:31], %[ix]\n\t" \
    "v_max3_u32 v217, v217, v245, v246\n\t" "v_max_u32 v217, v217, v247\n\t" "v_smfmac_f32_16x16x64_bf16 v[224:227], v[244:247], v[40:47], %[ix]\n\t" \
    "v_cmp_eq_u32 vcc, -1, v217\n\t" "s_cmp_lg_u32 s60, 0\n\t" "s_waitcnt lgkmcnt(8)\n\t" "v_smfmac_f32_16x16x64_bf16 v[228:231], v[244:247], v[168:175], %[ix]\n\t" \
    "s_cbranch_vccnz 31f\n\t" "32:\n\t" "ds_read_b128 v[168:171], %[l0] offset:10240\n\t" "s_waitcnt vmcnt(1)\n\t" \
    "v_smfmac_f32_16x16x64_bf16 v[232:235], v[248:251], a[32:39], %[ix]\n\t" "ds_write_b128 %[l1], v[248:251] offset:10240\n\t" "s_waitcnt lgkmcnt(2)\n\t" \
    "v_smfmac_f32_16x16x64_bf16 v[236:239], v[248:251], a[40:47], %[ix]\n\t" "s_cbranch_scc1 33f\n\t" "s_barrier\n\t" "33:\n\t" XT(86, 87) \
    "v_smfmac_f32_16x16x64_bf16 v[224:227], v[248:251], v[48:55], %[ix]\n\t" "ds_read_b128 v[192:195], %[l1] offset:12288\n\t" "ds_read_b128 v[196:199], %[l1] offset:0\n\t" \
    "v_smfmac_f32_16x16x64_bf16 v[228:231], v[248:251], v[176:183], %[ix]\n\t" "ds_read_b128 v[172:175], %[l0] offset:11264\n\t" \
    "ds_read_b128 v[176:179], %[l0] offset:12288\n\t" "s_waitcnt vmcnt(0)\n\t" "v_smfmac_f32_16x16x64_bf16 v[232:235], v[252:255], a[48:55], %[ix]\n\t" \
    "ds_write_b128 %[l1], v[252:255] offset:11264\n\t" "v_max3_u32 v217, v248, v249, v250\n\t" "v_smfmac_f32_16x16x64_bf16 v[236:239], v[252:255], a[56:63], %[ix]\n\t" \
    "ds_read_b128 v[180:183], %[l0] offset:13312\n\t" "v_max3_u32 v217, v217, v251, v252\n\t" "v_max3_u32 v217, v217, v253, v254\n\t" \
    "v_smfmac_f32_16x16x64_bf16 v[224:227], v[252:255], v[56:63], %[ix]\n\t" "v_max_u32 v217, v217, v255\n\t" "v_cmp_eq_u32 vcc, -1, v217\n\t" \
    "v_smfmac_f32_16x16x64_bf16 v[228:231], v[252:255], v[184:191], %[ix]\n\t" "s_cbranch_vccnz 34f\n\t" "35:\n\t" "ds_read_b128 v[240:243], %[l1] offset:4096\n\t" \
    "s_waitcnt lgkmcnt(6)\n\t" "v_smfmac_f32_16x16x64_bf16 v[232:235], v[192:195], a[64:71], %[ix]\n\t" "ds_read_b128 v[184:187], %[l0] offset:14336\n\t" \
    "ds_read_b128 v[188:191], %[l0] offset:15360\n\t" "v_smfmac_f32_16x16x64_bf16 v[236:239], v[192:195], a[72:79], %[ix]\n\t" \
    "ds_read_b128 v[244:247], %[l1] offset:13312\n\t" "s_waitcnt lgkmcnt(5)\n\t" "v_smfmac_f32_16x16x64_bf16 v[224:227], v[192:195], v[64:71], %[ix]\n\t" "s_barrier\n\t" \
    XT(88, 89) "v_smfmac_f32_16x16x64_bf16 v[228:231], v[192:195], v[160:167], %[ix]\n\t" "ds_read_b128 v[160:163], %[l0] offset:16384\n\t" \
    "ds_read_b128 v[164:167], %[l0] offset:17408\n\t" "v_smfmac_f32_16x16x64_bf16 v[232:235], v[196:199], a[80:87], %[ix]\n\t" \
    "ds_read_b128 v[248:251], %[l1] offset:1024\n\t" "ds_read_b128 v[252:255], %[l1] offset:5120\n\t" "v_smfmac_f32_16x16x64_bf16 v[236:239], v[196:199], a[88:95], %[ix]\n\t" \
    "ds_read_b128 v[192:195], %[l1] offset:14336\n\t" "v_smfmac_f32_16x16x64_bf16 v[224:227], v[196:199], v[72:79], %[ix]\n\t" \
    "buffer_load_dwordx4 v[16:19], %[hq], %[rs], %[qb] offen offset:0 sc1\n\t" "v_smfmac_f32_16x16x64_bf16 v[228:231], v[196:199], v[168:175], %[ix]\n\t" \
    "ds_read_b128 v[168:171], %[l0] offset:18432\n\t" "ds_read_b128 v[172:175], %[l0] offset:19456\n\t" "s_waitcnt lgkmcnt(10)\n\t" \
    "v_smfmac_f32_16x16x64_bf16 v[232:235], v[240:243], a[96:103], %[ix]\n\t" "ds_read_b128 v[196:199], %[l1] offset:2048\n\t" \
    "v_smfmac_f32_16x16x64_bf16 v[236:239], v[240:243], a[104:111], %[ix]\n\t" "v_smfmac_f32_16x16x64_bf16 v[224:227], v[240:243], v[80:87], %[ix]\n\t" \
    "buffer_load_dwordx4 v[20:23], %[hq], %[rs], %[qb] offen offset:2048 sc1\n\t" "v_smfmac_f32_16x16x64_bf16 v[228:231], v[240:243], v[176:183], %[ix]\n\t" \
    "ds_read_b128 v[176:179], %[l0] offset:20480\n\t" "ds_read_b128 v[180:183], %[l0] offset:21504\n\t" "s_waitcnt lgkmcnt(10)\n\t" \
    "v_smfmac_f32_16x16x64_bf16 v[232:235], v[244:247], a[112:119], %[ix]\n\t" "ds_read_b128 v[240:243], %[l1] offset:6144\n\t" \
    "v_smfmac_f32_16x16x64_bf16 v[236:239], v[244:247], a[120:127], %[ix]\n\t" "v_smfmac_f32_16x16x64_bf16 v[224:227], v[244:247], v[88:95], %[ix]\n\t" \
    "buffer_load_dwordx4 v[24:27], %[hq], %[rs], s61 offen offset:0 sc1\n\t" "v_smfmac_f32_16x16x64_bf16 v[228:231], v[244:247], v[184:191], %[ix]\n\t" \
    "ds_read_b128 v[184:187], %[l0] offset:22528\n\t" "ds_read_b128 v[188:191], %[l0] offset:23552\n\t" "s_waitcnt lgkmcnt(10)\n\t" \
    "v_smfmac_f32_16x16x64_bf16 v[232:235], v[248:251], a[128:135], %[ix]\n\t" "ds_read_b128 v[244:247], %[l1] offset:15360\n\t" \
    "v_smfmac_f32_16x16x64_bf16 v[236:239], v[248:251], a[136:143], %[ix]\n\t" "v_smfmac_f32_16x16x64_bf16 v[224:227], v[248:251], v[96:103], %[ix]\n\t" \
    "buffer_load_dwordx4 v[28:31], %[hq], %[rs], s61 offen offset:2048 sc1\n\t" "v_smfmac_f32_16x16x64_bf16 v[228:231], v[248:251], v[160:167], %[ix]\n\t" \
    "ds_read_b128 v[160:163], %[l0] offset:24576\n\t" "ds_read_b128 v[164:167], %[l0] offset:25600\n\t" "s_waitcnt lgkmcnt(12)\n\t" \
    "v_smfmac_f32_16x16x64_bf16 v[232:235], v[252:255], a[144:151], %[ix]\n\t" "ds_read_b128 v[248:251], %[l1] offset:3072\n\t" \
    "v_smfmac_f32_16x16x64_bf16 v[236:239], v[252:255], a[152:159], %[ix]\n\t" "v_smfmac_f32_16x16x64_bf16 v[224:227], v[252:255], v[104:111], %[ix]\n\t" \
    "s_waitcnt lgkmcnt(10)\n\t" "v_smfmac_f32_16x16x64_bf16 v[228:231], v[252:255], v[168:175], %[ix]\n\t" "ds_read_b128 v[168:171], %[l0] offset:26624\n\t" \
    "ds_read_b128 v[172:175], %[l0] offset:27648\n\t" "v_smfmac_f32_16x16x64_bf16 v[224:227], v[192:195], v[112:119], %[ix]\n\t" \
    "ds_read_b128 v[252:255], %[l1] offset:7168\n\t" "s_waitcnt lgkmcnt(10)\n\t" "v_smfmac_f32_16x16x64_bf16 v[228:231], v[192:195], v[176:183], %[ix]\n\t" \
    "ds_read_b128 v[176:179], %[l0] offset:28672\n\t" "ds_read_b128 v[180:183], %[l0] offset:29696\n\t" \
    "v_smfmac_f32_16x16x64_bf16 v[224:227], v[196:199], v[120:127], %[ix]\n\t" "s_waitcnt lgkmcnt(9)\n\t" \
    "v_smfmac_f32_16x16x64_bf16 v[228:231], v[196:199], v[184:191], %[ix]\n\t" "ds_read_b128 v[184:187], %[l0] offset:30720\n\t" \
    "ds_read_b128 v[188:191], %[l0] offset:31744\n\t" "v_smfmac_f32_16x16x64_bf16 v[224:227], v[240:243], v[128:135], %[ix]\n\t" "s_waitcnt lgkmcnt(8)\n\t" \
    "v_smfmac_f32_16x16x64_bf16 v[228:231], v[240:243], v[160:167], %[ix]\n\t" "v_smfmac_f32_16x16x64_bf16 v[224:227], v[244:247], v[136:143], %[ix]\n\t" \
    "s_waitcnt lgkmcnt(5)\n\t" "v_smfmac_f32_16x16x64_bf16 v[228:231], v[244:247], v[168:175], %[ix]\n\t" \
    "v_smfmac_f32_16x16x64_bf16 v[224:227], v[248:251], v[144:151], %[ix]\n\t" "s_waitcnt lgkmcnt(2)\n\t" \
    "v_smfmac_f32_16x16x64_bf16 v[228:231], v[248:251], v[176:183], %[ix]\n\t" "v_smfmac_f32_16x16x64_bf16 v[224:227], v[252:255], v[152:159], %[ix]\n\t" "s_nop 1\n\t" \
    "s_waitcnt lgkmcnt(0)\n\t" "v_smfmac_f32_16x16x64_bf16 v[228:231], v[252:255], v[184:191], %[ix]\n\t" XT(90, 91) "s_nop 1\n\t" \
    "v_smfmac_f32_16x16x64_bf16 v[232:235], v[192:195], a[160:167], %[ix]\n\t" "s_nop 1\n\t" "v_smfmac_f32_16x16x64_bf16 v[236:239], v[192:195], a[168:175], %[ix]\n\t" \
    "v_add_f32 v224, v224, v225\n\t" "v_add_f32 v226, v226, v227\n\t" "v_smfmac_f32_16x16x64_bf16 v[232:235], v[196:199], a[176:183], %[ix]\n\t" \
    "v_add_f32 v228, v228, v229\n\t" "v_add_f32 v230, v230, v231\n\t" "v_smfmac_f32_16x16x64_bf16 v[236:239], v[196:199], a[184:191], %[ix]\n\t" \
    "v_cvt_pk_bf16_f32 v218, v224, v228\n\t" "v_cvt_pk_bf16_f32 v219, v226, v230\n\t" "v_smfmac_f32_16x16x64_bf16 v[232:235], v[240:243], a[192:199], %[ix]\n\t" \
    "ds_write2_b32 %[qw], v218, v219 offset0:4 offset1:12\n\t" "v_smfmac_f32_16x16x64_bf16 v[236:239], v[240:243], a[200:207], %[ix]\n\t" \
    "v_smfmac_f32_16x16x64_bf16 v[232:235], v[244:247], a[208:215], %[ix]\n\t" "v_smfmac_f32_16x16x64_bf16 v[236:239], v[244:247], a[216:223], %[ix]\n\t" \
    "s_waitcnt lgkmcnt(0)\n\t" "s_barrier\n\t" "v_smfmac_f32_16x16x64_bf16 v[232:235], v[248:251], a[224:231], %[ix]\n\t" \
    "v_smfmac_f32_16x16x64_bf16 v[236:239], v[248:251], a[232:239], %[ix]\n\t" "v_smfmac_f32_16x16x64_bf16 v[232:235], v[252:255], a[240:247], %[ix]\n\t" \
    "v_smfmac_f32_16x16x64_bf16 v[236:239], v[252:255], a[248:255], %[ix]\n\t" XT(92, 93) "s_nop 6\n\t" "v_add_f32 v232, v232, v233\n\t" "v_add_f32 v234, v234, v235\n\t" \
    "v_add_f32 v236, v236, v237\n\t" "v_add_f32 v238, v238, v239\n\t" "v_cvt_pk_bf16_f32 v232, v232, v236\n\t" "v_cvt_pk_bf16_f32 v233, v234, v238\n\t" \
    "ds_write2_b32 %[kw], v232, v233 offset0:0 offset1:68\n\t" "s_waitcnt lgkmcnt(0)\n\t" "s_barrier\n\t" XT(94, 95) "s_cmp_ge_u32 %[sq], %[sn]\n\t" "s_cbranch_scc1 377f\n\t" \
    "375:\n\t" "global_load_dword v240, v210, %[fq] sc1\n\t" "s_waitcnt vmcnt(0)\n\t" "v_readfirstlane_b32 %[sq], v240\n\t" "s_nop 3\n\t" "s_cmp_ge_u32 %[sq], %[sn]\n\t" \
    "s_cbranch_scc1 376f\n\t" "s_cmp_eq_u32 %[bd], 0\n\t" "s_cbranch_scc1 376f\n\t" "s_sub_u32 %[bd], %[bd], 1\n\t" "s_sleep 2\n\t" "s_branch 375b\n\t" "376:\n\t" \
    "buffer_load_dwordx4 v[16:19], %[hq], %[rs], %[qb] offen offset:0 sc1\n\t" "buffer_load_dwordx4 v[20:23], %[hq], %[rs], %[qb] offen offset:2048 sc1\n\t" \
    "buffer_load_dwordx4 v[24:27], %[hq], %[rs], s61 offen offset:0 sc1\n\t" "buffer_load_dwordx4 v[28:31], %[hq], %[rs], s61 offen offset:2048 sc1\n\t" "377:\n\t" \
    "s_nop 3\n\t" "ds_read_b128 v[160:163], %[kx] offset:0\n\t" "ds_read_b128 v[164:167], %[kx] offset:2048\n\t" "ds_read_b128 v[168:171], %[kx] offset:4096\n\t" \
    "ds_read_b128 v[172:175], %[kx] offset:6144\n\t" "ds_read_b128 v[176:179], %[kx] offset:256\n\t" "ds_read_b128 v[180:183], %[kx] offset:2304\n\t" \
    "ds_read_b128 v[184:187], %[kx] offset:4352\n\t" "ds_read_b128 v[188:191], %[kx] offset:6400\n\t" "ds_read_b128 v[224:227], %[km] offset:0\n\t" \
    "ds_read_b128 v[228:231], %[km] offset:64\n\t" "ds_read_b128 v[232:235], %[km] offset:128\n\t" "ds_read_b128 v[236:239], %[km] offset:192\n\t" "s_waitcnt vmcnt(0)\n\t" \
    "global_load_dword v240, v210, %[fq] sc1\n\t" "s_waitcnt lgkmcnt(11)\n\t" "v_mfma_f32_16x16x32_bf16 v[248:251], v[160:163], v[16:19], 0\n\t" "s_waitcnt lgkmcnt(10)\n\t" \
    "v_mfma_f32_16x16x32_bf16 v[248:251], v[164:167], v[20:23], v[248:251]\n\t" "s_waitcnt lgkmcnt(9)\n\t" \
    "v_mfma_f32_16x16x32_bf16 v[248:251], v[168:171], v[24:27], v[248:251]\n\t" "s_waitcnt lgkmcnt(8)\n\t" \
    "v_mfma_f32_16x16x32_bf16 v[248:251], v[172:175], v[28:31], v[248:251]\n\t" "s_waitcnt lgkmcnt(7)\n\t" \
    "v_mfma_f32_16x16x32_bf16 v[252:255], v[176:179], v[16:19], v[200:203]\n\t" "s_waitcnt lgkmcnt(6)\n\t" \
    "v_mfma_f32_16x16x32_bf16 v[252:255], v[180:183], v[20:23], v[252:255]\n\t" "s_waitcnt lgkmcnt(5)\n\t" \
    "v_mfma_f32_16x16x32_bf16 v[252:255], v[184:187], v[24:27], v[252:255]\n\t" "s_waitcnt lgkmcnt(4)\n\t" \
    "v_mfma_f32_16x16x32_bf16 v[252:255], v[188:191], v[28:31], v[252:255]\n\t" "s_waitcnt lgkmcnt(3)\n\t" \
    "v_mfma_f32_16x16x32_bf16 v[244:247], v[224:227], v[16:19], v[204:207]\n\t" "s_waitcnt lgkmcnt(2)\n\t" \
    "v_mfma_f32_16x16x32_bf16 v[244:247], v[228:231], v[20:23], v[244:247]\n\t" "s_waitcnt lgkmcnt(1)\n\t" \
    "v_mfma_f32_16x16x32_bf16 v[244:247], v[232:235], v[24:27], v[244:247]\n\t" "s_waitcnt lgkmcnt(0)\n\t" \
    "v_mfma_f32_16x16x32_bf16 v[244:247], v[236:239], v[28:31], v[244:247]\n\t" "v_mov_b32 v192, -1\n\t" "v_mov_b32 v193, -1\n\t" "v_mov_b32 v194, -1\n\t" \
    "v_mov_b32 v195, -1\n\t" "v_max3_f32 v242, v248, v249, v250\n\t" "v_max3_f32 v242, v242, v251, v252\n\t" "v_max3_f32 v242, v242, v253, v254\n\t" \
    "v_max_f32 v242, v242, v255\n\t" "v_max3_f32 v242, v242, v244, v245\n\t" "v_max3_f32 v242, v242, v246, v247\n\t" "v_mov_b32 v243, v242\n\t" "s_nop 1\n\t" \
    "v_permlane32_swap_b32 v242, v243\n\t" "v_max_f32 v242, v242, v243\n\t" "v_mov_b32 v243, v242\n\t" "s_nop 1\n\t" "v_permlane16_swap_b32 v242, v243\n\t" \
    "v_max_f32 v242, v242, v243\n\t" "v_pk_add_f32 v[244:245], v[244:245], v[242:243] op_sel_hi:[1,0] neg_lo:[0,1] neg_hi:[0,1]\n\t" \
    "v_pk_add_f32 v[246:247], v[246:247], v[242:243] op_sel_hi:[1,0] neg_lo:[0,1] neg_hi:[0,1]\n\t" \
    "v_pk_add_f32 v[248:249], v[248:249], v[242:243] op_sel_hi:[1,0] neg_lo:[0,1] neg_hi:[0,1]\n\t" \
    "v_pk_add_f32 v[250:251], v[250:251], v[242:243] op_sel_hi:[1,0] neg_lo:[0,1] neg_hi:[0,1]\n\t" \
    "v_pk_add_f32 v[252:253], v[252:253], v[242:243] op_sel_hi:[1,0] neg_lo:[0,1] neg_hi:[0,1]\n\t" \
    "v_pk_add_f32 v[254:255], v[254:255], v[242:243] op_sel_hi:[1,0] neg_lo:[0,1] neg_hi:[0,1]\n\t" "v_pk_mul_f32 v[244:245], v[244:245], s[66:67]\n\t" \
    "v_pk_mul_f32 v[246:247], v[246:247], s[66:67]\n\t" "v_pk_mul_f32 v[248:249], v[248:249], s[66:67]\n\t" "v_pk_mul_f32 v[250:251], v[250:251], s[66:67]\n\t" \
    "v_pk_mul_f32 v[252:253], v[252:253], s[66:67]\n\t" "v_pk_mul_f32 v[254:255], v[254:255], s[66:67]\n\t" "v_exp_f32 v244, v244\n\t" "v_exp_f32 v245, v245\n\t" \
    "v_exp_f32 v246, v246\n\t" "v_exp_f32 v247, v247\n\t" "v_exp_f32 v248, v248\n\t" "v_exp_f32 v249, v249\n\t" "v_exp_f32 v250, v250\n\t" "v_exp_f32 v251, v251\n\t" \
    "v_exp_f32 v252, v252\n\t" "v_exp_f32 v253, v253\n\t" "v_exp_f32 v254, v254\n\t" "v_exp_f32 v255, v255\n\t" "v_pk_add_f32 v[242:243], v[244:245], v[246:247]\n\t" \
    "v_pk_add_f32 v[242:243], v[242:243], v[248:249]\n\t" "v_pk_add_f32 v[242:243], v[242:243], v[250:251]\n\t" "v_pk_add_f32 v[242:243], v[242:243], v[252:253]\n\t" \
    "v_pk_add_f32 v[242:243], v[242:243], v[254:255]\n\t" "v_add_f32 v242, v242, v243\n\t" "v_mov_b32 v243, v242\n\t" "s_nop 1\n\t" "v_permlane32_swap_b32 v242, v243\n\t" \
    "v_add_f32 v242, v242, v243\n\t" "v_mov_b32 v243, v242\n\t" "s_nop 1\n\t" "v_permlane16_swap_b32 v242, v243\n\t" "v_add_f32 v242, v242, v243\n\t" \
    "v_rcp_f32 v242, v242\n\t" "s_nop 0\n\t" "v_pk_mul_f32 v[244:245], v[244:245], v[242:243] op_sel_hi:[1,0]\n\t" \
    "v_pk_mul_f32 v[246:247], v[246:247], v[242:243] op_sel_hi:[1,0]\n\t" "v_pk_mul_f32 v[248:249], v[248:249], v[242:243] op_sel_hi:[1,0]\n\t" \
    "v_pk_mul_f32 v[250:251], v[250:251], v[242:243] op_sel_hi:[1,0]\n\t" "v_pk_mul_f32 v[252:253], v[252:253], v[242:243] op_sel_hi:[1,0]\n\t" \
    "v_pk_mul_f32 v[254:255], v[254:255], v[242:243] op_sel_hi:[1,0]\n\t" "v_cvt_pk_bf16_f32 v162, v244, v245\n\t" "v_cvt_pk_bf16_f32 v163, v246, v247\n\t" \
    "v_cvt_pk_bf16_f32 v164, v248, v249\n\t" "v_cvt_pk_bf16_f32 v165, v250, v251\n\t" "v_cvt_pk_bf16_f32 v166, v252, v253\n\t" "v_cvt_pk_bf16_f32 v167, v254, v255\n\t" \
    "buffer_store_dwordx4 v[164:167], %[px], %[rs], %[tb] offen sc1\n\t" "buffer_store_dwordx2 v[162:163], %[pm], %[rs], %[tb] offen sc1\n\t" \
    "buffer_store_dwordx4 v[192:195], %[px], %[rs], %[tr] offen sc1\n\t" "buffer_store_dwordx2 v[192:193], %[pm], %[rs], %[tr] offen sc1\n\t" "s_waitcnt vmcnt(4)\n\t" \
    "v_readfirstlane_b32 s62, v240\n\t" "s_nop 3\n\t" "s_max_u32 %[sq], %[sq], s62\n\t" XT(96, 97) "s_branch 99f\n\t" "31:\n\t" "s_cmp_eq_u32 %[bd], 0\n\t" \
    "s_cbranch_scc1 36f\n\t" "s_sub_u32 %[bd], %[bd], 1\n\t" XCNT(100) "s_waitcnt lgkmcnt(0)\n\t" "s_nop 7\n\t" "s_branch 30b\n\t" "36:\n\t" "s_cmp_lg_u32 s60, 0\n\t" \
    "s_branch 32b\n\t" "34:\n\t" "s_cmp_eq_u32 %[bd], 0\n\t" "s_cbranch_scc1 35b\n\t" "s_sub_u32 %[bd], %[bd], 1\n\t" XCNT(101) "s_mov_b32 s60, 1\n\t" \
    "s_waitcnt lgkmcnt(0)\n\t" "s_nop 7\n\t" "s_branch 30b\n\t" "99:\n\t"
#define QK_STEP3 \
    "v_xor_b32 v216, 0x400, %[mo]\n\t" "s_mov_b32 s60, 0\n\t" XT(80, 81) "s_getpc_b64 s[62:63]\n\t" "48:\n\t" "s_memtime s[64:65]\n\t" "s_waitcnt lgkmcnt(0)\n\t" \
    "s_sub_u32 s61, %[tg], s64\n\t" "s_cmp_gt_i32 s61, 124\n\t" "s_cbranch_scc1 48b\n\t" "s_max_i32 s61, s61, 0\n\t" "s_and_b32 s61, s61, 124\n\t" \
    "s_sub_u32 s61, 49f-48b, s61\n\t" "s_add_u32 s62, s62, s61\n\t" "s_addc_u32 s63, s63, 0\n\t" "s_setpc_b64 s[62:63]\n\t" ".rept 31\n\t" "s_nop 0\n\t" ".endr\n\t" "49:\n\t" \
    XT(98, 99) "s_barrier\n\t" XT(82, 83) "40:\n\t" "buffer_load_dwordx4 v[240:243], %[mo], %[rs], %[sb] offen offset:0 sc1\n\t" "v_mov_b64 v[224:225], 0\n\t" \
    "v_mov_b64 v[226:227], 0\n\t" "v_mov_b64 v[228:229], 0\n\t" "v_mov_b64 v[230:231], 0\n\t" "v_mov_b64 v[232:233], 0\n\t" "v_mov_b64 v[234:235], 0\n\t" \
    "v_mov_b64 v[236:237], 0\n\t" "v_mov_b64 v[238:239], 0\n\t" "buffer_load_dwordx4 v[244:247], v216, %[rs], %[sb] offen offset:0 sc1\n\t" \
    "ds_read_b128 v[160:163], %[l0] offset:0\n\t" "ds_read_b128 v[164:167], %[l0] offset:1024\n\t" "ds_read_b128 v[168:171], %[l0] offset:2048\n\t" \
    "ds_read_b128 v[172:175], %[l0] offset:3072\n\t" "s_nop 3\n\t" "buffer_load_dwordx4 v[248:251], %[mo], %[rs], %[sb] offen offset:2048 sc1\n\t" \
    "ds_read_b128 v[176:179], %[l0] offset:4096\n\t" "ds_read_b128 v[180:183], %[l0] offset:5120\n\t" "ds_read_b128 v[184:187], %[l0] offset:6144\n\t" \
    "ds_read_b128 v[188:191], %[l0] offset:7168\n\t" "s_nop 3\n\t" "buffer_load_dwordx4 v[252:255], v216, %[rs], %[sb] offen offset:2048 sc1\n\t" "s_waitcnt vmcnt(3)\n\t" \
    XT(84, 85) "v_smfmac_f32_16x16x64_bf16 v[232:235], v[240:243], a[0:7], %[ix]\n\t" "ds_write_b128 %[l1], v[240:243] offset:12288\n\t" \
    "v_smfmac_f32_16x16x64_bf16 v[236:239], v[240:243], a[8:15], %[ix]\n\t" "v_max3_u32 v217, v240, v241, v242\n\t" \
    "v_smfmac_f32_16x16x64_bf16 v[224:227], v[240:243], v[32:39], %[ix]\n\t" "s_waitcnt lgkmcnt(7)\n\t" \
    "v_smfmac_f32_16x16x64_bf16 v[228:231], v[240:243], v[160:167], %[ix]\n\t" "ds_read_b128 v[160:163], %[l0] offset:8192\n\t" \
    "ds_read_b128 v[164:167], %[l0] offset:9216\n\t" "s_waitcnt vmcnt(2)\n\t" "v_smfmac_f32_16x16x64_bf16 v[232:235], v[244:247], a[16:23], %[ix]\n\t" \
    "ds_write_b128 %[l1], v[244:247] offset:13312\n\t" "v_max3_u32 v217, v217, v243, v244\n\t" "v_smfmac_f32_16x16x64_bf16 v[236:239], v[244:247], a[24:31], %[ix]\n\t" \
    "v_max3_u32 v217, v217, v245, v246\n\t" "v_max_u32 v217, v217, v247\n\t" "v_smfmac_f32_16x16x64_bf16 v[224:227], v[244:247], v[40:47], %[ix]\n\t" \
    "v_cmp_eq_u32 vcc, -1, v217\n\t" "s_cmp_lg_u32 s60, 0\n\t" "s_waitcnt lgkmcnt(8)\n\t" "v_smfmac_f32_16x16x64_bf16 v[228:231], v[244:247], v[168:175], %[ix]\n\t" \
    "s_cbranch_vccnz 41f\n\t" "42:\n\t" "ds_read_b128 v[168:171], %[l0] offset:10240\n\t" "s_waitcnt vmcnt(1)\n\t" \
    "v_smfmac_f32_16x16x64_bf16 v[232:235], v[248:251], a[32:39], %[ix]\n\t" "ds_write_b128 %[l1], v[248:251] offset:14336\n\t" "s_waitcnt lgkmcnt(2)\n\t" \
    "v_smfmac_f32_16x16x64_bf16 v[236:239], v[248:251], a[40:47], %[ix]\n\t" "s_cbranch_scc1 43f\n\t" "s_barrier\n\t" "43:\n\t" XT(86, 87) \
    "v_smfmac_f32_16x16x64_bf16 v[224:227], v[248:251], v[48:55], %[ix]\n\t" "ds_read_b128 v[192:195], %[l1] offset:0\n\t" "ds_read_b128 v[196:199], %[l1] offset:4096\n\t" \
    "v_smfmac_f32_16x16x64_bf16 v[228:231], v[248:251], v[176:183], %[ix]\n\t" "ds_read_b128 v[172:175], %[l0] offset:11264\n\t" \
    "ds_read_b128 v[176:179], %[l0] offset:12288\n\t" "s_waitcnt vmcnt(0)\n\t" "v_smfmac_f32_16x16x64_bf16 v[232:235], v[252:255], a[48:55], %[ix]\n\t" \
    "ds_write_b128 %[l1], v[252:255] offset:15360\n\t" "v_max3_u32 v217, v248, v249, v250\n\t" "v_smfmac_f32_16x16x64_bf16 v[236:239], v[252:255], a[56:63], %[ix]\n\t" \
    "ds_read_b128 v[180:183], %[l0] offset:13312\n\t" "v_max3_u32 v217, v217, v251, v252\n\t" "v_max3_u32 v217, v217, v253, v254\n\t" \
    "v_smfmac_f32_16x16x64_bf16 v[224:227], v[252:255], v[56:63], %[ix]\n\t" "v_max_u32 v217, v217, v255\n\t" "v_cmp_eq_u32 vcc, -1, v217\n\t" \
    "v_smfmac_f32_16x16x64_bf16 v[228:231], v[252:255], v[184:191], %[ix]\n\t" "s_cbranch_vccnz 44f\n\t" "45:\n\t" "ds_read_b128 v[240:243], %[l1] offset:8192\n\t" \
    "s_waitcnt lgkmcnt(6)\n\t" "v_smfmac_f32_16x16x64_bf16 v[232:235], v[192:195], a[64:71], %[ix]\n\t" "ds_read_b128 v[184:187], %[l0] offset:14336\n\t" \
    "ds_read_b128 v[188:191], %[l0] offset:15360\n\t" "v_smfmac_f32_16x16x64_bf16 v[236:239], v[192:195], a[72:79], %[ix]\n\t" \
    "ds_read_b128 v[244:247], %[l1] offset:1024\n\t" "s_waitcnt lgkmcnt(5)\n\t" "v_smfmac_f32_16x16x64_bf16 v[224:227], v[192:195], v[64:71], %[ix]\n\t" "s_barrier\n\t" \
    XT(88, 89) "s_memtime s[64:65]\n\t" "v_smfmac_f32_16x16x64_bf16 v[228:231], v[192:195], v[160:167], %[ix]\n\t" "ds_read_b128 v[160:163], %[l0] offset:16384\n\t" \
    "ds_read_b128 v[164:167], %[l0] offset:17408\n\t" "v_smfmac_f32_16x16x64_bf16 v[232:235], v[196:199], a[80:87], %[ix]\n\t" \
    "ds_read_b128 v[248:251], %[l1] offset:5120\n\t" "ds_read_b128 v[252:255], %[l1] offset:9216\n\t" "v_smfmac_f32_16x16x64_bf16 v[236:239], v[196:199], a[88:95], %[ix]\n\t" \
    "ds_read_b128 v[192:195], %[l1] offset:2048\n\t" "v_smfmac_f32_16x16x64_bf16 v[224:227], v[196:199], v[72:79], %[ix]\n\t" "s_sub_u32 s62, s64, %[ca]\n\t" \
    "s_mul_i32 s62, s62, %[k1]\n\t" "v_smfmac_f32_16x16x64_bf16 v[228:231], v[196:199], v[168:175], %[ix]\n\t" "s_lshr_b32 s62, s62, 16\n\t" "s_add_u32 s62, s62, %[ga]\n\t" \
    "s_waitcnt lgkmcnt(8)\n\t" "v_smfmac_f32_16x16x64_bf16 v[232:235], v[240:243], a[96:103], %[ix]\n\t" "s_bitset0_b32 s62, 0\n\t" "v_mov_b32 v218, s62\n\t" \
    "v_smfmac_f32_16x16x64_bf16 v[236:239], v[240:243], a[104:111], %[ix]\n\t" "buffer_store_dword v218, %[hq], %[rs], %[pb] offen sc1\n\t" \
    "ds_read_b128 v[168:171], %[l0] offset:18432\n\t" "v_smfmac_f32_16x16x64_bf16 v[224:227], v[240:243], v[80:87], %[ix]\n\t" \
    "ds_read_b128 v[172:175], %[l0] offset:19456\n\t" "ds_read_b128 v[196:199], %[l1] offset:6144\n\t" \
    "v_smfmac_f32_16x16x64_bf16 v[228:231], v[240:243], v[176:183], %[ix]\n\t" "ds_read_b128 v[176:179], %[l0] offset:20480\n\t" \
    "ds_read_b128 v[180:183], %[l0] offset:21504\n\t" "s_waitcnt lgkmcnt(10)\n\t" "v_smfmac_f32_16x16x64_bf16 v[232:235], v[244:247], a[112:119], %[ix]\n\t" \
    "ds_read_b128 v[240:243], %[l1] offset:10240\n\t" "v_smfmac_f32_16x16x64_bf16 v[236:239], v[244:247], a[120:127], %[ix]\n\t" \
    "v_smfmac_f32_16x16x64_bf16 v[224:227], v[244:247], v[88:95], %[ix]\n\t" "v_smfmac_f32_16x16x64_bf16 v[228:231], v[244:247], v[184:191], %[ix]\n\t" \
    "ds_read_b128 v[184:187], %[l0] offset:22528\n\t" "ds_read_b128 v[188:191], %[l0] offset:23552\n\t" "s_waitcnt lgkmcnt(10)\n\t" \
    "v_smfmac_f32_16x16x64_bf16 v[232:235], v[248:251], a[128:135], %[ix]\n\t" "ds_read_b128 v[244:247], %[l1] offset:3072\n\t" \
    "v_smfmac_f32_16x16x64_bf16 v[236:239], v[248:251], a[136:143], %[ix]\n\t" "v_smfmac_f32_16x16x64_bf16 v[224:227], v[248:251], v[96:103], %[ix]\n\t" \
    "v_smfmac_f32_16x16x64_bf16 v[228:231], v[248:251], v[160:167], %[ix]\n\t" "ds_read_b128 v[160:163], %[l0] offset:24576\n\t" \
    "ds_read_b128 v[164:167], %[l0] offset:25600\n\t" "s_waitcnt lgkmcnt(12)\n\t" "v_smfmac_f32_16x16x64_bf16 v[232:235], v[252:255], a[144:151], %[ix]\n\t" \
    "ds_read_b128 v[248:251], %[l1] offset:7168\n\t" "v_smfmac_f32_16x16x64_bf16 v[236:239], v[252:255], a[152:159], %[ix]\n\t" \
    "v_smfmac_f32_16x16x64_bf16 v[224:227], v[252:255], v[104:111], %[ix]\n\t" "s_waitcnt lgkmcnt(10)\n\t" \
    "v_smfmac_f32_16x16x64_bf16 v[228:231], v[252:255], v[168:175], %[ix]\n\t" "ds_read_b128 v[168:171], %[l0] offset:26624\n\t" \
    "ds_read_b128 v[172:175], %[l0] offset:27648\n\t" "v_smfmac_f32_16x16x64_bf16 v[224:227], v[192:195], v[112:119], %[ix]\n\t" \
    "ds_read_b128 v[252:255], %[l1] offset:11264\n\t" "s_waitcnt lgkmcnt(10)\n\t" "v_smfmac_f32_16x16x64_bf16 v[228:231], v[192:195], v[176:183], %[ix]\n\t" \
    "ds_read_b128 v[176:179], %[l0] offset:28672\n\t" "ds_read_b128 v[180:183], %[l0] offset:29696\n\t" \
    "v_smfmac_f32_16x16x64_bf16 v[224:227], v[196:199], v[120:127], %[ix]\n\t" "s_waitcnt lgkmcnt(9)\n\t" \
    "v_smfmac_f32_16x16x64_bf16 v[228:231], v[196:199], v[184:191], %[ix]\n\t" "ds_read_b128 v[184:187], %[l0] offset:30720\n\t" \
    "ds_read_b128 v[188:191], %[l0] offset:31744\n\t" "v_smfmac_f32_16x16x64_bf16 v[224:227], v[240:243], v[128:135], %[ix]\n\t" "s_waitcnt lgkmcnt(8)\n\t" \
    "v_smfmac_f32_16x16x64_bf16 v[228:231], v[240:243], v[160:167], %[ix]\n\t" "v_smfmac_f32_16x16x64_bf16 v[224:227], v[244:247], v[136:143], %[ix]\n\t" \
    "s_waitcnt lgkmcnt(5)\n\t" "v_smfmac_f32_16x16x64_bf16 v[228:231], v[244:247], v[168:175], %[ix]\n\t" \
    "v_smfmac_f32_16x16x64_bf16 v[224:227], v[248:251], v[144:151], %[ix]\n\t" "s_waitcnt lgkmcnt(2)\n\t" \
    "v_smfmac_f32_16x16x64_bf16 v[228:231], v[248:251], v[176:183], %[ix]\n\t" "v_smfmac_f32_16x16x64_bf16 v[224:227], v[252:255], v[152:159], %[ix]\n\t" "s_nop 1\n\t" \
    "s_waitcnt lgkmcnt(0)\n\t" "v_smfmac_f32_16x16x64_bf16 v[228:231], v[252:255], v[184:191], %[ix]\n\t" XT(90, 91) "s_nop 1\n\t" \
    "v_smfmac_f32_16x16x64_bf16 v[232:235], v[192:195], a[160:167], %[ix]\n\t" "s_nop 1\n\t" "v_smfmac_f32_16x16x64_bf16 v[236:239], v[192:195], a[168:175], %[ix]\n\t" \
    "v_add_f32 v224, v224, v225\n\t" "v_add_f32 v226, v226, v227\n\t" "v_smfmac_f32_16x16x64_bf16 v[232:235], v[196:199], a[176:183], %[ix]\n\t" \
    "v_add_f32 v228, v228, v229\n\t" "v_add_f32 v230, v230, v231\n\t" "v_smfmac_f32_16x16x64_bf16 v[236:239], v[196:199], a[184:191], %[ix]\n\t" \
    "v_cvt_pk_bf16_f32 v218, v224, v228\n\t" "v_cvt_pk_bf16_f32 v219, v226, v230\n\t" "v_smfmac_f32_16x16x64_bf16 v[232:235], v[240:243], a[192:199], %[ix]\n\t" \
    "ds_write2_b32 %[qw], v218, v219 offset0:4 offset1:12\n\t" "v_smfmac_f32_16x16x64_bf16 v[236:239], v[240:243], a[200:207], %[ix]\n\t" \
    "v_smfmac_f32_16x16x64_bf16 v[232:235], v[244:247], a[208:215], %[ix]\n\t" "v_smfmac_f32_16x16x64_bf16 v[236:239], v[244:247], a[216:223], %[ix]\n\t" \
    "s_waitcnt lgkmcnt(0)\n\t" "s_barrier\n\t" "v_smfmac_f32_16x16x64_bf16 v[232:235], v[248:251], a[224:231], %[ix]\n\t" \
    "v_smfmac_f32_16x16x64_bf16 v[236:239], v[248:251], a[232:239], %[ix]\n\t" "v_smfmac_f32_16x16x64_bf16 v[232:235], v[252:255], a[240:247], %[ix]\n\t" \
    "v_smfmac_f32_16x16x64_bf16 v[236:239], v[252:255], a[248:255], %[ix]\n\t" XT(92, 93) "s_nop 6\n\t" "v_add_f32 v232, v232, v233\n\t" "v_add_f32 v234, v234, v235\n\t" \
    "v_add_f32 v236, v236, v237\n\t" "v_add_f32 v238, v238, v239\n\t" "v_cvt_pk_bf16_f32 v232, v232, v236\n\t" "v_cvt_pk_bf16_f32 v233, v234, v238\n\t" \
    "ds_write2_b32 %[kw], v232, v233 offset0:0 offset1:68\n\t" "s_waitcnt lgkmcnt(0)\n\t" "s_barrier\n\t" XT(94, 95) XT(96, 97) "s_branch 99f\n\t" "41:\n\t" \
    "s_cmp_eq_u32 %[bd], 0\n\t" "s_cbranch_scc1 46f\n\t" "s_sub_u32 %[bd], %[bd], 1\n\t" XCNT(100) "s_waitcnt lgkmcnt(0)\n\t" "s_nop 7\n\t" "s_branch 40b\n\t" "46:\n\t" \
    "s_cmp_lg_u32 s60, 0\n\t" "s_branch 42b\n\t" "44:\n\t" "s_cmp_eq_u32 %[bd], 0\n\t" "s_cbranch_scc1 45b\n\t" "s_sub_u32 %[bd], %[bd], 1\n\t" XCNT(101) \
    "s_mov_b32 s60, 1\n\t" "s_waitcnt lgkmcnt(0)\n\t" "s_nop 7\n\t" "s_branch 40b\n\t" "99:\n\t"
#define VO_STEP \
    "v_xor_b32 v224, 0x400, %[mo]\n\t" "s_mov_b32 s60, 0\n\t" "s_cmp_eq_u32 %[w], 1\n\t" "s_cbranch_scc1 91f\n\t" "s_cmp_eq_u32 %[w], 2\n\t" "s_cbranch_scc1 92f\n\t" \
    "s_cmp_eq_u32 %[w], 3\n\t" "s_cbranch_scc1 93f\n\t" XT(80, 81) "s_getpc_b64 s[62:63]\n\t" "18:\n\t" "s_memtime s[64:65]\n\t" "s_waitcnt lgkmcnt(0)\n\t" \
    "s_sub_u32 s61, %[tg], s64\n\t" "s_cmp_gt_i32 s61, 124\n\t" "s_cbranch_scc1 18b\n\t" "s_max_i32 s61, s61, 0\n\t" "s_and_b32 s61, s61, 124\n\t" \
    "s_sub_u32 s61, 19f-18b, s61\n\t" "s_add_u32 s62, s62, s61\n\t" "s_addc_u32 s63, s63, 0\n\t" "s_setpc_b64 s[62:63]\n\t" ".rept 31\n\t" "s_nop 0\n\t" ".endr\n\t" "19:\n\t" \
    "s_barrier\n\t" XT(82, 83) "10:\n\t" "buffer_load_dwordx4 v[240:243], %[mo], %[rs], %[sb] offen offset:0 sc1\n\t" "v_mov_b64 v[232:233], 0\n\t" \
    "v_mov_b64 v[234:235], 0\n\t" "v_mov_b64 v[236:237], 0\n\t" "v_mov_b64 v[238:239], 0\n\t" "v_mov_b64 v[192:193], 0\n\t" "v_mov_b64 v[194:195], 0\n\t" \
    "v_mov_b64 v[196:197], 0\n\t" "v_mov_b64 v[198:199], 0\n\t" "buffer_load_dwordx4 v[244:247], v224, %[rs], %[sb] offen offset:0 sc1\n\t" \
    "ds_read_b128 v[160:163], %[l0] offset:0\n\t" "ds_read_b128 v[164:167], %[l0] offset:1024\n\t" "ds_read_b128 v[168:171], %[l0] offset:2048\n\t" \
    "ds_read_b128 v[172:175], %[l0] offset:3072\n\t" "s_nop 3\n\t" "buffer_load_dwordx4 v[248:251], %[mo], %[rs], %[sb] offen offset:2048 sc1\n\t" \
    "ds_read_b128 v[176:179], %[l0] offset:4096\n\t" "ds_read_b128 v[180:183], %[l0] offset:5120\n\t" "ds_read_b128 v[184:187], %[l0] offset:6144\n\t" \
    "ds_read_b128 v[188:191], %[l0] offset:7168\n\t" "s_nop 3\n\t" "buffer_load_dwordx4 v[252:255], v224, %[rs], %[sb] offen offset:2048 sc1\n\t" \
    "v_mov_b64 v[200:201], 0\n\t" "v_mov_b64 v[202:203], 0\n\t" "v_mov_b64 v[204:205], 0\n\t" "v_mov_b64 v[206:207], 0\n\t" "v_mov_b64 v[208:209], 0\n\t" \
    "v_mov_b64 v[210:211], 0\n\t" "v_mov_b64 v[212:213], 0\n\t" "v_mov_b64 v[214:215], 0\n\t" "v_mov_b64 v[216:217], 0\n\t" "v_mov_b64 v[218:219], 0\n\t" \
    "v_mov_b64 v[220:221], 0\n\t" "v_mov_b64 v[222:223], 0\n\t" "s_waitcnt vmcnt(3)\n\t" XT(84, 85) "v_smfmac_f32_16x16x64_bf16 v[232:235], v[240:243], a[0:7], %[ix]\n\t" \
    "ds_write_b128 %[l1], v[240:243] offset:0\n\t" "v_smfmac_f32_16x16x64_bf16 v[236:239], v[240:243], a[8:15], %[ix]\n\t" "v_max3_u32 v225, v240, v241, v242\n\t" \
    "s_waitcnt vmcnt(2)\n\t" "v_smfmac_f32_16x16x64_bf16 v[232:235], v[244:247], a[16:23], %[ix]\n\t" "ds_write_b128 %[l1], v[244:247] offset:1024\n\t" \
    "v_max3_u32 v225, v225, v243, v244\n\t" "s_waitcnt lgkmcnt(8)\n\t" "v_smfmac_f32_16x16x64_bf16 v[236:239], v[244:247], v[160:167], %[ix]\n\t" \
    "v_max3_u32 v225, v225, v245, v246\n\t" "v_max_u32 v225, v225, v247\n\t" "v_cmp_eq_u32 vcc, -1, v225\n\t" "s_cmp_lg_u32 s60, 0\n\t" "s_cbranch_vccnz 11f\n\t" "12:\n\t" \
    "s_waitcnt lgkmcnt(0)\n\t" "s_cbranch_scc1 13f\n\t" "s_barrier\n\t" "13:\n\t" XT(86, 87) "ds_read_b128 v[16:19], %[l1] offset:4096\n\t" \
    "ds_read_b128 v[20:23], %[l1] offset:8192\n\t" "ds_read_b128 v[24:27], %[l1] offset:12288\n\t" "s_waitcnt vmcnt(1)\n\t" \
    "v_smfmac_f32_16x16x64_bf16 v[232:235], v[248:251], a[24:31], %[ix]\n\t" "ds_write_b128 %[l1], v[248:251] offset:2048\n\t" "v_max3_u32 v225, v248, v249, v250\n\t" \
    "v_smfmac_f32_16x16x64_bf16 v[236:239], v[248:251], a[32:39], %[ix]\n\t" "ds_read_b128 v[28:31], %[l1] offset:5120\n\t" "ds_read_b128 v[240:243], %[l1] offset:9216\n\t" \
    "ds_read_b128 v[244:247], %[l1] offset:13312\n\t" "s_waitcnt vmcnt(0)\n\t" "v_smfmac_f32_16x16x64_bf16 v[232:235], v[252:255], a[40:47], %[ix]\n\t" \
    "ds_write_b128 %[l1], v[252:255] offset:3072\n\t" "v_max3_u32 v225, v225, v251, v252\n\t" "v_smfmac_f32_16x16x64_bf16 v[236:239], v[252:255], v[168:175], %[ix]\n\t" \
    "v_max3_u32 v225, v225, v253, v254\n\t" "v_max_u32 v225, v225, v255\n\t" "s_waitcnt lgkmcnt(7)\n\t" "v_smfmac_f32_16x16x64_bf16 v[232:235], v[16:19], a[48:55], %[ix]\n\t" \
    "v_cmp_eq_u32 vcc, -1, v225\n\t" "s_cbranch_vccnz 14f\n\t" "15:\n\t" "v_smfmac_f32_16x16x64_bf16 v[236:239], v[16:19], a[56:63], %[ix]\n\t" \
    "ds_read_b128 v[160:163], %[l0] offset:8192\n\t" "ds_read_b128 v[164:167], %[l0] offset:9216\n\t" "s_waitcnt lgkmcnt(8)\n\t" \
    "v_smfmac_f32_16x16x64_bf16 v[232:235], v[20:23], a[64:71], %[ix]\n\t" "ds_read_b128 v[168:171], %[l0] offset:10240\n\t" "ds_read_b128 v[172:175], %[l0] offset:11264\n\t" \
    "v_smfmac_f32_16x16x64_bf16 v[236:239], v[20:23], v[176:183], %[ix]\n\t" "s_waitcnt lgkmcnt(4)\n\t" "s_barrier\n\t" XT(88, 89) "s_memtime s[64:65]\n\t" \
    "v_smfmac_f32_16x16x64_bf16 v[232:235], v[24:27], a[72:79], %[ix]\n\t" "ds_read_b128 v[176:179], %[l0] offset:12288\n\t" "ds_read_b128 v[180:183], %[l0] offset:13312\n\t" \
    "v_smfmac_f32_16x16x64_bf16 v[236:239], v[24:27], a[80:87], %[ix]\n\t" "ds_read_b128 v[248:251], %[l1] offset:6144\n\t" "ds_read_b128 v[252:255], %[l1] offset:10240\n\t" \
    "v_smfmac_f32_16x16x64_bf16 v[232:235], v[28:31], a[88:95], %[ix]\n\t" "ds_read_b128 v[16:19], %[l1] offset:14336\n\t" "ds_read_b128 v[20:23], %[l1] offset:7168\n\t" \
    "v_smfmac_f32_16x16x64_bf16 v[236:239], v[28:31], v[184:191], %[ix]\n\t" "ds_read_b128 v[24:27], %[l1] offset:11264\n\t" "ds_read_b128 v[184:187], %[l0] offset:14336\n\t" \
    "v_smfmac_f32_16x16x64_bf16 v[232:235], v[240:243], a[96:103], %[ix]\n\t" "ds_read_b128 v[188:191], %[l0] offset:15360\n\t" \
    "ds_read_b128 v[28:31], %[l1] offset:15360\n\t" "v_smfmac_f32_16x16x64_bf16 v[236:239], v[240:243], a[104:111], %[ix]\n\t" "s_sub_u32 s62, s64, %[ca]\n\t" \
    "s_mul_i32 s62, s62, %[k1]\n\t" "v_smfmac_f32_16x16x64_bf16 v[232:235], v[244:247], a[112:119], %[ix]\n\t" "s_lshr_b32 s62, s62, 16\n\t" "s_add_u32 s62, s62, %[ga]\n\t" \
    "s_waitcnt lgkmcnt(12)\n\t" "v_smfmac_f32_16x16x64_bf16 v[236:239], v[244:247], v[160:167], %[ix]\n\t" "s_bitset0_b32 s62, 0\n\t" "v_mov_b32 v226, s62\n\t" \
    "s_waitcnt lgkmcnt(7)\n\t" "v_smfmac_f32_16x16x64_bf16 v[232:235], v[248:251], a[120:127], %[ix]\n\t" "buffer_store_dword v226, %[hq], %[rs], %[pb] offen sc1\n\t" \
    "ds_read_b128 v[160:163], %[l0] offset:16384\n\t" "v_smfmac_f32_16x16x64_bf16 v[236:239], v[248:251], a[128:135], %[ix]\n\t" \
    "ds_read_b128 v[164:167], %[l0] offset:17408\n\t" "s_waitcnt lgkmcnt(8)\n\t" "v_smfmac_f32_16x16x64_bf16 v[232:235], v[252:255], a[136:143], %[ix]\n\t" \
    "v_smfmac_f32_16x16x64_bf16 v[236:239], v[252:255], v[168:175], %[ix]\n\t" "ds_read_b128 v[168:171], %[l0] offset:18432\n\t" \
    "ds_read_b128 v[172:175], %[l0] offset:19456\n\t" "s_waitcnt lgkmcnt(9)\n\t" "v_smfmac_f32_16x16x64_bf16 v[232:235], v[16:19], a[144:151], %[ix]\n\t" \
    "v_smfmac_f32_16x16x64_bf16 v[236:239], v[16:19], a[152:159], %[ix]\n\t" "s_waitcnt lgkmcnt(8)\n\t" \
    "v_smfmac_f32_16x16x64_bf16 v[232:235], v[20:23], a[160:167], %[ix]\n\t" "v_smfmac_f32_16x16x64_bf16 v[236:239], v[20:23], v[176:183], %[ix]\n\t" \
    "ds_read_b128 v[176:179], %[l0] offset:20480\n\t" "ds_read_b128 v[180:183], %[l0] offset:21504\n\t" "s_waitcnt lgkmcnt(9)\n\t" \
    "v_smfmac_f32_16x16x64_bf16 v[232:235], v[24:27], a[168:175], %[ix]\n\t" "v_smfmac_f32_16x16x64_bf16 v[236:239], v[24:27], a[176:183], %[ix]\n\t" \
    "s_waitcnt lgkmcnt(6)\n\t" "v_smfmac_f32_16x16x64_bf16 v[232:235], v[28:31], a[184:191], %[ix]\n\t" \
    "v_smfmac_f32_16x16x64_bf16 v[236:239], v[28:31], v[184:191], %[ix]\n\t" "ds_read_b128 v[184:187], %[l0] offset:22528\n\t" \
    "ds_read_b128 v[188:191], %[l0] offset:23552\n\t" XT(90, 91) "s_nop 4\n\t" "v_add_f32 v232, v232, v233\n\t" "v_add_f32 v234, v234, v235\n\t" \
    "v_add_f32 v236, v236, v237\n\t" "v_add_f32 v238, v238, v239\n\t" "v_cvt_pk_bf16_f32 v232, v232, v236\n\t" "v_cvt_pk_bf16_f32 v233, v234, v238\n\t" \
    "ds_write2_b32 %[vw], v232, v233 offset1:68\n\t" "s_waitcnt lgkmcnt(0)\n\t" "s_barrier\n\t" XT(92, 93) "ds_read_b128 v[240:243], %[vr] offset:0\n\t" \
    "ds_read_b128 v[244:247], %[vr] offset:64\n\t" "ds_read_b128 v[248:251], %[vr] offset:128\n\t" "ds_read_b128 v[252:255], %[vr] offset:192\n\t" "s_mov_b32 s61, %[ub]\n\t" \
    "s_waitcnt lgkmcnt(3)\n\t" "v_smfmac_f32_16x16x64_bf16 v[192:195], v[240:243], v[32:39], %[ix]\n\t" \
    "v_smfmac_f32_16x16x64_bf16 v[196:199], v[240:243], v[48:55], %[ix]\n\t" "s_waitcnt lgkmcnt(2)\n\t" \
    "v_smfmac_f32_16x16x64_bf16 v[192:195], v[244:247], v[40:47], %[ix]\n\t" "v_smfmac_f32_16x16x64_bf16 v[196:199], v[244:247], v[56:63], %[ix]\n\t" \
    "s_waitcnt lgkmcnt(1)\n\t" "v_smfmac_f32_16x16x64_bf16 v[192:195], v[248:251], a[192:199], %[ix]\n\t" \
    "v_smfmac_f32_16x16x64_bf16 v[196:199], v[248:251], a[200:207], %[ix]\n\t" "s_waitcnt lgkmcnt(0)\n\t" \
    "v_smfmac_f32_16x16x64_bf16 v[192:195], v[252:255], v[160:167], %[ix]\n\t" "ds_read_b128 v[160:163], %[l0] offset:24576\n\t" \
    "ds_read_b128 v[164:167], %[l0] offset:25600\n\t" "v_smfmac_f32_16x16x64_bf16 v[196:199], v[252:255], v[168:175], %[ix]\n\t" \
    "ds_read_b128 v[168:171], %[l0] offset:26624\n\t" "ds_read_b128 v[172:175], %[l0] offset:27648\n\t" \
    "v_smfmac_f32_16x16x64_bf16 v[200:203], v[240:243], v[64:71], %[ix]\n\t" "s_nop 1\n\t" "v_smfmac_f32_16x16x64_bf16 v[204:207], v[240:243], v[80:87], %[ix]\n\t" \
    "v_cvt_pk_bf16_f32 v224, v192, v194\n\t" "v_cvt_pk_bf16_f32 v225, v193, v195\n\t" "v_smfmac_f32_16x16x64_bf16 v[200:203], v[244:247], v[72:79], %[ix]\n\t" \
    "v_cvt_pk_bf16_f32 v226, v196, v198\n\t" "v_cvt_pk_bf16_f32 v227, v197, v199\n\t" "v_smfmac_f32_16x16x64_bf16 v[204:207], v[244:247], v[88:95], %[ix]\n\t" "s_nop 0\n\t" \
    "v_permlane32_swap_b32 v224, v226\n\t" "v_smfmac_f32_16x16x64_bf16 v[200:203], v[248:251], a[208:215], %[ix]\n\t" "v_permlane32_swap_b32 v225, v227\n\t" "s_nop 0\n\t" \
    "v_smfmac_f32_16x16x64_bf16 v[204:207], v[248:251], a[216:223], %[ix]\n\t" "v_permlane16_swap_b32 v224, v225\n\t" "v_permlane16_swap_b32 v226, v227\n\t" \
    "v_smfmac_f32_16x16x64_bf16 v[200:203], v[252:255], v[176:183], %[ix]\n\t" "buffer_store_dwordx4 v[224:227], %[uo], %[rs], s61 offen sc1\n\t" \
    "s_add_u32 s61, s61, 0x10000\n\t" XT(94, 95) "v_smfmac_f32_16x16x64_bf16 v[204:207], v[252:255], v[184:191], %[ix]\n\t" "ds_read_b128 v[176:179], %[l0] offset:28672\n\t" \
    "ds_read_b128 v[180:183], %[l0] offset:29696\n\t" "v_smfmac_f32_16x16x64_bf16 v[208:211], v[240:243], v[96:103], %[ix]\n\t" \
    "ds_read_b128 v[184:187], %[l0] offset:30720\n\t" "ds_read_b128 v[188:191], %[l0] offset:31744\n\t" \
    "v_smfmac_f32_16x16x64_bf16 v[212:215], v[240:243], v[112:119], %[ix]\n\t" "v_cvt_pk_bf16_f32 v228, v200, v202\n\t" "v_cvt_pk_bf16_f32 v229, v201, v203\n\t" \
    "v_smfmac_f32_16x16x64_bf16 v[208:211], v[244:247], v[104:111], %[ix]\n\t" "v_cvt_pk_bf16_f32 v230, v204, v206\n\t" "v_cvt_pk_bf16_f32 v231, v205, v207\n\t" \
    "v_smfmac_f32_16x16x64_bf16 v[212:215], v[244:247], v[120:127], %[ix]\n\t" "s_nop 0\n\t" "v_permlane32_swap_b32 v228, v230\n\t" \
    "v_smfmac_f32_16x16x64_bf16 v[208:211], v[248:251], a[224:231], %[ix]\n\t" "v_permlane32_swap_b32 v229, v231\n\t" "s_nop 0\n\t" \
    "v_smfmac_f32_16x16x64_bf16 v[212:215], v[248:251], a[232:239], %[ix]\n\t" "v_permlane16_swap_b32 v228, v229\n\t" "v_permlane16_swap_b32 v230, v231\n\t" \
    "s_waitcnt lgkmcnt(6)\n\t" "v_smfmac_f32_16x16x64_bf16 v[208:211], v[252:255], v[160:167], %[ix]\n\t" "buffer_store_dwordx4 v[228:231], %[uo], %[rs], s61 offen sc1\n\t" \
    "s_add_u32 s61, s61, 0x10000\n\t" "s_waitcnt lgkmcnt(4)\n\t" "v_smfmac_f32_16x16x64_bf16 v[212:215], v[252:255], v[168:175], %[ix]\n\t" "s_nop 1\n\t" \
    "v_smfmac_f32_16x16x64_bf16 v[216:219], v[240:243], v[128:135], %[ix]\n\t" "s_nop 1\n\t" "v_smfmac_f32_16x16x64_bf16 v[220:223], v[240:243], v[144:151], %[ix]\n\t" \
    "v_cvt_pk_bf16_f32 v224, v208, v210\n\t" "v_cvt_pk_bf16_f32 v225, v209, v211\n\t" "v_smfmac_f32_16x16x64_bf16 v[216:219], v[244:247], v[136:143], %[ix]\n\t" \
    "v_cvt_pk_bf16_f32 v226, v212, v214\n\t" "v_cvt_pk_bf16_f32 v227, v213, v215\n\t" "v_smfmac_f32_16x16x64_bf16 v[220:223], v[244:247], v[152:159], %[ix]\n\t" "s_nop 0\n\t" \
    "v_permlane32_swap_b32 v224, v226\n\t" "v_smfmac_f32_16x16x64_bf16 v[216:219], v[248:251], a[240:247], %[ix]\n\t" "v_permlane32_swap_b32 v225, v227\n\t" "s_nop 0\n\t" \
    "v_smfmac_f32_16x16x64_bf16 v[220:223], v[248:251], a[248:255], %[ix]\n\t" "v_permlane16_swap_b32 v224, v225\n\t" "v_permlane16_swap_b32 v226, v227\n\t" \
    "s_waitcnt lgkmcnt(2)\n\t" "v_smfmac_f32_16x16x64_bf16 v[216:219], v[252:255], v[176:183], %[ix]\n\t" "buffer_store_dwordx4 v[224:227], %[uo], %[rs], s61 offen sc1\n\t" \
    "s_add_u32 s61, s61, 0x10000\n\t" "s_waitcnt lgkmcnt(0)\n\t" "v_smfmac_f32_16x16x64_bf16 v[220:223], v[252:255], v[184:191], %[ix]\n\t" "s_nop 5\n\t" \
    "v_cvt_pk_bf16_f32 v228, v216, v218\n\t" "v_cvt_pk_bf16_f32 v229, v217, v219\n\t" "v_cvt_pk_bf16_f32 v230, v220, v222\n\t" "v_cvt_pk_bf16_f32 v231, v221, v223\n\t" \
    "s_nop 0\n\t" "v_permlane32_swap_b32 v228, v230\n\t" "v_permlane32_swap_b32 v229, v231\n\t" "s_nop 1\n\t" "v_permlane16_swap_b32 v228, v229\n\t" \
    "v_permlane16_swap_b32 v230, v231\n\t" "buffer_store_dwordx4 v[228:231], %[uo], %[rs], s61 offen sc1\n\t" "s_nop 1\n\t" XT(96, 97) "s_branch 99f\n\t" "11:\n\t" \
    "s_cmp_eq_u32 %[bd], 0\n\t" "s_cbranch_scc1 16f\n\t" "s_sub_u32 %[bd], %[bd], 1\n\t" XCNT(100) "s_waitcnt lgkmcnt(0)\n\t" "s_nop 7\n\t" "s_branch 10b\n\t" "16:\n\t" \
    "s_cmp_lg_u32 s60, 0\n\t" "s_branch 12b\n\t" "14:\n\t" "s_cmp_eq_u32 %[bd], 0\n\t" "s_cbranch_scc1 15b\n\t" "s_sub_u32 %[bd], %[bd], 1\n\t" XCNT(101) \
    "s_mov_b32 s60, 1\n\t" "s_waitcnt lgkmcnt(0)\n\t" "s_nop 7\n\t" "s_branch 10b\n\t" "91:\n\t" XT(80, 81) "s_barrier\n\t" XT(82, 83) "20:\n\t" \
    "buffer_load_dwordx4 v[240:243], %[mo], %[rs], %[sb] offen offset:0 sc1\n\t" "v_mov_b64 v[232:233], 0\n\t" "v_mov_b64 v[234:235], 0\n\t" "v_mov_b64 v[236:237], 0\n\t" \
    "v_mov_b64 v[238:239], 0\n\t" "v_mov_b64 v[192:193], 0\n\t" "v_mov_b64 v[194:195], 0\n\t" "v_mov_b64 v[196:197], 0\n\t" "v_mov_b64 v[198:199], 0\n\t" \
    "buffer_load_dwordx4 v[244:247], v224, %[rs], %[sb] offen offset:0 sc1\n\t" "ds_read_b128 v[160:163], %[l0] offset:0\n\t" "ds_read_b128 v[164:167], %[l0] offset:1024\n\t" \
    "ds_read_b128 v[168:171], %[l0] offset:2048\n\t" "ds_read_b128 v[172:175], %[l0] offset:3072\n\t" "s_nop 3\n\t" \
    "buffer_load_dwordx4 v[248:251], %[mo], %[rs], %[sb] offen offset:2048 sc1\n\t" "ds_read_b128 v[176:179], %[l0] offset:4096\n\t" \
    "ds_read_b128 v[180:183], %[l0] offset:5120\n\t" "ds_read_b128 v[184:187], %[l0] offset:6144\n\t" "ds_read_b128 v[188:191], %[l0] offset:7168\n\t" "s_nop 3\n\t" \
    "buffer_load_dwordx4 v[252:255], v224, %[rs], %[sb] offen offset:2048 sc1\n\t" "v_mov_b64 v[200:201], 0\n\t" "v_mov_b64 v[202:203], 0\n\t" "v_mov_b64 v[204:205], 0\n\t" \
    "v_mov_b64 v[206:207], 0\n\t" "v_mov_b64 v[208:209], 0\n\t" "v_mov_b64 v[210:211], 0\n\t" "v_mov_b64 v[212:213], 0\n\t" "v_mov_b64 v[214:215], 0\n\t" \
    "v_mov_b64 v[216:217], 0\n\t" "v_mov_b64 v[218:219], 0\n\t" "v_mov_b64 v[220:221], 0\n\t" "v_mov_b64 v[222:223], 0\n\t" "s_waitcnt vmcnt(3)\n\t" XT(84, 85) \
    "v_smfmac_f32_16x16x64_bf16 v[232:235], v[240:243], a[0:7], %[ix]\n\t" "ds_write_b128 %[l1], v[240:243] offset:4096\n\t" \
    "v_smfmac_f32_16x16x64_bf16 v[236:239], v[240:243], a[8:15], %[ix]\n\t" "v_max3_u32 v225, v240, v241, v242\n\t" "s_waitcnt vmcnt(2)\n\t" \
    "v_smfmac_f32_16x16x64_bf16 v[232:235], v[244:247], a[16:23], %[ix]\n\t" "ds_write_b128 %[l1], v[244:247] offset:5120\n\t" "v_max3_u32 v225, v225, v243, v244\n\t" \
    "s_waitcnt lgkmcnt(8)\n\t" "v_smfmac_f32_16x16x64_bf16 v[236:239], v[244:247], v[160:167], %[ix]\n\t" "v_max3_u32 v225, v225, v245, v246\n\t" \
    "v_max_u32 v225, v225, v247\n\t" "v_cmp_eq_u32 vcc, -1, v225\n\t" "s_cmp_lg_u32 s60, 0\n\t" "s_cbranch_vccnz 21f\n\t" "22:\n\t" "s_waitcnt lgkmcnt(0)\n\t" \
    "s_cbranch_scc1 23f\n\t" "s_barrier\n\t" "23:\n\t" XT(86, 87) "ds_read_b128 v[16:19], %[l1] offset:8192\n\t" "ds_read_b128 v[20:23], %[l1] offset:12288\n\t" \
    "ds_read_b128 v[24:27], %[l1] offset:0\n\t" "s_waitcnt vmcnt(1)\n\t" "v_smfmac_f32_16x16x64_bf16 v[232:235], v[248:251], a[24:31], %[ix]\n\t" \
    "ds_write_b128 %[l1], v[248:251] offset:6144\n\t" "v_max3_u32 v225, v248, v249, v250\n\t" "v_smfmac_f32_16x16x64_bf16 v[236:239], v[248:251], a[32:39], %[ix]\n\t" \
    "ds_read_b128 v[28:31], %[l1] offset:9216\n\t" "ds_read_b128 v[240:243], %[l1] offset:13312\n\t" "ds_read_b128 v[244:247], %[l1] offset:1024\n\t" "s_waitcnt vmcnt(0)\n\t" \
    "v_smfmac_f32_16x16x64_bf16 v[232:235], v[252:255], a[40:47], %[ix]\n\t" "ds_write_b128 %[l1], v[252:255] offset:7168\n\t" "v_max3_u32 v225, v225, v251, v252\n\t" \
    "v_smfmac_f32_16x16x64_bf16 v[236:239], v[252:255], v[168:175], %[ix]\n\t" "v_max3_u32 v225, v225, v253, v254\n\t" "v_max_u32 v225, v225, v255\n\t" \
    "s_waitcnt lgkmcnt(7)\n\t" "v_smfmac_f32_16x16x64_bf16 v[232:235], v[16:19], a[48:55], %[ix]\n\t" "v_cmp_eq_u32 vcc, -1, v225\n\t" "s_cbranch_vccnz 24f\n\t" "25:\n\t" \
    "v_smfmac_f32_16x16x64_bf16 v[236:239], v[16:19], a[56:63], %[ix]\n\t" "ds_read_b128 v[160:163], %[l0] offset:8192\n\t" "ds_read_b128 v[164:167], %[l0] offset:9216\n\t" \
    "s_waitcnt lgkmcnt(8)\n\t" "v_smfmac_f32_16x16x64_bf16 v[232:235], v[20:23], a[64:71], %[ix]\n\t" "ds_read_b128 v[168:171], %[l0] offset:10240\n\t" \
    "ds_read_b128 v[172:175], %[l0] offset:11264\n\t" "v_smfmac_f32_16x16x64_bf16 v[236:239], v[20:23], v[176:183], %[ix]\n\t" "s_waitcnt lgkmcnt(4)\n\t" "s_barrier\n\t" \
    XT(88, 89) "v_smfmac_f32_16x16x64_bf16 v[232:235], v[24:27], a[72:79], %[ix]\n\t" "ds_read_b128 v[176:179], %[l0] offset:12288\n\t" \
    "ds_read_b128 v[180:183], %[l0] offset:13312\n\t" "v_smfmac_f32_16x16x64_bf16 v[236:239], v[24:27], a[80:87], %[ix]\n\t" "ds_read_b128 v[248:251], %[l1] offset:10240\n\t" \
    "ds_read_b128 v[252:255], %[l1] offset:14336\n\t" "v_smfmac_f32_16x16x64_bf16 v[232:235], v[28:31], a[88:95], %[ix]\n\t" "ds_read_b128 v[16:19], %[l1] offset:2048\n\t" \
    "ds_read_b128 v[20:23], %[l1] offset:11264\n\t" "v_smfmac_f32_16x16x64_bf16 v[236:239], v[28:31], v[184:191], %[ix]\n\t" "ds_read_b128 v[24:27], %[l1] offset:15360\n\t" \
    "ds_read_b128 v[184:187], %[l0] offset:14336\n\t" "v_smfmac_f32_16x16x64_bf16 v[232:235], v[240:243], a[96:103], %[ix]\n\t" \
    "ds_read_b128 v[188:191], %[l0] offset:15360\n\t" "ds_read_b128 v[28:31], %[l1] offset:3072\n\t" \
    "v_smfmac_f32_16x16x64_bf16 v[236:239], v[240:243], a[104:111], %[ix]\n\t" "v_smfmac_f32_16x16x64_bf16 v[232:235], v[244:247], a[112:119], %[ix]\n\t" \
    "s_waitcnt lgkmcnt(12)\n\t" "v_smfmac_f32_16x16x64_bf16 v[236:239], v[244:247], v[160:167], %[ix]\n\t" "ds_read_b128 v[160:163], %[l0] offset:16384\n\t" \
    "ds_read_b128 v[164:167], %[l0] offset:17408\n\t" "s_waitcnt lgkmcnt(9)\n\t" "v_smfmac_f32_16x16x64_bf16 v[232:235], v[248:251], a[120:127], %[ix]\n\t" \
    "v_smfmac_f32_16x16x64_bf16 v[236:239], v[248:251], a[128:135], %[ix]\n\t" "s_waitcnt lgkmcnt(8)\n\t" \
    "v_smfmac_f32_16x16x64_bf16 v[232:235], v[252:255], a[136:143], %[ix]\n\t" "v_smfmac_f32_16x16x64_bf16 v[236:239], v[252:255], v[168:175], %[ix]\n\t" \
    "ds_read_b128 v[168:171], %[l0] offset:18432\n\t" "ds_read_b128 v[172:175], %[l0] offset:19456\n\t" "s_waitcnt lgkmcnt(9)\n\t" \
    "v_smfmac_f32_16x16x64_bf16 v[232:235], v[16:19], a[144:151], %[ix]\n\t" "v_smfmac_f32_16x16x64_bf16 v[236:239], v[16:19], a[152:159], %[ix]\n\t" \
    "s_waitcnt lgkmcnt(8)\n\t" "v_smfmac_f32_16x16x64_bf16 v[232:235], v[20:23], a[160:167], %[ix]\n\t" \
    "v_smfmac_f32_16x16x64_bf16 v[236:239], v[20:23], v[176:183], %[ix]\n\t" "ds_read_b128 v[176:179], %[l0] offset:20480\n\t" \
    "ds_read_b128 v[180:183], %[l0] offset:21504\n\t" "s_waitcnt lgkmcnt(9)\n\t" "v_smfmac_f32_16x16x64_bf16 v[232:235], v[24:27], a[168:175], %[ix]\n\t" \
    "v_smfmac_f32_16x16x64_bf16 v[236:239], v[24:27], a[176:183], %[ix]\n\t" "s_waitcnt lgkmcnt(6)\n\t" \
    "v_smfmac_f32_16x16x64_bf16 v[232:235], v[28:31], a[184:191], %[ix]\n\t" "v_smfmac_f32_16x16x64_bf16 v[236:239], v[28:31], v[184:191], %[ix]\n\t" \
    "ds_read_b128 v[184:187], %[l0] offset:22528\n\t" "ds_read_b128 v[188:191], %[l0] offset:23552\n\t" XT(90, 91) "s_nop 4\n\t" "v_add_f32 v232, v232, v233\n\t" \
    "v_add_f32 v234, v234, v235\n\t" "v_add_f32 v236, v236, v237\n\t" "v_add_f32 v238, v238, v239\n\t" "v_cvt_pk_bf16_f32 v232, v232, v236\n\t" \
    "v_cvt_pk_bf16_f32 v233, v234, v238\n\t" "ds_write2_b32 %[vw], v232, v233 offset1:68\n\t" "s_waitcnt lgkmcnt(0)\n\t" "s_barrier\n\t" XT(92, 93) \
    "ds_read_b128 v[240:243], %[vr] offset:0\n\t" "ds_read_b128 v[244:247], %[vr] offset:64\n\t" "ds_read_b128 v[248:251], %[vr] offset:128\n\t" \
    "ds_read_b128 v[252:255], %[vr] offset:192\n\t" "s_mov_b32 s61, %[ub]\n\t" "s_waitcnt lgkmcnt(3)\n\t" \
    "v_smfmac_f32_16x16x64_bf16 v[192:195], v[240:243], v[32:39], %[ix]\n\t" "v_smfmac_f32_16x16x64_bf16 v[196:199], v[240:243], v[48:55], %[ix]\n\t" \
    "s_waitcnt lgkmcnt(2)\n\t" "v_smfmac_f32_16x16x64_bf16 v[192:195], v[244:247], v[40:47], %[ix]\n\t" \
    "v_smfmac_f32_16x16x64_bf16 v[196:199], v[244:247], v[56:63], %[ix]\n\t" "s_waitcnt lgkmcnt(1)\n\t" \
    "v_smfmac_f32_16x16x64_bf16 v[192:195], v[248:251], a[192:199], %[ix]\n\t" "v_smfmac_f32_16x16x64_bf16 v[196:199], v[248:251], a[200:207], %[ix]\n\t" \
    "s_waitcnt lgkmcnt(0)\n\t" "v_smfmac_f32_16x16x64_bf16 v[192:195], v[252:255], v[160:167], %[ix]\n\t" "ds_read_b128 v[160:163], %[l0] offset:24576\n\t" \
    "ds_read_b128 v[164:167], %[l0] offset:25600\n\t" "v_smfmac_f32_16x16x64_bf16 v[196:199], v[252:255], v[168:175], %[ix]\n\t" \
    "ds_read_b128 v[168:171], %[l0] offset:26624\n\t" "ds_read_b128 v[172:175], %[l0] offset:27648\n\t" \
    "v_smfmac_f32_16x16x64_bf16 v[200:203], v[240:243], v[64:71], %[ix]\n\t" "s_nop 1\n\t" "v_smfmac_f32_16x16x64_bf16 v[204:207], v[240:243], v[80:87], %[ix]\n\t" \
    "v_cvt_pk_bf16_f32 v224, v192, v194\n\t" "v_cvt_pk_bf16_f32 v225, v193, v195\n\t" "v_smfmac_f32_16x16x64_bf16 v[200:203], v[244:247], v[72:79], %[ix]\n\t" \
    "v_cvt_pk_bf16_f32 v226, v196, v198\n\t" "v_cvt_pk_bf16_f32 v227, v197, v199\n\t" "v_smfmac_f32_16x16x64_bf16 v[204:207], v[244:247], v[88:95], %[ix]\n\t" "s_nop 0\n\t" \
    "v_permlane32_swap_b32 v224, v226\n\t" "v_smfmac_f32_16x16x64_bf16 v[200:203], v[248:251], a[208:215], %[ix]\n\t" "v_permlane32_swap_b32 v225, v227\n\t" "s_nop 0\n\t" \
    "v_smfmac_f32_16x16x64_bf16 v[204:207], v[248:251], a[216:223], %[ix]\n\t" "v_permlane16_swap_b32 v224, v225\n\t" "v_permlane16_swap_b32 v226, v227\n\t" \
    "v_smfmac_f32_16x16x64_bf16 v[200:203], v[252:255], v[176:183], %[ix]\n\t" "buffer_store_dwordx4 v[224:227], %[uo], %[rs], s61 offen sc1\n\t" \
    "s_add_u32 s61, s61, 0x10000\n\t" XT(94, 95) "v_smfmac_f32_16x16x64_bf16 v[204:207], v[252:255], v[184:191], %[ix]\n\t" "ds_read_b128 v[176:179], %[l0] offset:28672\n\t" \
    "ds_read_b128 v[180:183], %[l0] offset:29696\n\t" "v_smfmac_f32_16x16x64_bf16 v[208:211], v[240:243], v[96:103], %[ix]\n\t" \
    "ds_read_b128 v[184:187], %[l0] offset:30720\n\t" "ds_read_b128 v[188:191], %[l0] offset:31744\n\t" \
    "v_smfmac_f32_16x16x64_bf16 v[212:215], v[240:243], v[112:119], %[ix]\n\t" "v_cvt_pk_bf16_f32 v228, v200, v202\n\t" "v_cvt_pk_bf16_f32 v229, v201, v203\n\t" \
    "v_smfmac_f32_16x16x64_bf16 v[208:211], v[244:247], v[104:111], %[ix]\n\t" "v_cvt_pk_bf16_f32 v230, v204, v206\n\t" "v_cvt_pk_bf16_f32 v231, v205, v207\n\t" \
    "v_smfmac_f32_16x16x64_bf16 v[212:215], v[244:247], v[120:127], %[ix]\n\t" "s_nop 0\n\t" "v_permlane32_swap_b32 v228, v230\n\t" \
    "v_smfmac_f32_16x16x64_bf16 v[208:211], v[248:251], a[224:231], %[ix]\n\t" "v_permlane32_swap_b32 v229, v231\n\t" "s_nop 0\n\t" \
    "v_smfmac_f32_16x16x64_bf16 v[212:215], v[248:251], a[232:239], %[ix]\n\t" "v_permlane16_swap_b32 v228, v229\n\t" "v_permlane16_swap_b32 v230, v231\n\t" \
    "s_waitcnt lgkmcnt(6)\n\t" "v_smfmac_f32_16x16x64_bf16 v[208:211], v[252:255], v[160:167], %[ix]\n\t" "buffer_store_dwordx4 v[228:231], %[uo], %[rs], s61 offen sc1\n\t" \
    "s_add_u32 s61, s61, 0x10000\n\t" "s_waitcnt lgkmcnt(4)\n\t" "v_smfmac_f32_16x16x64_bf16 v[212:215], v[252:255], v[168:175], %[ix]\n\t" "s_nop 1\n\t" \
    "v_smfmac_f32_16x16x64_bf16 v[216:219], v[240:243], v[128:135], %[ix]\n\t" "s_nop 1\n\t" "v_smfmac_f32_16x16x64_bf16 v[220:223], v[240:243], v[144:151], %[ix]\n\t" \
    "v_cvt_pk_bf16_f32 v224, v208, v210\n\t" "v_cvt_pk_bf16_f32 v225, v209, v211\n\t" "v_smfmac_f32_16x16x64_bf16 v[216:219], v[244:247], v[136:143], %[ix]\n\t" \
    "v_cvt_pk_bf16_f32 v226, v212, v214\n\t" "v_cvt_pk_bf16_f32 v227, v213, v215\n\t" "v_smfmac_f32_16x16x64_bf16 v[220:223], v[244:247], v[152:159], %[ix]\n\t" "s_nop 0\n\t" \
    "v_permlane32_swap_b32 v224, v226\n\t" "v_smfmac_f32_16x16x64_bf16 v[216:219], v[248:251], a[240:247], %[ix]\n\t" "v_permlane32_swap_b32 v225, v227\n\t" "s_nop 0\n\t" \
    "v_smfmac_f32_16x16x64_bf16 v[220:223], v[248:251], a[248:255], %[ix]\n\t" "v_permlane16_swap_b32 v224, v225\n\t" "v_permlane16_swap_b32 v226, v227\n\t" \
    "s_waitcnt lgkmcnt(2)\n\t" "v_smfmac_f32_16x16x64_bf16 v[216:219], v[252:255], v[176:183], %[ix]\n\t" "buffer_store_dwordx4 v[224:227], %[uo], %[rs], s61 offen sc1\n\t" \
    "s_add_u32 s61, s61, 0x10000\n\t" "s_waitcnt lgkmcnt(0)\n\t" "v_smfmac_f32_16x16x64_bf16 v[220:223], v[252:255], v[184:191], %[ix]\n\t" "s_nop 5\n\t" \
    "v_cvt_pk_bf16_f32 v228, v216, v218\n\t" "v_cvt_pk_bf16_f32 v229, v217, v219\n\t" "v_cvt_pk_bf16_f32 v230, v220, v222\n\t" "v_cvt_pk_bf16_f32 v231, v221, v223\n\t" \
    "s_nop 0\n\t" "v_permlane32_swap_b32 v228, v230\n\t" "v_permlane32_swap_b32 v229, v231\n\t" "s_nop 1\n\t" "v_permlane16_swap_b32 v228, v229\n\t" \
    "v_permlane16_swap_b32 v230, v231\n\t" "buffer_store_dwordx4 v[228:231], %[uo], %[rs], s61 offen sc1\n\t" "s_nop 1\n\t" XT(96, 97) "s_branch 99f\n\t" "21:\n\t" \
    "s_cmp_eq_u32 %[bd], 0\n\t" "s_cbranch_scc1 26f\n\t" "s_sub_u32 %[bd], %[bd], 1\n\t" XCNT(100) "s_waitcnt lgkmcnt(0)\n\t" "s_nop 7\n\t" "s_branch 20b\n\t" "26:\n\t" \
    "s_cmp_lg_u32 s60, 0\n\t" "s_branch 22b\n\t" "24:\n\t" "s_cmp_eq_u32 %[bd], 0\n\t" "s_cbranch_scc1 25b\n\t" "s_sub_u32 %[bd], %[bd], 1\n\t" XCNT(101) \
    "s_mov_b32 s60, 1\n\t" "s_waitcnt lgkmcnt(0)\n\t" "s_nop 7\n\t" "s_branch 20b\n\t" "92:\n\t" XT(80, 81) "s_barrier\n\t" XT(82, 83) "30:\n\t" \
    "buffer_load_dwordx4 v[240:243], %[mo], %[rs], %[sb] offen offset:0 sc1\n\t" "v_mov_b64 v[232:233], 0\n\t" "v_mov_b64 v[234:235], 0\n\t" "v_mov_b64 v[236:237], 0\n\t" \
    "v_mov_b64 v[238:239], 0\n\t" "v_mov_b64 v[192:193], 0\n\t" "v_mov_b64 v[194:195], 0\n\t" "v_mov_b64 v[196:197], 0\n\t" "v_mov_b64 v[198:199], 0\n\t" \
    "buffer_load_dwordx4 v[244:247], v224, %[rs], %[sb] offen offset:0 sc1\n\t" "ds_read_b128 v[160:163], %[l0] offset:0\n\t" "ds_read_b128 v[164:167], %[l0] offset:1024\n\t" \
    "ds_read_b128 v[168:171], %[l0] offset:2048\n\t" "ds_read_b128 v[172:175], %[l0] offset:3072\n\t" "s_nop 3\n\t" \
    "buffer_load_dwordx4 v[248:251], %[mo], %[rs], %[sb] offen offset:2048 sc1\n\t" "ds_read_b128 v[176:179], %[l0] offset:4096\n\t" \
    "ds_read_b128 v[180:183], %[l0] offset:5120\n\t" "ds_read_b128 v[184:187], %[l0] offset:6144\n\t" "ds_read_b128 v[188:191], %[l0] offset:7168\n\t" "s_nop 3\n\t" \
    "buffer_load_dwordx4 v[252:255], v224, %[rs], %[sb] offen offset:2048 sc1\n\t" "v_mov_b64 v[200:201], 0\n\t" "v_mov_b64 v[202:203], 0\n\t" "v_mov_b64 v[204:205], 0\n\t" \
    "v_mov_b64 v[206:207], 0\n\t" "v_mov_b64 v[208:209], 0\n\t" "v_mov_b64 v[210:211], 0\n\t" "v_mov_b64 v[212:213], 0\n\t" "v_mov_b64 v[214:215], 0\n\t" \
    "v_mov_b64 v[216:217], 0\n\t" "v_mov_b64 v[218:219], 0\n\t" "v_mov_b64 v[220:221], 0\n\t" "v_mov_b64 v[222:223], 0\n\t" "s_waitcnt vmcnt(3)\n\t" XT(84, 85) \
    "v_smfmac_f32_16x16x64_bf16 v[232:235], v[240:243], a[0:7], %[ix]\n\t" "ds_write_b128 %[l1], v[240:243] offset:8192\n\t" \
    "v_smfmac_f32_16x16x64_bf16 v[236:239], v[240:243], a[8:15], %[ix]\n\t" "v_max3_u32 v225, v240, v241, v242\n\t" "s_waitcnt vmcnt(2)\n\t" \
    "v_smfmac_f32_16x16x64_bf16 v[232:235], v[244:247], a[16:23], %[ix]\n\t" "ds_write_b128 %[l1], v[244:247] offset:9216\n\t" "v_max3_u32 v225, v225, v243, v244\n\t" \
    "s_waitcnt lgkmcnt(8)\n\t" "v_smfmac_f32_16x16x64_bf16 v[236:239], v[244:247], v[160:167], %[ix]\n\t" "v_max3_u32 v225, v225, v245, v246\n\t" \
    "v_max_u32 v225, v225, v247\n\t" "v_cmp_eq_u32 vcc, -1, v225\n\t" "s_cmp_lg_u32 s60, 0\n\t" "s_cbranch_vccnz 31f\n\t" "32:\n\t" "s_waitcnt lgkmcnt(0)\n\t" \
    "s_cbranch_scc1 33f\n\t" "s_barrier\n\t" "33:\n\t" XT(86, 87) "ds_read_b128 v[16:19], %[l1] offset:12288\n\t" "ds_read_b128 v[20:23], %[l1] offset:0\n\t" \
    "ds_read_b128 v[24:27], %[l1] offset:4096\n\t" "s_waitcnt vmcnt(1)\n\t" "v_smfmac_f32_16x16x64_bf16 v[232:235], v[248:251], a[24:31], %[ix]\n\t" \
    "ds_write_b128 %[l1], v[248:251] offset:10240\n\t" "v_max3_u32 v225, v248, v249, v250\n\t" "v_smfmac_f32_16x16x64_bf16 v[236:239], v[248:251], a[32:39], %[ix]\n\t" \
    "ds_read_b128 v[28:31], %[l1] offset:13312\n\t" "ds_read_b128 v[240:243], %[l1] offset:1024\n\t" "ds_read_b128 v[244:247], %[l1] offset:5120\n\t" "s_waitcnt vmcnt(0)\n\t" \
    "v_smfmac_f32_16x16x64_bf16 v[232:235], v[252:255], a[40:47], %[ix]\n\t" "ds_write_b128 %[l1], v[252:255] offset:11264\n\t" "v_max3_u32 v225, v225, v251, v252\n\t" \
    "v_smfmac_f32_16x16x64_bf16 v[236:239], v[252:255], v[168:175], %[ix]\n\t" "v_max3_u32 v225, v225, v253, v254\n\t" "v_max_u32 v225, v225, v255\n\t" \
    "s_waitcnt lgkmcnt(7)\n\t" "v_smfmac_f32_16x16x64_bf16 v[232:235], v[16:19], a[48:55], %[ix]\n\t" "v_cmp_eq_u32 vcc, -1, v225\n\t" "s_cbranch_vccnz 34f\n\t" "35:\n\t" \
    "v_smfmac_f32_16x16x64_bf16 v[236:239], v[16:19], a[56:63], %[ix]\n\t" "ds_read_b128 v[160:163], %[l0] offset:8192\n\t" "ds_read_b128 v[164:167], %[l0] offset:9216\n\t" \
    "s_waitcnt lgkmcnt(8)\n\t" "v_smfmac_f32_16x16x64_bf16 v[232:235], v[20:23], a[64:71], %[ix]\n\t" "ds_read_b128 v[168:171], %[l0] offset:10240\n\t" \
    "ds_read_b128 v[172:175], %[l0] offset:11264\n\t" "v_smfmac_f32_16x16x64_bf16 v[236:239], v[20:23], v[176:183], %[ix]\n\t" "s_waitcnt lgkmcnt(4)\n\t" "s_barrier\n\t" \
    XT(88, 89) "v_smfmac_f32_16x16x64_bf16 v[232:235], v[24:27], a[72:79], %[ix]\n\t" "ds_read_b128 v[176:179], %[l0] offset:12288\n\t" \
    "ds_read_b128 v[180:183], %[l0] offset:13312\n\t" "v_smfmac_f32_16x16x64_bf16 v[236:239], v[24:27], a[80:87], %[ix]\n\t" "ds_read_b128 v[248:251], %[l1] offset:14336\n\t" \
    "ds_read_b128 v[252:255], %[l1] offset:2048\n\t" "v_smfmac_f32_16x16x64_bf16 v[232:235], v[28:31], a[88:95], %[ix]\n\t" "ds_read_b128 v[16:19], %[l1] offset:6144\n\t" \
    "ds_read_b128 v[20:23], %[l1] offset:15360\n\t" "v_smfmac_f32_16x16x64_bf16 v[236:239], v[28:31], v[184:191], %[ix]\n\t" "ds_read_b128 v[24:27], %[l1] offset:3072\n\t" \
    "ds_read_b128 v[184:187], %[l0] offset:14336\n\t" "v_smfmac_f32_16x16x64_bf16 v[232:235], v[240:243], a[96:103], %[ix]\n\t" \
    "ds_read_b128 v[188:191], %[l0] offset:15360\n\t" "ds_read_b128 v[28:31], %[l1] offset:7168\n\t" \
    "v_smfmac_f32_16x16x64_bf16 v[236:239], v[240:243], a[104:111], %[ix]\n\t" "v_smfmac_f32_16x16x64_bf16 v[232:235], v[244:247], a[112:119], %[ix]\n\t" \
    "s_waitcnt lgkmcnt(12)\n\t" "v_smfmac_f32_16x16x64_bf16 v[236:239], v[244:247], v[160:167], %[ix]\n\t" "ds_read_b128 v[160:163], %[l0] offset:16384\n\t" \
    "ds_read_b128 v[164:167], %[l0] offset:17408\n\t" "s_waitcnt lgkmcnt(9)\n\t" "v_smfmac_f32_16x16x64_bf16 v[232:235], v[248:251], a[120:127], %[ix]\n\t" \
    "v_smfmac_f32_16x16x64_bf16 v[236:239], v[248:251], a[128:135], %[ix]\n\t" "s_waitcnt lgkmcnt(8)\n\t" \
    "v_smfmac_f32_16x16x64_bf16 v[232:235], v[252:255], a[136:143], %[ix]\n\t" "v_smfmac_f32_16x16x64_bf16 v[236:239], v[252:255], v[168:175], %[ix]\n\t" \
    "ds_read_b128 v[168:171], %[l0] offset:18432\n\t" "ds_read_b128 v[172:175], %[l0] offset:19456\n\t" "s_waitcnt lgkmcnt(9)\n\t" \
    "v_smfmac_f32_16x16x64_bf16 v[232:235], v[16:19], a[144:151], %[ix]\n\t" "v_smfmac_f32_16x16x64_bf16 v[236:239], v[16:19], a[152:159], %[ix]\n\t" \
    "s_waitcnt lgkmcnt(8)\n\t" "v_smfmac_f32_16x16x64_bf16 v[232:235], v[20:23], a[160:167], %[ix]\n\t" \
    "v_smfmac_f32_16x16x64_bf16 v[236:239], v[20:23], v[176:183], %[ix]\n\t" "ds_read_b128 v[176:179], %[l0] offset:20480\n\t" \
    "ds_read_b128 v[180:183], %[l0] offset:21504\n\t" "s_waitcnt lgkmcnt(9)\n\t" "v_smfmac_f32_16x16x64_bf16 v[232:235], v[24:27], a[168:175], %[ix]\n\t" \
    "v_smfmac_f32_16x16x64_bf16 v[236:239], v[24:27], a[176:183], %[ix]\n\t" "s_waitcnt lgkmcnt(6)\n\t" \
    "v_smfmac_f32_16x16x64_bf16 v[232:235], v[28:31], a[184:191], %[ix]\n\t" "v_smfmac_f32_16x16x64_bf16 v[236:239], v[28:31], v[184:191], %[ix]\n\t" \
    "ds_read_b128 v[184:187], %[l0] offset:22528\n\t" "ds_read_b128 v[188:191], %[l0] offset:23552\n\t" XT(90, 91) "s_nop 4\n\t" "v_add_f32 v232, v232, v233\n\t" \
    "v_add_f32 v234, v234, v235\n\t" "v_add_f32 v236, v236, v237\n\t" "v_add_f32 v238, v238, v239\n\t" "v_cvt_pk_bf16_f32 v232, v232, v236\n\t" \
    "v_cvt_pk_bf16_f32 v233, v234, v238\n\t" "ds_write2_b32 %[vw], v232, v233 offset1:68\n\t" "s_waitcnt lgkmcnt(0)\n\t" "s_barrier\n\t" XT(92, 93) \
    "ds_read_b128 v[240:243], %[vr] offset:0\n\t" "ds_read_b128 v[244:247], %[vr] offset:64\n\t" "ds_read_b128 v[248:251], %[vr] offset:128\n\t" \
    "ds_read_b128 v[252:255], %[vr] offset:192\n\t" "s_mov_b32 s61, %[ub]\n\t" "s_waitcnt lgkmcnt(3)\n\t" \
    "v_smfmac_f32_16x16x64_bf16 v[192:195], v[240:243], v[32:39], %[ix]\n\t" "v_smfmac_f32_16x16x64_bf16 v[196:199], v[240:243], v[48:55], %[ix]\n\t" \
    "s_waitcnt lgkmcnt(2)\n\t" "v_smfmac_f32_16x16x64_bf16 v[192:195], v[244:247], v[40:47], %[ix]\n\t" \
    "v_smfmac_f32_16x16x64_bf16 v[196:199], v[244:247], v[56:63], %[ix]\n\t" "s_waitcnt lgkmcnt(1)\n\t" \
    "v_smfmac_f32_16x16x64_bf16 v[192:195], v[248:251], a[192:199], %[ix]\n\t" "v_smfmac_f32_16x16x64_bf16 v[196:199], v[248:251], a[200:207], %[ix]\n\t" \
    "s_waitcnt lgkmcnt(0)\n\t" "v_smfmac_f32_16x16x64_bf16 v[192:195], v[252:255], v[160:167], %[ix]\n\t" "ds_read_b128 v[160:163], %[l0] offset:24576\n\t" \
    "ds_read_b128 v[164:167], %[l0] offset:25600\n\t" "v_smfmac_f32_16x16x64_bf16 v[196:199], v[252:255], v[168:175], %[ix]\n\t" \
    "ds_read_b128 v[168:171], %[l0] offset:26624\n\t" "ds_read_b128 v[172:175], %[l0] offset:27648\n\t" \
    "v_smfmac_f32_16x16x64_bf16 v[200:203], v[240:243], v[64:71], %[ix]\n\t" "s_nop 1\n\t" "v_smfmac_f32_16x16x64_bf16 v[204:207], v[240:243], v[80:87], %[ix]\n\t" \
    "v_cvt_pk_bf16_f32 v224, v192, v194\n\t" "v_cvt_pk_bf16_f32 v225, v193, v195\n\t" "v_smfmac_f32_16x16x64_bf16 v[200:203], v[244:247], v[72:79], %[ix]\n\t" \
    "v_cvt_pk_bf16_f32 v226, v196, v198\n\t" "v_cvt_pk_bf16_f32 v227, v197, v199\n\t" "v_smfmac_f32_16x16x64_bf16 v[204:207], v[244:247], v[88:95], %[ix]\n\t" "s_nop 0\n\t" \
    "v_permlane32_swap_b32 v224, v226\n\t" "v_smfmac_f32_16x16x64_bf16 v[200:203], v[248:251], a[208:215], %[ix]\n\t" "v_permlane32_swap_b32 v225, v227\n\t" "s_nop 0\n\t" \
    "v_smfmac_f32_16x16x64_bf16 v[204:207], v[248:251], a[216:223], %[ix]\n\t" "v_permlane16_swap_b32 v224, v225\n\t" "v_permlane16_swap_b32 v226, v227\n\t" \
    "v_smfmac_f32_16x16x64_bf16 v[200:203], v[252:255], v[176:183], %[ix]\n\t" "buffer_store_dwordx4 v[224:227], %[uo], %[rs], s61 offen sc1\n\t" \
    "s_add_u32 s61, s61, 0x10000\n\t" XT(94, 95) "v_smfmac_f32_16x16x64_bf16 v[204:207], v[252:255], v[184:191], %[ix]\n\t" "ds_read_b128 v[176:179], %[l0] offset:28672\n\t" \
    "ds_read_b128 v[180:183], %[l0] offset:29696\n\t" "v_smfmac_f32_16x16x64_bf16 v[208:211], v[240:243], v[96:103], %[ix]\n\t" \
    "ds_read_b128 v[184:187], %[l0] offset:30720\n\t" "ds_read_b128 v[188:191], %[l0] offset:31744\n\t" \
    "v_smfmac_f32_16x16x64_bf16 v[212:215], v[240:243], v[112:119], %[ix]\n\t" "v_cvt_pk_bf16_f32 v228, v200, v202\n\t" "v_cvt_pk_bf16_f32 v229, v201, v203\n\t" \
    "v_smfmac_f32_16x16x64_bf16 v[208:211], v[244:247], v[104:111], %[ix]\n\t" "v_cvt_pk_bf16_f32 v230, v204, v206\n\t" "v_cvt_pk_bf16_f32 v231, v205, v207\n\t" \
    "v_smfmac_f32_16x16x64_bf16 v[212:215], v[244:247], v[120:127], %[ix]\n\t" "s_nop 0\n\t" "v_permlane32_swap_b32 v228, v230\n\t" \
    "v_smfmac_f32_16x16x64_bf16 v[208:211], v[248:251], a[224:231], %[ix]\n\t" "v_permlane32_swap_b32 v229, v231\n\t" "s_nop 0\n\t" \
    "v_smfmac_f32_16x16x64_bf16 v[212:215], v[248:251], a[232:239], %[ix]\n\t" "v_permlane16_swap_b32 v228, v229\n\t" "v_permlane16_swap_b32 v230, v231\n\t" \
    "s_waitcnt lgkmcnt(6)\n\t" "v_smfmac_f32_16x16x64_bf16 v[208:211], v[252:255], v[160:167], %[ix]\n\t" "buffer_store_dwordx4 v[228:231], %[uo], %[rs], s61 offen sc1\n\t" \
    "s_add_u32 s61, s61, 0x10000\n\t" "s_waitcnt lgkmcnt(4)\n\t" "v_smfmac_f32_16x16x64_bf16 v[212:215], v[252:255], v[168:175], %[ix]\n\t" "s_nop 1\n\t" \
    "v_smfmac_f32_16x16x64_bf16 v[216:219], v[240:243], v[128:135], %[ix]\n\t" "s_nop 1\n\t" "v_smfmac_f32_16x16x64_bf16 v[220:223], v[240:243], v[144:151], %[ix]\n\t" \
    "v_cvt_pk_bf16_f32 v224, v208, v210\n\t" "v_cvt_pk_bf16_f32 v225, v209, v211\n\t" "v_smfmac_f32_16x16x64_bf16 v[216:219], v[244:247], v[136:143], %[ix]\n\t" \
    "v_cvt_pk_bf16_f32 v226, v212, v214\n\t" "v_cvt_pk_bf16_f32 v227, v213, v215\n\t" "v_smfmac_f32_16x16x64_bf16 v[220:223], v[244:247], v[152:159], %[ix]\n\t" "s_nop 0\n\t" \
    "v_permlane32_swap_b32 v224, v226\n\t" "v_smfmac_f32_16x16x64_bf16 v[216:219], v[248:251], a[240:247], %[ix]\n\t" "v_permlane32_swap_b32 v225, v227\n\t" "s_nop 0\n\t" \
    "v_smfmac_f32_16x16x64_bf16 v[220:223], v[248:251], a[248:255], %[ix]\n\t" "v_permlane16_swap_b32 v224, v225\n\t" "v_permlane16_swap_b32 v226, v227\n\t" \
    "s_waitcnt lgkmcnt(2)\n\t" "v_smfmac_f32_16x16x64_bf16 v[216:219], v[252:255], v[176:183], %[ix]\n\t" "buffer_store_dwordx4 v[224:227], %[uo], %[rs], s61 offen sc1\n\t" \
    "s_add_u32 s61, s61, 0x10000\n\t" "s_waitcnt lgkmcnt(0)\n\t" "v_smfmac_f32_16x16x64_bf16 v[220:223], v[252:255], v[184:191], %[ix]\n\t" "s_nop 5\n\t" \
    "v_cvt_pk_bf16_f32 v228, v216, v218\n\t" "v_cvt_pk_bf16_f32 v229, v217, v219\n\t" "v_cvt_pk_bf16_f32 v230, v220, v222\n\t" "v_cvt_pk_bf16_f32 v231, v221, v223\n\t" \
    "s_nop 0\n\t" "v_permlane32_swap_b32 v228, v230\n\t" "v_permlane32_swap_b32 v229, v231\n\t" "s_nop 1\n\t" "v_permlane16_swap_b32 v228, v229\n\t" \
    "v_permlane16_swap_b32 v230, v231\n\t" "buffer_store_dwordx4 v[228:231], %[uo], %[rs], s61 offen sc1\n\t" "s_nop 1\n\t" XT(96, 97) "s_branch 99f\n\t" "31:\n\t" \
    "s_cmp_eq_u32 %[bd], 0\n\t" "s_cbranch_scc1 36f\n\t" "s_sub_u32 %[bd], %[bd], 1\n\t" XCNT(100) "s_waitcnt lgkmcnt(0)\n\t" "s_nop 7\n\t" "s_branch 30b\n\t" "36:\n\t" \
    "s_cmp_lg_u32 s60, 0\n\t" "s_branch 32b\n\t" "34:\n\t" "s_cmp_eq_u32 %[bd], 0\n\t" "s_cbranch_scc1 35b\n\t" "s_sub_u32 %[bd], %[bd], 1\n\t" XCNT(101) \
    "s_mov_b32 s60, 1\n\t" "s_waitcnt lgkmcnt(0)\n\t" "s_nop 7\n\t" "s_branch 30b\n\t" "93:\n\t" XT(80, 81) "s_barrier\n\t" XT(82, 83) "40:\n\t" \
    "buffer_load_dwordx4 v[240:243], %[mo], %[rs], %[sb] offen offset:0 sc1\n\t" "v_mov_b64 v[232:233], 0\n\t" "v_mov_b64 v[234:235], 0\n\t" "v_mov_b64 v[236:237], 0\n\t" \
    "v_mov_b64 v[238:239], 0\n\t" "v_mov_b64 v[192:193], 0\n\t" "v_mov_b64 v[194:195], 0\n\t" "v_mov_b64 v[196:197], 0\n\t" "v_mov_b64 v[198:199], 0\n\t" \
    "buffer_load_dwordx4 v[244:247], v224, %[rs], %[sb] offen offset:0 sc1\n\t" "ds_read_b128 v[160:163], %[l0] offset:0\n\t" "ds_read_b128 v[164:167], %[l0] offset:1024\n\t" \
    "ds_read_b128 v[168:171], %[l0] offset:2048\n\t" "ds_read_b128 v[172:175], %[l0] offset:3072\n\t" "s_nop 3\n\t" \
    "buffer_load_dwordx4 v[248:251], %[mo], %[rs], %[sb] offen offset:2048 sc1\n\t" "ds_read_b128 v[176:179], %[l0] offset:4096\n\t" \
    "ds_read_b128 v[180:183], %[l0] offset:5120\n\t" "ds_read_b128 v[184:187], %[l0] offset:6144\n\t" "ds_read_b128 v[188:191], %[l0] offset:7168\n\t" "s_nop 3\n\t" \
    "buffer_load_dwordx4 v[252:255], v224, %[rs], %[sb] offen offset:2048 sc1\n\t" "v_mov_b64 v[200:201], 0\n\t" "v_mov_b64 v[202:203], 0\n\t" "v_mov_b64 v[204:205], 0\n\t" \
    "v_mov_b64 v[206:207], 0\n\t" "v_mov_b64 v[208:209], 0\n\t" "v_mov_b64 v[210:211], 0\n\t" "v_mov_b64 v[212:213], 0\n\t" "v_mov_b64 v[214:215], 0\n\t" \
    "v_mov_b64 v[216:217], 0\n\t" "v_mov_b64 v[218:219], 0\n\t" "v_mov_b64 v[220:221], 0\n\t" "v_mov_b64 v[222:223], 0\n\t" "s_waitcnt vmcnt(3)\n\t" XT(84, 85) \
    "v_smfmac_f32_16x16x64_bf16 v[232:235], v[240:243], a[0:7], %[ix]\n\t" "ds_write_b128 %[l1], v[240:243] offset:12288\n\t" \
    "v_smfmac_f32_16x16x64_bf16 v[236:239], v[240:243], a[8:15], %[ix]\n\t" "v_max3_u32 v225, v240, v241, v242\n\t" "s_waitcnt vmcnt(2)\n\t" \
    "v_smfmac_f32_16x16x64_bf16 v[232:235], v[244:247], a[16:23], %[ix]\n\t" "ds_write_b128 %[l1], v[244:247] offset:13312\n\t" "v_max3_u32 v225, v225, v243, v244\n\t" \
    "s_waitcnt lgkmcnt(8)\n\t" "v_smfmac_f32_16x16x64_bf16 v[236:239], v[244:247], v[160:167], %[ix]\n\t" "v_max3_u32 v225, v225, v245, v246\n\t" \
    "v_max_u32 v225, v225, v247\n\t" "v_cmp_eq_u32 vcc, -1, v225\n\t" "s_cmp_lg_u32 s60, 0\n\t" "s_cbranch_vccnz 41f\n\t" "42:\n\t" "s_waitcnt lgkmcnt(0)\n\t" \
    "s_cbranch_scc1 43f\n\t" "s_barrier\n\t" "43:\n\t" XT(86, 87) "ds_read_b128 v[16:19], %[l1] offset:0\n\t" "ds_read_b128 v[20:23], %[l1] offset:4096\n\t" \
    "ds_read_b128 v[24:27], %[l1] offset:8192\n\t" "s_waitcnt vmcnt(1)\n\t" "v_smfmac_f32_16x16x64_bf16 v[232:235], v[248:251], a[24:31], %[ix]\n\t" \
    "ds_write_b128 %[l1], v[248:251] offset:14336\n\t" "v_max3_u32 v225, v248, v249, v250\n\t" "v_smfmac_f32_16x16x64_bf16 v[236:239], v[248:251], a[32:39], %[ix]\n\t" \
    "ds_read_b128 v[28:31], %[l1] offset:1024\n\t" "ds_read_b128 v[240:243], %[l1] offset:5120\n\t" "ds_read_b128 v[244:247], %[l1] offset:9216\n\t" "s_waitcnt vmcnt(0)\n\t" \
    "v_smfmac_f32_16x16x64_bf16 v[232:235], v[252:255], a[40:47], %[ix]\n\t" "ds_write_b128 %[l1], v[252:255] offset:15360\n\t" "v_max3_u32 v225, v225, v251, v252\n\t" \
    "v_smfmac_f32_16x16x64_bf16 v[236:239], v[252:255], v[168:175], %[ix]\n\t" "v_max3_u32 v225, v225, v253, v254\n\t" "v_max_u32 v225, v225, v255\n\t" \
    "s_waitcnt lgkmcnt(7)\n\t" "v_smfmac_f32_16x16x64_bf16 v[232:235], v[16:19], a[48:55], %[ix]\n\t" "v_cmp_eq_u32 vcc, -1, v225\n\t" "s_cbranch_vccnz 44f\n\t" "45:\n\t" \
    "v_smfmac_f32_16x16x64_bf16 v[236:239], v[16:19], a[56:63], %[ix]\n\t" "ds_read_b128 v[160:163], %[l0] offset:8192\n\t" "ds_read_b128 v[164:167], %[l0] offset:9216\n\t" \
    "s_waitcnt lgkmcnt(8)\n\t" "v_smfmac_f32_16x16x64_bf16 v[232:235], v[20:23], a[64:71], %[ix]\n\t" "ds_read_b128 v[168:171], %[l0] offset:10240\n\t" \
    "ds_read_b128 v[172:175], %[l0] offset:11264\n\t" "v_smfmac_f32_16x16x64_bf16 v[236:239], v[20:23], v[176:183], %[ix]\n\t" "s_waitcnt lgkmcnt(4)\n\t" "s_barrier\n\t" \
    XT(88, 89) "v_smfmac_f32_16x16x64_bf16 v[232:235], v[24:27], a[72:79], %[ix]\n\t" "ds_read_b128 v[176:179], %[l0] offset:12288\n\t" \
    "ds_read_b128 v[180:183], %[l0] offset:13312\n\t" "v_smfmac_f32_16x16x64_bf16 v[236:239], v[24:27], a[80:87], %[ix]\n\t" "ds_read_b128 v[248:251], %[l1] offset:2048\n\t" \
    "ds_read_b128 v[252:255], %[l1] offset:6144\n\t" "v_smfmac_f32_16x16x64_bf16 v[232:235], v[28:31], a[88:95], %[ix]\n\t" "ds_read_b128 v[16:19], %[l1] offset:10240\n\t" \
    "ds_read_b128 v[20:23], %[l1] offset:3072\n\t" "v_smfmac_f32_16x16x64_bf16 v[236:239], v[28:31], v[184:191], %[ix]\n\t" "ds_read_b128 v[24:27], %[l1] offset:7168\n\t" \
    "ds_read_b128 v[184:187], %[l0] offset:14336\n\t" "v_smfmac_f32_16x16x64_bf16 v[232:235], v[240:243], a[96:103], %[ix]\n\t" \
    "ds_read_b128 v[188:191], %[l0] offset:15360\n\t" "ds_read_b128 v[28:31], %[l1] offset:11264\n\t" \
    "v_smfmac_f32_16x16x64_bf16 v[236:239], v[240:243], a[104:111], %[ix]\n\t" "v_smfmac_f32_16x16x64_bf16 v[232:235], v[244:247], a[112:119], %[ix]\n\t" \
    "s_waitcnt lgkmcnt(12)\n\t" "v_smfmac_f32_16x16x64_bf16 v[236:239], v[244:247], v[160:167], %[ix]\n\t" "ds_read_b128 v[160:163], %[l0] offset:16384\n\t" \
    "ds_read_b128 v[164:167], %[l0] offset:17408\n\t" "s_waitcnt lgkmcnt(9)\n\t" "v_smfmac_f32_16x16x64_bf16 v[232:235], v[248:251], a[120:127], %[ix]\n\t" \
    "v_smfmac_f32_16x16x64_bf16 v[236:239], v[248:251], a[128:135], %[ix]\n\t" "s_waitcnt lgkmcnt(8)\n\t" \
    "v_smfmac_f32_16x16x64_bf16 v[232:235], v[252:255], a[136:143], %[ix]\n\t" "v_smfmac_f32_16x16x64_bf16 v[236:239], v[252:255], v[168:175], %[ix]\n\t" \
    "ds_read_b128 v[168:171], %[l0] offset:18432\n\t" "ds_read_b128 v[172:175], %[l0] offset:19456\n\t" "s_waitcnt lgkmcnt(9)\n\t" \
    "v_smfmac_f32_16x16x64_bf16 v[232:235], v[16:19], a[144:151], %[ix]\n\t" "v_smfmac_f32_16x16x64_bf16 v[236:239], v[16:19], a[152:159], %[ix]\n\t" \
    "s_waitcnt lgkmcnt(8)\n\t" "v_smfmac_f32_16x16x64_bf16 v[232:235], v[20:23], a[160:167], %[ix]\n\t" \
    "v_smfmac_f32_16x16x64_bf16 v[236:239], v[20:23], v[176:183], %[ix]\n\t" "ds_read_b128 v[176:179], %[l0] offset:20480\n\t" \
    "ds_read_b128 v[180:183], %[l0] offset:21504\n\t" "s_waitcnt lgkmcnt(9)\n\t" "v_smfmac_f32_16x16x64_bf16 v[232:235], v[24:27], a[168:175], %[ix]\n\t" \
    "v_smfmac_f32_16x16x64_bf16 v[236:239], v[24:27], a[176:183], %[ix]\n\t" "s_waitcnt lgkmcnt(6)\n\t" \
    "v_smfmac_f32_16x16x64_bf16 v[232:235], v[28:31], a[184:191], %[ix]\n\t" "v_smfmac_f32_16x16x64_bf16 v[236:239], v[28:31], v[184:191], %[ix]\n\t" \
    "ds_read_b128 v[184:187], %[l0] offset:22528\n\t" "ds_read_b128 v[188:191], %[l0] offset:23552\n\t" XT(90, 91) "s_nop 4\n\t" "v_add_f32 v232, v232, v233\n\t" \
    "v_add_f32 v234, v234, v235\n\t" "v_add_f32 v236, v236, v237\n\t" "v_add_f32 v238, v238, v239\n\t" "v_cvt_pk_bf16_f32 v232, v232, v236\n\t" \
    "v_cvt_pk_bf16_f32 v233, v234, v238\n\t" "ds_write2_b32 %[vw], v232, v233 offset1:68\n\t" "s_waitcnt lgkmcnt(0)\n\t" "s_barrier\n\t" XT(92, 93) \
    "ds_read_b128 v[240:243], %[vr] offset:0\n\t" "ds_read_b128 v[244:247], %[vr] offset:64\n\t" "ds_read_b128 v[248:251], %[vr] offset:128\n\t" \
    "ds_read_b128 v[252:255], %[vr] offset:192\n\t" "s_mov_b32 s61, %[ub]\n\t" "s_waitcnt lgkmcnt(3)\n\t" \
    "v_smfmac_f32_16x16x64_bf16 v[192:195], v[240:243], v[32:39], %[ix]\n\t" "v_smfmac_f32_16x16x64_bf16 v[196:199], v[240:243], v[48:55], %[ix]\n\t" \
    "s_waitcnt lgkmcnt(2)\n\t" "v_smfmac_f32_16x16x64_bf16 v[192:195], v[244:247], v[40:47], %[ix]\n\t" \
    "v_smfmac_f32_16x16x64_bf16 v[196:199], v[244:247], v[56:63], %[ix]\n\t" "s_waitcnt lgkmcnt(1)\n\t" \
    "v_smfmac_f32_16x16x64_bf16 v[192:195], v[248:251], a[192:199], %[ix]\n\t" "v_smfmac_f32_16x16x64_bf16 v[196:199], v[248:251], a[200:207], %[ix]\n\t" \
    "s_waitcnt lgkmcnt(0)\n\t" "v_smfmac_f32_16x16x64_bf16 v[192:195], v[252:255], v[160:167], %[ix]\n\t" "ds_read_b128 v[160:163], %[l0] offset:24576\n\t" \
    "ds_read_b128 v[164:167], %[l0] offset:25600\n\t" "v_smfmac_f32_16x16x64_bf16 v[196:199], v[252:255], v[168:175], %[ix]\n\t" \
    "ds_read_b128 v[168:171], %[l0] offset:26624\n\t" "ds_read_b128 v[172:175], %[l0] offset:27648\n\t" \
    "v_smfmac_f32_16x16x64_bf16 v[200:203], v[240:243], v[64:71], %[ix]\n\t" "s_nop 1\n\t" "v_smfmac_f32_16x16x64_bf16 v[204:207], v[240:243], v[80:87], %[ix]\n\t" \
    "v_cvt_pk_bf16_f32 v224, v192, v194\n\t" "v_cvt_pk_bf16_f32 v225, v193, v195\n\t" "v_smfmac_f32_16x16x64_bf16 v[200:203], v[244:247], v[72:79], %[ix]\n\t" \
    "v_cvt_pk_bf16_f32 v226, v196, v198\n\t" "v_cvt_pk_bf16_f32 v227, v197, v199\n\t" "v_smfmac_f32_16x16x64_bf16 v[204:207], v[244:247], v[88:95], %[ix]\n\t" "s_nop 0\n\t" \
    "v_permlane32_swap_b32 v224, v226\n\t" "v_smfmac_f32_16x16x64_bf16 v[200:203], v[248:251], a[208:215], %[ix]\n\t" "v_permlane32_swap_b32 v225, v227\n\t" "s_nop 0\n\t" \
    "v_smfmac_f32_16x16x64_bf16 v[204:207], v[248:251], a[216:223], %[ix]\n\t" "v_permlane16_swap_b32 v224, v225\n\t" "v_permlane16_swap_b32 v226, v227\n\t" \
    "v_smfmac_f32_16x16x64_bf16 v[200:203], v[252:255], v[176:183], %[ix]\n\t" "buffer_store_dwordx4 v[224:227], %[uo], %[rs], s61 offen sc1\n\t" \
    "s_add_u32 s61, s61, 0x10000\n\t" XT(94, 95) "v_smfmac_f32_16x16x64_bf16 v[204:207], v[252:255], v[184:191], %[ix]\n\t" "ds_read_b128 v[176:179], %[l0] offset:28672\n\t" \
    "ds_read_b128 v[180:183], %[l0] offset:29696\n\t" "v_smfmac_f32_16x16x64_bf16 v[208:211], v[240:243], v[96:103], %[ix]\n\t" \
    "ds_read_b128 v[184:187], %[l0] offset:30720\n\t" "ds_read_b128 v[188:191], %[l0] offset:31744\n\t" \
    "v_smfmac_f32_16x16x64_bf16 v[212:215], v[240:243], v[112:119], %[ix]\n\t" "v_cvt_pk_bf16_f32 v228, v200, v202\n\t" "v_cvt_pk_bf16_f32 v229, v201, v203\n\t" \
    "v_smfmac_f32_16x16x64_bf16 v[208:211], v[244:247], v[104:111], %[ix]\n\t" "v_cvt_pk_bf16_f32 v230, v204, v206\n\t" "v_cvt_pk_bf16_f32 v231, v205, v207\n\t" \
    "v_smfmac_f32_16x16x64_bf16 v[212:215], v[244:247], v[120:127], %[ix]\n\t" "s_nop 0\n\t" "v_permlane32_swap_b32 v228, v230\n\t" \
    "v_smfmac_f32_16x16x64_bf16 v[208:211], v[248:251], a[224:231], %[ix]\n\t" "v_permlane32_swap_b32 v229, v231\n\t" "s_nop 0\n\t" \
    "v_smfmac_f32_16x16x64_bf16 v[212:215], v[248:251], a[232:239], %[ix]\n\t" "v_permlane16_swap_b32 v228, v229\n\t" "v_permlane16_swap_b32 v230, v231\n\t" \
    "s_waitcnt lgkmcnt(6)\n\t" "v_smfmac_f32_16x16x64_bf16 v[208:211], v[252:255], v[160:167], %[ix]\n\t" "buffer_store_dwordx4 v[228:231], %[uo], %[rs], s61 offen sc1\n\t" \
    "s_add_u32 s61, s61, 0x10000\n\t" "s_waitcnt lgkmcnt(4)\n\t" "v_smfmac_f32_16x16x64_bf16 v[212:215], v[252:255], v[168:175], %[ix]\n\t" "s_nop 1\n\t" \
    "v_smfmac_f32_16x16x64_bf16 v[216:219], v[240:243], v[128:135], %[ix]\n\t" "s_nop 1\n\t" "v_smfmac_f32_16x16x64_bf16 v[220:223], v[240:243], v[144:151], %[ix]\n\t" \
    "v_cvt_pk_bf16_f32 v224, v208, v210\n\t" "v_cvt_pk_bf16_f32 v225, v209, v211\n\t" "v_smfmac_f32_16x16x64_bf16 v[216:219], v[244:247], v[136:143], %[ix]\n\t" \
    "v_cvt_pk_bf16_f32 v226, v212, v214\n\t" "v_cvt_pk_bf16_f32 v227, v213, v215\n\t" "v_smfmac_f32_16x16x64_bf16 v[220:223], v[244:247], v[152:159], %[ix]\n\t" "s_nop 0\n\t" \
    "v_permlane32_swap_b32 v224, v226\n\t" "v_smfmac_f32_16x16x64_bf16 v[216:219], v[248:251], a[240:247], %[ix]\n\t" "v_permlane32_swap_b32 v225, v227\n\t" "s_nop 0\n\t" \
    "v_smfmac_f32_16x16x64_bf16 v[220:223], v[248:251], a[248:255], %[ix]\n\t" "v_permlane16_swap_b32 v224, v225\n\t" "v_permlane16_swap_b32 v226, v227\n\t" \
    "s_waitcnt lgkmcnt(2)\n\t" "v_smfmac_f32_16x16x64_bf16 v[216:219], v[252:255], v[176:183], %[ix]\n\t" "buffer_store_dwordx4 v[224:227], %[uo], %[rs], s61 offen sc1\n\t" \
    "s_add_u32 s61, s61, 0x10000\n\t" "s_waitcnt lgkmcnt(0)\n\t" "v_smfmac_f32_16x16x64_bf16 v[220:223], v[252:255], v[184:191], %[ix]\n\t" "s_nop 5\n\t" \
    "v_cvt_pk_bf16_f32 v228, v216, v218\n\t" "v_cvt_pk_bf16_f32 v229, v217, v219\n\t" "v_cvt_pk_bf16_f32 v230, v220, v222\n\t" "v_cvt_pk_bf16_f32 v231, v221, v223\n\t" \
    "s_nop 0\n\t" "v_permlane32_swap_b32 v228, v230\n\t" "v_permlane32_swap_b32 v229, v231\n\t" "s_nop 1\n\t" "v_permlane16_swap_b32 v228, v229\n\t" \
    "v_permlane16_swap_b32 v230, v231\n\t" "buffer_store_dwordx4 v[228:231], %[uo], %[rs], s61 offen sc1\n\t" "s_nop 1\n\t" XT(96, 97) "s_branch 99f\n\t" "41:\n\t" \
    "s_cmp_eq_u32 %[bd], 0\n\t" "s_cbranch_scc1 46f\n\t" "s_sub_u32 %[bd], %[bd], 1\n\t" XCNT(100) "s_waitcnt lgkmcnt(0)\n\t" "s_nop 7\n\t" "s_branch 40b\n\t" "46:\n\t" \
    "s_cmp_lg_u32 s60, 0\n\t" "s_branch 42b\n\t" "44:\n\t" "s_cmp_eq_u32 %[bd], 0\n\t" "s_cbranch_scc1 45b\n\t" "s_sub_u32 %[bd], %[bd], 1\n\t" XCNT(101) \
    "s_mov_b32 s60, 1\n\t" "s_waitcnt lgkmcnt(0)\n\t" "s_nop 7\n\t" "s_branch 40b\n\t" "99:\n\t"
#define Y_STEP \
    "s_getpc_b64 s[62:63]\n\t" "11:\n\t" "s_memtime s[64:65]\n\t" "s_waitcnt lgkmcnt(0)\n\t" "s_sub_u32 s61, %[tu], s64\n\t" "s_cmp_gt_i32 s61, 124\n\t" \
    "s_cbranch_scc1 11b\n\t" "s_max_i32 s61, s61, 0\n\t" "s_and_b32 s61, s61, 124\n\t" "s_sub_u32 s61, 12f-11b, s61\n\t" "s_add_u32 s62, s62, s61\n\t" \
    "s_addc_u32 s63, s63, 0\n\t" "s_setpc_b64 s[62:63]\n\t" ".rept 31\n\t" "s_nop 0\n\t" ".endr\n\t" "12:\n\t" XT(90, 91) \
    "buffer_load_dwordx4 v[144:147], %[u0], %[rs], %[ub] offen offset:0 sc1\n\t" "buffer_load_dwordx4 v[148:151], %[u0], %[rs], %[ub] offen offset:1024 sc1\n\t" \
    "buffer_load_dwordx4 v[152:155], %[u0], %[rs], %[ub] offen offset:2048 sc1\n\t" "buffer_load_dwordx4 v[156:159], %[u0], %[rs], %[ub] offen offset:3072 sc1\n\t" \
    "buffer_load_dwordx4 v[160:163], %[u1], %[rs], %[ub] offen offset:0 sc1\n\t" "buffer_load_dwordx4 v[164:167], %[u1], %[rs], %[ub] offen offset:1024 sc1\n\t" \
    "buffer_load_dwordx4 v[168:171], %[u1], %[rs], %[ub] offen offset:2048 sc1\n\t" "buffer_load_dwordx4 v[172:175], %[u1], %[rs], %[ub] offen offset:3072 sc1\n\t" \
    "s_cmp_lg_u32 %[late], 0\n\t" "s_cbranch_scc1 14f\n\t" "s_getpc_b64 s[62:63]\n\t" "13:\n\t" "s_memtime s[64:65]\n\t" "s_waitcnt lgkmcnt(0)\n\t" \
    "s_sub_u32 s61, %[tg], s64\n\t" "s_cmp_gt_i32 s61, 124\n\t" "s_cbranch_scc1 13b\n\t" "s_max_i32 s61, s61, 0\n\t" "s_and_b32 s61, s61, 124\n\t" \
    "s_sub_u32 s61, 14f-13b, s61\n\t" "s_add_u32 s62, s62, s61\n\t" "s_addc_u32 s63, s63, 0\n\t" "s_setpc_b64 s[62:63]\n\t" ".rept 31\n\t" "s_nop 0\n\t" ".endr\n\t" "14:\n\t" \
    XT(92, 93) "20:\n\t" "buffer_load_dwordx4 v[192:195], %[x0], %[rs], %[pb] offen offset:0 sc1\n\t" \
    "buffer_load_dwordx4 v[196:199], %[x0], %[rs], %[pb] offen offset:1024 sc1\n\t" "buffer_load_dwordx4 v[200:203], %[x0], %[rs], %[pb] offen offset:2048 sc1\n\t" \
    "buffer_load_dwordx4 v[204:207], %[x0], %[rs], %[pb] offen offset:3072 sc1\n\t" "buffer_load_dwordx4 v[208:211], %[x1], %[rs], %[pb] offen offset:0 sc1\n\t" \
    "buffer_load_dwordx4 v[212:215], %[x1], %[rs], %[pb] offen offset:1024 sc1\n\t" "buffer_load_dwordx4 v[216:219], %[x1], %[rs], %[pb] offen offset:2048 sc1\n\t" \
    "buffer_load_dwordx4 v[220:223], %[x1], %[rs], %[pb] offen offset:3072 sc1\n\t" "buffer_load_dwordx4 v[224:227], %[x2], %[rs], %[pb] offen offset:0 sc1\n\t" \
    "buffer_load_dwordx4 v[228:231], %[x2], %[rs], %[pb] offen offset:1024 sc1\n\t" "buffer_load_dwordx4 v[232:235], %[x2], %[rs], %[pb] offen offset:2048 sc1\n\t" \
    "buffer_load_dwordx4 v[236:239], %[x2], %[rs], %[pb] offen offset:3072 sc1\n\t" "buffer_load_dwordx4 v[240:243], %[x3], %[rs], %[pb] offen offset:0 sc1\n\t" \
    "buffer_load_dwordx4 v[244:247], %[x3], %[rs], %[pb] offen offset:1024 sc1\n\t" "buffer_load_dwordx4 v[248:251], %[x3], %[rs], %[pb] offen offset:2048 sc1\n\t" \
    "buffer_load_dwordx4 v[252:255], %[x3], %[rs], %[pb] offen offset:3072 sc1\n\t" "v_mov_b64 v[120:121], 0\n\t" "v_mov_b64 v[122:123], 0\n\t" \
    "buffer_load_dwordx2 v[176:177], %[pm], %[rs], %[pb] offen offset:0 sc1\n\t" "buffer_load_dwordx2 v[178:179], %[pm], %[rs], %[pb] offen offset:128 sc1\n\t" \
    "buffer_load_dwordx2 v[180:181], %[pm], %[rs], %[pb] offen offset:1024 sc1\n\t" "buffer_load_dwordx2 v[182:183], %[pm], %[rs], %[pb] offen offset:1152 sc1\n\t" \
    "buffer_load_dwordx2 v[184:185], %[pm], %[rs], %[pb] offen offset:2048 sc1\n\t" "buffer_load_dwordx2 v[186:187], %[pm], %[rs], %[pb] offen offset:2176 sc1\n\t" \
    "buffer_load_dwordx2 v[188:189], %[pm], %[rs], %[pb] offen offset:3072 sc1\n\t" "buffer_load_dwordx2 v[190:191], %[pm], %[rs], %[pb] offen offset:3200 sc1\n\t" \
    "buffer_load_dword %[sv], %[so], %[rs], %[pb] offen sc1\n\t" "v_mov_b32 v127, -1\n\t" "s_waitcnt vmcnt(24)\n\t" XT(94, 95) \
    "v_smfmac_f32_16x16x64_bf16 v[120:123], v[192:195], a[0:7], %[ix]\n\t" "s_waitcnt vmcnt(23)\n\t" "v_smfmac_f32_16x16x64_bf16 v[120:123], v[196:199], a[8:15], %[ix]\n\t" \
    "s_waitcnt vmcnt(22)\n\t" "v_smfmac_f32_16x16x64_bf16 v[120:123], v[200:203], a[16:23], %[ix]\n\t" "s_waitcnt vmcnt(21)\n\t" \
    "v_smfmac_f32_16x16x64_bf16 v[120:123], v[204:207], a[24:31], %[ix]\n\t" "s_waitcnt vmcnt(20)\n\t" \
    "v_smfmac_f32_16x16x64_bf16 v[120:123], v[208:211], a[32:39], %[ix]\n\t" "s_waitcnt vmcnt(19)\n\t" \
    "v_smfmac_f32_16x16x64_bf16 v[120:123], v[212:215], a[40:47], %[ix]\n\t" "s_waitcnt vmcnt(18)\n\t" \
    "v_smfmac_f32_16x16x64_bf16 v[120:123], v[216:219], a[48:55], %[ix]\n\t" "s_waitcnt vmcnt(17)\n\t" \
    "v_smfmac_f32_16x16x64_bf16 v[120:123], v[220:223], a[56:63], %[ix]\n\t" "s_waitcnt vmcnt(16)\n\t" \
    "v_smfmac_f32_16x16x64_bf16 v[120:123], v[224:227], a[64:71], %[ix]\n\t" "s_waitcnt vmcnt(15)\n\t" \
    "v_smfmac_f32_16x16x64_bf16 v[120:123], v[228:231], a[72:79], %[ix]\n\t" "s_waitcnt vmcnt(14)\n\t" \
    "v_smfmac_f32_16x16x64_bf16 v[120:123], v[232:235], a[80:87], %[ix]\n\t" "s_waitcnt vmcnt(13)\n\t" \
    "v_smfmac_f32_16x16x64_bf16 v[120:123], v[236:239], a[88:95], %[ix]\n\t" "s_waitcnt vmcnt(12)\n\t" \
    "v_smfmac_f32_16x16x64_bf16 v[120:123], v[240:243], a[96:103], %[ix]\n\t" "s_waitcnt vmcnt(11)\n\t" \
    "v_smfmac_f32_16x16x64_bf16 v[120:123], v[244:247], a[104:111], %[ix]\n\t" "s_waitcnt vmcnt(10)\n\t" \
    "v_smfmac_f32_16x16x64_bf16 v[120:123], v[248:251], a[112:119], %[ix]\n\t" "s_waitcnt vmcnt(9)\n\t" \
    "v_smfmac_f32_16x16x64_bf16 v[120:123], v[252:255], a[120:127], %[ix]\n\t" "s_waitcnt vmcnt(7)\n\t" \
    "v_smfmac_f32_16x16x64_bf16 v[120:123], v[176:179], v[144:151], %[ix]\n\t" "s_waitcnt vmcnt(5)\n\t" \
    "v_smfmac_f32_16x16x64_bf16 v[120:123], v[180:183], v[152:159], %[ix]\n\t" "s_waitcnt vmcnt(3)\n\t" \
    "v_smfmac_f32_16x16x64_bf16 v[120:123], v[184:187], v[160:167], %[ix]\n\t" "s_waitcnt vmcnt(1)\n\t" \
    "v_smfmac_f32_16x16x64_bf16 v[120:123], v[188:191], v[168:175], %[ix]\n\t" XT(82, 83) "s_nop 7\n\t" "v_add_f32 v120, v120, v121\n\t" "v_add_f32 v122, v122, v123\n\t" \
    "v_cvt_pk_bf16_f32 v124, v120, v122\n\t" "v_cmp_u_f32 vcc, v120, v122\n\t" "s_cbranch_vccnz 30f\n\t" "21:\n\t" \
    "v_mov_b32_dpp v125, v124 quad_perm:[1,0,3,2] row_mask:0xf bank_mask:0xf\n\t" "v_perm_b32 v124, v125, v124, %[ps]\n\t" "v_min_u32 v124, -2, v124\n\t" \
    "buffer_store_dword v124, %[mw], %[rs], %[mb] offen sc1\n\t" XT(84, 85) "buffer_store_dword v127, %[mw], %[rs], %[mr] offen sc1\n\t" "s_memtime s[64:65]\n\t" \
    "s_barrier\n\t" XT(98, 99) "s_waitcnt lgkmcnt(0)\n\t" "s_and_b32 %[bad], s64, -2\n\t" "s_branch 40f\n\t" "30:\n\t" "v_max_u32 v126, v144, v145\n\t" \
    "v_max3_u32 v126, v126, v146, v147\n\t" "v_max3_u32 v126, v126, v148, v149\n\t" "v_max3_u32 v126, v126, v150, v151\n\t" "v_max3_u32 v126, v126, v152, v153\n\t" \
    "v_max3_u32 v126, v126, v154, v155\n\t" "v_max3_u32 v126, v126, v156, v157\n\t" "v_max3_u32 v126, v126, v158, v159\n\t" "v_max3_u32 v126, v126, v160, v161\n\t" \
    "v_max3_u32 v126, v126, v162, v163\n\t" "v_max3_u32 v126, v126, v164, v165\n\t" "v_max3_u32 v126, v126, v166, v167\n\t" "v_max3_u32 v126, v126, v168, v169\n\t" \
    "v_max3_u32 v126, v126, v170, v171\n\t" "v_max3_u32 v126, v126, v172, v173\n\t" "v_max3_u32 v126, v126, v174, v175\n\t" UCNT "v_max3_u32 v126, v126, v176, v177\n\t" \
    "v_max3_u32 v126, v126, v178, v179\n\t" "v_max3_u32 v126, v126, v180, v181\n\t" "v_max3_u32 v126, v126, v182, v183\n\t" "v_max3_u32 v126, v126, v184, v185\n\t" \
    "v_max3_u32 v126, v126, v186, v187\n\t" "v_max3_u32 v126, v126, v188, v189\n\t" "v_max3_u32 v126, v126, v190, v191\n\t" "v_max3_u32 v126, v126, v192, v193\n\t" \
    "v_max3_u32 v126, v126, v194, v195\n\t" "v_max3_u32 v126, v126, v196, v197\n\t" "v_max3_u32 v126, v126, v198, v199\n\t" "v_max3_u32 v126, v126, v200, v201\n\t" \
    "v_max3_u32 v126, v126, v202, v203\n\t" "v_max3_u32 v126, v126, v204, v205\n\t" "v_max3_u32 v126, v126, v206, v207\n\t" "v_max3_u32 v126, v126, v208, v209\n\t" \
    "v_max3_u32 v126, v126, v210, v211\n\t" "v_max3_u32 v126, v126, v212, v213\n\t" "v_max3_u32 v126, v126, v214, v215\n\t" "v_max3_u32 v126, v126, v216, v217\n\t" \
    "v_max3_u32 v126, v126, v218, v219\n\t" "v_max3_u32 v126, v126, v220, v221\n\t" "v_max3_u32 v126, v126, v222, v223\n\t" "v_max3_u32 v126, v126, v224, v225\n\t" \
    "v_max3_u32 v126, v126, v226, v227\n\t" "v_max3_u32 v126, v126, v228, v229\n\t" "v_max3_u32 v126, v126, v230, v231\n\t" "v_max3_u32 v126, v126, v232, v233\n\t" \
    "v_max3_u32 v126, v126, v234, v235\n\t" "v_max3_u32 v126, v126, v236, v237\n\t" "v_max3_u32 v126, v126, v238, v239\n\t" "v_max3_u32 v126, v126, v240, v241\n\t" \
    "v_max3_u32 v126, v126, v242, v243\n\t" "v_max3_u32 v126, v126, v244, v245\n\t" "v_max3_u32 v126, v126, v246, v247\n\t" "v_max3_u32 v126, v126, v248, v249\n\t" \
    "v_max3_u32 v126, v126, v250, v251\n\t" "v_max3_u32 v126, v126, v252, v253\n\t" "v_max3_u32 v126, v126, v254, v255\n\t" "v_cmp_eq_u32 vcc, -1, v126\n\t" \
    "s_cbranch_vccz 21b\n\t" "s_cmp_eq_u32 %[bd], 0\n\t" "s_cbranch_scc1 21b\n\t" XCNT(100) "s_mov_b32 %[bad], 1\n\t" "s_waitcnt vmcnt(0)\n\t" "40:\n\t"
#define G_LOOP \
    "v_xor_b32 v72, 0, %[xo]\n\t" "v_add_u32 v80, 0x10000, v72\n\t" "v_xor_b32 v88, 0, %[xs]\n\t" "v_add_u32 v88, 0, v88\n\t" "v_xor_b32 v73, 32, %[xo]\n\t" \
    "v_add_u32 v81, 0x10000, v73\n\t" "v_xor_b32 v89, 16, %[xs]\n\t" "v_add_u32 v89, 2048, v89\n\t" "v_xor_b32 v74, 64, %[xo]\n\t" "v_add_u32 v82, 0x10000, v74\n\t" \
    "v_xor_b32 v90, 32, %[xs]\n\t" "v_add_u32 v90, 4096, v90\n\t" "v_xor_b32 v75, 96, %[xo]\n\t" "v_add_u32 v83, 0x10000, v75\n\t" "v_xor_b32 v91, 48, %[xs]\n\t" \
    "v_add_u32 v91, 6144, v91\n\t" "v_xor_b32 v76, 128, %[xo]\n\t" "v_add_u32 v84, 0x10000, v76\n\t" "v_xor_b32 v92, 64, %[xs]\n\t" "v_add_u32 v92, 8192, v92\n\t" \
    "v_xor_b32 v77, 160, %[xo]\n\t" "v_add_u32 v85, 0x10000, v77\n\t" "v_xor_b32 v93, 80, %[xs]\n\t" "v_add_u32 v93, 10240, v93\n\t" "v_xor_b32 v78, 192, %[xo]\n\t" \
    "v_add_u32 v86, 0x10000, v78\n\t" "v_xor_b32 v94, 96, %[xs]\n\t" "v_add_u32 v94, 12288, v94\n\t" "v_xor_b32 v79, 224, %[xo]\n\t" "v_add_u32 v87, 0x10000, v79\n\t" \
    "v_xor_b32 v95, 112, %[xs]\n\t" "v_add_u32 v95, 14336, v95\n\t" "v_mov_b32 v96, 2.0\n\t" "s_mov_b32 s60, 0\n\t" "s_mov_b32 s62, 0\n\t" "s_waitcnt vmcnt(0)\n\t" \
    "s_barrier\n\t" "ds_read_b128 v[100:103], v72 offset:0\n\t" "ds_read_b128 v[104:107], v73 offset:0\n\t" "ds_read_b128 v[108:111], v74 offset:0\n\t" \
    "ds_read_b128 v[112:115], v75 offset:0\n\t" "ds_read_b128 v[116:119], v76 offset:0\n\t" "10:\n\t" XT(80, 81) "s_waitcnt lgkmcnt(4)\n\t" \
    "v_mfma_f32_32x32x16_bf16 v[32:47], a[0:3], v[100:103], 0\n\t" "ds_read_b128 v[120:123], v77 offset:0\n\t" "s_add_u32 s61, s60, 1\n\t" "s_and_b32 s61, s61, 63\n\t" \
    "s_lshl_b32 s61, s61, 16\n\t" "s_add_u32 s63, %[lw], 65536\n\t" "s_waitcnt lgkmcnt(4)\n\t" "v_mfma_f32_32x32x16_bf16 v[32:47], a[4:7], v[104:107], v[32:47]\n\t" \
    "ds_read_b128 v[124:127], v78 offset:0\n\t" "s_mov_b32 m0, s63\n\t" "s_add_u32 s63, s63, 0x800\n\t" "buffer_load_dwordx4 v88, %[rx], s61 offen lds\n\t" \
    "s_waitcnt lgkmcnt(4)\n\t" "v_mfma_f32_32x32x16_bf16 v[32:47], a[8:11], v[108:111], v[32:47]\n\t" "ds_read_b128 v[128:131], v79 offset:0\n\t" \
    "buffer_load_dwordx4 v88, %[rx], s61 offen offset:1024 lds\n\t" "s_waitcnt lgkmcnt(4)\n\t" "v_mfma_f32_32x32x16_bf16 v[32:47], a[12:15], v[112:115], v[32:47]\n\t" \
    "ds_read_b128 v[100:103], v72 offset:256\n\t" "s_mov_b32 m0, s63\n\t" "s_add_u32 s63, s63, 0x800\n\t" "buffer_load_dwordx4 v89, %[rx], s61 offen lds\n\t" \
    "s_waitcnt lgkmcnt(4)\n\t" "v_mfma_f32_32x32x16_bf16 v[32:47], a[16:19], v[116:119], v[32:47]\n\t" "ds_read_b128 v[104:107], v73 offset:256\n\t" \
    "buffer_load_dwordx4 v89, %[rx], s61 offen offset:1024 lds\n\t" "v_cvt_pk_bf16_f32 v64, v48, v49\n\t" "v_cvt_pk_bf16_f32 v65, v50, v51\n\t" "s_waitcnt lgkmcnt(4)\n\t" \
    "v_mfma_f32_32x32x16_bf16 v[32:47], a[20:23], v[120:123], v[32:47]\n\t" "ds_read_b128 v[108:111], v74 offset:256\n\t" "s_mov_b32 m0, s63\n\t" \
    "s_add_u32 s63, s63, 0x800\n\t" "buffer_load_dwordx4 v90, %[rx], s61 offen lds\n\t" "s_waitcnt lgkmcnt(4)\n\t" \
    "v_mfma_f32_32x32x16_bf16 v[32:47], a[24:27], v[124:127], v[32:47]\n\t" "ds_read_b128 v[112:115], v75 offset:256\n\t" \
    "buffer_load_dwordx4 v90, %[rx], s61 offen offset:1024 lds\n\t" "v_cvt_pk_bf16_f32 v66, v52, v53\n\t" "v_cvt_pk_bf16_f32 v67, v54, v55\n\t" "s_waitcnt lgkmcnt(4)\n\t" \
    "v_mfma_f32_32x32x16_bf16 v[32:47], a[28:31], v[128:131], v[32:47]\n\t" "ds_read_b128 v[116:119], v76 offset:256\n\t" "s_mov_b32 m0, s63\n\t" \
    "s_add_u32 s63, s63, 0x800\n\t" "buffer_load_dwordx4 v91, %[rx], s61 offen lds\n\t" "s_waitcnt lgkmcnt(4)\n\t" \
    "v_mfma_f32_32x32x16_bf16 v[32:47], a[32:35], v[100:103], v[32:47]\n\t" "ds_read_b128 v[120:123], v77 offset:256\n\t" \
    "buffer_load_dwordx4 v91, %[rx], s61 offen offset:1024 lds\n\t" "v_cvt_pk_bf16_f32 v68, v56, v57\n\t" "v_cvt_pk_bf16_f32 v69, v58, v59\n\t" "s_waitcnt lgkmcnt(4)\n\t" \
    "v_mfma_f32_32x32x16_bf16 v[32:47], a[36:39], v[104:107], v[32:47]\n\t" "ds_read_b128 v[124:127], v78 offset:256\n\t" "s_mov_b32 m0, s63\n\t" \
    "s_add_u32 s63, s63, 0x800\n\t" "buffer_load_dwordx4 v92, %[rx], s61 offen lds\n\t" "s_waitcnt lgkmcnt(4)\n\t" \
    "v_mfma_f32_32x32x16_bf16 v[32:47], a[40:43], v[108:111], v[32:47]\n\t" "ds_read_b128 v[128:131], v79 offset:256\n\t" \
    "buffer_load_dwordx4 v92, %[rx], s61 offen offset:1024 lds\n\t" "v_cvt_pk_bf16_f32 v70, v60, v61\n\t" "v_cvt_pk_bf16_f32 v71, v62, v63\n\t" "s_waitcnt lgkmcnt(4)\n\t" \
    "v_mfma_f32_32x32x16_bf16 v[32:47], a[44:47], v[112:115], v[32:47]\n\t" "ds_read_b128 v[100:103], v72 offset:512\n\t" "s_mov_b32 m0, s63\n\t" \
    "s_add_u32 s63, s63, 0x800\n\t" "buffer_load_dwordx4 v93, %[rx], s61 offen lds\n\t" "s_waitcnt lgkmcnt(4)\n\t" \
    "v_mfma_f32_32x32x16_bf16 v[32:47], a[48:51], v[116:119], v[32:47]\n\t" "ds_read_b128 v[104:107], v73 offset:512\n\t" \
    "buffer_load_dwordx4 v93, %[rx], s61 offen offset:1024 lds\n\t" "s_waitcnt lgkmcnt(4)\n\t" "v_mfma_f32_32x32x16_bf16 v[32:47], a[52:55], v[120:123], v[32:47]\n\t" \
    "ds_read_b128 v[108:111], v74 offset:512\n\t" "s_mov_b32 m0, s63\n\t" "s_add_u32 s63, s63, 0x800\n\t" "buffer_load_dwordx4 v94, %[rx], s61 offen lds\n\t" \
    "s_waitcnt lgkmcnt(4)\n\t" "v_mfma_f32_32x32x16_bf16 v[32:47], a[56:59], v[124:127], v[32:47]\n\t" "ds_read_b128 v[112:115], v75 offset:512\n\t" \
    "buffer_load_dwordx4 v94, %[rx], s61 offen offset:1024 lds\n\t" "s_waitcnt lgkmcnt(4)\n\t" "v_mfma_f32_32x32x16_bf16 v[32:47], a[60:63], v[128:131], v[32:47]\n\t" \
    "ds_read_b128 v[116:119], v76 offset:512\n\t" "s_mov_b32 m0, s63\n\t" "s_add_u32 s63, s63, 0x800\n\t" "buffer_load_dwordx4 v95, %[rx], s61 offen lds\n\t" \
    "s_waitcnt lgkmcnt(4)\n\t" "v_mfma_f32_32x32x16_bf16 v[32:47], a[64:67], v[100:103], v[32:47]\n\t" "ds_read_b128 v[120:123], v77 offset:512\n\t" \
    "buffer_load_dwordx4 v95, %[rx], s61 offen offset:1024 lds\n\t" "s_waitcnt lgkmcnt(4)\n\t" "v_mfma_f32_32x32x16_bf16 v[32:47], a[68:71], v[104:107], v[32:47]\n\t" \
    "ds_read_b128 v[124:127], v78 offset:512\n\t" "buffer_store_dwordx4 v[64:67], v96, %[rs], s62 offen offset:0 sc1\n\t" "s_waitcnt lgkmcnt(4)\n\t" \
    "v_mfma_f32_32x32x16_bf16 v[32:47], a[72:75], v[108:111], v[32:47]\n\t" "ds_read_b128 v[128:131], v79 offset:512\n\t" "s_waitcnt lgkmcnt(4)\n\t" \
    "v_mfma_f32_32x32x16_bf16 v[32:47], a[76:79], v[112:115], v[32:47]\n\t" "ds_read_b128 v[100:103], v72 offset:768\n\t" \
    "buffer_store_dwordx4 v[68:71], v96, %[rs], s62 offen offset:1024 sc1\n\t" "s_waitcnt lgkmcnt(4)\n\t" \
    "v_mfma_f32_32x32x16_bf16 v[32:47], a[80:83], v[116:119], v[32:47]\n\t" "ds_read_b128 v[104:107], v73 offset:768\n\t" "s_waitcnt lgkmcnt(4)\n\t" \
    "v_mfma_f32_32x32x16_bf16 v[32:47], a[84:87], v[120:123], v[32:47]\n\t" "ds_read_b128 v[108:111], v74 offset:768\n\t" "s_waitcnt lgkmcnt(4)\n\t" \
    "v_mfma_f32_32x32x16_bf16 v[32:47], a[88:91], v[124:127], v[32:47]\n\t" "ds_read_b128 v[112:115], v75 offset:768\n\t" "s_waitcnt lgkmcnt(4)\n\t" \
    "v_mfma_f32_32x32x16_bf16 v[32:47], a[92:95], v[128:131], v[32:47]\n\t" "ds_read_b128 v[116:119], v76 offset:768\n\t" "s_waitcnt lgkmcnt(4)\n\t" \
    "v_mfma_f32_32x32x16_bf16 v[32:47], a[96:99], v[100:103], v[32:47]\n\t" "ds_read_b128 v[120:123], v77 offset:768\n\t" "s_lshr_b32 s64, s60, 2\n\t" \
    "s_xor_b32 s64, s64, %[xp]\n\t" "s_and_b32 s64, s64, 1\n\t" "s_lshl_b32 s64, s64, 13\n\t" "s_waitcnt lgkmcnt(4)\n\t" \
    "v_mfma_f32_32x32x16_bf16 v[32:47], a[100:103], v[104:107], v[32:47]\n\t" "ds_read_b128 v[124:127], v78 offset:768\n\t" "s_lshl_b32 s62, s60, 19\n\t" \
    "s_add_u32 s62, s62, s64\n\t" "s_add_u32 s62, s62, %[ob]\n\t" "s_waitcnt lgkmcnt(4)\n\t" "v_mfma_f32_32x32x16_bf16 v[32:47], a[104:107], v[108:111], v[32:47]\n\t" \
    "ds_read_b128 v[128:131], v79 offset:768\n\t" "s_waitcnt lgkmcnt(4)\n\t" "v_mfma_f32_32x32x16_bf16 v[32:47], a[108:111], v[112:115], v[32:47]\n\t" \
    "ds_read_b128 v[100:103], v72 offset:1024\n\t" "s_waitcnt lgkmcnt(4)\n\t" "v_mfma_f32_32x32x16_bf16 v[32:47], a[112:115], v[116:119], v[32:47]\n\t" \
    "ds_read_b128 v[104:107], v73 offset:1024\n\t" "s_waitcnt lgkmcnt(4)\n\t" "v_mfma_f32_32x32x16_bf16 v[32:47], a[116:119], v[120:123], v[32:47]\n\t" \
    "ds_read_b128 v[108:111], v74 offset:1024\n\t" "s_waitcnt lgkmcnt(4)\n\t" "v_mfma_f32_32x32x16_bf16 v[32:47], a[120:123], v[124:127], v[32:47]\n\t" \
    "ds_read_b128 v[112:115], v75 offset:1024\n\t" "s_waitcnt lgkmcnt(4)\n\t" "v_mfma_f32_32x32x16_bf16 v[32:47], a[124:127], v[128:131], v[32:47]\n\t" \
    "ds_read_b128 v[116:119], v76 offset:1024\n\t" "s_waitcnt lgkmcnt(4)\n\t" "v_mfma_f32_32x32x16_bf16 v[32:47], a[128:131], v[100:103], v[32:47]\n\t" \
    "ds_read_b128 v[120:123], v77 offset:1024\n\t" "s_waitcnt lgkmcnt(4)\n\t" "v_mfma_f32_32x32x16_bf16 v[32:47], a[132:135], v[104:107], v[32:47]\n\t" \
    "ds_read_b128 v[124:127], v78 offset:1024\n\t" "s_waitcnt lgkmcnt(4)\n\t" "v_mfma_f32_32x32x16_bf16 v[32:47], a[136:139], v[108:111], v[32:47]\n\t" \
    "ds_read_b128 v[128:131], v79 offset:1024\n\t" "s_waitcnt lgkmcnt(4)\n\t" "v_mfma_f32_32x32x16_bf16 v[32:47], a[140:143], v[112:115], v[32:47]\n\t" \
    "ds_read_b128 v[100:103], v72 offset:1280\n\t" "s_waitcnt lgkmcnt(4)\n\t" "v_mfma_f32_32x32x16_bf16 v[32:47], a[144:147], v[116:119], v[32:47]\n\t" \
    "ds_read_b128 v[104:107], v73 offset:1280\n\t" "s_waitcnt lgkmcnt(4)\n\t" "v_mfma_f32_32x32x16_bf16 v[32:47], a[148:151], v[120:123], v[32:47]\n\t" \
    "ds_read_b128 v[108:111], v74 offset:1280\n\t" "s_waitcnt lgkmcnt(4)\n\t" "v_mfma_f32_32x32x16_bf16 v[32:47], a[152:155], v[124:127], v[32:47]\n\t" \
    "ds_read_b128 v[112:115], v75 offset:1280\n\t" "s_waitcnt lgkmcnt(4)\n\t" "v_mfma_f32_32x32x16_bf16 v[32:47], a[156:159], v[128:131], v[32:47]\n\t" \
    "ds_read_b128 v[116:119], v76 offset:1280\n\t" "s_waitcnt lgkmcnt(4)\n\t" "v_mfma_f32_32x32x16_bf16 v[32:47], a[160:163], v[100:103], v[32:47]\n\t" \
    "ds_read_b128 v[120:123], v77 offset:1280\n\t" "s_waitcnt lgkmcnt(4)\n\t" "v_mfma_f32_32x32x16_bf16 v[32:47], a[164:167], v[104:107], v[32:47]\n\t" \
    "ds_read_b128 v[124:127], v78 offset:1280\n\t" "s_waitcnt lgkmcnt(4)\n\t" "v_mfma_f32_32x32x16_bf16 v[32:47], a[168:171], v[108:111], v[32:47]\n\t" \
    "ds_read_b128 v[128:131], v79 offset:1280\n\t" "s_waitcnt lgkmcnt(4)\n\t" "v_mfma_f32_32x32x16_bf16 v[32:47], a[172:175], v[112:115], v[32:47]\n\t" \
    "ds_read_b128 v[100:103], v72 offset:1536\n\t" "s_waitcnt lgkmcnt(4)\n\t" "v_mfma_f32_32x32x16_bf16 v[32:47], a[176:179], v[116:119], v[32:47]\n\t" \
    "ds_read_b128 v[104:107], v73 offset:1536\n\t" "s_waitcnt lgkmcnt(4)\n\t" "v_mfma_f32_32x32x16_bf16 v[32:47], a[180:183], v[120:123], v[32:47]\n\t" \
    "ds_read_b128 v[108:111], v74 offset:1536\n\t" "s_waitcnt lgkmcnt(4)\n\t" "v_mfma_f32_32x32x16_bf16 v[32:47], a[184:187], v[124:127], v[32:47]\n\t" \
    "ds_read_b128 v[112:115], v75 offset:1536\n\t" "s_waitcnt lgkmcnt(4)\n\t" "v_mfma_f32_32x32x16_bf16 v[32:47], a[188:191], v[128:131], v[32:47]\n\t" \
    "ds_read_b128 v[116:119], v76 offset:1536\n\t" "s_waitcnt lgkmcnt(4)\n\t" "v_mfma_f32_32x32x16_bf16 v[32:47], a[192:195], v[100:103], v[32:47]\n\t" \
    "ds_read_b128 v[120:123], v77 offset:1536\n\t" "s_waitcnt lgkmcnt(4)\n\t" "v_mfma_f32_32x32x16_bf16 v[32:47], a[196:199], v[104:107], v[32:47]\n\t" \
    "ds_read_b128 v[124:127], v78 offset:1536\n\t" "s_waitcnt lgkmcnt(4)\n\t" "v_mfma_f32_32x32x16_bf16 v[32:47], a[200:203], v[108:111], v[32:47]\n\t" \
    "ds_read_b128 v[128:131], v79 offset:1536\n\t" "s_waitcnt lgkmcnt(4)\n\t" "v_mfma_f32_32x32x16_bf16 v[32:47], a[204:207], v[112:115], v[32:47]\n\t" \
    "ds_read_b128 v[100:103], v72 offset:1792\n\t" "s_waitcnt lgkmcnt(4)\n\t" "v_mfma_f32_32x32x16_bf16 v[32:47], a[208:211], v[116:119], v[32:47]\n\t" \
    "ds_read_b128 v[104:107], v73 offset:1792\n\t" "s_waitcnt lgkmcnt(4)\n\t" "v_mfma_f32_32x32x16_bf16 v[32:47], a[212:215], v[120:123], v[32:47]\n\t" \
    "ds_read_b128 v[108:111], v74 offset:1792\n\t" "s_waitcnt lgkmcnt(4)\n\t" "v_mfma_f32_32x32x16_bf16 v[32:47], a[216:219], v[124:127], v[32:47]\n\t" \
    "ds_read_b128 v[112:115], v75 offset:1792\n\t" "s_waitcnt lgkmcnt(4)\n\t" "v_mfma_f32_32x32x16_bf16 v[32:47], a[220:223], v[128:131], v[32:47]\n\t" \
    "ds_read_b128 v[116:119], v76 offset:1792\n\t" "s_waitcnt lgkmcnt(4)\n\t" "v_mfma_f32_32x32x16_bf16 v[32:47], a[224:227], v[100:103], v[32:47]\n\t" \
    "ds_read_b128 v[120:123], v77 offset:1792\n\t" "s_waitcnt lgkmcnt(4)\n\t" "v_mfma_f32_32x32x16_bf16 v[32:47], a[228:231], v[104:107], v[32:47]\n\t" \
    "ds_read_b128 v[124:127], v78 offset:1792\n\t" "s_waitcnt lgkmcnt(4)\n\t" "v_mfma_f32_32x32x16_bf16 v[32:47], a[232:235], v[108:111], v[32:47]\n\t" \
    "ds_read_b128 v[128:131], v79 offset:1792\n\t" "s_waitcnt lgkmcnt(4)\n\t" "v_mfma_f32_32x32x16_bf16 v[32:47], a[236:239], v[112:115], v[32:47]\n\t" \
    "s_waitcnt lgkmcnt(3)\n\t" "v_mfma_f32_32x32x16_bf16 v[32:47], a[240:243], v[116:119], v[32:47]\n\t" "s_waitcnt vmcnt(0) lgkmcnt(0)\n\t" "s_barrier\n\t" XT(82, 83) \
    "v_mfma_f32_32x32x16_bf16 v[32:47], a[244:247], v[120:123], v[32:47]\n\t" "ds_read_b128 v[100:103], v80 offset:0\n\t" \
    "v_mfma_f32_32x32x16_bf16 v[32:47], a[248:251], v[124:127], v[32:47]\n\t" "ds_read_b128 v[104:107], v81 offset:0\n\t" "ds_read_b128 v[108:111], v82 offset:0\n\t" \
    "v_mov_b32 v98, s60\n\t" "buffer_store_dword v98, %[fo], %[rc], %[fl] offen sc1\n\t" "v_mfma_f32_32x32x16_bf16 v[32:47], a[252:255], v[128:131], v[32:47]\n\t" \
    "ds_read_b128 v[112:115], v83 offset:0\n\t" "ds_read_b128 v[116:119], v84 offset:0\n\t" "s_add_u32 s60, s60, 1\n\t" GSUM "s_cmp_eq_u32 s60, 1\n\t" \
    "s_cbranch_scc1 20f\n\t" "11:\n\t" XT(80, 81) "s_waitcnt lgkmcnt(4)\n\t" "v_mfma_f32_32x32x16_bf16 v[48:63], a[0:3], v[100:103], 0\n\t" \
    "ds_read_b128 v[120:123], v85 offset:0\n\t" "s_add_u32 s61, s60, 1\n\t" "s_and_b32 s61, s61, 63\n\t" "s_lshl_b32 s61, s61, 16\n\t" "s_add_u32 s63, %[lw], 0\n\t" \
    "s_waitcnt lgkmcnt(4)\n\t" "v_mfma_f32_32x32x16_bf16 v[48:63], a[4:7], v[104:107], v[48:63]\n\t" "ds_read_b128 v[124:127], v86 offset:0\n\t" "s_mov_b32 m0, s63\n\t" \
    "s_add_u32 s63, s63, 0x800\n\t" "buffer_load_dwordx4 v88, %[rx], s61 offen lds\n\t" "s_waitcnt lgkmcnt(4)\n\t" \
    "v_mfma_f32_32x32x16_bf16 v[48:63], a[8:11], v[108:111], v[48:63]\n\t" "ds_read_b128 v[128:131], v87 offset:0\n\t" \
    "buffer_load_dwordx4 v88, %[rx], s61 offen offset:1024 lds\n\t" "s_waitcnt lgkmcnt(4)\n\t" "v_mfma_f32_32x32x16_bf16 v[48:63], a[12:15], v[112:115], v[48:63]\n\t" \
    "ds_read_b128 v[100:103], v80 offset:256\n\t" "s_mov_b32 m0, s63\n\t" "s_add_u32 s63, s63, 0x800\n\t" "buffer_load_dwordx4 v89, %[rx], s61 offen lds\n\t" \
    "s_waitcnt lgkmcnt(4)\n\t" "v_mfma_f32_32x32x16_bf16 v[48:63], a[16:19], v[116:119], v[48:63]\n\t" "ds_read_b128 v[104:107], v81 offset:256\n\t" \
    "buffer_load_dwordx4 v89, %[rx], s61 offen offset:1024 lds\n\t" "v_cvt_pk_bf16_f32 v64, v32, v33\n\t" "v_cvt_pk_bf16_f32 v65, v34, v35\n\t" "s_waitcnt lgkmcnt(4)\n\t" \
    "v_mfma_f32_32x32x16_bf16 v[48:63], a[20:23], v[120:123], v[48:63]\n\t" "ds_read_b128 v[108:111], v82 offset:256\n\t" "s_mov_b32 m0, s63\n\t" \
    "s_add_u32 s63, s63, 0x800\n\t" "buffer_load_dwordx4 v90, %[rx], s61 offen lds\n\t" "s_waitcnt lgkmcnt(4)\n\t" \
    "v_mfma_f32_32x32x16_bf16 v[48:63], a[24:27], v[124:127], v[48:63]\n\t" "ds_read_b128 v[112:115], v83 offset:256\n\t" \
    "buffer_load_dwordx4 v90, %[rx], s61 offen offset:1024 lds\n\t" "v_cvt_pk_bf16_f32 v66, v36, v37\n\t" "v_cvt_pk_bf16_f32 v67, v38, v39\n\t" "s_waitcnt lgkmcnt(4)\n\t" \
    "v_mfma_f32_32x32x16_bf16 v[48:63], a[28:31], v[128:131], v[48:63]\n\t" "ds_read_b128 v[116:119], v84 offset:256\n\t" "s_mov_b32 m0, s63\n\t" \
    "s_add_u32 s63, s63, 0x800\n\t" "buffer_load_dwordx4 v91, %[rx], s61 offen lds\n\t" "s_waitcnt lgkmcnt(4)\n\t" \
    "v_mfma_f32_32x32x16_bf16 v[48:63], a[32:35], v[100:103], v[48:63]\n\t" "ds_read_b128 v[120:123], v85 offset:256\n\t" \
    "buffer_load_dwordx4 v91, %[rx], s61 offen offset:1024 lds\n\t" "v_cvt_pk_bf16_f32 v68, v40, v41\n\t" "v_cvt_pk_bf16_f32 v69, v42, v43\n\t" "s_waitcnt lgkmcnt(4)\n\t" \
    "v_mfma_f32_32x32x16_bf16 v[48:63], a[36:39], v[104:107], v[48:63]\n\t" "ds_read_b128 v[124:127], v86 offset:256\n\t" "s_mov_b32 m0, s63\n\t" \
    "s_add_u32 s63, s63, 0x800\n\t" "buffer_load_dwordx4 v92, %[rx], s61 offen lds\n\t" "s_waitcnt lgkmcnt(4)\n\t" \
    "v_mfma_f32_32x32x16_bf16 v[48:63], a[40:43], v[108:111], v[48:63]\n\t" "ds_read_b128 v[128:131], v87 offset:256\n\t" \
    "buffer_load_dwordx4 v92, %[rx], s61 offen offset:1024 lds\n\t" "v_cvt_pk_bf16_f32 v70, v44, v45\n\t" "v_cvt_pk_bf16_f32 v71, v46, v47\n\t" "s_waitcnt lgkmcnt(4)\n\t" \
    "v_mfma_f32_32x32x16_bf16 v[48:63], a[44:47], v[112:115], v[48:63]\n\t" "ds_read_b128 v[100:103], v80 offset:512\n\t" "s_mov_b32 m0, s63\n\t" \
    "s_add_u32 s63, s63, 0x800\n\t" "buffer_load_dwordx4 v93, %[rx], s61 offen lds\n\t" "s_waitcnt lgkmcnt(4)\n\t" \
    "v_mfma_f32_32x32x16_bf16 v[48:63], a[48:51], v[116:119], v[48:63]\n\t" "ds_read_b128 v[104:107], v81 offset:512\n\t" \
    "buffer_load_dwordx4 v93, %[rx], s61 offen offset:1024 lds\n\t" "s_waitcnt lgkmcnt(4)\n\t" "v_mfma_f32_32x32x16_bf16 v[48:63], a[52:55], v[120:123], v[48:63]\n\t" \
    "ds_read_b128 v[108:111], v82 offset:512\n\t" "s_mov_b32 m0, s63\n\t" "s_add_u32 s63, s63, 0x800\n\t" "buffer_load_dwordx4 v94, %[rx], s61 offen lds\n\t" \
    "s_waitcnt lgkmcnt(4)\n\t" "v_mfma_f32_32x32x16_bf16 v[48:63], a[56:59], v[124:127], v[48:63]\n\t" "ds_read_b128 v[112:115], v83 offset:512\n\t" \
    "buffer_load_dwordx4 v94, %[rx], s61 offen offset:1024 lds\n\t" "s_waitcnt lgkmcnt(4)\n\t" "v_mfma_f32_32x32x16_bf16 v[48:63], a[60:63], v[128:131], v[48:63]\n\t" \
    "ds_read_b128 v[116:119], v84 offset:512\n\t" "s_mov_b32 m0, s63\n\t" "s_add_u32 s63, s63, 0x800\n\t" "buffer_load_dwordx4 v95, %[rx], s61 offen lds\n\t" \
    "s_waitcnt lgkmcnt(4)\n\t" "v_mfma_f32_32x32x16_bf16 v[48:63], a[64:67], v[100:103], v[48:63]\n\t" "ds_read_b128 v[120:123], v85 offset:512\n\t" \
    "buffer_load_dwordx4 v95, %[rx], s61 offen offset:1024 lds\n\t" "s_waitcnt lgkmcnt(4)\n\t" "v_mfma_f32_32x32x16_bf16 v[48:63], a[68:71], v[104:107], v[48:63]\n\t" \
    "ds_read_b128 v[124:127], v86 offset:512\n\t" "buffer_store_dwordx4 v[64:67], v96, %[rs], s62 offen offset:0 sc1\n\t" "s_waitcnt lgkmcnt(4)\n\t" \
    "v_mfma_f32_32x32x16_bf16 v[48:63], a[72:75], v[108:111], v[48:63]\n\t" "ds_read_b128 v[128:131], v87 offset:512\n\t" "s_waitcnt lgkmcnt(4)\n\t" \
    "v_mfma_f32_32x32x16_bf16 v[48:63], a[76:79], v[112:115], v[48:63]\n\t" "ds_read_b128 v[100:103], v80 offset:768\n\t" \
    "buffer_store_dwordx4 v[68:71], v96, %[rs], s62 offen offset:1024 sc1\n\t" "s_waitcnt lgkmcnt(4)\n\t" \
    "v_mfma_f32_32x32x16_bf16 v[48:63], a[80:83], v[116:119], v[48:63]\n\t" "ds_read_b128 v[104:107], v81 offset:768\n\t" "s_waitcnt lgkmcnt(4)\n\t" \
    "v_mfma_f32_32x32x16_bf16 v[48:63], a[84:87], v[120:123], v[48:63]\n\t" "ds_read_b128 v[108:111], v82 offset:768\n\t" "s_waitcnt lgkmcnt(4)\n\t" \
    "v_mfma_f32_32x32x16_bf16 v[48:63], a[88:91], v[124:127], v[48:63]\n\t" "ds_read_b128 v[112:115], v83 offset:768\n\t" "s_waitcnt lgkmcnt(4)\n\t" \
    "v_mfma_f32_32x32x16_bf16 v[48:63], a[92:95], v[128:131], v[48:63]\n\t" "ds_read_b128 v[116:119], v84 offset:768\n\t" "s_waitcnt lgkmcnt(4)\n\t" \
    "v_mfma_f32_32x32x16_bf16 v[48:63], a[96:99], v[100:103], v[48:63]\n\t" "ds_read_b128 v[120:123], v85 offset:768\n\t" "s_lshr_b32 s64, s60, 2\n\t" \
    "s_xor_b32 s64, s64, %[xp]\n\t" "s_and_b32 s64, s64, 1\n\t" "s_lshl_b32 s64, s64, 13\n\t" "s_waitcnt lgkmcnt(4)\n\t" \
    "v_mfma_f32_32x32x16_bf16 v[48:63], a[100:103], v[104:107], v[48:63]\n\t" "ds_read_b128 v[124:127], v86 offset:768\n\t" "s_lshl_b32 s62, s60, 19\n\t" \
    "s_add_u32 s62, s62, s64\n\t" "s_add_u32 s62, s62, %[ob]\n\t" "s_waitcnt lgkmcnt(4)\n\t" "v_mfma_f32_32x32x16_bf16 v[48:63], a[104:107], v[108:111], v[48:63]\n\t" \
    "ds_read_b128 v[128:131], v87 offset:768\n\t" "s_waitcnt lgkmcnt(4)\n\t" "v_mfma_f32_32x32x16_bf16 v[48:63], a[108:111], v[112:115], v[48:63]\n\t" \
    "ds_read_b128 v[100:103], v80 offset:1024\n\t" "s_waitcnt lgkmcnt(4)\n\t" "v_mfma_f32_32x32x16_bf16 v[48:63], a[112:115], v[116:119], v[48:63]\n\t" \
    "ds_read_b128 v[104:107], v81 offset:1024\n\t" "s_waitcnt lgkmcnt(4)\n\t" "v_mfma_f32_32x32x16_bf16 v[48:63], a[116:119], v[120:123], v[48:63]\n\t" \
    "ds_read_b128 v[108:111], v82 offset:1024\n\t" "s_waitcnt lgkmcnt(4)\n\t" "v_mfma_f32_32x32x16_bf16 v[48:63], a[120:123], v[124:127], v[48:63]\n\t" \
    "ds_read_b128 v[112:115], v83 offset:1024\n\t" "s_waitcnt lgkmcnt(4)\n\t" "v_mfma_f32_32x32x16_bf16 v[48:63], a[124:127], v[128:131], v[48:63]\n\t" \
    "ds_read_b128 v[116:119], v84 offset:1024\n\t" "s_waitcnt lgkmcnt(4)\n\t" "v_mfma_f32_32x32x16_bf16 v[48:63], a[128:131], v[100:103], v[48:63]\n\t" \
    "ds_read_b128 v[120:123], v85 offset:1024\n\t" "s_waitcnt lgkmcnt(4)\n\t" "v_mfma_f32_32x32x16_bf16 v[48:63], a[132:135], v[104:107], v[48:63]\n\t" \
    "ds_read_b128 v[124:127], v86 offset:1024\n\t" "s_waitcnt lgkmcnt(4)\n\t" "v_mfma_f32_32x32x16_bf16 v[48:63], a[136:139], v[108:111], v[48:63]\n\t" \
    "ds_read_b128 v[128:131], v87 offset:1024\n\t" "s_waitcnt lgkmcnt(4)\n\t" "v_mfma_f32_32x32x16_bf16 v[48:63], a[140:143], v[112:115], v[48:63]\n\t" \
    "ds_read_b128 v[100:103], v80 offset:1280\n\t" "s_waitcnt lgkmcnt(4)\n\t" "v_mfma_f32_32x32x16_bf16 v[48:63], a[144:147], v[116:119], v[48:63]\n\t" \
    "ds_read_b128 v[104:107], v81 offset:1280\n\t" "s_waitcnt lgkmcnt(4)\n\t" "v_mfma_f32_32x32x16_bf16 v[48:63], a[148:151], v[120:123], v[48:63]\n\t" \
    "ds_read_b128 v[108:111], v82 offset:1280\n\t" "s_waitcnt lgkmcnt(4)\n\t" "v_mfma_f32_32x32x16_bf16 v[48:63], a[152:155], v[124:127], v[48:63]\n\t" \
    "ds_read_b128 v[112:115], v83 offset:1280\n\t" "s_waitcnt lgkmcnt(4)\n\t" "v_mfma_f32_32x32x16_bf16 v[48:63], a[156:159], v[128:131], v[48:63]\n\t" \
    "ds_read_b128 v[116:119], v84 offset:1280\n\t" "s_waitcnt lgkmcnt(4)\n\t" "v_mfma_f32_32x32x16_bf16 v[48:63], a[160:163], v[100:103], v[48:63]\n\t" \
    "ds_read_b128 v[120:123], v85 offset:1280\n\t" "s_waitcnt lgkmcnt(4)\n\t" "v_mfma_f32_32x32x16_bf16 v[48:63], a[164:167], v[104:107], v[48:63]\n\t" \
    "ds_read_b128 v[124:127], v86 offset:1280\n\t" "s_waitcnt lgkmcnt(4)\n\t" "v_mfma_f32_32x32x16_bf16 v[48:63], a[168:171], v[108:111], v[48:63]\n\t" \
    "ds_read_b128 v[128:131], v87 offset:1280\n\t" "s_waitcnt lgkmcnt(4)\n\t" "v_mfma_f32_32x32x16_bf16 v[48:63], a[172:175], v[112:115], v[48:63]\n\t" \
    "ds_read_b128 v[100:103], v80 offset:1536\n\t" "s_waitcnt lgkmcnt(4)\n\t" "v_mfma_f32_32x32x16_bf16 v[48:63], a[176:179], v[116:119], v[48:63]\n\t" \
    "ds_read_b128 v[104:107], v81 offset:1536\n\t" "s_waitcnt lgkmcnt(4)\n\t" "v_mfma_f32_32x32x16_bf16 v[48:63], a[180:183], v[120:123], v[48:63]\n\t" \
    "ds_read_b128 v[108:111], v82 offset:1536\n\t" "s_waitcnt lgkmcnt(4)\n\t" "v_mfma_f32_32x32x16_bf16 v[48:63], a[184:187], v[124:127], v[48:63]\n\t" \
    "ds_read_b128 v[112:115], v83 offset:1536\n\t" "s_waitcnt lgkmcnt(4)\n\t" "v_mfma_f32_32x32x16_bf16 v[48:63], a[188:191], v[128:131], v[48:63]\n\t" \
    "ds_read_b128 v[116:119], v84 offset:1536\n\t" "s_waitcnt lgkmcnt(4)\n\t" "v_mfma_f32_32x32x16_bf16 v[48:63], a[192:195], v[100:103], v[48:63]\n\t" \
    "ds_read_b128 v[120:123], v85 offset:1536\n\t" "s_waitcnt lgkmcnt(4)\n\t" "v_mfma_f32_32x32x16_bf16 v[48:63], a[196:199], v[104:107], v[48:63]\n\t" \
    "ds_read_b128 v[124:127], v86 offset:1536\n\t" "s_waitcnt lgkmcnt(4)\n\t" "v_mfma_f32_32x32x16_bf16 v[48:63], a[200:203], v[108:111], v[48:63]\n\t" \
    "ds_read_b128 v[128:131], v87 offset:1536\n\t" "s_waitcnt lgkmcnt(4)\n\t" "v_mfma_f32_32x32x16_bf16 v[48:63], a[204:207], v[112:115], v[48:63]\n\t" \
    "ds_read_b128 v[100:103], v80 offset:1792\n\t" "s_waitcnt lgkmcnt(4)\n\t" "v_mfma_f32_32x32x16_bf16 v[48:63], a[208:211], v[116:119], v[48:63]\n\t" \
    "ds_read_b128 v[104:107], v81 offset:1792\n\t" "s_waitcnt lgkmcnt(4)\n\t" "v_mfma_f32_32x32x16_bf16 v[48:63], a[212:215], v[120:123], v[48:63]\n\t" \
    "ds_read_b128 v[108:111], v82 offset:1792\n\t" "s_waitcnt lgkmcnt(4)\n\t" "v_mfma_f32_32x32x16_bf16 v[48:63], a[216:219], v[124:127], v[48:63]\n\t" \
    "ds_read_b128 v[112:115], v83 offset:1792\n\t" "s_waitcnt lgkmcnt(4)\n\t" "v_mfma_f32_32x32x16_bf16 v[48:63], a[220:223], v[128:131], v[48:63]\n\t" \
    "ds_read_b128 v[116:119], v84 offset:1792\n\t" "s_waitcnt lgkmcnt(4)\n\t" "v_mfma_f32_32x32x16_bf16 v[48:63], a[224:227], v[100:103], v[48:63]\n\t" \
    "ds_read_b128 v[120:123], v85 offset:1792\n\t" "s_waitcnt lgkmcnt(4)\n\t" "v_mfma_f32_32x32x16_bf16 v[48:63], a[228:231], v[104:107], v[48:63]\n\t" \
    "ds_read_b128 v[124:127], v86 offset:1792\n\t" "s_waitcnt lgkmcnt(4)\n\t" "v_mfma_f32_32x32x16_bf16 v[48:63], a[232:235], v[108:111], v[48:63]\n\t" \
    "ds_read_b128 v[128:131], v87 offset:1792\n\t" "s_waitcnt lgkmcnt(4)\n\t" "v_mfma_f32_32x32x16_bf16 v[48:63], a[236:239], v[112:115], v[48:63]\n\t" \
    "s_waitcnt lgkmcnt(3)\n\t" "v_mfma_f32_32x32x16_bf16 v[48:63], a[240:243], v[116:119], v[48:63]\n\t" "s_waitcnt vmcnt(0) lgkmcnt(0)\n\t" "s_barrier\n\t" XT(82, 83) \
    "v_mfma_f32_32x32x16_bf16 v[48:63], a[244:247], v[120:123], v[48:63]\n\t" "ds_read_b128 v[100:103], v72 offset:0\n\t" \
    "v_mfma_f32_32x32x16_bf16 v[48:63], a[248:251], v[124:127], v[48:63]\n\t" "ds_read_b128 v[104:107], v73 offset:0\n\t" "ds_read_b128 v[108:111], v74 offset:0\n\t" \
    "v_mov_b32 v98, s60\n\t" "buffer_store_dword v98, %[fo], %[rc], %[fl] offen sc1\n\t" "v_mfma_f32_32x32x16_bf16 v[48:63], a[252:255], v[128:131], v[48:63]\n\t" \
    "ds_read_b128 v[112:115], v75 offset:0\n\t" "ds_read_b128 v[116:119], v76 offset:0\n\t" "s_add_u32 s60, s60, 1\n\t" GSUM "s_cmp_lt_u32 s60, 64\n\t" \
    "s_cbranch_scc1 10b\n\t" "s_nop 11\n\t" "v_cvt_pk_bf16_f32 v64, v48, v49\n\t" "v_cvt_pk_bf16_f32 v65, v50, v51\n\t" "v_cvt_pk_bf16_f32 v66, v52, v53\n\t" \
    "v_cvt_pk_bf16_f32 v67, v54, v55\n\t" "v_cvt_pk_bf16_f32 v68, v56, v57\n\t" "v_cvt_pk_bf16_f32 v69, v58, v59\n\t" "v_cvt_pk_bf16_f32 v70, v60, v61\n\t" \
    "v_cvt_pk_bf16_f32 v71, v62, v63\n\t" "buffer_store_dwordx4 v[64:67], v96, %[rs], s62 offen offset:0 sc1\n\t" \
    "buffer_store_dwordx4 v[68:71], v96, %[rs], s62 offen offset:1024 sc1\n\t" "s_waitcnt vmcnt(0)\n\t" "s_barrier\n\t" "v_mov_b32 v98, s60\n\t" \
    "buffer_store_dword v98, %[fo], %[rc], %[fl] offen sc1\n\t" "s_waitcnt lgkmcnt(0)\n\t" "s_branch 30f\n\t" "20:\n\t" "v_mov_b32 v96, %[so]\n\t" "s_nop 11\n\t" \
    "v_cvt_pk_bf16_f32 v64, v32, v33\n\t" "v_cvt_pk_bf16_f32 v65, v34, v35\n\t" "v_cvt_pk_bf16_f32 v66, v36, v37\n\t" "v_cvt_pk_bf16_f32 v67, v38, v39\n\t" \
    "v_cvt_pk_bf16_f32 v68, v40, v41\n\t" "v_cvt_pk_bf16_f32 v69, v42, v43\n\t" "v_cvt_pk_bf16_f32 v70, v44, v45\n\t" "v_cvt_pk_bf16_f32 v71, v46, v47\n\t" \
    "buffer_store_dwordx4 v[64:67], v96, %[rs], s62 offen offset:0 sc1\n\t" "buffer_store_dwordx4 v[68:71], v96, %[rs], s62 offen offset:1024 sc1\n\t" \
    "s_waitcnt vmcnt(0)\n\t" "s_barrier\n\t" "v_mov_b32 v98, s60\n\t" "buffer_store_dword v98, %[fo], %[rc], %[fl] offen sc1\n\t" "s_branch 11b\n\t" "30:\n\t"
#define ZC_LOOP \
    "s_mov_b32 s60, 0\n\t" "s_mov_b32 s61, 0\n\t" "s_mov_b32 s62, 0\n\t" "s_mov_b32 s63, 0x7f000000\n\t" "s_cmp_ge_u32 s61, 1\n\t" "s_cbranch_scc1 52f\n\t" "51:\n\t" \
    "buffer_load_dword v120, %[fo], %[rc], %[fv] offen sc1\n\t" "s_waitcnt vmcnt(0)\n\t" "v_readfirstlane_b32 s61, v120\n\t" "s_nop 3\n\t" "s_cmp_ge_u32 s61, 1\n\t" \
    "s_cbranch_scc1 52f\n\t" "s_cmp_eq_u32 %[bd], 0\n\t" "s_cbranch_scc1 52f\n\t" "s_sub_u32 %[bd], %[bd], 1\n\t" "s_sleep 2\n\t" "s_branch 51b\n\t" "52:\n\t" \
    "s_lshr_b32 s66, s60, 2\n\t" "s_xor_b32 s66, s66, %[xp]\n\t" "s_and_b32 s66, s66, 1\n\t" "s_lshl_b32 s66, s66, 13\n\t" "s_lshl_b32 s64, s60, 19\n\t" \
    "s_add_u32 s64, s64, s66\n\t" "s_add_u32 s64, s64, %[vb]\n\t" "buffer_load_dwordx4 v[16:19], %[ao], %[rs], s64 offen offset:0 sc1\n\t" \
    "buffer_load_dwordx4 v[20:23], %[ao], %[rs], s64 offen offset:1024 sc1\n\t" "buffer_load_dwordx4 v[24:27], %[ao], %[rs], s64 offen offset:2048 sc1\n\t" \
    "buffer_load_dwordx4 v[28:31], %[ao], %[rs], s64 offen offset:3072 sc1\n\t" "buffer_load_dwordx4 v[32:35], %[a1], %[rs], s64 offen offset:0 sc1\n\t" \
    "buffer_load_dwordx4 v[36:39], %[a1], %[rs], s64 offen offset:1024 sc1\n\t" "buffer_load_dwordx4 v[40:43], %[a1], %[rs], s64 offen offset:2048 sc1\n\t" \
    "buffer_load_dwordx4 v[44:47], %[a1], %[rs], s64 offen offset:3072 sc1\n\t" "s_waitcnt vmcnt(0)\n\t" "10:\n\t" XT(80, 81) "s_add_u32 s66, s62, %[rg]\n\t" \
    "s_cmp_lt_u32 s66, s60\n\t" "s_cbranch_scc1 60f\n\t" "63:\n\t" "v_mfma_f32_32x32x16_bf16 v[80:95], v[16:19], a[0:3], 0\n\t" "s_add_u32 s67, s60, 1\n\t" \
    "s_lshr_b32 s66, s67, 2\n\t" "s_xor_b32 s66, s66, %[xp]\n\t" "s_and_b32 s66, s66, 1\n\t" "s_lshl_b32 s66, s66, 13\n\t" \
    "v_mfma_f32_32x32x16_bf16 v[80:95], v[20:23], a[4:7], v[80:95]\n\t" "s_lshl_b32 s64, s67, 19\n\t" "s_add_u32 s64, s64, s66\n\t" "s_add_u32 s64, s64, %[vb]\n\t" \
    "s_cmp_gt_u32 s61, s67\n\t" "s_cselect_b32 s65, 1, 0\n\t" "v_mfma_f32_32x32x16_bf16 v[80:95], v[24:27], a[8:11], v[80:95]\n\t" "s_cselect_b32 s64, s64, 0x7f000000\n\t" \
    "v_mfma_f32_32x32x16_bf16 v[80:95], v[28:31], a[12:15], v[80:95]\n\t" "v_cvt_pk_bf16_f32 v112, v96, v97\n\t" "v_cvt_pk_bf16_f32 v113, v98, v99\n\t" \
    "v_cvt_pk_bf16_f32 v114, v100, v101\n\t" "v_mfma_f32_32x32x16_bf16 v[80:95], v[32:35], a[16:19], v[80:95]\n\t" "v_cvt_pk_bf16_f32 v115, v102, v103\n\t" \
    "v_cvt_pk_bf16_f32 v116, v104, v105\n\t" "v_cvt_pk_bf16_f32 v117, v106, v107\n\t" "v_mfma_f32_32x32x16_bf16 v[80:95], v[36:39], a[20:23], v[80:95]\n\t" \
    "buffer_store_dwordx4 v[112:115], %[zo], %[rs], s63 offen offset:0 sc1\n\t" "v_cvt_pk_bf16_f32 v118, v108, v109\n\t" "v_cvt_pk_bf16_f32 v119, v110, v111\n\t" \
    "v_mfma_f32_32x32x16_bf16 v[80:95], v[40:43], a[24:27], v[80:95]\n\t" "v_mfma_f32_32x32x16_bf16 v[80:95], v[44:47], a[28:31], v[80:95]\n\t" \
    "buffer_store_dwordx4 v[116:119], %[zo], %[rs], s63 offen offset:512 sc1\n\t" "s_and_b32 s63, s60, %[rm]\n\t" "s_lshl_b32 s63, s63, 22\n\t" \
    "s_add_u32 s63, s63, %[zb]\n\t" "v_mfma_f32_32x32x16_bf16 v[96:111], v[16:19], a[32:35], 0\n\t" "buffer_load_dwordx4 v[48:51], %[ao], %[rs], s64 offen offset:0 sc1\n\t" \
    "s_nop 2\n\t" "v_mfma_f32_32x32x16_bf16 v[96:111], v[20:23], a[36:39], v[96:111]\n\t" "buffer_load_dwordx4 v[52:55], %[ao], %[rs], s64 offen offset:1024 sc1\n\t" \
    "s_nop 2\n\t" "v_mfma_f32_32x32x16_bf16 v[96:111], v[24:27], a[40:43], v[96:111]\n\t" "buffer_load_dwordx4 v[56:59], %[ao], %[rs], s64 offen offset:2048 sc1\n\t" \
    "s_nop 1\n\t" "v_mfma_f32_32x32x16_bf16 v[96:111], v[28:31], a[44:47], v[96:111]\n\t" "buffer_load_dwordx4 v[60:63], %[ao], %[rs], s64 offen offset:3072 sc1\n\t" \
    "v_cvt_pk_bf16_f32 v112, v80, v81\n\t" "v_cvt_pk_bf16_f32 v113, v82, v83\n\t" "v_cvt_pk_bf16_f32 v114, v84, v85\n\t" \
    "v_mfma_f32_32x32x16_bf16 v[96:111], v[32:35], a[48:51], v[96:111]\n\t" "buffer_load_dwordx4 v[64:67], %[a1], %[rs], s64 offen offset:0 sc1\n\t" \
    "v_cvt_pk_bf16_f32 v115, v86, v87\n\t" "v_cvt_pk_bf16_f32 v116, v88, v89\n\t" "v_cvt_pk_bf16_f32 v117, v90, v91\n\t" \
    "v_mfma_f32_32x32x16_bf16 v[96:111], v[36:39], a[52:55], v[96:111]\n\t" "buffer_load_dwordx4 v[68:71], %[a1], %[rs], s64 offen offset:1024 sc1\n\t" \
    "buffer_store_dwordx4 v[112:115], %[zo], %[rs], s63 offen offset:0 sc1\n\t" "v_cvt_pk_bf16_f32 v118, v92, v93\n\t" "v_cvt_pk_bf16_f32 v119, v94, v95\n\t" \
    "v_mfma_f32_32x32x16_bf16 v[96:111], v[40:43], a[56:59], v[96:111]\n\t" "buffer_load_dwordx4 v[72:75], %[a1], %[rs], s64 offen offset:2048 sc1\n\t" \
    "v_mfma_f32_32x32x16_bf16 v[96:111], v[44:47], a[60:63], v[96:111]\n\t" "buffer_load_dwordx4 v[76:79], %[a1], %[rs], s64 offen offset:3072 sc1\n\t" \
    "buffer_store_dwordx4 v[116:119], %[zo], %[rs], s63 offen offset:512 sc1\n\t" "s_add_u32 s63, s63, 0x20000\n\t" \
    "v_mfma_f32_32x32x16_bf16 v[80:95], v[16:19], a[64:67], 0\n\t" "buffer_load_dword v120, %[fo], %[rc], %[fv] offen sc1\n\t" "s_nop 2\n\t" \
    "v_mfma_f32_32x32x16_bf16 v[80:95], v[20:23], a[68:71], v[80:95]\n\t" "buffer_load_dword v121, %[fo], %[rc], %[fs] offen sc1\n\t" "s_nop 2\n\t" \
    "v_mfma_f32_32x32x16_bf16 v[80:95], v[24:27], a[72:75], v[80:95]\n\t" "s_nop 1\n\t" "v_mfma_f32_32x32x16_bf16 v[80:95], v[28:31], a[76:79], v[80:95]\n\t" \
    "v_cvt_pk_bf16_f32 v112, v96, v97\n\t" "v_cvt_pk_bf16_f32 v113, v98, v99\n\t" "v_cvt_pk_bf16_f32 v114, v100, v101\n\t" \
    "v_mfma_f32_32x32x16_bf16 v[80:95], v[32:35], a[80:83], v[80:95]\n\t" "v_cvt_pk_bf16_f32 v115, v102, v103\n\t" "v_cvt_pk_bf16_f32 v116, v104, v105\n\t" \
    "v_cvt_pk_bf16_f32 v117, v106, v107\n\t" "v_mfma_f32_32x32x16_bf16 v[80:95], v[36:39], a[84:87], v[80:95]\n\t" \
    "buffer_store_dwordx4 v[112:115], %[zo], %[rs], s63 offen offset:0 sc1\n\t" "v_cvt_pk_bf16_f32 v118, v108, v109\n\t" "v_cvt_pk_bf16_f32 v119, v110, v111\n\t" \
    "v_mfma_f32_32x32x16_bf16 v[80:95], v[40:43], a[88:91], v[80:95]\n\t" "v_mfma_f32_32x32x16_bf16 v[80:95], v[44:47], a[92:95], v[80:95]\n\t" \
    "buffer_store_dwordx4 v[116:119], %[zo], %[rs], s63 offen offset:512 sc1\n\t" "s_add_u32 s63, s63, 0x20000\n\t" \
    "v_mfma_f32_32x32x16_bf16 v[96:111], v[16:19], a[96:99], 0\n\t" "s_nop 2\n\t" "v_mfma_f32_32x32x16_bf16 v[96:111], v[20:23], a[100:103], v[96:111]\n\t" "s_nop 2\n\t" \
    "v_mfma_f32_32x32x16_bf16 v[96:111], v[24:27], a[104:107], v[96:111]\n\t" "s_nop 1\n\t" "v_mfma_f32_32x32x16_bf16 v[96:111], v[28:31], a[108:111], v[96:111]\n\t" \
    "v_cvt_pk_bf16_f32 v112, v80, v81\n\t" "v_cvt_pk_bf16_f32 v113, v82, v83\n\t" "v_cvt_pk_bf16_f32 v114, v84, v85\n\t" \
    "v_mfma_f32_32x32x16_bf16 v[96:111], v[32:35], a[112:115], v[96:111]\n\t" "v_cvt_pk_bf16_f32 v115, v86, v87\n\t" "v_cvt_pk_bf16_f32 v116, v88, v89\n\t" \
    "v_cvt_pk_bf16_f32 v117, v90, v91\n\t" "v_mfma_f32_32x32x16_bf16 v[96:111], v[36:39], a[116:119], v[96:111]\n\t" \
    "buffer_store_dwordx4 v[112:115], %[zo], %[rs], s63 offen offset:0 sc1\n\t" "v_cvt_pk_bf16_f32 v118, v92, v93\n\t" "v_cvt_pk_bf16_f32 v119, v94, v95\n\t" \
    "v_mfma_f32_32x32x16_bf16 v[96:111], v[40:43], a[120:123], v[96:111]\n\t" "v_mfma_f32_32x32x16_bf16 v[96:111], v[44:47], a[124:127], v[96:111]\n\t" \
    "buffer_store_dwordx4 v[116:119], %[zo], %[rs], s63 offen offset:512 sc1\n\t" "s_add_u32 s63, s63, 0x20000\n\t" \
    "v_mfma_f32_32x32x16_bf16 v[80:95], v[16:19], a[128:131], 0\n\t" "s_nop 2\n\t" "v_mfma_f32_32x32x16_bf16 v[80:95], v[20:23], a[132:135], v[80:95]\n\t" "s_nop 2\n\t" \
    "v_mfma_f32_32x32x16_bf16 v[80:95], v[24:27], a[136:139], v[80:95]\n\t" "s_nop 1\n\t" "v_mfma_f32_32x32x16_bf16 v[80:95], v[28:31], a[140:143], v[80:95]\n\t" \
    "v_cvt_pk_bf16_f32 v112, v96, v97\n\t" "v_cvt_pk_bf16_f32 v113, v98, v99\n\t" "v_cvt_pk_bf16_f32 v114, v100, v101\n\t" \
    "v_mfma_f32_32x32x16_bf16 v[80:95], v[32:35], a[144:147], v[80:95]\n\t" "v_cvt_pk_bf16_f32 v115, v102, v103\n\t" "v_cvt_pk_bf16_f32 v116, v104, v105\n\t" \
    "v_cvt_pk_bf16_f32 v117, v106, v107\n\t" "v_mfma_f32_32x32x16_bf16 v[80:95], v[36:39], a[148:151], v[80:95]\n\t" \
    "buffer_store_dwordx4 v[112:115], %[zo], %[rs], s63 offen offset:0 sc1\n\t" "v_cvt_pk_bf16_f32 v118, v108, v109\n\t" "v_cvt_pk_bf16_f32 v119, v110, v111\n\t" \
    "v_mfma_f32_32x32x16_bf16 v[80:95], v[40:43], a[152:155], v[80:95]\n\t" "v_mfma_f32_32x32x16_bf16 v[80:95], v[44:47], a[156:159], v[80:95]\n\t" \
    "buffer_store_dwordx4 v[116:119], %[zo], %[rs], s63 offen offset:512 sc1\n\t" "s_add_u32 s63, s63, 0x20000\n\t" \
    "v_mfma_f32_32x32x16_bf16 v[96:111], v[16:19], a[160:163], 0\n\t" "s_nop 2\n\t" "v_mfma_f32_32x32x16_bf16 v[96:111], v[20:23], a[164:167], v[96:111]\n\t" "s_nop 2\n\t" \
    "v_mfma_f32_32x32x16_bf16 v[96:111], v[24:27], a[168:171], v[96:111]\n\t" "s_nop 1\n\t" "v_mfma_f32_32x32x16_bf16 v[96:111], v[28:31], a[172:175], v[96:111]\n\t" \
    "v_cvt_pk_bf16_f32 v112, v80, v81\n\t" "v_cvt_pk_bf16_f32 v113, v82, v83\n\t" "v_cvt_pk_bf16_f32 v114, v84, v85\n\t" \
    "v_mfma_f32_32x32x16_bf16 v[96:111], v[32:35], a[176:179], v[96:111]\n\t" "v_cvt_pk_bf16_f32 v115, v86, v87\n\t" "v_cvt_pk_bf16_f32 v116, v88, v89\n\t" \
    "v_cvt_pk_bf16_f32 v117, v90, v91\n\t" "v_mfma_f32_32x32x16_bf16 v[96:111], v[36:39], a[180:183], v[96:111]\n\t" \
    "buffer_store_dwordx4 v[112:115], %[zo], %[rs], s63 offen offset:0 sc1\n\t" "v_cvt_pk_bf16_f32 v118, v92, v93\n\t" "v_cvt_pk_bf16_f32 v119, v94, v95\n\t" \
    "v_mfma_f32_32x32x16_bf16 v[96:111], v[40:43], a[184:187], v[96:111]\n\t" "v_mfma_f32_32x32x16_bf16 v[96:111], v[44:47], a[188:191], v[96:111]\n\t" \
    "buffer_store_dwordx4 v[116:119], %[zo], %[rs], s63 offen offset:512 sc1\n\t" "s_add_u32 s63, s63, 0x20000\n\t" \
    "v_mfma_f32_32x32x16_bf16 v[80:95], v[16:19], a[192:195], 0\n\t" "s_nop 2\n\t" "v_mfma_f32_32x32x16_bf16 v[80:95], v[20:23], a[196:199], v[80:95]\n\t" "s_nop 2\n\t" \
    "v_mfma_f32_32x32x16_bf16 v[80:95], v[24:27], a[200:203], v[80:95]\n\t" "s_nop 1\n\t" "v_mfma_f32_32x32x16_bf16 v[80:95], v[28:31], a[204:207], v[80:95]\n\t" \
    "v_cvt_pk_bf16_f32 v112, v96, v97\n\t" "v_cvt_pk_bf16_f32 v113, v98, v99\n\t" "v_cvt_pk_bf16_f32 v114, v100, v101\n\t" \
    "v_mfma_f32_32x32x16_bf16 v[80:95], v[32:35], a[208:211], v[80:95]\n\t" "v_cvt_pk_bf16_f32 v115, v102, v103\n\t" "v_cvt_pk_bf16_f32 v116, v104, v105\n\t" \
    "v_cvt_pk_bf16_f32 v117, v106, v107\n\t" "v_mfma_f32_32x32x16_bf16 v[80:95], v[36:39], a[212:215], v[80:95]\n\t" \
    "buffer_store_dwordx4 v[112:115], %[zo], %[rs], s63 offen offset:0 sc1\n\t" "v_cvt_pk_bf16_f32 v118, v108, v109\n\t" "v_cvt_pk_bf16_f32 v119, v110, v111\n\t" \
    "v_mfma_f32_32x32x16_bf16 v[80:95], v[40:43], a[216:219], v[80:95]\n\t" "v_mfma_f32_32x32x16_bf16 v[80:95], v[44:47], a[220:223], v[80:95]\n\t" \
    "buffer_store_dwordx4 v[116:119], %[zo], %[rs], s63 offen offset:512 sc1\n\t" "s_add_u32 s63, s63, 0x20000\n\t" \
    "v_mfma_f32_32x32x16_bf16 v[96:111], v[16:19], a[224:227], 0\n\t" "s_nop 2\n\t" "v_mfma_f32_32x32x16_bf16 v[96:111], v[20:23], a[228:231], v[96:111]\n\t" "s_nop 2\n\t" \
    "v_mfma_f32_32x32x16_bf16 v[96:111], v[24:27], a[232:235], v[96:111]\n\t" "s_nop 1\n\t" "v_mfma_f32_32x32x16_bf16 v[96:111], v[28:31], a[236:239], v[96:111]\n\t" \
    "v_cvt_pk_bf16_f32 v112, v80, v81\n\t" "v_cvt_pk_bf16_f32 v113, v82, v83\n\t" "v_cvt_pk_bf16_f32 v114, v84, v85\n\t" \
    "v_mfma_f32_32x32x16_bf16 v[96:111], v[32:35], a[240:243], v[96:111]\n\t" "v_cvt_pk_bf16_f32 v115, v86, v87\n\t" "v_cvt_pk_bf16_f32 v116, v88, v89\n\t" \
    "v_cvt_pk_bf16_f32 v117, v90, v91\n\t" "v_mfma_f32_32x32x16_bf16 v[96:111], v[36:39], a[244:247], v[96:111]\n\t" \
    "buffer_store_dwordx4 v[112:115], %[zo], %[rs], s63 offen offset:0 sc1\n\t" "v_cvt_pk_bf16_f32 v118, v92, v93\n\t" "v_cvt_pk_bf16_f32 v119, v94, v95\n\t" \
    "v_mfma_f32_32x32x16_bf16 v[96:111], v[40:43], a[248:251], v[96:111]\n\t" "s_waitcnt vmcnt(11)\n\t" "v_readfirstlane_b32 s66, v120\n\t" \
    "v_readfirstlane_b32 s67, v121\n\t" "v_mov_b32 v122, s60\n\t" "buffer_store_dword v122, %[fo], %[rc], %[fz] offen sc1\n\t" \
    "v_mfma_f32_32x32x16_bf16 v[96:111], v[44:47], a[252:255], v[96:111]\n\t" "buffer_store_dwordx4 v[116:119], %[zo], %[rs], s63 offen offset:512 sc1\n\t" \
    "s_add_u32 s63, s63, 0x20000\n\t" "s_max_u32 s61, s61, s66\n\t" "s_max_u32 s62, s62, s67\n\t" "s_add_u32 s60, s60, 1\n\t" "s_cmp_eq_u32 s65, 0\n\t" ZSUM \
    "s_cbranch_scc1 20f\n\t" "23:\n\t" XT(80, 81) "s_add_u32 s66, s62, %[rg]\n\t" "s_cmp_lt_u32 s66, s60\n\t" "s_cbranch_scc1 70f\n\t" "73:\n\t" \
    "v_mfma_f32_32x32x16_bf16 v[80:95], v[48:51], a[0:3], 0\n\t" "s_add_u32 s67, s60, 1\n\t" "s_lshr_b32 s66, s67, 2\n\t" "s_xor_b32 s66, s66, %[xp]\n\t" \
    "s_and_b32 s66, s66, 1\n\t" "s_lshl_b32 s66, s66, 13\n\t" "v_mfma_f32_32x32x16_bf16 v[80:95], v[52:55], a[4:7], v[80:95]\n\t" "s_lshl_b32 s64, s67, 19\n\t" \
    "s_add_u32 s64, s64, s66\n\t" "s_add_u32 s64, s64, %[vb]\n\t" "s_cmp_gt_u32 s61, s67\n\t" "s_cselect_b32 s65, 1, 0\n\t" \
    "v_mfma_f32_32x32x16_bf16 v[80:95], v[56:59], a[8:11], v[80:95]\n\t" "s_cselect_b32 s64, s64, 0x7f000000\n\t" \
    "v_mfma_f32_32x32x16_bf16 v[80:95], v[60:63], a[12:15], v[80:95]\n\t" "v_cvt_pk_bf16_f32 v112, v96, v97\n\t" "v_cvt_pk_bf16_f32 v113, v98, v99\n\t" \
    "v_cvt_pk_bf16_f32 v114, v100, v101\n\t" "v_mfma_f32_32x32x16_bf16 v[80:95], v[64:67], a[16:19], v[80:95]\n\t" "v_cvt_pk_bf16_f32 v115, v102, v103\n\t" \
    "v_cvt_pk_bf16_f32 v116, v104, v105\n\t" "v_cvt_pk_bf16_f32 v117, v106, v107\n\t" "v_mfma_f32_32x32x16_bf16 v[80:95], v[68:71], a[20:23], v[80:95]\n\t" \
    "buffer_store_dwordx4 v[112:115], %[zo], %[rs], s63 offen offset:0 sc1\n\t" "v_cvt_pk_bf16_f32 v118, v108, v109\n\t" "v_cvt_pk_bf16_f32 v119, v110, v111\n\t" \
    "v_mfma_f32_32x32x16_bf16 v[80:95], v[72:75], a[24:27], v[80:95]\n\t" "v_mfma_f32_32x32x16_bf16 v[80:95], v[76:79], a[28:31], v[80:95]\n\t" \
    "buffer_store_dwordx4 v[116:119], %[zo], %[rs], s63 offen offset:512 sc1\n\t" "s_and_b32 s63, s60, %[rm]\n\t" "s_lshl_b32 s63, s63, 22\n\t" \
    "s_add_u32 s63, s63, %[zb]\n\t" "v_mfma_f32_32x32x16_bf16 v[96:111], v[48:51], a[32:35], 0\n\t" "buffer_load_dwordx4 v[16:19], %[ao], %[rs], s64 offen offset:0 sc1\n\t" \
    "s_nop 2\n\t" "v_mfma_f32_32x32x16_bf16 v[96:111], v[52:55], a[36:39], v[96:111]\n\t" "buffer_load_dwordx4 v[20:23], %[ao], %[rs], s64 offen offset:1024 sc1\n\t" \
    "s_nop 2\n\t" "v_mfma_f32_32x32x16_bf16 v[96:111], v[56:59], a[40:43], v[96:111]\n\t" "buffer_load_dwordx4 v[24:27], %[ao], %[rs], s64 offen offset:2048 sc1\n\t" \
    "s_nop 1\n\t" "v_mfma_f32_32x32x16_bf16 v[96:111], v[60:63], a[44:47], v[96:111]\n\t" "buffer_load_dwordx4 v[28:31], %[ao], %[rs], s64 offen offset:3072 sc1\n\t" \
    "v_cvt_pk_bf16_f32 v112, v80, v81\n\t" "v_cvt_pk_bf16_f32 v113, v82, v83\n\t" "v_cvt_pk_bf16_f32 v114, v84, v85\n\t" \
    "v_mfma_f32_32x32x16_bf16 v[96:111], v[64:67], a[48:51], v[96:111]\n\t" "buffer_load_dwordx4 v[32:35], %[a1], %[rs], s64 offen offset:0 sc1\n\t" \
    "v_cvt_pk_bf16_f32 v115, v86, v87\n\t" "v_cvt_pk_bf16_f32 v116, v88, v89\n\t" "v_cvt_pk_bf16_f32 v117, v90, v91\n\t" \
    "v_mfma_f32_32x32x16_bf16 v[96:111], v[68:71], a[52:55], v[96:111]\n\t" "buffer_load_dwordx4 v[36:39], %[a1], %[rs], s64 offen offset:1024 sc1\n\t" \
    "buffer_store_dwordx4 v[112:115], %[zo], %[rs], s63 offen offset:0 sc1\n\t" "v_cvt_pk_bf16_f32 v118, v92, v93\n\t" "v_cvt_pk_bf16_f32 v119, v94, v95\n\t" \
    "v_mfma_f32_32x32x16_bf16 v[96:111], v[72:75], a[56:59], v[96:111]\n\t" "buffer_load_dwordx4 v[40:43], %[a1], %[rs], s64 offen offset:2048 sc1\n\t" \
    "v_mfma_f32_32x32x16_bf16 v[96:111], v[76:79], a[60:63], v[96:111]\n\t" "buffer_load_dwordx4 v[44:47], %[a1], %[rs], s64 offen offset:3072 sc1\n\t" \
    "buffer_store_dwordx4 v[116:119], %[zo], %[rs], s63 offen offset:512 sc1\n\t" "s_add_u32 s63, s63, 0x20000\n\t" \
    "v_mfma_f32_32x32x16_bf16 v[80:95], v[48:51], a[64:67], 0\n\t" "buffer_load_dword v120, %[fo], %[rc], %[fv] offen sc1\n\t" "s_nop 2\n\t" \
    "v_mfma_f32_32x32x16_bf16 v[80:95], v[52:55], a[68:71], v[80:95]\n\t" "buffer_load_dword v121, %[fo], %[rc], %[fs] offen sc1\n\t" "s_nop 2\n\t" \
    "v_mfma_f32_32x32x16_bf16 v[80:95], v[56:59], a[72:75], v[80:95]\n\t" "s_nop 1\n\t" "v_mfma_f32_32x32x16_bf16 v[80:95], v[60:63], a[76:79], v[80:95]\n\t" \
    "v_cvt_pk_bf16_f32 v112, v96, v97\n\t" "v_cvt_pk_bf16_f32 v113, v98, v99\n\t" "v_cvt_pk_bf16_f32 v114, v100, v101\n\t" \
    "v_mfma_f32_32x32x16_bf16 v[80:95], v[64:67], a[80:83], v[80:95]\n\t" "v_cvt_pk_bf16_f32 v115, v102, v103\n\t" "v_cvt_pk_bf16_f32 v116, v104, v105\n\t" \
    "v_cvt_pk_bf16_f32 v117, v106, v107\n\t" "v_mfma_f32_32x32x16_bf16 v[80:95], v[68:71], a[84:87], v[80:95]\n\t" \
    "buffer_store_dwordx4 v[112:115], %[zo], %[rs], s63 offen offset:0 sc1\n\t" "v_cvt_pk_bf16_f32 v118, v108, v109\n\t" "v_cvt_pk_bf16_f32 v119, v110, v111\n\t" \
    "v_mfma_f32_32x32x16_bf16 v[80:95], v[72:75], a[88:91], v[80:95]\n\t" "v_mfma_f32_32x32x16_bf16 v[80:95], v[76:79], a[92:95], v[80:95]\n\t" \
    "buffer_store_dwordx4 v[116:119], %[zo], %[rs], s63 offen offset:512 sc1\n\t" "s_add_u32 s63, s63, 0x20000\n\t" \
    "v_mfma_f32_32x32x16_bf16 v[96:111], v[48:51], a[96:99], 0\n\t" "s_nop 2\n\t" "v_mfma_f32_32x32x16_bf16 v[96:111], v[52:55], a[100:103], v[96:111]\n\t" "s_nop 2\n\t" \
    "v_mfma_f32_32x32x16_bf16 v[96:111], v[56:59], a[104:107], v[96:111]\n\t" "s_nop 1\n\t" "v_mfma_f32_32x32x16_bf16 v[96:111], v[60:63], a[108:111], v[96:111]\n\t" \
    "v_cvt_pk_bf16_f32 v112, v80, v81\n\t" "v_cvt_pk_bf16_f32 v113, v82, v83\n\t" "v_cvt_pk_bf16_f32 v114, v84, v85\n\t" \
    "v_mfma_f32_32x32x16_bf16 v[96:111], v[64:67], a[112:115], v[96:111]\n\t" "v_cvt_pk_bf16_f32 v115, v86, v87\n\t" "v_cvt_pk_bf16_f32 v116, v88, v89\n\t" \
    "v_cvt_pk_bf16_f32 v117, v90, v91\n\t" "v_mfma_f32_32x32x16_bf16 v[96:111], v[68:71], a[116:119], v[96:111]\n\t" \
    "buffer_store_dwordx4 v[112:115], %[zo], %[rs], s63 offen offset:0 sc1\n\t" "v_cvt_pk_bf16_f32 v118, v92, v93\n\t" "v_cvt_pk_bf16_f32 v119, v94, v95\n\t" \
    "v_mfma_f32_32x32x16_bf16 v[96:111], v[72:75], a[120:123], v[96:111]\n\t" "v_mfma_f32_32x32x16_bf16 v[96:111], v[76:79], a[124:127], v[96:111]\n\t" \
    "buffer_store_dwordx4 v[116:119], %[zo], %[rs], s63 offen offset:512 sc1\n\t" "s_add_u32 s63, s63, 0x20000\n\t" \
    "v_mfma_f32_32x32x16_bf16 v[80:95], v[48:51], a[128:131], 0\n\t" "s_nop 2\n\t" "v_mfma_f32_32x32x16_bf16 v[80:95], v[52:55], a[132:135], v[80:95]\n\t" "s_nop 2\n\t" \
    "v_mfma_f32_32x32x16_bf16 v[80:95], v[56:59], a[136:139], v[80:95]\n\t" "s_nop 1\n\t" "v_mfma_f32_32x32x16_bf16 v[80:95], v[60:63], a[140:143], v[80:95]\n\t" \
    "v_cvt_pk_bf16_f32 v112, v96, v97\n\t" "v_cvt_pk_bf16_f32 v113, v98, v99\n\t" "v_cvt_pk_bf16_f32 v114, v100, v101\n\t" \
    "v_mfma_f32_32x32x16_bf16 v[80:95], v[64:67], a[144:147], v[80:95]\n\t" "v_cvt_pk_bf16_f32 v115, v102, v103\n\t" "v_cvt_pk_bf16_f32 v116, v104, v105\n\t" \
    "v_cvt_pk_bf16_f32 v117, v106, v107\n\t" "v_mfma_f32_32x32x16_bf16 v[80:95], v[68:71], a[148:151], v[80:95]\n\t" \
    "buffer_store_dwordx4 v[112:115], %[zo], %[rs], s63 offen offset:0 sc1\n\t" "v_cvt_pk_bf16_f32 v118, v108, v109\n\t" "v_cvt_pk_bf16_f32 v119, v110, v111\n\t" \
    "v_mfma_f32_32x32x16_bf16 v[80:95], v[72:75], a[152:155], v[80:95]\n\t" "v_mfma_f32_32x32x16_bf16 v[80:95], v[76:79], a[156:159], v[80:95]\n\t" \
    "buffer_store_dwordx4 v[116:119], %[zo], %[rs], s63 offen offset:512 sc1\n\t" "s_add_u32 s63, s63, 0x20000\n\t" \
    "v_mfma_f32_32x32x16_bf16 v[96:111], v[48:51], a[160:163], 0\n\t" "s_nop 2\n\t" "v_mfma_f32_32x32x16_bf16 v[96:111], v[52:55], a[164:167], v[96:111]\n\t" "s_nop 2\n\t" \
    "v_mfma_f32_32x32x16_bf16 v[96:111], v[56:59], a[168:171], v[96:111]\n\t" "s_nop 1\n\t" "v_mfma_f32_32x32x16_bf16 v[96:111], v[60:63], a[172:175], v[96:111]\n\t" \
    "v_cvt_pk_bf16_f32 v112, v80, v81\n\t" "v_cvt_pk_bf16_f32 v113, v82, v83\n\t" "v_cvt_pk_bf16_f32 v114, v84, v85\n\t" \
    "v_mfma_f32_32x32x16_bf16 v[96:111], v[64:67], a[176:179], v[96:111]\n\t" "v_cvt_pk_bf16_f32 v115, v86, v87\n\t" "v_cvt_pk_bf16_f32 v116, v88, v89\n\t" \
    "v_cvt_pk_bf16_f32 v117, v90, v91\n\t" "v_mfma_f32_32x32x16_bf16 v[96:111], v[68:71], a[180:183], v[96:111]\n\t" \
    "buffer_store_dwordx4 v[112:115], %[zo], %[rs], s63 offen offset:0 sc1\n\t" "v_cvt_pk_bf16_f32 v118, v92, v93\n\t" "v_cvt_pk_bf16_f32 v119, v94, v95\n\t" \
    "v_mfma_f32_32x32x16_bf16 v[96:111], v[72:75], a[184:187], v[96:111]\n\t" "v_mfma_f32_32x32x16_bf16 v[96:111], v[76:79], a[188:191], v[96:111]\n\t" \
    "buffer_store_dwordx4 v[116:119], %[zo], %[rs], s63 offen offset:512 sc1\n\t" "s_add_u32 s63, s63, 0x20000\n\t" \
    "v_mfma_f32_32x32x16_bf16 v[80:95], v[48:51], a[192:195], 0\n\t" "s_nop 2\n\t" "v_mfma_f32_32x32x16_bf16 v[80:95], v[52:55], a[196:199], v[80:95]\n\t" "s_nop 2\n\t" \
    "v_mfma_f32_32x32x16_bf16 v[80:95], v[56:59], a[200:203], v[80:95]\n\t" "s_nop 1\n\t" "v_mfma_f32_32x32x16_bf16 v[80:95], v[60:63], a[204:207], v[80:95]\n\t" \
    "v_cvt_pk_bf16_f32 v112, v96, v97\n\t" "v_cvt_pk_bf16_f32 v113, v98, v99\n\t" "v_cvt_pk_bf16_f32 v114, v100, v101\n\t" \
    "v_mfma_f32_32x32x16_bf16 v[80:95], v[64:67], a[208:211], v[80:95]\n\t" "v_cvt_pk_bf16_f32 v115, v102, v103\n\t" "v_cvt_pk_bf16_f32 v116, v104, v105\n\t" \
    "v_cvt_pk_bf16_f32 v117, v106, v107\n\t" "v_mfma_f32_32x32x16_bf16 v[80:95], v[68:71], a[212:215], v[80:95]\n\t" \
    "buffer_store_dwordx4 v[112:115], %[zo], %[rs], s63 offen offset:0 sc1\n\t" "v_cvt_pk_bf16_f32 v118, v108, v109\n\t" "v_cvt_pk_bf16_f32 v119, v110, v111\n\t" \
    "v_mfma_f32_32x32x16_bf16 v[80:95], v[72:75], a[216:219], v[80:95]\n\t" "v_mfma_f32_32x32x16_bf16 v[80:95], v[76:79], a[220:223], v[80:95]\n\t" \
    "buffer_store_dwordx4 v[116:119], %[zo], %[rs], s63 offen offset:512 sc1\n\t" "s_add_u32 s63, s63, 0x20000\n\t" \
    "v_mfma_f32_32x32x16_bf16 v[96:111], v[48:51], a[224:227], 0\n\t" "s_nop 2\n\t" "v_mfma_f32_32x32x16_bf16 v[96:111], v[52:55], a[228:231], v[96:111]\n\t" "s_nop 2\n\t" \
    "v_mfma_f32_32x32x16_bf16 v[96:111], v[56:59], a[232:235], v[96:111]\n\t" "s_nop 1\n\t" "v_mfma_f32_32x32x16_bf16 v[96:111], v[60:63], a[236:239], v[96:111]\n\t" \
    "v_cvt_pk_bf16_f32 v112, v80, v81\n\t" "v_cvt_pk_bf16_f32 v113, v82, v83\n\t" "v_cvt_pk_bf16_f32 v114, v84, v85\n\t" \
    "v_mfma_f32_32x32x16_bf16 v[96:111], v[64:67], a[240:243], v[96:111]\n\t" "v_cvt_pk_bf16_f32 v115, v86, v87\n\t" "v_cvt_pk_bf16_f32 v116, v88, v89\n\t" \
    "v_cvt_pk_bf16_f32 v117, v90, v91\n\t" "v_mfma_f32_32x32x16_bf16 v[96:111], v[68:71], a[244:247], v[96:111]\n\t" \
    "buffer_store_dwordx4 v[112:115], %[zo], %[rs], s63 offen offset:0 sc1\n\t" "v_cvt_pk_bf16_f32 v118, v92, v93\n\t" "v_cvt_pk_bf16_f32 v119, v94, v95\n\t" \
    "v_mfma_f32_32x32x16_bf16 v[96:111], v[72:75], a[248:251], v[96:111]\n\t" "s_waitcnt vmcnt(11)\n\t" "v_readfirstlane_b32 s66, v120\n\t" \
    "v_readfirstlane_b32 s67, v121\n\t" "v_mov_b32 v122, s60\n\t" "buffer_store_dword v122, %[fo], %[rc], %[fz] offen sc1\n\t" \
    "v_mfma_f32_32x32x16_bf16 v[96:111], v[76:79], a[252:255], v[96:111]\n\t" "buffer_store_dwordx4 v[116:119], %[zo], %[rs], s63 offen offset:512 sc1\n\t" \
    "s_add_u32 s63, s63, 0x20000\n\t" "s_max_u32 s61, s61, s66\n\t" "s_max_u32 s62, s62, s67\n\t" "s_add_u32 s60, s60, 1\n\t" "s_cmp_eq_u32 s65, 0\n\t" ZSUM \
    "s_cbranch_scc1 30f\n\t" "33:\n\t" "s_branch 10b\n\t" "20:\n\t" "s_nop 7\n\t" "v_cvt_pk_bf16_f32 v112, v96, v97\n\t" "v_cvt_pk_bf16_f32 v113, v98, v99\n\t" \
    "v_cvt_pk_bf16_f32 v114, v100, v101\n\t" "v_cvt_pk_bf16_f32 v115, v102, v103\n\t" "v_cvt_pk_bf16_f32 v116, v104, v105\n\t" "v_cvt_pk_bf16_f32 v117, v106, v107\n\t" \
    "v_cvt_pk_bf16_f32 v118, v108, v109\n\t" "v_cvt_pk_bf16_f32 v119, v110, v111\n\t" "buffer_store_dwordx4 v[112:115], %[zo], %[rs], s63 offen offset:0 sc1\n\t" \
    "buffer_store_dwordx4 v[116:119], %[zo], %[rs], s63 offen offset:512 sc1\n\t" "s_waitcnt vmcnt(0)\n\t" "v_mov_b32 v122, s60\n\t" \
    "buffer_store_dword v122, %[fo], %[rc], %[fz] offen sc1\n\t" "s_cmp_eq_u32 s60, 64\n\t" "s_cbranch_scc1 90f\n\t" "s_add_u32 s67, s60, 1\n\t" "s_cmp_ge_u32 s61, s67\n\t" \
    "s_cbranch_scc1 22f\n\t" "21:\n\t" "buffer_load_dword v120, %[fo], %[rc], %[fv] offen sc1\n\t" "s_waitcnt vmcnt(0)\n\t" "v_readfirstlane_b32 s61, v120\n\t" "s_nop 3\n\t" \
    "s_cmp_ge_u32 s61, s67\n\t" "s_cbranch_scc1 22f\n\t" "s_cmp_eq_u32 %[bd], 0\n\t" "s_cbranch_scc1 22f\n\t" "s_sub_u32 %[bd], %[bd], 1\n\t" "s_sleep 2\n\t" \
    "s_branch 21b\n\t" "22:\n\t" "s_lshr_b32 s66, s60, 2\n\t" "s_xor_b32 s66, s66, %[xp]\n\t" "s_and_b32 s66, s66, 1\n\t" "s_lshl_b32 s66, s66, 13\n\t" \
    "s_lshl_b32 s64, s60, 19\n\t" "s_add_u32 s64, s64, s66\n\t" "s_add_u32 s64, s64, %[vb]\n\t" "buffer_load_dwordx4 v[48:51], %[ao], %[rs], s64 offen offset:0 sc1\n\t" \
    "buffer_load_dwordx4 v[52:55], %[ao], %[rs], s64 offen offset:1024 sc1\n\t" "buffer_load_dwordx4 v[56:59], %[ao], %[rs], s64 offen offset:2048 sc1\n\t" \
    "buffer_load_dwordx4 v[60:63], %[ao], %[rs], s64 offen offset:3072 sc1\n\t" "buffer_load_dwordx4 v[64:67], %[a1], %[rs], s64 offen offset:0 sc1\n\t" \
    "buffer_load_dwordx4 v[68:71], %[a1], %[rs], s64 offen offset:1024 sc1\n\t" "buffer_load_dwordx4 v[72:75], %[a1], %[rs], s64 offen offset:2048 sc1\n\t" \
    "buffer_load_dwordx4 v[76:79], %[a1], %[rs], s64 offen offset:3072 sc1\n\t" "s_waitcnt vmcnt(0)\n\t" "s_branch 23b\n\t" "60:\n\t" "s_sub_u32 s67, s60, %[rg]\n\t" \
    "s_cmp_ge_u32 s62, s67\n\t" "s_cbranch_scc1 62f\n\t" "61:\n\t" "buffer_load_dword v120, %[fo], %[rc], %[fs] offen sc1\n\t" "s_waitcnt vmcnt(0)\n\t" \
    "v_readfirstlane_b32 s62, v120\n\t" "s_nop 3\n\t" "s_cmp_ge_u32 s62, s67\n\t" "s_cbranch_scc1 62f\n\t" "s_cmp_eq_u32 %[bd], 0\n\t" "s_cbranch_scc1 62f\n\t" \
    "s_sub_u32 %[bd], %[bd], 1\n\t" "s_sleep 2\n\t" "s_branch 61b\n\t" "62:\n\t" "s_branch 63b\n\t" "30:\n\t" "s_nop 7\n\t" "v_cvt_pk_bf16_f32 v112, v96, v97\n\t" \
    "v_cvt_pk_bf16_f32 v113, v98, v99\n\t" "v_cvt_pk_bf16_f32 v114, v100, v101\n\t" "v_cvt_pk_bf16_f32 v115, v102, v103\n\t" "v_cvt_pk_bf16_f32 v116, v104, v105\n\t" \
    "v_cvt_pk_bf16_f32 v117, v106, v107\n\t" "v_cvt_pk_bf16_f32 v118, v108, v109\n\t" "v_cvt_pk_bf16_f32 v119, v110, v111\n\t" \
    "buffer_store_dwordx4 v[112:115], %[zo], %[rs], s63 offen offset:0 sc1\n\t" "buffer_store_dwordx4 v[116:119], %[zo], %[rs], s63 offen offset:512 sc1\n\t" \
    "s_waitcnt vmcnt(0)\n\t" "v_mov_b32 v122, s60\n\t" "buffer_store_dword v122, %[fo], %[rc], %[fz] offen sc1\n\t" "s_cmp_eq_u32 s60, 64\n\t" "s_cbranch_scc1 90f\n\t" \
    "s_add_u32 s67, s60, 1\n\t" "s_cmp_ge_u32 s61, s67\n\t" "s_cbranch_scc1 32f\n\t" "31:\n\t" "buffer_load_dword v120, %[fo], %[rc], %[fv] offen sc1\n\t" \
    "s_waitcnt vmcnt(0)\n\t" "v_readfirstlane_b32 s61, v120\n\t" "s_nop 3\n\t" "s_cmp_ge_u32 s61, s67\n\t" "s_cbranch_scc1 32f\n\t" "s_cmp_eq_u32 %[bd], 0\n\t" \
    "s_cbranch_scc1 32f\n\t" "s_sub_u32 %[bd], %[bd], 1\n\t" "s_sleep 2\n\t" "s_branch 31b\n\t" "32:\n\t" "s_lshr_b32 s66, s60, 2\n\t" "s_xor_b32 s66, s66, %[xp]\n\t" \
    "s_and_b32 s66, s66, 1\n\t" "s_lshl_b32 s66, s66, 13\n\t" "s_lshl_b32 s64, s60, 19\n\t" "s_add_u32 s64, s64, s66\n\t" "s_add_u32 s64, s64, %[vb]\n\t" \
    "buffer_load_dwordx4 v[16:19], %[ao], %[rs], s64 offen offset:0 sc1\n\t" "buffer_load_dwordx4 v[20:23], %[ao], %[rs], s64 offen offset:1024 sc1\n\t" \
    "buffer_load_dwordx4 v[24:27], %[ao], %[rs], s64 offen offset:2048 sc1\n\t" "buffer_load_dwordx4 v[28:31], %[ao], %[rs], s64 offen offset:3072 sc1\n\t" \
    "buffer_load_dwordx4 v[32:35], %[a1], %[rs], s64 offen offset:0 sc1\n\t" "buffer_load_dwordx4 v[36:39], %[a1], %[rs], s64 offen offset:1024 sc1\n\t" \
    "buffer_load_dwordx4 v[40:43], %[a1], %[rs], s64 offen offset:2048 sc1\n\t" "buffer_load_dwordx4 v[44:47], %[a1], %[rs], s64 offen offset:3072 sc1\n\t" \
    "s_waitcnt vmcnt(0)\n\t" "s_branch 33b\n\t" "70:\n\t" "s_sub_u32 s67, s60, %[rg]\n\t" "s_cmp_ge_u32 s62, s67\n\t" "s_cbranch_scc1 72f\n\t" "71:\n\t" \
    "buffer_load_dword v120, %[fo], %[rc], %[fs] offen sc1\n\t" "s_waitcnt vmcnt(0)\n\t" "v_readfirstlane_b32 s62, v120\n\t" "s_nop 3\n\t" "s_cmp_ge_u32 s62, s67\n\t" \
    "s_cbranch_scc1 72f\n\t" "s_cmp_eq_u32 %[bd], 0\n\t" "s_cbranch_scc1 72f\n\t" "s_sub_u32 %[bd], %[bd], 1\n\t" "s_sleep 2\n\t" "s_branch 71b\n\t" "72:\n\t" \
    "s_branch 73b\n\t" "90:\n\t"

#define T_DQ 1480
#define T_DV 1540
#define T_M1 150
#define T_DY 700
#define T_DX 1250
#define T_MIN 35
#define T_PEN 600
#define T_RETRY 1100
#define T_POLL 3
#define T_RST 12
#define T_U0 540
constexpr int T_OVH = 118;
constexpr unsigned NS(int ns) { return (unsigned)(ns * 64 / 10); }
struct Clk { unsigned cyc, g, k1, k2; };
DEV void anchor(Clk& k) {
  unsigned long long c, r[8];
  asm volatile("s_memtime %0\n\ts_memrealtime %1\n\ts_nop 2\n\ts_memrealtime %2\n\ts_nop 2\n\ts_memrealtime %3\n\ts_nop 2\n\ts_memrealtime %4\n\ts_nop 2\n\t"
               "s_memrealtime %5\n\ts_nop 2\n\ts_memrealtime %6\n\ts_nop 2\n\ts_memrealtime %7\n\ts_waitcnt lgkmcnt(0)"
               : "=s"(c), "=s"(r[0]), "=s"(r[1]), "=s"(r[2]), "=s"(r[3]), "=s"(r[4]), "=s"(r[5]), "=s"(r[6]) :: "memory");
  const unsigned a = (unsigned)r[0];
  unsigned n = 0;
#pragma unroll
  for (int i = 1; i < 7; ++i) asm("s_cmp_eq_u32 %1, %2\n\ts_addc_u32 %0, %0, 0" : "+s"(n) : "s"((unsigned)r[i]), "s"(a) : "scc");
  const unsigned nx = (a + 4u) << 6, cy = (unsigned)c + 12u + 16u * n;
  const float dc = (float)(cy - k.cyc), dg = (float)(nx - k.g);
  asm volatile("s_nop 0\n\tv_readfirstlane_b32 %0, %2\n\tv_readfirstlane_b32 %1, %3\n\ts_nop 3\n\ts_min_u32 %0, %0, 0x80000\n\ts_min_u32 %1, %1, 0x8000"
               : "=&s"(k.k1), "=&s"(k.k2) : "v"((unsigned)(dg * __builtin_amdgcn_rcpf(dc) * 65536.f)), "v"((unsigned)(dc * __builtin_amdgcn_rcpf(dg) * 65536.f)) : "scc");
  k.cyc = cy; k.g = nx;
}
DEV unsigned to_cyc(const Clk& k, unsigned g) {
  unsigned d;
  asm("s_max_i32 %0, %1, 0xffff8000\n\ts_min_i32 %0, %0, 0x8000\n\ts_mul_i32 %0, %0, %2\n\ts_ashr_i32 %0, %0, 16" : "=&s"(d) : "s"(g - k.g), "s"(k.k2) : "scc");
  return k.cyc + d - T_OVH;
}
DEV void wait_cyc(unsigned t) { asm volatile(TIMED_WAIT :: "s"(t) : CL_S); }
DEV int u_slot(int n, int par, int para) { return n == 2 ? R_U2 + par * 8192 : R_U + n * MB + para * 8192; }
DEV unsigned ask_stamps(Ctx& c, int par, int slot) { return __builtin_amdgcn_raw_buffer_load_b32(c.rs, amap(24576, par) + fresh_lane() * 128, slot, AUX_SC1); }
DEV unsigned latest(Ctx& c, const Clk& k, int par, int slot, unsigned v) {
  if (__builtin_expect(!allok(v), 0)) {
#pragma unroll 1
    do {
      __builtin_amdgcn_s_sleep(T_POLL);
      asm volatile("" ::: "memory");
      const unsigned t = __builtin_amdgcn_raw_buffer_load_b32(c.rs, v == SENT ? amap(24576, par) + fresh_lane() * 128 : 1 << 30, slot, AUX_SC1);
      v = v == SENT ? t : v;
    } while (!allok(v) && !spend(c));
  }
  int d = (int)(v - k.g);
  d = max(d, __builtin_amdgcn_update_dpp(d, d, 0x128, 0xf, 0xf, true));
  d = max(d, __builtin_amdgcn_update_dpp(d, d, 0x124, 0xf, 0xf, true));
  d = max(d, __builtin_amdgcn_update_dpp(d, d, 0x122, 0xf, 0xf, true));
  d = max(d, __builtin_amdgcn_update_dpp(d, d, 0x121, 0xf, 0xf, true));
  unsigned m;
  asm("s_max_i32 %0, %1, %2\n\ts_max_i32 %0, %0, %3\n\ts_max_i32 %0, %0, %4" : "=&s"(m)
      : "s"(__builtin_amdgcn_readlane(d, 0)), "s"(__builtin_amdgcn_readlane(d, 16)), "s"(__builtin_amdgcn_readlane(d, 32)), "s"(__builtin_amdgcn_readlane(d, 48)) : "scc");
  return k.g + m;
}
struct Plan { unsigned gx, dx; };
DEV unsigned smax(unsigned a, unsigned b) { return (int)(a - b) > 0 ? a : b; }
DEV unsigned second_try(const Plan& t, unsigned last) { return smax(last + NS(T_PEN), t.gx + t.dx + NS(T_M1 + T_RETRY)); }
DEV void advance(Plan& t, unsigned last) {
  const unsigned gy = t.gx + t.dx + NS(T_M1), dur = last - t.gx;
  const bool missed = (int)(last + NS(T_MIN) - gy) > 0;
  const unsigned nx = (missed ? second_try(t, last) : gy) + NS(T_DY);
  t.dx = dur < t.dx + NS(60) ? (3u * t.dx + dur) >> 2 : t.dx + NS(8);
  t.gx = nx;
}
DEV unsigned next_go(Ctx& c, Clk& k, Plan& t, int par, int slot, unsigned v) {
  SCHED();
  anchor(k);
  SCHED();
  advance(t, latest(c, k, par, slot, v));
  return to_cyc(k, t.gx);
}

DEV constexpr int qk_r(int j) { return j < 4 ? 0 : 1 + (j - 4) % 3; }
DEV constexpr int qk_i(int j) { return j < 4 ? j : (j - 4) / 3; }
DEV constexpr int qk_mate(int j) { return qk_r(j) == 2 ? qk_i(j) : 4 + 3 * qk_i(j) + ((qk_r(j) + 2) & 3) - 1; }
DEV void qk_role(const Params& p, Ctx& c, int h) {
  constexpr int WL = 0, MEMS = 131072, KXL = 147456, QKX = 155648, QROW = 272;
  c.budget = SPIN_CAP;
  const int w = c.w;
  Clk k = {0u, 0u, 0u, 0u};
  anchor(k);
  v8u wa[32], wh[16];
  {
    const int lane = fresh_lane(), g = lane >> 4, cc = lane & 15;
    auto kq = [&](int j) { return 4 * ((w + qk_r(j)) & 3) + (qk_i(j) ^ (h & 1)); };
    const rsrc_t rw = mkrs((w >> 1) ? p.Wk : p.Wq, 1024 * 4096 * 2);
    const int vo = krow_lane(g) * 8192 + (h * 128 + 64 * (w & 1) + 4 * cc) * 2;
    v2u buf[3][16];
#pragma unroll
    for (int b = 0; b < 3; ++b) {
#pragma unroll
      for (int r = 0; r < 16; ++r) buf[b][r] = ld8(rw, vo, (64 * kq(b) + krow(r >> 1, r & 1)) * 8192);
    }
    SCHED();
    claim(c, 0);
    SCHED();
    const int sw = opq(MEMS + w * 2048 + lane * 16), sr = opq(MEMS + (w ^ 2) * 2048 + lane * 16), lw = opq(WL + (w ^ 2) * 32768 + lane * 16);
#pragma unroll
    for (int ks = 0; ks < 16; ++ks) {
      v8u f[4];
#pragma unroll
      for (int t = 0; t < 4; ++t)
#pragma unroll
        for (int r = 0; r < 8; ++r) f[t][r] = prm(buf[ks % 3][2 * r + 1][t >> 1], buf[ks % 3][2 * r][t >> 1], t & 1);
      wa[2 * ks] = f[0]; wa[2 * ks + 1] = f[1];
#define M_(r) asm volatile("" : "+" r(wa[2 * ks]))
      AT_ACC(M_, 2 * ks);
#undef M_
#define M_(r) asm volatile("" : "+" r(wa[2 * ks + 1]))
      AT_ACC(M_, 2 * ks + 1);
#undef M_
      const int jm = qk_mate(ks);
      sts16(c.L, sw + (ks & 1) * 8192, lo4(f[2]));
      sts16(c.L, sw + (ks & 1) * 8192 + 1024, hi4(f[2]));
      sts16(c.L, lw + jm * 2048, lo4(f[3]));
      sts16(c.L, lw + jm * 2048 + 1024, hi4(f[3]));
      SCHED();
      if (ks + 3 < 16) {
#pragma unroll
        for (int r = 0; r < 16; ++r) buf[ks % 3][r] = ld8(rw, vo, (64 * kq(ks + 3) + krow(r >> 1, r & 1)) * 8192);
      }
      SCHED();
      BAR();
      wh[jm] = cat(lds16(c.L, sr + (ks & 1) * 8192), lds16(c.L, sr + (ks & 1) * 8192 + 1024));
#define M_(r) asm volatile("" : "+" r(wh[jm]))
      AT_HOME(M_, jm);
#undef M_
      SCHED();
    }
  }
  STAMP(0);
  BAR();
  const unsigned vote = wait_verdict(c);
  const int par = vote_b(vote), para = vote_a(vote);
  const int lane = fresh_lane(), g = lane >> 4, cc = lane & 15;
  const int moff = opq(amap(w * 4096 + lane * 16, par) + (h & 1) * 1024);
  const int idx = opq((lane & 1) ? 0xEEEE : 0x4444);
  TICK_DECL;
  const int l0 = opq(WL + w * 32768 + lane * 16), l1 = opq(MEMS + lane * 16);
  const int qw = opq(QKX + (4 * (w & 1) + (cc >> 2)) * 256 + g * 64 + (cc & 3) * 4), kw = opq(QKX + (8 + 2 * g) * QROW + (64 * (w & 1) + 4 * cc) * 2);
#define QK_PINS PIN_ACC0(wa); PIN_ACC1(wa); PIN_HOME(wh)
#define QK_IN [l0] "v"(l0), [l1] "v"(l1), [mo] "v"(moff), [ix] "v"(idx), [qw] "v"(qw), [kw] "v"(kw), [rs] "s"(c.rs), [sb] "s"(R_MEM + (s & 3) * MEM_SLOT)
#define QK_CL CL_S, CL_16_32, CL_160_200, CL_200_216, CL_216_256
  if (w == 3) {
    const int hq = opq(lane ? 1 << 30 : amap(24576 + h * 128, par));
    anchor(k);
    unsigned tg = k.cyc;
    Plan tt = {k.g - NS(100000), NS(T_DX)};
    for (int s = 0; s < NSEG; ++s) {
      const int pb = R_P + (s & 3) * P_SLOT;
      QK_PINS;
      asm volatile(QK_STEP3 : [bd] "+s"(c.budget) : QK_IN, [hq] "v"(hq), [pb] "s"(pb), [tg] "s"(tg), [ca] "s"(k.cyc - T_DQ), [k1] "s"(k.k1), [ga] "s"(k.g) : QK_CL);
      QK_PINS;
      tg = next_go(c, k, tt, par, pb, ask_stamps(c, par, pb));
      st4s(c.rs, hq, R_P + ((s + 2) & 3) * P_SLOT, fresh_sent());
      st4s(mkrs((const void*)c.ctrl, CTRL_BYTES), hq & (1 << 30), C_FLAG + (F_S + h) * 128, (unsigned)s + 1u);
      TICK_SUM(10);
    }
  } else {
    const int kx0 = opq(KXL + g * 512 + cc * 16), kmrd = opq(QKX + g * 16 + (8 + (cc & 7)) * QROW);
    unsigned seen = NOFLAG;
    if (w == 0) {
      const int prow = (2 * g + (cc & 1)) * 2 + ((cc >> 1) & 1);
      const int px = opq(amap((h >> 1) * 1024 + ((2 * (h & 1) + (cc >> 3)) * 16 + prow) * 16 + ((cc >> 2) & 1) * 8, par));
      const int pm = opq(cc < 8 ? amap(16384 + h * 128 + prow * 8 + (cc >> 2) * 4, par) : 1 << 30);
      const int ps = opq((cc & 1) ? 0x03020706 : 0x05040100);
      for (int s = 0; s < NSEG; ++s) {
        if (seen < (unsigned)s + 1u) seen = wait_flag(c, F_K + h, s + 1);
        QK_PINS;
        asm volatile(QK_STEP0 : [bd] "+s"(c.budget)
                     : QK_IN, [kx] "v"(kx0), [km] "v"(kmrd), [px] "v"(px), [pm] "v"(pm), [ps] "v"(ps), [pb] "s"(R_P + (s & 3) * P_SLOT), [pr] "s"(R_P + ((s + 1) & 3) * P_SLOT),
                       [kb] "s"(R_KX - MEMS + xblk(s, h, para)) : QK_CL);
        QK_PINS;
        TICK_SUM(10);
      }
    } else {
      const int qt = w - 1;
      const int px = opq(amap(qt ? 16384 + h * 1024 + lane * 16 : h * 512 + (g >> 1) * 256 + cc * 16 + (g & 1) * 8, para));
      const int pm = opq(g < 2 ? amap(49152 + (qt * 32 + h) * 256 + cc * 16 + g * 8, para) : 1 << 30);
      const int ps = opq(cc - 4 * g), hq = opq(g * 512 + (16 * qt + cc) * 16);
#define QK_TOK(STEP) for (int s = 0; s < NSEG; ++s) { QK_PINS; \
        asm volatile(STEP : [bd] "+s"(c.budget), [sq] "+s"(seen) \
                     : QK_IN, [kx] "v"(kx0), [km] "v"(kmrd), [px] "v"(px), [pm] "v"(pm), [ps] "v"(ps), [hq] "v"(hq), [qb] "s"(R_QX + xblk(s, h, para)), [tb] "s"(R_PT + (s & 7) * PT_SLOT), \
                       [tr] "s"(R_PT + ((s + 3) & 7) * PT_SLOT), [sn] "s"(s + 1), [fq] "s"(c.ctrl + (C_FLAG + (F_Q + h) * 128) / 4) : QK_CL); \
        QK_PINS; TICK_SUM(10); }
      if (w == 1) { QK_TOK(QK_STEP1) } else { QK_TOK(QK_STEP2) }
    }
  }
  TICK_OUT();
  if (c.budget == 0u) __hip_atomic_store(c.ctrl + C_TMO / 4, 1u, RLX_AGENT);
}

DEV void shuffle_cols(Ctx& c, int scratch, const v4u (&rows)[8], v4u (&o)[8]) {
  const int g = c.lane >> 4, cc = c.lane & 15;
#pragma unroll
  for (int i = 0; i < 8; ++i) {
    v4u f;
#pragma unroll
    for (int r = 0; r < 4; ++r) f[r] = prm(rows[2 * r + 1][i >> 1], rows[2 * r][i >> 1], i & 1);
    sts16(c.L, scratch + (8 * cc + i) * 64 + ((g ^ (cc & 3)) << 4), f);
  }
  asm volatile("s_waitcnt lgkmcnt(0)" ::: "memory");
#pragma unroll
  for (int i = 0; i < 8; ++i) { const int col = 16 * i + cc; o[i] = lds16(c.L, scratch + col * 64 + ((g ^ ((col >> 3) & 3)) << 4)); }
  asm volatile("s_waitcnt lgkmcnt(0)" ::: "memory");
}

DEV void vo_role(const Params& p, Ctx& c, int h) {
  constexpr int WL = 0, MEMS = 131072, VM = 147456, VROW = 272;
  c.budget = SPIN_CAP;
  const int w = c.w, lane = c.lane = fresh_lane(), g = lane >> 4, cc = lane & 15;
  Clk k = {0u, 0u, 0u, 0u};
  anchor(k);
  v8u wa[32], wh[16];
  const rsrc_t rwo = mkrs(p.Wo, 4096 * 1024 * 2);
  const int voo = (h * 128 + 4 * g) * 2048 + (256 * w + 8 * cc) * 2;
  v4u buf[3][8];
  auto request_o = [&](int u) {
#pragma unroll
    for (int r = 0; r < 8; ++r) buf[u % 3][r] = ld16(rwo, voo, (32 * (u & 3) + 16 * (r >> 2) + (r & 3)) * 2048 + (u >> 2) * 256);
  };
  {
    const rsrc_t rw = mkrs(p.Wv, 1024 * 4096 * 2);
    const int vo = (256 * w + krow_lane(g)) * 8192 + (h * 128 + 8 * cc) * 2;
    v4u vrow[8][8];
    auto request = [&](int u) {
#pragma unroll
      for (int r = 0; r < 8; ++r) vrow[u][r] = ld16(rw, vo, (64 * ((u >> 1) ^ (h & 1)) + krow(4 * (u & 1) + (r >> 1), r & 1)) * 8192);
    };
#pragma unroll
    for (int u = 0; u < 5; ++u) request(u);
    SCHED();
    claim(c, 0);
    SCHED();
    const int wr_ = MEMS + w * 8192 + cc * 512 + ((g ^ (cc & 3)) << 4);
#pragma unroll
    for (int u = 0; u < 8; ++u) {
#pragma unroll
      for (int i = 0; i < 8; ++i) {
        v4u f;
#pragma unroll
        for (int r = 0; r < 4; ++r) f[r] = prm(vrow[u][2 * r + 1][i >> 1], vrow[u][2 * r][i >> 1], i & 1);
        sts16(c.L, wr_ + i * 64, f);
      }
      SCHED();
      if (u + 5 < 8) request(u + 5); else if (u < 6) request_o(u - 3);
      BAR();
#pragma unroll
      for (int j = 0; j < 16; ++j) {
        if (qk_i(j) != (u >> 1)) continue;
#pragma unroll
        for (int t = 0; t < 2; ++t) {
          const int col = 32 * w + 2 * cc + t;
          const v4u o = lds16(c.L, MEMS + ((w + qk_r(j)) & 3) * 8192 + col * 64 + ((g ^ ((col >> 3) & 3)) << 4));
          if (t == 1 && (j & 1)) sts16(c.L, WL + (w * 16 + (j >> 1)) * 2048 + (u & 1) * 1024 + lane * 16, o);
          else {
            const int n = 3 * (j >> 1) + ((j & 1) ? 2 : t);
#pragma unroll
            for (int r = 0; r < 4; ++r) wa[n][4 * (u & 1) + r] = o[r];
#define M_(r) asm volatile("" : "+" r(wa[n]))
            if (u & 1) AT_ACC(M_, n);
#undef M_
          }
        }
      }
      BAR();
    }
  }
#pragma unroll
  for (int u = 0; u < 8; ++u) {
    v4u o[8];
    shuffle_cols(c, MEMS + w * 8192, buf[u % 3], o);
    SCHED();
    if (u + 3 < 8) request_o(u + 3);
#pragma unroll
    for (int i = 0; i < 4; ++i) {
      const int pr = 4 * (u >> 2) + i, ch = u & 3;
      const v8u f = {o[2 * i][0], o[2 * i + 1][0], o[2 * i][1], o[2 * i + 1][1], o[2 * i][2], o[2 * i + 1][2], o[2 * i][3], o[2 * i + 1][3]};
      if (ch < 2) {
        wh[2 * pr + ch] = f;
#define M_(r) asm volatile("" : "+" r(wh[2 * pr + ch]))
        AT_HOME(M_, 2 * pr + ch);
#undef M_
      } else if (ch == 2) {
        wa[24 + pr] = f;
#define M_(r) asm volatile("" : "+" r(wa[24 + pr]))
        AT_ACC(M_, 24 + pr);
#undef M_
      } else {
        sts16(c.L, WL + (w * 16 + 8 + pr) * 2048 + lane * 16, lo4(f));
        sts16(c.L, WL + (w * 16 + 8 + pr) * 2048 + 1024 + lane * 16, hi4(f));
      }
    }
    SCHED();
  }
  BAR();
  STAMP(0);
  const unsigned vote = wait_verdict(c);
  const int par = vote_b(vote), para = vote_a(vote);
  const int moff = opq(amap(w * 4096 + lane * 16, par) + (h & 1) * 1024);
  const int idx = opq((lane & 1) ? 0xEEEE : 0x4444);
  const int u_off = opq((16 * w + g) * 16384 + h * 256 + cc * 16);
  const int hq = opq((lane | w) ? 1 << 30 : amap(24576 + (32 + h) * 128, par));
  const int l0 = opq(WL + w * 32768 + lane * 16), l1 = opq(MEMS + lane * 16);
  const int vmw = opq(VM + 2 * g * VROW + (32 * w + 2 * cc) * 2);
  const int vmr = opq(VM + (cc >> 1) * VROW + g * 16);
  TICK_DECL;
  anchor(k);
  unsigned tg = k.cyc;
  Plan tt = {k.g - NS(100000), NS(T_DX)};
  int u3 = 0;
  for (int s = 0; s < NSEG; ++s) {
    const int pb = R_P + (s & 3) * P_SLOT;
    PIN_ACC0(wa); PIN_ACC1(wa); PIN_HOME(wh);
    asm volatile(VO_STEP
        : [bd] "+s"(c.budget)
        : [l0] "v"(l0), [l1] "v"(l1), [mo] "v"(moff), [ix] "v"(idx), [vw] "v"(vmw), [vr] "v"(vmr), [uo] "v"(u_off), [w] "s"(w), [rs] "s"(c.rs),
          [sb] "s"(R_MEM + (s & 3) * MEM_SLOT), [ub] "s"(u_slot(u3, par, para)), [tg] "s"(tg), [hq] "v"(hq), [pb] "s"(pb),
          [ca] "s"(k.cyc - T_DV), [k1] "s"(k.k1), [ga] "s"(k.g)
        : CL_S, CL_16_32, CL_160_200, CL_200_216, CL_216_256);
    PIN_ACC0(wa); PIN_ACC1(wa); PIN_HOME(wh);
    if (w == 0) tg = next_go(c, k, tt, par, pb, ask_stamps(c, par, pb)); else __builtin_amdgcn_s_sleep(T_RST);
    {
      const int ur = u_slot(u3 ? u3 - 1 : 2, par, para);
      const v4u se = {fresh_sent(), fresh_sent(), fresh_sent(), fresh_sent()};
#pragma unroll
      for (int i = 0; i < 4; ++i) st16s(c.rs, u_off, ur + i * 65536, se);
      st4s(c.rs, hq, R_P + ((s + 2) & 3) * P_SLOT, se[0]);
      u3 = u3 == 2 ? 0 : u3 + 1;
    }
    TICK_SUM(10);
  }
  TICK_OUT();
  if (c.budget == 0u) __hip_atomic_store(c.ctrl + C_TMO / 4, 1u, RLX_AGENT);
}

DEV void y_role(const Params& p, Ctx& c, int y) {
  constexpr int ZR = 0, UL = 131072, CW = 155648;
  c.budget = SPIN_CAP;
  const int w = c.w, lane = c.lane = fresh_lane(), g = lane >> 4, cc = lane & 15;
  const v4u sent4 = {SENT, SENT, SENT, SENT};
  Clk k = {0u, 0u, 0u, 0u};
  anchor(k);
  unsigned m0 = 0;
  if (w == 0) {
    unsigned best[4] = {~0u, ~0u, ~0u, ~0u};
#pragma unroll
    for (int r = 0; r < 8; ++r) {
      VMWAIT(0);
      const unsigned t0 = (unsigned)__builtin_amdgcn_s_memtime();
      LGKM(0);
      st4s(c.rs, ((r & 2) ? amap(y * 256, 0) : y * 16384) + (r & 1) * 8192 + (lane & 31) * 4, (r & 2) ? R_MEM : R_U, SENT);
      VMWAIT(0);
      const unsigned t1 = (unsigned)__builtin_amdgcn_s_memtime();
      LGKM(0);
      best[r & 3] = min(best[r & 3], t1 - t0);
    }
    if (lane < 8) *(LAS unsigned*)(c.L + CW + lane * 4) = 0u;
#pragma unroll
    for (int i = 0; i < 32; ++i) st16s(c.rs, y * 16384 + (i & 3) * 1024 + lane * 16, R_U + (i >> 4) * MB + ((i >> 2) & 3) * 4096, sent4);
#pragma unroll
    for (int i = 0; i < 6; ++i) st16s(c.rs, y * 6144 + lane * 16, R_MEM + i * 1024, sent4);
#pragma unroll
    for (int i = 0; i < 16; ++i) st16s(c.rs, y * 16384 + lane * 16, R_PT + i * 1024, sent4);
    m0 = *(const GAS unsigned*)(p.mem0 + (lane >> 3) * 1024 + 16 * y + 2 * (lane & 7));
    m0 = m0 < SENT - 1u ? m0 : SENT - 1u;
    VMWAIT(0);
    claim(c, (best[1] < best[0] ? 1u << 16 : 0u) | (best[3] < best[2] ? 1u << 24 : 0u));
  } else if (w < 3) {
#pragma unroll 1
    for (int i = 0; i < PULL_AT; ++i) __builtin_amdgcn_s_sleep(35);
    const int o = ((2 * y + w - 1) * 64 + lane) * 128;
    auto pull = [&](const u16* q) {
#pragma unroll
      for (int i = 0; i < 4; ++i) m0 += __builtin_amdgcn_raw_buffer_load_b32(mkrs(q, 4 * MB), o, i * MB, 0);
    };
    pull(p.bk); pull(p.bv); pull(p.ssmk); pull(p.bq);
    asm volatile("" :: "v"(m0));
  }
  BAR();
  const unsigned vote = wait_verdict(c);
  const int par = vote_b(vote), para = vote_a(vote);
  const int mo = amap(y * 256 + lane * 4, par);
  if (w == 0) st4s(c.rs, mo, R_MEM, m0);
  STAMP(0);
  volatile LAS unsigned* const GO = (volatile LAS unsigned*)(c.L + CW);
  TICK_DECL;
  if (w == 0) {
    int xo[4];
#pragma unroll
    for (int i = 0; i < 4; ++i) xo[i] = amap(i * 4096 + lane * 16, par);
    const int pmo = amap(16384 + 2 * g * 128 + cc * 8, par), psel = (cc & 1) ? 0x03020706 : 0x05040100;
    const int lead = T_U0 - 128 * ((y >> 2) & 3), late = __builtin_amdgcn_readfirstlane(opq(lead < 264));
    anchor(k);
    Plan tt = {k.g - NS(100000), NS(T_DX)};
    unsigned done = k.cyc;
    const int so = amap(24576, par) + lane * 128;
    const int idx = opq((lane & 1) ? 0xEEEE : 0x4444);
    const int uo = y * 16384 + lane * 16;
    int u3 = 0;
    const int mw = amap(y * 256 + (2 * g + (cc & 1)) * 32 + (cc >> 1) * 4, par);
    for (int s = 0; s < NSEG - 1; ++s) {
      if (s == 0) RAWBAR();
      v32u z0, z1, z2, z3;
      asm volatile(
          "ds_read_b128 a[0:3], %[za] offset:0\n\t" "ds_read_b128 a[4:7], %[za] offset:1024\n\t" "ds_read_b128 a[8:11], %[za] offset:2048\n\t" "ds_read_b128 a[12:15], %[za] offset:3072\n\t"
          "ds_read_b128 a[16:19], %[za] offset:4096\n\t" "ds_read_b128 a[20:23], %[za] offset:5120\n\t" "ds_read_b128 a[24:27], %[za] offset:6144\n\t" "ds_read_b128 a[28:31], %[za] offset:7168\n\t"
          "ds_read_b128 a[32:35], %[za] offset:8192\n\t" "ds_read_b128 a[36:39], %[za] offset:9216\n\t" "ds_read_b128 a[40:43], %[za] offset:10240\n\t" "ds_read_b128 a[44:47], %[za] offset:11264\n\t"
          "ds_read_b128 a[48:51], %[za] offset:12288\n\t" "ds_read_b128 a[52:55], %[za] offset:13312\n\t" "ds_read_b128 a[56:59], %[za] offset:14336\n\t" "ds_read_b128 a[60:63], %[za] offset:15360\n\t"
          "ds_read_b128 a[64:67], %[za] offset:16384\n\t" "ds_read_b128 a[68:71], %[za] offset:17408\n\t" "ds_read_b128 a[72:75], %[za] offset:18432\n\t" "ds_read_b128 a[76:79], %[za] offset:19456\n\t"
          "ds_read_b128 a[80:83], %[za] offset:20480\n\t" "ds_read_b128 a[84:87], %[za] offset:21504\n\t" "ds_read_b128 a[88:91], %[za] offset:22528\n\t" "ds_read_b128 a[92:95], %[za] offset:23552\n\t"
          "ds_read_b128 a[96:99], %[za] offset:24576\n\t" "ds_read_b128 a[100:103], %[za] offset:25600\n\t" "ds_read_b128 a[104:107], %[za] offset:26624\n\t" "ds_read_b128 a[108:111], %[za] offset:27648\n\t"
          "ds_read_b128 a[112:115], %[za] offset:28672\n\t" "ds_read_b128 a[116:119], %[za] offset:29696\n\t" "ds_read_b128 a[120:123], %[za] offset:30720\n\t" "ds_read_b128 a[124:127], %[za] offset:31744\n\t"
          "s_waitcnt lgkmcnt(0)"
          : "={a[0:31]}"(z0), "={a[32:63]}"(z1), "={a[64:95]}"(z2), "={a[96:127]}"(z3) : [za] "v"(ZR + (s & 3) * 32768 + lane * 16) : "memory");
      const int pb = R_P + (s & 3) * P_SLOT;
      v32u uu;
      const int ub = u_slot(u3, par, para);
      TICK(0);
      anchor(k);
      TICK(8);
      unsigned tg = to_cyc(k, tt.gx + tt.dx + NS(T_M1)), lt = late, sv = SENT;
      unsigned bad = (int)(done - to_cyc(k, tt.gx)) > T_OVH;
      TICK(4);
#pragma unroll 1
      for (;;) {
        if (bad) {
          const bool all = allok(sv);
          const unsigned last = latest(c, k, par, pb, sv);
          if (!all) anchor(k);
          tg = to_cyc(k, second_try(tt, last));
          lt = 1;
        }
        asm volatile(Y_STEP
          : "=&{v[144:175]}"(uu), [bad] "=&s"(bad), [sv] "=&v"(sv)
          : [x0] "v"(xo[0]), [x1] "v"(xo[1]), [x2] "v"(xo[2]), [x3] "v"(xo[3]), [pm] "v"(pmo), [u0] "v"(uo), [u1] "v"(uo + 4096), [mw] "v"(mw), [ps] "v"(psel), [ix] "v"(idx),
            [rs] "s"(c.rs), [pb] "s"(pb), [ub] "s"(ub), [mb] "s"(R_MEM + ((s + 1) & 3) * MEM_SLOT), [mr] "s"(R_MEM + ((s + 3) & 3) * MEM_SLOT), [tu] "s"(tg - (lt ? 128 : lead)), [tg] "s"(tg), [late] "s"(lt),
            [so] "v"(so), [bd] "s"(c.budget),
            "{a[0:31]}"(z0), "{a[32:63]}"(z1), "{a[64:95]}"(z2), "{a[96:127]}"(z3)
          : CL_S, CL_120_128, CL_176_200, CL_200_216, CL_216_256);
        bad = __builtin_amdgcn_readfirstlane(bad);
        if (bad != 1u) break;
        spend(c);
      }
      done = bad;
      SCHED();
      const int ul = opq(UL + lane * 16);
      u3 = u3 == 2 ? 0 : u3 + 1;
#pragma unroll
      for (int i = 0; i < 8; ++i) sts16(c.L, ul + i * 1024, v4u{uu[4 * i], uu[4 * i + 1], uu[4 * i + 2], uu[4 * i + 3]});
      asm volatile("" ::: "memory");
      *GO = s + 1;
      asm volatile("s_waitcnt vmcnt(2)" : "+v"(sv));
      advance(tt, latest(c, k, par, pb, sv));
      TICK(3);
      TICK_LDS(CW + 256);
    }
    {
      anchor(k);
      wait_cyc(to_cyc(k, tt.gx + tt.dx + NS(T_M1)));
      const int ub = u_slot(u3, par, para);
      v4u u[8];
#pragma unroll 1
      for (;;) {
        asm volatile("" ::: "memory");
#pragma unroll
        for (int i = 0; i < 8; ++i) u[i] = ld16s(c.rs, uo + (i & 3) * 1024, ub + (i >> 2) * 4096);
        SCHED();
        unsigned t = 0;
#pragma unroll
        for (int i = 0; i < 8; ++i) t = umax(t, top(u[i]));
        if (allok(t) || spend(c)) break;
      }
      RAWBAR();
#pragma unroll
      for (int i = 0; i < 8; ++i) sts16(c.L, UL + i * 1024 + lane * 16, u[i]);
      asm volatile("" ::: "memory");
      *GO = NSEG;
    }
    TICK_DUMP(CW + 256);
  } else if (w < 3) {
    const rsrc_t ro = mkrs(p.out, 2048 * 2048 * 2);
    auto tokens = [&](auto QT) {
      constexpr int qt = QT.value, NX = qt ? 32 : 16;
      RAWBAR();
      for (int s = 0; s < NSEG; ++s) {
        const int tb = R_PT + (s & 7) * PT_SLOT + para * 8192;
        const int ua = opq(UL + lane * 16);
        const int za = opq(ZR + (s & 3) * 32768 + (qt ? lane * 16 : (g >> 1) * 1024 + (g & 1) * 512 + cc * 16));
        SCHED();
        RAWBAR();
        TICK(0);
        f32x4 a;
#pragma unroll 1
        for (;;) {
          asm volatile("" ::: "memory");
          v4u pt[NX + 8];
#pragma unroll
          for (int i = 0; i < NX; ++i) pt[i] = ld16s(c.rs, (i & 3) * 1024 + lane * 16, tb + amap(qt * 16384 + (i >> 2) * 4096, 0));
#pragma unroll
          for (int i = 0; i < 8; ++i) pt[NX + i] = ld16s(c.rs, (i & 3) * 1024 + lane * 16, tb + amap(49152 + qt * 8192 + (i >> 2) * 4096, 0));
          SCHED();
          TICK(1);
#pragma unroll 1
          while (__builtin_amdgcn_readfirstlane(*GO) < (unsigned)s + 1u) { if (spend(c)) break; }
          asm volatile("" ::: "memory");
          f32x4 d[2] = {f32x4{0.f, 0.f, 0.f, 0.f}, f32x4{0.f, 0.f, 0.f, 0.f}};
          v4u fa[8];
          auto frag = [&](int i) {
            if (i >= NX) fa[i & 7] = lds16(c.L, ua + (i - NX) * 1024);
            else if (qt) fa[i & 7] = lds16(c.L, za + i * 1024);
            else {
              const v2u lo = *(LAS v2u*)(c.L + za + i * 2048), hi = *(LAS v2u*)(c.L + za + i * 2048 + 256);
              fa[i & 7] = v4u{lo.x, lo.y, hi.x, hi.y};
            }
          };
#pragma unroll
          for (int i = 0; i < 7; ++i) frag(i);
          SCHED();
#pragma unroll
          for (int i = 0; i < NX + 8; ++i) {
            if (i + 7 < NX + 8) frag(i + 7);
            d[i & 1] = mfma16(fa[i & 7], pt[i], d[i & 1]);
            SCHED();
          }
          a = d[0] + d[1];
          if (!__any(a[0] != a[0])) break;
          unsigned t = 0;
#pragma unroll
          for (int i = 0; i < NX + 8; ++i) t = umax(t, top(pt[i]));
          if (allok(t) || spend(c)) break;
        }
        __builtin_amdgcn_raw_buffer_store_b64(v2u{pk(a[0], a[1]), pk(a[2], a[3])}, ro, (16 * qt + cc) * 4096 + (16 * y + 4 * g) * 2, s * 32 * 4096, AUX_NT);
        TICK(2);
        TICK_SUM(3);
      }
    };
    if (w == 1) tokens(IC<0>{}); else tokens(IC<1>{});
  } else {
    unsigned seenZ = NOFLAG;
    const rsrc_t rc = mkrs((const void*)c.ctrl, CTRL_BYTES);
    const int fo = lane < 32 ? lane * 128 : 1 << 30;
    __builtin_amdgcn_s_setprio(3);
    auto least = [&](unsigned v) {
      v |= lane < 32 ? 0u : ~0u;
#pragma unroll
      for (int o = 1; o < 32; o <<= 1) v = min(v, (unsigned)__shfl_xor((int)v, o));
      seenZ = umax(seenZ, __builtin_amdgcn_readfirstlane(v));
    };
    for (int n = 0; n < NSEG; ++n) {
      TICK(0);
#pragma unroll 1
      while (seenZ < (unsigned)n + 1u) {
        asm volatile("" ::: "memory");
        least(__builtin_amdgcn_raw_buffer_load_b32(rc, fo, C_FLAG + (F_Z + (y >> 4) * 32) * 128, AUX_SC1));
        if (seenZ >= (unsigned)n + 1u || spend(c)) break;
        __builtin_amdgcn_s_sleep(4);
      }
      __builtin_amdgcn_fence(__ATOMIC_ACQUIRE, "wavefront");
      TICK(1);
      v4u z[32];
#pragma unroll
      for (int i = 0; i < 32; ++i) {
        z[i] = ld16s(c.rs, (i & 3) * 1024 + lane * 16, R_Z + (n & (ZRING - 1)) * Z_SLOT + y * 65536 + ((para ^ (y >> 5)) & 1) * 8192 + amap((i >> 2) * 4096, 0));
        SCHED();
        asm volatile("s_nop " STR(Z_GAP));
        SCHED();
      }
      const unsigned look = __builtin_amdgcn_raw_buffer_load_b32(rc, seenZ < (unsigned)n + 2u ? fo : 1 << 30, C_FLAG + (F_Z + (y >> 4) * 32) * 128, AUX_SC1);
      SCHED();
      TICK(4);
      const int za = opq(ZR + (n & 3) * 32768 + lane * 16);
#pragma unroll
      for (int i = 0; i < 32; ++i) {
        sts16(c.L, za + i * 1024, z[i]);
        if (i == 30) { TICK(3); }
      }
      least(look);
      TICK(2);
      { const int s = n; (void)s; TICK_SUM(3); }
      BAR();
    }
    RAWBAR();
  }
  TICK_OUT();
  if (c.budget == 0u) __hip_atomic_store(c.ctrl + C_TMO / 4, 1u, RLX_AGENT);
}

#define MF1A(d, a, b) asm volatile(MF16 "%0, %1, %2, %0" : "+v"(d) : "a"(a), "v"(b))
#define MF32A(d, a, b) asm volatile("v_mfma_f32_32x32x16_bf16 %0, %1, %2, %0" : "+v"(d) : "a"(a), "v"(b))
#define MF32AZ(d, a, b) asm volatile("v_mfma_f32_32x32x16_bf16 %0, %1, %2, 0" : "=&v"(d) : "a"(a), "v"(b))
#define MF1B(d, a, b) asm volatile(MF16 "%0, %1, %2, %0" : "+v"(d) : "v"(a), "a"(b))
#define MF1BZ(d, a, b) asm volatile(MF16 "%0, %1, %2, 0" : "=&v"(d) : "v"(a), "a"(b))
#define MF1AZ(d, a, b) asm volatile(MF16 "%0, %1, %2, 0" : "=&v"(d) : "a"(a), "v"(b))
DEV void g_role(const Params& p, Ctx& c, int mat, int h, int gi) {
  constexpr int EX = 131072;
  c.budget = SPIN_CAP;
  const int w = c.w, lane = c.lane = fresh_lane(), g = lane >> 4, cc = lane & 15, h2 = lane >> 5, r32 = lane & 31;
  if (mat == 0) {
    claim(c, 0);
    __builtin_amdgcn_s_sleep(GQ_SLEEP);
    unsigned t = 0;
#pragma unroll
    for (int i = 0; i < 4; ++i) t += __builtin_amdgcn_raw_buffer_load_b32(mkrs(p.x, 4 * MB), ((4 * h + w) * 64 + lane) * 128, i * MB, 0);
    asm volatile("" :: "v"(t));
  }
  const rsrc_t rx = mkrs(p.x, 2048 * 2048);
  const int xs = w * 16384 + ((lane * 16) ^ ((w & 1) << 7));
#pragma unroll
  for (int u = 0; u < 16; ++u) dma16(rx, c.L + w * 16384 + u * 1024, (xs ^ ((u >> 1) << 4)) + u * 1024);
  SCHED();
  v32u wa[8];
  {
    const rsrc_t rw = mkrs(mat == 0 ? p.Wq : mat == 1 ? p.Wk : p.Wv, 1024 * 4096 * 2);
    const int vo = (256 * w + 8 * g) * 8192 + (h * 128 + 8 * cc) * 2;
    v4u row[8][8];
    auto request = [&](int u) {
#pragma unroll
      for (int r = 0; r < 8; ++r) row[u][r] = ld16(rw, vo, (32 * u + r) * 8192);
    };
#pragma unroll
    for (int u = 0; u < 6; ++u) request(u);
    SCHED();
    if (mat != 0) claim(c, 0);
    SCHED();
    const int col = 32 * w + 16 * (r32 >> 4) + 8 * ((r32 >> 2) & 1) + 4 * ((r32 >> 3) & 1) + (r32 & 3);
    const int sw_ = opq(EX + w * 8192 + cc * 512 + ((g ^ (cc & 3)) << 4)), sr_ = EX + col * 64;
    const int sr0 = opq(sr_ + ((h2 ^ ((col >> 3) & 3)) << 4)), sr1 = opq(sr_ + (((2 + h2) ^ ((col >> 3) & 3)) << 4));
#pragma unroll
    for (int u = 0; u < 8; ++u) {
#pragma unroll
      for (int i = 0; i < 8; ++i) {
        v4u f;
#pragma unroll
        for (int r = 0; r < 4; ++r) f[r] = prm(row[u][2 * r + 1][i >> 1], row[u][2 * r][i >> 1], i & 1);
        sts16(c.L, sw_ + i * 64, f);
      }
      SCHED();
      if (u + 6 < 8) request(u + 6);
      BAR();
#pragma unroll
      for (int q = 0; q < 4; ++q) {
        v8u f = cat(lds16(c.L, sr0 + q * 8192), lds16(c.L, sr1 + q * 8192));
#define M_(r) asm volatile("" : "+" r(f))
        AT_ACC(M_, 8 * q + u);
#undef M_
#pragma unroll
        for (int r = 0; r < 8; ++r) wa[2 * q + (u >> 2)][8 * (u & 3) + r] = f[r];
      }
      BAR();
    }
  }
  STAMP(0);
  const int xpar = vote_a(wait_verdict(c)) ^ (mat >> 1);
  TICK_DECL;
  asm volatile(G_LOOP
      :: [xo] "v"(r32 * 2048 + ((h2 ^ (r32 & 15)) << 4)), [xs] "v"(xs), [so] "v"((4 * w + h2) * 512 + r32 * 16), [fo] "v"((w | lane) ? 1 << 30 : 0),
         [rs] "s"(c.rs), [rx] "s"(rx), [rc] "s"(mkrs((const void*)c.ctrl, CTRL_BYTES)), [fl] "s"(C_FLAG + (mat * 32 + h) * 128), [xp] "s"(xpar),
         [ob] "s"((mat == 1 ? R_KX : R_QX) + h * 16384), [lw] "s"(w * 16384), GTRACE ACC_IN(wa)
      : CL_S, CL_16_32, CL_32_100);
}

DEV void zc_role(const Params& p, Ctx& c, int h) {
  c.budget = SPIN_CAP;
  const int w = c.w, lane = c.lane = fresh_lane(), g = lane >> 4, cc = lane & 15, h2 = lane >> 5, r32 = lane & 31;
  v32u wa[8];
  {
    const rsrc_t rw = mkrs(p.Wo, 4096 * 1024 * 2);
    const int vo = (h * 128 + 8 * g) * 2048 + (256 * w + 8 * cc) * 2;
    v4u row[8][8];
    auto request = [&](int u) {
#pragma unroll
      for (int r = 0; r < 8; ++r) row[u][r] = ld16(rw, vo, (32 * (u & 3) + r) * 2048 + (u >> 2) * 256);
    };
#pragma unroll
    for (int u = 0; u < 2; ++u) request(u);
    SCHED();
    claim(c, 0);
    SCHED();
#pragma unroll
    for (int u = 2; u < 6; ++u) request(u);
    const int sw_ = opq(w * 8192 + cc * 512 + ((g ^ (cc & 3)) << 4)), sr_ = w * 8192 + r32 * 64;
    const int sr0 = opq(sr_ + ((h2 ^ (r32 >> 3)) << 4)), sr1 = opq(sr_ + (((2 + h2) ^ (r32 >> 3)) << 4));
#pragma unroll
    for (int u = 0; u < 8; ++u) {
#pragma unroll
      for (int i = 0; i < 8; ++i) {
        v4u f;
#pragma unroll
        for (int r = 0; r < 4; ++r) f[r] = prm(row[u][2 * r + 1][i >> 1], row[u][2 * r][i >> 1], i & 1);
        sts16(c.L, sw_ + i * 64, f);
      }
      SCHED();
      if (u + 6 < 8) request(u + 6);
      LGKM(0);
#pragma unroll
      for (int q = 0; q < 4; ++q) {
        v8u f = cat(lds16(c.L, sr0 + q * 2048), lds16(c.L, sr1 + q * 2048));
#define M_(r) asm volatile("" : "+" r(f))
        AT_ACC(M_, 4 * (4 * (u >> 2) + q) + (u & 3));
#undef M_
#pragma unroll
        for (int r = 0; r < 8; ++r) wa[4 * (u >> 2) + q][8 * (u & 3) + r] = f[r];
      }
      LGKM(0);
      SCHED();
    }
  }
  STAMP(0);
  BAR();
  const int para = vote_a(wait_verdict(c));
  const int ao = h2 * 512 + (16 * ((r32 >> 3) & 1) + 8 * (r32 >> 4) + (r32 & 7)) * 16;
  TICK_DECL;
  asm volatile(ZC_LOOP
      : [bd] "+s"(c.budget)
      : [ao] "v"(ao), [a1] "v"(ao + 4096), [zo] "v"((16 * w + (r32 >> 4)) * 65536 + (h2 * 16 + cc) * 16 + ((para ^ (w >> 1)) & 1) * 8192 + amap(h * 1024, 0)), [fo] "v"(lane ? 1 << 30 : 0),
        [rs] "s"(c.rs), [rc] "s"(mkrs((const void*)c.ctrl, CTRL_BYTES)), [fv] "s"(C_FLAG + (F_V + h) * 128), [fs] "s"(C_FLAG + (F_S + h) * 128),
        [fz] "s"(C_FLAG + (F_Z + w * 32 + h) * 128), [xp] "s"(para ^ 1), [vb] "s"(R_QX + h * 16384), [zb] "n"(R_Z), [rg] "n"(ZRING - 2), [rm] "n"(ZRING - 1), GTRACE ACC_IN(wa)
      : CL_S, CL_16_32, CL_32_100);
  if (c.budget == 0u) __hip_atomic_store(c.ctrl + C_TMO / 4, 1u, RLX_AGENT);
}

DEV void conv_role(const Params& p, Ctx& c, int cg, int far) {
  constexpr int CH = 5200, U8 = 0;
  constexpr int CP = 4160, FC = 41600;
  constexpr int YS = FC + 16 * CP, RED = YS + 32768;
  static_assert(RED + 8192 <= LDS_BYTES - 16, "lds");
  c.budget = SPIN_CAP;
  const int w = c.w, lane = c.lane = fresh_lane(), tid = w * 64 + lane;
  const int d0 = 8 * cg;
  const int pg = ((far ^ (cg >> 5)) & 1) * 8192;
  auto crow = [](int ch) { return (ch >> 1) * 16384 + (ch & 1) * 4096; };
  const int de = (cg >> 3) * 64 + (tid & 7) * 8, t0 = (cg & 7) * 256 + (tid >> 3) * 8;
  auto group_sync = [&](int phase) {
    gu32* cnt = c.ctrl + (C_CONV + (cg >> 3) * 16 + phase * 8) / 4;
    VMWAIT(0);
    BAR();
    if (w == 0) {
      if (lane == 0) __hip_atomic_fetch_add(cnt, 1u, RLX_AGENT);
#pragma unroll 1
      while (__builtin_amdgcn_readfirstlane(__hip_atomic_load(cnt, RLX_AGENT)) < 8u) { if (spend(c)) break; __builtin_amdgcn_s_sleep(4); }
    }
    BAR();
  };
  v4u kv[8];
  BAR();
  {
    const rsrc_t rk = mkrs(p.bk, 2048 * 2048), rv = mkrs(p.bv, 2048 * 2048);
    v4u kr[11], vr[8], fr[8];
#pragma unroll
    for (int i = 0; i < 11; ++i) kr[i] = ld16(rk, (t0 + i - 3) * 2048 + de * 2, 0);
#pragma unroll
    for (int i = 0; i < 8; ++i) vr[i] = ld16(rv, (t0 + i) * 2048 + de * 2, 0);
    v4u sh[4];
#pragma unroll
    for (int i = 0; i < 4; ++i) sh[i] = *(const GAS v4u*)(p.shift + de * 4 + i * 8);
#pragma unroll
    for (int i = 0; i < 8; ++i) fr[i] = *(const GAS v4u*)(p.ssmk + (size_t)(d0 + i) * 2048 + tid * 8);
    if (tid < 40) sts16(c.L, U8 + (tid / 5) * CH + (tid % 5) * 16, v4u{0, 0, 0, 0});
    if (tid < 64) sts16(c.L, FC + (tid >> 2) * CP + 4096 + (tid & 3) * 16, v4u{0, 0, 0, 0});
#pragma unroll
    for (int ch = 0; ch < 8; ++ch) {
      const unsigned sa = sh[ch >> 1][(ch & 1) * 2], sb = sh[ch >> 1][(ch & 1) * 2 + 1];
      const float s0 = blo(sa), s1 = bhi(sa), s2 = blo(sb), s3 = bhi(sb);
      float kk[11], o[8];
#pragma unroll
      for (int i = 0; i < 11; ++i) kk[i] = (ch & 1) ? bhi(kr[i][ch >> 1]) : blo(kr[i][ch >> 1]);
#pragma unroll
      for (int i = 0; i < 8; ++i) {
        float a = rb(kk[i] * s3);
        a = rb(a + rb(kk[i + 1] * s2));
        a = rb(a + rb(kk[i + 2] * s1));
        a = rb(a + rb(kk[i + 3] * s0));
        o[i] = a * ((ch & 1) ? bhi(vr[i][ch >> 1]) : blo(vr[i][ch >> 1]));
      }
      kv[ch] = v4u{pk(o[0], o[1]), pk(o[2], o[3]), pk(o[4], o[5]), pk(o[6], o[7])};
      st16s(c.rs, de * 8192 + t0 * 2, R_KVT + pg + crow(ch), kv[ch]);
    }
#pragma unroll
    for (int i = 0; i < 8; ++i) sts16(c.L, YS + i * 4096 + tid * 16, fr[i]);
    group_sync(0);
#pragma unroll
    for (int ch = 0; ch < 8; ++ch) fr[ch] = ld16s(c.rs, tid * 16, R_KVT + pg + d0 * 8192 + crow(ch));
#pragma unroll
    for (int ch = 0; ch < 8; ++ch) sts16(c.L, U8 + ch * CH + 80 + (tid >> 2) * 80 + (tid & 3) * 16, fr[ch]);
  }
  BAR();
  STAMP(5);
  auto build = [&](int ch, int q0, int q1) {
    for (int q = q0; q < q1; ++q) {
      const int m = lane + 64 * q;
      const v4u hi = lds16(c.L, YS + ch * 4096 + (255 - m) * 16);
      v4u lo = lds16(c.L, YS + ch * 4096 + (m < 255 ? 254 - m : 0) * 16);
      if (m == 255) lo = v4u{0, 0, 0, 0};
      unsigned G[8];
#pragma unroll
      for (int i = 0; i < 4; ++i) { G[i] = __builtin_amdgcn_alignbyte(hi[3 - i], hi[3 - i], 2); G[4 + i] = __builtin_amdgcn_alignbyte(lo[3 - i], lo[3 - i], 2); }
#pragma unroll
      for (int sg = 0; sg < 8; ++sg) {
        v4u o;
#pragma unroll
        for (int i = 0; i < 4; ++i) o[i] = (sg & 1) ? __builtin_amdgcn_alignbyte(G[(sg >> 1) + i + 1], G[(sg >> 1) + i], 2) : G[(sg >> 1) + i];
        sts16(c.L, FC + ((ch & 1) * 8 + sg) * CP + m * 16, o);
      }
    }
  };
  build(0, w, w + 1);
  BAR();
  const int h2 = lane >> 5, r = lane & 31;
  for (int ch = 0; ch < 8; ++ch) {
    f32x16 acc;
    if (w == 3) {
      if (ch + 1 < 8) build(ch + 1, 0, 4);
    } else {
#pragma unroll
      for (int i = 0; i < 16; ++i) acc[i] = 0.f;
      const int dl0 = w == 2 ? 32 : 0, i0 = w == 0 ? 0 : 32;
      const int sg = (7 - r) & 7;
      int fo = FC + ((ch & 1) * 8 + sg) * CP + (2047 - 32 * dl0 - r + 8 * h2 - sg) * 2;
      const int ub = (i0 + r - dl0) * 80;
      const int ubase = U8 + ch * CH + 80 + h2 * 16;
      v4u fa[3][2], fb[3][2];
      fo = opq(fo - 31 * 64);
#pragma unroll
      for (int dl = -2; dl < 32; ++dl) {
        const int dn = dl + 2;
        if (dn < 32) {
          const int uo = ubase + max(ub - dn * 80, -80);
          fa[dn % 3][0] = lds16(c.L, fo + (31 - dn) * 64); fb[dn % 3][0] = lds16(c.L, uo);
          fa[dn % 3][1] = lds16(c.L, fo + (31 - dn) * 64 + 32); fb[dn % 3][1] = lds16(c.L, uo + 32);
        }
        SCHED();
        if (dl >= 0) {
          acc = mfma32(fa[dl % 3][0], fb[dl % 3][0], acc);
          acc = mfma32(fa[dl % 3][1], fb[dl % 3][1], acc);
        }
        SCHED();
      }
      if (w == 2) {
#pragma unroll
        for (int m = 0; m < 4; ++m) sts16(c.L, RED + (ch & 1) * 4096 + m * 1024 + lane * 16, v4u{fu(acc[4 * m]), fu(acc[4 * m + 1]), fu(acc[4 * m + 2]), fu(acc[4 * m + 3])});
      }
    }
    BAR();
    if (w < 2) {
#pragma unroll
      for (int m = 0; m < 4; ++m) {
        f32x4 v = {acc[4 * m], acc[4 * m + 1], acc[4 * m + 2], acc[4 * m + 3]};
        if (w == 1) v += __builtin_bit_cast(f32x4, lds16(c.L, RED + (ch & 1) * 4096 + m * 1024 + lane * 16));
        sts8(c.L, YS + ch * 4096 + (32 * (32 * w + r) + 8 * m + 4 * h2) * 2, v2u{pk(v[0], v[1]), pk(v[2], v[3])});
      }
    }
  }
  BAR();
  STAMP(6);
  {
    const rsrc_t ro = mkrs(p.out, 2048 * 2048 * 2);
    v4u qr[8], yv[8];
#pragma unroll
    for (int ch = 0; ch < 8; ++ch) st16s(c.rs, tid * 16, R_YT + pg + d0 * 8192 + crow(ch), lds16(c.L, YS + ch * 4096 + tid * 16));
#pragma unroll
    for (int i = 0; i < 8; ++i) qr[i] = *(const GAS v4u*)(p.bq + (size_t)(t0 + i) * 1024 + de);
    const v4u dd = *(const GAS v4u*)(p.ssmD + de);
    group_sync(1);
#pragma unroll
    for (int ch = 0; ch < 8; ++ch) yv[ch] = ld16s(c.rs, de * 8192 + t0 * 2, R_YT + pg + crow(ch));
#pragma unroll
    for (int i = 0; i < 8; ++i) {
      float o[8];
#pragma unroll
      for (int ch = 0; ch < 8; ++ch) {
        const float yy = (i & 1) ? bhi(yv[ch][i >> 1]) : blo(yv[ch][i >> 1]);
        const float uu = (i & 1) ? bhi(kv[ch][i >> 1]) : blo(kv[ch][i >> 1]);
        const float dv = (ch & 1) ? bhi(dd[ch >> 1]) : blo(dd[ch >> 1]);
        const float qq = (ch & 1) ? bhi(qr[i][ch >> 1]) : blo(qr[i][ch >> 1]);
        o[ch] = qq * rb(yy + rb(uu * dv));
      }
      __builtin_amdgcn_raw_buffer_store_b128(v4u{pk(o[0], o[1]), pk(o[2], o[3]), pk(o[4], o[5]), pk(o[6], o[7])}, ro, (t0 + i) * 4096 + (1024 + de) * 2, 0, AUX_NT);
    }
  }
  if (c.budget == 0u) __hip_atomic_store(c.ctrl + C_TMO / 4, 1u, RLX_AGENT);
}

extern "C" __global__ void __launch_bounds__(256, 1) mk_mega(Params p) {
  extern __shared__ __attribute__((aligned(16))) unsigned char lds_raw[];
  Ctx c;
  c.ctrl = (gu32*)p.ctrl;
  c.rs = mkrs(p.data, R_END);
  c.L = (lds_t)lds_raw;
  c.w = __builtin_amdgcn_readfirstlane(threadIdx.x >> 6);
  c.lane = threadIdx.x & 63;
  c.b = (int)(blockIdx.x & 255u);
  c.att = (int)(blockIdx.x >> 8);
  c.ask = 0;
  if (c.w == 0 && c.att == 0) {
    c.ask = __builtin_amdgcn_readfirstlane(__hip_atomic_load(c.ctrl + C_ECHO / 4 + c.b, RLX_AGENT)) + 1u;
    if (c.lane == 0) __hip_atomic_store(c.ctrl + (C_ASK + c.b * 64) / 4, c.ask, RLX_AGENT);
  }
  c.slot = (c.att ? (c.b & 7) : (int)((unsigned)__builtin_amdgcn_s_getreg(20 | (3 << 11)) & 7u)) * 32 + (c.b >> 3);
  if (c.b == 130 && c.w == 3) serve(c);
  const int x = c.slot >> 5, t = c.slot & 31;
  if (x < 4) {
    if (t < 8) qk_role(p, c, x * 8 + t);
    else if (t < 16) vo_role(p, c, x * 8 + t - 8);
    else y_role(p, c, x * 16 + t - 16);
  } else {
    if (t < 24) { const int i = (x - 4) * 24 + t; g_role(p, c, i >> 5, i & 31, t); }
    else zc_role(p, c, (x - 4) * 8 + t - 24);
    STAMP(2);
    conv_role(p, c, (x - 4) * 32 + t, vote_a(wait_verdict(c)) ^ 1);
  }
  STAMP(3);
}

}

extern "C" void kernel_launch(void* const* d_in, const int* in_sizes, int n_in, void* d_out, int out_size, void* d_ws, size_t ws_size,
                              hipStream_t stream) {
  using namespace mk;
  (void)in_sizes; (void)n_in; (void)out_size; (void)ws_size;
  Params p;
  p.x = (const u16*)d_in[0]; p.mem0 = (const u16*)d_in[1];
  p.Wq = (const u16*)d_in[2]; p.Wk = (const u16*)d_in[3]; p.Wv = (const u16*)d_in[4]; p.Wo = (const u16*)d_in[5];
  p.bq = (const u16*)d_in[7]; p.bk = (const u16*)d_in[8]; p.bv = (const u16*)d_in[9];
  p.shift = (const u16*)d_in[10]; p.ssmk = (const u16*)d_in[11]; p.ssmD = (const u16*)d_in[12];
  p.out = (u16*)d_out;
  p.ctrl = (unsigned char*)d_ws;
  p.data = (unsigned char*)(((size_t)d_ws + CTRL_BYTES + (2u << 20) - 1) & ~(size_t)((2u << 20) - 1));
  (void)hipFuncSetAttribute((const void*)mk_mega, hipFuncAttributeMaxDynamicSharedMemorySize, LDS_BYTES);
  hipLaunchKernelGGL(mk_mega, dim3(256), dim3(256), LDS_BYTES, stream, p);
}
```

```cpp
#include <hip/hip_runtime.h>
#include <hip/hip_bf16.h>

namespace mk {

#define GAS __attribute__((address_space(1)))
#define LAS __attribute__((address_space(3)))
#define DEV __device__ __forceinline__
#define RLX_AGENT __ATOMIC_RELAXED, __HIP_MEMORY_SCOPE_AGENT
typedef unsigned short u16;
typedef unsigned v2u __attribute__((ext_vector_type(2)));
typedef unsigned v4u __attribute__((ext_vector_type(4)));
typedef unsigned v8u __attribute__((ext_vector_type(8)));
typedef unsigned v16u __attribute__((ext_vector_type(16)));
typedef unsigned v32u __attribute__((ext_vector_type(32)));
typedef float f32x2 __attribute__((ext_vector_type(2)));
typedef float f32x4 __attribute__((ext_vector_type(4)));
typedef float f32x16 __attribute__((ext_vector_type(16)));
typedef __bf16 bf16x2 __attribute__((ext_vector_type(2)));
typedef __bf16 bf16x8 __attribute__((ext_vector_type(8)));
typedef __bf16 bf16x16 __attribute__((ext_vector_type(16)));
typedef GAS unsigned gu32;
typedef __amdgpu_buffer_rsrc_t rsrc_t;
typedef LAS unsigned char* lds_t;

#define XCNT(r) ""
#define XT(a, b) ""
#define UCNT ""
#define GSUM ""
#define ZSUM ""
#define GTRACE
#define MIXED 1
#define NOFLAG 0
#define TOK_KICK 1
#define ZRING 16
#define Z_AT 16
#define Z_GAP 10
#define STR_(x) #x
#define STR(x) STR_(x)
#define GQ_AT 6
#define GK_AT 0
#define PULL_AT 8
#define QK_AT 0
#define SERVER 130
constexpr int NSEG = 64;
constexpr unsigned SENT = 0xFFFFFFFFu;
constexpr int AUX_SC1 = 16, AUX_NT = 2;
constexpr int T_LAG = 61;
constexpr int G_AHEAD = 4;
constexpr unsigned SPIN_CAP = 1u << 21;
constexpr int LDS_BYTES = 163840;

constexpr int C_ECHO = 0, C_CLAIM = 1024, C_VERDICT = 3072, C_TMO = 3328, C_CONV = 3456, C_VOTE = 3840, C_FLAG = 4096, C_ASK = C_FLAG + 256 * 128;
constexpr int F_Q = 0, F_K = 32, F_V = 64, F_S = 96, F_Z = 128;
constexpr int CTRL_BYTES = C_ASK + 256 * 64;
constexpr int MB = 1 << 20;
constexpr int R_U = 0, R_U2 = 3 * MB;
constexpr int R_MEM = 2 * MB, MEM_SLOT = 32768;
constexpr int R_P = R_MEM + 4 * MEM_SLOT, P_SLOT = 65536;
constexpr int R_PT = 4 * MB, PT_SLOT = 131072;
constexpr int R_QX = 8 * MB, R_KX = 40 * MB, R_Z = 72 * MB, Z_SLOT = 4 * MB, R_KVT = R_KX, R_YT = R_KX + 8 * MB, R_END = 208 * MB;

struct Params {
  const u16 *x, *mem0, *Wq, *Wk, *Wv, *Wo, *bq, *bk, *bv, *shift, *ssmk, *ssmD;
  u16* out;
  unsigned char* ctrl;
  unsigned char* data;
};

DEV rsrc_t mkrs(const void* p, unsigned bytes) { return __builtin_amdgcn_make_buffer_rsrc((void*)p, 0, bytes, 0x00020000); }
DEV v4u ld16s(rsrc_t rs, int v, int s) { return __builtin_amdgcn_raw_buffer_load_b128(rs, v, s, AUX_SC1); }
DEV v2u ld8s(rsrc_t rs, int v, int s) { return __builtin_amdgcn_raw_buffer_load_b64(rs, v, s, AUX_SC1); }
DEV v4u ld16(rsrc_t rs, int v, int s) { return __builtin_amdgcn_raw_buffer_load_b128(rs, v, s, 0); }
DEV v2u ld8(rsrc_t rs, int v, int s) { return __builtin_amdgcn_raw_buffer_load_b64(rs, v, s, 0); }
DEV void dma16(rsrc_t rs, lds_t l, int v) {
#if __HIP_DEVICE_COMPILE__
  __builtin_amdgcn_raw_ptr_buffer_load_lds(rs, (LAS void*)l, 16, v, 0, 0, 0);
#endif
}
DEV void st16s(rsrc_t rs, int v, int s, v4u d) { __builtin_amdgcn_raw_buffer_store_b128(d, rs, v, s, AUX_SC1); }
DEV void st8s(rsrc_t rs, int v, int s, v2u d) { __builtin_amdgcn_raw_buffer_store_b64(d, rs, v, s, AUX_SC1); }
DEV void st4s(rsrc_t rs, int v, int s, unsigned d) { __builtin_amdgcn_raw_buffer_store_b32(d, rs, v, s, AUX_SC1); }
DEV int amap(int o, int par) { return ((o >> 13) << 14) | (par << 13) | (o & 8191); }
DEV int xblk(int s, int h, int par) { return (s * 32 + h) * 16384 + ((par ^ (s >> 2)) & 1) * 8192; }
DEV unsigned umax(unsigned a, unsigned b) { return a > b ? a : b; }
DEV unsigned top(v4u v) { return umax(umax(v.x, v.y), umax(v.z, v.w)); }
DEV unsigned pk(float a, float b) { bf16x2 v = {(__bf16)a, (__bf16)b}; return __builtin_bit_cast(unsigned, v); }
DEV unsigned pks(float a, float b) { unsigned u = pk(a, b); return u < SENT - 1u ? u : SENT - 1u; }
DEV float blo(unsigned u) { return __builtin_bit_cast(float, u << 16); }
DEV float bhi(unsigned u) { return __builtin_bit_cast(float, u & 0xFFFF0000u); }
DEV float rb(float a) { return blo(pk(a, a)); }
DEV unsigned fu(float f) { return __builtin_bit_cast(unsigned, f); }
DEV float uf(unsigned u) { return __builtin_bit_cast(float, u); }
DEV f32x4 mfma16(v4u a, v4u b, f32x4 c) {
  return __builtin_amdgcn_mfma_f32_16x16x32_bf16(__builtin_bit_cast(bf16x8, a), __builtin_bit_cast(bf16x8, b), c, 0, 0, 0);
}
DEV f32x16 mfma32(v4u a, v4u b, f32x16 c) {
  return __builtin_amdgcn_mfma_f32_32x32x16_bf16(__builtin_bit_cast(bf16x8, a), __builtin_bit_cast(bf16x8, b), c, 0, 0, 0);
}
DEV f32x4 smf(v4u a, v8u b, f32x4 c, int idx) {
  return __builtin_amdgcn_smfmac_f32_16x16x64_bf16(__builtin_bit_cast(bf16x8, a), __builtin_bit_cast(bf16x16, b), c, idx, 0, 0);
}
DEV v8u cat(v4u a, v4u b) { return __builtin_shufflevector(a, b, 0, 1, 2, 3, 4, 5, 6, 7); }
DEV v4u lo4(v8u a) { return __builtin_shufflevector(a, a, 0, 1, 2, 3); }
DEV v4u hi4(v8u a) { return __builtin_shufflevector(a, a, 4, 5, 6, 7); }
DEV v4u lds16(lds_t L, int o) { return *(LAS v4u*)(L + o); }
DEV void sts16(lds_t L, int o, v4u v) { *(LAS v4u*)(L + o) = v; }
DEV void sts8(lds_t L, int o, v2u v) { *(LAS v2u*)(L + o) = v; }
#define SCHED() __builtin_amdgcn_sched_barrier(0)
#define PIN_A(x) asm volatile("" : "+a"(x))
#define PIN_V(x) asm volatile("" : "+v"(x))
DEV int opq(int v) { asm volatile("" : "+v"(v)); return v; }
DEV int fresh_lane() {
  int l; asm volatile("v_mbcnt_lo_u32_b32 %0, -1, 0\n\tv_mbcnt_hi_u32_b32 %0, -1, %0" : "=v"(l)); return l;
}
DEV f32x4 fresh_zero() {
  f32x2 a, b;
  asm volatile("v_mov_b64 %0, 0" : "=v"(a));
  asm volatile("v_mov_b64 %0, 0" : "=v"(b));
  return __builtin_shufflevector(a, b, 0, 1, 2, 3);
}
DEV unsigned fresh_sent() { unsigned z; asm volatile("v_mov_b32 %0, -1" : "=v"(z)); return z; }
#define SMF "v_smfmac_f32_16x16x64_bf16 "
#define MF16 "v_mfma_f32_16x16x32_bf16 "
#define SMF1(cls, d, a, b) asm volatile(SMF "%0, %1, %2, %3" : "+v"(d) : "v"(a), cls(b), "v"(idx))
#define SMF1F(cls, lo, hi, d, a, b) asm volatile(SMF "v[" #lo ":" #hi "], %1, %2, %3" : "+{v[" #lo ":" #hi "]}"(d) : "v"(a), cls(b), "v"(idx))
#define AT_HOME(M, j) do { const int j_ = (j); if (j_ == 0) M("{v[32:39]}"); if (j_ == 1) M("{v[40:47]}"); if (j_ == 2) M("{v[48:55]}"); if (j_ == 3) M("{v[56:63]}"); if (j_ == 4) M("{v[64:71]}"); if (j_ == 5) M("{v[72:79]}"); if (j_ == 6) M("{v[80:87]}"); if (j_ == 7) M("{v[88:95]}"); if (j_ == 8) M("{v[96:103]}"); if (j_ == 9) M("{v[104:111]}"); if (j_ == 10) M("{v[112:119]}"); if (j_ == 11) M("{v[120:127]}"); if (j_ == 12) M("{v[128:135]}"); if (j_ == 13) M("{v[136:143]}"); if (j_ == 14) M("{v[144:151]}"); if (j_ == 15) M("{v[152:159]}"); } while (0)
#define MF1(d, a, b) asm volatile(MF16 "%0, %1, %2, %0" : "+v"(d) : "v"(a), "v"(b))
#define MF1Z(d, a, b) asm volatile(MF16 "%0, %1, %2, 0" : "=&v"(d) : "v"(a), "v"(b))
#define LGKM(n) asm volatile("s_waitcnt lgkmcnt(" #n ")" ::: "memory")
#define RAWBAR() do { __builtin_amdgcn_s_barrier(); asm volatile("" ::: "memory"); } while (0)
DEV void settle3(f32x4& s0, f32x4& s1, f32x4& s2) { asm volatile("s_nop 7" : "+v"(s0), "+v"(s1), "+v"(s2)); }
DEV void settle4(f32x4 (&d)[4]) { asm volatile("s_nop 7" : "+v"(d[0]), "+v"(d[1]), "+v"(d[2]), "+v"(d[3])); }
#define BAR() do { asm volatile("s_waitcnt lgkmcnt(0)" ::: "memory"); __builtin_amdgcn_s_barrier(); asm volatile("" ::: "memory"); } while (0)
#define VMWAIT(n) asm volatile("s_waitcnt vmcnt(" #n ")" ::: "memory")
DEV float add1(float a, float b) {
  float o; asm("v_add_f32 %0, %1, %2" : "=v"(o) : "v"(a), "v"(b)); return o;
}
DEV float xmax(float v) {
  auto r = __builtin_amdgcn_permlane32_swap(fu(v), fu(v), false, false); v = fmaxf(uf(r[0]), uf(r[1]));
  auto q = __builtin_amdgcn_permlane16_swap(fu(v), fu(v), false, false); return fmaxf(uf(q[0]), uf(q[1]));
}
DEV float xsum(float v) {
  auto r = __builtin_amdgcn_permlane32_swap(fu(v), fu(v), false, false); v = uf(r[0]) + uf(r[1]);
  auto q = __builtin_amdgcn_permlane16_swap(fu(v), fu(v), false, false); return uf(q[0]) + uf(q[1]);
}

DEV float ror8(float v) { return uf(__builtin_amdgcn_update_dpp(0u, fu(v), 0x128, 0xf, 0xf, true)); }
DEV float fmax2(float a, float b) { return __builtin_amdgcn_fmed3f(a, b, __builtin_inff()); }
DEV float fmax3(float a, float b, float c) { float r; asm("v_max3_f32 %0, %1, %2, %3" : "=v"(r) : "v"(a), "v"(b), "v"(c)); return r; }
DEV bool allok(unsigned t) { return __all(t != SENT); }

template <int N> struct IC { static constexpr int value = N; };
template <int N, class F> DEV void sfor(F&& f) { if constexpr (N > 0) { sfor<N - 1>(f); f(IC<N - 1>{}); } }
struct Ctx {
  gu32* ctrl;
  rsrc_t rs;
  lds_t L;
  int w, lane;
  int b, slot, att;
  unsigned ask;
  unsigned budget;
};
DEV bool spend(Ctx& c) {
  const unsigned old = c.budget;
  asm volatile("s_cmp_lg_u32 %0, 0\n\ts_subb_u32 %0, %0, 0" : "+s"(c.budget) :: "scc");
  return old == 0u;
}
DEV unsigned wait_flag(Ctx& c, int flag, unsigned need) {
  gu32* f = c.ctrl + (C_FLAG + flag * 128) / 4;
  unsigned v;
#pragma unroll 1
  for (;;) {
    v = __builtin_amdgcn_readfirstlane(__hip_atomic_load(f, RLX_AGENT));
    if (v >= need || spend(c)) break;
    __builtin_amdgcn_s_sleep(2);
  }
  __builtin_amdgcn_fence(__ATOMIC_ACQUIRE, "wavefront");
  return v;
}
DEV void post_flag(Ctx& c, int flag, unsigned v, int off = 0) {
  st4s(mkrs((const void*)c.ctrl, CTRL_BYTES), (c.w | off | fresh_lane()) ? 1 << 30 : 0, C_FLAG + flag * 128, v);
}
constexpr unsigned CL_A = 1u << 16, CL_B = 1u << 20, CL_VOTED = 1u << 24, CL_FILLED = 1u << 28;
DEV void claim(Ctx& c, unsigned votes) {
  if (c.w != 0) return;
  if (c.att == 0) {
#pragma unroll 1
    while (__builtin_amdgcn_readfirstlane(__hip_atomic_load(c.ctrl + C_ECHO / 4 + c.b, RLX_AGENT)) != c.ask) {
      if (spend(c)) break;
      __builtin_amdgcn_s_sleep(2);
    }
  }
  if (fresh_lane() == 0) __hip_atomic_fetch_add(c.ctrl + (C_CLAIM + c.att * 1024) / 4 + c.slot, 1u + votes, RLX_AGENT);
}
DEV unsigned wait_verdict(Ctx& c) {
  unsigned v;
#pragma unroll 1
  for (;;) {
    v = __builtin_amdgcn_readfirstlane(__hip_atomic_load(c.ctrl + (C_VERDICT + c.att * 128) / 4, RLX_AGENT));
    if ((v & 1u) || spend(c)) break;
    __builtin_amdgcn_s_sleep(2);
  }
  __builtin_amdgcn_fence(__ATOMIC_ACQUIRE, "wavefront");
  if ((v & 3u) == 3u && c.att == 0) {
    asm volatile("s_waitcnt vmcnt(0) lgkmcnt(0)\n\ts_barrier\n\ts_mov_b64 s[0:1], %0\n\ts_mov_b32 s2, %1\n\tv_mov_b32 v0, %2\n\t"
                 "s_getpc_b64 s[4:5]\n1:\n\ts_sub_u32 s4, s4, 1b-mk_mega\n\ts_subb_u32 s5, s5, 0\n\ts_setpc_b64 s[4:5]"
                 :: "s"(__builtin_amdgcn_kernarg_segment_ptr()), "s"(c.b + 256), "v"(c.w * 64 + fresh_lane()) : "s0", "s1", "s2", "s4", "s5", "v0", "scc", "memory");
    __builtin_unreachable();
  }
  return v;
}
DEV unsigned wait_vote(Ctx& c) {
  unsigned v;
#pragma unroll 1
  for (;;) {
    v = __builtin_amdgcn_readfirstlane(__hip_atomic_load(c.ctrl + C_VOTE / 4, RLX_AGENT));
    if ((v & 1u) || spend(c)) break;
    __builtin_amdgcn_s_sleep(2);
  }
  return v;
}
DEV void serve(Ctx& c) {
  c.budget = SPIN_CAP;
  const int lane = fresh_lane();
  const rsrc_t rc = mkrs((const void*)c.ctrl, CTRL_BYTES);
  if (c.att == 0) {
#pragma unroll
    for (int i = C_CLAIM / 1024; i < C_ASK / 1024; ++i) st16s(rc, lane * 16, i * 1024, v4u{0, 0, 0, 0});
    VMWAIT(0);
  }
  unsigned vote = c.att ? wait_vote(c) : 0u;
  bool echo = c.att == 0;
#pragma unroll 1
  for (;;) {
    asm volatile("" ::: "memory");
    const v4u cl = ld16s(rc, lane * 16, C_CLAIM + c.att * 1024);
    if (echo) {
      v4u e;
#pragma unroll
      for (int i = 0; i < 4; ++i) e[i] = __builtin_amdgcn_raw_buffer_load_b32(rc, (lane * 4 + i) * 64, C_ASK, AUX_SC1);
      st16s(rc, lane * 16, C_ECHO, e);
    }
    unsigned n = 0, one = 1;
    int va = 0, vb = 0, nv = 0, nf = 0;
#pragma unroll
    for (int i = 0; i < 4; ++i) {
      n += cl[i] & 0xFFFFu; one &= (cl[i] & 0xFFFFu) == 1u;
      va += __popcll(__ballot(cl[i] & CL_A)); vb += __popcll(__ballot(cl[i] & CL_B)); nv += __popcll(__ballot(cl[i] & CL_VOTED)); nf += __popcll(__ballot(cl[i] & CL_FILLED));
    }
#pragma unroll
    for (int o = 1; o < 64; o <<= 1) n += (unsigned)__shfl_xor((int)n, o);
    const bool all = __builtin_amdgcn_readfirstlane(n) >= 256u, good = __all(one);
    echo = echo && !all;
    if (!vote && (nv >= 64 || all)) {
      vote = 1u | (va > 32 ? 4u : 0u) | (vb > 32 ? 8u : 0u);
      if (lane == 0) __hip_atomic_store(c.ctrl + C_VOTE / 4, vote, RLX_AGENT);
    }
    if (all && (!good || nf >= 64)) {
      if (lane == 0) __hip_atomic_store(c.ctrl + (C_VERDICT + c.att * 128) / 4, vote | (good ? 0u : 2u), RLX_AGENT);
      break;
    }
    if (spend(c)) break;
  }
}
DEV int vote_a(unsigned v) { return (int)((v >> 2) & 1u); }
DEV int vote_b(unsigned v) { return (int)((v >> 3) & 1u); }

#define STAMP(slot) do {} while (0)
#define TICK_DECL
#define TICK(i) do {} while (0)
#define TICK_SUM(n) do {} while (0)
#define TICK_LDS(b) do {} while (0)
#define TICK_DUMP(b) do {} while (0)
#define TICK_OUT() do {} while (0)

DEV constexpr int krow(int rho8, int i) { return 32 * (rho8 >> 2) + 8 * (rho8 & 1) + 2 * ((rho8 >> 1) & 1) + i; }
DEV int krow_lane(int g) { return 16 * (g >> 1) + 4 * (g & 1); }
DEV unsigned prm(unsigned hi, unsigned lo, int odd) { return __builtin_amdgcn_perm(hi, lo, odd ? 0x07060302u : 0x05040100u); }

#define AT_ACC(M, n) do { const int n_ = (n); if (n_ == 0) M("{a[0:7]}"); if (n_ == 1) M("{a[8:15]}"); if (n_ == 2) M("{a[16:23]}"); if (n_ == 3) M("{a[24:31]}"); if (n_ == 4) M("{a[32:39]}"); if (n_ == 5) M("{a[40:47]}"); if (n_ == 6) M("{a[48:55]}"); if (n_ == 7) M("{a[56:63]}"); if (n_ == 8) M("{a[64:71]}"); if (n_ == 9) M("{a[72:79]}"); if (n_ == 10) M("{a[80:87]}"); if (n_ == 11) M("{a[88:95]}"); if (n_ == 12) M("{a[96:103]}"); if (n_ == 13) M("{a[104:111]}"); if (n_ == 14) M("{a[112:119]}"); if (n_ == 15) M("{a[120:127]}"); if (n_ == 16) M("{a[128:135]}"); if (n_ == 17) M("{a[136:143]}"); if (n_ == 18) M("{a[144:151]}"); if (n_ == 19) M("{a[152:159]}"); if (n_ == 20) M("{a[160:167]}"); if (n_ == 21) M("{a[168:175]}"); if (n_ == 22) M("{a[176:183]}"); if (n_ == 23) M("{a[184:191]}"); if (n_ == 24) M("{a[192:199]}"); if (n_ == 25) M("{a[200:207]}"); if (n_ == 26) M("{a[208:215]}"); if (n_ == 27) M("{a[216:223]}"); if (n_ == 28) M("{a[224:231]}"); if (n_ == 29) M("{a[232:239]}"); if (n_ == 30) M("{a[240:247]}"); if (n_ == 31) M("{a[248:255]}"); } while (0)
#define PIN_ACC0(x) asm volatile("" :: "{a[0:7]}"(x[0]), "{a[8:15]}"(x[1]), "{a[16:23]}"(x[2]), "{a[24:31]}"(x[3]), "{a[32:39]}"(x[4]), "{a[40:47]}"(x[5]), "{a[48:55]}"(x[6]), "{a[56:63]}"(x[7]), "{a[64:71]}"(x[8]), "{a[72:79]}"(x[9]), "{a[80:87]}"(x[10]), "{a[88:95]}"(x[11]), "{a[96:103]}"(x[12]), "{a[104:111]}"(x[13]), "{a[112:119]}"(x[14]), "{a[120:127]}"(x[15]))
#define PIN_ACC1(x) asm volatile("" :: "{a[128:135]}"(x[16]), "{a[136:143]}"(x[17]), "{a[144:151]}"(x[18]), "{a[152:159]}"(x[19]), "{a[160:167]}"(x[20]), "{a[168:175]}"(x[21]), "{a[176:183]}"(x[22]), "{a[184:191]}"(x[23]), "{a[192:199]}"(x[24]), "{a[200:207]}"(x[25]), "{a[208:215]}"(x[26]), "{a[216:223]}"(x[27]), "{a[224:231]}"(x[28]), "{a[232:239]}"(x[29]), "{a[240:247]}"(x[30]), "{a[248:255]}"(x[31]))
#define PIN_HOME(x) asm volatile("" :: "{v[32:39]}"(x[0]), "{v[40:47]}"(x[1]), "{v[48:55]}"(x[2]), "{v[56:63]}"(x[3]), "{v[64:71]}"(x[4]), "{v[72:79]}"(x[5]), "{v[80:87]}"(x[6]), "{v[88:95]}"(x[7]), "{v[96:103]}"(x[8]), "{v[104:111]}"(x[9]), "{v[112:119]}"(x[10]), "{v[120:127]}"(x[11]), "{v[128:135]}"(x[12]), "{v[136:143]}"(x[13]), "{v[144:151]}"(x[14]), "{v[152:159]}"(x[15]))
#define CL_32_100 "v32", "v33", "v34", "v35", "v36", "v37", "v38", "v39", "v40", "v41", "v42", "v43", "v44", "v45", "v46", "v47", "v48", "v49", "v50", "v51", "v52", "v53", "v54", "v55", "v56", "v57", "v58", "v59", "v60", "v61", "v62", "v63", "v64", "v65", "v66", "v67", "v68", "v69", "v70", "v71", "v72", "v73", "v74", "v75", "v76", "v77", "v78", "v79", "v80", "v81", "v82", "v83", "v84", "v85", "v86", "v87", "v88", "v89", "v90", "v91", "v92", "v93", "v94", "v95", "v96", "v97", "v98", "v99", "v100", "v101", "v102", "v103", "v104", "v105", "v106", "v107", "v108", "v109", "v110", "v111", "v112", "v113", "v114", "v115", "v116", "v117", "v118", "v119", "v120", "v121", "v122", "v123", "v124", "v125", "v126", "v127", "v128", "v129", "v130", "v131"
#define ACC_IN(x) "{a[0:31]}"(x[0]), "{a[32:63]}"(x[1]), "{a[64:95]}"(x[2]), "{a[96:127]}"(x[3]), "{a[128:159]}"(x[4]), "{a[160:191]}"(x[5]), "{a[192:223]}"(x[6]), "{a[224:255]}"(x[7])
#define CL_16_32 "v16", "v17", "v18", "v19", "v20", "v21", "v22", "v23", "v24", "v25", "v26", "v27", "v28", "v29", "v30", "v31"
#define CL_120_128 "v120", "v121", "v122", "v123", "v124", "v125", "v126", "v127"
#define CL_160_200 "v160", "v161", "v162", "v163", "v164", "v165", "v166", "v167", "v168", "v169", "v170", "v171", "v172", "v173", "v174", "v175", "v176", "v177", "v178", "v179", "v180", "v181", "v182", "v183", "v184", "v185", "v186", "v187", "v188", "v189", "v190", "v191", "v192", "v193", "v194", "v195", "v196", "v197", "v198", "v199"
#define CL_176_200 "v176", "v177", "v178", "v179", "v180", "v181", "v182", "v183", "v184", "v185", "v186", "v187", "v188", "v189", "v190", "v191", "v192", "v193", "v194", "v195", "v196", "v197", "v198", "v199"
#define CL_200_216 "v200", "v201", "v202", "v203", "v204", "v205", "v206", "v207", "v208", "v209", "v210", "v211", "v212", "v213", "v214", "v215"
#define CL_216_256 "v216", "v217", "v218", "v219", "v220", "v221", "v222", "v223", "v224", "v225", "v226", "v227", "v228", "v229", "v230", "v231", "v232", "v233", "v234", "v235", "v236", "v237", "v238", "v239", "v240", "v241", "v242", "v243", "v244", "v245", "v246", "v247", "v248", "v249", "v250", "v251", "v252", "v253", "v254", "v255"
#define CL_S "memory", "vcc", "scc", "s60", "s61", "s62", "s63", "s64", "s65", "s66", "s67"
#define TIMED_WAIT \
    "s_getpc_b64 s[62:63]\n\t" "1:\n\t" "s_memtime s[64:65]\n\t" "s_waitcnt lgkmcnt(0)\n\t" "s_sub_u32 s61, %0, s64\n\t" "s_cmp_gt_i32 s61, 124\n\t" "s_cbranch_scc1 1b\n\t" \
    "s_max_i32 s61, s61, 0\n\t" "s_and_b32 s61, s61, 124\n\t" "s_sub_u32 s61, 2f-1b, s61\n\t" "s_add_u32 s62, s62, s61\n\t" "s_addc_u32 s63, s63, 0\n\t" \
    "s_setpc_b64 s[62:63]\n\t" ".rept 31\n\t" "s_nop 0\n\t" ".endr\n\t" "2:\n\t"
#define QK_STEP0 \
    "v_xor_b32 v216, 0x400, %[mo]\n\t" "s_mov_b32 s60, 0\n\t" "s_mov_b32 s66, 0x3e0293ee\n\t" "s_mov_b32 s67, 0x3e0293ee\n\t" XT(80, 81) "s_add_u32 s61, %[kb], 0x1000\n\t" \
    "buffer_load_dwordx4 v[16:19], %[l1], %[rs], %[kb] offen offset:0 sc1\n\t" "buffer_load_dwordx4 v[20:23], %[l1], %[rs], %[kb] offen offset:1024 sc1\n\t" \
    "buffer_load_dwordx4 v[24:27], %[l1], %[rs], %[kb] offen offset:2048 sc1\n\t" "buffer_load_dwordx4 v[28:31], %[l1], %[rs], %[kb] offen offset:3072 sc1\n\t" \
    "buffer_load_dwordx4 v[200:203], %[l1], %[rs], s61 offen offset:0 sc1\n\t" "buffer_load_dwordx4 v[204:207], %[l1], %[rs], s61 offen offset:1024 sc1\n\t" \
    "buffer_load_dwordx4 v[208:211], %[l1], %[rs], s61 offen offset:2048 sc1\n\t" "buffer_load_dwordx4 v[212:215], %[l1], %[rs], s61 offen offset:3072 sc1\n\t" \
    "v_mov_b64 v[218:219], -1\n\t" "buffer_store_dwordx2 v[218:219], %[px], %[rs], %[pr] offen sc1\n\t" "buffer_store_dword v218, %[pm], %[rs], %[pr] offen sc1\n\t" \
    "s_waitcnt vmcnt(2)\n\t" XT(98, 99) "s_barrier\n\t" XT(82, 83) "10:\n\t" "buffer_load_dwordx4 v[240:243], %[mo], %[rs], %[sb] offen offset:0 sc1\n\t" \
    "v_mov_b64 v[224:225], 0\n\t" "v_mov_b64 v[226:227], 0\n\t" "v_mov_b64 v[228:229], 0\n\t" "v_mov_b64 v[230:231], 0\n\t" "v_mov_b64 v[232:233], 0\n\t" \
    "v_mov_b64 v[234:235], 0\n\t" "v_mov_b64 v[236:237], 0\n\t" "v_mov_b64 v[238:239], 0\n\t" "buffer_load_dwordx4 v[244:247], v216, %[rs], %[sb] offen offset:0 sc1\n\t" \
    "ds_read_b128 v[160:163], %[l0] offset:0\n\t" "ds_read_b128 v[164:167], %[l0] offset:1024\n\t" "ds_read_b128 v[168:171], %[l0] offset:2048\n\t" \
    "ds_read_b128 v[172:175], %[l0] offset:3072\n\t" "s_nop 3\n\t" "buffer_load_dwordx4 v[248:251], %[mo], %[rs], %[sb] offen offset:2048 sc1\n\t" \
    "ds_read_b128 v[176:179], %[l0] offset:4096\n\t" "ds_read_b128 v[180:183], %[l0] offset:5120\n\t" "ds_read_b128 v[184:187], %[l0] offset:6144\n\t" \
    "ds_read_b128 v[188:191], %[l0] offset:7168\n\t" "s_nop 3\n\t" "buffer_load_dwordx4 v[252:255], v216, %[rs], %[sb] offen offset:2048 sc1\n\t" "s_cmp_lg_u32 s61, 0\n\t" \
    "s_cbranch_scc0 177f\n\t" "ds_write_b128 %[l1], v[16:19] offset:16384\n\t" "ds_write_b128 %[l1], v[20:23] offset:17408\n\t" \
    "ds_write_b128 %[l1], v[24:27] offset:18432\n\t" "ds_write_b128 %[l1], v[28:31] offset:19456\n\t" "ds_write_b128 %[l1], v[200:203] offset:20480\n\t" \
    "ds_write_b128 %[l1], v[204:207] offset:21504\n\t" "ds_write_b128 %[l1], v[208:211] offset:22528\n\t" "ds_write_b128 %[l1], v[212:215] offset:23552\n\t" \
    "s_mov_b32 s61, 0\n\t" "s_waitcnt lgkmcnt(0)\n\t" "177:\n\t" "v_mov_b64 v[208:209], 0\n\t" "v_mov_b64 v[210:211], 0\n\t" "v_mov_b64 v[212:213], 0\n\t" \
    "v_mov_b64 v[214:215], 0\n\t" "s_waitcnt vmcnt(3)\n\t" XT(84, 85) "v_smfmac_f32_16x16x64_bf16 v[224:227], v[240:243], a[0:7], %[ix]\n\t" \
    "ds_write_b128 %[l1], v[240:243] offset:0\n\t" "v_smfmac_f32_16x16x64_bf16 v[228:231], v[240:243], a[8:15], %[ix]\n\t" "v_max3_u32 v217, v240, v241, v242\n\t" \
    "v_smfmac_f32_16x16x64_bf16 v[232:235], v[240:243], v[32:39], %[ix]\n\t" "s_waitcnt lgkmcnt(7)\n\t" \
    "v_smfmac_f32_16x16x64_bf16 v[236:239], v[240:243], v[160:167], %[ix]\n\t" "ds_read_b128 v[160:163], %[l0] offset:8192\n\t" \
    "ds_read_b128 v[164:167], %[l0] offset:9216\n\t" "s_waitcnt vmcnt(2)\n\t" "v_smfmac_f32_16x16x64_bf16 v[224:227], v[244:247], a[16:23], %[ix]\n\t" \
    "ds_write_b128 %[l1], v[244:247] offset:1024\n\t" "v_max3_u32 v217, v217, v243, v244\n\t" "v_smfmac_f32_16x16x64_bf16 v[228:231], v[244:247], a[24:31], %[ix]\n\t" \
    "v_max3_u32 v217, v217, v245, v246\n\t" "v_max_u32 v217, v217, v247\n\t" "v_smfmac_f32_16x16x64_bf16 v[232:235], v[244:247], v[40:47], %[ix]\n\t" \
    "v_cmp_eq_u32 vcc, -1, v217\n\t" "s_cmp_lg_u32 s60, 0\n\t" "s_waitcnt lgkmcnt(8)\n\t" "v_smfmac_f32_16x16x64_bf16 v[236:239], v[244:247], v[168:175], %[ix]\n\t" \
    "s_cbranch_vccnz 11f\n\t" "12:\n\t" "ds_read_b128 v[168:171], %[l0] offset:10240\n\t" "s_waitcnt vmcnt(1)\n\t" \
    "v_smfmac_f32_16x16x64_bf16 v[224:227], v[248:251], a[32:39], %[ix]\n\t" "ds_write_b128 %[l1], v[248:251] offset:2048\n\t" "s_waitcnt lgkmcnt(2)\n\t" \
    "v_smfmac_f32_16x16x64_bf16 v[228:231], v[248:251], a[40:47], %[ix]\n\t" "s_cbranch_scc1 13f\n\t" "s_barrier\n\t" "13:\n\t" XT(86, 87) \
    "v_smfmac_f32_16x16x64_bf16 v[232:235], v[248:251], v[48:55], %[ix]\n\t" "ds_read_b128 v[192:195], %[l1] offset:4096\n\t" "ds_read_b128 v[196:199], %[l1] offset:8192\n\t" \
    "v_smfmac_f32_16x16x64_bf16 v[236:239], v[248:251], v[176:183], %[ix]\n\t" "ds_read_b128 v[172:175], %[l0] offset:11264\n\t" \
    "ds_read_b128 v[176:179], %[l0] offset:12288\n\t" "s_waitcnt vmcnt(0)\n\t" "v_smfmac_f32_16x16x64_bf16 v[224:227], v[252:255], a[48:55], %[ix]\n\t" \
    "ds_write_b128 %[l1], v[252:255] offset:3072\n\t" "v_max3_u32 v217, v248, v249, v250\n\t" "v_smfmac_f32_16x16x64_bf16 v[228:231], v[252:255], a[56:63], %[ix]\n\t" \
    "ds_read_b128 v[180:183], %[l0] offset:13312\n\t" "v_max3_u32 v217, v217, v251, v252\n\t" "v_max3_u32 v217, v217, v253, v254\n\t" \
    "v_smfmac_f32_16x16x64_bf16 v[232:235], v[252:255], v[56:63], %[ix]\n\t" "v_max_u32 v217, v217, v255\n\t" "v_cmp_eq_u32 vcc, -1, v217\n\t" \
    "v_smfmac_f32_16x16x64_bf16 v[236:239], v[252:255], v[184:191], %[ix]\n\t" "s_cbranch_vccnz 14f\n\t" "15:\n\t" "ds_read_b128 v[240:243], %[l1] offset:12288\n\t" \
    "s_waitcnt lgkmcnt(6)\n\t" "v_smfmac_f32_16x16x64_bf16 v[224:227], v[192:195], a[64:71], %[ix]\n\t" "ds_read_b128 v[184:187], %[l0] offset:14336\n\t" \
    "ds_read_b128 v[188:191], %[l0] offset:15360\n\t" "v_smfmac_f32_16x16x64_bf16 v[228:231], v[192:195], a[72:79], %[ix]\n\t" \
    "ds_read_b128 v[244:247], %[l1] offset:5120\n\t" "s_waitcnt lgkmcnt(5)\n\t" "v_smfmac_f32_16x16x64_bf16 v[232:235], v[192:195], v[64:71], %[ix]\n\t" "s_barrier\n\t" \
    XT(88, 89) "v_smfmac_f32_16x16x64_bf16 v[236:239], v[192:195], v[160:167], %[ix]\n\t" "ds_read_b128 v[160:163], %[l0] offset:16384\n\t" \
    "ds_read_b128 v[164:167], %[l0] offset:17408\n\t" "v_smfmac_f32_16x16x64_bf16 v[224:227], v[196:199], a[80:87], %[ix]\n\t" \
    "ds_read_b128 v[248:251], %[l1] offset:9216\n\t" "ds_read_b128 v[252:255], %[l1] offset:13312\n\t" \
    "v_smfmac_f32_16x16x64_bf16 v[228:231], v[196:199], a[88:95], %[ix]\n\t" "ds_read_b128 v[192:195], %[l1] offset:6144\n\t" \
    "v_smfmac_f32_16x16x64_bf16 v[232:235], v[196:199], v[72:79], %[ix]\n\t" "ds_read_b128 v[16:19], %[kx] offset:0\n\t" \
    "v_smfmac_f32_16x16x64_bf16 v[236:239], v[196:199], v[168:175], %[ix]\n\t" "ds_read_b128 v[168:171], %[l0] offset:18432\n\t" \
    "ds_read_b128 v[172:175], %[l0] offset:19456\n\t" "s_waitcnt lgkmcnt(11)\n\t" "v_smfmac_f32_16x16x64_bf16 v[224:227], v[240:243], a[96:103], %[ix]\n\t" \
    "ds_read_b128 v[196:199], %[l1] offset:10240\n\t" "v_smfmac_f32_16x16x64_bf16 v[228:231], v[240:243], a[104:111], %[ix]\n\t" \
    "ds_read_b128 v[20:23], %[kx] offset:2048\n\t" "v_smfmac_f32_16x16x64_bf16 v[232:235], v[240:243], v[80:87], %[ix]\n\t" \
    "v_smfmac_f32_16x16x64_bf16 v[236:239], v[240:243], v[176:183], %[ix]\n\t" "ds_read_b128 v[176:179], %[l0] offset:20480\n\t" \
    "ds_read_b128 v[180:183], %[l0] offset:21504\n\t" "s_waitcnt lgkmcnt(12)\n\t" "v_smfmac_f32_16x16x64_bf16 v[224:227], v[244:247], a[112:119], %[ix]\n\t" \
    "ds_read_b128 v[240:243], %[l1] offset:14336\n\t" "ds_read_b128 v[24:27], %[kx] offset:4096\n\t" \
    "v_smfmac_f32_16x16x64_bf16 v[228:231], v[244:247], a[120:127], %[ix]\n\t" "v_smfmac_f32_16x16x64_bf16 v[232:235], v[244:247], v[88:95], %[ix]\n\t" \
    "v_smfmac_f32_16x16x64_bf16 v[236:239], v[244:247], v[184:191], %[ix]\n\t" "ds_read_b128 v[184:187], %[l0] offset:22528\n\t" \
    "ds_read_b128 v[188:191], %[l0] offset:23552\n\t" "s_waitcnt lgkmcnt(13)\n\t" "v_smfmac_f32_16x16x64_bf16 v[224:227], v[248:251], a[128:135], %[ix]\n\t" \
    "ds_read_b128 v[244:247], %[l1] offset:7168\n\t" "ds_read_b128 v[28:31], %[kx] offset:6144\n\t" "v_smfmac_f32_16x16x64_bf16 v[228:231], v[248:251], a[136:143], %[ix]\n\t" \
    "v_smfmac_f32_16x16x64_bf16 v[232:235], v[248:251], v[96:103], %[ix]\n\t" "ds_read_b128 v[200:203], %[kx] offset:256\n\t" \
    "v_smfmac_f32_16x16x64_bf16 v[236:239], v[248:251], v[160:167], %[ix]\n\t" "ds_read_b128 v[160:163], %[l0] offset:24576\n\t" \
    "ds_read_b128 v[164:167], %[l0] offset:25600\n\t" "s_waitcnt lgkmcnt(14)\n\t" "v_smfmac_f32_16x16x64_bf16 v[224:227], v[252:255], a[144:151], %[ix]\n\t" \
    "ds_read_b128 v[248:251], %[l1] offset:11264\n\t" "v_smfmac_f32_16x16x64_bf16 v[228:231], v[252:255], a[152:159], %[ix]\n\t" \
    "ds_read_b128 v[204:207], %[kx] offset:2304\n\t" "v_smfmac_f32_16x16x64_bf16 v[232:235], v[252:255], v[104:111], %[ix]\n\t" "s_waitcnt lgkmcnt(14)\n\t" \
    "v_smfmac_f32_16x16x64_bf16 v[236:239], v[252:255], v[168:175], %[ix]\n\t" "ds_read_b128 v[168:171], %[l0] offset:26624\n\t" \
    "ds_read_b128 v[172:175], %[l0] offset:27648\n\t" "v_smfmac_f32_16x16x64_bf16 v[224:227], v[192:195], a[160:167], %[ix]\n\t" \
    "ds_read_b128 v[252:255], %[l1] offset:15360\n\t" "v_smfmac_f32_16x16x64_bf16 v[228:231], v[192:195], a[168:175], %[ix]\n\t" \
    "v_smfmac_f32_16x16x64_bf16 v[224:227], v[196:199], a[176:183], %[ix]\n\t" "v_smfmac_f32_16x16x64_bf16 v[228:231], v[196:199], a[184:191], %[ix]\n\t" \
    "s_waitcnt lgkmcnt(13)\n\t" "v_smfmac_f32_16x16x64_bf16 v[224:227], v[240:243], a[192:199], %[ix]\n\t" \
    "v_smfmac_f32_16x16x64_bf16 v[228:231], v[240:243], a[200:207], %[ix]\n\t" "s_waitcnt lgkmcnt(9)\n\t" \
    "v_smfmac_f32_16x16x64_bf16 v[224:227], v[244:247], a[208:215], %[ix]\n\t" "v_smfmac_f32_16x16x64_bf16 v[228:231], v[244:247], a[216:223], %[ix]\n\t" \
    "s_waitcnt lgkmcnt(4)\n\t" "v_smfmac_f32_16x16x64_bf16 v[224:227], v[248:251], a[224:231], %[ix]\n\t" \
    "v_smfmac_f32_16x16x64_bf16 v[228:231], v[248:251], a[232:239], %[ix]\n\t" "s_waitcnt lgkmcnt(0)\n\t" \
    "v_smfmac_f32_16x16x64_bf16 v[224:227], v[252:255], a[240:247], %[ix]\n\t" "s_nop 1\n\t" "v_smfmac_f32_16x16x64_bf16 v[228:231], v[252:255], a[248:255], %[ix]\n\t" \
    XT(90, 91) "s_nop 1\n\t" "v_smfmac_f32_16x16x64_bf16 v[232:235], v[192:195], v[112:119], %[ix]\n\t" "s_nop 1\n\t" \
    "v_smfmac_f32_16x16x64_bf16 v[236:239], v[192:195], v[176:183], %[ix]\n\t" "ds_read_b128 v[176:179], %[l0] offset:28672\n\t" "v_add_f32 v224, v224, v225\n\t" \
    "v_add_f32 v226, v226, v227\n\t" "v_smfmac_f32_16x16x64_bf16 v[232:235], v[196:199], v[120:127], %[ix]\n\t" "ds_read_b128 v[180:183], %[l0] offset:29696\n\t" \
    "v_add_f32 v228, v228, v229\n\t" "v_add_f32 v230, v230, v231\n\t" "v_smfmac_f32_16x16x64_bf16 v[236:239], v[196:199], v[184:191], %[ix]\n\t" \
    "ds_read_b128 v[184:187], %[l0] offset:30720\n\t" "v_cvt_pk_bf16_f32 v218, v224, v228\n\t" "v_cvt_pk_bf16_f32 v219, v226, v230\n\t" \
    "v_smfmac_f32_16x16x64_bf16 v[232:235], v[240:243], v[128:135], %[ix]\n\t" "ds_write2_b32 %[qw], v218, v219 offset1:8\n\t" \
    "ds_read_b128 v[188:191], %[l0] offset:31744\n\t" "v_smfmac_f32_16x16x64_bf16 v[236:239], v[240:243], v[160:167], %[ix]\n\t" \
    "ds_read_b128 v[192:195], %[kx] offset:4352\n\t" "ds_read_b128 v[196:199], %[kx] offset:6400\n\t" \
    "v_smfmac_f32_16x16x64_bf16 v[232:235], v[244:247], v[136:143], %[ix]\n\t" "v_smfmac_f32_16x16x64_bf16 v[236:239], v[244:247], v[168:175], %[ix]\n\t" \
    "s_waitcnt lgkmcnt(3)\n\t" "s_barrier\n\t" "v_smfmac_f32_16x16x64_bf16 v[232:235], v[248:251], v[144:151], %[ix]\n\t" "ds_read_b128 v[216:219], %[l1] offset:24576\n\t" \
    "ds_read_b128 v[220:223], %[l1] offset:25600\n\t" "v_smfmac_f32_16x16x64_bf16 v[236:239], v[248:251], v[176:183], %[ix]\n\t" \
    "v_smfmac_f32_16x16x64_bf16 v[232:235], v[252:255], v[152:159], %[ix]\n\t" "s_waitcnt lgkmcnt(4)\n\t" \
    "v_smfmac_f32_16x16x64_bf16 v[236:239], v[252:255], v[184:191], %[ix]\n\t" XT(92, 93) "s_waitcnt lgkmcnt(1)\n\t" \
    "v_smfmac_f32_16x16x64_bf16 v[208:211], v[216:219], v[16:23], %[ix]\n\t" "s_nop 1\n\t" "v_smfmac_f32_16x16x64_bf16 v[212:215], v[216:219], v[200:207], %[ix]\n\t" \
    "s_waitcnt lgkmcnt(0)\n\t" "s_nop 0\n\t" "v_add_f32 v232, v232, v233\n\t" "v_add_f32 v234, v234, v235\n\t" "v_add_f32 v236, v236, v237\n\t" \
    "v_add_f32 v238, v238, v239\n\t" "v_cvt_pk_bf16_f32 v232, v232, v236\n\t" "v_cvt_pk_bf16_f32 v233, v234, v238\n\t" "ds_write2_b32 %[kw], v232, v233 offset1:68\n\t" \
    "v_smfmac_f32_16x16x64_bf16 v[208:211], v[220:223], v[24:31], %[ix]\n\t" "v_smfmac_f32_16x16x64_bf16 v[212:215], v[220:223], v[192:199], %[ix]\n\t" \
    "v_mov_b64 v[224:225], 0\n\t" "v_mov_b64 v[226:227], 0\n\t" "s_waitcnt lgkmcnt(0)\n\t" "s_barrier\n\t" XT(94, 95) "ds_read_b128 v[160:163], %[km] offset:0\n\t" \
    "ds_read_b128 v[164:167], %[km] offset:64\n\t" "ds_read_b128 v[168:171], %[km] offset:128\n\t" "ds_read_b128 v[172:175], %[km] offset:192\n\t" \
    "v_add_f32 v178, v208, v209\n\t" "v_add_f32 v179, v210, v211\n\t" "v_add_f32 v180, v212, v213\n\t" "v_add_f32 v181, v214, v215\n\t" "v_max_f32 v182, v178, v180\n\t" \
    "v_max_f32 v183, v179, v181\n\t" "s_nop 0\n\t" "v_max_f32_dpp v182, v182, v182 row_ror:8 row_mask:0xf bank_mask:0xf\n\t" \
    "v_max_f32_dpp v183, v183, v183 row_ror:8 row_mask:0xf bank_mask:0xf\n\t" "s_nop 0\n\t" "v_max_f32_dpp v182, v182, v182 row_ror:4 row_mask:0xf bank_mask:0xf\n\t" \
    "v_max_f32_dpp v183, v183, v183 row_ror:4 row_mask:0xf bank_mask:0xf\n\t" "s_nop 0\n\t" "v_max_f32_dpp v182, v182, v182 row_ror:2 row_mask:0xf bank_mask:0xf\n\t" \
    "v_max_f32_dpp v183, v183, v183 row_ror:2 row_mask:0xf bank_mask:0xf\n\t" "s_nop 0\n\t" "v_max_f32_dpp v182, v182, v182 row_ror:1 row_mask:0xf bank_mask:0xf\n\t" \
    "v_max_f32_dpp v183, v183, v183 row_ror:1 row_mask:0xf bank_mask:0xf\n\t" "s_waitcnt lgkmcnt(2)\n\t" \
    "v_smfmac_f32_16x16x64_bf16 v[224:227], v[216:219], v[160:167], %[ix]\n\t" "s_waitcnt lgkmcnt(0)\n\t" \
    "v_smfmac_f32_16x16x64_bf16 v[224:227], v[220:223], v[168:175], %[ix]\n\t" "s_nop 7\n\t" "v_add_f32 v176, v224, v225\n\t" "v_add_f32 v177, v226, v227\n\t" \
    "v_max_f32 v182, v176, v182\n\t" "v_max_f32 v183, v177, v183\n\t" "s_nop 0\n\t" "v_max_f32_dpp v182, v182, v182 row_ror:4 row_mask:0xf bank_mask:0xf\n\t" \
    "v_max_f32_dpp v183, v183, v183 row_ror:4 row_mask:0xf bank_mask:0xf\n\t" "s_nop 0\n\t" "v_max_f32_dpp v182, v182, v182 row_ror:2 row_mask:0xf bank_mask:0xf\n\t" \
    "v_max_f32_dpp v183, v183, v183 row_ror:2 row_mask:0xf bank_mask:0xf\n\t" "s_nop 0\n\t" "v_max_f32_dpp v182, v182, v182 row_ror:1 row_mask:0xf bank_mask:0xf\n\t" \
    "v_max_f32_dpp v183, v183, v183 row_ror:1 row_mask:0xf bank_mask:0xf\n\t" "v_pk_add_f32 v[178:179], v[178:179], v[182:183] neg_lo:[0,1] neg_hi:[0,1]\n\t" \
    "v_pk_add_f32 v[180:181], v[180:181], v[182:183] neg_lo:[0,1] neg_hi:[0,1]\n\t" "v_pk_add_f32 v[176:177], v[176:177], v[182:183] neg_lo:[0,1] neg_hi:[0,1]\n\t" \
    "v_pk_mul_f32 v[178:179], v[178:179], s[66:67]\n\t" "v_pk_mul_f32 v[180:181], v[180:181], s[66:67]\n\t" "v_pk_mul_f32 v[176:177], v[176:177], s[66:67]\n\t" \
    "v_exp_f32 v178, v178\n\t" "v_exp_f32 v179, v179\n\t" "v_exp_f32 v180, v180\n\t" "v_exp_f32 v181, v181\n\t" "v_exp_f32 v176, v176\n\t" \
    "v_pk_add_f32 v[182:183], v[178:179], v[180:181]\n\t" "v_exp_f32 v177, v177\n\t" "s_nop 0\n\t" "v_add_f32_dpp v182, v182, v182 row_ror:8 row_mask:0xf bank_mask:0xf\n\t" \
    "v_add_f32_dpp v183, v183, v183 row_ror:8 row_mask:0xf bank_mask:0xf\n\t" "v_add_f32 v182, v182, v176\n\t" "v_add_f32 v183, v183, v177\n\t" "s_nop 0\n\t" \
    "v_add_f32_dpp v182, v182, v182 row_ror:4 row_mask:0xf bank_mask:0xf\n\t" "v_add_f32_dpp v183, v183, v183 row_ror:4 row_mask:0xf bank_mask:0xf\n\t" "s_nop 0\n\t" \
    "v_add_f32_dpp v182, v182, v182 row_ror:2 row_mask:0xf bank_mask:0xf\n\t" "v_add_f32_dpp v183, v183, v183 row_ror:2 row_mask:0xf bank_mask:0xf\n\t" "s_nop 0\n\t" \
    "v_add_f32_dpp v182, v182, v182 row_ror:1 row_mask:0xf bank_mask:0xf\n\t" "v_add_f32_dpp v183, v183, v183 row_ror:1 row_mask:0xf bank_mask:0xf\n\t" \
    "v_rcp_f32 v182, v182\n\t" "v_rcp_f32 v183, v183\n\t" "s_nop 0\n\t" "v_pk_mul_f32 v[176:177], v[176:177], v[182:183]\n\t" \
    "v_pk_mul_f32 v[178:179], v[178:179], v[182:183]\n\t" "v_pk_mul_f32 v[180:181], v[180:181], v[182:183]\n\t" "v_cvt_pk_bf16_f32 v176, v176, v177\n\t" \
    "v_cvt_pk_bf16_f32 v178, v178, v179\n\t" "v_cvt_pk_bf16_f32 v180, v180, v181\n\t" "v_mov_b32_dpp v177, v176 quad_perm:[1,0,3,2] row_mask:0xf bank_mask:0xf\n\t" \
    "v_mov_b32_dpp v179, v178 quad_perm:[1,0,3,2] row_mask:0xf bank_mask:0xf\n\t" "v_mov_b32_dpp v181, v180 quad_perm:[1,0,3,2] row_mask:0xf bank_mask:0xf\n\t" \
    "v_perm_b32 v184, v179, v178, %[ps]\n\t" "v_perm_b32 v185, v181, v180, %[ps]\n\t" "buffer_store_dwordx2 v[184:185], %[px], %[rs], %[pb] offen sc1\n\t" \
    "v_perm_b32 v186, v177, v176, %[ps]\n\t" "buffer_store_dword v186, %[pm], %[rs], %[pb] offen sc1\n\t" XT(96, 97) "s_branch 99f\n\t" "11:\n\t" "s_cmp_eq_u32 %[bd], 0\n\t" \
    "s_cbranch_scc1 16f\n\t" "s_sub_u32 %[bd], %[bd], 1\n\t" XCNT(100) "s_waitcnt lgkmcnt(0)\n\t" "s_nop 7\n\t" "s_branch 10b\n\t" "16:\n\t" "s_cmp_lg_u32 s60, 0\n\t" \
    "s_branch 12b\n\t" "14:\n\t" "s_cmp_eq_u32 %[bd], 0\n\t" "s_cbranch_scc1 15b\n\t" "s_sub_u32 %[bd], %[bd], 1\n\t" XCNT(101) "s_mov_b32 s60, 1\n\t" \
    "s_waitcnt lgkmcnt(0)\n\t" "s_nop 7\n\t" "s_branch 10b\n\t" "99:\n\t"
#define QK_STEP1 \
    "v_xor_b32 v216, 0x400, %[mo]\n\t" "s_mov_b32 s60, 0\n\t" "s_add_u32 s61, %[qb], 0x1000\n\t" "s_mov_b32 s66, 0x3e0293ee\n\t" "s_mov_b32 s67, 0x3e0293ee\n\t" XT(80, 81) \
    XT(98, 99) "s_barrier\n\t" XT(82, 83) "20:\n\t" "buffer_load_dwordx4 v[240:243], %[mo], %[rs], %[sb] offen offset:0 sc1\n\t" "v_mov_b64 v[224:225], 0\n\t" \
    "v_mov_b64 v[226:227], 0\n\t" "v_mov_b64 v[228:229], 0\n\t" "v_mov_b64 v[230:231], 0\n\t" "v_mov_b64 v[232:233], 0\n\t" "v_mov_b64 v[234:235], 0\n\t" \
    "v_mov_b64 v[236:237], 0\n\t" "v_mov_b64 v[238:239], 0\n\t" "buffer_load_dwordx4 v[244:247], v216, %[rs], %[sb] offen offset:0 sc1\n\t" \
    "ds_read_b128 v[160:163], %[l0] offset:0\n\t" "ds_read_b128 v[164:167], %[l0] offset:1024\n\t" "ds_read_b128 v[168:171], %[l0] offset:2048\n\t" \
    "ds_read_b128 v[172:175], %[l0] offset:3072\n\t" "s_nop 3\n\t" "buffer_load_dwordx4 v[248:251], %[mo], %[rs], %[sb] offen offset:2048 sc1\n\t" \
    "ds_read_b128 v[176:179], %[l0] offset:4096\n\t" "ds_read_b128 v[180:183], %[l0] offset:5120\n\t" "ds_read_b128 v[184:187], %[l0] offset:6144\n\t" \
    "ds_read_b128 v[188:191], %[l0] offset:7168\n\t" "s_nop 3\n\t" "buffer_load_dwordx4 v[252:255], v216, %[rs], %[sb] offen offset:2048 sc1\n\t" \
    "v_mov_b32 v208, 0xd028965f\n\t" "v_mov_b32 v209, 0xff800000\n\t" "s_mov_b32 s62, 0\n\t" "s_mov_b32 s63, -1\n\t" "v_mov_b32 v210, 0\n\t" "v_cmp_gt_i32 vcc, 0, %[ps]\n\t" \
    "v_cndmask_b32 v204, 0, v209, s[62:63]\n\t" "s_nop 0\n\t" "v_cndmask_b32 v200, 0, v208, vcc\n\t" "v_cmp_gt_i32 vcc, 1, %[ps]\n\t" \
    "v_cndmask_b32 v205, 0, v209, s[62:63]\n\t" "s_nop 0\n\t" "v_cndmask_b32 v201, 0, v208, vcc\n\t" "v_cmp_gt_i32 vcc, 2, %[ps]\n\t" \
    "v_cndmask_b32 v206, 0, v209, s[62:63]\n\t" "s_nop 0\n\t" "v_cndmask_b32 v202, 0, v208, vcc\n\t" "v_cmp_gt_i32 vcc, 3, %[ps]\n\t" \
    "v_cndmask_b32 v207, 0, v209, s[62:63]\n\t" "s_nop 0\n\t" "v_cndmask_b32 v203, 0, v208, vcc\n\t" "s_waitcnt vmcnt(3)\n\t" XT(84, 85) \
    "v_smfmac_f32_16x16x64_bf16 v[224:227], v[240:243], a[0:7], %[ix]\n\t" "ds_write_b128 %[l1], v[240:243] offset:4096\n\t" \
    "v_smfmac_f32_16x16x64_bf16 v[228:231], v[240:243], a[8:15], %[ix]\n\t" "v_max3_u32 v217, v240, v241, v242\n\t" \
    "v_smfmac_f32_16x16x64_bf16 v[232:235], v[240:243], v[32:39], %[ix]\n\t" "s_waitcnt lgkmcnt(7)\n\t" \
    "v_smfmac_f32_16x16x64_bf16 v[236:239], v[240:243], v[160:167], %[ix]\n\t" "ds_read_b128 v[160:163], %[l0] offset:8192\n\t" \
    "ds_read_b128 v[164:167], %[l0] offset:9216\n\t" "s_waitcnt vmcnt(2)\n\t" "v_smfmac_f32_16x16x64_bf16 v[224:227], v[244:247], a[16:23], %[ix]\n\t" \
    "ds_write_b128 %[l1], v[244:247] offset:5120\n\t" "v_max3_u32 v217, v217, v243, v244\n\t" "v_smfmac_f32_16x16x64_bf16 v[228:231], v[244:247], a[24:31], %[ix]\n\t" \
    "v_max3_u32 v217, v217, v245, v246\n\t" "v_max_u32 v217, v217, v247\n\t" "v_smfmac_f32_16x16x64_bf16 v[232:235], v[244:247], v[40:47], %[ix]\n\t" \
    "v_cmp_eq_u32 vcc, -1, v217\n\t" "s_cmp_lg_u32 s60, 0\n\t" "s_waitcnt lgkmcnt(8)\n\t" "v_smfmac_f32_16x16x64_bf16 v[236:239], v[244:247], v[168:175], %[ix]\n\t" \
    "s_cbranch_vccnz 21f\n\t" "22:\n\t" "ds_read_b128 v[168:171], %[l0] offset:10240\n\t" "s_waitcnt vmcnt(1)\n\t" \
    "v_smfmac_f32_16x16x64_bf16 v[224:227], v[248:251], a[32:39], %[ix]\n\t" "ds_write_b128 %[l1], v[248:251] offset:6144\n\t" "s_waitcnt lgkmcnt(2)\n\t" \
    "v_smfmac_f32_16x16x64_bf16 v[228:231], v[248:251], a[40:47], %[ix]\n\t" "s_cbranch_scc1 23f\n\t" "s_barrier\n\t" "23:\n\t" XT(86, 87) \
    "v_smfmac_f32_16x16x64_bf16 v[232:235], v[248:251], v[48:55], %[ix]\n\t" "ds_read_b128 v[192:195], %[l1] offset:8192\n\t" \
    "ds_read_b128 v[196:199], %[l1] offset:12288\n\t" "v_smfmac_f32_16x16x64_bf16 v[236:239], v[248:251], v[176:183], %[ix]\n\t" \
    "ds_read_b128 v[172:175], %[l0] offset:11264\n\t" "ds_read_b128 v[176:179], %[l0] offset:12288\n\t" "s_waitcnt vmcnt(0)\n\t" \
    "v_smfmac_f32_16x16x64_bf16 v[224:227], v[252:255], a[48:55], %[ix]\n\t" "ds_write_b128 %[l1], v[252:255] offset:7168\n\t" "v_max3_u32 v217, v248, v249, v250\n\t" \
    "v_smfmac_f32_16x16x64_bf16 v[228:231], v[252:255], a[56:63], %[ix]\n\t" "ds_read_b128 v[180:183], %[l0] offset:13312\n\t" "v_max3_u32 v217, v217, v251, v252\n\t" \
    "v_max3_u32 v217, v217, v253, v254\n\t" "v_smfmac_f32_16x16x64_bf16 v[232:235], v[252:255], v[56:63], %[ix]\n\t" "v_max_u32 v217, v217, v255\n\t" \
    "v_cmp_eq_u32 vcc, -1, v217\n\t" "v_smfmac_f32_16x16x64_bf16 v[236:239], v[252:255], v[184:191], %[ix]\n\t" "s_cbranch_vccnz 24f\n\t" "25:\n\t" \
    "ds_read_b128 v[240:243], %[l1] offset:0\n\t" "s_waitcnt lgkmcnt(6)\n\t" "v_smfmac_f32_16x16x64_bf16 v[224:227], v[192:195], a[64:71], %[ix]\n\t" \
    "ds_read_b128 v[184:187], %[l0] offset:14336\n\t" "ds_read_b128 v[188:191], %[l0] offset:15360\n\t" \
    "v_smfmac_f32_16x16x64_bf16 v[228:231], v[192:195], a[72:79], %[ix]\n\t" "ds_read_b128 v[244:247], %[l1] offset:9216\n\t" "s_waitcnt lgkmcnt(5)\n\t" \
    "v_smfmac_f32_16x16x64_bf16 v[232:235], v[192:195], v[64:71], %[ix]\n\t" "s_barrier\n\t" XT(88, 89) \
    "v_smfmac_f32_16x16x64_bf16 v[236:239], v[192:195], v[160:167], %[ix]\n\t" "ds_read_b128 v[160:163], %[l0] offset:16384\n\t" \
    "ds_read_b128 v[164:167], %[l0] offset:17408\n\t" "v_smfmac_f32_16x16x64_bf16 v[224:227], v[196:199], a[80:87], %[ix]\n\t" \
    "ds_read_b128 v[248:251], %[l1] offset:13312\n\t" "ds_read_b128 v[252:255], %[l1] offset:1024\n\t" \
    "v_smfmac_f32_16x16x64_bf16 v[228:231], v[196:199], a[88:95], %[ix]\n\t" "ds_read_b128 v[192:195], %[l1] offset:10240\n\t" \
    "v_smfmac_f32_16x16x64_bf16 v[232:235], v[196:199], v[72:79], %[ix]\n\t" "buffer_load_dwordx4 v[16:19], %[hq], %[rs], %[qb] offen offset:0 sc1\n\t" \
    "v_smfmac_f32_16x16x64_bf16 v[236:239], v[196:199], v[168:175], %[ix]\n\t" "ds_read_b128 v[168:171], %[l0] offset:18432\n\t" \
    "ds_read_b128 v[172:175], %[l0] offset:19456\n\t" "s_waitcnt lgkmcnt(10)\n\t" "v_smfmac_f32_16x16x64_bf16 v[224:227], v[240:243], a[96:103], %[ix]\n\t" \
    "ds_read_b128 v[196:199], %[l1] offset:14336\n\t" "v_smfmac_f32_16x16x64_bf16 v[228:231], v[240:243], a[104:111], %[ix]\n\t" \
    "v_smfmac_f32_16x16x64_bf16 v[232:235], v[240:243], v[80:87], %[ix]\n\t" "buffer_load_dwordx4 v[20:23], %[hq], %[rs], %[qb] offen offset:2048 sc1\n\t" \
    "v_smfmac_f32_16x16x64_bf16 v[236:239], v[240:243], v[176:183], %[ix]\n\t" "ds_read_b128 v[176:179], %[l0] offset:20480\n\t" \
    "ds_read_b128 v[180:183], %[l0] offset:21504\n\t" "s_waitcnt lgkmcnt(10)\n\t" "v_smfmac_f32_16x16x64_bf16 v[224:227], v[244:247], a[112:119], %[ix]\n\t" \
    "ds_read_b128 v[240:243], %[l1] offset:2048\n\t" "v_smfmac_f32_16x16x64_bf16 v[228:231], v[244:247], a[120:127], %[ix]\n\t" \
    "v_smfmac_f32_16x16x64_bf16 v[232:235], v[244:247], v[88:95], %[ix]\n\t" "buffer_load_dwordx4 v[24:27], %[hq], %[rs], s61 offen offset:0 sc1\n\t" \
    "v_smfmac_f32_16x16x64_bf16 v[236:239], v[244:247], v[184:191], %[ix]\n\t" "ds_read_b128 v[184:187], %[l0] offset:22528\n\t" \
    "ds_read_b128 v[188:191], %[l0] offset:23552\n\t" "s_waitcnt lgkmcnt(10)\n\t" "v_smfmac_f32_16x16x64_bf16 v[224:227], v[248:251], a[128:135], %[ix]\n\t" \
    "ds_read_b128 v[244:247], %[l1] offset:11264\n\t" "v_smfmac_f32_16x16x64_bf16 v[228:231], v[248:251], a[136:143], %[ix]\n\t" \
    "v_smfmac_f32_16x16x64_bf16 v[232:235], v[248:251], v[96:103], %[ix]\n\t" "buffer_load_dwordx4 v[28:31], %[hq], %[rs], s61 offen offset:2048 sc1\n\t" \
    "v_smfmac_f32_16x16x64_bf16 v[236:239], v[248:251], v[160:167], %[ix]\n\t" "ds_read_b128 v[160:163], %[l0] offset:24576\n\t" \
    "ds_read_b128 v[164:167], %[l0] offset:25600\n\t" "s_waitcnt lgkmcnt(12)\n\t" "v_smfmac_f32_16x16x64_bf16 v[224:227], v[252:255], a[144:151], %[ix]\n\t" \
    "ds_read_b128 v[248:251], %[l1] offset:15360\n\t" "v_smfmac_f32_16x16x64_bf16 v[228:231], v[252:255], a[152:159], %[ix]\n\t" \
    "v_smfmac_f32_16x16x64_bf16 v[232:235], v[252:255], v[104:111], %[ix]\n\t" "s_waitcnt lgkmcnt(10)\n\t" \
    "v_smfmac_f32_16x16x64_bf16 v[236:239], v[252:255], v[168:175], %[ix]\n\t" "ds_read_b128 v[168:171], %[l0] offset:26624\n\t" \
    "ds_read_b128 v[172:175], %[l0] offset:27648\n\t" "v_smfmac_f32_16x16x64_bf16 v[224:227], v[192:195], a[160:167], %[ix]\n\t" \
    "ds_read_b128 v[252:255], %[l1] offset:3072\n\t" "v_smfmac_f32_16x16x64_bf16 v[228:231], v[192:195], a[168:175], %[ix]\n\t" "s_waitcnt lgkmcnt(12)\n\t" \
    "v_smfmac_f32_16x16x64_bf16 v[224:227], v[196:199], a[176:183], %[ix]\n\t" "v_smfmac_f32_16x16x64_bf16 v[228:231], v[196:199], a[184:191], %[ix]\n\t" \
    "s_waitcnt lgkmcnt(9)\n\t" "v_smfmac_f32_16x16x64_bf16 v[224:227], v[240:243], a[192:199], %[ix]\n\t" \
    "v_smfmac_f32_16x16x64_bf16 v[228:231], v[240:243], a[200:207], %[ix]\n\t" "s_waitcnt lgkmcnt(6)\n\t" \
    "v_smfmac_f32_16x16x64_bf16 v[224:227], v[244:247], a[208:215], %[ix]\n\t" "v_smfmac_f32_16x16x64_bf16 v[228:231], v[244:247], a[216:223], %[ix]\n\t" \
    "s_waitcnt lgkmcnt(3)\n\t" "v_smfmac_f32_16x16x64_bf16 v[224:227], v[248:251], a[224:231], %[ix]\n\t" \
    "v_smfmac_f32_16x16x64_bf16 v[228:231], v[248:251], a[232:239], %[ix]\n\t" "s_waitcnt lgkmcnt(0)\n\t" \
    "v_smfmac_f32_16x16x64_bf16 v[224:227], v[252:255], a[240:247], %[ix]\n\t" "s_nop 1\n\t" "v_smfmac_f32_16x16x64_bf16 v[228:231], v[252:255], a[248:255], %[ix]\n\t" \
    XT(90, 91) "s_nop 1\n\t" "v_smfmac_f32_16x16x64_bf16 v[232:235], v[192:195], v[112:119], %[ix]\n\t" "s_nop 1\n\t" \
    "v_smfmac_f32_16x16x64_bf16 v[236:239], v[192:195], v[176:183], %[ix]\n\t" "ds_read_b128 v[176:179], %[l0] offset:28672\n\t" "v_add_f32 v224, v224, v225\n\t" \
    "v_add_f32 v226, v226, v227\n\t" "v_smfmac_f32_16x16x64_bf16 v[232:235], v[196:199], v[120:127], %[ix]\n\t" "ds_read_b128 v[180:183], %[l0] offset:29696\n\t" \
    "v_add_f32 v228, v228, v229\n\t" "v_add_f32 v230, v230, v231\n\t" "v_smfmac_f32_16x16x64_bf16 v[236:239], v[196:199], v[184:191], %[ix]\n\t" \
    "ds_read_b128 v[184:187], %[l0] offset:30720\n\t" "v_cvt_pk_bf16_f32 v218, v224, v228\n\t" "v_cvt_pk_bf16_f32 v219, v226, v230\n\t" \
    "v_smfmac_f32_16x16x64_bf16 v[232:235], v[240:243], v[128:135], %[ix]\n\t" "ds_write2_b32 %[qw], v218, v219 offset1:8\n\t" \
    "ds_read_b128 v[188:191], %[l0] offset:31744\n\t" "v_smfmac_f32_16x16x64_bf16 v[236:239], v[240:243], v[160:167], %[ix]\n\t" \
    "v_smfmac_f32_16x16x64_bf16 v[232:235], v[244:247], v[136:143], %[ix]\n\t" "v_smfmac_f32_16x16x64_bf16 v[236:239], v[244:247], v[168:175], %[ix]\n\t" \
    "s_waitcnt lgkmcnt(1)\n\t" "s_barrier\n\t" "v_smfmac_f32_16x16x64_bf16 v[232:235], v[248:251], v[144:151], %[ix]\n\t" \
    "v_smfmac_f32_16x16x64_bf16 v[236:239], v[248:251], v[176:183], %[ix]\n\t" "v_smfmac_f32_16x16x64_bf16 v[232:235], v[252:255], v[152:159], %[ix]\n\t" \
    "s_waitcnt lgkmcnt(0)\n\t" "v_smfmac_f32_16x16x64_bf16 v[236:239], v[252:255], v[184:191], %[ix]\n\t" XT(92, 93) "s_nop 6\n\t" "v_add_f32 v232, v232, v233\n\t" \
    "v_add_f32 v234, v234, v235\n\t" "v_add_f32 v236, v236, v237\n\t" "v_add_f32 v238, v238, v239\n\t" "v_cvt_pk_bf16_f32 v232, v232, v236\n\t" \
    "v_cvt_pk_bf16_f32 v233, v234, v238\n\t" "ds_write2_b32 %[kw], v232, v233 offset1:68\n\t" "s_waitcnt lgkmcnt(0)\n\t" "s_barrier\n\t" XT(94, 95) \
    "s_cmp_ge_u32 %[sq], %[sn]\n\t" "s_cbranch_scc1 277f\n\t" "275:\n\t" "global_load_dword v240, v210, %[fq] sc1\n\t" "s_waitcnt vmcnt(0)\n\t" \
    "v_readfirstlane_b32 %[sq], v240\n\t" "s_nop 3\n\t" "s_cmp_ge_u32 %[sq], %[sn]\n\t" "s_cbranch_scc1 276f\n\t" "s_cmp_eq_u32 %[bd], 0\n\t" "s_cbranch_scc1 276f\n\t" \
    "s_sub_u32 %[bd], %[bd], 1\n\t" "s_sleep 2\n\t" "s_branch 275b\n\t" "276:\n\t" "buffer_load_dwordx4 v[16:19], %[hq], %[rs], %[qb] offen offset:0 sc1\n\t" \
    "buffer_load_dwordx4 v[20:23], %[hq], %[rs], %[qb] offen offset:2048 sc1\n\t" "buffer_load_dwordx4 v[24:27], %[hq], %[rs], s61 offen offset:0 sc1\n\t" \
    "buffer_load_dwordx4 v[28:31], %[hq], %[rs], s61 offen offset:2048 sc1\n\t" "277:\n\t" "s_nop 3\n\t" "ds_read_b128 v[160:163], %[kx] offset:0\n\t" \
    "ds_read_b128 v[164:167], %[kx] offset:2048\n\t" "ds_read_b128 v[168:171], %[kx] offset:4096\n\t" "ds_read_b128 v[172:175], %[kx] offset:6144\n\t" \
    "ds_read_b128 v[224:227], %[km] offset:0\n\t" "ds_read_b128 v[228:231], %[km] offset:64\n\t" "ds_read_b128 v[232:235], %[km] offset:128\n\t" \
    "ds_read_b128 v[236:239], %[km] offset:192\n\t" "s_waitcnt vmcnt(0)\n\t" "global_load_dword v240, v210, %[fq] sc1\n\t" "s_waitcnt lgkmcnt(7)\n\t" \
    "v_mfma_f32_16x16x32_bf16 v[248:251], v[160:163], v[16:19], v[200:203]\n\t" "s_waitcnt lgkmcnt(6)\n\t" \
    "v_mfma_f32_16x16x32_bf16 v[248:251], v[164:167], v[20:23], v[248:251]\n\t" "s_waitcnt lgkmcnt(5)\n\t" \
    "v_mfma_f32_16x16x32_bf16 v[248:251], v[168:171], v[24:27], v[248:251]\n\t" "s_waitcnt lgkmcnt(4)\n\t" \
    "v_mfma_f32_16x16x32_bf16 v[248:251], v[172:175], v[28:31], v[248:251]\n\t" "s_waitcnt lgkmcnt(3)\n\t" \
    "v_mfma_f32_16x16x32_bf16 v[244:247], v[224:227], v[16:19], v[204:207]\n\t" "s_waitcnt lgkmcnt(2)\n\t" \
    "v_mfma_f32_16x16x32_bf16 v[244:247], v[228:231], v[20:23], v[244:247]\n\t" "s_waitcnt lgkmcnt(1)\n\t" \
    "v_mfma_f32_16x16x32_bf16 v[244:247], v[232:235], v[24:27], v[244:247]\n\t" "s_waitcnt lgkmcnt(0)\n\t" \
    "v_mfma_f32_16x16x32_bf16 v[244:247], v[236:239], v[28:31], v[244:247]\n\t" "v_mov_b32 v192, -1\n\t" "v_mov_b32 v193, -1\n\t" "v_mov_b32 v194, -1\n\t" \
    "v_mov_b32 v195, -1\n\t" "v_max3_f32 v242, v248, v249, v250\n\t" "v_max_f32 v242, v242, v251\n\t" "s_nop 1\n\t" "v_max3_f32 v242, v242, v244, v245\n\t" \
    "v_max3_f32 v242, v242, v246, v247\n\t" "v_mov_b32 v243, v242\n\t" "s_nop 1\n\t" "v_permlane32_swap_b32 v242, v243\n\t" "v_max_f32 v242, v242, v243\n\t" \
    "v_mov_b32 v243, v242\n\t" "s_nop 1\n\t" "v_permlane16_swap_b32 v242, v243\n\t" "v_max_f32 v242, v242, v243\n\t" \
    "v_pk_add_f32 v[244:245], v[244:245], v[242:243] op_sel_hi:[1,0] neg_lo:[0,1] neg_hi:[0,1]\n\t" \
    "v_pk_add_f32 v[246:247], v[246:247], v[242:243] op_sel_hi:[1,0] neg_lo:[0,1] neg_hi:[0,1]\n\t" \
    "v_pk_add_f32 v[248:249], v[248:249], v[242:243] op_sel_hi:[1,0] neg_lo:[0,1] neg_hi:[0,1]\n\t" \
    "v_pk_add_f32 v[250:251], v[250:251], v[242:243] op_sel_hi:[1,0] neg_lo:[0,1] neg_hi:[0,1]\n\t" "v_pk_mul_f32 v[244:245], v[244:245], s[66:67]\n\t" \
    "v_pk_mul_f32 v[246:247], v[246:247], s[66:67]\n\t" "v_pk_mul_f32 v[248:249], v[248:249], s[66:67]\n\t" "v_pk_mul_f32 v[250:251], v[250:251], s[66:67]\n\t" \
    "v_exp_f32 v244, v244\n\t" "v_exp_f32 v245, v245\n\t" "v_exp_f32 v246, v246\n\t" "v_exp_f32 v247, v247\n\t" "v_exp_f32 v248, v248\n\t" "v_exp_f32 v249, v249\n\t" \
    "v_exp_f32 v250, v250\n\t" "v_exp_f32 v251, v251\n\t" "v_pk_add_f32 v[242:243], v[244:245], v[246:247]\n\t" "v_pk_add_f32 v[242:243], v[242:243], v[248:249]\n\t" \
    "v_pk_add_f32 v[242:243], v[242:243], v[250:251]\n\t" "v_add_f32 v242, v242, v243\n\t" "v_mov_b32 v243, v242\n\t" "s_nop 1\n\t" "v_permlane32_swap_b32 v242, v243\n\t" \
    "v_add_f32 v242, v242, v243\n\t" "v_mov_b32 v243, v242\n\t" "s_nop 1\n\t" "v_permlane16_swap_b32 v242, v243\n\t" "v_add_f32 v242, v242, v243\n\t" \
    "v_rcp_f32 v242, v242\n\t" "s_nop 0\n\t" "v_pk_mul_f32 v[244:245], v[244:245], v[242:243] op_sel_hi:[1,0]\n\t" \
    "v_pk_mul_f32 v[246:247], v[246:247], v[242:243] op_sel_hi:[1,0]\n\t" "v_pk_mul_f32 v[248:249], v[248:249], v[242:243] op_sel_hi:[1,0]\n\t" \
    "v_pk_mul_f32 v[250:251], v[250:251], v[242:243] op_sel_hi:[1,0]\n\t" "v_cvt_pk_bf16_f32 v162, v244, v245\n\t" "v_cvt_pk_bf16_f32 v163, v246, v247\n\t" \
    "v_cvt_pk_bf16_f32 v164, v248, v249\n\t" "v_cvt_pk_bf16_f32 v165, v250, v251\n\t" "buffer_store_dwordx2 v[164:165], %[px], %[rs], %[tb] offen sc1\n\t" \
    "buffer_store_dwordx2 v[162:163], %[pm], %[rs], %[tb] offen sc1\n\t" "buffer_store_dwordx2 v[192:193], %[px], %[rs], %[tr] offen sc1\n\t" \
    "buffer_store_dwordx2 v[192:193], %[pm], %[rs], %[tr] offen sc1\n\t" "s_waitcnt vmcnt(4)\n\t" "v_readfirstlane_b32 s62, v240\n\t" "s_nop 3\n\t" \
    "s_max_u32 %[sq], %[sq], s62\n\t" XT(96, 97) "s_branch 99f\n\t" "21:\n\t" "s_cmp_eq_u32 %[bd], 0\n\t" "s_cbranch_scc1 26f\n\t" "s_sub_u32 %[bd], %[bd], 1\n\t" XCNT(100) \
    "s_waitcnt lgkmcnt(0)\n\t" "s_nop 7\n\t" "s_branch 20b\n\t" "26:\n\t" "s_cmp_lg_u32 s60, 0\n\t" "s_branch 22b\n\t" "24:\n\t" "s_cmp_eq_u32 %[bd], 0\n\t" \
    "s_cbranch_scc1 25b\n\t" "s_sub_u32 %[bd], %[bd], 1\n\t" XCNT(101) "s_mov_b32 s60, 1\n\t" "s_waitcnt lgkmcnt(0)\n\t" "s_nop 7\n\t" "s_branch 20b\n\t" "99:\n\t"
#define QK_STEP2 \
    "v_xor_b32 v216, 0x400, %[mo]\n\t" "s_mov_b32 s60, 0\n\t" "s_add_u32 s61, %[qb], 0x1000\n\t" "s_mov_b32 s66, 0x3e0293ee\n\t" "s_mov_b32 s67, 0x3e0293ee\n\t" XT(80, 81) \
    XT(98, 99) "s_barrier\n\t" XT(82, 83) "30:\n\t" "buffer_load_dwordx4 v[240:243], %[mo], %[rs], %[sb] offen offset:0 sc1\n\t" "v_mov_b64 v[224:225], 0\n\t" \
    "v_mov_b64 v[226:227], 0\n\t" "v_mov_b64 v[228:229], 0\n\t" "v_mov_b64 v[230:231], 0\n\t" "v_mov_b64 v[232:233], 0\n\t" "v_mov_b64 v[234:235], 0\n\t" \
    "v_mov_b64 v[236:237], 0\n\t" "v_mov_b64 v[238:239], 0\n\t" "buffer_load_dwordx4 v[244:247], v216, %[rs], %[sb] offen offset:0 sc1\n\t" \
    "ds_read_b128 v[160:163], %[l0] offset:0\n\t" "ds_read_b128 v[164:167], %[l0] offset:1024\n\t" "ds_read_b128 v[168:171], %[l0] offset:2048\n\t" \
    "ds_read_b128 v[172:175], %[l0] offset:3072\n\t" "s_nop 3\n\t" "buffer_load_dwordx4 v[248:251], %[mo], %[rs], %[sb] offen offset:2048 sc1\n\t" \
    "ds_read_b128 v[176:179], %[l0] offset:4096\n\t" "ds_read_b128 v[180:183], %[l0] offset:5120\n\t" "ds_read_b128 v[184:187], %[l0] offset:6144\n\t" \
    "ds_read_b128 v[188:191], %[l0] offset:7168\n\t" "s_nop 3\n\t" "buffer_load_dwordx4 v[252:255], v216, %[rs], %[sb] offen offset:2048 sc1\n\t" \
    "v_mov_b32 v208, 0xd028965f\n\t" "v_mov_b32 v209, 0xff800000\n\t" "s_mov_b32 s62, 0\n\t" "s_mov_b32 s63, -1\n\t" "v_mov_b32 v210, 0\n\t" "v_cmp_gt_i32 vcc, 0, %[ps]\n\t" \
    "v_cndmask_b32 v204, 0, v209, s[62:63]\n\t" "s_nop 0\n\t" "v_cndmask_b32 v200, 0, v208, vcc\n\t" "v_cmp_gt_i32 vcc, 1, %[ps]\n\t" \
    "v_cndmask_b32 v205, 0, v209, s[62:63]\n\t" "s_nop 0\n\t" "v_cndmask_b32 v201, 0, v208, vcc\n\t" "v_cmp_gt_i32 vcc, 2, %[ps]\n\t" \
    "v_cndmask_b32 v206, 0, v209, s[62:63]\n\t" "s_nop 0\n\t" "v_cndmask_b32 v202, 0, v208, vcc\n\t" "v_cmp_gt_i32 vcc, 3, %[ps]\n\t" \
    "v_cndmask_b32 v207, 0, v209, s[62:63]\n\t" "s_nop 0\n\t" "v_cndmask_b32 v203, 0, v208, vcc\n\t" "s_waitcnt vmcnt(3)\n\t" XT(84, 85) \
    "v_smfmac_f32_16x16x64_bf16 v[224:227], v[240:243], a[0:7], %[ix]\n\t" "ds_write_b128 %[l1], v[240:243] offset:8192\n\t" \
    "v_smfmac_f32_16x16x64_bf16 v[228:231], v[240:243], a[8:15], %[ix]\n\t" "v_max3_u32 v217, v240, v241, v242\n\t" \
    "v_smfmac_f32_16x16x64_bf16 v[232:235], v[240:243], v[32:39], %[ix]\n\t" "s_waitcnt lgkmcnt(7)\n\t" \
    "v_smfmac_f32_16x16x64_bf16 v[236:239], v[240:243], v[160:167], %[ix]\n\t" "ds_read_b128 v[160:163], %[l0] offset:8192\n\t" \
    "ds_read_b128 v[164:167], %[l0] offset:9216\n\t" "s_waitcnt vmcnt(2)\n\t" "v_smfmac_f32_16x16x64_bf16 v[224:227], v[244:247], a[16:23], %[ix]\n\t" \
    "ds_write_b128 %[l1], v[244:247] offset:9216\n\t" "v_max3_u32 v217, v217, v243, v244\n\t" "v_smfmac_f32_16x16x64_bf16 v[228:231], v[244:247], a[24:31], %[ix]\n\t" \
    "v_max3_u32 v217, v217, v245, v246\n\t" "v_max_u32 v217, v217, v247\n\t" "v_smfmac_f32_16x16x64_bf16 v[232:235], v[244:247], v[40:47], %[ix]\n\t" \
    "v_cmp_eq_u32 vcc, -1, v217\n\t" "s_cmp_lg_u32 s60, 0\n\t" "s_waitcnt lgkmcnt(8)\n\t" "v_smfmac_f32_16x16x64_bf16 v[236:239], v[244:247], v[168:175], %[ix]\n\t" \
    "s_cbranch_vccnz 31f\n\t" "32:\n\t" "ds_read_b128 v[168:171], %[l0] offset:10240\n\t" "s_waitcnt vmcnt(1)\n\t" \
    "v_smfmac_f32_16x16x64_bf16 v[224:227], v[248:251], a[32:39], %[ix]\n\t" "ds_write_b128 %[l1], v[248:251] offset:10240\n\t" "s_waitcnt lgkmcnt(2)\n\t" \
    "v_smfmac_f32_16x16x64_bf16 v[228:231], v[248:251], a[40:47], %[ix]\n\t" "s_cbranch_scc1 33f\n\t" "s_barrier\n\t" "33:\n\t" XT(86, 87) \
    "v_smfmac_f32_16x16x64_bf16 v[232:235], v[248:251], v[48:55], %[ix]\n\t" "ds_read_b128 v[192:195], %[l1] offset:12288\n\t" "ds_read_b128 v[196:199], %[l1] offset:0\n\t" \
    "v_smfmac_f32_16x16x64_bf16 v[236:239], v[248:251], v[176:183], %[ix]\n\t" "ds_read_b128 v[172:175], %[l0] offset:11264\n\t" \
    "ds_read_b128 v[176:179], %[l0] offset:12288\n\t" "s_waitcnt vmcnt(0)\n\t" "v_smfmac_f32_16x16x64_bf16 v[224:227], v[252:255], a[48:55], %[ix]\n\t" \
    "ds_write_b128 %[l1], v[252:255] offset:11264\n\t" "v_max3_u32 v217, v248, v249, v250\n\t" "v_smfmac_f32_16x16x64_bf16 v[228:231], v[252:255], a[56:63], %[ix]\n\t" \
    "ds_read_b128 v[180:183], %[l0] offset:13312\n\t" "v_max3_u32 v217, v217, v251, v252\n\t" "v_max3_u32 v217, v217, v253, v254\n\t" \
    "v_smfmac_f32_16x16x64_bf16 v[232:235], v[252:255], v[56:63], %[ix]\n\t" "v_max_u32 v217, v217, v255\n\t" "v_cmp_eq_u32 vcc, -1, v217\n\t" \
    "v_smfmac_f32_16x16x64_bf16 v[236:239], v[252:255], v[184:191], %[ix]\n\t" "s_cbranch_vccnz 34f\n\t" "35:\n\t" "ds_read_b128 v[240:243], %[l1] offset:4096\n\t" \
    "s_waitcnt lgkmcnt(6)\n\t" "v_smfmac_f32_16x16x64_bf16 v[224:227], v[192:195], a[64:71], %[ix]\n\t" "ds_read_b128 v[184:187], %[l0] offset:14336\n\t" \
    "ds_read_b128 v[188:191], %[l0] offset:15360\n\t" "v_smfmac_f32_16x16x64_bf16 v[228:231], v[192:195], a[72:79], %[ix]\n\t" \
    "ds_read_b128 v[244:247], %[l1] offset:13312\n\t" "s_waitcnt lgkmcnt(5)\n\t" "v_smfmac_f32_16x16x64_bf16 v[232:235], v[192:195], v[64:71], %[ix]\n\t" "s_barrier\n\t" \
    XT(88, 89) "v_smfmac_f32_16x16x64_bf16 v[236:239], v[192:195], v[160:167], %[ix]\n\t" "ds_read_b128 v[160:163], %[l0] offset:16384\n\t" \
    "ds_read_b128 v[164:167], %[l0] offset:17408\n\t" "v_smfmac_f32_16x16x64_bf16 v[224:227], v[196:199], a[80:87], %[ix]\n\t" \
    "ds_read_b128 v[248:251], %[l1] offset:1024\n\t" "ds_read_b128 v[252:255], %[l1] offset:5120\n\t" "v_smfmac_f32_16x16x64_bf16 v[228:231], v[196:199], a[88:95], %[ix]\n\t" \
    "ds_read_b128 v[192:195], %[l1] offset:14336\n\t" "v_smfmac_f32_16x16x64_bf16 v[232:235], v[196:199], v[72:79], %[ix]\n\t" \
    "buffer_load_dwordx4 v[16:19], %[hq], %[rs], %[qb] offen offset:0 sc1\n\t" "v_smfmac_f32_16x16x64_bf16 v[236:239], v[196:199], v[168:175], %[ix]\n\t" \
    "ds_read_b128 v[168:171], %[l0] offset:18432\n\t" "ds_read_b128 v[172:175], %[l0] offset:19456\n\t" "s_waitcnt lgkmcnt(10)\n\t" \
    "v_smfmac_f32_16x16x64_bf16 v[224:227], v[240:243], a[96:103], %[ix]\n\t" "ds_read_b128 v[196:199], %[l1] offset:2048\n\t" \
    "v_smfmac_f32_16x16x64_bf16 v[228:231], v[240:243], a[104:111], %[ix]\n\t" "v_smfmac_f32_16x16x64_bf16 v[232:235], v[240:243], v[80:87], %[ix]\n\t" \
    "buffer_load_dwordx4 v[20:23], %[hq], %[rs], %[qb] offen offset:2048 sc1\n\t" "v_smfmac_f32_16x16x64_bf16 v[236:239], v[240:243], v[176:183], %[ix]\n\t" \
    "ds_read_b128 v[176:179], %[l0] offset:20480\n\t" "ds_read_b128 v[180:183], %[l0] offset:21504\n\t" "s_waitcnt lgkmcnt(10)\n\t" \
    "v_smfmac_f32_16x16x64_bf16 v[224:227], v[244:247], a[112:119], %[ix]\n\t" "ds_read_b128 v[240:243], %[l1] offset:6144\n\t" \
    "v_smfmac_f32_16x16x64_bf16 v[228:231], v[244:247], a[120:127], %[ix]\n\t" "v_smfmac_f32_16x16x64_bf16 v[232:235], v[244:247], v[88:95], %[ix]\n\t" \
    "buffer_load_dwordx4 v[24:27], %[hq], %[rs], s61 offen offset:0 sc1\n\t" "v_smfmac_f32_16x16x64_bf16 v[236:239], v[244:247], v[184:191], %[ix]\n\t" \
    "ds_read_b128 v[184:187], %[l0] offset:22528\n\t" "ds_read_b128 v[188:191], %[l0] offset:23552\n\t" "s_waitcnt lgkmcnt(10)\n\t" \
    "v_smfmac_f32_16x16x64_bf16 v[224:227], v[248:251], a[128:135], %[ix]\n\t" "ds_read_b128 v[244:247], %[l1] offset:15360\n\t" \
    "v_smfmac_f32_16x16x64_bf16 v[228:231], v[248:251], a[136:143], %[ix]\n\t" "v_smfmac_f32_16x16x64_bf16 v[232:235], v[248:251], v[96:103], %[ix]\n\t" \
    "buffer_load_dwordx4 v[28:31], %[hq], %[rs], s61 offen offset:2048 sc1\n\t" "v_smfmac_f32_16x16x64_bf16 v[236:239], v[248:251], v[160:167], %[ix]\n\t" \
    "ds_read_b128 v[160:163], %[l0] offset:24576\n\t" "ds_read_b128 v[164:167], %[l0] offset:25600\n\t" "s_waitcnt lgkmcnt(12)\n\t" \
    "v_smfmac_f32_16x16x64_bf16 v[224:227], v[252:255], a[144:151], %[ix]\n\t" "ds_read_b128 v[248:251], %[l1] offset:3072\n\t" \
    "v_smfmac_f32_16x16x64_bf16 v[228:231], v[252:255], a[152:159], %[ix]\n\t" "v_smfmac_f32_16x16x64_bf16 v[232:235], v[252:255], v[104:111], %[ix]\n\t" \
    "s_waitcnt lgkmcnt(10)\n\t" "v_smfmac_f32_16x16x64_bf16 v[236:239], v[252:255], v[168:175], %[ix]\n\t" "ds_read_b128 v[168:171], %[l0] offset:26624\n\t" \
    "ds_read_b128 v[172:175], %[l0] offset:27648\n\t" "v_smfmac_f32_16x16x64_bf16 v[224:227], v[192:195], a[160:167], %[ix]\n\t" \
    "ds_read_b128 v[252:255], %[l1] offset:7168\n\t" "v_smfmac_f32_16x16x64_bf16 v[228:231], v[192:195], a[168:175], %[ix]\n\t" "s_waitcnt lgkmcnt(12)\n\t" \
    "v_smfmac_f32_16x16x64_bf16 v[224:227], v[196:199], a[176:183], %[ix]\n\t" "v_smfmac_f32_16x16x64_bf16 v[228:231], v[196:199], a[184:191], %[ix]\n\t" \
    "s_waitcnt lgkmcnt(9)\n\t" "v_smfmac_f32_16x16x64_bf16 v[224:227], v[240:243], a[192:199], %[ix]\n\t" \
    "v_smfmac_f32_16x16x64_bf16 v[228:231], v[240:243], a[200:207], %[ix]\n\t" "s_waitcnt lgkmcnt(6)\n\t" \
    "v_smfmac_f32_16x16x64_bf16 v[224:227], v[244:247], a[208:215], %[ix]\n\t" "v_smfmac_f32_16x16x64_bf16 v[228:231], v[244:247], a[216:223], %[ix]\n\t" \
    "s_waitcnt lgkmcnt(3)\n\t" "v_smfmac_f32_16x16x64_bf16 v[224:227], v[248:251], a[224:231], %[ix]\n\t" \
    "v_smfmac_f32_16x16x64_bf16 v[228:231], v[248:251], a[232:239], %[ix]\n\t" "s_waitcnt lgkmcnt(0)\n\t" \
    "v_smfmac_f32_16x16x64_bf16 v[224:227], v[252:255], a[240:247], %[ix]\n\t" "s_nop 1\n\t" "v_smfmac_f32_16x16x64_bf16 v[228:231], v[252:255], a[248:255], %[ix]\n\t" \
    XT(90, 91) "s_nop 1\n\t" "v_smfmac_f32_16x16x64_bf16 v[232:235], v[192:195], v[112:119], %[ix]\n\t" "s_nop 1\n\t" \
    "v_smfmac_f32_16x16x64_bf16 v[236:239], v[192:195], v[176:183], %[ix]\n\t" "ds_read_b128 v[176:179], %[l0] offset:28672\n\t" "v_add_f32 v224, v224, v225\n\t" \
    "v_add_f32 v226, v226, v227\n\t" "v_smfmac_f32_16x16x64_bf16 v[232:235], v[196:199], v[120:127], %[ix]\n\t" "ds_read_b128 v[180:183], %[l0] offset:29696\n\t" \
    "v_add_f32 v228, v228, v229\n\t" "v_add_f32 v230, v230, v231\n\t" "v_smfmac_f32_16x16x64_bf16 v[236:239], v[196:199], v[184:191], %[ix]\n\t" \
    "ds_read_b128 v[184:187], %[l0] offset:30720\n\t" "v_cvt_pk_bf16_f32 v218, v224, v228\n\t" "v_cvt_pk_bf16_f32 v219, v226, v230\n\t" \
    "v_smfmac_f32_16x16x64_bf16 v[232:235], v[240:243], v[128:135], %[ix]\n\t" "ds_write2_b32 %[qw], v218, v219 offset1:8\n\t" \
    "ds_read_b128 v[188:191], %[l0] offset:31744\n\t" "v_smfmac_f32_16x16x64_bf16 v[236:239], v[240:243], v[160:167], %[ix]\n\t" \
    "v_smfmac_f32_16x16x64_bf16 v[232:235], v[244:247], v[136:143], %[ix]\n\t" "v_smfmac_f32_16x16x64_bf16 v[236:239], v[244:247], v[168:175], %[ix]\n\t" \
    "s_waitcnt lgkmcnt(1)\n\t" "s_barrier\n\t" "v_smfmac_f32_16x16x64_bf16 v[232:235], v[248:251], v[144:151], %[ix]\n\t" \
    "v_smfmac_f32_16x16x64_bf16 v[236:239], v[248:251], v[176:183], %[ix]\n\t" "v_smfmac_f32_16x16x64_bf16 v[232:235], v[252:255], v[152:159], %[ix]\n\t" \
    "s_waitcnt lgkmcnt(0)\n\t" "v_smfmac_f32_16x16x64_bf16 v[236:239], v[252:255], v[184:191], %[ix]\n\t" XT(92, 93) "s_nop 6\n\t" "v_add_f32 v232, v232, v233\n\t" \
    "v_add_f32 v234, v234, v235\n\t" "v_add_f32 v236, v236, v237\n\t" "v_add_f32 v238, v238, v239\n\t" "v_cvt_pk_bf16_f32 v232, v232, v236\n\t" \
    "v_cvt_pk_bf16_f32 v233, v234, v238\n\t" "ds_write2_b32 %[kw], v232, v233 offset1:68\n\t" "s_waitcnt lgkmcnt(0)\n\t" "s_barrier\n\t" XT(94, 95) \
    "s_cmp_ge_u32 %[sq], %[sn]\n\t" "s_cbranch_scc1 377f\n\t" "375:\n\t" "global_load_dword v240, v210, %[fq] sc1\n\t" "s_waitcnt vmcnt(0)\n\t" \
    "v_readfirstlane_b32 %[sq], v240\n\t" "s_nop 3\n\t" "s_cmp_ge_u32 %[sq], %[sn]\n\t" "s_cbranch_scc1 376f\n\t" "s_cmp_eq_u32 %[bd], 0\n\t" "s_cbranch_scc1 376f\n\t" \
    "s_sub_u32 %[bd], %[bd], 1\n\t" "s_sleep 2\n\t" "s_branch 375b\n\t" "376:\n\t" "buffer_load_dwordx4 v[16:19], %[hq], %[rs], %[qb] offen offset:0 sc1\n\t" \
    "buffer_load_dwordx4 v[20:23], %[hq], %[rs], %[qb] offen offset:2048 sc1\n\t" "buffer_load_dwordx4 v[24:27], %[hq], %[rs], s61 offen offset:0 sc1\n\t" \
    "buffer_load_dwordx4 v[28:31], %[hq], %[rs], s61 offen offset:2048 sc1\n\t" "377:\n\t" "s_nop 3\n\t" "ds_read_b128 v[160:163], %[kx] offset:0\n\t" \
    "ds_read_b128 v[164:167], %[kx] offset:2048\n\t" "ds_read_b128 v[168:171], %[kx] offset:4096\n\t" "ds_read_b128 v[172:175], %[kx] offset:6144\n\t" \
    "ds_read_b128 v[176:179], %[kx] offset:256\n\t" "ds_read_b128 v[180:183], %[kx] offset:2304\n\t" "ds_read_b128 v[184:187], %[kx] offset:4352\n\t" \
    "ds_read_b128 v[188:191], %[kx] offset:6400\n\t" "ds_read_b128 v[224:227], %[km] offset:0\n\t" "ds_read_b128 v[228:231], %[km] offset:64\n\t" \
    "ds_read_b128 v[232:235], %[km] offset:128\n\t" "ds_read_b128 v[236:239], %[km] offset:192\n\t" "s_waitcnt vmcnt(0)\n\t" "global_load_dword v240, v210, %[fq] sc1\n\t" \
    "s_waitcnt lgkmcnt(11)\n\t" "v_mfma_f32_16x16x32_bf16 v[248:251], v[160:163], v[16:19], 0\n\t" "s_waitcnt lgkmcnt(10)\n\t" \
    "v_mfma_f32_16x16x32_bf16 v[248:251], v[164:167], v[20:23], v[248:251]\n\t" "s_waitcnt lgkmcnt(9)\n\t" \
    "v_mfma_f32_16x16x32_bf16 v[248:251], v[168:171], v[24:27], v[248:251]\n\t" "s_waitcnt lgkmcnt(8)\n\t" \
    "v_mfma_f32_16x16x32_bf16 v[248:251], v[172:175], v[28:31], v[248:251]\n\t" "s_waitcnt lgkmcnt(7)\n\t" \
    "v_mfma_f32_16x16x32_bf16 v[252:255], v[176:179], v[16:19], v[200:203]\n\t" "s_waitcnt lgkmcnt(6)\n\t" \
    "v_mfma_f32_16x16x32_bf16 v[252:255], v[180:183], v[20:23], v[252:255]\n\t" "s_waitcnt lgkmcnt(5)\n\t" \
    "v_mfma_f32_16x16x32_bf16 v[252:255], v[184:187], v[24:27], v[252:255]\n\t" "s_waitcnt lgkmcnt(4)\n\t" \
    "v_mfma_f32_16x16x32_bf16 v[252:255], v[188:191], v[28:31], v[252:255]\n\t" "s_waitcnt lgkmcnt(3)\n\t" \
    "v_mfma_f32_16x16x32_bf16 v[244:247], v[224:227], v[16:19], v[204:207]\n\t" "s_waitcnt lgkmcnt(2)\n\t" \
    "v_mfma_f32_16x16x32_bf16 v[244:247], v[228:231], v[20:23], v[244:247]\n\t" "s_waitcnt lgkmcnt(1)\n\t" \
    "v_mfma_f32_16x16x32_bf16 v[244:247], v[232:235], v[24:27], v[244:247]\n\t" "s_waitcnt lgkmcnt(0)\n\t" \
    "v_mfma_f32_16x16x32_bf16 v[244:247], v[236:239], v[28:31], v[244:247]\n\t" "v_mov_b32 v192, -1\n\t" "v_mov_b32 v193, -1\n\t" "v_mov_b32 v194, -1\n\t" \
    "v_mov_b32 v195, -1\n\t" "v_max3_f32 v242, v248, v249, v250\n\t" "v_max3_f32 v242, v242, v251, v252\n\t" "v_max3_f32 v242, v242, v253, v254\n\t" \
    "v_max_f32 v242, v242, v255\n\t" "v_max3_f32 v242, v242, v244, v245\n\t" "v_max3_f32 v242, v242, v246, v247\n\t" "v_mov_b32 v243, v242\n\t" "s_nop 1\n\t" \
    "v_permlane32_swap_b32 v242, v243\n\t" "v_max_f32 v242, v242, v243\n\t" "v_mov_b32 v243, v242\n\t" "s_nop 1\n\t" "v_permlane16_swap_b32 v242, v243\n\t" \
    "v_max_f32 v242, v242, v243\n\t" "v_pk_add_f32 v[244:245], v[244:245], v[242:243] op_sel_hi:[1,0] neg_lo:[0,1] neg_hi:[0,1]\n\t" \
    "v_pk_add_f32 v[246:247], v[246:247], v[242:243] op_sel_hi:[1,0] neg_lo:[0,1] neg_hi:[0,1]\n\t" \
    "v_pk_add_f32 v[248:249], v[248:249], v[242:243] op_sel_hi:[1,0] neg_lo:[0,1] neg_hi:[0,1]\n\t" \
    "v_pk_add_f32 v[250:251], v[250:251], v[242:243] op_sel_hi:[1,0] neg_lo:[0,1] neg_hi:[0,1]\n\t" \
    "v_pk_add_f32 v[252:253], v[252:253], v[242:243] op_sel_hi:[1,0] neg_lo:[0,1] neg_hi:[0,1]\n\t" \
    "v_pk_add_f32 v[254:255], v[254:255], v[242:243] op_sel_hi:[1,0] neg_lo:[0,1] neg_hi:[0,1]\n\t" "v_pk_mul_f32 v[244:245], v[244:245], s[66:67]\n\t" \
    "v_pk_mul_f32 v[246:247], v[246:247], s[66:67]\n\t" "v_pk_mul_f32 v[248:249], v[248:249], s[66:67]\n\t" "v_pk_mul_f32 v[250:251], v[250:251], s[66:67]\n\t" \
    "v_pk_mul_f32 v[252:253], v[252:253], s[66:67]\n\t" "v_pk_mul_f32 v[254:255], v[254:255], s[66:67]\n\t" "v_exp_f32 v244, v244\n\t" "v_exp_f32 v245, v245\n\t" \
    "v_exp_f32 v246, v246\n\t" "v_exp_f32 v247, v247\n\t" "v_exp_f32 v248, v248\n\t" "v_exp_f32 v249, v249\n\t" "v_exp_f32 v250, v250\n\t" "v_exp_f32 v251, v251\n\t" \
    "v_exp_f32 v252, v252\n\t" "v_exp_f32 v253, v253\n\t" "v_exp_f32 v254, v254\n\t" "v_exp_f32 v255, v255\n\t" "v_pk_add_f32 v[242:243], v[244:245], v[246:247]\n\t" \
    "v_pk_add_f32 v[242:243], v[242:243], v[248:249]\n\t" "v_pk_add_f32 v[242:243], v[242:243], v[250:251]\n\t" "v_pk_add_f32 v[242:243], v[242:243], v[252:253]\n\t" \
    "v_pk_add_f32 v[242:243], v[242:243], v[254:255]\n\t" "v_add_f32 v242, v242, v243\n\t" "v_mov_b32 v243, v242\n\t" "s_nop 1\n\t" "v_permlane32_swap_b32 v242, v243\n\t" \
    "v_add_f32 v242, v242, v243\n\t" "v_mov_b32 v243, v242\n\t" "s_nop 1\n\t" "v_permlane16_swap_b32 v242, v243\n\t" "v_add_f32 v242, v242, v243\n\t" \
    "v_rcp_f32 v242, v242\n\t" "s_nop 0\n\t" "v_pk_mul_f32 v[244:245], v[244:245], v[242:243] op_sel_hi:[1,0]\n\t" \
    "v_pk_mul_f32 v[246:247], v[246:247], v[242:243] op_sel_hi:[1,0]\n\t" "v_pk_mul_f32 v[248:249], v[248:249], v[242:243] op_sel_hi:[1,0]\n\t" \
    "v_pk_mul_f32 v[250:251], v[250:251], v[242:243] op_sel_hi:[1,0]\n\t" "v_pk_mul_f32 v[252:253], v[252:253], v[242:243] op_sel_hi:[1,0]\n\t" \
    "v_pk_mul_f32 v[254:255], v[254:255], v[242:243] op_sel_hi:[1,0]\n\t" "v_cvt_pk_bf16_f32 v162, v244, v245\n\t" "v_cvt_pk_bf16_f32 v163, v246, v247\n\t" \
    "v_cvt_pk_bf16_f32 v164, v248, v249\n\t" "v_cvt_pk_bf16_f32 v165, v250, v251\n\t" "v_cvt_pk_bf16_f32 v166, v252, v253\n\t" "v_cvt_pk_bf16_f32 v167, v254, v255\n\t" \
    "buffer_store_dwordx4 v[164:167], %[px], %[rs], %[tb] offen sc1\n\t" "buffer_store_dwordx2 v[162:163], %[pm], %[rs], %[tb] offen sc1\n\t" \
    "buffer_store_dwordx4 v[192:195], %[px], %[rs], %[tr] offen sc1\n\t" "buffer_store_dwordx2 v[192:193], %[pm], %[rs], %[tr] offen sc1\n\t" "s_waitcnt vmcnt(4)\n\t" \
    "v_readfirstlane_b32 s62, v240\n\t" "s_nop 3\n\t" "s_max_u32 %[sq], %[sq], s62\n\t" XT(96, 97) "s_branch 99f\n\t" "31:\n\t" "s_cmp_eq_u32 %[bd], 0\n\t" \
    "s_cbranch_scc1 36f\n\t" "s_sub_u32 %[bd], %[bd], 1\n\t" XCNT(100) "s_waitcnt lgkmcnt(0)\n\t" "s_nop 7\n\t" "s_branch 30b\n\t" "36:\n\t" "s_cmp_lg_u32 s60, 0\n\t" \
    "s_branch 32b\n\t" "34:\n\t" "s_cmp_eq_u32 %[bd], 0\n\t" "s_cbranch_scc1 35b\n\t" "s_sub_u32 %[bd], %[bd], 1\n\t" XCNT(101) "s_mov_b32 s60, 1\n\t" \
    "s_waitcnt lgkmcnt(0)\n\t" "s_nop 7\n\t" "s_branch 30b\n\t" "99:\n\t"
#define QK_STEP3 \
    "v_xor_b32 v216, 0x400, %[mo]\n\t" "s_mov_b32 s60, 0\n\t" XT(80, 81) "s_getpc_b64 s[62:63]\n\t" "48:\n\t" "s_memtime s[64:65]\n\t" "s_waitcnt lgkmcnt(0)\n\t" \
    "s_sub_u32 s61, %[tg], s64\n\t" "s_cmp_gt_i32 s61, 124\n\t" "s_cbranch_scc1 48b\n\t" "s_max_i32 s61, s61, 0\n\t" "s_and_b32 s61, s61, 124\n\t" \
    "s_sub_u32 s61, 49f-48b, s61\n\t" "s_add_u32 s62, s62, s61\n\t" "s_addc_u32 s63, s63, 0\n\t" "s_setpc_b64 s[62:63]\n\t" ".rept 31\n\t" "s_nop 0\n\t" ".endr\n\t" "49:\n\t" \
    XT(98, 99) "s_barrier\n\t" XT(82, 83) "40:\n\t" "buffer_load_dwordx4 v[240:243], %[mo], %[rs], %[sb] offen offset:0 sc1\n\t" "v_mov_b64 v[224:225], 0\n\t" \
    "v_mov_b64 v[226:227], 0\n\t" "v_mov_b64 v[228:229], 0\n\t" "v_mov_b64 v[230:231], 0\n\t" "v_mov_b64 v[232:233], 0\n\t" "v_mov_b64 v[234:235], 0\n\t" \
    "v_mov_b64 v[236:237], 0\n\t" "v_mov_b64 v[238:239], 0\n\t" "buffer_load_dwordx4 v[244:247], v216, %[rs], %[sb] offen offset:0 sc1\n\t" \
    "ds_read_b128 v[160:163], %[l0] offset:0\n\t" "ds_read_b128 v[164:167], %[l0] offset:1024\n\t" "ds_read_b128 v[168:171], %[l0] offset:2048\n\t" \
    "ds_read_b128 v[172:175], %[l0] offset:3072\n\t" "s_nop 3\n\t" "buffer_load_dwordx4 v[248:251], %[mo], %[rs], %[sb] offen offset:2048 sc1\n\t" \
    "ds_read_b128 v[176:179], %[l0] offset:4096\n\t" "ds_read_b128 v[180:183], %[l0] offset:5120\n\t" "ds_read_b128 v[184:187], %[l0] offset:6144\n\t" \
    "ds_read_b128 v[188:191], %[l0] offset:7168\n\t" "s_nop 3\n\t" "buffer_load_dwordx4 v[252:255], v216, %[rs], %[sb] offen offset:2048 sc1\n\t" "s_waitcnt vmcnt(3)\n\t" \
    XT(84, 85) "v_smfmac_f32_16x16x64_bf16 v[224:227], v[240:243], a[0:7], %[ix]\n\t" "ds_write_b128 %[l1], v[240:243] offset:12288\n\t" \
    "v_smfmac_f32_16x16x64_bf16 v[228:231], v[240:243], a[8:15], %[ix]\n\t" "v_max3_u32 v217, v240, v241, v242\n\t" \
    "v_smfmac_f32_16x16x64_bf16 v[232:235], v[240:243], v[32:39], %[ix]\n\t" "s_waitcnt lgkmcnt(7)\n\t" \
    "v_smfmac_f32_16x16x64_bf16 v[236:239], v[240:243], v[160:167], %[ix]\n\t" "ds_read_b128 v[160:163], %[l0] offset:8192\n\t" \
    "ds_read_b128 v[164:167], %[l0] offset:9216\n\t" "s_waitcnt vmcnt(2)\n\t" "v_smfmac_f32_16x16x64_bf16 v[224:227], v[244:247], a[16:23], %[ix]\n\t" \
    "ds_write_b128 %[l1], v[244:247] offset:13312\n\t" "v_max3_u32 v217, v217, v243, v244\n\t" "v_smfmac_f32_16x16x64_bf16 v[228:231], v[244:247], a[24:31], %[ix]\n\t" \
    "v_max3_u32 v217, v217, v245, v246\n\t" "v_max_u32 v217, v217, v247\n\t" "v_smfmac_f32_16x16x64_bf16 v[232:235], v[244:247], v[40:47], %[ix]\n\t" \
    "v_cmp_eq_u32 vcc, -1, v217\n\t" "s_cmp_lg_u32 s60, 0\n\t" "s_waitcnt lgkmcnt(8)\n\t" "v_smfmac_f32_16x16x64_bf16 v[236:239], v[244:247], v[168:175], %[ix]\n\t" \
    "s_cbranch_vccnz 41f\n\t" "42:\n\t" "ds_read_b128 v[168:171], %[l0] offset:10240\n\t" "s_waitcnt vmcnt(1)\n\t" \
    "v_smfmac_f32_16x16x64_bf16 v[224:227], v[248:251], a[32:39], %[ix]\n\t" "ds_write_b128 %[l1], v[248:251] offset:14336\n\t" "s_waitcnt lgkmcnt(2)\n\t" \
    "v_smfmac_f32_16x16x64_bf16 v[228:231], v[248:251], a[40:47], %[ix]\n\t" "s_cbranch_scc1 43f\n\t" "s_barrier\n\t" "43:\n\t" XT(86, 87) \
    "v_smfmac_f32_16x16x64_bf16 v[232:235], v[248:251], v[48:55], %[ix]\n\t" "ds_read_b128 v[192:195], %[l1] offset:0\n\t" "ds_read_b128 v[196:199], %[l1] offset:4096\n\t" \
    "v_smfmac_f32_16x16x64_bf16 v[236:239], v[248:251], v[176:183], %[ix]\n\t" "ds_read_b128 v[172:175], %[l0] offset:11264\n\t" \
    "ds_read_b128 v[176:179], %[l0] offset:12288\n\t" "s_waitcnt vmcnt(0)\n\t" "v_smfmac_f32_16x16x64_bf16 v[224:227], v[252:255], a[48:55], %[ix]\n\t" \
    "ds_write_b128 %[l1], v[252:255] offset:15360\n\t" "v_max3_u32 v217, v248, v249, v250\n\t" "v_smfmac_f32_16x16x64_bf16 v[228:231], v[252:255], a[56:63], %[ix]\n\t" \
    "ds_read_b128 v[180:183], %[l0] offset:13312\n\t" "v_max3_u32 v217, v217, v251, v252\n\t" "v_max3_u32 v217, v217, v253, v254\n\t" \
    "v_smfmac_f32_16x16x64_bf16 v[232:235], v[252:255], v[56:63], %[ix]\n\t" "v_max_u32 v217, v217, v255\n\t" "v_cmp_eq_u32 vcc, -1, v217\n\t" \
    "v_smfmac_f32_16x16x64_bf16 v[236:239], v[252:255], v[184:191], %[ix]\n\t" "s_cbranch_vccnz 44f\n\t" "45:\n\t" "ds_read_b128 v[240:243], %[l1] offset:8192\n\t" \
    "s_waitcnt lgkmcnt(6)\n\t" "v_smfmac_f32_16x16x64_bf16 v[224:227], v[192:195], a[64:71], %[ix]\n\t" "ds_read_b128 v[184:187], %[l0] offset:14336\n\t" \
    "ds_read_b128 v[188:191], %[l0] offset:15360\n\t" "v_smfmac_f32_16x16x64_bf16 v[228:231], v[192:195], a[72:79], %[ix]\n\t" \
    "ds_read_b128 v[244:247], %[l1] offset:1024\n\t" "s_waitcnt lgkmcnt(5)\n\t" "v_smfmac_f32_16x16x64_bf16 v[232:235], v[192:195], v[64:71], %[ix]\n\t" "s_barrier\n\t" \
    XT(88, 89) "s_memtime s[64:65]\n\t" "v_smfmac_f32_16x16x64_bf16 v[236:239], v[192:195], v[160:167], %[ix]\n\t" "ds_read_b128 v[160:163], %[l0] offset:16384\n\t" \
    "ds_read_b128 v[164:167], %[l0] offset:17408\n\t" "v_smfmac_f32_16x16x64_bf16 v[224:227], v[196:199], a[80:87], %[ix]\n\t" \
    "ds_read_b128 v[248:251], %[l1] offset:5120\n\t" "ds_read_b128 v[252:255], %[l1] offset:9216\n\t" "v_smfmac_f32_16x16x64_bf16 v[228:231], v[196:199], a[88:95], %[ix]\n\t" \
    "ds_read_b128 v[192:195], %[l1] offset:2048\n\t" "v_smfmac_f32_16x16x64_bf16 v[232:235], v[196:199], v[72:79], %[ix]\n\t" "s_sub_u32 s62, s64, %[ca]\n\t" \
    "s_mul_i32 s62, s62, %[k1]\n\t" "v_smfmac_f32_16x16x64_bf16 v[236:239], v[196:199], v[168:175], %[ix]\n\t" "s_lshr_b32 s62, s62, 16\n\t" "s_add_u32 s62, s62, %[ga]\n\t" \
    "s_waitcnt lgkmcnt(8)\n\t" "v_smfmac_f32_16x16x64_bf16 v[224:227], v[240:243], a[96:103], %[ix]\n\t" "s_bitset0_b32 s62, 0\n\t" "v_mov_b32 v218, s62\n\t" \
    "v_smfmac_f32_16x16x64_bf16 v[228:231], v[240:243], a[104:111], %[ix]\n\t" "buffer_store_dword v218, %[hq], %[rs], %[pb] offen sc1\n\t" \
    "ds_read_b128 v[168:171], %[l0] offset:18432\n\t" "v_smfmac_f32_16x16x64_bf16 v[232:235], v[240:243], v[80:87], %[ix]\n\t" \
    "ds_read_b128 v[172:175], %[l0] offset:19456\n\t" "ds_read_b128 v[196:199], %[l1] offset:6144\n\t" \
    "v_smfmac_f32_16x16x64_bf16 v[236:239], v[240:243], v[176:183], %[ix]\n\t" "ds_read_b128 v[176:179], %[l0] offset:20480\n\t" \
    "ds_read_b128 v[180:183], %[l0] offset:21504\n\t" "s_waitcnt lgkmcnt(10)\n\t" "v_smfmac_f32_16x16x64_bf16 v[224:227], v[244:247], a[112:119], %[ix]\n\t" \
    "ds_read_b128 v[240:243], %[l1] offset:10240\n\t" "v_smfmac_f32_16x16x64_bf16 v[228:231], v[244:247], a[120:127], %[ix]\n\t" \
    "v_smfmac_f32_16x16x64_bf16 v[232:235], v[244:247], v[88:95], %[ix]\n\t" "v_smfmac_f32_16x16x64_bf16 v[236:239], v[244:247], v[184:191], %[ix]\n\t" \
    "ds_read_b128 v[184:187], %[l0] offset:22528\n\t" "ds_read_b128 v[188:191], %[l0] offset:23552\n\t" "s_waitcnt lgkmcnt(10)\n\t" \
    "v_smfmac_f32_16x16x64_bf16 v[224:227], v[248:251], a[128:135], %[ix]\n\t" "ds_read_b128 v[244:247], %[l1] offset:3072\n\t" \
    "v_smfmac_f32_16x16x64_bf16 v[228:231], v[248:251], a[136:143], %[ix]\n\t" "v_smfmac_f32_16x16x64_bf16 v[232:235], v[248:251], v[96:103], %[ix]\n\t" \
    "v_smfmac_f32_16x16x64_bf16 v[236:239], v[248:251], v[160:167], %[ix]\n\t" "ds_read_b128 v[160:163], %[l0] offset:24576\n\t" \
    "ds_read_b128 v[164:167], %[l0] offset:25600\n\t" "s_waitcnt lgkmcnt(12)\n\t" "v_smfmac_f32_16x16x64_bf16 v[224:227], v[252:255], a[144:151], %[ix]\n\t" \
    "ds_read_b128 v[248:251], %[l1] offset:7168\n\t" "v_smfmac_f32_16x16x64_bf16 v[228:231], v[252:255], a[152:159], %[ix]\n\t" \
    "v_smfmac_f32_16x16x64_bf16 v[232:235], v[252:255], v[104:111], %[ix]\n\t" "s_waitcnt lgkmcnt(10)\n\t" \
    "v_smfmac_f32_16x16x64_bf16 v[236:239], v[252:255], v[168:175], %[ix]\n\t" "ds_read_b128 v[168:171], %[l0] offset:26624\n\t" \
    "ds_read_b128 v[172:175], %[l0] offset:27648\n\t" "v_smfmac_f32_16x16x64_bf16 v[224:227], v[192:195], a[160:167], %[ix]\n\t" \
    "ds_read_b128 v[252:255], %[l1] offset:11264\n\t" "v_smfmac_f32_16x16x64_bf16 v[228:231], v[192:195], a[168:175], %[ix]\n\t" "s_waitcnt lgkmcnt(12)\n\t" \
    "v_smfmac_f32_16x16x64_bf16 v[224:227], v[196:199], a[176:183], %[ix]\n\t" "v_smfmac_f32_16x16x64_bf16 v[228:231], v[196:199], a[184:191], %[ix]\n\t" \
    "s_waitcnt lgkmcnt(9)\n\t" "v_smfmac_f32_16x16x64_bf16 v[224:227], v[240:243], a[192:199], %[ix]\n\t" \
    "v_smfmac_f32_16x16x64_bf16 v[228:231], v[240:243], a[200:207], %[ix]\n\t" "s_waitcnt lgkmcnt(6)\n\t" \
    "v_smfmac_f32_16x16x64_bf16 v[224:227], v[244:247], a[208:215], %[ix]\n\t" "v_smfmac_f32_16x16x64_bf16 v[228:231], v[244:247], a[216:223], %[ix]\n\t" \
    "s_waitcnt lgkmcnt(3)\n\t" "v_smfmac_f32_16x16x64_bf16 v[224:227], v[248:251], a[224:231], %[ix]\n\t" \
    "v_smfmac_f32_16x16x64_bf16 v[228:231], v[248:251], a[232:239], %[ix]\n\t" "s_waitcnt lgkmcnt(0)\n\t" \
    "v_smfmac_f32_16x16x64_bf16 v[224:227], v[252:255], a[240:247], %[ix]\n\t" "s_nop 1\n\t" "v_smfmac_f32_16x16x64_bf16 v[228:231], v[252:255], a[248:255], %[ix]\n\t" \
    XT(90, 91) "s_nop 1\n\t" "v_smfmac_f32_16x16x64_bf16 v[232:235], v[192:195], v[112:119], %[ix]\n\t" "s_nop 1\n\t" \
    "v_smfmac_f32_16x16x64_bf16 v[236:239], v[192:195], v[176:183], %[ix]\n\t" "ds_read_b128 v[176:179], %[l0] offset:28672\n\t" "v_add_f32 v224, v224, v225\n\t" \
    "v_add_f32 v226, v226, v227\n\t" "v_smfmac_f32_16x16x64_bf16 v[232:235], v[196:199], v[120:127], %[ix]\n\t" "ds_read_b128 v[180:183], %[l0] offset:29696\n\t" \
    "v_add_f32 v228, v228, v229\n\t" "v_add_f32 v230, v230, v231\n\t" "v_smfmac_f32_16x16x64_bf16 v[236:239], v[196:199], v[184:191], %[ix]\n\t" \
    "ds_read_b128 v[184:187], %[l0] offset:30720\n\t" "v_cvt_pk_bf16_f32 v218, v224, v228\n\t" "v_cvt_pk_bf16_f32 v219, v226, v230\n\t" \
    "v_smfmac_f32_16x16x64_bf16 v[232:235], v[240:243], v[128:135], %[ix]\n\t" "ds_write2_b32 %[qw], v218, v219 offset1:8\n\t" \
    "ds_read_b128 v[188:191], %[l0] offset:31744\n\t" "v_smfmac_f32_16x16x64_bf16 v[236:239], v[240:243], v[160:167], %[ix]\n\t" \
    "v_smfmac_f32_16x16x64_bf16 v[232:235], v[244:247], v[136:143], %[ix]\n\t" "v_smfmac_f32_16x16x64_bf16 v[236:239], v[244:247], v[168:175], %[ix]\n\t" \
    "s_waitcnt lgkmcnt(1)\n\t" "s_barrier\n\t" "v_smfmac_f32_16x16x64_bf16 v[232:235], v[248:251], v[144:151], %[ix]\n\t" \
    "v_smfmac_f32_16x16x64_bf16 v[236:239], v[248:251], v[176:183], %[ix]\n\t" "v_smfmac_f32_16x16x64_bf16 v[232:235], v[252:255], v[152:159], %[ix]\n\t" \
    "s_waitcnt lgkmcnt(0)\n\t" "v_smfmac_f32_16x16x64_bf16 v[236:239], v[252:255], v[184:191], %[ix]\n\t" XT(92, 93) "s_nop 6\n\t" "v_add_f32 v232, v232, v233\n\t" \
    "v_add_f32 v234, v234, v235\n\t" "v_add_f32 v236, v236, v237\n\t" "v_add_f32 v238, v238, v239\n\t" "v_cvt_pk_bf16_f32 v232, v232, v236\n\t" \
    "v_cvt_pk_bf16_f32 v233, v234, v238\n\t" "ds_write2_b32 %[kw], v232, v233 offset1:68\n\t" "s_waitcnt lgkmcnt(0)\n\t" "s_barrier\n\t" XT(94, 95) XT(96, 97) \
    "s_branch 99f\n\t" "41:\n\t" "s_cmp_eq_u32 %[bd], 0\n\t" "s_cbranch_scc1 46f\n\t" "s_sub_u32 %[bd], %[bd], 1\n\t" XCNT(100) "s_waitcnt lgkmcnt(0)\n\t" "s_nop 7\n\t" \
    "s_branch 40b\n\t" "46:\n\t" "s_cmp_lg_u32 s60, 0\n\t" "s_branch 42b\n\t" "44:\n\t" "s_cmp_eq_u32 %[bd], 0\n\t" "s_cbranch_scc1 45b\n\t" "s_sub_u32 %[bd], %[bd], 1\n\t" \
    XCNT(101) "s_mov_b32 s60, 1\n\t" "s_waitcnt lgkmcnt(0)\n\t" "s_nop 7\n\t" "s_branch 40b\n\t" "99:\n\t"
#define VO_STEP \
    "v_xor_b32 v224, 0x400, %[mo]\n\t" "s_mov_b32 s60, 0\n\t" "s_cmp_eq_u32 %[w], 1\n\t" "s_cbranch_scc1 91f\n\t" "s_cmp_eq_u32 %[w], 2\n\t" "s_cbranch_scc1 92f\n\t" \
    "s_cmp_eq_u32 %[w], 3\n\t" "s_cbranch_scc1 93f\n\t" XT(80, 81) "s_getpc_b64 s[62:63]\n\t" "18:\n\t" "s_memtime s[64:65]\n\t" "s_waitcnt lgkmcnt(0)\n\t" \
    "s_sub_u32 s61, %[tg], s64\n\t" "s_cmp_gt_i32 s61, 124\n\t" "s_cbranch_scc1 18b\n\t" "s_max_i32 s61, s61, 0\n\t" "s_and_b32 s61, s61, 124\n\t" \
    "s_sub_u32 s61, 19f-18b, s61\n\t" "s_add_u32 s62, s62, s61\n\t" "s_addc_u32 s63, s63, 0\n\t" "s_setpc_b64 s[62:63]\n\t" ".rept 31\n\t" "s_nop 0\n\t" ".endr\n\t" "19:\n\t" \
    "s_barrier\n\t" XT(82, 83) "10:\n\t" "buffer_load_dwordx4 v[240:243], %[mo], %[rs], %[sb] offen offset:0 sc1\n\t" "v_mov_b64 v[232:233], 0\n\t" \
    "v_mov_b64 v[234:235], 0\n\t" "v_mov_b64 v[236:237], 0\n\t" "v_mov_b64 v[238:239], 0\n\t" "v_mov_b64 v[192:193], 0\n\t" "v_mov_b64 v[194:195], 0\n\t" \
    "v_mov_b64 v[196:197], 0\n\t" "v_mov_b64 v[198:199], 0\n\t" "buffer_load_dwordx4 v[244:247], v224, %[rs], %[sb] offen offset:0 sc1\n\t" \
    "ds_read_b128 v[160:163], %[l0] offset:0\n\t" "ds_read_b128 v[164:167], %[l0] offset:1024\n\t" "ds_read_b128 v[168:171], %[l0] offset:2048\n\t" \
    "ds_read_b128 v[172:175], %[l0] offset:3072\n\t" "s_nop 3\n\t" "buffer_load_dwordx4 v[248:251], %[mo], %[rs], %[sb] offen offset:2048 sc1\n\t" \
    "ds_read_b128 v[176:179], %[l0] offset:4096\n\t" "ds_read_b128 v[180:183], %[l0] offset:5120\n\t" "ds_read_b128 v[184:187], %[l0] offset:6144\n\t" \
    "ds_read_b128 v[188:191], %[l0] offset:7168\n\t" "s_nop 3\n\t" "buffer_load_dwordx4 v[252:255], v224, %[rs], %[sb] offen offset:2048 sc1\n\t" \
    "v_mov_b64 v[200:201], 0\n\t" "v_mov_b64 v[202:203], 0\n\t" "v_mov_b64 v[204:205], 0\n\t" "v_mov_b64 v[206:207], 0\n\t" "v_mov_b64 v[208:209], 0\n\t" \
    "v_mov_b64 v[210:211], 0\n\t" "v_mov_b64 v[212:213], 0\n\t" "v_mov_b64 v[214:215], 0\n\t" "v_mov_b64 v[216:217], 0\n\t" "v_mov_b64 v[218:219], 0\n\t" \
    "v_mov_b64 v[220:221], 0\n\t" "v_mov_b64 v[222:223], 0\n\t" "s_waitcnt vmcnt(3)\n\t" XT(84, 85) "v_smfmac_f32_16x16x64_bf16 v[232:235], v[240:243], a[0:7], %[ix]\n\t" \
    "ds_write_b128 %[l1], v[240:243] offset:0\n\t" "v_smfmac_f32_16x16x64_bf16 v[236:239], v[240:243], a[8:15], %[ix]\n\t" "v_max3_u32 v225, v240, v241, v242\n\t" \
    "s_waitcnt vmcnt(2)\n\t" "v_smfmac_f32_16x16x64_bf16 v[232:235], v[244:247], a[16:23], %[ix]\n\t" "ds_write_b128 %[l1], v[244:247] offset:1024\n\t" \
    "v_max3_u32 v225, v225, v243, v244\n\t" "s_waitcnt lgkmcnt(8)\n\t" "v_smfmac_f32_16x16x64_bf16 v[236:239], v[244:247], v[160:167], %[ix]\n\t" \
    "v_max3_u32 v225, v225, v245, v246\n\t" "v_max_u32 v225, v225, v247\n\t" "v_cmp_eq_u32 vcc, -1, v225\n\t" "s_cmp_lg_u32 s60, 0\n\t" "s_cbranch_vccnz 11f\n\t" "12:\n\t" \
    "s_waitcnt lgkmcnt(0)\n\t" "s_cbranch_scc1 13f\n\t" "s_barrier\n\t" "13:\n\t" XT(86, 87) "ds_read_b128 v[16:19], %[l1] offset:4096\n\t" \
    "ds_read_b128 v[20:23], %[l1] offset:8192\n\t" "ds_read_b128 v[24:27], %[l1] offset:12288\n\t" "s_waitcnt vmcnt(1)\n\t" \
    "v_smfmac_f32_16x16x64_bf16 v[232:235], v[248:251], a[24:31], %[ix]\n\t" "ds_write_b128 %[l1], v[248:251] offset:2048\n\t" "v_max3_u32 v225, v248, v249, v250\n\t" \
    "v_smfmac_f32_16x16x64_bf16 v[236:239], v[248:251], a[32:39], %[ix]\n\t" "ds_read_b128 v[28:31], %[l1] offset:5120\n\t" "ds_read_b128 v[240:243], %[l1] offset:9216\n\t" \
    "ds_read_b128 v[244:247], %[l1] offset:13312\n\t" "s_waitcnt vmcnt(0)\n\t" "v_smfmac_f32_16x16x64_bf16 v[232:235], v[252:255], a[40:47], %[ix]\n\t" \
    "ds_write_b128 %[l1], v[252:255] offset:3072\n\t" "v_max3_u32 v225, v225, v251, v252\n\t" "v_smfmac_f32_16x16x64_bf16 v[236:239], v[252:255], v[168:175], %[ix]\n\t" \
    "v_max3_u32 v225, v225, v253, v254\n\t" "v_max_u32 v225, v225, v255\n\t" "s_waitcnt lgkmcnt(7)\n\t" "v_smfmac_f32_16x16x64_bf16 v[232:235], v[16:19], a[48:55], %[ix]\n\t" \
    "v_cmp_eq_u32 vcc, -1, v225\n\t" "s_cbranch_vccnz 14f\n\t" "15:\n\t" "v_smfmac_f32_16x16x64_bf16 v[236:239], v[16:19], a[56:63], %[ix]\n\t" \
    "ds_read_b128 v[160:163], %[l0] offset:8192\n\t" "ds_read_b128 v[164:167], %[l0] offset:9216\n\t" "s_waitcnt lgkmcnt(8)\n\t" \
    "v_smfmac_f32_16x16x64_bf16 v[232:235], v[20:23], a[64:71], %[ix]\n\t" "ds_read_b128 v[168:171], %[l0] offset:10240\n\t" "ds_read_b128 v[172:175], %[l0] offset:11264\n\t" \
    "v_smfmac_f32_16x16x64_bf16 v[236:239], v[20:23], v[176:183], %[ix]\n\t" "s_waitcnt lgkmcnt(4)\n\t" "s_barrier\n\t" XT(88, 89) "s_memtime s[64:65]\n\t" \
    "v_smfmac_f32_16x16x64_bf16 v[232:235], v[24:27], a[72:79], %[ix]\n\t" "ds_read_b128 v[176:179], %[l0] offset:12288\n\t" "ds_read_b128 v[180:183], %[l0] offset:13312\n\t" \
    "v_smfmac_f32_16x16x64_bf16 v[236:239], v[24:27], a[80:87], %[ix]\n\t" "ds_read_b128 v[248:251], %[l1] offset:6144\n\t" "ds_read_b128 v[252:255], %[l1] offset:10240\n\t" \
    "v_smfmac_f32_16x16x64_bf16 v[232:235], v[28:31], a[88:95], %[ix]\n\t" "ds_read_b128 v[16:19], %[l1] offset:14336\n\t" "ds_read_b128 v[20:23], %[l1] offset:7168\n\t" \
    "v_smfmac_f32_16x16x64_bf16 v[236:239], v[28:31], v[184:191], %[ix]\n\t" "ds_read_b128 v[24:27], %[l1] offset:11264\n\t" "ds_read_b128 v[184:187], %[l0] offset:14336\n\t" \
    "v_smfmac_f32_16x16x64_bf16 v[232:235], v[240:243], a[96:103], %[ix]\n\t" "ds_read_b128 v[188:191], %[l0] offset:15360\n\t" \
    "ds_read_b128 v[28:31], %[l1] offset:15360\n\t" "v_smfmac_f32_16x16x64_bf16 v[236:239], v[240:243], a[104:111], %[ix]\n\t" "s_sub_u32 s62, s64, %[ca]\n\t" \
    "s_mul_i32 s62, s62, %[k1]\n\t" "v_smfmac_f32_16x16x64_bf16 v[232:235], v[244:247], a[112:119], %[ix]\n\t" "s_lshr_b32 s62, s62, 16\n\t" "s_add_u32 s62, s62, %[ga]\n\t" \
    "s_waitcnt lgkmcnt(12)\n\t" "v_smfmac_f32_16x16x64_bf16 v[236:239], v[244:247], v[160:167], %[ix]\n\t" "s_bitset0_b32 s62, 0\n\t" "v_mov_b32 v226, s62\n\t" \
    "s_waitcnt lgkmcnt(7)\n\t" "v_smfmac_f32_16x16x64_bf16 v[232:235], v[248:251], a[120:127], %[ix]\n\t" "buffer_store_dword v226, %[hq], %[rs], %[pb] offen sc1\n\t" \
    "ds_read_b128 v[160:163], %[l0] offset:16384\n\t" "v_smfmac_f32_16x16x64_bf16 v[236:239], v[248:251], a[128:135], %[ix]\n\t" \
    "ds_read_b128 v[164:167], %[l0] offset:17408\n\t" "s_waitcnt lgkmcnt(8)\n\t" "v_smfmac_f32_16x16x64_bf16 v[232:235], v[252:255], a[136:143], %[ix]\n\t" \
    "v_smfmac_f32_16x16x64_bf16 v[236:239], v[252:255], v[168:175], %[ix]\n\t" "ds_read_b128 v[168:171], %[l0] offset:18432\n\t" \
    "ds_read_b128 v[172:175], %[l0] offset:19456\n\t" "s_waitcnt lgkmcnt(9)\n\t" "v_smfmac_f32_16x16x64_bf16 v[232:235], v[16:19], a[144:151], %[ix]\n\t" \
    "v_smfmac_f32_16x16x64_bf16 v[236:239], v[16:19], a[152:159], %[ix]\n\t" "s_waitcnt lgkmcnt(8)\n\t" \
    "v_smfmac_f32_16x16x64_bf16 v[232:235], v[20:23], a[160:167], %[ix]\n\t" "v_smfmac_f32_16x16x64_bf16 v[236:239], v[20:23], v[176:183], %[ix]\n\t" \
    "ds_read_b128 v[176:179], %[l0] offset:20480\n\t" "ds_read_b128 v[180:183], %[l0] offset:21504\n\t" "s_waitcnt lgkmcnt(9)\n\t" \
    "v_smfmac_f32_16x16x64_bf16 v[232:235], v[24:27], a[168:175], %[ix]\n\t" "v_smfmac_f32_16x16x64_bf16 v[236:239], v[24:27], a[176:183], %[ix]\n\t" \
    "s_waitcnt lgkmcnt(6)\n\t" "v_smfmac_f32_16x16x64_bf16 v[232:235], v[28:31], a[184:191], %[ix]\n\t" \
    "v_smfmac_f32_16x16x64_bf16 v[236:239], v[28:31], v[184:191], %[ix]\n\t" "ds_read_b128 v[184:187], %[l0] offset:22528\n\t" \
    "ds_read_b128 v[188:191], %[l0] offset:23552\n\t" XT(90, 91) "s_nop 4\n\t" "v_add_f32 v232, v232, v233\n\t" "v_add_f32 v234, v234, v235\n\t" \
    "v_add_f32 v236, v236, v237\n\t" "v_add_f32 v238, v238, v239\n\t" "v_cvt_pk_bf16_f32 v232, v232, v236\n\t" "v_cvt_pk_bf16_f32 v233, v234, v238\n\t" \
    "ds_write2_b32 %[vw], v232, v233 offset1:68\n\t" "s_waitcnt lgkmcnt(0)\n\t" "s_barrier\n\t" XT(92, 93) "ds_read_b128 v[240:243], %[vr] offset:0\n\t" \
    "ds_read_b128 v[244:247], %[vr] offset:64\n\t" "ds_read_b128 v[248:251], %[vr] offset:128\n\t" "ds_read_b128 v[252:255], %[vr] offset:192\n\t" "s_mov_b32 s61, %[ub]\n\t" \
    "s_waitcnt lgkmcnt(3)\n\t" "v_smfmac_f32_16x16x64_bf16 v[192:195], v[240:243], v[32:39], %[ix]\n\t" \
    "v_smfmac_f32_16x16x64_bf16 v[196:199], v[240:243], v[48:55], %[ix]\n\t" "s_waitcnt lgkmcnt(2)\n\t" \
    "v_smfmac_f32_16x16x64_bf16 v[192:195], v[244:247], v[40:47], %[ix]\n\t" "v_smfmac_f32_16x16x64_bf16 v[196:199], v[244:247], v[56:63], %[ix]\n\t" \
    "s_waitcnt lgkmcnt(1)\n\t" "v_smfmac_f32_16x16x64_bf16 v[192:195], v[248:251], a[192:199], %[ix]\n\t" \
    "v_smfmac_f32_16x16x64_bf16 v[196:199], v[248:251], a[200:207], %[ix]\n\t" "s_waitcnt lgkmcnt(0)\n\t" \
    "v_smfmac_f32_16x16x64_bf16 v[192:195], v[252:255], v[160:167], %[ix]\n\t" "ds_read_b128 v[160:163], %[l0] offset:24576\n\t" \
    "ds_read_b128 v[164:167], %[l0] offset:25600\n\t" "v_smfmac_f32_16x16x64_bf16 v[196:199], v[252:255], v[168:175], %[ix]\n\t" \
    "ds_read_b128 v[168:171], %[l0] offset:26624\n\t" "ds_read_b128 v[172:175], %[l0] offset:27648\n\t" \
    "v_smfmac_f32_16x16x64_bf16 v[200:203], v[240:243], v[64:71], %[ix]\n\t" "s_nop 1\n\t" "v_smfmac_f32_16x16x64_bf16 v[204:207], v[240:243], v[80:87], %[ix]\n\t" \
    "v_cvt_pk_bf16_f32 v224, v192, v194\n\t" "v_cvt_pk_bf16_f32 v225, v193, v195\n\t" "v_smfmac_f32_16x16x64_bf16 v[200:203], v[244:247], v[72:79], %[ix]\n\t" \
    "v_cvt_pk_bf16_f32 v226, v196, v198\n\t" "v_cvt_pk_bf16_f32 v227, v197, v199\n\t" "v_smfmac_f32_16x16x64_bf16 v[204:207], v[244:247], v[88:95], %[ix]\n\t" "s_nop 0\n\t" \
    "v_permlane32_swap_b32 v224, v226\n\t" "v_smfmac_f32_16x16x64_bf16 v[200:203], v[248:251], a[208:215], %[ix]\n\t" "v_permlane32_swap_b32 v225, v227\n\t" "s_nop 0\n\t" \
    "v_smfmac_f32_16x16x64_bf16 v[204:207], v[248:251], a[216:223], %[ix]\n\t" "v_permlane16_swap_b32 v224, v225\n\t" "v_permlane16_swap_b32 v226, v227\n\t" \
    "v_smfmac_f32_16x16x64_bf16 v[200:203], v[252:255], v[176:183], %[ix]\n\t" "buffer_store_dwordx4 v[224:227], %[uo], %[rs], s61 offen sc1\n\t" \
    "s_add_u32 s61, s61, 0x10000\n\t" XT(94, 95) "v_smfmac_f32_16x16x64_bf16 v[204:207], v[252:255], v[184:191], %[ix]\n\t" "ds_read_b128 v[176:179], %[l0] offset:28672\n\t" \
    "ds_read_b128 v[180:183], %[l0] offset:29696\n\t" "v_smfmac_f32_16x16x64_bf16 v[208:211], v[240:243], v[96:103], %[ix]\n\t" \
    "ds_read_b128 v[184:187], %[l0] offset:30720\n\t" "ds_read_b128 v[188:191], %[l0] offset:31744\n\t" \
    "v_smfmac_f32_16x16x64_bf16 v[212:215], v[240:243], v[112:119], %[ix]\n\t" "v_cvt_pk_bf16_f32 v228, v200, v202\n\t" "v_cvt_pk_bf16_f32 v229, v201, v203\n\t" \
    "v_smfmac_f32_16x16x64_bf16 v[208:211], v[244:247], v[104:111], %[ix]\n\t" "v_cvt_pk_bf16_f32 v230, v204, v206\n\t" "v_cvt_pk_bf16_f32 v231, v205, v207\n\t" \
    "v_smfmac_f32_16x16x64_bf16 v[212:215], v[244:247], v[120:127], %[ix]\n\t" "s_nop 0\n\t" "v_permlane32_swap_b32 v228, v230\n\t" \
    "v_smfmac_f32_16x16x64_bf16 v[208:211], v[248:251], a[224:231], %[ix]\n\t" "v_permlane32_swap_b32 v229, v231\n\t" "s_nop 0\n\t" \
    "v_smfmac_f32_16x16x64_bf16 v[212:215], v[248:251], a[232:239], %[ix]\n\t" "v_permlane16_swap_b32 v228, v229\n\t" "v_permlane16_swap_b32 v230, v231\n\t" \
    "s_waitcnt lgkmcnt(6)\n\t" "v_smfmac_f32_16x16x64_bf16 v[208:211], v[252:255], v[160:167], %[ix]\n\t" "buffer_store_dwordx4 v[228:231], %[uo], %[rs], s61 offen sc1\n\t" \
    "s_add_u32 s61, s61, 0x10000\n\t" "s_waitcnt lgkmcnt(4)\n\t" "v_smfmac_f32_16x16x64_bf16 v[212:215], v[252:255], v[168:175], %[ix]\n\t" "s_nop 1\n\t" \
    "v_smfmac_f32_16x16x64_bf16 v[216:219], v[240:243], v[128:135], %[ix]\n\t" "s_nop 1\n\t" "v_smfmac_f32_16x16x64_bf16 v[220:223], v[240:243], v[144:151], %[ix]\n\t" \
    "v_cvt_pk_bf16_f32 v224, v208, v210\n\t" "v_cvt_pk_bf16_f32 v225, v209, v211\n\t" "v_smfmac_f32_16x16x64_bf16 v[216:219], v[244:247], v[136:143], %[ix]\n\t" \
    "v_cvt_pk_bf16_f32 v226, v212, v214\n\t" "v_cvt_pk_bf16_f32 v227, v213, v215\n\t" "v_smfmac_f32_16x16x64_bf16 v[220:223], v[244:247], v[152:159], %[ix]\n\t" "s_nop 0\n\t" \
    "v_permlane32_swap_b32 v224, v226\n\t" "v_smfmac_f32_16x16x64_bf16 v[216:219], v[248:251], a[240:247], %[ix]\n\t" "v_permlane32_swap_b32 v225, v227\n\t" "s_nop 0\n\t" \
    "v_smfmac_f32_16x16x64_bf16 v[220:223], v[248:251], a[248:255], %[ix]\n\t" "v_permlane16_swap_b32 v224, v225\n\t" "v_permlane16_swap_b32 v226, v227\n\t" \
    "s_waitcnt lgkmcnt(2)\n\t" "v_smfmac_f32_16x16x64_bf16 v[216:219], v[252:255], v[176:183], %[ix]\n\t" "buffer_store_dwordx4 v[224:227], %[uo], %[rs], s61 offen sc1\n\t" \
    "s_add_u32 s61, s61, 0x10000\n\t" "s_waitcnt lgkmcnt(0)\n\t" "v_smfmac_f32_16x16x64_bf16 v[220:223], v[252:255], v[184:191], %[ix]\n\t" "s_nop 5\n\t" \
    "v_cvt_pk_bf16_f32 v228, v216, v218\n\t" "v_cvt_pk_bf16_f32 v229, v217, v219\n\t" "v_cvt_pk_bf16_f32 v230, v220, v222\n\t" "v_cvt_pk_bf16_f32 v231, v221, v223\n\t" \
    "s_nop 0\n\t" "v_permlane32_swap_b32 v228, v230\n\t" "v_permlane32_swap_b32 v229, v231\n\t" "s_nop 1\n\t" "v_permlane16_swap_b32 v228, v229\n\t" \
    "v_permlane16_swap_b32 v230, v231\n\t" "buffer_store_dwordx4 v[228:231], %[uo], %[rs], s61 offen sc1\n\t" "s_nop 1\n\t" XT(96, 97) "s_branch 99f\n\t" "11:\n\t" \
    "s_cmp_eq_u32 %[bd], 0\n\t" "s_cbranch_scc1 16f\n\t" "s_sub_u32 %[bd], %[bd], 1\n\t" XCNT(100) "s_waitcnt lgkmcnt(0)\n\t" "s_nop 7\n\t" "s_branch 10b\n\t" "16:\n\t" \
    "s_cmp_lg_u32 s60, 0\n\t" "s_branch 12b\n\t" "14:\n\t" "s_cmp_eq_u32 %[bd], 0\n\t" "s_cbranch_scc1 15b\n\t" "s_sub_u32 %[bd], %[bd], 1\n\t" XCNT(101) \
    "s_mov_b32 s60, 1\n\t" "s_waitcnt lgkmcnt(0)\n\t" "s_nop 7\n\t" "s_branch 10b\n\t" "91:\n\t" XT(80, 81) "s_barrier\n\t" XT(82, 83) "20:\n\t" \
    "buffer_load_dwordx4 v[240:243], %[mo], %[rs], %[sb] offen offset:0 sc1\n\t" "v_mov_b64 v[232:233], 0\n\t" "v_mov_b64 v[234:235], 0\n\t" "v_mov_b64 v[236:237], 0\n\t" \
    "v_mov_b64 v[238:239], 0\n\t" "v_mov_b64 v[192:193], 0\n\t" "v_mov_b64 v[194:195], 0\n\t" "v_mov_b64 v[196:197], 0\n\t" "v_mov_b64 v[198:199], 0\n\t" \
    "buffer_load_dwordx4 v[244:247], v224, %[rs], %[sb] offen offset:0 sc1\n\t" "ds_read_b128 v[160:163], %[l0] offset:0\n\t" "ds_read_b128 v[164:167], %[l0] offset:1024\n\t" \
    "ds_read_b128 v[168:171], %[l0] offset:2048\n\t" "ds_read_b128 v[172:175], %[l0] offset:3072\n\t" "s_nop 3\n\t" \
    "buffer_load_dwordx4 v[248:251], %[mo], %[rs], %[sb] offen offset:2048 sc1\n\t" "ds_read_b128 v[176:179], %[l0] offset:4096\n\t" \
    "ds_read_b128 v[180:183], %[l0] offset:5120\n\t" "ds_read_b128 v[184:187], %[l0] offset:6144\n\t" "ds_read_b128 v[188:191], %[l0] offset:7168\n\t" "s_nop 3\n\t" \
    "buffer_load_dwordx4 v[252:255], v224, %[rs], %[sb] offen offset:2048 sc1\n\t" "v_mov_b64 v[200:201], 0\n\t" "v_mov_b64 v[202:203], 0\n\t" "v_mov_b64 v[204:205], 0\n\t" \
    "v_mov_b64 v[206:207], 0\n\t" "v_mov_b64 v[208:209], 0\n\t" "v_mov_b64 v[210:211], 0\n\t" "v_mov_b64 v[212:213], 0\n\t" "v_mov_b64 v[214:215], 0\n\t" \
    "v_mov_b64 v[216:217], 0\n\t" "v_mov_b64 v[218:219], 0\n\t" "v_mov_b64 v[220:221], 0\n\t" "v_mov_b64 v[222:223], 0\n\t" "s_waitcnt vmcnt(3)\n\t" XT(84, 85) \
    "v_smfmac_f32_16x16x64_bf16 v[232:235], v[240:243], a[0:7], %[ix]\n\t" "ds_write_b128 %[l1], v[240:243] offset:4096\n\t" \
    "v_smfmac_f32_16x16x64_bf16 v[236:239], v[240:243], a[8:15], %[ix]\n\t" "v_max3_u32 v225, v240, v241, v242\n\t" "s_waitcnt vmcnt(2)\n\t" \
    "v_smfmac_f32_16x16x64_bf16 v[232:235], v[244:247], a[16:23], %[ix]\n\t" "ds_write_b128 %[l1], v[244:247] offset:5120\n\t" "v_max3_u32 v225, v225, v243, v244\n\t" \
    "s_waitcnt lgkmcnt(8)\n\t" "v_smfmac_f32_16x16x64_bf16 v[236:239], v[244:247], v[160:167], %[ix]\n\t" "v_max3_u32 v225, v225, v245, v246\n\t" \
    "v_max_u32 v225, v225, v247\n\t" "v_cmp_eq_u32 vcc, -1, v225\n\t" "s_cmp_lg_u32 s60, 0\n\t" "s_cbranch_vccnz 21f\n\t" "22:\n\t" "s_waitcnt lgkmcnt(0)\n\t" \
    "s_cbranch_scc1 23f\n\t" "s_barrier\n\t" "23:\n\t" XT(86, 87) "ds_read_b128 v[16:19], %[l1] offset:8192\n\t" "ds_read_b128 v[20:23], %[l1] offset:12288\n\t" \
    "ds_read_b128 v[24:27], %[l1] offset:0\n\t" "s_waitcnt vmcnt(1)\n\t" "v_smfmac_f32_16x16x64_bf16 v[232:235], v[248:251], a[24:31], %[ix]\n\t" \
    "ds_write_b128 %[l1], v[248:251] offset:6144\n\t" "v_max3_u32 v225, v248, v249, v250\n\t" "v_smfmac_f32_16x16x64_bf16 v[236:239], v[248:251], a[32:39], %[ix]\n\t" \
    "ds_read_b128 v[28:31], %[l1] offset:9216\n\t" "ds_read_b128 v[240:243], %[l1] offset:13312\n\t" "ds_read_b128 v[244:247], %[l1] offset:1024\n\t" "s_waitcnt vmcnt(0)\n\t" \
    "v_smfmac_f32_16x16x64_bf16 v[232:235], v[252:255], a[40:47], %[ix]\n\t" "ds_write_b128 %[l1], v[252:255] offset:7168\n\t" "v_max3_u32 v225, v225, v251, v252\n\t" \
    "v_smfmac_f32_16x16x64_bf16 v[236:239], v[252:255], v[168:175], %[ix]\n\t" "v_max3_u32 v225, v225, v253, v254\n\t" "v_max_u32 v225, v225, v255\n\t" \
    "s_waitcnt lgkmcnt(7)\n\t" "v_smfmac_f32_16x16x64_bf16 v[232:235], v[16:19], a[48:55], %[ix]\n\t" "v_cmp_eq_u32 vcc, -1, v225\n\t" "s_cbranch_vccnz 24f\n\t" "25:\n\t" \
    "v_smfmac_f32_16x16x64_bf16 v[236:239], v[16:19], a[56:63], %[ix]\n\t" "ds_read_b128 v[160:163], %[l0] offset:8192\n\t" "ds_read_b128 v[164:167], %[l0] offset:9216\n\t" \
    "s_waitcnt lgkmcnt(8)\n\t" "v_smfmac_f32_16x16x64_bf16 v[232:235], v[20:23], a[64:71], %[ix]\n\t" "ds_read_b128 v[168:171], %[l0] offset:10240\n\t" \
    "ds_read_b128 v[172:175], %[l0] offset:11264\n\t" "v_smfmac_f32_16x16x64_bf16 v[236:239], v[20:23], v[176:183], %[ix]\n\t" "s_waitcnt lgkmcnt(4)\n\t" "s_barrier\n\t" \
    XT(88, 89) "v_smfmac_f32_16x16x64_bf16 v[232:235], v[24:27], a[72:79], %[ix]\n\t" "ds_read_b128 v[176:179], %[l0] offset:12288\n\t" \
    "ds_read_b128 v[180:183], %[l0] offset:13312\n\t" "v_smfmac_f32_16x16x64_bf16 v[236:239], v[24:27], a[80:87], %[ix]\n\t" "ds_read_b128 v[248:251], %[l1] offset:10240\n\t" \
    "ds_read_b128 v[252:255], %[l1] offset:14336\n\t" "v_smfmac_f32_16x16x64_bf16 v[232:235], v[28:31], a[88:95], %[ix]\n\t" "ds_read_b128 v[16:19], %[l1] offset:2048\n\t" \
    "ds_read_b128 v[20:23], %[l1] offset:11264\n\t" "v_smfmac_f32_16x16x64_bf16 v[236:239], v[28:31], v[184:191], %[ix]\n\t" "ds_read_b128 v[24:27], %[l1] offset:15360\n\t" \
    "ds_read_b128 v[184:187], %[l0] offset:14336\n\t" "v_smfmac_f32_16x16x64_bf16 v[232:235], v[240:243], a[96:103], %[ix]\n\t" \
    "ds_read_b128 v[188:191], %[l0] offset:15360\n\t" "ds_read_b128 v[28:31], %[l1] offset:3072\n\t" \
    "v_smfmac_f32_16x16x64_bf16 v[236:239], v[240:243], a[104:111], %[ix]\n\t" "v_smfmac_f32_16x16x64_bf16 v[232:235], v[244:247], a[112:119], %[ix]\n\t" \
    "s_waitcnt lgkmcnt(12)\n\t" "v_smfmac_f32_16x16x64_bf16 v[236:239], v[244:247], v[160:167], %[ix]\n\t" "ds_read_b128 v[160:163], %[l0] offset:16384\n\t" \
    "ds_read_b128 v[164:167], %[l0] offset:17408\n\t" "s_waitcnt lgkmcnt(9)\n\t" "v_smfmac_f32_16x16x64_bf16 v[232:235], v[248:251], a[120:127], %[ix]\n\t" \
    "v_smfmac_f32_16x16x64_bf16 v[236:239], v[248:251], a[128:135], %[ix]\n\t" "s_waitcnt lgkmcnt(8)\n\t" \
    "v_smfmac_f32_16x16x64_bf16 v[232:235], v[252:255], a[136:143], %[ix]\n\t" "v_smfmac_f32_16x16x64_bf16 v[236:239], v[252:255], v[168:175], %[ix]\n\t" \
    "ds_read_b128 v[168:171], %[l0] offset:18432\n\t" "ds_read_b128 v[172:175], %[l0] offset:19456\n\t" "s_waitcnt lgkmcnt(9)\n\t" \
    "v_smfmac_f32_16x16x64_bf16 v[232:235], v[16:19], a[144:151], %[ix]\n\t" "v_smfmac_f32_16x16x64_bf16 v[236:239], v[16:19], a[152:159], %[ix]\n\t" \
    "s_waitcnt lgkmcnt(8)\n\t" "v_smfmac_f32_16x16x64_bf16 v[232:235], v[20:23], a[160:167], %[ix]\n\t" \
    "v_smfmac_f32_16x16x64_bf16 v[236:239], v[20:23], v[176:183], %[ix]\n\t" "ds_read_b128 v[176:179], %[l0] offset:20480\n\t" \
    "ds_read_b128 v[180:183], %[l0] offset:21504\n\t" "s_waitcnt lgkmcnt(9)\n\t" "v_smfmac_f32_16x16x64_bf16 v[232:235], v[24:27], a[168:175], %[ix]\n\t" \
    "v_smfmac_f32_16x16x64_bf16 v[236:239], v[24:27], a[176:183], %[ix]\n\t" "s_waitcnt lgkmcnt(6)\n\t" \
    "v_smfmac_f32_16x16x64_bf16 v[232:235], v[28:31], a[184:191], %[ix]\n\t" "v_smfmac_f32_16x16x64_bf16 v[236:239], v[28:31], v[184:191], %[ix]\n\t" \
    "ds_read_b128 v[184:187], %[l0] offset:22528\n\t" "ds_read_b128 v[188:191], %[l0] offset:23552\n\t" XT(90, 91) "s_nop 4\n\t" "v_add_f32 v232, v232, v233\n\t" \
    "v_add_f32 v234, v234, v235\n\t" "v_add_f32 v236, v236, v237\n\t" "v_add_f32 v238, v238, v239\n\t" "v_cvt_pk_bf16_f32 v232, v232, v236\n\t" \
    "v_cvt_pk_bf16_f32 v233, v234, v238\n\t" "ds_write2_b32 %[vw], v232, v233 offset1:68\n\t" "s_waitcnt lgkmcnt(0)\n\t" "s_barrier\n\t" XT(92, 93) \
    "ds_read_b128 v[240:243], %[vr] offset:0\n\t" "ds_read_b128 v[244:247], %[vr] offset:64\n\t" "ds_read_b128 v[248:251], %[vr] offset:128\n\t" \
    "ds_read_b128 v[252:255], %[vr] offset:192\n\t" "s_mov_b32 s61, %[ub]\n\t" "s_waitcnt lgkmcnt(3)\n\t" \
    "v_smfmac_f32_16x16x64_bf16 v[192:195], v[240:243], v[32:39], %[ix]\n\t" "v_smfmac_f32_16x16x64_bf16 v[196:199], v[240:243], v[48:55], %[ix]\n\t" \
    "s_waitcnt lgkmcnt(2)\n\t" "v_smfmac_f32_16x16x64_bf16 v[192:195], v[244:247], v[40:47], %[ix]\n\t" \
    "v_smfmac_f32_16x16x64_bf16 v[196:199], v[244:247], v[56:63], %[ix]\n\t" "s_waitcnt lgkmcnt(1)\n\t" \
    "v_smfmac_f32_16x16x64_bf16 v[192:195], v[248:251], a[192:199], %[ix]\n\t" "v_smfmac_f32_16x16x64_bf16 v[196:199], v[248:251], a[200:207], %[ix]\n\t" \
    "s_waitcnt lgkmcnt(0)\n\t" "v_smfmac_f32_16x16x64_bf16 v[192:195], v[252:255], v[160:167], %[ix]\n\t" "ds_read_b128 v[160:163], %[l0] offset:24576\n\t" \
    "ds_read_b128 v[164:167], %[l0] offset:25600\n\t" "v_smfmac_f32_16x16x64_bf16 v[196:199], v[252:255], v[168:175], %[ix]\n\t" \
    "ds_read_b128 v[168:171], %[l0] offset:26624\n\t" "ds_read_b128 v[172:175], %[l0] offset:27648\n\t" \
    "v_smfmac_f32_16x16x64_bf16 v[200:203], v[240:243], v[64:71], %[ix]\n\t" "s_nop 1\n\t" "v_smfmac_f32_16x16x64_bf16 v[204:207], v[240:243], v[80:87], %[ix]\n\t" \
    "v_cvt_pk_bf16_f32 v224, v192, v194\n\t" "v_cvt_pk_bf16_f32 v225, v193, v195\n\t" "v_smfmac_f32_16x16x64_bf16 v[200:203], v[244:247], v[72:79], %[ix]\n\t" \
    "v_cvt_pk_bf16_f32 v226, v196, v198\n\t" "v_cvt_pk_bf16_f32 v227, v197, v199\n\t" "v_smfmac_f32_16x16x64_bf16 v[204:207], v[244:247], v[88:95], %[ix]\n\t" "s_nop 0\n\t" \
    "v_permlane32_swap_b32 v224, v226\n\t" "v_smfmac_f32_16x16x64_bf16 v[200:203], v[248:251], a[208:215], %[ix]\n\t" "v_permlane32_swap_b32 v225, v227\n\t" "s_nop 0\n\t" \
    "v_smfmac_f32_16x16x64_bf16 v[204:207], v[248:251], a[216:223], %[ix]\n\t" "v_permlane16_swap_b32 v224, v225\n\t" "v_permlane16_swap_b32 v226, v227\n\t" \
    "v_smfmac_f32_16x16x64_bf16 v[200:203], v[252:255], v[176:183], %[ix]\n\t" "buffer_store_dwordx4 v[224:227], %[uo], %[rs], s61 offen sc1\n\t" \
    "s_add_u32 s61, s61, 0x10000\n\t" XT(94, 95) "v_smfmac_f32_16x16x64_bf16 v[204:207], v[252:255], v[184:191], %[ix]\n\t" "ds_read_b128 v[176:179], %[l0] offset:28672\n\t" \
    "ds_read_b128 v[180:183], %[l0] offset:29696\n\t" "v_smfmac_f32_16x16x64_bf16 v[208:211], v[240:243], v[96:103], %[ix]\n\t" \
    "ds_read_b128 v[184:187], %[l0] offset:30720\n\t" "ds_read_b128 v[188:191], %[l0] offset:31744\n\t" \
    "v_smfmac_f32_16x16x64_bf16 v[212:215], v[240:243], v[112:119], %[ix]\n\t" "v_cvt_pk_bf16_f32 v228, v200, v202\n\t" "v_cvt_pk_bf16_f32 v229, v201, v203\n\t" \
    "v_smfmac_f32_16x16x64_bf16 v[208:211], v[244:247], v[104:111], %[ix]\n\t" "v_cvt_pk_bf16_f32 v230, v204, v206\n\t" "v_cvt_pk_bf16_f32 v231, v205, v207\n\t" \
    "v_smfmac_f32_16x16x64_bf16 v[212:215], v[244:247], v[120:127], %[ix]\n\t" "s_nop 0\n\t" "v_permlane32_swap_b32 v228, v230\n\t" \
    "v_smfmac_f32_16x16x64_bf16 v[208:211], v[248:251], a[224:231], %[ix]\n\t" "v_permlane32_swap_b32 v229, v231\n\t" "s_nop 0\n\t" \
    "v_smfmac_f32_16x16x64_bf16 v[212:215], v[248:251], a[232:239], %[ix]\n\t" "v_permlane16_swap_b32 v228, v229\n\t" "v_permlane16_swap_b32 v230, v231\n\t" \
    "s_waitcnt lgkmcnt(6)\n\t" "v_smfmac_f32_16x16x64_bf16 v[208:211], v[252:255], v[160:167], %[ix]\n\t" "buffer_store_dwordx4 v[228:231], %[uo], %[rs], s61 offen sc1\n\t" \
    "s_add_u32 s61, s61, 0x10000\n\t" "s_waitcnt lgkmcnt(4)\n\t" "v_smfmac_f32_16x16x64_bf16 v[212:215], v[252:255], v[168:175], %[ix]\n\t" "s_nop 1\n\t" \
    "v_smfmac_f32_16x16x64_bf16 v[216:219], v[240:243], v[128:135], %[ix]\n\t" "s_nop 1\n\t" "v_smfmac_f32_16x16x64_bf16 v[220:223], v[240:243], v[144:151], %[ix]\n\t" \
    "v_cvt_pk_bf16_f32 v224, v208, v210\n\t" "v_cvt_pk_bf16_f32 v225, v209, v211\n\t" "v_smfmac_f32_16x16x64_bf16 v[216:219], v[244:247], v[136:143], %[ix]\n\t" \
    "v_cvt_pk_bf16_f32 v226, v212, v214\n\t" "v_cvt_pk_bf16_f32 v227, v213, v215\n\t" "v_smfmac_f32_16x16x64_bf16 v[220:223], v[244:247], v[152:159], %[ix]\n\t" "s_nop 0\n\t" \
    "v_permlane32_swap_b32 v224, v226\n\t" "v_smfmac_f32_16x16x64_bf16 v[216:219], v[248:251], a[240:247], %[ix]\n\t" "v_permlane32_swap_b32 v225, v227\n\t" "s_nop 0\n\t" \
    "v_smfmac_f32_16x16x64_bf16 v[220:223], v[248:251], a[248:255], %[ix]\n\t" "v_permlane16_swap_b32 v224, v225\n\t" "v_permlane16_swap_b32 v226, v227\n\t" \
    "s_waitcnt lgkmcnt(2)\n\t" "v_smfmac_f32_16x16x64_bf16 v[216:219], v[252:255], v[176:183], %[ix]\n\t" "buffer_store_dwordx4 v[224:227], %[uo], %[rs], s61 offen sc1\n\t" \
    "s_add_u32 s61, s61, 0x10000\n\t" "s_waitcnt lgkmcnt(0)\n\t" "v_smfmac_f32_16x16x64_bf16 v[220:223], v[252:255], v[184:191], %[ix]\n\t" "s_nop 5\n\t" \
    "v_cvt_pk_bf16_f32 v228, v216, v218\n\t" "v_cvt_pk_bf16_f32 v229, v217, v219\n\t" "v_cvt_pk_bf16_f32 v230, v220, v222\n\t" "v_cvt_pk_bf16_f32 v231, v221, v223\n\t" \
    "s_nop 0\n\t" "v_permlane32_swap_b32 v228, v230\n\t" "v_permlane32_swap_b32 v229, v231\n\t" "s_nop 1\n\t" "v_permlane16_swap_b32 v228, v229\n\t" \
    "v_permlane16_swap_b32 v230, v231\n\t" "buffer_store_dwordx4 v[228:231], %[uo], %[rs], s61 offen sc1\n\t" "s_nop 1\n\t" XT(96, 97) "s_branch 99f\n\t" "21:\n\t" \
    "s_cmp_eq_u32 %[bd], 0\n\t" "s_cbranch_scc1 26f\n\t" "s_sub_u32 %[bd], %[bd], 1\n\t" XCNT(100) "s_waitcnt lgkmcnt(0)\n\t" "s_nop 7\n\t" "s_branch 20b\n\t" "26:\n\t" \
    "s_cmp_lg_u32 s60, 0\n\t" "s_branch 22b\n\t" "24:\n\t" "s_cmp_eq_u32 %[bd], 0\n\t" "s_cbranch_scc1 25b\n\t" "s_sub_u32 %[bd], %[bd], 1\n\t" XCNT(101) \
    "s_mov_b32 s60, 1\n\t" "s_waitcnt lgkmcnt(0)\n\t" "s_nop 7\n\t" "s_branch 20b\n\t" "92:\n\t" XT(80, 81) "s_barrier\n\t" XT(82, 83) "30:\n\t" \
    "buffer_load_dwordx4 v[240:243], %[mo], %[rs], %[sb] offen offset:0 sc1\n\t" "v_mov_b64 v[232:233], 0\n\t" "v_mov_b64 v[234:235], 0\n\t" "v_mov_b64 v[236:237], 0\n\t" \
    "v_mov_b64 v[238:239], 0\n\t" "v_mov_b64 v[192:193], 0\n\t" "v_mov_b64 v[194:195], 0\n\t" "v_mov_b64 v[196:197], 0\n\t" "v_mov_b64 v[198:199], 0\n\t" \
    "buffer_load_dwordx4 v[244:247], v224, %[rs], %[sb] offen offset:0 sc1\n\t" "ds_read_b128 v[160:163], %[l0] offset:0\n\t" "ds_read_b128 v[164:167], %[l0] offset:1024\n\t" \
    "ds_read_b128 v[168:171], %[l0] offset:2048\n\t" "ds_read_b128 v[172:175], %[l0] offset:3072\n\t" "s_nop 3\n\t" \
    "buffer_load_dwordx4 v[248:251], %[mo], %[rs], %[sb] offen offset:2048 sc1\n\t" "ds_read_b128 v[176:179], %[l0] offset:4096\n\t" \
    "ds_read_b128 v[180:183], %[l0] offset:5120\n\t" "ds_read_b128 v[184:187], %[l0] offset:6144\n\t" "ds_read_b128 v[188:191], %[l0] offset:7168\n\t" "s_nop 3\n\t" \
    "buffer_load_dwordx4 v[252:255], v224, %[rs], %[sb] offen offset:2048 sc1\n\t" "v_mov_b64 v[200:201], 0\n\t" "v_mov_b64 v[202:203], 0\n\t" "v_mov_b64 v[204:205], 0\n\t" \
    "v_mov_b64 v[206:207], 0\n\t" "v_mov_b64 v[208:209], 0\n\t" "v_mov_b64 v[210:211], 0\n\t" "v_mov_b64 v[212:213], 0\n\t" "v_mov_b64 v[214:215], 0\n\t" \
    "v_mov_b64 v[216:217], 0\n\t" "v_mov_b64 v[218:219], 0\n\t" "v_mov_b64 v[220:221], 0\n\t" "v_mov_b64 v[222:223], 0\n\t" "s_waitcnt vmcnt(3)\n\t" XT(84, 85) \
    "v_smfmac_f32_16x16x64_bf16 v[232:235], v[240:243], a[0:7], %[ix]\n\t" "ds_write_b128 %[l1], v[240:243] offset:8192\n\t" \
    "v_smfmac_f32_16x16x64_bf16 v[236:239], v[240:243], a[8:15], %[ix]\n\t" "v_max3_u32 v225, v240, v241, v242\n\t" "s_waitcnt vmcnt(2)\n\t" \
    "v_smfmac_f32_16x16x64_bf16 v[232:235], v[244:247], a[16:23], %[ix]\n\t" "ds_write_b128 %[l1], v[244:247] offset:9216\n\t" "v_max3_u32 v225, v225, v243, v244\n\t" \
    "s_waitcnt lgkmcnt(8)\n\t" "v_smfmac_f32_16x16x64_bf16 v[236:239], v[244:247], v[160:167], %[ix]\n\t" "v_max3_u32 v225, v225, v245, v246\n\t" \
    "v_max_u32 v225, v225, v247\n\t" "v_cmp_eq_u32 vcc, -1, v225\n\t" "s_cmp_lg_u32 s60, 0\n\t" "s_cbranch_vccnz 31f\n\t" "32:\n\t" "s_waitcnt lgkmcnt(0)\n\t" \
    "s_cbranch_scc1 33f\n\t" "s_barrier\n\t" "33:\n\t" XT(86, 87) "ds_read_b128 v[16:19], %[l1] offset:12288\n\t" "ds_read_b128 v[20:23], %[l1] offset:0\n\t" \
    "ds_read_b128 v[24:27], %[l1] offset:4096\n\t" "s_waitcnt vmcnt(1)\n\t" "v_smfmac_f32_16x16x64_bf16 v[232:235], v[248:251], a[24:31], %[ix]\n\t" \
    "ds_write_b128 %[l1], v[248:251] offset:10240\n\t" "v_max3_u32 v225, v248, v249, v250\n\t" "v_smfmac_f32_16x16x64_bf16 v[236:239], v[248:251], a[32:39], %[ix]\n\t" \
    "ds_read_b128 v[28:31], %[l1] offset:13312\n\t" "ds_read_b128 v[240:243], %[l1] offset:1024\n\t" "ds_read_b128 v[244:247], %[l1] offset:5120\n\t" "s_waitcnt vmcnt(0)\n\t" \
    "v_smfmac_f32_16x16x64_bf16 v[232:235], v[252:255], a[40:47], %[ix]\n\t" "ds_write_b128 %[l1], v[252:255] offset:11264\n\t" "v_max3_u32 v225, v225, v251, v252\n\t" \
    "v_smfmac_f32_16x16x64_bf16 v[236:239], v[252:255], v[168:175], %[ix]\n\t" "v_max3_u32 v225, v225, v253, v254\n\t" "v_max_u32 v225, v225, v255\n\t" \
    "s_waitcnt lgkmcnt(7)\n\t" "v_smfmac_f32_16x16x64_bf16 v[232:235], v[16:19], a[48:55], %[ix]\n\t" "v_cmp_eq_u32 vcc, -1, v225\n\t" "s_cbranch_vccnz 34f\n\t" "35:\n\t" \
    "v_smfmac_f32_16x16x64_bf16 v[236:239], v[16:19], a[56:63], %[ix]\n\t" "ds_read_b128 v[160:163], %[l0] offset:8192\n\t" "ds_read_b128 v[164:167], %[l0] offset:9216\n\t" \
    "s_waitcnt lgkmcnt(8)\n\t" "v_smfmac_f32_16x16x64_bf16 v[232:235], v[20:23], a[64:71], %[ix]\n\t" "ds_read_b128 v[168:171], %[l0] offset:10240\n\t" \
    "ds_read_b128 v[172:175], %[l0] offset:11264\n\t" "v_smfmac_f32_16x16x64_bf16 v[236:239], v[20:23], v[176:183], %[ix]\n\t" "s_waitcnt lgkmcnt(4)\n\t" "s_barrier\n\t" \
    XT(88, 89) "v_smfmac_f32_16x16x64_bf16 v[232:235], v[24:27], a[72:79], %[ix]\n\t" "ds_read_b128 v[176:179], %[l0] offset:12288\n\t" \
    "ds_read_b128 v[180:183], %[l0] offset:13312\n\t" "v_smfmac_f32_16x16x64_bf16 v[236:239], v[24:27], a[80:87], %[ix]\n\t" "ds_read_b128 v[248:251], %[l1] offset:14336\n\t" \
    "ds_read_b128 v[252:255], %[l1] offset:2048\n\t" "v_smfmac_f32_16x16x64_bf16 v[232:235], v[28:31], a[88:95], %[ix]\n\t" "ds_read_b128 v[16:19], %[l1] offset:6144\n\t" \
    "ds_read_b128 v[20:23], %[l1] offset:15360\n\t" "v_smfmac_f32_16x16x64_bf16 v[236:239], v[28:31], v[184:191], %[ix]\n\t" "ds_read_b128 v[24:27], %[l1] offset:3072\n\t" \
    "ds_read_b128 v[184:187], %[l0] offset:14336\n\t" "v_smfmac_f32_16x16x64_bf16 v[232:235], v[240:243], a[96:103], %[ix]\n\t" \
    "ds_read_b128 v[188:191], %[l0] offset:15360\n\t" "ds_read_b128 v[28:31], %[l1] offset:7168\n\t" \
    "v_smfmac_f32_16x16x64_bf16 v[236:239], v[240:243], a[104:111], %[ix]\n\t" "v_smfmac_f32_16x16x64_bf16 v[232:235], v[244:247], a[112:119], %[ix]\n\t" \
    "s_waitcnt lgkmcnt(12)\n\t" "v_smfmac_f32_16x16x64_bf16 v[236:239], v[244:247], v[160:167], %[ix]\n\t" "ds_read_b128 v[160:163], %[l0] offset:16384\n\t" \
    "ds_read_b128 v[164:167], %[l0] offset:17408\n\t" "s_waitcnt lgkmcnt(9)\n\t" "v_smfmac_f32_16x16x64_bf16 v[232:235], v[248:251], a[120:127], %[ix]\n\t" \
    "v_smfmac_f32_16x16x64_bf16 v[236:239], v[248:251], a[128:135], %[ix]\n\t" "s_waitcnt lgkmcnt(8)\n\t" \
    "v_smfmac_f32_16x16x64_bf16 v[232:235], v[252:255], a[136:143], %[ix]\n\t" "v_smfmac_f32_16x16x64_bf16 v[236:239], v[252:255], v[168:175], %[ix]\n\t" \
    "ds_read_b128 v[168:171], %[l0] offset:18432\n\t" "ds_read_b128 v[172:175], %[l0] offset:19456\n\t" "s_waitcnt lgkmcnt(9)\n\t" \
    "v_smfmac_f32_16x16x64_bf16 v[232:235], v[16:19], a[144:151], %[ix]\n\t" "v_smfmac_f32_16x16x64_bf16 v[236:239], v[16:19], a[152:159], %[ix]\n\t" \
    "s_waitcnt lgkmcnt(8)\n\t" "v_smfmac_f32_16x16x64_bf16 v[232:235], v[20:23], a[160:167], %[ix]\n\t" \
    "v_smfmac_f32_16x16x64_bf16 v[236:239], v[20:23], v[176:183], %[ix]\n\t" "ds_read_b128 v[176:179], %[l0] offset:20480\n\t" \
    "ds_read_b128 v[180:183], %[l0] offset:21504\n\t" "s_waitcnt lgkmcnt(9)\n\t" "v_smfmac_f32_16x16x64_bf16 v[232:235], v[24:27], a[168:175], %[ix]\n\t" \
    "v_smfmac_f32_16x16x64_bf16 v[236:239], v[24:27], a[176:183], %[ix]\n\t" "s_waitcnt lgkmcnt(6)\n\t" \
    "v_smfmac_f32_16x16x64_bf16 v[232:235], v[28:31], a[184:191], %[ix]\n\t" "v_smfmac_f32_16x16x64_bf16 v[236:239], v[28:31], v[184:191], %[ix]\n\t" \
    "ds_read_b128 v[184:187], %[l0] offset:22528\n\t" "ds_read_b128 v[188:191], %[l0] offset:23552\n\t" XT(90, 91) "s_nop 4\n\t" "v_add_f32 v232, v232, v233\n\t" \
    "v_add_f32 v234, v234, v235\n\t" "v_add_f32 v236, v236, v237\n\t" "v_add_f32 v238, v238, v239\n\t" "v_cvt_pk_bf16_f32 v232, v232, v236\n\t" \
    "v_cvt_pk_bf16_f32 v233, v234, v238\n\t" "ds_write2_b32 %[vw], v232, v233 offset1:68\n\t" "s_waitcnt lgkmcnt(0)\n\t" "s_barrier\n\t" XT(92, 93) \
    "ds_read_b128 v[240:243], %[vr] offset:0\n\t" "ds_read_b128 v[244:247], %[vr] offset:64\n\t" "ds_read_b128 v[248:251], %[vr] offset:128\n\t" \
    "ds_read_b128 v[252:255], %[vr] offset:192\n\t" "s_mov_b32 s61, %[ub]\n\t" "s_waitcnt lgkmcnt(3)\n\t" \
    "v_smfmac_f32_16x16x64_bf16 v[192:195], v[240:243], v[32:39], %[ix]\n\t" "v_smfmac_f32_16x16x64_bf16 v[196:199], v[240:243], v[48:55], %[ix]\n\t" \
    "s_waitcnt lgkmcnt(2)\n\t" "v_smfmac_f32_16x16x64_bf16 v[192:195], v[244:247], v[40:47], %[ix]\n\t" \
    "v_smfmac_f32_16x16x64_bf16 v[196:199], v[244:247], v[56:63], %[ix]\n\t" "s_waitcnt lgkmcnt(1)\n\t" \
    "v_smfmac_f32_16x16x64_bf16 v[192:195], v[248:251], a[192:199], %[ix]\n\t" "v_smfmac_f32_16x16x64_bf16 v[196:199], v[248:251], a[200:207], %[ix]\n\t" \
    "s_waitcnt lgkmcnt(0)\n\t" "v_smfmac_f32_16x16x64_bf16 v[192:195], v[252:255], v[160:167], %[ix]\n\t" "ds_read_b128 v[160:163], %[l0] offset:24576\n\t" \
    "ds_read_b128 v[164:167], %[l0] offset:25600\n\t" "v_smfmac_f32_16x16x64_bf16 v[196:199], v[252:255], v[168:175], %[ix]\n\t" \
    "ds_read_b128 v[168:171], %[l0] offset:26624\n\t" "ds_read_b128 v[172:175], %[l0] offset:27648\n\t" \
    "v_smfmac_f32_16x16x64_bf16 v[200:203], v[240:243], v[64:71], %[ix]\n\t" "s_nop 1\n\t" "v_smfmac_f32_16x16x64_bf16 v[204:207], v[240:243], v[80:87], %[ix]\n\t" \
    "v_cvt_pk_bf16_f32 v224, v192, v194\n\t" "v_cvt_pk_bf16_f32 v225, v193, v195\n\t" "v_smfmac_f32_16x16x64_bf16 v[200:203], v[244:247], v[72:79], %[ix]\n\t" \
    "v_cvt_pk_bf16_f32 v226, v196, v198\n\t" "v_cvt_pk_bf16_f32 v227, v197, v199\n\t" "v_smfmac_f32_16x16x64_bf16 v[204:207], v[244:247], v[88:95], %[ix]\n\t" "s_nop 0\n\t" \
    "v_permlane32_swap_b32 v224, v226\n\t" "v_smfmac_f32_16x16x64_bf16 v[200:203], v[248:251], a[208:215], %[ix]\n\t" "v_permlane32_swap_b32 v225, v227\n\t" "s_nop 0\n\t" \
    "v_smfmac_f32_16x16x64_bf16 v[204:207], v[248:251], a[216:223], %[ix]\n\t" "v_permlane16_swap_b32 v224, v225\n\t" "v_permlane16_swap_b32 v226, v227\n\t" \
    "v_smfmac_f32_16x16x64_bf16 v[200:203], v[252:255], v[176:183], %[ix]\n\t" "buffer_store_dwordx4 v[224:227], %[uo], %[rs], s61 offen sc1\n\t" \
    "s_add_u32 s61, s61, 0x10000\n\t" XT(94, 95) "v_smfmac_f32_16x16x64_bf16 v[204:207], v[252:255], v[184:191], %[ix]\n\t" "ds_read_b128 v[176:179], %[l0] offset:28672\n\t" \
    "ds_read_b128 v[180:183], %[l0] offset:29696\n\t" "v_smfmac_f32_16x16x64_bf16 v[208:211], v[240:243], v[96:103], %[ix]\n\t" \
    "ds_read_b128 v[184:187], %[l0] offset:30720\n\t" "ds_read_b128 v[188:191], %[l0] offset:31744\n\t" \
    "v_smfmac_f32_16x16x64_bf16 v[212:215], v[240:243], v[112:119], %[ix]\n\t" "v_cvt_pk_bf16_f32 v228, v200, v202\n\t" "v_cvt_pk_bf16_f32 v229, v201, v203\n\t" \
    "v_smfmac_f32_16x16x64_bf16 v[208:211], v[244:247], v[104:111], %[ix]\n\t" "v_cvt_pk_bf16_f32 v230, v204, v206\n\t" "v_cvt_pk_bf16_f32 v231, v205, v207\n\t" \
    "v_smfmac_f32_16x16x64_bf16 v[212:215], v[244:247], v[120:127], %[ix]\n\t" "s_nop 0\n\t" "v_permlane32_swap_b32 v228, v230\n\t" \
    "v_smfmac_f32_16x16x64_bf16 v[208:211], v[248:251], a[224:231], %[ix]\n\t" "v_permlane32_swap_b32 v229, v231\n\t" "s_nop 0\n\t" \
    "v_smfmac_f32_16x16x64_bf16 v[212:215], v[248:251], a[232:239], %[ix]\n\t" "v_permlane16_swap_b32 v228, v229\n\t" "v_permlane16_swap_b32 v230, v231\n\t" \
    "s_waitcnt lgkmcnt(6)\n\t" "v_smfmac_f32_16x16x64_bf16 v[208:211], v[252:255], v[160:167], %[ix]\n\t" "buffer_store_dwordx4 v[228:231], %[uo], %[rs], s61 offen sc1\n\t" \
    "s_add_u32 s61, s61, 0x10000\n\t" "s_waitcnt lgkmcnt(4)\n\t" "v_smfmac_f32_16x16x64_bf16 v[212:215], v[252:255], v[168:175], %[ix]\n\t" "s_nop 1\n\t" \
    "v_smfmac_f32_16x16x64_bf16 v[216:219], v[240:243], v[128:135], %[ix]\n\t" "s_nop 1\n\t" "v_smfmac_f32_16x16x64_bf16 v[220:223], v[240:243], v[144:151], %[ix]\n\t" \
    "v_cvt_pk_bf16_f32 v224, v208, v210\n\t" "v_cvt_pk_bf16_f32 v225, v209, v211\n\t" "v_smfmac_f32_16x16x64_bf16 v[216:219], v[244:247], v[136:143], %[ix]\n\t" \
    "v_cvt_pk_bf16_f32 v226, v212, v214\n\t" "v_cvt_pk_bf16_f32 v227, v213, v215\n\t" "v_smfmac_f32_16x16x64_bf16 v[220:223], v[244:247], v[152:159], %[ix]\n\t" "s_nop 0\n\t" \
    "v_permlane32_swap_b32 v224, v226\n\t" "v_smfmac_f32_16x16x64_bf16 v[216:219], v[248:251], a[240:247], %[ix]\n\t" "v_permlane32_swap_b32 v225, v227\n\t" "s_nop 0\n\t" \
    "v_smfmac_f32_16x16x64_bf16 v[220:223], v[248:251], a[248:255], %[ix]\n\t" "v_permlane16_swap_b32 v224, v225\n\t" "v_permlane16_swap_b32 v226, v227\n\t" \
    "s_waitcnt lgkmcnt(2)\n\t" "v_smfmac_f32_16x16x64_bf16 v[216:219], v[252:255], v[176:183], %[ix]\n\t" "buffer_store_dwordx4 v[224:227], %[uo], %[rs], s61 offen sc1\n\t" \
    "s_add_u32 s61, s61, 0x10000\n\t" "s_waitcnt lgkmcnt(0)\n\t" "v_smfmac_f32_16x16x64_bf16 v[220:223], v[252:255], v[184:191], %[ix]\n\t" "s_nop 5\n\t" \
    "v_cvt_pk_bf16_f32 v228, v216, v218\n\t" "v_cvt_pk_bf16_f32 v229, v217, v219\n\t" "v_cvt_pk_bf16_f32 v230, v220, v222\n\t" "v_cvt_pk_bf16_f32 v231, v221, v223\n\t" \
    "s_nop 0\n\t" "v_permlane32_swap_b32 v228, v230\n\t" "v_permlane32_swap_b32 v229, v231\n\t" "s_nop 1\n\t" "v_permlane16_swap_b32 v228, v229\n\t" \
    "v_permlane16_swap_b32 v230, v231\n\t" "buffer_store_dwordx4 v[228:231], %[uo], %[rs], s61 offen sc1\n\t" "s_nop 1\n\t" XT(96, 97) "s_branch 99f\n\t" "31:\n\t" \
    "s_cmp_eq_u32 %[bd], 0\n\t" "s_cbranch_scc1 36f\n\t" "s_sub_u32 %[bd], %[bd], 1\n\t" XCNT(100) "s_waitcnt lgkmcnt(0)\n\t" "s_nop 7\n\t" "s_branch 30b\n\t" "36:\n\t" \
    "s_cmp_lg_u32 s60, 0\n\t" "s_branch 32b\n\t" "34:\n\t" "s_cmp_eq_u32 %[bd], 0\n\t" "s_cbranch_scc1 35b\n\t" "s_sub_u32 %[bd], %[bd], 1\n\t" XCNT(101) \
    "s_mov_b32 s60, 1\n\t" "s_waitcnt lgkmcnt(0)\n\t" "s_nop 7\n\t" "s_branch 30b\n\t" "93:\n\t" XT(80, 81) "s_barrier\n\t" XT(82, 83) "40:\n\t" \
    "buffer_load_dwordx4 v[240:243], %[mo], %[rs], %[sb] offen offset:0 sc1\n\t" "v_mov_b64 v[232:233], 0\n\t" "v_mov_b64 v[234:235], 0\n\t" "v_mov_b64 v[236:237], 0\n\t" \
    "v_mov_b64 v[238:239], 0\n\t" "v_mov_b64 v[192:193], 0\n\t" "v_mov_b64 v[194:195], 0\n\t" "v_mov_b64 v[196:197], 0\n\t" "v_mov_b64 v[198:199], 0\n\t" \
    "buffer_load_dwordx4 v[244:247], v224, %[rs], %[sb] offen offset:0 sc1\n\t" "ds_read_b128 v[160:163], %[l0] offset:0\n\t" "ds_read_b128 v[164:167], %[l0] offset:1024\n\t" \
    "ds_read_b128 v[168:171], %[l0] offset:2048\n\t" "ds_read_b128 v[172:175], %[l0] offset:3072\n\t" "s_nop 3\n\t" \
    "buffer_load_dwordx4 v[248:251], %[mo], %[rs], %[sb] offen offset:2048 sc1\n\t" "ds_read_b128 v[176:179], %[l0] offset:4096\n\t" \
    "ds_read_b128 v[180:183], %[l0] offset:5120\n\t" "ds_read_b128 v[184:187], %[l0] offset:6144\n\t" "ds_read_b128 v[188:191], %[l0] offset:7168\n\t" "s_nop 3\n\t" \
    "buffer_load_dwordx4 v[252:255], v224, %[rs], %[sb] offen offset:2048 sc1\n\t" "v_mov_b64 v[200:201], 0\n\t" "v_mov_b64 v[202:203], 0\n\t" "v_mov_b64 v[204:205], 0\n\t" \
    "v_mov_b64 v[206:207], 0\n\t" "v_mov_b64 v[208:209], 0\n\t" "v_mov_b64 v[210:211], 0\n\t" "v_mov_b64 v[212:213], 0\n\t" "v_mov_b64 v[214:215], 0\n\t" \
    "v_mov_b64 v[216:217], 0\n\t" "v_mov_b64 v[218:219], 0\n\t" "v_mov_b64 v[220:221], 0\n\t" "v_mov_b64 v[222:223], 0\n\t" "s_waitcnt vmcnt(3)\n\t" XT(84, 85) \
    "v_smfmac_f32_16x16x64_bf16 v[232:235], v[240:243], a[0:7], %[ix]\n\t" "ds_write_b128 %[l1], v[240:243] offset:12288\n\t" \
    "v_smfmac_f32_16x16x64_bf16 v[236:239], v[240:243], a[8:15], %[ix]\n\t" "v_max3_u32 v225, v240, v241, v242\n\t" "s_waitcnt vmcnt(2)\n\t" \
    "v_smfmac_f32_16x16x64_bf16 v[232:235], v[244:247], a[16:23], %[ix]\n\t" "ds_write_b128 %[l1], v[244:247] offset:13312\n\t" "v_max3_u32 v225, v225, v243, v244\n\t" \
    "s_waitcnt lgkmcnt(8)\n\t" "v_smfmac_f32_16x16x64_bf16 v[236:239], v[244:247], v[160:167], %[ix]\n\t" "v_max3_u32 v225, v225, v245, v246\n\t" \
    "v_max_u32 v225, v225, v247\n\t" "v_cmp_eq_u32 vcc, -1, v225\n\t" "s_cmp_lg_u32 s60, 0\n\t" "s_cbranch_vccnz 41f\n\t" "42:\n\t" "s_waitcnt lgkmcnt(0)\n\t" \
    "s_cbranch_scc1 43f\n\t" "s_barrier\n\t" "43:\n\t" XT(86, 87) "ds_read_b128 v[16:19], %[l1] offset:0\n\t" "ds_read_b128 v[20:23], %[l1] offset:4096\n\t" \
    "ds_read_b128 v[24:27], %[l1] offset:8192\n\t" "s_waitcnt vmcnt(1)\n\t" "v_smfmac_f32_16x16x64_bf16 v[232:235], v[248:251], a[24:31], %[ix]\n\t" \
    "ds_write_b128 %[l1], v[248:251] offset:14336\n\t" "v_max3_u32 v225, v248, v249, v250\n\t" "v_smfmac_f32_16x16x64_bf16 v[236:239], v[248:251], a[32:39], %[ix]\n\t" \
    "ds_read_b128 v[28:31], %[l1] offset:1024\n\t" "ds_read_b128 v[240:243], %[l1] offset:5120\n\t" "ds_read_b128 v[244:247], %[l1] offset:9216\n\t" "s_waitcnt vmcnt(0)\n\t" \
    "v_smfmac_f32_16x16x64_bf16 v[232:235], v[252:255], a[40:47], %[ix]\n\t" "ds_write_b128 %[l1], v[252:255] offset:15360\n\t" "v_max3_u32 v225, v225, v251, v252\n\t" \
    "v_smfmac_f32_16x16x64_bf16 v[236:239], v[252:255], v[168:175], %[ix]\n\t" "v_max3_u32 v225, v225, v253, v254\n\t" "v_max_u32 v225, v225, v255\n\t" \
    "s_waitcnt lgkmcnt(7)\n\t" "v_smfmac_f32_16x16x64_bf16 v[232:235], v[16:19], a[48:55], %[ix]\n\t" "v_cmp_eq_u32 vcc, -1, v225\n\t" "s_cbranch_vccnz 44f\n\t" "45:\n\t" \
    "v_smfmac_f32_16x16x64_bf16 v[236:239], v[16:19], a[56:63], %[ix]\n\t" "ds_read_b128 v[160:163], %[l0] offset:8192\n\t" "ds_read_b128 v[164:167], %[l0] offset:9216\n\t" \
    "s_waitcnt lgkmcnt(8)\n\t" "v_smfmac_f32_16x16x64_bf16 v[232:235], v[20:23], a[64:71], %[ix]\n\t" "ds_read_b128 v[168:171], %[l0] offset:10240\n\t" \
    "ds_read_b128 v[172:175], %[l0] offset:11264\n\t" "v_smfmac_f32_16x16x64_bf16 v[236:239], v[20:23], v[176:183], %[ix]\n\t" "s_waitcnt lgkmcnt(4)\n\t" "s_barrier\n\t" \
    XT(88, 89) "v_smfmac_f32_16x16x64_bf16 v[232:235], v[24:27], a[72:79], %[ix]\n\t" "ds_read_b128 v[176:179], %[l0] offset:12288\n\t" \
    "ds_read_b128 v[180:183], %[l0] offset:13312\n\t" "v_smfmac_f32_16x16x64_bf16 v[236:239], v[24:27], a[80:87], %[ix]\n\t" "ds_read_b128 v[248:251], %[l1] offset:2048\n\t" \
    "ds_read_b128 v[252:255], %[l1] offset:6144\n\t" "v_smfmac_f32_16x16x64_bf16 v[232:235], v[28:31], a[88:95], %[ix]\n\t" "ds_read_b128 v[16:19], %[l1] offset:10240\n\t" \
    "ds_read_b128 v[20:23], %[l1] offset:3072\n\t" "v_smfmac_f32_16x16x64_bf16 v[236:239], v[28:31], v[184:191], %[ix]\n\t" "ds_read_b128 v[24:27], %[l1] offset:7168\n\t" \
    "ds_read_b128 v[184:187], %[l0] offset:14336\n\t" "v_smfmac_f32_16x16x64_bf16 v[232:235], v[240:243], a[96:103], %[ix]\n\t" \
    "ds_read_b128 v[188:191], %[l0] offset:15360\n\t" "ds_read_b128 v[28:31], %[l1] offset:11264\n\t" \
    "v_smfmac_f32_16x16x64_bf16 v[236:239], v[240:243], a[104:111], %[ix]\n\t" "v_smfmac_f32_16x16x64_bf16 v[232:235], v[244:247], a[112:119], %[ix]\n\t" \
    "s_waitcnt lgkmcnt(12)\n\t" "v_smfmac_f32_16x16x64_bf16 v[236:239], v[244:247], v[160:167], %[ix]\n\t" "ds_read_b128 v[160:163], %[l0] offset:16384\n\t" \
    "ds_read_b128 v[164:167], %[l0] offset:17408\n\t" "s_waitcnt lgkmcnt(9)\n\t" "v_smfmac_f32_16x16x64_bf16 v[232:235], v[248:251], a[120:127], %[ix]\n\t" \
    "v_smfmac_f32_16x16x64_bf16 v[236:239], v[248:251], a[128:135], %[ix]\n\t" "s_waitcnt lgkmcnt(8)\n\t" \
    "v_smfmac_f32_16x16x64_bf16 v[232:235], v[252:255], a[136:143], %[ix]\n\t" "v_smfmac_f32_16x16x64_bf16 v[236:239], v[252:255], v[168:175], %[ix]\n\t" \
    "ds_read_b128 v[168:171], %[l0] offset:18432\n\t" "ds_read_b128 v[172:175], %[l0] offset:19456\n\t" "s_waitcnt lgkmcnt(9)\n\t" \
    "v_smfmac_f32_16x16x64_bf16 v[232:235], v[16:19], a[144:151], %[ix]\n\t" "v_smfmac_f32_16x16x64_bf16 v[236:239], v[16:19], a[152:159], %[ix]\n\t" \
    "s_waitcnt lgkmcnt(8)\n\t" "v_smfmac_f32_16x16x64_bf16 v[232:235], v[20:23], a[160:167], %[ix]\n\t" \
    "v_smfmac_f32_16x16x64_bf16 v[236:239], v[20:23], v[176:183], %[ix]\n\t" "ds_read_b128 v[176:179], %[l0] offset:20480\n\t" \
    "ds_read_b128 v[180:183], %[l0] offset:21504\n\t" "s_waitcnt lgkmcnt(9)\n\t" "v_smfmac_f32_16x16x64_bf16 v[232:235], v[24:27], a[168:175], %[ix]\n\t" \
    "v_smfmac_f32_16x16x64_bf16 v[236:239], v[24:27], a[176:183], %[ix]\n\t" "s_waitcnt lgkmcnt(6)\n\t" \
    "v_smfmac_f32_16x16x64_bf16 v[232:235], v[28:31], a[184:191], %[ix]\n\t" "v_smfmac_f32_16x16x64_bf16 v[236:239], v[28:31], v[184:191], %[ix]\n\t" \
    "ds_read_b128 v[184:187], %[l0] offset:22528\n\t" "ds_read_b128 v[188:191], %[l0] offset:23552\n\t" XT(90, 91) "s_nop 4\n\t" "v_add_f32 v232, v232, v233\n\t" \
    "v_add_f32 v234, v234, v235\n\t" "v_add_f32 v236, v236, v237\n\t" "v_add_f32 v238, v238, v239\n\t" "v_cvt_pk_bf16_f32 v232, v232, v236\n\t" \
    "v_cvt_pk_bf16_f32 v233, v234, v238\n\t" "ds_write2_b32 %[vw], v232, v233 offset1:68\n\t" "s_waitcnt lgkmcnt(0)\n\t" "s_barrier\n\t" XT(92, 93) \
    "ds_read_b128 v[240:243], %[vr] offset:0\n\t" "ds_read_b128 v[244:247], %[vr] offset:64\n\t" "ds_read_b128 v[248:251], %[vr] offset:128\n\t" \
    "ds_read_b128 v[252:255], %[vr] offset:192\n\t" "s_mov_b32 s61, %[ub]\n\t" "s_waitcnt lgkmcnt(3)\n\t" \
    "v_smfmac_f32_16x16x64_bf16 v[192:195], v[240:243], v[32:39], %[ix]\n\t" "v_smfmac_f32_16x16x64_bf16 v[196:199], v[240:243], v[48:55], %[ix]\n\t" \
    "s_waitcnt lgkmcnt(2)\n\t" "v_smfmac_f32_16x16x64_bf16 v[192:195], v[244:247], v[40:47], %[ix]\n\t" \
    "v_smfmac_f32_16x16x64_bf16 v[196:199], v[244:247], v[56:63], %[ix]\n\t" "s_waitcnt lgkmcnt(1)\n\t" \
    "v_smfmac_f32_16x16x64_bf16 v[192:195], v[248:251], a[192:199], %[ix]\n\t" "v_smfmac_f32_16x16x64_bf16 v[196:199], v[248:251], a[200:207], %[ix]\n\t" \
    "s_waitcnt lgkmcnt(0)\n\t" "v_smfmac_f32_16x16x64_bf16 v[192:195], v[252:255], v[160:167], %[ix]\n\t" "ds_read_b128 v[160:163], %[l0] offset:24576\n\t" \
    "ds_read_b128 v[164:167], %[l0] offset:25600\n\t" "v_smfmac_f32_16x16x64_bf16 v[196:199], v[252:255], v[168:175], %[ix]\n\t" \
    "ds_read_b128 v[168:171], %[l0] offset:26624\n\t" "ds_read_b128 v[172:175], %[l0] offset:27648\n\t" \
    "v_smfmac_f32_16x16x64_bf16 v[200:203], v[240:243], v[64:71], %[ix]\n\t" "s_nop 1\n\t" "v_smfmac_f32_16x16x64_bf16 v[204:207], v[240:243], v[80:87], %[ix]\n\t" \
    "v_cvt_pk_bf16_f32 v224, v192, v194\n\t" "v_cvt_pk_bf16_f32 v225, v193, v195\n\t" "v_smfmac_f32_16x16x64_bf16 v[200:203], v[244:247], v[72:79], %[ix]\n\t" \
    "v_cvt_pk_bf16_f32 v226, v196, v198\n\t" "v_cvt_pk_bf16_f32 v227, v197, v199\n\t" "v_smfmac_f32_16x16x64_bf16 v[204:207], v[244:247], v[88:95], %[ix]\n\t" "s_nop 0\n\t" \
    "v_permlane32_swap_b32 v224, v226\n\t" "v_smfmac_f32_16x16x64_bf16 v[200:203], v[248:251], a[208:215], %[ix]\n\t" "v_permlane32_swap_b32 v225, v227\n\t" "s_nop 0\n\t" \
    "v_smfmac_f32_16x16x64_bf16 v[204:207], v[248:251], a[216:223], %[ix]\n\t" "v_permlane16_swap_b32 v224, v225\n\t" "v_permlane16_swap_b32 v226, v227\n\t" \
    "v_smfmac_f32_16x16x64_bf16 v[200:203], v[252:255], v[176:183], %[ix]\n\t" "buffer_store_dwordx4 v[224:227], %[uo], %[rs], s61 offen sc1\n\t" \
    "s_add_u32 s61, s61, 0x10000\n\t" XT(94, 95) "v_smfmac_f32_16x16x64_bf16 v[204:207], v[252:255], v[184:191], %[ix]\n\t" "ds_read_b128 v[176:179], %[l0] offset:28672\n\t" \
    "ds_read_b128 v[180:183], %[l0] offset:29696\n\t" "v_smfmac_f32_16x16x64_bf16 v[208:211], v[240:243], v[96:103], %[ix]\n\t" \
    "ds_read_b128 v[184:187], %[l0] offset:30720\n\t" "ds_read_b128 v[188:191], %[l0] offset:31744\n\t" \
    "v_smfmac_f32_16x16x64_bf16 v[212:215], v[240:243], v[112:119], %[ix]\n\t" "v_cvt_pk_bf16_f32 v228, v200, v202\n\t" "v_cvt_pk_bf16_f32 v229, v201, v203\n\t" \
    "v_smfmac_f32_16x16x64_bf16 v[208:211], v[244:247], v[104:111], %[ix]\n\t" "v_cvt_pk_bf16_f32 v230, v204, v206\n\t" "v_cvt_pk_bf16_f32 v231, v205, v207\n\t" \
    "v_smfmac_f32_16x16x64_bf16 v[212:215], v[244:247], v[120:127], %[ix]\n\t" "s_nop 0\n\t" "v_permlane32_swap_b32 v228, v230\n\t" \
    "v_smfmac_f32_16x16x64_bf16 v[208:211], v[248:251], a[224:231], %[ix]\n\t" "v_permlane32_swap_b32 v229, v231\n\t" "s_nop 0\n\t" \
    "v_smfmac_f32_16x16x64_bf16 v[212:215], v[248:251], a[232:239], %[ix]\n\t" "v_permlane16_swap_b32 v228, v229\n\t" "v_permlane16_swap_b32 v230, v231\n\t" \
    "s_waitcnt lgkmcnt(6)\n\t" "v_smfmac_f32_16x16x64_bf16 v[208:211], v[252:255], v[160:167], %[ix]\n\t" "buffer_store_dwordx4 v[228:231], %[uo], %[rs], s61 offen sc1\n\t" \
    "s_add_u32 s61, s61, 0x10000\n\t" "s_waitcnt lgkmcnt(4)\n\t" "v_smfmac_f32_16x16x64_bf16 v[212:215], v[252:255], v[168:175], %[ix]\n\t" "s_nop 1\n\t" \
    "v_smfmac_f32_16x16x64_bf16 v[216:219], v[240:243], v[128:135], %[ix]\n\t" "s_nop 1\n\t" "v_smfmac_f32_16x16x64_bf16 v[220:223], v[240:243], v[144:151], %[ix]\n\t" \
    "v_cvt_pk_bf16_f32 v224, v208, v210\n\t" "v_cvt_pk_bf16_f32 v225, v209, v211\n\t" "v_smfmac_f32_16x16x64_bf16 v[216:219], v[244:247], v[136:143], %[ix]\n\t" \
    "v_cvt_pk_bf16_f32 v226, v212, v214\n\t" "v_cvt_pk_bf16_f32 v227, v213, v215\n\t" "v_smfmac_f32_16x16x64_bf16 v[220:223], v[244:247], v[152:159], %[ix]\n\t" "s_nop 0\n\t" \
    "v_permlane32_swap_b32 v224, v226\n\t" "v_smfmac_f32_16x16x64_bf16 v[216:219], v[248:251], a[240:247], %[ix]\n\t" "v_permlane32_swap_b32 v225, v227\n\t" "s_nop 0\n\t" \
    "v_smfmac_f32_16x16x64_bf16 v[220:223], v[248:251], a[248:255], %[ix]\n\t" "v_permlane16_swap_b32 v224, v225\n\t" "v_permlane16_swap_b32 v226, v227\n\t" \
    "s_waitcnt lgkmcnt(2)\n\t" "v_smfmac_f32_16x16x64_bf16 v[216:219], v[252:255], v[176:183], %[ix]\n\t" "buffer_store_dwordx4 v[224:227], %[uo], %[rs], s61 offen sc1\n\t" \
    "s_add_u32 s61, s61, 0x10000\n\t" "s_waitcnt lgkmcnt(0)\n\t" "v_smfmac_f32_16x16x64_bf16 v[220:223], v[252:255], v[184:191], %[ix]\n\t" "s_nop 5\n\t" \
    "v_cvt_pk_bf16_f32 v228, v216, v218\n\t" "v_cvt_pk_bf16_f32 v229, v217, v219\n\t" "v_cvt_pk_bf16_f32 v230, v220, v222\n\t" "v_cvt_pk_bf16_f32 v231, v221, v223\n\t" \
    "s_nop 0\n\t" "v_permlane32_swap_b32 v228, v230\n\t" "v_permlane32_swap_b32 v229, v231\n\t" "s_nop 1\n\t" "v_permlane16_swap_b32 v228, v229\n\t" \
    "v_permlane16_swap_b32 v230, v231\n\t" "buffer_store_dwordx4 v[228:231], %[uo], %[rs], s61 offen sc1\n\t" "s_nop 1\n\t" XT(96, 97) "s_branch 99f\n\t" "41:\n\t" \
    "s_cmp_eq_u32 %[bd], 0\n\t" "s_cbranch_scc1 46f\n\t" "s_sub_u32 %[bd], %[bd], 1\n\t" XCNT(100) "s_waitcnt lgkmcnt(0)\n\t" "s_nop 7\n\t" "s_branch 40b\n\t" "46:\n\t" \
    "s_cmp_lg_u32 s60, 0\n\t" "s_branch 42b\n\t" "44:\n\t" "s_cmp_eq_u32 %[bd], 0\n\t" "s_cbranch_scc1 45b\n\t" "s_sub_u32 %[bd], %[bd], 1\n\t" XCNT(101) \
    "s_mov_b32 s60, 1\n\t" "s_waitcnt lgkmcnt(0)\n\t" "s_nop 7\n\t" "s_branch 40b\n\t" "99:\n\t"
#define Y_STEP \
    "s_getpc_b64 s[62:63]\n\t" "11:\n\t" "s_memtime s[64:65]\n\t" "s_waitcnt lgkmcnt(0)\n\t" "s_sub_u32 s61, %[tu], s64\n\t" "s_cmp_gt_i32 s61, 124\n\t" \
    "s_cbranch_scc1 11b\n\t" "s_max_i32 s61, s61, 0\n\t" "s_and_b32 s61, s61, 124\n\t" "s_sub_u32 s61, 12f-11b, s61\n\t" "s_add_u32 s62, s62, s61\n\t" \
    "s_addc_u32 s63, s63, 0\n\t" "s_setpc_b64 s[62:63]\n\t" ".rept 31\n\t" "s_nop 0\n\t" ".endr\n\t" "12:\n\t" XT(90, 91) \
    "buffer_load_dwordx4 v[144:147], %[u0], %[rs], %[ub] offen offset:0 sc1\n\t" "buffer_load_dwordx4 v[148:151], %[u0], %[rs], %[ub] offen offset:1024 sc1\n\t" \
    "buffer_load_dwordx4 v[152:155], %[u0], %[rs], %[ub] offen offset:2048 sc1\n\t" "buffer_load_dwordx4 v[156:159], %[u0], %[rs], %[ub] offen offset:3072 sc1\n\t" \
    "buffer_load_dwordx4 v[160:163], %[u1], %[rs], %[ub] offen offset:0 sc1\n\t" "buffer_load_dwordx4 v[164:167], %[u1], %[rs], %[ub] offen offset:1024 sc1\n\t" \
    "buffer_load_dwordx4 v[168:171], %[u1], %[rs], %[ub] offen offset:2048 sc1\n\t" "buffer_load_dwordx4 v[172:175], %[u1], %[rs], %[ub] offen offset:3072 sc1\n\t" \
    "s_cmp_lg_u32 %[late], 0\n\t" "s_cbranch_scc1 14f\n\t" "s_getpc_b64 s[62:63]\n\t" "13:\n\t" "s_memtime s[64:65]\n\t" "s_waitcnt lgkmcnt(0)\n\t" \
    "s_sub_u32 s61, %[tg], s64\n\t" "s_cmp_gt_i32 s61, 124\n\t" "s_cbranch_scc1 13b\n\t" "s_max_i32 s61, s61, 0\n\t" "s_and_b32 s61, s61, 124\n\t" \
    "s_sub_u32 s61, 14f-13b, s61\n\t" "s_add_u32 s62, s62, s61\n\t" "s_addc_u32 s63, s63, 0\n\t" "s_setpc_b64 s[62:63]\n\t" ".rept 31\n\t" "s_nop 0\n\t" ".endr\n\t" "14:\n\t" \
    XT(92, 93) "20:\n\t" "buffer_load_dwordx4 v[192:195], %[x0], %[rs], %[pb] offen offset:0 sc1\n\t" \
    "buffer_load_dwordx4 v[196:199], %[x0], %[rs], %[pb] offen offset:1024 sc1\n\t" "buffer_load_dwordx4 v[200:203], %[x0], %[rs], %[pb] offen offset:2048 sc1\n\t" \
    "buffer_load_dwordx4 v[204:207], %[x0], %[rs], %[pb] offen offset:3072 sc1\n\t" "buffer_load_dwordx4 v[208:211], %[x1], %[rs], %[pb] offen offset:0 sc1\n\t" \
    "buffer_load_dwordx4 v[212:215], %[x1], %[rs], %[pb] offen offset:1024 sc1\n\t" "buffer_load_dwordx4 v[216:219], %[x1], %[rs], %[pb] offen offset:2048 sc1\n\t" \
    "buffer_load_dwordx4 v[220:223], %[x1], %[rs], %[pb] offen offset:3072 sc1\n\t" "buffer_load_dwordx4 v[224:227], %[x2], %[rs], %[pb] offen offset:0 sc1\n\t" \
    "buffer_load_dwordx4 v[228:231], %[x2], %[rs], %[pb] offen offset:1024 sc1\n\t" "buffer_load_dwordx4 v[232:235], %[x2], %[rs], %[pb] offen offset:2048 sc1\n\t" \
    "buffer_load_dwordx4 v[236:239], %[x2], %[rs], %[pb] offen offset:3072 sc1\n\t" "buffer_load_dwordx4 v[240:243], %[x3], %[rs], %[pb] offen offset:0 sc1\n\t" \
    "buffer_load_dwordx4 v[244:247], %[x3], %[rs], %[pb] offen offset:1024 sc1\n\t" "buffer_load_dwordx4 v[248:251], %[x3], %[rs], %[pb] offen offset:2048 sc1\n\t" \
    "buffer_load_dwordx4 v[252:255], %[x3], %[rs], %[pb] offen offset:3072 sc1\n\t" "v_mov_b64 v[120:121], 0\n\t" "v_mov_b64 v[122:123], 0\n\t" \
    "buffer_load_dwordx2 v[176:177], %[pm], %[rs], %[pb] offen offset:0 sc1\n\t" "buffer_load_dwordx2 v[178:179], %[pm], %[rs], %[pb] offen offset:128 sc1\n\t" \
    "buffer_load_dwordx2 v[180:181], %[pm], %[rs], %[pb] offen offset:1024 sc1\n\t" "buffer_load_dwordx2 v[182:183], %[pm], %[rs], %[pb] offen offset:1152 sc1\n\t" \
    "buffer_load_dwordx2 v[184:185], %[pm], %[rs], %[pb] offen offset:2048 sc1\n\t" "buffer_load_dwordx2 v[186:187], %[pm], %[rs], %[pb] offen offset:2176 sc1\n\t" \
    "buffer_load_dwordx2 v[188:189], %[pm], %[rs], %[pb] offen offset:3072 sc1\n\t" "buffer_load_dwordx2 v[190:191], %[pm], %[rs], %[pb] offen offset:3200 sc1\n\t" \
    "buffer_load_dword %[sv], %[so], %[rs], %[pb] offen sc1\n\t" "v_mov_b32 v127, -1\n\t" "s_waitcnt vmcnt(24)\n\t" XT(94, 95) \
    "v_smfmac_f32_16x16x64_bf16 v[120:123], v[192:195], a[0:7], %[ix]\n\t" "s_waitcnt vmcnt(23)\n\t" "v_smfmac_f32_16x16x64_bf16 v[120:123], v[196:199], a[8:15], %[ix]\n\t" \
    "s_waitcnt vmcnt(22)\n\t" "v_smfmac_f32_16x16x64_bf16 v[120:123], v[200:203], a[16:23], %[ix]\n\t" "s_waitcnt vmcnt(21)\n\t" \
    "v_smfmac_f32_16x16x64_bf16 v[120:123], v[204:207], a[24:31], %[ix]\n\t" "s_waitcnt vmcnt(20)\n\t" \
    "v_smfmac_f32_16x16x64_bf16 v[120:123], v[208:211], a[32:39], %[ix]\n\t" "s_waitcnt vmcnt(19)\n\t" \
    "v_smfmac_f32_16x16x64_bf16 v[120:123], v[212:215], a[40:47], %[ix]\n\t" "s_waitcnt vmcnt(18)\n\t" \
    "v_smfmac_f32_16x16x64_bf16 v[120:123], v[216:219], a[48:55], %[ix]\n\t" "s_waitcnt vmcnt(17)\n\t" \
    "v_smfmac_f32_16x16x64_bf16 v[120:123], v[220:223], a[56:63], %[ix]\n\t" "s_waitcnt vmcnt(16)\n\t" \
    "v_smfmac_f32_16x16x64_bf16 v[120:123], v[224:227], a[64:71], %[ix]\n\t" "s_waitcnt vmcnt(15)\n\t" \
    "v_smfmac_f32_16x16x64_bf16 v[120:123], v[228:231], a[72:79], %[ix]\n\t" "s_waitcnt vmcnt(14)\n\t" \
    "v_smfmac_f32_16x16x64_bf16 v[120:123], v[232:235], a[80:87], %[ix]\n\t" "s_waitcnt vmcnt(13)\n\t" \
    "v_smfmac_f32_16x16x64_bf16 v[120:123], v[236:239], a[88:95], %[ix]\n\t" "s_waitcnt vmcnt(12)\n\t" \
    "v_smfmac_f32_16x16x64_bf16 v[120:123], v[240:243], a[96:103], %[ix]\n\t" "s_waitcnt vmcnt(11)\n\t" \
    "v_smfmac_f32_16x16x64_bf16 v[120:123], v[244:247], a[104:111], %[ix]\n\t" "s_waitcnt vmcnt(10)\n\t" \
    "v_smfmac_f32_16x16x64_bf16 v[120:123], v[248:251], a[112:119], %[ix]\n\t" "s_waitcnt vmcnt(9)\n\t" \
    "v_smfmac_f32_16x16x64_bf16 v[120:123], v[252:255], a[120:127], %[ix]\n\t" "s_waitcnt vmcnt(7)\n\t" \
    "v_smfmac_f32_16x16x64_bf16 v[120:123], v[176:179], v[144:151], %[ix]\n\t" "s_waitcnt vmcnt(5)\n\t" \
    "v_smfmac_f32_16x16x64_bf16 v[120:123], v[180:183], v[152:159], %[ix]\n\t" "s_waitcnt vmcnt(3)\n\t" \
    "v_smfmac_f32_16x16x64_bf16 v[120:123], v[184:187], v[160:167], %[ix]\n\t" "s_waitcnt vmcnt(1)\n\t" \
    "v_smfmac_f32_16x16x64_bf16 v[120:123], v[188:191], v[168:175], %[ix]\n\t" XT(82, 83) "s_nop 7\n\t" "v_add_f32 v120, v120, v121\n\t" "v_add_f32 v122, v122, v123\n\t" \
    "v_cvt_pk_bf16_f32 v124, v120, v122\n\t" "v_cmp_u_f32 vcc, v120, v122\n\t" "s_cbranch_vccnz 30f\n\t" "21:\n\t" \
    "v_mov_b32_dpp v125, v124 quad_perm:[1,0,3,2] row_mask:0xf bank_mask:0xf\n\t" "v_perm_b32 v124, v125, v124, %[ps]\n\t" "v_min_u32 v124, -2, v124\n\t" \
    "buffer_store_dword v124, %[mw], %[rs], %[mb] offen sc1\n\t" XT(84, 85) "buffer_store_dword v127, %[mw], %[rs], %[mr] offen sc1\n\t" "s_memtime s[64:65]\n\t" \
    "s_barrier\n\t" XT(98, 99) "s_waitcnt lgkmcnt(0)\n\t" "s_and_b32 %[bad], s64, -2\n\t" "s_branch 40f\n\t" "30:\n\t" "v_max_u32 v126, v144, v145\n\t" \
    "v_max3_u32 v126, v126, v146, v147\n\t" "v_max3_u32 v126, v126, v148, v149\n\t" "v_max3_u32 v126, v126, v150, v151\n\t" "v_max3_u32 v126, v126, v152, v153\n\t" \
    "v_max3_u32 v126, v126, v154, v155\n\t" "v_max3_u32 v126, v126, v156, v157\n\t" "v_max3_u32 v126, v126, v158, v159\n\t" "v_max3_u32 v126, v126, v160, v161\n\t" \
    "v_max3_u32 v126, v126, v162, v163\n\t" "v_max3_u32 v126, v126, v164, v165\n\t" "v_max3_u32 v126, v126, v166, v167\n\t" "v_max3_u32 v126, v126, v168, v169\n\t" \
    "v_max3_u32 v126, v126, v170, v171\n\t" "v_max3_u32 v126, v126, v172, v173\n\t" "v_max3_u32 v126, v126, v174, v175\n\t" UCNT "v_max3_u32 v126, v126, v176, v177\n\t" \
    "v_max3_u32 v126, v126, v178, v179\n\t" "v_max3_u32 v126, v126, v180, v181\n\t" "v_max3_u32 v126, v126, v182, v183\n\t" "v_max3_u32 v126, v126, v184, v185\n\t" \
    "v_max3_u32 v126, v126, v186, v187\n\t" "v_max3_u32 v126, v126, v188, v189\n\t" "v_max3_u32 v126, v126, v190, v191\n\t" "v_max3_u32 v126, v126, v192, v193\n\t" \
    "v_max3_u32 v126, v126, v194, v195\n\t" "v_max3_u32 v126, v126, v196, v197\n\t" "v_max3_u32 v126, v126, v198, v199\n\t" "v_max3_u32 v126, v126, v200, v201\n\t" \
    "v_max3_u32 v126, v126, v202, v203\n\t" "v_max3_u32 v126, v126, v204, v205\n\t" "v_max3_u32 v126, v126, v206, v207\n\t" "v_max3_u32 v126, v126, v208, v209\n\t" \
    "v_max3_u32 v126, v126, v210, v211\n\t" "v_max3_u32 v126, v126, v212, v213\n\t" "v_max3_u32 v126, v126, v214, v215\n\t" "v_max3_u32 v126, v126, v216, v217\n\t" \
    "v_max3_u32 v126, v126, v218, v219\n\t" "v_max3_u32 v126, v126, v220, v221\n\t" "v_max3_u32 v126, v126, v222, v223\n\t" "v_max3_u32 v126, v126, v224, v225\n\t" \
    "v_max3_u32 v126, v126, v226, v227\n\t" "v_max3_u32 v126, v126, v228, v229\n\t" "v_max3_u32 v126, v126, v230, v231\n\t" "v_max3_u32 v126, v126, v232, v233\n\t" \
    "v_max3_u32 v126, v126, v234, v235\n\t" "v_max3_u32 v126, v126, v236, v237\n\t" "v_max3_u32 v126, v126, v238, v239\n\t" "v_max3_u32 v126, v126, v240, v241\n\t" \
    "v_max3_u32 v126, v126, v242, v243\n\t" "v_max3_u32 v126, v126, v244, v245\n\t" "v_max3_u32 v126, v126, v246, v247\n\t" "v_max3_u32 v126, v126, v248, v249\n\t" \
    "v_max3_u32 v126, v126, v250, v251\n\t" "v_max3_u32 v126, v126, v252, v253\n\t" "v_max3_u32 v126, v126, v254, v255\n\t" "v_cmp_eq_u32 vcc, -1, v126\n\t" \
    "s_cbranch_vccz 21b\n\t" "s_cmp_eq_u32 %[bd], 0\n\t" "s_cbranch_scc1 21b\n\t" XCNT(100) "s_mov_b32 %[bad], 1\n\t" "s_waitcnt vmcnt(0)\n\t" "40:\n\t"
#define G_LOOP \
    "v_xor_b32 v72, 0, %[xo]\n\t" "v_add_u32 v80, 0x10000, v72\n\t" "v_xor_b32 v88, 0, %[xs]\n\t" "v_add_u32 v88, 0, v88\n\t" "v_xor_b32 v73, 32, %[xo]\n\t" \
    "v_add_u32 v81, 0x10000, v73\n\t" "v_xor_b32 v89, 16, %[xs]\n\t" "v_add_u32 v89, 2048, v89\n\t" "v_xor_b32 v74, 64, %[xo]\n\t" "v_add_u32 v82, 0x10000, v74\n\t" \
    "v_xor_b32 v90, 32, %[xs]\n\t" "v_add_u32 v90, 4096, v90\n\t" "v_xor_b32 v75, 96, %[xo]\n\t" "v_add_u32 v83, 0x10000, v75\n\t" "v_xor_b32 v91, 48, %[xs]\n\t" \
    "v_add_u32 v91, 6144, v91\n\t" "v_xor_b32 v76, 128, %[xo]\n\t" "v_add_u32 v84, 0x10000, v76\n\t" "v_xor_b32 v92, 64, %[xs]\n\t" "v_add_u32 v92, 8192, v92\n\t" \
    "v_xor_b32 v77, 160, %[xo]\n\t" "v_add_u32 v85, 0x10000, v77\n\t" "v_xor_b32 v93, 80, %[xs]\n\t" "v_add_u32 v93, 10240, v93\n\t" "v_xor_b32 v78, 192, %[xo]\n\t" \
    "v_add_u32 v86, 0x10000, v78\n\t" "v_xor_b32 v94, 96, %[xs]\n\t" "v_add_u32 v94, 12288, v94\n\t" "v_xor_b32 v79, 224, %[xo]\n\t" "v_add_u32 v87, 0x10000, v79\n\t" \
    "v_xor_b32 v95, 112, %[xs]\n\t" "v_add_u32 v95, 14336, v95\n\t" "v_mov_b32 v96, 2.0\n\t" "s_mov_b32 s60, 0\n\t" "s_mov_b32 s62, 0\n\t" "s_waitcnt vmcnt(0)\n\t" \
    "s_barrier\n\t" "ds_read_b128 v[100:103], v72 offset:0\n\t" "ds_read_b128 v[104:107], v73 offset:0\n\t" "ds_read_b128 v[108:111], v74 offset:0\n\t" \
    "ds_read_b128 v[112:115], v75 offset:0\n\t" "ds_read_b128 v[116:119], v76 offset:0\n\t" "10:\n\t" XT(80, 81) "s_waitcnt lgkmcnt(4)\n\t" \
    "v_mfma_f32_32x32x16_bf16 v[32:47], a[0:3], v[100:103], 0\n\t" "ds_read_b128 v[120:123], v77 offset:0\n\t" "s_add_u32 s61, s60, 1\n\t" "s_and_b32 s61, s61, 63\n\t" \
    "s_lshl_b32 s61, s61, 16\n\t" "s_add_u32 s63, %[lw], 65536\n\t" "s_waitcnt lgkmcnt(4)\n\t" "v_mfma_f32_32x32x16_bf16 v[32:47], a[4:7], v[104:107], v[32:47]\n\t" \
    "ds_read_b128 v[124:127], v78 offset:0\n\t" "s_waitcnt lgkmcnt(4)\n\t" "v_mfma_f32_32x32x16_bf16 v[32:47], a[8:11], v[108:111], v[32:47]\n\t" \
    "ds_read_b128 v[128:131], v79 offset:0\n\t" "v_cvt_pk_bf16_f32 v64, v48, v49\n\t" "v_cvt_pk_bf16_f32 v65, v50, v51\n\t" "v_cvt_pk_bf16_f32 v66, v52, v53\n\t" \
    "s_waitcnt lgkmcnt(4)\n\t" "v_mfma_f32_32x32x16_bf16 v[32:47], a[12:15], v[112:115], v[32:47]\n\t" "ds_read_b128 v[100:103], v72 offset:256\n\t" \
    "v_cvt_pk_bf16_f32 v67, v54, v55\n\t" "v_cvt_pk_bf16_f32 v68, v56, v57\n\t" "v_cvt_pk_bf16_f32 v69, v58, v59\n\t" "s_waitcnt lgkmcnt(4)\n\t" \
    "v_mfma_f32_32x32x16_bf16 v[32:47], a[16:19], v[116:119], v[32:47]\n\t" "ds_read_b128 v[104:107], v73 offset:256\n\t" "v_cvt_pk_bf16_f32 v70, v60, v61\n\t" \
    "v_cvt_pk_bf16_f32 v71, v62, v63\n\t" "buffer_store_dwordx4 v[64:67], v96, %[rs], s62 offen offset:0 sc1\n\t" "s_waitcnt lgkmcnt(4)\n\t" \
    "v_mfma_f32_32x32x16_bf16 v[32:47], a[20:23], v[120:123], v[32:47]\n\t" "ds_read_b128 v[108:111], v74 offset:256\n\t" "s_waitcnt lgkmcnt(4)\n\t" \
    "v_mfma_f32_32x32x16_bf16 v[32:47], a[24:27], v[124:127], v[32:47]\n\t" "ds_read_b128 v[112:115], v75 offset:256\n\t" \
    "buffer_store_dwordx4 v[68:71], v96, %[rs], s62 offen offset:1024 sc1\n\t" "s_waitcnt lgkmcnt(4)\n\t" \
    "v_mfma_f32_32x32x16_bf16 v[32:47], a[28:31], v[128:131], v[32:47]\n\t" "ds_read_b128 v[116:119], v76 offset:256\n\t" "s_mov_b32 m0, s63\n\t" \
    "s_add_u32 s63, s63, 0x800\n\t" "buffer_load_dwordx4 v88, %[rx], s61 offen lds\n\t" "s_waitcnt lgkmcnt(4)\n\t" \
    "v_mfma_f32_32x32x16_bf16 v[32:47], a[32:35], v[100:103], v[32:47]\n\t" "ds_read_b128 v[120:123], v77 offset:256\n\t" \
    "buffer_load_dwordx4 v88, %[rx], s61 offen offset:1024 lds\n\t" "s_waitcnt lgkmcnt(4)\n\t" "v_mfma_f32_32x32x16_bf16 v[32:47], a[36:39], v[104:107], v[32:47]\n\t" \
    "ds_read_b128 v[124:127], v78 offset:256\n\t" "s_mov_b32 m0, s63\n\t" "s_add_u32 s63, s63, 0x800\n\t" "buffer_load_dwordx4 v89, %[rx], s61 offen lds\n\t" \
    "s_waitcnt lgkmcnt(4)\n\t" "v_mfma_f32_32x32x16_bf16 v[32:47], a[40:43], v[108:111], v[32:47]\n\t" "ds_read_b128 v[128:131], v79 offset:256\n\t" \
    "buffer_load_dwordx4 v89, %[rx], s61 offen offset:1024 lds\n\t" "s_waitcnt lgkmcnt(4)\n\t" "v_mfma_f32_32x32x16_bf16 v[32:47], a[44:47], v[112:115], v[32:47]\n\t" \
    "ds_read_b128 v[100:103], v72 offset:512\n\t" "s_mov_b32 m0, s63\n\t" "s_add_u32 s63, s63, 0x800\n\t" "buffer_load_dwordx4 v90, %[rx], s61 offen lds\n\t" \
    "s_waitcnt lgkmcnt(4)\n\t" "v_mfma_f32_32x32x16_bf16 v[32:47], a[48:51], v[116:119], v[32:47]\n\t" "ds_read_b128 v[104:107], v73 offset:512\n\t" \
    "buffer_load_dwordx4 v90, %[rx], s61 offen offset:1024 lds\n\t" "s_waitcnt lgkmcnt(4)\n\t" "v_mfma_f32_32x32x16_bf16 v[32:47], a[52:55], v[120:123], v[32:47]\n\t" \
    "ds_read_b128 v[108:111], v74 offset:512\n\t" "s_mov_b32 m0, s63\n\t" "s_add_u32 s63, s63, 0x800\n\t" "buffer_load_dwordx4 v91, %[rx], s61 offen lds\n\t" \
    "s_waitcnt lgkmcnt(4)\n\t" "v_mfma_f32_32x32x16_bf16 v[32:47], a[56:59], v[124:127], v[32:47]\n\t" "ds_read_b128 v[112:115], v75 offset:512\n\t" \
    "buffer_load_dwordx4 v91, %[rx], s61 offen offset:1024 lds\n\t" "s_waitcnt lgkmcnt(4)\n\t" "v_mfma_f32_32x32x16_bf16 v[32:47], a[60:63], v[128:131], v[32:47]\n\t" \
    "ds_read_b128 v[116:119], v76 offset:512\n\t" "s_mov_b32 m0, s63\n\t" "s_add_u32 s63, s63, 0x800\n\t" "buffer_load_dwordx4 v92, %[rx], s61 offen lds\n\t" \
    "s_waitcnt lgkmcnt(4)\n\t" "v_mfma_f32_32x32x16_bf16 v[32:47], a[64:67], v[100:103], v[32:47]\n\t" "ds_read_b128 v[120:123], v77 offset:512\n\t" \
    "buffer_load_dwordx4 v92, %[rx], s61 offen offset:1024 lds\n\t" "s_waitcnt lgkmcnt(4)\n\t" "v_mfma_f32_32x32x16_bf16 v[32:47], a[68:71], v[104:107], v[32:47]\n\t" \
    "ds_read_b128 v[124:127], v78 offset:512\n\t" "s_mov_b32 m0, s63\n\t" "s_add_u32 s63, s63, 0x800\n\t" "buffer_load_dwordx4 v93, %[rx], s61 offen lds\n\t" \
    "s_waitcnt lgkmcnt(4)\n\t" "v_mfma_f32_32x32x16_bf16 v[32:47], a[72:75], v[108:111], v[32:47]\n\t" "ds_read_b128 v[128:131], v79 offset:512\n\t" \
    "buffer_load_dwordx4 v93, %[rx], s61 offen offset:1024 lds\n\t" "s_waitcnt lgkmcnt(4)\n\t" "v_mfma_f32_32x32x16_bf16 v[32:47], a[76:79], v[112:115], v[32:47]\n\t" \
    "ds_read_b128 v[100:103], v72 offset:768\n\t" "s_mov_b32 m0, s63\n\t" "s_add_u32 s63, s63, 0x800\n\t" "buffer_load_dwordx4 v94, %[rx], s61 offen lds\n\t" \
    "s_waitcnt lgkmcnt(4)\n\t" "v_mfma_f32_32x32x16_bf16 v[32:47], a[80:83], v[116:119], v[32:47]\n\t" "ds_read_b128 v[104:107], v73 offset:768\n\t" \
    "buffer_load_dwordx4 v94, %[rx], s61 offen offset:1024 lds\n\t" "s_waitcnt lgkmcnt(4)\n\t" "v_mfma_f32_32x32x16_bf16 v[32:47], a[84:87], v[120:123], v[32:47]\n\t" \
    "ds_read_b128 v[108:111], v74 offset:768\n\t" "s_mov_b32 m0, s63\n\t" "s_add_u32 s63, s63, 0x800\n\t" "buffer_load_dwordx4 v95, %[rx], s61 offen lds\n\t" \
    "s_waitcnt lgkmcnt(4)\n\t" "v_mfma_f32_32x32x16_bf16 v[32:47], a[88:91], v[124:127], v[32:47]\n\t" "ds_read_b128 v[112:115], v75 offset:768\n\t" \
    "buffer_load_dwordx4 v95, %[rx], s61 offen offset:1024 lds\n\t" "s_waitcnt lgkmcnt(4)\n\t" "v_mfma_f32_32x32x16_bf16 v[32:47], a[92:95], v[128:131], v[32:47]\n\t" \
    "ds_read_b128 v[116:119], v76 offset:768\n\t" "s_waitcnt lgkmcnt(4)\n\t" "v_mfma_f32_32x32x16_bf16 v[32:47], a[96:99], v[100:103], v[32:47]\n\t" \
    "ds_read_b128 v[120:123], v77 offset:768\n\t" "s_lshr_b32 s64, s60, 2\n\t" "s_xor_b32 s64, s64, %[xp]\n\t" "s_and_b32 s64, s64, 1\n\t" "s_lshl_b32 s64, s64, 13\n\t" \
    "s_waitcnt lgkmcnt(4)\n\t" "v_mfma_f32_32x32x16_bf16 v[32:47], a[100:103], v[104:107], v[32:47]\n\t" "ds_read_b128 v[124:127], v78 offset:768\n\t" \
    "s_lshl_b32 s62, s60, 19\n\t" "s_add_u32 s62, s62, s64\n\t" "s_add_u32 s62, s62, %[ob]\n\t" "s_waitcnt lgkmcnt(4)\n\t" \
    "v_mfma_f32_32x32x16_bf16 v[32:47], a[104:107], v[108:111], v[32:47]\n\t" "ds_read_b128 v[128:131], v79 offset:768\n\t" "s_waitcnt lgkmcnt(4)\n\t" \
    "v_mfma_f32_32x32x16_bf16 v[32:47], a[108:111], v[112:115], v[32:47]\n\t" "ds_read_b128 v[100:103], v72 offset:1024\n\t" "s_waitcnt lgkmcnt(4)\n\t" \
    "v_mfma_f32_32x32x16_bf16 v[32:47], a[112:115], v[116:119], v[32:47]\n\t" "ds_read_b128 v[104:107], v73 offset:1024\n\t" "s_waitcnt lgkmcnt(4)\n\t" \
    "v_mfma_f32_32x32x16_bf16 v[32:47], a[116:119], v[120:123], v[32:47]\n\t" "ds_read_b128 v[108:111], v74 offset:1024\n\t" "s_waitcnt lgkmcnt(4)\n\t" \
    "v_mfma_f32_32x32x16_bf16 v[32:47], a[120:123], v[124:127], v[32:47]\n\t" "ds_read_b128 v[112:115], v75 offset:1024\n\t" "s_waitcnt lgkmcnt(4)\n\t" \
    "v_mfma_f32_32x32x16_bf16 v[32:47], a[124:127], v[128:131], v[32:47]\n\t" "ds_read_b128 v[116:119], v76 offset:1024\n\t" "s_waitcnt lgkmcnt(4)\n\t" \
    "v_mfma_f32_32x32x16_bf16 v[32:47], a[128:131], v[100:103], v[32:47]\n\t" "ds_read_b128 v[120:123], v77 offset:1024\n\t" "s_waitcnt lgkmcnt(4)\n\t" \
    "v_mfma_f32_32x32x16_bf16 v[32:47], a[132:135], v[104:107], v[32:47]\n\t" "ds_read_b128 v[124:127], v78 offset:1024\n\t" "s_waitcnt lgkmcnt(4)\n\t" \
    "v_mfma_f32_32x32x16_bf16 v[32:47], a[136:139], v[108:111], v[32:47]\n\t" "ds_read_b128 v[128:131], v79 offset:1024\n\t" "s_waitcnt vmcnt(16)\n\t" "s_barrier\n\t" \
    "v_mov_b32 v98, s60\n\t" "buffer_store_dword v98, %[fo], %[rc], %[fl] offen sc1\n\t" "s_waitcnt lgkmcnt(4)\n\t" \
    "v_mfma_f32_32x32x16_bf16 v[32:47], a[140:143], v[112:115], v[32:47]\n\t" "ds_read_b128 v[100:103], v72 offset:1280\n\t" "s_waitcnt lgkmcnt(4)\n\t" \
    "v_mfma_f32_32x32x16_bf16 v[32:47], a[144:147], v[116:119], v[32:47]\n\t" "ds_read_b128 v[104:107], v73 offset:1280\n\t" "s_waitcnt lgkmcnt(4)\n\t" \
    "v_mfma_f32_32x32x16_bf16 v[32:47], a[148:151], v[120:123], v[32:47]\n\t" "ds_read_b128 v[108:111], v74 offset:1280\n\t" "s_waitcnt lgkmcnt(4)\n\t" \
    "v_mfma_f32_32x32x16_bf16 v[32:47], a[152:155], v[124:127], v[32:47]\n\t" "ds_read_b128 v[112:115], v75 offset:1280\n\t" "s_waitcnt lgkmcnt(4)\n\t" \
    "v_mfma_f32_32x32x16_bf16 v[32:47], a[156:159], v[128:131], v[32:47]\n\t" "ds_read_b128 v[116:119], v76 offset:1280\n\t" "s_waitcnt lgkmcnt(4)\n\t" \
    "v_mfma_f32_32x32x16_bf16 v[32:47], a[160:163], v[100:103], v[32:47]\n\t" "ds_read_b128 v[120:123], v77 offset:1280\n\t" "s_waitcnt lgkmcnt(4)\n\t" \
    "v_mfma_f32_32x32x16_bf16 v[32:47], a[164:167], v[104:107], v[32:47]\n\t" "ds_read_b128 v[124:127], v78 offset:1280\n\t" "s_waitcnt lgkmcnt(4)\n\t" \
    "v_mfma_f32_32x32x16_bf16 v[32:47], a[168:171], v[108:111], v[32:47]\n\t" "ds_read_b128 v[128:131], v79 offset:1280\n\t" "s_waitcnt lgkmcnt(4)\n\t" \
    "v_mfma_f32_32x32x16_bf16 v[32:47], a[172:175], v[112:115], v[32:47]\n\t" "ds_read_b128 v[100:103], v72 offset:1536\n\t" "s_waitcnt lgkmcnt(4)\n\t" \
    "v_mfma_f32_32x32x16_bf16 v[32:47], a[176:179], v[116:119], v[32:47]\n\t" "ds_read_b128 v[104:107], v73 offset:1536\n\t" "s_waitcnt lgkmcnt(4)\n\t" \
    "v_mfma_f32_32x32x16_bf16 v[32:47], a[180:183], v[120:123], v[32:47]\n\t" "ds_read_b128 v[108:111], v74 offset:1536\n\t" "s_waitcnt lgkmcnt(4)\n\t" \
    "v_mfma_f32_32x32x16_bf16 v[32:47], a[184:187], v[124:127], v[32:47]\n\t" "ds_read_b128 v[112:115], v75 offset:1536\n\t" "s_waitcnt lgkmcnt(4)\n\t" \
    "v_mfma_f32_32x32x16_bf16 v[32:47], a[188:191], v[128:131], v[32:47]\n\t" "ds_read_b128 v[116:119], v76 offset:1536\n\t" "s_waitcnt lgkmcnt(4)\n\t" \
    "v_mfma_f32_32x32x16_bf16 v[32:47], a[192:195], v[100:103], v[32:47]\n\t" "ds_read_b128 v[120:123], v77 offset:1536\n\t" "s_waitcnt lgkmcnt(4)\n\t" \
    "v_mfma_f32_32x32x16_bf16 v[32:47], a[196:199], v[104:107], v[32:47]\n\t" "ds_read_b128 v[124:127], v78 offset:1536\n\t" "s_waitcnt lgkmcnt(4)\n\t" \
    "v_mfma_f32_32x32x16_bf16 v[32:47], a[200:203], v[108:111], v[32:47]\n\t" "ds_read_b128 v[128:131], v79 offset:1536\n\t" "s_waitcnt lgkmcnt(4)\n\t" \
    "v_mfma_f32_32x32x16_bf16 v[32:47], a[204:207], v[112:115], v[32:47]\n\t" "ds_read_b128 v[100:103], v72 offset:1792\n\t" "s_waitcnt lgkmcnt(4)\n\t" \
    "v_mfma_f32_32x32x16_bf16 v[32:47], a[208:211], v[116:119], v[32:47]\n\t" "ds_read_b128 v[104:107], v73 offset:1792\n\t" "s_waitcnt lgkmcnt(4)\n\t" \
    "v_mfma_f32_32x32x16_bf16 v[32:47], a[212:215], v[120:123], v[32:47]\n\t" "ds_read_b128 v[108:111], v74 offset:1792\n\t" "s_waitcnt lgkmcnt(4)\n\t" \
    "v_mfma_f32_32x32x16_bf16 v[32:47], a[216:219], v[124:127], v[32:47]\n\t" "ds_read_b128 v[112:115], v75 offset:1792\n\t" "s_waitcnt lgkmcnt(4)\n\t" \
    "v_mfma_f32_32x32x16_bf16 v[32:47], a[220:223], v[128:131], v[32:47]\n\t" "ds_read_b128 v[116:119], v76 offset:1792\n\t" "s_waitcnt lgkmcnt(4)\n\t" \
    "v_mfma_f32_32x32x16_bf16 v[32:47], a[224:227], v[100:103], v[32:47]\n\t" "ds_read_b128 v[120:123], v77 offset:1792\n\t" "s_waitcnt lgkmcnt(4)\n\t" \
    "v_mfma_f32_32x32x16_bf16 v[32:47], a[228:231], v[104:107], v[32:47]\n\t" "ds_read_b128 v[124:127], v78 offset:1792\n\t" "s_waitcnt lgkmcnt(4)\n\t" \
    "v_mfma_f32_32x32x16_bf16 v[32:47], a[232:235], v[108:111], v[32:47]\n\t" "ds_read_b128 v[128:131], v79 offset:1792\n\t" "s_waitcnt lgkmcnt(4)\n\t" \
    "v_mfma_f32_32x32x16_bf16 v[32:47], a[236:239], v[112:115], v[32:47]\n\t" "s_waitcnt lgkmcnt(3)\n\t" \
    "v_mfma_f32_32x32x16_bf16 v[32:47], a[240:243], v[116:119], v[32:47]\n\t" "s_waitcnt vmcnt(1) lgkmcnt(0)\n\t" "s_barrier\n\t" XT(82, 83) \
    "v_mfma_f32_32x32x16_bf16 v[32:47], a[244:247], v[120:123], v[32:47]\n\t" "ds_read_b128 v[100:103], v80 offset:0\n\t" \
    "v_mfma_f32_32x32x16_bf16 v[32:47], a[248:251], v[124:127], v[32:47]\n\t" "ds_read_b128 v[104:107], v81 offset:0\n\t" "ds_read_b128 v[108:111], v82 offset:0\n\t" \
    "v_mfma_f32_32x32x16_bf16 v[32:47], a[252:255], v[128:131], v[32:47]\n\t" "ds_read_b128 v[112:115], v83 offset:0\n\t" "ds_read_b128 v[116:119], v84 offset:0\n\t" \
    "s_add_u32 s60, s60, 1\n\t" GSUM "s_cmp_eq_u32 s60, 1\n\t" "s_cbranch_scc1 20f\n\t" "11:\n\t" XT(80, 81) "s_waitcnt lgkmcnt(4)\n\t" \
    "v_mfma_f32_32x32x16_bf16 v[48:63], a[0:3], v[100:103], 0\n\t" "ds_read_b128 v[120:123], v85 offset:0\n\t" "s_add_u32 s61, s60, 1\n\t" "s_and_b32 s61, s61, 63\n\t" \
    "s_lshl_b32 s61, s61, 16\n\t" "s_add_u32 s63, %[lw], 0\n\t" "s_waitcnt lgkmcnt(4)\n\t" "v_mfma_f32_32x32x16_bf16 v[48:63], a[4:7], v[104:107], v[48:63]\n\t" \
    "ds_read_b128 v[124:127], v86 offset:0\n\t" "s_waitcnt lgkmcnt(4)\n\t" "v_mfma_f32_32x32x16_bf16 v[48:63], a[8:11], v[108:111], v[48:63]\n\t" \
    "ds_read_b128 v[128:131], v87 offset:0\n\t" "v_cvt_pk_bf16_f32 v64, v32, v33\n\t" "v_cvt_pk_bf16_f32 v65, v34, v35\n\t" "v_cvt_pk_bf16_f32 v66, v36, v37\n\t" \
    "s_waitcnt lgkmcnt(4)\n\t" "v_mfma_f32_32x32x16_bf16 v[48:63], a[12:15], v[112:115], v[48:63]\n\t" "ds_read_b128 v[100:103], v80 offset:256\n\t" \
    "v_cvt_pk_bf16_f32 v67, v38, v39\n\t" "v_cvt_pk_bf16_f32 v68, v40, v41\n\t" "v_cvt_pk_bf16_f32 v69, v42, v43\n\t" "s_waitcnt lgkmcnt(4)\n\t" \
    "v_mfma_f32_32x32x16_bf16 v[48:63], a[16:19], v[116:119], v[48:63]\n\t" "ds_read_b128 v[104:107], v81 offset:256\n\t" "v_cvt_pk_bf16_f32 v70, v44, v45\n\t" \
    "v_cvt_pk_bf16_f32 v71, v46, v47\n\t" "buffer_store_dwordx4 v[64:67], v96, %[rs], s62 offen offset:0 sc1\n\t" "s_waitcnt lgkmcnt(4)\n\t" \
    "v_mfma_f32_32x32x16_bf16 v[48:63], a[20:23], v[120:123], v[48:63]\n\t" "ds_read_b128 v[108:111], v82 offset:256\n\t" "s_waitcnt lgkmcnt(4)\n\t" \
    "v_mfma_f32_32x32x16_bf16 v[48:63], a[24:27], v[124:127], v[48:63]\n\t" "ds_read_b128 v[112:115], v83 offset:256\n\t" \
    "buffer_store_dwordx4 v[68:71], v96, %[rs], s62 offen offset:1024 sc1\n\t" "s_waitcnt lgkmcnt(4)\n\t" \
    "v_mfma_f32_32x32x16_bf16 v[48:63], a[28:31], v[128:131], v[48:63]\n\t" "ds_read_b128 v[116:119], v84 offset:256\n\t" "s_mov_b32 m0, s63\n\t" \
    "s_add_u32 s63, s63, 0x800\n\t" "buffer_load_dwordx4 v88, %[rx], s61 offen lds\n\t" "s_waitcnt lgkmcnt(4)\n\t" \
    "v_mfma_f32_32x32x16_bf16 v[48:63], a[32:35], v[100:103], v[48:63]\n\t" "ds_read_b128 v[120:123], v85 offset:256\n\t" \
    "buffer_load_dwordx4 v88, %[rx], s61 offen offset:1024 lds\n\t" "s_waitcnt lgkmcnt(4)\n\t" "v_mfma_f32_32x32x16_bf16 v[48:63], a[36:39], v[104:107], v[48:63]\n\t" \
    "ds_read_b128 v[124:127], v86 offset:256\n\t" "s_mov_b32 m0, s63\n\t" "s_add_u32 s63, s63, 0x800\n\t" "buffer_load_dwordx4 v89, %[rx], s61 offen lds\n\t" \
    "s_waitcnt lgkmcnt(4)\n\t" "v_mfma_f32_32x32x16_bf16 v[48:63], a[40:43], v[108:111], v[48:63]\n\t" "ds_read_b128 v[128:131], v87 offset:256\n\t" \
    "buffer_load_dwordx4 v89, %[rx], s61 offen offset:1024 lds\n\t" "s_waitcnt lgkmcnt(4)\n\t" "v_mfma_f32_32x32x16_bf16 v[48:63], a[44:47], v[112:115], v[48:63]\n\t" \
    "ds_read_b128 v[100:103], v80 offset:512\n\t" "s_mov_b32 m0, s63\n\t" "s_add_u32 s63, s63, 0x800\n\t" "buffer_load_dwordx4 v90, %[rx], s61 offen lds\n\t" \
    "s_waitcnt lgkmcnt(4)\n\t" "v_mfma_f32_32x32x16_bf16 v[48:63], a[48:51], v[116:119], v[48:63]\n\t" "ds_read_b128 v[104:107], v81 offset:512\n\t" \
    "buffer_load_dwordx4 v90, %[rx], s61 offen offset:1024 lds\n\t" "s_waitcnt lgkmcnt(4)\n\t" "v_mfma_f32_32x32x16_bf16 v[48:63], a[52:55], v[120:123], v[48:63]\n\t" \
    "ds_read_b128 v[108:111], v82 offset:512\n\t" "s_mov_b32 m0, s63\n\t" "s_add_u32 s63, s63, 0x800\n\t" "buffer_load_dwordx4 v91, %[rx], s61 offen lds\n\t" \
    "s_waitcnt lgkmcnt(4)\n\t" "v_mfma_f32_32x32x16_bf16 v[48:63], a[56:59], v[124:127], v[48:63]\n\t" "ds_read_b128 v[112:115], v83 offset:512\n\t" \
    "buffer_load_dwordx4 v91, %[rx], s61 offen offset:1024 lds\n\t" "s_waitcnt lgkmcnt(4)\n\t" "v_mfma_f32_32x32x16_bf16 v[48:63], a[60:63], v[128:131], v[48:63]\n\t" \
    "ds_read_b128 v[116:119], v84 offset:512\n\t" "s_mov_b32 m0, s63\n\t" "s_add_u32 s63, s63, 0x800\n\t" "buffer_load_dwordx4 v92, %[rx], s61 offen lds\n\t" \
    "s_waitcnt lgkmcnt(4)\n\t" "v_mfma_f32_32x32x16_bf16 v[48:63], a[64:67], v[100:103], v[48:63]\n\t" "ds_read_b128 v[120:123], v85 offset:512\n\t" \
    "buffer_load_dwordx4 v92, %[rx], s61 offen offset:1024 lds\n\t" "s_waitcnt lgkmcnt(4)\n\t" "v_mfma_f32_32x32x16_bf16 v[48:63], a[68:71], v[104:107], v[48:63]\n\t" \
    "ds_read_b128 v[124:127], v86 offset:512\n\t" "s_mov_b32 m0, s63\n\t" "s_add_u32 s63, s63, 0x800\n\t" "buffer_load_dwordx4 v93, %[rx], s61 offen lds\n\t" \
    "s_waitcnt lgkmcnt(4)\n\t" "v_mfma_f32_32x32x16_bf16 v[48:63], a[72:75], v[108:111], v[48:63]\n\t" "ds_read_b128 v[128:131], v87 offset:512\n\t" \
    "buffer_load_dwordx4 v93, %[rx], s61 offen offset:1024 lds\n\t" "s_waitcnt lgkmcnt(4)\n\t" "v_mfma_f32_32x32x16_bf16 v[48:63], a[76:79], v[112:115], v[48:63]\n\t" \
    "ds_read_b128 v[100:103], v80 offset:768\n\t" "s_mov_b32 m0, s63\n\t" "s_add_u32 s63, s63, 0x800\n\t" "buffer_load_dwordx4 v94, %[rx], s61 offen lds\n\t" \
    "s_waitcnt lgkmcnt(4)\n\t" "v_mfma_f32_32x32x16_bf16 v[48:63], a[80:83], v[116:119], v[48:63]\n\t" "ds_read_b128 v[104:107], v81 offset:768\n\t" \
    "buffer_load_dwordx4 v94, %[rx], s61 offen offset:1024 lds\n\t" "s_waitcnt lgkmcnt(4)\n\t" "v_mfma_f32_32x32x16_bf16 v[48:63], a[84:87], v[120:123], v[48:63]\n\t" \
    "ds_read_b128 v[108:111], v82 offset:768\n\t" "s_mov_b32 m0, s63\n\t" "s_add_u32 s63, s63, 0x800\n\t" "buffer_load_dwordx4 v95, %[rx], s61 offen lds\n\t" \
    "s_waitcnt lgkmcnt(4)\n\t" "v_mfma_f32_32x32x16_bf16 v[48:63], a[88:91], v[124:127], v[48:63]\n\t" "ds_read_b128 v[112:115], v83 offset:768\n\t" \
    "buffer_load_dwordx4 v95, %[rx], s61 offen offset:1024 lds\n\t" "s_waitcnt lgkmcnt(4)\n\t" "v_mfma_f32_32x32x16_bf16 v[48:63], a[92:95], v[128:131], v[48:63]\n\t" \
    "ds_read_b128 v[116:119], v84 offset:768\n\t" "s_waitcnt lgkmcnt(4)\n\t" "v_mfma_f32_32x32x16_bf16 v[48:63], a[96:99], v[100:103], v[48:63]\n\t" \
    "ds_read_b128 v[120:123], v85 offset:768\n\t" "s_lshr_b32 s64, s60, 2\n\t" "s_xor_b32 s64, s64, %[xp]\n\t" "s_and_b32 s64, s64, 1\n\t" "s_lshl_b32 s64, s64, 13\n\t" \
    "s_waitcnt lgkmcnt(4)\n\t" "v_mfma_f32_32x32x16_bf16 v[48:63], a[100:103], v[104:107], v[48:63]\n\t" "ds_read_b128 v[124:127], v86 offset:768\n\t" \
    "s_lshl_b32 s62, s60, 19\n\t" "s_add_u32 s62, s62, s64\n\t" "s_add_u32 s62, s62, %[ob]\n\t" "s_waitcnt lgkmcnt(4)\n\t" \
    "v_mfma_f32_32x32x16_bf16 v[48:63], a[104:107], v[108:111], v[48:63]\n\t" "ds_read_b128 v[128:131], v87 offset:768\n\t" "s_waitcnt lgkmcnt(4)\n\t" \
    "v_mfma_f32_32x32x16_bf16 v[48:63], a[108:111], v[112:115], v[48:63]\n\t" "ds_read_b128 v[100:103], v80 offset:1024\n\t" "s_waitcnt lgkmcnt(4)\n\t" \
    "v_mfma_f32_32x32x16_bf16 v[48:63], a[112:115], v[116:119], v[48:63]\n\t" "ds_read_b128 v[104:107], v81 offset:1024\n\t" "s_waitcnt lgkmcnt(4)\n\t" \
    "v_mfma_f32_32x32x16_bf16 v[48:63], a[116:119], v[120:123], v[48:63]\n\t" "ds_read_b128 v[108:111], v82 offset:1024\n\t" "s_waitcnt lgkmcnt(4)\n\t" \
    "v_mfma_f32_32x32x16_bf16 v[48:63], a[120:123], v[124:127], v[48:63]\n\t" "ds_read_b128 v[112:115], v83 offset:1024\n\t" "s_waitcnt lgkmcnt(4)\n\t" \
    "v_mfma_f32_32x32x16_bf16 v[48:63], a[124:127], v[128:131], v[48:63]\n\t" "ds_read_b128 v[116:119], v84 offset:1024\n\t" "s_waitcnt lgkmcnt(4)\n\t" \
    "v_mfma_f32_32x32x16_bf16 v[48:63], a[128:131], v[100:103], v[48:63]\n\t" "ds_read_b128 v[120:123], v85 offset:1024\n\t" "s_waitcnt lgkmcnt(4)\n\t" \
    "v_mfma_f32_32x32x16_bf16 v[48:63], a[132:135], v[104:107], v[48:63]\n\t" "ds_read_b128 v[124:127], v86 offset:1024\n\t" "s_waitcnt lgkmcnt(4)\n\t" \
    "v_mfma_f32_32x32x16_bf16 v[48:63], a[136:139], v[108:111], v[48:63]\n\t" "ds_read_b128 v[128:131], v87 offset:1024\n\t" "s_waitcnt vmcnt(16)\n\t" "s_barrier\n\t" \
    "v_mov_b32 v98, s60\n\t" "buffer_store_dword v98, %[fo], %[rc], %[fl] offen sc1\n\t" "s_waitcnt lgkmcnt(4)\n\t" \
    "v_mfma_f32_32x32x16_bf16 v[48:63], a[140:143], v[112:115], v[48:63]\n\t" "ds_read_b128 v[100:103], v80 offset:1280\n\t" "s_waitcnt lgkmcnt(4)\n\t" \
    "v_mfma_f32_32x32x16_bf16 v[48:63], a[144:147], v[116:119], v[48:63]\n\t" "ds_read_b128 v[104:107], v81 offset:1280\n\t" "s_waitcnt lgkmcnt(4)\n\t" \
    "v_mfma_f32_32x32x16_bf16 v[48:63], a[148:151], v[120:123], v[48:63]\n\t" "ds_read_b128 v[108:111], v82 offset:1280\n\t" "s_waitcnt lgkmcnt(4)\n\t" \
    "v_mfma_f32_32x32x16_bf16 v[48:63], a[152:155], v[124:127], v[48:63]\n\t" "ds_read_b128 v[112:115], v83 offset:1280\n\t" "s_waitcnt lgkmcnt(4)\n\t" \
    "v_mfma_f32_32x32x16_bf16 v[48:63], a[156:159], v[128:131], v[48:63]\n\t" "ds_read_b128 v[116:119], v84 offset:1280\n\t" "s_waitcnt lgkmcnt(4)\n\t" \
    "v_mfma_f32_32x32x16_bf16 v[48:63], a[160:163], v[100:103], v[48:63]\n\t" "ds_read_b128 v[120:123], v85 offset:1280\n\t" "s_waitcnt lgkmcnt(4)\n\t" \
    "v_mfma_f32_32x32x16_bf16 v[48:63], a[164:167], v[104:107], v[48:63]\n\t" "ds_read_b128 v[124:127], v86 offset:1280\n\t" "s_waitcnt lgkmcnt(4)\n\t" \
    "v_mfma_f32_32x32x16_bf16 v[48:63], a[168:171], v[108:111], v[48:63]\n\t" "ds_read_b128 v[128:131], v87 offset:1280\n\t" "s_waitcnt lgkmcnt(4)\n\t" \
    "v_mfma_f32_32x32x16_bf16 v[48:63], a[172:175], v[112:115], v[48:63]\n\t" "ds_read_b128 v[100:103], v80 offset:1536\n\t" "s_waitcnt lgkmcnt(4)\n\t" \
    "v_mfma_f32_32x32x16_bf16 v[48:63], a[176:179], v[116:119], v[48:63]\n\t" "ds_read_b128 v[104:107], v81 offset:1536\n\t" "s_waitcnt lgkmcnt(4)\n\t" \
    "v_mfma_f32_32x32x16_bf16 v[48:63], a[180:183], v[120:123], v[48:63]\n\t" "ds_read_b128 v[108:111], v82 offset:1536\n\t" "s_waitcnt lgkmcnt(4)\n\t" \
    "v_mfma_f32_32x32x16_bf16 v[48:63], a[184:187], v[124:127], v[48:63]\n\t" "ds_read_b128 v[112:115], v83 offset:1536\n\t" "s_waitcnt lgkmcnt(4)\n\t" \
    "v_mfma_f32_32x32x16_bf16 v[48:63], a[188:191], v[128:131], v[48:63]\n\t" "ds_read_b128 v[116:119], v84 offset:1536\n\t" "s_waitcnt lgkmcnt(4)\n\t" \
    "v_mfma_f32_32x32x16_bf16 v[48:63], a[192:195], v[100:103], v[48:63]\n\t" "ds_read_b128 v[120:123], v85 offset:1536\n\t" "s_waitcnt lgkmcnt(4)\n\t" \
    "v_mfma_f32_32x32x16_bf16 v[48:63], a[196:199], v[104:107], v[48:63]\n\t" "ds_read_b128 v[124:127], v86 offset:1536\n\t" "s_waitcnt lgkmcnt(4)\n\t" \
    "v_mfma_f32_32x32x16_bf16 v[48:63], a[200:203], v[108:111], v[48:63]\n\t" "ds_read_b128 v[128:131], v87 offset:1536\n\t" "s_waitcnt lgkmcnt(4)\n\t" \
    "v_mfma_f32_32x32x16_bf16 v[48:63], a[204:207], v[112:115], v[48:63]\n\t" "ds_read_b128 v[100:103], v80 offset:1792\n\t" "s_waitcnt lgkmcnt(4)\n\t" \
    "v_mfma_f32_32x32x16_bf16 v[48:63], a[208:211], v[116:119], v[48:63]\n\t" "ds_read_b128 v[104:107], v81 offset:1792\n\t" "s_waitcnt lgkmcnt(4)\n\t" \
    "v_mfma_f32_32x32x16_bf16 v[48:63], a[212:215], v[120:123], v[48:63]\n\t" "ds_read_b128 v[108:111], v82 offset:1792\n\t" "s_waitcnt lgkmcnt(4)\n\t" \
    "v_mfma_f32_32x32x16_bf16 v[48:63], a[216:219], v[124:127], v[48:63]\n\t" "ds_read_b128 v[112:115], v83 offset:1792\n\t" "s_waitcnt lgkmcnt(4)\n\t" \
    "v_mfma_f32_32x32x16_bf16 v[48:63], a[220:223], v[128:131], v[48:63]\n\t" "ds_read_b128 v[116:119], v84 offset:1792\n\t" "s_waitcnt lgkmcnt(4)\n\t" \
    "v_mfma_f32_32x32x16_bf16 v[48:63], a[224:227], v[100:103], v[48:63]\n\t" "ds_read_b128 v[120:123], v85 offset:1792\n\t" "s_waitcnt lgkmcnt(4)\n\t" \
    "v_mfma_f32_32x32x16_bf16 v[48:63], a[228:231], v[104:107], v[48:63]\n\t" "ds_read_b128 v[124:127], v86 offset:1792\n\t" "s_waitcnt lgkmcnt(4)\n\t" \
    "v_mfma_f32_32x32x16_bf16 v[48:63], a[232:235], v[108:111], v[48:63]\n\t" "ds_read_b128 v[128:131], v87 offset:1792\n\t" "s_waitcnt lgkmcnt(4)\n\t" \
    "v_mfma_f32_32x32x16_bf16 v[48:63], a[236:239], v[112:115], v[48:63]\n\t" "s_waitcnt lgkmcnt(3)\n\t" \
    "v_mfma_f32_32x32x16_bf16 v[48:63], a[240:243], v[116:119], v[48:63]\n\t" "s_waitcnt vmcnt(1) lgkmcnt(0)\n\t" "s_barrier\n\t" XT(82, 83) \
    "v_mfma_f32_32x32x16_bf16 v[48:63], a[244:247], v[120:123], v[48:63]\n\t" "ds_read_b128 v[100:103], v72 offset:0\n\t" \
    "v_mfma_f32_32x32x16_bf16 v[48:63], a[248:251], v[124:127], v[48:63]\n\t" "ds_read_b128 v[104:107], v73 offset:0\n\t" "ds_read_b128 v[108:111], v74 offset:0\n\t" \
    "v_mfma_f32_32x32x16_bf16 v[48:63], a[252:255], v[128:131], v[48:63]\n\t" "ds_read_b128 v[112:115], v75 offset:0\n\t" "ds_read_b128 v[116:119], v76 offset:0\n\t" \
    "s_add_u32 s60, s60, 1\n\t" GSUM "s_cmp_lt_u32 s60, 64\n\t" "s_cbranch_scc1 10b\n\t" "s_nop 11\n\t" "v_cvt_pk_bf16_f32 v64, v48, v49\n\t" \
    "v_cvt_pk_bf16_f32 v65, v50, v51\n\t" "v_cvt_pk_bf16_f32 v66, v52, v53\n\t" "v_cvt_pk_bf16_f32 v67, v54, v55\n\t" "v_cvt_pk_bf16_f32 v68, v56, v57\n\t" \
    "v_cvt_pk_bf16_f32 v69, v58, v59\n\t" "v_cvt_pk_bf16_f32 v70, v60, v61\n\t" "v_cvt_pk_bf16_f32 v71, v62, v63\n\t" \
    "buffer_store_dwordx4 v[64:67], v96, %[rs], s62 offen offset:0 sc1\n\t" "buffer_store_dwordx4 v[68:71], v96, %[rs], s62 offen offset:1024 sc1\n\t" \
    "s_waitcnt vmcnt(0)\n\t" "s_barrier\n\t" "v_mov_b32 v98, s60\n\t" "buffer_store_dword v98, %[fo], %[rc], %[fl] offen sc1\n\t" "s_waitcnt lgkmcnt(0)\n\t" \
    "s_branch 30f\n\t" "20:\n\t" "v_mov_b32 v96, %[so]\n\t" "s_nop 11\n\t" "v_cvt_pk_bf16_f32 v64, v32, v33\n\t" "v_cvt_pk_bf16_f32 v65, v34, v35\n\t" \
    "v_cvt_pk_bf16_f32 v66, v36, v37\n\t" "v_cvt_pk_bf16_f32 v67, v38, v39\n\t" "v_cvt_pk_bf16_f32 v68, v40, v41\n\t" "v_cvt_pk_bf16_f32 v69, v42, v43\n\t" \
    "v_cvt_pk_bf16_f32 v70, v44, v45\n\t" "v_cvt_pk_bf16_f32 v71, v46, v47\n\t" "buffer_store_dwordx4 v[64:67], v96, %[rs], s62 offen offset:0 sc1\n\t" \
    "buffer_store_dwordx4 v[68:71], v96, %[rs], s62 offen offset:1024 sc1\n\t" "s_waitcnt vmcnt(0)\n\t" "s_barrier\n\t" "v_mov_b32 v98, s60\n\t" \
    "buffer_store_dword v98, %[fo], %[rc], %[fl] offen sc1\n\t" "s_branch 11b\n\t" "30:\n\t"
#define ZC_LOOP \
    "s_mov_b32 s60, 0\n\t" "s_mov_b32 s61, 0\n\t" "s_mov_b32 s62, 0\n\t" "s_mov_b32 s63, 0x7f000000\n\t" "s_cmp_ge_u32 s61, 1\n\t" "s_cbranch_scc1 52f\n\t" "51:\n\t" \
    "buffer_load_dword v120, %[fo], %[rc], %[fv] offen sc1\n\t" "s_waitcnt vmcnt(0)\n\t" "v_readfirstlane_b32 s61, v120\n\t" "s_nop 3\n\t" "s_cmp_ge_u32 s61, 1\n\t" \
    "s_cbranch_scc1 52f\n\t" "s_cmp_eq_u32 %[bd], 0\n\t" "s_cbranch_scc1 52f\n\t" "s_sub_u32 %[bd], %[bd], 1\n\t" "s_sleep 2\n\t" "s_branch 51b\n\t" "52:\n\t" \
    "s_lshr_b32 s66, s60, 2\n\t" "s_xor_b32 s66, s66, %[xp]\n\t" "s_and_b32 s66, s66, 1\n\t" "s_lshl_b32 s66, s66, 13\n\t" "s_lshl_b32 s64, s60, 19\n\t" \
    "s_add_u32 s64, s64, s66\n\t" "s_add_u32 s64, s64, %[vb]\n\t" "buffer_load_dwordx4 v[16:19], %[ao], %[rs], s64 offen offset:0 sc1\n\t" \
    "buffer_load_dwordx4 v[20:23], %[ao], %[rs], s64 offen offset:1024 sc1\n\t" "buffer_load_dwordx4 v[24:27], %[ao], %[rs], s64 offen offset:2048 sc1\n\t" \
    "buffer_load_dwordx4 v[28:31], %[ao], %[rs], s64 offen offset:3072 sc1\n\t" "buffer_load_dwordx4 v[32:35], %[a1], %[rs], s64 offen offset:0 sc1\n\t" \
    "buffer_load_dwordx4 v[36:39], %[a1], %[rs], s64 offen offset:1024 sc1\n\t" "buffer_load_dwordx4 v[40:43], %[a1], %[rs], s64 offen offset:2048 sc1\n\t" \
    "buffer_load_dwordx4 v[44:47], %[a1], %[rs], s64 offen offset:3072 sc1\n\t" "s_waitcnt vmcnt(0)\n\t" "10:\n\t" XT(80, 81) "s_add_u32 s66, s62, %[rg]\n\t" \
    "s_cmp_lt_u32 s66, s60\n\t" "s_cbranch_scc1 60f\n\t" "63:\n\t" "v_mfma_f32_32x32x16_bf16 v[80:95], v[16:19], a[0:3], 0\n\t" "s_add_u32 s67, s60, 1\n\t" \
    "s_lshr_b32 s66, s67, 2\n\t" "s_xor_b32 s66, s66, %[xp]\n\t" "s_and_b32 s66, s66, 1\n\t" "s_lshl_b32 s66, s66, 13\n\t" \
    "v_mfma_f32_32x32x16_bf16 v[80:95], v[20:23], a[4:7], v[80:95]\n\t" "s_lshl_b32 s64, s67, 19\n\t" "s_add_u32 s64, s64, s66\n\t" "s_add_u32 s64, s64, %[vb]\n\t" \
    "s_cmp_gt_u32 s61, s67\n\t" "s_cselect_b32 s65, 1, 0\n\t" "v_mfma_f32_32x32x16_bf16 v[80:95], v[24:27], a[8:11], v[80:95]\n\t" "s_cselect_b32 s64, s64, 0x7f000000\n\t" \
    "v_mfma_f32_32x32x16_bf16 v[80:95], v[28:31], a[12:15], v[80:95]\n\t" "v_cvt_pk_bf16_f32 v112, v96, v97\n\t" "v_cvt_pk_bf16_f32 v113, v98, v99\n\t" \
    "v_cvt_pk_bf16_f32 v114, v100, v101\n\t" "v_mfma_f32_32x32x16_bf16 v[80:95], v[32:35], a[16:19], v[80:95]\n\t" "v_cvt_pk_bf16_f32 v115, v102, v103\n\t" \
    "v_cvt_pk_bf16_f32 v116, v104, v105\n\t" "v_cvt_pk_bf16_f32 v117, v106, v107\n\t" "v_mfma_f32_32x32x16_bf16 v[80:95], v[36:39], a[20:23], v[80:95]\n\t" \
    "buffer_store_dwordx4 v[112:115], %[zo], %[rs], s63 offen offset:0 sc1\n\t" "v_cvt_pk_bf16_f32 v118, v108, v109\n\t" "v_cvt_pk_bf16_f32 v119, v110, v111\n\t" \
    "v_mfma_f32_32x32x16_bf16 v[80:95], v[40:43], a[24:27], v[80:95]\n\t" "v_mfma_f32_32x32x16_bf16 v[80:95], v[44:47], a[28:31], v[80:95]\n\t" \
    "buffer_store_dwordx4 v[116:119], %[zo], %[rs], s63 offen offset:512 sc1\n\t" "s_and_b32 s63, s60, %[rm]\n\t" "s_lshl_b32 s63, s63, 22\n\t" \
    "s_add_u32 s63, s63, %[zb]\n\t" "v_mfma_f32_32x32x16_bf16 v[96:111], v[16:19], a[32:35], 0\n\t" "buffer_load_dwordx4 v[48:51], %[ao], %[rs], s64 offen offset:0 sc1\n\t" \
    "s_nop 2\n\t" "v_mfma_f32_32x32x16_bf16 v[96:111], v[20:23], a[36:39], v[96:111]\n\t" "buffer_load_dwordx4 v[52:55], %[ao], %[rs], s64 offen offset:1024 sc1\n\t" \
    "s_nop 2\n\t" "v_mfma_f32_32x32x16_bf16 v[96:111], v[24:27], a[40:43], v[96:111]\n\t" "buffer_load_dwordx4 v[56:59], %[ao], %[rs], s64 offen offset:2048 sc1\n\t" \
    "s_nop 1\n\t" "v_mfma_f32_32x32x16_bf16 v[96:111], v[28:31], a[44:47], v[96:111]\n\t" "buffer_load_dwordx4 v[60:63], %[ao], %[rs], s64 offen offset:3072 sc1\n\t" \
    "v_cvt_pk_bf16_f32 v112, v80, v81\n\t" "v_cvt_pk_bf16_f32 v113, v82, v83\n\t" "v_cvt_pk_bf16_f32 v114, v84, v85\n\t" \
    "v_mfma_f32_32x32x16_bf16 v[96:111], v[32:35], a[48:51], v[96:111]\n\t" "buffer_load_dwordx4 v[64:67], %[a1], %[rs], s64 offen offset:0 sc1\n\t" \
    "v_cvt_pk_bf16_f32 v115, v86, v87\n\t" "v_cvt_pk_bf16_f32 v116, v88, v89\n\t" "v_cvt_pk_bf16_f32 v117, v90, v91\n\t" \
    "v_mfma_f32_32x32x16_bf16 v[96:111], v[36:39], a[52:55], v[96:111]\n\t" "buffer_load_dwordx4 v[68:71], %[a1], %[rs], s64 offen offset:1024 sc1\n\t" \
    "buffer_store_dwordx4 v[112:115], %[zo], %[rs], s63 offen offset:0 sc1\n\t" "v_cvt_pk_bf16_f32 v118, v92, v93\n\t" "v_cvt_pk_bf16_f32 v119, v94, v95\n\t" \
    "v_mfma_f32_32x32x16_bf16 v[96:111], v[40:43], a[56:59], v[96:111]\n\t" "buffer_load_dwordx4 v[72:75], %[a1], %[rs], s64 offen offset:2048 sc1\n\t" \
    "v_mfma_f32_32x32x16_bf16 v[96:111], v[44:47], a[60:63], v[96:111]\n\t" "buffer_load_dwordx4 v[76:79], %[a1], %[rs], s64 offen offset:3072 sc1\n\t" \
    "buffer_store_dwordx4 v[116:119], %[zo], %[rs], s63 offen offset:512 sc1\n\t" "s_add_u32 s63, s63, 0x20000\n\t" \
    "v_mfma_f32_32x32x16_bf16 v[80:95], v[16:19], a[64:67], 0\n\t" "s_nop 2\n\t" "v_mfma_f32_32x32x16_bf16 v[80:95], v[20:23], a[68:71], v[80:95]\n\t" "s_nop 2\n\t" \
    "v_mfma_f32_32x32x16_bf16 v[80:95], v[24:27], a[72:75], v[80:95]\n\t" "s_nop 1\n\t" "v_mfma_f32_32x32x16_bf16 v[80:95], v[28:31], a[76:79], v[80:95]\n\t" \
    "v_cvt_pk_bf16_f32 v112, v96, v97\n\t" "v_cvt_pk_bf16_f32 v113, v98, v99\n\t" "v_cvt_pk_bf16_f32 v114, v100, v101\n\t" \
    "v_mfma_f32_32x32x16_bf16 v[80:95], v[32:35], a[80:83], v[80:95]\n\t" "v_cvt_pk_bf16_f32 v115, v102, v103\n\t" "v_cvt_pk_bf16_f32 v116, v104, v105\n\t" \
    "v_cvt_pk_bf16_f32 v117, v106, v107\n\t" "v_mfma_f32_32x32x16_bf16 v[80:95], v[36:39], a[84:87], v[80:95]\n\t" \
    "buffer_store_dwordx4 v[112:115], %[zo], %[rs], s63 offen offset:0 sc1\n\t" "v_cvt_pk_bf16_f32 v118, v108, v109\n\t" "v_cvt_pk_bf16_f32 v119, v110, v111\n\t" \
    "v_mfma_f32_32x32x16_bf16 v[80:95], v[40:43], a[88:91], v[80:95]\n\t" "v_mfma_f32_32x32x16_bf16 v[80:95], v[44:47], a[92:95], v[80:95]\n\t" \
    "buffer_store_dwordx4 v[116:119], %[zo], %[rs], s63 offen offset:512 sc1\n\t" "s_add_u32 s63, s63, 0x20000\n\t" \
    "v_mfma_f32_32x32x16_bf16 v[96:111], v[16:19], a[96:99], 0\n\t" "s_nop 2\n\t" "v_mfma_f32_32x32x16_bf16 v[96:111], v[20:23], a[100:103], v[96:111]\n\t" "s_nop 2\n\t" \
    "v_mfma_f32_32x32x16_bf16 v[96:111], v[24:27], a[104:107], v[96:111]\n\t" "s_nop 1\n\t" "v_mfma_f32_32x32x16_bf16 v[96:111], v[28:31], a[108:111], v[96:111]\n\t" \
    "v_cvt_pk_bf16_f32 v112, v80, v81\n\t" "v_cvt_pk_bf16_f32 v113, v82, v83\n\t" "v_cvt_pk_bf16_f32 v114, v84, v85\n\t" \
    "v_mfma_f32_32x32x16_bf16 v[96:111], v[32:35], a[112:115], v[96:111]\n\t" "v_cvt_pk_bf16_f32 v115, v86, v87\n\t" "v_cvt_pk_bf16_f32 v116, v88, v89\n\t" \
    "v_cvt_pk_bf16_f32 v117, v90, v91\n\t" "v_mfma_f32_32x32x16_bf16 v[96:111], v[36:39], a[116:119], v[96:111]\n\t" \
    "buffer_store_dwordx4 v[112:115], %[zo], %[rs], s63 offen offset:0 sc1\n\t" "v_cvt_pk_bf16_f32 v118, v92, v93\n\t" "v_cvt_pk_bf16_f32 v119, v94, v95\n\t" \
    "v_mfma_f32_32x32x16_bf16 v[96:111], v[40:43], a[120:123], v[96:111]\n\t" "v_mfma_f32_32x32x16_bf16 v[96:111], v[44:47], a[124:127], v[96:111]\n\t" \
    "buffer_store_dwordx4 v[116:119], %[zo], %[rs], s63 offen offset:512 sc1\n\t" "s_add_u32 s63, s63, 0x20000\n\t" \
    "v_mfma_f32_32x32x16_bf16 v[80:95], v[16:19], a[128:131], 0\n\t" "buffer_load_dword v120, %[fo], %[rc], %[fv] offen sc1\n\t" "s_nop 2\n\t" \
    "v_mfma_f32_32x32x16_bf16 v[80:95], v[20:23], a[132:135], v[80:95]\n\t" "buffer_load_dword v121, %[fo], %[rc], %[fs] offen sc1\n\t" "s_nop 2\n\t" \
    "v_mfma_f32_32x32x16_bf16 v[80:95], v[24:27], a[136:139], v[80:95]\n\t" "s_nop 1\n\t" "v_mfma_f32_32x32x16_bf16 v[80:95], v[28:31], a[140:143], v[80:95]\n\t" \
    "v_cvt_pk_bf16_f32 v112, v96, v97\n\t" "v_cvt_pk_bf16_f32 v113, v98, v99\n\t" "v_cvt_pk_bf16_f32 v114, v100, v101\n\t" \
    "v_mfma_f32_32x32x16_bf16 v[80:95], v[32:35], a[144:147], v[80:95]\n\t" "v_cvt_pk_bf16_f32 v115, v102, v103\n\t" "v_cvt_pk_bf16_f32 v116, v104, v105\n\t" \
    "v_cvt_pk_bf16_f32 v117, v106, v107\n\t" "v_mfma_f32_32x32x16_bf16 v[80:95], v[36:39], a[148:151], v[80:95]\n\t" \
    "buffer_store_dwordx4 v[112:115], %[zo], %[rs], s63 offen offset:0 sc1\n\t" "v_cvt_pk_bf16_f32 v118, v108, v109\n\t" "v_cvt_pk_bf16_f32 v119, v110, v111\n\t" \
    "v_mfma_f32_32x32x16_bf16 v[80:95], v[40:43], a[152:155], v[80:95]\n\t" "v_mfma_f32_32x32x16_bf16 v[80:95], v[44:47], a[156:159], v[80:95]\n\t" \
    "buffer_store_dwordx4 v[116:119], %[zo], %[rs], s63 offen offset:512 sc1\n\t" "s_add_u32 s63, s63, 0x20000\n\t" \
    "v_mfma_f32_32x32x16_bf16 v[96:111], v[16:19], a[160:163], 0\n\t" "s_nop 2\n\t" "v_mfma_f32_32x32x16_bf16 v[96:111], v[20:23], a[164:167], v[96:111]\n\t" "s_nop 2\n\t" \
    "v_mfma_f32_32x32x16_bf16 v[96:111], v[24:27], a[168:171], v[96:111]\n\t" "s_nop 1\n\t" "s_waitcnt vmcnt(18)\n\t" "v_mov_b32 v122, s60\n\t" \
    "buffer_store_dword v122, %[fo], %[rc], %[fz] offen sc1\n\t" "v_mfma_f32_32x32x16_bf16 v[96:111], v[28:31], a[172:175], v[96:111]\n\t" \
    "v_cvt_pk_bf16_f32 v112, v80, v81\n\t" "v_cvt_pk_bf16_f32 v113, v82, v83\n\t" "v_cvt_pk_bf16_f32 v114, v84, v85\n\t" \
    "v_mfma_f32_32x32x16_bf16 v[96:111], v[32:35], a[176:179], v[96:111]\n\t" "v_cvt_pk_bf16_f32 v115, v86, v87\n\t" "v_cvt_pk_bf16_f32 v116, v88, v89\n\t" \
    "v_cvt_pk_bf16_f32 v117, v90, v91\n\t" "v_mfma_f32_32x32x16_bf16 v[96:111], v[36:39], a[180:183], v[96:111]\n\t" \
    "buffer_store_dwordx4 v[112:115], %[zo], %[rs], s63 offen offset:0 sc1\n\t" "v_cvt_pk_bf16_f32 v118, v92, v93\n\t" "v_cvt_pk_bf16_f32 v119, v94, v95\n\t" \
    "v_mfma_f32_32x32x16_bf16 v[96:111], v[40:43], a[184:187], v[96:111]\n\t" "v_mfma_f32_32x32x16_bf16 v[96:111], v[44:47], a[188:191], v[96:111]\n\t" \
    "buffer_store_dwordx4 v[116:119], %[zo], %[rs], s63 offen offset:512 sc1\n\t" "s_add_u32 s63, s63, 0x20000\n\t" \
    "v_mfma_f32_32x32x16_bf16 v[80:95], v[16:19], a[192:195], 0\n\t" "s_nop 2\n\t" "v_mfma_f32_32x32x16_bf16 v[80:95], v[20:23], a[196:199], v[80:95]\n\t" "s_nop 2\n\t" \
    "v_mfma_f32_32x32x16_bf16 v[80:95], v[24:27], a[200:203], v[80:95]\n\t" "s_nop 1\n\t" "v_mfma_f32_32x32x16_bf16 v[80:95], v[28:31], a[204:207], v[80:95]\n\t" \
    "v_cvt_pk_bf16_f32 v112, v96, v97\n\t" "v_cvt_pk_bf16_f32 v113, v98, v99\n\t" "v_cvt_pk_bf16_f32 v114, v100, v101\n\t" \
    "v_mfma_f32_32x32x16_bf16 v[80:95], v[32:35], a[208:211], v[80:95]\n\t" "v_cvt_pk_bf16_f32 v115, v102, v103\n\t" "v_cvt_pk_bf16_f32 v116, v104, v105\n\t" \
    "v_cvt_pk_bf16_f32 v117, v106, v107\n\t" "v_mfma_f32_32x32x16_bf16 v[80:95], v[36:39], a[212:215], v[80:95]\n\t" \
    "buffer_store_dwordx4 v[112:115], %[zo], %[rs], s63 offen offset:0 sc1\n\t" "v_cvt_pk_bf16_f32 v118, v108, v109\n\t" "v_cvt_pk_bf16_f32 v119, v110, v111\n\t" \
    "v_mfma_f32_32x32x16_bf16 v[80:95], v[40:43], a[216:219], v[80:95]\n\t" "v_mfma_f32_32x32x16_bf16 v[80:95], v[44:47], a[220:223], v[80:95]\n\t" \
    "buffer_store_dwordx4 v[116:119], %[zo], %[rs], s63 offen offset:512 sc1\n\t" "s_add_u32 s63, s63, 0x20000\n\t" \
    "v_mfma_f32_32x32x16_bf16 v[96:111], v[16:19], a[224:227], 0\n\t" "s_nop 2\n\t" "v_mfma_f32_32x32x16_bf16 v[96:111], v[20:23], a[228:231], v[96:111]\n\t" "s_nop 2\n\t" \
    "v_mfma_f32_32x32x16_bf16 v[96:111], v[24:27], a[232:235], v[96:111]\n\t" "s_nop 1\n\t" "v_mfma_f32_32x32x16_bf16 v[96:111], v[28:31], a[236:239], v[96:111]\n\t" \
    "v_cvt_pk_bf16_f32 v112, v80, v81\n\t" "v_cvt_pk_bf16_f32 v113, v82, v83\n\t" "v_cvt_pk_bf16_f32 v114, v84, v85\n\t" \
    "v_mfma_f32_32x32x16_bf16 v[96:111], v[32:35], a[240:243], v[96:111]\n\t" "v_cvt_pk_bf16_f32 v115, v86, v87\n\t" "v_cvt_pk_bf16_f32 v116, v88, v89\n\t" \
    "v_cvt_pk_bf16_f32 v117, v90, v91\n\t" "v_mfma_f32_32x32x16_bf16 v[96:111], v[36:39], a[244:247], v[96:111]\n\t" \
    "buffer_store_dwordx4 v[112:115], %[zo], %[rs], s63 offen offset:0 sc1\n\t" "v_cvt_pk_bf16_f32 v118, v92, v93\n\t" "v_cvt_pk_bf16_f32 v119, v94, v95\n\t" \
    "v_mfma_f32_32x32x16_bf16 v[96:111], v[40:43], a[248:251], v[96:111]\n\t" "s_waitcnt vmcnt(8)\n\t" "v_readfirstlane_b32 s66, v120\n\t" "v_readfirstlane_b32 s67, v121\n\t" \
    "v_mfma_f32_32x32x16_bf16 v[96:111], v[44:47], a[252:255], v[96:111]\n\t" "buffer_store_dwordx4 v[116:119], %[zo], %[rs], s63 offen offset:512 sc1\n\t" \
    "s_add_u32 s63, s63, 0x20000\n\t" "s_max_u32 s61, s61, s66\n\t" "s_max_u32 s62, s62, s67\n\t" "s_add_u32 s60, s60, 1\n\t" "s_cmp_eq_u32 s65, 0\n\t" ZSUM \
    "s_cbranch_scc1 20f\n\t" "23:\n\t" XT(80, 81) "s_add_u32 s66, s62, %[rg]\n\t" "s_cmp_lt_u32 s66, s60\n\t" "s_cbranch_scc1 70f\n\t" "73:\n\t" \
    "v_mfma_f32_32x32x16_bf16 v[80:95], v[48:51], a[0:3], 0\n\t" "s_add_u32 s67, s60, 1\n\t" "s_lshr_b32 s66, s67, 2\n\t" "s_xor_b32 s66, s66, %[xp]\n\t" \
    "s_and_b32 s66, s66, 1\n\t" "s_lshl_b32 s66, s66, 13\n\t" "v_mfma_f32_32x32x16_bf16 v[80:95], v[52:55], a[4:7], v[80:95]\n\t" "s_lshl_b32 s64, s67, 19\n\t" \
    "s_add_u32 s64, s64, s66\n\t" "s_add_u32 s64, s64, %[vb]\n\t" "s_cmp_gt_u32 s61, s67\n\t" "s_cselect_b32 s65, 1, 0\n\t" \
    "v_mfma_f32_32x32x16_bf16 v[80:95], v[56:59], a[8:11], v[80:95]\n\t" "s_cselect_b32 s64, s64, 0x7f000000\n\t" \
    "v_mfma_f32_32x32x16_bf16 v[80:95], v[60:63], a[12:15], v[80:95]\n\t" "v_cvt_pk_bf16_f32 v112, v96, v97\n\t" "v_cvt_pk_bf16_f32 v113, v98, v99\n\t" \
    "v_cvt_pk_bf16_f32 v114, v100, v101\n\t" "v_mfma_f32_32x32x16_bf16 v[80:95], v[64:67], a[16:19], v[80:95]\n\t" "v_cvt_pk_bf16_f32 v115, v102, v103\n\t" \
    "v_cvt_pk_bf16_f32 v116, v104, v105\n\t" "v_cvt_pk_bf16_f32 v117, v106, v107\n\t" "v_mfma_f32_32x32x16_bf16 v[80:95], v[68:71], a[20:23], v[80:95]\n\t" \
    "buffer_store_dwordx4 v[112:115], %[zo], %[rs], s63 offen offset:0 sc1\n\t" "v_cvt_pk_bf16_f32 v118, v108, v109\n\t" "v_cvt_pk_bf16_f32 v119, v110, v111\n\t" \
    "v_mfma_f32_32x32x16_bf16 v[80:95], v[72:75], a[24:27], v[80:95]\n\t" "v_mfma_f32_32x32x16_bf16 v[80:95], v[76:79], a[28:31], v[80:95]\n\t" \
    "buffer_store_dwordx4 v[116:119], %[zo], %[rs], s63 offen offset:512 sc1\n\t" "s_and_b32 s63, s60, %[rm]\n\t" "s_lshl_b32 s63, s63, 22\n\t" \
    "s_add_u32 s63, s63, %[zb]\n\t" "v_mfma_f32_32x32x16_bf16 v[96:111], v[48:51], a[32:35], 0\n\t" "buffer_load_dwordx4 v[16:19], %[ao], %[rs], s64 offen offset:0 sc1\n\t" \
    "s_nop 2\n\t" "v_mfma_f32_32x32x16_bf16 v[96:111], v[52:55], a[36:39], v[96:111]\n\t" "buffer_load_dwordx4 v[20:23], %[ao], %[rs], s64 offen offset:1024 sc1\n\t" \
    "s_nop 2\n\t" "v_mfma_f32_32x32x16_bf16 v[96:111], v[56:59], a[40:43], v[96:111]\n\t" "buffer_load_dwordx4 v[24:27], %[ao], %[rs], s64 offen offset:2048 sc1\n\t" \
    "s_nop 1\n\t" "v_mfma_f32_32x32x16_bf16 v[96:111], v[60:63], a[44:47], v[96:111]\n\t" "buffer_load_dwordx4 v[28:31], %[ao], %[rs], s64 offen offset:3072 sc1\n\t" \
    "v_cvt_pk_bf16_f32 v112, v80, v81\n\t" "v_cvt_pk_bf16_f32 v113, v82, v83\n\t" "v_cvt_pk_bf16_f32 v114, v84, v85\n\t" \
    "v_mfma_f32_32x32x16_bf16 v[96:111], v[64:67], a[48:51], v[96:111]\n\t" "buffer_load_dwordx4 v[32:35], %[a1], %[rs], s64 offen offset:0 sc1\n\t" \
    "v_cvt_pk_bf16_f32 v115, v86, v87\n\t" "v_cvt_pk_bf16_f32 v116, v88, v89\n\t" "v_cvt_pk_bf16_f32 v117, v90, v91\n\t" \
    "v_mfma_f32_32x32x16_bf16 v[96:111], v[68:71], a[52:55], v[96:111]\n\t" "buffer_load_dwordx4 v[36:39], %[a1], %[rs], s64 offen offset:1024 sc1\n\t" \
    "buffer_store_dwordx4 v[112:115], %[zo], %[rs], s63 offen offset:0 sc1\n\t" "v_cvt_pk_bf16_f32 v118, v92, v93\n\t" "v_cvt_pk_bf16_f32 v119, v94, v95\n\t" \
    "v_mfma_f32_32x32x16_bf16 v[96:111], v[72:75], a[56:59], v[96:111]\n\t" "buffer_load_dwordx4 v[40:43], %[a1], %[rs], s64 offen offset:2048 sc1\n\t" \
    "v_mfma_f32_32x32x16_bf16 v[96:111], v[76:79], a[60:63], v[96:111]\n\t" "buffer_load_dwordx4 v[44:47], %[a1], %[rs], s64 offen offset:3072 sc1\n\t" \
    "buffer_store_dwordx4 v[116:119], %[zo], %[rs], s63 offen offset:512 sc1\n\t" "s_add_u32 s63, s63, 0x20000\n\t" \
    "v_mfma_f32_32x32x16_bf16 v[80:95], v[48:51], a[64:67], 0\n\t" "s_nop 2\n\t" "v_mfma_f32_32x32x16_bf16 v[80:95], v[52:55], a[68:71], v[80:95]\n\t" "s_nop 2\n\t" \
    "v_mfma_f32_32x32x16_bf16 v[80:95], v[56:59], a[72:75], v[80:95]\n\t" "s_nop 1\n\t" "v_mfma_f32_32x32x16_bf16 v[80:95], v[60:63], a[76:79], v[80:95]\n\t" \
    "v_cvt_pk_bf16_f32 v112, v96, v97\n\t" "v_cvt_pk_bf16_f32 v113, v98, v99\n\t" "v_cvt_pk_bf16_f32 v114, v100, v101\n\t" \
    "v_mfma_f32_32x32x16_bf16 v[80:95], v[64:67], a[80:83], v[80:95]\n\t" "v_cvt_pk_bf16_f32 v115, v102, v103\n\t" "v_cvt_pk_bf16_f32 v116, v104, v105\n\t" \
    "v_cvt_pk_bf16_f32 v117, v106, v107\n\t" "v_mfma_f32_32x32x16_bf16 v[80:95], v[68:71], a[84:87], v[80:95]\n\t" \
    "buffer_store_dwordx4 v[112:115], %[zo], %[rs], s63 offen offset:0 sc1\n\t" "v_cvt_pk_bf16_f32 v118, v108, v109\n\t" "v_cvt_pk_bf16_f32 v119, v110, v111\n\t" \
    "v_mfma_f32_32x32x16_bf16 v[80:95], v[72:75], a[88:91], v[80:95]\n\t" "v_mfma_f32_32x32x16_bf16 v[80:95], v[76:79], a[92:95], v[80:95]\n\t" \
    "buffer_store_dwordx4 v[116:119], %[zo], %[rs], s63 offen offset:512 sc1\n\t" "s_add_u32 s63, s63, 0x20000\n\t" \
    "v_mfma_f32_32x32x16_bf16 v[96:111], v[48:51], a[96:99], 0\n\t" "s_nop 2\n\t" "v_mfma_f32_32x32x16_bf16 v[96:111], v[52:55], a[100:103], v[96:111]\n\t" "s_nop 2\n\t" \
    "v_mfma_f32_32x32x16_bf16 v[96:111], v[56:59], a[104:107], v[96:111]\n\t" "s_nop 1\n\t" "v_mfma_f32_32x32x16_bf16 v[96:111], v[60:63], a[108:111], v[96:111]\n\t" \
    "v_cvt_pk_bf16_f32 v112, v80, v81\n\t" "v_cvt_pk_bf16_f32 v113, v82, v83\n\t" "v_cvt_pk_bf16_f32 v114, v84, v85\n\t" \
    "v_mfma_f32_32x32x16_bf16 v[96:111], v[64:67], a[112:115], v[96:111]\n\t" "v_cvt_pk_bf16_f32 v115, v86, v87\n\t" "v_cvt_pk_bf16_f32 v116, v88, v89\n\t" \
    "v_cvt_pk_bf16_f32 v117, v90, v91\n\t" "v_mfma_f32_32x32x16_bf16 v[96:111], v[68:71], a[116:119], v[96:111]\n\t" \
    "buffer_store_dwordx4 v[112:115], %[zo], %[rs], s63 offen offset:0 sc1\n\t" "v_cvt_pk_bf16_f32 v118, v92, v93\n\t" "v_cvt_pk_bf16_f32 v119, v94, v95\n\t" \
    "v_mfma_f32_32x32x16_bf16 v[96:111], v[72:75], a[120:123], v[96:111]\n\t" "v_mfma_f32_32x32x16_bf16 v[96:111], v[76:79], a[124:127], v[96:111]\n\t" \
    "buffer_store_dwordx4 v[116:119], %[zo], %[rs], s63 offen offset:512 sc1\n\t" "s_add_u32 s63, s63, 0x20000\n\t" \
    "v_mfma_f32_32x32x16_bf16 v[80:95], v[48:51], a[128:131], 0\n\t" "buffer_load_dword v120, %[fo], %[rc], %[fv] offen sc1\n\t" "s_nop 2\n\t" \
    "v_mfma_f32_32x32x16_bf16 v[80:95], v[52:55], a[132:135], v[80:95]\n\t" "buffer_load_dword v121, %[fo], %[rc], %[fs] offen sc1\n\t" "s_nop 2\n\t" \
    "v_mfma_f32_32x32x16_bf16 v[80:95], v[56:59], a[136:139], v[80:95]\n\t" "s_nop 1\n\t" "v_mfma_f32_32x32x16_bf16 v[80:95], v[60:63], a[140:143], v[80:95]\n\t" \
    "v_cvt_pk_bf16_f32 v112, v96, v97\n\t" "v_cvt_pk_bf16_f32 v113, v98, v99\n\t" "v_cvt_pk_bf16_f32 v114, v100, v101\n\t" \
    "v_mfma_f32_32x32x16_bf16 v[80:95], v[64:67], a[144:147], v[80:95]\n\t" "v_cvt_pk_bf16_f32 v115, v102, v103\n\t" "v_cvt_pk_bf16_f32 v116, v104, v105\n\t" \
    "v_cvt_pk_bf16_f32 v117, v106, v107\n\t" "v_mfma_f32_32x32x16_bf16 v[80:95], v[68:71], a[148:151], v[80:95]\n\t" \
    "buffer_store_dwordx4 v[112:115], %[zo], %[rs], s63 offen offset:0 sc1\n\t" "v_cvt_pk_bf16_f32 v118, v108, v109\n\t" "v_cvt_pk_bf16_f32 v119, v110, v111\n\t" \
    "v_mfma_f32_32x32x16_bf16 v[80:95], v[72:75], a[152:155], v[80:95]\n\t" "v_mfma_f32_32x32x16_bf16 v[80:95], v[76:79], a[156:159], v[80:95]\n\t" \
    "buffer_store_dwordx4 v[116:119], %[zo], %[rs], s63 offen offset:512 sc1\n\t" "s_add_u32 s63, s63, 0x20000\n\t" \
    "v_mfma_f32_32x32x16_bf16 v[96:111], v[48:51], a[160:163], 0\n\t" "s_nop 2\n\t" "v_mfma_f32_32x32x16_bf16 v[96:111], v[52:55], a[164:167], v[96:111]\n\t" "s_nop 2\n\t" \
    "v_mfma_f32_32x32x16_bf16 v[96:111], v[56:59], a[168:171], v[96:111]\n\t" "s_nop 1\n\t" "s_waitcnt vmcnt(18)\n\t" "v_mov_b32 v122, s60\n\t" \
    "buffer_store_dword v122, %[fo], %[rc], %[fz] offen sc1\n\t" "v_mfma_f32_32x32x16_bf16 v[96:111], v[60:63], a[172:175], v[96:111]\n\t" \
    "v_cvt_pk_bf16_f32 v112, v80, v81\n\t" "v_cvt_pk_bf16_f32 v113, v82, v83\n\t" "v_cvt_pk_bf16_f32 v114, v84, v85\n\t" \
    "v_mfma_f32_32x32x16_bf16 v[96:111], v[64:67], a[176:179], v[96:111]\n\t" "v_cvt_pk_bf16_f32 v115, v86, v87\n\t" "v_cvt_pk_bf16_f32 v116, v88, v89\n\t" \
    "v_cvt_pk_bf16_f32 v117, v90, v91\n\t" "v_mfma_f32_32x32x16_bf16 v[96:111], v[68:71], a[180:183], v[96:111]\n\t" \
    "buffer_store_dwordx4 v[112:115], %[zo], %[rs], s63 offen offset:0 sc1\n\t" "v_cvt_pk_bf16_f32 v118, v92, v93\n\t" "v_cvt_pk_bf16_f32 v119, v94, v95\n\t" \
    "v_mfma_f32_32x32x16_bf16 v[96:111], v[72:75], a[184:187], v[96:111]\n\t" "v_mfma_f32_32x32x16_bf16 v[96:111], v[76:79], a[188:191], v[96:111]\n\t" \
    "buffer_store_dwordx4 v[116:119], %[zo], %[rs], s63 offen offset:512 sc1\n\t" "s_add_u32 s63, s63, 0x20000\n\t" \
    "v_mfma_f32_32x32x16_bf16 v[80:95], v[48:51], a[192:195], 0\n\t" "s_nop 2\n\t" "v_mfma_f32_32x32x16_bf16 v[80:95], v[52:55], a[196:199], v[80:95]\n\t" "s_nop 2\n\t" \
    "v_mfma_f32_32x32x16_bf16 v[80:95], v[56:59], a[200:203], v[80:95]\n\t" "s_nop 1\n\t" "v_mfma_f32_32x32x16_bf16 v[80:95], v[60:63], a[204:207], v[80:95]\n\t" \
    "v_cvt_pk_bf16_f32 v112, v96, v97\n\t" "v_cvt_pk_bf16_f32 v113, v98, v99\n\t" "v_cvt_pk_bf16_f32 v114, v100, v101\n\t" \
    "v_mfma_f32_32x32x16_bf16 v[80:95], v[64:67], a[208:211], v[80:95]\n\t" "v_cvt_pk_bf16_f32 v115, v102, v103\n\t" "v_cvt_pk_bf16_f32 v116, v104, v105\n\t" \
    "v_cvt_pk_bf16_f32 v117, v106, v107\n\t" "v_mfma_f32_32x32x16_bf16 v[80:95], v[68:71], a[212:215], v[80:95]\n\t" \
    "buffer_store_dwordx4 v[112:115], %[zo], %[rs], s63 offen offset:0 sc1\n\t" "v_cvt_pk_bf16_f32 v118, v108, v109\n\t" "v_cvt_pk_bf16_f32 v119, v110, v111\n\t" \
    "v_mfma_f32_32x32x16_bf16 v[80:95], v[72:75], a[216:219], v[80:95]\n\t" "v_mfma_f32_32x32x16_bf16 v[80:95], v[76:79], a[220:223], v[80:95]\n\t" \
    "buffer_store_dwordx4 v[116:119], %[zo], %[rs], s63 offen offset:512 sc1\n\t" "s_add_u32 s63, s63, 0x20000\n\t" \
    "v_mfma_f32_32x32x16_bf16 v[96:111], v[48:51], a[224:227], 0\n\t" "s_nop 2\n\t" "v_mfma_f32_32x32x16_bf16 v[96:111], v[52:55], a[228:231], v[96:111]\n\t" "s_nop 2\n\t" \
    "v_mfma_f32_32x32x16_bf16 v[96:111], v[56:59], a[232:235], v[96:111]\n\t" "s_nop 1\n\t" "v_mfma_f32_32x32x16_bf16 v[96:111], v[60:63], a[236:239], v[96:111]\n\t" \
    "v_cvt_pk_bf16_f32 v112, v80, v81\n\t" "v_cvt_pk_bf16_f32 v113, v82, v83\n\t" "v_cvt_pk_bf16_f32 v114, v84, v85\n\t" \
    "v_mfma_f32_32x32x16_bf16 v[96:111], v[64:67], a[240:243], v[96:111]\n\t" "v_cvt_pk_bf16_f32 v115, v86, v87\n\t" "v_cvt_pk_bf16_f32 v116, v88, v89\n\t" \
    "v_cvt_pk_bf16_f32 v117, v90, v91\n\t" "v_mfma_f32_32x32x16_bf16 v[96:111], v[68:71], a[244:247], v[96:111]\n\t" \
    "buffer_store_dwordx4 v[112:115], %[zo], %[rs], s63 offen offset:0 sc1\n\t" "v_cvt_pk_bf16_f32 v118, v92, v93\n\t" "v_cvt_pk_bf16_f32 v119, v94, v95\n\t" \
    "v_mfma_f32_32x32x16_bf16 v[96:111], v[72:75], a[248:251], v[96:111]\n\t" "s_waitcnt vmcnt(8)\n\t" "v_readfirstlane_b32 s66, v120\n\t" "v_readfirstlane_b32 s67, v121\n\t" \
    "v_mfma_f32_32x32x16_bf16 v[96:111], v[76:79], a[252:255], v[96:111]\n\t" "buffer_store_dwordx4 v[116:119], %[zo], %[rs], s63 offen offset:512 sc1\n\t" \
    "s_add_u32 s63, s63, 0x20000\n\t" "s_max_u32 s61, s61, s66\n\t" "s_max_u32 s62, s62, s67\n\t" "s_add_u32 s60, s60, 1\n\t" "s_cmp_eq_u32 s65, 0\n\t" ZSUM \
    "s_cbranch_scc1 30f\n\t" "33:\n\t" "s_branch 10b\n\t" "20:\n\t" "s_nop 7\n\t" "v_cvt_pk_bf16_f32 v112, v96, v97\n\t" "v_cvt_pk_bf16_f32 v113, v98, v99\n\t" \
    "v_cvt_pk_bf16_f32 v114, v100, v101\n\t" "v_cvt_pk_bf16_f32 v115, v102, v103\n\t" "v_cvt_pk_bf16_f32 v116, v104, v105\n\t" "v_cvt_pk_bf16_f32 v117, v106, v107\n\t" \
    "v_cvt_pk_bf16_f32 v118, v108, v109\n\t" "v_cvt_pk_bf16_f32 v119, v110, v111\n\t" "buffer_store_dwordx4 v[112:115], %[zo], %[rs], s63 offen offset:0 sc1\n\t" \
    "buffer_store_dwordx4 v[116:119], %[zo], %[rs], s63 offen offset:512 sc1\n\t" "s_waitcnt vmcnt(0)\n\t" "v_mov_b32 v122, s60\n\t" \
    "buffer_store_dword v122, %[fo], %[rc], %[fz] offen sc1\n\t" "s_cmp_eq_u32 s60, 64\n\t" "s_cbranch_scc1 90f\n\t" "s_add_u32 s67, s60, 1\n\t" "s_cmp_ge_u32 s61, s67\n\t" \
    "s_cbranch_scc1 22f\n\t" "21:\n\t" "buffer_load_dword v120, %[fo], %[rc], %[fv] offen sc1\n\t" "s_waitcnt vmcnt(0)\n\t" "v_readfirstlane_b32 s61, v120\n\t" "s_nop 3\n\t" \
    "s_cmp_ge_u32 s61, s67\n\t" "s_cbranch_scc1 22f\n\t" "s_cmp_eq_u32 %[bd], 0\n\t" "s_cbranch_scc1 22f\n\t" "s_sub_u32 %[bd], %[bd], 1\n\t" "s_sleep 2\n\t" \
    "s_branch 21b\n\t" "22:\n\t" "s_lshr_b32 s66, s60, 2\n\t" "s_xor_b32 s66, s66, %[xp]\n\t" "s_and_b32 s66, s66, 1\n\t" "s_lshl_b32 s66, s66, 13\n\t" \
    "s_lshl_b32 s64, s60, 19\n\t" "s_add_u32 s64, s64, s66\n\t" "s_add_u32 s64, s64, %[vb]\n\t" "buffer_load_dwordx4 v[48:51], %[ao], %[rs], s64 offen offset:0 sc1\n\t" \
    "buffer_load_dwordx4 v[52:55], %[ao], %[rs], s64 offen offset:1024 sc1\n\t" "buffer_load_dwordx4 v[56:59], %[ao], %[rs], s64 offen offset:2048 sc1\n\t" \
    "buffer_load_dwordx4 v[60:63], %[ao], %[rs], s64 offen offset:3072 sc1\n\t" "buffer_load_dwordx4 v[64:67], %[a1], %[rs], s64 offen offset:0 sc1\n\t" \
    "buffer_load_dwordx4 v[68:71], %[a1], %[rs], s64 offen offset:1024 sc1\n\t" "buffer_load_dwordx4 v[72:75], %[a1], %[rs], s64 offen offset:2048 sc1\n\t" \
    "buffer_load_dwordx4 v[76:79], %[a1], %[rs], s64 offen offset:3072 sc1\n\t" "s_waitcnt vmcnt(0)\n\t" "s_branch 23b\n\t" "60:\n\t" "s_sub_u32 s67, s60, %[rg]\n\t" \
    "s_cmp_ge_u32 s62, s67\n\t" "s_cbranch_scc1 62f\n\t" "61:\n\t" "buffer_load_dword v120, %[fo], %[rc], %[fs] offen sc1\n\t" "s_waitcnt vmcnt(0)\n\t" \
    "v_readfirstlane_b32 s62, v120\n\t" "s_nop 3\n\t" "s_cmp_ge_u32 s62, s67\n\t" "s_cbranch_scc1 62f\n\t" "s_cmp_eq_u32 %[bd], 0\n\t" "s_cbranch_scc1 62f\n\t" \
    "s_sub_u32 %[bd], %[bd], 1\n\t" "s_sleep 2\n\t" "s_branch 61b\n\t" "62:\n\t" "s_branch 63b\n\t" "30:\n\t" "s_nop 7\n\t" "v_cvt_pk_bf16_f32 v112, v96, v97\n\t" \
    "v_cvt_pk_bf16_f32 v113, v98, v99\n\t" "v_cvt_pk_bf16_f32 v114, v100, v101\n\t" "v_cvt_pk_bf16_f32 v115, v102, v103\n\t" "v_cvt_pk_bf16_f32 v116, v104, v105\n\t" \
    "v_cvt_pk_bf16_f32 v117, v106, v107\n\t" "v_cvt_pk_bf16_f32 v118, v108, v109\n\t" "v_cvt_pk_bf16_f32 v119, v110, v111\n\t" \
    "buffer_store_dwordx4 v[112:115], %[zo], %[rs], s63 offen offset:0 sc1\n\t" "buffer_store_dwordx4 v[116:119], %[zo], %[rs], s63 offen offset:512 sc1\n\t" \
    "s_waitcnt vmcnt(0)\n\t" "v_mov_b32 v122, s60\n\t" "buffer_store_dword v122, %[fo], %[rc], %[fz] offen sc1\n\t" "s_cmp_eq_u32 s60, 64\n\t" "s_cbranch_scc1 90f\n\t" \
    "s_add_u32 s67, s60, 1\n\t" "s_cmp_ge_u32 s61, s67\n\t" "s_cbranch_scc1 32f\n\t" "31:\n\t" "buffer_load_dword v120, %[fo], %[rc], %[fv] offen sc1\n\t" \
    "s_waitcnt vmcnt(0)\n\t" "v_readfirstlane_b32 s61, v120\n\t" "s_nop 3\n\t" "s_cmp_ge_u32 s61, s67\n\t" "s_cbranch_scc1 32f\n\t" "s_cmp_eq_u32 %[bd], 0\n\t" \
    "s_cbranch_scc1 32f\n\t" "s_sub_u32 %[bd], %[bd], 1\n\t" "s_sleep 2\n\t" "s_branch 31b\n\t" "32:\n\t" "s_lshr_b32 s66, s60, 2\n\t" "s_xor_b32 s66, s66, %[xp]\n\t" \
    "s_and_b32 s66, s66, 1\n\t" "s_lshl_b32 s66, s66, 13\n\t" "s_lshl_b32 s64, s60, 19\n\t" "s_add_u32 s64, s64, s66\n\t" "s_add_u32 s64, s64, %[vb]\n\t" \
    "buffer_load_dwordx4 v[16:19], %[ao], %[rs], s64 offen offset:0 sc1\n\t" "buffer_load_dwordx4 v[20:23], %[ao], %[rs], s64 offen offset:1024 sc1\n\t" \
    "buffer_load_dwordx4 v[24:27], %[ao], %[rs], s64 offen offset:2048 sc1\n\t" "buffer_load_dwordx4 v[28:31], %[ao], %[rs], s64 offen offset:3072 sc1\n\t" \
    "buffer_load_dwordx4 v[32:35], %[a1], %[rs], s64 offen offset:0 sc1\n\t" "buffer_load_dwordx4 v[36:39], %[a1], %[rs], s64 offen offset:1024 sc1\n\t" \
    "buffer_load_dwordx4 v[40:43], %[a1], %[rs], s64 offen offset:2048 sc1\n\t" "buffer_load_dwordx4 v[44:47], %[a1], %[rs], s64 offen offset:3072 sc1\n\t" \
    "s_waitcnt vmcnt(0)\n\t" "s_branch 33b\n\t" "70:\n\t" "s_sub_u32 s67, s60, %[rg]\n\t" "s_cmp_ge_u32 s62, s67\n\t" "s_cbranch_scc1 72f\n\t" "71:\n\t" \
    "buffer_load_dword v120, %[fo], %[rc], %[fs] offen sc1\n\t" "s_waitcnt vmcnt(0)\n\t" "v_readfirstlane_b32 s62, v120\n\t" "s_nop 3\n\t" "s_cmp_ge_u32 s62, s67\n\t" \
    "s_cbranch_scc1 72f\n\t" "s_cmp_eq_u32 %[bd], 0\n\t" "s_cbranch_scc1 72f\n\t" "s_sub_u32 %[bd], %[bd], 1\n\t" "s_sleep 2\n\t" "s_branch 71b\n\t" "72:\n\t" \
    "s_branch 73b\n\t" "90:\n\t"

#define T_DQ 1480
#define T_DV 1540
#define T_M1 150
#define T_DY 700
#define T_DX 1250
#define T_MIN 35
#define T_PEN 600
#define T_RETRY 1100
#define T_POLL 3
#define T_RST 12
#define T_U0 540
constexpr int T_OVH = 118;
constexpr unsigned NS(int ns) { return (unsigned)(ns * 64 / 10); }
struct Clk { unsigned cyc, g, k1, k2; };
DEV void anchor(Clk& k) {
  unsigned long long c, r[8];
  asm volatile("s_memtime %0\n\ts_memrealtime %1\n\ts_nop 2\n\ts_memrealtime %2\n\ts_nop 2\n\ts_memrealtime %3\n\ts_nop 2\n\ts_memrealtime %4\n\ts_nop 2\n\t"
               "s_memrealtime %5\n\ts_nop 2\n\ts_memrealtime %6\n\ts_nop 2\n\ts_memrealtime %7\n\ts_waitcnt lgkmcnt(0)"
               : "=s"(c), "=s"(r[0]), "=s"(r[1]), "=s"(r[2]), "=s"(r[3]), "=s"(r[4]), "=s"(r[5]), "=s"(r[6]) :: "memory");
  const unsigned a = (unsigned)r[0];
  unsigned n = 0;
#pragma unroll
  for (int i = 1; i < 7; ++i) asm("s_cmp_eq_u32 %1, %2\n\ts_addc_u32 %0, %0, 0" : "+s"(n) : "s"((unsigned)r[i]), "s"(a) : "scc");
  const unsigned nx = (a + 4u) << 6, cy = (unsigned)c + 12u + 16u * n;
  const float dc = (float)(cy - k.cyc), dg = (float)(nx - k.g);
  asm volatile("s_nop 0\n\tv_readfirstlane_b32 %0, %2\n\tv_readfirstlane_b32 %1, %3\n\ts_nop 3\n\ts_min_u32 %0, %0, 0x80000\n\ts_min_u32 %1, %1, 0x8000"
               : "=&s"(k.k1), "=&s"(k.k2) : "v"((unsigned)(dg * __builtin_amdgcn_rcpf(dc) * 65536.f)), "v"((unsigned)(dc * __builtin_amdgcn_rcpf(dg) * 65536.f)) : "scc");
  k.cyc = cy; k.g = nx;
}
DEV unsigned to_cyc(const Clk& k, unsigned g) {
  unsigned d;
  asm("s_max_i32 %0, %1, 0xffff8000\n\ts_min_i32 %0, %0, 0x8000\n\ts_mul_i32 %0, %0, %2\n\ts_ashr_i32 %0, %0, 16" : "=&s"(d) : "s"(g - k.g), "s"(k.k2) : "scc");
  return k.cyc + d - T_OVH;
}
DEV void wait_cyc(unsigned t) { asm volatile(TIMED_WAIT :: "s"(t) : CL_S); }
DEV int u_slot(int n, int par, int para) { return n == 2 ? R_U2 + par * 8192 : R_U + n * MB + para * 8192; }
DEV unsigned ask_stamps(Ctx& c, int par, int slot) { return __builtin_amdgcn_raw_buffer_load_b32(c.rs, amap(24576, par) + fresh_lane() * 128, slot, AUX_SC1); }
DEV unsigned latest(Ctx& c, const Clk& k, int par, int slot, unsigned v) {
  if (__builtin_expect(!allok(v), 0)) {
#pragma unroll 1
    do {
      __builtin_amdgcn_s_sleep(T_POLL);
      asm volatile("" ::: "memory");
      const unsigned t = __builtin_amdgcn_raw_buffer_load_b32(c.rs, v == SENT ? amap(24576, par) + fresh_lane() * 128 : 1 << 30, slot, AUX_SC1);
      v = v == SENT ? t : v;
    } while (!allok(v) && !spend(c));
  }
  int d = (int)(v - k.g);
  d = max(d, __builtin_amdgcn_update_dpp(d, d, 0x128, 0xf, 0xf, true));
  d = max(d, __builtin_amdgcn_update_dpp(d, d, 0x124, 0xf, 0xf, true));
  d = max(d, __builtin_amdgcn_update_dpp(d, d, 0x122, 0xf, 0xf, true));
  d = max(d, __builtin_amdgcn_update_dpp(d, d, 0x121, 0xf, 0xf, true));
  unsigned m;
  asm("s_max_i32 %0, %1, %2\n\ts_max_i32 %0, %0, %3\n\ts_max_i32 %0, %0, %4" : "=&s"(m)
      : "s"(__builtin_amdgcn_readlane(d, 0)), "s"(__builtin_amdgcn_readlane(d, 16)), "s"(__builtin_amdgcn_readlane(d, 32)), "s"(__builtin_amdgcn_readlane(d, 48)) : "scc");
  return k.g + m;
}
struct Plan { unsigned gx, dx; };
DEV unsigned smax(unsigned a, unsigned b) { return (int)(a - b) > 0 ? a : b; }
DEV unsigned second_try(const Plan& t, unsigned last) { return smax(last + NS(T_PEN), t.gx + t.dx + NS(T_M1 + T_RETRY)); }
DEV void advance(Plan& t, unsigned last) {
  const unsigned gy = t.gx + t.dx + NS(T_M1), dur = last - t.gx;
  const bool missed = (int)(last + NS(T_MIN) - gy) > 0;
  const unsigned nx = (missed ? second_try(t, last) : gy) + NS(T_DY);
  t.dx = dur < t.dx + NS(60) ? (3u * t.dx + dur) >> 2 : t.dx + NS(8);
  t.gx = nx;
}
DEV unsigned next_go(Ctx& c, Clk& k, Plan& t, int par, int slot, unsigned v) {
  SCHED();
  anchor(k);
  SCHED();
  advance(t, latest(c, k, par, slot, v));
  return to_cyc(k, t.gx);
}

DEV constexpr int qk_r(int j) { return j < 4 ? 0 : 1 + (j - 4) % 3; }
DEV constexpr int qk_i(int j) { return j < 4 ? j : (j - 4) / 3; }
DEV constexpr int qk_upto(int u) { return u < 6 ? u + 7 : u < 10 ? 12 + (u - 4) / 2 : u < 12 ? 15 : 16; }
DEV void qk_role(const Params& p, Ctx& c, int h) {
  constexpr int WL = 0, MEMS = 131072, KXL = 147456, QKX = 155648, QROW = 272;
  c.budget = SPIN_CAP;
  const int w = c.w;
  Clk k = {0u, 0u, 0u, 0u};
  anchor(k);
  v8u wa[32], wh[16];
  claim(c, 0);
#pragma unroll 1
  for (int i = 0; i < QK_AT; ++i) __builtin_amdgcn_s_sleep(16);
  {
    const int lane = fresh_lane(), g = lane >> 4, cc = lane & 15;
    const rsrc_t rq = mkrs(p.Wq, 1024 * 4096 * 2), rk = mkrs(p.Wk, 1024 * 4096 * 2);
    const int vo0 = opq((256 * w + 64 * (h & 1) + krow_lane(g)) * 8192 + (h * 128 + 8 * cc) * 2), vo1 = opq(vo0 ^ (64 * 8192));
    v4u row[16][8];
    auto request = [&](int u) {
#pragma unroll
      for (int r = 0; r < 8; ++r) row[u][r] = ld16(u < 8 ? rq : rk, (u & 2) ? vo1 : vo0, (32 * (u & 4) + krow(4 * (u & 1) + (r >> 1), r & 1)) * 8192);
    };
#pragma unroll
    for (int u = 0; u < qk_upto(-1); ++u) request(u);
    SCHED();
    const int sw = MEMS + w * 8192 + g * 256, lw = opq(WL + w * 32768 + lane * 16);
    int sr[2];
#pragma unroll
    for (int t = 0; t < 2; ++t) { const int i = (2 * cc + t) & 7; sr[t] = opq(i * 1024 + g * 256 + ((4 * w + (cc >> 2) + 2 * i) & 15) * 16); }
    sfor<16>([&](auto U) __attribute__((always_inline)) {
      constexpr int u = U.value;
#pragma unroll
      for (int i = 0; i < 8; ++i) {
        v4u f;
#pragma unroll
        for (int r = 0; r < 4; ++r) f[r] = prm(row[u][2 * r + 1][i >> 1], row[u][2 * r][i >> 1], i & 1);
        sts16(c.L, sw + i * 1024 + ((cc + 2 * i) & 15) * 16, f);
      }
      SCHED();
      if (qk_upto(u) > qk_upto(u - 1)) request(qk_upto(u - 1));
      BAR();
#pragma unroll
      for (int j = 0; j < 16; ++j) {
        if (qk_i(j) != ((u >> 1) & 3)) continue;
#pragma unroll
        for (int t = 0; t < 2; ++t) {
          const v4u o = lds16(c.L, sr[t] + MEMS + ((w + qk_r(j)) & 3) * 8192);
          if (u >= 8 && t == 1) sts16(c.L, lw + j * 2048 + (u & 1) * 1024, o);
          else {
            v8u& d = u < 8 ? wa[2 * j + t] : wh[j];
#pragma unroll
            for (int r = 0; r < 4; ++r) d[4 * (u & 1) + r] = o[r];
#define M_(r) asm volatile("" : "+" r(d))
            if (u & 1) { if (u < 8) AT_ACC(M_, 2 * j + t); else AT_HOME(M_, j); }
#undef M_
          }
        }
      }
      BAR();
    });
  }
  STAMP(0);
  BAR();
  const unsigned vote = wait_verdict(c);
  const int par = vote_b(vote), para = vote_a(vote);
  const int lane = fresh_lane(), g = lane >> 4, cc = lane & 15;
  const int moff = opq(amap(w * 4096 + lane * 16, par) + (h & 1) * 1024);
  const int idx = opq((lane & 1) ? 0xEEEE : 0x4444);
  TICK_DECL;
  const int l0 = opq(WL + w * 32768 + lane * 16), l1 = opq(MEMS + lane * 16);
  const int qw = opq(QKX + (2 * w + (cc >> 3)) * 256 + g * 64 + (cc & 1) * 16 + ((cc >> 1) & 3) * 4), kw = opq(QKX + (8 + 2 * g) * QROW + (32 * w + 2 * cc) * 2);
#define QK_PINS PIN_ACC0(wa); PIN_ACC1(wa); PIN_HOME(wh)
#define QK_IN [l0] "v"(l0), [l1] "v"(l1), [mo] "v"(moff), [ix] "v"(idx), [qw] "v"(qw), [kw] "v"(kw), [rs] "s"(c.rs), [sb] "s"(R_MEM + (s & 3) * MEM_SLOT)
#define QK_CL CL_S, CL_16_32, CL_160_200, CL_200_216, CL_216_256
  if (w == 3) {
    const int hq = opq(lane ? 1 << 30 : amap(24576 + h * 128, par));
    anchor(k);
    unsigned tg = k.cyc;
    Plan tt = {k.g - NS(100000), NS(T_DX)};
    for (int s = 0; s < NSEG; ++s) {
      const int pb = R_P + (s & 3) * P_SLOT;
      QK_PINS;
      asm volatile(QK_STEP3 : [bd] "+s"(c.budget) : QK_IN, [hq] "v"(hq), [pb] "s"(pb), [tg] "s"(tg), [ca] "s"(k.cyc - T_DQ), [k1] "s"(k.k1), [ga] "s"(k.g) : QK_CL);
      QK_PINS;
      tg = next_go(c, k, tt, par, pb, ask_stamps(c, par, pb));
      st4s(c.rs, hq, R_P + ((s + 2) & 3) * P_SLOT, fresh_sent());
      st4s(mkrs((const void*)c.ctrl, CTRL_BYTES), hq & (1 << 30), C_FLAG + (F_S + h) * 128, (unsigned)s + 1u);
      TICK_SUM(10);
    }
  } else {
    const int kx0 = opq(KXL + g * 512 + cc * 16), kmrd = opq(QKX + g * 16 + (8 + (cc & 7)) * QROW);
    unsigned seen = NOFLAG;
    if (w == 0) {
      const int prow = (2 * g + (cc & 1)) * 2 + ((cc >> 1) & 1);
      const int px = opq(amap((h >> 1) * 1024 + ((2 * (h & 1) + (cc >> 3)) * 16 + prow) * 16 + ((cc >> 2) & 1) * 8, par));
      const int pm = opq(cc < 8 ? amap(16384 + h * 128 + prow * 8 + (cc >> 2) * 4, par) : 1 << 30);
      const int ps = opq((cc & 1) ? 0x03020706 : 0x05040100);
      for (int s = 0; s < NSEG; ++s) {
        if (seen < (unsigned)s + 1u) seen = wait_flag(c, F_K + h, s + 1);
        QK_PINS;
        asm volatile(QK_STEP0 : [bd] "+s"(c.budget)
                     : QK_IN, [kx] "v"(kx0), [km] "v"(kmrd), [px] "v"(px), [pm] "v"(pm), [ps] "v"(ps), [pb] "s"(R_P + (s & 3) * P_SLOT), [pr] "s"(R_P + ((s + 1) & 3) * P_SLOT),
                       [kb] "s"(R_KX - MEMS + xblk(s, h, para)) : QK_CL);
        QK_PINS;
        TICK_SUM(10);
      }
    } else {
      const int qt = w - 1;
      const int px = opq(amap(qt ? 16384 + h * 1024 + lane * 16 : h * 512 + (g >> 1) * 256 + cc * 16 + (g & 1) * 8, para));
      const int pm = opq(g < 2 ? amap(49152 + (qt * 32 + h) * 256 + cc * 16 + g * 8, para) : 1 << 30);
      const int ps = opq(cc - 4 * g), hq = opq(g * 512 + (16 * qt + cc) * 16);
#define QK_TOK(STEP) for (int s = 0; s < NSEG; ++s) { QK_PINS; \
        asm volatile(STEP : [bd] "+s"(c.budget), [sq] "+s"(seen) \
                     : QK_IN, [kx] "v"(kx0), [km] "v"(kmrd), [px] "v"(px), [pm] "v"(pm), [ps] "v"(ps), [hq] "v"(hq), [qb] "s"(R_QX + xblk(s, h, para)), [tb] "s"(R_PT + (s & 7) * PT_SLOT), \
                       [tr] "s"(R_PT + ((s + 3) & 7) * PT_SLOT), [sn] "s"(s + 1), [fq] "s"(c.ctrl + (C_FLAG + (F_Q + h) * 128) / 4) : QK_CL); \
        QK_PINS; TICK_SUM(10); }
      if (w == 1) { QK_TOK(QK_STEP1) } else { QK_TOK(QK_STEP2) }
    }
  }
  TICK_OUT();
  if (c.budget == 0u) __hip_atomic_store(c.ctrl + C_TMO / 4, 1u, RLX_AGENT);
}

DEV void shuffle_cols(Ctx& c, int scratch, const v4u (&rows)[8], v4u (&o)[8]) {
  const int g = c.lane >> 4, cc = c.lane & 15;
#pragma unroll
  for (int i = 0; i < 8; ++i) {
    v4u f;
#pragma unroll
    for (int r = 0; r < 4; ++r) f[r] = prm(rows[2 * r + 1][i >> 1], rows[2 * r][i >> 1], i & 1);
    sts16(c.L, scratch + (8 * cc + i) * 64 + ((g ^ (cc & 3)) << 4), f);
  }
  asm volatile("s_waitcnt lgkmcnt(0)" ::: "memory");
#pragma unroll
  for (int i = 0; i < 8; ++i) { const int col = 16 * i + cc; o[i] = lds16(c.L, scratch + col * 64 + ((g ^ ((col >> 3) & 3)) << 4)); }
  asm volatile("s_waitcnt lgkmcnt(0)" ::: "memory");
}

DEV void vo_role(const Params& p, Ctx& c, int h) {
  constexpr int WL = 0, MEMS = 131072, VM = 147456, VROW = 272;
  c.budget = SPIN_CAP;
  const int w = c.w, lane = c.lane = fresh_lane(), g = lane >> 4, cc = lane & 15;
  Clk k = {0u, 0u, 0u, 0u};
  anchor(k);
  v8u wa[32], wh[16];
  const rsrc_t rwo = mkrs(p.Wo, 4096 * 1024 * 2);
  const int voo = (h * 128 + 4 * g) * 2048 + (256 * w + 8 * cc) * 2;
  v4u buf[3][8];
  auto request_o = [&](int u) {
#pragma unroll
    for (int r = 0; r < 8; ++r) buf[u % 3][r] = ld16(rwo, voo, (32 * (u & 3) + 16 * (r >> 2) + (r & 3)) * 2048 + (u >> 2) * 256);
  };
  {
    const rsrc_t rw = mkrs(p.Wv, 1024 * 4096 * 2);
    const int vo = (256 * w + krow_lane(g)) * 8192 + (h * 128 + 8 * cc) * 2;
    v4u vrow[8][8];
    auto request = [&](int u) {
#pragma unroll
      for (int r = 0; r < 8; ++r) vrow[u][r] = ld16(rw, vo, (64 * ((u >> 1) ^ (h & 1)) + krow(4 * (u & 1) + (r >> 1), r & 1)) * 8192);
    };
#pragma unroll
    for (int u = 0; u < 5; ++u) request(u);
    SCHED();
    claim(c, 0);
    SCHED();
    const int wr_ = MEMS + w * 8192 + cc * 512 + ((g ^ (cc & 3)) << 4);
#pragma unroll
    for (int u = 0; u < 8; ++u) {
#pragma unroll
      for (int i = 0; i < 8; ++i) {
        v4u f;
#pragma unroll
        for (int r = 0; r < 4; ++r) f[r] = prm(vrow[u][2 * r + 1][i >> 1], vrow[u][2 * r][i >> 1], i & 1);
        sts16(c.L, wr_ + i * 64, f);
      }
      SCHED();
      if (u + 5 < 8) request(u + 5); else if (u < 6) request_o(u - 3);
      BAR();
#pragma unroll
      for (int j = 0; j < 16; ++j) {
        if (qk_i(j) != (u >> 1)) continue;
#pragma unroll
        for (int t = 0; t < 2; ++t) {
          const int col = 32 * w + 2 * cc + t;
          const v4u o = lds16(c.L, MEMS + ((w + qk_r(j)) & 3) * 8192 + col * 64 + ((g ^ ((col >> 3) & 3)) << 4));
          if (t == 1 && (j & 1)) sts16(c.L, WL + (w * 16 + (j >> 1)) * 2048 + (u & 1) * 1024 + lane * 16, o);
          else {
            const int n = 3 * (j >> 1) + ((j & 1) ? 2 : t);
#pragma unroll
            for (int r = 0; r < 4; ++r) wa[n][4 * (u & 1) + r] = o[r];
#define M_(r) asm volatile("" : "+" r(wa[n]))
            if (u & 1) AT_ACC(M_, n);
#undef M_
          }
        }
      }
      BAR();
    }
  }
#pragma unroll
  for (int u = 0; u < 8; ++u) {
    v4u o[8];
    shuffle_cols(c, MEMS + w * 8192, buf[u % 3], o);
    SCHED();
    if (u + 3 < 8) request_o(u + 3);
#pragma unroll
    for (int i = 0; i < 4; ++i) {
      const int pr = 4 * (u >> 2) + i, ch = u & 3;
      const v8u f = {o[2 * i][0], o[2 * i + 1][0], o[2 * i][1], o[2 * i + 1][1], o[2 * i][2], o[2 * i + 1][2], o[2 * i][3], o[2 * i + 1][3]};
      if (ch < 2) {
        wh[2 * pr + ch] = f;
#define M_(r) asm volatile("" : "+" r(wh[2 * pr + ch]))
        AT_HOME(M_, 2 * pr + ch);
#undef M_
      } else if (ch == 2) {
        wa[24 + pr] = f;
#define M_(r) asm volatile("" : "+" r(wa[24 + pr]))
        AT_ACC(M_, 24 + pr);
#undef M_
      } else {
        sts16(c.L, WL + (w * 16 + 8 + pr) * 2048 + lane * 16, lo4(f));
        sts16(c.L, WL + (w * 16 + 8 + pr) * 2048 + 1024 + lane * 16, hi4(f));
      }
    }
    SCHED();
  }
  BAR();
  STAMP(0);
  const unsigned vote = wait_verdict(c);
  const int par = vote_b(vote), para = vote_a(vote);
  const int moff = opq(amap(w * 4096 + lane * 16, par) + (h & 1) * 1024);
  const int idx = opq((lane & 1) ? 0xEEEE : 0x4444);
  const int u_off = opq((16 * w + g) * 16384 + h * 256 + cc * 16);
  const int hq = opq((lane | w) ? 1 << 30 : amap(24576 + (32 + h) * 128, par));
  const int l0 = opq(WL + w * 32768 + lane * 16), l1 = opq(MEMS + lane * 16);
  const int vmw = opq(VM + 2 * g * VROW + (32 * w + 2 * cc) * 2);
  const int vmr = opq(VM + (cc >> 1) * VROW + g * 16);
  TICK_DECL;
  anchor(k);
  unsigned tg = k.cyc;
  Plan tt = {k.g - NS(100000), NS(T_DX)};
  int u3 = 0;
  for (int s = 0; s < NSEG; ++s) {
    const int pb = R_P + (s & 3) * P_SLOT;
    PIN_ACC0(wa); PIN_ACC1(wa); PIN_HOME(wh);
    asm volatile(VO_STEP
        : [bd] "+s"(c.budget)
        : [l0] "v"(l0), [l1] "v"(l1), [mo] "v"(moff), [ix] "v"(idx), [vw] "v"(vmw), [vr] "v"(vmr), [uo] "v"(u_off), [w] "s"(w), [rs] "s"(c.rs),
          [sb] "s"(R_MEM + (s & 3) * MEM_SLOT), [ub] "s"(u_slot(u3, par, para)), [tg] "s"(tg), [hq] "v"(hq), [pb] "s"(pb),
          [ca] "s"(k.cyc - T_DV), [k1] "s"(k.k1), [ga] "s"(k.g)
        : CL_S, CL_16_32, CL_160_200, CL_200_216, CL_216_256);
    PIN_ACC0(wa); PIN_ACC1(wa); PIN_HOME(wh);
    if (w == 0) tg = next_go(c, k, tt, par, pb, ask_stamps(c, par, pb)); else __builtin_amdgcn_s_sleep(T_RST);
    {
      const int ur = u_slot(u3 ? u3 - 1 : 2, par, para);
      const v4u se = {fresh_sent(), fresh_sent(), fresh_sent(), fresh_sent()};
#pragma unroll
      for (int i = 0; i < 4; ++i) st16s(c.rs, u_off, ur + i * 65536, se);
      st4s(c.rs, hq, R_P + ((s + 2) & 3) * P_SLOT, se[0]);
      u3 = u3 == 2 ? 0 : u3 + 1;
    }
    TICK_SUM(10);
  }
  TICK_OUT();
  if (c.budget == 0u) __hip_atomic_store(c.ctrl + C_TMO / 4, 1u, RLX_AGENT);
}

DEV void y_role(const Params& p, Ctx& c, int y) {
  constexpr int ZR = 0, UL = 131072, CW = 155648;
  c.budget = SPIN_CAP;
  const int w = c.w, lane = c.lane = fresh_lane(), g = lane >> 4, cc = lane & 15;
  const v4u sent4 = {SENT, SENT, SENT, SENT};
  STAMP(2);
  Clk k = {0u, 0u, 0u, 0u};
  anchor(k);
  unsigned m0 = 0;
  if (w == 0) {
    unsigned best[4] = {~0u, ~0u, ~0u, ~0u};
#pragma unroll
    for (int r = 0; r < 8; ++r) {
      VMWAIT(0);
      const unsigned t0 = (unsigned)__builtin_amdgcn_s_memtime();
      LGKM(0);
      st4s(c.rs, ((r & 2) ? amap(y * 256, 0) : y * 16384) + (r & 1) * 8192 + (lane & 31) * 4, (r & 2) ? R_MEM : R_U, SENT);
      VMWAIT(0);
      const unsigned t1 = (unsigned)__builtin_amdgcn_s_memtime();
      LGKM(0);
      best[r & 3] = min(best[r & 3], t1 - t0);
    }
    STAMP(5);
    claim(c, CL_VOTED | (best[1] < best[0] ? CL_A : 0u) | (best[3] < best[2] ? CL_B : 0u));
    m0 = *(const GAS unsigned*)(p.mem0 + (lane >> 3) * 1024 + 16 * y + 2 * (lane & 7));
    m0 = m0 < SENT - 1u ? m0 : SENT - 1u;
    if (lane < 8) *(LAS unsigned*)(c.L + CW + lane * 4) = 0u;
    const unsigned v = wait_vote(c);
    STAMP(7);
    const int oa = y * 16384 + vote_a(v) * 8192 + lane * 16, ob = amap(y * 3072 + lane * 16, vote_b(v));
#pragma unroll
    for (int i = 0; i < 16; ++i) st16s(c.rs, oa + (i & 3) * 1024, R_U + (i >> 3) * MB + ((i >> 2) & 1) * 4096, sent4);
#pragma unroll
    for (int i = 0; i < 3; ++i) st16s(c.rs, amap(y * 3072 + i * 1024 + lane * 16, vote_b(v)), R_MEM, sent4);
#pragma unroll
    for (int i = 0; i < 8; ++i) st16s(c.rs, oa + (i & 3) * 1024, R_PT + (i >> 2) * 4096, sent4);
    (void)ob;
    VMWAIT(0);
    if (lane == 0) __hip_atomic_fetch_add(c.ctrl + (C_CLAIM + c.att * 1024) / 4 + c.slot, CL_FILLED, RLX_AGENT);
    STAMP(6);
  } else if (w < 3) {
#pragma unroll 1
    for (int i = 0; i < PULL_AT; ++i) __builtin_amdgcn_s_sleep(35);
    const int o = ((2 * y + w - 1) * 64 + lane) * 128;
    auto pull = [&](const u16* q) {
#pragma unroll
      for (int i = 0; i < 4; ++i) m0 += __builtin_amdgcn_raw_buffer_load_b32(mkrs(q, 4 * MB), o, i * MB, 0);
    };
    pull(p.bk); pull(p.bv); pull(p.ssmk); pull(p.bq);
    asm volatile("" :: "v"(m0));
  }
  BAR();
  const unsigned vote = wait_verdict(c);
  const int par = vote_b(vote), para = vote_a(vote);
  const int mo = amap(y * 256 + lane * 4, par);
  if (w == 0) st4s(c.rs, mo, R_MEM, m0);
  STAMP(0);
  volatile LAS unsigned* const GO = (volatile LAS unsigned*)(c.L + CW);
  TICK_DECL;
  if (w == 0) {
    int xo[4];
#pragma unroll
    for (int i = 0; i < 4; ++i) xo[i] = amap(i * 4096 + lane * 16, par);
    const int pmo = amap(16384 + 2 * g * 128 + cc * 8, par), psel = (cc & 1) ? 0x03020706 : 0x05040100;
    const int lead = T_U0 - 128 * ((y >> 2) & 3), late = __builtin_amdgcn_readfirstlane(opq(lead < 264));
    anchor(k);
    Plan tt = {k.g - NS(100000), NS(T_DX)};
    unsigned done = k.cyc;
    const int so = amap(24576, par) + lane * 128;
    const int idx = opq((lane & 1) ? 0xEEEE : 0x4444);
    const int uo = y * 16384 + lane * 16;
    int u3 = 0;
    const int mw = amap(y * 256 + (2 * g + (cc & 1)) * 32 + (cc >> 1) * 4, par);
    for (int s = 0; s < NSEG - 1; ++s) {
      if (s == 0) RAWBAR();
      v32u z0, z1, z2, z3;
      asm volatile(
          "ds_read_b128 a[0:3], %[za] offset:0\n\t" "ds_read_b128 a[4:7], %[za] offset:1024\n\t" "ds_read_b128 a[8:11], %[za] offset:2048\n\t" "ds_read_b128 a[12:15], %[za] offset:3072\n\t"
          "ds_read_b128 a[16:19], %[za] offset:4096\n\t" "ds_read_b128 a[20:23], %[za] offset:5120\n\t" "ds_read_b128 a[24:27], %[za] offset:6144\n\t" "ds_read_b128 a[28:31], %[za] offset:7168\n\t"
          "ds_read_b128 a[32:35], %[za] offset:8192\n\t" "ds_read_b128 a[36:39], %[za] offset:9216\n\t" "ds_read_b128 a[40:43], %[za] offset:10240\n\t" "ds_read_b128 a[44:47], %[za] offset:11264\n\t"
          "ds_read_b128 a[48:51], %[za] offset:12288\n\t" "ds_read_b128 a[52:55], %[za] offset:13312\n\t" "ds_read_b128 a[56:59], %[za] offset:14336\n\t" "ds_read_b128 a[60:63], %[za] offset:15360\n\t"
          "ds_read_b128 a[64:67], %[za] offset:16384\n\t" "ds_read_b128 a[68:71], %[za] offset:17408\n\t" "ds_read_b128 a[72:75], %[za] offset:18432\n\t" "ds_read_b128 a[76:79], %[za] offset:19456\n\t"
          "ds_read_b128 a[80:83], %[za] offset:20480\n\t" "ds_read_b128 a[84:87], %[za] offset:21504\n\t" "ds_read_b128 a[88:91], %[za] offset:22528\n\t" "ds_read_b128 a[92:95], %[za] offset:23552\n\t"
          "ds_read_b128 a[96:99], %[za] offset:24576\n\t" "ds_read_b128 a[100:103], %[za] offset:25600\n\t" "ds_read_b128 a[104:107], %[za] offset:26624\n\t" "ds_read_b128 a[108:111], %[za] offset:27648\n\t"
          "ds_read_b128 a[112:115], %[za] offset:28672\n\t" "ds_read_b128 a[116:119], %[za] offset:29696\n\t" "ds_read_b128 a[120:123], %[za] offset:30720\n\t" "ds_read_b128 a[124:127], %[za] offset:31744\n\t"
          "s_waitcnt lgkmcnt(0)"
          : "={a[0:31]}"(z0), "={a[32:63]}"(z1), "={a[64:95]}"(z2), "={a[96:127]}"(z3) : [za] "v"(ZR + (s & 3) * 32768 + lane * 16) : "memory");
      const int pb = R_P + (s & 3) * P_SLOT;
      v32u uu;
      const int ub = u_slot(u3, par, para);
      TICK(0);
      anchor(k);
      TICK(8);
      unsigned tg = to_cyc(k, tt.gx + tt.dx + NS(T_M1)), lt = late, sv = SENT;
      unsigned bad = (int)(done - to_cyc(k, tt.gx)) > T_OVH;
      TICK(4);
#pragma unroll 1
      for (;;) {
        if (bad) {
          const bool all = allok(sv);
          const unsigned last = latest(c, k, par, pb, sv);
          if (!all) anchor(k);
          tg = to_cyc(k, second_try(tt, last));
          lt = 1;
        }
        asm volatile(Y_STEP
          : "=&{v[144:175]}"(uu), [bad] "=&s"(bad), [sv] "=&v"(sv)
          : [x0] "v"(xo[0]), [x1] "v"(xo[1]), [x2] "v"(xo[2]), [x3] "v"(xo[3]), [pm] "v"(pmo), [u0] "v"(uo), [u1] "v"(uo + 4096), [mw] "v"(mw), [ps] "v"(psel), [ix] "v"(idx),
            [rs] "s"(c.rs), [pb] "s"(pb), [ub] "s"(ub), [mb] "s"(R_MEM + ((s + 1) & 3) * MEM_SLOT), [mr] "s"(R_MEM + ((s + 3) & 3) * MEM_SLOT), [tu] "s"(tg - (lt ? 128 : lead)), [tg] "s"(tg), [late] "s"(lt),
            [so] "v"(so), [bd] "s"(c.budget),
            "{a[0:31]}"(z0), "{a[32:63]}"(z1), "{a[64:95]}"(z2), "{a[96:127]}"(z3)
          : CL_S, CL_120_128, CL_176_200, CL_200_216, CL_216_256);
        bad = __builtin_amdgcn_readfirstlane(bad);
        if (bad != 1u) break;
        spend(c);
      }
      done = bad;
      SCHED();
      const int ul = opq(UL + lane * 16);
      u3 = u3 == 2 ? 0 : u3 + 1;
#pragma unroll
      for (int i = 0; i < 8; ++i) sts16(c.L, ul + i * 1024, v4u{uu[4 * i], uu[4 * i + 1], uu[4 * i + 2], uu[4 * i + 3]});
      asm volatile("" ::: "memory");
      *GO = s + 1;
      asm volatile("s_waitcnt vmcnt(2)" : "+v"(sv));
      advance(tt, latest(c, k, par, pb, sv));
      TICK(3);
      TICK_LDS(CW + 256);
    }
    {
      anchor(k);
      wait_cyc(to_cyc(k, tt.gx + tt.dx + NS(T_M1)));
      const int ub = u_slot(u3, par, para);
      v4u u[8];
#pragma unroll 1
      for (;;) {
        asm volatile("" ::: "memory");
#pragma unroll
        for (int i = 0; i < 8; ++i) u[i] = ld16s(c.rs, uo + (i & 3) * 1024, ub + (i >> 2) * 4096);
        SCHED();
        unsigned t = 0;
#pragma unroll
        for (int i = 0; i < 8; ++i) t = umax(t, top(u[i]));
        if (allok(t) || spend(c)) break;
      }
      RAWBAR();
#pragma unroll
      for (int i = 0; i < 8; ++i) sts16(c.L, UL + i * 1024 + lane * 16, u[i]);
      asm volatile("" ::: "memory");
      *GO = NSEG;
    }
    TICK_DUMP(CW + 256);
  } else if (w < 3) {
    const rsrc_t ro = mkrs(p.out, 2048 * 2048 * 2);
    auto tokens = [&](auto QT) {
      constexpr int qt = QT.value, NX = qt ? 32 : 16;
      RAWBAR();
      for (int s = 0; s < NSEG; ++s) {
        const int tb = R_PT + (s & 7) * PT_SLOT + para * 8192;
        const int ua = opq(UL + lane * 16);
        const int za = opq(ZR + (s & 3) * 32768 + (qt ? lane * 16 : (g >> 1) * 1024 + (g & 1) * 512 + cc * 16));
        SCHED();
        RAWBAR();
        TICK(0);
        f32x4 a;
#pragma unroll 1
        for (;;) {
          asm volatile("" ::: "memory");
          v4u pt[NX + 8];
#pragma unroll
          for (int i = 0; i < NX; ++i) pt[i] = ld16s(c.rs, (i & 3) * 1024 + lane * 16, tb + amap(qt * 16384 + (i >> 2) * 4096, 0));
#pragma unroll
          for (int i = 0; i < 8; ++i) pt[NX + i] = ld16s(c.rs, (i & 3) * 1024 + lane * 16, tb + amap(49152 + qt * 8192 + (i >> 2) * 4096, 0));
          SCHED();
          TICK(1);
#pragma unroll 1
          while (__builtin_amdgcn_readfirstlane(*GO) < (unsigned)s + 1u) { if (spend(c)) break; }
          asm volatile("" ::: "memory");
          f32x4 d[2] = {f32x4{0.f, 0.f, 0.f, 0.f}, f32x4{0.f, 0.f, 0.f, 0.f}};
          v4u fa[8];
          auto frag = [&](int i) {
            if (i >= NX) fa[i & 7] = lds16(c.L, ua + (i - NX) * 1024);
            else if (qt) fa[i & 7] = lds16(c.L, za + i * 1024);
            else {
              const v2u lo = *(LAS v2u*)(c.L + za + i * 2048), hi = *(LAS v2u*)(c.L + za + i * 2048 + 256);
              fa[i & 7] = v4u{lo.x, lo.y, hi.x, hi.y};
            }
          };
#pragma unroll
          for (int i = 0; i < 7; ++i) frag(i);
          SCHED();
#pragma unroll
          for (int i = 0; i < NX + 8; ++i) {
            if (i + 7 < NX + 8) frag(i + 7);
            d[i & 1] = mfma16(fa[i & 7], pt[i], d[i & 1]);
            SCHED();
          }
          a = d[0] + d[1];
          if (!__any(a[0] != a[0])) break;
          unsigned t = 0;
#pragma unroll
          for (int i = 0; i < NX + 8; ++i) t = umax(t, top(pt[i]));
          if (allok(t) || spend(c)) break;
        }
        __builtin_amdgcn_raw_buffer_store_b64(v2u{pk(a[0], a[1]), pk(a[2], a[3])}, ro, (16 * qt + cc) * 4096 + (16 * y + 4 * g) * 2, s * 32 * 4096, AUX_NT);
        TICK(2);
        TICK_SUM(3);
      }
    };
    if (w == 1) tokens(IC<0>{}); else tokens(IC<1>{});
  } else {
    unsigned seenZ = NOFLAG;
    const rsrc_t rc = mkrs((const void*)c.ctrl, CTRL_BYTES);
    const int fo = lane < 32 ? lane * 128 : 1 << 30;
    __builtin_amdgcn_s_setprio(3);
    auto least = [&](unsigned v) {
      v |= lane < 32 ? 0u : ~0u;
#pragma unroll
      for (int o = 1; o < 32; o <<= 1) v = min(v, (unsigned)__shfl_xor((int)v, o));
      seenZ = umax(seenZ, __builtin_amdgcn_readfirstlane(v));
    };
    for (int n = 0; n < NSEG; ++n) {
      TICK(0);
#pragma unroll 1
      while (seenZ < (unsigned)n + 1u) {
        asm volatile("" ::: "memory");
        least(__builtin_amdgcn_raw_buffer_load_b32(rc, fo, C_FLAG + (F_Z + (y >> 4) * 32) * 128, AUX_SC1));
        if (seenZ >= (unsigned)n + 1u || spend(c)) break;
        __builtin_amdgcn_s_sleep(4);
      }
      __builtin_amdgcn_fence(__ATOMIC_ACQUIRE, "wavefront");
      TICK(1);
      v4u z[32];
#pragma unroll
      for (int i = 0; i < 32; ++i) {
        z[i] = ld16s(c.rs, (i & 3) * 1024 + lane * 16, R_Z + (n & (ZRING - 1)) * Z_SLOT + y * 65536 + ((para ^ (y >> 5)) & 1) * 8192 + amap((i >> 2) * 4096, 0));
        SCHED();
        asm volatile("s_nop " STR(Z_GAP));
        SCHED();
      }
      const unsigned look = __builtin_amdgcn_raw_buffer_load_b32(rc, seenZ < (unsigned)n + 2u ? fo : 1 << 30, C_FLAG + (F_Z + (y >> 4) * 32) * 128, AUX_SC1);
      SCHED();
      TICK(4);
      const int za = opq(ZR + (n & 3) * 32768 + lane * 16);
#pragma unroll
      for (int i = 0; i < 32; ++i) {
        sts16(c.L, za + i * 1024, z[i]);
        if (i == 30) { TICK(3); }
      }
      least(look);
      TICK(2);
      { const int s = n; (void)s; TICK_SUM(3); }
      BAR();
    }
    RAWBAR();
  }
  TICK_OUT();
  if (c.budget == 0u) __hip_atomic_store(c.ctrl + C_TMO / 4, 1u, RLX_AGENT);
}

#define MF1A(d, a, b) asm volatile(MF16 "%0, %1, %2, %0" : "+v"(d) : "a"(a), "v"(b))
#define MF32A(d, a, b) asm volatile("v_mfma_f32_32x32x16_bf16 %0, %1, %2, %0" : "+v"(d) : "a"(a), "v"(b))
#define MF32AZ(d, a, b) asm volatile("v_mfma_f32_32x32x16_bf16 %0, %1, %2, 0" : "=&v"(d) : "a"(a), "v"(b))
#define MF1B(d, a, b) asm volatile(MF16 "%0, %1, %2, %0" : "+v"(d) : "v"(a), "a"(b))
#define MF1BZ(d, a, b) asm volatile(MF16 "%0, %1, %2, 0" : "=&v"(d) : "v"(a), "a"(b))
#define MF1AZ(d, a, b) asm volatile(MF16 "%0, %1, %2, 0" : "=&v"(d) : "a"(a), "v"(b))
DEV void g_role(const Params& p, Ctx& c, int mat, int h, int gi) {
  constexpr int EX = 131072;
  c.budget = SPIN_CAP;
  const int w = c.w, lane = c.lane = fresh_lane(), g = lane >> 4, cc = lane & 15, h2 = lane >> 5, r32 = lane & 31;
  if (mat < 2) {
    claim(c, 0);
#pragma unroll 1
    for (int i = 0; i < (mat ? GK_AT : GQ_AT); ++i) __builtin_amdgcn_s_sleep(16);
  }
  if (mat == 0) {
    unsigned t = 0;
#pragma unroll
    for (int i = 0; i < 4; ++i) t += __builtin_amdgcn_raw_buffer_load_b32(mkrs(p.x, 4 * MB), ((4 * h + w) * 64 + lane) * 128, i * MB, 0);
    asm volatile("" :: "v"(t));
  }
  const rsrc_t rx = mkrs(p.x, 2048 * 2048);
  const int xs = w * 16384 + ((lane * 16) ^ ((w & 1) << 7));
#pragma unroll
  for (int u = 0; u < 16; ++u) dma16(rx, c.L + w * 16384 + u * 1024, (xs ^ ((u >> 1) << 4)) + u * 1024);
  SCHED();
  v32u wa[8];
  {
    const rsrc_t rw = mkrs(mat == 0 ? p.Wq : mat == 1 ? p.Wk : p.Wv, 1024 * 4096 * 2);
    const int vo = (256 * w + 8 * g) * 8192 + (h * 128 + 8 * cc) * 2;
    v4u row[8][8];
    auto request = [&](int u) {
#pragma unroll
      for (int r = 0; r < 8; ++r) row[u][r] = ld16(rw, vo, (32 * u + r) * 8192);
    };
#pragma unroll
    for (int u = 0; u < 6; ++u) request(u);
    SCHED();
    if (mat == 2) claim(c, 0);
    SCHED();
    const int col = 32 * w + 16 * (r32 >> 4) + 8 * ((r32 >> 2) & 1) + 4 * ((r32 >> 3) & 1) + (r32 & 3);
    const int sw_ = opq(EX + w * 8192 + cc * 512 + ((g ^ (cc & 3)) << 4)), sr_ = EX + col * 64;
    const int sr0 = opq(sr_ + ((h2 ^ ((col >> 3) & 3)) << 4)), sr1 = opq(sr_ + (((2 + h2) ^ ((col >> 3) & 3)) << 4));
#pragma unroll
    for (int u = 0; u < 8; ++u) {
#pragma unroll
      for (int i = 0; i < 8; ++i) {
        v4u f;
#pragma unroll
        for (int r = 0; r < 4; ++r) f[r] = prm(row[u][2 * r + 1][i >> 1], row[u][2 * r][i >> 1], i & 1);
        sts16(c.L, sw_ + i * 64, f);
      }
      SCHED();
      if (u + 6 < 8) request(u + 6);
      BAR();
#pragma unroll
      for (int q = 0; q < 4; ++q) {
        v8u f = cat(lds16(c.L, sr0 + q * 8192), lds16(c.L, sr1 + q * 8192));
#define M_(r) asm volatile("" : "+" r(f))
        AT_ACC(M_, 8 * q + u);
#undef M_
#pragma unroll
        for (int r = 0; r < 8; ++r) wa[2 * q + (u >> 2)][8 * (u & 3) + r] = f[r];
      }
      BAR();
    }
  }
  STAMP(0);
  const int xpar = vote_a(wait_vote(c)) ^ (mat >> 1);
  TICK_DECL;
  asm volatile(G_LOOP
      :: [xo] "v"(r32 * 2048 + ((h2 ^ (r32 & 15)) << 4)), [xs] "v"(xs), [so] "v"((4 * w + h2) * 512 + r32 * 16), [fo] "v"((w | lane) ? 1 << 30 : 0),
         [rs] "s"(c.rs), [rx] "s"(rx), [rc] "s"(mkrs((const void*)c.ctrl, CTRL_BYTES)), [fl] "s"(C_FLAG + (mat * 32 + h) * 128), [xp] "s"(xpar),
         [ob] "s"((mat == 1 ? R_KX : R_QX) + h * 16384), [lw] "s"(w * 16384), GTRACE ACC_IN(wa)
      : CL_S, CL_16_32, CL_32_100);
}

DEV void zc_role(const Params& p, Ctx& c, int h) {
  c.budget = SPIN_CAP;
  const int w = c.w, lane = c.lane = fresh_lane(), g = lane >> 4, cc = lane & 15, h2 = lane >> 5, r32 = lane & 31;
  v32u wa[8];
  {
    const rsrc_t rw = mkrs(p.Wo, 4096 * 1024 * 2);
    const int vo = (h * 128 + 8 * g) * 2048 + (256 * w + 8 * cc) * 2;
    v4u row[8][8];
    auto request = [&](int u) {
#pragma unroll
      for (int r = 0; r < 8; ++r) row[u][r] = ld16(rw, vo, (32 * (u & 3) + r) * 2048 + (u >> 2) * 256);
    };
#pragma unroll
    for (int u = 0; u < 2; ++u) request(u);
    SCHED();
    claim(c, 0);
    SCHED();
#pragma unroll
    for (int u = 2; u < 6; ++u) request(u);
    const int sw_ = opq(w * 8192 + cc * 512 + ((g ^ (cc & 3)) << 4)), sr_ = w * 8192 + r32 * 64;
    const int sr0 = opq(sr_ + ((h2 ^ (r32 >> 3)) << 4)), sr1 = opq(sr_ + (((2 + h2) ^ (r32 >> 3)) << 4));
#pragma unroll
    for (int u = 0; u < 8; ++u) {
#pragma unroll
      for (int i = 0; i < 8; ++i) {
        v4u f;
#pragma unroll
        for (int r = 0; r < 4; ++r) f[r] = prm(row[u][2 * r + 1][i >> 1], row[u][2 * r][i >> 1], i & 1);
        sts16(c.L, sw_ + i * 64, f);
      }
      SCHED();
      if (u + 6 < 8) request(u + 6);
      LGKM(0);
#pragma unroll
      for (int q = 0; q < 4; ++q) {
        v8u f = cat(lds16(c.L, sr0 + q * 2048), lds16(c.L, sr1 + q * 2048));
#define M_(r) asm volatile("" : "+" r(f))
        AT_ACC(M_, 4 * (4 * (u >> 2) + q) + (u & 3));
#undef M_
#pragma unroll
        for (int r = 0; r < 8; ++r) wa[4 * (u >> 2) + q][8 * (u & 3) + r] = f[r];
      }
      LGKM(0);
      SCHED();
    }
  }
  STAMP(0);
  BAR();
  const int para = vote_a(wait_verdict(c));
  const int ao = h2 * 512 + (16 * ((r32 >> 3) & 1) + 8 * (r32 >> 4) + (r32 & 7)) * 16;
  TICK_DECL;
  asm volatile(ZC_LOOP
      : [bd] "+s"(c.budget)
      : [ao] "v"(ao), [a1] "v"(ao + 4096), [zo] "v"((16 * w + (r32 >> 4)) * 65536 + (h2 * 16 + cc) * 16 + ((para ^ (w >> 1)) & 1) * 8192 + amap(h * 1024, 0)), [fo] "v"(lane ? 1 << 30 : 0),
        [rs] "s"(c.rs), [rc] "s"(mkrs((const void*)c.ctrl, CTRL_BYTES)), [fv] "s"(C_FLAG + (F_V + h) * 128), [fs] "s"(C_FLAG + (F_S + h) * 128),
        [fz] "s"(C_FLAG + (F_Z + w * 32 + h) * 128), [xp] "s"(para ^ 1), [vb] "s"(R_QX + h * 16384), [zb] "n"(R_Z), [rg] "n"(ZRING - 2), [rm] "n"(ZRING - 1), GTRACE ACC_IN(wa)
      : CL_S, CL_16_32, CL_32_100);
  if (c.budget == 0u) __hip_atomic_store(c.ctrl + C_TMO / 4, 1u, RLX_AGENT);
}

DEV void conv_role(const Params& p, Ctx& c, int cg, int far) {
  constexpr int CH = 5200, U8 = 0;
  constexpr int CP = 4160, FC = 41600;
  constexpr int YS = FC + 16 * CP, RED = YS + 32768;
  static_assert(RED + 8192 <= LDS_BYTES - 16, "lds");
  c.budget = SPIN_CAP;
  const int w = c.w, lane = c.lane = fresh_lane(), tid = w * 64 + lane;
  const int d0 = 8 * cg;
  const int pg = ((far ^ (cg >> 5)) & 1) * 8192;
  auto crow = [](int ch) { return (ch >> 1) * 16384 + (ch & 1) * 4096; };
  const int de = (cg >> 3) * 64 + (tid & 7) * 8, t0 = (cg & 7) * 256 + (tid >> 3) * 8;
  auto group_sync = [&](int phase) {
    gu32* cnt = c.ctrl + (C_CONV + (cg >> 3) * 16 + phase * 8) / 4;
    VMWAIT(0);
    BAR();
    if (w == 0) {
      if (lane == 0) __hip_atomic_fetch_add(cnt, 1u, RLX_AGENT);
#pragma unroll 1
      while (__builtin_amdgcn_readfirstlane(__hip_atomic_load(cnt, RLX_AGENT)) < 8u) { if (spend(c)) break; __builtin_amdgcn_s_sleep(4); }
    }
    BAR();
  };
  v4u kv[8];
  BAR();
  {
    const rsrc_t rk = mkrs(p.bk, 2048 * 2048), rv = mkrs(p.bv, 2048 * 2048);
    v4u kr[11], vr[8], fr[8];
#pragma unroll
    for (int i = 0; i < 11; ++i) kr[i] = ld16(rk, (t0 + i - 3) * 2048 + de * 2, 0);
#pragma unroll
    for (int i = 0; i < 8; ++i) vr[i] = ld16(rv, (t0 + i) * 2048 + de * 2, 0);
    v4u sh[4];
#pragma unroll
    for (int i = 0; i < 4; ++i) sh[i] = *(const GAS v4u*)(p.shift + de * 4 + i * 8);
#pragma unroll
    for (int i = 0; i < 8; ++i) fr[i] = *(const GAS v4u*)(p.ssmk + (size_t)(d0 + i) * 2048 + tid * 8);
    if (tid < 40) sts16(c.L, U8 + (tid / 5) * CH + (tid % 5) * 16, v4u{0, 0, 0, 0});
    if (tid < 64) sts16(c.L, FC + (tid >> 2) * CP + 4096 + (tid & 3) * 16, v4u{0, 0, 0, 0});
#pragma unroll
    for (int ch = 0; ch < 8; ++ch) {
      const unsigned sa = sh[ch >> 1][(ch & 1) * 2], sb = sh[ch >> 1][(ch & 1) * 2 + 1];
      const float s0 = blo(sa), s1 = bhi(sa), s2 = blo(sb), s3 = bhi(sb);
      float kk[11], o[8];
#pragma unroll
      for (int i = 0; i < 11; ++i) kk[i] = (ch & 1) ? bhi(kr[i][ch >> 1]) : blo(kr[i][ch >> 1]);
#pragma unroll
      for (int i = 0; i < 8; ++i) {
        float a = rb(kk[i] * s3);
        a = rb(a + rb(kk[i + 1] * s2));
        a = rb(a + rb(kk[i + 2] * s1));
        a = rb(a + rb(kk[i + 3] * s0));
        o[i] = a * ((ch & 1) ? bhi(vr[i][ch >> 1]) : blo(vr[i][ch >> 1]));
      }
      kv[ch] = v4u{pk(o[0], o[1]), pk(o[2], o[3]), pk(o[4], o[5]), pk(o[6], o[7])};
      st16s(c.rs, de * 8192 + t0 * 2, R_KVT + pg + crow(ch), kv[ch]);
    }
#pragma unroll
    for (int i = 0; i < 8; ++i) sts16(c.L, YS + i * 4096 + tid * 16, fr[i]);
    group_sync(0);
#pragma unroll
    for (int ch = 0; ch < 8; ++ch) fr[ch] = ld16s(c.rs, tid * 16, R_KVT + pg + d0 * 8192 + crow(ch));
#pragma unroll
    for (int ch = 0; ch < 8; ++ch) sts16(c.L, U8 + ch * CH + 80 + (tid >> 2) * 80 + (tid & 3) * 16, fr[ch]);
  }
  BAR();
  STAMP(5);
  auto build = [&](int ch, int q0, int q1) {
    for (int q = q0; q < q1; ++q) {
      const int m = lane + 64 * q;
      const v4u hi = lds16(c.L, YS + ch * 4096 + (255 - m) * 16);
      v4u lo = lds16(c.L, YS + ch * 4096 + (m < 255 ? 254 - m : 0) * 16);
      if (m == 255) lo = v4u{0, 0, 0, 0};
      unsigned G[8];
#pragma unroll
      for (int i = 0; i < 4; ++i) { G[i] = __builtin_amdgcn_alignbyte(hi[3 - i], hi[3 - i], 2); G[4 + i] = __builtin_amdgcn_alignbyte(lo[3 - i], lo[3 - i], 2); }
#pragma unroll
      for (int sg = 0; sg < 8; ++sg) {
        v4u o;
#pragma unroll
        for (int i = 0; i < 4; ++i) o[i] = (sg & 1) ? __builtin_amdgcn_alignbyte(G[(sg >> 1) + i + 1], G[(sg >> 1) + i], 2) : G[(sg >> 1) + i];
        sts16(c.L, FC + ((ch & 1) * 8 + sg) * CP + m * 16, o);
      }
    }
  };
  build(0, w, w + 1);
  BAR();
  const int h2 = lane >> 5, r = lane & 31;
  for (int ch = 0; ch < 8; ++ch) {
    f32x16 acc;
    if (w == 3) {
      if (ch + 1 < 8) build(ch + 1, 0, 4);
    } else {
#pragma unroll
      for (int i = 0; i < 16; ++i) acc[i] = 0.f;
      const int dl0 = w == 2 ? 32 : 0, i0 = w == 0 ? 0 : 32;
      const int sg = (7 - r) & 7;
      int fo = FC + ((ch & 1) * 8 + sg) * CP + (2047 - 32 * dl0 - r + 8 * h2 - sg) * 2;
      const int ub = (i0 + r - dl0) * 80;
      const int ubase = U8 + ch * CH + 80 + h2 * 16;
      v4u fa[3][2], fb[3][2];
      fo = opq(fo - 31 * 64);
#pragma unroll
      for (int dl = -2; dl < 32; ++dl) {
        const int dn = dl + 2;
        if (dn < 32) {
          const int uo = ubase + max(ub - dn * 80, -80);
          fa[dn % 3][0] = lds16(c.L, fo + (31 - dn) * 64); fb[dn % 3][0] = lds16(c.L, uo);
          fa[dn % 3][1] = lds16(c.L, fo + (31 - dn) * 64 + 32); fb[dn % 3][1] = lds16(c.L, uo + 32);
        }
        SCHED();
        if (dl >= 0) {
          acc = mfma32(fa[dl % 3][0], fb[dl % 3][0], acc);
          acc = mfma32(fa[dl % 3][1], fb[dl % 3][1], acc);
        }
        SCHED();
      }
      if (w == 2) {
#pragma unroll
        for (int m = 0; m < 4; ++m) sts16(c.L, RED + (ch & 1) * 4096 + m * 1024 + lane * 16, v4u{fu(acc[4 * m]), fu(acc[4 * m + 1]), fu(acc[4 * m + 2]), fu(acc[4 * m + 3])});
      }
    }
    BAR();
    if (w < 2) {
#pragma unroll
      for (int m = 0; m < 4; ++m) {
        f32x4 v = {acc[4 * m], acc[4 * m + 1], acc[4 * m + 2], acc[4 * m + 3]};
        if (w == 1) v += __builtin_bit_cast(f32x4, lds16(c.L, RED + (ch & 1) * 4096 + m * 1024 + lane * 16));
        sts8(c.L, YS + ch * 4096 + (32 * (32 * w + r) + 8 * m + 4 * h2) * 2, v2u{pk(v[0], v[1]), pk(v[2], v[3])});
      }
    }
  }
  BAR();
  STAMP(6);
  {
    const rsrc_t ro = mkrs(p.out, 2048 * 2048 * 2);
    v4u qr[8], yv[8];
#pragma unroll
    for (int ch = 0; ch < 8; ++ch) st16s(c.rs, tid * 16, R_YT + pg + d0 * 8192 + crow(ch), lds16(c.L, YS + ch * 4096 + tid * 16));
#pragma unroll
    for (int i = 0; i < 8; ++i) qr[i] = *(const GAS v4u*)(p.bq + (size_t)(t0 + i) * 1024 + de);
    const v4u dd = *(const GAS v4u*)(p.ssmD + de);
    group_sync(1);
#pragma unroll
    for (int ch = 0; ch < 8; ++ch) yv[ch] = ld16s(c.rs, de * 8192 + t0 * 2, R_YT + pg + crow(ch));
#pragma unroll
    for (int i = 0; i < 8; ++i) {
      float o[8];
#pragma unroll
      for (int ch = 0; ch < 8; ++ch) {
        const float yy = (i & 1) ? bhi(yv[ch][i >> 1]) : blo(yv[ch][i >> 1]);
        const float uu = (i & 1) ? bhi(kv[ch][i >> 1]) : blo(kv[ch][i >> 1]);
        const float dv = (ch & 1) ? bhi(dd[ch >> 1]) : blo(dd[ch >> 1]);
        const float qq = (ch & 1) ? bhi(qr[i][ch >> 1]) : blo(qr[i][ch >> 1]);
        o[ch] = qq * rb(yy + rb(uu * dv));
      }
      __builtin_amdgcn_raw_buffer_store_b128(v4u{pk(o[0], o[1]), pk(o[2], o[3]), pk(o[4], o[5]), pk(o[6], o[7])}, ro, (t0 + i) * 4096 + (1024 + de) * 2, 0, AUX_NT);
    }
  }
  if (c.budget == 0u) __hip_atomic_store(c.ctrl + C_TMO / 4, 1u, RLX_AGENT);
}

extern "C" __global__ void __launch_bounds__(256, 1) mk_mega(Params p) {
  extern __shared__ __attribute__((aligned(16))) unsigned char lds_raw[];
  Ctx c;
  c.ctrl = (gu32*)p.ctrl;
  c.rs = mkrs(p.data, R_END);
  c.L = (lds_t)lds_raw;
  c.w = __builtin_amdgcn_readfirstlane(threadIdx.x >> 6);
  c.lane = threadIdx.x & 63;
  c.b = (int)(blockIdx.x & 255u);
  c.att = (int)(blockIdx.x >> 8);
  c.ask = 0;
  if (c.w == 0 && c.att == 0) {
    c.ask = __builtin_amdgcn_readfirstlane(__hip_atomic_load(c.ctrl + C_ECHO / 4 + c.b, RLX_AGENT)) + 1u;
    if (c.lane == 0) __hip_atomic_store(c.ctrl + (C_ASK + c.b * 64) / 4, c.ask, RLX_AGENT);
  }
  c.slot = (c.att ? (c.b & 7) : (int)((unsigned)__builtin_amdgcn_s_getreg(20 | (3 << 11)) & 7u)) * 32 + (c.b >> 3);
  if (c.b == SERVER && c.w == 3) serve(c);
  const int x = c.slot >> 5, t = c.slot & 31;
  if (x < 4) {
    if (t < 8) qk_role(p, c, x * 8 + t);
    else if (t < 16) vo_role(p, c, x * 8 + t - 8);
    else y_role(p, c, x * 16 + t - 16);
  } else {
    if (t < 24) { const int i = (x - 4) * 24 + t; g_role(p, c, i >> 5, i & 31, t); }
    else zc_role(p, c, (x - 4) * 8 + t - 24);
    STAMP(2);
    c.budget = SPIN_CAP;
    conv_role(p, c, (x - 4) * 32 + t, vote_a(wait_verdict(c)) ^ 1);
  }
  STAMP(3);
}

}

extern "C" void kernel_launch(void* const* d_in, const int* in_sizes, int n_in, void* d_out, int out_size, void* d_ws, size_t ws_size,
                              hipStream_t stream) {
  using namespace mk;
  (void)in_sizes; (void)n_in; (void)out_size; (void)ws_size;
  Params p;
  p.x = (const u16*)d_in[0]; p.mem0 = (const u16*)d_in[1];
  p.Wq = (const u16*)d_in[2]; p.Wk = (const u16*)d_in[3]; p.Wv = (const u16*)d_in[4]; p.Wo = (const u16*)d_in[5];
  p.bq = (const u16*)d_in[7]; p.bk = (const u16*)d_in[8]; p.bv = (const u16*)d_in[9];
  p.shift = (const u16*)d_in[10]; p.ssmk = (const u16*)d_in[11]; p.ssmD = (const u16*)d_in[12];
  p.out = (u16*)d_out;
  p.ctrl = (unsigned char*)d_ws;
  p.data = (unsigned char*)(((size_t)d_ws + CTRL_BYTES + (2u << 20) - 1) & ~(size_t)((2u << 20) - 1));
  (void)hipFuncSetAttribute((const void*)mk_mega, hipFuncAttributeMaxDynamicSharedMemorySize, LDS_BYTES);
  hipLaunchKernelGGL(mk_mega, dim3(256), dim3(256), LDS_BYTES, stream, p);
}
```

```cpp
#include <hip/hip_runtime.h>
#include <hip/hip_bf16.h>

namespace mk {

#define GAS __attribute__((address_space(1)))
#define LAS __attribute__((address_space(3)))
#define DEV __device__ __forceinline__
#define RLX_AGENT __ATOMIC_RELAXED, __HIP_MEMORY_SCOPE_AGENT
typedef unsigned short u16;
typedef unsigned v2u __attribute__((ext_vector_type(2)));
typedef unsigned v4u __attribute__((ext_vector_type(4)));
typedef unsigned v8u __attribute__((ext_vector_type(8)));
typedef unsigned v16u __attribute__((ext_vector_type(16)));
typedef unsigned v32u __attribute__((ext_vector_type(32)));
typedef float f32x2 __attribute__((ext_vector_type(2)));
typedef float f32x4 __attribute__((ext_vector_type(4)));
typedef float f32x16 __attribute__((ext_vector_type(16)));
typedef __bf16 bf16x2 __attribute__((ext_vector_type(2)));
typedef __bf16 bf16x8 __attribute__((ext_vector_type(8)));
typedef __bf16 bf16x16 __attribute__((ext_vector_type(16)));
typedef GAS unsigned gu32;
typedef __amdgpu_buffer_rsrc_t rsrc_t;
typedef LAS unsigned char* lds_t;

#define XCNT(r) ""
#define XT(a, b) ""
#define UCNT ""
#define CL_T
#define GSUM ""
#define ZSUM ""
#define GTRACE
#define MIXED 1
#define NOFLAG 0
#define TOK_KICK 1
#define ZRING 16
#define Z_AT 16
#define Z_GAP 10
#define STR_(x) #x
#define STR(x) STR_(x)
#define GQ_AT 600
#define GK_AT 300
#define PULL_AT 800
#define QK_AT 100
#define SERVER 194
#define SLOW_LEAD 100
#define PULL_DEPTH 1
#define PRIME 2
#define X_AT 150
#define VO_AT 250
#define ZC_AT 0
#define CONV_AUX 2
#define W_AUX 0
#define ZC_PRIME 2
constexpr int NSEG = 64;
constexpr unsigned SENT = 0xFFFFFFFFu;
constexpr int AUX_SC1 = 16, AUX_NT = 2;
constexpr int T_LAG = 61;
constexpr int G_AHEAD = 4;
constexpr unsigned SPIN_CAP = 1u << 21;
constexpr int LDS_BYTES = 163840;

constexpr int C_ECHO = 0, C_CLAIM = 1024, C_VERDICT = 3072, C_TMO = 3328, C_CONV = 3456, C_VOTE = 3840, C_FLAG = 4096, C_ASK = C_FLAG + 256 * 128;
constexpr int F_Q = 0, F_K = 32, F_V = 64, F_S = 96, F_Z = 128;
constexpr int CTRL_BYTES = C_ASK + 256 * 64;
constexpr int MB = 1 << 20;
constexpr int R_U = 0, R_U2 = 3 * MB;
constexpr int R_MEM = 2 * MB, MEM_SLOT = 32768;
constexpr int R_P = R_MEM + 4 * MEM_SLOT, P_SLOT = 65536;
constexpr int R_PT = 4 * MB, PT_SLOT = 131072;
constexpr int R_QX = 8 * MB, R_KX = 40 * MB, R_Z = 72 * MB, Z_SLOT = 4 * MB, R_KVT = R_KX, R_YT = R_KX + 8 * MB, R_END = 208 * MB;

struct Params {
  const u16 *x, *mem0, *Wq, *Wk, *Wv, *Wo, *bq, *bk, *bv, *shift, *ssmk, *ssmD;
  u16* out;
  unsigned char* ctrl;
  unsigned char* data;
};

DEV rsrc_t mkrs(const void* p, unsigned bytes) { return __builtin_amdgcn_make_buffer_rsrc((void*)p, 0, bytes, 0x00020000); }
DEV v4u ld16s(rsrc_t rs, int v, int s) { return __builtin_amdgcn_raw_buffer_load_b128(rs, v, s, AUX_SC1); }
DEV v2u ld8s(rsrc_t rs, int v, int s) { return __builtin_amdgcn_raw_buffer_load_b64(rs, v, s, AUX_SC1); }
DEV v4u ld16(rsrc_t rs, int v, int s) { return __builtin_amdgcn_raw_buffer_load_b128(rs, v, s, 0); }
DEV v4u ld16c(rsrc_t rs, int v, int s) { return __builtin_amdgcn_raw_buffer_load_b128(rs, v, s, CONV_AUX); }
DEV v4u ld16w(rsrc_t rs, int v, int s) { return __builtin_amdgcn_raw_buffer_load_b128(rs, v, s, W_AUX); }
DEV v2u ld8(rsrc_t rs, int v, int s) { return __builtin_amdgcn_raw_buffer_load_b64(rs, v, s, 0); }
DEV void dma16(rsrc_t rs, lds_t l, int v) {
#if __HIP_DEVICE_COMPILE__
  __builtin_amdgcn_raw_ptr_buffer_load_lds(rs, (LAS void*)l, 16, v, 0, 0, 0);
#endif
}
DEV void st16s(rsrc_t rs, int v, int s, v4u d) { __builtin_amdgcn_raw_buffer_store_b128(d, rs, v, s, AUX_SC1); }
DEV void st8s(rsrc_t rs, int v, int s, v2u d) { __builtin_amdgcn_raw_buffer_store_b64(d, rs, v, s, AUX_SC1); }
DEV void st4s(rsrc_t rs, int v, int s, unsigned d) { __builtin_amdgcn_raw_buffer_store_b32(d, rs, v, s, AUX_SC1); }
DEV int amap(int o, int par) { return ((o >> 13) << 14) | (par << 13) | (o & 8191); }
DEV int xblk(int s, int h, int par) { return (s * 32 + h) * 16384 + ((par ^ (s >> 2)) & 1) * 8192; }
DEV unsigned umax(unsigned a, unsigned b) { return a > b ? a : b; }
DEV unsigned top(v4u v) { return umax(umax(v.x, v.y), umax(v.z, v.w)); }
DEV unsigned pk(float a, float b) { bf16x2 v = {(__bf16)a, (__bf16)b}; return __builtin_bit_cast(unsigned, v); }
DEV unsigned pks(float a, float b) { unsigned u = pk(a, b); return u < SENT - 1u ? u : SENT - 1u; }
DEV float blo(unsigned u) { return __builtin_bit_cast(float, u << 16); }
DEV float bhi(unsigned u) { return __builtin_bit_cast(float, u & 0xFFFF0000u); }
DEV float rb(float a) { return blo(pk(a, a)); }
DEV unsigned fu(float f) { return __builtin_bit_cast(unsigned, f); }
DEV float uf(unsigned u) { return __builtin_bit_cast(float, u); }
DEV f32x4 mfma16(v4u a, v4u b, f32x4 c) {
  return __builtin_amdgcn_mfma_f32_16x16x32_bf16(__builtin_bit_cast(bf16x8, a), __builtin_bit_cast(bf16x8, b), c, 0, 0, 0);
}
DEV f32x16 mfma32(v4u a, v4u b, f32x16 c) {
  return __builtin_amdgcn_mfma_f32_32x32x16_bf16(__builtin_bit_cast(bf16x8, a), __builtin_bit_cast(bf16x8, b), c, 0, 0, 0);
}
DEV f32x4 smf(v4u a, v8u b, f32x4 c, int idx) {
  return __builtin_amdgcn_smfmac_f32_16x16x64_bf16(__builtin_bit_cast(bf16x8, a), __builtin_bit_cast(bf16x16, b), c, idx, 0, 0);
}
DEV v8u cat(v4u a, v4u b) { return __builtin_shufflevector(a, b, 0, 1, 2, 3, 4, 5, 6, 7); }
DEV v4u lo4(v8u a) { return __builtin_shufflevector(a, a, 0, 1, 2, 3); }
DEV v4u hi4(v8u a) { return __builtin_shufflevector(a, a, 4, 5, 6, 7); }
DEV v4u lds16(lds_t L, int o) { return *(LAS v4u*)(L + o); }
DEV void sts16(lds_t L, int o, v4u v) { *(LAS v4u*)(L + o) = v; }
DEV void sts8(lds_t L, int o, v2u v) { *(LAS v2u*)(L + o) = v; }
#define SCHED() __builtin_amdgcn_sched_barrier(0)
#define PIN_A(x) asm volatile("" : "+a"(x))
#define PIN_V(x) asm volatile("" : "+v"(x))
DEV int opq(int v) { asm volatile("" : "+v"(v)); return v; }
DEV int fresh_lane() {
  int l; asm volatile("v_mbcnt_lo_u32_b32 %0, -1, 0\n\tv_mbcnt_hi_u32_b32 %0, -1, %0" : "=v"(l)); return l;
}
DEV f32x4 fresh_zero() {
  f32x2 a, b;
  asm volatile("v_mov_b64 %0, 0" : "=v"(a));
  asm volatile("v_mov_b64 %0, 0" : "=v"(b));
  return __builtin_shufflevector(a, b, 0, 1, 2, 3);
}
DEV unsigned fresh_sent() { unsigned z; asm volatile("v_mov_b32 %0, -1" : "=v"(z)); return z; }
#define SMF "v_smfmac_f32_16x16x64_bf16 "
#define MF16 "v_mfma_f32_16x16x32_bf16 "
#define SMF1(cls, d, a, b) asm volatile(SMF "%0, %1, %2, %3" : "+v"(d) : "v"(a), cls(b), "v"(idx))
#define SMF1F(cls, lo, hi, d, a, b) asm volatile(SMF "v[" #lo ":" #hi "], %1, %2, %3" : "+{v[" #lo ":" #hi "]}"(d) : "v"(a), cls(b), "v"(idx))
#define AT_HOME(M, j) do { const int j_ = (j); if (j_ == 0) M("{v[32:39]}"); if (j_ == 1) M("{v[40:47]}"); if (j_ == 2) M("{v[48:55]}"); if (j_ == 3) M("{v[56:63]}"); if (j_ == 4) M("{v[64:71]}"); if (j_ == 5) M("{v[72:79]}"); if (j_ == 6) M("{v[80:87]}"); if (j_ == 7) M("{v[88:95]}"); if (j_ == 8) M("{v[96:103]}"); if (j_ == 9) M("{v[104:111]}"); if (j_ == 10) M("{v[112:119]}"); if (j_ == 11) M("{v[120:127]}"); if (j_ == 12) M("{v[128:135]}"); if (j_ == 13) M("{v[136:143]}"); if (j_ == 14) M("{v[144:151]}"); if (j_ == 15) M("{v[152:159]}"); } while (0)
#define MF1(d, a, b) asm volatile(MF16 "%0, %1, %2, %0" : "+v"(d) : "v"(a), "v"(b))
#define MF1Z(d, a, b) asm volatile(MF16 "%0, %1, %2, 0" : "=&v"(d) : "v"(a), "v"(b))
#define LGKM(n) asm volatile("s_waitcnt lgkmcnt(" #n ")" ::: "memory")
#define RAWBAR() do { __builtin_amdgcn_s_barrier(); asm volatile("" ::: "memory"); } while (0)
DEV void settle3(f32x4& s0, f32x4& s1, f32x4& s2) { asm volatile("s_nop 7" : "+v"(s0), "+v"(s1), "+v"(s2)); }
DEV void settle4(f32x4 (&d)[4]) { asm volatile("s_nop 7" : "+v"(d[0]), "+v"(d[1]), "+v"(d[2]), "+v"(d[3])); }
#define BAR() do { asm volatile("s_waitcnt lgkmcnt(0)" ::: "memory"); __builtin_amdgcn_s_barrier(); asm volatile("" ::: "memory"); } while (0)
#define VMWAIT(n) asm volatile("s_waitcnt vmcnt(" #n ")" ::: "memory")
DEV float add1(float a, float b) {
  float o; asm("v_add_f32 %0, %1, %2" : "=v"(o) : "v"(a), "v"(b)); return o;
}
DEV float xmax(float v) {
  auto r = __builtin_amdgcn_permlane32_swap(fu(v), fu(v), false, false); v = fmaxf(uf(r[0]), uf(r[1]));
  auto q = __builtin_amdgcn_permlane16_swap(fu(v), fu(v), false, false); return fmaxf(uf(q[0]), uf(q[1]));
}
DEV float xsum(float v) {
  auto r = __builtin_amdgcn_permlane32_swap(fu(v), fu(v), false, false); v = uf(r[0]) + uf(r[1]);
  auto q = __builtin_amdgcn_permlane16_swap(fu(v), fu(v), false, false); return uf(q[0]) + uf(q[1]);
}

DEV float ror8(float v) { return uf(__builtin_amdgcn_update_dpp(0u, fu(v), 0x128, 0xf, 0xf, true)); }
DEV float fmax2(float a, float b) { return __builtin_amdgcn_fmed3f(a, b, __builtin_inff()); }
DEV float fmax3(float a, float b, float c) { float r; asm("v_max3_f32 %0, %1, %2, %3" : "=v"(r) : "v"(a), "v"(b), "v"(c)); return r; }
DEV bool allok(unsigned t) { return __all(t != SENT); }

template <int N> struct IC { static constexpr int value = N; };
template <int N, class F> DEV void sfor(F&& f) { if constexpr (N > 0) { sfor<N - 1>(f); f(IC<N - 1>{}); } }
struct Ctx {
  gu32* ctrl;
  rsrc_t rs;
  lds_t L;
  int w, lane;
  int b, slot, att;
  unsigned ask;
  unsigned t0;
  unsigned budget;
};
DEV bool spend(Ctx& c) {
  const unsigned old = c.budget;
  asm volatile("s_cmp_lg_u32 %0, 0\n\ts_subb_u32 %0, %0, 0" : "+s"(c.budget) :: "scc");
  return old == 0u;
}
DEV unsigned wait_flag(Ctx& c, int flag, unsigned need) {
  gu32* f = c.ctrl + (C_FLAG + flag * 128) / 4;
  unsigned v;
#pragma unroll 1
  for (;;) {
    v = __builtin_amdgcn_readfirstlane(__hip_atomic_load(f, RLX_AGENT));
    if (v >= need || spend(c)) break;
    __builtin_amdgcn_s_sleep(2);
  }
  __builtin_amdgcn_fence(__ATOMIC_ACQUIRE, "wavefront");
  return v;
}
DEV void post_flag(Ctx& c, int flag, unsigned v, int off = 0) {
  st4s(mkrs((const void*)c.ctrl, CTRL_BYTES), (c.w | off | fresh_lane()) ? 1 << 30 : 0, C_FLAG + flag * 128, v);
}
constexpr unsigned CL_A = 1u << 16, CL_B = 1u << 20, CL_VOTED = 1u << 24, CL_FILLED = 1u << 28;
DEV void claim(Ctx& c, unsigned votes) {
  if (c.w != 0) return;
  if (c.att == 0) {
#pragma unroll 1
    while (__builtin_amdgcn_readfirstlane(__hip_atomic_load(c.ctrl + C_ECHO / 4 + c.b, RLX_AGENT)) != c.ask) {
      if (spend(c)) break;
      __builtin_amdgcn_s_sleep(2);
    }
  }
  if (fresh_lane() == 0) __hip_atomic_fetch_add(c.ctrl + (C_CLAIM + c.att * 1024) / 4 + c.slot, 1u + votes, RLX_AGENT);
}
DEV unsigned wait_verdict(Ctx& c) {
  unsigned v;
#pragma unroll 1
  for (;;) {
    v = __builtin_amdgcn_readfirstlane(__hip_atomic_load(c.ctrl + (C_VERDICT + c.att * 128) / 4, RLX_AGENT));
    if ((v & 1u) || spend(c)) break;
    __builtin_amdgcn_s_sleep(2);
  }
  __builtin_amdgcn_fence(__ATOMIC_ACQUIRE, "wavefront");
  if ((v & 3u) == 3u && c.att == 0) {
    asm volatile("s_waitcnt vmcnt(0) lgkmcnt(0)\n\ts_barrier\n\ts_mov_b64 s[0:1], %0\n\ts_mov_b32 s2, %1\n\tv_mov_b32 v0, %2\n\t"
                 "s_getpc_b64 s[4:5]\n1:\n\ts_sub_u32 s4, s4, 1b-mk_mega\n\ts_subb_u32 s5, s5, 0\n\ts_setpc_b64 s[4:5]"
                 :: "s"(__builtin_amdgcn_kernarg_segment_ptr()), "s"(c.b + 256), "v"(c.w * 64 + fresh_lane()) : "s0", "s1", "s2", "s4", "s5", "v0", "scc", "memory");
    __builtin_unreachable();
  }
  return v;
}
DEV unsigned wait_vote(Ctx& c) {
  unsigned v;
#pragma unroll 1
  for (;;) {
    v = __builtin_amdgcn_readfirstlane(__hip_atomic_load(c.ctrl + C_VOTE / 4, RLX_AGENT));
    if ((v & 1u) || spend(c)) break;
    __builtin_amdgcn_s_sleep(2);
  }
  return v;
}
DEV void serve(Ctx& c) {
  c.budget = SPIN_CAP;
  const int lane = fresh_lane();
  const rsrc_t rc = mkrs((const void*)c.ctrl, CTRL_BYTES);
  if (c.att == 0) {
#pragma unroll
    for (int i = C_CLAIM / 1024; i < C_ASK / 1024; ++i) st16s(rc, lane * 16, i * 1024, v4u{0, 0, 0, 0});
    VMWAIT(0);
  }
  unsigned vote = c.att ? wait_vote(c) : 0u;
  bool echo = c.att == 0, first = true;
  v4u last = {0u, 0u, 0u, 0u};
#pragma unroll 1
  for (;;) {
    asm volatile("" ::: "memory");
    const v4u cl = ld16s(rc, lane * 16, C_CLAIM + c.att * 1024);
    if (echo) {
      v4u e;
#pragma unroll
      for (int i = 0; i < 4; ++i) e[i] = __builtin_amdgcn_raw_buffer_load_b32(rc, (lane * 4 + i) * 64, C_ASK, AUX_SC1);
      if (first || __any(e[0] != last[0] || e[1] != last[1] || e[2] != last[2] || e[3] != last[3])) st16s(rc, lane * 16, C_ECHO, e);
      last = e; first = false;
    }
    unsigned n = 0, one = 1;
    int va = 0, vb = 0, nv = 0, nf = 0;
#pragma unroll
    for (int i = 0; i < 4; ++i) {
      n += cl[i] & 0xFFFFu; one &= (cl[i] & 0xFFFFu) == 1u;
      va += __popcll(__ballot(cl[i] & CL_A)); vb += __popcll(__ballot(cl[i] & CL_B)); nv += __popcll(__ballot(cl[i] & CL_VOTED)); nf += __popcll(__ballot(cl[i] & CL_FILLED));
    }
#pragma unroll
    for (int o = 1; o < 64; o <<= 1) n += (unsigned)__shfl_xor((int)n, o);
    const bool all = __builtin_amdgcn_readfirstlane(n) >= 256u, good = __all(one);
    echo = echo && !all;
    if (!vote && (all || (max(va, nv - va) > 32 && max(vb, nv - vb) > 32))) {
      vote = 1u | (va > 32 ? 4u : 0u) | (vb > 32 ? 8u : 0u);
      if (lane == 0) __hip_atomic_store(c.ctrl + C_VOTE / 4, vote, RLX_AGENT);
    }
    if (all && (!good || nf >= 64)) {
      if (lane == 0) __hip_atomic_store(c.ctrl + (C_VERDICT + c.att * 128) / 4, vote | (good ? 0u : 2u), RLX_AGENT);
      break;
    }
    if (spend(c)) break;
  }
}
DEV int vote_a(unsigned v) { return (int)((v >> 2) & 1u); }
DEV int vote_b(unsigned v) { return (int)((v >> 3) & 1u); }

#define STAMP(slot) do {} while (0)
#define TICK_DECL
#define TICK(i) do {} while (0)
#define TICK_SUM(n) do {} while (0)
#define TICK_LDS(b) do {} while (0)
#define TICK_DUMP(b) do {} while (0)
#define TICK_OUT() do {} while (0)

DEV constexpr int krow(int rho8, int i) { return 32 * (rho8 >> 2) + 8 * (rho8 & 1) + 2 * ((rho8 >> 1) & 1) + i; }
DEV int krow_lane(int g) { return 16 * (g >> 1) + 4 * (g & 1); }
DEV unsigned prm(unsigned hi, unsigned lo, int odd) { return __builtin_amdgcn_perm(hi, lo, odd ? 0x07060302u : 0x05040100u); }

#define AT_ACC(M, n) do { const int n_ = (n); if (n_ == 0) M("{a[0:7]}"); if (n_ == 1) M("{a[8:15]}"); if (n_ == 2) M("{a[16:23]}"); if (n_ == 3) M("{a[24:31]}"); if (n_ == 4) M("{a[32:39]}"); if (n_ == 5) M("{a[40:47]}"); if (n_ == 6) M("{a[48:55]}"); if (n_ == 7) M("{a[56:63]}"); if (n_ == 8) M("{a[64:71]}"); if (n_ == 9) M("{a[72:79]}"); if (n_ == 10) M("{a[80:87]}"); if (n_ == 11) M("{a[88:95]}"); if (n_ == 12) M("{a[96:103]}"); if (n_ == 13) M("{a[104:111]}"); if (n_ == 14) M("{a[112:119]}"); if (n_ == 15) M("{a[120:127]}"); if (n_ == 16) M("{a[128:135]}"); if (n_ == 17) M("{a[136:143]}"); if (n_ == 18) M("{a[144:151]}"); if (n_ == 19) M("{a[152:159]}"); if (n_ == 20) M("{a[160:167]}"); if (n_ == 21) M("{a[168:175]}"); if (n_ == 22) M("{a[176:183]}"); if (n_ == 23) M("{a[184:191]}"); if (n_ == 24) M("{a[192:199]}"); if (n_ == 25) M("{a[200:207]}"); if (n_ == 26) M("{a[208:215]}"); if (n_ == 27) M("{a[216:223]}"); if (n_ == 28) M("{a[224:231]}"); if (n_ == 29) M("{a[232:239]}"); if (n_ == 30) M("{a[240:247]}"); if (n_ == 31) M("{a[248:255]}"); } while (0)
#define PIN_ACC0(x) asm volatile("" :: "{a[0:7]}"(x[0]), "{a[8:15]}"(x[1]), "{a[16:23]}"(x[2]), "{a[24:31]}"(x[3]), "{a[32:39]}"(x[4]), "{a[40:47]}"(x[5]), "{a[48:55]}"(x[6]), "{a[56:63]}"(x[7]), "{a[64:71]}"(x[8]), "{a[72:79]}"(x[9]), "{a[80:87]}"(x[10]), "{a[88:95]}"(x[11]), "{a[96:103]}"(x[12]), "{a[104:111]}"(x[13]), "{a[112:119]}"(x[14]), "{a[120:127]}"(x[15]))
#define PIN_ACC1(x) asm volatile("" :: "{a[128:135]}"(x[16]), "{a[136:143]}"(x[17]), "{a[144:151]}"(x[18]), "{a[152:159]}"(x[19]), "{a[160:167]}"(x[20]), "{a[168:175]}"(x[21]), "{a[176:183]}"(x[22]), "{a[184:191]}"(x[23]), "{a[192:199]}"(x[24]), "{a[200:207]}"(x[25]), "{a[208:215]}"(x[26]), "{a[216:223]}"(x[27]), "{a[224:231]}"(x[28]), "{a[232:239]}"(x[29]), "{a[240:247]}"(x[30]), "{a[248:255]}"(x[31]))
#define PIN_HOME(x) asm volatile("" :: "{v[32:39]}"(x[0]), "{v[40:47]}"(x[1]), "{v[48:55]}"(x[2]), "{v[56:63]}"(x[3]), "{v[64:71]}"(x[4]), "{v[72:79]}"(x[5]), "{v[80:87]}"(x[6]), "{v[88:95]}"(x[7]), "{v[96:103]}"(x[8]), "{v[104:111]}"(x[9]), "{v[112:119]}"(x[10]), "{v[120:127]}"(x[11]), "{v[128:135]}"(x[12]), "{v[136:143]}"(x[13]), "{v[144:151]}"(x[14]), "{v[152:159]}"(x[15]))
#define CL_32_100 "v32", "v33", "v34", "v35", "v36", "v37", "v38", "v39", "v40", "v41", "v42", "v43", "v44", "v45", "v46", "v47", "v48", "v49", "v50", "v51", "v52", "v53", "v54", "v55", "v56", "v57", "v58", "v59", "v60", "v61", "v62", "v63", "v64", "v65", "v66", "v67", "v68", "v69", "v70", "v71", "v72", "v73", "v74", "v75", "v76", "v77", "v78", "v79", "v80", "v81", "v82", "v83", "v84", "v85", "v86", "v87", "v88", "v89", "v90", "v91", "v92", "v93", "v94", "v95", "v96", "v97", "v98", "v99", "v100", "v101", "v102", "v103", "v104", "v105", "v106", "v107", "v108", "v109", "v110", "v111", "v112", "v113", "v114", "v115", "v116", "v117", "v118", "v119", "v120", "v121", "v122", "v123", "v124", "v125", "v126", "v127", "v128", "v129", "v130", "v131"
#define ACC_IN(x) "{a[0:31]}"(x[0]), "{a[32:63]}"(x[1]), "{a[64:95]}"(x[2]), "{a[96:127]}"(x[3]), "{a[128:159]}"(x[4]), "{a[160:191]}"(x[5]), "{a[192:223]}"(x[6]), "{a[224:255]}"(x[7])
#define CL_16_32 "v16", "v17", "v18", "v19", "v20", "v21", "v22", "v23", "v24", "v25", "v26", "v27", "v28", "v29", "v30", "v31"
#define CL_120_128 "v120", "v121", "v122", "v123", "v124", "v125", "v126", "v127"
#define CL_160_200 "v160", "v161", "v162", "v163", "v164", "v165", "v166", "v167", "v168", "v169", "v170", "v171", "v172", "v173", "v174", "v175", "v176", "v177", "v178", "v179", "v180", "v181", "v182", "v183", "v184", "v185", "v186", "v187", "v188", "v189", "v190", "v191", "v192", "v193", "v194", "v195", "v196", "v197", "v198", "v199"
#define CL_176_200 "v176", "v177", "v178", "v179", "v180", "v181", "v182", "v183", "v184", "v185", "v186", "v187", "v188", "v189", "v190", "v191", "v192", "v193", "v194", "v195", "v196", "v197", "v198", "v199"
#define CL_200_216 "v200", "v201", "v202", "v203", "v204", "v205", "v206", "v207", "v208", "v209", "v210", "v211", "v212", "v213", "v214", "v215"
#define CL_216_256 "v216", "v217", "v218", "v219", "v220", "v221", "v222", "v223", "v224", "v225", "v226", "v227", "v228", "v229", "v230", "v231", "v232", "v233", "v234", "v235", "v236", "v237", "v238", "v239", "v240", "v241", "v242", "v243", "v244", "v245", "v246", "v247", "v248", "v249", "v250", "v251", "v252", "v253", "v254", "v255"
#define CL_S "memory", "vcc", "scc", "s60", "s61", "s62", "s63", "s64", "s65", "s66", "s67" CL_T
#define TIMED_WAIT \
    "s_getpc_b64 s[62:63]\n\t" "1:\n\t" "s_memtime s[64:65]\n\t" "s_waitcnt lgkmcnt(0)\n\t" "s_sub_u32 s61, %0, s64\n\t" "s_cmp_gt_i32 s61, 124\n\t" "s_cbranch_scc1 1b\n\t" \
    "s_max_i32 s61, s61, 0\n\t" "s_and_b32 s61, s61, 124\n\t" "s_sub_u32 s61, 2f-1b, s61\n\t" "s_add_u32 s62, s62, s61\n\t" "s_addc_u32 s63, s63, 0\n\t" \
    "s_setpc_b64 s[62:63]\n\t" ".rept 31\n\t" "s_nop 0\n\t" ".endr\n\t" "2:\n\t"
#define QK_STEP0 \
    "v_xor_b32 v216, 0x400, %[mo]\n\t" "s_mov_b32 s60, 0\n\t" "s_mov_b32 s66, 0x3e0293ee\n\t" "s_mov_b32 s67, 0x3e0293ee\n\t" XT(80, 81) "s_add_u32 s61, %[kb], 0x1000\n\t" \
    "buffer_load_dwordx4 v[16:19], %[l1], %[rs], %[kb] offen offset:0 sc1\n\t" "buffer_load_dwordx4 v[20:23], %[l1], %[rs], %[kb] offen offset:1024 sc1\n\t" \
    "buffer_load_dwordx4 v[24:27], %[l1], %[rs], %[kb] offen offset:2048 sc1\n\t" "buffer_load_dwordx4 v[28:31], %[l1], %[rs], %[kb] offen offset:3072 sc1\n\t" \
    "buffer_load_dwordx4 v[200:203], %[l1], %[rs], s61 offen offset:0 sc1\n\t" "buffer_load_dwordx4 v[204:207], %[l1], %[rs], s61 offen offset:1024 sc1\n\t" \
    "buffer_load_dwordx4 v[208:211], %[l1], %[rs], s61 offen offset:2048 sc1\n\t" "buffer_load_dwordx4 v[212:215], %[l1], %[rs], s61 offen offset:3072 sc1\n\t" \
    "v_mov_b64 v[218:219], -1\n\t" "buffer_store_dwordx2 v[218:219], %[px], %[rs], %[pr] offen sc1\n\t" "buffer_store_dword v218, %[pm], %[rs], %[pr] offen sc1\n\t" \
    "s_waitcnt vmcnt(2)\n\t" XT(98, 99) "s_barrier\n\t" XT(82, 83) "10:\n\t" "buffer_load_dwordx4 v[240:243], %[mo], %[rs], %[sb] offen offset:0 sc1\n\t" \
    "v_mov_b64 v[224:225], 0\n\t" "v_mov_b64 v[226:227], 0\n\t" "v_mov_b64 v[228:229], 0\n\t" "v_mov_b64 v[230:231], 0\n\t" "v_mov_b64 v[232:233], 0\n\t" \
    "v_mov_b64 v[234:235], 0\n\t" "v_mov_b64 v[236:237], 0\n\t" "v_mov_b64 v[238:239], 0\n\t" "buffer_load_dwordx4 v[244:247], v216, %[rs], %[sb] offen offset:0 sc1\n\t" \
    "ds_read_b128 v[160:163], %[l0] offset:0\n\t" "ds_read_b128 v[164:167], %[l0] offset:1024\n\t" "ds_read_b128 v[168:171], %[l0] offset:2048\n\t" \
    "ds_read_b128 v[172:175], %[l0] offset:3072\n\t" "s_nop 3\n\t" "buffer_load_dwordx4 v[248:251], %[mo], %[rs], %[sb] offen offset:2048 sc1\n\t" \
    "ds_read_b128 v[176:179], %[l0] offset:4096\n\t" "ds_read_b128 v[180:183], %[l0] offset:5120\n\t" "ds_read_b128 v[184:187], %[l0] offset:6144\n\t" \
    "ds_read_b128 v[188:191], %[l0] offset:7168\n\t" "s_nop 3\n\t" "buffer_load_dwordx4 v[252:255], v216, %[rs], %[sb] offen offset:2048 sc1\n\t" "s_cmp_lg_u32 s61, 0\n\t" \
    "s_cbranch_scc0 177f\n\t" "ds_write_b128 %[l1], v[16:19] offset:16384\n\t" "ds_write_b128 %[l1], v[20:23] offset:17408\n\t" \
    "ds_write_b128 %[l1], v[24:27] offset:18432\n\t" "ds_write_b128 %[l1], v[28:31] offset:19456\n\t" "ds_write_b128 %[l1], v[200:203] offset:20480\n\t" \
    "ds_write_b128 %[l1], v[204:207] offset:21504\n\t" "ds_write_b128 %[l1], v[208:211] offset:22528\n\t" "ds_write_b128 %[l1], v[212:215] offset:23552\n\t" \
    "s_mov_b32 s61, 0\n\t" "s_waitcnt lgkmcnt(0)\n\t" "177:\n\t" "v_mov_b64 v[208:209], 0\n\t" "v_mov_b64 v[210:211], 0\n\t" "v_mov_b64 v[212:213], 0\n\t" \
    "v_mov_b64 v[214:215], 0\n\t" "s_waitcnt vmcnt(3)\n\t" XT(84, 85) "v_smfmac_f32_16x16x64_bf16 v[224:227], v[240:243], a[0:7], %[ix]\n\t" \
    "ds_write_b128 %[l1], v[240:243] offset:0\n\t" "v_smfmac_f32_16x16x64_bf16 v[228:231], v[240:243], a[8:15], %[ix]\n\t" "v_max3_u32 v217, v240, v241, v242\n\t" \
    "v_smfmac_f32_16x16x64_bf16 v[232:235], v[240:243], v[32:39], %[ix]\n\t" "s_waitcnt lgkmcnt(7)\n\t" \
    "v_smfmac_f32_16x16x64_bf16 v[236:239], v[240:243], v[160:167], %[ix]\n\t" "ds_read_b128 v[160:163], %[l0] offset:8192\n\t" \
    "ds_read_b128 v[164:167], %[l0] offset:9216\n\t" "s_waitcnt vmcnt(2)\n\t" "v_smfmac_f32_16x16x64_bf16 v[224:227], v[244:247], a[16:23], %[ix]\n\t" \
    "ds_write_b128 %[l1], v[244:247] offset:1024\n\t" "v_max3_u32 v217, v217, v243, v244\n\t" "v_smfmac_f32_16x16x64_bf16 v[228:231], v[244:247], a[24:31], %[ix]\n\t" \
    "v_max3_u32 v217, v217, v245, v246\n\t" "v_max_u32 v217, v217, v247\n\t" "v_smfmac_f32_16x16x64_bf16 v[232:235], v[244:247], v[40:47], %[ix]\n\t" \
    "v_cmp_eq_u32 vcc, -1, v217\n\t" "s_cmp_lg_u32 s60, 0\n\t" "s_waitcnt lgkmcnt(8)\n\t" "v_smfmac_f32_16x16x64_bf16 v[236:239], v[244:247], v[168:175], %[ix]\n\t" \
    "s_cbranch_vccnz 11f\n\t" "12:\n\t" "ds_read_b128 v[168:171], %[l0] offset:10240\n\t" "s_waitcnt vmcnt(1)\n\t" \
    "v_smfmac_f32_16x16x64_bf16 v[224:227], v[248:251], a[32:39], %[ix]\n\t" "ds_write_b128 %[l1], v[248:251] offset:2048\n\t" "s_waitcnt lgkmcnt(2)\n\t" \
    "v_smfmac_f32_16x16x64_bf16 v[228:231], v[248:251], a[40:47], %[ix]\n\t" "s_cbranch_scc1 13f\n\t" "s_barrier\n\t" "13:\n\t" XT(86, 87) \
    "v_smfmac_f32_16x16x64_bf16 v[232:235], v[248:251], v[48:55], %[ix]\n\t" "ds_read_b128 v[192:195], %[l1] offset:4096\n\t" "ds_read_b128 v[196:199], %[l1] offset:8192\n\t" \
    "v_smfmac_f32_16x16x64_bf16 v[236:239], v[248:251], v[176:183], %[ix]\n\t" "ds_read_b128 v[172:175], %[l0] offset:11264\n\t" \
    "ds_read_b128 v[176:179], %[l0] offset:12288\n\t" "s_waitcnt vmcnt(0)\n\t" "v_smfmac_f32_16x16x64_bf16 v[224:227], v[252:255], a[48:55], %[ix]\n\t" \
    "ds_write_b128 %[l1], v[252:255] offset:3072\n\t" "v_max3_u32 v217, v248, v249, v250\n\t" "v_smfmac_f32_16x16x64_bf16 v[228:231], v[252:255], a[56:63], %[ix]\n\t" \
    "ds_read_b128 v[180:183], %[l0] offset:13312\n\t" "v_max3_u32 v217, v217, v251, v252\n\t" "v_max3_u32 v217, v217, v253, v254\n\t" \
    "v_smfmac_f32_16x16x64_bf16 v[232:235], v[252:255], v[56:63], %[ix]\n\t" "v_max_u32 v217, v217, v255\n\t" "v_cmp_eq_u32 vcc, -1, v217\n\t" \
    "v_smfmac_f32_16x16x64_bf16 v[236:239], v[252:255], v[184:191], %[ix]\n\t" "s_cbranch_vccnz 14f\n\t" "15:\n\t" "ds_read_b128 v[240:243], %[l1] offset:12288\n\t" \
    "s_waitcnt lgkmcnt(6)\n\t" "v_smfmac_f32_16x16x64_bf16 v[224:227], v[192:195], a[64:71], %[ix]\n\t" "ds_read_b128 v[184:187], %[l0] offset:14336\n\t" \
    "ds_read_b128 v[188:191], %[l0] offset:15360\n\t" "v_smfmac_f32_16x16x64_bf16 v[228:231], v[192:195], a[72:79], %[ix]\n\t" \
    "ds_read_b128 v[244:247], %[l1] offset:5120\n\t" "s_waitcnt lgkmcnt(5)\n\t" "v_smfmac_f32_16x16x64_bf16 v[232:235], v[192:195], v[64:71], %[ix]\n\t" "s_barrier\n\t" \
    XT(88, 89) "v_smfmac_f32_16x16x64_bf16 v[236:239], v[192:195], v[160:167], %[ix]\n\t" "ds_read_b128 v[160:163], %[l0] offset:16384\n\t" \
    "ds_read_b128 v[164:167], %[l0] offset:17408\n\t" "v_smfmac_f32_16x16x64_bf16 v[224:227], v[196:199], a[80:87], %[ix]\n\t" \
    "ds_read_b128 v[248:251], %[l1] offset:9216\n\t" "ds_read_b128 v[252:255], %[l1] offset:13312\n\t" \
    "v_smfmac_f32_16x16x64_bf16 v[228:231], v[196:199], a[88:95], %[ix]\n\t" "ds_read_b128 v[192:195], %[l1] offset:6144\n\t" \
    "v_smfmac_f32_16x16x64_bf16 v[232:235], v[196:199], v[72:79], %[ix]\n\t" "ds_read_b128 v[16:19], %[kx] offset:0\n\t" \
    "v_smfmac_f32_16x16x64_bf16 v[236:239], v[196:199], v[168:175], %[ix]\n\t" "ds_read_b128 v[168:171], %[l0] offset:18432\n\t" \
    "ds_read_b128 v[172:175], %[l0] offset:19456\n\t" "s_waitcnt lgkmcnt(11)\n\t" "v_smfmac_f32_16x16x64_bf16 v[224:227], v[240:243], a[96:103], %[ix]\n\t" \
    "ds_read_b128 v[196:199], %[l1] offset:10240\n\t" "v_smfmac_f32_16x16x64_bf16 v[228:231], v[240:243], a[104:111], %[ix]\n\t" \
    "ds_read_b128 v[20:23], %[kx] offset:2048\n\t" "v_smfmac_f32_16x16x64_bf16 v[232:235], v[240:243], v[80:87], %[ix]\n\t" \
    "v_smfmac_f32_16x16x64_bf16 v[236:239], v[240:243], v[176:183], %[ix]\n\t" "ds_read_b128 v[176:179], %[l0] offset:20480\n\t" \
    "ds_read_b128 v[180:183], %[l0] offset:21504\n\t" "s_waitcnt lgkmcnt(12)\n\t" "v_smfmac_f32_16x16x64_bf16 v[224:227], v[244:247], a[112:119], %[ix]\n\t" \
    "ds_read_b128 v[240:243], %[l1] offset:14336\n\t" "ds_read_b128 v[24:27], %[kx] offset:4096\n\t" \
    "v_smfmac_f32_16x16x64_bf16 v[228:231], v[244:247], a[120:127], %[ix]\n\t" "v_smfmac_f32_16x16x64_bf16 v[232:235], v[244:247], v[88:95], %[ix]\n\t" \
    "v_smfmac_f32_16x16x64_bf16 v[236:239], v[244:247], v[184:191], %[ix]\n\t" "ds_read_b128 v[184:187], %[l0] offset:22528\n\t" \
    "ds_read_b128 v[188:191], %[l0] offset:23552\n\t" "s_waitcnt lgkmcnt(13)\n\t" "v_smfmac_f32_16x16x64_bf16 v[224:227], v[248:251], a[128:135], %[ix]\n\t" \
    "ds_read_b128 v[244:247], %[l1] offset:7168\n\t" "ds_read_b128 v[28:31], %[kx] offset:6144\n\t" "v_smfmac_f32_16x16x64_bf16 v[228:231], v[248:251], a[136:143], %[ix]\n\t" \
    "v_smfmac_f32_16x16x64_bf16 v[232:235], v[248:251], v[96:103], %[ix]\n\t" "ds_read_b128 v[200:203], %[kx] offset:256\n\t" \
    "v_smfmac_f32_16x16x64_bf16 v[236:239], v[248:251], v[160:167], %[ix]\n\t" "ds_read_b128 v[160:163], %[l0] offset:24576\n\t" \
    "ds_read_b128 v[164:167], %[l0] offset:25600\n\t" "s_waitcnt lgkmcnt(14)\n\t" "v_smfmac_f32_16x16x64_bf16 v[224:227], v[252:255], a[144:151], %[ix]\n\t" \
    "ds_read_b128 v[248:251], %[l1] offset:11264\n\t" "v_smfmac_f32_16x16x64_bf16 v[228:231], v[252:255], a[152:159], %[ix]\n\t" \
    "ds_read_b128 v[204:207], %[kx] offset:2304\n\t" "v_smfmac_f32_16x16x64_bf16 v[232:235], v[252:255], v[104:111], %[ix]\n\t" "s_waitcnt lgkmcnt(14)\n\t" \
    "v_smfmac_f32_16x16x64_bf16 v[236:239], v[252:255], v[168:175], %[ix]\n\t" "ds_read_b128 v[168:171], %[l0] offset:26624\n\t" \
    "ds_read_b128 v[172:175], %[l0] offset:27648\n\t" "v_smfmac_f32_16x16x64_bf16 v[224:227], v[192:195], a[160:167], %[ix]\n\t" \
    "ds_read_b128 v[252:255], %[l1] offset:15360\n\t" "v_smfmac_f32_16x16x64_bf16 v[228:231], v[192:195], a[168:175], %[ix]\n\t" \
    "v_smfmac_f32_16x16x64_bf16 v[224:227], v[196:199], a[176:183], %[ix]\n\t" "v_smfmac_f32_16x16x64_bf16 v[228:231], v[196:199], a[184:191], %[ix]\n\t" \
    "s_waitcnt lgkmcnt(13)\n\t" "v_smfmac_f32_16x16x64_bf16 v[224:227], v[240:243], a[192:199], %[ix]\n\t" \
    "v_smfmac_f32_16x16x64_bf16 v[228:231], v[240:243], a[200:207], %[ix]\n\t" "s_waitcnt lgkmcnt(9)\n\t" \
    "v_smfmac_f32_16x16x64_bf16 v[224:227], v[244:247], a[208:215], %[ix]\n\t" "v_smfmac_f32_16x16x64_bf16 v[228:231], v[244:247], a[216:223], %[ix]\n\t" \
    "s_waitcnt lgkmcnt(4)\n\t" "v_smfmac_f32_16x16x64_bf16 v[224:227], v[248:251], a[224:231], %[ix]\n\t" \
    "v_smfmac_f32_16x16x64_bf16 v[228:231], v[248:251], a[232:239], %[ix]\n\t" "s_waitcnt lgkmcnt(0)\n\t" \
    "v_smfmac_f32_16x16x64_bf16 v[224:227], v[252:255], a[240:247], %[ix]\n\t" "s_nop 1\n\t" "v_smfmac_f32_16x16x64_bf16 v[228:231], v[252:255], a[248:255], %[ix]\n\t" \
    XT(90, 91) "s_nop 1\n\t" "v_smfmac_f32_16x16x64_bf16 v[232:235], v[192:195], v[112:119], %[ix]\n\t" "s_nop 1\n\t" \
    "v_smfmac_f32_16x16x64_bf16 v[236:239], v[192:195], v[176:183], %[ix]\n\t" "ds_read_b128 v[176:179], %[l0] offset:28672\n\t" "v_add_f32 v224, v224, v225\n\t" \
    "v_add_f32 v226, v226, v227\n\t" "v_smfmac_f32_16x16x64_bf16 v[232:235], v[196:199], v[120:127], %[ix]\n\t" "ds_read_b128 v[180:183], %[l0] offset:29696\n\t" \
    "v_add_f32 v228, v228, v229\n\t" "v_add_f32 v230, v230, v231\n\t" "v_smfmac_f32_16x16x64_bf16 v[236:239], v[196:199], v[184:191], %[ix]\n\t" \
    "ds_read_b128 v[184:187], %[l0] offset:30720\n\t" "v_cvt_pk_bf16_f32 v218, v224, v228\n\t" "v_cvt_pk_bf16_f32 v219, v226, v230\n\t" \
    "v_smfmac_f32_16x16x64_bf16 v[232:235], v[240:243], v[128:135], %[ix]\n\t" "ds_write2_b32 %[qw], v218, v219 offset1:8\n\t" \
    "ds_read_b128 v[188:191], %[l0] offset:31744\n\t" "v_smfmac_f32_16x16x64_bf16 v[236:239], v[240:243], v[160:167], %[ix]\n\t" \
    "ds_read_b128 v[192:195], %[kx] offset:4352\n\t" "ds_read_b128 v[196:199], %[kx] offset:6400\n\t" \
    "v_smfmac_f32_16x16x64_bf16 v[232:235], v[244:247], v[136:143], %[ix]\n\t" "v_smfmac_f32_16x16x64_bf16 v[236:239], v[244:247], v[168:175], %[ix]\n\t" \
    "s_waitcnt lgkmcnt(3)\n\t" "s_barrier\n\t" "v_smfmac_f32_16x16x64_bf16 v[232:235], v[248:251], v[144:151], %[ix]\n\t" "ds_read_b128 v[216:219], %[l1] offset:24576\n\t" \
    "ds_read_b128 v[220:223], %[l1] offset:25600\n\t" "v_smfmac_f32_16x16x64_bf16 v[236:239], v[248:251], v[176:183], %[ix]\n\t" \
    "v_smfmac_f32_16x16x64_bf16 v[232:235], v[252:255], v[152:159], %[ix]\n\t" "s_waitcnt lgkmcnt(4)\n\t" \
    "v_smfmac_f32_16x16x64_bf16 v[236:239], v[252:255], v[184:191], %[ix]\n\t" XT(92, 93) "s_waitcnt lgkmcnt(1)\n\t" \
    "v_smfmac_f32_16x16x64_bf16 v[208:211], v[216:219], v[16:23], %[ix]\n\t" "s_nop 1\n\t" "v_smfmac_f32_16x16x64_bf16 v[212:215], v[216:219], v[200:207], %[ix]\n\t" \
    "s_waitcnt lgkmcnt(0)\n\t" "s_nop 0\n\t" "v_add_f32 v232, v232, v233\n\t" "v_add_f32 v234, v234, v235\n\t" "v_add_f32 v236, v236, v237\n\t" \
    "v_add_f32 v238, v238, v239\n\t" "v_cvt_pk_bf16_f32 v232, v232, v236\n\t" "v_cvt_pk_bf16_f32 v233, v234, v238\n\t" "ds_write2_b32 %[kw], v232, v233 offset1:68\n\t" \
    "v_smfmac_f32_16x16x64_bf16 v[208:211], v[220:223], v[24:31], %[ix]\n\t" "v_smfmac_f32_16x16x64_bf16 v[212:215], v[220:223], v[192:199], %[ix]\n\t" \
    "v_mov_b64 v[224:225], 0\n\t" "v_mov_b64 v[226:227], 0\n\t" "s_waitcnt lgkmcnt(0)\n\t" "s_barrier\n\t" XT(94, 95) "ds_read_b128 v[160:163], %[km] offset:0\n\t" \
    "ds_read_b128 v[164:167], %[km] offset:64\n\t" "ds_read_b128 v[168:171], %[km] offset:128\n\t" "ds_read_b128 v[172:175], %[km] offset:192\n\t" \
    "v_add_f32 v178, v208, v209\n\t" "v_add_f32 v179, v210, v211\n\t" "v_add_f32 v180, v212, v213\n\t" "v_add_f32 v181, v214, v215\n\t" "v_max_f32 v182, v178, v180\n\t" \
    "v_max_f32 v183, v179, v181\n\t" "s_nop 0\n\t" "v_max_f32_dpp v182, v182, v182 row_ror:8 row_mask:0xf bank_mask:0xf\n\t" \
    "v_max_f32_dpp v183, v183, v183 row_ror:8 row_mask:0xf bank_mask:0xf\n\t" "s_nop 0\n\t" "v_max_f32_dpp v182, v182, v182 row_ror:4 row_mask:0xf bank_mask:0xf\n\t" \
    "v_max_f32_dpp v183, v183, v183 row_ror:4 row_mask:0xf bank_mask:0xf\n\t" "s_nop 0\n\t" "v_max_f32_dpp v182, v182, v182 row_ror:2 row_mask:0xf bank_mask:0xf\n\t" \
    "v_max_f32_dpp v183, v183, v183 row_ror:2 row_mask:0xf bank_mask:0xf\n\t" "s_nop 0\n\t" "v_max_f32_dpp v182, v182, v182 row_ror:1 row_mask:0xf bank_mask:0xf\n\t" \
    "v_max_f32_dpp v183, v183, v183 row_ror:1 row_mask:0xf bank_mask:0xf\n\t" "s_waitcnt lgkmcnt(2)\n\t" \
    "v_smfmac_f32_16x16x64_bf16 v[224:227], v[216:219], v[160:167], %[ix]\n\t" "s_waitcnt lgkmcnt(0)\n\t" \
    "v_smfmac_f32_16x16x64_bf16 v[224:227], v[220:223], v[168:175], %[ix]\n\t" "s_nop 7\n\t" "v_add_f32 v176, v224, v225\n\t" "v_add_f32 v177, v226, v227\n\t" \
    "v_max_f32 v182, v176, v182\n\t" "v_max_f32 v183, v177, v183\n\t" "s_nop 0\n\t" "v_max_f32_dpp v182, v182, v182 row_ror:4 row_mask:0xf bank_mask:0xf\n\t" \
    "v_max_f32_dpp v183, v183, v183 row_ror:4 row_mask:0xf bank_mask:0xf\n\t" "s_nop 0\n\t" "v_max_f32_dpp v182, v182, v182 row_ror:2 row_mask:0xf bank_mask:0xf\n\t" \
    "v_max_f32_dpp v183, v183, v183 row_ror:2 row_mask:0xf bank_mask:0xf\n\t" "s_nop 0\n\t" "v_max_f32_dpp v182, v182, v182 row_ror:1 row_mask:0xf bank_mask:0xf\n\t" \
    "v_max_f32_dpp v183, v183, v183 row_ror:1 row_mask:0xf bank_mask:0xf\n\t" "v_pk_add_f32 v[178:179], v[178:179], v[182:183] neg_lo:[0,1] neg_hi:[0,1]\n\t" \
    "v_pk_add_f32 v[180:181], v[180:181], v[182:183] neg_lo:[0,1] neg_hi:[0,1]\n\t" "v_pk_add_f32 v[176:177], v[176:177], v[182:183] neg_lo:[0,1] neg_hi:[0,1]\n\t" \
    "v_pk_mul_f32 v[178:179], v[178:179], s[66:67]\n\t" "v_pk_mul_f32 v[180:181], v[180:181], s[66:67]\n\t" "v_pk_mul_f32 v[176:177], v[176:177], s[66:67]\n\t" \
    "v_exp_f32 v178, v178\n\t" "v_exp_f32 v179, v179\n\t" "v_exp_f32 v180, v180\n\t" "v_exp_f32 v181, v181\n\t" "v_exp_f32 v176, v176\n\t" \
    "v_pk_add_f32 v[182:183], v[178:179], v[180:181]\n\t" "v_exp_f32 v177, v177\n\t" "s_nop 0\n\t" "v_add_f32_dpp v182, v182, v182 row_ror:8 row_mask:0xf bank_mask:0xf\n\t" \
    "v_add_f32_dpp v183, v183, v183 row_ror:8 row_mask:0xf bank_mask:0xf\n\t" "v_add_f32 v182, v182, v176\n\t" "v_add_f32 v183, v183, v177\n\t" "s_nop 0\n\t" \
    "v_add_f32_dpp v182, v182, v182 row_ror:4 row_mask:0xf bank_mask:0xf\n\t" "v_add_f32_dpp v183, v183, v183 row_ror:4 row_mask:0xf bank_mask:0xf\n\t" "s_nop 0\n\t" \
    "v_add_f32_dpp v182, v182, v182 row_ror:2 row_mask:0xf bank_mask:0xf\n\t" "v_add_f32_dpp v183, v183, v183 row_ror:2 row_mask:0xf bank_mask:0xf\n\t" "s_nop 0\n\t" \
    "v_add_f32_dpp v182, v182, v182 row_ror:1 row_mask:0xf bank_mask:0xf\n\t" "v_add_f32_dpp v183, v183, v183 row_ror:1 row_mask:0xf bank_mask:0xf\n\t" \
    "v_rcp_f32 v182, v182\n\t" "v_rcp_f32 v183, v183\n\t" "s_nop 0\n\t" "v_pk_mul_f32 v[176:177], v[176:177], v[182:183]\n\t" \
    "v_pk_mul_f32 v[178:179], v[178:179], v[182:183]\n\t" "v_pk_mul_f32 v[180:181], v[180:181], v[182:183]\n\t" "v_cvt_pk_bf16_f32 v176, v176, v177\n\t" \
    "v_cvt_pk_bf16_f32 v178, v178, v179\n\t" "v_cvt_pk_bf16_f32 v180, v180, v181\n\t" "v_mov_b32_dpp v177, v176 quad_perm:[1,0,3,2] row_mask:0xf bank_mask:0xf\n\t" \
    "v_mov_b32_dpp v179, v178 quad_perm:[1,0,3,2] row_mask:0xf bank_mask:0xf\n\t" "v_mov_b32_dpp v181, v180 quad_perm:[1,0,3,2] row_mask:0xf bank_mask:0xf\n\t" \
    "v_perm_b32 v184, v179, v178, %[ps]\n\t" "v_perm_b32 v185, v181, v180, %[ps]\n\t" "buffer_store_dwordx2 v[184:185], %[px], %[rs], %[pb] offen sc1\n\t" \
    "v_perm_b32 v186, v177, v176, %[ps]\n\t" "buffer_store_dword v186, %[pm], %[rs], %[pb] offen sc1\n\t" XT(96, 97) "s_branch 99f\n\t" "11:\n\t" "s_cmp_eq_u32 %[bd], 0\n\t" \
    "s_cbranch_scc1 16f\n\t" "s_sub_u32 %[bd], %[bd], 1\n\t" XCNT(100) "s_waitcnt lgkmcnt(0)\n\t" "s_nop 7\n\t" "s_branch 10b\n\t" "16:\n\t" "s_cmp_lg_u32 s60, 0\n\t" \
    "s_branch 12b\n\t" "14:\n\t" "s_cmp_eq_u32 %[bd], 0\n\t" "s_cbranch_scc1 15b\n\t" "s_sub_u32 %[bd], %[bd], 1\n\t" XCNT(101) "s_mov_b32 s60, 1\n\t" \
    "s_waitcnt lgkmcnt(0)\n\t" "s_nop 7\n\t" "s_branch 10b\n\t" "99:\n\t"
#define QK_STEP1 \
    "v_xor_b32 v216, 0x400, %[mo]\n\t" "s_mov_b32 s60, 0\n\t" "s_add_u32 s61, %[qb], 0x1000\n\t" "s_mov_b32 s66, 0x3e0293ee\n\t" "s_mov_b32 s67, 0x3e0293ee\n\t" XT(80, 81) \
    XT(98, 99) "s_barrier\n\t" XT(82, 83) "20:\n\t" "buffer_load_dwordx4 v[240:243], %[mo], %[rs], %[sb] offen offset:0 sc1\n\t" "v_mov_b64 v[224:225], 0\n\t" \
    "v_mov_b64 v[226:227], 0\n\t" "v_mov_b64 v[228:229], 0\n\t" "v_mov_b64 v[230:231], 0\n\t" "v_mov_b64 v[232:233], 0\n\t" "v_mov_b64 v[234:235], 0\n\t" \
    "v_mov_b64 v[236:237], 0\n\t" "v_mov_b64 v[238:239], 0\n\t" "buffer_load_dwordx4 v[244:247], v216, %[rs], %[sb] offen offset:0 sc1\n\t" \
    "ds_read_b128 v[160:163], %[l0] offset:0\n\t" "ds_read_b128 v[164:167], %[l0] offset:1024\n\t" "ds_read_b128 v[168:171], %[l0] offset:2048\n\t" \
    "ds_read_b128 v[172:175], %[l0] offset:3072\n\t" "s_nop 3\n\t" "buffer_load_dwordx4 v[248:251], %[mo], %[rs], %[sb] offen offset:2048 sc1\n\t" \
    "ds_read_b128 v[176:179], %[l0] offset:4096\n\t" "ds_read_b128 v[180:183], %[l0] offset:5120\n\t" "ds_read_b128 v[184:187], %[l0] offset:6144\n\t" \
    "ds_read_b128 v[188:191], %[l0] offset:7168\n\t" "s_nop 3\n\t" "buffer_load_dwordx4 v[252:255], v216, %[rs], %[sb] offen offset:2048 sc1\n\t" \
    "v_mov_b32 v208, 0xd028965f\n\t" "v_mov_b32 v209, 0xff800000\n\t" "s_mov_b32 s62, 0\n\t" "s_mov_b32 s63, -1\n\t" "v_mov_b32 v210, 0\n\t" "v_cmp_gt_i32 vcc, 0, %[ps]\n\t" \
    "v_cndmask_b32 v204, 0, v209, s[62:63]\n\t" "s_nop 0\n\t" "v_cndmask_b32 v200, 0, v208, vcc\n\t" "v_cmp_gt_i32 vcc, 1, %[ps]\n\t" \
    "v_cndmask_b32 v205, 0, v209, s[62:63]\n\t" "s_nop 0\n\t" "v_cndmask_b32 v201, 0, v208, vcc\n\t" "v_cmp_gt_i32 vcc, 2, %[ps]\n\t" \
    "v_cndmask_b32 v206, 0, v209, s[62:63]\n\t" "s_nop 0\n\t" "v_cndmask_b32 v202, 0, v208, vcc\n\t" "v_cmp_gt_i32 vcc, 3, %[ps]\n\t" \
    "v_cndmask_b32 v207, 0, v209, s[62:63]\n\t" "s_nop 0\n\t" "v_cndmask_b32 v203, 0, v208, vcc\n\t" "s_waitcnt vmcnt(3)\n\t" XT(84, 85) \
    "v_smfmac_f32_16x16x64_bf16 v[224:227], v[240:243], a[0:7], %[ix]\n\t" "ds_write_b128 %[l1], v[240:243] offset:4096\n\t" \
    "v_smfmac_f32_16x16x64_bf16 v[228:231], v[240:243], a[8:15], %[ix]\n\t" "v_max3_u32 v217, v240, v241, v242\n\t" \
    "v_smfmac_f32_16x16x64_bf16 v[232:235], v[240:243], v[32:39], %[ix]\n\t" "s_waitcnt lgkmcnt(7)\n\t" \
    "v_smfmac_f32_16x16x64_bf16 v[236:239], v[240:243], v[160:167], %[ix]\n\t" "ds_read_b128 v[160:163], %[l0] offset:8192\n\t" \
    "ds_read_b128 v[164:167], %[l0] offset:9216\n\t" "s_waitcnt vmcnt(2)\n\t" "v_smfmac_f32_16x16x64_bf16 v[224:227], v[244:247], a[16:23], %[ix]\n\t" \
    "ds_write_b128 %[l1], v[244:247] offset:5120\n\t" "v_max3_u32 v217, v217, v243, v244\n\t" "v_smfmac_f32_16x16x64_bf16 v[228:231], v[244:247], a[24:31], %[ix]\n\t" \
    "v_max3_u32 v217, v217, v245, v246\n\t" "v_max_u32 v217, v217, v247\n\t" "v_smfmac_f32_16x16x64_bf16 v[232:235], v[244:247], v[40:47], %[ix]\n\t" \
    "v_cmp_eq_u32 vcc, -1, v217\n\t" "s_cmp_lg_u32 s60, 0\n\t" "s_waitcnt lgkmcnt(8)\n\t" "v_smfmac_f32_16x16x64_bf16 v[236:239], v[244:247], v[168:175], %[ix]\n\t" \
    "s_cbranch_vccnz 21f\n\t" "22:\n\t" "ds_read_b128 v[168:171], %[l0] offset:10240\n\t" "s_waitcnt vmcnt(1)\n\t" \
    "v_smfmac_f32_16x16x64_bf16 v[224:227], v[248:251], a[32:39], %[ix]\n\t" "ds_write_b128 %[l1], v[248:251] offset:6144\n\t" "s_waitcnt lgkmcnt(2)\n\t" \
    "v_smfmac_f32_16x16x64_bf16 v[228:231], v[248:251], a[40:47], %[ix]\n\t" "s_cbranch_scc1 23f\n\t" "s_barrier\n\t" "23:\n\t" XT(86, 87) \
    "v_smfmac_f32_16x16x64_bf16 v[232:235], v[248:251], v[48:55], %[ix]\n\t" "ds_read_b128 v[192:195], %[l1] offset:8192\n\t" \
    "ds_read_b128 v[196:199], %[l1] offset:12288\n\t" "v_smfmac_f32_16x16x64_bf16 v[236:239], v[248:251], v[176:183], %[ix]\n\t" \
    "ds_read_b128 v[172:175], %[l0] offset:11264\n\t" "ds_read_b128 v[176:179], %[l0] offset:12288\n\t" "s_waitcnt vmcnt(0)\n\t" \
    "v_smfmac_f32_16x16x64_bf16 v[224:227], v[252:255], a[48:55], %[ix]\n\t" "ds_write_b128 %[l1], v[252:255] offset:7168\n\t" "v_max3_u32 v217, v248, v249, v250\n\t" \
    "v_smfmac_f32_16x16x64_bf16 v[228:231], v[252:255], a[56:63], %[ix]\n\t" "ds_read_b128 v[180:183], %[l0] offset:13312\n\t" "v_max3_u32 v217, v217, v251, v252\n\t" \
    "v_max3_u32 v217, v217, v253, v254\n\t" "v_smfmac_f32_16x16x64_bf16 v[232:235], v[252:255], v[56:63], %[ix]\n\t" "v_max_u32 v217, v217, v255\n\t" \
    "v_cmp_eq_u32 vcc, -1, v217\n\t" "v_smfmac_f32_16x16x64_bf16 v[236:239], v[252:255], v[184:191], %[ix]\n\t" "s_cbranch_vccnz 24f\n\t" "25:\n\t" \
    "ds_read_b128 v[240:243], %[l1] offset:0\n\t" "s_waitcnt lgkmcnt(6)\n\t" "v_smfmac_f32_16x16x64_bf16 v[224:227], v[192:195], a[64:71], %[ix]\n\t" \
    "ds_read_b128 v[184:187], %[l0] offset:14336\n\t" "ds_read_b128 v[188:191], %[l0] offset:15360\n\t" \
    "v_smfmac_f32_16x16x64_bf16 v[228:231], v[192:195], a[72:79], %[ix]\n\t" "ds_read_b128 v[244:247], %[l1] offset:9216\n\t" "s_waitcnt lgkmcnt(5)\n\t" \
    "v_smfmac_f32_16x16x64_bf16 v[232:235], v[192:195], v[64:71], %[ix]\n\t" "s_barrier\n\t" XT(88, 89) \
    "v_smfmac_f32_16x16x64_bf16 v[236:239], v[192:195], v[160:167], %[ix]\n\t" "ds_read_b128 v[160:163], %[l0] offset:16384\n\t" \
    "ds_read_b128 v[164:167], %[l0] offset:17408\n\t" "v_smfmac_f32_16x16x64_bf16 v[224:227], v[196:199], a[80:87], %[ix]\n\t" \
    "ds_read_b128 v[248:251], %[l1] offset:13312\n\t" "ds_read_b128 v[252:255], %[l1] offset:1024\n\t" \
    "v_smfmac_f32_16x16x64_bf16 v[228:231], v[196:199], a[88:95], %[ix]\n\t" "ds_read_b128 v[192:195], %[l1] offset:10240\n\t" \
    "v_smfmac_f32_16x16x64_bf16 v[232:235], v[196:199], v[72:79], %[ix]\n\t" "buffer_load_dwordx4 v[16:19], %[hq], %[rs], %[qb] offen offset:0 sc1\n\t" \
    "v_smfmac_f32_16x16x64_bf16 v[236:239], v[196:199], v[168:175], %[ix]\n\t" "ds_read_b128 v[168:171], %[l0] offset:18432\n\t" \
    "ds_read_b128 v[172:175], %[l0] offset:19456\n\t" "s_waitcnt lgkmcnt(10)\n\t" "v_smfmac_f32_16x16x64_bf16 v[224:227], v[240:243], a[96:103], %[ix]\n\t" \
    "ds_read_b128 v[196:199], %[l1] offset:14336\n\t" "v_smfmac_f32_16x16x64_bf16 v[228:231], v[240:243], a[104:111], %[ix]\n\t" \
    "v_smfmac_f32_16x16x64_bf16 v[232:235], v[240:243], v[80:87], %[ix]\n\t" "buffer_load_dwordx4 v[20:23], %[hq], %[rs], %[qb] offen offset:2048 sc1\n\t" \
    "v_smfmac_f32_16x16x64_bf16 v[236:239], v[240:243], v[176:183], %[ix]\n\t" "ds_read_b128 v[176:179], %[l0] offset:20480\n\t" \
    "ds_read_b128 v[180:183], %[l0] offset:21504\n\t" "s_waitcnt lgkmcnt(10)\n\t" "v_smfmac_f32_16x16x64_bf16 v[224:227], v[244:247], a[112:119], %[ix]\n\t" \
    "ds_read_b128 v[240:243], %[l1] offset:2048\n\t" "v_smfmac_f32_16x16x64_bf16 v[228:231], v[244:247], a[120:127], %[ix]\n\t" \
    "v_smfmac_f32_16x16x64_bf16 v[232:235], v[244:247], v[88:95], %[ix]\n\t" "buffer_load_dwordx4 v[24:27], %[hq], %[rs], s61 offen offset:0 sc1\n\t" \
    "v_smfmac_f32_16x16x64_bf16 v[236:239], v[244:247], v[184:191], %[ix]\n\t" "ds_read_b128 v[184:187], %[l0] offset:22528\n\t" \
    "ds_read_b128 v[188:191], %[l0] offset:23552\n\t" "s_waitcnt lgkmcnt(10)\n\t" "v_smfmac_f32_16x16x64_bf16 v[224:227], v[248:251], a[128:135], %[ix]\n\t" \
    "ds_read_b128 v[244:247], %[l1] offset:11264\n\t" "v_smfmac_f32_16x16x64_bf16 v[228:231], v[248:251], a[136:143], %[ix]\n\t" \
    "v_smfmac_f32_16x16x64_bf16 v[232:235], v[248:251], v[96:103], %[ix]\n\t" "buffer_load_dwordx4 v[28:31], %[hq], %[rs], s61 offen offset:2048 sc1\n\t" \
    "v_smfmac_f32_16x16x64_bf16 v[236:239], v[248:251], v[160:167], %[ix]\n\t" "ds_read_b128 v[160:163], %[l0] offset:24576\n\t" \
    "ds_read_b128 v[164:167], %[l0] offset:25600\n\t" "s_waitcnt lgkmcnt(12)\n\t" "v_smfmac_f32_16x16x64_bf16 v[224:227], v[252:255], a[144:151], %[ix]\n\t" \
    "ds_read_b128 v[248:251], %[l1] offset:15360\n\t" "v_smfmac_f32_16x16x64_bf16 v[228:231], v[252:255], a[152:159], %[ix]\n\t" \
    "v_smfmac_f32_16x16x64_bf16 v[232:235], v[252:255], v[104:111], %[ix]\n\t" "s_waitcnt lgkmcnt(10)\n\t" \
    "v_smfmac_f32_16x16x64_bf16 v[236:239], v[252:255], v[168:175], %[ix]\n\t" "ds_read_b128 v[168:171], %[l0] offset:26624\n\t" \
    "ds_read_b128 v[172:175], %[l0] offset:27648\n\t" "v_smfmac_f32_16x16x64_bf16 v[224:227], v[192:195], a[160:167], %[ix]\n\t" \
    "ds_read_b128 v[252:255], %[l1] offset:3072\n\t" "v_smfmac_f32_16x16x64_bf16 v[228:231], v[192:195], a[168:175], %[ix]\n\t" "s_waitcnt lgkmcnt(12)\n\t" \
    "v_smfmac_f32_16x16x64_bf16 v[224:227], v[196:199], a[176:183], %[ix]\n\t" "v_smfmac_f32_16x16x64_bf16 v[228:231], v[196:199], a[184:191], %[ix]\n\t" \
    "s_waitcnt lgkmcnt(9)\n\t" "v_smfmac_f32_16x16x64_bf16 v[224:227], v[240:243], a[192:199], %[ix]\n\t" \
    "v_smfmac_f32_16x16x64_bf16 v[228:231], v[240:243], a[200:207], %[ix]\n\t" "s_waitcnt lgkmcnt(6)\n\t" \
    "v_smfmac_f32_16x16x64_bf16 v[224:227], v[244:247], a[208:215], %[ix]\n\t" "v_smfmac_f32_16x16x64_bf16 v[228:231], v[244:247], a[216:223], %[ix]\n\t" \
    "s_waitcnt lgkmcnt(3)\n\t" "v_smfmac_f32_16x16x64_bf16 v[224:227], v[248:251], a[224:231], %[ix]\n\t" \
    "v_smfmac_f32_16x16x64_bf16 v[228:231], v[248:251], a[232:239], %[ix]\n\t" "s_waitcnt lgkmcnt(0)\n\t" \
    "v_smfmac_f32_16x16x64_bf16 v[224:227], v[252:255], a[240:247], %[ix]\n\t" "s_nop 1\n\t" "v_smfmac_f32_16x16x64_bf16 v[228:231], v[252:255], a[248:255], %[ix]\n\t" \
    XT(90, 91) "s_nop 1\n\t" "v_smfmac_f32_16x16x64_bf16 v[232:235], v[192:195], v[112:119], %[ix]\n\t" "s_nop 1\n\t" \
    "v_smfmac_f32_16x16x64_bf16 v[236:239], v[192:195], v[176:183], %[ix]\n\t" "ds_read_b128 v[176:179], %[l0] offset:28672\n\t" "v_add_f32 v224, v224, v225\n\t" \
    "v_add_f32 v226, v226, v227\n\t" "v_smfmac_f32_16x16x64_bf16 v[232:235], v[196:199], v[120:127], %[ix]\n\t" "ds_read_b128 v[180:183], %[l0] offset:29696\n\t" \
    "v_add_f32 v228, v228, v229\n\t" "v_add_f32 v230, v230, v231\n\t" "v_smfmac_f32_16x16x64_bf16 v[236:239], v[196:199], v[184:191], %[ix]\n\t" \
    "ds_read_b128 v[184:187], %[l0] offset:30720\n\t" "v_cvt_pk_bf16_f32 v218, v224, v228\n\t" "v_cvt_pk_bf16_f32 v219, v226, v230\n\t" \
    "v_smfmac_f32_16x16x64_bf16 v[232:235], v[240:243], v[128:135], %[ix]\n\t" "ds_write2_b32 %[qw], v218, v219 offset1:8\n\t" \
    "ds_read_b128 v[188:191], %[l0] offset:31744\n\t" "v_smfmac_f32_16x16x64_bf16 v[236:239], v[240:243], v[160:167], %[ix]\n\t" \
    "v_smfmac_f32_16x16x64_bf16 v[232:235], v[244:247], v[136:143], %[ix]\n\t" "v_smfmac_f32_16x16x64_bf16 v[236:239], v[244:247], v[168:175], %[ix]\n\t" \
    "s_waitcnt lgkmcnt(1)\n\t" "s_barrier\n\t" "v_smfmac_f32_16x16x64_bf16 v[232:235], v[248:251], v[144:151], %[ix]\n\t" \
    "v_smfmac_f32_16x16x64_bf16 v[236:239], v[248:251], v[176:183], %[ix]\n\t" "v_smfmac_f32_16x16x64_bf16 v[232:235], v[252:255], v[152:159], %[ix]\n\t" \
    "s_waitcnt lgkmcnt(0)\n\t" "v_smfmac_f32_16x16x64_bf16 v[236:239], v[252:255], v[184:191], %[ix]\n\t" XT(92, 93) "s_nop 6\n\t" "v_add_f32 v232, v232, v233\n\t" \
    "v_add_f32 v234, v234, v235\n\t" "v_add_f32 v236, v236, v237\n\t" "v_add_f32 v238, v238, v239\n\t" "v_cvt_pk_bf16_f32 v232, v232, v236\n\t" \
    "v_cvt_pk_bf16_f32 v233, v234, v238\n\t" "ds_write2_b32 %[kw], v232, v233 offset1:68\n\t" "s_waitcnt lgkmcnt(0)\n\t" "s_barrier\n\t" XT(94, 95) \
    "s_cmp_ge_u32 %[sq], %[sn]\n\t" "s_cbranch_scc1 277f\n\t" "275:\n\t" "global_load_dword v240, v210, %[fq] sc1\n\t" "s_waitcnt vmcnt(0)\n\t" \
    "v_readfirstlane_b32 %[sq], v240\n\t" "s_nop 3\n\t" "s_cmp_ge_u32 %[sq], %[sn]\n\t" "s_cbranch_scc1 276f\n\t" "s_cmp_eq_u32 %[bd], 0\n\t" "s_cbranch_scc1 276f\n\t" \
    "s_sub_u32 %[bd], %[bd], 1\n\t" "s_sleep 2\n\t" "s_branch 275b\n\t" "276:\n\t" "buffer_load_dwordx4 v[16:19], %[hq], %[rs], %[qb] offen offset:0 sc1\n\t" \
    "buffer_load_dwordx4 v[20:23], %[hq], %[rs], %[qb] offen offset:2048 sc1\n\t" "buffer_load_dwordx4 v[24:27], %[hq], %[rs], s61 offen offset:0 sc1\n\t" \
    "buffer_load_dwordx4 v[28:31], %[hq], %[rs], s61 offen offset:2048 sc1\n\t" "277:\n\t" "s_nop 3\n\t" "ds_read_b128 v[160:163], %[kx] offset:0\n\t" \
    "ds_read_b128 v[164:167], %[kx] offset:2048\n\t" "ds_read_b128 v[168:171], %[kx] offset:4096\n\t" "ds_read_b128 v[172:175], %[kx] offset:6144\n\t" \
    "ds_read_b128 v[224:227], %[km] offset:0\n\t" "ds_read_b128 v[228:231], %[km] offset:64\n\t" "ds_read_b128 v[232:235], %[km] offset:128\n\t" \
    "ds_read_b128 v[236:239], %[km] offset:192\n\t" "s_waitcnt vmcnt(0)\n\t" "global_load_dword v240, v210, %[fq] sc1\n\t" "s_waitcnt lgkmcnt(7)\n\t" \
    "v_mfma_f32_16x16x32_bf16 v[248:251], v[160:163], v[16:19], v[200:203]\n\t" "s_waitcnt lgkmcnt(6)\n\t" \
    "v_mfma_f32_16x16x32_bf16 v[248:251], v[164:167], v[20:23], v[248:251]\n\t" "s_waitcnt lgkmcnt(5)\n\t" \
    "v_mfma_f32_16x16x32_bf16 v[248:251], v[168:171], v[24:27], v[248:251]\n\t" "s_waitcnt lgkmcnt(4)\n\t" \
    "v_mfma_f32_16x16x32_bf16 v[248:251], v[172:175], v[28:31], v[248:251]\n\t" "s_waitcnt lgkmcnt(3)\n\t" \
    "v_mfma_f32_16x16x32_bf16 v[244:247], v[224:227], v[16:19], v[204:207]\n\t" "s_waitcnt lgkmcnt(2)\n\t" \
    "v_mfma_f32_16x16x32_bf16 v[244:247], v[228:231], v[20:23], v[244:247]\n\t" "s_waitcnt lgkmcnt(1)\n\t" \
    "v_mfma_f32_16x16x32_bf16 v[244:247], v[232:235], v[24:27], v[244:247]\n\t" "s_waitcnt lgkmcnt(0)\n\t" \
    "v_mfma_f32_16x16x32_bf16 v[244:247], v[236:239], v[28:31], v[244:247]\n\t" "v_mov_b32 v192, -1\n\t" "v_mov_b32 v193, -1\n\t" "v_mov_b32 v194, -1\n\t" \
    "v_mov_b32 v195, -1\n\t" "v_max3_f32 v242, v248, v249, v250\n\t" "v_max_f32 v242, v242, v251\n\t" "s_nop 1\n\t" "v_max3_f32 v242, v242, v244, v245\n\t" \
    "v_max3_f32 v242, v242, v246, v247\n\t" "v_mov_b32 v243, v242\n\t" "s_nop 1\n\t" "v_permlane32_swap_b32 v242, v243\n\t" "v_max_f32 v242, v242, v243\n\t" \
    "v_mov_b32 v243, v242\n\t" "s_nop 1\n\t" "v_permlane16_swap_b32 v242, v243\n\t" "v_max_f32 v242, v242, v243\n\t" \
    "v_pk_add_f32 v[244:245], v[244:245], v[242:243] op_sel_hi:[1,0] neg_lo:[0,1] neg_hi:[0,1]\n\t" \
    "v_pk_add_f32 v[246:247], v[246:247], v[242:243] op_sel_hi:[1,0] neg_lo:[0,1] neg_hi:[0,1]\n\t" \
    "v_pk_add_f32 v[248:249], v[248:249], v[242:243] op_sel_hi:[1,0] neg_lo:[0,1] neg_hi:[0,1]\n\t" \
    "v_pk_add_f32 v[250:251], v[250:251], v[242:243] op_sel_hi:[1,0] neg_lo:[0,1] neg_hi:[0,1]\n\t" "v_pk_mul_f32 v[244:245], v[244:245], s[66:67]\n\t" \
    "v_pk_mul_f32 v[246:247], v[246:247], s[66:67]\n\t" "v_pk_mul_f32 v[248:249], v[248:249], s[66:67]\n\t" "v_pk_mul_f32 v[250:251], v[250:251], s[66:67]\n\t" \
    "v_exp_f32 v244, v244\n\t" "v_exp_f32 v245, v245\n\t" "v_exp_f32 v246, v246\n\t" "v_exp_f32 v247, v247\n\t" "v_exp_f32 v248, v248\n\t" "v_exp_f32 v249, v249\n\t" \
    "v_exp_f32 v250, v250\n\t" "v_exp_f32 v251, v251\n\t" "v_pk_add_f32 v[242:243], v[244:245], v[246:247]\n\t" "v_pk_add_f32 v[242:243], v[242:243], v[248:249]\n\t" \
    "v_pk_add_f32 v[242:243], v[242:243], v[250:251]\n\t" "v_add_f32 v242, v242, v243\n\t" "v_mov_b32 v243, v242\n\t" "s_nop 1\n\t" "v_permlane32_swap_b32 v242, v243\n\t" \
    "v_add_f32 v242, v242, v243\n\t" "v_mov_b32 v243, v242\n\t" "s_nop 1\n\t" "v_permlane16_swap_b32 v242, v243\n\t" "v_add_f32 v242, v242, v243\n\t" \
    "v_rcp_f32 v242, v242\n\t" "s_nop 0\n\t" "v_pk_mul_f32 v[244:245], v[244:245], v[242:243] op_sel_hi:[1,0]\n\t" \
    "v_pk_mul_f32 v[246:247], v[246:247], v[242:243] op_sel_hi:[1,0]\n\t" "v_pk_mul_f32 v[248:249], v[248:249], v[242:243] op_sel_hi:[1,0]\n\t" \
    "v_pk_mul_f32 v[250:251], v[250:251], v[242:243] op_sel_hi:[1,0]\n\t" "v_cvt_pk_bf16_f32 v162, v244, v245\n\t" "v_cvt_pk_bf16_f32 v163, v246, v247\n\t" \
    "v_cvt_pk_bf16_f32 v164, v248, v249\n\t" "v_cvt_pk_bf16_f32 v165, v250, v251\n\t" "buffer_store_dwordx2 v[164:165], %[px], %[rs], %[tb] offen sc1\n\t" \
    "buffer_store_dwordx2 v[162:163], %[pm], %[rs], %[tb] offen sc1\n\t" "buffer_store_dwordx2 v[192:193], %[px], %[rs], %[tr] offen sc1\n\t" \
    "buffer_store_dwordx2 v[192:193], %[pm], %[rs], %[tr] offen sc1\n\t" "s_waitcnt vmcnt(4)\n\t" "v_readfirstlane_b32 s62, v240\n\t" "s_nop 3\n\t" \
    "s_max_u32 %[sq], %[sq], s62\n\t" XT(96, 97) "s_branch 99f\n\t" "21:\n\t" "s_cmp_eq_u32 %[bd], 0\n\t" "s_cbranch_scc1 26f\n\t" "s_sub_u32 %[bd], %[bd], 1\n\t" XCNT(100) \
    "s_waitcnt lgkmcnt(0)\n\t" "s_nop 7\n\t" "s_branch 20b\n\t" "26:\n\t" "s_cmp_lg_u32 s60, 0\n\t" "s_branch 22b\n\t" "24:\n\t" "s_cmp_eq_u32 %[bd], 0\n\t" \
    "s_cbranch_scc1 25b\n\t" "s_sub_u32 %[bd], %[bd], 1\n\t" XCNT(101) "s_mov_b32 s60, 1\n\t" "s_waitcnt lgkmcnt(0)\n\t" "s_nop 7\n\t" "s_branch 20b\n\t" "99:\n\t"
#define QK_STEP2 \
    "v_xor_b32 v216, 0x400, %[mo]\n\t" "s_mov_b32 s60, 0\n\t" "s_add_u32 s61, %[qb], 0x1000\n\t" "s_mov_b32 s66, 0x3e0293ee\n\t" "s_mov_b32 s67, 0x3e0293ee\n\t" XT(80, 81) \
    XT(98, 99) "s_barrier\n\t" XT(82, 83) "30:\n\t" "buffer_load_dwordx4 v[240:243], %[mo], %[rs], %[sb] offen offset:0 sc1\n\t" "v_mov_b64 v[224:225], 0\n\t" \
    "v_mov_b64 v[226:227], 0\n\t" "v_mov_b64 v[228:229], 0\n\t" "v_mov_b64 v[230:231], 0\n\t" "v_mov_b64 v[232:233], 0\n\t" "v_mov_b64 v[234:235], 0\n\t" \
    "v_mov_b64 v[236:237], 0\n\t" "v_mov_b64 v[238:239], 0\n\t" "buffer_load_dwordx4 v[244:247], v216, %[rs], %[sb] offen offset:0 sc1\n\t" \
    "ds_read_b128 v[160:163], %[l0] offset:0\n\t" "ds_read_b128 v[164:167], %[l0] offset:1024\n\t" "ds_read_b128 v[168:171], %[l0] offset:2048\n\t" \
    "ds_read_b128 v[172:175], %[l0] offset:3072\n\t" "s_nop 3\n\t" "buffer_load_dwordx4 v[248:251], %[mo], %[rs], %[sb] offen offset:2048 sc1\n\t" \
    "ds_read_b128 v[176:179], %[l0] offset:4096\n\t" "ds_read_b128 v[180:183], %[l0] offset:5120\n\t" "ds_read_b128 v[184:187], %[l0] offset:6144\n\t" \
    "ds_read_b128 v[188:191], %[l0] offset:7168\n\t" "s_nop 3\n\t" "buffer_load_dwordx4 v[252:255], v216, %[rs], %[sb] offen offset:2048 sc1\n\t" \
    "v_mov_b32 v208, 0xd028965f\n\t" "v_mov_b32 v209, 0xff800000\n\t" "s_mov_b32 s62, 0\n\t" "s_mov_b32 s63, -1\n\t" "v_mov_b32 v210, 0\n\t" "v_cmp_gt_i32 vcc, 0, %[ps]\n\t" \
    "v_cndmask_b32 v204, 0, v209, s[62:63]\n\t" "s_nop 0\n\t" "v_cndmask_b32 v200, 0, v208, vcc\n\t" "v_cmp_gt_i32 vcc, 1, %[ps]\n\t" \
    "v_cndmask_b32 v205, 0, v209, s[62:63]\n\t" "s_nop 0\n\t" "v_cndmask_b32 v201, 0, v208, vcc\n\t" "v_cmp_gt_i32 vcc, 2, %[ps]\n\t" \
    "v_cndmask_b32 v206, 0, v209, s[62:63]\n\t" "s_nop 0\n\t" "v_cndmask_b32 v202, 0, v208, vcc\n\t" "v_cmp_gt_i32 vcc, 3, %[ps]\n\t" \
    "v_cndmask_b32 v207, 0, v209, s[62:63]\n\t" "s_nop 0\n\t" "v_cndmask_b32 v203, 0, v208, vcc\n\t" "s_waitcnt vmcnt(3)\n\t" XT(84, 85) \
    "v_smfmac_f32_16x16x64_bf16 v[224:227], v[240:243], a[0:7], %[ix]\n\t" "ds_write_b128 %[l1], v[240:243] offset:8192\n\t" \
    "v_smfmac_f32_16x16x64_bf16 v[228:231], v[240:243], a[8:15], %[ix]\n\t" "v_max3_u32 v217, v240, v241, v242\n\t" \
    "v_smfmac_f32_16x16x64_bf16 v[232:235], v[240:243], v[32:39], %[ix]\n\t" "s_waitcnt lgkmcnt(7)\n\t" \
    "v_smfmac_f32_16x16x64_bf16 v[236:239], v[240:243], v[160:167], %[ix]\n\t" "ds_read_b128 v[160:163], %[l0] offset:8192\n\t" \
    "ds_read_b128 v[164:167], %[l0] offset:9216\n\t" "s_waitcnt vmcnt(2)\n\t" "v_smfmac_f32_16x16x64_bf16 v[224:227], v[244:247], a[16:23], %[ix]\n\t" \
    "ds_write_b128 %[l1], v[244:247] offset:9216\n\t" "v_max3_u32 v217, v217, v243, v244\n\t" "v_smfmac_f32_16x16x64_bf16 v[228:231], v[244:247], a[24:31], %[ix]\n\t" \
    "v_max3_u32 v217, v217, v245, v246\n\t" "v_max_u32 v217, v217, v247\n\t" "v_smfmac_f32_16x16x64_bf16 v[232:235], v[244:247], v[40:47], %[ix]\n\t" \
    "v_cmp_eq_u32 vcc, -1, v217\n\t" "s_cmp_lg_u32 s60, 0\n\t" "s_waitcnt lgkmcnt(8)\n\t" "v_smfmac_f32_16x16x64_bf16 v[236:239], v[244:247], v[168:175], %[ix]\n\t" \
    "s_cbranch_vccnz 31f\n\t" "32:\n\t" "ds_read_b128 v[168:171], %[l0] offset:10240\n\t" "s_waitcnt vmcnt(1)\n\t" \
    "v_smfmac_f32_16x16x64_bf16 v[224:227], v[248:251], a[32:39], %[ix]\n\t" "ds_write_b128 %[l1], v[248:251] offset:10240\n\t" "s_waitcnt lgkmcnt(2)\n\t" \
    "v_smfmac_f32_16x16x64_bf16 v[228:231], v[248:251], a[40:47], %[ix]\n\t" "s_cbranch_scc1 33f\n\t" "s_barrier\n\t" "33:\n\t" XT(86, 87) \
    "v_smfmac_f32_16x16x64_bf16 v[232:235], v[248:251], v[48:55], %[ix]\n\t" "ds_read_b128 v[192:195], %[l1] offset:12288\n\t" "ds_read_b128 v[196:199], %[l1] offset:0\n\t" \
    "v_smfmac_f32_16x16x64_bf16 v[236:239], v[248:251], v[176:183], %[ix]\n\t" "ds_read_b128 v[172:175], %[l0] offset:11264\n\t" \
    "ds_read_b128 v[176:179], %[l0] offset:12288\n\t" "s_waitcnt vmcnt(0)\n\t" "v_smfmac_f32_16x16x64_bf16 v[224:227], v[252:255], a[48:55], %[ix]\n\t" \
    "ds_write_b128 %[l1], v[252:255] offset:11264\n\t" "v_max3_u32 v217, v248, v249, v250\n\t" "v_smfmac_f32_16x16x64_bf16 v[228:231], v[252:255], a[56:63], %[ix]\n\t" \
    "ds_read_b128 v[180:183], %[l0] offset:13312\n\t" "v_max3_u32 v217, v217, v251, v252\n\t" "v_max3_u32 v217, v217, v253, v254\n\t" \
    "v_smfmac_f32_16x16x64_bf16 v[232:235], v[252:255], v[56:63], %[ix]\n\t" "v_max_u32 v217, v217, v255\n\t" "v_cmp_eq_u32 vcc, -1, v217\n\t" \
    "v_smfmac_f32_16x16x64_bf16 v[236:239], v[252:255], v[184:191], %[ix]\n\t" "s_cbranch_vccnz 34f\n\t" "35:\n\t" "ds_read_b128 v[240:243], %[l1] offset:4096\n\t" \
    "s_waitcnt lgkmcnt(6)\n\t" "v_smfmac_f32_16x16x64_bf16 v[224:227], v[192:195], a[64:71], %[ix]\n\t" "ds_read_b128 v[184:187], %[l0] offset:14336\n\t" \
    "ds_read_b128 v[188:191], %[l0] offset:15360\n\t" "v_smfmac_f32_16x16x64_bf16 v[228:231], v[192:195], a[72:79], %[ix]\n\t" \
    "ds_read_b128 v[244:247], %[l1] offset:13312\n\t" "s_waitcnt lgkmcnt(5)\n\t" "v_smfmac_f32_16x16x64_bf16 v[232:235], v[192:195], v[64:71], %[ix]\n\t" "s_barrier\n\t" \
    XT(88, 89) "v_smfmac_f32_16x16x64_bf16 v[236:239], v[192:195], v[160:167], %[ix]\n\t" "ds_read_b128 v[160:163], %[l0] offset:16384\n\t" \
    "ds_read_b128 v[164:167], %[l0] offset:17408\n\t" "v_smfmac_f32_16x16x64_bf16 v[224:227], v[196:199], a[80:87], %[ix]\n\t" \
    "ds_read_b128 v[248:251], %[l1] offset:1024\n\t" "ds_read_b128 v[252:255], %[l1] offset:5120\n\t" "v_smfmac_f32_16x16x64_bf16 v[228:231], v[196:199], a[88:95], %[ix]\n\t" \
    "ds_read_b128 v[192:195], %[l1] offset:14336\n\t" "v_smfmac_f32_16x16x64_bf16 v[232:235], v[196:199], v[72:79], %[ix]\n\t" \
    "buffer_load_dwordx4 v[16:19], %[hq], %[rs], %[qb] offen offset:0 sc1\n\t" "v_smfmac_f32_16x16x64_bf16 v[236:239], v[196:199], v[168:175], %[ix]\n\t" \
    "ds_read_b128 v[168:171], %[l0] offset:18432\n\t" "ds_read_b128 v[172:175], %[l0] offset:19456\n\t" "s_waitcnt lgkmcnt(10)\n\t" \
    "v_smfmac_f32_16x16x64_bf16 v[224:227], v[240:243], a[96:103], %[ix]\n\t" "ds_read_b128 v[196:199], %[l1] offset:2048\n\t" \
    "v_smfmac_f32_16x16x64_bf16 v[228:231], v[240:243], a[104:111], %[ix]\n\t" "v_smfmac_f32_16x16x64_bf16 v[232:235], v[240:243], v[80:87], %[ix]\n\t" \
    "buffer_load_dwordx4 v[20:23], %[hq], %[rs], %[qb] offen offset:2048 sc1\n\t" "v_smfmac_f32_16x16x64_bf16 v[236:239], v[240:243], v[176:183], %[ix]\n\t" \
    "ds_read_b128 v[176:179], %[l0] offset:20480\n\t" "ds_read_b128 v[180:183], %[l0] offset:21504\n\t" "s_waitcnt lgkmcnt(10)\n\t" \
    "v_smfmac_f32_16x16x64_bf16 v[224:227], v[244:247], a[112:119], %[ix]\n\t" "ds_read_b128 v[240:243], %[l1] offset:6144\n\t" \
    "v_smfmac_f32_16x16x64_bf16 v[228:231], v[244:247], a[120:127], %[ix]\n\t" "v_smfmac_f32_16x16x64_bf16 v[232:235], v[244:247], v[88:95], %[ix]\n\t" \
    "buffer_load_dwordx4 v[24:27], %[hq], %[rs], s61 offen offset:0 sc1\n\t" "v_smfmac_f32_16x16x64_bf16 v[236:239], v[244:247], v[184:191], %[ix]\n\t" \
    "ds_read_b128 v[184:187], %[l0] offset:22528\n\t" "ds_read_b128 v[188:191], %[l0] offset:23552\n\t" "s_waitcnt lgkmcnt(10)\n\t" \
    "v_smfmac_f32_16x16x64_bf16 v[224:227], v[248:251], a[128:135], %[ix]\n\t" "ds_read_b128 v[244:247], %[l1] offset:15360\n\t" \
    "v_smfmac_f32_16x16x64_bf16 v[228:231], v[248:251], a[136:143], %[ix]\n\t" "v_smfmac_f32_16x16x64_bf16 v[232:235], v[248:251], v[96:103], %[ix]\n\t" \
    "buffer_load_dwordx4 v[28:31], %[hq], %[rs], s61 offen offset:2048 sc1\n\t" "v_smfmac_f32_16x16x64_bf16 v[236:239], v[248:251], v[160:167], %[ix]\n\t" \
    "ds_read_b128 v[160:163], %[l0] offset:24576\n\t" "ds_read_b128 v[164:167], %[l0] offset:25600\n\t" "s_waitcnt lgkmcnt(12)\n\t" \
    "v_smfmac_f32_16x16x64_bf16 v[224:227], v[252:255], a[144:151], %[ix]\n\t" "ds_read_b128 v[248:251], %[l1] offset:3072\n\t" \
    "v_smfmac_f32_16x16x64_bf16 v[228:231], v[252:255], a[152:159], %[ix]\n\t" "v_smfmac_f32_16x16x64_bf16 v[232:235], v[252:255], v[104:111], %[ix]\n\t" \
    "s_waitcnt lgkmcnt(10)\n\t" "v_smfmac_f32_16x16x64_bf16 v[236:239], v[252:255], v[168:175], %[ix]\n\t" "ds_read_b128 v[168:171], %[l0] offset:26624\n\t" \
    "ds_read_b128 v[172:175], %[l0] offset:27648\n\t" "v_smfmac_f32_16x16x64_bf16 v[224:227], v[192:195], a[160:167], %[ix]\n\t" \
    "ds_read_b128 v[252:255], %[l1] offset:7168\n\t" "v_smfmac_f32_16x16x64_bf16 v[228:231], v[192:195], a[168:175], %[ix]\n\t" "s_waitcnt lgkmcnt(12)\n\t" \
    "v_smfmac_f32_16x16x64_bf16 v[224:227], v[196:199], a[176:183], %[ix]\n\t" "v_smfmac_f32_16x16x64_bf16 v[228:231], v[196:199], a[184:191], %[ix]\n\t" \
    "s_waitcnt lgkmcnt(9)\n\t" "v_smfmac_f32_16x16x64_bf16 v[224:227], v[240:243], a[192:199], %[ix]\n\t" \
    "v_smfmac_f32_16x16x64_bf16 v[228:231], v[240:243], a[200:207], %[ix]\n\t" "s_waitcnt lgkmcnt(6)\n\t" \
    "v_smfmac_f32_16x16x64_bf16 v[224:227], v[244:247], a[208:215], %[ix]\n\t" "v_smfmac_f32_16x16x64_bf16 v[228:231], v[244:247], a[216:223], %[ix]\n\t" \
    "s_waitcnt lgkmcnt(3)\n\t" "v_smfmac_f32_16x16x64_bf16 v[224:227], v[248:251], a[224:231], %[ix]\n\t" \
    "v_smfmac_f32_16x16x64_bf16 v[228:231], v[248:251], a[232:239], %[ix]\n\t" "s_waitcnt lgkmcnt(0)\n\t" \
    "v_smfmac_f32_16x16x64_bf16 v[224:227], v[252:255], a[240:247], %[ix]\n\t" "s_nop 1\n\t" "v_smfmac_f32_16x16x64_bf16 v[228:231], v[252:255], a[248:255], %[ix]\n\t" \
    XT(90, 91) "s_nop 1\n\t" "v_smfmac_f32_16x16x64_bf16 v[232:235], v[192:195], v[112:119], %[ix]\n\t" "s_nop 1\n\t" \
    "v_smfmac_f32_16x16x64_bf16 v[236:239], v[192:195], v[176:183], %[ix]\n\t" "ds_read_b128 v[176:179], %[l0] offset:28672\n\t" "v_add_f32 v224, v224, v225\n\t" \
    "v_add_f32 v226, v226, v227\n\t" "v_smfmac_f32_16x16x64_bf16 v[232:235], v[196:199], v[120:127], %[ix]\n\t" "ds_read_b128 v[180:183], %[l0] offset:29696\n\t" \
    "v_add_f32 v228, v228, v229\n\t" "v_add_f32 v230, v230, v231\n\t" "v_smfmac_f32_16x16x64_bf16 v[236:239], v[196:199], v[184:191], %[ix]\n\t" \
    "ds_read_b128 v[184:187], %[l0] offset:30720\n\t" "v_cvt_pk_bf16_f32 v218, v224, v228\n\t" "v_cvt_pk_bf16_f32 v219, v226, v230\n\t" \
    "v_smfmac_f32_16x16x64_bf16 v[232:235], v[240:243], v[128:135], %[ix]\n\t" "ds_write2_b32 %[qw], v218, v219 offset1:8\n\t" \
    "ds_read_b128 v[188:191], %[l0] offset:31744\n\t" "v_smfmac_f32_16x16x64_bf16 v[236:239], v[240:243], v[160:167], %[ix]\n\t" \
    "v_smfmac_f32_16x16x64_bf16 v[232:235], v[244:247], v[136:143], %[ix]\n\t" "v_smfmac_f32_16x16x64_bf16 v[236:239], v[244:247], v[168:175], %[ix]\n\t" \
    "s_waitcnt lgkmcnt(1)\n\t" "s_barrier\n\t" "v_smfmac_f32_16x16x64_bf16 v[232:235], v[248:251], v[144:151], %[ix]\n\t" \
    "v_smfmac_f32_16x16x64_bf16 v[236:239], v[248:251], v[176:183], %[ix]\n\t" "v_smfmac_f32_16x16x64_bf16 v[232:235], v[252:255], v[152:159], %[ix]\n\t" \
    "s_waitcnt lgkmcnt(0)\n\t" "v_smfmac_f32_16x16x64_bf16 v[236:239], v[252:255], v[184:191], %[ix]\n\t" XT(92, 93) "s_nop 6\n\t" "v_add_f32 v232, v232, v233\n\t" \
    "v_add_f32 v234, v234, v235\n\t" "v_add_f32 v236, v236, v237\n\t" "v_add_f32 v238, v238, v239\n\t" "v_cvt_pk_bf16_f32 v232, v232, v236\n\t" \
    "v_cvt_pk_bf16_f32 v233, v234, v238\n\t" "ds_write2_b32 %[kw], v232, v233 offset1:68\n\t" "s_waitcnt lgkmcnt(0)\n\t" "s_barrier\n\t" XT(94, 95) \
    "s_cmp_ge_u32 %[sq], %[sn]\n\t" "s_cbranch_scc1 377f\n\t" "375:\n\t" "global_load_dword v240, v210, %[fq] sc1\n\t" "s_waitcnt vmcnt(0)\n\t" \
    "v_readfirstlane_b32 %[sq], v240\n\t" "s_nop 3\n\t" "s_cmp_ge_u32 %[sq], %[sn]\n\t" "s_cbranch_scc1 376f\n\t" "s_cmp_eq_u32 %[bd], 0\n\t" "s_cbranch_scc1 376f\n\t" \
    "s_sub_u32 %[bd], %[bd], 1\n\t" "s_sleep 2\n\t" "s_branch 375b\n\t" "376:\n\t" "buffer_load_dwordx4 v[16:19], %[hq], %[rs], %[qb] offen offset:0 sc1\n\t" \
    "buffer_load_dwordx4 v[20:23], %[hq], %[rs], %[qb] offen offset:2048 sc1\n\t" "buffer_load_dwordx4 v[24:27], %[hq], %[rs], s61 offen offset:0 sc1\n\t" \
    "buffer_load_dwordx4 v[28:31], %[hq], %[rs], s61 offen offset:2048 sc1\n\t" "377:\n\t" "s_nop 3\n\t" "ds_read_b128 v[160:163], %[kx] offset:0\n\t" \
    "ds_read_b128 v[164:167], %[kx] offset:2048\n\t" "ds_read_b128 v[168:171], %[kx] offset:4096\n\t" "ds_read_b128 v[172:175], %[kx] offset:6144\n\t" \
    "ds_read_b128 v[176:179], %[kx] offset:256\n\t" "ds_read_b128 v[180:183], %[kx] offset:2304\n\t" "ds_read_b128 v[184:187], %[kx] offset:4352\n\t" \
    "ds_read_b128 v[188:191], %[kx] offset:6400\n\t" "ds_read_b128 v[224:227], %[km] offset:0\n\t" "ds_read_b128 v[228:231], %[km] offset:64\n\t" \
    "ds_read_b128 v[232:235], %[km] offset:128\n\t" "ds_read_b128 v[236:239], %[km] offset:192\n\t" "s_waitcnt vmcnt(0)\n\t" "global_load_dword v240, v210, %[fq] sc1\n\t" \
    "s_waitcnt lgkmcnt(11)\n\t" "v_mfma_f32_16x16x32_bf16 v[248:251], v[160:163], v[16:19], 0\n\t" "s_waitcnt lgkmcnt(10)\n\t" \
    "v_mfma_f32_16x16x32_bf16 v[248:251], v[164:167], v[20:23], v[248:251]\n\t" "s_waitcnt lgkmcnt(9)\n\t" \
    "v_mfma_f32_16x16x32_bf16 v[248:251], v[168:171], v[24:27], v[248:251]\n\t" "s_waitcnt lgkmcnt(8)\n\t" \
    "v_mfma_f32_16x16x32_bf16 v[248:251], v[172:175], v[28:31], v[248:251]\n\t" "s_waitcnt lgkmcnt(7)\n\t" \
    "v_mfma_f32_16x16x32_bf16 v[252:255], v[176:179], v[16:19], v[200:203]\n\t" "s_waitcnt lgkmcnt(6)\n\t" \
    "v_mfma_f32_16x16x32_bf16 v[252:255], v[180:183], v[20:23], v[252:255]\n\t" "s_waitcnt lgkmcnt(5)\n\t" \
    "v_mfma_f32_16x16x32_bf16 v[252:255], v[184:187], v[24:27], v[252:255]\n\t" "s_waitcnt lgkmcnt(4)\n\t" \
    "v_mfma_f32_16x16x32_bf16 v[252:255], v[188:191], v[28:31], v[252:255]\n\t" "s_waitcnt lgkmcnt(3)\n\t" \
    "v_mfma_f32_16x16x32_bf16 v[244:247], v[224:227], v[16:19], v[204:207]\n\t" "s_waitcnt lgkmcnt(2)\n\t" \
    "v_mfma_f32_16x16x32_bf16 v[244:247], v[228:231], v[20:23], v[244:247]\n\t" "s_waitcnt lgkmcnt(1)\n\t" \
    "v_mfma_f32_16x16x32_bf16 v[244:247], v[232:235], v[24:27], v[244:247]\n\t" "s_waitcnt lgkmcnt(0)\n\t" \
    "v_mfma_f32_16x16x32_bf16 v[244:247], v[236:239], v[28:31], v[244:247]\n\t" "v_mov_b32 v192, -1\n\t" "v_mov_b32 v193, -1\n\t" "v_mov_b32 v194, -1\n\t" \
    "v_mov_b32 v195, -1\n\t" "v_max3_f32 v242, v248, v249, v250\n\t" "v_max3_f32 v242, v242, v251, v252\n\t" "v_max3_f32 v242, v242, v253, v254\n\t" \
    "v_max_f32 v242, v242, v255\n\t" "v_max3_f32 v242, v242, v244, v245\n\t" "v_max3_f32 v242, v242, v246, v247\n\t" "v_mov_b32 v243, v242\n\t" "s_nop 1\n\t" \
    "v_permlane32_swap_b32 v242, v243\n\t" "v_max_f32 v242, v242, v243\n\t" "v_mov_b32 v243, v242\n\t" "s_nop 1\n\t" "v_permlane16_swap_b32 v242, v243\n\t" \
    "v_max_f32 v242, v242, v243\n\t" "v_pk_add_f32 v[244:245], v[244:245], v[242:243] op_sel_hi:[1,0] neg_lo:[0,1] neg_hi:[0,1]\n\t" \
    "v_pk_add_f32 v[246:247], v[246:247], v[242:243] op_sel_hi:[1,0] neg_lo:[0,1] neg_hi:[0,1]\n\t" \
    "v_pk_add_f32 v[248:249], v[248:249], v[242:243] op_sel_hi:[1,0] neg_lo:[0,1] neg_hi:[0,1]\n\t" \
    "v_pk_add_f32 v[250:251], v[250:251], v[242:243] op_sel_hi:[1,0] neg_lo:[0,1] neg_hi:[0,1]\n\t" \
    "v_pk_add_f32 v[252:253], v[252:253], v[242:243] op_sel_hi:[1,0] neg_lo:[0,1] neg_hi:[0,1]\n\t" \
    "v_pk_add_f32 v[254:255], v[254:255], v[242:243] op_sel_hi:[1,0] neg_lo:[0,1] neg_hi:[0,1]\n\t" "v_pk_mul_f32 v[244:245], v[244:245], s[66:67]\n\t" \
    "v_pk_mul_f32 v[246:247], v[246:247], s[66:67]\n\t" "v_pk_mul_f32 v[248:249], v[248:249], s[66:67]\n\t" "v_pk_mul_f32 v[250:251], v[250:251], s[66:67]\n\t" \
    "v_pk_mul_f32 v[252:253], v[252:253], s[66:67]\n\t" "v_pk_mul_f32 v[254:255], v[254:255], s[66:67]\n\t" "v_exp_f32 v244, v244\n\t" "v_exp_f32 v245, v245\n\t" \
    "v_exp_f32 v246, v246\n\t" "v_exp_f32 v247, v247\n\t" "v_exp_f32 v248, v248\n\t" "v_exp_f32 v249, v249\n\t" "v_exp_f32 v250, v250\n\t" "v_exp_f32 v251, v251\n\t" \
    "v_exp_f32 v252, v252\n\t" "v_exp_f32 v253, v253\n\t" "v_exp_f32 v254, v254\n\t" "v_exp_f32 v255, v255\n\t" "v_pk_add_f32 v[242:243], v[244:245], v[246:247]\n\t" \
    "v_pk_add_f32 v[242:243], v[242:243], v[248:249]\n\t" "v_pk_add_f32 v[242:243], v[242:243], v[250:251]\n\t" "v_pk_add_f32 v[242:243], v[242:243], v[252:253]\n\t" \
    "v_pk_add_f32 v[242:243], v[242:243], v[254:255]\n\t" "v_add_f32 v242, v242, v243\n\t" "v_mov_b32 v243, v242\n\t" "s_nop 1\n\t" "v_permlane32_swap_b32 v242, v243\n\t" \
    "v_add_f32 v242, v242, v243\n\t" "v_mov_b32 v243, v242\n\t" "s_nop 1\n\t" "v_permlane16_swap_b32 v242, v243\n\t" "v_add_f32 v242, v242, v243\n\t" \
    "v_rcp_f32 v242, v242\n\t" "s_nop 0\n\t" "v_pk_mul_f32 v[244:245], v[244:245], v[242:243] op_sel_hi:[1,0]\n\t" \
    "v_pk_mul_f32 v[246:247], v[246:247], v[242:243] op_sel_hi:[1,0]\n\t" "v_pk_mul_f32 v[248:249], v[248:249], v[242:243] op_sel_hi:[1,0]\n\t" \
    "v_pk_mul_f32 v[250:251], v[250:251], v[242:243] op_sel_hi:[1,0]\n\t" "v_pk_mul_f32 v[252:253], v[252:253], v[242:243] op_sel_hi:[1,0]\n\t" \
    "v_pk_mul_f32 v[254:255], v[254:255], v[242:243] op_sel_hi:[1,0]\n\t" "v_cvt_pk_bf16_f32 v162, v244, v245\n\t" "v_cvt_pk_bf16_f32 v163, v246, v247\n\t" \
    "v_cvt_pk_bf16_f32 v164, v248, v249\n\t" "v_cvt_pk_bf16_f32 v165, v250, v251\n\t" "v_cvt_pk_bf16_f32 v166, v252, v253\n\t" "v_cvt_pk_bf16_f32 v167, v254, v255\n\t" \
    "buffer_store_dwordx4 v[164:167], %[px], %[rs], %[tb] offen sc1\n\t" "buffer_store_dwordx2 v[162:163], %[pm], %[rs], %[tb] offen sc1\n\t" \
    "buffer_store_dwordx4 v[192:195], %[px], %[rs], %[tr] offen sc1\n\t" "buffer_store_dwordx2 v[192:193], %[pm], %[rs], %[tr] offen sc1\n\t" "s_waitcnt vmcnt(4)\n\t" \
    "v_readfirstlane_b32 s62, v240\n\t" "s_nop 3\n\t" "s_max_u32 %[sq], %[sq], s62\n\t" XT(96, 97) "s_branch 99f\n\t" "31:\n\t" "s_cmp_eq_u32 %[bd], 0\n\t" \
    "s_cbranch_scc1 36f\n\t" "s_sub_u32 %[bd], %[bd], 1\n\t" XCNT(100) "s_waitcnt lgkmcnt(0)\n\t" "s_nop 7\n\t" "s_branch 30b\n\t" "36:\n\t" "s_cmp_lg_u32 s60, 0\n\t" \
    "s_branch 32b\n\t" "34:\n\t" "s_cmp_eq_u32 %[bd], 0\n\t" "s_cbranch_scc1 35b\n\t" "s_sub_u32 %[bd], %[bd], 1\n\t" XCNT(101) "s_mov_b32 s60, 1\n\t" \
    "s_waitcnt lgkmcnt(0)\n\t" "s_nop 7\n\t" "s_branch 30b\n\t" "99:\n\t"
#define QK_STEP3 \
    "v_xor_b32 v216, 0x400, %[mo]\n\t" "s_mov_b32 s60, 0\n\t" XT(80, 81) "s_getpc_b64 s[62:63]\n\t" "48:\n\t" "s_memtime s[64:65]\n\t" "s_waitcnt lgkmcnt(0)\n\t" \
    "s_sub_u32 s61, %[tg], s64\n\t" "s_cmp_gt_i32 s61, 124\n\t" "s_cbranch_scc1 48b\n\t" "s_max_i32 s61, s61, 0\n\t" "s_and_b32 s61, s61, 124\n\t" \
    "s_sub_u32 s61, 49f-48b, s61\n\t" "s_add_u32 s62, s62, s61\n\t" "s_addc_u32 s63, s63, 0\n\t" "s_setpc_b64 s[62:63]\n\t" ".rept 31\n\t" "s_nop 0\n\t" ".endr\n\t" "49:\n\t" \
    XT(98, 99) "s_barrier\n\t" XT(82, 83) "40:\n\t" "buffer_load_dwordx4 v[240:243], %[mo], %[rs], %[sb] offen offset:0 sc1\n\t" "v_mov_b64 v[224:225], 0\n\t" \
    "v_mov_b64 v[226:227], 0\n\t" "v_mov_b64 v[228:229], 0\n\t" "v_mov_b64 v[230:231], 0\n\t" "v_mov_b64 v[232:233], 0\n\t" "v_mov_b64 v[234:235], 0\n\t" \
    "v_mov_b64 v[236:237], 0\n\t" "v_mov_b64 v[238:239], 0\n\t" "buffer_load_dwordx4 v[244:247], v216, %[rs], %[sb] offen offset:0 sc1\n\t" \
    "ds_read_b128 v[160:163], %[l0] offset:0\n\t" "ds_read_b128 v[164:167], %[l0] offset:1024\n\t" "ds_read_b128 v[168:171], %[l0] offset:2048\n\t" \
    "ds_read_b128 v[172:175], %[l0] offset:3072\n\t" "s_nop 3\n\t" "buffer_load_dwordx4 v[248:251], %[mo], %[rs], %[sb] offen offset:2048 sc1\n\t" \
    "ds_read_b128 v[176:179], %[l0] offset:4096\n\t" "ds_read_b128 v[180:183], %[l0] offset:5120\n\t" "ds_read_b128 v[184:187], %[l0] offset:6144\n\t" \
    "ds_read_b128 v[188:191], %[l0] offset:7168\n\t" "s_nop 3\n\t" "buffer_load_dwordx4 v[252:255], v216, %[rs], %[sb] offen offset:2048 sc1\n\t" "s_waitcnt vmcnt(3)\n\t" \
    XT(84, 85) "v_smfmac_f32_16x16x64_bf16 v[224:227], v[240:243], a[0:7], %[ix]\n\t" "ds_write_b128 %[l1], v[240:243] offset:12288\n\t" \
    "v_smfmac_f32_16x16x64_bf16 v[228:231], v[240:243], a[8:15], %[ix]\n\t" "v_max3_u32 v217, v240, v241, v242\n\t" \
    "v_smfmac_f32_16x16x64_bf16 v[232:235], v[240:243], v[32:39], %[ix]\n\t" "s_waitcnt lgkmcnt(7)\n\t" \
    "v_smfmac_f32_16x16x64_bf16 v[236:239], v[240:243], v[160:167], %[ix]\n\t" "ds_read_b128 v[160:163], %[l0] offset:8192\n\t" \
    "ds_read_b128 v[164:167], %[l0] offset:9216\n\t" "s_waitcnt vmcnt(2)\n\t" "v_smfmac_f32_16x16x64_bf16 v[224:227], v[244:247], a[16:23], %[ix]\n\t" \
    "ds_write_b128 %[l1], v[244:247] offset:13312\n\t" "v_max3_u32 v217, v217, v243, v244\n\t" "v_smfmac_f32_16x16x64_bf16 v[228:231], v[244:247], a[24:31], %[ix]\n\t" \
    "v_max3_u32 v217, v217, v245, v246\n\t" "v_max_u32 v217, v217, v247\n\t" "v_smfmac_f32_16x16x64_bf16 v[232:235], v[244:247], v[40:47], %[ix]\n\t" \
    "v_cmp_eq_u32 vcc, -1, v217\n\t" "s_cmp_lg_u32 s60, 0\n\t" "s_waitcnt lgkmcnt(8)\n\t" "v_smfmac_f32_16x16x64_bf16 v[236:239], v[244:247], v[168:175], %[ix]\n\t" \
    "s_cbranch_vccnz 41f\n\t" "42:\n\t" "ds_read_b128 v[168:171], %[l0] offset:10240\n\t" "s_waitcnt vmcnt(1)\n\t" \
    "v_smfmac_f32_16x16x64_bf16 v[224:227], v[248:251], a[32:39], %[ix]\n\t" "ds_write_b128 %[l1], v[248:251] offset:14336\n\t" "s_waitcnt lgkmcnt(2)\n\t" \
    "v_smfmac_f32_16x16x64_bf16 v[228:231], v[248:251], a[40:47], %[ix]\n\t" "s_cbranch_scc1 43f\n\t" "s_barrier\n\t" "43:\n\t" XT(86, 87) \
    "v_smfmac_f32_16x16x64_bf16 v[232:235], v[248:251], v[48:55], %[ix]\n\t" "ds_read_b128 v[192:195], %[l1] offset:0\n\t" "ds_read_b128 v[196:199], %[l1] offset:4096\n\t" \
    "v_smfmac_f32_16x16x64_bf16 v[236:239], v[248:251], v[176:183], %[ix]\n\t" "ds_read_b128 v[172:175], %[l0] offset:11264\n\t" \
    "ds_read_b128 v[176:179], %[l0] offset:12288\n\t" "s_waitcnt vmcnt(0)\n\t" "v_smfmac_f32_16x16x64_bf16 v[224:227], v[252:255], a[48:55], %[ix]\n\t" \
    "ds_write_b128 %[l1], v[252:255] offset:15360\n\t" "v_max3_u32 v217, v248, v249, v250\n\t" "v_smfmac_f32_16x16x64_bf16 v[228:231], v[252:255], a[56:63], %[ix]\n\t" \
    "ds_read_b128 v[180:183], %[l0] offset:13312\n\t" "v_max3_u32 v217, v217, v251, v252\n\t" "v_max3_u32 v217, v217, v253, v254\n\t" \
    "v_smfmac_f32_16x16x64_bf16 v[232:235], v[252:255], v[56:63], %[ix]\n\t" "v_max_u32 v217, v217, v255\n\t" "v_cmp_eq_u32 vcc, -1, v217\n\t" \
    "v_smfmac_f32_16x16x64_bf16 v[236:239], v[252:255], v[184:191], %[ix]\n\t" "s_cbranch_vccnz 44f\n\t" "45:\n\t" "ds_read_b128 v[240:243], %[l1] offset:8192\n\t" \
    "s_waitcnt lgkmcnt(6)\n\t" "v_smfmac_f32_16x16x64_bf16 v[224:227], v[192:195], a[64:71], %[ix]\n\t" "ds_read_b128 v[184:187], %[l0] offset:14336\n\t" \
    "ds_read_b128 v[188:191], %[l0] offset:15360\n\t" "v_smfmac_f32_16x16x64_bf16 v[228:231], v[192:195], a[72:79], %[ix]\n\t" \
    "ds_read_b128 v[244:247], %[l1] offset:1024\n\t" "s_waitcnt lgkmcnt(5)\n\t" "v_smfmac_f32_16x16x64_bf16 v[232:235], v[192:195], v[64:71], %[ix]\n\t" "s_barrier\n\t" \
    XT(88, 89) "s_memtime s[64:65]\n\t" "v_smfmac_f32_16x16x64_bf16 v[236:239], v[192:195], v[160:167], %[ix]\n\t" "ds_read_b128 v[160:163], %[l0] offset:16384\n\t" \
    "ds_read_b128 v[164:167], %[l0] offset:17408\n\t" "v_smfmac_f32_16x16x64_bf16 v[224:227], v[196:199], a[80:87], %[ix]\n\t" \
    "ds_read_b128 v[248:251], %[l1] offset:5120\n\t" "ds_read_b128 v[252:255], %[l1] offset:9216\n\t" "v_smfmac_f32_16x16x64_bf16 v[228:231], v[196:199], a[88:95], %[ix]\n\t" \
    "ds_read_b128 v[192:195], %[l1] offset:2048\n\t" "v_smfmac_f32_16x16x64_bf16 v[232:235], v[196:199], v[72:79], %[ix]\n\t" "s_sub_u32 s62, s64, %[ca]\n\t" \
    "s_mul_i32 s62, s62, %[k1]\n\t" "v_smfmac_f32_16x16x64_bf16 v[236:239], v[196:199], v[168:175], %[ix]\n\t" "s_lshr_b32 s62, s62, 16\n\t" "s_add_u32 s62, s62, %[ga]\n\t" \
    "s_waitcnt lgkmcnt(8)\n\t" "v_smfmac_f32_16x16x64_bf16 v[224:227], v[240:243], a[96:103], %[ix]\n\t" "s_bitset0_b32 s62, 0\n\t" "v_mov_b32 v218, s62\n\t" \
    "v_smfmac_f32_16x16x64_bf16 v[228:231], v[240:243], a[104:111], %[ix]\n\t" "buffer_store_dword v218, %[hq], %[rs], %[pb] offen sc1\n\t" \
    "ds_read_b128 v[168:171], %[l0] offset:18432\n\t" "v_smfmac_f32_16x16x64_bf16 v[232:235], v[240:243], v[80:87], %[ix]\n\t" \
    "ds_read_b128 v[172:175], %[l0] offset:19456\n\t" "ds_read_b128 v[196:199], %[l1] offset:6144\n\t" \
    "v_smfmac_f32_16x16x64_bf16 v[236:239], v[240:243], v[176:183], %[ix]\n\t" "ds_read_b128 v[176:179], %[l0] offset:20480\n\t" \
    "ds_read_b128 v[180:183], %[l0] offset:21504\n\t" "s_waitcnt lgkmcnt(10)\n\t" "v_smfmac_f32_16x16x64_bf16 v[224:227], v[244:247], a[112:119], %[ix]\n\t" \
    "ds_read_b128 v[240:243], %[l1] offset:10240\n\t" "v_smfmac_f32_16x16x64_bf16 v[228:231], v[244:247], a[120:127], %[ix]\n\t" \
    "v_smfmac_f32_16x16x64_bf16 v[232:235], v[244:247], v[88:95], %[ix]\n\t" "v_smfmac_f32_16x16x64_bf16 v[236:239], v[244:247], v[184:191], %[ix]\n\t" \
    "ds_read_b128 v[184:187], %[l0] offset:22528\n\t" "ds_read_b128 v[188:191], %[l0] offset:23552\n\t" "s_waitcnt lgkmcnt(10)\n\t" \
    "v_smfmac_f32_16x16x64_bf16 v[224:227], v[248:251], a[128:135], %[ix]\n\t" "ds_read_b128 v[244:247], %[l1] offset:3072\n\t" \
    "v_smfmac_f32_16x16x64_bf16 v[228:231], v[248:251], a[136:143], %[ix]\n\t" "v_smfmac_f32_16x16x64_bf16 v[232:235], v[248:251], v[96:103], %[ix]\n\t" \
    "v_smfmac_f32_16x16x64_bf16 v[236:239], v[248:251], v[160:167], %[ix]\n\t" "ds_read_b128 v[160:163], %[l0] offset:24576\n\t" \
    "ds_read_b128 v[164:167], %[l0] offset:25600\n\t" "s_waitcnt lgkmcnt(12)\n\t" "v_smfmac_f32_16x16x64_bf16 v[224:227], v[252:255], a[144:151], %[ix]\n\t" \
    "ds_read_b128 v[248:251], %[l1] offset:7168\n\t" "v_smfmac_f32_16x16x64_bf16 v[228:231], v[252:255], a[152:159], %[ix]\n\t" \
    "v_smfmac_f32_16x16x64_bf16 v[232:235], v[252:255], v[104:111], %[ix]\n\t" "s_waitcnt lgkmcnt(10)\n\t" \
    "v_smfmac_f32_16x16x64_bf16 v[236:239], v[252:255], v[168:175], %[ix]\n\t" "ds_read_b128 v[168:171], %[l0] offset:26624\n\t" \
    "ds_read_b128 v[172:175], %[l0] offset:27648\n\t" "v_smfmac_f32_16x16x64_bf16 v[224:227], v[192:195], a[160:167], %[ix]\n\t" \
    "ds_read_b128 v[252:255], %[l1] offset:11264\n\t" "v_smfmac_f32_16x16x64_bf16 v[228:231], v[192:195], a[168:175], %[ix]\n\t" "s_waitcnt lgkmcnt(12)\n\t" \
    "v_smfmac_f32_16x16x64_bf16 v[224:227], v[196:199], a[176:183], %[ix]\n\t" "v_smfmac_f32_16x16x64_bf16 v[228:231], v[196:199], a[184:191], %[ix]\n\t" \
    "s_waitcnt lgkmcnt(9)\n\t" "v_smfmac_f32_16x16x64_bf16 v[224:227], v[240:243], a[192:199], %[ix]\n\t" \
    "v_smfmac_f32_16x16x64_bf16 v[228:231], v[240:243], a[200:207], %[ix]\n\t" "s_waitcnt lgkmcnt(6)\n\t" \
    "v_smfmac_f32_16x16x64_bf16 v[224:227], v[244:247], a[208:215], %[ix]\n\t" "v_smfmac_f32_16x16x64_bf16 v[228:231], v[244:247], a[216:223], %[ix]\n\t" \
    "s_waitcnt lgkmcnt(3)\n\t" "v_smfmac_f32_16x16x64_bf16 v[224:227], v[248:251], a[224:231], %[ix]\n\t" \
    "v_smfmac_f32_16x16x64_bf16 v[228:231], v[248:251], a[232:239], %[ix]\n\t" "s_waitcnt lgkmcnt(0)\n\t" \
    "v_smfmac_f32_16x16x64_bf16 v[224:227], v[252:255], a[240:247], %[ix]\n\t" "s_nop 1\n\t" "v_smfmac_f32_16x16x64_bf16 v[228:231], v[252:255], a[248:255], %[ix]\n\t" \
    XT(90, 91) "s_nop 1\n\t" "v_smfmac_f32_16x16x64_bf16 v[232:235], v[192:195], v[112:119], %[ix]\n\t" "s_nop 1\n\t" \
    "v_smfmac_f32_16x16x64_bf16 v[236:239], v[192:195], v[176:183], %[ix]\n\t" "ds_read_b128 v[176:179], %[l0] offset:28672\n\t" "v_add_f32 v224, v224, v225\n\t" \
    "v_add_f32 v226, v226, v227\n\t" "v_smfmac_f32_16x16x64_bf16 v[232:235], v[196:199], v[120:127], %[ix]\n\t" "ds_read_b128 v[180:183], %[l0] offset:29696\n\t" \
    "v_add_f32 v228, v228, v229\n\t" "v_add_f32 v230, v230, v231\n\t" "v_smfmac_f32_16x16x64_bf16 v[236:239], v[196:199], v[184:191], %[ix]\n\t" \
    "ds_read_b128 v[184:187], %[l0] offset:30720\n\t" "v_cvt_pk_bf16_f32 v218, v224, v228\n\t" "v_cvt_pk_bf16_f32 v219, v226, v230\n\t" \
    "v_smfmac_f32_16x16x64_bf16 v[232:235], v[240:243], v[128:135], %[ix]\n\t" "ds_write2_b32 %[qw], v218, v219 offset1:8\n\t" \
    "ds_read_b128 v[188:191], %[l0] offset:31744\n\t" "v_smfmac_f32_16x16x64_bf16 v[236:239], v[240:243], v[160:167], %[ix]\n\t" \
    "v_smfmac_f32_16x16x64_bf16 v[232:235], v[244:247], v[136:143], %[ix]\n\t" "v_smfmac_f32_16x16x64_bf16 v[236:239], v[244:247], v[168:175], %[ix]\n\t" \
    "s_waitcnt lgkmcnt(1)\n\t" "s_barrier\n\t" "v_smfmac_f32_16x16x64_bf16 v[232:235], v[248:251], v[144:151], %[ix]\n\t" \
    "v_smfmac_f32_16x16x64_bf16 v[236:239], v[248:251], v[176:183], %[ix]\n\t" "v_smfmac_f32_16x16x64_bf16 v[232:235], v[252:255], v[152:159], %[ix]\n\t" \
    "s_waitcnt lgkmcnt(0)\n\t" "v_smfmac_f32_16x16x64_bf16 v[236:239], v[252:255], v[184:191], %[ix]\n\t" XT(92, 93) "s_nop 6\n\t" "v_add_f32 v232, v232, v233\n\t" \
    "v_add_f32 v234, v234, v235\n\t" "v_add_f32 v236, v236, v237\n\t" "v_add_f32 v238, v238, v239\n\t" "v_cvt_pk_bf16_f32 v232, v232, v236\n\t" \
    "v_cvt_pk_bf16_f32 v233, v234, v238\n\t" "ds_write2_b32 %[kw], v232, v233 offset1:68\n\t" "s_waitcnt lgkmcnt(0)\n\t" "s_barrier\n\t" XT(94, 95) XT(96, 97) \
    "s_branch 99f\n\t" "41:\n\t" "s_cmp_eq_u32 %[bd], 0\n\t" "s_cbranch_scc1 46f\n\t" "s_sub_u32 %[bd], %[bd], 1\n\t" XCNT(100) "s_waitcnt lgkmcnt(0)\n\t" "s_nop 7\n\t" \
    "s_branch 40b\n\t" "46:\n\t" "s_cmp_lg_u32 s60, 0\n\t" "s_branch 42b\n\t" "44:\n\t" "s_cmp_eq_u32 %[bd], 0\n\t" "s_cbranch_scc1 45b\n\t" "s_sub_u32 %[bd], %[bd], 1\n\t" \
    XCNT(101) "s_mov_b32 s60, 1\n\t" "s_waitcnt lgkmcnt(0)\n\t" "s_nop 7\n\t" "s_branch 40b\n\t" "99:\n\t"
#define VO_STEP \
    "v_xor_b32 v224, 0x400, %[mo]\n\t" "s_mov_b32 s60, 0\n\t" "s_cmp_eq_u32 %[w], 1\n\t" "s_cbranch_scc1 91f\n\t" "s_cmp_eq_u32 %[w], 2\n\t" "s_cbranch_scc1 92f\n\t" \
    "s_cmp_eq_u32 %[w], 3\n\t" "s_cbranch_scc1 93f\n\t" XT(80, 81) "s_getpc_b64 s[62:63]\n\t" "18:\n\t" "s_memtime s[64:65]\n\t" "s_waitcnt lgkmcnt(0)\n\t" \
    "s_sub_u32 s61, %[tg], s64\n\t" "s_cmp_gt_i32 s61, 124\n\t" "s_cbranch_scc1 18b\n\t" "s_max_i32 s61, s61, 0\n\t" "s_and_b32 s61, s61, 124\n\t" \
    "s_sub_u32 s61, 19f-18b, s61\n\t" "s_add_u32 s62, s62, s61\n\t" "s_addc_u32 s63, s63, 0\n\t" "s_setpc_b64 s[62:63]\n\t" ".rept 31\n\t" "s_nop 0\n\t" ".endr\n\t" "19:\n\t" \
    "s_barrier\n\t" XT(82, 83) "10:\n\t" "buffer_load_dwordx4 v[240:243], %[mo], %[rs], %[sb] offen offset:0 sc1\n\t" "v_mov_b64 v[232:233], 0\n\t" \
    "v_mov_b64 v[234:235], 0\n\t" "v_mov_b64 v[236:237], 0\n\t" "v_mov_b64 v[238:239], 0\n\t" "v_mov_b64 v[192:193], 0\n\t" "v_mov_b64 v[194:195], 0\n\t" \
    "v_mov_b64 v[196:197], 0\n\t" "v_mov_b64 v[198:199], 0\n\t" "buffer_load_dwordx4 v[244:247], v224, %[rs], %[sb] offen offset:0 sc1\n\t" \
    "ds_read_b128 v[160:163], %[l0] offset:0\n\t" "ds_read_b128 v[164:167], %[l0] offset:1024\n\t" "ds_read_b128 v[168:171], %[l0] offset:2048\n\t" \
    "ds_read_b128 v[172:175], %[l0] offset:3072\n\t" "s_nop 3\n\t" "buffer_load_dwordx4 v[248:251], %[mo], %[rs], %[sb] offen offset:2048 sc1\n\t" \
    "ds_read_b128 v[176:179], %[l0] offset:4096\n\t" "ds_read_b128 v[180:183], %[l0] offset:5120\n\t" "ds_read_b128 v[184:187], %[l0] offset:6144\n\t" \
    "ds_read_b128 v[188:191], %[l0] offset:7168\n\t" "s_nop 3\n\t" "buffer_load_dwordx4 v[252:255], v224, %[rs], %[sb] offen offset:2048 sc1\n\t" \
    "v_mov_b64 v[200:201], 0\n\t" "v_mov_b64 v[202:203], 0\n\t" "v_mov_b64 v[204:205], 0\n\t" "v_mov_b64 v[206:207], 0\n\t" "v_mov_b64 v[208:209], 0\n\t" \
    "v_mov_b64 v[210:211], 0\n\t" "v_mov_b64 v[212:213], 0\n\t" "v_mov_b64 v[214:215], 0\n\t" "v_mov_b64 v[216:217], 0\n\t" "v_mov_b64 v[218:219], 0\n\t" \
    "v_mov_b64 v[220:221], 0\n\t" "v_mov_b64 v[222:223], 0\n\t" "s_waitcnt vmcnt(3)\n\t" XT(84, 85) "v_smfmac_f32_16x16x64_bf16 v[232:235], v[240:243], a[0:7], %[ix]\n\t" \
    "ds_write_b128 %[l1], v[240:243] offset:0\n\t" "v_smfmac_f32_16x16x64_bf16 v[236:239], v[240:243], a[8:15], %[ix]\n\t" "v_max3_u32 v225, v240, v241, v242\n\t" \
    "s_waitcnt vmcnt(2)\n\t" "v_smfmac_f32_16x16x64_bf16 v[232:235], v[244:247], a[16:23], %[ix]\n\t" "ds_write_b128 %[l1], v[244:247] offset:1024\n\t" \
    "v_max3_u32 v225, v225, v243, v244\n\t" "s_waitcnt lgkmcnt(8)\n\t" "v_smfmac_f32_16x16x64_bf16 v[236:239], v[244:247], v[160:167], %[ix]\n\t" \
    "v_max3_u32 v225, v225, v245, v246\n\t" "v_max_u32 v225, v225, v247\n\t" "v_cmp_eq_u32 vcc, -1, v225\n\t" "s_cmp_lg_u32 s60, 0\n\t" "s_cbranch_vccnz 11f\n\t" "12:\n\t" \
    "s_waitcnt lgkmcnt(0)\n\t" "s_cbranch_scc1 13f\n\t" "s_barrier\n\t" "13:\n\t" XT(86, 87) "ds_read_b128 v[16:19], %[l1] offset:4096\n\t" \
    "ds_read_b128 v[20:23], %[l1] offset:8192\n\t" "ds_read_b128 v[24:27], %[l1] offset:12288\n\t" "s_waitcnt vmcnt(1)\n\t" \
    "v_smfmac_f32_16x16x64_bf16 v[232:235], v[248:251], a[24:31], %[ix]\n\t" "ds_write_b128 %[l1], v[248:251] offset:2048\n\t" "v_max3_u32 v225, v248, v249, v250\n\t" \
    "v_smfmac_f32_16x16x64_bf16 v[236:239], v[248:251], a[32:39], %[ix]\n\t" "ds_read_b128 v[28:31], %[l1] offset:5120\n\t" "ds_read_b128 v[240:243], %[l1] offset:9216\n\t" \
    "ds_read_b128 v[244:247], %[l1] offset:13312\n\t" "s_waitcnt vmcnt(0)\n\t" "v_smfmac_f32_16x16x64_bf16 v[232:235], v[252:255], a[40:47], %[ix]\n\t" \
    "ds_write_b128 %[l1], v[252:255] offset:3072\n\t" "v_max3_u32 v225, v225, v251, v252\n\t" "v_smfmac_f32_16x16x64_bf16 v[236:239], v[252:255], v[168:175], %[ix]\n\t" \
    "v_max3_u32 v225, v225, v253, v254\n\t" "v_max_u32 v225, v225, v255\n\t" "s_waitcnt lgkmcnt(7)\n\t" "v_smfmac_f32_16x16x64_bf16 v[232:235], v[16:19], a[48:55], %[ix]\n\t" \
    "v_cmp_eq_u32 vcc, -1, v225\n\t" "s_cbranch_vccnz 14f\n\t" "15:\n\t" "v_smfmac_f32_16x16x64_bf16 v[236:239], v[16:19], a[56:63], %[ix]\n\t" \
    "ds_read_b128 v[160:163], %[l0] offset:8192\n\t" "ds_read_b128 v[164:167], %[l0] offset:9216\n\t" "s_waitcnt lgkmcnt(8)\n\t" \
    "v_smfmac_f32_16x16x64_bf16 v[232:235], v[20:23], a[64:71], %[ix]\n\t" "ds_read_b128 v[168:171], %[l0] offset:10240\n\t" "ds_read_b128 v[172:175], %[l0] offset:11264\n\t" \
    "v_smfmac_f32_16x16x64_bf16 v[236:239], v[20:23], v[176:183], %[ix]\n\t" "s_waitcnt lgkmcnt(4)\n\t" "s_barrier\n\t" XT(88, 89) "s_memtime s[64:65]\n\t" \
    "v_smfmac_f32_16x16x64_bf16 v[232:235], v[24:27], a[72:79], %[ix]\n\t" "ds_read_b128 v[176:179], %[l0] offset:12288\n\t" "ds_read_b128 v[180:183], %[l0] offset:13312\n\t" \
    "v_smfmac_f32_16x16x64_bf16 v[236:239], v[24:27], a[80:87], %[ix]\n\t" "ds_read_b128 v[248:251], %[l1] offset:6144\n\t" "ds_read_b128 v[252:255], %[l1] offset:10240\n\t" \
    "v_smfmac_f32_16x16x64_bf16 v[232:235], v[28:31], a[88:95], %[ix]\n\t" "ds_read_b128 v[16:19], %[l1] offset:14336\n\t" "ds_read_b128 v[20:23], %[l1] offset:7168\n\t" \
    "v_smfmac_f32_16x16x64_bf16 v[236:239], v[28:31], v[184:191], %[ix]\n\t" "ds_read_b128 v[24:27], %[l1] offset:11264\n\t" "ds_read_b128 v[184:187], %[l0] offset:14336\n\t" \
    "v_smfmac_f32_16x16x64_bf16 v[232:235], v[240:243], a[96:103], %[ix]\n\t" "ds_read_b128 v[188:191], %[l0] offset:15360\n\t" \
    "ds_read_b128 v[28:31], %[l1] offset:15360\n\t" "v_smfmac_f32_16x16x64_bf16 v[236:239], v[240:243], a[104:111], %[ix]\n\t" "s_sub_u32 s62, s64, %[ca]\n\t" \
    "s_mul_i32 s62, s62, %[k1]\n\t" "v_smfmac_f32_16x16x64_bf16 v[232:235], v[244:247], a[112:119], %[ix]\n\t" "s_lshr_b32 s62, s62, 16\n\t" "s_add_u32 s62, s62, %[ga]\n\t" \
    "s_waitcnt lgkmcnt(12)\n\t" "v_smfmac_f32_16x16x64_bf16 v[236:239], v[244:247], v[160:167], %[ix]\n\t" "s_bitset0_b32 s62, 0\n\t" "v_mov_b32 v226, s62\n\t" \
    "s_waitcnt lgkmcnt(7)\n\t" "v_smfmac_f32_16x16x64_bf16 v[232:235], v[248:251], a[120:127], %[ix]\n\t" "buffer_store_dword v226, %[hq], %[rs], %[pb] offen sc1\n\t" \
    "ds_read_b128 v[160:163], %[l0] offset:16384\n\t" "v_smfmac_f32_16x16x64_bf16 v[236:239], v[248:251], a[128:135], %[ix]\n\t" \
    "ds_read_b128 v[164:167], %[l0] offset:17408\n\t" "s_waitcnt lgkmcnt(8)\n\t" "v_smfmac_f32_16x16x64_bf16 v[232:235], v[252:255], a[136:143], %[ix]\n\t" \
    "v_smfmac_f32_16x16x64_bf16 v[236:239], v[252:255], v[168:175], %[ix]\n\t" "ds_read_b128 v[168:171], %[l0] offset:18432\n\t" \
    "ds_read_b128 v[172:175], %[l0] offset:19456\n\t" "s_waitcnt lgkmcnt(9)\n\t" "v_smfmac_f32_16x16x64_bf16 v[232:235], v[16:19], a[144:151], %[ix]\n\t" \
    "v_smfmac_f32_16x16x64_bf16 v[236:239], v[16:19], a[152:159], %[ix]\n\t" "s_waitcnt lgkmcnt(8)\n\t" \
    "v_smfmac_f32_16x16x64_bf16 v[232:235], v[20:23], a[160:167], %[ix]\n\t" "v_smfmac_f32_16x16x64_bf16 v[236:239], v[20:23], v[176:183], %[ix]\n\t" \
    "ds_read_b128 v[176:179], %[l0] offset:20480\n\t" "ds_read_b128 v[180:183], %[l0] offset:21504\n\t" "s_waitcnt lgkmcnt(9)\n\t" \
    "v_smfmac_f32_16x16x64_bf16 v[232:235], v[24:27], a[168:175], %[ix]\n\t" "v_smfmac_f32_16x16x64_bf16 v[236:239], v[24:27], a[176:183], %[ix]\n\t" \
    "s_waitcnt lgkmcnt(6)\n\t" "v_smfmac_f32_16x16x64_bf16 v[232:235], v[28:31], a[184:191], %[ix]\n\t" \
    "v_smfmac_f32_16x16x64_bf16 v[236:239], v[28:31], v[184:191], %[ix]\n\t" "ds_read_b128 v[184:187], %[l0] offset:22528\n\t" \
    "ds_read_b128 v[188:191], %[l0] offset:23552\n\t" XT(90, 91) "s_nop 4\n\t" "v_add_f32 v232, v232, v233\n\t" "v_add_f32 v234, v234, v235\n\t" \
    "v_add_f32 v236, v236, v237\n\t" "v_add_f32 v238, v238, v239\n\t" "v_cvt_pk_bf16_f32 v232, v232, v236\n\t" "v_cvt_pk_bf16_f32 v233, v234, v238\n\t" \
    "ds_write2_b32 %[vw], v232, v233 offset1:68\n\t" "s_waitcnt lgkmcnt(0)\n\t" "s_barrier\n\t" XT(92, 93) "ds_read_b128 v[240:243], %[vr] offset:0\n\t" \
    "ds_read_b128 v[244:247], %[vr] offset:64\n\t" "ds_read_b128 v[248:251], %[vr] offset:128\n\t" "ds_read_b128 v[252:255], %[vr] offset:192\n\t" "s_mov_b32 s61, %[ub]\n\t" \
    "s_waitcnt lgkmcnt(3)\n\t" "v_smfmac_f32_16x16x64_bf16 v[192:195], v[240:243], v[32:39], %[ix]\n\t" \
    "v_smfmac_f32_16x16x64_bf16 v[196:199], v[240:243], v[48:55], %[ix]\n\t" "s_waitcnt lgkmcnt(2)\n\t" \
    "v_smfmac_f32_16x16x64_bf16 v[192:195], v[244:247], v[40:47], %[ix]\n\t" "v_smfmac_f32_16x16x64_bf16 v[196:199], v[244:247], v[56:63], %[ix]\n\t" \
    "s_waitcnt lgkmcnt(1)\n\t" "v_smfmac_f32_16x16x64_bf16 v[192:195], v[248:251], a[192:199], %[ix]\n\t" \
    "v_smfmac_f32_16x16x64_bf16 v[196:199], v[248:251], a[200:207], %[ix]\n\t" "s_waitcnt lgkmcnt(0)\n\t" \
    "v_smfmac_f32_16x16x64_bf16 v[192:195], v[252:255], v[160:167], %[ix]\n\t" "ds_read_b128 v[160:163], %[l0] offset:24576\n\t" \
    "ds_read_b128 v[164:167], %[l0] offset:25600\n\t" "v_smfmac_f32_16x16x64_bf16 v[196:199], v[252:255], v[168:175], %[ix]\n\t" \
    "ds_read_b128 v[168:171], %[l0] offset:26624\n\t" "ds_read_b128 v[172:175], %[l0] offset:27648\n\t" \
    "v_smfmac_f32_16x16x64_bf16 v[200:203], v[240:243], v[64:71], %[ix]\n\t" "s_nop 1\n\t" "v_smfmac_f32_16x16x64_bf16 v[204:207], v[240:243], v[80:87], %[ix]\n\t" \
    "v_cvt_pk_bf16_f32 v224, v192, v194\n\t" "v_cvt_pk_bf16_f32 v225, v193, v195\n\t" "v_smfmac_f32_16x16x64_bf16 v[200:203], v[244:247], v[72:79], %[ix]\n\t" \
    "v_cvt_pk_bf16_f32 v226, v196, v198\n\t" "v_cvt_pk_bf16_f32 v227, v197, v199\n\t" "v_smfmac_f32_16x16x64_bf16 v[204:207], v[244:247], v[88:95], %[ix]\n\t" "s_nop 0\n\t" \
    "v_permlane32_swap_b32 v224, v226\n\t" "v_smfmac_f32_16x16x64_bf16 v[200:203], v[248:251], a[208:215], %[ix]\n\t" "v_permlane32_swap_b32 v225, v227\n\t" "s_nop 0\n\t" \
    "v_smfmac_f32_16x16x64_bf16 v[204:207], v[248:251], a[216:223], %[ix]\n\t" "v_permlane16_swap_b32 v224, v225\n\t" "v_permlane16_swap_b32 v226, v227\n\t" \
    "v_smfmac_f32_16x16x64_bf16 v[200:203], v[252:255], v[176:183], %[ix]\n\t" "buffer_store_dwordx4 v[224:227], %[uo], %[rs], s61 offen sc1\n\t" \
    "s_add_u32 s61, s61, 0x10000\n\t" XT(94, 95) "v_smfmac_f32_16x16x64_bf16 v[204:207], v[252:255], v[184:191], %[ix]\n\t" "ds_read_b128 v[176:179], %[l0] offset:28672\n\t" \
    "ds_read_b128 v[180:183], %[l0] offset:29696\n\t" "v_smfmac_f32_16x16x64_bf16 v[208:211], v[240:243], v[96:103], %[ix]\n\t" \
    "ds_read_b128 v[184:187], %[l0] offset:30720\n\t" "ds_read_b128 v[188:191], %[l0] offset:31744\n\t" \
    "v_smfmac_f32_16x16x64_bf16 v[212:215], v[240:243], v[112:119], %[ix]\n\t" "v_cvt_pk_bf16_f32 v228, v200, v202\n\t" "v_cvt_pk_bf16_f32 v229, v201, v203\n\t" \
    "v_smfmac_f32_16x16x64_bf16 v[208:211], v[244:247], v[104:111], %[ix]\n\t" "v_cvt_pk_bf16_f32 v230, v204, v206\n\t" "v_cvt_pk_bf16_f32 v231, v205, v207\n\t" \
    "v_smfmac_f32_16x16x64_bf16 v[212:215], v[244:247], v[120:127], %[ix]\n\t" "s_nop 0\n\t" "v_permlane32_swap_b32 v228, v230\n\t" \
    "v_smfmac_f32_16x16x64_bf16 v[208:211], v[248:251], a[224:231], %[ix]\n\t" "v_permlane32_swap_b32 v229, v231\n\t" "s_nop 0\n\t" \
    "v_smfmac_f32_16x16x64_bf16 v[212:215], v[248:251], a[232:239], %[ix]\n\t" "v_permlane16_swap_b32 v228, v229\n\t" "v_permlane16_swap_b32 v230, v231\n\t" \
    "s_waitcnt lgkmcnt(6)\n\t" "v_smfmac_f32_16x16x64_bf16 v[208:211], v[252:255], v[160:167], %[ix]\n\t" "buffer_store_dwordx4 v[228:231], %[uo], %[rs], s61 offen sc1\n\t" \
    "s_add_u32 s61, s61, 0x10000\n\t" "s_waitcnt lgkmcnt(4)\n\t" "v_smfmac_f32_16x16x64_bf16 v[212:215], v[252:255], v[168:175], %[ix]\n\t" "s_nop 1\n\t" \
    "v_smfmac_f32_16x16x64_bf16 v[216:219], v[240:243], v[128:135], %[ix]\n\t" "s_nop 1\n\t" "v_smfmac_f32_16x16x64_bf16 v[220:223], v[240:243], v[144:151], %[ix]\n\t" \
    "v_cvt_pk_bf16_f32 v224, v208, v210\n\t" "v_cvt_pk_bf16_f32 v225, v209, v211\n\t" "v_smfmac_f32_16x16x64_bf16 v[216:219], v[244:247], v[136:143], %[ix]\n\t" \
    "v_cvt_pk_bf16_f32 v226, v212, v214\n\t" "v_cvt_pk_bf16_f32 v227, v213, v215\n\t" "v_smfmac_f32_16x16x64_bf16 v[220:223], v[244:247], v[152:159], %[ix]\n\t" "s_nop 0\n\t" \
    "v_permlane32_swap_b32 v224, v226\n\t" "v_smfmac_f32_16x16x64_bf16 v[216:219], v[248:251], a[240:247], %[ix]\n\t" "v_permlane32_swap_b32 v225, v227\n\t" "s_nop 0\n\t" \
    "v_smfmac_f32_16x16x64_bf16 v[220:223], v[248:251], a[248:255], %[ix]\n\t" "v_permlane16_swap_b32 v224, v225\n\t" "v_permlane16_swap_b32 v226, v227\n\t" \
    "s_waitcnt lgkmcnt(2)\n\t" "v_smfmac_f32_16x16x64_bf16 v[216:219], v[252:255], v[176:183], %[ix]\n\t" "buffer_store_dwordx4 v[224:227], %[uo], %[rs], s61 offen sc1\n\t" \
    "s_add_u32 s61, s61, 0x10000\n\t" "s_waitcnt lgkmcnt(0)\n\t" "v_smfmac_f32_16x16x64_bf16 v[220:223], v[252:255], v[184:191], %[ix]\n\t" "s_nop 5\n\t" \
    "v_cvt_pk_bf16_f32 v228, v216, v218\n\t" "v_cvt_pk_bf16_f32 v229, v217, v219\n\t" "v_cvt_pk_bf16_f32 v230, v220, v222\n\t" "v_cvt_pk_bf16_f32 v231, v221, v223\n\t" \
    "s_nop 0\n\t" "v_permlane32_swap_b32 v228, v230\n\t" "v_permlane32_swap_b32 v229, v231\n\t" "s_nop 1\n\t" "v_permlane16_swap_b32 v228, v229\n\t" \
    "v_permlane16_swap_b32 v230, v231\n\t" "buffer_store_dwordx4 v[228:231], %[uo], %[rs], s61 offen sc1\n\t" "s_nop 1\n\t" XT(96, 97) "s_branch 99f\n\t" "11:\n\t" \
    "s_cmp_eq_u32 %[bd], 0\n\t" "s_cbranch_scc1 16f\n\t" "s_sub_u32 %[bd], %[bd], 1\n\t" XCNT(100) "s_waitcnt lgkmcnt(0)\n\t" "s_nop 7\n\t" "s_branch 10b\n\t" "16:\n\t" \
    "s_cmp_lg_u32 s60, 0\n\t" "s_branch 12b\n\t" "14:\n\t" "s_cmp_eq_u32 %[bd], 0\n\t" "s_cbranch_scc1 15b\n\t" "s_sub_u32 %[bd], %[bd], 1\n\t" XCNT(101) \
    "s_mov_b32 s60, 1\n\t" "s_waitcnt lgkmcnt(0)\n\t" "s_nop 7\n\t" "s_branch 10b\n\t" "91:\n\t" XT(80, 81) "s_barrier\n\t" XT(82, 83) "20:\n\t" \
    "buffer_load_dwordx4 v[240:243], %[mo], %[rs], %[sb] offen offset:0 sc1\n\t" "v_mov_b64 v[232:233], 0\n\t" "v_mov_b64 v[234:235], 0\n\t" "v_mov_b64 v[236:237], 0\n\t" \
    "v_mov_b64 v[238:239], 0\n\t" "v_mov_b64 v[192:193], 0\n\t" "v_mov_b64 v[194:195], 0\n\t" "v_mov_b64 v[196:197], 0\n\t" "v_mov_b64 v[198:199], 0\n\t" \
    "buffer_load_dwordx4 v[244:247], v224, %[rs], %[sb] offen offset:0 sc1\n\t" "ds_read_b128 v[160:163], %[l0] offset:0\n\t" "ds_read_b128 v[164:167], %[l0] offset:1024\n\t" \
    "ds_read_b128 v[168:171], %[l0] offset:2048\n\t" "ds_read_b128 v[172:175], %[l0] offset:3072\n\t" "s_nop 3\n\t" \
    "buffer_load_dwordx4 v[248:251], %[mo], %[rs], %[sb] offen offset:2048 sc1\n\t" "ds_read_b128 v[176:179], %[l0] offset:4096\n\t" \
    "ds_read_b128 v[180:183], %[l0] offset:5120\n\t" "ds_read_b128 v[184:187], %[l0] offset:6144\n\t" "ds_read_b128 v[188:191], %[l0] offset:7168\n\t" "s_nop 3\n\t" \
    "buffer_load_dwordx4 v[252:255], v224, %[rs], %[sb] offen offset:2048 sc1\n\t" "v_mov_b64 v[200:201], 0\n\t" "v_mov_b64 v[202:203], 0\n\t" "v_mov_b64 v[204:205], 0\n\t" \
    "v_mov_b64 v[206:207], 0\n\t" "v_mov_b64 v[208:209], 0\n\t" "v_mov_b64 v[210:211], 0\n\t" "v_mov_b64 v[212:213], 0\n\t" "v_mov_b64 v[214:215], 0\n\t" \
    "v_mov_b64 v[216:217], 0\n\t" "v_mov_b64 v[218:219], 0\n\t" "v_mov_b64 v[220:221], 0\n\t" "v_mov_b64 v[222:223], 0\n\t" "s_waitcnt vmcnt(3)\n\t" XT(84, 85) \
    "v_smfmac_f32_16x16x64_bf16 v[232:235], v[240:243], a[0:7], %[ix]\n\t" "ds_write_b128 %[l1], v[240:243] offset:4096\n\t" \
    "v_smfmac_f32_16x16x64_bf16 v[236:239], v[240:243], a[8:15], %[ix]\n\t" "v_max3_u32 v225, v240, v241, v242\n\t" "s_waitcnt vmcnt(2)\n\t" \
    "v_smfmac_f32_16x16x64_bf16 v[232:235], v[244:247], a[16:23], %[ix]\n\t" "ds_write_b128 %[l1], v[244:247] offset:5120\n\t" "v_max3_u32 v225, v225, v243, v244\n\t" \
    "s_waitcnt lgkmcnt(8)\n\t" "v_smfmac_f32_16x16x64_bf16 v[236:239], v[244:247], v[160:167], %[ix]\n\t" "v_max3_u32 v225, v225, v245, v246\n\t" \
    "v_max_u32 v225, v225, v247\n\t" "v_cmp_eq_u32 vcc, -1, v225\n\t" "s_cmp_lg_u32 s60, 0\n\t" "s_cbranch_vccnz 21f\n\t" "22:\n\t" "s_waitcnt lgkmcnt(0)\n\t" \
    "s_cbranch_scc1 23f\n\t" "s_barrier\n\t" "23:\n\t" XT(86, 87) "ds_read_b128 v[16:19], %[l1] offset:8192\n\t" "ds_read_b128 v[20:23], %[l1] offset:12288\n\t" \
    "ds_read_b128 v[24:27], %[l1] offset:0\n\t" "s_waitcnt vmcnt(1)\n\t" "v_smfmac_f32_16x16x64_bf16 v[232:235], v[248:251], a[24:31], %[ix]\n\t" \
    "ds_write_b128 %[l1], v[248:251] offset:6144\n\t" "v_max3_u32 v225, v248, v249, v250\n\t" "v_smfmac_f32_16x16x64_bf16 v[236:239], v[248:251], a[32:39], %[ix]\n\t" \
    "ds_read_b128 v[28:31], %[l1] offset:9216\n\t" "ds_read_b128 v[240:243], %[l1] offset:13312\n\t" "ds_read_b128 v[244:247], %[l1] offset:1024\n\t" "s_waitcnt vmcnt(0)\n\t" \
    "v_smfmac_f32_16x16x64_bf16 v[232:235], v[252:255], a[40:47], %[ix]\n\t" "ds_write_b128 %[l1], v[252:255] offset:7168\n\t" "v_max3_u32 v225, v225, v251, v252\n\t" \
    "v_smfmac_f32_16x16x64_bf16 v[236:239], v[252:255], v[168:175], %[ix]\n\t" "v_max3_u32 v225, v225, v253, v254\n\t" "v_max_u32 v225, v225, v255\n\t" \
    "s_waitcnt lgkmcnt(7)\n\t" "v_smfmac_f32_16x16x64_bf16 v[232:235], v[16:19], a[48:55], %[ix]\n\t" "v_cmp_eq_u32 vcc, -1, v225\n\t" "s_cbranch_vccnz 24f\n\t" "25:\n\t" \
    "v_smfmac_f32_16x16x64_bf16 v[236:239], v[16:19], a[56:63], %[ix]\n\t" "ds_read_b128 v[160:163], %[l0] offset:8192\n\t" "ds_read_b128 v[164:167], %[l0] offset:9216\n\t" \
    "s_waitcnt lgkmcnt(8)\n\t" "v_smfmac_f32_16x16x64_bf16 v[232:235], v[20:23], a[64:71], %[ix]\n\t" "ds_read_b128 v[168:171], %[l0] offset:10240\n\t" \
    "ds_read_b128 v[172:175], %[l0] offset:11264\n\t" "v_smfmac_f32_16x16x64_bf16 v[236:239], v[20:23], v[176:183], %[ix]\n\t" "s_waitcnt lgkmcnt(4)\n\t" "s_barrier\n\t" \
    XT(88, 89) "v_smfmac_f32_16x16x64_bf16 v[232:235], v[24:27], a[72:79], %[ix]\n\t" "ds_read_b128 v[176:179], %[l0] offset:12288\n\t" \
    "ds_read_b128 v[180:183], %[l0] offset:13312\n\t" "v_smfmac_f32_16x16x64_bf16 v[236:239], v[24:27], a[80:87], %[ix]\n\t" "ds_read_b128 v[248:251], %[l1] offset:10240\n\t" \
    "ds_read_b128 v[252:255], %[l1] offset:14336\n\t" "v_smfmac_f32_16x16x64_bf16 v[232:235], v[28:31], a[88:95], %[ix]\n\t" "ds_read_b128 v[16:19], %[l1] offset:2048\n\t" \
    "ds_read_b128 v[20:23], %[l1] offset:11264\n\t" "v_smfmac_f32_16x16x64_bf16 v[236:239], v[28:31], v[184:191], %[ix]\n\t" "ds_read_b128 v[24:27], %[l1] offset:15360\n\t" \
    "ds_read_b128 v[184:187], %[l0] offset:14336\n\t" "v_smfmac_f32_16x16x64_bf16 v[232:235], v[240:243], a[96:103], %[ix]\n\t" \
    "ds_read_b128 v[188:191], %[l0] offset:15360\n\t" "ds_read_b128 v[28:31], %[l1] offset:3072\n\t" \
    "v_smfmac_f32_16x16x64_bf16 v[236:239], v[240:243], a[104:111], %[ix]\n\t" "v_smfmac_f32_16x16x64_bf16 v[232:235], v[244:247], a[112:119], %[ix]\n\t" \
    "s_waitcnt lgkmcnt(12)\n\t" "v_smfmac_f32_16x16x64_bf16 v[236:239], v[244:247], v[160:167], %[ix]\n\t" "ds_read_b128 v[160:163], %[l0] offset:16384\n\t" \
    "ds_read_b128 v[164:167], %[l0] offset:17408\n\t" "s_waitcnt lgkmcnt(9)\n\t" "v_smfmac_f32_16x16x64_bf16 v[232:235], v[248:251], a[120:127], %[ix]\n\t" \
    "v_smfmac_f32_16x16x64_bf16 v[236:239], v[248:251], a[128:135], %[ix]\n\t" "s_waitcnt lgkmcnt(8)\n\t" \
    "v_smfmac_f32_16x16x64_bf16 v[232:235], v[252:255], a[136:143], %[ix]\n\t" "v_smfmac_f32_16x16x64_bf16 v[236:239], v[252:255], v[168:175], %[ix]\n\t" \
    "ds_read_b128 v[168:171], %[l0] offset:18432\n\t" "ds_read_b128 v[172:175], %[l0] offset:19456\n\t" "s_waitcnt lgkmcnt(9)\n\t" \
    "v_smfmac_f32_16x16x64_bf16 v[232:235], v[16:19], a[144:151], %[ix]\n\t" "v_smfmac_f32_16x16x64_bf16 v[236:239], v[16:19], a[152:159], %[ix]\n\t" \
    "s_waitcnt lgkmcnt(8)\n\t" "v_smfmac_f32_16x16x64_bf16 v[232:235], v[20:23], a[160:167], %[ix]\n\t" \
    "v_smfmac_f32_16x16x64_bf16 v[236:239], v[20:23], v[176:183], %[ix]\n\t" "ds_read_b128 v[176:179], %[l0] offset:20480\n\t" \
    "ds_read_b128 v[180:183], %[l0] offset:21504\n\t" "s_waitcnt lgkmcnt(9)\n\t" "v_smfmac_f32_16x16x64_bf16 v[232:235], v[24:27], a[168:175], %[ix]\n\t" \
    "v_smfmac_f32_16x16x64_bf16 v[236:239], v[24:27], a[176:183], %[ix]\n\t" "s_waitcnt lgkmcnt(6)\n\t" \
    "v_smfmac_f32_16x16x64_bf16 v[232:235], v[28:31], a[184:191], %[ix]\n\t" "v_smfmac_f32_16x16x64_bf16 v[236:239], v[28:31], v[184:191], %[ix]\n\t" \
    "ds_read_b128 v[184:187], %[l0] offset:22528\n\t" "ds_read_b128 v[188:191], %[l0] offset:23552\n\t" XT(90, 91) "s_nop 4\n\t" "v_add_f32 v232, v232, v233\n\t" \
    "v_add_f32 v234, v234, v235\n\t" "v_add_f32 v236, v236, v237\n\t" "v_add_f32 v238, v238, v239\n\t" "v_cvt_pk_bf16_f32 v232, v232, v236\n\t" \
    "v_cvt_pk_bf16_f32 v233, v234, v238\n\t" "ds_write2_b32 %[vw], v232, v233 offset1:68\n\t" "s_waitcnt lgkmcnt(0)\n\t" "s_barrier\n\t" XT(92, 93) \
    "ds_read_b128 v[240:243], %[vr] offset:0\n\t" "ds_read_b128 v[244:247], %[vr] offset:64\n\t" "ds_read_b128 v[248:251], %[vr] offset:128\n\t" \
    "ds_read_b128 v[252:255], %[vr] offset:192\n\t" "s_mov_b32 s61, %[ub]\n\t" "s_waitcnt lgkmcnt(3)\n\t" \
    "v_smfmac_f32_16x16x64_bf16 v[192:195], v[240:243], v[32:39], %[ix]\n\t" "v_smfmac_f32_16x16x64_bf16 v[196:199], v[240:243], v[48:55], %[ix]\n\t" \
    "s_waitcnt lgkmcnt(2)\n\t" "v_smfmac_f32_16x16x64_bf16 v[192:195], v[244:247], v[40:47], %[ix]\n\t" \
    "v_smfmac_f32_16x16x64_bf16 v[196:199], v[244:247], v[56:63], %[ix]\n\t" "s_waitcnt lgkmcnt(1)\n\t" \
    "v_smfmac_f32_16x16x64_bf16 v[192:195], v[248:251], a[192:199], %[ix]\n\t" "v_smfmac_f32_16x16x64_bf16 v[196:199], v[248:251], a[200:207], %[ix]\n\t" \
    "s_waitcnt lgkmcnt(0)\n\t" "v_smfmac_f32_16x16x64_bf16 v[192:195], v[252:255], v[160:167], %[ix]\n\t" "ds_read_b128 v[160:163], %[l0] offset:24576\n\t" \
    "ds_read_b128 v[164:167], %[l0] offset:25600\n\t" "v_smfmac_f32_16x16x64_bf16 v[196:199], v[252:255], v[168:175], %[ix]\n\t" \
    "ds_read_b128 v[168:171], %[l0] offset:26624\n\t" "ds_read_b128 v[172:175], %[l0] offset:27648\n\t" \
    "v_smfmac_f32_16x16x64_bf16 v[200:203], v[240:243], v[64:71], %[ix]\n\t" "s_nop 1\n\t" "v_smfmac_f32_16x16x64_bf16 v[204:207], v[240:243], v[80:87], %[ix]\n\t" \
    "v_cvt_pk_bf16_f32 v224, v192, v194\n\t" "v_cvt_pk_bf16_f32 v225, v193, v195\n\t" "v_smfmac_f32_16x16x64_bf16 v[200:203], v[244:247], v[72:79], %[ix]\n\t" \
    "v_cvt_pk_bf16_f32 v226, v196, v198\n\t" "v_cvt_pk_bf16_f32 v227, v197, v199\n\t" "v_smfmac_f32_16x16x64_bf16 v[204:207], v[244:247], v[88:95], %[ix]\n\t" "s_nop 0\n\t" \
    "v_permlane32_swap_b32 v224, v226\n\t" "v_smfmac_f32_16x16x64_bf16 v[200:203], v[248:251], a[208:215], %[ix]\n\t" "v_permlane32_swap_b32 v225, v227\n\t" "s_nop 0\n\t" \
    "v_smfmac_f32_16x16x64_bf16 v[204:207], v[248:251], a[216:223], %[ix]\n\t" "v_permlane16_swap_b32 v224, v225\n\t" "v_permlane16_swap_b32 v226, v227\n\t" \
    "v_smfmac_f32_16x16x64_bf16 v[200:203], v[252:255], v[176:183], %[ix]\n\t" "buffer_store_dwordx4 v[224:227], %[uo], %[rs], s61 offen sc1\n\t" \
    "s_add_u32 s61, s61, 0x10000\n\t" XT(94, 95) "v_smfmac_f32_16x16x64_bf16 v[204:207], v[252:255], v[184:191], %[ix]\n\t" "ds_read_b128 v[176:179], %[l0] offset:28672\n\t" \
    "ds_read_b128 v[180:183], %[l0] offset:29696\n\t" "v_smfmac_f32_16x16x64_bf16 v[208:211], v[240:243], v[96:103], %[ix]\n\t" \
    "ds_read_b128 v[184:187], %[l0] offset:30720\n\t" "ds_read_b128 v[188:191], %[l0] offset:31744\n\t" \
    "v_smfmac_f32_16x16x64_bf16 v[212:215], v[240:243], v[112:119], %[ix]\n\t" "v_cvt_pk_bf16_f32 v228, v200, v202\n\t" "v_cvt_pk_bf16_f32 v229, v201, v203\n\t" \
    "v_smfmac_f32_16x16x64_bf16 v[208:211], v[244:247], v[104:111], %[ix]\n\t" "v_cvt_pk_bf16_f32 v230, v204, v206\n\t" "v_cvt_pk_bf16_f32 v231, v205, v207\n\t" \
    "v_smfmac_f32_16x16x64_bf16 v[212:215], v[244:247], v[120:127], %[ix]\n\t" "s_nop 0\n\t" "v_permlane32_swap_b32 v228, v230\n\t" \
    "v_smfmac_f32_16x16x64_bf16 v[208:211], v[248:251], a[224:231], %[ix]\n\t" "v_permlane32_swap_b32 v229, v231\n\t" "s_nop 0\n\t" \
    "v_smfmac_f32_16x16x64_bf16 v[212:215], v[248:251], a[232:239], %[ix]\n\t" "v_permlane16_swap_b32 v228, v229\n\t" "v_permlane16_swap_b32 v230, v231\n\t" \
    "s_waitcnt lgkmcnt(6)\n\t" "v_smfmac_f32_16x16x64_bf16 v[208:211], v[252:255], v[160:167], %[ix]\n\t" "buffer_store_dwordx4 v[228:231], %[uo], %[rs], s61 offen sc1\n\t" \
    "s_add_u32 s61, s61, 0x10000\n\t" "s_waitcnt lgkmcnt(4)\n\t" "v_smfmac_f32_16x16x64_bf16 v[212:215], v[252:255], v[168:175], %[ix]\n\t" "s_nop 1\n\t" \
    "v_smfmac_f32_16x16x64_bf16 v[216:219], v[240:243], v[128:135], %[ix]\n\t" "s_nop 1\n\t" "v_smfmac_f32_16x16x64_bf16 v[220:223], v[240:243], v[144:151], %[ix]\n\t" \
    "v_cvt_pk_bf16_f32 v224, v208, v210\n\t" "v_cvt_pk_bf16_f32 v225, v209, v211\n\t" "v_smfmac_f32_16x16x64_bf16 v[216:219], v[244:247], v[136:143], %[ix]\n\t" \
    "v_cvt_pk_bf16_f32 v226, v212, v214\n\t" "v_cvt_pk_bf16_f32 v227, v213, v215\n\t" "v_smfmac_f32_16x16x64_bf16 v[220:223], v[244:247], v[152:159], %[ix]\n\t" "s_nop 0\n\t" \
    "v_permlane32_swap_b32 v224, v226\n\t" "v_smfmac_f32_16x16x64_bf16 v[216:219], v[248:251], a[240:247], %[ix]\n\t" "v_permlane32_swap_b32 v225, v227\n\t" "s_nop 0\n\t" \
    "v_smfmac_f32_16x16x64_bf16 v[220:223], v[248:251], a[248:255], %[ix]\n\t" "v_permlane16_swap_b32 v224, v225\n\t" "v_permlane16_swap_b32 v226, v227\n\t" \
    "s_waitcnt lgkmcnt(2)\n\t" "v_smfmac_f32_16x16x64_bf16 v[216:219], v[252:255], v[176:183], %[ix]\n\t" "buffer_store_dwordx4 v[224:227], %[uo], %[rs], s61 offen sc1\n\t" \
    "s_add_u32 s61, s61, 0x10000\n\t" "s_waitcnt lgkmcnt(0)\n\t" "v_smfmac_f32_16x16x64_bf16 v[220:223], v[252:255], v[184:191], %[ix]\n\t" "s_nop 5\n\t" \
    "v_cvt_pk_bf16_f32 v228, v216, v218\n\t" "v_cvt_pk_bf16_f32 v229, v217, v219\n\t" "v_cvt_pk_bf16_f32 v230, v220, v222\n\t" "v_cvt_pk_bf16_f32 v231, v221, v223\n\t" \
    "s_nop 0\n\t" "v_permlane32_swap_b32 v228, v230\n\t" "v_permlane32_swap_b32 v229, v231\n\t" "s_nop 1\n\t" "v_permlane16_swap_b32 v228, v229\n\t" \
    "v_permlane16_swap_b32 v230, v231\n\t" "buffer_store_dwordx4 v[228:231], %[uo], %[rs], s61 offen sc1\n\t" "s_nop 1\n\t" XT(96, 97) "s_branch 99f\n\t" "21:\n\t" \
    "s_cmp_eq_u32 %[bd], 0\n\t" "s_cbranch_scc1 26f\n\t" "s_sub_u32 %[bd], %[bd], 1\n\t" XCNT(100) "s_waitcnt lgkmcnt(0)\n\t" "s_nop 7\n\t" "s_branch 20b\n\t" "26:\n\t" \
    "s_cmp_lg_u32 s60, 0\n\t" "s_branch 22b\n\t" "24:\n\t" "s_cmp_eq_u32 %[bd], 0\n\t" "s_cbranch_scc1 25b\n\t" "s_sub_u32 %[bd], %[bd], 1\n\t" XCNT(101) \
    "s_mov_b32 s60, 1\n\t" "s_waitcnt lgkmcnt(0)\n\t" "s_nop 7\n\t" "s_branch 20b\n\t" "92:\n\t" XT(80, 81) "s_barrier\n\t" XT(82, 83) "30:\n\t" \
    "buffer_load_dwordx4 v[240:243], %[mo], %[rs], %[sb] offen offset:0 sc1\n\t" "v_mov_b64 v[232:233], 0\n\t" "v_mov_b64 v[234:235], 0\n\t" "v_mov_b64 v[236:237], 0\n\t" \
    "v_mov_b64 v[238:239], 0\n\t" "v_mov_b64 v[192:193], 0\n\t" "v_mov_b64 v[194:195], 0\n\t" "v_mov_b64 v[196:197], 0\n\t" "v_mov_b64 v[198:199], 0\n\t" \
    "buffer_load_dwordx4 v[244:247], v224, %[rs], %[sb] offen offset:0 sc1\n\t" "ds_read_b128 v[160:163], %[l0] offset:0\n\t" "ds_read_b128 v[164:167], %[l0] offset:1024\n\t" \
    "ds_read_b128 v[168:171], %[l0] offset:2048\n\t" "ds_read_b128 v[172:175], %[l0] offset:3072\n\t" "s_nop 3\n\t" \
    "buffer_load_dwordx4 v[248:251], %[mo], %[rs], %[sb] offen offset:2048 sc1\n\t" "ds_read_b128 v[176:179], %[l0] offset:4096\n\t" \
    "ds_read_b128 v[180:183], %[l0] offset:5120\n\t" "ds_read_b128 v[184:187], %[l0] offset:6144\n\t" "ds_read_b128 v[188:191], %[l0] offset:7168\n\t" "s_nop 3\n\t" \
    "buffer_load_dwordx4 v[252:255], v224, %[rs], %[sb] offen offset:2048 sc1\n\t" "v_mov_b64 v[200:201], 0\n\t" "v_mov_b64 v[202:203], 0\n\t" "v_mov_b64 v[204:205], 0\n\t" \
    "v_mov_b64 v[206:207], 0\n\t" "v_mov_b64 v[208:209], 0\n\t" "v_mov_b64 v[210:211], 0\n\t" "v_mov_b64 v[212:213], 0\n\t" "v_mov_b64 v[214:215], 0\n\t" \
    "v_mov_b64 v[216:217], 0\n\t" "v_mov_b64 v[218:219], 0\n\t" "v_mov_b64 v[220:221], 0\n\t" "v_mov_b64 v[222:223], 0\n\t" "s_waitcnt vmcnt(3)\n\t" XT(84, 85) \
    "v_smfmac_f32_16x16x64_bf16 v[232:235], v[240:243], a[0:7], %[ix]\n\t" "ds_write_b128 %[l1], v[240:243] offset:8192\n\t" \
    "v_smfmac_f32_16x16x64_bf16 v[236:239], v[240:243], a[8:15], %[ix]\n\t" "v_max3_u32 v225, v240, v241, v242\n\t" "s_waitcnt vmcnt(2)\n\t" \
    "v_smfmac_f32_16x16x64_bf16 v[232:235], v[244:247], a[16:23], %[ix]\n\t" "ds_write_b128 %[l1], v[244:247] offset:9216\n\t" "v_max3_u32 v225, v225, v243, v244\n\t" \
    "s_waitcnt lgkmcnt(8)\n\t" "v_smfmac_f32_16x16x64_bf16 v[236:239], v[244:247], v[160:167], %[ix]\n\t" "v_max3_u32 v225, v225, v245, v246\n\t" \
    "v_max_u32 v225, v225, v247\n\t" "v_cmp_eq_u32 vcc, -1, v225\n\t" "s_cmp_lg_u32 s60, 0\n\t" "s_cbranch_vccnz 31f\n\t" "32:\n\t" "s_waitcnt lgkmcnt(0)\n\t" \
    "s_cbranch_scc1 33f\n\t" "s_barrier\n\t" "33:\n\t" XT(86, 87) "ds_read_b128 v[16:19], %[l1] offset:12288\n\t" "ds_read_b128 v[20:23], %[l1] offset:0\n\t" \
    "ds_read_b128 v[24:27], %[l1] offset:4096\n\t" "s_waitcnt vmcnt(1)\n\t" "v_smfmac_f32_16x16x64_bf16 v[232:235], v[248:251], a[24:31], %[ix]\n\t" \
    "ds_write_b128 %[l1], v[248:251] offset:10240\n\t" "v_max3_u32 v225, v248, v249, v250\n\t" "v_smfmac_f32_16x16x64_bf16 v[236:239], v[248:251], a[32:39], %[ix]\n\t" \
    "ds_read_b128 v[28:31], %[l1] offset:13312\n\t" "ds_read_b128 v[240:243], %[l1] offset:1024\n\t" "ds_read_b128 v[244:247], %[l1] offset:5120\n\t" "s_waitcnt vmcnt(0)\n\t" \
    "v_smfmac_f32_16x16x64_bf16 v[232:235], v[252:255], a[40:47], %[ix]\n\t" "ds_write_b128 %[l1], v[252:255] offset:11264\n\t" "v_max3_u32 v225, v225, v251, v252\n\t" \
    "v_smfmac_f32_16x16x64_bf16 v[236:239], v[252:255], v[168:175], %[ix]\n\t" "v_max3_u32 v225, v225, v253, v254\n\t" "v_max_u32 v225, v225, v255\n\t" \
    "s_waitcnt lgkmcnt(7)\n\t" "v_smfmac_f32_16x16x64_bf16 v[232:235], v[16:19], a[48:55], %[ix]\n\t" "v_cmp_eq_u32 vcc, -1, v225\n\t" "s_cbranch_vccnz 34f\n\t" "35:\n\t" \
    "v_smfmac_f32_16x16x64_bf16 v[236:239], v[16:19], a[56:63], %[ix]\n\t" "ds_read_b128 v[160:163], %[l0] offset:8192\n\t" "ds_read_b128 v[164:167], %[l0] offset:9216\n\t" \
    "s_waitcnt lgkmcnt(8)\n\t" "v_smfmac_f32_16x16x64_bf16 v[232:235], v[20:23], a[64:71], %[ix]\n\t" "ds_read_b128 v[168:171], %[l0] offset:10240\n\t" \
    "ds_read_b128 v[172:175], %[l0] offset:11264\n\t" "v_smfmac_f32_16x16x64_bf16 v[236:239], v[20:23], v[176:183], %[ix]\n\t" "s_waitcnt lgkmcnt(4)\n\t" "s_barrier\n\t" \
    XT(88, 89) "v_smfmac_f32_16x16x64_bf16 v[232:235], v[24:27], a[72:79], %[ix]\n\t" "ds_read_b128 v[176:179], %[l0] offset:12288\n\t" \
    "ds_read_b128 v[180:183], %[l0] offset:13312\n\t" "v_smfmac_f32_16x16x64_bf16 v[236:239], v[24:27], a[80:87], %[ix]\n\t" "ds_read_b128 v[248:251], %[l1] offset:14336\n\t" \
    "ds_read_b128 v[252:255], %[l1] offset:2048\n\t" "v_smfmac_f32_16x16x64_bf16 v[232:235], v[28:31], a[88:95], %[ix]\n\t" "ds_read_b128 v[16:19], %[l1] offset:6144\n\t" \
    "ds_read_b128 v[20:23], %[l1] offset:15360\n\t" "v_smfmac_f32_16x16x64_bf16 v[236:239], v[28:31], v[184:191], %[ix]\n\t" "ds_read_b128 v[24:27], %[l1] offset:3072\n\t" \
    "ds_read_b128 v[184:187], %[l0] offset:14336\n\t" "v_smfmac_f32_16x16x64_bf16 v[232:235], v[240:243], a[96:103], %[ix]\n\t" \
    "ds_read_b128 v[188:191], %[l0] offset:15360\n\t" "ds_read_b128 v[28:31], %[l1] offset:7168\n\t" \
    "v_smfmac_f32_16x16x64_bf16 v[236:239], v[240:243], a[104:111], %[ix]\n\t" "v_smfmac_f32_16x16x64_bf16 v[232:235], v[244:247], a[112:119], %[ix]\n\t" \
    "s_waitcnt lgkmcnt(12)\n\t" "v_smfmac_f32_16x16x64_bf16 v[236:239], v[244:247], v[160:167], %[ix]\n\t" "ds_read_b128 v[160:163], %[l0] offset:16384\n\t" \
    "ds_read_b128 v[164:167], %[l0] offset:17408\n\t" "s_waitcnt lgkmcnt(9)\n\t" "v_smfmac_f32_16x16x64_bf16 v[232:235], v[248:251], a[120:127], %[ix]\n\t" \
    "v_smfmac_f32_16x16x64_bf16 v[236:239], v[248:251], a[128:135], %[ix]\n\t" "s_waitcnt lgkmcnt(8)\n\t" \
    "v_smfmac_f32_16x16x64_bf16 v[232:235], v[252:255], a[136:143], %[ix]\n\t" "v_smfmac_f32_16x16x64_bf16 v[236:239], v[252:255], v[168:175], %[ix]\n\t" \
    "ds_read_b128 v[168:171], %[l0] offset:18432\n\t" "ds_read_b128 v[172:175], %[l0] offset:19456\n\t" "s_waitcnt lgkmcnt(9)\n\t" \
    "v_smfmac_f32_16x16x64_bf16 v[232:235], v[16:19], a[144:151], %[ix]\n\t" "v_smfmac_f32_16x16x64_bf16 v[236:239], v[16:19], a[152:159], %[ix]\n\t" \
    "s_waitcnt lgkmcnt(8)\n\t" "v_smfmac_f32_16x16x64_bf16 v[232:235], v[20:23], a[160:167], %[ix]\n\t" \
    "v_smfmac_f32_16x16x64_bf16 v[236:239], v[20:23], v[176:183], %[ix]\n\t" "ds_read_b128 v[176:179], %[l0] offset:20480\n\t" \
    "ds_read_b128 v[180:183], %[l0] offset:21504\n\t" "s_waitcnt lgkmcnt(9)\n\t" "v_smfmac_f32_16x16x64_bf16 v[232:235], v[24:27], a[168:175], %[ix]\n\t" \
    "v_smfmac_f32_16x16x64_bf16 v[236:239], v[24:27], a[176:183], %[ix]\n\t" "s_waitcnt lgkmcnt(6)\n\t" \
    "v_smfmac_f32_16x16x64_bf16 v[232:235], v[28:31], a[184:191], %[ix]\n\t" "v_smfmac_f32_16x16x64_bf16 v[236:239], v[28:31], v[184:191], %[ix]\n\t" \
    "ds_read_b128 v[184:187], %[l0] offset:22528\n\t" "ds_read_b128 v[188:191], %[l0] offset:23552\n\t" XT(90, 91) "s_nop 4\n\t" "v_add_f32 v232, v232, v233\n\t" \
    "v_add_f32 v234, v234, v235\n\t" "v_add_f32 v236, v236, v237\n\t" "v_add_f32 v238, v238, v239\n\t" "v_cvt_pk_bf16_f32 v232, v232, v236\n\t" \
    "v_cvt_pk_bf16_f32 v233, v234, v238\n\t" "ds_write2_b32 %[vw], v232, v233 offset1:68\n\t" "s_waitcnt lgkmcnt(0)\n\t" "s_barrier\n\t" XT(92, 93) \
    "ds_read_b128 v[240:243], %[vr] offset:0\n\t" "ds_read_b128 v[244:247], %[vr] offset:64\n\t" "ds_read_b128 v[248:251], %[vr] offset:128\n\t" \
    "ds_read_b128 v[252:255], %[vr] offset:192\n\t" "s_mov_b32 s61, %[ub]\n\t" "s_waitcnt lgkmcnt(3)\n\t" \
    "v_smfmac_f32_16x16x64_bf16 v[192:195], v[240:243], v[32:39], %[ix]\n\t" "v_smfmac_f32_16x16x64_bf16 v[196:199], v[240:243], v[48:55], %[ix]\n\t" \
    "s_waitcnt lgkmcnt(2)\n\t" "v_smfmac_f32_16x16x64_bf16 v[192:195], v[244:247], v[40:47], %[ix]\n\t" \
    "v_smfmac_f32_16x16x64_bf16 v[196:199], v[244:247], v[56:63], %[ix]\n\t" "s_waitcnt lgkmcnt(1)\n\t" \
    "v_smfmac_f32_16x16x64_bf16 v[192:195], v[248:251], a[192:199], %[ix]\n\t" "v_smfmac_f32_16x16x64_bf16 v[196:199], v[248:251], a[200:207], %[ix]\n\t" \
    "s_waitcnt lgkmcnt(0)\n\t" "v_smfmac_f32_16x16x64_bf16 v[192:195], v[252:255], v[160:167], %[ix]\n\t" "ds_read_b128 v[160:163], %[l0] offset:24576\n\t" \
    "ds_read_b128 v[164:167], %[l0] offset:25600\n\t" "v_smfmac_f32_16x16x64_bf16 v[196:199], v[252:255], v[168:175], %[ix]\n\t" \
    "ds_read_b128 v[168:171], %[l0] offset:26624\n\t" "ds_read_b128 v[172:175], %[l0] offset:27648\n\t" \
    "v_smfmac_f32_16x16x64_bf16 v[200:203], v[240:243], v[64:71], %[ix]\n\t" "s_nop 1\n\t" "v_smfmac_f32_16x16x64_bf16 v[204:207], v[240:243], v[80:87], %[ix]\n\t" \
    "v_cvt_pk_bf16_f32 v224, v192, v194\n\t" "v_cvt_pk_bf16_f32 v225, v193, v195\n\t" "v_smfmac_f32_16x16x64_bf16 v[200:203], v[244:247], v[72:79], %[ix]\n\t" \
    "v_cvt_pk_bf16_f32 v226, v196, v198\n\t" "v_cvt_pk_bf16_f32 v227, v197, v199\n\t" "v_smfmac_f32_16x16x64_bf16 v[204:207], v[244:247], v[88:95], %[ix]\n\t" "s_nop 0\n\t" \
    "v_permlane32_swap_b32 v224, v226\n\t" "v_smfmac_f32_16x16x64_bf16 v[200:203], v[248:251], a[208:215], %[ix]\n\t" "v_permlane32_swap_b32 v225, v227\n\t" "s_nop 0\n\t" \
    "v_smfmac_f32_16x16x64_bf16 v[204:207], v[248:251], a[216:223], %[ix]\n\t" "v_permlane16_swap_b32 v224, v225\n\t" "v_permlane16_swap_b32 v226, v227\n\t" \
    "v_smfmac_f32_16x16x64_bf16 v[200:203], v[252:255], v[176:183], %[ix]\n\t" "buffer_store_dwordx4 v[224:227], %[uo], %[rs], s61 offen sc1\n\t" \
    "s_add_u32 s61, s61, 0x10000\n\t" XT(94, 95) "v_smfmac_f32_16x16x64_bf16 v[204:207], v[252:255], v[184:191], %[ix]\n\t" "ds_read_b128 v[176:179], %[l0] offset:28672\n\t" \
    "ds_read_b128 v[180:183], %[l0] offset:29696\n\t" "v_smfmac_f32_16x16x64_bf16 v[208:211], v[240:243], v[96:103], %[ix]\n\t" \
    "ds_read_b128 v[184:187], %[l0] offset:30720\n\t" "ds_read_b128 v[188:191], %[l0] offset:31744\n\t" \
    "v_smfmac_f32_16x16x64_bf16 v[212:215], v[240:243], v[112:119], %[ix]\n\t" "v_cvt_pk_bf16_f32 v228, v200, v202\n\t" "v_cvt_pk_bf16_f32 v229, v201, v203\n\t" \
    "v_smfmac_f32_16x16x64_bf16 v[208:211], v[244:247], v[104:111], %[ix]\n\t" "v_cvt_pk_bf16_f32 v230, v204, v206\n\t" "v_cvt_pk_bf16_f32 v231, v205, v207\n\t" \
    "v_smfmac_f32_16x16x64_bf16 v[212:215], v[244:247], v[120:127], %[ix]\n\t" "s_nop 0\n\t" "v_permlane32_swap_b32 v228, v230\n\t" \
    "v_smfmac_f32_16x16x64_bf16 v[208:211], v[248:251], a[224:231], %[ix]\n\t" "v_permlane32_swap_b32 v229, v231\n\t" "s_nop 0\n\t" \
    "v_smfmac_f32_16x16x64_bf16 v[212:215], v[248:251], a[232:239], %[ix]\n\t" "v_permlane16_swap_b32 v228, v229\n\t" "v_permlane16_swap_b32 v230, v231\n\t" \
    "s_waitcnt lgkmcnt(6)\n\t" "v_smfmac_f32_16x16x64_bf16 v[208:211], v[252:255], v[160:167], %[ix]\n\t" "buffer_store_dwordx4 v[228:231], %[uo], %[rs], s61 offen sc1\n\t" \
    "s_add_u32 s61, s61, 0x10000\n\t" "s_waitcnt lgkmcnt(4)\n\t" "v_smfmac_f32_16x16x64_bf16 v[212:215], v[252:255], v[168:175], %[ix]\n\t" "s_nop 1\n\t" \
    "v_smfmac_f32_16x16x64_bf16 v[216:219], v[240:243], v[128:135], %[ix]\n\t" "s_nop 1\n\t" "v_smfmac_f32_16x16x64_bf16 v[220:223], v[240:243], v[144:151], %[ix]\n\t" \
    "v_cvt_pk_bf16_f32 v224, v208, v210\n\t" "v_cvt_pk_bf16_f32 v225, v209, v211\n\t" "v_smfmac_f32_16x16x64_bf16 v[216:219], v[244:247], v[136:143], %[ix]\n\t" \
    "v_cvt_pk_bf16_f32 v226, v212, v214\n\t" "v_cvt_pk_bf16_f32 v227, v213, v215\n\t" "v_smfmac_f32_16x16x64_bf16 v[220:223], v[244:247], v[152:159], %[ix]\n\t" "s_nop 0\n\t" \
    "v_permlane32_swap_b32 v224, v226\n\t" "v_smfmac_f32_16x16x64_bf16 v[216:219], v[248:251], a[240:247], %[ix]\n\t" "v_permlane32_swap_b32 v225, v227\n\t" "s_nop 0\n\t" \
    "v_smfmac_f32_16x16x64_bf16 v[220:223], v[248:251], a[248:255], %[ix]\n\t" "v_permlane16_swap_b32 v224, v225\n\t" "v_permlane16_swap_b32 v226, v227\n\t" \
    "s_waitcnt lgkmcnt(2)\n\t" "v_smfmac_f32_16x16x64_bf16 v[216:219], v[252:255], v[176:183], %[ix]\n\t" "buffer_store_dwordx4 v[224:227], %[uo], %[rs], s61 offen sc1\n\t" \
    "s_add_u32 s61, s61, 0x10000\n\t" "s_waitcnt lgkmcnt(0)\n\t" "v_smfmac_f32_16x16x64_bf16 v[220:223], v[252:255], v[184:191], %[ix]\n\t" "s_nop 5\n\t" \
    "v_cvt_pk_bf16_f32 v228, v216, v218\n\t" "v_cvt_pk_bf16_f32 v229, v217, v219\n\t" "v_cvt_pk_bf16_f32 v230, v220, v222\n\t" "v_cvt_pk_bf16_f32 v231, v221, v223\n\t" \
    "s_nop 0\n\t" "v_permlane32_swap_b32 v228, v230\n\t" "v_permlane32_swap_b32 v229, v231\n\t" "s_nop 1\n\t" "v_permlane16_swap_b32 v228, v229\n\t" \
    "v_permlane16_swap_b32 v230, v231\n\t" "buffer_store_dwordx4 v[228:231], %[uo], %[rs], s61 offen sc1\n\t" "s_nop 1\n\t" XT(96, 97) "s_branch 99f\n\t" "31:\n\t" \
    "s_cmp_eq_u32 %[bd], 0\n\t" "s_cbranch_scc1 36f\n\t" "s_sub_u32 %[bd], %[bd], 1\n\t" XCNT(100) "s_waitcnt lgkmcnt(0)\n\t" "s_nop 7\n\t" "s_branch 30b\n\t" "36:\n\t" \
    "s_cmp_lg_u32 s60, 0\n\t" "s_branch 32b\n\t" "34:\n\t" "s_cmp_eq_u32 %[bd], 0\n\t" "s_cbranch_scc1 35b\n\t" "s_sub_u32 %[bd], %[bd], 1\n\t" XCNT(101) \
    "s_mov_b32 s60, 1\n\t" "s_waitcnt lgkmcnt(0)\n\t" "s_nop 7\n\t" "s_branch 30b\n\t" "93:\n\t" XT(80, 81) "s_barrier\n\t" XT(82, 83) "40:\n\t" \
    "buffer_load_dwordx4 v[240:243], %[mo], %[rs], %[sb] offen offset:0 sc1\n\t" "v_mov_b64 v[232:233], 0\n\t" "v_mov_b64 v[234:235], 0\n\t" "v_mov_b64 v[236:237], 0\n\t" \
    "v_mov_b64 v[238:239], 0\n\t" "v_mov_b64 v[192:193], 0\n\t" "v_mov_b64 v[194:195], 0\n\t" "v_mov_b64 v[196:197], 0\n\t" "v_mov_b64 v[198:199], 0\n\t" \
    "buffer_load_dwordx4 v[244:247], v224, %[rs], %[sb] offen offset:0 sc1\n\t" "ds_read_b128 v[160:163], %[l0] offset:0\n\t" "ds_read_b128 v[164:167], %[l0] offset:1024\n\t" \
    "ds_read_b128 v[168:171], %[l0] offset:2048\n\t" "ds_read_b128 v[172:175], %[l0] offset:3072\n\t" "s_nop 3\n\t" \
    "buffer_load_dwordx4 v[248:251], %[mo], %[rs], %[sb] offen offset:2048 sc1\n\t" "ds_read_b128 v[176:179], %[l0] offset:4096\n\t" \
    "ds_read_b128 v[180:183], %[l0] offset:5120\n\t" "ds_read_b128 v[184:187], %[l0] offset:6144\n\t" "ds_read_b128 v[188:191], %[l0] offset:7168\n\t" "s_nop 3\n\t" \
    "buffer_load_dwordx4 v[252:255], v224, %[rs], %[sb] offen offset:2048 sc1\n\t" "v_mov_b64 v[200:201], 0\n\t" "v_mov_b64 v[202:203], 0\n\t" "v_mov_b64 v[204:205], 0\n\t" \
    "v_mov_b64 v[206:207], 0\n\t" "v_mov_b64 v[208:209], 0\n\t" "v_mov_b64 v[210:211], 0\n\t" "v_mov_b64 v[212:213], 0\n\t" "v_mov_b64 v[214:215], 0\n\t" \
    "v_mov_b64 v[216:217], 0\n\t" "v_mov_b64 v[218:219], 0\n\t" "v_mov_b64 v[220:221], 0\n\t" "v_mov_b64 v[222:223], 0\n\t" "s_waitcnt vmcnt(3)\n\t" XT(84, 85) \
    "v_smfmac_f32_16x16x64_bf16 v[232:235], v[240:243], a[0:7], %[ix]\n\t" "ds_write_b128 %[l1], v[240:243] offset:12288\n\t" \
    "v_smfmac_f32_16x16x64_bf16 v[236:239], v[240:243], a[8:15], %[ix]\n\t" "v_max3_u32 v225, v240, v241, v242\n\t" "s_waitcnt vmcnt(2)\n\t" \
    "v_smfmac_f32_16x16x64_bf16 v[232:235], v[244:247], a[16:23], %[ix]\n\t" "ds_write_b128 %[l1], v[244:247] offset:13312\n\t" "v_max3_u32 v225, v225, v243, v244\n\t" \
    "s_waitcnt lgkmcnt(8)\n\t" "v_smfmac_f32_16x16x64_bf16 v[236:239], v[244:247], v[160:167], %[ix]\n\t" "v_max3_u32 v225, v225, v245, v246\n\t" \
    "v_max_u32 v225, v225, v247\n\t" "v_cmp_eq_u32 vcc, -1, v225\n\t" "s_cmp_lg_u32 s60, 0\n\t" "s_cbranch_vccnz 41f\n\t" "42:\n\t" "s_waitcnt lgkmcnt(0)\n\t" \
    "s_cbranch_scc1 43f\n\t" "s_barrier\n\t" "43:\n\t" XT(86, 87) "ds_read_b128 v[16:19], %[l1] offset:0\n\t" "ds_read_b128 v[20:23], %[l1] offset:4096\n\t" \
    "ds_read_b128 v[24:27], %[l1] offset:8192\n\t" "s_waitcnt vmcnt(1)\n\t" "v_smfmac_f32_16x16x64_bf16 v[232:235], v[248:251], a[24:31], %[ix]\n\t" \
    "ds_write_b128 %[l1], v[248:251] offset:14336\n\t" "v_max3_u32 v225, v248, v249, v250\n\t" "v_smfmac_f32_16x16x64_bf16 v[236:239], v[248:251], a[32:39], %[ix]\n\t" \
    "ds_read_b128 v[28:31], %[l1] offset:1024\n\t" "ds_read_b128 v[240:243], %[l1] offset:5120\n\t" "ds_read_b128 v[244:247], %[l1] offset:9216\n\t" "s_waitcnt vmcnt(0)\n\t" \
    "v_smfmac_f32_16x16x64_bf16 v[232:235], v[252:255], a[40:47], %[ix]\n\t" "ds_write_b128 %[l1], v[252:255] offset:15360\n\t" "v_max3_u32 v225, v225, v251, v252\n\t" \
    "v_smfmac_f32_16x16x64_bf16 v[236:239], v[252:255], v[168:175], %[ix]\n\t" "v_max3_u32 v225, v225, v253, v254\n\t" "v_max_u32 v225, v225, v255\n\t" \
    "s_waitcnt lgkmcnt(7)\n\t" "v_smfmac_f32_16x16x64_bf16 v[232:235], v[16:19], a[48:55], %[ix]\n\t" "v_cmp_eq_u32 vcc, -1, v225\n\t" "s_cbranch_vccnz 44f\n\t" "45:\n\t" \
    "v_smfmac_f32_16x16x64_bf16 v[236:239], v[16:19], a[56:63], %[ix]\n\t" "ds_read_b128 v[160:163], %[l0] offset:8192\n\t" "ds_read_b128 v[164:167], %[l0] offset:9216\n\t" \
    "s_waitcnt lgkmcnt(8)\n\t" "v_smfmac_f32_16x16x64_bf16 v[232:235], v[20:23], a[64:71], %[ix]\n\t" "ds_read_b128 v[168:171], %[l0] offset:10240\n\t" \
    "ds_read_b128 v[172:175], %[l0] offset:11264\n\t" "v_smfmac_f32_16x16x64_bf16 v[236:239], v[20:23], v[176:183], %[ix]\n\t" "s_waitcnt lgkmcnt(4)\n\t" "s_barrier\n\t" \
    XT(88, 89) "v_smfmac_f32_16x16x64_bf16 v[232:235], v[24:27], a[72:79], %[ix]\n\t" "ds_read_b128 v[176:179], %[l0] offset:12288\n\t" \
    "ds_read_b128 v[180:183], %[l0] offset:13312\n\t" "v_smfmac_f32_16x16x64_bf16 v[236:239], v[24:27], a[80:87], %[ix]\n\t" "ds_read_b128 v[248:251], %[l1] offset:2048\n\t" \
    "ds_read_b128 v[252:255], %[l1] offset:6144\n\t" "v_smfmac_f32_16x16x64_bf16 v[232:235], v[28:31], a[88:95], %[ix]\n\t" "ds_read_b128 v[16:19], %[l1] offset:10240\n\t" \
    "ds_read_b128 v[20:23], %[l1] offset:3072\n\t" "v_smfmac_f32_16x16x64_bf16 v[236:239], v[28:31], v[184:191], %[ix]\n\t" "ds_read_b128 v[24:27], %[l1] offset:7168\n\t" \
    "ds_read_b128 v[184:187], %[l0] offset:14336\n\t" "v_smfmac_f32_16x16x64_bf16 v[232:235], v[240:243], a[96:103], %[ix]\n\t" \
    "ds_read_b128 v[188:191], %[l0] offset:15360\n\t" "ds_read_b128 v[28:31], %[l1] offset:11264\n\t" \
    "v_smfmac_f32_16x16x64_bf16 v[236:239], v[240:243], a[104:111], %[ix]\n\t" "v_smfmac_f32_16x16x64_bf16 v[232:235], v[244:247], a[112:119], %[ix]\n\t" \
    "s_waitcnt lgkmcnt(12)\n\t" "v_smfmac_f32_16x16x64_bf16 v[236:239], v[244:247], v[160:167], %[ix]\n\t" "ds_read_b128 v[160:163], %[l0] offset:16384\n\t" \
    "ds_read_b128 v[164:167], %[l0] offset:17408\n\t" "s_waitcnt lgkmcnt(9)\n\t" "v_smfmac_f32_16x16x64_bf16 v[232:235], v[248:251], a[120:127], %[ix]\n\t" \
    "v_smfmac_f32_16x16x64_bf16 v[236:239], v[248:251], a[128:135], %[ix]\n\t" "s_waitcnt lgkmcnt(8)\n\t" \
    "v_smfmac_f32_16x16x64_bf16 v[232:235], v[252:255], a[136:143], %[ix]\n\t" "v_smfmac_f32_16x16x64_bf16 v[236:239], v[252:255], v[168:175], %[ix]\n\t" \
    "ds_read_b128 v[168:171], %[l0] offset:18432\n\t" "ds_read_b128 v[172:175], %[l0] offset:19456\n\t" "s_waitcnt lgkmcnt(9)\n\t" \
    "v_smfmac_f32_16x16x64_bf16 v[232:235], v[16:19], a[144:151], %[ix]\n\t" "v_smfmac_f32_16x16x64_bf16 v[236:239], v[16:19], a[152:159], %[ix]\n\t" \
    "s_waitcnt lgkmcnt(8)\n\t" "v_smfmac_f32_16x16x64_bf16 v[232:235], v[20:23], a[160:167], %[ix]\n\t" \
    "v_smfmac_f32_16x16x64_bf16 v[236:239], v[20:23], v[176:183], %[ix]\n\t" "ds_read_b128 v[176:179], %[l0] offset:20480\n\t" \
    "ds_read_b128 v[180:183], %[l0] offset:21504\n\t" "s_waitcnt lgkmcnt(9)\n\t" "v_smfmac_f32_16x16x64_bf16 v[232:235], v[24:27], a[168:175], %[ix]\n\t" \
    "v_smfmac_f32_16x16x64_bf16 v[236:239], v[24:27], a[176:183], %[ix]\n\t" "s_waitcnt lgkmcnt(6)\n\t" \
    "v_smfmac_f32_16x16x64_bf16 v[232:235], v[28:31], a[184:191], %[ix]\n\t" "v_smfmac_f32_16x16x64_bf16 v[236:239], v[28:31], v[184:191], %[ix]\n\t" \
    "ds_read_b128 v[184:187], %[l0] offset:22528\n\t" "ds_read_b128 v[188:191], %[l0] offset:23552\n\t" XT(90, 91) "s_nop 4\n\t" "v_add_f32 v232, v232, v233\n\t" \
    "v_add_f32 v234, v234, v235\n\t" "v_add_f32 v236, v236, v237\n\t" "v_add_f32 v238, v238, v239\n\t" "v_cvt_pk_bf16_f32 v232, v232, v236\n\t" \
    "v_cvt_pk_bf16_f32 v233, v234, v238\n\t" "ds_write2_b32 %[vw], v232, v233 offset1:68\n\t" "s_waitcnt lgkmcnt(0)\n\t" "s_barrier\n\t" XT(92, 93) \
    "ds_read_b128 v[240:243], %[vr] offset:0\n\t" "ds_read_b128 v[244:247], %[vr] offset:64\n\t" "ds_read_b128 v[248:251], %[vr] offset:128\n\t" \
    "ds_read_b128 v[252:255], %[vr] offset:192\n\t" "s_mov_b32 s61, %[ub]\n\t" "s_waitcnt lgkmcnt(3)\n\t" \
    "v_smfmac_f32_16x16x64_bf16 v[192:195], v[240:243], v[32:39], %[ix]\n\t" "v_smfmac_f32_16x16x64_bf16 v[196:199], v[240:243], v[48:55], %[ix]\n\t" \
    "s_waitcnt lgkmcnt(2)\n\t" "v_smfmac_f32_16x16x64_bf16 v[192:195], v[244:247], v[40:47], %[ix]\n\t" \
    "v_smfmac_f32_16x16x64_bf16 v[196:199], v[244:247], v[56:63], %[ix]\n\t" "s_waitcnt lgkmcnt(1)\n\t" \
    "v_smfmac_f32_16x16x64_bf16 v[192:195], v[248:251], a[192:199], %[ix]\n\t" "v_smfmac_f32_16x16x64_bf16 v[196:199], v[248:251], a[200:207], %[ix]\n\t" \
    "s_waitcnt lgkmcnt(0)\n\t" "v_smfmac_f32_16x16x64_bf16 v[192:195], v[252:255], v[160:167], %[ix]\n\t" "ds_read_b128 v[160:163], %[l0] offset:24576\n\t" \
    "ds_read_b128 v[164:167], %[l0] offset:25600\n\t" "v_smfmac_f32_16x16x64_bf16 v[196:199], v[252:255], v[168:175], %[ix]\n\t" \
    "ds_read_b128 v[168:171], %[l0] offset:26624\n\t" "ds_read_b128 v[172:175], %[l0] offset:27648\n\t" \
    "v_smfmac_f32_16x16x64_bf16 v[200:203], v[240:243], v[64:71], %[ix]\n\t" "s_nop 1\n\t" "v_smfmac_f32_16x16x64_bf16 v[204:207], v[240:243], v[80:87], %[ix]\n\t" \
    "v_cvt_pk_bf16_f32 v224, v192, v194\n\t" "v_cvt_pk_bf16_f32 v225, v193, v195\n\t" "v_smfmac_f32_16x16x64_bf16 v[200:203], v[244:247], v[72:79], %[ix]\n\t" \
    "v_cvt_pk_bf16_f32 v226, v196, v198\n\t" "v_cvt_pk_bf16_f32 v227, v197, v199\n\t" "v_smfmac_f32_16x16x64_bf16 v[204:207], v[244:247], v[88:95], %[ix]\n\t" "s_nop 0\n\t" \
    "v_permlane32_swap_b32 v224, v226\n\t" "v_smfmac_f32_16x16x64_bf16 v[200:203], v[248:251], a[208:215], %[ix]\n\t" "v_permlane32_swap_b32 v225, v227\n\t" "s_nop 0\n\t" \
    "v_smfmac_f32_16x16x64_bf16 v[204:207], v[248:251], a[216:223], %[ix]\n\t" "v_permlane16_swap_b32 v224, v225\n\t" "v_permlane16_swap_b32 v226, v227\n\t" \
    "v_smfmac_f32_16x16x64_bf16 v[200:203], v[252:255], v[176:183], %[ix]\n\t" "buffer_store_dwordx4 v[224:227], %[uo], %[rs], s61 offen sc1\n\t" \
    "s_add_u32 s61, s61, 0x10000\n\t" XT(94, 95) "v_smfmac_f32_16x16x64_bf16 v[204:207], v[252:255], v[184:191], %[ix]\n\t" "ds_read_b128 v[176:179], %[l0] offset:28672\n\t" \
    "ds_read_b128 v[180:183], %[l0] offset:29696\n\t" "v_smfmac_f32_16x16x64_bf16 v[208:211], v[240:243], v[96:103], %[ix]\n\t" \
    "ds_read_b128 v[184:187], %[l0] offset:30720\n\t" "ds_read_b128 v[188:191], %[l0] offset:31744\n\t" \
    "v_smfmac_f32_16x16x64_bf16 v[212:215], v[240:243], v[112:119], %[ix]\n\t" "v_cvt_pk_bf16_f32 v228, v200, v202\n\t" "v_cvt_pk_bf16_f32 v229, v201, v203\n\t" \
    "v_smfmac_f32_16x16x64_bf16 v[208:211], v[244:247], v[104:111], %[ix]\n\t" "v_cvt_pk_bf16_f32 v230, v204, v206\n\t" "v_cvt_pk_bf16_f32 v231, v205, v207\n\t" \
    "v_smfmac_f32_16x16x64_bf16 v[212:215], v[244:247], v[120:127], %[ix]\n\t" "s_nop 0\n\t" "v_permlane32_swap_b32 v228, v230\n\t" \
    "v_smfmac_f32_16x16x64_bf16 v[208:211], v[248:251], a[224:231], %[ix]\n\t" "v_permlane32_swap_b32 v229, v231\n\t" "s_nop 0\n\t" \
    "v_smfmac_f32_16x16x64_bf16 v[212:215], v[248:251], a[232:239], %[ix]\n\t" "v_permlane16_swap_b32 v228, v229\n\t" "v_permlane16_swap_b32 v230, v231\n\t" \
    "s_waitcnt lgkmcnt(6)\n\t" "v_smfmac_f32_16x16x64_bf16 v[208:211], v[252:255], v[160:167], %[ix]\n\t" "buffer_store_dwordx4 v[228:231], %[uo], %[rs], s61 offen sc1\n\t" \
    "s_add_u32 s61, s61, 0x10000\n\t" "s_waitcnt lgkmcnt(4)\n\t" "v_smfmac_f32_16x16x64_bf16 v[212:215], v[252:255], v[168:175], %[ix]\n\t" "s_nop 1\n\t" \
    "v_smfmac_f32_16x16x64_bf16 v[216:219], v[240:243], v[128:135], %[ix]\n\t" "s_nop 1\n\t" "v_smfmac_f32_16x16x64_bf16 v[220:223], v[240:243], v[144:151], %[ix]\n\t" \
    "v_cvt_pk_bf16_f32 v224, v208, v210\n\t" "v_cvt_pk_bf16_f32 v225, v209, v211\n\t" "v_smfmac_f32_16x16x64_bf16 v[216:219], v[244:247], v[136:143], %[ix]\n\t" \
    "v_cvt_pk_bf16_f32 v226, v212, v214\n\t" "v_cvt_pk_bf16_f32 v227, v213, v215\n\t" "v_smfmac_f32_16x16x64_bf16 v[220:223], v[244:247], v[152:159], %[ix]\n\t" "s_nop 0\n\t" \
    "v_permlane32_swap_b32 v224, v226\n\t" "v_smfmac_f32_16x16x64_bf16 v[216:219], v[248:251], a[240:247], %[ix]\n\t" "v_permlane32_swap_b32 v225, v227\n\t" "s_nop 0\n\t" \
    "v_smfmac_f32_16x16x64_bf16 v[220:223], v[248:251], a[248:255], %[ix]\n\t" "v_permlane16_swap_b32 v224, v225\n\t" "v_permlane16_swap_b32 v226, v227\n\t" \
    "s_waitcnt lgkmcnt(2)\n\t" "v_smfmac_f32_16x16x64_bf16 v[216:219], v[252:255], v[176:183], %[ix]\n\t" "buffer_store_dwordx4 v[224:227], %[uo], %[rs], s61 offen sc1\n\t" \
    "s_add_u32 s61, s61, 0x10000\n\t" "s_waitcnt lgkmcnt(0)\n\t" "v_smfmac_f32_16x16x64_bf16 v[220:223], v[252:255], v[184:191], %[ix]\n\t" "s_nop 5\n\t" \
    "v_cvt_pk_bf16_f32 v228, v216, v218\n\t" "v_cvt_pk_bf16_f32 v229, v217, v219\n\t" "v_cvt_pk_bf16_f32 v230, v220, v222\n\t" "v_cvt_pk_bf16_f32 v231, v221, v223\n\t" \
    "s_nop 0\n\t" "v_permlane32_swap_b32 v228, v230\n\t" "v_permlane32_swap_b32 v229, v231\n\t" "s_nop 1\n\t" "v_permlane16_swap_b32 v228, v229\n\t" \
    "v_permlane16_swap_b32 v230, v231\n\t" "buffer_store_dwordx4 v[228:231], %[uo], %[rs], s61 offen sc1\n\t" "s_nop 1\n\t" XT(96, 97) "s_branch 99f\n\t" "41:\n\t" \
    "s_cmp_eq_u32 %[bd], 0\n\t" "s_cbranch_scc1 46f\n\t" "s_sub_u32 %[bd], %[bd], 1\n\t" XCNT(100) "s_waitcnt lgkmcnt(0)\n\t" "s_nop 7\n\t" "s_branch 40b\n\t" "46:\n\t" \
    "s_cmp_lg_u32 s60, 0\n\t" "s_branch 42b\n\t" "44:\n\t" "s_cmp_eq_u32 %[bd], 0\n\t" "s_cbranch_scc1 45b\n\t" "s_sub_u32 %[bd], %[bd], 1\n\t" XCNT(101) \
    "s_mov_b32 s60, 1\n\t" "s_waitcnt lgkmcnt(0)\n\t" "s_nop 7\n\t" "s_branch 40b\n\t" "99:\n\t"
#define Y_STEP \
    "s_getpc_b64 s[62:63]\n\t" "11:\n\t" "s_memtime s[64:65]\n\t" "s_waitcnt lgkmcnt(0)\n\t" "s_sub_u32 s61, %[tu], s64\n\t" "s_cmp_gt_i32 s61, 124\n\t" \
    "s_cbranch_scc1 11b\n\t" "s_max_i32 s61, s61, 0\n\t" "s_and_b32 s61, s61, 124\n\t" "s_sub_u32 s61, 12f-11b, s61\n\t" "s_add_u32 s62, s62, s61\n\t" \
    "s_addc_u32 s63, s63, 0\n\t" "s_setpc_b64 s[62:63]\n\t" ".rept 31\n\t" "s_nop 0\n\t" ".endr\n\t" "12:\n\t" XT(90, 91) \
    "buffer_load_dwordx4 v[144:147], %[u0], %[rs], %[ub] offen offset:0 sc1\n\t" "buffer_load_dwordx4 v[148:151], %[u0], %[rs], %[ub] offen offset:1024 sc1\n\t" \
    "buffer_load_dwordx4 v[152:155], %[u0], %[rs], %[ub] offen offset:2048 sc1\n\t" "buffer_load_dwordx4 v[156:159], %[u0], %[rs], %[ub] offen offset:3072 sc1\n\t" \
    "buffer_load_dwordx4 v[160:163], %[u1], %[rs], %[ub] offen offset:0 sc1\n\t" "buffer_load_dwordx4 v[164:167], %[u1], %[rs], %[ub] offen offset:1024 sc1\n\t" \
    "buffer_load_dwordx4 v[168:171], %[u1], %[rs], %[ub] offen offset:2048 sc1\n\t" "buffer_load_dwordx4 v[172:175], %[u1], %[rs], %[ub] offen offset:3072 sc1\n\t" \
    "s_cmp_lg_u32 %[late], 0\n\t" "s_cbranch_scc1 14f\n\t" "s_getpc_b64 s[62:63]\n\t" "13:\n\t" "s_memtime s[64:65]\n\t" "s_waitcnt lgkmcnt(0)\n\t" \
    "s_sub_u32 s61, %[tg], s64\n\t" "s_cmp_gt_i32 s61, 124\n\t" "s_cbranch_scc1 13b\n\t" "s_max_i32 s61, s61, 0\n\t" "s_and_b32 s61, s61, 124\n\t" \
    "s_sub_u32 s61, 14f-13b, s61\n\t" "s_add_u32 s62, s62, s61\n\t" "s_addc_u32 s63, s63, 0\n\t" "s_setpc_b64 s[62:63]\n\t" ".rept 31\n\t" "s_nop 0\n\t" ".endr\n\t" "14:\n\t" \
    XT(92, 93) "20:\n\t" "buffer_load_dwordx4 v[192:195], %[x0], %[rs], %[pb] offen offset:0 sc1\n\t" \
    "buffer_load_dwordx4 v[196:199], %[x0], %[rs], %[pb] offen offset:1024 sc1\n\t" "buffer_load_dwordx4 v[200:203], %[x0], %[rs], %[pb] offen offset:2048 sc1\n\t" \
    "buffer_load_dwordx4 v[204:207], %[x0], %[rs], %[pb] offen offset:3072 sc1\n\t" "buffer_load_dwordx4 v[208:211], %[x1], %[rs], %[pb] offen offset:0 sc1\n\t" \
    "buffer_load_dwordx4 v[212:215], %[x1], %[rs], %[pb] offen offset:1024 sc1\n\t" "buffer_load_dwordx4 v[216:219], %[x1], %[rs], %[pb] offen offset:2048 sc1\n\t" \
    "buffer_load_dwordx4 v[220:223], %[x1], %[rs], %[pb] offen offset:3072 sc1\n\t" "buffer_load_dwordx4 v[224:227], %[x2], %[rs], %[pb] offen offset:0 sc1\n\t" \
    "buffer_load_dwordx4 v[228:231], %[x2], %[rs], %[pb] offen offset:1024 sc1\n\t" "buffer_load_dwordx4 v[232:235], %[x2], %[rs], %[pb] offen offset:2048 sc1\n\t" \
    "buffer_load_dwordx4 v[236:239], %[x2], %[rs], %[pb] offen offset:3072 sc1\n\t" "buffer_load_dwordx4 v[240:243], %[x3], %[rs], %[pb] offen offset:0 sc1\n\t" \
    "buffer_load_dwordx4 v[244:247], %[x3], %[rs], %[pb] offen offset:1024 sc1\n\t" "buffer_load_dwordx4 v[248:251], %[x3], %[rs], %[pb] offen offset:2048 sc1\n\t" \
    "buffer_load_dwordx4 v[252:255], %[x3], %[rs], %[pb] offen offset:3072 sc1\n\t" "v_mov_b64 v[120:121], 0\n\t" "v_mov_b64 v[122:123], 0\n\t" \
    "buffer_load_dwordx2 v[176:177], %[pm], %[rs], %[pb] offen offset:0 sc1\n\t" "buffer_load_dwordx2 v[178:179], %[pm], %[rs], %[pb] offen offset:128 sc1\n\t" \
    "buffer_load_dwordx2 v[180:181], %[pm], %[rs], %[pb] offen offset:1024 sc1\n\t" "buffer_load_dwordx2 v[182:183], %[pm], %[rs], %[pb] offen offset:1152 sc1\n\t" \
    "buffer_load_dwordx2 v[184:185], %[pm], %[rs], %[pb] offen offset:2048 sc1\n\t" "buffer_load_dwordx2 v[186:187], %[pm], %[rs], %[pb] offen offset:2176 sc1\n\t" \
    "buffer_load_dwordx2 v[188:189], %[pm], %[rs], %[pb] offen offset:3072 sc1\n\t" "buffer_load_dwordx2 v[190:191], %[pm], %[rs], %[pb] offen offset:3200 sc1\n\t" \
    "buffer_load_dword %[sv], %[so], %[rs], %[pb] offen sc1\n\t" "v_mov_b32 v127, -1\n\t" "s_waitcnt vmcnt(24)\n\t" XT(94, 95) \
    "v_smfmac_f32_16x16x64_bf16 v[120:123], v[192:195], a[0:7], %[ix]\n\t" "s_waitcnt vmcnt(23)\n\t" "v_smfmac_f32_16x16x64_bf16 v[120:123], v[196:199], a[8:15], %[ix]\n\t" \
    "s_waitcnt vmcnt(22)\n\t" "v_smfmac_f32_16x16x64_bf16 v[120:123], v[200:203], a[16:23], %[ix]\n\t" "s_waitcnt vmcnt(21)\n\t" \
    "v_smfmac_f32_16x16x64_bf16 v[120:123], v[204:207], a[24:31], %[ix]\n\t" "s_waitcnt vmcnt(20)\n\t" \
    "v_smfmac_f32_16x16x64_bf16 v[120:123], v[208:211], a[32:39], %[ix]\n\t" "s_waitcnt vmcnt(19)\n\t" \
    "v_smfmac_f32_16x16x64_bf16 v[120:123], v[212:215], a[40:47], %[ix]\n\t" "s_waitcnt vmcnt(18)\n\t" \
    "v_smfmac_f32_16x16x64_bf16 v[120:123], v[216:219], a[48:55], %[ix]\n\t" "s_waitcnt vmcnt(17)\n\t" \
    "v_smfmac_f32_16x16x64_bf16 v[120:123], v[220:223], a[56:63], %[ix]\n\t" "s_waitcnt vmcnt(16)\n\t" \
    "v_smfmac_f32_16x16x64_bf16 v[120:123], v[224:227], a[64:71], %[ix]\n\t" "s_waitcnt vmcnt(15)\n\t" \
    "v_smfmac_f32_16x16x64_bf16 v[120:123], v[228:231], a[72:79], %[ix]\n\t" "s_waitcnt vmcnt(14)\n\t" \
    "v_smfmac_f32_16x16x64_bf16 v[120:123], v[232:235], a[80:87], %[ix]\n\t" "s_waitcnt vmcnt(13)\n\t" \
    "v_smfmac_f32_16x16x64_bf16 v[120:123], v[236:239], a[88:95], %[ix]\n\t" "s_waitcnt vmcnt(12)\n\t" \
    "v_smfmac_f32_16x16x64_bf16 v[120:123], v[240:243], a[96:103], %[ix]\n\t" "s_waitcnt vmcnt(11)\n\t" \
    "v_smfmac_f32_16x16x64_bf16 v[120:123], v[244:247], a[104:111], %[ix]\n\t" "s_waitcnt vmcnt(10)\n\t" \
    "v_smfmac_f32_16x16x64_bf16 v[120:123], v[248:251], a[112:119], %[ix]\n\t" "s_waitcnt vmcnt(9)\n\t" \
    "v_smfmac_f32_16x16x64_bf16 v[120:123], v[252:255], a[120:127], %[ix]\n\t" "s_waitcnt vmcnt(7)\n\t" \
    "v_smfmac_f32_16x16x64_bf16 v[120:123], v[176:179], v[144:151], %[ix]\n\t" "s_waitcnt vmcnt(5)\n\t" \
    "v_smfmac_f32_16x16x64_bf16 v[120:123], v[180:183], v[152:159], %[ix]\n\t" "s_waitcnt vmcnt(3)\n\t" \
    "v_smfmac_f32_16x16x64_bf16 v[120:123], v[184:187], v[160:167], %[ix]\n\t" "s_waitcnt vmcnt(1)\n\t" \
    "v_smfmac_f32_16x16x64_bf16 v[120:123], v[188:191], v[168:175], %[ix]\n\t" XT(82, 83) "s_nop 7\n\t" "v_add_f32 v120, v120, v121\n\t" "v_add_f32 v122, v122, v123\n\t" \
    "v_cvt_pk_bf16_f32 v124, v120, v122\n\t" "v_cmp_u_f32 vcc, v120, v122\n\t" "s_cbranch_vccnz 30f\n\t" "21:\n\t" \
    "v_mov_b32_dpp v125, v124 quad_perm:[1,0,3,2] row_mask:0xf bank_mask:0xf\n\t" "v_perm_b32 v124, v125, v124, %[ps]\n\t" "v_min_u32 v124, -2, v124\n\t" \
    "buffer_store_dword v124, %[mw], %[rs], %[mb] offen sc1\n\t" XT(84, 85) "buffer_store_dword v127, %[mw], %[rs], %[mr] offen sc1\n\t" "s_memtime s[64:65]\n\t" \
    "s_barrier\n\t" XT(98, 99) "s_waitcnt lgkmcnt(0)\n\t" "s_and_b32 %[bad], s64, -2\n\t" "s_branch 40f\n\t" "30:\n\t" "v_max_u32 v126, v144, v145\n\t" \
    "v_max3_u32 v126, v126, v146, v147\n\t" "v_max3_u32 v126, v126, v148, v149\n\t" "v_max3_u32 v126, v126, v150, v151\n\t" "v_max3_u32 v126, v126, v152, v153\n\t" \
    "v_max3_u32 v126, v126, v154, v155\n\t" "v_max3_u32 v126, v126, v156, v157\n\t" "v_max3_u32 v126, v126, v158, v159\n\t" "v_max3_u32 v126, v126, v160, v161\n\t" \
    "v_max3_u32 v126, v126, v162, v163\n\t" "v_max3_u32 v126, v126, v164, v165\n\t" "v_max3_u32 v126, v126, v166, v167\n\t" "v_max3_u32 v126, v126, v168, v169\n\t" \
    "v_max3_u32 v126, v126, v170, v171\n\t" "v_max3_u32 v126, v126, v172, v173\n\t" "v_max3_u32 v126, v126, v174, v175\n\t" UCNT "v_max3_u32 v126, v126, v176, v177\n\t" \
    "v_max3_u32 v126, v126, v178, v179\n\t" "v_max3_u32 v126, v126, v180, v181\n\t" "v_max3_u32 v126, v126, v182, v183\n\t" "v_max3_u32 v126, v126, v184, v185\n\t" \
    "v_max3_u32 v126, v126, v186, v187\n\t" "v_max3_u32 v126, v126, v188, v189\n\t" "v_max3_u32 v126, v126, v190, v191\n\t" "v_max3_u32 v126, v126, v192, v193\n\t" \
    "v_max3_u32 v126, v126, v194, v195\n\t" "v_max3_u32 v126, v126, v196, v197\n\t" "v_max3_u32 v126, v126, v198, v199\n\t" "v_max3_u32 v126, v126, v200, v201\n\t" \
    "v_max3_u32 v126, v126, v202, v203\n\t" "v_max3_u32 v126, v126, v204, v205\n\t" "v_max3_u32 v126, v126, v206, v207\n\t" "v_max3_u32 v126, v126, v208, v209\n\t" \
    "v_max3_u32 v126, v126, v210, v211\n\t" "v_max3_u32 v126, v126, v212, v213\n\t" "v_max3_u32 v126, v126, v214, v215\n\t" "v_max3_u32 v126, v126, v216, v217\n\t" \
    "v_max3_u32 v126, v126, v218, v219\n\t" "v_max3_u32 v126, v126, v220, v221\n\t" "v_max3_u32 v126, v126, v222, v223\n\t" "v_max3_u32 v126, v126, v224, v225\n\t" \
    "v_max3_u32 v126, v126, v226, v227\n\t" "v_max3_u32 v126, v126, v228, v229\n\t" "v_max3_u32 v126, v126, v230, v231\n\t" "v_max3_u32 v126, v126, v232, v233\n\t" \
    "v_max3_u32 v126, v126, v234, v235\n\t" "v_max3_u32 v126, v126, v236, v237\n\t" "v_max3_u32 v126, v126, v238, v239\n\t" "v_max3_u32 v126, v126, v240, v241\n\t" \
    "v_max3_u32 v126, v126, v242, v243\n\t" "v_max3_u32 v126, v126, v244, v245\n\t" "v_max3_u32 v126, v126, v246, v247\n\t" "v_max3_u32 v126, v126, v248, v249\n\t" \
    "v_max3_u32 v126, v126, v250, v251\n\t" "v_max3_u32 v126, v126, v252, v253\n\t" "v_max3_u32 v126, v126, v254, v255\n\t" "v_cmp_eq_u32 vcc, -1, v126\n\t" \
    "s_cbranch_vccz 21b\n\t" "s_cmp_eq_u32 %[bd], 0\n\t" "s_cbranch_scc1 21b\n\t" XCNT(100) "s_mov_b32 %[bad], 1\n\t" "s_waitcnt vmcnt(0)\n\t" "40:\n\t"
#define G_LOOP \
    "v_xor_b32 v72, 0, %[xo]\n\t" "v_add_u32 v80, 0x10000, v72\n\t" "v_xor_b32 v88, 0, %[xs]\n\t" "v_add_u32 v88, 0, v88\n\t" "v_xor_b32 v73, 32, %[xo]\n\t" \
    "v_add_u32 v81, 0x10000, v73\n\t" "v_xor_b32 v89, 16, %[xs]\n\t" "v_add_u32 v89, 2048, v89\n\t" "v_xor_b32 v74, 64, %[xo]\n\t" "v_add_u32 v82, 0x10000, v74\n\t" \
    "v_xor_b32 v90, 32, %[xs]\n\t" "v_add_u32 v90, 4096, v90\n\t" "v_xor_b32 v75, 96, %[xo]\n\t" "v_add_u32 v83, 0x10000, v75\n\t" "v_xor_b32 v91, 48, %[xs]\n\t" \
    "v_add_u32 v91, 6144, v91\n\t" "v_xor_b32 v76, 128, %[xo]\n\t" "v_add_u32 v84, 0x10000, v76\n\t" "v_xor_b32 v92, 64, %[xs]\n\t" "v_add_u32 v92, 8192, v92\n\t" \
    "v_xor_b32 v77, 160, %[xo]\n\t" "v_add_u32 v85, 0x10000, v77\n\t" "v_xor_b32 v93, 80, %[xs]\n\t" "v_add_u32 v93, 10240, v93\n\t" "v_xor_b32 v78, 192, %[xo]\n\t" \
    "v_add_u32 v86, 0x10000, v78\n\t" "v_xor_b32 v94, 96, %[xs]\n\t" "v_add_u32 v94, 12288, v94\n\t" "v_xor_b32 v79, 224, %[xo]\n\t" "v_add_u32 v87, 0x10000, v79\n\t" \
    "v_xor_b32 v95, 112, %[xs]\n\t" "v_add_u32 v95, 14336, v95\n\t" "v_mov_b32 v96, 2.0\n\t" "s_mov_b32 s60, 0\n\t" "s_mov_b32 s62, 0\n\t" "s_waitcnt vmcnt(0)\n\t" \
    "s_barrier\n\t" "ds_read_b128 v[100:103], v72 offset:0\n\t" "ds_read_b128 v[104:107], v73 offset:0\n\t" "ds_read_b128 v[108:111], v74 offset:0\n\t" \
    "ds_read_b128 v[112:115], v75 offset:0\n\t" "ds_read_b128 v[116:119], v76 offset:0\n\t" "10:\n\t" XT(80, 81) "s_waitcnt lgkmcnt(4)\n\t" \
    "v_mfma_f32_32x32x16_bf16 v[32:47], a[0:3], v[100:103], 0\n\t" "ds_read_b128 v[120:123], v77 offset:0\n\t" "s_add_u32 s61, s60, 1\n\t" "s_and_b32 s61, s61, 63\n\t" \
    "s_lshl_b32 s61, s61, 16\n\t" "s_add_u32 s63, %[lw], 65536\n\t" "s_waitcnt lgkmcnt(4)\n\t" "v_mfma_f32_32x32x16_bf16 v[32:47], a[4:7], v[104:107], v[32:47]\n\t" \
    "ds_read_b128 v[124:127], v78 offset:0\n\t" "s_waitcnt lgkmcnt(4)\n\t" "v_mfma_f32_32x32x16_bf16 v[32:47], a[8:11], v[108:111], v[32:47]\n\t" \
    "ds_read_b128 v[128:131], v79 offset:0\n\t" "v_cvt_pk_bf16_f32 v64, v48, v49\n\t" "v_cvt_pk_bf16_f32 v65, v50, v51\n\t" "v_cvt_pk_bf16_f32 v66, v52, v53\n\t" \
    "s_waitcnt lgkmcnt(4)\n\t" "v_mfma_f32_32x32x16_bf16 v[32:47], a[12:15], v[112:115], v[32:47]\n\t" "ds_read_b128 v[100:103], v72 offset:256\n\t" \
    "v_cvt_pk_bf16_f32 v67, v54, v55\n\t" "v_cvt_pk_bf16_f32 v68, v56, v57\n\t" "v_cvt_pk_bf16_f32 v69, v58, v59\n\t" "s_waitcnt lgkmcnt(4)\n\t" \
    "v_mfma_f32_32x32x16_bf16 v[32:47], a[16:19], v[116:119], v[32:47]\n\t" "ds_read_b128 v[104:107], v73 offset:256\n\t" "v_cvt_pk_bf16_f32 v70, v60, v61\n\t" \
    "v_cvt_pk_bf16_f32 v71, v62, v63\n\t" "buffer_store_dwordx4 v[64:67], v96, %[rs], s62 offen offset:0 sc1\n\t" "s_waitcnt lgkmcnt(4)\n\t" \
    "v_mfma_f32_32x32x16_bf16 v[32:47], a[20:23], v[120:123], v[32:47]\n\t" "ds_read_b128 v[108:111], v74 offset:256\n\t" "s_waitcnt lgkmcnt(4)\n\t" \
    "v_mfma_f32_32x32x16_bf16 v[32:47], a[24:27], v[124:127], v[32:47]\n\t" "ds_read_b128 v[112:115], v75 offset:256\n\t" \
    "buffer_store_dwordx4 v[68:71], v96, %[rs], s62 offen offset:1024 sc1\n\t" "s_waitcnt lgkmcnt(4)\n\t" \
    "v_mfma_f32_32x32x16_bf16 v[32:47], a[28:31], v[128:131], v[32:47]\n\t" "ds_read_b128 v[116:119], v76 offset:256\n\t" "s_mov_b32 m0, s63\n\t" \
    "s_add_u32 s63, s63, 0x800\n\t" "buffer_load_dwordx4 v88, %[rx], s61 offen lds\n\t" "s_waitcnt lgkmcnt(4)\n\t" \
    "v_mfma_f32_32x32x16_bf16 v[32:47], a[32:35], v[100:103], v[32:47]\n\t" "ds_read_b128 v[120:123], v77 offset:256\n\t" \
    "buffer_load_dwordx4 v88, %[rx], s61 offen offset:1024 lds\n\t" "s_waitcnt lgkmcnt(4)\n\t" "v_mfma_f32_32x32x16_bf16 v[32:47], a[36:39], v[104:107], v[32:47]\n\t" \
    "ds_read_b128 v[124:127], v78 offset:256\n\t" "s_mov_b32 m0, s63\n\t" "s_add_u32 s63, s63, 0x800\n\t" "buffer_load_dwordx4 v89, %[rx], s61 offen lds\n\t" \
    "s_waitcnt lgkmcnt(4)\n\t" "v_mfma_f32_32x32x16_bf16 v[32:47], a[40:43], v[108:111], v[32:47]\n\t" "ds_read_b128 v[128:131], v79 offset:256\n\t" \
    "buffer_load_dwordx4 v89, %[rx], s61 offen offset:1024 lds\n\t" "s_waitcnt lgkmcnt(4)\n\t" "v_mfma_f32_32x32x16_bf16 v[32:47], a[44:47], v[112:115], v[32:47]\n\t" \
    "ds_read_b128 v[100:103], v72 offset:512\n\t" "s_mov_b32 m0, s63\n\t" "s_add_u32 s63, s63, 0x800\n\t" "buffer_load_dwordx4 v90, %[rx], s61 offen lds\n\t" \
    "s_waitcnt lgkmcnt(4)\n\t" "v_mfma_f32_32x32x16_bf16 v[32:47], a[48:51], v[116:119], v[32:47]\n\t" "ds_read_b128 v[104:107], v73 offset:512\n\t" \
    "buffer_load_dwordx4 v90, %[rx], s61 offen offset:1024 lds\n\t" "s_waitcnt lgkmcnt(4)\n\t" "v_mfma_f32_32x32x16_bf16 v[32:47], a[52:55], v[120:123], v[32:47]\n\t" \
    "ds_read_b128 v[108:111], v74 offset:512\n\t" "s_mov_b32 m0, s63\n\t" "s_add_u32 s63, s63, 0x800\n\t" "buffer_load_dwordx4 v91, %[rx], s61 offen lds\n\t" \
    "s_waitcnt lgkmcnt(4)\n\t" "v_mfma_f32_32x32x16_bf16 v[32:47], a[56:59], v[124:127], v[32:47]\n\t" "ds_read_b128 v[112:115], v75 offset:512\n\t" \
    "buffer_load_dwordx4 v91, %[rx], s61 offen offset:1024 lds\n\t" "s_waitcnt lgkmcnt(4)\n\t" "v_mfma_f32_32x32x16_bf16 v[32:47], a[60:63], v[128:131], v[32:47]\n\t" \
    "ds_read_b128 v[116:119], v76 offset:512\n\t" "s_mov_b32 m0, s63\n\t" "s_add_u32 s63, s63, 0x800\n\t" "buffer_load_dwordx4 v92, %[rx], s61 offen lds\n\t" \
    "s_waitcnt lgkmcnt(4)\n\t" "v_mfma_f32_32x32x16_bf16 v[32:47], a[64:67], v[100:103], v[32:47]\n\t" "ds_read_b128 v[120:123], v77 offset:512\n\t" \
    "buffer_load_dwordx4 v92, %[rx], s61 offen offset:1024 lds\n\t" "s_waitcnt lgkmcnt(4)\n\t" "v_mfma_f32_32x32x16_bf16 v[32:47], a[68:71], v[104:107], v[32:47]\n\t" \
    "ds_read_b128 v[124:127], v78 offset:512\n\t" "s_mov_b32 m0, s63\n\t" "s_add_u32 s63, s63, 0x800\n\t" "buffer_load_dwordx4 v93, %[rx], s61 offen lds\n\t" \
    "s_waitcnt lgkmcnt(4)\n\t" "v_mfma_f32_32x32x16_bf16 v[32:47], a[72:75], v[108:111], v[32:47]\n\t" "ds_read_b128 v[128:131], v79 offset:512\n\t" \
    "buffer_load_dwordx4 v93, %[rx], s61 offen offset:1024 lds\n\t" "s_waitcnt lgkmcnt(4)\n\t" "v_mfma_f32_32x32x16_bf16 v[32:47], a[76:79], v[112:115], v[32:47]\n\t" \
    "ds_read_b128 v[100:103], v72 offset:768\n\t" "s_mov_b32 m0, s63\n\t" "s_add_u32 s63, s63, 0x800\n\t" "buffer_load_dwordx4 v94, %[rx], s61 offen lds\n\t" \
    "s_waitcnt lgkmcnt(4)\n\t" "v_mfma_f32_32x32x16_bf16 v[32:47], a[80:83], v[116:119], v[32:47]\n\t" "ds_read_b128 v[104:107], v73 offset:768\n\t" \
    "buffer_load_dwordx4 v94, %[rx], s61 offen offset:1024 lds\n\t" "s_waitcnt lgkmcnt(4)\n\t" "v_mfma_f32_32x32x16_bf16 v[32:47], a[84:87], v[120:123], v[32:47]\n\t" \
    "ds_read_b128 v[108:111], v74 offset:768\n\t" "s_mov_b32 m0, s63\n\t" "s_add_u32 s63, s63, 0x800\n\t" "buffer_load_dwordx4 v95, %[rx], s61 offen lds\n\t" \
    "s_waitcnt lgkmcnt(4)\n\t" "v_mfma_f32_32x32x16_bf16 v[32:47], a[88:91], v[124:127], v[32:47]\n\t" "ds_read_b128 v[112:115], v75 offset:768\n\t" \
    "buffer_load_dwordx4 v95, %[rx], s61 offen offset:1024 lds\n\t" "s_waitcnt lgkmcnt(4)\n\t" "v_mfma_f32_32x32x16_bf16 v[32:47], a[92:95], v[128:131], v[32:47]\n\t" \
    "ds_read_b128 v[116:119], v76 offset:768\n\t" "s_waitcnt lgkmcnt(4)\n\t" "v_mfma_f32_32x32x16_bf16 v[32:47], a[96:99], v[100:103], v[32:47]\n\t" \
    "ds_read_b128 v[120:123], v77 offset:768\n\t" "s_lshr_b32 s64, s60, 2\n\t" "s_xor_b32 s64, s64, %[xp]\n\t" "s_and_b32 s64, s64, 1\n\t" "s_lshl_b32 s64, s64, 13\n\t" \
    "s_waitcnt lgkmcnt(4)\n\t" "v_mfma_f32_32x32x16_bf16 v[32:47], a[100:103], v[104:107], v[32:47]\n\t" "ds_read_b128 v[124:127], v78 offset:768\n\t" \
    "s_lshl_b32 s62, s60, 19\n\t" "s_add_u32 s62, s62, s64\n\t" "s_add_u32 s62, s62, %[ob]\n\t" "s_waitcnt lgkmcnt(4)\n\t" \
    "v_mfma_f32_32x32x16_bf16 v[32:47], a[104:107], v[108:111], v[32:47]\n\t" "ds_read_b128 v[128:131], v79 offset:768\n\t" "s_waitcnt lgkmcnt(4)\n\t" \
    "v_mfma_f32_32x32x16_bf16 v[32:47], a[108:111], v[112:115], v[32:47]\n\t" "ds_read_b128 v[100:103], v72 offset:1024\n\t" "s_waitcnt lgkmcnt(4)\n\t" \
    "v_mfma_f32_32x32x16_bf16 v[32:47], a[112:115], v[116:119], v[32:47]\n\t" "ds_read_b128 v[104:107], v73 offset:1024\n\t" "s_waitcnt lgkmcnt(4)\n\t" \
    "v_mfma_f32_32x32x16_bf16 v[32:47], a[116:119], v[120:123], v[32:47]\n\t" "ds_read_b128 v[108:111], v74 offset:1024\n\t" "s_waitcnt lgkmcnt(4)\n\t" \
    "v_mfma_f32_32x32x16_bf16 v[32:47], a[120:123], v[124:127], v[32:47]\n\t" "ds_read_b128 v[112:115], v75 offset:1024\n\t" "s_waitcnt lgkmcnt(4)\n\t" \
    "v_mfma_f32_32x32x16_bf16 v[32:47], a[124:127], v[128:131], v[32:47]\n\t" "ds_read_b128 v[116:119], v76 offset:1024\n\t" "s_waitcnt lgkmcnt(4)\n\t" \
    "v_mfma_f32_32x32x16_bf16 v[32:47], a[128:131], v[100:103], v[32:47]\n\t" "ds_read_b128 v[120:123], v77 offset:1024\n\t" "s_waitcnt lgkmcnt(4)\n\t" \
    "v_mfma_f32_32x32x16_bf16 v[32:47], a[132:135], v[104:107], v[32:47]\n\t" "ds_read_b128 v[124:127], v78 offset:1024\n\t" "s_waitcnt lgkmcnt(4)\n\t" \
    "v_mfma_f32_32x32x16_bf16 v[32:47], a[136:139], v[108:111], v[32:47]\n\t" "ds_read_b128 v[128:131], v79 offset:1024\n\t" "s_waitcnt vmcnt(16)\n\t" "s_barrier\n\t" \
    "v_mov_b32 v98, s60\n\t" "buffer_store_dword v98, %[fo], %[rc], %[fl] offen sc1\n\t" "s_waitcnt lgkmcnt(4)\n\t" \
    "v_mfma_f32_32x32x16_bf16 v[32:47], a[140:143], v[112:115], v[32:47]\n\t" "ds_read_b128 v[100:103], v72 offset:1280\n\t" "s_waitcnt lgkmcnt(4)\n\t" \
    "v_mfma_f32_32x32x16_bf16 v[32:47], a[144:147], v[116:119], v[32:47]\n\t" "ds_read_b128 v[104:107], v73 offset:1280\n\t" "s_waitcnt lgkmcnt(4)\n\t" \
    "v_mfma_f32_32x32x16_bf16 v[32:47], a[148:151], v[120:123], v[32:47]\n\t" "ds_read_b128 v[108:111], v74 offset:1280\n\t" "s_waitcnt lgkmcnt(4)\n\t" \
    "v_mfma_f32_32x32x16_bf16 v[32:47], a[152:155], v[124:127], v[32:47]\n\t" "ds_read_b128 v[112:115], v75 offset:1280\n\t" "s_waitcnt lgkmcnt(4)\n\t" \
    "v_mfma_f32_32x32x16_bf16 v[32:47], a[156:159], v[128:131], v[32:47]\n\t" "ds_read_b128 v[116:119], v76 offset:1280\n\t" "s_waitcnt lgkmcnt(4)\n\t" \
    "v_mfma_f32_32x32x16_bf16 v[32:47], a[160:163], v[100:103], v[32:47]\n\t" "ds_read_b128 v[120:123], v77 offset:1280\n\t" "s_waitcnt lgkmcnt(4)\n\t" \
    "v_mfma_f32_32x32x16_bf16 v[32:47], a[164:167], v[104:107], v[32:47]\n\t" "ds_read_b128 v[124:127], v78 offset:1280\n\t" "s_waitcnt lgkmcnt(4)\n\t" \
    "v_mfma_f32_32x32x16_bf16 v[32:47], a[168:171], v[108:111], v[32:47]\n\t" "ds_read_b128 v[128:131], v79 offset:1280\n\t" "s_waitcnt lgkmcnt(4)\n\t" \
    "v_mfma_f32_32x32x16_bf16 v[32:47], a[172:175], v[112:115], v[32:47]\n\t" "ds_read_b128 v[100:103], v72 offset:1536\n\t" "s_waitcnt lgkmcnt(4)\n\t" \
    "v_mfma_f32_32x32x16_bf16 v[32:47], a[176:179], v[116:119], v[32:47]\n\t" "ds_read_b128 v[104:107], v73 offset:1536\n\t" "s_waitcnt lgkmcnt(4)\n\t" \
    "v_mfma_f32_32x32x16_bf16 v[32:47], a[180:183], v[120:123], v[32:47]\n\t" "ds_read_b128 v[108:111], v74 offset:1536\n\t" "s_waitcnt lgkmcnt(4)\n\t" \
    "v_mfma_f32_32x32x16_bf16 v[32:47], a[184:187], v[124:127], v[32:47]\n\t" "ds_read_b128 v[112:115], v75 offset:1536\n\t" "s_waitcnt lgkmcnt(4)\n\t" \
    "v_mfma_f32_32x32x16_bf16 v[32:47], a[188:191], v[128:131], v[32:47]\n\t" "ds_read_b128 v[116:119], v76 offset:1536\n\t" "s_waitcnt lgkmcnt(4)\n\t" \
    "v_mfma_f32_32x32x16_bf16 v[32:47], a[192:195], v[100:103], v[32:47]\n\t" "ds_read_b128 v[120:123], v77 offset:1536\n\t" "s_waitcnt lgkmcnt(4)\n\t" \
    "v_mfma_f32_32x32x16_bf16 v[32:47], a[196:199], v[104:107], v[32:47]\n\t" "ds_read_b128 v[124:127], v78 offset:1536\n\t" "s_waitcnt lgkmcnt(4)\n\t" \
    "v_mfma_f32_32x32x16_bf16 v[32:47], a[200:203], v[108:111], v[32:47]\n\t" "ds_read_b128 v[128:131], v79 offset:1536\n\t" "s_waitcnt lgkmcnt(4)\n\t" \
    "v_mfma_f32_32x32x16_bf16 v[32:47], a[204:207], v[112:115], v[32:47]\n\t" "ds_read_b128 v[100:103], v72 offset:1792\n\t" "s_waitcnt lgkmcnt(4)\n\t" \
    "v_mfma_f32_32x32x16_bf16 v[32:47], a[208:211], v[116:119], v[32:47]\n\t" "ds_read_b128 v[104:107], v73 offset:1792\n\t" "s_waitcnt lgkmcnt(4)\n\t" \
    "v_mfma_f32_32x32x16_bf16 v[32:47], a[212:215], v[120:123], v[32:47]\n\t" "ds_read_b128 v[108:111], v74 offset:1792\n\t" "s_waitcnt lgkmcnt(4)\n\t" \
    "v_mfma_f32_32x32x16_bf16 v[32:47], a[216:219], v[124:127], v[32:47]\n\t" "ds_read_b128 v[112:115], v75 offset:1792\n\t" "s_waitcnt lgkmcnt(4)\n\t" \
    "v_mfma_f32_32x32x16_bf16 v[32:47], a[220:223], v[128:131], v[32:47]\n\t" "ds_read_b128 v[116:119], v76 offset:1792\n\t" "s_waitcnt lgkmcnt(4)\n\t" \
    "v_mfma_f32_32x32x16_bf16 v[32:47], a[224:227], v[100:103], v[32:47]\n\t" "ds_read_b128 v[120:123], v77 offset:1792\n\t" "s_waitcnt lgkmcnt(4)\n\t" \
    "v_mfma_f32_32x32x16_bf16 v[32:47], a[228:231], v[104:107], v[32:47]\n\t" "ds_read_b128 v[124:127], v78 offset:1792\n\t" "s_waitcnt lgkmcnt(4)\n\t" \
    "v_mfma_f32_32x32x16_bf16 v[32:47], a[232:235], v[108:111], v[32:47]\n\t" "ds_read_b128 v[128:131], v79 offset:1792\n\t" "s_waitcnt lgkmcnt(4)\n\t" \
    "v_mfma_f32_32x32x16_bf16 v[32:47], a[236:239], v[112:115], v[32:47]\n\t" "s_waitcnt lgkmcnt(3)\n\t" \
    "v_mfma_f32_32x32x16_bf16 v[32:47], a[240:243], v[116:119], v[32:47]\n\t" "s_waitcnt vmcnt(1) lgkmcnt(0)\n\t" "s_barrier\n\t" XT(82, 83) \
    "v_mfma_f32_32x32x16_bf16 v[32:47], a[244:247], v[120:123], v[32:47]\n\t" "ds_read_b128 v[100:103], v80 offset:0\n\t" \
    "v_mfma_f32_32x32x16_bf16 v[32:47], a[248:251], v[124:127], v[32:47]\n\t" "ds_read_b128 v[104:107], v81 offset:0\n\t" "ds_read_b128 v[108:111], v82 offset:0\n\t" \
    "v_mfma_f32_32x32x16_bf16 v[32:47], a[252:255], v[128:131], v[32:47]\n\t" "ds_read_b128 v[112:115], v83 offset:0\n\t" "ds_read_b128 v[116:119], v84 offset:0\n\t" \
    "s_add_u32 s60, s60, 1\n\t" GSUM "s_cmp_eq_u32 s60, 1\n\t" "s_cbranch_scc1 20f\n\t" "11:\n\t" XT(80, 81) "s_waitcnt lgkmcnt(4)\n\t" \
    "v_mfma_f32_32x32x16_bf16 v[48:63], a[0:3], v[100:103], 0\n\t" "ds_read_b128 v[120:123], v85 offset:0\n\t" "s_add_u32 s61, s60, 1\n\t" "s_and_b32 s61, s61, 63\n\t" \
    "s_lshl_b32 s61, s61, 16\n\t" "s_add_u32 s63, %[lw], 0\n\t" "s_waitcnt lgkmcnt(4)\n\t" "v_mfma_f32_32x32x16_bf16 v[48:63], a[4:7], v[104:107], v[48:63]\n\t" \
    "ds_read_b128 v[124:127], v86 offset:0\n\t" "s_waitcnt lgkmcnt(4)\n\t" "v_mfma_f32_32x32x16_bf16 v[48:63], a[8:11], v[108:111], v[48:63]\n\t" \
    "ds_read_b128 v[128:131], v87 offset:0\n\t" "v_cvt_pk_bf16_f32 v64, v32, v33\n\t" "v_cvt_pk_bf16_f32 v65, v34, v35\n\t" "v_cvt_pk_bf16_f32 v66, v36, v37\n\t" \
    "s_waitcnt lgkmcnt(4)\n\t" "v_mfma_f32_32x32x16_bf16 v[48:63], a[12:15], v[112:115], v[48:63]\n\t" "ds_read_b128 v[100:103], v80 offset:256\n\t" \
    "v_cvt_pk_bf16_f32 v67, v38, v39\n\t" "v_cvt_pk_bf16_f32 v68, v40, v41\n\t" "v_cvt_pk_bf16_f32 v69, v42, v43\n\t" "s_waitcnt lgkmcnt(4)\n\t" \
    "v_mfma_f32_32x32x16_bf16 v[48:63], a[16:19], v[116:119], v[48:63]\n\t" "ds_read_b128 v[104:107], v81 offset:256\n\t" "v_cvt_pk_bf16_f32 v70, v44, v45\n\t" \
    "v_cvt_pk_bf16_f32 v71, v46, v47\n\t" "buffer_store_dwordx4 v[64:67], v96, %[rs], s62 offen offset:0 sc1\n\t" "s_waitcnt lgkmcnt(4)\n\t" \
    "v_mfma_f32_32x32x16_bf16 v[48:63], a[20:23], v[120:123], v[48:63]\n\t" "ds_read_b128 v[108:111], v82 offset:256\n\t" "s_waitcnt lgkmcnt(4)\n\t" \
    "v_mfma_f32_32x32x16_bf16 v[48:63], a[24:27], v[124:127], v[48:63]\n\t" "ds_read_b128 v[112:115], v83 offset:256\n\t" \
    "buffer_store_dwordx4 v[68:71], v96, %[rs], s62 offen offset:1024 sc1\n\t" "s_waitcnt lgkmcnt(4)\n\t" \
    "v_mfma_f32_32x32x16_bf16 v[48:63], a[28:31], v[128:131], v[48:63]\n\t" "ds_read_b128 v[116:119], v84 offset:256\n\t" "s_mov_b32 m0, s63\n\t" \
    "s_add_u32 s63, s63, 0x800\n\t" "buffer_load_dwordx4 v88, %[rx], s61 offen lds\n\t" "s_waitcnt lgkmcnt(4)\n\t" \
    "v_mfma_f32_32x32x16_bf16 v[48:63], a[32:35], v[100:103], v[48:63]\n\t" "ds_read_b128 v[120:123], v85 offset:256\n\t" \
    "buffer_load_dwordx4 v88, %[rx], s61 offen offset:1024 lds\n\t" "s_waitcnt lgkmcnt(4)\n\t" "v_mfma_f32_32x32x16_bf16 v[48:63], a[36:39], v[104:107], v[48:63]\n\t" \
    "ds_read_b128 v[124:127], v86 offset:256\n\t" "s_mov_b32 m0, s63\n\t" "s_add_u32 s63, s63, 0x800\n\t" "buffer_load_dwordx4 v89, %[rx], s61 offen lds\n\t" \
    "s_waitcnt lgkmcnt(4)\n\t" "v_mfma_f32_32x32x16_bf16 v[48:63], a[40:43], v[108:111], v[48:63]\n\t" "ds_read_b128 v[128:131], v87 offset:256\n\t" \
    "buffer_load_dwordx4 v89, %[rx], s61 offen offset:1024 lds\n\t" "s_waitcnt lgkmcnt(4)\n\t" "v_mfma_f32_32x32x16_bf16 v[48:63], a[44:47], v[112:115], v[48:63]\n\t" \
    "ds_read_b128 v[100:103], v80 offset:512\n\t" "s_mov_b32 m0, s63\n\t" "s_add_u32 s63, s63, 0x800\n\t" "buffer_load_dwordx4 v90, %[rx], s61 offen lds\n\t" \
    "s_waitcnt lgkmcnt(4)\n\t" "v_mfma_f32_32x32x16_bf16 v[48:63], a[48:51], v[116:119], v[48:63]\n\t" "ds_read_b128 v[104:107], v81 offset:512\n\t" \
    "buffer_load_dwordx4 v90, %[rx], s61 offen offset:1024 lds\n\t" "s_waitcnt lgkmcnt(4)\n\t" "v_mfma_f32_32x32x16_bf16 v[48:63], a[52:55], v[120:123], v[48:63]\n\t" \
    "ds_read_b128 v[108:111], v82 offset:512\n\t" "s_mov_b32 m0, s63\n\t" "s_add_u32 s63, s63, 0x800\n\t" "buffer_load_dwordx4 v91, %[rx], s61 offen lds\n\t" \
    "s_waitcnt lgkmcnt(4)\n\t" "v_mfma_f32_32x32x16_bf16 v[48:63], a[56:59], v[124:127], v[48:63]\n\t" "ds_read_b128 v[112:115], v83 offset:512\n\t" \
    "buffer_load_dwordx4 v91, %[rx], s61 offen offset:1024 lds\n\t" "s_waitcnt lgkmcnt(4)\n\t" "v_mfma_f32_32x32x16_bf16 v[48:63], a[60:63], v[128:131], v[48:63]\n\t" \
    "ds_read_b128 v[116:119], v84 offset:512\n\t" "s_mov_b32 m0, s63\n\t" "s_add_u32 s63, s63, 0x800\n\t" "buffer_load_dwordx4 v92, %[rx], s61 offen lds\n\t" \
    "s_waitcnt lgkmcnt(4)\n\t" "v_mfma_f32_32x32x16_bf16 v[48:63], a[64:67], v[100:103], v[48:63]\n\t" "ds_read_b128 v[120:123], v85 offset:512\n\t" \
    "buffer_load_dwordx4 v92, %[rx], s61 offen offset:1024 lds\n\t" "s_waitcnt lgkmcnt(4)\n\t" "v_mfma_f32_32x32x16_bf16 v[48:63], a[68:71], v[104:107], v[48:63]\n\t" \
    "ds_read_b128 v[124:127], v86 offset:512\n\t" "s_mov_b32 m0, s63\n\t" "s_add_u32 s63, s63, 0x800\n\t" "buffer_load_dwordx4 v93, %[rx], s61 offen lds\n\t" \
    "s_waitcnt lgkmcnt(4)\n\t" "v_mfma_f32_32x32x16_bf16 v[48:63], a[72:75], v[108:111], v[48:63]\n\t" "ds_read_b128 v[128:131], v87 offset:512\n\t" \
    "buffer_load_dwordx4 v93, %[rx], s61 offen offset:1024 lds\n\t" "s_waitcnt lgkmcnt(4)\n\t" "v_mfma_f32_32x32x16_bf16 v[48:63], a[76:79], v[112:115], v[48:63]\n\t" \
    "ds_read_b128 v[100:103], v80 offset:768\n\t" "s_mov_b32 m0, s63\n\t" "s_add_u32 s63, s63, 0x800\n\t" "buffer_load_dwordx4 v94, %[rx], s61 offen lds\n\t" \
    "s_waitcnt lgkmcnt(4)\n\t" "v_mfma_f32_32x32x16_bf16 v[48:63], a[80:83], v[116:119], v[48:63]\n\t" "ds_read_b128 v[104:107], v81 offset:768\n\t" \
    "buffer_load_dwordx4 v94, %[rx], s61 offen offset:1024 lds\n\t" "s_waitcnt lgkmcnt(4)\n\t" "v_mfma_f32_32x32x16_bf16 v[48:63], a[84:87], v[120:123], v[48:63]\n\t" \
    "ds_read_b128 v[108:111], v82 offset:768\n\t" "s_mov_b32 m0, s63\n\t" "s_add_u32 s63, s63, 0x800\n\t" "buffer_load_dwordx4 v95, %[rx], s61 offen lds\n\t" \
    "s_waitcnt lgkmcnt(4)\n\t" "v_mfma_f32_32x32x16_bf16 v[48:63], a[88:91], v[124:127], v[48:63]\n\t" "ds_read_b128 v[112:115], v83 offset:768\n\t" \
    "buffer_load_dwordx4 v95, %[rx], s61 offen offset:1024 lds\n\t" "s_waitcnt lgkmcnt(4)\n\t" "v_mfma_f32_32x32x16_bf16 v[48:63], a[92:95], v[128:131], v[48:63]\n\t" \
    "ds_read_b128 v[116:119], v84 offset:768\n\t" "s_waitcnt lgkmcnt(4)\n\t" "v_mfma_f32_32x32x16_bf16 v[48:63], a[96:99], v[100:103], v[48:63]\n\t" \
    "ds_read_b128 v[120:123], v85 offset:768\n\t" "s_lshr_b32 s64, s60, 2\n\t" "s_xor_b32 s64, s64, %[xp]\n\t" "s_and_b32 s64, s64, 1\n\t" "s_lshl_b32 s64, s64, 13\n\t" \
    "s_waitcnt lgkmcnt(4)\n\t" "v_mfma_f32_32x32x16_bf16 v[48:63], a[100:103], v[104:107], v[48:63]\n\t" "ds_read_b128 v[124:127], v86 offset:768\n\t" \
    "s_lshl_b32 s62, s60, 19\n\t" "s_add_u32 s62, s62, s64\n\t" "s_add_u32 s62, s62, %[ob]\n\t" "s_waitcnt lgkmcnt(4)\n\t" \
    "v_mfma_f32_32x32x16_bf16 v[48:63], a[104:107], v[108:111], v[48:63]\n\t" "ds_read_b128 v[128:131], v87 offset:768\n\t" "s_waitcnt lgkmcnt(4)\n\t" \
    "v_mfma_f32_32x32x16_bf16 v[48:63], a[108:111], v[112:115], v[48:63]\n\t" "ds_read_b128 v[100:103], v80 offset:1024\n\t" "s_waitcnt lgkmcnt(4)\n\t" \
    "v_mfma_f32_32x32x16_bf16 v[48:63], a[112:115], v[116:119], v[48:63]\n\t" "ds_read_b128 v[104:107], v81 offset:1024\n\t" "s_waitcnt lgkmcnt(4)\n\t" \
    "v_mfma_f32_32x32x16_bf16 v[48:63], a[116:119], v[120:123], v[48:63]\n\t" "ds_read_b128 v[108:111], v82 offset:1024\n\t" "s_waitcnt lgkmcnt(4)\n\t" \
    "v_mfma_f32_32x32x16_bf16 v[48:63], a[120:123], v[124:127], v[48:63]\n\t" "ds_read_b128 v[112:115], v83 offset:1024\n\t" "s_waitcnt lgkmcnt(4)\n\t" \
    "v_mfma_f32_32x32x16_bf16 v[48:63], a[124:127], v[128:131], v[48:63]\n\t" "ds_read_b128 v[116:119], v84 offset:1024\n\t" "s_waitcnt lgkmcnt(4)\n\t" \
    "v_mfma_f32_32x32x16_bf16 v[48:63], a[128:131], v[100:103], v[48:63]\n\t" "ds_read_b128 v[120:123], v85 offset:1024\n\t" "s_waitcnt lgkmcnt(4)\n\t" \
    "v_mfma_f32_32x32x16_bf16 v[48:63], a[132:135], v[104:107], v[48:63]\n\t" "ds_read_b128 v[124:127], v86 offset:1024\n\t" "s_waitcnt lgkmcnt(4)\n\t" \
    "v_mfma_f32_32x32x16_bf16 v[48:63], a[136:139], v[108:111], v[48:63]\n\t" "ds_read_b128 v[128:131], v87 offset:1024\n\t" "s_waitcnt vmcnt(16)\n\t" "s_barrier\n\t" \
    "v_mov_b32 v98, s60\n\t" "buffer_store_dword v98, %[fo], %[rc], %[fl] offen sc1\n\t" "s_waitcnt lgkmcnt(4)\n\t" \
    "v_mfma_f32_32x32x16_bf16 v[48:63], a[140:143], v[112:115], v[48:63]\n\t" "ds_read_b128 v[100:103], v80 offset:1280\n\t" "s_waitcnt lgkmcnt(4)\n\t" \
    "v_mfma_f32_32x32x16_bf16 v[48:63], a[144:147], v[116:119], v[48:63]\n\t" "ds_read_b128 v[104:107], v81 offset:1280\n\t" "s_waitcnt lgkmcnt(4)\n\t" \
    "v_mfma_f32_32x32x16_bf16 v[48:63], a[148:151], v[120:123], v[48:63]\n\t" "ds_read_b128 v[108:111], v82 offset:1280\n\t" "s_waitcnt lgkmcnt(4)\n\t" \
    "v_mfma_f32_32x32x16_bf16 v[48:63], a[152:155], v[124:127], v[48:63]\n\t" "ds_read_b128 v[112:115], v83 offset:1280\n\t" "s_waitcnt lgkmcnt(4)\n\t" \
    "v_mfma_f32_32x32x16_bf16 v[48:63], a[156:159], v[128:131], v[48:63]\n\t" "ds_read_b128 v[116:119], v84 offset:1280\n\t" "s_waitcnt lgkmcnt(4)\n\t" \
    "v_mfma_f32_32x32x16_bf16 v[48:63], a[160:163], v[100:103], v[48:63]\n\t" "ds_read_b128 v[120:123], v85 offset:1280\n\t" "s_waitcnt lgkmcnt(4)\n\t" \
    "v_mfma_f32_32x32x16_bf16 v[48:63], a[164:167], v[104:107], v[48:63]\n\t" "ds_read_b128 v[124:127], v86 offset:1280\n\t" "s_waitcnt lgkmcnt(4)\n\t" \
    "v_mfma_f32_32x32x16_bf16 v[48:63], a[168:171], v[108:111], v[48:63]\n\t" "ds_read_b128 v[128:131], v87 offset:1280\n\t" "s_waitcnt lgkmcnt(4)\n\t" \
    "v_mfma_f32_32x32x16_bf16 v[48:63], a[172:175], v[112:115], v[48:63]\n\t" "ds_read_b128 v[100:103], v80 offset:1536\n\t" "s_waitcnt lgkmcnt(4)\n\t" \
    "v_mfma_f32_32x32x16_bf16 v[48:63], a[176:179], v[116:119], v[48:63]\n\t" "ds_read_b128 v[104:107], v81 offset:1536\n\t" "s_waitcnt lgkmcnt(4)\n\t" \
    "v_mfma_f32_32x32x16_bf16 v[48:63], a[180:183], v[120:123], v[48:63]\n\t" "ds_read_b128 v[108:111], v82 offset:1536\n\t" "s_waitcnt lgkmcnt(4)\n\t" \
    "v_mfma_f32_32x32x16_bf16 v[48:63], a[184:187], v[124:127], v[48:63]\n\t" "ds_read_b128 v[112:115], v83 offset:1536\n\t" "s_waitcnt lgkmcnt(4)\n\t" \
    "v_mfma_f32_32x32x16_bf16 v[48:63], a[188:191], v[128:131], v[48:63]\n\t" "ds_read_b128 v[116:119], v84 offset:1536\n\t" "s_waitcnt lgkmcnt(4)\n\t" \
    "v_mfma_f32_32x32x16_bf16 v[48:63], a[192:195], v[100:103], v[48:63]\n\t" "ds_read_b128 v[120:123], v85 offset:1536\n\t" "s_waitcnt lgkmcnt(4)\n\t" \
    "v_mfma_f32_32x32x16_bf16 v[48:63], a[196:199], v[104:107], v[48:63]\n\t" "ds_read_b128 v[124:127], v86 offset:1536\n\t" "s_waitcnt lgkmcnt(4)\n\t" \
    "v_mfma_f32_32x32x16_bf16 v[48:63], a[200:203], v[108:111], v[48:63]\n\t" "ds_read_b128 v[128:131], v87 offset:1536\n\t" "s_waitcnt lgkmcnt(4)\n\t" \
    "v_mfma_f32_32x32x16_bf16 v[48:63], a[204:207], v[112:115], v[48:63]\n\t" "ds_read_b128 v[100:103], v80 offset:1792\n\t" "s_waitcnt lgkmcnt(4)\n\t" \
    "v_mfma_f32_32x32x16_bf16 v[48:63], a[208:211], v[116:119], v[48:63]\n\t" "ds_read_b128 v[104:107], v81 offset:1792\n\t" "s_waitcnt lgkmcnt(4)\n\t" \
    "v_mfma_f32_32x32x16_bf16 v[48:63], a[212:215], v[120:123], v[48:63]\n\t" "ds_read_b128 v[108:111], v82 offset:1792\n\t" "s_waitcnt lgkmcnt(4)\n\t" \
    "v_mfma_f32_32x32x16_bf16 v[48:63], a[216:219], v[124:127], v[48:63]\n\t" "ds_read_b128 v[112:115], v83 offset:1792\n\t" "s_waitcnt lgkmcnt(4)\n\t" \
    "v_mfma_f32_32x32x16_bf16 v[48:63], a[220:223], v[128:131], v[48:63]\n\t" "ds_read_b128 v[116:119], v84 offset:1792\n\t" "s_waitcnt lgkmcnt(4)\n\t" \
    "v_mfma_f32_32x32x16_bf16 v[48:63], a[224:227], v[100:103], v[48:63]\n\t" "ds_read_b128 v[120:123], v85 offset:1792\n\t" "s_waitcnt lgkmcnt(4)\n\t" \
    "v_mfma_f32_32x32x16_bf16 v[48:63], a[228:231], v[104:107], v[48:63]\n\t" "ds_read_b128 v[124:127], v86 offset:1792\n\t" "s_waitcnt lgkmcnt(4)\n\t" \
    "v_mfma_f32_32x32x16_bf16 v[48:63], a[232:235], v[108:111], v[48:63]\n\t" "ds_read_b128 v[128:131], v87 offset:1792\n\t" "s_waitcnt lgkmcnt(4)\n\t" \
    "v_mfma_f32_32x32x16_bf16 v[48:63], a[236:239], v[112:115], v[48:63]\n\t" "s_waitcnt lgkmcnt(3)\n\t" \
    "v_mfma_f32_32x32x16_bf16 v[48:63], a[240:243], v[116:119], v[48:63]\n\t" "s_waitcnt vmcnt(1) lgkmcnt(0)\n\t" "s_barrier\n\t" XT(82, 83) \
    "v_mfma_f32_32x32x16_bf16 v[48:63], a[244:247], v[120:123], v[48:63]\n\t" "ds_read_b128 v[100:103], v72 offset:0\n\t" \
    "v_mfma_f32_32x32x16_bf16 v[48:63], a[248:251], v[124:127], v[48:63]\n\t" "ds_read_b128 v[104:107], v73 offset:0\n\t" "ds_read_b128 v[108:111], v74 offset:0\n\t" \
    "v_mfma_f32_32x32x16_bf16 v[48:63], a[252:255], v[128:131], v[48:63]\n\t" "ds_read_b128 v[112:115], v75 offset:0\n\t" "ds_read_b128 v[116:119], v76 offset:0\n\t" \
    "s_add_u32 s60, s60, 1\n\t" GSUM "s_cmp_lt_u32 s60, 64\n\t" "s_cbranch_scc1 10b\n\t" "s_nop 11\n\t" "v_cvt_pk_bf16_f32 v64, v48, v49\n\t" \
    "v_cvt_pk_bf16_f32 v65, v50, v51\n\t" "v_cvt_pk_bf16_f32 v66, v52, v53\n\t" "v_cvt_pk_bf16_f32 v67, v54, v55\n\t" "v_cvt_pk_bf16_f32 v68, v56, v57\n\t" \
    "v_cvt_pk_bf16_f32 v69, v58, v59\n\t" "v_cvt_pk_bf16_f32 v70, v60, v61\n\t" "v_cvt_pk_bf16_f32 v71, v62, v63\n\t" \
    "buffer_store_dwordx4 v[64:67], v96, %[rs], s62 offen offset:0 sc1\n\t" "buffer_store_dwordx4 v[68:71], v96, %[rs], s62 offen offset:1024 sc1\n\t" \
    "s_waitcnt vmcnt(0)\n\t" "s_barrier\n\t" "v_mov_b32 v98, s60\n\t" "buffer_store_dword v98, %[fo], %[rc], %[fl] offen sc1\n\t" "s_waitcnt lgkmcnt(0)\n\t" \
    "s_branch 30f\n\t" "20:\n\t" "v_mov_b32 v96, %[so]\n\t" "s_nop 11\n\t" "v_cvt_pk_bf16_f32 v64, v32, v33\n\t" "v_cvt_pk_bf16_f32 v65, v34, v35\n\t" \
    "v_cvt_pk_bf16_f32 v66, v36, v37\n\t" "v_cvt_pk_bf16_f32 v67, v38, v39\n\t" "v_cvt_pk_bf16_f32 v68, v40, v41\n\t" "v_cvt_pk_bf16_f32 v69, v42, v43\n\t" \
    "v_cvt_pk_bf16_f32 v70, v44, v45\n\t" "v_cvt_pk_bf16_f32 v71, v46, v47\n\t" "buffer_store_dwordx4 v[64:67], v96, %[rs], s62 offen offset:0 sc1\n\t" \
    "buffer_store_dwordx4 v[68:71], v96, %[rs], s62 offen offset:1024 sc1\n\t" "s_waitcnt vmcnt(0)\n\t" "s_barrier\n\t" "v_mov_b32 v98, s60\n\t" \
    "buffer_store_dword v98, %[fo], %[rc], %[fl] offen sc1\n\t" "s_branch 11b\n\t" "30:\n\t"
#define ZC_LOOP \
    "s_mov_b32 s60, 0\n\t" "s_mov_b32 s61, 0\n\t" "s_mov_b32 s62, 0\n\t" "s_mov_b32 s63, 0x7f000000\n\t" "s_cmp_ge_u32 s61, %[z0]\n\t" "s_cbranch_scc1 52f\n\t" "51:\n\t" \
    "buffer_load_dword v120, %[fo], %[rc], %[fv] offen sc1\n\t" "buffer_load_dword v121, %[fo], %[rc], %[vd] offen sc1\n\t" "s_waitcnt vmcnt(0)\n\t" \
    "v_readfirstlane_b32 s61, v120\n\t" "v_readfirstlane_b32 s66, v121\n\t" "s_nop 3\n\t" "s_cmp_ge_u32 s61, %[z0]\n\t" "s_cbranch_scc1 52f\n\t" "s_bitcmp1_b32 s66, 1\n\t" \
    "s_cbranch_scc1 90f\n\t" "s_cmp_eq_u32 %[bd], 0\n\t" "s_cbranch_scc1 52f\n\t" "s_sub_u32 %[bd], %[bd], 1\n\t" "s_sleep 2\n\t" "s_branch 51b\n\t" "52:\n\t" \
    "s_lshr_b32 s66, s60, 2\n\t" "s_xor_b32 s66, s66, %[xp]\n\t" "s_and_b32 s66, s66, 1\n\t" "s_lshl_b32 s66, s66, 13\n\t" "s_lshl_b32 s64, s60, 19\n\t" \
    "s_add_u32 s64, s64, s66\n\t" "s_add_u32 s64, s64, %[vb]\n\t" "buffer_load_dwordx4 v[16:19], %[ao], %[rs], s64 offen offset:0 sc1\n\t" \
    "buffer_load_dwordx4 v[20:23], %[ao], %[rs], s64 offen offset:1024 sc1\n\t" "buffer_load_dwordx4 v[24:27], %[ao], %[rs], s64 offen offset:2048 sc1\n\t" \
    "buffer_load_dwordx4 v[28:31], %[ao], %[rs], s64 offen offset:3072 sc1\n\t" "buffer_load_dwordx4 v[32:35], %[a1], %[rs], s64 offen offset:0 sc1\n\t" \
    "buffer_load_dwordx4 v[36:39], %[a1], %[rs], s64 offen offset:1024 sc1\n\t" "buffer_load_dwordx4 v[40:43], %[a1], %[rs], s64 offen offset:2048 sc1\n\t" \
    "buffer_load_dwordx4 v[44:47], %[a1], %[rs], s64 offen offset:3072 sc1\n\t" "s_waitcnt vmcnt(0)\n\t" "10:\n\t" XT(80, 81) "s_add_u32 s66, s62, %[rg]\n\t" \
    "s_cmp_lt_u32 s66, s60\n\t" "s_cbranch_scc1 60f\n\t" "63:\n\t" "v_mfma_f32_32x32x16_bf16 v[80:95], v[16:19], a[0:3], 0\n\t" "s_nop 1\n\t" \
    "v_mfma_f32_32x32x16_bf16 v[80:95], v[20:23], a[4:7], v[80:95]\n\t" "s_nop 1\n\t" "v_mfma_f32_32x32x16_bf16 v[80:95], v[24:27], a[8:11], v[80:95]\n\t" "s_nop 1\n\t" \
    "v_mfma_f32_32x32x16_bf16 v[80:95], v[28:31], a[12:15], v[80:95]\n\t" "v_cvt_pk_bf16_f32 v112, v96, v97\n\t" "v_cvt_pk_bf16_f32 v113, v98, v99\n\t" \
    "v_cvt_pk_bf16_f32 v114, v100, v101\n\t" "v_mfma_f32_32x32x16_bf16 v[80:95], v[32:35], a[16:19], v[80:95]\n\t" "v_cvt_pk_bf16_f32 v115, v102, v103\n\t" \
    "v_cvt_pk_bf16_f32 v116, v104, v105\n\t" "v_cvt_pk_bf16_f32 v117, v106, v107\n\t" "v_mfma_f32_32x32x16_bf16 v[80:95], v[36:39], a[20:23], v[80:95]\n\t" \
    "buffer_store_dwordx4 v[112:115], %[zo], %[rs], s63 offen offset:0 sc1\n\t" "v_cvt_pk_bf16_f32 v118, v108, v109\n\t" "v_cvt_pk_bf16_f32 v119, v110, v111\n\t" \
    "v_mfma_f32_32x32x16_bf16 v[80:95], v[40:43], a[24:27], v[80:95]\n\t" "v_mfma_f32_32x32x16_bf16 v[80:95], v[44:47], a[28:31], v[80:95]\n\t" \
    "buffer_store_dwordx4 v[116:119], %[zo], %[rs], s63 offen offset:512 sc1\n\t" "s_and_b32 s63, s60, %[rm]\n\t" "s_lshl_b32 s63, s63, 22\n\t" \
    "s_add_u32 s63, s63, %[zb]\n\t" "v_mfma_f32_32x32x16_bf16 v[96:111], v[16:19], a[32:35], 0\n\t" "buffer_load_dword v120, %[fo], %[rc], %[fv] offen sc1\n\t" "s_nop 0\n\t" \
    "v_mfma_f32_32x32x16_bf16 v[96:111], v[20:23], a[36:39], v[96:111]\n\t" "buffer_load_dword v121, %[fo], %[rc], %[fs] offen sc1\n\t" "s_nop 0\n\t" \
    "v_mfma_f32_32x32x16_bf16 v[96:111], v[24:27], a[40:43], v[96:111]\n\t" "s_nop 1\n\t" "v_mfma_f32_32x32x16_bf16 v[96:111], v[28:31], a[44:47], v[96:111]\n\t" \
    "v_cvt_pk_bf16_f32 v112, v80, v81\n\t" "v_cvt_pk_bf16_f32 v113, v82, v83\n\t" "v_cvt_pk_bf16_f32 v114, v84, v85\n\t" \
    "v_mfma_f32_32x32x16_bf16 v[96:111], v[32:35], a[48:51], v[96:111]\n\t" "v_cvt_pk_bf16_f32 v115, v86, v87\n\t" "v_cvt_pk_bf16_f32 v116, v88, v89\n\t" \
    "v_cvt_pk_bf16_f32 v117, v90, v91\n\t" "v_mfma_f32_32x32x16_bf16 v[96:111], v[36:39], a[52:55], v[96:111]\n\t" \
    "buffer_store_dwordx4 v[112:115], %[zo], %[rs], s63 offen offset:0 sc1\n\t" "v_cvt_pk_bf16_f32 v118, v92, v93\n\t" "v_cvt_pk_bf16_f32 v119, v94, v95\n\t" \
    "v_mfma_f32_32x32x16_bf16 v[96:111], v[40:43], a[56:59], v[96:111]\n\t" "s_add_u32 s65, s60, 1\n\t" "s_lshr_b32 s66, s65, 2\n\t" "s_xor_b32 s66, s66, %[xp]\n\t" \
    "s_and_b32 s66, s66, 1\n\t" "v_mfma_f32_32x32x16_bf16 v[96:111], v[44:47], a[60:63], v[96:111]\n\t" \
    "buffer_store_dwordx4 v[116:119], %[zo], %[rs], s63 offen offset:512 sc1\n\t" "s_add_u32 s63, s63, 0x20000\n\t" \
    "v_mfma_f32_32x32x16_bf16 v[80:95], v[16:19], a[64:67], 0\n\t" "s_nop 1\n\t" "v_mfma_f32_32x32x16_bf16 v[80:95], v[20:23], a[68:71], v[80:95]\n\t" "s_nop 1\n\t" \
    "v_mfma_f32_32x32x16_bf16 v[80:95], v[24:27], a[72:75], v[80:95]\n\t" "s_nop 1\n\t" "v_mfma_f32_32x32x16_bf16 v[80:95], v[28:31], a[76:79], v[80:95]\n\t" \
    "v_cvt_pk_bf16_f32 v112, v96, v97\n\t" "v_cvt_pk_bf16_f32 v113, v98, v99\n\t" "v_cvt_pk_bf16_f32 v114, v100, v101\n\t" \
    "v_mfma_f32_32x32x16_bf16 v[80:95], v[32:35], a[80:83], v[80:95]\n\t" "v_cvt_pk_bf16_f32 v115, v102, v103\n\t" "v_cvt_pk_bf16_f32 v116, v104, v105\n\t" \
    "v_cvt_pk_bf16_f32 v117, v106, v107\n\t" "v_mfma_f32_32x32x16_bf16 v[80:95], v[36:39], a[84:87], v[80:95]\n\t" \
    "buffer_store_dwordx4 v[112:115], %[zo], %[rs], s63 offen offset:0 sc1\n\t" "v_cvt_pk_bf16_f32 v118, v108, v109\n\t" "v_cvt_pk_bf16_f32 v119, v110, v111\n\t" \
    "v_mfma_f32_32x32x16_bf16 v[80:95], v[40:43], a[88:91], v[80:95]\n\t" "s_lshl_b32 s66, s66, 13\n\t" "s_lshl_b32 s64, s65, 19\n\t" "s_add_u32 s64, s64, s66\n\t" \
    "s_add_u32 s64, s64, %[vb]\n\t" "v_mfma_f32_32x32x16_bf16 v[80:95], v[44:47], a[92:95], v[80:95]\n\t" \
    "buffer_store_dwordx4 v[116:119], %[zo], %[rs], s63 offen offset:512 sc1\n\t" "s_add_u32 s63, s63, 0x20000\n\t" \
    "v_mfma_f32_32x32x16_bf16 v[96:111], v[16:19], a[96:99], 0\n\t" "s_nop 1\n\t" "v_mfma_f32_32x32x16_bf16 v[96:111], v[20:23], a[100:103], v[96:111]\n\t" "s_nop 1\n\t" \
    "v_mfma_f32_32x32x16_bf16 v[96:111], v[24:27], a[104:107], v[96:111]\n\t" "s_nop 1\n\t" "v_mfma_f32_32x32x16_bf16 v[96:111], v[28:31], a[108:111], v[96:111]\n\t" \
    "v_cvt_pk_bf16_f32 v112, v80, v81\n\t" "v_cvt_pk_bf16_f32 v113, v82, v83\n\t" "v_cvt_pk_bf16_f32 v114, v84, v85\n\t" \
    "v_mfma_f32_32x32x16_bf16 v[96:111], v[32:35], a[112:115], v[96:111]\n\t" "v_cvt_pk_bf16_f32 v115, v86, v87\n\t" "v_cvt_pk_bf16_f32 v116, v88, v89\n\t" \
    "v_cvt_pk_bf16_f32 v117, v90, v91\n\t" "v_mfma_f32_32x32x16_bf16 v[96:111], v[36:39], a[116:119], v[96:111]\n\t" \
    "buffer_store_dwordx4 v[112:115], %[zo], %[rs], s63 offen offset:0 sc1\n\t" "v_cvt_pk_bf16_f32 v118, v92, v93\n\t" "v_cvt_pk_bf16_f32 v119, v94, v95\n\t" \
    "v_mfma_f32_32x32x16_bf16 v[96:111], v[40:43], a[120:123], v[96:111]\n\t" "s_waitcnt vmcnt(5)\n\t" "v_readfirstlane_b32 s66, v120\n\t" "v_readfirstlane_b32 s67, v121\n\t" \
    "v_mov_b32 v122, s60\n\t" "buffer_store_dword v122, %[fo], %[rc], %[fz] offen sc1\n\t" "v_mfma_f32_32x32x16_bf16 v[96:111], v[44:47], a[124:127], v[96:111]\n\t" \
    "buffer_store_dwordx4 v[116:119], %[zo], %[rs], s63 offen offset:512 sc1\n\t" "s_add_u32 s63, s63, 0x20000\n\t" \
    "v_mfma_f32_32x32x16_bf16 v[80:95], v[16:19], a[128:131], 0\n\t" "s_max_u32 s61, s61, s66\n\t" "s_max_u32 s62, s62, s67\n\t" "s_cmp_gt_u32 s61, s65\n\t" \
    "s_cselect_b32 s65, 1, 0\n\t" "s_cselect_b32 s64, s64, 0x7f000000\n\t" "v_mfma_f32_32x32x16_bf16 v[80:95], v[20:23], a[132:135], v[80:95]\n\t" \
    "buffer_load_dwordx4 v[48:51], %[ao], %[rs], s64 offen offset:0 sc1\n\t" "s_nop 0\n\t" "v_mfma_f32_32x32x16_bf16 v[80:95], v[24:27], a[136:139], v[80:95]\n\t" \
    "buffer_load_dwordx4 v[52:55], %[ao], %[rs], s64 offen offset:1024 sc1\n\t" "s_nop 0\n\t" "v_mfma_f32_32x32x16_bf16 v[80:95], v[28:31], a[140:143], v[80:95]\n\t" \
    "buffer_load_dwordx4 v[56:59], %[ao], %[rs], s64 offen offset:2048 sc1\n\t" "v_cvt_pk_bf16_f32 v112, v96, v97\n\t" "v_cvt_pk_bf16_f32 v113, v98, v99\n\t" \
    "v_cvt_pk_bf16_f32 v114, v100, v101\n\t" "v_mfma_f32_32x32x16_bf16 v[80:95], v[32:35], a[144:147], v[80:95]\n\t" \
    "buffer_load_dwordx4 v[60:63], %[ao], %[rs], s64 offen offset:3072 sc1\n\t" "v_cvt_pk_bf16_f32 v115, v102, v103\n\t" "v_cvt_pk_bf16_f32 v116, v104, v105\n\t" \
    "v_cvt_pk_bf16_f32 v117, v106, v107\n\t" "v_mfma_f32_32x32x16_bf16 v[80:95], v[36:39], a[148:151], v[80:95]\n\t" \
    "buffer_load_dwordx4 v[64:67], %[a1], %[rs], s64 offen offset:0 sc1\n\t" "buffer_store_dwordx4 v[112:115], %[zo], %[rs], s63 offen offset:0 sc1\n\t" \
    "v_cvt_pk_bf16_f32 v118, v108, v109\n\t" "v_cvt_pk_bf16_f32 v119, v110, v111\n\t" "v_mfma_f32_32x32x16_bf16 v[80:95], v[40:43], a[152:155], v[80:95]\n\t" \
    "buffer_load_dwordx4 v[68:71], %[a1], %[rs], s64 offen offset:1024 sc1\n\t" "v_mfma_f32_32x32x16_bf16 v[80:95], v[44:47], a[156:159], v[80:95]\n\t" \
    "buffer_load_dwordx4 v[72:75], %[a1], %[rs], s64 offen offset:2048 sc1\n\t" "buffer_store_dwordx4 v[116:119], %[zo], %[rs], s63 offen offset:512 sc1\n\t" \
    "s_add_u32 s63, s63, 0x20000\n\t" "v_mfma_f32_32x32x16_bf16 v[96:111], v[16:19], a[160:163], 0\n\t" \
    "buffer_load_dwordx4 v[76:79], %[a1], %[rs], s64 offen offset:3072 sc1\n\t" "s_nop 0\n\t" "v_mfma_f32_32x32x16_bf16 v[96:111], v[20:23], a[164:167], v[96:111]\n\t" \
    "s_nop 1\n\t" "v_mfma_f32_32x32x16_bf16 v[96:111], v[24:27], a[168:171], v[96:111]\n\t" "s_nop 1\n\t" \
    "v_mfma_f32_32x32x16_bf16 v[96:111], v[28:31], a[172:175], v[96:111]\n\t" "v_cvt_pk_bf16_f32 v112, v80, v81\n\t" "v_cvt_pk_bf16_f32 v113, v82, v83\n\t" \
    "v_cvt_pk_bf16_f32 v114, v84, v85\n\t" "v_mfma_f32_32x32x16_bf16 v[96:111], v[32:35], a[176:179], v[96:111]\n\t" "v_cvt_pk_bf16_f32 v115, v86, v87\n\t" \
    "v_cvt_pk_bf16_f32 v116, v88, v89\n\t" "v_cvt_pk_bf16_f32 v117, v90, v91\n\t" "v_mfma_f32_32x32x16_bf16 v[96:111], v[36:39], a[180:183], v[96:111]\n\t" \
    "buffer_store_dwordx4 v[112:115], %[zo], %[rs], s63 offen offset:0 sc1\n\t" "v_cvt_pk_bf16_f32 v118, v92, v93\n\t" "v_cvt_pk_bf16_f32 v119, v94, v95\n\t" \
    "v_mfma_f32_32x32x16_bf16 v[96:111], v[40:43], a[184:187], v[96:111]\n\t" "v_mfma_f32_32x32x16_bf16 v[96:111], v[44:47], a[188:191], v[96:111]\n\t" \
    "buffer_store_dwordx4 v[116:119], %[zo], %[rs], s63 offen offset:512 sc1\n\t" "s_add_u32 s63, s63, 0x20000\n\t" \
    "v_mfma_f32_32x32x16_bf16 v[80:95], v[16:19], a[192:195], 0\n\t" "s_nop 1\n\t" "v_mfma_f32_32x32x16_bf16 v[80:95], v[20:23], a[196:199], v[80:95]\n\t" "s_nop 1\n\t" \
    "v_mfma_f32_32x32x16_bf16 v[80:95], v[24:27], a[200:203], v[80:95]\n\t" "s_nop 1\n\t" "v_mfma_f32_32x32x16_bf16 v[80:95], v[28:31], a[204:207], v[80:95]\n\t" \
    "v_cvt_pk_bf16_f32 v112, v96, v97\n\t" "v_cvt_pk_bf16_f32 v113, v98, v99\n\t" "v_cvt_pk_bf16_f32 v114, v100, v101\n\t" \
    "v_mfma_f32_32x32x16_bf16 v[80:95], v[32:35], a[208:211], v[80:95]\n\t" "v_cvt_pk_bf16_f32 v115, v102, v103\n\t" "v_cvt_pk_bf16_f32 v116, v104, v105\n\t" \
    "v_cvt_pk_bf16_f32 v117, v106, v107\n\t" "v_mfma_f32_32x32x16_bf16 v[80:95], v[36:39], a[212:215], v[80:95]\n\t" \
    "buffer_store_dwordx4 v[112:115], %[zo], %[rs], s63 offen offset:0 sc1\n\t" "v_cvt_pk_bf16_f32 v118, v108, v109\n\t" "v_cvt_pk_bf16_f32 v119, v110, v111\n\t" \
    "v_mfma_f32_32x32x16_bf16 v[80:95], v[40:43], a[216:219], v[80:95]\n\t" "v_mfma_f32_32x32x16_bf16 v[80:95], v[44:47], a[220:223], v[80:95]\n\t" \
    "buffer_store_dwordx4 v[116:119], %[zo], %[rs], s63 offen offset:512 sc1\n\t" "s_add_u32 s63, s63, 0x20000\n\t" \
    "v_mfma_f32_32x32x16_bf16 v[96:111], v[16:19], a[224:227], 0\n\t" "s_nop 1\n\t" "v_mfma_f32_32x32x16_bf16 v[96:111], v[20:23], a[228:231], v[96:111]\n\t" "s_nop 1\n\t" \
    "v_mfma_f32_32x32x16_bf16 v[96:111], v[24:27], a[232:235], v[96:111]\n\t" "s_nop 1\n\t" "v_mfma_f32_32x32x16_bf16 v[96:111], v[28:31], a[236:239], v[96:111]\n\t" \
    "v_cvt_pk_bf16_f32 v112, v80, v81\n\t" "v_cvt_pk_bf16_f32 v113, v82, v83\n\t" "v_cvt_pk_bf16_f32 v114, v84, v85\n\t" \
    "v_mfma_f32_32x32x16_bf16 v[96:111], v[32:35], a[240:243], v[96:111]\n\t" "v_cvt_pk_bf16_f32 v115, v86, v87\n\t" "v_cvt_pk_bf16_f32 v116, v88, v89\n\t" \
    "v_cvt_pk_bf16_f32 v117, v90, v91\n\t" "v_mfma_f32_32x32x16_bf16 v[96:111], v[36:39], a[244:247], v[96:111]\n\t" \
    "buffer_store_dwordx4 v[112:115], %[zo], %[rs], s63 offen offset:0 sc1\n\t" "v_cvt_pk_bf16_f32 v118, v92, v93\n\t" "v_cvt_pk_bf16_f32 v119, v94, v95\n\t" \
    "v_mfma_f32_32x32x16_bf16 v[96:111], v[40:43], a[248:251], v[96:111]\n\t" "v_mfma_f32_32x32x16_bf16 v[96:111], v[44:47], a[252:255], v[96:111]\n\t" \
    "buffer_store_dwordx4 v[116:119], %[zo], %[rs], s63 offen offset:512 sc1\n\t" "s_add_u32 s63, s63, 0x20000\n\t" "s_add_u32 s60, s60, 1\n\t" "s_cmp_eq_u32 s65, 0\n\t" \
    "s_waitcnt vmcnt(6)\n\t" ZSUM "s_cbranch_scc1 20f\n\t" "23:\n\t" XT(80, 81) "s_add_u32 s66, s62, %[rg]\n\t" "s_cmp_lt_u32 s66, s60\n\t" "s_cbranch_scc1 70f\n\t" "73:\n\t" \
    "v_mfma_f32_32x32x16_bf16 v[80:95], v[48:51], a[0:3], 0\n\t" "s_nop 1\n\t" "v_mfma_f32_32x32x16_bf16 v[80:95], v[52:55], a[4:7], v[80:95]\n\t" "s_nop 1\n\t" \
    "v_mfma_f32_32x32x16_bf16 v[80:95], v[56:59], a[8:11], v[80:95]\n\t" "s_nop 1\n\t" "v_mfma_f32_32x32x16_bf16 v[80:95], v[60:63], a[12:15], v[80:95]\n\t" \
    "v_cvt_pk_bf16_f32 v112, v96, v97\n\t" "v_cvt_pk_bf16_f32 v113, v98, v99\n\t" "v_cvt_pk_bf16_f32 v114, v100, v101\n\t" \
    "v_mfma_f32_32x32x16_bf16 v[80:95], v[64:67], a[16:19], v[80:95]\n\t" "v_cvt_pk_bf16_f32 v115, v102, v103\n\t" "v_cvt_pk_bf16_f32 v116, v104, v105\n\t" \
    "v_cvt_pk_bf16_f32 v117, v106, v107\n\t" "v_mfma_f32_32x32x16_bf16 v[80:95], v[68:71], a[20:23], v[80:95]\n\t" \
    "buffer_store_dwordx4 v[112:115], %[zo], %[rs], s63 offen offset:0 sc1\n\t" "v_cvt_pk_bf16_f32 v118, v108, v109\n\t" "v_cvt_pk_bf16_f32 v119, v110, v111\n\t" \
    "v_mfma_f32_32x32x16_bf16 v[80:95], v[72:75], a[24:27], v[80:95]\n\t" "v_mfma_f32_32x32x16_bf16 v[80:95], v[76:79], a[28:31], v[80:95]\n\t" \
    "buffer_store_dwordx4 v[116:119], %[zo], %[rs], s63 offen offset:512 sc1\n\t" "s_and_b32 s63, s60, %[rm]\n\t" "s_lshl_b32 s63, s63, 22\n\t" \
    "s_add_u32 s63, s63, %[zb]\n\t" "v_mfma_f32_32x32x16_bf16 v[96:111], v[48:51], a[32:35], 0\n\t" "buffer_load_dword v120, %[fo], %[rc], %[fv] offen sc1\n\t" "s_nop 0\n\t" \
    "v_mfma_f32_32x32x16_bf16 v[96:111], v[52:55], a[36:39], v[96:111]\n\t" "buffer_load_dword v121, %[fo], %[rc], %[fs] offen sc1\n\t" "s_nop 0\n\t" \
    "v_mfma_f32_32x32x16_bf16 v[96:111], v[56:59], a[40:43], v[96:111]\n\t" "s_nop 1\n\t" "v_mfma_f32_32x32x16_bf16 v[96:111], v[60:63], a[44:47], v[96:111]\n\t" \
    "v_cvt_pk_bf16_f32 v112, v80, v81\n\t" "v_cvt_pk_bf16_f32 v113, v82, v83\n\t" "v_cvt_pk_bf16_f32 v114, v84, v85\n\t" \
    "v_mfma_f32_32x32x16_bf16 v[96:111], v[64:67], a[48:51], v[96:111]\n\t" "v_cvt_pk_bf16_f32 v115, v86, v87\n\t" "v_cvt_pk_bf16_f32 v116, v88, v89\n\t" \
    "v_cvt_pk_bf16_f32 v117, v90, v91\n\t" "v_mfma_f32_32x32x16_bf16 v[96:111], v[68:71], a[52:55], v[96:111]\n\t" \
    "buffer_store_dwordx4 v[112:115], %[zo], %[rs], s63 offen offset:0 sc1\n\t" "v_cvt_pk_bf16_f32 v118, v92, v93\n\t" "v_cvt_pk_bf16_f32 v119, v94, v95\n\t" \
    "v_mfma_f32_32x32x16_bf16 v[96:111], v[72:75], a[56:59], v[96:111]\n\t" "s_add_u32 s65, s60, 1\n\t" "s_lshr_b32 s66, s65, 2\n\t" "s_xor_b32 s66, s66, %[xp]\n\t" \
    "s_and_b32 s66, s66, 1\n\t" "v_mfma_f32_32x32x16_bf16 v[96:111], v[76:79], a[60:63], v[96:111]\n\t" \
    "buffer_store_dwordx4 v[116:119], %[zo], %[rs], s63 offen offset:512 sc1\n\t" "s_add_u32 s63, s63, 0x20000\n\t" \
    "v_mfma_f32_32x32x16_bf16 v[80:95], v[48:51], a[64:67], 0\n\t" "s_nop 1\n\t" "v_mfma_f32_32x32x16_bf16 v[80:95], v[52:55], a[68:71], v[80:95]\n\t" "s_nop 1\n\t" \
    "v_mfma_f32_32x32x16_bf16 v[80:95], v[56:59], a[72:75], v[80:95]\n\t" "s_nop 1\n\t" "v_mfma_f32_32x32x16_bf16 v[80:95], v[60:63], a[76:79], v[80:95]\n\t" \
    "v_cvt_pk_bf16_f32 v112, v96, v97\n\t" "v_cvt_pk_bf16_f32 v113, v98, v99\n\t" "v_cvt_pk_bf16_f32 v114, v100, v101\n\t" \
    "v_mfma_f32_32x32x16_bf16 v[80:95], v[64:67], a[80:83], v[80:95]\n\t" "v_cvt_pk_bf16_f32 v115, v102, v103\n\t" "v_cvt_pk_bf16_f32 v116, v104, v105\n\t" \
    "v_cvt_pk_bf16_f32 v117, v106, v107\n\t" "v_mfma_f32_32x32x16_bf16 v[80:95], v[68:71], a[84:87], v[80:95]\n\t" \
    "buffer_store_dwordx4 v[112:115], %[zo], %[rs], s63 offen offset:0 sc1\n\t" "v_cvt_pk_bf16_f32 v118, v108, v109\n\t" "v_cvt_pk_bf16_f32 v119, v110, v111\n\t" \
    "v_mfma_f32_32x32x16_bf16 v[80:95], v[72:75], a[88:91], v[80:95]\n\t" "s_lshl_b32 s66, s66, 13\n\t" "s_lshl_b32 s64, s65, 19\n\t" "s_add_u32 s64, s64, s66\n\t" \
    "s_add_u32 s64, s64, %[vb]\n\t" "v_mfma_f32_32x32x16_bf16 v[80:95], v[76:79], a[92:95], v[80:95]\n\t" \
    "buffer_store_dwordx4 v[116:119], %[zo], %[rs], s63 offen offset:512 sc1\n\t" "s_add_u32 s63, s63, 0x20000\n\t" \
    "v_mfma_f32_32x32x16_bf16 v[96:111], v[48:51], a[96:99], 0\n\t" "s_nop 1\n\t" "v_mfma_f32_32x32x16_bf16 v[96:111], v[52:55], a[100:103], v[96:111]\n\t" "s_nop 1\n\t" \
    "v_mfma_f32_32x32x16_bf16 v[96:111], v[56:59], a[104:107], v[96:111]\n\t" "s_nop 1\n\t" "v_mfma_f32_32x32x16_bf16 v[96:111], v[60:63], a[108:111], v[96:111]\n\t" \
    "v_cvt_pk_bf16_f32 v112, v80, v81\n\t" "v_cvt_pk_bf16_f32 v113, v82, v83\n\t" "v_cvt_pk_bf16_f32 v114, v84, v85\n\t" \
    "v_mfma_f32_32x32x16_bf16 v[96:111], v[64:67], a[112:115], v[96:111]\n\t" "v_cvt_pk_bf16_f32 v115, v86, v87\n\t" "v_cvt_pk_bf16_f32 v116, v88, v89\n\t" \
    "v_cvt_pk_bf16_f32 v117, v90, v91\n\t" "v_mfma_f32_32x32x16_bf16 v[96:111], v[68:71], a[116:119], v[96:111]\n\t" \
    "buffer_store_dwordx4 v[112:115], %[zo], %[rs], s63 offen offset:0 sc1\n\t" "v_cvt_pk_bf16_f32 v118, v92, v93\n\t" "v_cvt_pk_bf16_f32 v119, v94, v95\n\t" \
    "v_mfma_f32_32x32x16_bf16 v[96:111], v[72:75], a[120:123], v[96:111]\n\t" "s_waitcnt vmcnt(5)\n\t" "v_readfirstlane_b32 s66, v120\n\t" "v_readfirstlane_b32 s67, v121\n\t" \
    "v_mov_b32 v122, s60\n\t" "buffer_store_dword v122, %[fo], %[rc], %[fz] offen sc1\n\t" "v_mfma_f32_32x32x16_bf16 v[96:111], v[76:79], a[124:127], v[96:111]\n\t" \
    "buffer_store_dwordx4 v[116:119], %[zo], %[rs], s63 offen offset:512 sc1\n\t" "s_add_u32 s63, s63, 0x20000\n\t" \
    "v_mfma_f32_32x32x16_bf16 v[80:95], v[48:51], a[128:131], 0\n\t" "s_max_u32 s61, s61, s66\n\t" "s_max_u32 s62, s62, s67\n\t" "s_cmp_gt_u32 s61, s65\n\t" \
    "s_cselect_b32 s65, 1, 0\n\t" "s_cselect_b32 s64, s64, 0x7f000000\n\t" "v_mfma_f32_32x32x16_bf16 v[80:95], v[52:55], a[132:135], v[80:95]\n\t" \
    "buffer_load_dwordx4 v[16:19], %[ao], %[rs], s64 offen offset:0 sc1\n\t" "s_nop 0\n\t" "v_mfma_f32_32x32x16_bf16 v[80:95], v[56:59], a[136:139], v[80:95]\n\t" \
    "buffer_load_dwordx4 v[20:23], %[ao], %[rs], s64 offen offset:1024 sc1\n\t" "s_nop 0\n\t" "v_mfma_f32_32x32x16_bf16 v[80:95], v[60:63], a[140:143], v[80:95]\n\t" \
    "buffer_load_dwordx4 v[24:27], %[ao], %[rs], s64 offen offset:2048 sc1\n\t" "v_cvt_pk_bf16_f32 v112, v96, v97\n\t" "v_cvt_pk_bf16_f32 v113, v98, v99\n\t" \
    "v_cvt_pk_bf16_f32 v114, v100, v101\n\t" "v_mfma_f32_32x32x16_bf16 v[80:95], v[64:67], a[144:147], v[80:95]\n\t" \
    "buffer_load_dwordx4 v[28:31], %[ao], %[rs], s64 offen offset:3072 sc1\n\t" "v_cvt_pk_bf16_f32 v115, v102, v103\n\t" "v_cvt_pk_bf16_f32 v116, v104, v105\n\t" \
    "v_cvt_pk_bf16_f32 v117, v106, v107\n\t" "v_mfma_f32_32x32x16_bf16 v[80:95], v[68:71], a[148:151], v[80:95]\n\t" \
    "buffer_load_dwordx4 v[32:35], %[a1], %[rs], s64 offen offset:0 sc1\n\t" "buffer_store_dwordx4 v[112:115], %[zo], %[rs], s63 offen offset:0 sc1\n\t" \
    "v_cvt_pk_bf16_f32 v118, v108, v109\n\t" "v_cvt_pk_bf16_f32 v119, v110, v111\n\t" "v_mfma_f32_32x32x16_bf16 v[80:95], v[72:75], a[152:155], v[80:95]\n\t" \
    "buffer_load_dwordx4 v[36:39], %[a1], %[rs], s64 offen offset:1024 sc1\n\t" "v_mfma_f32_32x32x16_bf16 v[80:95], v[76:79], a[156:159], v[80:95]\n\t" \
    "buffer_load_dwordx4 v[40:43], %[a1], %[rs], s64 offen offset:2048 sc1\n\t" "buffer_store_dwordx4 v[116:119], %[zo], %[rs], s63 offen offset:512 sc1\n\t" \
    "s_add_u32 s63, s63, 0x20000\n\t" "v_mfma_f32_32x32x16_bf16 v[96:111], v[48:51], a[160:163], 0\n\t" \
    "buffer_load_dwordx4 v[44:47], %[a1], %[rs], s64 offen offset:3072 sc1\n\t" "s_nop 0\n\t" "v_mfma_f32_32x32x16_bf16 v[96:111], v[52:55], a[164:167], v[96:111]\n\t" \
    "s_nop 1\n\t" "v_mfma_f32_32x32x16_bf16 v[96:111], v[56:59], a[168:171], v[96:111]\n\t" "s_nop 1\n\t" \
    "v_mfma_f32_32x32x16_bf16 v[96:111], v[60:63], a[172:175], v[96:111]\n\t" "v_cvt_pk_bf16_f32 v112, v80, v81\n\t" "v_cvt_pk_bf16_f32 v113, v82, v83\n\t" \
    "v_cvt_pk_bf16_f32 v114, v84, v85\n\t" "v_mfma_f32_32x32x16_bf16 v[96:111], v[64:67], a[176:179], v[96:111]\n\t" "v_cvt_pk_bf16_f32 v115, v86, v87\n\t" \
    "v_cvt_pk_bf16_f32 v116, v88, v89\n\t" "v_cvt_pk_bf16_f32 v117, v90, v91\n\t" "v_mfma_f32_32x32x16_bf16 v[96:111], v[68:71], a[180:183], v[96:111]\n\t" \
    "buffer_store_dwordx4 v[112:115], %[zo], %[rs], s63 offen offset:0 sc1\n\t" "v_cvt_pk_bf16_f32 v118, v92, v93\n\t" "v_cvt_pk_bf16_f32 v119, v94, v95\n\t" \
    "v_mfma_f32_32x32x16_bf16 v[96:111], v[72:75], a[184:187], v[96:111]\n\t" "v_mfma_f32_32x32x16_bf16 v[96:111], v[76:79], a[188:191], v[96:111]\n\t" \
    "buffer_store_dwordx4 v[116:119], %[zo], %[rs], s63 offen offset:512 sc1\n\t" "s_add_u32 s63, s63, 0x20000\n\t" \
    "v_mfma_f32_32x32x16_bf16 v[80:95], v[48:51], a[192:195], 0\n\t" "s_nop 1\n\t" "v_mfma_f32_32x32x16_bf16 v[80:95], v[52:55], a[196:199], v[80:95]\n\t" "s_nop 1\n\t" \
    "v_mfma_f32_32x32x16_bf16 v[80:95], v[56:59], a[200:203], v[80:95]\n\t" "s_nop 1\n\t" "v_mfma_f32_32x32x16_bf16 v[80:95], v[60:63], a[204:207], v[80:95]\n\t" \
    "v_cvt_pk_bf16_f32 v112, v96, v97\n\t" "v_cvt_pk_bf16_f32 v113, v98, v99\n\t" "v_cvt_pk_bf16_f32 v114, v100, v101\n\t" \
    "v_mfma_f32_32x32x16_bf16 v[80:95], v[64:67], a[208:211], v[80:95]\n\t" "v_cvt_pk_bf16_f32 v115, v102, v103\n\t" "v_cvt_pk_bf16_f32 v116, v104, v105\n\t" \
    "v_cvt_pk_bf16_f32 v117, v106, v107\n\t" "v_mfma_f32_32x32x16_bf16 v[80:95], v[68:71], a[212:215], v[80:95]\n\t" \
    "buffer_store_dwordx4 v[112:115], %[zo], %[rs], s63 offen offset:0 sc1\n\t" "v_cvt_pk_bf16_f32 v118, v108, v109\n\t" "v_cvt_pk_bf16_f32 v119, v110, v111\n\t" \
    "v_mfma_f32_32x32x16_bf16 v[80:95], v[72:75], a[216:219], v[80:95]\n\t" "v_mfma_f32_32x32x16_bf16 v[80:95], v[76:79], a[220:223], v[80:95]\n\t" \
    "buffer_store_dwordx4 v[116:119], %[zo], %[rs], s63 offen offset:512 sc1\n\t" "s_add_u32 s63, s63, 0x20000\n\t" \
    "v_mfma_f32_32x32x16_bf16 v[96:111], v[48:51], a[224:227], 0\n\t" "s_nop 1\n\t" "v_mfma_f32_32x32x16_bf16 v[96:111], v[52:55], a[228:231], v[96:111]\n\t" "s_nop 1\n\t" \
    "v_mfma_f32_32x32x16_bf16 v[96:111], v[56:59], a[232:235], v[96:111]\n\t" "s_nop 1\n\t" "v_mfma_f32_32x32x16_bf16 v[96:111], v[60:63], a[236:239], v[96:111]\n\t" \
    "v_cvt_pk_bf16_f32 v112, v80, v81\n\t" "v_cvt_pk_bf16_f32 v113, v82, v83\n\t" "v_cvt_pk_bf16_f32 v114, v84, v85\n\t" \
    "v_mfma_f32_32x32x16_bf16 v[96:111], v[64:67], a[240:243], v[96:111]\n\t" "v_cvt_pk_bf16_f32 v115, v86, v87\n\t" "v_cvt_pk_bf16_f32 v116, v88, v89\n\t" \
    "v_cvt_pk_bf16_f32 v117, v90, v91\n\t" "v_mfma_f32_32x32x16_bf16 v[96:111], v[68:71], a[244:247], v[96:111]\n\t" \
    "buffer_store_dwordx4 v[112:115], %[zo], %[rs], s63 offen offset:0 sc1\n\t" "v_cvt_pk_bf16_f32 v118, v92, v93\n\t" "v_cvt_pk_bf16_f32 v119, v94, v95\n\t" \
    "v_mfma_f32_32x32x16_bf16 v[96:111], v[72:75], a[248:251], v[96:111]\n\t" "v_mfma_f32_32x32x16_bf16 v[96:111], v[76:79], a[252:255], v[96:111]\n\t" \
    "buffer_store_dwordx4 v[116:119], %[zo], %[rs], s63 offen offset:512 sc1\n\t" "s_add_u32 s63, s63, 0x20000\n\t" "s_add_u32 s60, s60, 1\n\t" "s_cmp_eq_u32 s65, 0\n\t" \
    "s_waitcnt vmcnt(6)\n\t" ZSUM "s_cbranch_scc1 30f\n\t" "33:\n\t" "s_branch 10b\n\t" "20:\n\t" "s_nop 7\n\t" "v_cvt_pk_bf16_f32 v112, v96, v97\n\t" \
    "v_cvt_pk_bf16_f32 v113, v98, v99\n\t" "v_cvt_pk_bf16_f32 v114, v100, v101\n\t" "v_cvt_pk_bf16_f32 v115, v102, v103\n\t" "v_cvt_pk_bf16_f32 v116, v104, v105\n\t" \
    "v_cvt_pk_bf16_f32 v117, v106, v107\n\t" "v_cvt_pk_bf16_f32 v118, v108, v109\n\t" "v_cvt_pk_bf16_f32 v119, v110, v111\n\t" \
    "buffer_store_dwordx4 v[112:115], %[zo], %[rs], s63 offen offset:0 sc1\n\t" "buffer_store_dwordx4 v[116:119], %[zo], %[rs], s63 offen offset:512 sc1\n\t" \
    "buffer_load_dword v120, %[fo], %[rc], %[fv] offen sc1\n\t" "s_waitcnt vmcnt(0)\n\t" "v_readfirstlane_b32 s61, v120\n\t" "s_lshr_b32 s66, s60, 2\n\t" \
    "s_xor_b32 s66, s66, %[xp]\n\t" "s_and_b32 s66, s66, 1\n\t" "s_lshl_b32 s66, s66, 13\n\t" "s_lshl_b32 s64, s60, 19\n\t" "s_add_u32 s64, s64, s66\n\t" \
    "s_add_u32 s64, s64, %[vb]\n\t" "s_cmp_gt_u32 s61, s60\n\t" "s_cbranch_scc0 24f\n\t" "s_cmp_eq_u32 s60, 64\n\t" "s_cbranch_scc1 24f\n\t" \
    "buffer_load_dwordx4 v[48:51], %[ao], %[rs], s64 offen offset:0 sc1\n\t" "buffer_load_dwordx4 v[52:55], %[ao], %[rs], s64 offen offset:1024 sc1\n\t" \
    "buffer_load_dwordx4 v[56:59], %[ao], %[rs], s64 offen offset:2048 sc1\n\t" "buffer_load_dwordx4 v[60:63], %[ao], %[rs], s64 offen offset:3072 sc1\n\t" \
    "buffer_load_dwordx4 v[64:67], %[a1], %[rs], s64 offen offset:0 sc1\n\t" "buffer_load_dwordx4 v[68:71], %[a1], %[rs], s64 offen offset:1024 sc1\n\t" \
    "buffer_load_dwordx4 v[72:75], %[a1], %[rs], s64 offen offset:2048 sc1\n\t" "buffer_load_dwordx4 v[76:79], %[a1], %[rs], s64 offen offset:3072 sc1\n\t" \
    "v_mov_b32 v122, s60\n\t" "buffer_store_dword v122, %[fo], %[rc], %[fz] offen sc1\n\t" "s_waitcnt vmcnt(1)\n\t" "s_branch 23b\n\t" "24:\n\t" "v_mov_b32 v122, s60\n\t" \
    "buffer_store_dword v122, %[fo], %[rc], %[fz] offen sc1\n\t" "s_cmp_eq_u32 s60, 64\n\t" "s_cbranch_scc1 90f\n\t" "s_add_u32 s67, s60, 1\n\t" "s_cmp_ge_u32 s61, s67\n\t" \
    "s_cbranch_scc1 22f\n\t" "21:\n\t" "buffer_load_dword v120, %[fo], %[rc], %[fv] offen sc1\n\t" "buffer_load_dword v121, %[fo], %[rc], %[vd] offen sc1\n\t" \
    "s_waitcnt vmcnt(0)\n\t" "v_readfirstlane_b32 s61, v120\n\t" "v_readfirstlane_b32 s66, v121\n\t" "s_nop 3\n\t" "s_cmp_ge_u32 s61, s67\n\t" "s_cbranch_scc1 22f\n\t" \
    "s_bitcmp1_b32 s66, 1\n\t" "s_cbranch_scc1 90f\n\t" "s_cmp_eq_u32 %[bd], 0\n\t" "s_cbranch_scc1 22f\n\t" "s_sub_u32 %[bd], %[bd], 1\n\t" "s_sleep 2\n\t" \
    "s_branch 21b\n\t" "22:\n\t" "s_lshr_b32 s66, s60, 2\n\t" "s_xor_b32 s66, s66, %[xp]\n\t" "s_and_b32 s66, s66, 1\n\t" "s_lshl_b32 s66, s66, 13\n\t" \
    "s_lshl_b32 s64, s60, 19\n\t" "s_add_u32 s64, s64, s66\n\t" "s_add_u32 s64, s64, %[vb]\n\t" "buffer_load_dwordx4 v[48:51], %[ao], %[rs], s64 offen offset:0 sc1\n\t" \
    "buffer_load_dwordx4 v[52:55], %[ao], %[rs], s64 offen offset:1024 sc1\n\t" "buffer_load_dwordx4 v[56:59], %[ao], %[rs], s64 offen offset:2048 sc1\n\t" \
    "buffer_load_dwordx4 v[60:63], %[ao], %[rs], s64 offen offset:3072 sc1\n\t" "buffer_load_dwordx4 v[64:67], %[a1], %[rs], s64 offen offset:0 sc1\n\t" \
    "buffer_load_dwordx4 v[68:71], %[a1], %[rs], s64 offen offset:1024 sc1\n\t" "buffer_load_dwordx4 v[72:75], %[a1], %[rs], s64 offen offset:2048 sc1\n\t" \
    "buffer_load_dwordx4 v[76:79], %[a1], %[rs], s64 offen offset:3072 sc1\n\t" "s_waitcnt vmcnt(0)\n\t" "s_branch 23b\n\t" "60:\n\t" "s_sub_u32 s67, s60, %[rg]\n\t" \
    "s_cmp_ge_u32 s62, s67\n\t" "s_cbranch_scc1 62f\n\t" "61:\n\t" "buffer_load_dword v120, %[fo], %[rc], %[fs] offen sc1\n\t" \
    "buffer_load_dword v121, %[fo], %[rc], %[vd] offen sc1\n\t" "s_waitcnt vmcnt(0)\n\t" "v_readfirstlane_b32 s62, v120\n\t" "v_readfirstlane_b32 s66, v121\n\t" "s_nop 3\n\t" \
    "s_cmp_ge_u32 s62, s67\n\t" "s_cbranch_scc1 62f\n\t" "s_bitcmp1_b32 s66, 1\n\t" "s_cbranch_scc1 90f\n\t" "s_cmp_eq_u32 %[bd], 0\n\t" "s_cbranch_scc1 62f\n\t" \
    "s_sub_u32 %[bd], %[bd], 1\n\t" "s_sleep 2\n\t" "s_branch 61b\n\t" "62:\n\t" "s_branch 63b\n\t" "30:\n\t" "s_nop 7\n\t" "v_cvt_pk_bf16_f32 v112, v96, v97\n\t" \
    "v_cvt_pk_bf16_f32 v113, v98, v99\n\t" "v_cvt_pk_bf16_f32 v114, v100, v101\n\t" "v_cvt_pk_bf16_f32 v115, v102, v103\n\t" "v_cvt_pk_bf16_f32 v116, v104, v105\n\t" \
    "v_cvt_pk_bf16_f32 v117, v106, v107\n\t" "v_cvt_pk_bf16_f32 v118, v108, v109\n\t" "v_cvt_pk_bf16_f32 v119, v110, v111\n\t" \
    "buffer_store_dwordx4 v[112:115], %[zo], %[rs], s63 offen offset:0 sc1\n\t" "buffer_store_dwordx4 v[116:119], %[zo], %[rs], s63 offen offset:512 sc1\n\t" \
    "buffer_load_dword v120, %[fo], %[rc], %[fv] offen sc1\n\t" "s_waitcnt vmcnt(0)\n\t" "v_readfirstlane_b32 s61, v120\n\t" "s_lshr_b32 s66, s60, 2\n\t" \
    "s_xor_b32 s66, s66, %[xp]\n\t" "s_and_b32 s66, s66, 1\n\t" "s_lshl_b32 s66, s66, 13\n\t" "s_lshl_b32 s64, s60, 19\n\t" "s_add_u32 s64, s64, s66\n\t" \
    "s_add_u32 s64, s64, %[vb]\n\t" "s_cmp_gt_u32 s61, s60\n\t" "s_cbranch_scc0 34f\n\t" "s_cmp_eq_u32 s60, 64\n\t" "s_cbranch_scc1 34f\n\t" \
    "buffer_load_dwordx4 v[16:19], %[ao], %[rs], s64 offen offset:0 sc1\n\t" "buffer_load_dwordx4 v[20:23], %[ao], %[rs], s64 offen offset:1024 sc1\n\t" \
    "buffer_load_dwordx4 v[24:27], %[ao], %[rs], s64 offen offset:2048 sc1\n\t" "buffer_load_dwordx4 v[28:31], %[ao], %[rs], s64 offen offset:3072 sc1\n\t" \
    "buffer_load_dwordx4 v[32:35], %[a1], %[rs], s64 offen offset:0 sc1\n\t" "buffer_load_dwordx4 v[36:39], %[a1], %[rs], s64 offen offset:1024 sc1\n\t" \
    "buffer_load_dwordx4 v[40:43], %[a1], %[rs], s64 offen offset:2048 sc1\n\t" "buffer_load_dwordx4 v[44:47], %[a1], %[rs], s64 offen offset:3072 sc1\n\t" \
    "v_mov_b32 v122, s60\n\t" "buffer_store_dword v122, %[fo], %[rc], %[fz] offen sc1\n\t" "s_waitcnt vmcnt(1)\n\t" "s_branch 33b\n\t" "34:\n\t" "v_mov_b32 v122, s60\n\t" \
    "buffer_store_dword v122, %[fo], %[rc], %[fz] offen sc1\n\t" "s_cmp_eq_u32 s60, 64\n\t" "s_cbranch_scc1 90f\n\t" "s_add_u32 s67, s60, 1\n\t" "s_cmp_ge_u32 s61, s67\n\t" \
    "s_cbranch_scc1 32f\n\t" "31:\n\t" "buffer_load_dword v120, %[fo], %[rc], %[fv] offen sc1\n\t" "buffer_load_dword v121, %[fo], %[rc], %[vd] offen sc1\n\t" \
    "s_waitcnt vmcnt(0)\n\t" "v_readfirstlane_b32 s61, v120\n\t" "v_readfirstlane_b32 s66, v121\n\t" "s_nop 3\n\t" "s_cmp_ge_u32 s61, s67\n\t" "s_cbranch_scc1 32f\n\t" \
    "s_bitcmp1_b32 s66, 1\n\t" "s_cbranch_scc1 90f\n\t" "s_cmp_eq_u32 %[bd], 0\n\t" "s_cbranch_scc1 32f\n\t" "s_sub_u32 %[bd], %[bd], 1\n\t" "s_sleep 2\n\t" \
    "s_branch 31b\n\t" "32:\n\t" "s_lshr_b32 s66, s60, 2\n\t" "s_xor_b32 s66, s66, %[xp]\n\t" "s_and_b32 s66, s66, 1\n\t" "s_lshl_b32 s66, s66, 13\n\t" \
    "s_lshl_b32 s64, s60, 19\n\t" "s_add_u32 s64, s64, s66\n\t" "s_add_u32 s64, s64, %[vb]\n\t" "buffer_load_dwordx4 v[16:19], %[ao], %[rs], s64 offen offset:0 sc1\n\t" \
    "buffer_load_dwordx4 v[20:23], %[ao], %[rs], s64 offen offset:1024 sc1\n\t" "buffer_load_dwordx4 v[24:27], %[ao], %[rs], s64 offen offset:2048 sc1\n\t" \
    "buffer_load_dwordx4 v[28:31], %[ao], %[rs], s64 offen offset:3072 sc1\n\t" "buffer_load_dwordx4 v[32:35], %[a1], %[rs], s64 offen offset:0 sc1\n\t" \
    "buffer_load_dwordx4 v[36:39], %[a1], %[rs], s64 offen offset:1024 sc1\n\t" "buffer_load_dwordx4 v[40:43], %[a1], %[rs], s64 offen offset:2048 sc1\n\t" \
    "buffer_load_dwordx4 v[44:47], %[a1], %[rs], s64 offen offset:3072 sc1\n\t" "s_waitcnt vmcnt(0)\n\t" "s_branch 33b\n\t" "70:\n\t" "s_sub_u32 s67, s60, %[rg]\n\t" \
    "s_cmp_ge_u32 s62, s67\n\t" "s_cbranch_scc1 72f\n\t" "71:\n\t" "buffer_load_dword v120, %[fo], %[rc], %[fs] offen sc1\n\t" \
    "buffer_load_dword v121, %[fo], %[rc], %[vd] offen sc1\n\t" "s_waitcnt vmcnt(0)\n\t" "v_readfirstlane_b32 s62, v120\n\t" "v_readfirstlane_b32 s66, v121\n\t" "s_nop 3\n\t" \
    "s_cmp_ge_u32 s62, s67\n\t" "s_cbranch_scc1 72f\n\t" "s_bitcmp1_b32 s66, 1\n\t" "s_cbranch_scc1 90f\n\t" "s_cmp_eq_u32 %[bd], 0\n\t" "s_cbranch_scc1 72f\n\t" \
    "s_sub_u32 %[bd], %[bd], 1\n\t" "s_sleep 2\n\t" "s_branch 71b\n\t" "72:\n\t" "s_branch 73b\n\t" "90:\n\t"

#define T_DQ 1480
#define T_DV 1540
#define T_M1 150
#define T_DY 700
#define T_DX 1250
#define T_MIN 35
#define T_PEN 600
#define T_RETRY 1100
#define T_POLL 3
#define T_RST 12
#define T_U0 540
constexpr int T_OVH = 118;
constexpr unsigned NS(int ns) { return (unsigned)(ns * 64 / 10); }
struct Clk { unsigned cyc, g, k1, k2; };
DEV void anchor(Clk& k) {
  unsigned long long c, r[8];
  asm volatile("s_memtime %0\n\ts_memrealtime %1\n\ts_nop 2\n\ts_memrealtime %2\n\ts_nop 2\n\ts_memrealtime %3\n\ts_nop 2\n\ts_memrealtime %4\n\ts_nop 2\n\t"
               "s_memrealtime %5\n\ts_nop 2\n\ts_memrealtime %6\n\ts_nop 2\n\ts_memrealtime %7\n\ts_waitcnt lgkmcnt(0)"
               : "=s"(c), "=s"(r[0]), "=s"(r[1]), "=s"(r[2]), "=s"(r[3]), "=s"(r[4]), "=s"(r[5]), "=s"(r[6]) :: "memory");
  const unsigned a = (unsigned)r[0];
  unsigned n = 0;
#pragma unroll
  for (int i = 1; i < 7; ++i) asm("s_cmp_eq_u32 %1, %2\n\ts_addc_u32 %0, %0, 0" : "+s"(n) : "s"((unsigned)r[i]), "s"(a) : "scc");
  const unsigned nx = (a + 4u) << 6, cy = (unsigned)c + 12u + 16u * n;
  const float dc = (float)(cy - k.cyc), dg = (float)(nx - k.g);
  asm volatile("s_nop 0\n\tv_readfirstlane_b32 %0, %2\n\tv_readfirstlane_b32 %1, %3\n\ts_nop 3\n\ts_min_u32 %0, %0, 0x80000\n\ts_min_u32 %1, %1, 0x8000"
               : "=&s"(k.k1), "=&s"(k.k2) : "v"((unsigned)(dg * __builtin_amdgcn_rcpf(dc) * 65536.f)), "v"((unsigned)(dc * __builtin_amdgcn_rcpf(dg) * 65536.f)) : "scc");
  k.cyc = cy; k.g = nx;
}
DEV unsigned to_cyc(const Clk& k, unsigned g) {
  unsigned d;
  asm("s_max_i32 %0, %1, 0xffff8000\n\ts_min_i32 %0, %0, 0x8000\n\ts_mul_i32 %0, %0, %2\n\ts_ashr_i32 %0, %0, 16" : "=&s"(d) : "s"(g - k.g), "s"(k.k2) : "scc");
  return k.cyc + d - T_OVH;
}
DEV void wait_cyc(unsigned t) { asm volatile(TIMED_WAIT :: "s"(t) : CL_S); }
DEV bool slow_head(const u16* q, int h) { return ((((unsigned)(size_t)q >> 8) + h) & 3) == 1; }
DEV void hold(const Ctx& c, int ticks) {
#pragma unroll 1
  while ((unsigned)__builtin_amdgcn_s_memrealtime() - c.t0 < (unsigned)ticks) __builtin_amdgcn_s_sleep(4);
}
DEV int u_slot(int n, int par, int para) { return n == 2 ? R_U2 + par * 8192 : R_U + n * MB + para * 8192; }
DEV unsigned ask_stamps(Ctx& c, int par, int slot) { return __builtin_amdgcn_raw_buffer_load_b32(c.rs, amap(24576, par) + fresh_lane() * 128, slot, AUX_SC1); }
DEV unsigned latest(Ctx& c, const Clk& k, int par, int slot, unsigned v) {
  if (__builtin_expect(!allok(v), 0)) {
#pragma unroll 1
    do {
      __builtin_amdgcn_s_sleep(T_POLL);
      asm volatile("" ::: "memory");
      const unsigned t = __builtin_amdgcn_raw_buffer_load_b32(c.rs, v == SENT ? amap(24576, par) + fresh_lane() * 128 : 1 << 30, slot, AUX_SC1);
      v = v == SENT ? t : v;
    } while (!allok(v) && !spend(c));
  }
  int d = (int)(v - k.g);
  d = max(d, __builtin_amdgcn_update_dpp(d, d, 0x128, 0xf, 0xf, true));
  d = max(d, __builtin_amdgcn_update_dpp(d, d, 0x124, 0xf, 0xf, true));
  d = max(d, __builtin_amdgcn_update_dpp(d, d, 0x122, 0xf, 0xf, true));
  d = max(d, __builtin_amdgcn_update_dpp(d, d, 0x121, 0xf, 0xf, true));
  unsigned m;
  asm("s_max_i32 %0, %1, %2\n\ts_max_i32 %0, %0, %3\n\ts_max_i32 %0, %0, %4" : "=&s"(m)
      : "s"(__builtin_amdgcn_readlane(d, 0)), "s"(__builtin_amdgcn_readlane(d, 16)), "s"(__builtin_amdgcn_readlane(d, 32)), "s"(__builtin_amdgcn_readlane(d, 48)) : "scc");
  return k.g + m;
}
struct Plan { unsigned gx, dx; };
DEV unsigned smax(unsigned a, unsigned b) { return (int)(a - b) > 0 ? a : b; }
DEV unsigned second_try(const Plan& t, unsigned last) { return smax(last + NS(T_PEN), t.gx + t.dx + NS(T_M1 + T_RETRY)); }
DEV void advance(Plan& t, unsigned last) {
  const unsigned gy = t.gx + t.dx + NS(T_M1), dur = last - t.gx;
  const bool missed = (int)(last + NS(T_MIN) - gy) > 0;
  const unsigned nx = (missed ? second_try(t, last) : gy) + NS(T_DY);
  t.dx = dur < t.dx + NS(60) ? (3u * t.dx + dur) >> 2 : t.dx + NS(8);
  t.gx = nx;
}
DEV unsigned next_go(Ctx& c, Clk& k, Plan& t, int par, int slot, unsigned v) {
  SCHED();
  anchor(k);
  SCHED();
  advance(t, latest(c, k, par, slot, v));
  return to_cyc(k, t.gx);
}

DEV constexpr int qk_r(int j) { return j < 4 ? 0 : 1 + (j - 4) % 3; }
DEV constexpr int qk_i(int j) { return j < 4 ? j : (j - 4) / 3; }
DEV constexpr int qk_upto(int u) { return u < 6 ? u + 7 : u < 10 ? 12 + (u - 4) / 2 : u < 12 ? 15 : 16; }
DEV void qk_role(const Params& p, Ctx& c, int h) {
  constexpr int WL = 0, MEMS = 131072, KXL = 147456, QKX = 155648, QROW = 272;
  c.budget = SPIN_CAP;
  const int w = c.w;
  Clk k = {0u, 0u, 0u, 0u};
  anchor(k);
  v8u wa[32], wh[16];
  claim(c, 0);
  hold(c, QK_AT - (slow_head(p.Wq, h) ? SLOW_LEAD : 0));
  {
    const int lane = fresh_lane(), g = lane >> 4, cc = lane & 15;
    const rsrc_t rq = mkrs(p.Wq, 1024 * 4096 * 2), rk = mkrs(p.Wk, 1024 * 4096 * 2);
    const int vo0 = opq((256 * w + 64 * (h & 1) + krow_lane(g)) * 8192 + (h * 128 + 8 * cc) * 2), vo1 = opq(vo0 ^ (64 * 8192));
    v4u row[16][8];
    auto request = [&](int u) {
#pragma unroll
      for (int r = 0; r < 8; ++r) row[u][r] = ld16w(u < 8 ? rq : rk, (u & 2) ? vo1 : vo0, (32 * (u & 4) + krow(4 * (u & 1) + (r >> 1), r & 1)) * 8192);
    };
#pragma unroll
    for (int u = 0; u < qk_upto(-1); ++u) request(u);
    SCHED();
    const int sw = MEMS + w * 8192 + g * 256, lw = opq(WL + w * 32768 + lane * 16);
    int sr[2];
#pragma unroll
    for (int t = 0; t < 2; ++t) { const int i = (2 * cc + t) & 7; sr[t] = opq(i * 1024 + g * 256 + ((4 * w + (cc >> 2) + 2 * i) & 15) * 16); }
    sfor<16>([&](auto U) __attribute__((always_inline)) {
      constexpr int u = U.value;
#pragma unroll
      for (int i = 0; i < 8; ++i) {
        v4u f;
#pragma unroll
        for (int r = 0; r < 4; ++r) f[r] = prm(row[u][2 * r + 1][i >> 1], row[u][2 * r][i >> 1], i & 1);
        sts16(c.L, sw + i * 1024 + ((cc + 2 * i) & 15) * 16, f);
      }
      SCHED();
      if (qk_upto(u) > qk_upto(u - 1)) request(qk_upto(u - 1));
      BAR();
      v4u in[4][2];
#pragma unroll
      for (int t = 1; t >= 0; --t)
#pragma unroll
        for (int r = 0; r < 4; ++r) in[r][t] = lds16(c.L, sr[t] + MEMS + ((w + r) & 3) * 8192);
      SCHED();
#pragma unroll
      for (int j = 0; j < 16; ++j) {
        if (qk_i(j) != ((u >> 1) & 3)) continue;
#pragma unroll
        for (int t = 1; t >= 0; --t) {
          const v4u o = in[qk_r(j)][t];
          if (u >= 8 && t == 1) sts16(c.L, lw + j * 2048 + (u & 1) * 1024, o);
          else {
            v8u& d = u < 8 ? wa[2 * j + t] : wh[j];
#pragma unroll
            for (int r = 0; r < 4; ++r) d[4 * (u & 1) + r] = o[r];
#define M_(r) asm volatile("" : "+" r(d))
            if (u & 1) { if (u < 8) AT_ACC(M_, 2 * j + t); else AT_HOME(M_, j); }
#undef M_
          }
        }
      }
      BAR();
    });
  }
  STAMP(0);
  BAR();
  const unsigned vote = wait_verdict(c);
  const int par = vote_b(vote), para = vote_a(vote);
  const int lane = fresh_lane(), g = lane >> 4, cc = lane & 15;
  const int moff = opq(amap(w * 4096 + lane * 16, par) + (h & 1) * 1024);
  const int idx = opq((lane & 1) ? 0xEEEE : 0x4444);
  TICK_DECL;
  const int l0 = opq(WL + w * 32768 + lane * 16), l1 = opq(MEMS + lane * 16);
  const int qw = opq(QKX + (2 * w + (cc >> 3)) * 256 + g * 64 + (cc & 1) * 16 + ((cc >> 1) & 3) * 4), kw = opq(QKX + (8 + 2 * g) * QROW + (32 * w + 2 * cc) * 2);
#define QK_PINS PIN_ACC0(wa); PIN_ACC1(wa); PIN_HOME(wh)
#define QK_IN [l0] "v"(l0), [l1] "v"(l1), [mo] "v"(moff), [ix] "v"(idx), [qw] "v"(qw), [kw] "v"(kw), [rs] "s"(c.rs), [sb] "s"(R_MEM + (s & 3) * MEM_SLOT)
#define QK_CL CL_S, CL_16_32, CL_160_200, CL_200_216, CL_216_256
  if (w == 3) {
    const int hq = opq(lane ? 1 << 30 : amap(24576 + h * 128, par));
    anchor(k);
    unsigned tg = k.cyc;
    Plan tt = {k.g - NS(100000), NS(T_DX)};
    for (int s = 0; s < NSEG; ++s) {
      const int pb = R_P + (s & 3) * P_SLOT;
      QK_PINS;
      asm volatile(QK_STEP3 : [bd] "+s"(c.budget) : QK_IN, [hq] "v"(hq), [pb] "s"(pb), [tg] "s"(tg), [ca] "s"(k.cyc - T_DQ), [k1] "s"(k.k1), [ga] "s"(k.g) : QK_CL);
      QK_PINS;
      tg = next_go(c, k, tt, par, pb, ask_stamps(c, par, pb));
      st4s(c.rs, hq, R_P + ((s + 2) & 3) * P_SLOT, fresh_sent());
      st4s(mkrs((const void*)c.ctrl, CTRL_BYTES), hq & (1 << 30), C_FLAG + (F_S + h) * 128, (unsigned)s + 1u);
      TICK_SUM(10);
    }
  } else {
    const int kx0 = opq(KXL + g * 512 + cc * 16), kmrd = opq(QKX + g * 16 + (8 + (cc & 7)) * QROW);
    unsigned seen = NOFLAG;
    if (w == 0) {
      const int prow = (2 * g + (cc & 1)) * 2 + ((cc >> 1) & 1);
      const int px = opq(amap((h >> 1) * 1024 + ((2 * (h & 1) + (cc >> 3)) * 16 + prow) * 16 + ((cc >> 2) & 1) * 8, par));
      const int pm = opq(cc < 8 ? amap(16384 + h * 128 + prow * 8 + (cc >> 2) * 4, par) : 1 << 30);
      const int ps = opq((cc & 1) ? 0x03020706 : 0x05040100);
      for (int s = 0; s < NSEG; ++s) {
        if (seen < (unsigned)s + 1u) seen = wait_flag(c, F_K + h, s + 1);
        QK_PINS;
        asm volatile(QK_STEP0 : [bd] "+s"(c.budget)
                     : QK_IN, [kx] "v"(kx0), [km] "v"(kmrd), [px] "v"(px), [pm] "v"(pm), [ps] "v"(ps), [pb] "s"(R_P + (s & 3) * P_SLOT), [pr] "s"(R_P + ((s + 1) & 3) * P_SLOT),
                       [kb] "s"(R_KX - MEMS + xblk(s, h, para)) : QK_CL);
        QK_PINS;
        TICK_SUM(10);
      }
    } else {
      const int qt = w - 1;
      const int px = opq(amap(qt ? 16384 + h * 1024 + lane * 16 : h * 512 + (g >> 1) * 256 + cc * 16 + (g & 1) * 8, para));
      const int pm = opq(g < 2 ? amap(49152 + (qt * 32 + h) * 256 + cc * 16 + g * 8, para) : 1 << 30);
      const int ps = opq(cc - 4 * g), hq = opq(g * 512 + (16 * qt + cc) * 16);
#define QK_TOK(STEP) for (int s = 0; s < NSEG; ++s) { QK_PINS; \
        asm volatile(STEP : [bd] "+s"(c.budget), [sq] "+s"(seen) \
                     : QK_IN, [kx] "v"(kx0), [km] "v"(kmrd), [px] "v"(px), [pm] "v"(pm), [ps] "v"(ps), [hq] "v"(hq), [qb] "s"(R_QX + xblk(s, h, para)), [tb] "s"(R_PT + (s & 7) * PT_SLOT), \
                       [tr] "s"(R_PT + ((s + 3) & 7) * PT_SLOT), [sn] "s"(s + 1), [fq] "s"(c.ctrl + (C_FLAG + (F_Q + h) * 128) / 4) : QK_CL); \
        QK_PINS; TICK_SUM(10); }
      if (w == 1) { QK_TOK(QK_STEP1) } else { QK_TOK(QK_STEP2) }
    }
  }
  TICK_OUT();
  if (c.budget == 0u) __hip_atomic_store(c.ctrl + C_TMO / 4, 1u, RLX_AGENT);
}

DEV void shuffle_cols(Ctx& c, int scratch, const v4u (&rows)[8], v4u (&o)[8]) {
  const int g = c.lane >> 4, cc = c.lane & 15;
#pragma unroll
  for (int i = 0; i < 8; ++i) {
    v4u f;
#pragma unroll
    for (int r = 0; r < 4; ++r) f[r] = prm(rows[2 * r + 1][i >> 1], rows[2 * r][i >> 1], i & 1);
    sts16(c.L, scratch + (8 * cc + i) * 64 + ((g ^ (cc & 3)) << 4), f);
  }
  asm volatile("s_waitcnt lgkmcnt(0)" ::: "memory");
#pragma unroll
  for (int i = 0; i < 8; ++i) { const int col = 16 * i + cc; o[i] = lds16(c.L, scratch + col * 64 + ((g ^ ((col >> 3) & 3)) << 4)); }
  asm volatile("s_waitcnt lgkmcnt(0)" ::: "memory");
}

DEV void vo_role(const Params& p, Ctx& c, int h) {
  constexpr int WL = 0, MEMS = 131072, VM = 147456, VROW = 272;
  c.budget = SPIN_CAP;
  const int w = c.w, lane = c.lane = fresh_lane(), g = lane >> 4, cc = lane & 15;
  Clk k = {0u, 0u, 0u, 0u};
  anchor(k);
  v8u wa[32], wh[16];
  claim(c, 0);
  hold(c, VO_AT);
  const rsrc_t rwo = mkrs(p.Wo, 4096 * 1024 * 2);
  const int voo = (h * 128 + 4 * g) * 2048 + (256 * w + 8 * cc) * 2;
  v4u buf[3][8];
  auto request_o = [&](int u) {
#pragma unroll
    for (int r = 0; r < 8; ++r) buf[u % 3][r] = ld16w(rwo, voo, (32 * (u & 3) + 16 * (r >> 2) + (r & 3)) * 2048 + (u >> 2) * 256);
  };
  {
    const rsrc_t rw = mkrs(p.Wv, 1024 * 4096 * 2);
    const int vo = (256 * w + krow_lane(g)) * 8192 + (h * 128 + 8 * cc) * 2;
    v4u vrow[8][8];
    auto request = [&](int u) {
#pragma unroll
      for (int r = 0; r < 8; ++r) vrow[u][r] = ld16w(rw, vo, (64 * ((u >> 1) ^ (h & 1)) + krow(4 * (u & 1) + (r >> 1), r & 1)) * 8192);
    };
#pragma unroll
    for (int u = 0; u < 5; ++u) request(u);
    SCHED();
    const int wr_ = MEMS + w * 8192 + g * 256;
    int sr[2];
#pragma unroll
    for (int t = 0; t < 2; ++t) { const int i = (2 * cc + t) & 7; sr[t] = opq(i * 1024 + g * 256 + ((4 * w + (cc >> 2) + 2 * i) & 15) * 16); }
#pragma unroll
    for (int u = 0; u < 8; ++u) {
#pragma unroll
      for (int i = 0; i < 8; ++i) {
        v4u f;
#pragma unroll
        for (int r = 0; r < 4; ++r) f[r] = prm(vrow[u][2 * r + 1][i >> 1], vrow[u][2 * r][i >> 1], i & 1);
        sts16(c.L, wr_ + i * 1024 + ((cc + 2 * i) & 15) * 16, f);
      }
      SCHED();
      if (u + 5 < 8) request(u + 5); else if (u < 6) request_o(u - 3);
      BAR();
      v4u in[4][2];
#pragma unroll
      for (int t = 1; t >= 0; --t)
#pragma unroll
        for (int r = 0; r < 4; ++r) in[r][t] = lds16(c.L, sr[t] + MEMS + ((w + r) & 3) * 8192);
      SCHED();
#pragma unroll
      for (int j = 0; j < 16; ++j) {
        if (qk_i(j) != (u >> 1)) continue;
#pragma unroll
        for (int t = 1; t >= 0; --t) {
          const v4u o = in[qk_r(j)][t];
          if (t == 1 && (j & 1)) sts16(c.L, WL + (w * 16 + (j >> 1)) * 2048 + (u & 1) * 1024 + lane * 16, o);
          else {
            const int n = 3 * (j >> 1) + ((j & 1) ? 2 : t);
#pragma unroll
            for (int r = 0; r < 4; ++r) wa[n][4 * (u & 1) + r] = o[r];
#define M_(r) asm volatile("" : "+" r(wa[n]))
            if (u & 1) AT_ACC(M_, n);
#undef M_
          }
        }
      }
      BAR();
    }
  }
#pragma unroll
  for (int u = 0; u < 8; ++u) {
    v4u o[8];
    shuffle_cols(c, MEMS + w * 8192, buf[u % 3], o);
    SCHED();
    if (u + 3 < 8) request_o(u + 3);
#pragma unroll
    for (int i = 0; i < 4; ++i) {
      const int pr = 4 * (u >> 2) + i, ch = u & 3;
      const v8u f = {o[2 * i][0], o[2 * i + 1][0], o[2 * i][1], o[2 * i + 1][1], o[2 * i][2], o[2 * i + 1][2], o[2 * i][3], o[2 * i + 1][3]};
      if (ch < 2) {
        wh[2 * pr + ch] = f;
#define M_(r) asm volatile("" : "+" r(wh[2 * pr + ch]))
        AT_HOME(M_, 2 * pr + ch);
#undef M_
      } else if (ch == 2) {
        wa[24 + pr] = f;
#define M_(r) asm volatile("" : "+" r(wa[24 + pr]))
        AT_ACC(M_, 24 + pr);
#undef M_
      } else {
        sts16(c.L, WL + (w * 16 + 8 + pr) * 2048 + lane * 16, lo4(f));
        sts16(c.L, WL + (w * 16 + 8 + pr) * 2048 + 1024 + lane * 16, hi4(f));
      }
    }
    SCHED();
  }
  BAR();
  STAMP(0);
  const unsigned vote = wait_verdict(c);
  const int par = vote_b(vote), para = vote_a(vote);
  const int moff = opq(amap(w * 4096 + lane * 16, par) + (h & 1) * 1024);
  const int idx = opq((lane & 1) ? 0xEEEE : 0x4444);
  const int u_off = opq((16 * w + g) * 16384 + h * 256 + cc * 16);
  const int hq = opq((lane | w) ? 1 << 30 : amap(24576 + (32 + h) * 128, par));
  const int l0 = opq(WL + w * 32768 + lane * 16), l1 = opq(MEMS + lane * 16);
  const int vmw = opq(VM + 2 * g * VROW + (32 * w + 2 * cc) * 2);
  const int vmr = opq(VM + (cc >> 1) * VROW + g * 16);
  TICK_DECL;
  anchor(k);
  unsigned tg = k.cyc;
  Plan tt = {k.g - NS(100000), NS(T_DX)};
  int u3 = 0;
  for (int s = 0; s < NSEG; ++s) {
    const int pb = R_P + (s & 3) * P_SLOT;
    PIN_ACC0(wa); PIN_ACC1(wa); PIN_HOME(wh);
    asm volatile(VO_STEP
        : [bd] "+s"(c.budget)
        : [l0] "v"(l0), [l1] "v"(l1), [mo] "v"(moff), [ix] "v"(idx), [vw] "v"(vmw), [vr] "v"(vmr), [uo] "v"(u_off), [w] "s"(w), [rs] "s"(c.rs),
          [sb] "s"(R_MEM + (s & 3) * MEM_SLOT), [ub] "s"(u_slot(u3, par, para)), [tg] "s"(tg), [hq] "v"(hq), [pb] "s"(pb),
          [ca] "s"(k.cyc - T_DV), [k1] "s"(k.k1), [ga] "s"(k.g)
        : CL_S, CL_16_32, CL_160_200, CL_200_216, CL_216_256);
    PIN_ACC0(wa); PIN_ACC1(wa); PIN_HOME(wh);
    if (w == 0) tg = next_go(c, k, tt, par, pb, ask_stamps(c, par, pb)); else __builtin_amdgcn_s_sleep(T_RST);
    {
      const int ur = u_slot(u3 ? u3 - 1 : 2, par, para);
      const v4u se = {fresh_sent(), fresh_sent(), fresh_sent(), fresh_sent()};
#pragma unroll
      for (int i = 0; i < 4; ++i) st16s(c.rs, u_off, ur + i * 65536, se);
      st4s(c.rs, hq, R_P + ((s + 2) & 3) * P_SLOT, se[0]);
      u3 = u3 == 2 ? 0 : u3 + 1;
    }
    TICK_SUM(10);
  }
  TICK_OUT();
  if (c.budget == 0u) __hip_atomic_store(c.ctrl + C_TMO / 4, 1u, RLX_AGENT);
}

DEV void y_role(const Params& p, Ctx& c, int y) {
  constexpr int ZR = 0, UL = 131072, CW = 155648;
  c.budget = SPIN_CAP;
  const int w = c.w, lane = c.lane = fresh_lane(), g = lane >> 4, cc = lane & 15;
  const v4u sent4 = {SENT, SENT, SENT, SENT};
  STAMP(2);
  Clk k = {0u, 0u, 0u, 0u};
  anchor(k);
  unsigned m0 = 0;
  if (w == 0) {
    unsigned best[4] = {~0u, ~0u, ~0u, ~0u};
#pragma unroll
    for (int r = 0; r < 8; ++r) {
      VMWAIT(0);
      const unsigned t0 = (unsigned)__builtin_amdgcn_s_memtime();
      LGKM(0);
      st4s(c.rs, ((r & 2) ? amap(y * 256, 0) : y * 16384) + (r & 1) * 8192 + (lane & 31) * 4, (r & 2) ? R_MEM : R_U, SENT);
      VMWAIT(0);
      const unsigned t1 = (unsigned)__builtin_amdgcn_s_memtime();
      LGKM(0);
      best[r & 3] = min(best[r & 3], t1 - t0);
    }
    STAMP(5);
    claim(c, CL_VOTED | (best[1] < best[0] ? CL_A : 0u) | (best[3] < best[2] ? CL_B : 0u));
    m0 = *(const GAS unsigned*)(p.mem0 + (lane >> 3) * 1024 + 16 * y + 2 * (lane & 7));
    m0 = m0 < SENT - 1u ? m0 : SENT - 1u;
    if (lane < 8) *(LAS unsigned*)(c.L + CW + lane * 4) = 0u;
    const unsigned v = wait_vote(c);
    STAMP(7);
    const int oa = y * 16384 + vote_a(v) * 8192 + lane * 16, ob = amap(y * 3072 + lane * 16, vote_b(v));
#pragma unroll
    for (int i = 0; i < 16; ++i) st16s(c.rs, oa + (i & 3) * 1024, R_U + (i >> 3) * MB + ((i >> 2) & 1) * 4096, sent4);
#pragma unroll
    for (int i = 0; i < 3; ++i) st16s(c.rs, amap(y * 3072 + i * 1024 + lane * 16, vote_b(v)), R_MEM, sent4);
#pragma unroll
    for (int i = 0; i < 8; ++i) st16s(c.rs, oa + (i & 3) * 1024, R_PT + (i >> 2) * 4096, sent4);
    (void)ob;
    VMWAIT(0);
    if (lane == 0) __hip_atomic_fetch_add(c.ctrl + (C_CLAIM + c.att * 1024) / 4 + c.slot, CL_FILLED, RLX_AGENT);
    STAMP(6);
  }
  BAR();
  const unsigned vote = wait_verdict(c);
  const int par = vote_b(vote), para = vote_a(vote);
  const int mo = amap(y * 256 + lane * 4, par);
  if (w == 0) st4s(c.rs, mo, R_MEM, m0);
  STAMP(0);
  volatile LAS unsigned* const GO = (volatile LAS unsigned*)(c.L + CW);
  TICK_DECL;
  if (w == 0) {
    int xo[4];
#pragma unroll
    for (int i = 0; i < 4; ++i) xo[i] = amap(i * 4096 + lane * 16, par);
    const int pmo = amap(16384 + 2 * g * 128 + cc * 8, par), psel = (cc & 1) ? 0x03020706 : 0x05040100;
    const int lead = T_U0 - 128 * ((y >> 2) & 3), late = __builtin_amdgcn_readfirstlane(opq(lead < 264));
    anchor(k);
    Plan tt = {k.g - NS(100000), NS(T_DX)};
    unsigned done = k.cyc;
    const int so = amap(24576, par) + lane * 128;
    const int idx = opq((lane & 1) ? 0xEEEE : 0x4444);
    const int uo = y * 16384 + lane * 16;
    int u3 = 0;
    const int mw = amap(y * 256 + (2 * g + (cc & 1)) * 32 + (cc >> 1) * 4, par);
    for (int s = 0; s < NSEG - 1; ++s) {
      if (s == 0) RAWBAR();
      v32u z0, z1, z2, z3;
      asm volatile(
          "ds_read_b128 a[0:3], %[za] offset:0\n\t" "ds_read_b128 a[4:7], %[za] offset:1024\n\t" "ds_read_b128 a[8:11], %[za] offset:2048\n\t" "ds_read_b128 a[12:15], %[za] offset:3072\n\t"
          "ds_read_b128 a[16:19], %[za] offset:4096\n\t" "ds_read_b128 a[20:23], %[za] offset:5120\n\t" "ds_read_b128 a[24:27], %[za] offset:6144\n\t" "ds_read_b128 a[28:31], %[za] offset:7168\n\t"
          "ds_read_b128 a[32:35], %[za] offset:8192\n\t" "ds_read_b128 a[36:39], %[za] offset:9216\n\t" "ds_read_b128 a[40:43], %[za] offset:10240\n\t" "ds_read_b128 a[44:47], %[za] offset:11264\n\t"
          "ds_read_b128 a[48:51], %[za] offset:12288\n\t" "ds_read_b128 a[52:55], %[za] offset:13312\n\t" "ds_read_b128 a[56:59], %[za] offset:14336\n\t" "ds_read_b128 a[60:63], %[za] offset:15360\n\t"
          "ds_read_b128 a[64:67], %[za] offset:16384\n\t" "ds_read_b128 a[68:71], %[za] offset:17408\n\t" "ds_read_b128 a[72:75], %[za] offset:18432\n\t" "ds_read_b128 a[76:79], %[za] offset:19456\n\t"
          "ds_read_b128 a[80:83], %[za] offset:20480\n\t" "ds_read_b128 a[84:87], %[za] offset:21504\n\t" "ds_read_b128 a[88:91], %[za] offset:22528\n\t" "ds_read_b128 a[92:95], %[za] offset:23552\n\t"
          "ds_read_b128 a[96:99], %[za] offset:24576\n\t" "ds_read_b128 a[100:103], %[za] offset:25600\n\t" "ds_read_b128 a[104:107], %[za] offset:26624\n\t" "ds_read_b128 a[108:111], %[za] offset:27648\n\t"
          "ds_read_b128 a[112:115], %[za] offset:28672\n\t" "ds_read_b128 a[116:119], %[za] offset:29696\n\t" "ds_read_b128 a[120:123], %[za] offset:30720\n\t" "ds_read_b128 a[124:127], %[za] offset:31744\n\t"
          "s_waitcnt lgkmcnt(0)"
          : "={a[0:31]}"(z0), "={a[32:63]}"(z1), "={a[64:95]}"(z2), "={a[96:127]}"(z3) : [za] "v"(ZR + (s & 3) * 32768 + lane * 16) : "memory");
      const int pb = R_P + (s & 3) * P_SLOT;
      v32u uu;
      const int ub = u_slot(u3, par, para);
      TICK(0);
      anchor(k);
      TICK(8);
      unsigned tg = to_cyc(k, tt.gx + tt.dx + NS(T_M1)), lt = late, sv = SENT;
      unsigned bad = (int)(done - to_cyc(k, tt.gx)) > T_OVH;
      TICK(4);
#pragma unroll 1
      for (;;) {
        if (bad) {
          const bool all = allok(sv);
          const unsigned last = latest(c, k, par, pb, sv);
          if (!all) anchor(k);
          tg = to_cyc(k, second_try(tt, last));
          lt = 1;
        }
        asm volatile(Y_STEP
          : "=&{v[144:175]}"(uu), [bad] "=&s"(bad), [sv] "=&v"(sv)
          : [x0] "v"(xo[0]), [x1] "v"(xo[1]), [x2] "v"(xo[2]), [x3] "v"(xo[3]), [pm] "v"(pmo), [u0] "v"(uo), [u1] "v"(uo + 4096), [mw] "v"(mw), [ps] "v"(psel), [ix] "v"(idx),
            [rs] "s"(c.rs), [pb] "s"(pb), [ub] "s"(ub), [mb] "s"(R_MEM + ((s + 1) & 3) * MEM_SLOT), [mr] "s"(R_MEM + ((s + 3) & 3) * MEM_SLOT), [tu] "s"(tg - (lt ? 128 : lead)), [tg] "s"(tg), [late] "s"(lt),
            [so] "v"(so), [bd] "s"(c.budget),
            "{a[0:31]}"(z0), "{a[32:63]}"(z1), "{a[64:95]}"(z2), "{a[96:127]}"(z3)
          : CL_S, CL_120_128, CL_176_200, CL_200_216, CL_216_256);
        bad = __builtin_amdgcn_readfirstlane(bad);
        if (bad != 1u) break;
        spend(c);
      }
      done = bad;
      SCHED();
      const int ul = opq(UL + lane * 16);
      u3 = u3 == 2 ? 0 : u3 + 1;
#pragma unroll
      for (int i = 0; i < 8; ++i) sts16(c.L, ul + i * 1024, v4u{uu[4 * i], uu[4 * i + 1], uu[4 * i + 2], uu[4 * i + 3]});
      asm volatile("" ::: "memory");
      *GO = s + 1;
      asm volatile("s_waitcnt vmcnt(2)" : "+v"(sv));
      advance(tt, latest(c, k, par, pb, sv));
      TICK(3);
      TICK_LDS(CW + 256);
    }
    {
      anchor(k);
      wait_cyc(to_cyc(k, tt.gx + tt.dx + NS(T_M1)));
      const int ub = u_slot(u3, par, para);
      v4u u[8];
#pragma unroll 1
      for (;;) {
        asm volatile("" ::: "memory");
#pragma unroll
        for (int i = 0; i < 8; ++i) u[i] = ld16s(c.rs, uo + (i & 3) * 1024, ub + (i >> 2) * 4096);
        SCHED();
        unsigned t = 0;
#pragma unroll
        for (int i = 0; i < 8; ++i) t = umax(t, top(u[i]));
        if (allok(t) || spend(c)) break;
      }
      RAWBAR();
#pragma unroll
      for (int i = 0; i < 8; ++i) sts16(c.L, UL + i * 1024 + lane * 16, u[i]);
      asm volatile("" ::: "memory");
      *GO = NSEG;
    }
    TICK_DUMP(CW + 256);
  } else if (w < 3) {
    const rsrc_t ro = mkrs(p.out, 2048 * 2048 * 2);
    auto tokens = [&](auto QT) {
      constexpr int qt = QT.value, NX = qt ? 32 : 16;
      RAWBAR();
      for (int s = 0; s < NSEG; ++s) {
        const int tb = R_PT + (s & 7) * PT_SLOT + para * 8192;
        const int ua = opq(UL + lane * 16);
        const int za = opq(ZR + (s & 3) * 32768 + (qt ? lane * 16 : (g >> 1) * 1024 + (g & 1) * 512 + cc * 16));
        SCHED();
        RAWBAR();
        TICK(0);
        f32x4 a;
#pragma unroll 1
        for (;;) {
          asm volatile("" ::: "memory");
          v4u pt[NX + 8];
#pragma unroll
          for (int i = 0; i < NX; ++i) pt[i] = ld16s(c.rs, (i & 3) * 1024 + lane * 16, tb + amap(qt * 16384 + (i >> 2) * 4096, 0));
#pragma unroll
          for (int i = 0; i < 8; ++i) pt[NX + i] = ld16s(c.rs, (i & 3) * 1024 + lane * 16, tb + amap(49152 + qt * 8192 + (i >> 2) * 4096, 0));
          SCHED();
          TICK(1);
#pragma unroll 1
          while (__builtin_amdgcn_readfirstlane(*GO) < (unsigned)s + 1u) { if (spend(c)) break; }
          asm volatile("" ::: "memory");
          f32x4 d[2] = {f32x4{0.f, 0.f, 0.f, 0.f}, f32x4{0.f, 0.f, 0.f, 0.f}};
          v4u fa[8];
          auto frag = [&](int i) {
            if (i >= NX) fa[i & 7] = lds16(c.L, ua + (i - NX) * 1024);
            else if (qt) fa[i & 7] = lds16(c.L, za + i * 1024);
            else {
              const v2u lo = *(LAS v2u*)(c.L + za + i * 2048), hi = *(LAS v2u*)(c.L + za + i * 2048 + 256);
              fa[i & 7] = v4u{lo.x, lo.y, hi.x, hi.y};
            }
          };
#pragma unroll
          for (int i = 0; i < 7; ++i) frag(i);
          SCHED();
#pragma unroll
          for (int i = 0; i < NX + 8; ++i) {
            if (i + 7 < NX + 8) frag(i + 7);
            d[i & 1] = mfma16(fa[i & 7], pt[i], d[i & 1]);
            SCHED();
          }
          a = d[0] + d[1];
          if (!__any(a[0] != a[0])) break;
          unsigned t = 0;
#pragma unroll
          for (int i = 0; i < NX + 8; ++i) t = umax(t, top(pt[i]));
          if (allok(t) || spend(c)) break;
        }
        __builtin_amdgcn_raw_buffer_store_b64(v2u{pk(a[0], a[1]), pk(a[2], a[3])}, ro, (16 * qt + cc) * 4096 + (16 * y + 4 * g) * 2, s * 32 * 4096, AUX_NT);
        TICK(2);
        TICK_SUM(3);
      }
    };
    if (w == 1) tokens(IC<0>{}); else tokens(IC<1>{});
  } else {
    unsigned seenZ = NOFLAG;
    const rsrc_t rc = mkrs((const void*)c.ctrl, CTRL_BYTES);
    const int fo = lane < 32 ? lane * 128 : 1 << 30;
    __builtin_amdgcn_s_setprio(3);
    auto least = [&](unsigned v) {
      v |= lane < 32 ? 0u : ~0u;
#pragma unroll
      for (int o = 1; o < 32; o <<= 1) v = min(v, (unsigned)__shfl_xor((int)v, o));
      seenZ = umax(seenZ, __builtin_amdgcn_readfirstlane(v));
    };
    auto await = [&](unsigned need) {
#pragma unroll 1
      while (seenZ < need) {
        asm volatile("" ::: "memory");
        least(__builtin_amdgcn_raw_buffer_load_b32(rc, fo, C_FLAG + (F_Z + (y >> 4) * 32) * 128, AUX_SC1));
        if (seenZ >= need || spend(c)) break;
        __builtin_amdgcn_s_sleep(4);
      }
      __builtin_amdgcn_fence(__ATOMIC_ACQUIRE, "wavefront");
    };
    for (int n = 0; n < NSEG; ++n) {
      TICK(0);
      await(n + 1);
      TICK(1);
      v4u z[32];
#pragma unroll
      for (int i = 0; i < 32; ++i) {
        z[i] = ld16s(c.rs, (i & 3) * 1024 + lane * 16, R_Z + (n & (ZRING - 1)) * Z_SLOT + y * 65536 + ((para ^ (y >> 5)) & 1) * 8192 + amap((i >> 2) * 4096, 0));
        SCHED();
        asm volatile("s_nop " STR(Z_GAP));
        SCHED();
      }
      const unsigned look = __builtin_amdgcn_raw_buffer_load_b32(rc, seenZ < (unsigned)n + 2u ? fo : 1 << 30, C_FLAG + (F_Z + (y >> 4) * 32) * 128, AUX_SC1);
      SCHED();
      TICK(4);
      const int za = opq(ZR + (n & 3) * 32768 + lane * 16);
#pragma unroll
      for (int i = 0; i < 32; ++i) {
        sts16(c.L, za + i * 1024, z[i]);
        if (i == 30) { TICK(3); }
      }
      least(look);
      if (n == 0) await(PRIME);
      TICK(2);
      { const int s = n; (void)s; TICK_SUM(3); }
      BAR();
    }
    RAWBAR();
  }
  TICK_OUT();
  if (c.budget == 0u) __hip_atomic_store(c.ctrl + C_TMO / 4, 1u, RLX_AGENT);
}

#define MF1A(d, a, b) asm volatile(MF16 "%0, %1, %2, %0" : "+v"(d) : "a"(a), "v"(b))
#define MF32A(d, a, b) asm volatile("v_mfma_f32_32x32x16_bf16 %0, %1, %2, %0" : "+v"(d) : "a"(a), "v"(b))
#define MF32AZ(d, a, b) asm volatile("v_mfma_f32_32x32x16_bf16 %0, %1, %2, 0" : "=&v"(d) : "a"(a), "v"(b))
#define MF1B(d, a, b) asm volatile(MF16 "%0, %1, %2, %0" : "+v"(d) : "v"(a), "a"(b))
#define MF1BZ(d, a, b) asm volatile(MF16 "%0, %1, %2, 0" : "=&v"(d) : "v"(a), "a"(b))
#define MF1AZ(d, a, b) asm volatile(MF16 "%0, %1, %2, 0" : "=&v"(d) : "a"(a), "v"(b))
DEV void g_role(const Params& p, Ctx& c, int mat, int h, int gi) {
  constexpr int EX = 131072;
  c.budget = SPIN_CAP;
  const int w = c.w, lane = c.lane = fresh_lane(), g = lane >> 4, cc = lane & 15, h2 = lane >> 5, r32 = lane & 31;
  if (mat < 2) {
    unsigned t = 0;
#pragma unroll
    for (int i = 0; i < 3; ++i) t += __builtin_amdgcn_raw_buffer_load_b32(mkrs(p.Wv, 8 * MB), (256 * w + (lane >> 1)) * 8192 + h * 256 + (lane & 1) * 128, (2 + 2 * i + mat) * 32 * 8192, 0);
    if (mat == 0) {
      hold(c, X_AT);
#pragma unroll
      for (int i = 0; i < 4; ++i) t += __builtin_amdgcn_raw_buffer_load_b32(mkrs(p.x, 4 * MB), ((4 * h + w) * 64 + lane) * 128, i * MB, 0);
    }
    asm volatile("" :: "v"(t));
    claim(c, 0);
    hold(c, (mat ? GK_AT : GQ_AT) - (slow_head(p.Wq, h) ? SLOW_LEAD : 0));
  }
  const rsrc_t rx = mkrs(p.x, 2048 * 2048);
  const int xs = w * 16384 + ((lane * 16) ^ ((w & 1) << 7));

  v32u wa[8];
  {
    const rsrc_t rw = mkrs(mat == 0 ? p.Wq : mat == 1 ? p.Wk : p.Wv, 1024 * 4096 * 2);
    const int vo = (256 * w + 8 * g) * 8192 + (h * 128 + 8 * cc) * 2;
    v4u row[8][8];
    auto request = [&](int u) {
#pragma unroll
      for (int r = 0; r < 8; ++r) row[u][r] = ld16w(rw, vo, (32 * u + r) * 8192);
    };
#pragma unroll
    for (int u = 0; u < 2; ++u) request(u);
#pragma unroll
    for (int u = 0; u < 16; ++u) dma16(rx, c.L + w * 16384 + u * 1024, (xs ^ ((u >> 1) << 4)) + u * 1024);
#pragma unroll
    for (int u = 2; u < 6; ++u) request(u);
    SCHED();
    const int col = 32 * w + 16 * (r32 >> 4) + 8 * ((r32 >> 2) & 1) + 4 * ((r32 >> 3) & 1) + (r32 & 3);
    const int sw_ = opq(EX + w * 8192 + cc * 512 + ((g ^ (cc & 3)) << 4)), sr_ = EX + col * 64;
    const int sr0 = opq(sr_ + ((h2 ^ ((col >> 3) & 3)) << 4)), sr1 = opq(sr_ + (((2 + h2) ^ ((col >> 3) & 3)) << 4));
#pragma unroll
    for (int u = 0; u < 8; ++u) {
#pragma unroll
      for (int i = 0; i < 8; ++i) {
        v4u f;
#pragma unroll
        for (int r = 0; r < 4; ++r) f[r] = prm(row[u][2 * r + 1][i >> 1], row[u][2 * r][i >> 1], i & 1);
        sts16(c.L, sw_ + i * 64, f);
      }
      SCHED();
      if (u + 6 < 8) request(u + 6);
      if (u == 7 && mat == 2) claim(c, 0);
      BAR();
      v8u in[4];
#pragma unroll
      for (int q = 0; q < 4; ++q) in[q] = cat(lds16(c.L, sr0 + q * 8192), lds16(c.L, sr1 + q * 8192));
      SCHED();
#pragma unroll
      for (int q = 0; q < 4; ++q) {
        v8u f = in[q];
#define M_(r) asm volatile("" : "+" r(f))
        AT_ACC(M_, 8 * q + u);
#undef M_
#pragma unroll
        for (int r = 0; r < 8; ++r) wa[2 * q + (u >> 2)][8 * (u & 3) + r] = f[r];
      }
      BAR();
    }
  }
  STAMP(0);
  const int xpar = vote_a(wait_vote(c)) ^ (mat >> 1);
  TICK_DECL;
  asm volatile(G_LOOP
      :: [xo] "v"(r32 * 2048 + ((h2 ^ (r32 & 15)) << 4)), [xs] "v"(xs), [so] "v"((4 * w + h2) * 512 + r32 * 16), [fo] "v"((w | lane) ? 1 << 30 : 0),
         [rs] "s"(c.rs), [rx] "s"(rx), [rc] "s"(mkrs((const void*)c.ctrl, CTRL_BYTES)), [fl] "s"(C_FLAG + (mat * 32 + h) * 128), [xp] "s"(xpar),
         [ob] "s"((mat == 1 ? R_KX : R_QX) + h * 16384), [lw] "s"(w * 16384), GTRACE ACC_IN(wa)
      : CL_S, CL_16_32, CL_32_100);
}

DEV void zc_role(const Params& p, Ctx& c, int h) {
  c.budget = SPIN_CAP;
  const int w = c.w, lane = c.lane = fresh_lane(), g = lane >> 4, cc = lane & 15, h2 = lane >> 5, r32 = lane & 31;
  v32u wa[8];
  hold(c, ZC_AT);
  {
    const rsrc_t rw = mkrs(p.Wo, 4096 * 1024 * 2);
    const int vo = (h * 128 + 8 * g) * 2048 + (256 * w + 8 * cc) * 2;
    v4u row[8][8];
    auto request = [&](int u) {
#pragma unroll
      for (int r = 0; r < 8; ++r) row[u][r] = ld16w(rw, vo, (32 * (u & 3) + r) * 2048 + (u >> 2) * 256);
    };
#pragma unroll
    for (int u = 0; u < 2; ++u) request(u);
    SCHED();
    claim(c, 0);
    SCHED();
#pragma unroll
    for (int u = 2; u < 6; ++u) request(u);
    const int sw_ = opq(w * 8192 + cc * 512 + ((g ^ (cc & 3)) << 4)), sr_ = w * 8192 + r32 * 64;
    const int sr0 = opq(sr_ + ((h2 ^ (r32 >> 3)) << 4)), sr1 = opq(sr_ + (((2 + h2) ^ (r32 >> 3)) << 4));
#pragma unroll
    for (int u = 0; u < 8; ++u) {
#pragma unroll
      for (int i = 0; i < 8; ++i) {
        v4u f;
#pragma unroll
        for (int r = 0; r < 4; ++r) f[r] = prm(row[u][2 * r + 1][i >> 1], row[u][2 * r][i >> 1], i & 1);
        sts16(c.L, sw_ + i * 64, f);
      }
      SCHED();
      if (u + 6 < 8) request(u + 6);
      LGKM(0);
      v8u in[4];
#pragma unroll
      for (int q = 0; q < 4; ++q) in[q] = cat(lds16(c.L, sr0 + q * 2048), lds16(c.L, sr1 + q * 2048));
      SCHED();
#pragma unroll
      for (int q = 0; q < 4; ++q) {
        v8u f = in[q];
#define M_(r) asm volatile("" : "+" r(f))
        AT_ACC(M_, 4 * (4 * (u >> 2) + q) + (u & 3));
#undef M_
#pragma unroll
        for (int r = 0; r < 8; ++r) wa[4 * (u >> 2) + q][8 * (u & 3) + r] = f[r];
      }
      LGKM(0);
      SCHED();
    }
  }
  STAMP(0);
  BAR();
  const int para = vote_a(wait_vote(c));
  const int ao = h2 * 512 + (16 * ((r32 >> 3) & 1) + 8 * (r32 >> 4) + (r32 & 7)) * 16;
  TICK_DECL;
  asm volatile(ZC_LOOP
      : [bd] "+s"(c.budget)
      : [ao] "v"(ao), [a1] "v"(ao + 4096), [zo] "v"((16 * w + (r32 >> 4)) * 65536 + (h2 * 16 + cc) * 16 + ((para ^ (w >> 1)) & 1) * 8192 + amap(h * 1024, 0)), [fo] "v"(lane ? 1 << 30 : 0),
        [rs] "s"(c.rs), [rc] "s"(mkrs((const void*)c.ctrl, CTRL_BYTES)), [fv] "s"(C_FLAG + (F_V + h) * 128), [fs] "s"(C_FLAG + (F_S + h) * 128),
        [fz] "s"(C_FLAG + (F_Z + w * 32 + h) * 128), [xp] "s"(para ^ 1), [vb] "s"(R_QX + h * 16384), [vd] "s"(C_VERDICT + c.att * 128), [z0] "n"(ZC_PRIME), [zb] "n"(R_Z), [rg] "n"(ZRING - 2), [rm] "n"(ZRING - 1), GTRACE ACC_IN(wa)
      : CL_S, CL_16_32, CL_32_100);
  if (c.budget == 0u) __hip_atomic_store(c.ctrl + C_TMO / 4, 1u, RLX_AGENT);
}

DEV void conv_role(const Params& p, Ctx& c, int cg, int far) {
  constexpr int CH = 5200, U8 = 0;
  constexpr int CP = 4160, FC = 41600;
  constexpr int YS = FC + 16 * CP, RED = YS + 32768;
  static_assert(RED + 8192 <= LDS_BYTES - 16, "lds");
  c.budget = SPIN_CAP;
  const int w = c.w, lane = c.lane = fresh_lane(), tid = w * 64 + lane;
  const int d0 = 8 * cg;
  const int pg = ((far ^ (cg >> 5)) & 1) * 8192;
  auto crow = [](int ch) { return (ch >> 1) * 16384 + (ch & 1) * 4096; };
  const int de = (cg >> 3) * 64 + (tid & 7) * 8, t0 = (cg & 7) * 256 + (tid >> 3) * 8;
  auto group_sync = [&](int phase) {
    gu32* cnt = c.ctrl + (C_CONV + (cg >> 3) * 16 + phase * 8) / 4;
    VMWAIT(0);
    BAR();
    if (w == 0) {
      if (lane == 0) __hip_atomic_fetch_add(cnt, 1u, RLX_AGENT);
#pragma unroll 1
      while (__builtin_amdgcn_readfirstlane(__hip_atomic_load(cnt, RLX_AGENT)) < 8u) { if (spend(c)) break; __builtin_amdgcn_s_sleep(4); }
    }
    BAR();
  };
  v4u kv[8];
  BAR();
  {
    const rsrc_t rk = mkrs(p.bk, 2048 * 2048), rv = mkrs(p.bv, 2048 * 2048);
    v4u kr[11], vr[8], fr[8];
#pragma unroll
    for (int i = 0; i < 11; ++i) kr[i] = ld16c(rk, (t0 + i - 3) * 2048 + de * 2, 0);
#pragma unroll
    for (int i = 0; i < 8; ++i) vr[i] = ld16c(rv, (t0 + i) * 2048 + de * 2, 0);
    v4u sh[4];
#pragma unroll
    for (int i = 0; i < 4; ++i) sh[i] = *(const GAS v4u*)(p.shift + de * 4 + i * 8);
#pragma unroll
    for (int i = 0; i < 8; ++i) fr[i] = ld16c(mkrs(p.ssmk, 4 * MB), tid * 16, (d0 + i) * 4096);
    if (tid < 40) sts16(c.L, U8 + (tid / 5) * CH + (tid % 5) * 16, v4u{0, 0, 0, 0});
    if (tid < 64) sts16(c.L, FC + (tid >> 2) * CP + 4096 + (tid & 3) * 16, v4u{0, 0, 0, 0});
#pragma unroll
    for (int ch = 0; ch < 8; ++ch) {
      const unsigned sa = sh[ch >> 1][(ch & 1) * 2], sb = sh[ch >> 1][(ch & 1) * 2 + 1];
      const float s0 = blo(sa), s1 = bhi(sa), s2 = blo(sb), s3 = bhi(sb);
      float kk[11], o[8];
#pragma unroll
      for (int i = 0; i < 11; ++i) kk[i] = (ch & 1) ? bhi(kr[i][ch >> 1]) : blo(kr[i][ch >> 1]);
#pragma unroll
      for (int i = 0; i < 8; ++i) {
        float a = rb(kk[i] * s3);
        a = rb(a + rb(kk[i + 1] * s2));
        a = rb(a + rb(kk[i + 2] * s1));
        a = rb(a + rb(kk[i + 3] * s0));
        o[i] = a * ((ch & 1) ? bhi(vr[i][ch >> 1]) : blo(vr[i][ch >> 1]));
      }
      kv[ch] = v4u{pk(o[0], o[1]), pk(o[2], o[3]), pk(o[4], o[5]), pk(o[6], o[7])};
      st16s(c.rs, de * 8192 + t0 * 2, R_KVT + pg + crow(ch), kv[ch]);
    }
#pragma unroll
    for (int i = 0; i < 8; ++i) sts16(c.L, YS + i * 4096 + tid * 16, fr[i]);
    group_sync(0);
#pragma unroll
    for (int ch = 0; ch < 8; ++ch) fr[ch] = ld16s(c.rs, tid * 16, R_KVT + pg + d0 * 8192 + crow(ch));
#pragma unroll
    for (int ch = 0; ch < 8; ++ch) sts16(c.L, U8 + ch * CH + 80 + (tid >> 2) * 80 + (tid & 3) * 16, fr[ch]);
  }
  BAR();
  STAMP(5);
  auto build = [&](int ch, int q0, int q1) {
    for (int q = q0; q < q1; ++q) {
      const int m = lane + 64 * q;
      const v4u hi = lds16(c.L, YS + ch * 4096 + (255 - m) * 16);
      v4u lo = lds16(c.L, YS + ch * 4096 + (m < 255 ? 254 - m : 0) * 16);
      if (m == 255) lo = v4u{0, 0, 0, 0};
      unsigned G[8];
#pragma unroll
      for (int i = 0; i < 4; ++i) { G[i] = __builtin_amdgcn_alignbyte(hi[3 - i], hi[3 - i], 2); G[4 + i] = __builtin_amdgcn_alignbyte(lo[3 - i], lo[3 - i], 2); }
#pragma unroll
      for (int sg = 0; sg < 8; ++sg) {
        v4u o;
#pragma unroll
        for (int i = 0; i < 4; ++i) o[i] = (sg & 1) ? __builtin_amdgcn_alignbyte(G[(sg >> 1) + i + 1], G[(sg >> 1) + i], 2) : G[(sg >> 1) + i];
        sts16(c.L, FC + ((ch & 1) * 8 + sg) * CP + m * 16, o);
      }
    }
  };
  build(0, w, w + 1);
  BAR();
  const int h2 = lane >> 5, r = lane & 31;
  for (int ch = 0; ch < 8; ++ch) {
    f32x16 acc;
    if (w == 3) {
      if (ch + 1 < 8) build(ch + 1, 0, 4);
    } else {
#pragma unroll
      for (int i = 0; i < 16; ++i) acc[i] = 0.f;
      const int dl0 = w == 2 ? 32 : 0, i0 = w == 0 ? 0 : 32;
      const int sg = (7 - r) & 7;
      int fo = FC + ((ch & 1) * 8 + sg) * CP + (2047 - 32 * dl0 - r + 8 * h2 - sg) * 2;
      const int ub = (i0 + r - dl0) * 80;
      const int ubase = U8 + ch * CH + 80 + h2 * 16;
      v4u fa[3][2], fb[3][2];
      fo = opq(fo - 31 * 64);
#pragma unroll
      for (int dl = -2; dl < 32; ++dl) {
        const int dn = dl + 2;
        if (dn < 32) {
          const int uo = ubase + max(ub - dn * 80, -80);
          fa[dn % 3][0] = lds16(c.L, fo + (31 - dn) * 64); fb[dn % 3][0] = lds16(c.L, uo);
          fa[dn % 3][1] = lds16(c.L, fo + (31 - dn) * 64 + 32); fb[dn % 3][1] = lds16(c.L, uo + 32);
        }
        SCHED();
        if (dl >= 0) {
          acc = mfma32(fa[dl % 3][0], fb[dl % 3][0], acc);
          acc = mfma32(fa[dl % 3][1], fb[dl % 3][1], acc);
        }
        SCHED();
      }
      if (w == 2) {
#pragma unroll
        for (int m = 0; m < 4; ++m) sts16(c.L, RED + (ch & 1) * 4096 + m * 1024 + lane * 16, v4u{fu(acc[4 * m]), fu(acc[4 * m + 1]), fu(acc[4 * m + 2]), fu(acc[4 * m + 3])});
      }
    }
    BAR();
    if (w < 2) {
#pragma unroll
      for (int m = 0; m < 4; ++m) {
        f32x4 v = {acc[4 * m], acc[4 * m + 1], acc[4 * m + 2], acc[4 * m + 3]};
        if (w == 1) v += __builtin_bit_cast(f32x4, lds16(c.L, RED + (ch & 1) * 4096 + m * 1024 + lane * 16));
        sts8(c.L, YS + ch * 4096 + (32 * (32 * w + r) + 8 * m + 4 * h2) * 2, v2u{pk(v[0], v[1]), pk(v[2], v[3])});
      }
    }
  }
  BAR();
  STAMP(6);
  {
    const rsrc_t ro = mkrs(p.out, 2048 * 2048 * 2);
    v4u qr[8], yv[8];
#pragma unroll
    for (int ch = 0; ch < 8; ++ch) st16s(c.rs, tid * 16, R_YT + pg + d0 * 8192 + crow(ch), lds16(c.L, YS + ch * 4096 + tid * 16));
#pragma unroll
    for (int i = 0; i < 8; ++i) qr[i] = ld16c(mkrs(p.bq, 4 * MB), (t0 + i) * 2048 + de * 2, 0);
    const v4u dd = *(const GAS v4u*)(p.ssmD + de);
    group_sync(1);
#pragma unroll
    for (int ch = 0; ch < 8; ++ch) yv[ch] = ld16s(c.rs, de * 8192 + t0 * 2, R_YT + pg + crow(ch));
#pragma unroll
    for (int i = 0; i < 8; ++i) {
      float o[8];
#pragma unroll
      for (int ch = 0; ch < 8; ++ch) {
        const float yy = (i & 1) ? bhi(yv[ch][i >> 1]) : blo(yv[ch][i >> 1]);
        const float uu = (i & 1) ? bhi(kv[ch][i >> 1]) : blo(kv[ch][i >> 1]);
        const float dv = (ch & 1) ? bhi(dd[ch >> 1]) : blo(dd[ch >> 1]);
        const float qq = (ch & 1) ? bhi(qr[i][ch >> 1]) : blo(qr[i][ch >> 1]);
        o[ch] = qq * rb(yy + rb(uu * dv));
      }
      __builtin_amdgcn_raw_buffer_store_b128(v4u{pk(o[0], o[1]), pk(o[2], o[3]), pk(o[4], o[5]), pk(o[6], o[7])}, ro, (t0 + i) * 4096 + (1024 + de) * 2, 0, AUX_NT);
    }
  }
  if (c.budget == 0u) __hip_atomic_store(c.ctrl + C_TMO / 4, 1u, RLX_AGENT);
}

extern "C" __global__ void __launch_bounds__(256, 1) mk_mega(Params p) {
  extern __shared__ __attribute__((aligned(16))) unsigned char lds_raw[];
  Ctx c;
  c.ctrl = (gu32*)p.ctrl;
  c.rs = mkrs(p.data, R_END);
  c.L = (lds_t)lds_raw;
  c.w = __builtin_amdgcn_readfirstlane(threadIdx.x >> 6);
  c.lane = threadIdx.x & 63;
  c.t0 = (unsigned)__builtin_amdgcn_s_memrealtime();
  c.b = (int)(blockIdx.x & 255u);
  c.att = (int)(blockIdx.x >> 8);
  c.ask = 0;
  if (c.w == 0 && c.att == 0) {
    c.ask = __builtin_amdgcn_readfirstlane(__hip_atomic_load(c.ctrl + C_ECHO / 4 + c.b, RLX_AGENT)) + 1u;
    if (c.lane == 0) __hip_atomic_store(c.ctrl + (C_ASK + c.b * 64) / 4, c.ask, RLX_AGENT);
  }
  c.slot = (c.att ? (c.b & 7) : (int)((unsigned)__builtin_amdgcn_s_getreg(20 | (3 << 11)) & 7u)) * 32 + (c.b >> 3);
  if (c.b == SERVER && c.w == 3) serve(c);
  const int x = c.slot >> 5, t = c.slot & 31;
  if (x < 4) {
    if (t < 8) qk_role(p, c, x * 8 + t);
    else if (t < 16) vo_role(p, c, x * 8 + t - 8);
    else y_role(p, c, x * 16 + t - 16);
  } else {
    if (t < 24) g_role(p, c, t >> 3, (x - 4) * 8 + (t & 7), t);
    else zc_role(p, c, (x - 4) * 8 + t - 24);
    STAMP(2);
    c.budget = SPIN_CAP;
    conv_role(p, c, (x - 4) * 32 + t, vote_a(wait_verdict(c)) ^ 1);
  }
  STAMP(3);
}

}

extern "C" void kernel_launch(void* const* d_in, const int* in_sizes, int n_in, void* d_out, int out_size, void* d_ws, size_t ws_size,
                              hipStream_t stream) {
  using namespace mk;
  (void)in_sizes; (void)n_in; (void)out_size; (void)ws_size;
  Params p;
  p.x = (const u16*)d_in[0]; p.mem0 = (const u16*)d_in[1];
  p.Wq = (const u16*)d_in[2]; p.Wk = (const u16*)d_in[3]; p.Wv = (const u16*)d_in[4]; p.Wo = (const u16*)d_in[5];
  p.bq = (const u16*)d_in[7]; p.bk = (const u16*)d_in[8]; p.bv = (const u16*)d_in[9];
  p.shift = (const u16*)d_in[10]; p.ssmk = (const u16*)d_in[11]; p.ssmD = (const u16*)d_in[12];
  p.out = (u16*)d_out;
  p.ctrl = (unsigned char*)d_ws;
  p.data = (unsigned char*)(((size_t)d_ws + CTRL_BYTES + (2u << 20) - 1) & ~(size_t)((2u << 20) - 1));
  (void)hipFuncSetAttribute((const void*)mk_mega, hipFuncAttributeMaxDynamicSharedMemorySize, LDS_BYTES);
  hipLaunchKernelGGL(mk_mega, dim3(256), dim3(256), LDS_BYTES, stream, p);
}
```

```cpp
#include <hip/hip_runtime.h>
#include <hip/hip_bf16.h>

namespace mk {

#define GAS __attribute__((address_space(1)))
#define LAS __attribute__((address_space(3)))
#define DEV __device__ __forceinline__
#define RLX_AGENT __ATOMIC_RELAXED, __HIP_MEMORY_SCOPE_AGENT
typedef unsigned short u16;
typedef unsigned v2u __attribute__((ext_vector_type(2)));
typedef unsigned v4u __attribute__((ext_vector_type(4)));
typedef unsigned v8u __attribute__((ext_vector_type(8)));
typedef unsigned v16u __attribute__((ext_vector_type(16)));
typedef unsigned v32u __attribute__((ext_vector_type(32)));
typedef float f32x2 __attribute__((ext_vector_type(2)));
typedef float f32x4 __attribute__((ext_vector_type(4)));
typedef float f32x16 __attribute__((ext_vector_type(16)));
typedef __bf16 bf16x2 __attribute__((ext_vector_type(2)));
typedef __bf16 bf16x8 __attribute__((ext_vector_type(8)));
typedef __bf16 bf16x16 __attribute__((ext_vector_type(16)));
typedef GAS unsigned gu32;
typedef __amdgpu_buffer_rsrc_t rsrc_t;
typedef LAS unsigned char* lds_t;

#define XCNT(r) ""
#define XT(a, b) ""
#define UCNT ""
#define CL_T
#define GSUM ""
#define ZSUM ""
#define GTRACE
#define MIXED 1
#define NOFLAG 0
#define TOK_KICK 1
#define ZRING 16
#define Z_AT 16
#define Z_GAP 10
#define STR_(x) #x
#define STR(x) STR_(x)
#define GQ_AT 600
#define GK_AT 300
#define PULL_AT 800
#define QK_AT 100
#define SERVER 194
#define SLOW_LEAD 100
#define PULL_DEPTH 1
#define PRIME 2
#define X_AT 150
#define VO_AT 250
#define ZC_AT 0
#define CONV_AUX 2
#define W_AUX 0
#define ZC_PRIME 1
#define CODE_PULL 1
#define PROBES 3
constexpr int NSEG = 64;
constexpr unsigned SENT = 0xFFFFFFFFu;
constexpr int AUX_SC1 = 16, AUX_NT = 2;
constexpr int T_LAG = 61;
constexpr int G_AHEAD = 4;
constexpr unsigned SPIN_CAP = 1u << 21;
constexpr int LDS_BYTES = 163840;

constexpr int C_ECHO = 0, C_CLAIM = 1024, C_VERDICT = 3072, C_TMO = 3328, C_CONV = 3456, C_VOTE = 3840, C_FLAG = 4096, C_ASK = C_FLAG + 256 * 128;
constexpr int F_Q = 0, F_K = 32, F_V = 64, F_S = 96, F_Z = 128;
constexpr int CTRL_BYTES = C_ASK + 256 * 64;
constexpr int MB = 1 << 20;
constexpr int R_U = 0, R_U2 = 3 * MB;
constexpr int R_MEM = 2 * MB, MEM_SLOT = 32768;
constexpr int R_P = R_MEM + 4 * MEM_SLOT, P_SLOT = 65536;
constexpr int R_PT = 4 * MB, PT_SLOT = 131072;
constexpr int R_QX = 8 * MB, R_KX = 40 * MB, R_Z = 72 * MB, Z_SLOT = 4 * MB, R_KVT = R_KX, R_YT = R_KX + 8 * MB, R_END = 208 * MB;

struct Params {
  const u16 *x, *mem0, *Wq, *Wk, *Wv, *Wo, *bq, *bk, *bv, *shift, *ssmk, *ssmD;
  u16* out;
  unsigned char* ctrl;
  unsigned char* data;
};

DEV rsrc_t mkrs(const void* p, unsigned bytes) { return __builtin_amdgcn_make_buffer_rsrc((void*)p, 0, bytes, 0x00020000); }
DEV v4u ld16s(rsrc_t rs, int v, int s) { return __builtin_amdgcn_raw_buffer_load_b128(rs, v, s, AUX_SC1); }
DEV v2u ld8s(rsrc_t rs, int v, int s) { return __builtin_amdgcn_raw_buffer_load_b64(rs, v, s, AUX_SC1); }
DEV v4u ld16(rsrc_t rs, int v, int s) { return __builtin_amdgcn_raw_buffer_load_b128(rs, v, s, 0); }
DEV v4u ld16c(rsrc_t rs, int v, int s) { return __builtin_amdgcn_raw_buffer_load_b128(rs, v, s, CONV_AUX); }
DEV v4u ld16w(rsrc_t rs, int v, int s) { return __builtin_amdgcn_raw_buffer_load_b128(rs, v, s, W_AUX); }
DEV v2u ld8(rsrc_t rs, int v, int s) { return __builtin_amdgcn_raw_buffer_load_b64(rs, v, s, 0); }
DEV void dma16(rsrc_t rs, lds_t l, int v) {
#if __HIP_DEVICE_COMPILE__
  __builtin_amdgcn_raw_ptr_buffer_load_lds(rs, (LAS void*)l, 16, v, 0, 0, 0);
#endif
}
DEV void st16s(rsrc_t rs, int v, int s, v4u d) { __builtin_amdgcn_raw_buffer_store_b128(d, rs, v, s, AUX_SC1); }
DEV void st8s(rsrc_t rs, int v, int s, v2u d) { __builtin_amdgcn_raw_buffer_store_b64(d, rs, v, s, AUX_SC1); }
DEV void st4s(rsrc_t rs, int v, int s, unsigned d) { __builtin_amdgcn_raw_buffer_store_b32(d, rs, v, s, AUX_SC1); }
DEV int amap(int o, int par) { return ((o >> 13) << 14) | (par << 13) | (o & 8191); }
DEV int xblk(int s, int h, int par) { return (s * 32 + h) * 16384 + ((par ^ (s >> 2)) & 1) * 8192; }
DEV unsigned umax(unsigned a, unsigned b) { return a > b ? a : b; }
DEV unsigned top(v4u v) { return umax(umax(v.x, v.y), umax(v.z, v.w)); }
DEV unsigned pk(float a, float b) { bf16x2 v = {(__bf16)a, (__bf16)b}; return __builtin_bit_cast(unsigned, v); }
DEV unsigned pks(float a, float b) { unsigned u = pk(a, b); return u < SENT - 1u ? u : SENT - 1u; }
DEV float blo(unsigned u) { return __builtin_bit_cast(float, u << 16); }
DEV float bhi(unsigned u) { return __builtin_bit_cast(float, u & 0xFFFF0000u); }
DEV float rb(float a) { return blo(pk(a, a)); }
DEV unsigned fu(float f) { return __builtin_bit_cast(unsigned, f); }
DEV float uf(unsigned u) { return __builtin_bit_cast(float, u); }
DEV f32x4 mfma16(v4u a, v4u b, f32x4 c) {
  return __builtin_amdgcn_mfma_f32_16x16x32_bf16(__builtin_bit_cast(bf16x8, a), __builtin_bit_cast(bf16x8, b), c, 0, 0, 0);
}
DEV f32x16 mfma32(v4u a, v4u b, f32x16 c) {
  return __builtin_amdgcn_mfma_f32_32x32x16_bf16(__builtin_bit_cast(bf16x8, a), __builtin_bit_cast(bf16x8, b), c, 0, 0, 0);
}
DEV f32x4 smf(v4u a, v8u b, f32x4 c, int idx) {
  return __builtin_amdgcn_smfmac_f32_16x16x64_bf16(__builtin_bit_cast(bf16x8, a), __builtin_bit_cast(bf16x16, b), c, idx, 0, 0);
}
DEV v8u cat(v4u a, v4u b) { return __builtin_shufflevector(a, b, 0, 1, 2, 3, 4, 5, 6, 7); }
DEV v4u lo4(v8u a) { return __builtin_shufflevector(a, a, 0, 1, 2, 3); }
DEV v4u hi4(v8u a) { return __builtin_shufflevector(a, a, 4, 5, 6, 7); }
DEV v4u lds16(lds_t L, int o) { return *(LAS v4u*)(L + o); }
DEV void sts16(lds_t L, int o, v4u v) { *(LAS v4u*)(L + o) = v; }
DEV void sts8(lds_t L, int o, v2u v) { *(LAS v2u*)(L + o) = v; }
#define SCHED() __builtin_amdgcn_sched_barrier(0)
#define PIN_A(x) asm volatile("" : "+a"(x))
#define PIN_V(x) asm volatile("" : "+v"(x))
DEV int opq(int v) { asm volatile("" : "+v"(v)); return v; }
DEV int fresh_lane() {
  int l; asm volatile("v_mbcnt_lo_u32_b32 %0, -1, 0\n\tv_mbcnt_hi_u32_b32 %0, -1, %0" : "=v"(l)); return l;
}
DEV f32x4 fresh_zero() {
  f32x2 a, b;
  asm volatile("v_mov_b64 %0, 0" : "=v"(a));
  asm volatile("v_mov_b64 %0, 0" : "=v"(b));
  return __builtin_shufflevector(a, b, 0, 1, 2, 3);
}
DEV unsigned fresh_sent() { unsigned z; asm volatile("v_mov_b32 %0, -1" : "=v"(z)); return z; }
#define SMF "v_smfmac_f32_16x16x64_bf16 "
#define MF16 "v_mfma_f32_16x16x32_bf16 "
#define SMF1(cls, d, a, b) asm volatile(SMF "%0, %1, %2, %3" : "+v"(d) : "v"(a), cls(b), "v"(idx))
#define SMF1F(cls, lo, hi, d, a, b) asm volatile(SMF "v[" #lo ":" #hi "], %1, %2, %3" : "+{v[" #lo ":" #hi "]}"(d) : "v"(a), cls(b), "v"(idx))
#define AT_HOME(M, j) do { const int j_ = (j); if (j_ == 0) M("{v[32:39]}"); if (j_ == 1) M("{v[40:47]}"); if (j_ == 2) M("{v[48:55]}"); if (j_ == 3) M("{v[56:63]}"); if (j_ == 4) M("{v[64:71]}"); if (j_ == 5) M("{v[72:79]}"); if (j_ == 6) M("{v[80:87]}"); if (j_ == 7) M("{v[88:95]}"); if (j_ == 8) M("{v[96:103]}"); if (j_ == 9) M("{v[104:111]}"); if (j_ == 10) M("{v[112:119]}"); if (j_ == 11) M("{v[120:127]}"); if (j_ == 12) M("{v[128:135]}"); if (j_ == 13) M("{v[136:143]}"); if (j_ == 14) M("{v[144:151]}"); if (j_ == 15) M("{v[152:159]}"); } while (0)
#define MF1(d, a, b) asm volatile(MF16 "%0, %1, %2, %0" : "+v"(d) : "v"(a), "v"(b))
#define MF1Z(d, a, b) asm volatile(MF16 "%0, %1, %2, 0" : "=&v"(d) : "v"(a), "v"(b))
#define LGKM(n) asm volatile("s_waitcnt lgkmcnt(" #n ")" ::: "memory")
#define RAWBAR() do { __builtin_amdgcn_s_barrier(); asm volatile("" ::: "memory"); } while (0)
DEV void settle3(f32x4& s0, f32x4& s1, f32x4& s2) { asm volatile("s_nop 7" : "+v"(s0), "+v"(s1), "+v"(s2)); }
DEV void settle4(f32x4 (&d)[4]) { asm volatile("s_nop 7" : "+v"(d[0]), "+v"(d[1]), "+v"(d[2]), "+v"(d[3])); }
#define BAR() do { asm volatile("s_waitcnt lgkmcnt(0)" ::: "memory"); __builtin_amdgcn_s_barrier(); asm volatile("" ::: "memory"); } while (0)
#define VMWAIT(n) asm volatile("s_waitcnt vmcnt(" #n ")" ::: "memory")
DEV float add1(float a, float b) {
  float o; asm("v_add_f32 %0, %1, %2" : "=v"(o) : "v"(a), "v"(b)); return o;
}
DEV float xmax(float v) {
  auto r = __builtin_amdgcn_permlane32_swap(fu(v), fu(v), false, false); v = fmaxf(uf(r[0]), uf(r[1]));
  auto q = __builtin_amdgcn_permlane16_swap(fu(v), fu(v), false, false); return fmaxf(uf(q[0]), uf(q[1]));
}
DEV float xsum(float v) {
  auto r = __builtin_amdgcn_permlane32_swap(fu(v), fu(v), false, false); v = uf(r[0]) + uf(r[1]);
  auto q = __builtin_amdgcn_permlane16_swap(fu(v), fu(v), false, false); return uf(q[0]) + uf(q[1]);
}

DEV float ror8(float v) { return uf(__builtin_amdgcn_update_dpp(0u, fu(v), 0x128, 0xf, 0xf, true)); }
DEV float fmax2(float a, float b) { return __builtin_amdgcn_fmed3f(a, b, __builtin_inff()); }
DEV float fmax3(float a, float b, float c) { float r; asm("v_max3_f32 %0, %1, %2, %3" : "=v"(r) : "v"(a), "v"(b), "v"(c)); return r; }
DEV bool allok(unsigned t) { return __all(t != SENT); }

template <int N> struct IC { static constexpr int value = N; };
template <int N, class F> DEV void sfor(F&& f) { if constexpr (N > 0) { sfor<N - 1>(f); f(IC<N - 1>{}); } }
struct Ctx {
  gu32* ctrl;
  rsrc_t rs;
  lds_t L;
  int w, lane;
  int b, slot, att;
  unsigned ask;
  unsigned t0;
  unsigned budget;
};
DEV bool spend(Ctx& c) {
  const unsigned old = c.budget;
  asm volatile("s_cmp_lg_u32 %0, 0\n\ts_subb_u32 %0, %0, 0" : "+s"(c.budget) :: "scc");
  return old == 0u;
}
DEV unsigned wait_flag(Ctx& c, int flag, unsigned need) {
  gu32* f = c.ctrl + (C_FLAG + flag * 128) / 4;
  unsigned v;
#pragma unroll 1
  for (;;) {
    v = __builtin_amdgcn_readfirstlane(__hip_atomic_load(f, RLX_AGENT));
    if (v >= need || spend(c)) break;
    __builtin_amdgcn_s_sleep(2);
  }
  __builtin_amdgcn_fence(__ATOMIC_ACQUIRE, "wavefront");
  return v;
}
DEV void post_flag(Ctx& c, int flag, unsigned v, int off = 0) {
  st4s(mkrs((const void*)c.ctrl, CTRL_BYTES), (c.w | off | fresh_lane()) ? 1 << 30 : 0, C_FLAG + flag * 128, v);
}
constexpr unsigned CL_A = 1u << 16, CL_VOTED = 1u << 24, CL_FILLED = 1u << 28;
DEV void claim(Ctx& c, unsigned votes) {
  if (c.w != 0) return;
  if (c.att == 0) {
#pragma unroll 1
    while (__builtin_amdgcn_readfirstlane(__hip_atomic_load(c.ctrl + C_ECHO / 4 + c.b, RLX_AGENT)) != c.ask) {
      if (spend(c)) break;
      __builtin_amdgcn_s_sleep(2);
    }
  }
  if (fresh_lane() == 0) __hip_atomic_fetch_add(c.ctrl + (C_CLAIM + c.att * 1024) / 4 + c.slot, 1u + votes, RLX_AGENT);
}
DEV unsigned wait_verdict(Ctx& c) {
  unsigned v;
#pragma unroll 1
  for (;;) {
    v = __builtin_amdgcn_readfirstlane(__hip_atomic_load(c.ctrl + (C_VERDICT + c.att * 128) / 4, RLX_AGENT));
    if ((v & 1u) || spend(c)) break;
    __builtin_amdgcn_s_sleep(2);
  }
  __builtin_amdgcn_fence(__ATOMIC_ACQUIRE, "wavefront");
  if ((v & 3u) == 3u && c.att == 0) {
    asm volatile("s_waitcnt vmcnt(0) lgkmcnt(0)\n\ts_barrier\n\ts_mov_b64 s[0:1], %0\n\ts_mov_b32 s2, %1\n\tv_mov_b32 v0, %2\n\t"
                 "s_getpc_b64 s[4:5]\n1:\n\ts_sub_u32 s4, s4, 1b-mk_mega\n\ts_subb_u32 s5, s5, 0\n\ts_setpc_b64 s[4:5]"
                 :: "s"(__builtin_amdgcn_kernarg_segment_ptr()), "s"(c.b + 256), "v"(c.w * 64 + fresh_lane()) : "s0", "s1", "s2", "s4", "s5", "v0", "scc", "memory");
    __builtin_unreachable();
  }
  return v;
}
DEV unsigned wait_vote(Ctx& c) {
  unsigned v;
#pragma unroll 1
  for (;;) {
    v = __builtin_amdgcn_readfirstlane(__hip_atomic_load(c.ctrl + C_VOTE / 4, RLX_AGENT));
    if ((v & 1u) || spend(c)) break;
    __builtin_amdgcn_s_sleep(2);
  }
  return v;
}
DEV void serve(Ctx& c) {
  c.budget = SPIN_CAP;
  const int lane = fresh_lane();
  const rsrc_t rc = mkrs((const void*)c.ctrl, CTRL_BYTES);
  if (c.att == 0) {
#pragma unroll
    for (int i = C_CLAIM / 1024; i < C_ASK / 1024; ++i) st16s(rc, lane * 16, i * 1024, v4u{0, 0, 0, 0});
    VMWAIT(0);
  }
  unsigned vote = c.att ? wait_vote(c) : 0u;
  bool echo = c.att == 0, first = true;
  v4u last = {0u, 0u, 0u, 0u};
#pragma unroll 1
  for (;;) {
    asm volatile("" ::: "memory");
    const v4u cl = ld16s(rc, lane * 16, C_CLAIM + c.att * 1024);
    if (echo) {
      v4u e;
#pragma unroll
      for (int i = 0; i < 4; ++i) e[i] = __builtin_amdgcn_raw_buffer_load_b32(rc, (lane * 4 + i) * 64, C_ASK, AUX_SC1);
      if (first || __any(e[0] != last[0] || e[1] != last[1] || e[2] != last[2] || e[3] != last[3])) st16s(rc, lane * 16, C_ECHO, e);
      last = e; first = false;
    }
    unsigned n = 0, one = 1;
    int va = 0, nv = 0, nf = 0;
#pragma unroll
    for (int i = 0; i < 4; ++i) {
      n += cl[i] & 0xFFFFu; one &= (cl[i] & 0xFFFFu) == 1u;
      va += __popcll(__ballot(cl[i] & CL_A)); nv += __popcll(__ballot(cl[i] & CL_VOTED)); nf += __popcll(__ballot(cl[i] & CL_FILLED));
    }
#pragma unroll
    for (int o = 1; o < 64; o <<= 1) n += (unsigned)__shfl_xor((int)n, o);
    const bool all = __builtin_amdgcn_readfirstlane(n) >= 256u, good = __all(one);
    echo = echo && !all;
    if (!vote && (all || max(va, nv - va) > 32)) {
      vote = va > 32 ? 5u : 9u;
      if (lane == 0) __hip_atomic_store(c.ctrl + C_VOTE / 4, vote, RLX_AGENT);
    }
    if (all && (!good || nf >= 64)) {
      if (lane == 0) __hip_atomic_store(c.ctrl + (C_VERDICT + c.att * 128) / 4, vote | (good ? 0u : 2u), RLX_AGENT);
      break;
    }
    if (spend(c)) break;
  }
}
DEV int vote_a(unsigned v) { return (int)((v >> 2) & 1u); }
DEV int vote_b(unsigned v) { return (int)((v >> 3) & 1u); }

#define STAMP(slot) do {} while (0)
#define TICK_DECL
#define TICK(i) do {} while (0)
#define TICK_SUM(n) do {} while (0)
#define TICK_LDS(b) do {} while (0)
#define TICK_DUMP(b) do {} while (0)
#define TICK_OUT() do {} while (0)

DEV constexpr int krow(int rho8, int i) { return 32 * (rho8 >> 2) + 8 * (rho8 & 1) + 2 * ((rho8 >> 1) & 1) + i; }
DEV int krow_lane(int g) { return 16 * (g >> 1) + 4 * (g & 1); }
DEV unsigned prm(unsigned hi, unsigned lo, int odd) { return __builtin_amdgcn_perm(hi, lo, odd ? 0x07060302u : 0x05040100u); }

#define AT_ACC(M, n) do { const int n_ = (n); if (n_ == 0) M("{a[0:7]}"); if (n_ == 1) M("{a[8:15]}"); if (n_ == 2) M("{a[16:23]}"); if (n_ == 3) M("{a[24:31]}"); if (n_ == 4) M("{a[32:39]}"); if (n_ == 5) M("{a[40:47]}"); if (n_ == 6) M("{a[48:55]}"); if (n_ == 7) M("{a[56:63]}"); if (n_ == 8) M("{a[64:71]}"); if (n_ == 9) M("{a[72:79]}"); if (n_ == 10) M("{a[80:87]}"); if (n_ == 11) M("{a[88:95]}"); if (n_ == 12) M("{a[96:103]}"); if (n_ == 13) M("{a[104:111]}"); if (n_ == 14) M("{a[112:119]}"); if (n_ == 15) M("{a[120:127]}"); if (n_ == 16) M("{a[128:135]}"); if (n_ == 17) M("{a[136:143]}"); if (n_ == 18) M("{a[144:151]}"); if (n_ == 19) M("{a[152:159]}"); if (n_ == 20) M("{a[160:167]}"); if (n_ == 21) M("{a[168:175]}"); if (n_ == 22) M("{a[176:183]}"); if (n_ == 23) M("{a[184:191]}"); if (n_ == 24) M("{a[192:199]}"); if (n_ == 25) M("{a[200:207]}"); if (n_ == 26) M("{a[208:215]}"); if (n_ == 27) M("{a[216:223]}"); if (n_ == 28) M("{a[224:231]}"); if (n_ == 29) M("{a[232:239]}"); if (n_ == 30) M("{a[240:247]}"); if (n_ == 31) M("{a[248:255]}"); } while (0)
#define PIN_ACC0(x) asm volatile("" :: "{a[0:7]}"(x[0]), "{a[8:15]}"(x[1]), "{a[16:23]}"(x[2]), "{a[24:31]}"(x[3]), "{a[32:39]}"(x[4]), "{a[40:47]}"(x[5]), "{a[48:55]}"(x[6]), "{a[56:63]}"(x[7]), "{a[64:71]}"(x[8]), "{a[72:79]}"(x[9]), "{a[80:87]}"(x[10]), "{a[88:95]}"(x[11]), "{a[96:103]}"(x[12]), "{a[104:111]}"(x[13]), "{a[112:119]}"(x[14]), "{a[120:127]}"(x[15]))
#define PIN_ACC1(x) asm volatile("" :: "{a[128:135]}"(x[16]), "{a[136:143]}"(x[17]), "{a[144:151]}"(x[18]), "{a[152:159]}"(x[19]), "{a[160:167]}"(x[20]), "{a[168:175]}"(x[21]), "{a[176:183]}"(x[22]), "{a[184:191]}"(x[23]), "{a[192:199]}"(x[24]), "{a[200:207]}"(x[25]), "{a[208:215]}"(x[26]), "{a[216:223]}"(x[27]), "{a[224:231]}"(x[28]), "{a[232:239]}"(x[29]), "{a[240:247]}"(x[30]), "{a[248:255]}"(x[31]))
#define PIN_HOME(x) asm volatile("" :: "{v[32:39]}"(x[0]), "{v[40:47]}"(x[1]), "{v[48:55]}"(x[2]), "{v[56:63]}"(x[3]), "{v[64:71]}"(x[4]), "{v[72:79]}"(x[5]), "{v[80:87]}"(x[6]), "{v[88:95]}"(x[7]), "{v[96:103]}"(x[8]), "{v[104:111]}"(x[9]), "{v[112:119]}"(x[10]), "{v[120:127]}"(x[11]), "{v[128:135]}"(x[12]), "{v[136:143]}"(x[13]), "{v[144:151]}"(x[14]), "{v[152:159]}"(x[15]))
#define CL_32_100 "v32", "v33", "v34", "v35", "v36", "v37", "v38", "v39", "v40", "v41", "v42", "v43", "v44", "v45", "v46", "v47", "v48", "v49", "v50", "v51", "v52", "v53", "v54", "v55", "v56", "v57", "v58", "v59", "v60", "v61", "v62", "v63", "v64", "v65", "v66", "v67", "v68", "v69", "v70", "v71", "v72", "v73", "v74", "v75", "v76", "v77", "v78", "v79", "v80", "v81", "v82", "v83", "v84", "v85", "v86", "v87", "v88", "v89", "v90", "v91", "v92", "v93", "v94", "v95", "v96", "v97", "v98", "v99", "v100", "v101", "v102", "v103", "v104", "v105", "v106", "v107", "v108", "v109", "v110", "v111", "v112", "v113", "v114", "v115", "v116", "v117", "v118", "v119", "v120", "v121", "v122", "v123", "v124", "v125", "v126", "v127", "v128", "v129", "v130", "v131"
#define ACC_IN(x) "{a[0:31]}"(x[0]), "{a[32:63]}"(x[1]), "{a[64:95]}"(x[2]), "{a[96:127]}"(x[3]), "{a[128:159]}"(x[4]), "{a[160:191]}"(x[5]), "{a[192:223]}"(x[6]), "{a[224:255]}"(x[7])
#define CL_16_32 "v16", "v17", "v18", "v19", "v20", "v21", "v22", "v23", "v24", "v25", "v26", "v27", "v28", "v29", "v30", "v31"
#define CL_120_128 "v120", "v121", "v122", "v123", "v124", "v125", "v126", "v127"
#define CL_160_200 "v160", "v161", "v162", "v163", "v164", "v165", "v166", "v167", "v168", "v169", "v170", "v171", "v172", "v173", "v174", "v175", "v176", "v177", "v178", "v179", "v180", "v181", "v182", "v183", "v184", "v185", "v186", "v187", "v188", "v189", "v190", "v191", "v192", "v193", "v194", "v195", "v196", "v197", "v198", "v199"
#define CL_176_200 "v176", "v177", "v178", "v179", "v180", "v181", "v182", "v183", "v184", "v185", "v186", "v187", "v188", "v189", "v190", "v191", "v192", "v193", "v194", "v195", "v196", "v197", "v198", "v199"
#define CL_200_216 "v200", "v201", "v202", "v203", "v204", "v205", "v206", "v207", "v208", "v209", "v210", "v211", "v212", "v213", "v214", "v215"
#define CL_216_256 "v216", "v217", "v218", "v219", "v220", "v221", "v222", "v223", "v224", "v225", "v226", "v227", "v228", "v229", "v230", "v231", "v232", "v233", "v234", "v235", "v236", "v237", "v238", "v239", "v240", "v241", "v242", "v243", "v244", "v245", "v246", "v247", "v248", "v249", "v250", "v251", "v252", "v253", "v254", "v255"
#define CL_S "memory", "vcc", "scc", "s60", "s61", "s62", "s63", "s64", "s65", "s66", "s67" CL_T
#define TIMED_WAIT \
    "s_getpc_b64 s[62:63]\n\t" "1:\n\t" "s_memtime s[64:65]\n\t" "s_waitcnt lgkmcnt(0)\n\t" "s_sub_u32 s61, %0, s64\n\t" "s_cmp_gt_i32 s61, 124\n\t" "s_cbranch_scc1 1b\n\t" \
    "s_max_i32 s61, s61, 0\n\t" "s_and_b32 s61, s61, 124\n\t" "s_sub_u32 s61, 2f-1b, s61\n\t" "s_add_u32 s62, s62, s61\n\t" "s_addc_u32 s63, s63, 0\n\t" \
    "s_setpc_b64 s[62:63]\n\t" ".rept 31\n\t" "s_nop 0\n\t" ".endr\n\t" "2:\n\t"
#define QK_STEP0 \
    "v_xor_b32 v216, 0x400, %[mo]\n\t" "s_mov_b32 s60, 0\n\t" "s_mov_b32 s66, 0x3e0293ee\n\t" "s_mov_b32 s67, 0x3e0293ee\n\t" XT(80, 81) "s_add_u32 s61, %[kb], 0x1000\n\t" \
    "buffer_load_dwordx4 v[16:19], %[l1], %[rs], %[kb] offen offset:0 sc1\n\t" "buffer_load_dwordx4 v[20:23], %[l1], %[rs], %[kb] offen offset:1024 sc1\n\t" \
    "buffer_load_dwordx4 v[24:27], %[l1], %[rs], %[kb] offen offset:2048 sc1\n\t" "buffer_load_dwordx4 v[28:31], %[l1], %[rs], %[kb] offen offset:3072 sc1\n\t" \
    "buffer_load_dwordx4 v[200:203], %[l1], %[rs], s61 offen offset:0 sc1\n\t" "buffer_load_dwordx4 v[204:207], %[l1], %[rs], s61 offen offset:1024 sc1\n\t" \
    "buffer_load_dwordx4 v[208:211], %[l1], %[rs], s61 offen offset:2048 sc1\n\t" "buffer_load_dwordx4 v[212:215], %[l1], %[rs], s61 offen offset:3072 sc1\n\t" \
    "v_mov_b64 v[218:219], -1\n\t" "buffer_store_dwordx2 v[218:219], %[px], %[rs], %[pr] offen sc1\n\t" "buffer_store_dword v218, %[pm], %[rs], %[pr] offen sc1\n\t" \
    "s_waitcnt vmcnt(2)\n\t" XT(98, 99) "s_barrier\n\t" XT(82, 83) "10:\n\t" "buffer_load_dwordx4 v[240:243], %[mo], %[rs], %[sb] offen offset:0 sc1\n\t" \
    "v_mov_b64 v[224:225], 0\n\t" "v_mov_b64 v[226:227], 0\n\t" "v_mov_b64 v[228:229], 0\n\t" "v_mov_b64 v[230:231], 0\n\t" "v_mov_b64 v[232:233], 0\n\t" \
    "v_mov_b64 v[234:235], 0\n\t" "v_mov_b64 v[236:237], 0\n\t" "v_mov_b64 v[238:239], 0\n\t" "buffer_load_dwordx4 v[244:247], v216, %[rs], %[sb] offen offset:0 sc1\n\t" \
    "ds_read_b128 v[160:163], %[l0] offset:0\n\t" "ds_read_b128 v[164:167], %[l0] offset:1024\n\t" "ds_read_b128 v[168:171], %[l0] offset:2048\n\t" \
    "ds_read_b128 v[172:175], %[l0] offset:3072\n\t" "s_nop 3\n\t" "buffer_load_dwordx4 v[248:251], %[mo], %[rs], %[sb] offen offset:2048 sc1\n\t" \
    "ds_read_b128 v[176:179], %[l0] offset:4096\n\t" "ds_read_b128 v[180:183], %[l0] offset:5120\n\t" "ds_read_b128 v[184:187], %[l0] offset:6144\n\t" \
    "ds_read_b128 v[188:191], %[l0] offset:7168\n\t" "s_nop 3\n\t" "buffer_load_dwordx4 v[252:255], v216, %[rs], %[sb] offen offset:2048 sc1\n\t" "s_cmp_lg_u32 s61, 0\n\t" \
    "s_cbranch_scc0 177f\n\t" "ds_write_b128 %[l1], v[16:19] offset:16384\n\t" "ds_write_b128 %[l1], v[20:23] offset:17408\n\t" \
    "ds_write_b128 %[l1], v[24:27] offset:18432\n\t" "ds_write_b128 %[l1], v[28:31] offset:19456\n\t" "ds_write_b128 %[l1], v[200:203] offset:20480\n\t" \
    "ds_write_b128 %[l1], v[204:207] offset:21504\n\t" "ds_write_b128 %[l1], v[208:211] offset:22528\n\t" "ds_write_b128 %[l1], v[212:215] offset:23552\n\t" \
    "s_mov_b32 s61, 0\n\t" "s_waitcnt lgkmcnt(0)\n\t" "177:\n\t" "v_mov_b64 v[208:209], 0\n\t" "v_mov_b64 v[210:211], 0\n\t" "v_mov_b64 v[212:213], 0\n\t" \
    "v_mov_b64 v[214:215], 0\n\t" "s_waitcnt vmcnt(3)\n\t" XT(84, 85) "v_smfmac_f32_16x16x64_bf16 v[224:227], v[240:243], a[0:7], %[ix]\n\t" \
    "ds_write_b128 %[l1], v[240:243] offset:0\n\t" "v_smfmac_f32_16x16x64_bf16 v[228:231], v[240:243], a[8:15], %[ix]\n\t" "v_max3_u32 v217, v240, v241, v242\n\t" \
    "v_smfmac_f32_16x16x64_bf16 v[232:235], v[240:243], v[32:39], %[ix]\n\t" "s_waitcnt lgkmcnt(7)\n\t" \
    "v_smfmac_f32_16x16x64_bf16 v[236:239], v[240:243], v[160:167], %[ix]\n\t" "ds_read_b128 v[160:163], %[l0] offset:8192\n\t" \
    "ds_read_b128 v[164:167], %[l0] offset:9216\n\t" "s_waitcnt vmcnt(2)\n\t" "v_smfmac_f32_16x16x64_bf16 v[224:227], v[244:247], a[16:23], %[ix]\n\t" \
    "ds_write_b128 %[l1], v[244:247] offset:1024\n\t" "v_max3_u32 v217, v217, v243, v244\n\t" "v_smfmac_f32_16x16x64_bf16 v[228:231], v[244:247], a[24:31], %[ix]\n\t" \
    "v_max3_u32 v217, v217, v245, v246\n\t" "v_max_u32 v217, v217, v247\n\t" "v_smfmac_f32_16x16x64_bf16 v[232:235], v[244:247], v[40:47], %[ix]\n\t" \
    "v_cmp_eq_u32 vcc, -1, v217\n\t" "s_cmp_lg_u32 s60, 0\n\t" "s_waitcnt lgkmcnt(8)\n\t" "v_smfmac_f32_16x16x64_bf16 v[236:239], v[244:247], v[168:175], %[ix]\n\t" \
    "s_cbranch_vccnz 11f\n\t" "12:\n\t" "ds_read_b128 v[168:171], %[l0] offset:10240\n\t" "s_waitcnt vmcnt(1)\n\t" \
    "v_smfmac_f32_16x16x64_bf16 v[224:227], v[248:251], a[32:39], %[ix]\n\t" "ds_write_b128 %[l1], v[248:251] offset:2048\n\t" "s_waitcnt lgkmcnt(2)\n\t" \
    "v_smfmac_f32_16x16x64_bf16 v[228:231], v[248:251], a[40:47], %[ix]\n\t" "s_cbranch_scc1 13f\n\t" "s_barrier\n\t" "13:\n\t" XT(86, 87) \
    "v_smfmac_f32_16x16x64_bf16 v[232:235], v[248:251], v[48:55], %[ix]\n\t" "ds_read_b128 v[192:195], %[l1] offset:4096\n\t" "ds_read_b128 v[196:199], %[l1] offset:8192\n\t" \
    "v_smfmac_f32_16x16x64_bf16 v[236:239], v[248:251], v[176:183], %[ix]\n\t" "ds_read_b128 v[172:175], %[l0] offset:11264\n\t" \
    "ds_read_b128 v[176:179], %[l0] offset:12288\n\t" "s_waitcnt vmcnt(0)\n\t" "v_smfmac_f32_16x16x64_bf16 v[224:227], v[252:255], a[48:55], %[ix]\n\t" \
    "ds_write_b128 %[l1], v[252:255] offset:3072\n\t" "v_max3_u32 v217, v248, v249, v250\n\t" "v_smfmac_f32_16x16x64_bf16 v[228:231], v[252:255], a[56:63], %[ix]\n\t" \
    "ds_read_b128 v[180:183], %[l0] offset:13312\n\t" "v_max3_u32 v217, v217, v251, v252\n\t" "v_max3_u32 v217, v217, v253, v254\n\t" \
    "v_smfmac_f32_16x16x64_bf16 v[232:235], v[252:255], v[56:63], %[ix]\n\t" "v_max_u32 v217, v217, v255\n\t" "v_cmp_eq_u32 vcc, -1, v217\n\t" \
    "v_smfmac_f32_16x16x64_bf16 v[236:239], v[252:255], v[184:191], %[ix]\n\t" "s_cbranch_vccnz 14f\n\t" "15:\n\t" "ds_read_b128 v[240:243], %[l1] offset:12288\n\t" \
    "s_waitcnt lgkmcnt(6)\n\t" "v_smfmac_f32_16x16x64_bf16 v[224:227], v[192:195], a[64:71], %[ix]\n\t" "ds_read_b128 v[184:187], %[l0] offset:14336\n\t" \
    "ds_read_b128 v[188:191], %[l0] offset:15360\n\t" "v_smfmac_f32_16x16x64_bf16 v[228:231], v[192:195], a[72:79], %[ix]\n\t" \
    "ds_read_b128 v[244:247], %[l1] offset:5120\n\t" "s_waitcnt lgkmcnt(5)\n\t" "v_smfmac_f32_16x16x64_bf16 v[232:235], v[192:195], v[64:71], %[ix]\n\t" "s_barrier\n\t" \
    XT(88, 89) "v_smfmac_f32_16x16x64_bf16 v[236:239], v[192:195], v[160:167], %[ix]\n\t" "ds_read_b128 v[160:163], %[l0] offset:16384\n\t" \
    "ds_read_b128 v[164:167], %[l0] offset:17408\n\t" "v_smfmac_f32_16x16x64_bf16 v[224:227], v[196:199], a[80:87], %[ix]\n\t" \
    "ds_read_b128 v[248:251], %[l1] offset:9216\n\t" "ds_read_b128 v[252:255], %[l1] offset:13312\n\t" \
    "v_smfmac_f32_16x16x64_bf16 v[228:231], v[196:199], a[88:95], %[ix]\n\t" "ds_read_b128 v[192:195], %[l1] offset:6144\n\t" \
    "v_smfmac_f32_16x16x64_bf16 v[232:235], v[196:199], v[72:79], %[ix]\n\t" "ds_read_b128 v[16:19], %[kx] offset:0\n\t" \
    "v_smfmac_f32_16x16x64_bf16 v[236:239], v[196:199], v[168:175], %[ix]\n\t" "ds_read_b128 v[168:171], %[l0] offset:18432\n\t" \
    "ds_read_b128 v[172:175], %[l0] offset:19456\n\t" "s_waitcnt lgkmcnt(11)\n\t" "v_smfmac_f32_16x16x64_bf16 v[224:227], v[240:243], a[96:103], %[ix]\n\t" \
    "ds_read_b128 v[196:199], %[l1] offset:10240\n\t" "v_smfmac_f32_16x16x64_bf16 v[228:231], v[240:243], a[104:111], %[ix]\n\t" \
    "ds_read_b128 v[20:23], %[kx] offset:2048\n\t" "v_smfmac_f32_16x16x64_bf16 v[232:235], v[240:243], v[80:87], %[ix]\n\t" \
    "v_smfmac_f32_16x16x64_bf16 v[236:239], v[240:243], v[176:183], %[ix]\n\t" "ds_read_b128 v[176:179], %[l0] offset:20480\n\t" \
    "ds_read_b128 v[180:183], %[l0] offset:21504\n\t" "s_waitcnt lgkmcnt(12)\n\t" "v_smfmac_f32_16x16x64_bf16 v[224:227], v[244:247], a[112:119], %[ix]\n\t" \
    "ds_read_b128 v[240:243], %[l1] offset:14336\n\t" "ds_read_b128 v[24:27], %[kx] offset:4096\n\t" \
    "v_smfmac_f32_16x16x64_bf16 v[228:231], v[244:247], a[120:127], %[ix]\n\t" "v_smfmac_f32_16x16x64_bf16 v[232:235], v[244:247], v[88:95], %[ix]\n\t" \
    "v_smfmac_f32_16x16x64_bf16 v[236:239], v[244:247], v[184:191], %[ix]\n\t" "ds_read_b128 v[184:187], %[l0] offset:22528\n\t" \
    "ds_read_b128 v[188:191], %[l0] offset:23552\n\t" "s_waitcnt lgkmcnt(13)\n\t" "v_smfmac_f32_16x16x64_bf16 v[224:227], v[248:251], a[128:135], %[ix]\n\t" \
    "ds_read_b128 v[244:247], %[l1] offset:7168\n\t" "ds_read_b128 v[28:31], %[kx] offset:6144\n\t" "v_smfmac_f32_16x16x64_bf16 v[228:231], v[248:251], a[136:143], %[ix]\n\t" \
    "v_smfmac_f32_16x16x64_bf16 v[232:235], v[248:251], v[96:103], %[ix]\n\t" "ds_read_b128 v[200:203], %[kx] offset:256\n\t" \
    "v_smfmac_f32_16x16x64_bf16 v[236:239], v[248:251], v[160:167], %[ix]\n\t" "ds_read_b128 v[160:163], %[l0] offset:24576\n\t" \
    "ds_read_b128 v[164:167], %[l0] offset:25600\n\t" "s_waitcnt lgkmcnt(14)\n\t" "v_smfmac_f32_16x16x64_bf16 v[224:227], v[252:255], a[144:151], %[ix]\n\t" \
    "ds_read_b128 v[248:251], %[l1] offset:11264\n\t" "v_smfmac_f32_16x16x64_bf16 v[228:231], v[252:255], a[152:159], %[ix]\n\t" \
    "ds_read_b128 v[204:207], %[kx] offset:2304\n\t" "v_smfmac_f32_16x16x64_bf16 v[232:235], v[252:255], v[104:111], %[ix]\n\t" "s_waitcnt lgkmcnt(14)\n\t" \
    "v_smfmac_f32_16x16x64_bf16 v[236:239], v[252:255], v[168:175], %[ix]\n\t" "ds_read_b128 v[168:171], %[l0] offset:26624\n\t" \
    "ds_read_b128 v[172:175], %[l0] offset:27648\n\t" "v_smfmac_f32_16x16x64_bf16 v[224:227], v[192:195], a[160:167], %[ix]\n\t" \
    "ds_read_b128 v[252:255], %[l1] offset:15360\n\t" "v_smfmac_f32_16x16x64_bf16 v[228:231], v[192:195], a[168:175], %[ix]\n\t" \
    "v_smfmac_f32_16x16x64_bf16 v[224:227], v[196:199], a[176:183], %[ix]\n\t" "v_smfmac_f32_16x16x64_bf16 v[228:231], v[196:199], a[184:191], %[ix]\n\t" \
    "s_waitcnt lgkmcnt(13)\n\t" "v_smfmac_f32_16x16x64_bf16 v[224:227], v[240:243], a[192:199], %[ix]\n\t" \
    "v_smfmac_f32_16x16x64_bf16 v[228:231], v[240:243], a[200:207], %[ix]\n\t" "s_waitcnt lgkmcnt(9)\n\t" \
    "v_smfmac_f32_16x16x64_bf16 v[224:227], v[244:247], a[208:215], %[ix]\n\t" "v_smfmac_f32_16x16x64_bf16 v[228:231], v[244:247], a[216:223], %[ix]\n\t" \
    "s_waitcnt lgkmcnt(4)\n\t" "v_smfmac_f32_16x16x64_bf16 v[224:227], v[248:251], a[224:231], %[ix]\n\t" \
    "v_smfmac_f32_16x16x64_bf16 v[228:231], v[248:251], a[232:239], %[ix]\n\t" "s_waitcnt lgkmcnt(0)\n\t" \
    "v_smfmac_f32_16x16x64_bf16 v[224:227], v[252:255], a[240:247], %[ix]\n\t" "s_nop 1\n\t" "v_smfmac_f32_16x16x64_bf16 v[228:231], v[252:255], a[248:255], %[ix]\n\t" \
    XT(90, 91) "s_nop 1\n\t" "v_smfmac_f32_16x16x64_bf16 v[232:235], v[192:195], v[112:119], %[ix]\n\t" "s_nop 1\n\t" \
    "v_smfmac_f32_16x16x64_bf16 v[236:239], v[192:195], v[176:183], %[ix]\n\t" "ds_read_b128 v[176:179], %[l0] offset:28672\n\t" "v_add_f32 v224, v224, v225\n\t" \
    "v_add_f32 v226, v226, v227\n\t" "v_smfmac_f32_16x16x64_bf16 v[232:235], v[196:199], v[120:127], %[ix]\n\t" "ds_read_b128 v[180:183], %[l0] offset:29696\n\t" \
    "v_add_f32 v228, v228, v229\n\t" "v_add_f32 v230, v230, v231\n\t" "v_smfmac_f32_16x16x64_bf16 v[236:239], v[196:199], v[184:191], %[ix]\n\t" \
    "ds_read_b128 v[184:187], %[l0] offset:30720\n\t" "v_cvt_pk_bf16_f32 v218, v224, v228\n\t" "v_cvt_pk_bf16_f32 v219, v226, v230\n\t" \
    "v_smfmac_f32_16x16x64_bf16 v[232:235], v[240:243], v[128:135], %[ix]\n\t" "ds_write2_b32 %[qw], v218, v219 offset1:8\n\t" \
    "ds_read_b128 v[188:191], %[l0] offset:31744\n\t" "v_smfmac_f32_16x16x64_bf16 v[236:239], v[240:243], v[160:167], %[ix]\n\t" \
    "ds_read_b128 v[192:195], %[kx] offset:4352\n\t" "ds_read_b128 v[196:199], %[kx] offset:6400\n\t" \
    "v_smfmac_f32_16x16x64_bf16 v[232:235], v[244:247], v[136:143], %[ix]\n\t" "v_smfmac_f32_16x16x64_bf16 v[236:239], v[244:247], v[168:175], %[ix]\n\t" \
    "s_waitcnt lgkmcnt(3)\n\t" "s_barrier\n\t" "v_smfmac_f32_16x16x64_bf16 v[232:235], v[248:251], v[144:151], %[ix]\n\t" "ds_read_b128 v[216:219], %[l1] offset:24576\n\t" \
    "ds_read_b128 v[220:223], %[l1] offset:25600\n\t" "v_smfmac_f32_16x16x64_bf16 v[236:239], v[248:251], v[176:183], %[ix]\n\t" \
    "v_smfmac_f32_16x16x64_bf16 v[232:235], v[252:255], v[152:159], %[ix]\n\t" "s_waitcnt lgkmcnt(4)\n\t" \
    "v_smfmac_f32_16x16x64_bf16 v[236:239], v[252:255], v[184:191], %[ix]\n\t" XT(92, 93) "s_waitcnt lgkmcnt(1)\n\t" \
    "v_smfmac_f32_16x16x64_bf16 v[208:211], v[216:219], v[16:23], %[ix]\n\t" "s_nop 1\n\t" "v_smfmac_f32_16x16x64_bf16 v[212:215], v[216:219], v[200:207], %[ix]\n\t" \
    "s_waitcnt lgkmcnt(0)\n\t" "s_nop 0\n\t" "v_add_f32 v232, v232, v233\n\t" "v_add_f32 v234, v234, v235\n\t" "v_add_f32 v236, v236, v237\n\t" \
    "v_add_f32 v238, v238, v239\n\t" "v_cvt_pk_bf16_f32 v232, v232, v236\n\t" "v_cvt_pk_bf16_f32 v233, v234, v238\n\t" "ds_write2_b32 %[kw], v232, v233 offset1:68\n\t" \
    "v_smfmac_f32_16x16x64_bf16 v[208:211], v[220:223], v[24:31], %[ix]\n\t" "v_smfmac_f32_16x16x64_bf16 v[212:215], v[220:223], v[192:199], %[ix]\n\t" \
    "v_mov_b64 v[224:225], 0\n\t" "v_mov_b64 v[226:227], 0\n\t" "s_waitcnt lgkmcnt(0)\n\t" "s_barrier\n\t" XT(94, 95) "ds_read_b128 v[160:163], %[km] offset:0\n\t" \
    "ds_read_b128 v[164:167], %[km] offset:64\n\t" "ds_read_b128 v[168:171], %[km] offset:128\n\t" "ds_read_b128 v[172:175], %[km] offset:192\n\t" \
    "v_add_f32 v178, v208, v209\n\t" "v_add_f32 v179, v210, v211\n\t" "v_add_f32 v180, v212, v213\n\t" "v_add_f32 v181, v214, v215\n\t" "v_max_f32 v182, v178, v180\n\t" \
    "v_max_f32 v183, v179, v181\n\t" "s_nop 0\n\t" "v_max_f32_dpp v182, v182, v182 row_ror:8 row_mask:0xf bank_mask:0xf\n\t" \
    "v_max_f32_dpp v183, v183, v183 row_ror:8 row_mask:0xf bank_mask:0xf\n\t" "s_nop 0\n\t" "v_max_f32_dpp v182, v182, v182 row_ror:4 row_mask:0xf bank_mask:0xf\n\t" \
    "v_max_f32_dpp v183, v183, v183 row_ror:4 row_mask:0xf bank_mask:0xf\n\t" "s_nop 0\n\t" "v_max_f32_dpp v182, v182, v182 row_ror:2 row_mask:0xf bank_mask:0xf\n\t" \
    "v_max_f32_dpp v183, v183, v183 row_ror:2 row_mask:0xf bank_mask:0xf\n\t" "s_nop 0\n\t" "v_max_f32_dpp v182, v182, v182 row_ror:1 row_mask:0xf bank_mask:0xf\n\t" \
    "v_max_f32_dpp v183, v183, v183 row_ror:1 row_mask:0xf bank_mask:0xf\n\t" "s_waitcnt lgkmcnt(2)\n\t" \
    "v_smfmac_f32_16x16x64_bf16 v[224:227], v[216:219], v[160:167], %[ix]\n\t" "s_waitcnt lgkmcnt(0)\n\t" \
    "v_smfmac_f32_16x16x64_bf16 v[224:227], v[220:223], v[168:175], %[ix]\n\t" "s_nop 7\n\t" "v_add_f32 v176, v224, v225\n\t" "v_add_f32 v177, v226, v227\n\t" \
    "v_max_f32 v182, v176, v182\n\t" "v_max_f32 v183, v177, v183\n\t" "s_nop 0\n\t" "v_max_f32_dpp v182, v182, v182 row_ror:4 row_mask:0xf bank_mask:0xf\n\t" \
    "v_max_f32_dpp v183, v183, v183 row_ror:4 row_mask:0xf bank_mask:0xf\n\t" "s_nop 0\n\t" "v_max_f32_dpp v182, v182, v182 row_ror:2 row_mask:0xf bank_mask:0xf\n\t" \
    "v_max_f32_dpp v183, v183, v183 row_ror:2 row_mask:0xf bank_mask:0xf\n\t" "s_nop 0\n\t" "v_max_f32_dpp v182, v182, v182 row_ror:1 row_mask:0xf bank_mask:0xf\n\t" \
    "v_max_f32_dpp v183, v183, v183 row_ror:1 row_mask:0xf bank_mask:0xf\n\t" "v_pk_add_f32 v[178:179], v[178:179], v[182:183] neg_lo:[0,1] neg_hi:[0,1]\n\t" \
    "v_pk_add_f32 v[180:181], v[180:181], v[182:183] neg_lo:[0,1] neg_hi:[0,1]\n\t" "v_pk_add_f32 v[176:177], v[176:177], v[182:183] neg_lo:[0,1] neg_hi:[0,1]\n\t" \
    "v_pk_mul_f32 v[178:179], v[178:179], s[66:67]\n\t" "v_pk_mul_f32 v[180:181], v[180:181], s[66:67]\n\t" "v_pk_mul_f32 v[176:177], v[176:177], s[66:67]\n\t" \
    "v_exp_f32 v178, v178\n\t" "v_exp_f32 v179, v179\n\t" "v_exp_f32 v180, v180\n\t" "v_exp_f32 v181, v181\n\t" "v_exp_f32 v176, v176\n\t" \
    "v_pk_add_f32 v[182:183], v[178:179], v[180:181]\n\t" "v_exp_f32 v177, v177\n\t" "s_nop 0\n\t" "v_add_f32_dpp v182, v182, v182 row_ror:8 row_mask:0xf bank_mask:0xf\n\t" \
    "v_add_f32_dpp v183, v183, v183 row_ror:8 row_mask:0xf bank_mask:0xf\n\t" "v_add_f32 v182, v182, v176\n\t" "v_add_f32 v183, v183, v177\n\t" "s_nop 0\n\t" \
    "v_add_f32_dpp v182, v182, v182 row_ror:4 row_mask:0xf bank_mask:0xf\n\t" "v_add_f32_dpp v183, v183, v183 row_ror:4 row_mask:0xf bank_mask:0xf\n\t" "s_nop 0\n\t" \
    "v_add_f32_dpp v182, v182, v182 row_ror:2 row_mask:0xf bank_mask:0xf\n\t" "v_add_f32_dpp v183, v183, v183 row_ror:2 row_mask:0xf bank_mask:0xf\n\t" "s_nop 0\n\t" \
    "v_add_f32_dpp v182, v182, v182 row_ror:1 row_mask:0xf bank_mask:0xf\n\t" "v_add_f32_dpp v183, v183, v183 row_ror:1 row_mask:0xf bank_mask:0xf\n\t" \
    "v_rcp_f32 v182, v182\n\t" "v_rcp_f32 v183, v183\n\t" "s_nop 0\n\t" "v_pk_mul_f32 v[176:177], v[176:177], v[182:183]\n\t" \
    "v_pk_mul_f32 v[178:179], v[178:179], v[182:183]\n\t" "v_pk_mul_f32 v[180:181], v[180:181], v[182:183]\n\t" "v_cvt_pk_bf16_f32 v176, v176, v177\n\t" \
    "v_cvt_pk_bf16_f32 v178, v178, v179\n\t" "v_cvt_pk_bf16_f32 v180, v180, v181\n\t" "v_mov_b32_dpp v177, v176 quad_perm:[1,0,3,2] row_mask:0xf bank_mask:0xf\n\t" \
    "v_mov_b32_dpp v179, v178 quad_perm:[1,0,3,2] row_mask:0xf bank_mask:0xf\n\t" "v_mov_b32_dpp v181, v180 quad_perm:[1,0,3,2] row_mask:0xf bank_mask:0xf\n\t" \
    "v_perm_b32 v184, v179, v178, %[ps]\n\t" "v_perm_b32 v185, v181, v180, %[ps]\n\t" "buffer_store_dwordx2 v[184:185], %[px], %[rs], %[pb] offen sc1\n\t" \
    "v_perm_b32 v186, v177, v176, %[ps]\n\t" "buffer_store_dword v186, %[pm], %[rs], %[pb] offen sc1\n\t" XT(96, 97) "s_branch 99f\n\t" "11:\n\t" "s_cmp_eq_u32 %[bd], 0\n\t" \
    "s_cbranch_scc1 16f\n\t" "s_sub_u32 %[bd], %[bd], 1\n\t" XCNT(100) "s_waitcnt lgkmcnt(0)\n\t" "s_nop 7\n\t" "s_branch 10b\n\t" "16:\n\t" "s_cmp_lg_u32 s60, 0\n\t" \
    "s_branch 12b\n\t" "14:\n\t" "s_cmp_eq_u32 %[bd], 0\n\t" "s_cbranch_scc1 15b\n\t" "s_sub_u32 %[bd], %[bd], 1\n\t" XCNT(101) "s_mov_b32 s60, 1\n\t" \
    "s_waitcnt lgkmcnt(0)\n\t" "s_nop 7\n\t" "s_branch 10b\n\t" "99:\n\t"
#define QK_STEP1 \
    "v_xor_b32 v216, 0x400, %[mo]\n\t" "s_mov_b32 s60, 0\n\t" "s_add_u32 s61, %[qb], 0x1000\n\t" "s_mov_b32 s66, 0x3e0293ee\n\t" "s_mov_b32 s67, 0x3e0293ee\n\t" XT(80, 81) \
    XT(98, 99) "s_barrier\n\t" XT(82, 83) "20:\n\t" "buffer_load_dwordx4 v[240:243], %[mo], %[rs], %[sb] offen offset:0 sc1\n\t" "v_mov_b64 v[224:225], 0\n\t" \
    "v_mov_b64 v[226:227], 0\n\t" "v_mov_b64 v[228:229], 0\n\t" "v_mov_b64 v[230:231], 0\n\t" "v_mov_b64 v[232:233], 0\n\t" "v_mov_b64 v[234:235], 0\n\t" \
    "v_mov_b64 v[236:237], 0\n\t" "v_mov_b64 v[238:239], 0\n\t" "buffer_load_dwordx4 v[244:247], v216, %[rs], %[sb] offen offset:0 sc1\n\t" \
    "ds_read_b128 v[160:163], %[l0] offset:0\n\t" "ds_read_b128 v[164:167], %[l0] offset:1024\n\t" "ds_read_b128 v[168:171], %[l0] offset:2048\n\t" \
    "ds_read_b128 v[172:175], %[l0] offset:3072\n\t" "s_nop 3\n\t" "buffer_load_dwordx4 v[248:251], %[mo], %[rs], %[sb] offen offset:2048 sc1\n\t" \
    "ds_read_b128 v[176:179], %[l0] offset:4096\n\t" "ds_read_b128 v[180:183], %[l0] offset:5120\n\t" "ds_read_b128 v[184:187], %[l0] offset:6144\n\t" \
    "ds_read_b128 v[188:191], %[l0] offset:7168\n\t" "s_nop 3\n\t" "buffer_load_dwordx4 v[252:255], v216, %[rs], %[sb] offen offset:2048 sc1\n\t" \
    "v_mov_b32 v208, 0xd028965f\n\t" "v_mov_b32 v209, 0xff800000\n\t" "s_mov_b32 s62, 0\n\t" "s_mov_b32 s63, -1\n\t" "v_mov_b32 v210, 0\n\t" "v_cmp_gt_i32 vcc, 0, %[ps]\n\t" \
    "v_cndmask_b32 v204, 0, v209, s[62:63]\n\t" "s_nop 0\n\t" "v_cndmask_b32 v200, 0, v208, vcc\n\t" "v_cmp_gt_i32 vcc, 1, %[ps]\n\t" \
    "v_cndmask_b32 v205, 0, v209, s[62:63]\n\t" "s_nop 0\n\t" "v_cndmask_b32 v201, 0, v208, vcc\n\t" "v_cmp_gt_i32 vcc, 2, %[ps]\n\t" \
    "v_cndmask_b32 v206, 0, v209, s[62:63]\n\t" "s_nop 0\n\t" "v_cndmask_b32 v202, 0, v208, vcc\n\t" "v_cmp_gt_i32 vcc, 3, %[ps]\n\t" \
    "v_cndmask_b32 v207, 0, v209, s[62:63]\n\t" "s_nop 0\n\t" "v_cndmask_b32 v203, 0, v208, vcc\n\t" "s_waitcnt vmcnt(3)\n\t" XT(84, 85) \
    "v_smfmac_f32_16x16x64_bf16 v[224:227], v[240:243], a[0:7], %[ix]\n\t" "ds_write_b128 %[l1], v[240:243] offset:4096\n\t" \
    "v_smfmac_f32_16x16x64_bf16 v[228:231], v[240:243], a[8:15], %[ix]\n\t" "v_max3_u32 v217, v240, v241, v242\n\t" \
    "v_smfmac_f32_16x16x64_bf16 v[232:235], v[240:243], v[32:39], %[ix]\n\t" "s_waitcnt lgkmcnt(7)\n\t" \
    "v_smfmac_f32_16x16x64_bf16 v[236:239], v[240:243], v[160:167], %[ix]\n\t" "ds_read_b128 v[160:163], %[l0] offset:8192\n\t" \
    "ds_read_b128 v[164:167], %[l0] offset:9216\n\t" "s_waitcnt vmcnt(2)\n\t" "v_smfmac_f32_16x16x64_bf16 v[224:227], v[244:247], a[16:23], %[ix]\n\t" \
    "ds_write_b128 %[l1], v[244:247] offset:5120\n\t" "v_max3_u32 v217, v217, v243, v244\n\t" "v_smfmac_f32_16x16x64_bf16 v[228:231], v[244:247], a[24:31], %[ix]\n\t" \
    "v_max3_u32 v217, v217, v245, v246\n\t" "v_max_u32 v217, v217, v247\n\t" "v_smfmac_f32_16x16x64_bf16 v[232:235], v[244:247], v[40:47], %[ix]\n\t" \
    "v_cmp_eq_u32 vcc, -1, v217\n\t" "s_cmp_lg_u32 s60, 0\n\t" "s_waitcnt lgkmcnt(8)\n\t" "v_smfmac_f32_16x16x64_bf16 v[236:239], v[244:247], v[168:175], %[ix]\n\t" \
    "s_cbranch_vccnz 21f\n\t" "22:\n\t" "ds_read_b128 v[168:171], %[l0] offset:10240\n\t" "s_waitcnt vmcnt(1)\n\t" \
    "v_smfmac_f32_16x16x64_bf16 v[224:227], v[248:251], a[32:39], %[ix]\n\t" "ds_write_b128 %[l1], v[248:251] offset:6144\n\t" "s_waitcnt lgkmcnt(2)\n\t" \
    "v_smfmac_f32_16x16x64_bf16 v[228:231], v[248:251], a[40:47], %[ix]\n\t" "s_cbranch_scc1 23f\n\t" "s_barrier\n\t" "23:\n\t" XT(86, 87) \
    "v_smfmac_f32_16x16x64_bf16 v[232:235], v[248:251], v[48:55], %[ix]\n\t" "ds_read_b128 v[192:195], %[l1] offset:8192\n\t" \
    "ds_read_b128 v[196:199], %[l1] offset:12288\n\t" "v_smfmac_f32_16x16x64_bf16 v[236:239], v[248:251], v[176:183], %[ix]\n\t" \
    "ds_read_b128 v[172:175], %[l0] offset:11264\n\t" "ds_read_b128 v[176:179], %[l0] offset:12288\n\t" "s_waitcnt vmcnt(0)\n\t" \
    "v_smfmac_f32_16x16x64_bf16 v[224:227], v[252:255], a[48:55], %[ix]\n\t" "ds_write_b128 %[l1], v[252:255] offset:7168\n\t" "v_max3_u32 v217, v248, v249, v250\n\t" \
    "v_smfmac_f32_16x16x64_bf16 v[228:231], v[252:255], a[56:63], %[ix]\n\t" "ds_read_b128 v[180:183], %[l0] offset:13312\n\t" "v_max3_u32 v217, v217, v251, v252\n\t" \
    "v_max3_u32 v217, v217, v253, v254\n\t" "v_smfmac_f32_16x16x64_bf16 v[232:235], v[252:255], v[56:63], %[ix]\n\t" "v_max_u32 v217, v217, v255\n\t" \
    "v_cmp_eq_u32 vcc, -1, v217\n\t" "v_smfmac_f32_16x16x64_bf16 v[236:239], v[252:255], v[184:191], %[ix]\n\t" "s_cbranch_vccnz 24f\n\t" "25:\n\t" \
    "ds_read_b128 v[240:243], %[l1] offset:0\n\t" "s_waitcnt lgkmcnt(6)\n\t" "v_smfmac_f32_16x16x64_bf16 v[224:227], v[192:195], a[64:71], %[ix]\n\t" \
    "ds_read_b128 v[184:187], %[l0] offset:14336\n\t" "ds_read_b128 v[188:191], %[l0] offset:15360\n\t" \
    "v_smfmac_f32_16x16x64_bf16 v[228:231], v[192:195], a[72:79], %[ix]\n\t" "ds_read_b128 v[244:247], %[l1] offset:9216\n\t" "s_waitcnt lgkmcnt(5)\n\t" \
    "v_smfmac_f32_16x16x64_bf16 v[232:235], v[192:195], v[64:71], %[ix]\n\t" "s_barrier\n\t" XT(88, 89) \
    "v_smfmac_f32_16x16x64_bf16 v[236:239], v[192:195], v[160:167], %[ix]\n\t" "ds_read_b128 v[160:163], %[l0] offset:16384\n\t" \
    "ds_read_b128 v[164:167], %[l0] offset:17408\n\t" "v_smfmac_f32_16x16x64_bf16 v[224:227], v[196:199], a[80:87], %[ix]\n\t" \
    "ds_read_b128 v[248:251], %[l1] offset:13312\n\t" "ds_read_b128 v[252:255], %[l1] offset:1024\n\t" \
    "v_smfmac_f32_16x16x64_bf16 v[228:231], v[196:199], a[88:95], %[ix]\n\t" "ds_read_b128 v[192:195], %[l1] offset:10240\n\t" \
    "v_smfmac_f32_16x16x64_bf16 v[232:235], v[196:199], v[72:79], %[ix]\n\t" "buffer_load_dwordx4 v[16:19], %[hq], %[rs], %[qb] offen offset:0 sc1\n\t" \
    "v_smfmac_f32_16x16x64_bf16 v[236:239], v[196:199], v[168:175], %[ix]\n\t" "ds_read_b128 v[168:171], %[l0] offset:18432\n\t" \
    "ds_read_b128 v[172:175], %[l0] offset:19456\n\t" "s_waitcnt lgkmcnt(10)\n\t" "v_smfmac_f32_16x16x64_bf16 v[224:227], v[240:243], a[96:103], %[ix]\n\t" \
    "ds_read_b128 v[196:199], %[l1] offset:14336\n\t" "v_smfmac_f32_16x16x64_bf16 v[228:231], v[240:243], a[104:111], %[ix]\n\t" \
    "v_smfmac_f32_16x16x64_bf16 v[232:235], v[240:243], v[80:87], %[ix]\n\t" "buffer_load_dwordx4 v[20:23], %[hq], %[rs], %[qb] offen offset:2048 sc1\n\t" \
    "v_smfmac_f32_16x16x64_bf16 v[236:239], v[240:243], v[176:183], %[ix]\n\t" "ds_read_b128 v[176:179], %[l0] offset:20480\n\t" \
    "ds_read_b128 v[180:183], %[l0] offset:21504\n\t" "s_waitcnt lgkmcnt(10)\n\t" "v_smfmac_f32_16x16x64_bf16 v[224:227], v[244:247], a[112:119], %[ix]\n\t" \
    "ds_read_b128 v[240:243], %[l1] offset:2048\n\t" "v_smfmac_f32_16x16x64_bf16 v[228:231], v[244:247], a[120:127], %[ix]\n\t" \
    "v_smfmac_f32_16x16x64_bf16 v[232:235], v[244:247], v[88:95], %[ix]\n\t" "buffer_load_dwordx4 v[24:27], %[hq], %[rs], s61 offen offset:0 sc1\n\t" \
    "v_smfmac_f32_16x16x64_bf16 v[236:239], v[244:247], v[184:191], %[ix]\n\t" "ds_read_b128 v[184:187], %[l0] offset:22528\n\t" \
    "ds_read_b128 v[188:191], %[l0] offset:23552\n\t" "s_waitcnt lgkmcnt(10)\n\t" "v_smfmac_f32_16x16x64_bf16 v[224:227], v[248:251], a[128:135], %[ix]\n\t" \
    "ds_read_b128 v[244:247], %[l1] offset:11264\n\t" "v_smfmac_f32_16x16x64_bf16 v[228:231], v[248:251], a[136:143], %[ix]\n\t" \
    "v_smfmac_f32_16x16x64_bf16 v[232:235], v[248:251], v[96:103], %[ix]\n\t" "buffer_load_dwordx4 v[28:31], %[hq], %[rs], s61 offen offset:2048 sc1\n\t" \
    "v_smfmac_f32_16x16x64_bf16 v[236:239], v[248:251], v[160:167], %[ix]\n\t" "ds_read_b128 v[160:163], %[l0] offset:24576\n\t" \
    "ds_read_b128 v[164:167], %[l0] offset:25600\n\t" "s_waitcnt lgkmcnt(12)\n\t" "v_smfmac_f32_16x16x64_bf16 v[224:227], v[252:255], a[144:151], %[ix]\n\t" \
    "ds_read_b128 v[248:251], %[l1] offset:15360\n\t" "v_smfmac_f32_16x16x64_bf16 v[228:231], v[252:255], a[152:159], %[ix]\n\t" \
    "v_smfmac_f32_16x16x64_bf16 v[232:235], v[252:255], v[104:111], %[ix]\n\t" "s_waitcnt lgkmcnt(10)\n\t" \
    "v_smfmac_f32_16x16x64_bf16 v[236:239], v[252:255], v[168:175], %[ix]\n\t" "ds_read_b128 v[168:171], %[l0] offset:26624\n\t" \
    "ds_read_b128 v[172:175], %[l0] offset:27648\n\t" "v_smfmac_f32_16x16x64_bf16 v[224:227], v[192:195], a[160:167], %[ix]\n\t" \
    "ds_read_b128 v[252:255], %[l1] offset:3072\n\t" "v_smfmac_f32_16x16x64_bf16 v[228:231], v[192:195], a[168:175], %[ix]\n\t" "s_waitcnt lgkmcnt(12)\n\t" \
    "v_smfmac_f32_16x16x64_bf16 v[224:227], v[196:199], a[176:183], %[ix]\n\t" "v_smfmac_f32_16x16x64_bf16 v[228:231], v[196:199], a[184:191], %[ix]\n\t" \
    "s_waitcnt lgkmcnt(9)\n\t" "v_smfmac_f32_16x16x64_bf16 v[224:227], v[240:243], a[192:199], %[ix]\n\t" \
    "v_smfmac_f32_16x16x64_bf16 v[228:231], v[240:243], a[200:207], %[ix]\n\t" "s_waitcnt lgkmcnt(6)\n\t" \
    "v_smfmac_f32_16x16x64_bf16 v[224:227], v[244:247], a[208:215], %[ix]\n\t" "v_smfmac_f32_16x16x64_bf16 v[228:231], v[244:247], a[216:223], %[ix]\n\t" \
    "s_waitcnt lgkmcnt(3)\n\t" "v_smfmac_f32_16x16x64_bf16 v[224:227], v[248:251], a[224:231], %[ix]\n\t" \
    "v_smfmac_f32_16x16x64_bf16 v[228:231], v[248:251], a[232:239], %[ix]\n\t" "s_waitcnt lgkmcnt(0)\n\t" \
    "v_smfmac_f32_16x16x64_bf16 v[224:227], v[252:255], a[240:247], %[ix]\n\t" "s_nop 1\n\t" "v_smfmac_f32_16x16x64_bf16 v[228:231], v[252:255], a[248:255], %[ix]\n\t" \
    XT(90, 91) "s_nop 1\n\t" "v_smfmac_f32_16x16x64_bf16 v[232:235], v[192:195], v[112:119], %[ix]\n\t" "s_nop 1\n\t" \
    "v_smfmac_f32_16x16x64_bf16 v[236:239], v[192:195], v[176:183], %[ix]\n\t" "ds_read_b128 v[176:179], %[l0] offset:28672\n\t" "v_add_f32 v224, v224, v225\n\t" \
    "v_add_f32 v226, v226, v227\n\t" "v_smfmac_f32_16x16x64_bf16 v[232:235], v[196:199], v[120:127], %[ix]\n\t" "ds_read_b128 v[180:183], %[l0] offset:29696\n\t" \
    "v_add_f32 v228, v228, v229\n\t" "v_add_f32 v230, v230, v231\n\t" "v_smfmac_f32_16x16x64_bf16 v[236:239], v[196:199], v[184:191], %[ix]\n\t" \
    "ds_read_b128 v[184:187], %[l0] offset:30720\n\t" "v_cvt_pk_bf16_f32 v218, v224, v228\n\t" "v_cvt_pk_bf16_f32 v219, v226, v230\n\t" \
    "v_smfmac_f32_16x16x64_bf16 v[232:235], v[240:243], v[128:135], %[ix]\n\t" "ds_write2_b32 %[qw], v218, v219 offset1:8\n\t" \
    "ds_read_b128 v[188:191], %[l0] offset:31744\n\t" "v_smfmac_f32_16x16x64_bf16 v[236:239], v[240:243], v[160:167], %[ix]\n\t" \
    "v_smfmac_f32_16x16x64_bf16 v[232:235], v[244:247], v[136:143], %[ix]\n\t" "v_smfmac_f32_16x16x64_bf16 v[236:239], v[244:247], v[168:175], %[ix]\n\t" \
    "s_waitcnt lgkmcnt(1)\n\t" "s_barrier\n\t" "v_smfmac_f32_16x16x64_bf16 v[232:235], v[248:251], v[144:151], %[ix]\n\t" \
    "v_smfmac_f32_16x16x64_bf16 v[236:239], v[248:251], v[176:183], %[ix]\n\t" "v_smfmac_f32_16x16x64_bf16 v[232:235], v[252:255], v[152:159], %[ix]\n\t" \
    "s_waitcnt lgkmcnt(0)\n\t" "v_smfmac_f32_16x16x64_bf16 v[236:239], v[252:255], v[184:191], %[ix]\n\t" XT(92, 93) "s_nop 6\n\t" "v_add_f32 v232, v232, v233\n\t" \
    "v_add_f32 v234, v234, v235\n\t" "v_add_f32 v236, v236, v237\n\t" "v_add_f32 v238, v238, v239\n\t" "v_cvt_pk_bf16_f32 v232, v232, v236\n\t" \
    "v_cvt_pk_bf16_f32 v233, v234, v238\n\t" "ds_write2_b32 %[kw], v232, v233 offset1:68\n\t" "s_waitcnt lgkmcnt(0)\n\t" "s_barrier\n\t" XT(94, 95) \
    "s_cmp_ge_u32 %[sq], %[sn]\n\t" "s_cbranch_scc1 277f\n\t" "275:\n\t" "global_load_dword v240, v210, %[fq] sc1\n\t" "s_waitcnt vmcnt(0)\n\t" \
    "v_readfirstlane_b32 %[sq], v240\n\t" "s_nop 3\n\t" "s_cmp_ge_u32 %[sq], %[sn]\n\t" "s_cbranch_scc1 276f\n\t" "s_cmp_eq_u32 %[bd], 0\n\t" "s_cbranch_scc1 276f\n\t" \
    "s_sub_u32 %[bd], %[bd], 1\n\t" "s_sleep 2\n\t" "s_branch 275b\n\t" "276:\n\t" "buffer_load_dwordx4 v[16:19], %[hq], %[rs], %[qb] offen offset:0 sc1\n\t" \
    "buffer_load_dwordx4 v[20:23], %[hq], %[rs], %[qb] offen offset:2048 sc1\n\t" "buffer_load_dwordx4 v[24:27], %[hq], %[rs], s61 offen offset:0 sc1\n\t" \
    "buffer_load_dwordx4 v[28:31], %[hq], %[rs], s61 offen offset:2048 sc1\n\t" "277:\n\t" "s_nop 3\n\t" "ds_read_b128 v[160:163], %[kx] offset:0\n\t" \
    "ds_read_b128 v[164:167], %[kx] offset:2048\n\t" "ds_read_b128 v[168:171], %[kx] offset:4096\n\t" "ds_read_b128 v[172:175], %[kx] offset:6144\n\t" \
    "ds_read_b128 v[224:227], %[km] offset:0\n\t" "ds_read_b128 v[228:231], %[km] offset:64\n\t" "ds_read_b128 v[232:235], %[km] offset:128\n\t" \
    "ds_read_b128 v[236:239], %[km] offset:192\n\t" "s_waitcnt vmcnt(0)\n\t" "global_load_dword v240, v210, %[fq] sc1\n\t" "s_waitcnt lgkmcnt(7)\n\t" \
    "v_mfma_f32_16x16x32_bf16 v[248:251], v[160:163], v[16:19], v[200:203]\n\t" "s_waitcnt lgkmcnt(6)\n\t" \
    "v_mfma_f32_16x16x32_bf16 v[248:251], v[164:167], v[20:23], v[248:251]\n\t" "s_waitcnt lgkmcnt(5)\n\t" \
    "v_mfma_f32_16x16x32_bf16 v[248:251], v[168:171], v[24:27], v[248:251]\n\t" "s_waitcnt lgkmcnt(4)\n\t" \
    "v_mfma_f32_16x16x32_bf16 v[248:251], v[172:175], v[28:31], v[248:251]\n\t" "s_waitcnt lgkmcnt(3)\n\t" \
    "v_mfma_f32_16x16x32_bf16 v[244:247], v[224:227], v[16:19], v[204:207]\n\t" "s_waitcnt lgkmcnt(2)\n\t" \
    "v_mfma_f32_16x16x32_bf16 v[244:247], v[228:231], v[20:23], v[244:247]\n\t" "s_waitcnt lgkmcnt(1)\n\t" \
    "v_mfma_f32_16x16x32_bf16 v[244:247], v[232:235], v[24:27], v[244:247]\n\t" "s_waitcnt lgkmcnt(0)\n\t" \
    "v_mfma_f32_16x16x32_bf16 v[244:247], v[236:239], v[28:31], v[244:247]\n\t" "v_mov_b32 v192, -1\n\t" "v_mov_b32 v193, -1\n\t" "v_mov_b32 v194, -1\n\t" \
    "v_mov_b32 v195, -1\n\t" "v_max3_f32 v242, v248, v249, v250\n\t" "v_max_f32 v242, v242, v251\n\t" "s_nop 1\n\t" "v_max3_f32 v242, v242, v244, v245\n\t" \
    "v_max3_f32 v242, v242, v246, v247\n\t" "v_mov_b32 v243, v242\n\t" "s_nop 1\n\t" "v_permlane32_swap_b32 v242, v243\n\t" "v_max_f32 v242, v242, v243\n\t" \
    "v_mov_b32 v243, v242\n\t" "s_nop 1\n\t" "v_permlane16_swap_b32 v242, v243\n\t" "v_max_f32 v242, v242, v243\n\t" \
    "v_pk_add_f32 v[244:245], v[244:245], v[242:243] op_sel_hi:[1,0] neg_lo:[0,1] neg_hi:[0,1]\n\t" \
    "v_pk_add_f32 v[246:247], v[246:247], v[242:243] op_sel_hi:[1,0] neg_lo:[0,1] neg_hi:[0,1]\n\t" \
    "v_pk_add_f32 v[248:249], v[248:249], v[242:243] op_sel_hi:[1,0] neg_lo:[0,1] neg_hi:[0,1]\n\t" \
    "v_pk_add_f32 v[250:251], v[250:251], v[242:243] op_sel_hi:[1,0] neg_lo:[0,1] neg_hi:[0,1]\n\t" "v_pk_mul_f32 v[244:245], v[244:245], s[66:67]\n\t" \
    "v_pk_mul_f32 v[246:247], v[246:247], s[66:67]\n\t" "v_pk_mul_f32 v[248:249], v[248:249], s[66:67]\n\t" "v_pk_mul_f32 v[250:251], v[250:251], s[66:67]\n\t" \
    "v_exp_f32 v244, v244\n\t" "v_exp_f32 v245, v245\n\t" "v_exp_f32 v246, v246\n\t" "v_exp_f32 v247, v247\n\t" "v_exp_f32 v248, v248\n\t" "v_exp_f32 v249, v249\n\t" \
    "v_exp_f32 v250, v250\n\t" "v_exp_f32 v251, v251\n\t" "v_pk_add_f32 v[242:243], v[244:245], v[246:247]\n\t" "v_pk_add_f32 v[242:243], v[242:243], v[248:249]\n\t" \
    "v_pk_add_f32 v[242:243], v[242:243], v[250:251]\n\t" "v_add_f32 v242, v242, v243\n\t" "v_mov_b32 v243, v242\n\t" "s_nop 1\n\t" "v_permlane32_swap_b32 v242, v243\n\t" \
    "v_add_f32 v242, v242, v243\n\t" "v_mov_b32 v243, v242\n\t" "s_nop 1\n\t" "v_permlane16_swap_b32 v242, v243\n\t" "v_add_f32 v242, v242, v243\n\t" \
    "v_rcp_f32 v242, v242\n\t" "s_nop 0\n\t" "v_pk_mul_f32 v[244:245], v[244:245], v[242:243] op_sel_hi:[1,0]\n\t" \
    "v_pk_mul_f32 v[246:247], v[246:247], v[242:243] op_sel_hi:[1,0]\n\t" "v_pk_mul_f32 v[248:249], v[248:249], v[242:243] op_sel_hi:[1,0]\n\t" \
    "v_pk_mul_f32 v[250:251], v[250:251], v[242:243] op_sel_hi:[1,0]\n\t" "v_cvt_pk_bf16_f32 v162, v244, v245\n\t" "v_cvt_pk_bf16_f32 v163, v246, v247\n\t" \
    "v_cvt_pk_bf16_f32 v164, v248, v249\n\t" "v_cvt_pk_bf16_f32 v165, v250, v251\n\t" "buffer_store_dwordx2 v[164:165], %[px], %[rs], %[tb] offen sc1\n\t" \
    "buffer_store_dwordx2 v[162:163], %[pm], %[rs], %[tb] offen sc1\n\t" "buffer_store_dwordx2 v[192:193], %[px], %[rs], %[tr] offen sc1\n\t" \
    "buffer_store_dwordx2 v[192:193], %[pm], %[rs], %[tr] offen sc1\n\t" "s_waitcnt vmcnt(4)\n\t" "v_readfirstlane_b32 s62, v240\n\t" "s_nop 3\n\t" \
    "s_max_u32 %[sq], %[sq], s62\n\t" XT(96, 97) "s_branch 99f\n\t" "21:\n\t" "s_cmp_eq_u32 %[bd], 0\n\t" "s_cbranch_scc1 26f\n\t" "s_sub_u32 %[bd], %[bd], 1\n\t" XCNT(100) \
    "s_waitcnt lgkmcnt(0)\n\t" "s_nop 7\n\t" "s_branch 20b\n\t" "26:\n\t" "s_cmp_lg_u32 s60, 0\n\t" "s_branch 22b\n\t" "24:\n\t" "s_cmp_eq_u32 %[bd], 0\n\t" \
    "s_cbranch_scc1 25b\n\t" "s_sub_u32 %[bd], %[bd], 1\n\t" XCNT(101) "s_mov_b32 s60, 1\n\t" "s_waitcnt lgkmcnt(0)\n\t" "s_nop 7\n\t" "s_branch 20b\n\t" "99:\n\t"
#define QK_STEP2 \
    "v_xor_b32 v216, 0x400, %[mo]\n\t" "s_mov_b32 s60, 0\n\t" "s_add_u32 s61, %[qb], 0x1000\n\t" "s_mov_b32 s66, 0x3e0293ee\n\t" "s_mov_b32 s67, 0x3e0293ee\n\t" XT(80, 81) \
    XT(98, 99) "s_barrier\n\t" XT(82, 83) "30:\n\t" "buffer_load_dwordx4 v[240:243], %[mo], %[rs], %[sb] offen offset:0 sc1\n\t" "v_mov_b64 v[224:225], 0\n\t" \
    "v_mov_b64 v[226:227], 0\n\t" "v_mov_b64 v[228:229], 0\n\t" "v_mov_b64 v[230:231], 0\n\t" "v_mov_b64 v[232:233], 0\n\t" "v_mov_b64 v[234:235], 0\n\t" \
    "v_mov_b64 v[236:237], 0\n\t" "v_mov_b64 v[238:239], 0\n\t" "buffer_load_dwordx4 v[244:247], v216, %[rs], %[sb] offen offset:0 sc1\n\t" \
    "ds_read_b128 v[160:163], %[l0] offset:0\n\t" "ds_read_b128 v[164:167], %[l0] offset:1024\n\t" "ds_read_b128 v[168:171], %[l0] offset:2048\n\t" \
    "ds_read_b128 v[172:175], %[l0] offset:3072\n\t" "s_nop 3\n\t" "buffer_load_dwordx4 v[248:251], %[mo], %[rs], %[sb] offen offset:2048 sc1\n\t" \
    "ds_read_b128 v[176:179], %[l0] offset:4096\n\t" "ds_read_b128 v[180:183], %[l0] offset:5120\n\t" "ds_read_b128 v[184:187], %[l0] offset:6144\n\t" \
    "ds_read_b128 v[188:191], %[l0] offset:7168\n\t" "s_nop 3\n\t" "buffer_load_dwordx4 v[252:255], v216, %[rs], %[sb] offen offset:2048 sc1\n\t" \
    "v_mov_b32 v208, 0xd028965f\n\t" "v_mov_b32 v209, 0xff800000\n\t" "s_mov_b32 s62, 0\n\t" "s_mov_b32 s63, -1\n\t" "v_mov_b32 v210, 0\n\t" "v_cmp_gt_i32 vcc, 0, %[ps]\n\t" \
    "v_cndmask_b32 v204, 0, v209, s[62:63]\n\t" "s_nop 0\n\t" "v_cndmask_b32 v200, 0, v208, vcc\n\t" "v_cmp_gt_i32 vcc, 1, %[ps]\n\t" \
    "v_cndmask_b32 v205, 0, v209, s[62:63]\n\t" "s_nop 0\n\t" "v_cndmask_b32 v201, 0, v208, vcc\n\t" "v_cmp_gt_i32 vcc, 2, %[ps]\n\t" \
    "v_cndmask_b32 v206, 0, v209, s[62:63]\n\t" "s_nop 0\n\t" "v_cndmask_b32 v202, 0, v208, vcc\n\t" "v_cmp_gt_i32 vcc, 3, %[ps]\n\t" \
    "v_cndmask_b32 v207, 0, v209, s[62:63]\n\t" "s_nop 0\n\t" "v_cndmask_b32 v203, 0, v208, vcc\n\t" "s_waitcnt vmcnt(3)\n\t" XT(84, 85) \
    "v_smfmac_f32_16x16x64_bf16 v[224:227], v[240:243], a[0:7], %[ix]\n\t" "ds_write_b128 %[l1], v[240:243] offset:8192\n\t" \
    "v_smfmac_f32_16x16x64_bf16 v[228:231], v[240:243], a[8:15], %[ix]\n\t" "v_max3_u32 v217, v240, v241, v242\n\t" \
    "v_smfmac_f32_16x16x64_bf16 v[232:235], v[240:243], v[32:39], %[ix]\n\t" "s_waitcnt lgkmcnt(7)\n\t" \
    "v_smfmac_f32_16x16x64_bf16 v[236:239], v[240:243], v[160:167], %[ix]\n\t" "ds_read_b128 v[160:163], %[l0] offset:8192\n\t" \
    "ds_read_b128 v[164:167], %[l0] offset:9216\n\t" "s_waitcnt vmcnt(2)\n\t" "v_smfmac_f32_16x16x64_bf16 v[224:227], v[244:247], a[16:23], %[ix]\n\t" \
    "ds_write_b128 %[l1], v[244:247] offset:9216\n\t" "v_max3_u32 v217, v217, v243, v244\n\t" "v_smfmac_f32_16x16x64_bf16 v[228:231], v[244:247], a[24:31], %[ix]\n\t" \
    "v_max3_u32 v217, v217, v245, v246\n\t" "v_max_u32 v217, v217, v247\n\t" "v_smfmac_f32_16x16x64_bf16 v[232:235], v[244:247], v[40:47], %[ix]\n\t" \
    "v_cmp_eq_u32 vcc, -1, v217\n\t" "s_cmp_lg_u32 s60, 0\n\t" "s_waitcnt lgkmcnt(8)\n\t" "v_smfmac_f32_16x16x64_bf16 v[236:239], v[244:247], v[168:175], %[ix]\n\t" \
    "s_cbranch_vccnz 31f\n\t" "32:\n\t" "ds_read_b128 v[168:171], %[l0] offset:10240\n\t" "s_waitcnt vmcnt(1)\n\t" \
    "v_smfmac_f32_16x16x64_bf16 v[224:227], v[248:251], a[32:39], %[ix]\n\t" "ds_write_b128 %[l1], v[248:251] offset:10240\n\t" "s_waitcnt lgkmcnt(2)\n\t" \
    "v_smfmac_f32_16x16x64_bf16 v[228:231], v[248:251], a[40:47], %[ix]\n\t" "s_cbranch_scc1 33f\n\t" "s_barrier\n\t" "33:\n\t" XT(86, 87) \
    "v_smfmac_f32_16x16x64_bf16 v[232:235], v[248:251], v[48:55], %[ix]\n\t" "ds_read_b128 v[192:195], %[l1] offset:12288\n\t" "ds_read_b128 v[196:199], %[l1] offset:0\n\t" \
    "v_smfmac_f32_16x16x64_bf16 v[236:239], v[248:251], v[176:183], %[ix]\n\t" "ds_read_b128 v[172:175], %[l0] offset:11264\n\t" \
    "ds_read_b128 v[176:179], %[l0] offset:12288\n\t" "s_waitcnt vmcnt(0)\n\t" "v_smfmac_f32_16x16x64_bf16 v[224:227], v[252:255], a[48:55], %[ix]\n\t" \
    "ds_write_b128 %[l1], v[252:255] offset:11264\n\t" "v_max3_u32 v217, v248, v249, v250\n\t" "v_smfmac_f32_16x16x64_bf16 v[228:231], v[252:255], a[56:63], %[ix]\n\t" \
    "ds_read_b128 v[180:183], %[l0] offset:13312\n\t" "v_max3_u32 v217, v217, v251, v252\n\t" "v_max3_u32 v217, v217, v253, v254\n\t" \
    "v_smfmac_f32_16x16x64_bf16 v[232:235], v[252:255], v[56:63], %[ix]\n\t" "v_max_u32 v217, v217, v255\n\t" "v_cmp_eq_u32 vcc, -1, v217\n\t" \
    "v_smfmac_f32_16x16x64_bf16 v[236:239], v[252:255], v[184:191], %[ix]\n\t" "s_cbranch_vccnz 34f\n\t" "35:\n\t" "ds_read_b128 v[240:243], %[l1] offset:4096\n\t" \
    "s_waitcnt lgkmcnt(6)\n\t" "v_smfmac_f32_16x16x64_bf16 v[224:227], v[192:195], a[64:71], %[ix]\n\t" "ds_read_b128 v[184:187], %[l0] offset:14336\n\t" \
    "ds_read_b128 v[188:191], %[l0] offset:15360\n\t" "v_smfmac_f32_16x16x64_bf16 v[228:231], v[192:195], a[72:79], %[ix]\n\t" \
    "ds_read_b128 v[244:247], %[l1] offset:13312\n\t" "s_waitcnt lgkmcnt(5)\n\t" "v_smfmac_f32_16x16x64_bf16 v[232:235], v[192:195], v[64:71], %[ix]\n\t" "s_barrier\n\t" \
    XT(88, 89) "v_smfmac_f32_16x16x64_bf16 v[236:239], v[192:195], v[160:167], %[ix]\n\t" "ds_read_b128 v[160:163], %[l0] offset:16384\n\t" \
    "ds_read_b128 v[164:167], %[l0] offset:17408\n\t" "v_smfmac_f32_16x16x64_bf16 v[224:227], v[196:199], a[80:87], %[ix]\n\t" \
    "ds_read_b128 v[248:251], %[l1] offset:1024\n\t" "ds_read_b128 v[252:255], %[l1] offset:5120\n\t" "v_smfmac_f32_16x16x64_bf16 v[228:231], v[196:199], a[88:95], %[ix]\n\t" \
    "ds_read_b128 v[192:195], %[l1] offset:14336\n\t" "v_smfmac_f32_16x16x64_bf16 v[232:235], v[196:199], v[72:79], %[ix]\n\t" \
    "buffer_load_dwordx4 v[16:19], %[hq], %[rs], %[qb] offen offset:0 sc1\n\t" "v_smfmac_f32_16x16x64_bf16 v[236:239], v[196:199], v[168:175], %[ix]\n\t" \
    "ds_read_b128 v[168:171], %[l0] offset:18432\n\t" "ds_read_b128 v[172:175], %[l0] offset:19456\n\t" "s_waitcnt lgkmcnt(10)\n\t" \
    "v_smfmac_f32_16x16x64_bf16 v[224:227], v[240:243], a[96:103], %[ix]\n\t" "ds_read_b128 v[196:199], %[l1] offset:2048\n\t" \
    "v_smfmac_f32_16x16x64_bf16 v[228:231], v[240:243], a[104:111], %[ix]\n\t" "v_smfmac_f32_16x16x64_bf16 v[232:235], v[240:243], v[80:87], %[ix]\n\t" \
    "buffer_load_dwordx4 v[20:23], %[hq], %[rs], %[qb] offen offset:2048 sc1\n\t" "v_smfmac_f32_16x16x64_bf16 v[236:239], v[240:243], v[176:183], %[ix]\n\t" \
    "ds_read_b128 v[176:179], %[l0] offset:20480\n\t" "ds_read_b128 v[180:183], %[l0] offset:21504\n\t" "s_waitcnt lgkmcnt(10)\n\t" \
    "v_smfmac_f32_16x16x64_bf16 v[224:227], v[244:247], a[112:119], %[ix]\n\t" "ds_read_b128 v[240:243], %[l1] offset:6144\n\t" \
    "v_smfmac_f32_16x16x64_bf16 v[228:231], v[244:247], a[120:127], %[ix]\n\t" "v_smfmac_f32_16x16x64_bf16 v[232:235], v[244:247], v[88:95], %[ix]\n\t" \
    "buffer_load_dwordx4 v[24:27], %[hq], %[rs], s61 offen offset:0 sc1\n\t" "v_smfmac_f32_16x16x64_bf16 v[236:239], v[244:247], v[184:191], %[ix]\n\t" \
    "ds_read_b128 v[184:187], %[l0] offset:22528\n\t" "ds_read_b128 v[188:191], %[l0] offset:23552\n\t" "s_waitcnt lgkmcnt(10)\n\t" \
    "v_smfmac_f32_16x16x64_bf16 v[224:227], v[248:251], a[128:135], %[ix]\n\t" "ds_read_b128 v[244:247], %[l1] offset:15360\n\t" \
    "v_smfmac_f32_16x16x64_bf16 v[228:231], v[248:251], a[136:143], %[ix]\n\t" "v_smfmac_f32_16x16x64_bf16 v[232:235], v[248:251], v[96:103], %[ix]\n\t" \
    "buffer_load_dwordx4 v[28:31], %[hq], %[rs], s61 offen offset:2048 sc1\n\t" "v_smfmac_f32_16x16x64_bf16 v[236:239], v[248:251], v[160:167], %[ix]\n\t" \
    "ds_read_b128 v[160:163], %[l0] offset:24576\n\t" "ds_read_b128 v[164:167], %[l0] offset:25600\n\t" "s_waitcnt lgkmcnt(12)\n\t" \
    "v_smfmac_f32_16x16x64_bf16 v[224:227], v[252:255], a[144:151], %[ix]\n\t" "ds_read_b128 v[248:251], %[l1] offset:3072\n\t" \
    "v_smfmac_f32_16x16x64_bf16 v[228:231], v[252:255], a[152:159], %[ix]\n\t" "v_smfmac_f32_16x16x64_bf16 v[232:235], v[252:255], v[104:111], %[ix]\n\t" \
    "s_waitcnt lgkmcnt(10)\n\t" "v_smfmac_f32_16x16x64_bf16 v[236:239], v[252:255], v[168:175], %[ix]\n\t" "ds_read_b128 v[168:171], %[l0] offset:26624\n\t" \
    "ds_read_b128 v[172:175], %[l0] offset:27648\n\t" "v_smfmac_f32_16x16x64_bf16 v[224:227], v[192:195], a[160:167], %[ix]\n\t" \
    "ds_read_b128 v[252:255], %[l1] offset:7168\n\t" "v_smfmac_f32_16x16x64_bf16 v[228:231], v[192:195], a[168:175], %[ix]\n\t" "s_waitcnt lgkmcnt(12)\n\t" \
    "v_smfmac_f32_16x16x64_bf16 v[224:227], v[196:199], a[176:183], %[ix]\n\t" "v_smfmac_f32_16x16x64_bf16 v[228:231], v[196:199], a[184:191], %[ix]\n\t" \
    "s_waitcnt lgkmcnt(9)\n\t" "v_smfmac_f32_16x16x64_bf16 v[224:227], v[240:243], a[192:199], %[ix]\n\t" \
    "v_smfmac_f32_16x16x64_bf16 v[228:231], v[240:243], a[200:207], %[ix]\n\t" "s_waitcnt lgkmcnt(6)\n\t" \
    "v_smfmac_f32_16x16x64_bf16 v[224:227], v[244:247], a[208:215], %[ix]\n\t" "v_smfmac_f32_16x16x64_bf16 v[228:231], v[244:247], a[216:223], %[ix]\n\t" \
    "s_waitcnt lgkmcnt(3)\n\t" "v_smfmac_f32_16x16x64_bf16 v[224:227], v[248:251], a[224:231], %[ix]\n\t" \
    "v_smfmac_f32_16x16x64_bf16 v[228:231], v[248:251], a[232:239], %[ix]\n\t" "s_waitcnt lgkmcnt(0)\n\t" \
    "v_smfmac_f32_16x16x64_bf16 v[224:227], v[252:255], a[240:247], %[ix]\n\t" "s_nop 1\n\t" "v_smfmac_f32_16x16x64_bf16 v[228:231], v[252:255], a[248:255], %[ix]\n\t" \
    XT(90, 91) "s_nop 1\n\t" "v_smfmac_f32_16x16x64_bf16 v[232:235], v[192:195], v[112:119], %[ix]\n\t" "s_nop 1\n\t" \
    "v_smfmac_f32_16x16x64_bf16 v[236:239], v[192:195], v[176:183], %[ix]\n\t" "ds_read_b128 v[176:179], %[l0] offset:28672\n\t" "v_add_f32 v224, v224, v225\n\t" \
    "v_add_f32 v226, v226, v227\n\t" "v_smfmac_f32_16x16x64_bf16 v[232:235], v[196:199], v[120:127], %[ix]\n\t" "ds_read_b128 v[180:183], %[l0] offset:29696\n\t" \
    "v_add_f32 v228, v228, v229\n\t" "v_add_f32 v230, v230, v231\n\t" "v_smfmac_f32_16x16x64_bf16 v[236:239], v[196:199], v[184:191], %[ix]\n\t" \
    "ds_read_b128 v[184:187], %[l0] offset:30720\n\t" "v_cvt_pk_bf16_f32 v218, v224, v228\n\t" "v_cvt_pk_bf16_f32 v219, v226, v230\n\t" \
    "v_smfmac_f32_16x16x64_bf16 v[232:235], v[240:243], v[128:135], %[ix]\n\t" "ds_write2_b32 %[qw], v218, v219 offset1:8\n\t" \
    "ds_read_b128 v[188:191], %[l0] offset:31744\n\t" "v_smfmac_f32_16x16x64_bf16 v[236:239], v[240:243], v[160:167], %[ix]\n\t" \
    "v_smfmac_f32_16x16x64_bf16 v[232:235], v[244:247], v[136:143], %[ix]\n\t" "v_smfmac_f32_16x16x64_bf16 v[236:239], v[244:247], v[168:175], %[ix]\n\t" \
    "s_waitcnt lgkmcnt(1)\n\t" "s_barrier\n\t" "v_smfmac_f32_16x16x64_bf16 v[232:235], v[248:251], v[144:151], %[ix]\n\t" \
    "v_smfmac_f32_16x16x64_bf16 v[236:239], v[248:251], v[176:183], %[ix]\n\t" "v_smfmac_f32_16x16x64_bf16 v[232:235], v[252:255], v[152:159], %[ix]\n\t" \
    "s_waitcnt lgkmcnt(0)\n\t" "v_smfmac_f32_16x16x64_bf16 v[236:239], v[252:255], v[184:191], %[ix]\n\t" XT(92, 93) "s_nop 6\n\t" "v_add_f32 v232, v232, v233\n\t" \
    "v_add_f32 v234, v234, v235\n\t" "v_add_f32 v236, v236, v237\n\t" "v_add_f32 v238, v238, v239\n\t" "v_cvt_pk_bf16_f32 v232, v232, v236\n\t" \
    "v_cvt_pk_bf16_f32 v233, v234, v238\n\t" "ds_write2_b32 %[kw], v232, v233 offset1:68\n\t" "s_waitcnt lgkmcnt(0)\n\t" "s_barrier\n\t" XT(94, 95) \
    "s_cmp_ge_u32 %[sq], %[sn]\n\t" "s_cbranch_scc1 377f\n\t" "375:\n\t" "global_load_dword v240, v210, %[fq] sc1\n\t" "s_waitcnt vmcnt(0)\n\t" \
    "v_readfirstlane_b32 %[sq], v240\n\t" "s_nop 3\n\t" "s_cmp_ge_u32 %[sq], %[sn]\n\t" "s_cbranch_scc1 376f\n\t" "s_cmp_eq_u32 %[bd], 0\n\t" "s_cbranch_scc1 376f\n\t" \
    "s_sub_u32 %[bd], %[bd], 1\n\t" "s_sleep 2\n\t" "s_branch 375b\n\t" "376:\n\t" "buffer_load_dwordx4 v[16:19], %[hq], %[rs], %[qb] offen offset:0 sc1\n\t" \
    "buffer_load_dwordx4 v[20:23], %[hq], %[rs], %[qb] offen offset:2048 sc1\n\t" "buffer_load_dwordx4 v[24:27], %[hq], %[rs], s61 offen offset:0 sc1\n\t" \
    "buffer_load_dwordx4 v[28:31], %[hq], %[rs], s61 offen offset:2048 sc1\n\t" "377:\n\t" "s_nop 3\n\t" "ds_read_b128 v[160:163], %[kx] offset:0\n\t" \
    "ds_read_b128 v[164:167], %[kx] offset:2048\n\t" "ds_read_b128 v[168:171], %[kx] offset:4096\n\t" "ds_read_b128 v[172:175], %[kx] offset:6144\n\t" \
    "ds_read_b128 v[176:179], %[kx] offset:256\n\t" "ds_read_b128 v[180:183], %[kx] offset:2304\n\t" "ds_read_b128 v[184:187], %[kx] offset:4352\n\t" \
    "ds_read_b128 v[188:191], %[kx] offset:6400\n\t" "ds_read_b128 v[224:227], %[km] offset:0\n\t" "ds_read_b128 v[228:231], %[km] offset:64\n\t" \
    "ds_read_b128 v[232:235], %[km] offset:128\n\t" "ds_read_b128 v[236:239], %[km] offset:192\n\t" "s_waitcnt vmcnt(0)\n\t" "global_load_dword v240, v210, %[fq] sc1\n\t" \
    "s_waitcnt lgkmcnt(11)\n\t" "v_mfma_f32_16x16x32_bf16 v[248:251], v[160:163], v[16:19], 0\n\t" "s_waitcnt lgkmcnt(10)\n\t" \
    "v_mfma_f32_16x16x32_bf16 v[248:251], v[164:167], v[20:23], v[248:251]\n\t" "s_waitcnt lgkmcnt(9)\n\t" \
    "v_mfma_f32_16x16x32_bf16 v[248:251], v[168:171], v[24:27], v[248:251]\n\t" "s_waitcnt lgkmcnt(8)\n\t" \
    "v_mfma_f32_16x16x32_bf16 v[248:251], v[172:175], v[28:31], v[248:251]\n\t" "s_waitcnt lgkmcnt(7)\n\t" \
    "v_mfma_f32_16x16x32_bf16 v[252:255], v[176:179], v[16:19], v[200:203]\n\t" "s_waitcnt lgkmcnt(6)\n\t" \
    "v_mfma_f32_16x16x32_bf16 v[252:255], v[180:183], v[20:23], v[252:255]\n\t" "s_waitcnt lgkmcnt(5)\n\t" \
    "v_mfma_f32_16x16x32_bf16 v[252:255], v[184:187], v[24:27], v[252:255]\n\t" "s_waitcnt lgkmcnt(4)\n\t" \
    "v_mfma_f32_16x16x32_bf16 v[252:255], v[188:191], v[28:31], v[252:255]\n\t" "s_waitcnt lgkmcnt(3)\n\t" \
    "v_mfma_f32_16x16x32_bf16 v[244:247], v[224:227], v[16:19], v[204:207]\n\t" "s_waitcnt lgkmcnt(2)\n\t" \
    "v_mfma_f32_16x16x32_bf16 v[244:247], v[228:231], v[20:23], v[244:247]\n\t" "s_waitcnt lgkmcnt(1)\n\t" \
    "v_mfma_f32_16x16x32_bf16 v[244:247], v[232:235], v[24:27], v[244:247]\n\t" "s_waitcnt lgkmcnt(0)\n\t" \
    "v_mfma_f32_16x16x32_bf16 v[244:247], v[236:239], v[28:31], v[244:247]\n\t" "v_mov_b32 v192, -1\n\t" "v_mov_b32 v193, -1\n\t" "v_mov_b32 v194, -1\n\t" \
    "v_mov_b32 v195, -1\n\t" "v_max3_f32 v242, v248, v249, v250\n\t" "v_max3_f32 v242, v242, v251, v252\n\t" "v_max3_f32 v242, v242, v253, v254\n\t" \
    "v_max_f32 v242, v242, v255\n\t" "v_max3_f32 v242, v242, v244, v245\n\t" "v_max3_f32 v242, v242, v246, v247\n\t" "v_mov_b32 v243, v242\n\t" "s_nop 1\n\t" \
    "v_permlane32_swap_b32 v242, v243\n\t" "v_max_f32 v242, v242, v243\n\t" "v_mov_b32 v243, v242\n\t" "s_nop 1\n\t" "v_permlane16_swap_b32 v242, v243\n\t" \
    "v_max_f32 v242, v242, v243\n\t" "v_pk_add_f32 v[244:245], v[244:245], v[242:243] op_sel_hi:[1,0] neg_lo:[0,1] neg_hi:[0,1]\n\t" \
    "v_pk_add_f32 v[246:247], v[246:247], v[242:243] op_sel_hi:[1,0] neg_lo:[0,1] neg_hi:[0,1]\n\t" \
    "v_pk_add_f32 v[248:249], v[248:249], v[242:243] op_sel_hi:[1,0] neg_lo:[0,1] neg_hi:[0,1]\n\t" \
    "v_pk_add_f32 v[250:251], v[250:251], v[242:243] op_sel_hi:[1,0] neg_lo:[0,1] neg_hi:[0,1]\n\t" \
    "v_pk_add_f32 v[252:253], v[252:253], v[242:243] op_sel_hi:[1,0] neg_lo:[0,1] neg_hi:[0,1]\n\t" \
    "v_pk_add_f32 v[254:255], v[254:255], v[242:243] op_sel_hi:[1,0] neg_lo:[0,1] neg_hi:[0,1]\n\t" "v_pk_mul_f32 v[244:245], v[244:245], s[66:67]\n\t" \
    "v_pk_mul_f32 v[246:247], v[246:247], s[66:67]\n\t" "v_pk_mul_f32 v[248:249], v[248:249], s[66:67]\n\t" "v_pk_mul_f32 v[250:251], v[250:251], s[66:67]\n\t" \
    "v_pk_mul_f32 v[252:253], v[252:253], s[66:67]\n\t" "v_pk_mul_f32 v[254:255], v[254:255], s[66:67]\n\t" "v_exp_f32 v244, v244\n\t" "v_exp_f32 v245, v245\n\t" \
    "v_exp_f32 v246, v246\n\t" "v_exp_f32 v247, v247\n\t" "v_exp_f32 v248, v248\n\t" "v_exp_f32 v249, v249\n\t" "v_exp_f32 v250, v250\n\t" "v_exp_f32 v251, v251\n\t" \
    "v_exp_f32 v252, v252\n\t" "v_exp_f32 v253, v253\n\t" "v_exp_f32 v254, v254\n\t" "v_exp_f32 v255, v255\n\t" "v_pk_add_f32 v[242:243], v[244:245], v[246:247]\n\t" \
    "v_pk_add_f32 v[242:243], v[242:243], v[248:249]\n\t" "v_pk_add_f32 v[242:243], v[242:243], v[250:251]\n\t" "v_pk_add_f32 v[242:243], v[242:243], v[252:253]\n\t" \
    "v_pk_add_f32 v[242:243], v[242:243], v[254:255]\n\t" "v_add_f32 v242, v242, v243\n\t" "v_mov_b32 v243, v242\n\t" "s_nop 1\n\t" "v_permlane32_swap_b32 v242, v243\n\t" \
    "v_add_f32 v242, v242, v243\n\t" "v_mov_b32 v243, v242\n\t" "s_nop 1\n\t" "v_permlane16_swap_b32 v242, v243\n\t" "v_add_f32 v242, v242, v243\n\t" \
    "v_rcp_f32 v242, v242\n\t" "s_nop 0\n\t" "v_pk_mul_f32 v[244:245], v[244:245], v[242:243] op_sel_hi:[1,0]\n\t" \
    "v_pk_mul_f32 v[246:247], v[246:247], v[242:243] op_sel_hi:[1,0]\n\t" "v_pk_mul_f32 v[248:249], v[248:249], v[242:243] op_sel_hi:[1,0]\n\t" \
    "v_pk_mul_f32 v[250:251], v[250:251], v[242:243] op_sel_hi:[1,0]\n\t" "v_pk_mul_f32 v[252:253], v[252:253], v[242:243] op_sel_hi:[1,0]\n\t" \
    "v_pk_mul_f32 v[254:255], v[254:255], v[242:243] op_sel_hi:[1,0]\n\t" "v_cvt_pk_bf16_f32 v162, v244, v245\n\t" "v_cvt_pk_bf16_f32 v163, v246, v247\n\t" \
    "v_cvt_pk_bf16_f32 v164, v248, v249\n\t" "v_cvt_pk_bf16_f32 v165, v250, v251\n\t" "v_cvt_pk_bf16_f32 v166, v252, v253\n\t" "v_cvt_pk_bf16_f32 v167, v254, v255\n\t" \
    "buffer_store_dwordx4 v[164:167], %[px], %[rs], %[tb] offen sc1\n\t" "buffer_store_dwordx2 v[162:163], %[pm], %[rs], %[tb] offen sc1\n\t" \
    "buffer_store_dwordx4 v[192:195], %[px], %[rs], %[tr] offen sc1\n\t" "buffer_store_dwordx2 v[192:193], %[pm], %[rs], %[tr] offen sc1\n\t" "s_waitcnt vmcnt(4)\n\t" \
    "v_readfirstlane_b32 s62, v240\n\t" "s_nop 3\n\t" "s_max_u32 %[sq], %[sq], s62\n\t" XT(96, 97) "s_branch 99f\n\t" "31:\n\t" "s_cmp_eq_u32 %[bd], 0\n\t" \
    "s_cbranch_scc1 36f\n\t" "s_sub_u32 %[bd], %[bd], 1\n\t" XCNT(100) "s_waitcnt lgkmcnt(0)\n\t" "s_nop 7\n\t" "s_branch 30b\n\t" "36:\n\t" "s_cmp_lg_u32 s60, 0\n\t" \
    "s_branch 32b\n\t" "34:\n\t" "s_cmp_eq_u32 %[bd], 0\n\t" "s_cbranch_scc1 35b\n\t" "s_sub_u32 %[bd], %[bd], 1\n\t" XCNT(101) "s_mov_b32 s60, 1\n\t" \
    "s_waitcnt lgkmcnt(0)\n\t" "s_nop 7\n\t" "s_branch 30b\n\t" "99:\n\t"
#define QK_STEP3 \
    "v_xor_b32 v216, 0x400, %[mo]\n\t" "s_mov_b32 s60, 0\n\t" XT(80, 81) "s_getpc_b64 s[62:63]\n\t" "48:\n\t" "s_memtime s[64:65]\n\t" "s_waitcnt lgkmcnt(0)\n\t" \
    "s_sub_u32 s61, %[tg], s64\n\t" "s_cmp_gt_i32 s61, 124\n\t" "s_cbranch_scc1 48b\n\t" "s_max_i32 s61, s61, 0\n\t" "s_and_b32 s61, s61, 124\n\t" \
    "s_sub_u32 s61, 49f-48b, s61\n\t" "s_add_u32 s62, s62, s61\n\t" "s_addc_u32 s63, s63, 0\n\t" "s_setpc_b64 s[62:63]\n\t" ".rept 31\n\t" "s_nop 0\n\t" ".endr\n\t" "49:\n\t" \
    XT(98, 99) "s_barrier\n\t" XT(82, 83) "40:\n\t" "buffer_load_dwordx4 v[240:243], %[mo], %[rs], %[sb] offen offset:0 sc1\n\t" "v_mov_b64 v[224:225], 0\n\t" \
    "v_mov_b64 v[226:227], 0\n\t" "v_mov_b64 v[228:229], 0\n\t" "v_mov_b64 v[230:231], 0\n\t" "v_mov_b64 v[232:233], 0\n\t" "v_mov_b64 v[234:235], 0\n\t" \
    "v_mov_b64 v[236:237], 0\n\t" "v_mov_b64 v[238:239], 0\n\t" "buffer_load_dwordx4 v[244:247], v216, %[rs], %[sb] offen offset:0 sc1\n\t" \
    "ds_read_b128 v[160:163], %[l0] offset:0\n\t" "ds_read_b128 v[164:167], %[l0] offset:1024\n\t" "ds_read_b128 v[168:171], %[l0] offset:2048\n\t" \
    "ds_read_b128 v[172:175], %[l0] offset:3072\n\t" "s_nop 3\n\t" "buffer_load_dwordx4 v[248:251], %[mo], %[rs], %[sb] offen offset:2048 sc1\n\t" \
    "ds_read_b128 v[176:179], %[l0] offset:4096\n\t" "ds_read_b128 v[180:183], %[l0] offset:5120\n\t" "ds_read_b128 v[184:187], %[l0] offset:6144\n\t" \
    "ds_read_b128 v[188:191], %[l0] offset:7168\n\t" "s_nop 3\n\t" "buffer_load_dwordx4 v[252:255], v216, %[rs], %[sb] offen offset:2048 sc1\n\t" "s_waitcnt vmcnt(3)\n\t" \
    XT(84, 85) "v_smfmac_f32_16x16x64_bf16 v[224:227], v[240:243], a[0:7], %[ix]\n\t" "ds_write_b128 %[l1], v[240:243] offset:12288\n\t" \
    "v_smfmac_f32_16x16x64_bf16 v[228:231], v[240:243], a[8:15], %[ix]\n\t" "v_max3_u32 v217, v240, v241, v242\n\t" \
    "v_smfmac_f32_16x16x64_bf16 v[232:235], v[240:243], v[32:39], %[ix]\n\t" "s_waitcnt lgkmcnt(7)\n\t" \
    "v_smfmac_f32_16x16x64_bf16 v[236:239], v[240:243], v[160:167], %[ix]\n\t" "ds_read_b128 v[160:163], %[l0] offset:8192\n\t" \
    "ds_read_b128 v[164:167], %[l0] offset:9216\n\t" "s_waitcnt vmcnt(2)\n\t" "v_smfmac_f32_16x16x64_bf16 v[224:227], v[244:247], a[16:23], %[ix]\n\t" \
    "ds_write_b128 %[l1], v[244:247] offset:13312\n\t" "v_max3_u32 v217, v217, v243, v244\n\t" "v_smfmac_f32_16x16x64_bf16 v[228:231], v[244:247], a[24:31], %[ix]\n\t" \
    "v_max3_u32 v217, v217, v245, v246\n\t" "v_max_u32 v217, v217, v247\n\t" "v_smfmac_f32_16x16x64_bf16 v[232:235], v[244:247], v[40:47], %[ix]\n\t" \
    "v_cmp_eq_u32 vcc, -1, v217\n\t" "s_cmp_lg_u32 s60, 0\n\t" "s_waitcnt lgkmcnt(8)\n\t" "v_smfmac_f32_16x16x64_bf16 v[236:239], v[244:247], v[168:175], %[ix]\n\t" \
    "s_cbranch_vccnz 41f\n\t" "42:\n\t" "ds_read_b128 v[168:171], %[l0] offset:10240\n\t" "s_waitcnt vmcnt(1)\n\t" \
    "v_smfmac_f32_16x16x64_bf16 v[224:227], v[248:251], a[32:39], %[ix]\n\t" "ds_write_b128 %[l1], v[248:251] offset:14336\n\t" "s_waitcnt lgkmcnt(2)\n\t" \
    "v_smfmac_f32_16x16x64_bf16 v[228:231], v[248:251], a[40:47], %[ix]\n\t" "s_cbranch_scc1 43f\n\t" "s_barrier\n\t" "43:\n\t" XT(86, 87) \
    "v_smfmac_f32_16x16x64_bf16 v[232:235], v[248:251], v[48:55], %[ix]\n\t" "ds_read_b128 v[192:195], %[l1] offset:0\n\t" "ds_read_b128 v[196:199], %[l1] offset:4096\n\t" \
    "v_smfmac_f32_16x16x64_bf16 v[236:239], v[248:251], v[176:183], %[ix]\n\t" "ds_read_b128 v[172:175], %[l0] offset:11264\n\t" \
    "ds_read_b128 v[176:179], %[l0] offset:12288\n\t" "s_waitcnt vmcnt(0)\n\t" "v_smfmac_f32_16x16x64_bf16 v[224:227], v[252:255], a[48:55], %[ix]\n\t" \
    "ds_write_b128 %[l1], v[252:255] offset:15360\n\t" "v_max3_u32 v217, v248, v249, v250\n\t" "v_smfmac_f32_16x16x64_bf16 v[228:231], v[252:255], a[56:63], %[ix]\n\t" \
    "ds_read_b128 v[180:183], %[l0] offset:13312\n\t" "v_max3_u32 v217, v217, v251, v252\n\t" "v_max3_u32 v217, v217, v253, v254\n\t" \
    "v_smfmac_f32_16x16x64_bf16 v[232:235], v[252:255], v[56:63], %[ix]\n\t" "v_max_u32 v217, v217, v255\n\t" "v_cmp_eq_u32 vcc, -1, v217\n\t" \
    "v_smfmac_f32_16x16x64_bf16 v[236:239], v[252:255], v[184:191], %[ix]\n\t" "s_cbranch_vccnz 44f\n\t" "45:\n\t" "ds_read_b128 v[240:243], %[l1] offset:8192\n\t" \
    "s_waitcnt lgkmcnt(6)\n\t" "v_smfmac_f32_16x16x64_bf16 v[224:227], v[192:195], a[64:71], %[ix]\n\t" "ds_read_b128 v[184:187], %[l0] offset:14336\n\t" \
    "ds_read_b128 v[188:191], %[l0] offset:15360\n\t" "v_smfmac_f32_16x16x64_bf16 v[228:231], v[192:195], a[72:79], %[ix]\n\t" \
    "ds_read_b128 v[244:247], %[l1] offset:1024\n\t" "s_waitcnt lgkmcnt(5)\n\t" "v_smfmac_f32_16x16x64_bf16 v[232:235], v[192:195], v[64:71], %[ix]\n\t" "s_barrier\n\t" \
    XT(88, 89) "s_memtime s[64:65]\n\t" "v_smfmac_f32_16x16x64_bf16 v[236:239], v[192:195], v[160:167], %[ix]\n\t" "ds_read_b128 v[160:163], %[l0] offset:16384\n\t" \
    "ds_read_b128 v[164:167], %[l0] offset:17408\n\t" "v_smfmac_f32_16x16x64_bf16 v[224:227], v[196:199], a[80:87], %[ix]\n\t" \
    "ds_read_b128 v[248:251], %[l1] offset:5120\n\t" "ds_read_b128 v[252:255], %[l1] offset:9216\n\t" "v_smfmac_f32_16x16x64_bf16 v[228:231], v[196:199], a[88:95], %[ix]\n\t" \
    "ds_read_b128 v[192:195], %[l1] offset:2048\n\t" "v_smfmac_f32_16x16x64_bf16 v[232:235], v[196:199], v[72:79], %[ix]\n\t" "s_sub_u32 s62, s64, %[ca]\n\t" \
    "s_mul_i32 s62, s62, %[k1]\n\t" "v_smfmac_f32_16x16x64_bf16 v[236:239], v[196:199], v[168:175], %[ix]\n\t" "s_lshr_b32 s62, s62, 16\n\t" "s_add_u32 s62, s62, %[ga]\n\t" \
    "s_waitcnt lgkmcnt(8)\n\t" "v_smfmac_f32_16x16x64_bf16 v[224:227], v[240:243], a[96:103], %[ix]\n\t" "s_bitset0_b32 s62, 0\n\t" "v_mov_b32 v218, s62\n\t" \
    "v_smfmac_f32_16x16x64_bf16 v[228:231], v[240:243], a[104:111], %[ix]\n\t" "buffer_store_dword v218, %[hq], %[rs], %[pb] offen sc1\n\t" \
    "ds_read_b128 v[168:171], %[l0] offset:18432\n\t" "v_smfmac_f32_16x16x64_bf16 v[232:235], v[240:243], v[80:87], %[ix]\n\t" \
    "ds_read_b128 v[172:175], %[l0] offset:19456\n\t" "ds_read_b128 v[196:199], %[l1] offset:6144\n\t" \
    "v_smfmac_f32_16x16x64_bf16 v[236:239], v[240:243], v[176:183], %[ix]\n\t" "ds_read_b128 v[176:179], %[l0] offset:20480\n\t" \
    "ds_read_b128 v[180:183], %[l0] offset:21504\n\t" "s_waitcnt lgkmcnt(10)\n\t" "v_smfmac_f32_16x16x64_bf16 v[224:227], v[244:247], a[112:119], %[ix]\n\t" \
    "ds_read_b128 v[240:243], %[l1] offset:10240\n\t" "v_smfmac_f32_16x16x64_bf16 v[228:231], v[244:247], a[120:127], %[ix]\n\t" \
    "v_smfmac_f32_16x16x64_bf16 v[232:235], v[244:247], v[88:95], %[ix]\n\t" "v_smfmac_f32_16x16x64_bf16 v[236:239], v[244:247], v[184:191], %[ix]\n\t" \
    "ds_read_b128 v[184:187], %[l0] offset:22528\n\t" "ds_read_b128 v[188:191], %[l0] offset:23552\n\t" "s_waitcnt lgkmcnt(10)\n\t" \
    "v_smfmac_f32_16x16x64_bf16 v[224:227], v[248:251], a[128:135], %[ix]\n\t" "ds_read_b128 v[244:247], %[l1] offset:3072\n\t" \
    "v_smfmac_f32_16x16x64_bf16 v[228:231], v[248:251], a[136:143], %[ix]\n\t" "v_smfmac_f32_16x16x64_bf16 v[232:235], v[248:251], v[96:103], %[ix]\n\t" \
    "v_smfmac_f32_16x16x64_bf16 v[236:239], v[248:251], v[160:167], %[ix]\n\t" "ds_read_b128 v[160:163], %[l0] offset:24576\n\t" \
    "ds_read_b128 v[164:167], %[l0] offset:25600\n\t" "s_waitcnt lgkmcnt(12)\n\t" "v_smfmac_f32_16x16x64_bf16 v[224:227], v[252:255], a[144:151], %[ix]\n\t" \
    "ds_read_b128 v[248:251], %[l1] offset:7168\n\t" "v_smfmac_f32_16x16x64_bf16 v[228:231], v[252:255], a[152:159], %[ix]\n\t" \
    "v_smfmac_f32_16x16x64_bf16 v[232:235], v[252:255], v[104:111], %[ix]\n\t" "s_waitcnt lgkmcnt(10)\n\t" \
    "v_smfmac_f32_16x16x64_bf16 v[236:239], v[252:255], v[168:175], %[ix]\n\t" "ds_read_b128 v[168:171], %[l0] offset:26624\n\t" \
    "ds_read_b128 v[172:175], %[l0] offset:27648\n\t" "v_smfmac_f32_16x16x64_bf16 v[224:227], v[192:195], a[160:167], %[ix]\n\t" \
    "ds_read_b128 v[252:255], %[l1] offset:11264\n\t" "v_smfmac_f32_16x16x64_bf16 v[228:231], v[192:195], a[168:175], %[ix]\n\t" "s_waitcnt lgkmcnt(12)\n\t" \
    "v_smfmac_f32_16x16x64_bf16 v[224:227], v[196:199], a[176:183], %[ix]\n\t" "v_smfmac_f32_16x16x64_bf16 v[228:231], v[196:199], a[184:191], %[ix]\n\t" \
    "s_waitcnt lgkmcnt(9)\n\t" "v_smfmac_f32_16x16x64_bf16 v[224:227], v[240:243], a[192:199], %[ix]\n\t" \
    "v_smfmac_f32_16x16x64_bf16 v[228:231], v[240:243], a[200:207], %[ix]\n\t" "s_waitcnt lgkmcnt(6)\n\t" \
    "v_smfmac_f32_16x16x64_bf16 v[224:227], v[244:247], a[208:215], %[ix]\n\t" "v_smfmac_f32_16x16x64_bf16 v[228:231], v[244:247], a[216:223], %[ix]\n\t" \
    "s_waitcnt lgkmcnt(3)\n\t" "v_smfmac_f32_16x16x64_bf16 v[224:227], v[248:251], a[224:231], %[ix]\n\t" \
    "v_smfmac_f32_16x16x64_bf16 v[228:231], v[248:251], a[232:239], %[ix]\n\t" "s_waitcnt lgkmcnt(0)\n\t" \
    "v_smfmac_f32_16x16x64_bf16 v[224:227], v[252:255], a[240:247], %[ix]\n\t" "s_nop 1\n\t" "v_smfmac_f32_16x16x64_bf16 v[228:231], v[252:255], a[248:255], %[ix]\n\t" \
    XT(90, 91) "s_nop 1\n\t" "v_smfmac_f32_16x16x64_bf16 v[232:235], v[192:195], v[112:119], %[ix]\n\t" "s_nop 1\n\t" \
    "v_smfmac_f32_16x16x64_bf16 v[236:239], v[192:195], v[176:183], %[ix]\n\t" "ds_read_b128 v[176:179], %[l0] offset:28672\n\t" "v_add_f32 v224, v224, v225\n\t" \
    "v_add_f32 v226, v226, v227\n\t" "v_smfmac_f32_16x16x64_bf16 v[232:235], v[196:199], v[120:127], %[ix]\n\t" "ds_read_b128 v[180:183], %[l0] offset:29696\n\t" \
    "v_add_f32 v228, v228, v229\n\t" "v_add_f32 v230, v230, v231\n\t" "v_smfmac_f32_16x16x64_bf16 v[236:239], v[196:199], v[184:191], %[ix]\n\t" \
    "ds_read_b128 v[184:187], %[l0] offset:30720\n\t" "v_cvt_pk_bf16_f32 v218, v224, v228\n\t" "v_cvt_pk_bf16_f32 v219, v226, v230\n\t" \
    "v_smfmac_f32_16x16x64_bf16 v[232:235], v[240:243], v[128:135], %[ix]\n\t" "ds_write2_b32 %[qw], v218, v219 offset1:8\n\t" \
    "ds_read_b128 v[188:191], %[l0] offset:31744\n\t" "v_smfmac_f32_16x16x64_bf16 v[236:239], v[240:243], v[160:167], %[ix]\n\t" \
    "v_smfmac_f32_16x16x64_bf16 v[232:235], v[244:247], v[136:143], %[ix]\n\t" "v_smfmac_f32_16x16x64_bf16 v[236:239], v[244:247], v[168:175], %[ix]\n\t" \
    "s_waitcnt lgkmcnt(1)\n\t" "s_barrier\n\t" "v_smfmac_f32_16x16x64_bf16 v[232:235], v[248:251], v[144:151], %[ix]\n\t" \
    "v_smfmac_f32_16x16x64_bf16 v[236:239], v[248:251], v[176:183], %[ix]\n\t" "v_smfmac_f32_16x16x64_bf16 v[232:235], v[252:255], v[152:159], %[ix]\n\t" \
    "s_waitcnt lgkmcnt(0)\n\t" "v_smfmac_f32_16x16x64_bf16 v[236:239], v[252:255], v[184:191], %[ix]\n\t" XT(92, 93) "s_nop 6\n\t" "v_add_f32 v232, v232, v233\n\t" \
    "v_add_f32 v234, v234, v235\n\t" "v_add_f32 v236, v236, v237\n\t" "v_add_f32 v238, v238, v239\n\t" "v_cvt_pk_bf16_f32 v232, v232, v236\n\t" \
    "v_cvt_pk_bf16_f32 v233, v234, v238\n\t" "ds_write2_b32 %[kw], v232, v233 offset1:68\n\t" "s_waitcnt lgkmcnt(0)\n\t" "s_barrier\n\t" XT(94, 95) XT(96, 97) \
    "s_branch 99f\n\t" "41:\n\t" "s_cmp_eq_u32 %[bd], 0\n\t" "s_cbranch_scc1 46f\n\t" "s_sub_u32 %[bd], %[bd], 1\n\t" XCNT(100) "s_waitcnt lgkmcnt(0)\n\t" "s_nop 7\n\t" \
    "s_branch 40b\n\t" "46:\n\t" "s_cmp_lg_u32 s60, 0\n\t" "s_branch 42b\n\t" "44:\n\t" "s_cmp_eq_u32 %[bd], 0\n\t" "s_cbranch_scc1 45b\n\t" "s_sub_u32 %[bd], %[bd], 1\n\t" \
    XCNT(101) "s_mov_b32 s60, 1\n\t" "s_waitcnt lgkmcnt(0)\n\t" "s_nop 7\n\t" "s_branch 40b\n\t" "99:\n\t" "mk_tail:\n\t"
#define VO_STEP \
    "v_xor_b32 v224, 0x400, %[mo]\n\t" "s_mov_b32 s60, 0\n\t" "s_cmp_eq_u32 %[w], 1\n\t" "s_cbranch_scc1 91f\n\t" "s_cmp_eq_u32 %[w], 2\n\t" "s_cbranch_scc1 92f\n\t" \
    "s_cmp_eq_u32 %[w], 3\n\t" "s_cbranch_scc1 93f\n\t" XT(80, 81) "s_getpc_b64 s[62:63]\n\t" "18:\n\t" "s_memtime s[64:65]\n\t" "s_waitcnt lgkmcnt(0)\n\t" \
    "s_sub_u32 s61, %[tg], s64\n\t" "s_cmp_gt_i32 s61, 124\n\t" "s_cbranch_scc1 18b\n\t" "s_max_i32 s61, s61, 0\n\t" "s_and_b32 s61, s61, 124\n\t" \
    "s_sub_u32 s61, 19f-18b, s61\n\t" "s_add_u32 s62, s62, s61\n\t" "s_addc_u32 s63, s63, 0\n\t" "s_setpc_b64 s[62:63]\n\t" ".rept 31\n\t" "s_nop 0\n\t" ".endr\n\t" "19:\n\t" \
    "s_barrier\n\t" XT(82, 83) "10:\n\t" "buffer_load_dwordx4 v[240:243], %[mo], %[rs], %[sb] offen offset:0 sc1\n\t" "v_mov_b64 v[232:233], 0\n\t" \
    "v_mov_b64 v[234:235], 0\n\t" "v_mov_b64 v[236:237], 0\n\t" "v_mov_b64 v[238:239], 0\n\t" "v_mov_b64 v[192:193], 0\n\t" "v_mov_b64 v[194:195], 0\n\t" \
    "v_mov_b64 v[196:197], 0\n\t" "v_mov_b64 v[198:199], 0\n\t" "buffer_load_dwordx4 v[244:247], v224, %[rs], %[sb] offen offset:0 sc1\n\t" \
    "ds_read_b128 v[160:163], %[l0] offset:0\n\t" "ds_read_b128 v[164:167], %[l0] offset:1024\n\t" "ds_read_b128 v[168:171], %[l0] offset:2048\n\t" \
    "ds_read_b128 v[172:175], %[l0] offset:3072\n\t" "s_nop 3\n\t" "buffer_load_dwordx4 v[248:251], %[mo], %[rs], %[sb] offen offset:2048 sc1\n\t" \
    "ds_read_b128 v[176:179], %[l0] offset:4096\n\t" "ds_read_b128 v[180:183], %[l0] offset:5120\n\t" "ds_read_b128 v[184:187], %[l0] offset:6144\n\t" \
    "ds_read_b128 v[188:191], %[l0] offset:7168\n\t" "s_nop 3\n\t" "buffer_load_dwordx4 v[252:255], v224, %[rs], %[sb] offen offset:2048 sc1\n\t" \
    "v_mov_b64 v[200:201], 0\n\t" "v_mov_b64 v[202:203], 0\n\t" "v_mov_b64 v[204:205], 0\n\t" "v_mov_b64 v[206:207], 0\n\t" "v_mov_b64 v[208:209], 0\n\t" \
    "v_mov_b64 v[210:211], 0\n\t" "v_mov_b64 v[212:213], 0\n\t" "v_mov_b64 v[214:215], 0\n\t" "v_mov_b64 v[216:217], 0\n\t" "v_mov_b64 v[218:219], 0\n\t" \
    "v_mov_b64 v[220:221], 0\n\t" "v_mov_b64 v[222:223], 0\n\t" "s_waitcnt vmcnt(3)\n\t" XT(84, 85) "v_smfmac_f32_16x16x64_bf16 v[232:235], v[240:243], a[0:7], %[ix]\n\t" \
    "ds_write_b128 %[l1], v[240:243] offset:0\n\t" "v_smfmac_f32_16x16x64_bf16 v[236:239], v[240:243], a[8:15], %[ix]\n\t" "v_max3_u32 v225, v240, v241, v242\n\t" \
    "s_waitcnt vmcnt(2)\n\t" "v_smfmac_f32_16x16x64_bf16 v[232:235], v[244:247], a[16:23], %[ix]\n\t" "ds_write_b128 %[l1], v[244:247] offset:1024\n\t" \
    "v_max3_u32 v225, v225, v243, v244\n\t" "s_waitcnt lgkmcnt(8)\n\t" "v_smfmac_f32_16x16x64_bf16 v[236:239], v[244:247], v[160:167], %[ix]\n\t" \
    "v_max3_u32 v225, v225, v245, v246\n\t" "v_max_u32 v225, v225, v247\n\t" "v_cmp_eq_u32 vcc, -1, v225\n\t" "s_cmp_lg_u32 s60, 0\n\t" "s_cbranch_vccnz 11f\n\t" "12:\n\t" \
    "s_waitcnt lgkmcnt(0)\n\t" "s_cbranch_scc1 13f\n\t" "s_barrier\n\t" "13:\n\t" XT(86, 87) "ds_read_b128 v[16:19], %[l1] offset:4096\n\t" \
    "ds_read_b128 v[20:23], %[l1] offset:8192\n\t" "ds_read_b128 v[24:27], %[l1] offset:12288\n\t" "s_waitcnt vmcnt(1)\n\t" \
    "v_smfmac_f32_16x16x64_bf16 v[232:235], v[248:251], a[24:31], %[ix]\n\t" "ds_write_b128 %[l1], v[248:251] offset:2048\n\t" "v_max3_u32 v225, v248, v249, v250\n\t" \
    "v_smfmac_f32_16x16x64_bf16 v[236:239], v[248:251], a[32:39], %[ix]\n\t" "ds_read_b128 v[28:31], %[l1] offset:5120\n\t" "ds_read_b128 v[240:243], %[l1] offset:9216\n\t" \
    "ds_read_b128 v[244:247], %[l1] offset:13312\n\t" "s_waitcnt vmcnt(0)\n\t" "v_smfmac_f32_16x16x64_bf16 v[232:235], v[252:255], a[40:47], %[ix]\n\t" \
    "ds_write_b128 %[l1], v[252:255] offset:3072\n\t" "v_max3_u32 v225, v225, v251, v252\n\t" "v_smfmac_f32_16x16x64_bf16 v[236:239], v[252:255], v[168:175], %[ix]\n\t" \
    "v_max3_u32 v225, v225, v253, v254\n\t" "v_max_u32 v225, v225, v255\n\t" "s_waitcnt lgkmcnt(7)\n\t" "v_smfmac_f32_16x16x64_bf16 v[232:235], v[16:19], a[48:55], %[ix]\n\t" \
    "v_cmp_eq_u32 vcc, -1, v225\n\t" "s_cbranch_vccnz 14f\n\t" "15:\n\t" "v_smfmac_f32_16x16x64_bf16 v[236:239], v[16:19], a[56:63], %[ix]\n\t" \
    "ds_read_b128 v[160:163], %[l0] offset:8192\n\t" "ds_read_b128 v[164:167], %[l0] offset:9216\n\t" "s_waitcnt lgkmcnt(8)\n\t" \
    "v_smfmac_f32_16x16x64_bf16 v[232:235], v[20:23], a[64:71], %[ix]\n\t" "ds_read_b128 v[168:171], %[l0] offset:10240\n\t" "ds_read_b128 v[172:175], %[l0] offset:11264\n\t" \
    "v_smfmac_f32_16x16x64_bf16 v[236:239], v[20:23], v[176:183], %[ix]\n\t" "s_waitcnt lgkmcnt(4)\n\t" "s_barrier\n\t" XT(88, 89) "s_memtime s[64:65]\n\t" \
    "v_smfmac_f32_16x16x64_bf16 v[232:235], v[24:27], a[72:79], %[ix]\n\t" "ds_read_b128 v[176:179], %[l0] offset:12288\n\t" "ds_read_b128 v[180:183], %[l0] offset:13312\n\t" \
    "v_smfmac_f32_16x16x64_bf16 v[236:239], v[24:27], a[80:87], %[ix]\n\t" "ds_read_b128 v[248:251], %[l1] offset:6144\n\t" "ds_read_b128 v[252:255], %[l1] offset:10240\n\t" \
    "v_smfmac_f32_16x16x64_bf16 v[232:235], v[28:31], a[88:95], %[ix]\n\t" "ds_read_b128 v[16:19], %[l1] offset:14336\n\t" "ds_read_b128 v[20:23], %[l1] offset:7168\n\t" \
    "v_smfmac_f32_16x16x64_bf16 v[236:239], v[28:31], v[184:191], %[ix]\n\t" "ds_read_b128 v[24:27], %[l1] offset:11264\n\t" "ds_read_b128 v[184:187], %[l0] offset:14336\n\t" \
    "v_smfmac_f32_16x16x64_bf16 v[232:235], v[240:243], a[96:103], %[ix]\n\t" "ds_read_b128 v[188:191], %[l0] offset:15360\n\t" \
    "ds_read_b128 v[28:31], %[l1] offset:15360\n\t" "v_smfmac_f32_16x16x64_bf16 v[236:239], v[240:243], a[104:111], %[ix]\n\t" "s_sub_u32 s62, s64, %[ca]\n\t" \
    "s_mul_i32 s62, s62, %[k1]\n\t" "v_smfmac_f32_16x16x64_bf16 v[232:235], v[244:247], a[112:119], %[ix]\n\t" "s_lshr_b32 s62, s62, 16\n\t" "s_add_u32 s62, s62, %[ga]\n\t" \
    "s_waitcnt lgkmcnt(12)\n\t" "v_smfmac_f32_16x16x64_bf16 v[236:239], v[244:247], v[160:167], %[ix]\n\t" "s_bitset0_b32 s62, 0\n\t" "v_mov_b32 v226, s62\n\t" \
    "s_waitcnt lgkmcnt(7)\n\t" "v_smfmac_f32_16x16x64_bf16 v[232:235], v[248:251], a[120:127], %[ix]\n\t" "buffer_store_dword v226, %[hq], %[rs], %[pb] offen sc1\n\t" \
    "ds_read_b128 v[160:163], %[l0] offset:16384\n\t" "v_smfmac_f32_16x16x64_bf16 v[236:239], v[248:251], a[128:135], %[ix]\n\t" \
    "ds_read_b128 v[164:167], %[l0] offset:17408\n\t" "s_waitcnt lgkmcnt(8)\n\t" "v_smfmac_f32_16x16x64_bf16 v[232:235], v[252:255], a[136:143], %[ix]\n\t" \
    "v_smfmac_f32_16x16x64_bf16 v[236:239], v[252:255], v[168:175], %[ix]\n\t" "ds_read_b128 v[168:171], %[l0] offset:18432\n\t" \
    "ds_read_b128 v[172:175], %[l0] offset:19456\n\t" "s_waitcnt lgkmcnt(9)\n\t" "v_smfmac_f32_16x16x64_bf16 v[232:235], v[16:19], a[144:151], %[ix]\n\t" \
    "v_smfmac_f32_16x16x64_bf16 v[236:239], v[16:19], a[152:159], %[ix]\n\t" "s_waitcnt lgkmcnt(8)\n\t" \
    "v_smfmac_f32_16x16x64_bf16 v[232:235], v[20:23], a[160:167], %[ix]\n\t" "v_smfmac_f32_16x16x64_bf16 v[236:239], v[20:23], v[176:183], %[ix]\n\t" \
    "ds_read_b128 v[176:179], %[l0] offset:20480\n\t" "ds_read_b128 v[180:183], %[l0] offset:21504\n\t" "s_waitcnt lgkmcnt(9)\n\t" \
    "v_smfmac_f32_16x16x64_bf16 v[232:235], v[24:27], a[168:175], %[ix]\n\t" "v_smfmac_f32_16x16x64_bf16 v[236:239], v[24:27], a[176:183], %[ix]\n\t" \
    "s_waitcnt lgkmcnt(6)\n\t" "v_smfmac_f32_16x16x64_bf16 v[232:235], v[28:31], a[184:191], %[ix]\n\t" \
    "v_smfmac_f32_16x16x64_bf16 v[236:239], v[28:31], v[184:191], %[ix]\n\t" "ds_read_b128 v[184:187], %[l0] offset:22528\n\t" \
    "ds_read_b128 v[188:191], %[l0] offset:23552\n\t" XT(90, 91) "s_nop 4\n\t" "v_add_f32 v232, v232, v233\n\t" "v_add_f32 v234, v234, v235\n\t" \
    "v_add_f32 v236, v236, v237\n\t" "v_add_f32 v238, v238, v239\n\t" "v_cvt_pk_bf16_f32 v232, v232, v236\n\t" "v_cvt_pk_bf16_f32 v233, v234, v238\n\t" \
    "ds_write2_b32 %[vw], v232, v233 offset1:68\n\t" "s_waitcnt lgkmcnt(0)\n\t" "s_barrier\n\t" XT(92, 93) "ds_read_b128 v[240:243], %[vr] offset:0\n\t" \
    "ds_read_b128 v[244:247], %[vr] offset:64\n\t" "ds_read_b128 v[248:251], %[vr] offset:128\n\t" "ds_read_b128 v[252:255], %[vr] offset:192\n\t" "s_mov_b32 s61, %[ub]\n\t" \
    "s_waitcnt lgkmcnt(3)\n\t" "v_smfmac_f32_16x16x64_bf16 v[192:195], v[240:243], v[32:39], %[ix]\n\t" \
    "v_smfmac_f32_16x16x64_bf16 v[196:199], v[240:243], v[48:55], %[ix]\n\t" "s_waitcnt lgkmcnt(2)\n\t" \
    "v_smfmac_f32_16x16x64_bf16 v[192:195], v[244:247], v[40:47], %[ix]\n\t" "v_smfmac_f32_16x16x64_bf16 v[196:199], v[244:247], v[56:63], %[ix]\n\t" \
    "s_waitcnt lgkmcnt(1)\n\t" "v_smfmac_f32_16x16x64_bf16 v[192:195], v[248:251], a[192:199], %[ix]\n\t" \
    "v_smfmac_f32_16x16x64_bf16 v[196:199], v[248:251], a[200:207], %[ix]\n\t" "s_waitcnt lgkmcnt(0)\n\t" \
    "v_smfmac_f32_16x16x64_bf16 v[192:195], v[252:255], v[160:167], %[ix]\n\t" "ds_read_b128 v[160:163], %[l0] offset:24576\n\t" \
    "ds_read_b128 v[164:167], %[l0] offset:25600\n\t" "v_smfmac_f32_16x16x64_bf16 v[196:199], v[252:255], v[168:175], %[ix]\n\t" \
    "ds_read_b128 v[168:171], %[l0] offset:26624\n\t" "ds_read_b128 v[172:175], %[l0] offset:27648\n\t" \
    "v_smfmac_f32_16x16x64_bf16 v[200:203], v[240:243], v[64:71], %[ix]\n\t" "s_nop 1\n\t" "v_smfmac_f32_16x16x64_bf16 v[204:207], v[240:243], v[80:87], %[ix]\n\t" \
    "v_cvt_pk_bf16_f32 v224, v192, v194\n\t" "v_cvt_pk_bf16_f32 v225, v193, v195\n\t" "v_smfmac_f32_16x16x64_bf16 v[200:203], v[244:247], v[72:79], %[ix]\n\t" \
    "v_cvt_pk_bf16_f32 v226, v196, v198\n\t" "v_cvt_pk_bf16_f32 v227, v197, v199\n\t" "v_smfmac_f32_16x16x64_bf16 v[204:207], v[244:247], v[88:95], %[ix]\n\t" "s_nop 0\n\t" \
    "v_permlane32_swap_b32 v224, v226\n\t" "v_smfmac_f32_16x16x64_bf16 v[200:203], v[248:251], a[208:215], %[ix]\n\t" "v_permlane32_swap_b32 v225, v227\n\t" "s_nop 0\n\t" \
    "v_smfmac_f32_16x16x64_bf16 v[204:207], v[248:251], a[216:223], %[ix]\n\t" "v_permlane16_swap_b32 v224, v225\n\t" "v_permlane16_swap_b32 v226, v227\n\t" \
    "v_smfmac_f32_16x16x64_bf16 v[200:203], v[252:255], v[176:183], %[ix]\n\t" "buffer_store_dwordx4 v[224:227], %[uo], %[rs], s61 offen sc1\n\t" \
    "s_add_u32 s61, s61, 0x10000\n\t" XT(94, 95) "v_smfmac_f32_16x16x64_bf16 v[204:207], v[252:255], v[184:191], %[ix]\n\t" "ds_read_b128 v[176:179], %[l0] offset:28672\n\t" \
    "ds_read_b128 v[180:183], %[l0] offset:29696\n\t" "v_smfmac_f32_16x16x64_bf16 v[208:211], v[240:243], v[96:103], %[ix]\n\t" \
    "ds_read_b128 v[184:187], %[l0] offset:30720\n\t" "ds_read_b128 v[188:191], %[l0] offset:31744\n\t" \
    "v_smfmac_f32_16x16x64_bf16 v[212:215], v[240:243], v[112:119], %[ix]\n\t" "v_cvt_pk_bf16_f32 v228, v200, v202\n\t" "v_cvt_pk_bf16_f32 v229, v201, v203\n\t" \
    "v_smfmac_f32_16x16x64_bf16 v[208:211], v[244:247], v[104:111], %[ix]\n\t" "v_cvt_pk_bf16_f32 v230, v204, v206\n\t" "v_cvt_pk_bf16_f32 v231, v205, v207\n\t" \
    "v_smfmac_f32_16x16x64_bf16 v[212:215], v[244:247], v[120:127], %[ix]\n\t" "s_nop 0\n\t" "v_permlane32_swap_b32 v228, v230\n\t" \
    "v_smfmac_f32_16x16x64_bf16 v[208:211], v[248:251], a[224:231], %[ix]\n\t" "v_permlane32_swap_b32 v229, v231\n\t" "s_nop 0\n\t" \
    "v_smfmac_f32_16x16x64_bf16 v[212:215], v[248:251], a[232:239], %[ix]\n\t" "v_permlane16_swap_b32 v228, v229\n\t" "v_permlane16_swap_b32 v230, v231\n\t" \
    "s_waitcnt lgkmcnt(6)\n\t" "v_smfmac_f32_16x16x64_bf16 v[208:211], v[252:255], v[160:167], %[ix]\n\t" "buffer_store_dwordx4 v[228:231], %[uo], %[rs], s61 offen sc1\n\t" \
    "s_add_u32 s61, s61, 0x10000\n\t" "s_waitcnt lgkmcnt(4)\n\t" "v_smfmac_f32_16x16x64_bf16 v[212:215], v[252:255], v[168:175], %[ix]\n\t" "s_nop 1\n\t" \
    "v_smfmac_f32_16x16x64_bf16 v[216:219], v[240:243], v[128:135], %[ix]\n\t" "s_nop 1\n\t" "v_smfmac_f32_16x16x64_bf16 v[220:223], v[240:243], v[144:151], %[ix]\n\t" \
    "v_cvt_pk_bf16_f32 v224, v208, v210\n\t" "v_cvt_pk_bf16_f32 v225, v209, v211\n\t" "v_smfmac_f32_16x16x64_bf16 v[216:219], v[244:247], v[136:143], %[ix]\n\t" \
    "v_cvt_pk_bf16_f32 v226, v212, v214\n\t" "v_cvt_pk_bf16_f32 v227, v213, v215\n\t" "v_smfmac_f32_16x16x64_bf16 v[220:223], v[244:247], v[152:159], %[ix]\n\t" "s_nop 0\n\t" \
    "v_permlane32_swap_b32 v224, v226\n\t" "v_smfmac_f32_16x16x64_bf16 v[216:219], v[248:251], a[240:247], %[ix]\n\t" "v_permlane32_swap_b32 v225, v227\n\t" "s_nop 0\n\t" \
    "v_smfmac_f32_16x16x64_bf16 v[220:223], v[248:251], a[248:255], %[ix]\n\t" "v_permlane16_swap_b32 v224, v225\n\t" "v_permlane16_swap_b32 v226, v227\n\t" \
    "s_waitcnt lgkmcnt(2)\n\t" "v_smfmac_f32_16x16x64_bf16 v[216:219], v[252:255], v[176:183], %[ix]\n\t" "buffer_store_dwordx4 v[224:227], %[uo], %[rs], s61 offen sc1\n\t" \
    "s_add_u32 s61, s61, 0x10000\n\t" "s_waitcnt lgkmcnt(0)\n\t" "v_smfmac_f32_16x16x64_bf16 v[220:223], v[252:255], v[184:191], %[ix]\n\t" "s_nop 5\n\t" \
    "v_cvt_pk_bf16_f32 v228, v216, v218\n\t" "v_cvt_pk_bf16_f32 v229, v217, v219\n\t" "v_cvt_pk_bf16_f32 v230, v220, v222\n\t" "v_cvt_pk_bf16_f32 v231, v221, v223\n\t" \
    "s_nop 0\n\t" "v_permlane32_swap_b32 v228, v230\n\t" "v_permlane32_swap_b32 v229, v231\n\t" "s_nop 1\n\t" "v_permlane16_swap_b32 v228, v229\n\t" \
    "v_permlane16_swap_b32 v230, v231\n\t" "buffer_store_dwordx4 v[228:231], %[uo], %[rs], s61 offen sc1\n\t" "s_nop 1\n\t" XT(96, 97) "s_branch 99f\n\t" "11:\n\t" \
    "s_cmp_eq_u32 %[bd], 0\n\t" "s_cbranch_scc1 16f\n\t" "s_sub_u32 %[bd], %[bd], 1\n\t" XCNT(100) "s_waitcnt lgkmcnt(0)\n\t" "s_nop 7\n\t" "s_branch 10b\n\t" "16:\n\t" \
    "s_cmp_lg_u32 s60, 0\n\t" "s_branch 12b\n\t" "14:\n\t" "s_cmp_eq_u32 %[bd], 0\n\t" "s_cbranch_scc1 15b\n\t" "s_sub_u32 %[bd], %[bd], 1\n\t" XCNT(101) \
    "s_mov_b32 s60, 1\n\t" "s_waitcnt lgkmcnt(0)\n\t" "s_nop 7\n\t" "s_branch 10b\n\t" "91:\n\t" XT(80, 81) "s_barrier\n\t" XT(82, 83) "20:\n\t" \
    "buffer_load_dwordx4 v[240:243], %[mo], %[rs], %[sb] offen offset:0 sc1\n\t" "v_mov_b64 v[232:233], 0\n\t" "v_mov_b64 v[234:235], 0\n\t" "v_mov_b64 v[236:237], 0\n\t" \
    "v_mov_b64 v[238:239], 0\n\t" "v_mov_b64 v[192:193], 0\n\t" "v_mov_b64 v[194:195], 0\n\t" "v_mov_b64 v[196:197], 0\n\t" "v_mov_b64 v[198:199], 0\n\t" \
    "buffer_load_dwordx4 v[244:247], v224, %[rs], %[sb] offen offset:0 sc1\n\t" "ds_read_b128 v[160:163], %[l0] offset:0\n\t" "ds_read_b128 v[164:167], %[l0] offset:1024\n\t" \
    "ds_read_b128 v[168:171], %[l0] offset:2048\n\t" "ds_read_b128 v[172:175], %[l0] offset:3072\n\t" "s_nop 3\n\t" \
    "buffer_load_dwordx4 v[248:251], %[mo], %[rs], %[sb] offen offset:2048 sc1\n\t" "ds_read_b128 v[176:179], %[l0] offset:4096\n\t" \
    "ds_read_b128 v[180:183], %[l0] offset:5120\n\t" "ds_read_b128 v[184:187], %[l0] offset:6144\n\t" "ds_read_b128 v[188:191], %[l0] offset:7168\n\t" "s_nop 3\n\t" \
    "buffer_load_dwordx4 v[252:255], v224, %[rs], %[sb] offen offset:2048 sc1\n\t" "v_mov_b64 v[200:201], 0\n\t" "v_mov_b64 v[202:203], 0\n\t" "v_mov_b64 v[204:205], 0\n\t" \
    "v_mov_b64 v[206:207], 0\n\t" "v_mov_b64 v[208:209], 0\n\t" "v_mov_b64 v[210:211], 0\n\t" "v_mov_b64 v[212:213], 0\n\t" "v_mov_b64 v[214:215], 0\n\t" \
    "v_mov_b64 v[216:217], 0\n\t" "v_mov_b64 v[218:219], 0\n\t" "v_mov_b64 v[220:221], 0\n\t" "v_mov_b64 v[222:223], 0\n\t" "s_waitcnt vmcnt(3)\n\t" XT(84, 85) \
    "v_smfmac_f32_16x16x64_bf16 v[232:235], v[240:243], a[0:7], %[ix]\n\t" "ds_write_b128 %[l1], v[240:243] offset:4096\n\t" \
    "v_smfmac_f32_16x16x64_bf16 v[236:239], v[240:243], a[8:15], %[ix]\n\t" "v_max3_u32 v225, v240, v241, v242\n\t" "s_waitcnt vmcnt(2)\n\t" \
    "v_smfmac_f32_16x16x64_bf16 v[232:235], v[244:247], a[16:23], %[ix]\n\t" "ds_write_b128 %[l1], v[244:247] offset:5120\n\t" "v_max3_u32 v225, v225, v243, v244\n\t" \
    "s_waitcnt lgkmcnt(8)\n\t" "v_smfmac_f32_16x16x64_bf16 v[236:239], v[244:247], v[160:167], %[ix]\n\t" "v_max3_u32 v225, v225, v245, v246\n\t" \
    "v_max_u32 v225, v225, v247\n\t" "v_cmp_eq_u32 vcc, -1, v225\n\t" "s_cmp_lg_u32 s60, 0\n\t" "s_cbranch_vccnz 21f\n\t" "22:\n\t" "s_waitcnt lgkmcnt(0)\n\t" \
    "s_cbranch_scc1 23f\n\t" "s_barrier\n\t" "23:\n\t" XT(86, 87) "ds_read_b128 v[16:19], %[l1] offset:8192\n\t" "ds_read_b128 v[20:23], %[l1] offset:12288\n\t" \
    "ds_read_b128 v[24:27], %[l1] offset:0\n\t" "s_waitcnt vmcnt(1)\n\t" "v_smfmac_f32_16x16x64_bf16 v[232:235], v[248:251], a[24:31], %[ix]\n\t" \
    "ds_write_b128 %[l1], v[248:251] offset:6144\n\t" "v_max3_u32 v225, v248, v249, v250\n\t" "v_smfmac_f32_16x16x64_bf16 v[236:239], v[248:251], a[32:39], %[ix]\n\t" \
    "ds_read_b128 v[28:31], %[l1] offset:9216\n\t" "ds_read_b128 v[240:243], %[l1] offset:13312\n\t" "ds_read_b128 v[244:247], %[l1] offset:1024\n\t" "s_waitcnt vmcnt(0)\n\t" \
    "v_smfmac_f32_16x16x64_bf16 v[232:235], v[252:255], a[40:47], %[ix]\n\t" "ds_write_b128 %[l1], v[252:255] offset:7168\n\t" "v_max3_u32 v225, v225, v251, v252\n\t" \
    "v_smfmac_f32_16x16x64_bf16 v[236:239], v[252:255], v[168:175], %[ix]\n\t" "v_max3_u32 v225, v225, v253, v254\n\t" "v_max_u32 v225, v225, v255\n\t" \
    "s_waitcnt lgkmcnt(7)\n\t" "v_smfmac_f32_16x16x64_bf16 v[232:235], v[16:19], a[48:55], %[ix]\n\t" "v_cmp_eq_u32 vcc, -1, v225\n\t" "s_cbranch_vccnz 24f\n\t" "25:\n\t" \
    "v_smfmac_f32_16x16x64_bf16 v[236:239], v[16:19], a[56:63], %[ix]\n\t" "ds_read_b128 v[160:163], %[l0] offset:8192\n\t" "ds_read_b128 v[164:167], %[l0] offset:9216\n\t" \
    "s_waitcnt lgkmcnt(8)\n\t" "v_smfmac_f32_16x16x64_bf16 v[232:235], v[20:23], a[64:71], %[ix]\n\t" "ds_read_b128 v[168:171], %[l0] offset:10240\n\t" \
    "ds_read_b128 v[172:175], %[l0] offset:11264\n\t" "v_smfmac_f32_16x16x64_bf16 v[236:239], v[20:23], v[176:183], %[ix]\n\t" "s_waitcnt lgkmcnt(4)\n\t" "s_barrier\n\t" \
    XT(88, 89) "v_smfmac_f32_16x16x64_bf16 v[232:235], v[24:27], a[72:79], %[ix]\n\t" "ds_read_b128 v[176:179], %[l0] offset:12288\n\t" \
    "ds_read_b128 v[180:183], %[l0] offset:13312\n\t" "v_smfmac_f32_16x16x64_bf16 v[236:239], v[24:27], a[80:87], %[ix]\n\t" "ds_read_b128 v[248:251], %[l1] offset:10240\n\t" \
    "ds_read_b128 v[252:255], %[l1] offset:14336\n\t" "v_smfmac_f32_16x16x64_bf16 v[232:235], v[28:31], a[88:95], %[ix]\n\t" "ds_read_b128 v[16:19], %[l1] offset:2048\n\t" \
    "ds_read_b128 v[20:23], %[l1] offset:11264\n\t" "v_smfmac_f32_16x16x64_bf16 v[236:239], v[28:31], v[184:191], %[ix]\n\t" "ds_read_b128 v[24:27], %[l1] offset:15360\n\t" \
    "ds_read_b128 v[184:187], %[l0] offset:14336\n\t" "v_smfmac_f32_16x16x64_bf16 v[232:235], v[240:243], a[96:103], %[ix]\n\t" \
    "ds_read_b128 v[188:191], %[l0] offset:15360\n\t" "ds_read_b128 v[28:31], %[l1] offset:3072\n\t" \
    "v_smfmac_f32_16x16x64_bf16 v[236:239], v[240:243], a[104:111], %[ix]\n\t" "v_smfmac_f32_16x16x64_bf16 v[232:235], v[244:247], a[112:119], %[ix]\n\t" \
    "s_waitcnt lgkmcnt(12)\n\t" "v_smfmac_f32_16x16x64_bf16 v[236:239], v[244:247], v[160:167], %[ix]\n\t" "ds_read_b128 v[160:163], %[l0] offset:16384\n\t" \
    "ds_read_b128 v[164:167], %[l0] offset:17408\n\t" "s_waitcnt lgkmcnt(9)\n\t" "v_smfmac_f32_16x16x64_bf16 v[232:235], v[248:251], a[120:127], %[ix]\n\t" \
    "v_smfmac_f32_16x16x64_bf16 v[236:239], v[248:251], a[128:135], %[ix]\n\t" "s_waitcnt lgkmcnt(8)\n\t" \
    "v_smfmac_f32_16x16x64_bf16 v[232:235], v[252:255], a[136:143], %[ix]\n\t" "v_smfmac_f32_16x16x64_bf16 v[236:239], v[252:255], v[168:175], %[ix]\n\t" \
    "ds_read_b128 v[168:171], %[l0] offset:18432\n\t" "ds_read_b128 v[172:175], %[l0] offset:19456\n\t" "s_waitcnt lgkmcnt(9)\n\t" \
    "v_smfmac_f32_16x16x64_bf16 v[232:235], v[16:19], a[144:151], %[ix]\n\t" "v_smfmac_f32_16x16x64_bf16 v[236:239], v[16:19], a[152:159], %[ix]\n\t" \
    "s_waitcnt lgkmcnt(8)\n\t" "v_smfmac_f32_16x16x64_bf16 v[232:235], v[20:23], a[160:167], %[ix]\n\t" \
    "v_smfmac_f32_16x16x64_bf16 v[236:239], v[20:23], v[176:183], %[ix]\n\t" "ds_read_b128 v[176:179], %[l0] offset:20480\n\t" \
    "ds_read_b128 v[180:183], %[l0] offset:21504\n\t" "s_waitcnt lgkmcnt(9)\n\t" "v_smfmac_f32_16x16x64_bf16 v[232:235], v[24:27], a[168:175], %[ix]\n\t" \
    "v_smfmac_f32_16x16x64_bf16 v[236:239], v[24:27], a[176:183], %[ix]\n\t" "s_waitcnt lgkmcnt(6)\n\t" \
    "v_smfmac_f32_16x16x64_bf16 v[232:235], v[28:31], a[184:191], %[ix]\n\t" "v_smfmac_f32_16x16x64_bf16 v[236:239], v[28:31], v[184:191], %[ix]\n\t" \
    "ds_read_b128 v[184:187], %[l0] offset:22528\n\t" "ds_read_b128 v[188:191], %[l0] offset:23552\n\t" XT(90, 91) "s_nop 4\n\t" "v_add_f32 v232, v232, v233\n\t" \
    "v_add_f32 v234, v234, v235\n\t" "v_add_f32 v236, v236, v237\n\t" "v_add_f32 v238, v238, v239\n\t" "v_cvt_pk_bf16_f32 v232, v232, v236\n\t" \
    "v_cvt_pk_bf16_f32 v233, v234, v238\n\t" "ds_write2_b32 %[vw], v232, v233 offset1:68\n\t" "s_waitcnt lgkmcnt(0)\n\t" "s_barrier\n\t" XT(92, 93) \
    "ds_read_b128 v[240:243], %[vr] offset:0\n\t" "ds_read_b128 v[244:247], %[vr] offset:64\n\t" "ds_read_b128 v[248:251], %[vr] offset:128\n\t" \
    "ds_read_b128 v[252:255], %[vr] offset:192\n\t" "s_mov_b32 s61, %[ub]\n\t" "s_waitcnt lgkmcnt(3)\n\t" \
    "v_smfmac_f32_16x16x64_bf16 v[192:195], v[240:243], v[32:39], %[ix]\n\t" "v_smfmac_f32_16x16x64_bf16 v[196:199], v[240:243], v[48:55], %[ix]\n\t" \
    "s_waitcnt lgkmcnt(2)\n\t" "v_smfmac_f32_16x16x64_bf16 v[192:195], v[244:247], v[40:47], %[ix]\n\t" \
    "v_smfmac_f32_16x16x64_bf16 v[196:199], v[244:247], v[56:63], %[ix]\n\t" "s_waitcnt lgkmcnt(1)\n\t" \
    "v_smfmac_f32_16x16x64_bf16 v[192:195], v[248:251], a[192:199], %[ix]\n\t" "v_smfmac_f32_16x16x64_bf16 v[196:199], v[248:251], a[200:207], %[ix]\n\t" \
    "s_waitcnt lgkmcnt(0)\n\t" "v_smfmac_f32_16x16x64_bf16 v[192:195], v[252:255], v[160:167], %[ix]\n\t" "ds_read_b128 v[160:163], %[l0] offset:24576\n\t" \
    "ds_read_b128 v[164:167], %[l0] offset:25600\n\t" "v_smfmac_f32_16x16x64_bf16 v[196:199], v[252:255], v[168:175], %[ix]\n\t" \
    "ds_read_b128 v[168:171], %[l0] offset:26624\n\t" "ds_read_b128 v[172:175], %[l0] offset:27648\n\t" \
    "v_smfmac_f32_16x16x64_bf16 v[200:203], v[240:243], v[64:71], %[ix]\n\t" "s_nop 1\n\t" "v_smfmac_f32_16x16x64_bf16 v[204:207], v[240:243], v[80:87], %[ix]\n\t" \
    "v_cvt_pk_bf16_f32 v224, v192, v194\n\t" "v_cvt_pk_bf16_f32 v225, v193, v195\n\t" "v_smfmac_f32_16x16x64_bf16 v[200:203], v[244:247], v[72:79], %[ix]\n\t" \
    "v_cvt_pk_bf16_f32 v226, v196, v198\n\t" "v_cvt_pk_bf16_f32 v227, v197, v199\n\t" "v_smfmac_f32_16x16x64_bf16 v[204:207], v[244:247], v[88:95], %[ix]\n\t" "s_nop 0\n\t" \
    "v_permlane32_swap_b32 v224, v226\n\t" "v_smfmac_f32_16x16x64_bf16 v[200:203], v[248:251], a[208:215], %[ix]\n\t" "v_permlane32_swap_b32 v225, v227\n\t" "s_nop 0\n\t" \
    "v_smfmac_f32_16x16x64_bf16 v[204:207], v[248:251], a[216:223], %[ix]\n\t" "v_permlane16_swap_b32 v224, v225\n\t" "v_permlane16_swap_b32 v226, v227\n\t" \
    "v_smfmac_f32_16x16x64_bf16 v[200:203], v[252:255], v[176:183], %[ix]\n\t" "buffer_store_dwordx4 v[224:227], %[uo], %[rs], s61 offen sc1\n\t" \
    "s_add_u32 s61, s61, 0x10000\n\t" XT(94, 95) "v_smfmac_f32_16x16x64_bf16 v[204:207], v[252:255], v[184:191], %[ix]\n\t" "ds_read_b128 v[176:179], %[l0] offset:28672\n\t" \
    "ds_read_b128 v[180:183], %[l0] offset:29696\n\t" "v_smfmac_f32_16x16x64_bf16 v[208:211], v[240:243], v[96:103], %[ix]\n\t" \
    "ds_read_b128 v[184:187], %[l0] offset:30720\n\t" "ds_read_b128 v[188:191], %[l0] offset:31744\n\t" \
    "v_smfmac_f32_16x16x64_bf16 v[212:215], v[240:243], v[112:119], %[ix]\n\t" "v_cvt_pk_bf16_f32 v228, v200, v202\n\t" "v_cvt_pk_bf16_f32 v229, v201, v203\n\t" \
    "v_smfmac_f32_16x16x64_bf16 v[208:211], v[244:247], v[104:111], %[ix]\n\t" "v_cvt_pk_bf16_f32 v230, v204, v206\n\t" "v_cvt_pk_bf16_f32 v231, v205, v207\n\t" \
    "v_smfmac_f32_16x16x64_bf16 v[212:215], v[244:247], v[120:127], %[ix]\n\t" "s_nop 0\n\t" "v_permlane32_swap_b32 v228, v230\n\t" \
    "v_smfmac_f32_16x16x64_bf16 v[208:211], v[248:251], a[224:231], %[ix]\n\t" "v_permlane32_swap_b32 v229, v231\n\t" "s_nop 0\n\t" \
    "v_smfmac_f32_16x16x64_bf16 v[212:215], v[248:251], a[232:239], %[ix]\n\t" "v_permlane16_swap_b32 v228, v229\n\t" "v_permlane16_swap_b32 v230, v231\n\t" \
    "s_waitcnt lgkmcnt(6)\n\t" "v_smfmac_f32_16x16x64_bf16 v[208:211], v[252:255], v[160:167], %[ix]\n\t" "buffer_store_dwordx4 v[228:231], %[uo], %[rs], s61 offen sc1\n\t" \
    "s_add_u32 s61, s61, 0x10000\n\t" "s_waitcnt lgkmcnt(4)\n\t" "v_smfmac_f32_16x16x64_bf16 v[212:215], v[252:255], v[168:175], %[ix]\n\t" "s_nop 1\n\t" \
    "v_smfmac_f32_16x16x64_bf16 v[216:219], v[240:243], v[128:135], %[ix]\n\t" "s_nop 1\n\t" "v_smfmac_f32_16x16x64_bf16 v[220:223], v[240:243], v[144:151], %[ix]\n\t" \
    "v_cvt_pk_bf16_f32 v224, v208, v210\n\t" "v_cvt_pk_bf16_f32 v225, v209, v211\n\t" "v_smfmac_f32_16x16x64_bf16 v[216:219], v[244:247], v[136:143], %[ix]\n\t" \
    "v_cvt_pk_bf16_f32 v226, v212, v214\n\t" "v_cvt_pk_bf16_f32 v227, v213, v215\n\t" "v_smfmac_f32_16x16x64_bf16 v[220:223], v[244:247], v[152:159], %[ix]\n\t" "s_nop 0\n\t" \
    "v_permlane32_swap_b32 v224, v226\n\t" "v_smfmac_f32_16x16x64_bf16 v[216:219], v[248:251], a[240:247], %[ix]\n\t" "v_permlane32_swap_b32 v225, v227\n\t" "s_nop 0\n\t" \
    "v_smfmac_f32_16x16x64_bf16 v[220:223], v[248:251], a[248:255], %[ix]\n\t" "v_permlane16_swap_b32 v224, v225\n\t" "v_permlane16_swap_b32 v226, v227\n\t" \
    "s_waitcnt lgkmcnt(2)\n\t" "v_smfmac_f32_16x16x64_bf16 v[216:219], v[252:255], v[176:183], %[ix]\n\t" "buffer_store_dwordx4 v[224:227], %[uo], %[rs], s61 offen sc1\n\t" \
    "s_add_u32 s61, s61, 0x10000\n\t" "s_waitcnt lgkmcnt(0)\n\t" "v_smfmac_f32_16x16x64_bf16 v[220:223], v[252:255], v[184:191], %[ix]\n\t" "s_nop 5\n\t" \
    "v_cvt_pk_bf16_f32 v228, v216, v218\n\t" "v_cvt_pk_bf16_f32 v229, v217, v219\n\t" "v_cvt_pk_bf16_f32 v230, v220, v222\n\t" "v_cvt_pk_bf16_f32 v231, v221, v223\n\t" \
    "s_nop 0\n\t" "v_permlane32_swap_b32 v228, v230\n\t" "v_permlane32_swap_b32 v229, v231\n\t" "s_nop 1\n\t" "v_permlane16_swap_b32 v228, v229\n\t" \
    "v_permlane16_swap_b32 v230, v231\n\t" "buffer_store_dwordx4 v[228:231], %[uo], %[rs], s61 offen sc1\n\t" "s_nop 1\n\t" XT(96, 97) "s_branch 99f\n\t" "21:\n\t" \
    "s_cmp_eq_u32 %[bd], 0\n\t" "s_cbranch_scc1 26f\n\t" "s_sub_u32 %[bd], %[bd], 1\n\t" XCNT(100) "s_waitcnt lgkmcnt(0)\n\t" "s_nop 7\n\t" "s_branch 20b\n\t" "26:\n\t" \
    "s_cmp_lg_u32 s60, 0\n\t" "s_branch 22b\n\t" "24:\n\t" "s_cmp_eq_u32 %[bd], 0\n\t" "s_cbranch_scc1 25b\n\t" "s_sub_u32 %[bd], %[bd], 1\n\t" XCNT(101) \
    "s_mov_b32 s60, 1\n\t" "s_waitcnt lgkmcnt(0)\n\t" "s_nop 7\n\t" "s_branch 20b\n\t" "92:\n\t" XT(80, 81) "s_barrier\n\t" XT(82, 83) "30:\n\t" \
    "buffer_load_dwordx4 v[240:243], %[mo], %[rs], %[sb] offen offset:0 sc1\n\t" "v_mov_b64 v[232:233], 0\n\t" "v_mov_b64 v[234:235], 0\n\t" "v_mov_b64 v[236:237], 0\n\t" \
    "v_mov_b64 v[238:239], 0\n\t" "v_mov_b64 v[192:193], 0\n\t" "v_mov_b64 v[194:195], 0\n\t" "v_mov_b64 v[196:197], 0\n\t" "v_mov_b64 v[198:199], 0\n\t" \
    "buffer_load_dwordx4 v[244:247], v224, %[rs], %[sb] offen offset:0 sc1\n\t" "ds_read_b128 v[160:163], %[l0] offset:0\n\t" "ds_read_b128 v[164:167], %[l0] offset:1024\n\t" \
    "ds_read_b128 v[168:171], %[l0] offset:2048\n\t" "ds_read_b128 v[172:175], %[l0] offset:3072\n\t" "s_nop 3\n\t" \
    "buffer_load_dwordx4 v[248:251], %[mo], %[rs], %[sb] offen offset:2048 sc1\n\t" "ds_read_b128 v[176:179], %[l0] offset:4096\n\t" \
    "ds_read_b128 v[180:183], %[l0] offset:5120\n\t" "ds_read_b128 v[184:187], %[l0] offset:6144\n\t" "ds_read_b128 v[188:191], %[l0] offset:7168\n\t" "s_nop 3\n\t" \
    "buffer_load_dwordx4 v[252:255], v224, %[rs], %[sb] offen offset:2048 sc1\n\t" "v_mov_b64 v[200:201], 0\n\t" "v_mov_b64 v[202:203], 0\n\t" "v_mov_b64 v[204:205], 0\n\t" \
    "v_mov_b64 v[206:207], 0\n\t" "v_mov_b64 v[208:209], 0\n\t" "v_mov_b64 v[210:211], 0\n\t" "v_mov_b64 v[212:213], 0\n\t" "v_mov_b64 v[214:215], 0\n\t" \
    "v_mov_b64 v[216:217], 0\n\t" "v_mov_b64 v[218:219], 0\n\t" "v_mov_b64 v[220:221], 0\n\t" "v_mov_b64 v[222:223], 0\n\t" "s_waitcnt vmcnt(3)\n\t" XT(84, 85) \
    "v_smfmac_f32_16x16x64_bf16 v[232:235], v[240:243], a[0:7], %[ix]\n\t" "ds_write_b128 %[l1], v[240:243] offset:8192\n\t" \
    "v_smfmac_f32_16x16x64_bf16 v[236:239], v[240:243], a[8:15], %[ix]\n\t" "v_max3_u32 v225, v240, v241, v242\n\t" "s_waitcnt vmcnt(2)\n\t" \
    "v_smfmac_f32_16x16x64_bf16 v[232:235], v[244:247], a[16:23], %[ix]\n\t" "ds_write_b128 %[l1], v[244:247] offset:9216\n\t" "v_max3_u32 v225, v225, v243, v244\n\t" \
    "s_waitcnt lgkmcnt(8)\n\t" "v_smfmac_f32_16x16x64_bf16 v[236:239], v[244:247], v[160:167], %[ix]\n\t" "v_max3_u32 v225, v225, v245, v246\n\t" \
    "v_max_u32 v225, v225, v247\n\t" "v_cmp_eq_u32 vcc, -1, v225\n\t" "s_cmp_lg_u32 s60, 0\n\t" "s_cbranch_vccnz 31f\n\t" "32:\n\t" "s_waitcnt lgkmcnt(0)\n\t" \
    "s_cbranch_scc1 33f\n\t" "s_barrier\n\t" "33:\n\t" XT(86, 87) "ds_read_b128 v[16:19], %[l1] offset:12288\n\t" "ds_read_b128 v[20:23], %[l1] offset:0\n\t" \
    "ds_read_b128 v[24:27], %[l1] offset:4096\n\t" "s_waitcnt vmcnt(1)\n\t" "v_smfmac_f32_16x16x64_bf16 v[232:235], v[248:251], a[24:31], %[ix]\n\t" \
    "ds_write_b128 %[l1], v[248:251] offset:10240\n\t" "v_max3_u32 v225, v248, v249, v250\n\t" "v_smfmac_f32_16x16x64_bf16 v[236:239], v[248:251], a[32:39], %[ix]\n\t" \
    "ds_read_b128 v[28:31], %[l1] offset:13312\n\t" "ds_read_b128 v[240:243], %[l1] offset:1024\n\t" "ds_read_b128 v[244:247], %[l1] offset:5120\n\t" "s_waitcnt vmcnt(0)\n\t" \
    "v_smfmac_f32_16x16x64_bf16 v[232:235], v[252:255], a[40:47], %[ix]\n\t" "ds_write_b128 %[l1], v[252:255] offset:11264\n\t" "v_max3_u32 v225, v225, v251, v252\n\t" \
    "v_smfmac_f32_16x16x64_bf16 v[236:239], v[252:255], v[168:175], %[ix]\n\t" "v_max3_u32 v225, v225, v253, v254\n\t" "v_max_u32 v225, v225, v255\n\t" \
    "s_waitcnt lgkmcnt(7)\n\t" "v_smfmac_f32_16x16x64_bf16 v[232:235], v[16:19], a[48:55], %[ix]\n\t" "v_cmp_eq_u32 vcc, -1, v225\n\t" "s_cbranch_vccnz 34f\n\t" "35:\n\t" \
    "v_smfmac_f32_16x16x64_bf16 v[236:239], v[16:19], a[56:63], %[ix]\n\t" "ds_read_b128 v[160:163], %[l0] offset:8192\n\t" "ds_read_b128 v[164:167], %[l0] offset:9216\n\t" \
    "s_waitcnt lgkmcnt(8)\n\t" "v_smfmac_f32_16x16x64_bf16 v[232:235], v[20:23], a[64:71], %[ix]\n\t" "ds_read_b128 v[168:171], %[l0] offset:10240\n\t" \
    "ds_read_b128 v[172:175], %[l0] offset:11264\n\t" "v_smfmac_f32_16x16x64_bf16 v[236:239], v[20:23], v[176:183], %[ix]\n\t" "s_waitcnt lgkmcnt(4)\n\t" "s_barrier\n\t" \
    XT(88, 89) "v_smfmac_f32_16x16x64_bf16 v[232:235], v[24:27], a[72:79], %[ix]\n\t" "ds_read_b128 v[176:179], %[l0] offset:12288\n\t" \
    "ds_read_b128 v[180:183], %[l0] offset:13312\n\t" "v_smfmac_f32_16x16x64_bf16 v[236:239], v[24:27], a[80:87], %[ix]\n\t" "ds_read_b128 v[248:251], %[l1] offset:14336\n\t" \
    "ds_read_b128 v[252:255], %[l1] offset:2048\n\t" "v_smfmac_f32_16x16x64_bf16 v[232:235], v[28:31], a[88:95], %[ix]\n\t" "ds_read_b128 v[16:19], %[l1] offset:6144\n\t" \
    "ds_read_b128 v[20:23], %[l1] offset:15360\n\t" "v_smfmac_f32_16x16x64_bf16 v[236:239], v[28:31], v[184:191], %[ix]\n\t" "ds_read_b128 v[24:27], %[l1] offset:3072\n\t" \
    "ds_read_b128 v[184:187], %[l0] offset:14336\n\t" "v_smfmac_f32_16x16x64_bf16 v[232:235], v[240:243], a[96:103], %[ix]\n\t" \
    "ds_read_b128 v[188:191], %[l0] offset:15360\n\t" "ds_read_b128 v[28:31], %[l1] offset:7168\n\t" \
    "v_smfmac_f32_16x16x64_bf16 v[236:239], v[240:243], a[104:111], %[ix]\n\t" "v_smfmac_f32_16x16x64_bf16 v[232:235], v[244:247], a[112:119], %[ix]\n\t" \
    "s_waitcnt lgkmcnt(12)\n\t" "v_smfmac_f32_16x16x64_bf16 v[236:239], v[244:247], v[160:167], %[ix]\n\t" "ds_read_b128 v[160:163], %[l0] offset:16384\n\t" \
    "ds_read_b128 v[164:167], %[l0] offset:17408\n\t" "s_waitcnt lgkmcnt(9)\n\t" "v_smfmac_f32_16x16x64_bf16 v[232:235], v[248:251], a[120:127], %[ix]\n\t" \
    "v_smfmac_f32_16x16x64_bf16 v[236:239], v[248:251], a[128:135], %[ix]\n\t" "s_waitcnt lgkmcnt(8)\n\t" \
    "v_smfmac_f32_16x16x64_bf16 v[232:235], v[252:255], a[136:143], %[ix]\n\t" "v_smfmac_f32_16x16x64_bf16 v[236:239], v[252:255], v[168:175], %[ix]\n\t" \
    "ds_read_b128 v[168:171], %[l0] offset:18432\n\t" "ds_read_b128 v[172:175], %[l0] offset:19456\n\t" "s_waitcnt lgkmcnt(9)\n\t" \
    "v_smfmac_f32_16x16x64_bf16 v[232:235], v[16:19], a[144:151], %[ix]\n\t" "v_smfmac_f32_16x16x64_bf16 v[236:239], v[16:19], a[152:159], %[ix]\n\t" \
    "s_waitcnt lgkmcnt(8)\n\t" "v_smfmac_f32_16x16x64_bf16 v[232:235], v[20:23], a[160:167], %[ix]\n\t" \
    "v_smfmac_f32_16x16x64_bf16 v[236:239], v[20:23], v[176:183], %[ix]\n\t" "ds_read_b128 v[176:179], %[l0] offset:20480\n\t" \
    "ds_read_b128 v[180:183], %[l0] offset:21504\n\t" "s_waitcnt lgkmcnt(9)\n\t" "v_smfmac_f32_16x16x64_bf16 v[232:235], v[24:27], a[168:175], %[ix]\n\t" \
    "v_smfmac_f32_16x16x64_bf16 v[236:239], v[24:27], a[176:183], %[ix]\n\t" "s_waitcnt lgkmcnt(6)\n\t" \
    "v_smfmac_f32_16x16x64_bf16 v[232:235], v[28:31], a[184:191], %[ix]\n\t" "v_smfmac_f32_16x16x64_bf16 v[236:239], v[28:31], v[184:191], %[ix]\n\t" \
    "ds_read_b128 v[184:187], %[l0] offset:22528\n\t" "ds_read_b128 v[188:191], %[l0] offset:23552\n\t" XT(90, 91) "s_nop 4\n\t" "v_add_f32 v232, v232, v233\n\t" \
    "v_add_f32 v234, v234, v235\n\t" "v_add_f32 v236, v236, v237\n\t" "v_add_f32 v238, v238, v239\n\t" "v_cvt_pk_bf16_f32 v232, v232, v236\n\t" \
    "v_cvt_pk_bf16_f32 v233, v234, v238\n\t" "ds_write2_b32 %[vw], v232, v233 offset1:68\n\t" "s_waitcnt lgkmcnt(0)\n\t" "s_barrier\n\t" XT(92, 93) \
    "ds_read_b128 v[240:243], %[vr] offset:0\n\t" "ds_read_b128 v[244:247], %[vr] offset:64\n\t" "ds_read_b128 v[248:251], %[vr] offset:128\n\t" \
    "ds_read_b128 v[252:255], %[vr] offset:192\n\t" "s_mov_b32 s61, %[ub]\n\t" "s_waitcnt lgkmcnt(3)\n\t" \
    "v_smfmac_f32_16x16x64_bf16 v[192:195], v[240:243], v[32:39], %[ix]\n\t" "v_smfmac_f32_16x16x64_bf16 v[196:199], v[240:243], v[48:55], %[ix]\n\t" \
    "s_waitcnt lgkmcnt(2)\n\t" "v_smfmac_f32_16x16x64_bf16 v[192:195], v[244:247], v[40:47], %[ix]\n\t" \
    "v_smfmac_f32_16x16x64_bf16 v[196:199], v[244:247], v[56:63], %[ix]\n\t" "s_waitcnt lgkmcnt(1)\n\t" \
    "v_smfmac_f32_16x16x64_bf16 v[192:195], v[248:251], a[192:199], %[ix]\n\t" "v_smfmac_f32_16x16x64_bf16 v[196:199], v[248:251], a[200:207], %[ix]\n\t" \
    "s_waitcnt lgkmcnt(0)\n\t" "v_smfmac_f32_16x16x64_bf16 v[192:195], v[252:255], v[160:167], %[ix]\n\t" "ds_read_b128 v[160:163], %[l0] offset:24576\n\t" \
    "ds_read_b128 v[164:167], %[l0] offset:25600\n\t" "v_smfmac_f32_16x16x64_bf16 v[196:199], v[252:255], v[168:175], %[ix]\n\t" \
    "ds_read_b128 v[168:171], %[l0] offset:26624\n\t" "ds_read_b128 v[172:175], %[l0] offset:27648\n\t" \
    "v_smfmac_f32_16x16x64_bf16 v[200:203], v[240:243], v[64:71], %[ix]\n\t" "s_nop 1\n\t" "v_smfmac_f32_16x16x64_bf16 v[204:207], v[240:243], v[80:87], %[ix]\n\t" \
    "v_cvt_pk_bf16_f32 v224, v192, v194\n\t" "v_cvt_pk_bf16_f32 v225, v193, v195\n\t" "v_smfmac_f32_16x16x64_bf16 v[200:203], v[244:247], v[72:79], %[ix]\n\t" \
    "v_cvt_pk_bf16_f32 v226, v196, v198\n\t" "v_cvt_pk_bf16_f32 v227, v197, v199\n\t" "v_smfmac_f32_16x16x64_bf16 v[204:207], v[244:247], v[88:95], %[ix]\n\t" "s_nop 0\n\t" \
    "v_permlane32_swap_b32 v224, v226\n\t" "v_smfmac_f32_16x16x64_bf16 v[200:203], v[248:251], a[208:215], %[ix]\n\t" "v_permlane32_swap_b32 v225, v227\n\t" "s_nop 0\n\t" \
    "v_smfmac_f32_16x16x64_bf16 v[204:207], v[248:251], a[216:223], %[ix]\n\t" "v_permlane16_swap_b32 v224, v225\n\t" "v_permlane16_swap_b32 v226, v227\n\t" \
    "v_smfmac_f32_16x16x64_bf16 v[200:203], v[252:255], v[176:183], %[ix]\n\t" "buffer_store_dwordx4 v[224:227], %[uo], %[rs], s61 offen sc1\n\t" \
    "s_add_u32 s61, s61, 0x10000\n\t" XT(94, 95) "v_smfmac_f32_16x16x64_bf16 v[204:207], v[252:255], v[184:191], %[ix]\n\t" "ds_read_b128 v[176:179], %[l0] offset:28672\n\t" \
    "ds_read_b128 v[180:183], %[l0] offset:29696\n\t" "v_smfmac_f32_16x16x64_bf16 v[208:211], v[240:243], v[96:103], %[ix]\n\t" \
    "ds_read_b128 v[184:187], %[l0] offset:30720\n\t" "ds_read_b128 v[188:191], %[l0] offset:31744\n\t" \
    "v_smfmac_f32_16x16x64_bf16 v[212:215], v[240:243], v[112:119], %[ix]\n\t" "v_cvt_pk_bf16_f32 v228, v200, v202\n\t" "v_cvt_pk_bf16_f32 v229, v201, v203\n\t" \
    "v_smfmac_f32_16x16x64_bf16 v[208:211], v[244:247], v[104:111], %[ix]\n\t" "v_cvt_pk_bf16_f32 v230, v204, v206\n\t" "v_cvt_pk_bf16_f32 v231, v205, v207\n\t" \
    "v_smfmac_f32_16x16x64_bf16 v[212:215], v[244:247], v[120:127], %[ix]\n\t" "s_nop 0\n\t" "v_permlane32_swap_b32 v228, v230\n\t" \
    "v_smfmac_f32_16x16x64_bf16 v[208:211], v[248:251], a[224:231], %[ix]\n\t" "v_permlane32_swap_b32 v229, v231\n\t" "s_nop 0\n\t" \
    "v_smfmac_f32_16x16x64_bf16 v[212:215], v[248:251], a[232:239], %[ix]\n\t" "v_permlane16_swap_b32 v228, v229\n\t" "v_permlane16_swap_b32 v230, v231\n\t" \
    "s_waitcnt lgkmcnt(6)\n\t" "v_smfmac_f32_16x16x64_bf16 v[208:211], v[252:255], v[160:167], %[ix]\n\t" "buffer_store_dwordx4 v[228:231], %[uo], %[rs], s61 offen sc1\n\t" \
    "s_add_u32 s61, s61, 0x10000\n\t" "s_waitcnt lgkmcnt(4)\n\t" "v_smfmac_f32_16x16x64_bf16 v[212:215], v[252:255], v[168:175], %[ix]\n\t" "s_nop 1\n\t" \
    "v_smfmac_f32_16x16x64_bf16 v[216:219], v[240:243], v[128:135], %[ix]\n\t" "s_nop 1\n\t" "v_smfmac_f32_16x16x64_bf16 v[220:223], v[240:243], v[144:151], %[ix]\n\t" \
    "v_cvt_pk_bf16_f32 v224, v208, v210\n\t" "v_cvt_pk_bf16_f32 v225, v209, v211\n\t" "v_smfmac_f32_16x16x64_bf16 v[216:219], v[244:247], v[136:143], %[ix]\n\t" \
    "v_cvt_pk_bf16_f32 v226, v212, v214\n\t" "v_cvt_pk_bf16_f32 v227, v213, v215\n\t" "v_smfmac_f32_16x16x64_bf16 v[220:223], v[244:247], v[152:159], %[ix]\n\t" "s_nop 0\n\t" \
    "v_permlane32_swap_b32 v224, v226\n\t" "v_smfmac_f32_16x16x64_bf16 v[216:219], v[248:251], a[240:247], %[ix]\n\t" "v_permlane32_swap_b32 v225, v227\n\t" "s_nop 0\n\t" \
    "v_smfmac_f32_16x16x64_bf16 v[220:223], v[248:251], a[248:255], %[ix]\n\t" "v_permlane16_swap_b32 v224, v225\n\t" "v_permlane16_swap_b32 v226, v227\n\t" \
    "s_waitcnt lgkmcnt(2)\n\t" "v_smfmac_f32_16x16x64_bf16 v[216:219], v[252:255], v[176:183], %[ix]\n\t" "buffer_store_dwordx4 v[224:227], %[uo], %[rs], s61 offen sc1\n\t" \
    "s_add_u32 s61, s61, 0x10000\n\t" "s_waitcnt lgkmcnt(0)\n\t" "v_smfmac_f32_16x16x64_bf16 v[220:223], v[252:255], v[184:191], %[ix]\n\t" "s_nop 5\n\t" \
    "v_cvt_pk_bf16_f32 v228, v216, v218\n\t" "v_cvt_pk_bf16_f32 v229, v217, v219\n\t" "v_cvt_pk_bf16_f32 v230, v220, v222\n\t" "v_cvt_pk_bf16_f32 v231, v221, v223\n\t" \
    "s_nop 0\n\t" "v_permlane32_swap_b32 v228, v230\n\t" "v_permlane32_swap_b32 v229, v231\n\t" "s_nop 1\n\t" "v_permlane16_swap_b32 v228, v229\n\t" \
    "v_permlane16_swap_b32 v230, v231\n\t" "buffer_store_dwordx4 v[228:231], %[uo], %[rs], s61 offen sc1\n\t" "s_nop 1\n\t" XT(96, 97) "s_branch 99f\n\t" "31:\n\t" \
    "s_cmp_eq_u32 %[bd], 0\n\t" "s_cbranch_scc1 36f\n\t" "s_sub_u32 %[bd], %[bd], 1\n\t" XCNT(100) "s_waitcnt lgkmcnt(0)\n\t" "s_nop 7\n\t" "s_branch 30b\n\t" "36:\n\t" \
    "s_cmp_lg_u32 s60, 0\n\t" "s_branch 32b\n\t" "34:\n\t" "s_cmp_eq_u32 %[bd], 0\n\t" "s_cbranch_scc1 35b\n\t" "s_sub_u32 %[bd], %[bd], 1\n\t" XCNT(101) \
    "s_mov_b32 s60, 1\n\t" "s_waitcnt lgkmcnt(0)\n\t" "s_nop 7\n\t" "s_branch 30b\n\t" "93:\n\t" XT(80, 81) "s_barrier\n\t" XT(82, 83) "40:\n\t" \
    "buffer_load_dwordx4 v[240:243], %[mo], %[rs], %[sb] offen offset:0 sc1\n\t" "v_mov_b64 v[232:233], 0\n\t" "v_mov_b64 v[234:235], 0\n\t" "v_mov_b64 v[236:237], 0\n\t" \
    "v_mov_b64 v[238:239], 0\n\t" "v_mov_b64 v[192:193], 0\n\t" "v_mov_b64 v[194:195], 0\n\t" "v_mov_b64 v[196:197], 0\n\t" "v_mov_b64 v[198:199], 0\n\t" \
    "buffer_load_dwordx4 v[244:247], v224, %[rs], %[sb] offen offset:0 sc1\n\t" "ds_read_b128 v[160:163], %[l0] offset:0\n\t" "ds_read_b128 v[164:167], %[l0] offset:1024\n\t" \
    "ds_read_b128 v[168:171], %[l0] offset:2048\n\t" "ds_read_b128 v[172:175], %[l0] offset:3072\n\t" "s_nop 3\n\t" \
    "buffer_load_dwordx4 v[248:251], %[mo], %[rs], %[sb] offen offset:2048 sc1\n\t" "ds_read_b128 v[176:179], %[l0] offset:4096\n\t" \
    "ds_read_b128 v[180:183], %[l0] offset:5120\n\t" "ds_read_b128 v[184:187], %[l0] offset:6144\n\t" "ds_read_b128 v[188:191], %[l0] offset:7168\n\t" "s_nop 3\n\t" \
    "buffer_load_dwordx4 v[252:255], v224, %[rs], %[sb] offen offset:2048 sc1\n\t" "v_mov_b64 v[200:201], 0\n\t" "v_mov_b64 v[202:203], 0\n\t" "v_mov_b64 v[204:205], 0\n\t" \
    "v_mov_b64 v[206:207], 0\n\t" "v_mov_b64 v[208:209], 0\n\t" "v_mov_b64 v[210:211], 0\n\t" "v_mov_b64 v[212:213], 0\n\t" "v_mov_b64 v[214:215], 0\n\t" \
    "v_mov_b64 v[216:217], 0\n\t" "v_mov_b64 v[218:219], 0\n\t" "v_mov_b64 v[220:221], 0\n\t" "v_mov_b64 v[222:223], 0\n\t" "s_waitcnt vmcnt(3)\n\t" XT(84, 85) \
    "v_smfmac_f32_16x16x64_bf16 v[232:235], v[240:243], a[0:7], %[ix]\n\t" "ds_write_b128 %[l1], v[240:243] offset:12288\n\t" \
    "v_smfmac_f32_16x16x64_bf16 v[236:239], v[240:243], a[8:15], %[ix]\n\t" "v_max3_u32 v225, v240, v241, v242\n\t" "s_waitcnt vmcnt(2)\n\t" \
    "v_smfmac_f32_16x16x64_bf16 v[232:235], v[244:247], a[16:23], %[ix]\n\t" "ds_write_b128 %[l1], v[244:247] offset:13312\n\t" "v_max3_u32 v225, v225, v243, v244\n\t" \
    "s_waitcnt lgkmcnt(8)\n\t" "v_smfmac_f32_16x16x64_bf16 v[236:239], v[244:247], v[160:167], %[ix]\n\t" "v_max3_u32 v225, v225, v245, v246\n\t" \
    "v_max_u32 v225, v225, v247\n\t" "v_cmp_eq_u32 vcc, -1, v225\n\t" "s_cmp_lg_u32 s60, 0\n\t" "s_cbranch_vccnz 41f\n\t" "42:\n\t" "s_waitcnt lgkmcnt(0)\n\t" \
    "s_cbranch_scc1 43f\n\t" "s_barrier\n\t" "43:\n\t" XT(86, 87) "ds_read_b128 v[16:19], %[l1] offset:0\n\t" "ds_read_b128 v[20:23], %[l1] offset:4096\n\t" \
    "ds_read_b128 v[24:27], %[l1] offset:8192\n\t" "s_waitcnt vmcnt(1)\n\t" "v_smfmac_f32_16x16x64_bf16 v[232:235], v[248:251], a[24:31], %[ix]\n\t" \
    "ds_write_b128 %[l1], v[248:251] offset:14336\n\t" "v_max3_u32 v225, v248, v249, v250\n\t" "v_smfmac_f32_16x16x64_bf16 v[236:239], v[248:251], a[32:39], %[ix]\n\t" \
    "ds_read_b128 v[28:31], %[l1] offset:1024\n\t" "ds_read_b128 v[240:243], %[l1] offset:5120\n\t" "ds_read_b128 v[244:247], %[l1] offset:9216\n\t" "s_waitcnt vmcnt(0)\n\t" \
    "v_smfmac_f32_16x16x64_bf16 v[232:235], v[252:255], a[40:47], %[ix]\n\t" "ds_write_b128 %[l1], v[252:255] offset:15360\n\t" "v_max3_u32 v225, v225, v251, v252\n\t" \
    "v_smfmac_f32_16x16x64_bf16 v[236:239], v[252:255], v[168:175], %[ix]\n\t" "v_max3_u32 v225, v225, v253, v254\n\t" "v_max_u32 v225, v225, v255\n\t" \
    "s_waitcnt lgkmcnt(7)\n\t" "v_smfmac_f32_16x16x64_bf16 v[232:235], v[16:19], a[48:55], %[ix]\n\t" "v_cmp_eq_u32 vcc, -1, v225\n\t" "s_cbranch_vccnz 44f\n\t" "45:\n\t" \
    "v_smfmac_f32_16x16x64_bf16 v[236:239], v[16:19], a[56:63], %[ix]\n\t" "ds_read_b128 v[160:163], %[l0] offset:8192\n\t" "ds_read_b128 v[164:167], %[l0] offset:9216\n\t" \
    "s_waitcnt lgkmcnt(8)\n\t" "v_smfmac_f32_16x16x64_bf16 v[232:235], v[20:23], a[64:71], %[ix]\n\t" "ds_read_b128 v[168:171], %[l0] offset:10240\n\t" \
    "ds_read_b128 v[172:175], %[l0] offset:11264\n\t" "v_smfmac_f32_16x16x64_bf16 v[236:239], v[20:23], v[176:183], %[ix]\n\t" "s_waitcnt lgkmcnt(4)\n\t" "s_barrier\n\t" \
    XT(88, 89) "v_smfmac_f32_16x16x64_bf16 v[232:235], v[24:27], a[72:79], %[ix]\n\t" "ds_read_b128 v[176:179], %[l0] offset:12288\n\t" \
    "ds_read_b128 v[180:183], %[l0] offset:13312\n\t" "v_smfmac_f32_16x16x64_bf16 v[236:239], v[24:27], a[80:87], %[ix]\n\t" "ds_read_b128 v[248:251], %[l1] offset:2048\n\t" \
    "ds_read_b128 v[252:255], %[l1] offset:6144\n\t" "v_smfmac_f32_16x16x64_bf16 v[232:235], v[28:31], a[88:95], %[ix]\n\t" "ds_read_b128 v[16:19], %[l1] offset:10240\n\t" \
    "ds_read_b128 v[20:23], %[l1] offset:3072\n\t" "v_smfmac_f32_16x16x64_bf16 v[236:239], v[28:31], v[184:191], %[ix]\n\t" "ds_read_b128 v[24:27], %[l1] offset:7168\n\t" \
    "ds_read_b128 v[184:187], %[l0] offset:14336\n\t" "v_smfmac_f32_16x16x64_bf16 v[232:235], v[240:243], a[96:103], %[ix]\n\t" \
    "ds_read_b128 v[188:191], %[l0] offset:15360\n\t" "ds_read_b128 v[28:31], %[l1] offset:11264\n\t" \
    "v_smfmac_f32_16x16x64_bf16 v[236:239], v[240:243], a[104:111], %[ix]\n\t" "v_smfmac_f32_16x16x64_bf16 v[232:235], v[244:247], a[112:119], %[ix]\n\t" \
    "s_waitcnt lgkmcnt(12)\n\t" "v_smfmac_f32_16x16x64_bf16 v[236:239], v[244:247], v[160:167], %[ix]\n\t" "ds_read_b128 v[160:163], %[l0] offset:16384\n\t" \
    "ds_read_b128 v[164:167], %[l0] offset:17408\n\t" "s_waitcnt lgkmcnt(9)\n\t" "v_smfmac_f32_16x16x64_bf16 v[232:235], v[248:251], a[120:127], %[ix]\n\t" \
    "v_smfmac_f32_16x16x64_bf16 v[236:239], v[248:251], a[128:135], %[ix]\n\t" "s_waitcnt lgkmcnt(8)\n\t" \
    "v_smfmac_f32_16x16x64_bf16 v[232:235], v[252:255], a[136:143], %[ix]\n\t" "v_smfmac_f32_16x16x64_bf16 v[236:239], v[252:255], v[168:175], %[ix]\n\t" \
    "ds_read_b128 v[168:171], %[l0] offset:18432\n\t" "ds_read_b128 v[172:175], %[l0] offset:19456\n\t" "s_waitcnt lgkmcnt(9)\n\t" \
    "v_smfmac_f32_16x16x64_bf16 v[232:235], v[16:19], a[144:151], %[ix]\n\t" "v_smfmac_f32_16x16x64_bf16 v[236:239], v[16:19], a[152:159], %[ix]\n\t" \
    "s_waitcnt lgkmcnt(8)\n\t" "v_smfmac_f32_16x16x64_bf16 v[232:235], v[20:23], a[160:167], %[ix]\n\t" \
    "v_smfmac_f32_16x16x64_bf16 v[236:239], v[20:23], v[176:183], %[ix]\n\t" "ds_read_b128 v[176:179], %[l0] offset:20480\n\t" \
    "ds_read_b128 v[180:183], %[l0] offset:21504\n\t" "s_waitcnt lgkmcnt(9)\n\t" "v_smfmac_f32_16x16x64_bf16 v[232:235], v[24:27], a[168:175], %[ix]\n\t" \
    "v_smfmac_f32_16x16x64_bf16 v[236:239], v[24:27], a[176:183], %[ix]\n\t" "s_waitcnt lgkmcnt(6)\n\t" \
    "v_smfmac_f32_16x16x64_bf16 v[232:235], v[28:31], a[184:191], %[ix]\n\t" "v_smfmac_f32_16x16x64_bf16 v[236:239], v[28:31], v[184:191], %[ix]\n\t" \
    "ds_read_b128 v[184:187], %[l0] offset:22528\n\t" "ds_read_b128 v[188:191], %[l0] offset:23552\n\t" XT(90, 91) "s_nop 4\n\t" "v_add_f32 v232, v232, v233\n\t" \
    "v_add_f32 v234, v234, v235\n\t" "v_add_f32 v236, v236, v237\n\t" "v_add_f32 v238, v238, v239\n\t" "v_cvt_pk_bf16_f32 v232, v232, v236\n\t" \
    "v_cvt_pk_bf16_f32 v233, v234, v238\n\t" "ds_write2_b32 %[vw], v232, v233 offset1:68\n\t" "s_waitcnt lgkmcnt(0)\n\t" "s_barrier\n\t" XT(92, 93) \
    "ds_read_b128 v[240:243], %[vr] offset:0\n\t" "ds_read_b128 v[244:247], %[vr] offset:64\n\t" "ds_read_b128 v[248:251], %[vr] offset:128\n\t" \
    "ds_read_b128 v[252:255], %[vr] offset:192\n\t" "s_mov_b32 s61, %[ub]\n\t" "s_waitcnt lgkmcnt(3)\n\t" \
    "v_smfmac_f32_16x16x64_bf16 v[192:195], v[240:243], v[32:39], %[ix]\n\t" "v_smfmac_f32_16x16x64_bf16 v[196:199], v[240:243], v[48:55], %[ix]\n\t" \
    "s_waitcnt lgkmcnt(2)\n\t" "v_smfmac_f32_16x16x64_bf16 v[192:195], v[244:247], v[40:47], %[ix]\n\t" \
    "v_smfmac_f32_16x16x64_bf16 v[196:199], v[244:247], v[56:63], %[ix]\n\t" "s_waitcnt lgkmcnt(1)\n\t" \
    "v_smfmac_f32_16x16x64_bf16 v[192:195], v[248:251], a[192:199], %[ix]\n\t" "v_smfmac_f32_16x16x64_bf16 v[196:199], v[248:251], a[200:207], %[ix]\n\t" \
    "s_waitcnt lgkmcnt(0)\n\t" "v_smfmac_f32_16x16x64_bf16 v[192:195], v[252:255], v[160:167], %[ix]\n\t" "ds_read_b128 v[160:163], %[l0] offset:24576\n\t" \
    "ds_read_b128 v[164:167], %[l0] offset:25600\n\t" "v_smfmac_f32_16x16x64_bf16 v[196:199], v[252:255], v[168:175], %[ix]\n\t" \
    "ds_read_b128 v[168:171], %[l0] offset:26624\n\t" "ds_read_b128 v[172:175], %[l0] offset:27648\n\t" \
    "v_smfmac_f32_16x16x64_bf16 v[200:203], v[240:243], v[64:71], %[ix]\n\t" "s_nop 1\n\t" "v_smfmac_f32_16x16x64_bf16 v[204:207], v[240:243], v[80:87], %[ix]\n\t" \
    "v_cvt_pk_bf16_f32 v224, v192, v194\n\t" "v_cvt_pk_bf16_f32 v225, v193, v195\n\t" "v_smfmac_f32_16x16x64_bf16 v[200:203], v[244:247], v[72:79], %[ix]\n\t" \
    "v_cvt_pk_bf16_f32 v226, v196, v198\n\t" "v_cvt_pk_bf16_f32 v227, v197, v199\n\t" "v_smfmac_f32_16x16x64_bf16 v[204:207], v[244:247], v[88:95], %[ix]\n\t" "s_nop 0\n\t" \
    "v_permlane32_swap_b32 v224, v226\n\t" "v_smfmac_f32_16x16x64_bf16 v[200:203], v[248:251], a[208:215], %[ix]\n\t" "v_permlane32_swap_b32 v225, v227\n\t" "s_nop 0\n\t" \
    "v_smfmac_f32_16x16x64_bf16 v[204:207], v[248:251], a[216:223], %[ix]\n\t" "v_permlane16_swap_b32 v224, v225\n\t" "v_permlane16_swap_b32 v226, v227\n\t" \
    "v_smfmac_f32_16x16x64_bf16 v[200:203], v[252:255], v[176:183], %[ix]\n\t" "buffer_store_dwordx4 v[224:227], %[uo], %[rs], s61 offen sc1\n\t" \
    "s_add_u32 s61, s61, 0x10000\n\t" XT(94, 95) "v_smfmac_f32_16x16x64_bf16 v[204:207], v[252:255], v[184:191], %[ix]\n\t" "ds_read_b128 v[176:179], %[l0] offset:28672\n\t" \
    "ds_read_b128 v[180:183], %[l0] offset:29696\n\t" "v_smfmac_f32_16x16x64_bf16 v[208:211], v[240:243], v[96:103], %[ix]\n\t" \
    "ds_read_b128 v[184:187], %[l0] offset:30720\n\t" "ds_read_b128 v[188:191], %[l0] offset:31744\n\t" \
    "v_smfmac_f32_16x16x64_bf16 v[212:215], v[240:243], v[112:119], %[ix]\n\t" "v_cvt_pk_bf16_f32 v228, v200, v202\n\t" "v_cvt_pk_bf16_f32 v229, v201, v203\n\t" \
    "v_smfmac_f32_16x16x64_bf16 v[208:211], v[244:247], v[104:111], %[ix]\n\t" "v_cvt_pk_bf16_f32 v230, v204, v206\n\t" "v_cvt_pk_bf16_f32 v231, v205, v207\n\t" \
    "v_smfmac_f32_16x16x64_bf16 v[212:215], v[244:247], v[120:127], %[ix]\n\t" "s_nop 0\n\t" "v_permlane32_swap_b32 v228, v230\n\t" \
    "v_smfmac_f32_16x16x64_bf16 v[208:211], v[248:251], a[224:231], %[ix]\n\t" "v_permlane32_swap_b32 v229, v231\n\t" "s_nop 0\n\t" \
    "v_smfmac_f32_16x16x64_bf16 v[212:215], v[248:251], a[232:239], %[ix]\n\t" "v_permlane16_swap_b32 v228, v229\n\t" "v_permlane16_swap_b32 v230, v231\n\t" \
    "s_waitcnt lgkmcnt(6)\n\t" "v_smfmac_f32_16x16x64_bf16 v[208:211], v[252:255], v[160:167], %[ix]\n\t" "buffer_store_dwordx4 v[228:231], %[uo], %[rs], s61 offen sc1\n\t" \
    "s_add_u32 s61, s61, 0x10000\n\t" "s_waitcnt lgkmcnt(4)\n\t" "v_smfmac_f32_16x16x64_bf16 v[212:215], v[252:255], v[168:175], %[ix]\n\t" "s_nop 1\n\t" \
    "v_smfmac_f32_16x16x64_bf16 v[216:219], v[240:243], v[128:135], %[ix]\n\t" "s_nop 1\n\t" "v_smfmac_f32_16x16x64_bf16 v[220:223], v[240:243], v[144:151], %[ix]\n\t" \
    "v_cvt_pk_bf16_f32 v224, v208, v210\n\t" "v_cvt_pk_bf16_f32 v225, v209, v211\n\t" "v_smfmac_f32_16x16x64_bf16 v[216:219], v[244:247], v[136:143], %[ix]\n\t" \
    "v_cvt_pk_bf16_f32 v226, v212, v214\n\t" "v_cvt_pk_bf16_f32 v227, v213, v215\n\t" "v_smfmac_f32_16x16x64_bf16 v[220:223], v[244:247], v[152:159], %[ix]\n\t" "s_nop 0\n\t" \
    "v_permlane32_swap_b32 v224, v226\n\t" "v_smfmac_f32_16x16x64_bf16 v[216:219], v[248:251], a[240:247], %[ix]\n\t" "v_permlane32_swap_b32 v225, v227\n\t" "s_nop 0\n\t" \
    "v_smfmac_f32_16x16x64_bf16 v[220:223], v[248:251], a[248:255], %[ix]\n\t" "v_permlane16_swap_b32 v224, v225\n\t" "v_permlane16_swap_b32 v226, v227\n\t" \
    "s_waitcnt lgkmcnt(2)\n\t" "v_smfmac_f32_16x16x64_bf16 v[216:219], v[252:255], v[176:183], %[ix]\n\t" "buffer_store_dwordx4 v[224:227], %[uo], %[rs], s61 offen sc1\n\t" \
    "s_add_u32 s61, s61, 0x10000\n\t" "s_waitcnt lgkmcnt(0)\n\t" "v_smfmac_f32_16x16x64_bf16 v[220:223], v[252:255], v[184:191], %[ix]\n\t" "s_nop 5\n\t" \
    "v_cvt_pk_bf16_f32 v228, v216, v218\n\t" "v_cvt_pk_bf16_f32 v229, v217, v219\n\t" "v_cvt_pk_bf16_f32 v230, v220, v222\n\t" "v_cvt_pk_bf16_f32 v231, v221, v223\n\t" \
    "s_nop 0\n\t" "v_permlane32_swap_b32 v228, v230\n\t" "v_permlane32_swap_b32 v229, v231\n\t" "s_nop 1\n\t" "v_permlane16_swap_b32 v228, v229\n\t" \
    "v_permlane16_swap_b32 v230, v231\n\t" "buffer_store_dwordx4 v[228:231], %[uo], %[rs], s61 offen sc1\n\t" "s_nop 1\n\t" XT(96, 97) "s_branch 99f\n\t" "41:\n\t" \
    "s_cmp_eq_u32 %[bd], 0\n\t" "s_cbranch_scc1 46f\n\t" "s_sub_u32 %[bd], %[bd], 1\n\t" XCNT(100) "s_waitcnt lgkmcnt(0)\n\t" "s_nop 7\n\t" "s_branch 40b\n\t" "46:\n\t" \
    "s_cmp_lg_u32 s60, 0\n\t" "s_branch 42b\n\t" "44:\n\t" "s_cmp_eq_u32 %[bd], 0\n\t" "s_cbranch_scc1 45b\n\t" "s_sub_u32 %[bd], %[bd], 1\n\t" XCNT(101) \
    "s_mov_b32 s60, 1\n\t" "s_waitcnt lgkmcnt(0)\n\t" "s_nop 7\n\t" "s_branch 40b\n\t" "99:\n\t"
#define Y_STEP \
    "s_getpc_b64 s[62:63]\n\t" "11:\n\t" "s_memtime s[64:65]\n\t" "s_waitcnt lgkmcnt(0)\n\t" "s_sub_u32 s61, %[tu], s64\n\t" "s_cmp_gt_i32 s61, 124\n\t" \
    "s_cbranch_scc1 11b\n\t" "s_max_i32 s61, s61, 0\n\t" "s_and_b32 s61, s61, 124\n\t" "s_sub_u32 s61, 12f-11b, s61\n\t" "s_add_u32 s62, s62, s61\n\t" \
    "s_addc_u32 s63, s63, 0\n\t" "s_setpc_b64 s[62:63]\n\t" ".rept 31\n\t" "s_nop 0\n\t" ".endr\n\t" "12:\n\t" XT(90, 91) \
    "buffer_load_dwordx4 v[144:147], %[u0], %[rs], %[ub] offen offset:0 sc1\n\t" "buffer_load_dwordx4 v[148:151], %[u0], %[rs], %[ub] offen offset:1024 sc1\n\t" \
    "buffer_load_dwordx4 v[152:155], %[u0], %[rs], %[ub] offen offset:2048 sc1\n\t" "buffer_load_dwordx4 v[156:159], %[u0], %[rs], %[ub] offen offset:3072 sc1\n\t" \
    "buffer_load_dwordx4 v[160:163], %[u1], %[rs], %[ub] offen offset:0 sc1\n\t" "buffer_load_dwordx4 v[164:167], %[u1], %[rs], %[ub] offen offset:1024 sc1\n\t" \
    "buffer_load_dwordx4 v[168:171], %[u1], %[rs], %[ub] offen offset:2048 sc1\n\t" "buffer_load_dwordx4 v[172:175], %[u1], %[rs], %[ub] offen offset:3072 sc1\n\t" \
    "s_cmp_lg_u32 %[late], 0\n\t" "s_cbranch_scc1 14f\n\t" "s_getpc_b64 s[62:63]\n\t" "13:\n\t" "s_memtime s[64:65]\n\t" "s_waitcnt lgkmcnt(0)\n\t" \
    "s_sub_u32 s61, %[tg], s64\n\t" "s_cmp_gt_i32 s61, 124\n\t" "s_cbranch_scc1 13b\n\t" "s_max_i32 s61, s61, 0\n\t" "s_and_b32 s61, s61, 124\n\t" \
    "s_sub_u32 s61, 14f-13b, s61\n\t" "s_add_u32 s62, s62, s61\n\t" "s_addc_u32 s63, s63, 0\n\t" "s_setpc_b64 s[62:63]\n\t" ".rept 31\n\t" "s_nop 0\n\t" ".endr\n\t" "14:\n\t" \
    XT(92, 93) "20:\n\t" "buffer_load_dwordx4 v[192:195], %[x0], %[rs], %[pb] offen offset:0 sc1\n\t" \
    "buffer_load_dwordx4 v[196:199], %[x0], %[rs], %[pb] offen offset:1024 sc1\n\t" "buffer_load_dwordx4 v[200:203], %[x0], %[rs], %[pb] offen offset:2048 sc1\n\t" \
    "buffer_load_dwordx4 v[204:207], %[x0], %[rs], %[pb] offen offset:3072 sc1\n\t" "buffer_load_dwordx4 v[208:211], %[x1], %[rs], %[pb] offen offset:0 sc1\n\t" \
    "buffer_load_dwordx4 v[212:215], %[x1], %[rs], %[pb] offen offset:1024 sc1\n\t" "buffer_load_dwordx4 v[216:219], %[x1], %[rs], %[pb] offen offset:2048 sc1\n\t" \
    "buffer_load_dwordx4 v[220:223], %[x1], %[rs], %[pb] offen offset:3072 sc1\n\t" "buffer_load_dwordx4 v[224:227], %[x2], %[rs], %[pb] offen offset:0 sc1\n\t" \
    "buffer_load_dwordx4 v[228:231], %[x2], %[rs], %[pb] offen offset:1024 sc1\n\t" "buffer_load_dwordx4 v[232:235], %[x2], %[rs], %[pb] offen offset:2048 sc1\n\t" \
    "buffer_load_dwordx4 v[236:239], %[x2], %[rs], %[pb] offen offset:3072 sc1\n\t" "buffer_load_dwordx4 v[240:243], %[x3], %[rs], %[pb] offen offset:0 sc1\n\t" \
    "buffer_load_dwordx4 v[244:247], %[x3], %[rs], %[pb] offen offset:1024 sc1\n\t" "buffer_load_dwordx4 v[248:251], %[x3], %[rs], %[pb] offen offset:2048 sc1\n\t" \
    "buffer_load_dwordx4 v[252:255], %[x3], %[rs], %[pb] offen offset:3072 sc1\n\t" "v_mov_b64 v[120:121], 0\n\t" "v_mov_b64 v[122:123], 0\n\t" \
    "buffer_load_dwordx2 v[176:177], %[pm], %[rs], %[pb] offen offset:0 sc1\n\t" "buffer_load_dwordx2 v[178:179], %[pm], %[rs], %[pb] offen offset:128 sc1\n\t" \
    "buffer_load_dwordx2 v[180:181], %[pm], %[rs], %[pb] offen offset:1024 sc1\n\t" "buffer_load_dwordx2 v[182:183], %[pm], %[rs], %[pb] offen offset:1152 sc1\n\t" \
    "buffer_load_dwordx2 v[184:185], %[pm], %[rs], %[pb] offen offset:2048 sc1\n\t" "buffer_load_dwordx2 v[186:187], %[pm], %[rs], %[pb] offen offset:2176 sc1\n\t" \
    "buffer_load_dwordx2 v[188:189], %[pm], %[rs], %[pb] offen offset:3072 sc1\n\t" "buffer_load_dwordx2 v[190:191], %[pm], %[rs], %[pb] offen offset:3200 sc1\n\t" \
    "buffer_load_dword %[sv], %[so], %[rs], %[pb] offen sc1\n\t" "v_mov_b32 v127, -1\n\t" "s_waitcnt vmcnt(24)\n\t" XT(94, 95) \
    "v_smfmac_f32_16x16x64_bf16 v[120:123], v[192:195], a[0:7], %[ix]\n\t" "s_waitcnt vmcnt(23)\n\t" "v_smfmac_f32_16x16x64_bf16 v[120:123], v[196:199], a[8:15], %[ix]\n\t" \
    "s_waitcnt vmcnt(22)\n\t" "v_smfmac_f32_16x16x64_bf16 v[120:123], v[200:203], a[16:23], %[ix]\n\t" "s_waitcnt vmcnt(21)\n\t" \
    "v_smfmac_f32_16x16x64_bf16 v[120:123], v[204:207], a[24:31], %[ix]\n\t" "s_waitcnt vmcnt(20)\n\t" \
    "v_smfmac_f32_16x16x64_bf16 v[120:123], v[208:211], a[32:39], %[ix]\n\t" "s_waitcnt vmcnt(19)\n\t" \
    "v_smfmac_f32_16x16x64_bf16 v[120:123], v[212:215], a[40:47], %[ix]\n\t" "s_waitcnt vmcnt(18)\n\t" \
    "v_smfmac_f32_16x16x64_bf16 v[120:123], v[216:219], a[48:55], %[ix]\n\t" "s_waitcnt vmcnt(17)\n\t" \
    "v_smfmac_f32_16x16x64_bf16 v[120:123], v[220:223], a[56:63], %[ix]\n\t" "s_waitcnt vmcnt(16)\n\t" \
    "v_smfmac_f32_16x16x64_bf16 v[120:123], v[224:227], a[64:71], %[ix]\n\t" "s_waitcnt vmcnt(15)\n\t" \
    "v_smfmac_f32_16x16x64_bf16 v[120:123], v[228:231], a[72:79], %[ix]\n\t" "s_waitcnt vmcnt(14)\n\t" \
    "v_smfmac_f32_16x16x64_bf16 v[120:123], v[232:235], a[80:87], %[ix]\n\t" "s_waitcnt vmcnt(13)\n\t" \
    "v_smfmac_f32_16x16x64_bf16 v[120:123], v[236:239], a[88:95], %[ix]\n\t" "s_waitcnt vmcnt(12)\n\t" \
    "v_smfmac_f32_16x16x64_bf16 v[120:123], v[240:243], a[96:103], %[ix]\n\t" "s_waitcnt vmcnt(11)\n\t" \
    "v_smfmac_f32_16x16x64_bf16 v[120:123], v[244:247], a[104:111], %[ix]\n\t" "s_waitcnt vmcnt(10)\n\t" \
    "v_smfmac_f32_16x16x64_bf16 v[120:123], v[248:251], a[112:119], %[ix]\n\t" "s_waitcnt vmcnt(9)\n\t" \
    "v_smfmac_f32_16x16x64_bf16 v[120:123], v[252:255], a[120:127], %[ix]\n\t" "s_waitcnt vmcnt(7)\n\t" \
    "v_smfmac_f32_16x16x64_bf16 v[120:123], v[176:179], v[144:151], %[ix]\n\t" "s_waitcnt vmcnt(5)\n\t" \
    "v_smfmac_f32_16x16x64_bf16 v[120:123], v[180:183], v[152:159], %[ix]\n\t" "s_waitcnt vmcnt(3)\n\t" \
    "v_smfmac_f32_16x16x64_bf16 v[120:123], v[184:187], v[160:167], %[ix]\n\t" "s_waitcnt vmcnt(1)\n\t" \
    "v_smfmac_f32_16x16x64_bf16 v[120:123], v[188:191], v[168:175], %[ix]\n\t" XT(82, 83) "s_nop 7\n\t" "v_add_f32 v120, v120, v121\n\t" "v_add_f32 v122, v122, v123\n\t" \
    "v_cvt_pk_bf16_f32 v124, v120, v122\n\t" "v_cmp_u_f32 vcc, v120, v122\n\t" "s_cbranch_vccnz 30f\n\t" "21:\n\t" \
    "v_mov_b32_dpp v125, v124 quad_perm:[1,0,3,2] row_mask:0xf bank_mask:0xf\n\t" "v_perm_b32 v124, v125, v124, %[ps]\n\t" "v_min_u32 v124, -2, v124\n\t" \
    "buffer_store_dword v124, %[mw], %[rs], %[mb] offen sc1\n\t" XT(84, 85) "buffer_store_dword v127, %[mw], %[rs], %[mr] offen sc1\n\t" "s_memtime s[64:65]\n\t" \
    "s_barrier\n\t" XT(98, 99) "s_waitcnt lgkmcnt(0)\n\t" "s_and_b32 %[bad], s64, -2\n\t" "s_branch 40f\n\t" "30:\n\t" "v_max_u32 v126, v144, v145\n\t" \
    "v_max3_u32 v126, v126, v146, v147\n\t" "v_max3_u32 v126, v126, v148, v149\n\t" "v_max3_u32 v126, v126, v150, v151\n\t" "v_max3_u32 v126, v126, v152, v153\n\t" \
    "v_max3_u32 v126, v126, v154, v155\n\t" "v_max3_u32 v126, v126, v156, v157\n\t" "v_max3_u32 v126, v126, v158, v159\n\t" "v_max3_u32 v126, v126, v160, v161\n\t" \
    "v_max3_u32 v126, v126, v162, v163\n\t" "v_max3_u32 v126, v126, v164, v165\n\t" "v_max3_u32 v126, v126, v166, v167\n\t" "v_max3_u32 v126, v126, v168, v169\n\t" \
    "v_max3_u32 v126, v126, v170, v171\n\t" "v_max3_u32 v126, v126, v172, v173\n\t" "v_max3_u32 v126, v126, v174, v175\n\t" UCNT "v_max3_u32 v126, v126, v176, v177\n\t" \
    "v_max3_u32 v126, v126, v178, v179\n\t" "v_max3_u32 v126, v126, v180, v181\n\t" "v_max3_u32 v126, v126, v182, v183\n\t" "v_max3_u32 v126, v126, v184, v185\n\t" \
    "v_max3_u32 v126, v126, v186, v187\n\t" "v_max3_u32 v126, v126, v188, v189\n\t" "v_max3_u32 v126, v126, v190, v191\n\t" "v_max3_u32 v126, v126, v192, v193\n\t" \
    "v_max3_u32 v126, v126, v194, v195\n\t" "v_max3_u32 v126, v126, v196, v197\n\t" "v_max3_u32 v126, v126, v198, v199\n\t" "v_max3_u32 v126, v126, v200, v201\n\t" \
    "v_max3_u32 v126, v126, v202, v203\n\t" "v_max3_u32 v126, v126, v204, v205\n\t" "v_max3_u32 v126, v126, v206, v207\n\t" "v_max3_u32 v126, v126, v208, v209\n\t" \
    "v_max3_u32 v126, v126, v210, v211\n\t" "v_max3_u32 v126, v126, v212, v213\n\t" "v_max3_u32 v126, v126, v214, v215\n\t" "v_max3_u32 v126, v126, v216, v217\n\t" \
    "v_max3_u32 v126, v126, v218, v219\n\t" "v_max3_u32 v126, v126, v220, v221\n\t" "v_max3_u32 v126, v126, v222, v223\n\t" "v_max3_u32 v126, v126, v224, v225\n\t" \
    "v_max3_u32 v126, v126, v226, v227\n\t" "v_max3_u32 v126, v126, v228, v229\n\t" "v_max3_u32 v126, v126, v230, v231\n\t" "v_max3_u32 v126, v126, v232, v233\n\t" \
    "v_max3_u32 v126, v126, v234, v235\n\t" "v_max3_u32 v126, v126, v236, v237\n\t" "v_max3_u32 v126, v126, v238, v239\n\t" "v_max3_u32 v126, v126, v240, v241\n\t" \
    "v_max3_u32 v126, v126, v242, v243\n\t" "v_max3_u32 v126, v126, v244, v245\n\t" "v_max3_u32 v126, v126, v246, v247\n\t" "v_max3_u32 v126, v126, v248, v249\n\t" \
    "v_max3_u32 v126, v126, v250, v251\n\t" "v_max3_u32 v126, v126, v252, v253\n\t" "v_max3_u32 v126, v126, v254, v255\n\t" "v_cmp_eq_u32 vcc, -1, v126\n\t" \
    "s_cbranch_vccz 21b\n\t" "s_cmp_eq_u32 %[bd], 0\n\t" "s_cbranch_scc1 21b\n\t" XCNT(100) "s_mov_b32 %[bad], 1\n\t" "s_waitcnt vmcnt(0)\n\t" "40:\n\t"
#define G_LOOP \
    "v_xor_b32 v72, 0, %[xo]\n\t" "v_add_u32 v80, 0x10000, v72\n\t" "v_xor_b32 v88, 0, %[xs]\n\t" "v_add_u32 v88, 0, v88\n\t" "v_xor_b32 v73, 32, %[xo]\n\t" \
    "v_add_u32 v81, 0x10000, v73\n\t" "v_xor_b32 v89, 16, %[xs]\n\t" "v_add_u32 v89, 2048, v89\n\t" "v_xor_b32 v74, 64, %[xo]\n\t" "v_add_u32 v82, 0x10000, v74\n\t" \
    "v_xor_b32 v90, 32, %[xs]\n\t" "v_add_u32 v90, 4096, v90\n\t" "v_xor_b32 v75, 96, %[xo]\n\t" "v_add_u32 v83, 0x10000, v75\n\t" "v_xor_b32 v91, 48, %[xs]\n\t" \
    "v_add_u32 v91, 6144, v91\n\t" "v_xor_b32 v76, 128, %[xo]\n\t" "v_add_u32 v84, 0x10000, v76\n\t" "v_xor_b32 v92, 64, %[xs]\n\t" "v_add_u32 v92, 8192, v92\n\t" \
    "v_xor_b32 v77, 160, %[xo]\n\t" "v_add_u32 v85, 0x10000, v77\n\t" "v_xor_b32 v93, 80, %[xs]\n\t" "v_add_u32 v93, 10240, v93\n\t" "v_xor_b32 v78, 192, %[xo]\n\t" \
    "v_add_u32 v86, 0x10000, v78\n\t" "v_xor_b32 v94, 96, %[xs]\n\t" "v_add_u32 v94, 12288, v94\n\t" "v_xor_b32 v79, 224, %[xo]\n\t" "v_add_u32 v87, 0x10000, v79\n\t" \
    "v_xor_b32 v95, 112, %[xs]\n\t" "v_add_u32 v95, 14336, v95\n\t" "v_mov_b32 v96, 2.0\n\t" "s_mov_b32 s60, 0\n\t" "s_mov_b32 s62, 0\n\t" "s_waitcnt vmcnt(0)\n\t" \
    "s_barrier\n\t" "ds_read_b128 v[100:103], v72 offset:0\n\t" "ds_read_b128 v[104:107], v73 offset:0\n\t" "ds_read_b128 v[108:111], v74 offset:0\n\t" \
    "ds_read_b128 v[112:115], v75 offset:0\n\t" "ds_read_b128 v[116:119], v76 offset:0\n\t" "10:\n\t" XT(80, 81) "s_waitcnt lgkmcnt(4)\n\t" \
    "v_mfma_f32_32x32x16_bf16 v[32:47], a[0:3], v[100:103], 0\n\t" "ds_read_b128 v[120:123], v77 offset:0\n\t" "s_add_u32 s61, s60, 1\n\t" "s_and_b32 s61, s61, 63\n\t" \
    "s_lshl_b32 s61, s61, 16\n\t" "s_add_u32 s63, %[lw], 65536\n\t" "s_waitcnt lgkmcnt(4)\n\t" "v_mfma_f32_32x32x16_bf16 v[32:47], a[4:7], v[104:107], v[32:47]\n\t" \
    "ds_read_b128 v[124:127], v78 offset:0\n\t" "s_waitcnt lgkmcnt(4)\n\t" "v_mfma_f32_32x32x16_bf16 v[32:47], a[8:11], v[108:111], v[32:47]\n\t" \
    "ds_read_b128 v[128:131], v79 offset:0\n\t" "v_cvt_pk_bf16_f32 v64, v48, v49\n\t" "v_cvt_pk_bf16_f32 v65, v50, v51\n\t" "v_cvt_pk_bf16_f32 v66, v52, v53\n\t" \
    "s_waitcnt lgkmcnt(4)\n\t" "v_mfma_f32_32x32x16_bf16 v[32:47], a[12:15], v[112:115], v[32:47]\n\t" "ds_read_b128 v[100:103], v72 offset:256\n\t" \
    "v_cvt_pk_bf16_f32 v67, v54, v55\n\t" "v_cvt_pk_bf16_f32 v68, v56, v57\n\t" "v_cvt_pk_bf16_f32 v69, v58, v59\n\t" "s_waitcnt lgkmcnt(4)\n\t" \
    "v_mfma_f32_32x32x16_bf16 v[32:47], a[16:19], v[116:119], v[32:47]\n\t" "ds_read_b128 v[104:107], v73 offset:256\n\t" "v_cvt_pk_bf16_f32 v70, v60, v61\n\t" \
    "v_cvt_pk_bf16_f32 v71, v62, v63\n\t" "buffer_store_dwordx4 v[64:67], v96, %[rs], s62 offen offset:0 sc1\n\t" "s_waitcnt lgkmcnt(4)\n\t" \
    "v_mfma_f32_32x32x16_bf16 v[32:47], a[20:23], v[120:123], v[32:47]\n\t" "ds_read_b128 v[108:111], v74 offset:256\n\t" "s_waitcnt lgkmcnt(4)\n\t" \
    "v_mfma_f32_32x32x16_bf16 v[32:47], a[24:27], v[124:127], v[32:47]\n\t" "ds_read_b128 v[112:115], v75 offset:256\n\t" \
    "buffer_store_dwordx4 v[68:71], v96, %[rs], s62 offen offset:1024 sc1\n\t" "s_waitcnt lgkmcnt(4)\n\t" \
    "v_mfma_f32_32x32x16_bf16 v[32:47], a[28:31], v[128:131], v[32:47]\n\t" "ds_read_b128 v[116:119], v76 offset:256\n\t" "s_mov_b32 m0, s63\n\t" \
    "s_add_u32 s63, s63, 0x800\n\t" "buffer_load_dwordx4 v88, %[rx], s61 offen lds\n\t" "s_waitcnt lgkmcnt(4)\n\t" \
    "v_mfma_f32_32x32x16_bf16 v[32:47], a[32:35], v[100:103], v[32:47]\n\t" "ds_read_b128 v[120:123], v77 offset:256\n\t" \
    "buffer_load_dwordx4 v88, %[rx], s61 offen offset:1024 lds\n\t" "s_waitcnt lgkmcnt(4)\n\t" "v_mfma_f32_32x32x16_bf16 v[32:47], a[36:39], v[104:107], v[32:47]\n\t" \
    "ds_read_b128 v[124:127], v78 offset:256\n\t" "s_mov_b32 m0, s63\n\t" "s_add_u32 s63, s63, 0x800\n\t" "buffer_load_dwordx4 v89, %[rx], s61 offen lds\n\t" \
    "s_waitcnt lgkmcnt(4)\n\t" "v_mfma_f32_32x32x16_bf16 v[32:47], a[40:43], v[108:111], v[32:47]\n\t" "ds_read_b128 v[128:131], v79 offset:256\n\t" \
    "buffer_load_dwordx4 v89, %[rx], s61 offen offset:1024 lds\n\t" "s_waitcnt lgkmcnt(4)\n\t" "v_mfma_f32_32x32x16_bf16 v[32:47], a[44:47], v[112:115], v[32:47]\n\t" \
    "ds_read_b128 v[100:103], v72 offset:512\n\t" "s_mov_b32 m0, s63\n\t" "s_add_u32 s63, s63, 0x800\n\t" "buffer_load_dwordx4 v90, %[rx], s61 offen lds\n\t" \
    "s_waitcnt lgkmcnt(4)\n\t" "v_mfma_f32_32x32x16_bf16 v[32:47], a[48:51], v[116:119], v[32:47]\n\t" "ds_read_b128 v[104:107], v73 offset:512\n\t" \
    "buffer_load_dwordx4 v90, %[rx], s61 offen offset:1024 lds\n\t" "s_waitcnt lgkmcnt(4)\n\t" "v_mfma_f32_32x32x16_bf16 v[32:47], a[52:55], v[120:123], v[32:47]\n\t" \
    "ds_read_b128 v[108:111], v74 offset:512\n\t" "s_mov_b32 m0, s63\n\t" "s_add_u32 s63, s63, 0x800\n\t" "buffer_load_dwordx4 v91, %[rx], s61 offen lds\n\t" \
    "s_waitcnt lgkmcnt(4)\n\t" "v_mfma_f32_32x32x16_bf16 v[32:47], a[56:59], v[124:127], v[32:47]\n\t" "ds_read_b128 v[112:115], v75 offset:512\n\t" \
    "buffer_load_dwordx4 v91, %[rx], s61 offen offset:1024 lds\n\t" "s_waitcnt lgkmcnt(4)\n\t" "v_mfma_f32_32x32x16_bf16 v[32:47], a[60:63], v[128:131], v[32:47]\n\t" \
    "ds_read_b128 v[116:119], v76 offset:512\n\t" "s_mov_b32 m0, s63\n\t" "s_add_u32 s63, s63, 0x800\n\t" "buffer_load_dwordx4 v92, %[rx], s61 offen lds\n\t" \
    "s_waitcnt lgkmcnt(4)\n\t" "v_mfma_f32_32x32x16_bf16 v[32:47], a[64:67], v[100:103], v[32:47]\n\t" "ds_read_b128 v[120:123], v77 offset:512\n\t" \
    "buffer_load_dwordx4 v92, %[rx], s61 offen offset:1024 lds\n\t" "s_waitcnt lgkmcnt(4)\n\t" "v_mfma_f32_32x32x16_bf16 v[32:47], a[68:71], v[104:107], v[32:47]\n\t" \
    "ds_read_b128 v[124:127], v78 offset:512\n\t" "s_mov_b32 m0, s63\n\t" "s_add_u32 s63, s63, 0x800\n\t" "buffer_load_dwordx4 v93, %[rx], s61 offen lds\n\t" \
    "s_waitcnt lgkmcnt(4)\n\t" "v_mfma_f32_32x32x16_bf16 v[32:47], a[72:75], v[108:111], v[32:47]\n\t" "ds_read_b128 v[128:131], v79 offset:512\n\t" \
    "buffer_load_dwordx4 v93, %[rx], s61 offen offset:1024 lds\n\t" "s_waitcnt lgkmcnt(4)\n\t" "v_mfma_f32_32x32x16_bf16 v[32:47], a[76:79], v[112:115], v[32:47]\n\t" \
    "ds_read_b128 v[100:103], v72 offset:768\n\t" "s_mov_b32 m0, s63\n\t" "s_add_u32 s63, s63, 0x800\n\t" "buffer_load_dwordx4 v94, %[rx], s61 offen lds\n\t" \
    "s_waitcnt lgkmcnt(4)\n\t" "v_mfma_f32_32x32x16_bf16 v[32:47], a[80:83], v[116:119], v[32:47]\n\t" "ds_read_b128 v[104:107], v73 offset:768\n\t" \
    "buffer_load_dwordx4 v94, %[rx], s61 offen offset:1024 lds\n\t" "s_waitcnt lgkmcnt(4)\n\t" "v_mfma_f32_32x32x16_bf16 v[32:47], a[84:87], v[120:123], v[32:47]\n\t" \
    "ds_read_b128 v[108:111], v74 offset:768\n\t" "s_mov_b32 m0, s63\n\t" "s_add_u32 s63, s63, 0x800\n\t" "buffer_load_dwordx4 v95, %[rx], s61 offen lds\n\t" \
    "s_waitcnt lgkmcnt(4)\n\t" "v_mfma_f32_32x32x16_bf16 v[32:47], a[88:91], v[124:127], v[32:47]\n\t" "ds_read_b128 v[112:115], v75 offset:768\n\t" \
    "buffer_load_dwordx4 v95, %[rx], s61 offen offset:1024 lds\n\t" "s_waitcnt lgkmcnt(4)\n\t" "v_mfma_f32_32x32x16_bf16 v[32:47], a[92:95], v[128:131], v[32:47]\n\t" \
    "ds_read_b128 v[116:119], v76 offset:768\n\t" "s_waitcnt lgkmcnt(4)\n\t" "v_mfma_f32_32x32x16_bf16 v[32:47], a[96:99], v[100:103], v[32:47]\n\t" \
    "ds_read_b128 v[120:123], v77 offset:768\n\t" "s_lshr_b32 s64, s60, 2\n\t" "s_xor_b32 s64, s64, %[xp]\n\t" "s_and_b32 s64, s64, 1\n\t" "s_lshl_b32 s64, s64, 13\n\t" \
    "s_waitcnt lgkmcnt(4)\n\t" "v_mfma_f32_32x32x16_bf16 v[32:47], a[100:103], v[104:107], v[32:47]\n\t" "ds_read_b128 v[124:127], v78 offset:768\n\t" \
    "s_lshl_b32 s62, s60, 19\n\t" "s_add_u32 s62, s62, s64\n\t" "s_add_u32 s62, s62, %[ob]\n\t" "s_waitcnt lgkmcnt(4)\n\t" \
    "v_mfma_f32_32x32x16_bf16 v[32:47], a[104:107], v[108:111], v[32:47]\n\t" "ds_read_b128 v[128:131], v79 offset:768\n\t" "s_waitcnt lgkmcnt(4)\n\t" \
    "v_mfma_f32_32x32x16_bf16 v[32:47], a[108:111], v[112:115], v[32:47]\n\t" "ds_read_b128 v[100:103], v72 offset:1024\n\t" "s_waitcnt lgkmcnt(4)\n\t" \
    "v_mfma_f32_32x32x16_bf16 v[32:47], a[112:115], v[116:119], v[32:47]\n\t" "ds_read_b128 v[104:107], v73 offset:1024\n\t" "s_waitcnt lgkmcnt(4)\n\t" \
    "v_mfma_f32_32x32x16_bf16 v[32:47], a[116:119], v[120:123], v[32:47]\n\t" "ds_read_b128 v[108:111], v74 offset:1024\n\t" "s_waitcnt lgkmcnt(4)\n\t" \
    "v_mfma_f32_32x32x16_bf16 v[32:47], a[120:123], v[124:127], v[32:47]\n\t" "ds_read_b128 v[112:115], v75 offset:1024\n\t" "s_waitcnt lgkmcnt(4)\n\t" \
    "v_mfma_f32_32x32x16_bf16 v[32:47], a[124:127], v[128:131], v[32:47]\n\t" "ds_read_b128 v[116:119], v76 offset:1024\n\t" "s_waitcnt lgkmcnt(4)\n\t" \
    "v_mfma_f32_32x32x16_bf16 v[32:47], a[128:131], v[100:103], v[32:47]\n\t" "ds_read_b128 v[120:123], v77 offset:1024\n\t" "s_waitcnt lgkmcnt(4)\n\t" \
    "v_mfma_f32_32x32x16_bf16 v[32:47], a[132:135], v[104:107], v[32:47]\n\t" "ds_read_b128 v[124:127], v78 offset:1024\n\t" "s_waitcnt lgkmcnt(4)\n\t" \
    "v_mfma_f32_32x32x16_bf16 v[32:47], a[136:139], v[108:111], v[32:47]\n\t" "ds_read_b128 v[128:131], v79 offset:1024\n\t" "s_waitcnt vmcnt(16)\n\t" "s_barrier\n\t" \
    "v_mov_b32 v98, s60\n\t" "buffer_store_dword v98, %[fo], %[rc], %[fl] offen sc1\n\t" "s_waitcnt lgkmcnt(4)\n\t" \
    "v_mfma_f32_32x32x16_bf16 v[32:47], a[140:143], v[112:115], v[32:47]\n\t" "ds_read_b128 v[100:103], v72 offset:1280\n\t" "s_waitcnt lgkmcnt(4)\n\t" \
    "v_mfma_f32_32x32x16_bf16 v[32:47], a[144:147], v[116:119], v[32:47]\n\t" "ds_read_b128 v[104:107], v73 offset:1280\n\t" "s_waitcnt lgkmcnt(4)\n\t" \
    "v_mfma_f32_32x32x16_bf16 v[32:47], a[148:151], v[120:123], v[32:47]\n\t" "ds_read_b128 v[108:111], v74 offset:1280\n\t" "s_waitcnt lgkmcnt(4)\n\t" \
    "v_mfma_f32_32x32x16_bf16 v[32:47], a[152:155], v[124:127], v[32:47]\n\t" "ds_read_b128 v[112:115], v75 offset:1280\n\t" "s_waitcnt lgkmcnt(4)\n\t" \
    "v_mfma_f32_32x32x16_bf16 v[32:47], a[156:159], v[128:131], v[32:47]\n\t" "ds_read_b128 v[116:119], v76 offset:1280\n\t" "s_waitcnt lgkmcnt(4)\n\t" \
    "v_mfma_f32_32x32x16_bf16 v[32:47], a[160:163], v[100:103], v[32:47]\n\t" "ds_read_b128 v[120:123], v77 offset:1280\n\t" "s_waitcnt lgkmcnt(4)\n\t" \
    "v_mfma_f32_32x32x16_bf16 v[32:47], a[164:167], v[104:107], v[32:47]\n\t" "ds_read_b128 v[124:127], v78 offset:1280\n\t" "s_waitcnt lgkmcnt(4)\n\t" \
    "v_mfma_f32_32x32x16_bf16 v[32:47], a[168:171], v[108:111], v[32:47]\n\t" "ds_read_b128 v[128:131], v79 offset:1280\n\t" "s_waitcnt lgkmcnt(4)\n\t" \
    "v_mfma_f32_32x32x16_bf16 v[32:47], a[172:175], v[112:115], v[32:47]\n\t" "ds_read_b128 v[100:103], v72 offset:1536\n\t" "s_waitcnt lgkmcnt(4)\n\t" \
    "v_mfma_f32_32x32x16_bf16 v[32:47], a[176:179], v[116:119], v[32:47]\n\t" "ds_read_b128 v[104:107], v73 offset:1536\n\t" "s_waitcnt lgkmcnt(4)\n\t" \
    "v_mfma_f32_32x32x16_bf16 v[32:47], a[180:183], v[120:123], v[32:47]\n\t" "ds_read_b128 v[108:111], v74 offset:1536\n\t" "s_waitcnt lgkmcnt(4)\n\t" \
    "v_mfma_f32_32x32x16_bf16 v[32:47], a[184:187], v[124:127], v[32:47]\n\t" "ds_read_b128 v[112:115], v75 offset:1536\n\t" "s_waitcnt lgkmcnt(4)\n\t" \
    "v_mfma_f32_32x32x16_bf16 v[32:47], a[188:191], v[128:131], v[32:47]\n\t" "ds_read_b128 v[116:119], v76 offset:1536\n\t" "s_waitcnt lgkmcnt(4)\n\t" \
    "v_mfma_f32_32x32x16_bf16 v[32:47], a[192:195], v[100:103], v[32:47]\n\t" "ds_read_b128 v[120:123], v77 offset:1536\n\t" "s_waitcnt lgkmcnt(4)\n\t" \
    "v_mfma_f32_32x32x16_bf16 v[32:47], a[196:199], v[104:107], v[32:47]\n\t" "ds_read_b128 v[124:127], v78 offset:1536\n\t" "s_waitcnt lgkmcnt(4)\n\t" \
    "v_mfma_f32_32x32x16_bf16 v[32:47], a[200:203], v[108:111], v[32:47]\n\t" "ds_read_b128 v[128:131], v79 offset:1536\n\t" "s_waitcnt lgkmcnt(4)\n\t" \
    "v_mfma_f32_32x32x16_bf16 v[32:47], a[204:207], v[112:115], v[32:47]\n\t" "ds_read_b128 v[100:103], v72 offset:1792\n\t" "s_waitcnt lgkmcnt(4)\n\t" \
    "v_mfma_f32_32x32x16_bf16 v[32:47], a[208:211], v[116:119], v[32:47]\n\t" "ds_read_b128 v[104:107], v73 offset:1792\n\t" "s_waitcnt lgkmcnt(4)\n\t" \
    "v_mfma_f32_32x32x16_bf16 v[32:47], a[212:215], v[120:123], v[32:47]\n\t" "ds_read_b128 v[108:111], v74 offset:1792\n\t" "s_waitcnt lgkmcnt(4)\n\t" \
    "v_mfma_f32_32x32x16_bf16 v[32:47], a[216:219], v[124:127], v[32:47]\n\t" "ds_read_b128 v[112:115], v75 offset:1792\n\t" "s_waitcnt lgkmcnt(4)\n\t" \
    "v_mfma_f32_32x32x16_bf16 v[32:47], a[220:223], v[128:131], v[32:47]\n\t" "ds_read_b128 v[116:119], v76 offset:1792\n\t" "s_waitcnt lgkmcnt(4)\n\t" \
    "v_mfma_f32_32x32x16_bf16 v[32:47], a[224:227], v[100:103], v[32:47]\n\t" "ds_read_b128 v[120:123], v77 offset:1792\n\t" "s_waitcnt lgkmcnt(4)\n\t" \
    "v_mfma_f32_32x32x16_bf16 v[32:47], a[228:231], v[104:107], v[32:47]\n\t" "ds_read_b128 v[124:127], v78 offset:1792\n\t" "s_waitcnt lgkmcnt(4)\n\t" \
    "v_mfma_f32_32x32x16_bf16 v[32:47], a[232:235], v[108:111], v[32:47]\n\t" "ds_read_b128 v[128:131], v79 offset:1792\n\t" "s_waitcnt lgkmcnt(4)\n\t" \
    "v_mfma_f32_32x32x16_bf16 v[32:47], a[236:239], v[112:115], v[32:47]\n\t" "s_waitcnt lgkmcnt(3)\n\t" \
    "v_mfma_f32_32x32x16_bf16 v[32:47], a[240:243], v[116:119], v[32:47]\n\t" "s_waitcnt vmcnt(1) lgkmcnt(0)\n\t" "s_barrier\n\t" XT(82, 83) \
    "v_mfma_f32_32x32x16_bf16 v[32:47], a[244:247], v[120:123], v[32:47]\n\t" "ds_read_b128 v[100:103], v80 offset:0\n\t" \
    "v_mfma_f32_32x32x16_bf16 v[32:47], a[248:251], v[124:127], v[32:47]\n\t" "ds_read_b128 v[104:107], v81 offset:0\n\t" "ds_read_b128 v[108:111], v82 offset:0\n\t" \
    "v_mfma_f32_32x32x16_bf16 v[32:47], a[252:255], v[128:131], v[32:47]\n\t" "ds_read_b128 v[112:115], v83 offset:0\n\t" "ds_read_b128 v[116:119], v84 offset:0\n\t" \
    "s_add_u32 s60, s60, 1\n\t" GSUM "s_cmp_eq_u32 s60, 1\n\t" "s_cbranch_scc1 20f\n\t" "11:\n\t" XT(80, 81) "s_waitcnt lgkmcnt(4)\n\t" \
    "v_mfma_f32_32x32x16_bf16 v[48:63], a[0:3], v[100:103], 0\n\t" "ds_read_b128 v[120:123], v85 offset:0\n\t" "s_add_u32 s61, s60, 1\n\t" "s_and_b32 s61, s61, 63\n\t" \
    "s_lshl_b32 s61, s61, 16\n\t" "s_add_u32 s63, %[lw], 0\n\t" "s_waitcnt lgkmcnt(4)\n\t" "v_mfma_f32_32x32x16_bf16 v[48:63], a[4:7], v[104:107], v[48:63]\n\t" \
    "ds_read_b128 v[124:127], v86 offset:0\n\t" "s_waitcnt lgkmcnt(4)\n\t" "v_mfma_f32_32x32x16_bf16 v[48:63], a[8:11], v[108:111], v[48:63]\n\t" \
    "ds_read_b128 v[128:131], v87 offset:0\n\t" "v_cvt_pk_bf16_f32 v64, v32, v33\n\t" "v_cvt_pk_bf16_f32 v65, v34, v35\n\t" "v_cvt_pk_bf16_f32 v66, v36, v37\n\t" \
    "s_waitcnt lgkmcnt(4)\n\t" "v_mfma_f32_32x32x16_bf16 v[48:63], a[12:15], v[112:115], v[48:63]\n\t" "ds_read_b128 v[100:103], v80 offset:256\n\t" \
    "v_cvt_pk_bf16_f32 v67, v38, v39\n\t" "v_cvt_pk_bf16_f32 v68, v40, v41\n\t" "v_cvt_pk_bf16_f32 v69, v42, v43\n\t" "s_waitcnt lgkmcnt(4)\n\t" \
    "v_mfma_f32_32x32x16_bf16 v[48:63], a[16:19], v[116:119], v[48:63]\n\t" "ds_read_b128 v[104:107], v81 offset:256\n\t" "v_cvt_pk_bf16_f32 v70, v44, v45\n\t" \
    "v_cvt_pk_bf16_f32 v71, v46, v47\n\t" "buffer_store_dwordx4 v[64:67], v96, %[rs], s62 offen offset:0 sc1\n\t" "s_waitcnt lgkmcnt(4)\n\t" \
    "v_mfma_f32_32x32x16_bf16 v[48:63], a[20:23], v[120:123], v[48:63]\n\t" "ds_read_b128 v[108:111], v82 offset:256\n\t" "s_waitcnt lgkmcnt(4)\n\t" \
    "v_mfma_f32_32x32x16_bf16 v[48:63], a[24:27], v[124:127], v[48:63]\n\t" "ds_read_b128 v[112:115], v83 offset:256\n\t" \
    "buffer_store_dwordx4 v[68:71], v96, %[rs], s62 offen offset:1024 sc1\n\t" "s_waitcnt lgkmcnt(4)\n\t" \
    "v_mfma_f32_32x32x16_bf16 v[48:63], a[28:31], v[128:131], v[48:63]\n\t" "ds_read_b128 v[116:119], v84 offset:256\n\t" "s_mov_b32 m0, s63\n\t" \
    "s_add_u32 s63, s63, 0x800\n\t" "buffer_load_dwordx4 v88, %[rx], s61 offen lds\n\t" "s_waitcnt lgkmcnt(4)\n\t" \
    "v_mfma_f32_32x32x16_bf16 v[48:63], a[32:35], v[100:103], v[48:63]\n\t" "ds_read_b128 v[120:123], v85 offset:256\n\t" \
    "buffer_load_dwordx4 v88, %[rx], s61 offen offset:1024 lds\n\t" "s_waitcnt lgkmcnt(4)\n\t" "v_mfma_f32_32x32x16_bf16 v[48:63], a[36:39], v[104:107], v[48:63]\n\t" \
    "ds_read_b128 v[124:127], v86 offset:256\n\t" "s_mov_b32 m0, s63\n\t" "s_add_u32 s63, s63, 0x800\n\t" "buffer_load_dwordx4 v89, %[rx], s61 offen lds\n\t" \
    "s_waitcnt lgkmcnt(4)\n\t" "v_mfma_f32_32x32x16_bf16 v[48:63], a[40:43], v[108:111], v[48:63]\n\t" "ds_read_b128 v[128:131], v87 offset:256\n\t" \
    "buffer_load_dwordx4 v89, %[rx], s61 offen offset:1024 lds\n\t" "s_waitcnt lgkmcnt(4)\n\t" "v_mfma_f32_32x32x16_bf16 v[48:63], a[44:47], v[112:115], v[48:63]\n\t" \
    "ds_read_b128 v[100:103], v80 offset:512\n\t" "s_mov_b32 m0, s63\n\t" "s_add_u32 s63, s63, 0x800\n\t" "buffer_load_dwordx4 v90, %[rx], s61 offen lds\n\t" \
    "s_waitcnt lgkmcnt(4)\n\t" "v_mfma_f32_32x32x16_bf16 v[48:63], a[48:51], v[116:119], v[48:63]\n\t" "ds_read_b128 v[104:107], v81 offset:512\n\t" \
    "buffer_load_dwordx4 v90, %[rx], s61 offen offset:1024 lds\n\t" "s_waitcnt lgkmcnt(4)\n\t" "v_mfma_f32_32x32x16_bf16 v[48:63], a[52:55], v[120:123], v[48:63]\n\t" \
    "ds_read_b128 v[108:111], v82 offset:512\n\t" "s_mov_b32 m0, s63\n\t" "s_add_u32 s63, s63, 0x800\n\t" "buffer_load_dwordx4 v91, %[rx], s61 offen lds\n\t" \
    "s_waitcnt lgkmcnt(4)\n\t" "v_mfma_f32_32x32x16_bf16 v[48:63], a[56:59], v[124:127], v[48:63]\n\t" "ds_read_b128 v[112:115], v83 offset:512\n\t" \
    "buffer_load_dwordx4 v91, %[rx], s61 offen offset:1024 lds\n\t" "s_waitcnt lgkmcnt(4)\n\t" "v_mfma_f32_32x32x16_bf16 v[48:63], a[60:63], v[128:131], v[48:63]\n\t" \
    "ds_read_b128 v[116:119], v84 offset:512\n\t" "s_mov_b32 m0, s63\n\t" "s_add_u32 s63, s63, 0x800\n\t" "buffer_load_dwordx4 v92, %[rx], s61 offen lds\n\t" \
    "s_waitcnt lgkmcnt(4)\n\t" "v_mfma_f32_32x32x16_bf16 v[48:63], a[64:67], v[100:103], v[48:63]\n\t" "ds_read_b128 v[120:123], v85 offset:512\n\t" \
    "buffer_load_dwordx4 v92, %[rx], s61 offen offset:1024 lds\n\t" "s_waitcnt lgkmcnt(4)\n\t" "v_mfma_f32_32x32x16_bf16 v[48:63], a[68:71], v[104:107], v[48:63]\n\t" \
    "ds_read_b128 v[124:127], v86 offset:512\n\t" "s_mov_b32 m0, s63\n\t" "s_add_u32 s63, s63, 0x800\n\t" "buffer_load_dwordx4 v93, %[rx], s61 offen lds\n\t" \
    "s_waitcnt lgkmcnt(4)\n\t" "v_mfma_f32_32x32x16_bf16 v[48:63], a[72:75], v[108:111], v[48:63]\n\t" "ds_read_b128 v[128:131], v87 offset:512\n\t" \
    "buffer_load_dwordx4 v93, %[rx], s61 offen offset:1024 lds\n\t" "s_waitcnt lgkmcnt(4)\n\t" "v_mfma_f32_32x32x16_bf16 v[48:63], a[76:79], v[112:115], v[48:63]\n\t" \
    "ds_read_b128 v[100:103], v80 offset:768\n\t" "s_mov_b32 m0, s63\n\t" "s_add_u32 s63, s63, 0x800\n\t" "buffer_load_dwordx4 v94, %[rx], s61 offen lds\n\t" \
    "s_waitcnt lgkmcnt(4)\n\t" "v_mfma_f32_32x32x16_bf16 v[48:63], a[80:83], v[116:119], v[48:63]\n\t" "ds_read_b128 v[104:107], v81 offset:768\n\t" \
    "buffer_load_dwordx4 v94, %[rx], s61 offen offset:1024 lds\n\t" "s_waitcnt lgkmcnt(4)\n\t" "v_mfma_f32_32x32x16_bf16 v[48:63], a[84:87], v[120:123], v[48:63]\n\t" \
    "ds_read_b128 v[108:111], v82 offset:768\n\t" "s_mov_b32 m0, s63\n\t" "s_add_u32 s63, s63, 0x800\n\t" "buffer_load_dwordx4 v95, %[rx], s61 offen lds\n\t" \
    "s_waitcnt lgkmcnt(4)\n\t" "v_mfma_f32_32x32x16_bf16 v[48:63], a[88:91], v[124:127], v[48:63]\n\t" "ds_read_b128 v[112:115], v83 offset:768\n\t" \
    "buffer_load_dwordx4 v95, %[rx], s61 offen offset:1024 lds\n\t" "s_waitcnt lgkmcnt(4)\n\t" "v_mfma_f32_32x32x16_bf16 v[48:63], a[92:95], v[128:131], v[48:63]\n\t" \
    "ds_read_b128 v[116:119], v84 offset:768\n\t" "s_waitcnt lgkmcnt(4)\n\t" "v_mfma_f32_32x32x16_bf16 v[48:63], a[96:99], v[100:103], v[48:63]\n\t" \
    "ds_read_b128 v[120:123], v85 offset:768\n\t" "s_lshr_b32 s64, s60, 2\n\t" "s_xor_b32 s64, s64, %[xp]\n\t" "s_and_b32 s64, s64, 1\n\t" "s_lshl_b32 s64, s64, 13\n\t" \
    "s_waitcnt lgkmcnt(4)\n\t" "v_mfma_f32_32x32x16_bf16 v[48:63], a[100:103], v[104:107], v[48:63]\n\t" "ds_read_b128 v[124:127], v86 offset:768\n\t" \
    "s_lshl_b32 s62, s60, 19\n\t" "s_add_u32 s62, s62, s64\n\t" "s_add_u32 s62, s62, %[ob]\n\t" "s_waitcnt lgkmcnt(4)\n\t" \
    "v_mfma_f32_32x32x16_bf16 v[48:63], a[104:107], v[108:111], v[48:63]\n\t" "ds_read_b128 v[128:131], v87 offset:768\n\t" "s_waitcnt lgkmcnt(4)\n\t" \
    "v_mfma_f32_32x32x16_bf16 v[48:63], a[108:111], v[112:115], v[48:63]\n\t" "ds_read_b128 v[100:103], v80 offset:1024\n\t" "s_waitcnt lgkmcnt(4)\n\t" \
    "v_mfma_f32_32x32x16_bf16 v[48:63], a[112:115], v[116:119], v[48:63]\n\t" "ds_read_b128 v[104:107], v81 offset:1024\n\t" "s_waitcnt lgkmcnt(4)\n\t" \
    "v_mfma_f32_32x32x16_bf16 v[48:63], a[116:119], v[120:123], v[48:63]\n\t" "ds_read_b128 v[108:111], v82 offset:1024\n\t" "s_waitcnt lgkmcnt(4)\n\t" \
    "v_mfma_f32_32x32x16_bf16 v[48:63], a[120:123], v[124:127], v[48:63]\n\t" "ds_read_b128 v[112:115], v83 offset:1024\n\t" "s_waitcnt lgkmcnt(4)\n\t" \
    "v_mfma_f32_32x32x16_bf16 v[48:63], a[124:127], v[128:131], v[48:63]\n\t" "ds_read_b128 v[116:119], v84 offset:1024\n\t" "s_waitcnt lgkmcnt(4)\n\t" \
    "v_mfma_f32_32x32x16_bf16 v[48:63], a[128:131], v[100:103], v[48:63]\n\t" "ds_read_b128 v[120:123], v85 offset:1024\n\t" "s_waitcnt lgkmcnt(4)\n\t" \
    "v_mfma_f32_32x32x16_bf16 v[48:63], a[132:135], v[104:107], v[48:63]\n\t" "ds_read_b128 v[124:127], v86 offset:1024\n\t" "s_waitcnt lgkmcnt(4)\n\t" \
    "v_mfma_f32_32x32x16_bf16 v[48:63], a[136:139], v[108:111], v[48:63]\n\t" "ds_read_b128 v[128:131], v87 offset:1024\n\t" "s_waitcnt vmcnt(16)\n\t" "s_barrier\n\t" \
    "v_mov_b32 v98, s60\n\t" "buffer_store_dword v98, %[fo], %[rc], %[fl] offen sc1\n\t" "s_waitcnt lgkmcnt(4)\n\t" \
    "v_mfma_f32_32x32x16_bf16 v[48:63], a[140:143], v[112:115], v[48:63]\n\t" "ds_read_b128 v[100:103], v80 offset:1280\n\t" "s_waitcnt lgkmcnt(4)\n\t" \
    "v_mfma_f32_32x32x16_bf16 v[48:63], a[144:147], v[116:119], v[48:63]\n\t" "ds_read_b128 v[104:107], v81 offset:1280\n\t" "s_waitcnt lgkmcnt(4)\n\t" \
    "v_mfma_f32_32x32x16_bf16 v[48:63], a[148:151], v[120:123], v[48:63]\n\t" "ds_read_b128 v[108:111], v82 offset:1280\n\t" "s_waitcnt lgkmcnt(4)\n\t" \
    "v_mfma_f32_32x32x16_bf16 v[48:63], a[152:155], v[124:127], v[48:63]\n\t" "ds_read_b128 v[112:115], v83 offset:1280\n\t" "s_waitcnt lgkmcnt(4)\n\t" \
    "v_mfma_f32_32x32x16_bf16 v[48:63], a[156:159], v[128:131], v[48:63]\n\t" "ds_read_b128 v[116:119], v84 offset:1280\n\t" "s_waitcnt lgkmcnt(4)\n\t" \
    "v_mfma_f32_32x32x16_bf16 v[48:63], a[160:163], v[100:103], v[48:63]\n\t" "ds_read_b128 v[120:123], v85 offset:1280\n\t" "s_waitcnt lgkmcnt(4)\n\t" \
    "v_mfma_f32_32x32x16_bf16 v[48:63], a[164:167], v[104:107], v[48:63]\n\t" "ds_read_b128 v[124:127], v86 offset:1280\n\t" "s_waitcnt lgkmcnt(4)\n\t" \
    "v_mfma_f32_32x32x16_bf16 v[48:63], a[168:171], v[108:111], v[48:63]\n\t" "ds_read_b128 v[128:131], v87 offset:1280\n\t" "s_waitcnt lgkmcnt(4)\n\t" \
    "v_mfma_f32_32x32x16_bf16 v[48:63], a[172:175], v[112:115], v[48:63]\n\t" "ds_read_b128 v[100:103], v80 offset:1536\n\t" "s_waitcnt lgkmcnt(4)\n\t" \
    "v_mfma_f32_32x32x16_bf16 v[48:63], a[176:179], v[116:119], v[48:63]\n\t" "ds_read_b128 v[104:107], v81 offset:1536\n\t" "s_waitcnt lgkmcnt(4)\n\t" \
    "v_mfma_f32_32x32x16_bf16 v[48:63], a[180:183], v[120:123], v[48:63]\n\t" "ds_read_b128 v[108:111], v82 offset:1536\n\t" "s_waitcnt lgkmcnt(4)\n\t" \
    "v_mfma_f32_32x32x16_bf16 v[48:63], a[184:187], v[124:127], v[48:63]\n\t" "ds_read_b128 v[112:115], v83 offset:1536\n\t" "s_waitcnt lgkmcnt(4)\n\t" \
    "v_mfma_f32_32x32x16_bf16 v[48:63], a[188:191], v[128:131], v[48:63]\n\t" "ds_read_b128 v[116:119], v84 offset:1536\n\t" "s_waitcnt lgkmcnt(4)\n\t" \
    "v_mfma_f32_32x32x16_bf16 v[48:63], a[192:195], v[100:103], v[48:63]\n\t" "ds_read_b128 v[120:123], v85 offset:1536\n\t" "s_waitcnt lgkmcnt(4)\n\t" \
    "v_mfma_f32_32x32x16_bf16 v[48:63], a[196:199], v[104:107], v[48:63]\n\t" "ds_read_b128 v[124:127], v86 offset:1536\n\t" "s_waitcnt lgkmcnt(4)\n\t" \
    "v_mfma_f32_32x32x16_bf16 v[48:63], a[200:203], v[108:111], v[48:63]\n\t" "ds_read_b128 v[128:131], v87 offset:1536\n\t" "s_waitcnt lgkmcnt(4)\n\t" \
    "v_mfma_f32_32x32x16_bf16 v[48:63], a[204:207], v[112:115], v[48:63]\n\t" "ds_read_b128 v[100:103], v80 offset:1792\n\t" "s_waitcnt lgkmcnt(4)\n\t" \
    "v_mfma_f32_32x32x16_bf16 v[48:63], a[208:211], v[116:119], v[48:63]\n\t" "ds_read_b128 v[104:107], v81 offset:1792\n\t" "s_waitcnt lgkmcnt(4)\n\t" \
    "v_mfma_f32_32x32x16_bf16 v[48:63], a[212:215], v[120:123], v[48:63]\n\t" "ds_read_b128 v[108:111], v82 offset:1792\n\t" "s_waitcnt lgkmcnt(4)\n\t" \
    "v_mfma_f32_32x32x16_bf16 v[48:63], a[216:219], v[124:127], v[48:63]\n\t" "ds_read_b128 v[112:115], v83 offset:1792\n\t" "s_waitcnt lgkmcnt(4)\n\t" \
    "v_mfma_f32_32x32x16_bf16 v[48:63], a[220:223], v[128:131], v[48:63]\n\t" "ds_read_b128 v[116:119], v84 offset:1792\n\t" "s_waitcnt lgkmcnt(4)\n\t" \
    "v_mfma_f32_32x32x16_bf16 v[48:63], a[224:227], v[100:103], v[48:63]\n\t" "ds_read_b128 v[120:123], v85 offset:1792\n\t" "s_waitcnt lgkmcnt(4)\n\t" \
    "v_mfma_f32_32x32x16_bf16 v[48:63], a[228:231], v[104:107], v[48:63]\n\t" "ds_read_b128 v[124:127], v86 offset:1792\n\t" "s_waitcnt lgkmcnt(4)\n\t" \
    "v_mfma_f32_32x32x16_bf16 v[48:63], a[232:235], v[108:111], v[48:63]\n\t" "ds_read_b128 v[128:131], v87 offset:1792\n\t" "s_waitcnt lgkmcnt(4)\n\t" \
    "v_mfma_f32_32x32x16_bf16 v[48:63], a[236:239], v[112:115], v[48:63]\n\t" "s_waitcnt lgkmcnt(3)\n\t" \
    "v_mfma_f32_32x32x16_bf16 v[48:63], a[240:243], v[116:119], v[48:63]\n\t" "s_waitcnt vmcnt(1) lgkmcnt(0)\n\t" "s_barrier\n\t" XT(82, 83) \
    "v_mfma_f32_32x32x16_bf16 v[48:63], a[244:247], v[120:123], v[48:63]\n\t" "ds_read_b128 v[100:103], v72 offset:0\n\t" \
    "v_mfma_f32_32x32x16_bf16 v[48:63], a[248:251], v[124:127], v[48:63]\n\t" "ds_read_b128 v[104:107], v73 offset:0\n\t" "ds_read_b128 v[108:111], v74 offset:0\n\t" \
    "v_mfma_f32_32x32x16_bf16 v[48:63], a[252:255], v[128:131], v[48:63]\n\t" "ds_read_b128 v[112:115], v75 offset:0\n\t" "ds_read_b128 v[116:119], v76 offset:0\n\t" \
    "s_add_u32 s60, s60, 1\n\t" GSUM "s_cmp_lt_u32 s60, 64\n\t" "s_cbranch_scc1 10b\n\t" "s_nop 11\n\t" "v_cvt_pk_bf16_f32 v64, v48, v49\n\t" \
    "v_cvt_pk_bf16_f32 v65, v50, v51\n\t" "v_cvt_pk_bf16_f32 v66, v52, v53\n\t" "v_cvt_pk_bf16_f32 v67, v54, v55\n\t" "v_cvt_pk_bf16_f32 v68, v56, v57\n\t" \
    "v_cvt_pk_bf16_f32 v69, v58, v59\n\t" "v_cvt_pk_bf16_f32 v70, v60, v61\n\t" "v_cvt_pk_bf16_f32 v71, v62, v63\n\t" \
    "buffer_store_dwordx4 v[64:67], v96, %[rs], s62 offen offset:0 sc1\n\t" "buffer_store_dwordx4 v[68:71], v96, %[rs], s62 offen offset:1024 sc1\n\t" \
    "s_waitcnt vmcnt(0)\n\t" "s_barrier\n\t" "v_mov_b32 v98, s60\n\t" "buffer_store_dword v98, %[fo], %[rc], %[fl] offen sc1\n\t" "s_waitcnt lgkmcnt(0)\n\t" \
    "s_branch 30f\n\t" "20:\n\t" "v_mov_b32 v96, %[so]\n\t" "s_nop 11\n\t" "v_cvt_pk_bf16_f32 v64, v32, v33\n\t" "v_cvt_pk_bf16_f32 v65, v34, v35\n\t" \
    "v_cvt_pk_bf16_f32 v66, v36, v37\n\t" "v_cvt_pk_bf16_f32 v67, v38, v39\n\t" "v_cvt_pk_bf16_f32 v68, v40, v41\n\t" "v_cvt_pk_bf16_f32 v69, v42, v43\n\t" \
    "v_cvt_pk_bf16_f32 v70, v44, v45\n\t" "v_cvt_pk_bf16_f32 v71, v46, v47\n\t" "buffer_store_dwordx4 v[64:67], v96, %[rs], s62 offen offset:0 sc1\n\t" \
    "buffer_store_dwordx4 v[68:71], v96, %[rs], s62 offen offset:1024 sc1\n\t" "s_waitcnt vmcnt(0)\n\t" "s_barrier\n\t" "v_mov_b32 v98, s60\n\t" \
    "buffer_store_dword v98, %[fo], %[rc], %[fl] offen sc1\n\t" "s_branch 11b\n\t" "30:\n\t"
#define ZC_LOOP \
    "s_mov_b32 s60, 0\n\t" "s_mov_b32 s61, 0\n\t" "s_mov_b32 s62, 0\n\t" "s_mov_b32 s63, 0x7f000000\n\t" "s_cmp_ge_u32 s61, %[z0]\n\t" "s_cbranch_scc1 52f\n\t" "51:\n\t" \
    "buffer_load_dword v120, %[fo], %[rc], %[fv] offen sc1\n\t" "buffer_load_dword v121, %[fo], %[rc], %[vd] offen sc1\n\t" "s_waitcnt vmcnt(0)\n\t" \
    "v_readfirstlane_b32 s61, v120\n\t" "v_readfirstlane_b32 s66, v121\n\t" "s_nop 3\n\t" "s_cmp_ge_u32 s61, %[z0]\n\t" "s_cbranch_scc1 52f\n\t" "s_bitcmp1_b32 s66, 1\n\t" \
    "s_cbranch_scc1 90f\n\t" "s_cmp_eq_u32 %[bd], 0\n\t" "s_cbranch_scc1 52f\n\t" "s_sub_u32 %[bd], %[bd], 1\n\t" "s_sleep 2\n\t" "s_branch 51b\n\t" "52:\n\t" \
    "s_lshr_b32 s66, s60, 2\n\t" "s_xor_b32 s66, s66, %[xp]\n\t" "s_and_b32 s66, s66, 1\n\t" "s_lshl_b32 s66, s66, 13\n\t" "s_lshl_b32 s64, s60, 19\n\t" \
    "s_add_u32 s64, s64, s66\n\t" "s_add_u32 s64, s64, %[vb]\n\t" "buffer_load_dwordx4 v[16:19], %[ao], %[rs], s64 offen offset:0 sc1\n\t" \
    "buffer_load_dwordx4 v[20:23], %[ao], %[rs], s64 offen offset:1024 sc1\n\t" "buffer_load_dwordx4 v[24:27], %[ao], %[rs], s64 offen offset:2048 sc1\n\t" \
    "buffer_load_dwordx4 v[28:31], %[ao], %[rs], s64 offen offset:3072 sc1\n\t" "buffer_load_dwordx4 v[32:35], %[a1], %[rs], s64 offen offset:0 sc1\n\t" \
    "buffer_load_dwordx4 v[36:39], %[a1], %[rs], s64 offen offset:1024 sc1\n\t" "buffer_load_dwordx4 v[40:43], %[a1], %[rs], s64 offen offset:2048 sc1\n\t" \
    "buffer_load_dwordx4 v[44:47], %[a1], %[rs], s64 offen offset:3072 sc1\n\t" "s_waitcnt vmcnt(0)\n\t" "10:\n\t" XT(80, 81) "s_add_u32 s66, s62, %[rg]\n\t" \
    "s_cmp_lt_u32 s66, s60\n\t" "s_cbranch_scc1 60f\n\t" "63:\n\t" "v_mfma_f32_32x32x16_bf16 v[80:95], v[16:19], a[0:3], 0\n\t" "s_nop 1\n\t" \
    "v_mfma_f32_32x32x16_bf16 v[80:95], v[20:23], a[4:7], v[80:95]\n\t" "s_nop 1\n\t" "v_mfma_f32_32x32x16_bf16 v[80:95], v[24:27], a[8:11], v[80:95]\n\t" "s_nop 1\n\t" \
    "v_mfma_f32_32x32x16_bf16 v[80:95], v[28:31], a[12:15], v[80:95]\n\t" "v_cvt_pk_bf16_f32 v112, v96, v97\n\t" "v_cvt_pk_bf16_f32 v113, v98, v99\n\t" \
    "v_cvt_pk_bf16_f32 v114, v100, v101\n\t" "v_mfma_f32_32x32x16_bf16 v[80:95], v[32:35], a[16:19], v[80:95]\n\t" "v_cvt_pk_bf16_f32 v115, v102, v103\n\t" \
    "v_cvt_pk_bf16_f32 v116, v104, v105\n\t" "v_cvt_pk_bf16_f32 v117, v106, v107\n\t" "v_mfma_f32_32x32x16_bf16 v[80:95], v[36:39], a[20:23], v[80:95]\n\t" \
    "buffer_store_dwordx4 v[112:115], %[zo], %[rs], s63 offen offset:0 sc1\n\t" "v_cvt_pk_bf16_f32 v118, v108, v109\n\t" "v_cvt_pk_bf16_f32 v119, v110, v111\n\t" \
    "v_mfma_f32_32x32x16_bf16 v[80:95], v[40:43], a[24:27], v[80:95]\n\t" "v_mfma_f32_32x32x16_bf16 v[80:95], v[44:47], a[28:31], v[80:95]\n\t" \
    "buffer_store_dwordx4 v[116:119], %[zo], %[rs], s63 offen offset:512 sc1\n\t" "s_and_b32 s63, s60, %[rm]\n\t" "s_lshl_b32 s63, s63, 22\n\t" \
    "s_add_u32 s63, s63, %[zb]\n\t" "v_mfma_f32_32x32x16_bf16 v[96:111], v[16:19], a[32:35], 0\n\t" "buffer_load_dword v120, %[fo], %[rc], %[fv] offen sc1\n\t" "s_nop 0\n\t" \
    "v_mfma_f32_32x32x16_bf16 v[96:111], v[20:23], a[36:39], v[96:111]\n\t" "buffer_load_dword v121, %[fo], %[rc], %[fs] offen sc1\n\t" "s_nop 0\n\t" \
    "v_mfma_f32_32x32x16_bf16 v[96:111], v[24:27], a[40:43], v[96:111]\n\t" "s_nop 1\n\t" "v_mfma_f32_32x32x16_bf16 v[96:111], v[28:31], a[44:47], v[96:111]\n\t" \
    "v_cvt_pk_bf16_f32 v112, v80, v81\n\t" "v_cvt_pk_bf16_f32 v113, v82, v83\n\t" "v_cvt_pk_bf16_f32 v114, v84, v85\n\t" \
    "v_mfma_f32_32x32x16_bf16 v[96:111], v[32:35], a[48:51], v[96:111]\n\t" "v_cvt_pk_bf16_f32 v115, v86, v87\n\t" "v_cvt_pk_bf16_f32 v116, v88, v89\n\t" \
    "v_cvt_pk_bf16_f32 v117, v90, v91\n\t" "v_mfma_f32_32x32x16_bf16 v[96:111], v[36:39], a[52:55], v[96:111]\n\t" \
    "buffer_store_dwordx4 v[112:115], %[zo], %[rs], s63 offen offset:0 sc1\n\t" "v_cvt_pk_bf16_f32 v118, v92, v93\n\t" "v_cvt_pk_bf16_f32 v119, v94, v95\n\t" \
    "v_mfma_f32_32x32x16_bf16 v[96:111], v[40:43], a[56:59], v[96:111]\n\t" "s_add_u32 s65, s60, 1\n\t" "s_lshr_b32 s66, s65, 2\n\t" "s_xor_b32 s66, s66, %[xp]\n\t" \
    "s_and_b32 s66, s66, 1\n\t" "v_mfma_f32_32x32x16_bf16 v[96:111], v[44:47], a[60:63], v[96:111]\n\t" \
    "buffer_store_dwordx4 v[116:119], %[zo], %[rs], s63 offen offset:512 sc1\n\t" "s_add_u32 s63, s63, 0x20000\n\t" \
    "v_mfma_f32_32x32x16_bf16 v[80:95], v[16:19], a[64:67], 0\n\t" "s_nop 1\n\t" "v_mfma_f32_32x32x16_bf16 v[80:95], v[20:23], a[68:71], v[80:95]\n\t" "s_nop 1\n\t" \
    "v_mfma_f32_32x32x16_bf16 v[80:95], v[24:27], a[72:75], v[80:95]\n\t" "s_nop 1\n\t" "v_mfma_f32_32x32x16_bf16 v[80:95], v[28:31], a[76:79], v[80:95]\n\t" \
    "v_cvt_pk_bf16_f32 v112, v96, v97\n\t" "v_cvt_pk_bf16_f32 v113, v98, v99\n\t" "v_cvt_pk_bf16_f32 v114, v100, v101\n\t" \
    "v_mfma_f32_32x32x16_bf16 v[80:95], v[32:35], a[80:83], v[80:95]\n\t" "v_cvt_pk_bf16_f32 v115, v102, v103\n\t" "v_cvt_pk_bf16_f32 v116, v104, v105\n\t" \
    "v_cvt_pk_bf16_f32 v117, v106, v107\n\t" "v_mfma_f32_32x32x16_bf16 v[80:95], v[36:39], a[84:87], v[80:95]\n\t" \
    "buffer_store_dwordx4 v[112:115], %[zo], %[rs], s63 offen offset:0 sc1\n\t" "v_cvt_pk_bf16_f32 v118, v108, v109\n\t" "v_cvt_pk_bf16_f32 v119, v110, v111\n\t" \
    "v_mfma_f32_32x32x16_bf16 v[80:95], v[40:43], a[88:91], v[80:95]\n\t" "s_lshl_b32 s66, s66, 13\n\t" "s_lshl_b32 s64, s65, 19\n\t" "s_add_u32 s64, s64, s66\n\t" \
    "s_add_u32 s64, s64, %[vb]\n\t" "v_mfma_f32_32x32x16_bf16 v[80:95], v[44:47], a[92:95], v[80:95]\n\t" \
    "buffer_store_dwordx4 v[116:119], %[zo], %[rs], s63 offen offset:512 sc1\n\t" "s_add_u32 s63, s63, 0x20000\n\t" \
    "v_mfma_f32_32x32x16_bf16 v[96:111], v[16:19], a[96:99], 0\n\t" "s_nop 1\n\t" "v_mfma_f32_32x32x16_bf16 v[96:111], v[20:23], a[100:103], v[96:111]\n\t" "s_nop 1\n\t" \
    "v_mfma_f32_32x32x16_bf16 v[96:111], v[24:27], a[104:107], v[96:111]\n\t" "s_nop 1\n\t" "v_mfma_f32_32x32x16_bf16 v[96:111], v[28:31], a[108:111], v[96:111]\n\t" \
    "v_cvt_pk_bf16_f32 v112, v80, v81\n\t" "v_cvt_pk_bf16_f32 v113, v82, v83\n\t" "v_cvt_pk_bf16_f32 v114, v84, v85\n\t" \
    "v_mfma_f32_32x32x16_bf16 v[96:111], v[32:35], a[112:115], v[96:111]\n\t" "v_cvt_pk_bf16_f32 v115, v86, v87\n\t" "v_cvt_pk_bf16_f32 v116, v88, v89\n\t" \
    "v_cvt_pk_bf16_f32 v117, v90, v91\n\t" "v_mfma_f32_32x32x16_bf16 v[96:111], v[36:39], a[116:119], v[96:111]\n\t" \
    "buffer_store_dwordx4 v[112:115], %[zo], %[rs], s63 offen offset:0 sc1\n\t" "v_cvt_pk_bf16_f32 v118, v92, v93\n\t" "v_cvt_pk_bf16_f32 v119, v94, v95\n\t" \
    "v_mfma_f32_32x32x16_bf16 v[96:111], v[40:43], a[120:123], v[96:111]\n\t" "s_waitcnt vmcnt(5)\n\t" "v_readfirstlane_b32 s66, v120\n\t" "v_readfirstlane_b32 s67, v121\n\t" \
    "v_mov_b32 v122, s60\n\t" "buffer_store_dword v122, %[fo], %[rc], %[fz] offen sc1\n\t" "v_mfma_f32_32x32x16_bf16 v[96:111], v[44:47], a[124:127], v[96:111]\n\t" \
    "buffer_store_dwordx4 v[116:119], %[zo], %[rs], s63 offen offset:512 sc1\n\t" "s_add_u32 s63, s63, 0x20000\n\t" \
    "v_mfma_f32_32x32x16_bf16 v[80:95], v[16:19], a[128:131], 0\n\t" "s_max_u32 s61, s61, s66\n\t" "s_max_u32 s62, s62, s67\n\t" "s_cmp_gt_u32 s61, s65\n\t" \
    "s_cselect_b32 s65, 1, 0\n\t" "s_cselect_b32 s64, s64, 0x7f000000\n\t" "v_mfma_f32_32x32x16_bf16 v[80:95], v[20:23], a[132:135], v[80:95]\n\t" \
    "buffer_load_dwordx4 v[48:51], %[ao], %[rs], s64 offen offset:0 sc1\n\t" "s_nop 0\n\t" "v_mfma_f32_32x32x16_bf16 v[80:95], v[24:27], a[136:139], v[80:95]\n\t" \
    "buffer_load_dwordx4 v[52:55], %[ao], %[rs], s64 offen offset:1024 sc1\n\t" "s_nop 0\n\t" "v_mfma_f32_32x32x16_bf16 v[80:95], v[28:31], a[140:143], v[80:95]\n\t" \
    "buffer_load_dwordx4 v[56:59], %[ao], %[rs], s64 offen offset:2048 sc1\n\t" "v_cvt_pk_bf16_f32 v112, v96, v97\n\t" "v_cvt_pk_bf16_f32 v113, v98, v99\n\t" \
    "v_cvt_pk_bf16_f32 v114, v100, v101\n\t" "v_mfma_f32_32x32x16_bf16 v[80:95], v[32:35], a[144:147], v[80:95]\n\t" \
    "buffer_load_dwordx4 v[60:63], %[ao], %[rs], s64 offen offset:3072 sc1\n\t" "v_cvt_pk_bf16_f32 v115, v102, v103\n\t" "v_cvt_pk_bf16_f32 v116, v104, v105\n\t" \
    "v_cvt_pk_bf16_f32 v117, v106, v107\n\t" "v_mfma_f32_32x32x16_bf16 v[80:95], v[36:39], a[148:151], v[80:95]\n\t" \
    "buffer_load_dwordx4 v[64:67], %[a1], %[rs], s64 offen offset:0 sc1\n\t" "buffer_store_dwordx4 v[112:115], %[zo], %[rs], s63 offen offset:0 sc1\n\t" \
    "v_cvt_pk_bf16_f32 v118, v108, v109\n\t" "v_cvt_pk_bf16_f32 v119, v110, v111\n\t" "v_mfma_f32_32x32x16_bf16 v[80:95], v[40:43], a[152:155], v[80:95]\n\t" \
    "buffer_load_dwordx4 v[68:71], %[a1], %[rs], s64 offen offset:1024 sc1\n\t" "v_mfma_f32_32x32x16_bf16 v[80:95], v[44:47], a[156:159], v[80:95]\n\t" \
    "buffer_load_dwordx4 v[72:75], %[a1], %[rs], s64 offen offset:2048 sc1\n\t" "buffer_store_dwordx4 v[116:119], %[zo], %[rs], s63 offen offset:512 sc1\n\t" \
    "s_add_u32 s63, s63, 0x20000\n\t" "v_mfma_f32_32x32x16_bf16 v[96:111], v[16:19], a[160:163], 0\n\t" \
    "buffer_load_dwordx4 v[76:79], %[a1], %[rs], s64 offen offset:3072 sc1\n\t" "s_nop 0\n\t" "v_mfma_f32_32x32x16_bf16 v[96:111], v[20:23], a[164:167], v[96:111]\n\t" \
    "s_nop 1\n\t" "v_mfma_f32_32x32x16_bf16 v[96:111], v[24:27], a[168:171], v[96:111]\n\t" "s_nop 1\n\t" \
    "v_mfma_f32_32x32x16_bf16 v[96:111], v[28:31], a[172:175], v[96:111]\n\t" "v_cvt_pk_bf16_f32 v112, v80, v81\n\t" "v_cvt_pk_bf16_f32 v113, v82, v83\n\t" \
    "v_cvt_pk_bf16_f32 v114, v84, v85\n\t" "v_mfma_f32_32x32x16_bf16 v[96:111], v[32:35], a[176:179], v[96:111]\n\t" "v_cvt_pk_bf16_f32 v115, v86, v87\n\t" \
    "v_cvt_pk_bf16_f32 v116, v88, v89\n\t" "v_cvt_pk_bf16_f32 v117, v90, v91\n\t" "v_mfma_f32_32x32x16_bf16 v[96:111], v[36:39], a[180:183], v[96:111]\n\t" \
    "buffer_store_dwordx4 v[112:115], %[zo], %[rs], s63 offen offset:0 sc1\n\t" "v_cvt_pk_bf16_f32 v118, v92, v93\n\t" "v_cvt_pk_bf16_f32 v119, v94, v95\n\t" \
    "v_mfma_f32_32x32x16_bf16 v[96:111], v[40:43], a[184:187], v[96:111]\n\t" "v_mfma_f32_32x32x16_bf16 v[96:111], v[44:47], a[188:191], v[96:111]\n\t" \
    "buffer_store_dwordx4 v[116:119], %[zo], %[rs], s63 offen offset:512 sc1\n\t" "s_add_u32 s63, s63, 0x20000\n\t" \
    "v_mfma_f32_32x32x16_bf16 v[80:95], v[16:19], a[192:195], 0\n\t" "s_nop 1\n\t" "v_mfma_f32_32x32x16_bf16 v[80:95], v[20:23], a[196:199], v[80:95]\n\t" "s_nop 1\n\t" \
    "v_mfma_f32_32x32x16_bf16 v[80:95], v[24:27], a[200:203], v[80:95]\n\t" "s_nop 1\n\t" "v_mfma_f32_32x32x16_bf16 v[80:95], v[28:31], a[204:207], v[80:95]\n\t" \
    "v_cvt_pk_bf16_f32 v112, v96, v97\n\t" "v_cvt_pk_bf16_f32 v113, v98, v99\n\t" "v_cvt_pk_bf16_f32 v114, v100, v101\n\t" \
    "v_mfma_f32_32x32x16_bf16 v[80:95], v[32:35], a[208:211], v[80:95]\n\t" "v_cvt_pk_bf16_f32 v115, v102, v103\n\t" "v_cvt_pk_bf16_f32 v116, v104, v105\n\t" \
    "v_cvt_pk_bf16_f32 v117, v106, v107\n\t" "v_mfma_f32_32x32x16_bf16 v[80:95], v[36:39], a[212:215], v[80:95]\n\t" \
    "buffer_store_dwordx4 v[112:115], %[zo], %[rs], s63 offen offset:0 sc1\n\t" "v_cvt_pk_bf16_f32 v118, v108, v109\n\t" "v_cvt_pk_bf16_f32 v119, v110, v111\n\t" \
    "v_mfma_f32_32x32x16_bf16 v[80:95], v[40:43], a[216:219], v[80:95]\n\t" "v_mfma_f32_32x32x16_bf16 v[80:95], v[44:47], a[220:223], v[80:95]\n\t" \
    "buffer_store_dwordx4 v[116:119], %[zo], %[rs], s63 offen offset:512 sc1\n\t" "s_add_u32 s63, s63, 0x20000\n\t" \
    "v_mfma_f32_32x32x16_bf16 v[96:111], v[16:19], a[224:227], 0\n\t" "s_nop 1\n\t" "v_mfma_f32_32x32x16_bf16 v[96:111], v[20:23], a[228:231], v[96:111]\n\t" "s_nop 1\n\t" \
    "v_mfma_f32_32x32x16_bf16 v[96:111], v[24:27], a[232:235], v[96:111]\n\t" "s_nop 1\n\t" "v_mfma_f32_32x32x16_bf16 v[96:111], v[28:31], a[236:239], v[96:111]\n\t" \
    "v_cvt_pk_bf16_f32 v112, v80, v81\n\t" "v_cvt_pk_bf16_f32 v113, v82, v83\n\t" "v_cvt_pk_bf16_f32 v114, v84, v85\n\t" \
    "v_mfma_f32_32x32x16_bf16 v[96:111], v[32:35], a[240:243], v[96:111]\n\t" "v_cvt_pk_bf16_f32 v115, v86, v87\n\t" "v_cvt_pk_bf16_f32 v116, v88, v89\n\t" \
    "v_cvt_pk_bf16_f32 v117, v90, v91\n\t" "v_mfma_f32_32x32x16_bf16 v[96:111], v[36:39], a[244:247], v[96:111]\n\t" \
    "buffer_store_dwordx4 v[112:115], %[zo], %[rs], s63 offen offset:0 sc1\n\t" "v_cvt_pk_bf16_f32 v118, v92, v93\n\t" "v_cvt_pk_bf16_f32 v119, v94, v95\n\t" \
    "v_mfma_f32_32x32x16_bf16 v[96:111], v[40:43], a[248:251], v[96:111]\n\t" "v_mfma_f32_32x32x16_bf16 v[96:111], v[44:47], a[252:255], v[96:111]\n\t" \
    "buffer_store_dwordx4 v[116:119], %[zo], %[rs], s63 offen offset:512 sc1\n\t" "s_add_u32 s63, s63, 0x20000\n\t" "s_add_u32 s60, s60, 1\n\t" "s_cmp_eq_u32 s65, 0\n\t" \
    "s_waitcnt vmcnt(6)\n\t" ZSUM "s_cbranch_scc1 20f\n\t" "23:\n\t" XT(80, 81) "s_add_u32 s66, s62, %[rg]\n\t" "s_cmp_lt_u32 s66, s60\n\t" "s_cbranch_scc1 70f\n\t" "73:\n\t" \
    "v_mfma_f32_32x32x16_bf16 v[80:95], v[48:51], a[0:3], 0\n\t" "s_nop 1\n\t" "v_mfma_f32_32x32x16_bf16 v[80:95], v[52:55], a[4:7], v[80:95]\n\t" "s_nop 1\n\t" \
    "v_mfma_f32_32x32x16_bf16 v[80:95], v[56:59], a[8:11], v[80:95]\n\t" "s_nop 1\n\t" "v_mfma_f32_32x32x16_bf16 v[80:95], v[60:63], a[12:15], v[80:95]\n\t" \
    "v_cvt_pk_bf16_f32 v112, v96, v97\n\t" "v_cvt_pk_bf16_f32 v113, v98, v99\n\t" "v_cvt_pk_bf16_f32 v114, v100, v101\n\t" \
    "v_mfma_f32_32x32x16_bf16 v[80:95], v[64:67], a[16:19], v[80:95]\n\t" "v_cvt_pk_bf16_f32 v115, v102, v103\n\t" "v_cvt_pk_bf16_f32 v116, v104, v105\n\t" \
    "v_cvt_pk_bf16_f32 v117, v106, v107\n\t" "v_mfma_f32_32x32x16_bf16 v[80:95], v[68:71], a[20:23], v[80:95]\n\t" \
    "buffer_store_dwordx4 v[112:115], %[zo], %[rs], s63 offen offset:0 sc1\n\t" "v_cvt_pk_bf16_f32 v118, v108, v109\n\t" "v_cvt_pk_bf16_f32 v119, v110, v111\n\t" \
    "v_mfma_f32_32x32x16_bf16 v[80:95], v[72:75], a[24:27], v[80:95]\n\t" "v_mfma_f32_32x32x16_bf16 v[80:95], v[76:79], a[28:31], v[80:95]\n\t" \
    "buffer_store_dwordx4 v[116:119], %[zo], %[rs], s63 offen offset:512 sc1\n\t" "s_and_b32 s63, s60, %[rm]\n\t" "s_lshl_b32 s63, s63, 22\n\t" \
    "s_add_u32 s63, s63, %[zb]\n\t" "v_mfma_f32_32x32x16_bf16 v[96:111], v[48:51], a[32:35], 0\n\t" "buffer_load_dword v120, %[fo], %[rc], %[fv] offen sc1\n\t" "s_nop 0\n\t" \
    "v_mfma_f32_32x32x16_bf16 v[96:111], v[52:55], a[36:39], v[96:111]\n\t" "buffer_load_dword v121, %[fo], %[rc], %[fs] offen sc1\n\t" "s_nop 0\n\t" \
    "v_mfma_f32_32x32x16_bf16 v[96:111], v[56:59], a[40:43], v[96:111]\n\t" "s_nop 1\n\t" "v_mfma_f32_32x32x16_bf16 v[96:111], v[60:63], a[44:47], v[96:111]\n\t" \
    "v_cvt_pk_bf16_f32 v112, v80, v81\n\t" "v_cvt_pk_bf16_f32 v113, v82, v83\n\t" "v_cvt_pk_bf16_f32 v114, v84, v85\n\t" \
    "v_mfma_f32_32x32x16_bf16 v[96:111], v[64:67], a[48:51], v[96:111]\n\t" "v_cvt_pk_bf16_f32 v115, v86, v87\n\t" "v_cvt_pk_bf16_f32 v116, v88, v89\n\t" \
    "v_cvt_pk_bf16_f32 v117, v90, v91\n\t" "v_mfma_f32_32x32x16_bf16 v[96:111], v[68:71], a[52:55], v[96:111]\n\t" \
    "buffer_store_dwordx4 v[112:115], %[zo], %[rs], s63 offen offset:0 sc1\n\t" "v_cvt_pk_bf16_f32 v118, v92, v93\n\t" "v_cvt_pk_bf16_f32 v119, v94, v95\n\t" \
    "v_mfma_f32_32x32x16_bf16 v[96:111], v[72:75], a[56:59], v[96:111]\n\t" "s_add_u32 s65, s60, 1\n\t" "s_lshr_b32 s66, s65, 2\n\t" "s_xor_b32 s66, s66, %[xp]\n\t" \
    "s_and_b32 s66, s66, 1\n\t" "v_mfma_f32_32x32x16_bf16 v[96:111], v[76:79], a[60:63], v[96:111]\n\t" \
    "buffer_store_dwordx4 v[116:119], %[zo], %[rs], s63 offen offset:512 sc1\n\t" "s_add_u32 s63, s63, 0x20000\n\t" \
    "v_mfma_f32_32x32x16_bf16 v[80:95], v[48:51], a[64:67], 0\n\t" "s_nop 1\n\t" "v_mfma_f32_32x32x16_bf16 v[80:95], v[52:55], a[68:71], v[80:95]\n\t" "s_nop 1\n\t" \
    "v_mfma_f32_32x32x16_bf16 v[80:95], v[56:59], a[72:75], v[80:95]\n\t" "s_nop 1\n\t" "v_mfma_f32_32x32x16_bf16 v[80:95], v[60:63], a[76:79], v[80:95]\n\t" \
    "v_cvt_pk_bf16_f32 v112, v96, v97\n\t" "v_cvt_pk_bf16_f32 v113, v98, v99\n\t" "v_cvt_pk_bf16_f32 v114, v100, v101\n\t" \
    "v_mfma_f32_32x32x16_bf16 v[80:95], v[64:67], a[80:83], v[80:95]\n\t" "v_cvt_pk_bf16_f32 v115, v102, v103\n\t" "v_cvt_pk_bf16_f32 v116, v104, v105\n\t" \
    "v_cvt_pk_bf16_f32 v117, v106, v107\n\t" "v_mfma_f32_32x32x16_bf16 v[80:95], v[68:71], a[84:87], v[80:95]\n\t" \
    "buffer_store_dwordx4 v[112:115], %[zo], %[rs], s63 offen offset:0 sc1\n\t" "v_cvt_pk_bf16_f32 v118, v108, v109\n\t" "v_cvt_pk_bf16_f32 v119, v110, v111\n\t" \
    "v_mfma_f32_32x32x16_bf16 v[80:95], v[72:75], a[88:91], v[80:95]\n\t" "s_lshl_b32 s66, s66, 13\n\t" "s_lshl_b32 s64, s65, 19\n\t" "s_add_u32 s64, s64, s66\n\t" \
    "s_add_u32 s64, s64, %[vb]\n\t" "v_mfma_f32_32x32x16_bf16 v[80:95], v[76:79], a[92:95], v[80:95]\n\t" \
    "buffer_store_dwordx4 v[116:119], %[zo], %[rs], s63 offen offset:512 sc1\n\t" "s_add_u32 s63, s63, 0x20000\n\t" \
    "v_mfma_f32_32x32x16_bf16 v[96:111], v[48:51], a[96:99], 0\n\t" "s_nop 1\n\t" "v_mfma_f32_32x32x16_bf16 v[96:111], v[52:55], a[100:103], v[96:111]\n\t" "s_nop 1\n\t" \
    "v_mfma_f32_32x32x16_bf16 v[96:111], v[56:59], a[104:107], v[96:111]\n\t" "s_nop 1\n\t" "v_mfma_f32_32x32x16_bf16 v[96:111], v[60:63], a[108:111], v[96:111]\n\t" \
    "v_cvt_pk_bf16_f32 v112, v80, v81\n\t" "v_cvt_pk_bf16_f32 v113, v82, v83\n\t" "v_cvt_pk_bf16_f32 v114, v84, v85\n\t" \
    "v_mfma_f32_32x32x16_bf16 v[96:111], v[64:67], a[112:115], v[96:111]\n\t" "v_cvt_pk_bf16_f32 v115, v86, v87\n\t" "v_cvt_pk_bf16_f32 v116, v88, v89\n\t" \
    "v_cvt_pk_bf16_f32 v117, v90, v91\n\t" "v_mfma_f32_32x32x16_bf16 v[96:111], v[68:71], a[116:119], v[96:111]\n\t" \
    "buffer_store_dwordx4 v[112:115], %[zo], %[rs], s63 offen offset:0 sc1\n\t" "v_cvt_pk_bf16_f32 v118, v92, v93\n\t" "v_cvt_pk_bf16_f32 v119, v94, v95\n\t" \
    "v_mfma_f32_32x32x16_bf16 v[96:111], v[72:75], a[120:123], v[96:111]\n\t" "s_waitcnt vmcnt(5)\n\t" "v_readfirstlane_b32 s66, v120\n\t" "v_readfirstlane_b32 s67, v121\n\t" \
    "v_mov_b32 v122, s60\n\t" "buffer_store_dword v122, %[fo], %[rc], %[fz] offen sc1\n\t" "v_mfma_f32_32x32x16_bf16 v[96:111], v[76:79], a[124:127], v[96:111]\n\t" \
    "buffer_store_dwordx4 v[116:119], %[zo], %[rs], s63 offen offset:512 sc1\n\t" "s_add_u32 s63, s63, 0x20000\n\t" \
    "v_mfma_f32_32x32x16_bf16 v[80:95], v[48:51], a[128:131], 0\n\t" "s_max_u32 s61, s61, s66\n\t" "s_max_u32 s62, s62, s67\n\t" "s_cmp_gt_u32 s61, s65\n\t" \
    "s_cselect_b32 s65, 1, 0\n\t" "s_cselect_b32 s64, s64, 0x7f000000\n\t" "v_mfma_f32_32x32x16_bf16 v[80:95], v[52:55], a[132:135], v[80:95]\n\t" \
    "buffer_load_dwordx4 v[16:19], %[ao], %[rs], s64 offen offset:0 sc1\n\t" "s_nop 0\n\t" "v_mfma_f32_32x32x16_bf16 v[80:95], v[56:59], a[136:139], v[80:95]\n\t" \
    "buffer_load_dwordx4 v[20:23], %[ao], %[rs], s64 offen offset:1024 sc1\n\t" "s_nop 0\n\t" "v_mfma_f32_32x32x16_bf16 v[80:95], v[60:63], a[140:143], v[80:95]\n\t" \
    "buffer_load_dwordx4 v[24:27], %[ao], %[rs], s64 offen offset:2048 sc1\n\t" "v_cvt_pk_bf16_f32 v112, v96, v97\n\t" "v_cvt_pk_bf16_f32 v113, v98, v99\n\t" \
    "v_cvt_pk_bf16_f32 v114, v100, v101\n\t" "v_mfma_f32_32x32x16_bf16 v[80:95], v[64:67], a[144:147], v[80:95]\n\t" \
    "buffer_load_dwordx4 v[28:31], %[ao], %[rs], s64 offen offset:3072 sc1\n\t" "v_cvt_pk_bf16_f32 v115, v102, v103\n\t" "v_cvt_pk_bf16_f32 v116, v104, v105\n\t" \
    "v_cvt_pk_bf16_f32 v117, v106, v107\n\t" "v_mfma_f32_32x32x16_bf16 v[80:95], v[68:71], a[148:151], v[80:95]\n\t" \
    "buffer_load_dwordx4 v[32:35], %[a1], %[rs], s64 offen offset:0 sc1\n\t" "buffer_store_dwordx4 v[112:115], %[zo], %[rs], s63 offen offset:0 sc1\n\t" \
    "v_cvt_pk_bf16_f32 v118, v108, v109\n\t" "v_cvt_pk_bf16_f32 v119, v110, v111\n\t" "v_mfma_f32_32x32x16_bf16 v[80:95], v[72:75], a[152:155], v[80:95]\n\t" \
    "buffer_load_dwordx4 v[36:39], %[a1], %[rs], s64 offen offset:1024 sc1\n\t" "v_mfma_f32_32x32x16_bf16 v[80:95], v[76:79], a[156:159], v[80:95]\n\t" \
    "buffer_load_dwordx4 v[40:43], %[a1], %[rs], s64 offen offset:2048 sc1\n\t" "buffer_store_dwordx4 v[116:119], %[zo], %[rs], s63 offen offset:512 sc1\n\t" \
    "s_add_u32 s63, s63, 0x20000\n\t" "v_mfma_f32_32x32x16_bf16 v[96:111], v[48:51], a[160:163], 0\n\t" \
    "buffer_load_dwordx4 v[44:47], %[a1], %[rs], s64 offen offset:3072 sc1\n\t" "s_nop 0\n\t" "v_mfma_f32_32x32x16_bf16 v[96:111], v[52:55], a[164:167], v[96:111]\n\t" \
    "s_nop 1\n\t" "v_mfma_f32_32x32x16_bf16 v[96:111], v[56:59], a[168:171], v[96:111]\n\t" "s_nop 1\n\t" \
    "v_mfma_f32_32x32x16_bf16 v[96:111], v[60:63], a[172:175], v[96:111]\n\t" "v_cvt_pk_bf16_f32 v112, v80, v81\n\t" "v_cvt_pk_bf16_f32 v113, v82, v83\n\t" \
    "v_cvt_pk_bf16_f32 v114, v84, v85\n\t" "v_mfma_f32_32x32x16_bf16 v[96:111], v[64:67], a[176:179], v[96:111]\n\t" "v_cvt_pk_bf16_f32 v115, v86, v87\n\t" \
    "v_cvt_pk_bf16_f32 v116, v88, v89\n\t" "v_cvt_pk_bf16_f32 v117, v90, v91\n\t" "v_mfma_f32_32x32x16_bf16 v[96:111], v[68:71], a[180:183], v[96:111]\n\t" \
    "buffer_store_dwordx4 v[112:115], %[zo], %[rs], s63 offen offset:0 sc1\n\t" "v_cvt_pk_bf16_f32 v118, v92, v93\n\t" "v_cvt_pk_bf16_f32 v119, v94, v95\n\t" \
    "v_mfma_f32_32x32x16_bf16 v[96:111], v[72:75], a[184:187], v[96:111]\n\t" "v_mfma_f32_32x32x16_bf16 v[96:111], v[76:79], a[188:191], v[96:111]\n\t" \
    "buffer_store_dwordx4 v[116:119], %[zo], %[rs], s63 offen offset:512 sc1\n\t" "s_add_u32 s63, s63, 0x20000\n\t" \
    "v_mfma_f32_32x32x16_bf16 v[80:95], v[48:51], a[192:195], 0\n\t" "s_nop 1\n\t" "v_mfma_f32_32x32x16_bf16 v[80:95], v[52:55], a[196:199], v[80:95]\n\t" "s_nop 1\n\t" \
    "v_mfma_f32_32x32x16_bf16 v[80:95], v[56:59], a[200:203], v[80:95]\n\t" "s_nop 1\n\t" "v_mfma_f32_32x32x16_bf16 v[80:95], v[60:63], a[204:207], v[80:95]\n\t" \
    "v_cvt_pk_bf16_f32 v112, v96, v97\n\t" "v_cvt_pk_bf16_f32 v113, v98, v99\n\t" "v_cvt_pk_bf16_f32 v114, v100, v101\n\t" \
    "v_mfma_f32_32x32x16_bf16 v[80:95], v[64:67], a[208:211], v[80:95]\n\t" "v_cvt_pk_bf16_f32 v115, v102, v103\n\t" "v_cvt_pk_bf16_f32 v116, v104, v105\n\t" \
    "v_cvt_pk_bf16_f32 v117, v106, v107\n\t" "v_mfma_f32_32x32x16_bf16 v[80:95], v[68:71], a[212:215], v[80:95]\n\t" \
    "buffer_store_dwordx4 v[112:115], %[zo], %[rs], s63 offen offset:0 sc1\n\t" "v_cvt_pk_bf16_f32 v118, v108, v109\n\t" "v_cvt_pk_bf16_f32 v119, v110, v111\n\t" \
    "v_mfma_f32_32x32x16_bf16 v[80:95], v[72:75], a[216:219], v[80:95]\n\t" "v_mfma_f32_32x32x16_bf16 v[80:95], v[76:79], a[220:223], v[80:95]\n\t" \
    "buffer_store_dwordx4 v[116:119], %[zo], %[rs], s63 offen offset:512 sc1\n\t" "s_add_u32 s63, s63, 0x20000\n\t" \
    "v_mfma_f32_32x32x16_bf16 v[96:111], v[48:51], a[224:227], 0\n\t" "s_nop 1\n\t" "v_mfma_f32_32x32x16_bf16 v[96:111], v[52:55], a[228:231], v[96:111]\n\t" "s_nop 1\n\t" \
    "v_mfma_f32_32x32x16_bf16 v[96:111], v[56:59], a[232:235], v[96:111]\n\t" "s_nop 1\n\t" "v_mfma_f32_32x32x16_bf16 v[96:111], v[60:63], a[236:239], v[96:111]\n\t" \
    "v_cvt_pk_bf16_f32 v112, v80, v81\n\t" "v_cvt_pk_bf16_f32 v113, v82, v83\n\t" "v_cvt_pk_bf16_f32 v114, v84, v85\n\t" \
    "v_mfma_f32_32x32x16_bf16 v[96:111], v[64:67], a[240:243], v[96:111]\n\t" "v_cvt_pk_bf16_f32 v115, v86, v87\n\t" "v_cvt_pk_bf16_f32 v116, v88, v89\n\t" \
    "v_cvt_pk_bf16_f32 v117, v90, v91\n\t" "v_mfma_f32_32x32x16_bf16 v[96:111], v[68:71], a[244:247], v[96:111]\n\t" \
    "buffer_store_dwordx4 v[112:115], %[zo], %[rs], s63 offen offset:0 sc1\n\t" "v_cvt_pk_bf16_f32 v118, v92, v93\n\t" "v_cvt_pk_bf16_f32 v119, v94, v95\n\t" \
    "v_mfma_f32_32x32x16_bf16 v[96:111], v[72:75], a[248:251], v[96:111]\n\t" "v_mfma_f32_32x32x16_bf16 v[96:111], v[76:79], a[252:255], v[96:111]\n\t" \
    "buffer_store_dwordx4 v[116:119], %[zo], %[rs], s63 offen offset:512 sc1\n\t" "s_add_u32 s63, s63, 0x20000\n\t" "s_add_u32 s60, s60, 1\n\t" "s_cmp_eq_u32 s65, 0\n\t" \
    "s_waitcnt vmcnt(6)\n\t" ZSUM "s_cbranch_scc1 30f\n\t" "33:\n\t" "s_branch 10b\n\t" "20:\n\t" "s_nop 7\n\t" "v_cvt_pk_bf16_f32 v112, v96, v97\n\t" \
    "v_cvt_pk_bf16_f32 v113, v98, v99\n\t" "v_cvt_pk_bf16_f32 v114, v100, v101\n\t" "v_cvt_pk_bf16_f32 v115, v102, v103\n\t" "v_cvt_pk_bf16_f32 v116, v104, v105\n\t" \
    "v_cvt_pk_bf16_f32 v117, v106, v107\n\t" "v_cvt_pk_bf16_f32 v118, v108, v109\n\t" "v_cvt_pk_bf16_f32 v119, v110, v111\n\t" \
    "buffer_store_dwordx4 v[112:115], %[zo], %[rs], s63 offen offset:0 sc1\n\t" "buffer_store_dwordx4 v[116:119], %[zo], %[rs], s63 offen offset:512 sc1\n\t" \
    "buffer_load_dword v120, %[fo], %[rc], %[fv] offen sc1\n\t" "s_waitcnt vmcnt(0)\n\t" "v_readfirstlane_b32 s61, v120\n\t" "s_lshr_b32 s66, s60, 2\n\t" \
    "s_xor_b32 s66, s66, %[xp]\n\t" "s_and_b32 s66, s66, 1\n\t" "s_lshl_b32 s66, s66, 13\n\t" "s_lshl_b32 s64, s60, 19\n\t" "s_add_u32 s64, s64, s66\n\t" \
    "s_add_u32 s64, s64, %[vb]\n\t" "s_cmp_gt_u32 s61, s60\n\t" "s_cbranch_scc0 24f\n\t" "s_cmp_eq_u32 s60, 64\n\t" "s_cbranch_scc1 24f\n\t" \
    "buffer_load_dwordx4 v[48:51], %[ao], %[rs], s64 offen offset:0 sc1\n\t" "buffer_load_dwordx4 v[52:55], %[ao], %[rs], s64 offen offset:1024 sc1\n\t" \
    "buffer_load_dwordx4 v[56:59], %[ao], %[rs], s64 offen offset:2048 sc1\n\t" "buffer_load_dwordx4 v[60:63], %[ao], %[rs], s64 offen offset:3072 sc1\n\t" \
    "buffer_load_dwordx4 v[64:67], %[a1], %[rs], s64 offen offset:0 sc1\n\t" "buffer_load_dwordx4 v[68:71], %[a1], %[rs], s64 offen offset:1024 sc1\n\t" \
    "buffer_load_dwordx4 v[72:75], %[a1], %[rs], s64 offen offset:2048 sc1\n\t" "buffer_load_dwordx4 v[76:79], %[a1], %[rs], s64 offen offset:3072 sc1\n\t" \
    "v_mov_b32 v122, s60\n\t" "buffer_store_dword v122, %[fo], %[rc], %[fz] offen sc1\n\t" "s_waitcnt vmcnt(1)\n\t" "s_branch 23b\n\t" "24:\n\t" "v_mov_b32 v122, s60\n\t" \
    "buffer_store_dword v122, %[fo], %[rc], %[fz] offen sc1\n\t" "s_cmp_eq_u32 s60, 64\n\t" "s_cbranch_scc1 90f\n\t" "s_add_u32 s67, s60, 1\n\t" "s_cmp_ge_u32 s61, s67\n\t" \
    "s_cbranch_scc1 22f\n\t" "21:\n\t" "buffer_load_dword v120, %[fo], %[rc], %[fv] offen sc1\n\t" "buffer_load_dword v121, %[fo], %[rc], %[vd] offen sc1\n\t" \
    "s_waitcnt vmcnt(0)\n\t" "v_readfirstlane_b32 s61, v120\n\t" "v_readfirstlane_b32 s66, v121\n\t" "s_nop 3\n\t" "s_cmp_ge_u32 s61, s67\n\t" "s_cbranch_scc1 22f\n\t" \
    "s_bitcmp1_b32 s66, 1\n\t" "s_cbranch_scc1 90f\n\t" "s_cmp_eq_u32 %[bd], 0\n\t" "s_cbranch_scc1 22f\n\t" "s_sub_u32 %[bd], %[bd], 1\n\t" "s_sleep 2\n\t" \
    "s_branch 21b\n\t" "22:\n\t" "s_lshr_b32 s66, s60, 2\n\t" "s_xor_b32 s66, s66, %[xp]\n\t" "s_and_b32 s66, s66, 1\n\t" "s_lshl_b32 s66, s66, 13\n\t" \
    "s_lshl_b32 s64, s60, 19\n\t" "s_add_u32 s64, s64, s66\n\t" "s_add_u32 s64, s64, %[vb]\n\t" "buffer_load_dwordx4 v[48:51], %[ao], %[rs], s64 offen offset:0 sc1\n\t" \
    "buffer_load_dwordx4 v[52:55], %[ao], %[rs], s64 offen offset:1024 sc1\n\t" "buffer_load_dwordx4 v[56:59], %[ao], %[rs], s64 offen offset:2048 sc1\n\t" \
    "buffer_load_dwordx4 v[60:63], %[ao], %[rs], s64 offen offset:3072 sc1\n\t" "buffer_load_dwordx4 v[64:67], %[a1], %[rs], s64 offen offset:0 sc1\n\t" \
    "buffer_load_dwordx4 v[68:71], %[a1], %[rs], s64 offen offset:1024 sc1\n\t" "buffer_load_dwordx4 v[72:75], %[a1], %[rs], s64 offen offset:2048 sc1\n\t" \
    "buffer_load_dwordx4 v[76:79], %[a1], %[rs], s64 offen offset:3072 sc1\n\t" "s_waitcnt vmcnt(0)\n\t" "s_branch 23b\n\t" "60:\n\t" "s_sub_u32 s67, s60, %[rg]\n\t" \
    "s_cmp_ge_u32 s62, s67\n\t" "s_cbranch_scc1 62f\n\t" "61:\n\t" "buffer_load_dword v120, %[fo], %[rc], %[fs] offen sc1\n\t" \
    "buffer_load_dword v121, %[fo], %[rc], %[vd] offen sc1\n\t" "s_waitcnt vmcnt(0)\n\t" "v_readfirstlane_b32 s62, v120\n\t" "v_readfirstlane_b32 s66, v121\n\t" "s_nop 3\n\t" \
    "s_cmp_ge_u32 s62, s67\n\t" "s_cbranch_scc1 62f\n\t" "s_bitcmp1_b32 s66, 1\n\t" "s_cbranch_scc1 90f\n\t" "s_cmp_eq_u32 %[bd], 0\n\t" "s_cbranch_scc1 62f\n\t" \
    "s_sub_u32 %[bd], %[bd], 1\n\t" "s_sleep 2\n\t" "s_branch 61b\n\t" "62:\n\t" "s_branch 63b\n\t" "30:\n\t" "s_nop 7\n\t" "v_cvt_pk_bf16_f32 v112, v96, v97\n\t" \
    "v_cvt_pk_bf16_f32 v113, v98, v99\n\t" "v_cvt_pk_bf16_f32 v114, v100, v101\n\t" "v_cvt_pk_bf16_f32 v115, v102, v103\n\t" "v_cvt_pk_bf16_f32 v116, v104, v105\n\t" \
    "v_cvt_pk_bf16_f32 v117, v106, v107\n\t" "v_cvt_pk_bf16_f32 v118, v108, v109\n\t" "v_cvt_pk_bf16_f32 v119, v110, v111\n\t" \
    "buffer_store_dwordx4 v[112:115], %[zo], %[rs], s63 offen offset:0 sc1\n\t" "buffer_store_dwordx4 v[116:119], %[zo], %[rs], s63 offen offset:512 sc1\n\t" \
    "buffer_load_dword v120, %[fo], %[rc], %[fv] offen sc1\n\t" "s_waitcnt vmcnt(0)\n\t" "v_readfirstlane_b32 s61, v120\n\t" "s_lshr_b32 s66, s60, 2\n\t" \
    "s_xor_b32 s66, s66, %[xp]\n\t" "s_and_b32 s66, s66, 1\n\t" "s_lshl_b32 s66, s66, 13\n\t" "s_lshl_b32 s64, s60, 19\n\t" "s_add_u32 s64, s64, s66\n\t" \
    "s_add_u32 s64, s64, %[vb]\n\t" "s_cmp_gt_u32 s61, s60\n\t" "s_cbranch_scc0 34f\n\t" "s_cmp_eq_u32 s60, 64\n\t" "s_cbranch_scc1 34f\n\t" \
    "buffer_load_dwordx4 v[16:19], %[ao], %[rs], s64 offen offset:0 sc1\n\t" "buffer_load_dwordx4 v[20:23], %[ao], %[rs], s64 offen offset:1024 sc1\n\t" \
    "buffer_load_dwordx4 v[24:27], %[ao], %[rs], s64 offen offset:2048 sc1\n\t" "buffer_load_dwordx4 v[28:31], %[ao], %[rs], s64 offen offset:3072 sc1\n\t" \
    "buffer_load_dwordx4 v[32:35], %[a1], %[rs], s64 offen offset:0 sc1\n\t" "buffer_load_dwordx4 v[36:39], %[a1], %[rs], s64 offen offset:1024 sc1\n\t" \
    "buffer_load_dwordx4 v[40:43], %[a1], %[rs], s64 offen offset:2048 sc1\n\t" "buffer_load_dwordx4 v[44:47], %[a1], %[rs], s64 offen offset:3072 sc1\n\t" \
    "v_mov_b32 v122, s60\n\t" "buffer_store_dword v122, %[fo], %[rc], %[fz] offen sc1\n\t" "s_waitcnt vmcnt(1)\n\t" "s_branch 33b\n\t" "34:\n\t" "v_mov_b32 v122, s60\n\t" \
    "buffer_store_dword v122, %[fo], %[rc], %[fz] offen sc1\n\t" "s_cmp_eq_u32 s60, 64\n\t" "s_cbranch_scc1 90f\n\t" "s_add_u32 s67, s60, 1\n\t" "s_cmp_ge_u32 s61, s67\n\t" \
    "s_cbranch_scc1 32f\n\t" "31:\n\t" "buffer_load_dword v120, %[fo], %[rc], %[fv] offen sc1\n\t" "buffer_load_dword v121, %[fo], %[rc], %[vd] offen sc1\n\t" \
    "s_waitcnt vmcnt(0)\n\t" "v_readfirstlane_b32 s61, v120\n\t" "v_readfirstlane_b32 s66, v121\n\t" "s_nop 3\n\t" "s_cmp_ge_u32 s61, s67\n\t" "s_cbranch_scc1 32f\n\t" \
    "s_bitcmp1_b32 s66, 1\n\t" "s_cbranch_scc1 90f\n\t" "s_cmp_eq_u32 %[bd], 0\n\t" "s_cbranch_scc1 32f\n\t" "s_sub_u32 %[bd], %[bd], 1\n\t" "s_sleep 2\n\t" \
    "s_branch 31b\n\t" "32:\n\t" "s_lshr_b32 s66, s60, 2\n\t" "s_xor_b32 s66, s66, %[xp]\n\t" "s_and_b32 s66, s66, 1\n\t" "s_lshl_b32 s66, s66, 13\n\t" \
    "s_lshl_b32 s64, s60, 19\n\t" "s_add_u32 s64, s64, s66\n\t" "s_add_u32 s64, s64, %[vb]\n\t" "buffer_load_dwordx4 v[16:19], %[ao], %[rs], s64 offen offset:0 sc1\n\t" \
    "buffer_load_dwordx4 v[20:23], %[ao], %[rs], s64 offen offset:1024 sc1\n\t" "buffer_load_dwordx4 v[24:27], %[ao], %[rs], s64 offen offset:2048 sc1\n\t" \
    "buffer_load_dwordx4 v[28:31], %[ao], %[rs], s64 offen offset:3072 sc1\n\t" "buffer_load_dwordx4 v[32:35], %[a1], %[rs], s64 offen offset:0 sc1\n\t" \
    "buffer_load_dwordx4 v[36:39], %[a1], %[rs], s64 offen offset:1024 sc1\n\t" "buffer_load_dwordx4 v[40:43], %[a1], %[rs], s64 offen offset:2048 sc1\n\t" \
    "buffer_load_dwordx4 v[44:47], %[a1], %[rs], s64 offen offset:3072 sc1\n\t" "s_waitcnt vmcnt(0)\n\t" "s_branch 33b\n\t" "70:\n\t" "s_sub_u32 s67, s60, %[rg]\n\t" \
    "s_cmp_ge_u32 s62, s67\n\t" "s_cbranch_scc1 72f\n\t" "71:\n\t" "buffer_load_dword v120, %[fo], %[rc], %[fs] offen sc1\n\t" \
    "buffer_load_dword v121, %[fo], %[rc], %[vd] offen sc1\n\t" "s_waitcnt vmcnt(0)\n\t" "v_readfirstlane_b32 s62, v120\n\t" "v_readfirstlane_b32 s66, v121\n\t" "s_nop 3\n\t" \
    "s_cmp_ge_u32 s62, s67\n\t" "s_cbranch_scc1 72f\n\t" "s_bitcmp1_b32 s66, 1\n\t" "s_cbranch_scc1 90f\n\t" "s_cmp_eq_u32 %[bd], 0\n\t" "s_cbranch_scc1 72f\n\t" \
    "s_sub_u32 %[bd], %[bd], 1\n\t" "s_sleep 2\n\t" "s_branch 71b\n\t" "72:\n\t" "s_branch 73b\n\t" "90:\n\t"

#define T_DQ 1480
#define T_DV 1540
#define T_M1 150
#define T_DY 700
#define T_DX 1250
#define T_MIN 35
#define T_PEN 600
#define T_RETRY 1100
#define T_POLL 3
#define T_RST 12
#define T_U0 540
constexpr int T_OVH = 118;
constexpr unsigned NS(int ns) { return (unsigned)(ns * 64 / 10); }
struct Clk { unsigned cyc, g, k1, k2; };
DEV void anchor(Clk& k) {
  unsigned long long c, r[8];
  asm volatile("s_memtime %0\n\ts_memrealtime %1\n\ts_nop 2\n\ts_memrealtime %2\n\ts_nop 2\n\ts_memrealtime %3\n\ts_nop 2\n\ts_memrealtime %4\n\ts_nop 2\n\t"
               "s_memrealtime %5\n\ts_nop 2\n\ts_memrealtime %6\n\ts_nop 2\n\ts_memrealtime %7\n\ts_waitcnt lgkmcnt(0)"
               : "=s"(c), "=s"(r[0]), "=s"(r[1]), "=s"(r[2]), "=s"(r[3]), "=s"(r[4]), "=s"(r[5]), "=s"(r[6]) :: "memory");
  const unsigned a = (unsigned)r[0];
  unsigned n = 0;
#pragma unroll
  for (int i = 1; i < 7; ++i) asm("s_cmp_eq_u32 %1, %2\n\ts_addc_u32 %0, %0, 0" : "+s"(n) : "s"((unsigned)r[i]), "s"(a) : "scc");
  const unsigned nx = (a + 4u) << 6, cy = (unsigned)c + 12u + 16u * n;
  const float dc = (float)(cy - k.cyc), dg = (float)(nx - k.g);
  asm volatile("s_nop 0\n\tv_readfirstlane_b32 %0, %2\n\tv_readfirstlane_b32 %1, %3\n\ts_nop 3\n\ts_min_u32 %0, %0, 0x80000\n\ts_min_u32 %1, %1, 0x8000"
               : "=&s"(k.k1), "=&s"(k.k2) : "v"((unsigned)(dg * __builtin_amdgcn_rcpf(dc) * 65536.f)), "v"((unsigned)(dc * __builtin_amdgcn_rcpf(dg) * 65536.f)) : "scc");
  k.cyc = cy; k.g = nx;
}
DEV unsigned to_cyc(const Clk& k, unsigned g) {
  unsigned d;
  asm("s_max_i32 %0, %1, 0xffff8000\n\ts_min_i32 %0, %0, 0x8000\n\ts_mul_i32 %0, %0, %2\n\ts_ashr_i32 %0, %0, 16" : "=&s"(d) : "s"(g - k.g), "s"(k.k2) : "scc");
  return k.cyc + d - T_OVH;
}
DEV void wait_cyc(unsigned t) { asm volatile(TIMED_WAIT :: "s"(t) : CL_S); }
DEV bool slow_head(const u16* q, int h) { return ((((unsigned)(size_t)q >> 8) + h) & 3) == 1; }
DEV void hold(const Ctx& c, int ticks) {
#pragma unroll 1
  while ((unsigned)__builtin_amdgcn_s_memrealtime() - c.t0 < (unsigned)ticks) __builtin_amdgcn_s_sleep(4);
}
DEV int u_slot(int n, int par, int para) { return n == 2 ? R_U2 + par * 8192 : R_U + n * MB + para * 8192; }
DEV unsigned ask_stamps(Ctx& c, int par, int slot) { return __builtin_amdgcn_raw_buffer_load_b32(c.rs, amap(24576, par) + fresh_lane() * 128, slot, AUX_SC1); }
DEV unsigned latest(Ctx& c, const Clk& k, int par, int slot, unsigned v) {
  if (__builtin_expect(!allok(v), 0)) {
#pragma unroll 1
    do {
      __builtin_amdgcn_s_sleep(T_POLL);
      asm volatile("" ::: "memory");
      const unsigned t = __builtin_amdgcn_raw_buffer_load_b32(c.rs, v == SENT ? amap(24576, par) + fresh_lane() * 128 : 1 << 30, slot, AUX_SC1);
      v = v == SENT ? t : v;
    } while (!allok(v) && !spend(c));
  }
  int d = (int)(v - k.g);
  d = max(d, __builtin_amdgcn_update_dpp(d, d, 0x128, 0xf, 0xf, true));
  d = max(d, __builtin_amdgcn_update_dpp(d, d, 0x124, 0xf, 0xf, true));
  d = max(d, __builtin_amdgcn_update_dpp(d, d, 0x122, 0xf, 0xf, true));
  d = max(d, __builtin_amdgcn_update_dpp(d, d, 0x121, 0xf, 0xf, true));
  unsigned m;
  asm("s_max_i32 %0, %1, %2\n\ts_max_i32 %0, %0, %3\n\ts_max_i32 %0, %0, %4" : "=&s"(m)
      : "s"(__builtin_amdgcn_readlane(d, 0)), "s"(__builtin_amdgcn_readlane(d, 16)), "s"(__builtin_amdgcn_readlane(d, 32)), "s"(__builtin_amdgcn_readlane(d, 48)) : "scc");
  return k.g + m;
}
struct Plan { unsigned gx, dx; };
DEV unsigned smax(unsigned a, unsigned b) { return (int)(a - b) > 0 ? a : b; }
DEV unsigned second_try(const Plan& t, unsigned last) { return smax(last + NS(T_PEN), t.gx + t.dx + NS(T_M1 + T_RETRY)); }
DEV void advance(Plan& t, unsigned last) {
  const unsigned gy = t.gx + t.dx + NS(T_M1), dur = last - t.gx;
  const bool missed = (int)(last + NS(T_MIN) - gy) > 0;
  const unsigned nx = (missed ? second_try(t, last) : gy) + NS(T_DY);
  t.dx = dur < t.dx + NS(60) ? (3u * t.dx + dur) >> 2 : t.dx + NS(8);
  t.gx = nx;
}
DEV unsigned next_go(Ctx& c, Clk& k, Plan& t, int par, int slot, unsigned v) {
  SCHED();
  anchor(k);
  SCHED();
  advance(t, latest(c, k, par, slot, v));
  return to_cyc(k, t.gx);
}

DEV constexpr int qk_r(int j) { return j < 4 ? 0 : 1 + (j - 4) % 3; }
DEV constexpr int qk_i(int j) { return j < 4 ? j : (j - 4) / 3; }
DEV constexpr int qk_upto(int u) { return u < 6 ? u + 7 : u < 10 ? 12 + (u - 4) / 2 : u < 12 ? 15 : 16; }
DEV void qk_role(const Params& p, Ctx& c, int h) {
  constexpr int WL = 0, MEMS = 131072, KXL = 147456, QKX = 155648, QROW = 272;
  c.budget = SPIN_CAP;
  const int w = c.w;
  Clk k = {0u, 0u, 0u, 0u};
  anchor(k);
  v8u wa[32], wh[16];
  claim(c, 0);
  hold(c, QK_AT - (slow_head(p.Wq, h) ? SLOW_LEAD : 0));
  {
    const int lane = fresh_lane(), g = lane >> 4, cc = lane & 15;
    const rsrc_t rq = mkrs(p.Wq, 1024 * 4096 * 2), rk = mkrs(p.Wk, 1024 * 4096 * 2);
    const int vo0 = opq((256 * w + 64 * (h & 1) + krow_lane(g)) * 8192 + (h * 128 + 8 * cc) * 2), vo1 = opq(vo0 ^ (64 * 8192));
    v4u row[16][8];
    auto request = [&](int u) {
#pragma unroll
      for (int r = 0; r < 8; ++r) row[u][r] = ld16w(u < 8 ? rq : rk, (u & 2) ? vo1 : vo0, (32 * (u & 4) + krow(4 * (u & 1) + (r >> 1), r & 1)) * 8192);
    };
#pragma unroll
    for (int u = 0; u < qk_upto(-1); ++u) request(u);
    SCHED();
    const int sw = MEMS + w * 8192 + g * 256, lw = opq(WL + w * 32768 + lane * 16);
    int sr[2];
#pragma unroll
    for (int t = 0; t < 2; ++t) { const int i = (2 * cc + t) & 7; sr[t] = opq(i * 1024 + g * 256 + ((4 * w + (cc >> 2) + 2 * i) & 15) * 16); }
    sfor<16>([&](auto U) __attribute__((always_inline)) {
      constexpr int u = U.value;
#pragma unroll
      for (int i = 0; i < 8; ++i) {
        v4u f;
#pragma unroll
        for (int r = 0; r < 4; ++r) f[r] = prm(row[u][2 * r + 1][i >> 1], row[u][2 * r][i >> 1], i & 1);
        sts16(c.L, sw + i * 1024 + ((cc + 2 * i) & 15) * 16, f);
      }
      SCHED();
      if (qk_upto(u) > qk_upto(u - 1)) request(qk_upto(u - 1));
      BAR();
      v4u in[4][2];
#pragma unroll
      for (int t = 1; t >= 0; --t)
#pragma unroll
        for (int r = 0; r < 4; ++r) in[r][t] = lds16(c.L, sr[t] + MEMS + ((w + r) & 3) * 8192);
      SCHED();
#pragma unroll
      for (int j = 0; j < 16; ++j) {
        if (qk_i(j) != ((u >> 1) & 3)) continue;
#pragma unroll
        for (int t = 1; t >= 0; --t) {
          const v4u o = in[qk_r(j)][t];
          if (u >= 8 && t == 1) sts16(c.L, lw + j * 2048 + (u & 1) * 1024, o);
          else {
            v8u& d = u < 8 ? wa[2 * j + t] : wh[j];
#pragma unroll
            for (int r = 0; r < 4; ++r) d[4 * (u & 1) + r] = o[r];
#define M_(r) asm volatile("" : "+" r(d))
            if (u & 1) { if (u < 8) AT_ACC(M_, 2 * j + t); else AT_HOME(M_, j); }
#undef M_
          }
        }
      }
      BAR();
    });
  }
  STAMP(0);
  BAR();
  const unsigned vote = wait_verdict(c);
  const int par = vote_b(vote), para = vote_a(vote);
  const int lane = fresh_lane(), g = lane >> 4, cc = lane & 15;
  const int moff = opq(amap(w * 4096 + lane * 16, par) + (h & 1) * 1024);
  const int idx = opq((lane & 1) ? 0xEEEE : 0x4444);
  TICK_DECL;
  const int l0 = opq(WL + w * 32768 + lane * 16), l1 = opq(MEMS + lane * 16);
  const int qw = opq(QKX + (2 * w + (cc >> 3)) * 256 + g * 64 + (cc & 1) * 16 + ((cc >> 1) & 3) * 4), kw = opq(QKX + (8 + 2 * g) * QROW + (32 * w + 2 * cc) * 2);
#define QK_PINS PIN_ACC0(wa); PIN_ACC1(wa); PIN_HOME(wh)
#define QK_IN [l0] "v"(l0), [l1] "v"(l1), [mo] "v"(moff), [ix] "v"(idx), [qw] "v"(qw), [kw] "v"(kw), [rs] "s"(c.rs), [sb] "s"(R_MEM + (s & 3) * MEM_SLOT)
#define QK_CL CL_S, CL_16_32, CL_160_200, CL_200_216, CL_216_256
  if (w == 3) {
    const int hq = opq(lane ? 1 << 30 : amap(24576 + h * 128, par));
    anchor(k);
    unsigned tg = k.cyc;
    Plan tt = {k.g - NS(100000), NS(T_DX)};
    for (int s = 0; s < NSEG; ++s) {
      const int pb = R_P + (s & 3) * P_SLOT;
      QK_PINS;
      asm volatile(QK_STEP3 : [bd] "+s"(c.budget) : QK_IN, [hq] "v"(hq), [pb] "s"(pb), [tg] "s"(tg), [ca] "s"(k.cyc - T_DQ), [k1] "s"(k.k1), [ga] "s"(k.g) : QK_CL);
      QK_PINS;
      tg = next_go(c, k, tt, par, pb, ask_stamps(c, par, pb));
      st4s(c.rs, hq, R_P + ((s + 2) & 3) * P_SLOT, fresh_sent());
      st4s(mkrs((const void*)c.ctrl, CTRL_BYTES), hq & (1 << 30), C_FLAG + (F_S + h) * 128, (unsigned)s + 1u);
      TICK_SUM(10);
    }
  } else {
    const int kx0 = opq(KXL + g * 512 + cc * 16), kmrd = opq(QKX + g * 16 + (8 + (cc & 7)) * QROW);
    unsigned seen = NOFLAG;
    if (w == 0) {
      const int prow = (2 * g + (cc & 1)) * 2 + ((cc >> 1) & 1);
      const int px = opq(amap((h >> 1) * 1024 + ((2 * (h & 1) + (cc >> 3)) * 16 + prow) * 16 + ((cc >> 2) & 1) * 8, par));
      const int pm = opq(cc < 8 ? amap(16384 + h * 128 + prow * 8 + (cc >> 2) * 4, par) : 1 << 30);
      const int ps = opq((cc & 1) ? 0x03020706 : 0x05040100);
      for (int s = 0; s < NSEG; ++s) {
        if (seen < (unsigned)s + 1u) seen = wait_flag(c, F_K + h, s + 1);
        QK_PINS;
        asm volatile(QK_STEP0 : [bd] "+s"(c.budget)
                     : QK_IN, [kx] "v"(kx0), [km] "v"(kmrd), [px] "v"(px), [pm] "v"(pm), [ps] "v"(ps), [pb] "s"(R_P + (s & 3) * P_SLOT), [pr] "s"(R_P + ((s + 1) & 3) * P_SLOT),
                       [kb] "s"(R_KX - MEMS + xblk(s, h, para)) : QK_CL);
        QK_PINS;
        TICK_SUM(10);
      }
    } else {
      const int qt = w - 1;
      const int px = opq(amap(qt ? 16384 + h * 1024 + lane * 16 : h * 512 + (g >> 1) * 256 + cc * 16 + (g & 1) * 8, para));
      const int pm = opq(g < 2 ? amap(49152 + (qt * 32 + h) * 256 + cc * 16 + g * 8, para) : 1 << 30);
      const int ps = opq(cc - 4 * g), hq = opq(g * 512 + (16 * qt + cc) * 16);
#define QK_TOK(STEP) for (int s = 0; s < NSEG; ++s) { QK_PINS; \
        asm volatile(STEP : [bd] "+s"(c.budget), [sq] "+s"(seen) \
                     : QK_IN, [kx] "v"(kx0), [km] "v"(kmrd), [px] "v"(px), [pm] "v"(pm), [ps] "v"(ps), [hq] "v"(hq), [qb] "s"(R_QX + xblk(s, h, para)), [tb] "s"(R_PT + (s & 7) * PT_SLOT), \
                       [tr] "s"(R_PT + ((s + 3) & 7) * PT_SLOT), [sn] "s"(s + 1), [fq] "s"(c.ctrl + (C_FLAG + (F_Q + h) * 128) / 4) : QK_CL); \
        QK_PINS; TICK_SUM(10); }
      if (w == 1) { QK_TOK(QK_STEP1) } else { QK_TOK(QK_STEP2) }
    }
  }
  TICK_OUT();
  if (c.budget == 0u) __hip_atomic_store(c.ctrl + C_TMO / 4, 1u, RLX_AGENT);
}

DEV void shuffle_cols(Ctx& c, int scratch, const v4u (&rows)[8], v4u (&o)[8]) {
  const int g = c.lane >> 4, cc = c.lane & 15;
#pragma unroll
  for (int i = 0; i < 8; ++i) {
    v4u f;
#pragma unroll
    for (int r = 0; r < 4; ++r) f[r] = prm(rows[2 * r + 1][i >> 1], rows[2 * r][i >> 1], i & 1);
    sts16(c.L, scratch + (8 * cc + i) * 64 + ((g ^ (cc & 3)) << 4), f);
  }
  asm volatile("s_waitcnt lgkmcnt(0)" ::: "memory");
#pragma unroll
  for (int i = 0; i < 8; ++i) { const int col = 16 * i + cc; o[i] = lds16(c.L, scratch + col * 64 + ((g ^ ((col >> 3) & 3)) << 4)); }
  asm volatile("s_waitcnt lgkmcnt(0)" ::: "memory");
}

DEV void vo_role(const Params& p, Ctx& c, int h) {
  constexpr int WL = 0, MEMS = 131072, VM = 147456, VROW = 272;
  c.budget = SPIN_CAP;
  const int w = c.w, lane = c.lane = fresh_lane(), g = lane >> 4, cc = lane & 15;
  Clk k = {0u, 0u, 0u, 0u};
  anchor(k);
  v8u wa[32], wh[16];
  claim(c, 0);
  hold(c, VO_AT);
  const rsrc_t rwo = mkrs(p.Wo, 4096 * 1024 * 2);
  const int voo = (h * 128 + 4 * g) * 2048 + (256 * w + 8 * cc) * 2;
  v4u buf[3][8];
  auto request_o = [&](int u) {
#pragma unroll
    for (int r = 0; r < 8; ++r) buf[u % 3][r] = ld16w(rwo, voo, (32 * (u & 3) + 16 * (r >> 2) + (r & 3)) * 2048 + (u >> 2) * 256);
  };
  {
    const rsrc_t rw = mkrs(p.Wv, 1024 * 4096 * 2);
    const int vo = (256 * w + krow_lane(g)) * 8192 + (h * 128 + 8 * cc) * 2;
    v4u vrow[8][8];
    auto request = [&](int u) {
#pragma unroll
      for (int r = 0; r < 8; ++r) vrow[u][r] = ld16w(rw, vo, (64 * ((u >> 1) ^ (h & 1)) + krow(4 * (u & 1) + (r >> 1), r & 1)) * 8192);
    };
#pragma unroll
    for (int u = 0; u < 5; ++u) request(u);
    SCHED();
    const int wr_ = MEMS + w * 8192 + g * 256;
    int sr[2];
#pragma unroll
    for (int t = 0; t < 2; ++t) { const int i = (2 * cc + t) & 7; sr[t] = opq(i * 1024 + g * 256 + ((4 * w + (cc >> 2) + 2 * i) & 15) * 16); }
#pragma unroll
    for (int u = 0; u < 8; ++u) {
#pragma unroll
      for (int i = 0; i < 8; ++i) {
        v4u f;
#pragma unroll
        for (int r = 0; r < 4; ++r) f[r] = prm(vrow[u][2 * r + 1][i >> 1], vrow[u][2 * r][i >> 1], i & 1);
        sts16(c.L, wr_ + i * 1024 + ((cc + 2 * i) & 15) * 16, f);
      }
      SCHED();
      if (u + 5 < 8) request(u + 5); else if (u < 6) request_o(u - 3);
      BAR();
      v4u in[4][2];
#pragma unroll
      for (int t = 1; t >= 0; --t)
#pragma unroll
        for (int r = 0; r < 4; ++r) in[r][t] = lds16(c.L, sr[t] + MEMS + ((w + r) & 3) * 8192);
      SCHED();
#pragma unroll
      for (int j = 0; j < 16; ++j) {
        if (qk_i(j) != (u >> 1)) continue;
#pragma unroll
        for (int t = 1; t >= 0; --t) {
          const v4u o = in[qk_r(j)][t];
          if (t == 1 && (j & 1)) sts16(c.L, WL + (w * 16 + (j >> 1)) * 2048 + (u & 1) * 1024 + lane * 16, o);
          else {
            const int n = 3 * (j >> 1) + ((j & 1) ? 2 : t);
#pragma unroll
            for (int r = 0; r < 4; ++r) wa[n][4 * (u & 1) + r] = o[r];
#define M_(r) asm volatile("" : "+" r(wa[n]))
            if (u & 1) AT_ACC(M_, n);
#undef M_
          }
        }
      }
      BAR();
    }
  }
#pragma unroll
  for (int u = 0; u < 8; ++u) {
    v4u o[8];
    shuffle_cols(c, MEMS + w * 8192, buf[u % 3], o);
    SCHED();
    if (u + 3 < 8) request_o(u + 3);
#pragma unroll
    for (int i = 0; i < 4; ++i) {
      const int pr = 4 * (u >> 2) + i, ch = u & 3;
      const v8u f = {o[2 * i][0], o[2 * i + 1][0], o[2 * i][1], o[2 * i + 1][1], o[2 * i][2], o[2 * i + 1][2], o[2 * i][3], o[2 * i + 1][3]};
      if (ch < 2) {
        wh[2 * pr + ch] = f;
#define M_(r) asm volatile("" : "+" r(wh[2 * pr + ch]))
        AT_HOME(M_, 2 * pr + ch);
#undef M_
      } else if (ch == 2) {
        wa[24 + pr] = f;
#define M_(r) asm volatile("" : "+" r(wa[24 + pr]))
        AT_ACC(M_, 24 + pr);
#undef M_
      } else {
        sts16(c.L, WL + (w * 16 + 8 + pr) * 2048 + lane * 16, lo4(f));
        sts16(c.L, WL + (w * 16 + 8 + pr) * 2048 + 1024 + lane * 16, hi4(f));
      }
    }
    SCHED();
  }
  BAR();
  STAMP(0);
  const unsigned vote = wait_verdict(c);
  const int par = vote_b(vote), para = vote_a(vote);
  const int moff = opq(amap(w * 4096 + lane * 16, par) + (h & 1) * 1024);
  const int idx = opq((lane & 1) ? 0xEEEE : 0x4444);
  const int u_off = opq((16 * w + g) * 16384 + h * 256 + cc * 16);
  const int hq = opq((lane | w) ? 1 << 30 : amap(24576 + (32 + h) * 128, par));
  const int l0 = opq(WL + w * 32768 + lane * 16), l1 = opq(MEMS + lane * 16);
  const int vmw = opq(VM + 2 * g * VROW + (32 * w + 2 * cc) * 2);
  const int vmr = opq(VM + (cc >> 1) * VROW + g * 16);
  TICK_DECL;
  anchor(k);
  unsigned tg = k.cyc;
  Plan tt = {k.g - NS(100000), NS(T_DX)};
  int u3 = 0;
  for (int s = 0; s < NSEG; ++s) {
    const int pb = R_P + (s & 3) * P_SLOT;
    PIN_ACC0(wa); PIN_ACC1(wa); PIN_HOME(wh);
    asm volatile(VO_STEP
        : [bd] "+s"(c.budget)
        : [l0] "v"(l0), [l1] "v"(l1), [mo] "v"(moff), [ix] "v"(idx), [vw] "v"(vmw), [vr] "v"(vmr), [uo] "v"(u_off), [w] "s"(w), [rs] "s"(c.rs),
          [sb] "s"(R_MEM + (s & 3) * MEM_SLOT), [ub] "s"(u_slot(u3, par, para)), [tg] "s"(tg), [hq] "v"(hq), [pb] "s"(pb),
          [ca] "s"(k.cyc - T_DV), [k1] "s"(k.k1), [ga] "s"(k.g)
        : CL_S, CL_16_32, CL_160_200, CL_200_216, CL_216_256);
    PIN_ACC0(wa); PIN_ACC1(wa); PIN_HOME(wh);
    if (w == 0) tg = next_go(c, k, tt, par, pb, ask_stamps(c, par, pb)); else __builtin_amdgcn_s_sleep(T_RST);
    {
      const int ur = u_slot(u3 ? u3 - 1 : 2, par, para);
      const v4u se = {fresh_sent(), fresh_sent(), fresh_sent(), fresh_sent()};
#pragma unroll
      for (int i = 0; i < 4; ++i) st16s(c.rs, u_off, ur + i * 65536, se);
      st4s(c.rs, hq, R_P + ((s + 2) & 3) * P_SLOT, se[0]);
      u3 = u3 == 2 ? 0 : u3 + 1;
    }
    TICK_SUM(10);
  }
  TICK_OUT();
  if (c.budget == 0u) __hip_atomic_store(c.ctrl + C_TMO / 4, 1u, RLX_AGENT);
}

DEV void y_role(const Params& p, Ctx& c, int y) {
  constexpr int ZR = 0, UL = 131072, CW = 155648;
  c.budget = SPIN_CAP;
  const int w = c.w, lane = c.lane = fresh_lane(), g = lane >> 4, cc = lane & 15;
  const v4u sent4 = {SENT, SENT, SENT, SENT};
  STAMP(2);
  Clk k = {0u, 0u, 0u, 0u};
  anchor(k);
  unsigned m0 = 0;
  if (w == 0) {
    unsigned best[2] = {~0u, ~0u};
    st4s(c.rs, y * 16384 + (lane >> 5) * 8192 + (lane & 31) * 4, R_U, SENT);
#pragma unroll
    for (int r = 0; r < 2 * PROBES; ++r) {
      VMWAIT(0);
      const unsigned t0 = (unsigned)__builtin_amdgcn_s_memtime();
      LGKM(0);
      st4s(c.rs, y * 16384 + (lane & 31) * 4, R_U + (r & 1) * 8192, SENT);
      VMWAIT(0);
      const unsigned t1 = (unsigned)__builtin_amdgcn_s_memtime();
      LGKM(0);
      best[r & 1] = min(best[r & 1], t1 - t0);
    }
    STAMP(5);
    claim(c, CL_VOTED | (best[1] < best[0] ? CL_A : 0u));
    m0 = *(const GAS unsigned*)(p.mem0 + (lane >> 3) * 1024 + 16 * y + 2 * (lane & 7));
    m0 = m0 < SENT - 1u ? m0 : SENT - 1u;
    if (lane < 8) *(LAS unsigned*)(c.L + CW + lane * 4) = 0u;
    const unsigned v = wait_vote(c);
    STAMP(7);
    const int oa = y * 16384 + vote_a(v) * 8192 + lane * 16, ob = amap(y * 3072 + lane * 16, vote_b(v));
#pragma unroll
    for (int i = 0; i < 16; ++i) st16s(c.rs, oa + (i & 3) * 1024, R_U + (i >> 3) * MB + ((i >> 2) & 1) * 4096, sent4);
#pragma unroll
    for (int i = 0; i < 3; ++i) st16s(c.rs, amap(y * 3072 + i * 1024 + lane * 16, vote_b(v)), R_MEM, sent4);
#pragma unroll
    for (int i = 0; i < 8; ++i) st16s(c.rs, oa + (i & 3) * 1024, R_PT + (i >> 2) * 4096, sent4);
    (void)ob;
    VMWAIT(0);
    if (lane == 0) __hip_atomic_fetch_add(c.ctrl + (C_CLAIM + c.att * 1024) / 4 + c.slot, CL_FILLED, RLX_AGENT);
    STAMP(6);
  }
  BAR();
  const unsigned vote = wait_verdict(c);
  const int par = vote_b(vote), para = vote_a(vote);
  const int mo = amap(y * 256 + lane * 4, par);
  if (w == 0) st4s(c.rs, mo, R_MEM, m0);
  STAMP(0);
  volatile LAS unsigned* const GO = (volatile LAS unsigned*)(c.L + CW);
  TICK_DECL;
  if (w == 0) {
    int xo[4];
#pragma unroll
    for (int i = 0; i < 4; ++i) xo[i] = amap(i * 4096 + lane * 16, par);
    const int pmo = amap(16384 + 2 * g * 128 + cc * 8, par), psel = (cc & 1) ? 0x03020706 : 0x05040100;
    const int lead = T_U0 - 128 * ((y >> 2) & 3), late = __builtin_amdgcn_readfirstlane(opq(lead < 264));
    anchor(k);
    Plan tt = {k.g - NS(100000), NS(T_DX)};
    unsigned done = k.cyc;
    const int so = amap(24576, par) + lane * 128;
    const int idx = opq((lane & 1) ? 0xEEEE : 0x4444);
    const int uo = y * 16384 + lane * 16;
    int u3 = 0;
    const int mw = amap(y * 256 + (2 * g + (cc & 1)) * 32 + (cc >> 1) * 4, par);
    for (int s = 0; s < NSEG - 1; ++s) {
      if (s == 0) RAWBAR();
      v32u z0, z1, z2, z3;
      asm volatile(
          "ds_read_b128 a[0:3], %[za] offset:0\n\t" "ds_read_b128 a[4:7], %[za] offset:1024\n\t" "ds_read_b128 a[8:11], %[za] offset:2048\n\t" "ds_read_b128 a[12:15], %[za] offset:3072\n\t"
          "ds_read_b128 a[16:19], %[za] offset:4096\n\t" "ds_read_b128 a[20:23], %[za] offset:5120\n\t" "ds_read_b128 a[24:27], %[za] offset:6144\n\t" "ds_read_b128 a[28:31], %[za] offset:7168\n\t"
          "ds_read_b128 a[32:35], %[za] offset:8192\n\t" "ds_read_b128 a[36:39], %[za] offset:9216\n\t" "ds_read_b128 a[40:43], %[za] offset:10240\n\t" "ds_read_b128 a[44:47], %[za] offset:11264\n\t"
          "ds_read_b128 a[48:51], %[za] offset:12288\n\t" "ds_read_b128 a[52:55], %[za] offset:13312\n\t" "ds_read_b128 a[56:59], %[za] offset:14336\n\t" "ds_read_b128 a[60:63], %[za] offset:15360\n\t"
          "ds_read_b128 a[64:67], %[za] offset:16384\n\t" "ds_read_b128 a[68:71], %[za] offset:17408\n\t" "ds_read_b128 a[72:75], %[za] offset:18432\n\t" "ds_read_b128 a[76:79], %[za] offset:19456\n\t"
          "ds_read_b128 a[80:83], %[za] offset:20480\n\t" "ds_read_b128 a[84:87], %[za] offset:21504\n\t" "ds_read_b128 a[88:91], %[za] offset:22528\n\t" "ds_read_b128 a[92:95], %[za] offset:23552\n\t"
          "ds_read_b128 a[96:99], %[za] offset:24576\n\t" "ds_read_b128 a[100:103], %[za] offset:25600\n\t" "ds_read_b128 a[104:107], %[za] offset:26624\n\t" "ds_read_b128 a[108:111], %[za] offset:27648\n\t"
          "ds_read_b128 a[112:115], %[za] offset:28672\n\t" "ds_read_b128 a[116:119], %[za] offset:29696\n\t" "ds_read_b128 a[120:123], %[za] offset:30720\n\t" "ds_read_b128 a[124:127], %[za] offset:31744\n\t"
          "s_waitcnt lgkmcnt(0)"
          : "={a[0:31]}"(z0), "={a[32:63]}"(z1), "={a[64:95]}"(z2), "={a[96:127]}"(z3) : [za] "v"(ZR + (s & 3) * 32768 + lane * 16) : "memory");
      const int pb = R_P + (s & 3) * P_SLOT;
      v32u uu;
      const int ub = u_slot(u3, par, para);
      TICK(0);
      anchor(k);
      TICK(8);
      unsigned tg = to_cyc(k, tt.gx + tt.dx + NS(T_M1)), lt = late, sv = SENT;
      unsigned bad = (int)(done - to_cyc(k, tt.gx)) > T_OVH;
      TICK(4);
#pragma unroll 1
      for (;;) {
        if (bad) {
          const bool all = allok(sv);
          const unsigned last = latest(c, k, par, pb, sv);
          if (!all) anchor(k);
          tg = to_cyc(k, second_try(tt, last));
          lt = 1;
        }
        asm volatile(Y_STEP
          : "=&{v[144:175]}"(uu), [bad] "=&s"(bad), [sv] "=&v"(sv)
          : [x0] "v"(xo[0]), [x1] "v"(xo[1]), [x2] "v"(xo[2]), [x3] "v"(xo[3]), [pm] "v"(pmo), [u0] "v"(uo), [u1] "v"(uo + 4096), [mw] "v"(mw), [ps] "v"(psel), [ix] "v"(idx),
            [rs] "s"(c.rs), [pb] "s"(pb), [ub] "s"(ub), [mb] "s"(R_MEM + ((s + 1) & 3) * MEM_SLOT), [mr] "s"(R_MEM + ((s + 3) & 3) * MEM_SLOT), [tu] "s"(tg - (lt ? 128 : lead)), [tg] "s"(tg), [late] "s"(lt),
            [so] "v"(so), [bd] "s"(c.budget),
            "{a[0:31]}"(z0), "{a[32:63]}"(z1), "{a[64:95]}"(z2), "{a[96:127]}"(z3)
          : CL_S, CL_120_128, CL_176_200, CL_200_216, CL_216_256);
        bad = __builtin_amdgcn_readfirstlane(bad);
        if (bad != 1u) break;
        spend(c);
      }
      done = bad;
      SCHED();
      const int ul = opq(UL + lane * 16);
      u3 = u3 == 2 ? 0 : u3 + 1;
#pragma unroll
      for (int i = 0; i < 8; ++i) sts16(c.L, ul + i * 1024, v4u{uu[4 * i], uu[4 * i + 1], uu[4 * i + 2], uu[4 * i + 3]});
      asm volatile("" ::: "memory");
      *GO = s + 1;
      asm volatile("s_waitcnt vmcnt(2)" : "+v"(sv));
      advance(tt, latest(c, k, par, pb, sv));
      TICK(3);
      TICK_LDS(CW + 256);
    }
    {
      anchor(k);
      wait_cyc(to_cyc(k, tt.gx + tt.dx + NS(T_M1)));
      const int ub = u_slot(u3, par, para);
      v4u u[8];
#pragma unroll 1
      for (;;) {
        asm volatile("" ::: "memory");
#pragma unroll
        for (int i = 0; i < 8; ++i) u[i] = ld16s(c.rs, uo + (i & 3) * 1024, ub + (i >> 2) * 4096);
        SCHED();
        unsigned t = 0;
#pragma unroll
        for (int i = 0; i < 8; ++i) t = umax(t, top(u[i]));
        if (allok(t) || spend(c)) break;
      }
      RAWBAR();
#pragma unroll
      for (int i = 0; i < 8; ++i) sts16(c.L, UL + i * 1024 + lane * 16, u[i]);
      asm volatile("" ::: "memory");
      *GO = NSEG;
    }
    TICK_DUMP(CW + 256);
  } else if (w < 3) {
    const rsrc_t ro = mkrs(p.out, 2048 * 2048 * 2);
    auto tokens = [&](auto QT) {
      constexpr int qt = QT.value, NX = qt ? 32 : 16;
      RAWBAR();
      for (int s = 0; s < NSEG; ++s) {
        const int tb = R_PT + (s & 7) * PT_SLOT + para * 8192;
        const int ua = opq(UL + lane * 16);
        const int za = opq(ZR + (s & 3) * 32768 + (qt ? lane * 16 : (g >> 1) * 1024 + (g & 1) * 512 + cc * 16));
        SCHED();
        RAWBAR();
        TICK(0);
        f32x4 a;
#pragma unroll 1
        for (;;) {
          asm volatile("" ::: "memory");
          v4u pt[NX + 8];
#pragma unroll
          for (int i = 0; i < NX; ++i) pt[i] = ld16s(c.rs, (i & 3) * 1024 + lane * 16, tb + amap(qt * 16384 + (i >> 2) * 4096, 0));
#pragma unroll
          for (int i = 0; i < 8; ++i) pt[NX + i] = ld16s(c.rs, (i & 3) * 1024 + lane * 16, tb + amap(49152 + qt * 8192 + (i >> 2) * 4096, 0));
          SCHED();
          TICK(1);
#pragma unroll 1
          while (__builtin_amdgcn_readfirstlane(*GO) < (unsigned)s + 1u) { if (spend(c)) break; }
          asm volatile("" ::: "memory");
          f32x4 d[2] = {f32x4{0.f, 0.f, 0.f, 0.f}, f32x4{0.f, 0.f, 0.f, 0.f}};
          v4u fa[8];
          auto frag = [&](int i) {
            if (i >= NX) fa[i & 7] = lds16(c.L, ua + (i - NX) * 1024);
            else if (qt) fa[i & 7] = lds16(c.L, za + i * 1024);
            else {
              const v2u lo = *(LAS v2u*)(c.L + za + i * 2048), hi = *(LAS v2u*)(c.L + za + i * 2048 + 256);
              fa[i & 7] = v4u{lo.x, lo.y, hi.x, hi.y};
            }
          };
#pragma unroll
          for (int i = 0; i < 7; ++i) frag(i);
          SCHED();
#pragma unroll
          for (int i = 0; i < NX + 8; ++i) {
            if (i + 7 < NX + 8) frag(i + 7);
            d[i & 1] = mfma16(fa[i & 7], pt[i], d[i & 1]);
            SCHED();
          }
          a = d[0] + d[1];
          if (!__any(a[0] != a[0])) break;
          unsigned t = 0;
#pragma unroll
          for (int i = 0; i < NX + 8; ++i) t = umax(t, top(pt[i]));
          if (allok(t) || spend(c)) break;
        }
        __builtin_amdgcn_raw_buffer_store_b64(v2u{pk(a[0], a[1]), pk(a[2], a[3])}, ro, (16 * qt + cc) * 4096 + (16 * y + 4 * g) * 2, s * 32 * 4096, AUX_NT);
        TICK(2);
        TICK_SUM(3);
      }
    };
    if (w == 1) tokens(IC<0>{}); else tokens(IC<1>{});
  } else {
    unsigned seenZ = NOFLAG;
    const rsrc_t rc = mkrs((const void*)c.ctrl, CTRL_BYTES);
    const int fo = lane < 32 ? lane * 128 : 1 << 30;
    __builtin_amdgcn_s_setprio(3);
    auto least = [&](unsigned v) {
      v |= lane < 32 ? 0u : ~0u;
#pragma unroll
      for (int o = 1; o < 32; o <<= 1) v = min(v, (unsigned)__shfl_xor((int)v, o));
      seenZ = umax(seenZ, __builtin_amdgcn_readfirstlane(v));
    };
    auto await = [&](unsigned need) {
#pragma unroll 1
      while (seenZ < need) {
        asm volatile("" ::: "memory");
        least(__builtin_amdgcn_raw_buffer_load_b32(rc, fo, C_FLAG + (F_Z + (y >> 4) * 32) * 128, AUX_SC1));
        if (seenZ >= need || spend(c)) break;
        __builtin_amdgcn_s_sleep(4);
      }
      __builtin_amdgcn_fence(__ATOMIC_ACQUIRE, "wavefront");
    };
    for (int n = 0; n < NSEG; ++n) {
      TICK(0);
      await(n + 1);
      TICK(1);
      v4u z[32];
#pragma unroll
      for (int i = 0; i < 32; ++i) {
        z[i] = ld16s(c.rs, (i & 3) * 1024 + lane * 16, R_Z + (n & (ZRING - 1)) * Z_SLOT + y * 65536 + ((para ^ (y >> 5)) & 1) * 8192 + amap((i >> 2) * 4096, 0));
        SCHED();
        asm volatile("s_nop " STR(Z_GAP));
        SCHED();
      }
      const unsigned look = __builtin_amdgcn_raw_buffer_load_b32(rc, seenZ < (unsigned)n + 2u ? fo : 1 << 30, C_FLAG + (F_Z + (y >> 4) * 32) * 128, AUX_SC1);
      SCHED();
      TICK(4);
      const int za = opq(ZR + (n & 3) * 32768 + lane * 16);
#pragma unroll
      for (int i = 0; i < 32; ++i) {
        sts16(c.L, za + i * 1024, z[i]);
        if (i == 30) { TICK(3); }
      }
      least(look);
      if (n == 0) await(PRIME);
      TICK(2);
      { const int s = n; (void)s; TICK_SUM(3); }
      BAR();
    }
    RAWBAR();
  }
  TICK_OUT();
  if (c.budget == 0u) __hip_atomic_store(c.ctrl + C_TMO / 4, 1u, RLX_AGENT);
}

#define MF1A(d, a, b) asm volatile(MF16 "%0, %1, %2, %0" : "+v"(d) : "a"(a), "v"(b))
#define MF32A(d, a, b) asm volatile("v_mfma_f32_32x32x16_bf16 %0, %1, %2, %0" : "+v"(d) : "a"(a), "v"(b))
#define MF32AZ(d, a, b) asm volatile("v_mfma_f32_32x32x16_bf16 %0, %1, %2, 0" : "=&v"(d) : "a"(a), "v"(b))
#define MF1B(d, a, b) asm volatile(MF16 "%0, %1, %2, %0" : "+v"(d) : "v"(a), "a"(b))
#define MF1BZ(d, a, b) asm volatile(MF16 "%0, %1, %2, 0" : "=&v"(d) : "v"(a), "a"(b))
#define MF1AZ(d, a, b) asm volatile(MF16 "%0, %1, %2, 0" : "=&v"(d) : "a"(a), "v"(b))
DEV void g_role(const Params& p, Ctx& c, int mat, int h, int gi) {
  constexpr int EX = 131072;
  c.budget = SPIN_CAP;
  const int w = c.w, lane = c.lane = fresh_lane(), g = lane >> 4, cc = lane & 15, h2 = lane >> 5, r32 = lane & 31;
  if (mat < 2) {
    unsigned t = 0;
#pragma unroll
    for (int i = 0; i < 3; ++i) t += __builtin_amdgcn_raw_buffer_load_b32(mkrs(p.Wv, 8 * MB), (256 * w + (lane >> 1)) * 8192 + h * 256 + (lane & 1) * 128, (2 + 2 * i + mat) * 32 * 8192, 0);
    if (mat == 0) {
      hold(c, X_AT);
#pragma unroll
      for (int i = 0; i < 4; ++i) t += __builtin_amdgcn_raw_buffer_load_b32(mkrs(p.x, 4 * MB), ((4 * h + w) * 64 + lane) * 128, i * MB, 0);
    }
    asm volatile("" :: "v"(t));
    claim(c, 0);
    hold(c, (mat ? GK_AT : GQ_AT) - (slow_head(p.Wq, h) ? SLOW_LEAD : 0));
  }
  const rsrc_t rx = mkrs(p.x, 2048 * 2048);
  const int xs = w * 16384 + ((lane * 16) ^ ((w & 1) << 7));

  v32u wa[8];
  {
    const rsrc_t rw = mkrs(mat == 0 ? p.Wq : mat == 1 ? p.Wk : p.Wv, 1024 * 4096 * 2);
    const int vo = (256 * w + 8 * g) * 8192 + (h * 128 + 8 * cc) * 2;
    v4u row[8][8];
    auto request = [&](int u) {
#pragma unroll
      for (int r = 0; r < 8; ++r) row[u][r] = ld16w(rw, vo, (32 * u + r) * 8192);
    };
#pragma unroll
    for (int u = 0; u < 2; ++u) request(u);
#pragma unroll
    for (int u = 0; u < 16; ++u) dma16(rx, c.L + w * 16384 + u * 1024, (xs ^ ((u >> 1) << 4)) + u * 1024);
#pragma unroll
    for (int u = 2; u < 6; ++u) request(u);
    SCHED();
    const int col = 32 * w + 16 * (r32 >> 4) + 8 * ((r32 >> 2) & 1) + 4 * ((r32 >> 3) & 1) + (r32 & 3);
    const int sw_ = opq(EX + w * 8192 + cc * 512 + ((g ^ (cc & 3)) << 4)), sr_ = EX + col * 64;
    const int sr0 = opq(sr_ + ((h2 ^ ((col >> 3) & 3)) << 4)), sr1 = opq(sr_ + (((2 + h2) ^ ((col >> 3) & 3)) << 4));
#pragma unroll
    for (int u = 0; u < 8; ++u) {
#pragma unroll
      for (int i = 0; i < 8; ++i) {
        v4u f;
#pragma unroll
        for (int r = 0; r < 4; ++r) f[r] = prm(row[u][2 * r + 1][i >> 1], row[u][2 * r][i >> 1], i & 1);
        sts16(c.L, sw_ + i * 64, f);
      }
      SCHED();
      if (u + 6 < 8) request(u + 6);
      if (u == 7 && mat == 2) claim(c, 0);
      BAR();
      v8u in[4];
#pragma unroll
      for (int q = 0; q < 4; ++q) in[q] = cat(lds16(c.L, sr0 + q * 8192), lds16(c.L, sr1 + q * 8192));
      SCHED();
#pragma unroll
      for (int q = 0; q < 4; ++q) {
        v8u f = in[q];
#define M_(r) asm volatile("" : "+" r(f))
        AT_ACC(M_, 8 * q + u);
#undef M_
#pragma unroll
        for (int r = 0; r < 8; ++r) wa[2 * q + (u >> 2)][8 * (u & 3) + r] = f[r];
      }
      BAR();
    }
  }
  STAMP(0);
  const int xpar = vote_a(wait_vote(c)) ^ (mat >> 1);
  TICK_DECL;
  asm volatile(G_LOOP
      :: [xo] "v"(r32 * 2048 + ((h2 ^ (r32 & 15)) << 4)), [xs] "v"(xs), [so] "v"((4 * w + h2) * 512 + r32 * 16), [fo] "v"((w | lane) ? 1 << 30 : 0),
         [rs] "s"(c.rs), [rx] "s"(rx), [rc] "s"(mkrs((const void*)c.ctrl, CTRL_BYTES)), [fl] "s"(C_FLAG + (mat * 32 + h) * 128), [xp] "s"(xpar),
         [ob] "s"((mat == 1 ? R_KX : R_QX) + h * 16384), [lw] "s"(w * 16384), GTRACE ACC_IN(wa)
      : CL_S, CL_16_32, CL_32_100);
}

DEV void zc_role(const Params& p, Ctx& c, int h) {
  c.budget = SPIN_CAP;
  const int w = c.w, lane = c.lane = fresh_lane(), g = lane >> 4, cc = lane & 15, h2 = lane >> 5, r32 = lane & 31;
  v32u wa[8];
  hold(c, ZC_AT);
  {
    const rsrc_t rw = mkrs(p.Wo, 4096 * 1024 * 2);
    const int vo = (h * 128 + 8 * g) * 2048 + (256 * w + 8 * cc) * 2;
    v4u row[8][8];
    auto request = [&](int u) {
#pragma unroll
      for (int r = 0; r < 8; ++r) row[u][r] = ld16w(rw, vo, (32 * (u & 3) + r) * 2048 + (u >> 2) * 256);
    };
#pragma unroll
    for (int u = 0; u < 2; ++u) request(u);
    SCHED();
    claim(c, 0);
    SCHED();
#pragma unroll
    for (int u = 2; u < 6; ++u) request(u);
    const int sw_ = opq(w * 8192 + cc * 512 + ((g ^ (cc & 3)) << 4)), sr_ = w * 8192 + r32 * 64;
    const int sr0 = opq(sr_ + ((h2 ^ (r32 >> 3)) << 4)), sr1 = opq(sr_ + (((2 + h2) ^ (r32 >> 3)) << 4));
#pragma unroll
    for (int u = 0; u < 8; ++u) {
#pragma unroll
      for (int i = 0; i < 8; ++i) {
        v4u f;
#pragma unroll
        for (int r = 0; r < 4; ++r) f[r] = prm(row[u][2 * r + 1][i >> 1], row[u][2 * r][i >> 1], i & 1);
        sts16(c.L, sw_ + i * 64, f);
      }
      SCHED();
      if (u + 6 < 8) request(u + 6);
      LGKM(0);
      v8u in[4];
#pragma unroll
      for (int q = 0; q < 4; ++q) in[q] = cat(lds16(c.L, sr0 + q * 2048), lds16(c.L, sr1 + q * 2048));
      SCHED();
#pragma unroll
      for (int q = 0; q < 4; ++q) {
        v8u f = in[q];
#define M_(r) asm volatile("" : "+" r(f))
        AT_ACC(M_, 4 * (4 * (u >> 2) + q) + (u & 3));
#undef M_
#pragma unroll
        for (int r = 0; r < 8; ++r) wa[4 * (u >> 2) + q][8 * (u & 3) + r] = f[r];
      }
      LGKM(0);
      SCHED();
    }
  }
  STAMP(0);
  BAR();
  const int para = vote_a(wait_vote(c));
  const int ao = h2 * 512 + (16 * ((r32 >> 3) & 1) + 8 * (r32 >> 4) + (r32 & 7)) * 16;
  TICK_DECL;
  asm volatile(ZC_LOOP
      : [bd] "+s"(c.budget)
      : [ao] "v"(ao), [a1] "v"(ao + 4096), [zo] "v"((16 * w + (r32 >> 4)) * 65536 + (h2 * 16 + cc) * 16 + ((para ^ (w >> 1)) & 1) * 8192 + amap(h * 1024, 0)), [fo] "v"(lane ? 1 << 30 : 0),
        [rs] "s"(c.rs), [rc] "s"(mkrs((const void*)c.ctrl, CTRL_BYTES)), [fv] "s"(C_FLAG + (F_V + h) * 128), [fs] "s"(C_FLAG + (F_S + h) * 128),
        [fz] "s"(C_FLAG + (F_Z + w * 32 + h) * 128), [xp] "s"(para ^ 1), [vb] "s"(R_QX + h * 16384), [vd] "s"(C_VERDICT + c.att * 128), [z0] "n"(ZC_PRIME), [zb] "n"(R_Z), [rg] "n"(ZRING - 2), [rm] "n"(ZRING - 1), GTRACE ACC_IN(wa)
      : CL_S, CL_16_32, CL_32_100);
  if (c.budget == 0u) __hip_atomic_store(c.ctrl + C_TMO / 4, 1u, RLX_AGENT);
}

DEV void conv_role(const Params& p, Ctx& c, int cg, int far) {
  constexpr int CH = 5200, U8 = 0;
  constexpr int CP = 4160, FC = 41600;
  constexpr int YS = FC + 16 * CP, RED = YS + 32768;
  static_assert(RED + 8192 <= LDS_BYTES - 16, "lds");
  c.budget = SPIN_CAP;
  const int w = c.w, lane = c.lane = fresh_lane(), tid = w * 64 + lane;
  const int d0 = 8 * cg;
  const int pg = ((far ^ (cg >> 5)) & 1) * 8192;
  auto crow = [](int ch) { return (ch >> 1) * 16384 + (ch & 1) * 4096; };
  const int de = (cg >> 3) * 64 + (tid & 7) * 8, t0 = (cg & 7) * 256 + (tid >> 3) * 8;
  auto group_sync = [&](int phase) {
    gu32* cnt = c.ctrl + (C_CONV + (cg >> 3) * 16 + phase * 8) / 4;
    VMWAIT(0);
    BAR();
    if (w == 0) {
      if (lane == 0) __hip_atomic_fetch_add(cnt, 1u, RLX_AGENT);
#pragma unroll 1
      while (__builtin_amdgcn_readfirstlane(__hip_atomic_load(cnt, RLX_AGENT)) < 8u) { if (spend(c)) break; __builtin_amdgcn_s_sleep(4); }
    }
    BAR();
  };
  v4u kv[8];
  BAR();
  {
    const rsrc_t rk = mkrs(p.bk, 2048 * 2048), rv = mkrs(p.bv, 2048 * 2048);
    v4u kr[11], vr[8], fr[8];
#pragma unroll
    for (int i = 0; i < 11; ++i) kr[i] = ld16c(rk, (t0 + i - 3) * 2048 + de * 2, 0);
#pragma unroll
    for (int i = 0; i < 8; ++i) vr[i] = ld16c(rv, (t0 + i) * 2048 + de * 2, 0);
    v4u sh[4];
#pragma unroll
    for (int i = 0; i < 4; ++i) sh[i] = *(const GAS v4u*)(p.shift + de * 4 + i * 8);
#pragma unroll
    for (int i = 0; i < 8; ++i) fr[i] = ld16c(mkrs(p.ssmk, 4 * MB), tid * 16, (d0 + i) * 4096);
    if (tid < 40) sts16(c.L, U8 + (tid / 5) * CH + (tid % 5) * 16, v4u{0, 0, 0, 0});
    if (tid < 64) sts16(c.L, FC + (tid >> 2) * CP + 4096 + (tid & 3) * 16, v4u{0, 0, 0, 0});
#pragma unroll
    for (int ch = 0; ch < 8; ++ch) {
      const unsigned sa = sh[ch >> 1][(ch & 1) * 2], sb = sh[ch >> 1][(ch & 1) * 2 + 1];
      const float s0 = blo(sa), s1 = bhi(sa), s2 = blo(sb), s3 = bhi(sb);
      float kk[11], o[8];
#pragma unroll
      for (int i = 0; i < 11; ++i) kk[i] = (ch & 1) ? bhi(kr[i][ch >> 1]) : blo(kr[i][ch >> 1]);
#pragma unroll
      for (int i = 0; i < 8; ++i) {
        float a = rb(kk[i] * s3);
        a = rb(a + rb(kk[i + 1] * s2));
        a = rb(a + rb(kk[i + 2] * s1));
        a = rb(a + rb(kk[i + 3] * s0));
        o[i] = a * ((ch & 1) ? bhi(vr[i][ch >> 1]) : blo(vr[i][ch >> 1]));
      }
      kv[ch] = v4u{pk(o[0], o[1]), pk(o[2], o[3]), pk(o[4], o[5]), pk(o[6], o[7])};
      st16s(c.rs, de * 8192 + t0 * 2, R_KVT + pg + crow(ch), kv[ch]);
    }
#pragma unroll
    for (int i = 0; i < 8; ++i) sts16(c.L, YS + i * 4096 + tid * 16, fr[i]);
    group_sync(0);
#pragma unroll
    for (int ch = 0; ch < 8; ++ch) fr[ch] = ld16s(c.rs, tid * 16, R_KVT + pg + d0 * 8192 + crow(ch));
#pragma unroll
    for (int ch = 0; ch < 8; ++ch) sts16(c.L, U8 + ch * CH + 80 + (tid >> 2) * 80 + (tid & 3) * 16, fr[ch]);
  }
  BAR();
  STAMP(5);
  auto build = [&](int ch, int q0, int q1) {
    for (int q = q0; q < q1; ++q) {
      const int m = lane + 64 * q;
      const v4u hi = lds16(c.L, YS + ch * 4096 + (255 - m) * 16);
      v4u lo = lds16(c.L, YS + ch * 4096 + (m < 255 ? 254 - m : 0) * 16);
      if (m == 255) lo = v4u{0, 0, 0, 0};
      unsigned G[8];
#pragma unroll
      for (int i = 0; i < 4; ++i) { G[i] = __builtin_amdgcn_alignbyte(hi[3 - i], hi[3 - i], 2); G[4 + i] = __builtin_amdgcn_alignbyte(lo[3 - i], lo[3 - i], 2); }
#pragma unroll
      for (int sg = 0; sg < 8; ++sg) {
        v4u o;
#pragma unroll
        for (int i = 0; i < 4; ++i) o[i] = (sg & 1) ? __builtin_amdgcn_alignbyte(G[(sg >> 1) + i + 1], G[(sg >> 1) + i], 2) : G[(sg >> 1) + i];
        sts16(c.L, FC + ((ch & 1) * 8 + sg) * CP + m * 16, o);
      }
    }
  };
  build(0, w, w + 1);
  BAR();
  const int h2 = lane >> 5, r = lane & 31;
  for (int ch = 0; ch < 8; ++ch) {
    f32x16 acc;
    if (w == 3) {
      if (ch + 1 < 8) build(ch + 1, 0, 4);
    } else {
#pragma unroll
      for (int i = 0; i < 16; ++i) acc[i] = 0.f;
      const int dl0 = w == 2 ? 32 : 0, i0 = w == 0 ? 0 : 32;
      const int sg = (7 - r) & 7;
      int fo = FC + ((ch & 1) * 8 + sg) * CP + (2047 - 32 * dl0 - r + 8 * h2 - sg) * 2;
      const int ub = (i0 + r - dl0) * 80;
      const int ubase = U8 + ch * CH + 80 + h2 * 16;
      v4u fa[3][2], fb[3][2];
      fo = opq(fo - 31 * 64);
#pragma unroll
      for (int dl = -2; dl < 32; ++dl) {
        const int dn = dl + 2;
        if (dn < 32) {
          const int uo = ubase + max(ub - dn * 80, -80);
          fa[dn % 3][0] = lds16(c.L, fo + (31 - dn) * 64); fb[dn % 3][0] = lds16(c.L, uo);
          fa[dn % 3][1] = lds16(c.L, fo + (31 - dn) * 64 + 32); fb[dn % 3][1] = lds16(c.L, uo + 32);
        }
        SCHED();
        if (dl >= 0) {
          acc = mfma32(fa[dl % 3][0], fb[dl % 3][0], acc);
          acc = mfma32(fa[dl % 3][1], fb[dl % 3][1], acc);
        }
        SCHED();
      }
      if (w == 2) {
#pragma unroll
        for (int m = 0; m < 4; ++m) sts16(c.L, RED + (ch & 1) * 4096 + m * 1024 + lane * 16, v4u{fu(acc[4 * m]), fu(acc[4 * m + 1]), fu(acc[4 * m + 2]), fu(acc[4 * m + 3])});
      }
    }
    BAR();
    if (w < 2) {
#pragma unroll
      for (int m = 0; m < 4; ++m) {
        f32x4 v = {acc[4 * m], acc[4 * m + 1], acc[4 * m + 2], acc[4 * m + 3]};
        if (w == 1) v += __builtin_bit_cast(f32x4, lds16(c.L, RED + (ch & 1) * 4096 + m * 1024 + lane * 16));
        sts8(c.L, YS + ch * 4096 + (32 * (32 * w + r) + 8 * m + 4 * h2) * 2, v2u{pk(v[0], v[1]), pk(v[2], v[3])});
      }
    }
  }
  BAR();
  STAMP(6);
  {
    const rsrc_t ro = mkrs(p.out, 2048 * 2048 * 2);
    v4u qr[8], yv[8];
#pragma unroll
    for (int ch = 0; ch < 8; ++ch) st16s(c.rs, tid * 16, R_YT + pg + d0 * 8192 + crow(ch), lds16(c.L, YS + ch * 4096 + tid * 16));
#pragma unroll
    for (int i = 0; i < 8; ++i) qr[i] = ld16c(mkrs(p.bq, 4 * MB), (t0 + i) * 2048 + de * 2, 0);
    const v4u dd = *(const GAS v4u*)(p.ssmD + de);
    group_sync(1);
#pragma unroll
    for (int ch = 0; ch < 8; ++ch) yv[ch] = ld16s(c.rs, de * 8192 + t0 * 2, R_YT + pg + crow(ch));
#pragma unroll
    for (int i = 0; i < 8; ++i) {
      float o[8];
#pragma unroll
      for (int ch = 0; ch < 8; ++ch) {
        const float yy = (i & 1) ? bhi(yv[ch][i >> 1]) : blo(yv[ch][i >> 1]);
        const float uu = (i & 1) ? bhi(kv[ch][i >> 1]) : blo(kv[ch][i >> 1]);
        const float dv = (ch & 1) ? bhi(dd[ch >> 1]) : blo(dd[ch >> 1]);
        const float qq = (ch & 1) ? bhi(qr[i][ch >> 1]) : blo(qr[i][ch >> 1]);
        o[ch] = qq * rb(yy + rb(uu * dv));
      }
      __builtin_amdgcn_raw_buffer_store_b128(v4u{pk(o[0], o[1]), pk(o[2], o[3]), pk(o[4], o[5]), pk(o[6], o[7])}, ro, (t0 + i) * 4096 + (1024 + de) * 2, 0, AUX_NT);
    }
  }
  if (c.budget == 0u) __hip_atomic_store(c.ctrl + C_TMO / 4, 1u, RLX_AGENT);
}

extern "C" __global__ void __launch_bounds__(256, 1) mk_mega(Params p) {
  extern __shared__ __attribute__((aligned(16))) unsigned char lds_raw[];
  Ctx c;
  c.ctrl = (gu32*)p.ctrl;
  c.rs = mkrs(p.data, R_END);
  c.L = (lds_t)lds_raw;
  c.w = __builtin_amdgcn_readfirstlane(threadIdx.x >> 6);
  c.lane = threadIdx.x & 63;
  c.t0 = (unsigned)__builtin_amdgcn_s_memrealtime();
  c.b = (int)(blockIdx.x & 255u);
  c.att = (int)(blockIdx.x >> 8);
  c.ask = 0;
  if (c.w == 1) {
    unsigned long long base; unsigned len, t;
    asm volatile("s_getpc_b64 s[60:61]\n1:\n\ts_sub_u32 s60, s60, 1b-mk_mega\n\ts_subb_u32 s61, s61, 0\n\ts_mov_b64 %0, s[60:61]\n\ts_mov_b32 %1, mk_tail-mk_mega"
                 : "=s"(base), "=s"(len) :: "s60", "s61", "scc");
    t = __builtin_amdgcn_raw_buffer_load_b32(mkrs((const void*)base, len), (c.lane * 32 + (c.b >> 3)) * 128, 0, 0);
    asm volatile("" :: "v"(t));
  }
  if (c.w == 0 && c.att == 0) {
    c.ask = __builtin_amdgcn_readfirstlane(__hip_atomic_load(c.ctrl + C_ECHO / 4 + c.b, RLX_AGENT)) + 1u;
    if (c.lane == 0) __hip_atomic_store(c.ctrl + (C_ASK + c.b * 64) / 4, c.ask, RLX_AGENT);
  }
  c.slot = (c.att ? (c.b & 7) : (int)((unsigned)__builtin_amdgcn_s_getreg(20 | (3 << 11)) & 7u)) * 32 + (c.b >> 3);
  if (c.b == SERVER && c.w == 3) serve(c);
  const int x = c.slot >> 5, t = c.slot & 31;
  if (x < 4) {
    if (t < 8) qk_role(p, c, x * 8 + t);
    else if (t < 16) vo_role(p, c, x * 8 + t - 8);
    else y_role(p, c, x * 16 + t - 16);
  } else {
    if (t < 24) g_role(p, c, t >> 3, (x - 4) * 8 + (t & 7), t);
    else zc_role(p, c, (x - 4) * 8 + t - 24);
    STAMP(2);
    c.budget = SPIN_CAP;
    conv_role(p, c, (x - 4) * 32 + t, vote_a(wait_verdict(c)) ^ 1);
  }
  STAMP(3);
}

}

extern "C" void kernel_launch(void* const* d_in, const int* in_sizes, int n_in, void* d_out, int out_size, void* d_ws, size_t ws_size,
                              hipStream_t stream) {
  using namespace mk;
  (void)in_sizes; (void)n_in; (void)out_size; (void)ws_size;
  Params p;
  p.x = (const u16*)d_in[0]; p.mem0 = (const u16*)d_in[1];
  p.Wq = (const u16*)d_in[2]; p.Wk = (const u16*)d_in[3]; p.Wv = (const u16*)d_in[4]; p.Wo = (const u16*)d_in[5];
  p.bq = (const u16*)d_in[7]; p.bk = (const u16*)d_in[8]; p.bv = (const u16*)d_in[9];
  p.shift = (const u16*)d_in[10]; p.ssmk = (const u16*)d_in[11]; p.ssmD = (const u16*)d_in[12];
  p.out = (u16*)d_out;
  p.ctrl = (unsigned char*)d_ws;
  p.data = (unsigned char*)(((size_t)d_ws + CTRL_BYTES + (2u << 20) - 1) & ~(size_t)((2u << 20) - 1));
  (void)hipFuncSetAttribute((const void*)mk_mega, hipFuncAttributeMaxDynamicSharedMemorySize, LDS_BYTES);
  hipLaunchKernelGGL(mk_mega, dim3(256), dim3(256), LDS_BYTES, stream, p);
}
```

```cpp
#include <hip/hip_runtime.h>
#include <hip/hip_bf16.h>

namespace mk {

#define GAS __attribute__((address_space(1)))
#define LAS __attribute__((address_space(3)))
#define DEV __device__ __forceinline__
#define RLX_AGENT __ATOMIC_RELAXED, __HIP_MEMORY_SCOPE_AGENT
typedef unsigned short u16;
typedef unsigned v2u __attribute__((ext_vector_type(2)));
typedef unsigned v4u __attribute__((ext_vector_type(4)));
typedef unsigned v8u __attribute__((ext_vector_type(8)));
typedef unsigned v16u __attribute__((ext_vector_type(16)));
typedef unsigned v32u __attribute__((ext_vector_type(32)));
typedef float f32x2 __attribute__((ext_vector_type(2)));
typedef float f32x4 __attribute__((ext_vector_type(4)));
typedef float f32x16 __attribute__((ext_vector_type(16)));
typedef __bf16 bf16x2 __attribute__((ext_vector_type(2)));
typedef __bf16 bf16x8 __attribute__((ext_vector_type(8)));
typedef __bf16 bf16x16 __attribute__((ext_vector_type(16)));
typedef GAS unsigned gu32;
typedef __amdgpu_buffer_rsrc_t rsrc_t;
typedef LAS unsigned char* lds_t;

#define XCNT(r) ""
#define XT(a, b) ""
#define UCNT ""
#define CL_T
#define GSUM ""
#define ZSUM ""
#define GTRACE
#define MIXED 1
#define NOFLAG 0
#define TOK_KICK 1
#define ZRING 16
#define Z_AT 16
#define Z_GAP 10
#define STR_(x) #x
#define STR(x) STR_(x)
#define GQ_AT 600
#define GK_AT 300
#define PULL_AT 800
#define QK_AT 100
#define SERVER 194
#define SLOW_LEAD 100
#define PULL_DEPTH 1
#define PRIME 2
#define X_AT 150
#define VO_AT 250
#define ZC_AT 0
#define CONV_AUX 2
#define W_AUX 0
#define ZC_PRIME 1
#define CODE_PULL 1
#define PROBES 2
constexpr int NSEG = 64;
constexpr unsigned SENT = 0xFFFFFFFFu;
constexpr int AUX_SC1 = 16, AUX_NT = 2;
constexpr int T_LAG = 61;
constexpr int G_AHEAD = 4;
constexpr unsigned SPIN_CAP = 1u << 21;
constexpr int LDS_BYTES = 163840;

constexpr int C_ECHO = 0, C_CLAIM = 1024, C_VERDICT = 3072, C_TMO = 3328, C_CONV = 3456, C_VOTE = 3840, C_FLAG = 4096, C_ASK = C_FLAG + 256 * 128;
constexpr int F_Q = 0, F_K = 32, F_V = 64, F_S = 96, F_Z = 128;
constexpr int CTRL_BYTES = C_ASK + 256 * 64;
constexpr int MB = 1 << 20;
constexpr int R_U = 0, R_U2 = 3 * MB;
constexpr int R_MEM = 2 * MB, MEM_SLOT = 32768;
constexpr int R_P = R_MEM + 4 * MEM_SLOT, P_SLOT = 65536;
constexpr int R_PT = 4 * MB, PT_SLOT = 131072;
constexpr int R_QX = 8 * MB, R_KX = 40 * MB, R_Z = 72 * MB, Z_SLOT = 4 * MB, R_KVT = R_KX, R_YT = R_KX + 8 * MB, R_END = 208 * MB;

struct Params {
  const u16 *x, *mem0, *Wq, *Wk, *Wv, *Wo, *bq, *bk, *bv, *shift, *ssmk, *ssmD;
  u16* out;
  unsigned char* ctrl;
  unsigned char* data;
};

DEV rsrc_t mkrs(const void* p, unsigned bytes) { return __builtin_amdgcn_make_buffer_rsrc((void*)p, 0, bytes, 0x00020000); }
DEV v4u ld16s(rsrc_t rs, int v, int s) { return __builtin_amdgcn_raw_buffer_load_b128(rs, v, s, AUX_SC1); }
DEV v2u ld8s(rsrc_t rs, int v, int s) { return __builtin_amdgcn_raw_buffer_load_b64(rs, v, s, AUX_SC1); }
DEV v4u ld16(rsrc_t rs, int v, int s) { return __builtin_amdgcn_raw_buffer_load_b128(rs, v, s, 0); }
DEV v4u ld16c(rsrc_t rs, int v, int s) { return __builtin_amdgcn_raw_buffer_load_b128(rs, v, s, CONV_AUX); }
DEV v4u ld16w(rsrc_t rs, int v, int s) { return __builtin_amdgcn_raw_buffer_load_b128(rs, v, s, W_AUX); }
DEV v2u ld8(rsrc_t rs, int v, int s) { return __builtin_amdgcn_raw_buffer_load_b64(rs, v, s, 0); }
DEV void dma16(rsrc_t rs, lds_t l, int v) {
#if __HIP_DEVICE_COMPILE__
  __builtin_amdgcn_raw_ptr_buffer_load_lds(rs, (LAS void*)l, 16, v, 0, 0, 0);
#endif
}
DEV void st16s(rsrc_t rs, int v, int s, v4u d) { __builtin_amdgcn_raw_buffer_store_b128(d, rs, v, s, AUX_SC1); }
DEV void st8s(rsrc_t rs, int v, int s, v2u d) { __builtin_amdgcn_raw_buffer_store_b64(d, rs, v, s, AUX_SC1); }
DEV void st4s(rsrc_t rs, int v, int s, unsigned d) { __builtin_amdgcn_raw_buffer_store_b32(d, rs, v, s, AUX_SC1); }
DEV int amap(int o, int par) { return ((o >> 13) << 14) | (par << 13) | (o & 8191); }
DEV int xblk(int s, int h, int par) { return (s * 32 + h) * 16384 + ((par ^ (s >> 2)) & 1) * 8192; }
DEV unsigned umax(unsigned a, unsigned b) { return a > b ? a : b; }
DEV unsigned top(v4u v) { return umax(umax(v.x, v.y), umax(v.z, v.w)); }
DEV unsigned pk(float a, float b) { bf16x2 v = {(__bf16)a, (__bf16)b}; return __builtin_bit_cast(unsigned, v); }
DEV unsigned pks(float a, float b) { unsigned u = pk(a, b); return u < SENT - 1u ? u : SENT - 1u; }
DEV float blo(unsigned u) { return __builtin_bit_cast(float, u << 16); }
DEV float bhi(unsigned u) { return __builtin_bit_cast(float, u & 0xFFFF0000u); }
DEV float rb(float a) { return blo(pk(a, a)); }
DEV unsigned fu(float f) { return __builtin_bit_cast(unsigned, f); }
DEV float uf(unsigned u) { return __builtin_bit_cast(float, u); }
DEV f32x4 mfma16(v4u a, v4u b, f32x4 c) {
  return __builtin_amdgcn_mfma_f32_16x16x32_bf16(__builtin_bit_cast(bf16x8, a), __builtin_bit_cast(bf16x8, b), c, 0, 0, 0);
}
DEV f32x16 mfma32(v4u a, v4u b, f32x16 c) {
  return __builtin_amdgcn_mfma_f32_32x32x16_bf16(__builtin_bit_cast(bf16x8, a), __builtin_bit_cast(bf16x8, b), c, 0, 0, 0);
}
DEV f32x4 smf(v4u a, v8u b, f32x4 c, int idx) {
  return __builtin_amdgcn_smfmac_f32_16x16x64_bf16(__builtin_bit_cast(bf16x8, a), __builtin_bit_cast(bf16x16, b), c, idx, 0, 0);
}
DEV v8u cat(v4u a, v4u b) { return __builtin_shufflevector(a, b, 0, 1, 2, 3, 4, 5, 6, 7); }
DEV v4u lo4(v8u a) { return __builtin_shufflevector(a, a, 0, 1, 2, 3); }
DEV v4u hi4(v8u a) { return __builtin_shufflevector(a, a, 4, 5, 6, 7); }
DEV v4u lds16(lds_t L, int o) { return *(LAS v4u*)(L + o); }
DEV void sts16(lds_t L, int o, v4u v) { *(LAS v4u*)(L + o) = v; }
DEV void sts8(lds_t L, int o, v2u v) { *(LAS v2u*)(L + o) = v; }
#define SCHED() __builtin_amdgcn_sched_barrier(0)
#define PIN_A(x) asm volatile("" : "+a"(x))
#define PIN_V(x) asm volatile("" : "+v"(x))
DEV int opq(int v) { asm volatile("" : "+v"(v)); return v; }
DEV int fresh_lane() {
  int l; asm volatile("v_mbcnt_lo_u32_b32 %0, -1, 0\n\tv_mbcnt_hi_u32_b32 %0, -1, %0" : "=v"(l)); return l;
}
DEV f32x4 fresh_zero() {
  f32x2 a, b;
  asm volatile("v_mov_b64 %0, 0" : "=v"(a));
  asm volatile("v_mov_b64 %0, 0" : "=v"(b));
  return __builtin_shufflevector(a, b, 0, 1, 2, 3);
}
DEV unsigned fresh_sent() { unsigned z; asm volatile("v_mov_b32 %0, -1" : "=v"(z)); return z; }
#define SMF "v_smfmac_f32_16x16x64_bf16 "
#define MF16 "v_mfma_f32_16x16x32_bf16 "
#define SMF1(cls, d, a, b) asm volatile(SMF "%0, %1, %2, %3" : "+v"(d) : "v"(a), cls(b), "v"(idx))
#define SMF1F(cls, lo, hi, d, a, b) asm volatile(SMF "v[" #lo ":" #hi "], %1, %2, %3" : "+{v[" #lo ":" #hi "]}"(d) : "v"(a), cls(b), "v"(idx))
#define AT_HOME(M, j) do { const int j_ = (j); if (j_ == 0) M("{v[32:39]}"); if (j_ == 1) M("{v[40:47]}"); if (j_ == 2) M("{v[48:55]}"); if (j_ == 3) M("{v[56:63]}"); if (j_ == 4) M("{v[64:71]}"); if (j_ == 5) M("{v[72:79]}"); if (j_ == 6) M("{v[80:87]}"); if (j_ == 7) M("{v[88:95]}"); if (j_ == 8) M("{v[96:103]}"); if (j_ == 9) M("{v[104:111]}"); if (j_ == 10) M("{v[112:119]}"); if (j_ == 11) M("{v[120:127]}"); if (j_ == 12) M("{v[128:135]}"); if (j_ == 13) M("{v[136:143]}"); if (j_ == 14) M("{v[144:151]}"); if (j_ == 15) M("{v[152:159]}"); } while (0)
#define MF1(d, a, b) asm volatile(MF16 "%0, %1, %2, %0" : "+v"(d) : "v"(a), "v"(b))
#define MF1Z(d, a, b) asm volatile(MF16 "%0, %1, %2, 0" : "=&v"(d) : "v"(a), "v"(b))
#define LGKM(n) asm volatile("s_waitcnt lgkmcnt(" #n ")" ::: "memory")
#define RAWBAR() do { __builtin_amdgcn_s_barrier(); asm volatile("" ::: "memory"); } while (0)
DEV void settle3(f32x4& s0, f32x4& s1, f32x4& s2) { asm volatile("s_nop 7" : "+v"(s0), "+v"(s1), "+v"(s2)); }
DEV void settle4(f32x4 (&d)[4]) { asm volatile("s_nop 7" : "+v"(d[0]), "+v"(d[1]), "+v"(d[2]), "+v"(d[3])); }
#define BAR() do { asm volatile("s_waitcnt lgkmcnt(0)" ::: "memory"); __builtin_amdgcn_s_barrier(); asm volatile("" ::: "memory"); } while (0)
#define VMWAIT(n) asm volatile("s_waitcnt vmcnt(" #n ")" ::: "memory")
DEV float add1(float a, float b) {
  float o; asm("v_add_f32 %0, %1, %2" : "=v"(o) : "v"(a), "v"(b)); return o;
}
DEV float xmax(float v) {
  auto r = __builtin_amdgcn_permlane32_swap(fu(v), fu(v), false, false); v = fmaxf(uf(r[0]), uf(r[1]));
  auto q = __builtin_amdgcn_permlane16_swap(fu(v), fu(v), false, false); return fmaxf(uf(q[0]), uf(q[1]));
}
DEV float xsum(float v) {
  auto r = __builtin_amdgcn_permlane32_swap(fu(v), fu(v), false, false); v = uf(r[0]) + uf(r[1]);
  auto q = __builtin_amdgcn_permlane16_swap(fu(v), fu(v), false, false); return uf(q[0]) + uf(q[1]);
}

DEV float ror8(float v) { return uf(__builtin_amdgcn_update_dpp(0u, fu(v), 0x128, 0xf, 0xf, true)); }
DEV float fmax2(float a, float b) { return __builtin_amdgcn_fmed3f(a, b, __builtin_inff()); }
DEV float fmax3(float a, float b, float c) { float r; asm("v_max3_f32 %0, %1, %2, %3" : "=v"(r) : "v"(a), "v"(b), "v"(c)); return r; }
DEV bool allok(unsigned t) { return __all(t != SENT); }

template <int N> struct IC { static constexpr int value = N; };
template <int N, class F> DEV void sfor(F&& f) { if constexpr (N > 0) { sfor<N - 1>(f); f(IC<N - 1>{}); } }
struct Ctx {
  gu32* ctrl;
  rsrc_t rs;
  lds_t L;
  int w, lane;
  int b, slot, att;
  unsigned ask;
  unsigned t0;
  unsigned budget;
};
DEV bool spend(Ctx& c) {
  const unsigned old = c.budget;
  asm volatile("s_cmp_lg_u32 %0, 0\n\ts_subb_u32 %0, %0, 0" : "+s"(c.budget) :: "scc");
  return old == 0u;
}
DEV unsigned wait_flag(Ctx& c, int flag, unsigned need) {
  gu32* f = c.ctrl + (C_FLAG + flag * 128) / 4;
  unsigned v;
#pragma unroll 1
  for (;;) {
    v = __builtin_amdgcn_readfirstlane(__hip_atomic_load(f, RLX_AGENT));
    if (v >= need || spend(c)) break;
    __builtin_amdgcn_s_sleep(2);
  }
  __builtin_amdgcn_fence(__ATOMIC_ACQUIRE, "wavefront");
  return v;
}
DEV void post_flag(Ctx& c, int flag, unsigned v, int off = 0) {
  st4s(mkrs((const void*)c.ctrl, CTRL_BYTES), (c.w | off | fresh_lane()) ? 1 << 30 : 0, C_FLAG + flag * 128, v);
}
constexpr unsigned CL_A = 1u << 16, CL_VOTED = 1u << 24, CL_FILLED = 1u << 28;
DEV void claim(Ctx& c, unsigned votes) {
  if (c.w != 0) return;
  if (c.att == 0) {
#pragma unroll 1
    while (__builtin_amdgcn_readfirstlane(__hip_atomic_load(c.ctrl + C_ECHO / 4 + c.b, RLX_AGENT)) != c.ask) {
      if (spend(c)) break;
      __builtin_amdgcn_s_sleep(2);
    }
  }
  if (fresh_lane() == 0) __hip_atomic_fetch_add(c.ctrl + (C_CLAIM + c.att * 1024) / 4 + c.slot, 1u + votes, RLX_AGENT);
}
DEV unsigned wait_verdict(Ctx& c) {
  unsigned v;
#pragma unroll 1
  for (;;) {
    v = __builtin_amdgcn_readfirstlane(__hip_atomic_load(c.ctrl + (C_VERDICT + c.att * 128) / 4, RLX_AGENT));
    if ((v & 1u) || spend(c)) break;
    __builtin_amdgcn_s_sleep(2);
  }
  __builtin_amdgcn_fence(__ATOMIC_ACQUIRE, "wavefront");
  if ((v & 3u) == 3u && c.att == 0) {
    asm volatile("s_waitcnt vmcnt(0) lgkmcnt(0)\n\ts_barrier\n\ts_mov_b64 s[0:1], %0\n\ts_mov_b32 s2, %1\n\tv_mov_b32 v0, %2\n\t"
                 "s_getpc_b64 s[4:5]\n1:\n\ts_sub_u32 s4, s4, 1b-mk_mega\n\ts_subb_u32 s5, s5, 0\n\ts_setpc_b64 s[4:5]"
                 :: "s"(__builtin_amdgcn_kernarg_segment_ptr()), "s"(c.b + 256), "v"(c.w * 64 + fresh_lane()) : "s0", "s1", "s2", "s4", "s5", "v0", "scc", "memory");
    __builtin_unreachable();
  }
  return v;
}
DEV unsigned wait_vote(Ctx& c) {
  unsigned v;
#pragma unroll 1
  for (;;) {
    v = __builtin_amdgcn_readfirstlane(__hip_atomic_load(c.ctrl + C_VOTE / 4, RLX_AGENT));
    if ((v & 1u) || spend(c)) break;
    __builtin_amdgcn_s_sleep(2);
  }
  return v;
}
DEV void serve(Ctx& c) {
  c.budget = SPIN_CAP;
  const int lane = fresh_lane();
  const rsrc_t rc = mkrs((const void*)c.ctrl, CTRL_BYTES);
  if (c.att == 0) {
#pragma unroll
    for (int i = C_CLAIM / 1024; i < C_ASK / 1024; ++i) st16s(rc, lane * 16, i * 1024, v4u{0, 0, 0, 0});
    VMWAIT(0);
  }
  unsigned vote = c.att ? wait_vote(c) : 0u;
  bool echo = c.att == 0, first = true;
  v4u last = {0u, 0u, 0u, 0u};
#pragma unroll 1
  for (;;) {
    asm volatile("" ::: "memory");
    const v4u cl = ld16s(rc, lane * 16, C_CLAIM + c.att * 1024);
    if (echo) {
      v4u e;
#pragma unroll
      for (int i = 0; i < 4; ++i) e[i] = __builtin_amdgcn_raw_buffer_load_b32(rc, (lane * 4 + i) * 64, C_ASK, AUX_SC1);
      if (first || __any(e[0] != last[0] || e[1] != last[1] || e[2] != last[2] || e[3] != last[3])) st16s(rc, lane * 16, C_ECHO, e);
      last = e; first = false;
    }
    unsigned n = 0, one = 1;
    int va = 0, nv = 0, nf = 0;
#pragma unroll
    for (int i = 0; i < 4; ++i) {
      n += cl[i] & 0xFFFFu; one &= (cl[i] & 0xFFFFu) == 1u;
      va += __popcll(__ballot(cl[i] & CL_A)); nv += __popcll(__ballot(cl[i] & CL_VOTED)); nf += __popcll(__ballot(cl[i] & CL_FILLED));
    }
#pragma unroll
    for (int o = 1; o < 64; o <<= 1) n += (unsigned)__shfl_xor((int)n, o);
    const bool all = __builtin_amdgcn_readfirstlane(n) >= 256u, good = __all(one);
    echo = echo && !all;
    if (!vote && (all || max(va, nv - va) > 32)) {
      vote = va > 32 ? 5u : 9u;
      if (lane == 0) __hip_atomic_store(c.ctrl + C_VOTE / 4, vote, RLX_AGENT);
    }
    if (all && (!good || nf >= 64)) {
      if (lane == 0) __hip_atomic_store(c.ctrl + (C_VERDICT + c.att * 128) / 4, vote | (good ? 0u : 2u), RLX_AGENT);
      break;
    }
    if (spend(c)) break;
  }
}
DEV int vote_a(unsigned v) { return (int)((v >> 2) & 1u); }
DEV int vote_b(unsigned v) { return (int)((v >> 3) & 1u); }

#define STAMP(slot) do {} while (0)
#define TICK_DECL
#define TICK(i) do {} while (0)
#define TICK_SUM(n) do {} while (0)
#define TICK_LDS(b) do {} while (0)
#define TICK_DUMP(b) do {} while (0)
#define TICK_OUT() do {} while (0)

DEV constexpr int krow(int rho8, int i) { return 32 * (rho8 >> 2) + 8 * (rho8 & 1) + 2 * ((rho8 >> 1) & 1) + i; }
DEV int krow_lane(int g) { return 16 * (g >> 1) + 4 * (g & 1); }
DEV unsigned prm(unsigned hi, unsigned lo, int odd) { return __builtin_amdgcn_perm(hi, lo, odd ? 0x07060302u : 0x05040100u); }

#define AT_ACC(M, n) do { const int n_ = (n); if (n_ == 0) M("{a[0:7]}"); if (n_ == 1) M("{a[8:15]}"); if (n_ == 2) M("{a[16:23]}"); if (n_ == 3) M("{a[24:31]}"); if (n_ == 4) M("{a[32:39]}"); if (n_ == 5) M("{a[40:47]}"); if (n_ == 6) M("{a[48:55]}"); if (n_ == 7) M("{a[56:63]}"); if (n_ == 8) M("{a[64:71]}"); if (n_ == 9) M("{a[72:79]}"); if (n_ == 10) M("{a[80:87]}"); if (n_ == 11) M("{a[88:95]}"); if (n_ == 12) M("{a[96:103]}"); if (n_ == 13) M("{a[104:111]}"); if (n_ == 14) M("{a[112:119]}"); if (n_ == 15) M("{a[120:127]}"); if (n_ == 16) M("{a[128:135]}"); if (n_ == 17) M("{a[136:143]}"); if (n_ == 18) M("{a[144:151]}"); if (n_ == 19) M("{a[152:159]}"); if (n_ == 20) M("{a[160:167]}"); if (n_ == 21) M("{a[168:175]}"); if (n_ == 22) M("{a[176:183]}"); if (n_ == 23) M("{a[184:191]}"); if (n_ == 24) M("{a[192:199]}"); if (n_ == 25) M("{a[200:207]}"); if (n_ == 26) M("{a[208:215]}"); if (n_ == 27) M("{a[216:223]}"); if (n_ == 28) M("{a[224:231]}"); if (n_ == 29) M("{a[232:239]}"); if (n_ == 30) M("{a[240:247]}"); if (n_ == 31) M("{a[248:255]}"); } while (0)
#define PIN_ACC0(x) asm volatile("" :: "{a[0:7]}"(x[0]), "{a[8:15]}"(x[1]), "{a[16:23]}"(x[2]), "{a[24:31]}"(x[3]), "{a[32:39]}"(x[4]), "{a[40:47]}"(x[5]), "{a[48:55]}"(x[6]), "{a[56:63]}"(x[7]), "{a[64:71]}"(x[8]), "{a[72:79]}"(x[9]), "{a[80:87]}"(x[10]), "{a[88:95]}"(x[11]), "{a[96:103]}"(x[12]), "{a[104:111]}"(x[13]), "{a[112:119]}"(x[14]), "{a[120:127]}"(x[15]))
#define PIN_ACC1(x) asm volatile("" :: "{a[128:135]}"(x[16]), "{a[136:143]}"(x[17]), "{a[144:151]}"(x[18]), "{a[152:159]}"(x[19]), "{a[160:167]}"(x[20]), "{a[168:175]}"(x[21]), "{a[176:183]}"(x[22]), "{a[184:191]}"(x[23]), "{a[192:199]}"(x[24]), "{a[200:207]}"(x[25]), "{a[208:215]}"(x[26]), "{a[216:223]}"(x[27]), "{a[224:231]}"(x[28]), "{a[232:239]}"(x[29]), "{a[240:247]}"(x[30]), "{a[248:255]}"(x[31]))
#define PIN_HOME(x) asm volatile("" :: "{v[32:39]}"(x[0]), "{v[40:47]}"(x[1]), "{v[48:55]}"(x[2]), "{v[56:63]}"(x[3]), "{v[64:71]}"(x[4]), "{v[72:79]}"(x[5]), "{v[80:87]}"(x[6]), "{v[88:95]}"(x[7]), "{v[96:103]}"(x[8]), "{v[104:111]}"(x[9]), "{v[112:119]}"(x[10]), "{v[120:127]}"(x[11]), "{v[128:135]}"(x[12]), "{v[136:143]}"(x[13]), "{v[144:151]}"(x[14]), "{v[152:159]}"(x[15]))
#define CL_32_100 "v32", "v33", "v34", "v35", "v36", "v37", "v38", "v39", "v40", "v41", "v42", "v43", "v44", "v45", "v46", "v47", "v48", "v49", "v50", "v51", "v52", "v53", "v54", "v55", "v56", "v57", "v58", "v59", "v60", "v61", "v62", "v63", "v64", "v65", "v66", "v67", "v68", "v69", "v70", "v71", "v72", "v73", "v74", "v75", "v76", "v77", "v78", "v79", "v80", "v81", "v82", "v83", "v84", "v85", "v86", "v87", "v88", "v89", "v90", "v91", "v92", "v93", "v94", "v95", "v96", "v97", "v98", "v99", "v100", "v101", "v102", "v103", "v104", "v105", "v106", "v107", "v108", "v109", "v110", "v111", "v112", "v113", "v114", "v115", "v116", "v117", "v118", "v119", "v120", "v121", "v122", "v123", "v124", "v125", "v126", "v127", "v128", "v129", "v130", "v131"
#define ACC_IN(x) "{a[0:31]}"(x[0]), "{a[32:63]}"(x[1]), "{a[64:95]}"(x[2]), "{a[96:127]}"(x[3]), "{a[128:159]}"(x[4]), "{a[160:191]}"(x[5]), "{a[192:223]}"(x[6]), "{a[224:255]}"(x[7])
#define CL_16_32 "v16", "v17", "v18", "v19", "v20", "v21", "v22", "v23", "v24", "v25", "v26", "v27", "v28", "v29", "v30", "v31"
#define CL_120_128 "v120", "v121", "v122", "v123", "v124", "v125", "v126", "v127"
#define CL_160_200 "v160", "v161", "v162", "v163", "v164", "v165", "v166", "v167", "v168", "v169", "v170", "v171", "v172", "v173", "v174", "v175", "v176", "v177", "v178", "v179", "v180", "v181", "v182", "v183", "v184", "v185", "v186", "v187", "v188", "v189", "v190", "v191", "v192", "v193", "v194", "v195", "v196", "v197", "v198", "v199"
#define CL_176_200 "v176", "v177", "v178", "v179", "v180", "v181", "v182", "v183", "v184", "v185", "v186", "v187", "v188", "v189", "v190", "v191", "v192", "v193", "v194", "v195", "v196", "v197", "v198", "v199"
#define CL_200_216 "v200", "v201", "v202", "v203", "v204", "v205", "v206", "v207", "v208", "v209", "v210", "v211", "v212", "v213", "v214", "v215"
#define CL_216_256 "v216", "v217", "v218", "v219", "v220", "v221", "v222", "v223", "v224", "v225", "v226", "v227", "v228", "v229", "v230", "v231", "v232", "v233", "v234", "v235", "v236", "v237", "v238", "v239", "v240", "v241", "v242", "v243", "v244", "v245", "v246", "v247", "v248", "v249", "v250", "v251", "v252", "v253", "v254", "v255"
#define CL_S "memory", "vcc", "scc", "s60", "s61", "s62", "s63", "s64", "s65", "s66", "s67" CL_T
#define TIMED_WAIT \
    "s_getpc_b64 s[62:63]\n\t" "1:\n\t" "s_memtime s[64:65]\n\t" "s_waitcnt lgkmcnt(0)\n\t" "s_sub_u32 s61, %0, s64\n\t" "s_cmp_gt_i32 s61, 124\n\t" "s_cbranch_scc1 1b\n\t" \
    "s_max_i32 s61, s61, 0\n\t" "s_and_b32 s61, s61, 124\n\t" "s_sub_u32 s61, 2f-1b, s61\n\t" "s_add_u32 s62, s62, s61\n\t" "s_addc_u32 s63, s63, 0\n\t" \
    "s_setpc_b64 s[62:63]\n\t" ".rept 31\n\t" "s_nop 0\n\t" ".endr\n\t" "2:\n\t"
#define QK_STEP0 \
    "v_xor_b32 v216, 0x400, %[mo]\n\t" "s_mov_b32 s60, 0\n\t" "s_mov_b32 s66, 0x3e0293ee\n\t" "s_mov_b32 s67, 0x3e0293ee\n\t" XT(80, 81) "s_add_u32 s61, %[kb], 0x1000\n\t" \
    "buffer_load_dwordx4 v[16:19], %[l1], %[rs], %[kb] offen offset:0 sc1\n\t" "buffer_load_dwordx4 v[20:23], %[l1], %[rs], %[kb] offen offset:1024 sc1\n\t" \
    "buffer_load_dwordx4 v[24:27], %[l1], %[rs], %[kb] offen offset:2048 sc1\n\t" "buffer_load_dwordx4 v[28:31], %[l1], %[rs], %[kb] offen offset:3072 sc1\n\t" \
    "buffer_load_dwordx4 v[200:203], %[l1], %[rs], s61 offen offset:0 sc1\n\t" "buffer_load_dwordx4 v[204:207], %[l1], %[rs], s61 offen offset:1024 sc1\n\t" \
    "buffer_load_dwordx4 v[208:211], %[l1], %[rs], s61 offen offset:2048 sc1\n\t" "buffer_load_dwordx4 v[212:215], %[l1], %[rs], s61 offen offset:3072 sc1\n\t" \
    "v_mov_b64 v[218:219], -1\n\t" "buffer_store_dwordx2 v[218:219], %[px], %[rs], %[pr] offen sc1\n\t" "buffer_store_dword v218, %[pm], %[rs], %[pr] offen sc1\n\t" \
    "s_waitcnt vmcnt(2)\n\t" XT(98, 99) "s_barrier\n\t" XT(82, 83) "10:\n\t" "buffer_load_dwordx4 v[240:243], %[mo], %[rs], %[sb] offen offset:0 sc1\n\t" \
    "v_mov_b64 v[224:225], 0\n\t" "v_mov_b64 v[226:227], 0\n\t" "v_mov_b64 v[228:229], 0\n\t" "v_mov_b64 v[230:231], 0\n\t" "v_mov_b64 v[232:233], 0\n\t" \
    "v_mov_b64 v[234:235], 0\n\t" "v_mov_b64 v[236:237], 0\n\t" "v_mov_b64 v[238:239], 0\n\t" "buffer_load_dwordx4 v[244:247], v216, %[rs], %[sb] offen offset:0 sc1\n\t" \
    "ds_read_b128 v[160:163], %[l0] offset:0\n\t" "ds_read_b128 v[164:167], %[l0] offset:1024\n\t" "ds_read_b128 v[168:171], %[l0] offset:2048\n\t" \
    "ds_read_b128 v[172:175], %[l0] offset:3072\n\t" "s_nop 3\n\t" "buffer_load_dwordx4 v[248:251], %[mo], %[rs], %[sb] offen offset:2048 sc1\n\t" \
    "ds_read_b128 v[176:179], %[l0] offset:4096\n\t" "ds_read_b128 v[180:183], %[l0] offset:5120\n\t" "ds_read_b128 v[184:187], %[l0] offset:6144\n\t" \
    "ds_read_b128 v[188:191], %[l0] offset:7168\n\t" "s_nop 3\n\t" "buffer_load_dwordx4 v[252:255], v216, %[rs], %[sb] offen offset:2048 sc1\n\t" "s_cmp_lg_u32 s61, 0\n\t" \
    "s_cbranch_scc0 177f\n\t" "ds_write_b128 %[l1], v[16:19] offset:16384\n\t" "ds_write_b128 %[l1], v[20:23] offset:17408\n\t" \
    "ds_write_b128 %[l1], v[24:27] offset:18432\n\t" "ds_write_b128 %[l1], v[28:31] offset:19456\n\t" "ds_write_b128 %[l1], v[200:203] offset:20480\n\t" \
    "ds_write_b128 %[l1], v[204:207] offset:21504\n\t" "ds_write_b128 %[l1], v[208:211] offset:22528\n\t" "ds_write_b128 %[l1], v[212:215] offset:23552\n\t" \
    "s_mov_b32 s61, 0\n\t" "s_waitcnt lgkmcnt(0)\n\t" "177:\n\t" "v_mov_b64 v[208:209], 0\n\t" "v_mov_b64 v[210:211], 0\n\t" "v_mov_b64 v[212:213], 0\n\t" \
    "v_mov_b64 v[214:215], 0\n\t" "s_waitcnt vmcnt(3)\n\t" XT(84, 85) "v_smfmac_f32_16x16x64_bf16 v[224:227], v[240:243], a[0:7], %[ix]\n\t" \
    "ds_write_b128 %[l1], v[240:243] offset:0\n\t" "v_smfmac_f32_16x16x64_bf16 v[228:231], v[240:243], a[8:15], %[ix]\n\t" "v_max3_u32 v217, v240, v241, v242\n\t" \
    "v_smfmac_f32_16x16x64_bf16 v[232:235], v[240:243], v[32:39], %[ix]\n\t" "s_waitcnt lgkmcnt(7)\n\t" \
    "v_smfmac_f32_16x16x64_bf16 v[236:239], v[240:243], v[160:167], %[ix]\n\t" "ds_read_b128 v[160:163], %[l0] offset:8192\n\t" \
    "ds_read_b128 v[164:167], %[l0] offset:9216\n\t" "s_waitcnt vmcnt(2)\n\t" "v_smfmac_f32_16x16x64_bf16 v[224:227], v[244:247], a[16:23], %[ix]\n\t" \
    "ds_write_b128 %[l1], v[244:247] offset:1024\n\t" "v_max3_u32 v217, v217, v243, v244\n\t" "v_smfmac_f32_16x16x64_bf16 v[228:231], v[244:247], a[24:31], %[ix]\n\t" \
    "v_max3_u32 v217, v217, v245, v246\n\t" "v_max_u32 v217, v217, v247\n\t" "v_smfmac_f32_16x16x64_bf16 v[232:235], v[244:247], v[40:47], %[ix]\n\t" \
    "v_cmp_eq_u32 vcc, -1, v217\n\t" "s_cmp_lg_u32 s60, 0\n\t" "s_waitcnt lgkmcnt(8)\n\t" "v_smfmac_f32_16x16x64_bf16 v[236:239], v[244:247], v[168:175], %[ix]\n\t" \
    "s_cbranch_vccnz 11f\n\t" "12:\n\t" "ds_read_b128 v[168:171], %[l0] offset:10240\n\t" "s_waitcnt vmcnt(1)\n\t" \
    "v_smfmac_f32_16x16x64_bf16 v[224:227], v[248:251], a[32:39], %[ix]\n\t" "ds_write_b128 %[l1], v[248:251] offset:2048\n\t" "s_waitcnt lgkmcnt(2)\n\t" \
    "v_smfmac_f32_16x16x64_bf16 v[228:231], v[248:251], a[40:47], %[ix]\n\t" "s_cbranch_scc1 13f\n\t" "s_barrier\n\t" "13:\n\t" XT(86, 87) \
    "v_smfmac_f32_16x16x64_bf16 v[232:235], v[248:251], v[48:55], %[ix]\n\t" "ds_read_b128 v[192:195], %[l1] offset:4096\n\t" "ds_read_b128 v[196:199], %[l1] offset:8192\n\t" \
    "v_smfmac_f32_16x16x64_bf16 v[236:239], v[248:251], v[176:183], %[ix]\n\t" "ds_read_b128 v[172:175], %[l0] offset:11264\n\t" \
    "ds_read_b128 v[176:179], %[l0] offset:12288\n\t" "s_waitcnt vmcnt(0)\n\t" "v_smfmac_f32_16x16x64_bf16 v[224:227], v[252:255], a[48:55], %[ix]\n\t" \
    "ds_write_b128 %[l1], v[252:255] offset:3072\n\t" "v_max3_u32 v217, v248, v249, v250\n\t" "v_smfmac_f32_16x16x64_bf16 v[228:231], v[252:255], a[56:63], %[ix]\n\t" \
    "ds_read_b128 v[180:183], %[l0] offset:13312\n\t" "v_max3_u32 v217, v217, v251, v252\n\t" "v_max3_u32 v217, v217, v253, v254\n\t" \
    "v_smfmac_f32_16x16x64_bf16 v[232:235], v[252:255], v[56:63], %[ix]\n\t" "v_max_u32 v217, v217, v255\n\t" "v_cmp_eq_u32 vcc, -1, v217\n\t" \
    "v_smfmac_f32_16x16x64_bf16 v[236:239], v[252:255], v[184:191], %[ix]\n\t" "s_cbranch_vccnz 14f\n\t" "15:\n\t" "ds_read_b128 v[240:243], %[l1] offset:12288\n\t" \
    "s_waitcnt lgkmcnt(6)\n\t" "v_smfmac_f32_16x16x64_bf16 v[224:227], v[192:195], a[64:71], %[ix]\n\t" "ds_read_b128 v[184:187], %[l0] offset:14336\n\t" \
    "ds_read_b128 v[188:191], %[l0] offset:15360\n\t" "v_smfmac_f32_16x16x64_bf16 v[228:231], v[192:195], a[72:79], %[ix]\n\t" \
    "ds_read_b128 v[244:247], %[l1] offset:5120\n\t" "s_waitcnt lgkmcnt(5)\n\t" "v_smfmac_f32_16x16x64_bf16 v[232:235], v[192:195], v[64:71], %[ix]\n\t" "s_barrier\n\t" \
    XT(88, 89) "v_smfmac_f32_16x16x64_bf16 v[236:239], v[192:195], v[160:167], %[ix]\n\t" "ds_read_b128 v[160:163], %[l0] offset:16384\n\t" \
    "ds_read_b128 v[164:167], %[l0] offset:17408\n\t" "v_smfmac_f32_16x16x64_bf16 v[224:227], v[196:199], a[80:87], %[ix]\n\t" \
    "ds_read_b128 v[248:251], %[l1] offset:9216\n\t" "ds_read_b128 v[252:255], %[l1] offset:13312\n\t" \
    "v_smfmac_f32_16x16x64_bf16 v[228:231], v[196:199], a[88:95], %[ix]\n\t" "ds_read_b128 v[192:195], %[l1] offset:6144\n\t" \
    "v_smfmac_f32_16x16x64_bf16 v[232:235], v[196:199], v[72:79], %[ix]\n\t" "ds_read_b128 v[16:19], %[kx] offset:0\n\t" \
    "v_smfmac_f32_16x16x64_bf16 v[236:239], v[196:199], v[168:175], %[ix]\n\t" "ds_read_b128 v[168:171], %[l0] offset:18432\n\t" \
    "ds_read_b128 v[172:175], %[l0] offset:19456\n\t" "s_waitcnt lgkmcnt(11)\n\t" "v_smfmac_f32_16x16x64_bf16 v[224:227], v[240:243], a[96:103], %[ix]\n\t" \
    "ds_read_b128 v[196:199], %[l1] offset:10240\n\t" "v_smfmac_f32_16x16x64_bf16 v[228:231], v[240:243], a[104:111], %[ix]\n\t" \
    "ds_read_b128 v[20:23], %[kx] offset:2048\n\t" "v_smfmac_f32_16x16x64_bf16 v[232:235], v[240:243], v[80:87], %[ix]\n\t" \
    "v_smfmac_f32_16x16x64_bf16 v[236:239], v[240:243], v[176:183], %[ix]\n\t" "ds_read_b128 v[176:179], %[l0] offset:20480\n\t" \
    "ds_read_b128 v[180:183], %[l0] offset:21504\n\t" "s_waitcnt lgkmcnt(12)\n\t" "v_smfmac_f32_16x16x64_bf16 v[224:227], v[244:247], a[112:119], %[ix]\n\t" \
    "ds_read_b128 v[240:243], %[l1] offset:14336\n\t" "ds_read_b128 v[24:27], %[kx] offset:4096\n\t" \
    "v_smfmac_f32_16x16x64_bf16 v[228:231], v[244:247], a[120:127], %[ix]\n\t" "v_smfmac_f32_16x16x64_bf16 v[232:235], v[244:247], v[88:95], %[ix]\n\t" \
    "v_smfmac_f32_16x16x64_bf16 v[236:239], v[244:247], v[184:191], %[ix]\n\t" "ds_read_b128 v[184:187], %[l0] offset:22528\n\t" \
    "ds_read_b128 v[188:191], %[l0] offset:23552\n\t" "s_waitcnt lgkmcnt(13)\n\t" "v_smfmac_f32_16x16x64_bf16 v[224:227], v[248:251], a[128:135], %[ix]\n\t" \
    "ds_read_b128 v[244:247], %[l1] offset:7168\n\t" "ds_read_b128 v[28:31], %[kx] offset:6144\n\t" "v_smfmac_f32_16x16x64_bf16 v[228:231], v[248:251], a[136:143], %[ix]\n\t" \
    "v_smfmac_f32_16x16x64_bf16 v[232:235], v[248:251], v[96:103], %[ix]\n\t" "ds_read_b128 v[200:203], %[kx] offset:256\n\t" \
    "v_smfmac_f32_16x16x64_bf16 v[236:239], v[248:251], v[160:167], %[ix]\n\t" "ds_read_b128 v[160:163], %[l0] offset:24576\n\t" \
    "ds_read_b128 v[164:167], %[l0] offset:25600\n\t" "s_waitcnt lgkmcnt(14)\n\t" "v_smfmac_f32_16x16x64_bf16 v[224:227], v[252:255], a[144:151], %[ix]\n\t" \
    "ds_read_b128 v[248:251], %[l1] offset:11264\n\t" "v_smfmac_f32_16x16x64_bf16 v[228:231], v[252:255], a[152:159], %[ix]\n\t" \
    "ds_read_b128 v[204:207], %[kx] offset:2304\n\t" "v_smfmac_f32_16x16x64_bf16 v[232:235], v[252:255], v[104:111], %[ix]\n\t" "s_waitcnt lgkmcnt(14)\n\t" \
    "v_smfmac_f32_16x16x64_bf16 v[236:239], v[252:255], v[168:175], %[ix]\n\t" "ds_read_b128 v[168:171], %[l0] offset:26624\n\t" \
    "ds_read_b128 v[172:175], %[l0] offset:27648\n\t" "v_smfmac_f32_16x16x64_bf16 v[224:227], v[192:195], a[160:167], %[ix]\n\t" \
    "ds_read_b128 v[252:255], %[l1] offset:15360\n\t" "v_smfmac_f32_16x16x64_bf16 v[228:231], v[192:195], a[168:175], %[ix]\n\t" \
    "v_smfmac_f32_16x16x64_bf16 v[224:227], v[196:199], a[176:183], %[ix]\n\t" "v_smfmac_f32_16x16x64_bf16 v[228:231], v[196:199], a[184:191], %[ix]\n\t" \
    "s_waitcnt lgkmcnt(13)\n\t" "v_smfmac_f32_16x16x64_bf16 v[224:227], v[240:243], a[192:199], %[ix]\n\t" \
    "v_smfmac_f32_16x16x64_bf16 v[228:231], v[240:243], a[200:207], %[ix]\n\t" "s_waitcnt lgkmcnt(9)\n\t" \
    "v_smfmac_f32_16x16x64_bf16 v[224:227], v[244:247], a[208:215], %[ix]\n\t" "v_smfmac_f32_16x16x64_bf16 v[228:231], v[244:247], a[216:223], %[ix]\n\t" \
    "s_waitcnt lgkmcnt(4)\n\t" "v_smfmac_f32_16x16x64_bf16 v[224:227], v[248:251], a[224:231], %[ix]\n\t" \
    "v_smfmac_f32_16x16x64_bf16 v[228:231], v[248:251], a[232:239], %[ix]\n\t" "s_waitcnt lgkmcnt(0)\n\t" \
    "v_smfmac_f32_16x16x64_bf16 v[224:227], v[252:255], a[240:247], %[ix]\n\t" "s_nop 1\n\t" "v_smfmac_f32_16x16x64_bf16 v[228:231], v[252:255], a[248:255], %[ix]\n\t" \
    XT(90, 91) "s_nop 1\n\t" "v_smfmac_f32_16x16x64_bf16 v[232:235], v[192:195], v[112:119], %[ix]\n\t" "s_nop 1\n\t" \
    "v_smfmac_f32_16x16x64_bf16 v[236:239], v[192:195], v[176:183], %[ix]\n\t" "ds_read_b128 v[176:179], %[l0] offset:28672\n\t" "v_add_f32 v224, v224, v225\n\t" \
    "v_add_f32 v226, v226, v227\n\t" "v_smfmac_f32_16x16x64_bf16 v[232:235], v[196:199], v[120:127], %[ix]\n\t" "ds_read_b128 v[180:183], %[l0] offset:29696\n\t" \
    "v_add_f32 v228, v228, v229\n\t" "v_add_f32 v230, v230, v231\n\t" "v_smfmac_f32_16x16x64_bf16 v[236:239], v[196:199], v[184:191], %[ix]\n\t" \
    "ds_read_b128 v[184:187], %[l0] offset:30720\n\t" "v_cvt_pk_bf16_f32 v218, v224, v228\n\t" "v_cvt_pk_bf16_f32 v219, v226, v230\n\t" \
    "v_smfmac_f32_16x16x64_bf16 v[232:235], v[240:243], v[128:135], %[ix]\n\t" "ds_write2_b32 %[qw], v218, v219 offset1:8\n\t" \
    "ds_read_b128 v[188:191], %[l0] offset:31744\n\t" "v_smfmac_f32_16x16x64_bf16 v[236:239], v[240:243], v[160:167], %[ix]\n\t" \
    "ds_read_b128 v[192:195], %[kx] offset:4352\n\t" "ds_read_b128 v[196:199], %[kx] offset:6400\n\t" \
    "v_smfmac_f32_16x16x64_bf16 v[232:235], v[244:247], v[136:143], %[ix]\n\t" "v_smfmac_f32_16x16x64_bf16 v[236:239], v[244:247], v[168:175], %[ix]\n\t" \
    "s_waitcnt lgkmcnt(3)\n\t" "s_barrier\n\t" "v_smfmac_f32_16x16x64_bf16 v[232:235], v[248:251], v[144:151], %[ix]\n\t" "ds_read_b128 v[216:219], %[l1] offset:24576\n\t" \
    "ds_read_b128 v[220:223], %[l1] offset:25600\n\t" "v_smfmac_f32_16x16x64_bf16 v[236:239], v[248:251], v[176:183], %[ix]\n\t" \
    "v_smfmac_f32_16x16x64_bf16 v[232:235], v[252:255], v[152:159], %[ix]\n\t" "s_waitcnt lgkmcnt(4)\n\t" \
    "v_smfmac_f32_16x16x64_bf16 v[236:239], v[252:255], v[184:191], %[ix]\n\t" XT(92, 93) "s_waitcnt lgkmcnt(1)\n\t" \
    "v_smfmac_f32_16x16x64_bf16 v[208:211], v[216:219], v[16:23], %[ix]\n\t" "s_nop 1\n\t" "v_smfmac_f32_16x16x64_bf16 v[212:215], v[216:219], v[200:207], %[ix]\n\t" \
    "s_waitcnt lgkmcnt(0)\n\t" "s_nop 0\n\t" "v_add_f32 v232, v232, v233\n\t" "v_add_f32 v234, v234, v235\n\t" "v_add_f32 v236, v236, v237\n\t" \
    "v_add_f32 v238, v238, v239\n\t" "v_cvt_pk_bf16_f32 v232, v232, v236\n\t" "v_cvt_pk_bf16_f32 v233, v234, v238\n\t" "ds_write2_b32 %[kw], v232, v233 offset1:68\n\t" \
    "v_smfmac_f32_16x16x64_bf16 v[208:211], v[220:223], v[24:31], %[ix]\n\t" "v_smfmac_f32_16x16x64_bf16 v[212:215], v[220:223], v[192:199], %[ix]\n\t" \
    "v_mov_b64 v[224:225], 0\n\t" "v_mov_b64 v[226:227], 0\n\t" "s_waitcnt lgkmcnt(0)\n\t" "s_barrier\n\t" XT(94, 95) "ds_read_b128 v[160:163], %[km] offset:0\n\t" \
    "ds_read_b128 v[164:167], %[km] offset:64\n\t" "ds_read_b128 v[168:171], %[km] offset:128\n\t" "ds_read_b128 v[172:175], %[km] offset:192\n\t" \
    "v_add_f32 v178, v208, v209\n\t" "v_add_f32 v179, v210, v211\n\t" "v_add_f32 v180, v212, v213\n\t" "v_add_f32 v181, v214, v215\n\t" "v_max_f32 v182, v178, v180\n\t" \
    "v_max_f32 v183, v179, v181\n\t" "s_nop 0\n\t" "v_max_f32_dpp v182, v182, v182 row_ror:8 row_mask:0xf bank_mask:0xf\n\t" \
    "v_max_f32_dpp v183, v183, v183 row_ror:8 row_mask:0xf bank_mask:0xf\n\t" "s_nop 0\n\t" "v_max_f32_dpp v182, v182, v182 row_ror:4 row_mask:0xf bank_mask:0xf\n\t" \
    "v_max_f32_dpp v183, v183, v183 row_ror:4 row_mask:0xf bank_mask:0xf\n\t" "s_nop 0\n\t" "v_max_f32_dpp v182, v182, v182 row_ror:2 row_mask:0xf bank_mask:0xf\n\t" \
    "v_max_f32_dpp v183, v183, v183 row_ror:2 row_mask:0xf bank_mask:0xf\n\t" "s_nop 0\n\t" "v_max_f32_dpp v182, v182, v182 row_ror:1 row_mask:0xf bank_mask:0xf\n\t" \
    "v_max_f32_dpp v183, v183, v183 row_ror:1 row_mask:0xf bank_mask:0xf\n\t" "s_waitcnt lgkmcnt(2)\n\t" \
    "v_smfmac_f32_16x16x64_bf16 v[224:227], v[216:219], v[160:167], %[ix]\n\t" "s_waitcnt lgkmcnt(0)\n\t" \
    "v_smfmac_f32_16x16x64_bf16 v[224:227], v[220:223], v[168:175], %[ix]\n\t" "s_nop 7\n\t" "v_add_f32 v176, v224, v225\n\t" "v_add_f32 v177, v226, v227\n\t" \
    "v_max_f32 v182, v176, v182\n\t" "v_max_f32 v183, v177, v183\n\t" "s_nop 0\n\t" "v_max_f32_dpp v182, v182, v182 row_ror:4 row_mask:0xf bank_mask:0xf\n\t" \
    "v_max_f32_dpp v183, v183, v183 row_ror:4 row_mask:0xf bank_mask:0xf\n\t" "s_nop 0\n\t" "v_max_f32_dpp v182, v182, v182 row_ror:2 row_mask:0xf bank_mask:0xf\n\t" \
    "v_max_f32_dpp v183, v183, v183 row_ror:2 row_mask:0xf bank_mask:0xf\n\t" "s_nop 0\n\t" "v_max_f32_dpp v182, v182, v182 row_ror:1 row_mask:0xf bank_mask:0xf\n\t" \
    "v_max_f32_dpp v183, v183, v183 row_ror:1 row_mask:0xf bank_mask:0xf\n\t" "v_pk_add_f32 v[178:179], v[178:179], v[182:183] neg_lo:[0,1] neg_hi:[0,1]\n\t" \
    "v_pk_add_f32 v[180:181], v[180:181], v[182:183] neg_lo:[0,1] neg_hi:[0,1]\n\t" "v_pk_add_f32 v[176:177], v[176:177], v[182:183] neg_lo:[0,1] neg_hi:[0,1]\n\t" \
    "v_pk_mul_f32 v[178:179], v[178:179], s[66:67]\n\t" "v_pk_mul_f32 v[180:181], v[180:181], s[66:67]\n\t" "v_pk_mul_f32 v[176:177], v[176:177], s[66:67]\n\t" \
    "v_exp_f32 v178, v178\n\t" "v_exp_f32 v179, v179\n\t" "v_exp_f32 v180, v180\n\t" "v_exp_f32 v181, v181\n\t" "v_exp_f32 v176, v176\n\t" \
    "v_pk_add_f32 v[182:183], v[178:179], v[180:181]\n\t" "v_exp_f32 v177, v177\n\t" "s_nop 0\n\t" "v_add_f32_dpp v182, v182, v182 row_ror:8 row_mask:0xf bank_mask:0xf\n\t" \
    "v_add_f32_dpp v183, v183, v183 row_ror:8 row_mask:0xf bank_mask:0xf\n\t" "v_add_f32 v182, v182, v176\n\t" "v_add_f32 v183, v183, v177\n\t" "s_nop 0\n\t" \
    "v_add_f32_dpp v182, v182, v182 row_ror:4 row_mask:0xf bank_mask:0xf\n\t" "v_add_f32_dpp v183, v183, v183 row_ror:4 row_mask:0xf bank_mask:0xf\n\t" "s_nop 0\n\t" \
    "v_add_f32_dpp v182, v182, v182 row_ror:2 row_mask:0xf bank_mask:0xf\n\t" "v_add_f32_dpp v183, v183, v183 row_ror:2 row_mask:0xf bank_mask:0xf\n\t" "s_nop 0\n\t" \
    "v_add_f32_dpp v182, v182, v182 row_ror:1 row_mask:0xf bank_mask:0xf\n\t" "v_add_f32_dpp v183, v183, v183 row_ror:1 row_mask:0xf bank_mask:0xf\n\t" \
    "v_rcp_f32 v182, v182\n\t" "v_rcp_f32 v183, v183\n\t" "s_nop 0\n\t" "v_pk_mul_f32 v[176:177], v[176:177], v[182:183]\n\t" \
    "v_pk_mul_f32 v[178:179], v[178:179], v[182:183]\n\t" "v_pk_mul_f32 v[180:181], v[180:181], v[182:183]\n\t" "v_cvt_pk_bf16_f32 v176, v176, v177\n\t" \
    "v_cvt_pk_bf16_f32 v178, v178, v179\n\t" "v_cvt_pk_bf16_f32 v180, v180, v181\n\t" "v_mov_b32_dpp v177, v176 quad_perm:[1,0,3,2] row_mask:0xf bank_mask:0xf\n\t" \
    "v_mov_b32_dpp v179, v178 quad_perm:[1,0,3,2] row_mask:0xf bank_mask:0xf\n\t" "v_mov_b32_dpp v181, v180 quad_perm:[1,0,3,2] row_mask:0xf bank_mask:0xf\n\t" \
    "v_perm_b32 v184, v179, v178, %[ps]\n\t" "v_perm_b32 v185, v181, v180, %[ps]\n\t" "buffer_store_dwordx2 v[184:185], %[px], %[rs], %[pb] offen sc1\n\t" \
    "v_perm_b32 v186, v177, v176, %[ps]\n\t" "buffer_store_dword v186, %[pm], %[rs], %[pb] offen sc1\n\t" XT(96, 97) "s_branch 99f\n\t" "11:\n\t" "s_cmp_eq_u32 %[bd], 0\n\t" \
    "s_cbranch_scc1 16f\n\t" "s_sub_u32 %[bd], %[bd], 1\n\t" XCNT(100) "s_waitcnt lgkmcnt(0)\n\t" "s_nop 7\n\t" "s_branch 10b\n\t" "16:\n\t" "s_cmp_lg_u32 s60, 0\n\t" \
    "s_branch 12b\n\t" "14:\n\t" "s_cmp_eq_u32 %[bd], 0\n\t" "s_cbranch_scc1 15b\n\t" "s_sub_u32 %[bd], %[bd], 1\n\t" XCNT(101) "s_mov_b32 s60, 1\n\t" \
    "s_waitcnt lgkmcnt(0)\n\t" "s_nop 7\n\t" "s_branch 10b\n\t" "99:\n\t"
#define QK_STEP1 \
    "v_xor_b32 v216, 0x400, %[mo]\n\t" "s_mov_b32 s60, 0\n\t" "s_add_u32 s61, %[qb], 0x1000\n\t" "s_mov_b32 s66, 0x3e0293ee\n\t" "s_mov_b32 s67, 0x3e0293ee\n\t" XT(80, 81) \
    XT(98, 99) "s_barrier\n\t" XT(82, 83) "20:\n\t" "buffer_load_dwordx4 v[240:243], %[mo], %[rs], %[sb] offen offset:0 sc1\n\t" "v_mov_b64 v[224:225], 0\n\t" \
    "v_mov_b64 v[226:227], 0\n\t" "v_mov_b64 v[228:229], 0\n\t" "v_mov_b64 v[230:231], 0\n\t" "v_mov_b64 v[232:233], 0\n\t" "v_mov_b64 v[234:235], 0\n\t" \
    "v_mov_b64 v[236:237], 0\n\t" "v_mov_b64 v[238:239], 0\n\t" "buffer_load_dwordx4 v[244:247], v216, %[rs], %[sb] offen offset:0 sc1\n\t" \
    "ds_read_b128 v[160:163], %[l0] offset:0\n\t" "ds_read_b128 v[164:167], %[l0] offset:1024\n\t" "ds_read_b128 v[168:171], %[l0] offset:2048\n\t" \
    "ds_read_b128 v[172:175], %[l0] offset:3072\n\t" "s_nop 3\n\t" "buffer_load_dwordx4 v[248:251], %[mo], %[rs], %[sb] offen offset:2048 sc1\n\t" \
    "ds_read_b128 v[176:179], %[l0] offset:4096\n\t" "ds_read_b128 v[180:183], %[l0] offset:5120\n\t" "ds_read_b128 v[184:187], %[l0] offset:6144\n\t" \
    "ds_read_b128 v[188:191], %[l0] offset:7168\n\t" "s_nop 3\n\t" "buffer_load_dwordx4 v[252:255], v216, %[rs], %[sb] offen offset:2048 sc1\n\t" \
    "v_mov_b32 v208, 0xd028965f\n\t" "v_mov_b32 v209, 0xff800000\n\t" "s_mov_b32 s62, 0\n\t" "s_mov_b32 s63, -1\n\t" "v_mov_b32 v210, 0\n\t" "v_cmp_gt_i32 vcc, 0, %[ps]\n\t" \
    "v_cndmask_b32 v204, 0, v209, s[62:63]\n\t" "s_nop 0\n\t" "v_cndmask_b32 v200, 0, v208, vcc\n\t" "v_cmp_gt_i32 vcc, 1, %[ps]\n\t" \
    "v_cndmask_b32 v205, 0, v209, s[62:63]\n\t" "s_nop 0\n\t" "v_cndmask_b32 v201, 0, v208, vcc\n\t" "v_cmp_gt_i32 vcc, 2, %[ps]\n\t" \
    "v_cndmask_b32 v206, 0, v209, s[62:63]\n\t" "s_nop 0\n\t" "v_cndmask_b32 v202, 0, v208, vcc\n\t" "v_cmp_gt_i32 vcc, 3, %[ps]\n\t" \
    "v_cndmask_b32 v207, 0, v209, s[62:63]\n\t" "s_nop 0\n\t" "v_cndmask_b32 v203, 0, v208, vcc\n\t" "s_waitcnt vmcnt(3)\n\t" XT(84, 85) \
    "v_smfmac_f32_16x16x64_bf16 v[224:227], v[240:243], a[0:7], %[ix]\n\t" "ds_write_b128 %[l1], v[240:243] offset:4096\n\t" \
    "v_smfmac_f32_16x16x64_bf16 v[228:231], v[240:243], a[8:15], %[ix]\n\t" "v_max3_u32 v217, v240, v241, v242\n\t" \
    "v_smfmac_f32_16x16x64_bf16 v[232:235], v[240:243], v[32:39], %[ix]\n\t" "s_waitcnt lgkmcnt(7)\n\t" \
    "v_smfmac_f32_16x16x64_bf16 v[236:239], v[240:243], v[160:167], %[ix]\n\t" "ds_read_b128 v[160:163], %[l0] offset:8192\n\t" \
    "ds_read_b128 v[164:167], %[l0] offset:9216\n\t" "s_waitcnt vmcnt(2)\n\t" "v_smfmac_f32_16x16x64_bf16 v[224:227], v[244:247], a[16:23], %[ix]\n\t" \
    "ds_write_b128 %[l1], v[244:247] offset:5120\n\t" "v_max3_u32 v217, v217, v243, v244\n\t" "v_smfmac_f32_16x16x64_bf16 v[228:231], v[244:247], a[24:31], %[ix]\n\t" \
    "v_max3_u32 v217, v217, v245, v246\n\t" "v_max_u32 v217, v217, v247\n\t" "v_smfmac_f32_16x16x64_bf16 v[232:235], v[244:247], v[40:47], %[ix]\n\t" \
    "v_cmp_eq_u32 vcc, -1, v217\n\t" "s_cmp_lg_u32 s60, 0\n\t" "s_waitcnt lgkmcnt(8)\n\t" "v_smfmac_f32_16x16x64_bf16 v[236:239], v[244:247], v[168:175], %[ix]\n\t" \
    "s_cbranch_vccnz 21f\n\t" "22:\n\t" "ds_read_b128 v[168:171], %[l0] offset:10240\n\t" "s_waitcnt vmcnt(1)\n\t" \
    "v_smfmac_f32_16x16x64_bf16 v[224:227], v[248:251], a[32:39], %[ix]\n\t" "ds_write_b128 %[l1], v[248:251] offset:6144\n\t" "s_waitcnt lgkmcnt(2)\n\t" \
    "v_smfmac_f32_16x16x64_bf16 v[228:231], v[248:251], a[40:47], %[ix]\n\t" "s_cbranch_scc1 23f\n\t" "s_barrier\n\t" "23:\n\t" XT(86, 87) \
    "v_smfmac_f32_16x16x64_bf16 v[232:235], v[248:251], v[48:55], %[ix]\n\t" "ds_read_b128 v[192:195], %[l1] offset:8192\n\t" \
    "ds_read_b128 v[196:199], %[l1] offset:12288\n\t" "v_smfmac_f32_16x16x64_bf16 v[236:239], v[248:251], v[176:183], %[ix]\n\t" \
    "ds_read_b128 v[172:175], %[l0] offset:11264\n\t" "ds_read_b128 v[176:179], %[l0] offset:12288\n\t" "s_waitcnt vmcnt(0)\n\t" \
    "v_smfmac_f32_16x16x64_bf16 v[224:227], v[252:255], a[48:55], %[ix]\n\t" "ds_write_b128 %[l1], v[252:255] offset:7168\n\t" "v_max3_u32 v217, v248, v249, v250\n\t" \
    "v_smfmac_f32_16x16x64_bf16 v[228:231], v[252:255], a[56:63], %[ix]\n\t" "ds_read_b128 v[180:183], %[l0] offset:13312\n\t" "v_max3_u32 v217, v217, v251, v252\n\t" \
    "v_max3_u32 v217, v217, v253, v254\n\t" "v_smfmac_f32_16x16x64_bf16 v[232:235], v[252:255], v[56:63], %[ix]\n\t" "v_max_u32 v217, v217, v255\n\t" \
    "v_cmp_eq_u32 vcc, -1, v217\n\t" "v_smfmac_f32_16x16x64_bf16 v[236:239], v[252:255], v[184:191], %[ix]\n\t" "s_cbranch_vccnz 24f\n\t" "25:\n\t" \
    "ds_read_b128 v[240:243], %[l1] offset:0\n\t" "s_waitcnt lgkmcnt(6)\n\t" "v_smfmac_f32_16x16x64_bf16 v[224:227], v[192:195], a[64:71], %[ix]\n\t" \
    "ds_read_b128 v[184:187], %[l0] offset:14336\n\t" "ds_read_b128 v[188:191], %[l0] offset:15360\n\t" \
    "v_smfmac_f32_16x16x64_bf16 v[228:231], v[192:195], a[72:79], %[ix]\n\t" "ds_read_b128 v[244:247], %[l1] offset:9216\n\t" "s_waitcnt lgkmcnt(5)\n\t" \
    "v_smfmac_f32_16x16x64_bf16 v[232:235], v[192:195], v[64:71], %[ix]\n\t" "s_barrier\n\t" XT(88, 89) \
    "v_smfmac_f32_16x16x64_bf16 v[236:239], v[192:195], v[160:167], %[ix]\n\t" "ds_read_b128 v[160:163], %[l0] offset:16384\n\t" \
    "ds_read_b128 v[164:167], %[l0] offset:17408\n\t" "v_smfmac_f32_16x16x64_bf16 v[224:227], v[196:199], a[80:87], %[ix]\n\t" \
    "ds_read_b128 v[248:251], %[l1] offset:13312\n\t" "ds_read_b128 v[252:255], %[l1] offset:1024\n\t" \
    "v_smfmac_f32_16x16x64_bf16 v[228:231], v[196:199], a[88:95], %[ix]\n\t" "ds_read_b128 v[192:195], %[l1] offset:10240\n\t" \
    "v_smfmac_f32_16x16x64_bf16 v[232:235], v[196:199], v[72:79], %[ix]\n\t" "buffer_load_dwordx4 v[16:19], %[hq], %[rs], %[qb] offen offset:0 sc1\n\t" \
    "v_smfmac_f32_16x16x64_bf16 v[236:239], v[196:199], v[168:175], %[ix]\n\t" "ds_read_b128 v[168:171], %[l0] offset:18432\n\t" \
    "ds_read_b128 v[172:175], %[l0] offset:19456\n\t" "s_waitcnt lgkmcnt(10)\n\t" "v_smfmac_f32_16x16x64_bf16 v[224:227], v[240:243], a[96:103], %[ix]\n\t" \
    "ds_read_b128 v[196:199], %[l1] offset:14336\n\t" "v_smfmac_f32_16x16x64_bf16 v[228:231], v[240:243], a[104:111], %[ix]\n\t" \
    "v_smfmac_f32_16x16x64_bf16 v[232:235], v[240:243], v[80:87], %[ix]\n\t" "buffer_load_dwordx4 v[20:23], %[hq], %[rs], %[qb] offen offset:2048 sc1\n\t" \
    "v_smfmac_f32_16x16x64_bf16 v[236:239], v[240:243], v[176:183], %[ix]\n\t" "ds_read_b128 v[176:179], %[l0] offset:20480\n\t" \
    "ds_read_b128 v[180:183], %[l0] offset:21504\n\t" "s_waitcnt lgkmcnt(10)\n\t" "v_smfmac_f32_16x16x64_bf16 v[224:227], v[244:247], a[112:119], %[ix]\n\t" \
    "ds_read_b128 v[240:243], %[l1] offset:2048\n\t" "v_smfmac_f32_16x16x64_bf16 v[228:231], v[244:247], a[120:127], %[ix]\n\t" \
    "v_smfmac_f32_16x16x64_bf16 v[232:235], v[244:247], v[88:95], %[ix]\n\t" "buffer_load_dwordx4 v[24:27], %[hq], %[rs], s61 offen offset:0 sc1\n\t" \
    "v_smfmac_f32_16x16x64_bf16 v[236:239], v[244:247], v[184:191], %[ix]\n\t" "ds_read_b128 v[184:187], %[l0] offset:22528\n\t" \
    "ds_read_b128 v[188:191], %[l0] offset:23552\n\t" "s_waitcnt lgkmcnt(10)\n\t" "v_smfmac_f32_16x16x64_bf16 v[224:227], v[248:251], a[128:135], %[ix]\n\t" \
    "ds_read_b128 v[244:247], %[l1] offset:11264\n\t" "v_smfmac_f32_16x16x64_bf16 v[228:231], v[248:251], a[136:143], %[ix]\n\t" \
    "v_smfmac_f32_16x16x64_bf16 v[232:235], v[248:251], v[96:103], %[ix]\n\t" "buffer_load_dwordx4 v[28:31], %[hq], %[rs], s61 offen offset:2048 sc1\n\t" \
    "v_smfmac_f32_16x16x64_bf16 v[236:239], v[248:251], v[160:167], %[ix]\n\t" "ds_read_b128 v[160:163], %[l0] offset:24576\n\t" \
    "ds_read_b128 v[164:167], %[l0] offset:25600\n\t" "s_waitcnt lgkmcnt(12)\n\t" "v_smfmac_f32_16x16x64_bf16 v[224:227], v[252:255], a[144:151], %[ix]\n\t" \
    "ds_read_b128 v[248:251], %[l1] offset:15360\n\t" "v_smfmac_f32_16x16x64_bf16 v[228:231], v[252:255], a[152:159], %[ix]\n\t" \
    "v_smfmac_f32_16x16x64_bf16 v[232:235], v[252:255], v[104:111], %[ix]\n\t" "s_waitcnt lgkmcnt(10)\n\t" \
    "v_smfmac_f32_16x16x64_bf16 v[236:239], v[252:255], v[168:175], %[ix]\n\t" "ds_read_b128 v[168:171], %[l0] offset:26624\n\t" \
    "ds_read_b128 v[172:175], %[l0] offset:27648\n\t" "v_smfmac_f32_16x16x64_bf16 v[224:227], v[192:195], a[160:167], %[ix]\n\t" \
    "ds_read_b128 v[252:255], %[l1] offset:3072\n\t" "v_smfmac_f32_16x16x64_bf16 v[228:231], v[192:195], a[168:175], %[ix]\n\t" "s_waitcnt lgkmcnt(12)\n\t" \
    "v_smfmac_f32_16x16x64_bf16 v[224:227], v[196:199], a[176:183], %[ix]\n\t" "v_smfmac_f32_16x16x64_bf16 v[228:231], v[196:199], a[184:191], %[ix]\n\t" \
    "s_waitcnt lgkmcnt(9)\n\t" "v_smfmac_f32_16x16x64_bf16 v[224:227], v[240:243], a[192:199], %[ix]\n\t" \
    "v_smfmac_f32_16x16x64_bf16 v[228:231], v[240:243], a[200:207], %[ix]\n\t" "s_waitcnt lgkmcnt(6)\n\t" \
    "v_smfmac_f32_16x16x64_bf16 v[224:227], v[244:247], a[208:215], %[ix]\n\t" "v_smfmac_f32_16x16x64_bf16 v[228:231], v[244:247], a[216:223], %[ix]\n\t" \
    "s_waitcnt lgkmcnt(3)\n\t" "v_smfmac_f32_16x16x64_bf16 v[224:227], v[248:251], a[224:231], %[ix]\n\t" \
    "v_smfmac_f32_16x16x64_bf16 v[228:231], v[248:251], a[232:239], %[ix]\n\t" "s_waitcnt lgkmcnt(0)\n\t" \
    "v_smfmac_f32_16x16x64_bf16 v[224:227], v[252:255], a[240:247], %[ix]\n\t" "s_nop 1\n\t" "v_smfmac_f32_16x16x64_bf16 v[228:231], v[252:255], a[248:255], %[ix]\n\t" \
    XT(90, 91) "s_nop 1\n\t" "v_smfmac_f32_16x16x64_bf16 v[232:235], v[192:195], v[112:119], %[ix]\n\t" "s_nop 1\n\t" \
    "v_smfmac_f32_16x16x64_bf16 v[236:239], v[192:195], v[176:183], %[ix]\n\t" "ds_read_b128 v[176:179], %[l0] offset:28672\n\t" "v_add_f32 v224, v224, v225\n\t" \
    "v_add_f32 v226, v226, v227\n\t" "v_smfmac_f32_16x16x64_bf16 v[232:235], v[196:199], v[120:127], %[ix]\n\t" "ds_read_b128 v[180:183], %[l0] offset:29696\n\t" \
    "v_add_f32 v228, v228, v229\n\t" "v_add_f32 v230, v230, v231\n\t" "v_smfmac_f32_16x16x64_bf16 v[236:239], v[196:199], v[184:191], %[ix]\n\t" \
    "ds_read_b128 v[184:187], %[l0] offset:30720\n\t" "v_cvt_pk_bf16_f32 v218, v224, v228\n\t" "v_cvt_pk_bf16_f32 v219, v226, v230\n\t" \
    "v_smfmac_f32_16x16x64_bf16 v[232:235], v[240:243], v[128:135], %[ix]\n\t" "ds_write2_b32 %[qw], v218, v219 offset1:8\n\t" \
    "ds_read_b128 v[188:191], %[l0] offset:31744\n\t" "v_smfmac_f32_16x16x64_bf16 v[236:239], v[240:243], v[160:167], %[ix]\n\t" \
    "v_smfmac_f32_16x16x64_bf16 v[232:235], v[244:247], v[136:143], %[ix]\n\t" "v_smfmac_f32_16x16x64_bf16 v[236:239], v[244:247], v[168:175], %[ix]\n\t" \
    "s_waitcnt lgkmcnt(1)\n\t" "s_barrier\n\t" "v_smfmac_f32_16x16x64_bf16 v[232:235], v[248:251], v[144:151], %[ix]\n\t" \
    "v_smfmac_f32_16x16x64_bf16 v[236:239], v[248:251], v[176:183], %[ix]\n\t" "v_smfmac_f32_16x16x64_bf16 v[232:235], v[252:255], v[152:159], %[ix]\n\t" \
    "s_waitcnt lgkmcnt(0)\n\t" "v_smfmac_f32_16x16x64_bf16 v[236:239], v[252:255], v[184:191], %[ix]\n\t" XT(92, 93) "s_nop 6\n\t" "v_add_f32 v232, v232, v233\n\t" \
    "v_add_f32 v234, v234, v235\n\t" "v_add_f32 v236, v236, v237\n\t" "v_add_f32 v238, v238, v239\n\t" "v_cvt_pk_bf16_f32 v232, v232, v236\n\t" \
    "v_cvt_pk_bf16_f32 v233, v234, v238\n\t" "ds_write2_b32 %[kw], v232, v233 offset1:68\n\t" "s_waitcnt lgkmcnt(0)\n\t" "s_barrier\n\t" XT(94, 95) \
    "s_cmp_ge_u32 %[sq], %[sn]\n\t" "s_cbranch_scc1 277f\n\t" "275:\n\t" "global_load_dword v240, v210, %[fq] sc1\n\t" "s_waitcnt vmcnt(0)\n\t" \
    "v_readfirstlane_b32 %[sq], v240\n\t" "s_nop 3\n\t" "s_cmp_ge_u32 %[sq], %[sn]\n\t" "s_cbranch_scc1 276f\n\t" "s_cmp_eq_u32 %[bd], 0\n\t" "s_cbranch_scc1 276f\n\t" \
    "s_sub_u32 %[bd], %[bd], 1\n\t" "s_sleep 2\n\t" "s_branch 275b\n\t" "276:\n\t" "buffer_load_dwordx4 v[16:19], %[hq], %[rs], %[qb] offen offset:0 sc1\n\t" \
    "buffer_load_dwordx4 v[20:23], %[hq], %[rs], %[qb] offen offset:2048 sc1\n\t" "buffer_load_dwordx4 v[24:27], %[hq], %[rs], s61 offen offset:0 sc1\n\t" \
    "buffer_load_dwordx4 v[28:31], %[hq], %[rs], s61 offen offset:2048 sc1\n\t" "277:\n\t" "s_nop 3\n\t" "ds_read_b128 v[160:163], %[kx] offset:0\n\t" \
    "ds_read_b128 v[164:167], %[kx] offset:2048\n\t" "ds_read_b128 v[168:171], %[kx] offset:4096\n\t" "ds_read_b128 v[172:175], %[kx] offset:6144\n\t" \
    "ds_read_b128 v[224:227], %[km] offset:0\n\t" "ds_read_b128 v[228:231], %[km] offset:64\n\t" "ds_read_b128 v[232:235], %[km] offset:128\n\t" \
    "ds_read_b128 v[236:239], %[km] offset:192\n\t" "s_waitcnt vmcnt(0)\n\t" "global_load_dword v240, v210, %[fq] sc1\n\t" "s_waitcnt lgkmcnt(7)\n\t" \
    "v_mfma_f32_16x16x32_bf16 v[248:251], v[160:163], v[16:19], v[200:203]\n\t" "s_waitcnt lgkmcnt(6)\n\t" \
    "v_mfma_f32_16x16x32_bf16 v[248:251], v[164:167], v[20:23], v[248:251]\n\t" "s_waitcnt lgkmcnt(5)\n\t" \
    "v_mfma_f32_16x16x32_bf16 v[248:251], v[168:171], v[24:27], v[248:251]\n\t" "s_waitcnt lgkmcnt(4)\n\t" \
    "v_mfma_f32_16x16x32_bf16 v[248:251], v[172:175], v[28:31], v[248:251]\n\t" "s_waitcnt lgkmcnt(3)\n\t" \
    "v_mfma_f32_16x16x32_bf16 v[244:247], v[224:227], v[16:19], v[204:207]\n\t" "s_waitcnt lgkmcnt(2)\n\t" \
    "v_mfma_f32_16x16x32_bf16 v[244:247], v[228:231], v[20:23], v[244:247]\n\t" "s_waitcnt lgkmcnt(1)\n\t" \
    "v_mfma_f32_16x16x32_bf16 v[244:247], v[232:235], v[24:27], v[244:247]\n\t" "s_waitcnt lgkmcnt(0)\n\t" \
    "v_mfma_f32_16x16x32_bf16 v[244:247], v[236:239], v[28:31], v[244:247]\n\t" "v_mov_b32 v192, -1\n\t" "v_mov_b32 v193, -1\n\t" "v_mov_b32 v194, -1\n\t" \
    "v_mov_b32 v195, -1\n\t" "v_max3_f32 v242, v248, v249, v250\n\t" "v_max_f32 v242, v242, v251\n\t" "s_nop 1\n\t" "v_max3_f32 v242, v242, v244, v245\n\t" \
    "v_max3_f32 v242, v242, v246, v247\n\t" "v_mov_b32 v243, v242\n\t" "s_nop 1\n\t" "v_permlane32_swap_b32 v242, v243\n\t" "v_max_f32 v242, v242, v243\n\t" \
    "v_mov_b32 v243, v242\n\t" "s_nop 1\n\t" "v_permlane16_swap_b32 v242, v243\n\t" "v_max_f32 v242, v242, v243\n\t" \
    "v_pk_add_f32 v[244:245], v[244:245], v[242:243] op_sel_hi:[1,0] neg_lo:[0,1] neg_hi:[0,1]\n\t" \
    "v_pk_add_f32 v[246:247], v[246:247], v[242:243] op_sel_hi:[1,0] neg_lo:[0,1] neg_hi:[0,1]\n\t" \
    "v_pk_add_f32 v[248:249], v[248:249], v[242:243] op_sel_hi:[1,0] neg_lo:[0,1] neg_hi:[0,1]\n\t" \
    "v_pk_add_f32 v[250:251], v[250:251], v[242:243] op_sel_hi:[1,0] neg_lo:[0,1] neg_hi:[0,1]\n\t" "v_pk_mul_f32 v[244:245], v[244:245], s[66:67]\n\t" \
    "v_pk_mul_f32 v[246:247], v[246:247], s[66:67]\n\t" "v_pk_mul_f32 v[248:249], v[248:249], s[66:67]\n\t" "v_pk_mul_f32 v[250:251], v[250:251], s[66:67]\n\t" \
    "v_exp_f32 v244, v244\n\t" "v_exp_f32 v245, v245\n\t" "v_exp_f32 v246, v246\n\t" "v_exp_f32 v247, v247\n\t" "v_exp_f32 v248, v248\n\t" "v_exp_f32 v249, v249\n\t" \
    "v_exp_f32 v250, v250\n\t" "v_exp_f32 v251, v251\n\t" "v_pk_add_f32 v[242:243], v[244:245], v[246:247]\n\t" "v_pk_add_f32 v[242:243], v[242:243], v[248:249]\n\t" \
    "v_pk_add_f32 v[242:243], v[242:243], v[250:251]\n\t" "v_add_f32 v242, v242, v243\n\t" "v_mov_b32 v243, v242\n\t" "s_nop 1\n\t" "v_permlane32_swap_b32 v242, v243\n\t" \
    "v_add_f32 v242, v242, v243\n\t" "v_mov_b32 v243, v242\n\t" "s_nop 1\n\t" "v_permlane16_swap_b32 v242, v243\n\t" "v_add_f32 v242, v242, v243\n\t" \
    "v_rcp_f32 v242, v242\n\t" "s_nop 0\n\t" "v_pk_mul_f32 v[244:245], v[244:245], v[242:243] op_sel_hi:[1,0]\n\t" \
    "v_pk_mul_f32 v[246:247], v[246:247], v[242:243] op_sel_hi:[1,0]\n\t" "v_pk_mul_f32 v[248:249], v[248:249], v[242:243] op_sel_hi:[1,0]\n\t" \
    "v_pk_mul_f32 v[250:251], v[250:251], v[242:243] op_sel_hi:[1,0]\n\t" "v_cvt_pk_bf16_f32 v162, v244, v245\n\t" "v_cvt_pk_bf16_f32 v163, v246, v247\n\t" \
    "v_cvt_pk_bf16_f32 v164, v248, v249\n\t" "v_cvt_pk_bf16_f32 v165, v250, v251\n\t" "buffer_store_dwordx2 v[164:165], %[px], %[rs], %[tb] offen sc1\n\t" \
    "buffer_store_dwordx2 v[162:163], %[pm], %[rs], %[tb] offen sc1\n\t" "buffer_store_dwordx2 v[192:193], %[px], %[rs], %[tr] offen sc1\n\t" \
    "buffer_store_dwordx2 v[192:193], %[pm], %[rs], %[tr] offen sc1\n\t" "s_waitcnt vmcnt(4)\n\t" "v_readfirstlane_b32 s62, v240\n\t" "s_nop 3\n\t" \
    "s_max_u32 %[sq], %[sq], s62\n\t" XT(96, 97) "s_branch 99f\n\t" "21:\n\t" "s_cmp_eq_u32 %[bd], 0\n\t" "s_cbranch_scc1 26f\n\t" "s_sub_u32 %[bd], %[bd], 1\n\t" XCNT(100) \
    "s_waitcnt lgkmcnt(0)\n\t" "s_nop 7\n\t" "s_branch 20b\n\t" "26:\n\t" "s_cmp_lg_u32 s60, 0\n\t" "s_branch 22b\n\t" "24:\n\t" "s_cmp_eq_u32 %[bd], 0\n\t" \
    "s_cbranch_scc1 25b\n\t" "s_sub_u32 %[bd], %[bd], 1\n\t" XCNT(101) "s_mov_b32 s60, 1\n\t" "s_waitcnt lgkmcnt(0)\n\t" "s_nop 7\n\t" "s_branch 20b\n\t" "99:\n\t"
#define QK_STEP2 \
    "v_xor_b32 v216, 0x400, %[mo]\n\t" "s_mov_b32 s60, 0\n\t" "s_add_u32 s61, %[qb], 0x1000\n\t" "s_mov_b32 s66, 0x3e0293ee\n\t" "s_mov_b32 s67, 0x3e0293ee\n\t" XT(80, 81) \
    XT(98, 99) "s_barrier\n\t" XT(82, 83) "30:\n\t" "buffer_load_dwordx4 v[240:243], %[mo], %[rs], %[sb] offen offset:0 sc1\n\t" "v_mov_b64 v[224:225], 0\n\t" \
    "v_mov_b64 v[226:227], 0\n\t" "v_mov_b64 v[228:229], 0\n\t" "v_mov_b64 v[230:231], 0\n\t" "v_mov_b64 v[232:233], 0\n\t" "v_mov_b64 v[234:235], 0\n\t" \
    "v_mov_b64 v[236:237], 0\n\t" "v_mov_b64 v[238:239], 0\n\t" "buffer_load_dwordx4 v[244:247], v216, %[rs], %[sb] offen offset:0 sc1\n\t" \
    "ds_read_b128 v[160:163], %[l0] offset:0\n\t" "ds_read_b128 v[164:167], %[l0] offset:1024\n\t" "ds_read_b128 v[168:171], %[l0] offset:2048\n\t" \
    "ds_read_b128 v[172:175], %[l0] offset:3072\n\t" "s_nop 3\n\t" "buffer_load_dwordx4 v[248:251], %[mo], %[rs], %[sb] offen offset:2048 sc1\n\t" \
    "ds_read_b128 v[176:179], %[l0] offset:4096\n\t" "ds_read_b128 v[180:183], %[l0] offset:5120\n\t" "ds_read_b128 v[184:187], %[l0] offset:6144\n\t" \
    "ds_read_b128 v[188:191], %[l0] offset:7168\n\t" "s_nop 3\n\t" "buffer_load_dwordx4 v[252:255], v216, %[rs], %[sb] offen offset:2048 sc1\n\t" \
    "v_mov_b32 v208, 0xd028965f\n\t" "v_mov_b32 v209, 0xff800000\n\t" "s_mov_b32 s62, 0\n\t" "s_mov_b32 s63, -1\n\t" "v_mov_b32 v210, 0\n\t" "v_cmp_gt_i32 vcc, 0, %[ps]\n\t" \
    "v_cndmask_b32 v204, 0, v209, s[62:63]\n\t" "s_nop 0\n\t" "v_cndmask_b32 v200, 0, v208, vcc\n\t" "v_cmp_gt_i32 vcc, 1, %[ps]\n\t" \
    "v_cndmask_b32 v205, 0, v209, s[62:63]\n\t" "s_nop 0\n\t" "v_cndmask_b32 v201, 0, v208, vcc\n\t" "v_cmp_gt_i32 vcc, 2, %[ps]\n\t" \
    "v_cndmask_b32 v206, 0, v209, s[62:63]\n\t" "s_nop 0\n\t" "v_cndmask_b32 v202, 0, v208, vcc\n\t" "v_cmp_gt_i32 vcc, 3, %[ps]\n\t" \
    "v_cndmask_b32 v207, 0, v209, s[62:63]\n\t" "s_nop 0\n\t" "v_cndmask_b32 v203, 0, v208, vcc\n\t" "s_waitcnt vmcnt(3)\n\t" XT(84, 85) \
    "v_smfmac_f32_16x16x64_bf16 v[224:227], v[240:243], a[0:7], %[ix]\n\t" "ds_write_b128 %[l1], v[240:243] offset:8192\n\t" \
    "v_smfmac_f32_16x16x64_bf16 v[228:231], v[240:243], a[8:15], %[ix]\n\t" "v_max3_u32 v217, v240, v241, v242\n\t" \
    "v_smfmac_f32_16x16x64_bf16 v[232:235], v[240:243], v[32:39], %[ix]\n\t" "s_waitcnt lgkmcnt(7)\n\t" \
    "v_smfmac_f32_16x16x64_bf16 v[236:239], v[240:243], v[160:167], %[ix]\n\t" "ds_read_b128 v[160:163], %[l0] offset:8192\n\t" \
    "ds_read_b128 v[164:167], %[l0] offset:9216\n\t" "s_waitcnt vmcnt(2)\n\t" "v_smfmac_f32_16x16x64_bf16 v[224:227], v[244:247], a[16:23], %[ix]\n\t" \
    "ds_write_b128 %[l1], v[244:247] offset:9216\n\t" "v_max3_u32 v217, v217, v243, v244\n\t" "v_smfmac_f32_16x16x64_bf16 v[228:231], v[244:247], a[24:31], %[ix]\n\t" \
    "v_max3_u32 v217, v217, v245, v246\n\t" "v_max_u32 v217, v217, v247\n\t" "v_smfmac_f32_16x16x64_bf16 v[232:235], v[244:247], v[40:47], %[ix]\n\t" \
    "v_cmp_eq_u32 vcc, -1, v217\n\t" "s_cmp_lg_u32 s60, 0\n\t" "s_waitcnt lgkmcnt(8)\n\t" "v_smfmac_f32_16x16x64_bf16 v[236:239], v[244:247], v[168:175], %[ix]\n\t" \
    "s_cbranch_vccnz 31f\n\t" "32:\n\t" "ds_read_b128 v[168:171], %[l0] offset:10240\n\t" "s_waitcnt vmcnt(1)\n\t" \
    "v_smfmac_f32_16x16x64_bf16 v[224:227], v[248:251], a[32:39], %[ix]\n\t" "ds_write_b128 %[l1], v[248:251] offset:10240\n\t" "s_waitcnt lgkmcnt(2)\n\t" \
    "v_smfmac_f32_16x16x64_bf16 v[228:231], v[248:251], a[40:47], %[ix]\n\t" "s_cbranch_scc1 33f\n\t" "s_barrier\n\t" "33:\n\t" XT(86, 87) \
    "v_smfmac_f32_16x16x64_bf16 v[232:235], v[248:251], v[48:55], %[ix]\n\t" "ds_read_b128 v[192:195], %[l1] offset:12288\n\t" "ds_read_b128 v[196:199], %[l1] offset:0\n\t" \
    "v_smfmac_f32_16x16x64_bf16 v[236:239], v[248:251], v[176:183], %[ix]\n\t" "ds_read_b128 v[172:175], %[l0] offset:11264\n\t" \
    "ds_read_b128 v[176:179], %[l0] offset:12288\n\t" "s_waitcnt vmcnt(0)\n\t" "v_smfmac_f32_16x16x64_bf16 v[224:227], v[252:255], a[48:55], %[ix]\n\t" \
    "ds_write_b128 %[l1], v[252:255] offset:11264\n\t" "v_max3_u32 v217, v248, v249, v250\n\t" "v_smfmac_f32_16x16x64_bf16 v[228:231], v[252:255], a[56:63], %[ix]\n\t" \
    "ds_read_b128 v[180:183], %[l0] offset:13312\n\t" "v_max3_u32 v217, v217, v251, v252\n\t" "v_max3_u32 v217, v217, v253, v254\n\t" \
    "v_smfmac_f32_16x16x64_bf16 v[232:235], v[252:255], v[56:63], %[ix]\n\t" "v_max_u32 v217, v217, v255\n\t" "v_cmp_eq_u32 vcc, -1, v217\n\t" \
    "v_smfmac_f32_16x16x64_bf16 v[236:239], v[252:255], v[184:191], %[ix]\n\t" "s_cbranch_vccnz 34f\n\t" "35:\n\t" "ds_read_b128 v[240:243], %[l1] offset:4096\n\t" \
    "s_waitcnt lgkmcnt(6)\n\t" "v_smfmac_f32_16x16x64_bf16 v[224:227], v[192:195], a[64:71], %[ix]\n\t" "ds_read_b128 v[184:187], %[l0] offset:14336\n\t" \
    "ds_read_b128 v[188:191], %[l0] offset:15360\n\t" "v_smfmac_f32_16x16x64_bf16 v[228:231], v[192:195], a[72:79], %[ix]\n\t" \
    "ds_read_b128 v[244:247], %[l1] offset:13312\n\t" "s_waitcnt lgkmcnt(5)\n\t" "v_smfmac_f32_16x16x64_bf16 v[232:235], v[192:195], v[64:71], %[ix]\n\t" "s_barrier\n\t" \
    XT(88, 89) "v_smfmac_f32_16x16x64_bf16 v[236:239], v[192:195], v[160:167], %[ix]\n\t" "ds_read_b128 v[160:163], %[l0] offset:16384\n\t" \
    "ds_read_b128 v[164:167], %[l0] offset:17408\n\t" "v_smfmac_f32_16x16x64_bf16 v[224:227], v[196:199], a[80:87], %[ix]\n\t" \
    "ds_read_b128 v[248:251], %[l1] offset:1024\n\t" "ds_read_b128 v[252:255], %[l1] offset:5120\n\t" "v_smfmac_f32_16x16x64_bf16 v[228:231], v[196:199], a[88:95], %[ix]\n\t" \
    "ds_read_b128 v[192:195], %[l1] offset:14336\n\t" "v_smfmac_f32_16x16x64_bf16 v[232:235], v[196:199], v[72:79], %[ix]\n\t" \
    "buffer_load_dwordx4 v[16:19], %[hq], %[rs], %[qb] offen offset:0 sc1\n\t" "v_smfmac_f32_16x16x64_bf16 v[236:239], v[196:199], v[168:175], %[ix]\n\t" \
    "ds_read_b128 v[168:171], %[l0] offset:18432\n\t" "ds_read_b128 v[172:175], %[l0] offset:19456\n\t" "s_waitcnt lgkmcnt(10)\n\t" \
    "v_smfmac_f32_16x16x64_bf16 v[224:227], v[240:243], a[96:103], %[ix]\n\t" "ds_read_b128 v[196:199], %[l1] offset:2048\n\t" \
    "v_smfmac_f32_16x16x64_bf16 v[228:231], v[240:243], a[104:111], %[ix]\n\t" "v_smfmac_f32_16x16x64_bf16 v[232:235], v[240:243], v[80:87], %[ix]\n\t" \
    "buffer_load_dwordx4 v[20:23], %[hq], %[rs], %[qb] offen offset:2048 sc1\n\t" "v_smfmac_f32_16x16x64_bf16 v[236:239], v[240:243], v[176:183], %[ix]\n\t" \
    "ds_read_b128 v[176:179], %[l0] offset:20480\n\t" "ds_read_b128 v[180:183], %[l0] offset:21504\n\t" "s_waitcnt lgkmcnt(10)\n\t" \
    "v_smfmac_f32_16x16x64_bf16 v[224:227], v[244:247], a[112:119], %[ix]\n\t" "ds_read_b128 v[240:243], %[l1] offset:6144\n\t" \
    "v_smfmac_f32_16x16x64_bf16 v[228:231], v[244:247], a[120:127], %[ix]\n\t" "v_smfmac_f32_16x16x64_bf16 v[232:235], v[244:247], v[88:95], %[ix]\n\t" \
    "buffer_load_dwordx4 v[24:27], %[hq], %[rs], s61 offen offset:0 sc1\n\t" "v_smfmac_f32_16x16x64_bf16 v[236:239], v[244:247], v[184:191], %[ix]\n\t" \
    "ds_read_b128 v[184:187], %[l0] offset:22528\n\t" "ds_read_b128 v[188:191], %[l0] offset:23552\n\t" "s_waitcnt lgkmcnt(10)\n\t" \
    "v_smfmac_f32_16x16x64_bf16 v[224:227], v[248:251], a[128:135], %[ix]\n\t" "ds_read_b128 v[244:247], %[l1] offset:15360\n\t" \
    "v_smfmac_f32_16x16x64_bf16 v[228:231], v[248:251], a[136:143], %[ix]\n\t" "v_smfmac_f32_16x16x64_bf16 v[232:235], v[248:251], v[96:103], %[ix]\n\t" \
    "buffer_load_dwordx4 v[28:31], %[hq], %[rs], s61 offen offset:2048 sc1\n\t" "v_smfmac_f32_16x16x64_bf16 v[236:239], v[248:251], v[160:167], %[ix]\n\t" \
    "ds_read_b128 v[160:163], %[l0] offset:24576\n\t" "ds_read_b128 v[164:167], %[l0] offset:25600\n\t" "s_waitcnt lgkmcnt(12)\n\t" \
    "v_smfmac_f32_16x16x64_bf16 v[224:227], v[252:255], a[144:151], %[ix]\n\t" "ds_read_b128 v[248:251], %[l1] offset:3072\n\t" \
    "v_smfmac_f32_16x16x64_bf16 v[228:231], v[252:255], a[152:159], %[ix]\n\t" "v_smfmac_f32_16x16x64_bf16 v[232:235], v[252:255], v[104:111], %[ix]\n\t" \
    "s_waitcnt lgkmcnt(10)\n\t" "v_smfmac_f32_16x16x64_bf16 v[236:239], v[252:255], v[168:175], %[ix]\n\t" "ds_read_b128 v[168:171], %[l0] offset:26624\n\t" \
    "ds_read_b128 v[172:175], %[l0] offset:27648\n\t" "v_smfmac_f32_16x16x64_bf16 v[224:227], v[192:195], a[160:167], %[ix]\n\t" \
    "ds_read_b128 v[252:255], %[l1] offset:7168\n\t" "v_smfmac_f32_16x16x64_bf16 v[228:231], v[192:195], a[168:175], %[ix]\n\t" "s_waitcnt lgkmcnt(12)\n\t" \
    "v_smfmac_f32_16x16x64_bf16 v[224:227], v[196:199], a[176:183], %[ix]\n\t" "v_smfmac_f32_16x16x64_bf16 v[228:231], v[196:199], a[184:191], %[ix]\n\t" \
    "s_waitcnt lgkmcnt(9)\n\t" "v_smfmac_f32_16x16x64_bf16 v[224:227], v[240:243], a[192:199], %[ix]\n\t" \
    "v_smfmac_f32_16x16x64_bf16 v[228:231], v[240:243], a[200:207], %[ix]\n\t" "s_waitcnt lgkmcnt(6)\n\t" \
    "v_smfmac_f32_16x16x64_bf16 v[224:227], v[244:247], a[208:215], %[ix]\n\t" "v_smfmac_f32_16x16x64_bf16 v[228:231], v[244:247], a[216:223], %[ix]\n\t" \
    "s_waitcnt lgkmcnt(3)\n\t" "v_smfmac_f32_16x16x64_bf16 v[224:227], v[248:251], a[224:231], %[ix]\n\t" \
    "v_smfmac_f32_16x16x64_bf16 v[228:231], v[248:251], a[232:239], %[ix]\n\t" "s_waitcnt lgkmcnt(0)\n\t" \
    "v_smfmac_f32_16x16x64_bf16 v[224:227], v[252:255], a[240:247], %[ix]\n\t" "s_nop 1\n\t" "v_smfmac_f32_16x16x64_bf16 v[228:231], v[252:255], a[248:255], %[ix]\n\t" \
    XT(90, 91) "s_nop 1\n\t" "v_smfmac_f32_16x16x64_bf16 v[232:235], v[192:195], v[112:119], %[ix]\n\t" "s_nop 1\n\t" \
    "v_smfmac_f32_16x16x64_bf16 v[236:239], v[192:195], v[176:183], %[ix]\n\t" "ds_read_b128 v[176:179], %[l0] offset:28672\n\t" "v_add_f32 v224, v224, v225\n\t" \
    "v_add_f32 v226, v226, v227\n\t" "v_smfmac_f32_16x16x64_bf16 v[232:235], v[196:199], v[120:127], %[ix]\n\t" "ds_read_b128 v[180:183], %[l0] offset:29696\n\t" \
    "v_add_f32 v228, v228, v229\n\t" "v_add_f32 v230, v230, v231\n\t" "v_smfmac_f32_16x16x64_bf16 v[236:239], v[196:199], v[184:191], %[ix]\n\t" \
    "ds_read_b128 v[184:187], %[l0] offset:30720\n\t" "v_cvt_pk_bf16_f32 v218, v224, v228\n\t" "v_cvt_pk_bf16_f32 v219, v226, v230\n\t" \
    "v_smfmac_f32_16x16x64_bf16 v[232:235], v[240:243], v[128:135], %[ix]\n\t" "ds_write2_b32 %[qw], v218, v219 offset1:8\n\t" \
    "ds_read_b128 v[188:191], %[l0] offset:31744\n\t" "v_smfmac_f32_16x16x64_bf16 v[236:239], v[240:243], v[160:167], %[ix]\n\t" \
    "v_smfmac_f32_16x16x64_bf16 v[232:235], v[244:247], v[136:143], %[ix]\n\t" "v_smfmac_f32_16x16x64_bf16 v[236:239], v[244:247], v[168:175], %[ix]\n\t" \
    "s_waitcnt lgkmcnt(1)\n\t" "s_barrier\n\t" "v_smfmac_f32_16x16x64_bf16 v[232:235], v[248:251], v[144:151], %[ix]\n\t" \
    "v_smfmac_f32_16x16x64_bf16 v[236:239], v[248:251], v[176:183], %[ix]\n\t" "v_smfmac_f32_16x16x64_bf16 v[232:235], v[252:255], v[152:159], %[ix]\n\t" \
    "s_waitcnt lgkmcnt(0)\n\t" "v_smfmac_f32_16x16x64_bf16 v[236:239], v[252:255], v[184:191], %[ix]\n\t" XT(92, 93) "s_nop 6\n\t" "v_add_f32 v232, v232, v233\n\t" \
    "v_add_f32 v234, v234, v235\n\t" "v_add_f32 v236, v236, v237\n\t" "v_add_f32 v238, v238, v239\n\t" "v_cvt_pk_bf16_f32 v232, v232, v236\n\t" \
    "v_cvt_pk_bf16_f32 v233, v234, v238\n\t" "ds_write2_b32 %[kw], v232, v233 offset1:68\n\t" "s_waitcnt lgkmcnt(0)\n\t" "s_barrier\n\t" XT(94, 95) \
    "s_cmp_ge_u32 %[sq], %[sn]\n\t" "s_cbranch_scc1 377f\n\t" "375:\n\t" "global_load_dword v240, v210, %[fq] sc1\n\t" "s_waitcnt vmcnt(0)\n\t" \
    "v_readfirstlane_b32 %[sq], v240\n\t" "s_nop 3\n\t" "s_cmp_ge_u32 %[sq], %[sn]\n\t" "s_cbranch_scc1 376f\n\t" "s_cmp_eq_u32 %[bd], 0\n\t" "s_cbranch_scc1 376f\n\t" \
    "s_sub_u32 %[bd], %[bd], 1\n\t" "s_sleep 2\n\t" "s_branch 375b\n\t" "376:\n\t" "buffer_load_dwordx4 v[16:19], %[hq], %[rs], %[qb] offen offset:0 sc1\n\t" \
    "buffer_load_dwordx4 v[20:23], %[hq], %[rs], %[qb] offen offset:2048 sc1\n\t" "buffer_load_dwordx4 v[24:27], %[hq], %[rs], s61 offen offset:0 sc1\n\t" \
    "buffer_load_dwordx4 v[28:31], %[hq], %[rs], s61 offen offset:2048 sc1\n\t" "377:\n\t" "s_nop 3\n\t" "ds_read_b128 v[160:163], %[kx] offset:0\n\t" \
    "ds_read_b128 v[164:167], %[kx] offset:2048\n\t" "ds_read_b128 v[168:171], %[kx] offset:4096\n\t" "ds_read_b128 v[172:175], %[kx] offset:6144\n\t" \
    "ds_read_b128 v[176:179], %[kx] offset:256\n\t" "ds_read_b128 v[180:183], %[kx] offset:2304\n\t" "ds_read_b128 v[184:187], %[kx] offset:4352\n\t" \
    "ds_read_b128 v[188:191], %[kx] offset:6400\n\t" "ds_read_b128 v[224:227], %[km] offset:0\n\t" "ds_read_b128 v[228:231], %[km] offset:64\n\t" \
    "ds_read_b128 v[232:235], %[km] offset:128\n\t" "ds_read_b128 v[236:239], %[km] offset:192\n\t" "s_waitcnt vmcnt(0)\n\t" "global_load_dword v240, v210, %[fq] sc1\n\t" \
    "s_waitcnt lgkmcnt(11)\n\t" "v_mfma_f32_16x16x32_bf16 v[248:251], v[160:163], v[16:19], 0\n\t" "s_waitcnt lgkmcnt(10)\n\t" \
    "v_mfma_f32_16x16x32_bf16 v[248:251], v[164:167], v[20:23], v[248:251]\n\t" "s_waitcnt lgkmcnt(9)\n\t" \
    "v_mfma_f32_16x16x32_bf16 v[248:251], v[168:171], v[24:27], v[248:251]\n\t" "s_waitcnt lgkmcnt(8)\n\t" \
    "v_mfma_f32_16x16x32_bf16 v[248:251], v[172:175], v[28:31], v[248:251]\n\t" "s_waitcnt lgkmcnt(7)\n\t" \
    "v_mfma_f32_16x16x32_bf16 v[252:255], v[176:179], v[16:19], v[200:203]\n\t" "s_waitcnt lgkmcnt(6)\n\t" \
    "v_mfma_f32_16x16x32_bf16 v[252:255], v[180:183], v[20:23], v[252:255]\n\t" "s_waitcnt lgkmcnt(5)\n\t" \
    "v_mfma_f32_16x16x32_bf16 v[252:255], v[184:187], v[24:27], v[252:255]\n\t" "s_waitcnt lgkmcnt(4)\n\t" \
    "v_mfma_f32_16x16x32_bf16 v[252:255], v[188:191], v[28:31], v[252:255]\n\t" "s_waitcnt lgkmcnt(3)\n\t" \
    "v_mfma_f32_16x16x32_bf16 v[244:247], v[224:227], v[16:19], v[204:207]\n\t" "s_waitcnt lgkmcnt(2)\n\t" \
    "v_mfma_f32_16x16x32_bf16 v[244:247], v[228:231], v[20:23], v[244:247]\n\t" "s_waitcnt lgkmcnt(1)\n\t" \
    "v_mfma_f32_16x16x32_bf16 v[244:247], v[232:235], v[24:27], v[244:247]\n\t" "s_waitcnt lgkmcnt(0)\n\t" \
    "v_mfma_f32_16x16x32_bf16 v[244:247], v[236:239], v[28:31], v[244:247]\n\t" "v_mov_b32 v192, -1\n\t" "v_mov_b32 v193, -1\n\t" "v_mov_b32 v194, -1\n\t" \
    "v_mov_b32 v195, -1\n\t" "v_max3_f32 v242, v248, v249, v250\n\t" "v_max3_f32 v242, v242, v251, v252\n\t" "v_max3_f32 v242, v242, v253, v254\n\t" \
    "v_max_f32 v242, v242, v255\n\t" "v_max3_f32 v242, v242, v244, v245\n\t" "v_max3_f32 v242, v242, v246, v247\n\t" "v_mov_b32 v243, v242\n\t" "s_nop 1\n\t" \
    "v_permlane32_swap_b32 v242, v243\n\t" "v_max_f32 v242, v242, v243\n\t" "v_mov_b32 v243, v242\n\t" "s_nop 1\n\t" "v_permlane16_swap_b32 v242, v243\n\t" \
    "v_max_f32 v242, v242, v243\n\t" "v_pk_add_f32 v[244:245], v[244:245], v[242:243] op_sel_hi:[1,0] neg_lo:[0,1] neg_hi:[0,1]\n\t" \
    "v_pk_add_f32 v[246:247], v[246:247], v[242:243] op_sel_hi:[1,0] neg_lo:[0,1] neg_hi:[0,1]\n\t" \
    "v_pk_add_f32 v[248:249], v[248:249], v[242:243] op_sel_hi:[1,0] neg_lo:[0,1] neg_hi:[0,1]\n\t" \
    "v_pk_add_f32 v[250:251], v[250:251], v[242:243] op_sel_hi:[1,0] neg_lo:[0,1] neg_hi:[0,1]\n\t" \
    "v_pk_add_f32 v[252:253], v[252:253], v[242:243] op_sel_hi:[1,0] neg_lo:[0,1] neg_hi:[0,1]\n\t" \
    "v_pk_add_f32 v[254:255], v[254:255], v[242:243] op_sel_hi:[1,0] neg_lo:[0,1] neg_hi:[0,1]\n\t" "v_pk_mul_f32 v[244:245], v[244:245], s[66:67]\n\t" \
    "v_pk_mul_f32 v[246:247], v[246:247], s[66:67]\n\t" "v_pk_mul_f32 v[248:249], v[248:249], s[66:67]\n\t" "v_pk_mul_f32 v[250:251], v[250:251], s[66:67]\n\t" \
    "v_pk_mul_f32 v[252:253], v[252:253], s[66:67]\n\t" "v_pk_mul_f32 v[254:255], v[254:255], s[66:67]\n\t" "v_exp_f32 v244, v244\n\t" "v_exp_f32 v245, v245\n\t" \
    "v_exp_f32 v246, v246\n\t" "v_exp_f32 v247, v247\n\t" "v_exp_f32 v248, v248\n\t" "v_exp_f32 v249, v249\n\t" "v_exp_f32 v250, v250\n\t" "v_exp_f32 v251, v251\n\t" \
    "v_exp_f32 v252, v252\n\t" "v_exp_f32 v253, v253\n\t" "v_exp_f32 v254, v254\n\t" "v_exp_f32 v255, v255\n\t" "v_pk_add_f32 v[242:243], v[244:245], v[246:247]\n\t" \
    "v_pk_add_f32 v[242:243], v[242:243], v[248:249]\n\t" "v_pk_add_f32 v[242:243], v[242:243], v[250:251]\n\t" "v_pk_add_f32 v[242:243], v[242:243], v[252:253]\n\t" \
    "v_pk_add_f32 v[242:243], v[242:243], v[254:255]\n\t" "v_add_f32 v242, v242, v243\n\t" "v_mov_b32 v243, v242\n\t" "s_nop 1\n\t" "v_permlane32_swap_b32 v242, v243\n\t" \
    "v_add_f32 v242, v242, v243\n\t" "v_mov_b32 v243, v242\n\t" "s_nop 1\n\t" "v_permlane16_swap_b32 v242, v243\n\t" "v_add_f32 v242, v242, v243\n\t" \
    "v_rcp_f32 v242, v242\n\t" "s_nop 0\n\t" "v_pk_mul_f32 v[244:245], v[244:245], v[242:243] op_sel_hi:[1,0]\n\t" \
    "v_pk_mul_f32 v[246:247], v[246:247], v[242:243] op_sel_hi:[1,0]\n\t" "v_pk_mul_f32 v[248:249], v[248:249], v[242:243] op_sel_hi:[1,0]\n\t" \
    "v_pk_mul_f32 v[250:251], v[250:251], v[242:243] op_sel_hi:[1,0]\n\t" "v_pk_mul_f32 v[252:253], v[252:253], v[242:243] op_sel_hi:[1,0]\n\t" \
    "v_pk_mul_f32 v[254:255], v[254:255], v[242:243] op_sel_hi:[1,0]\n\t" "v_cvt_pk_bf16_f32 v162, v244, v245\n\t" "v_cvt_pk_bf16_f32 v163, v246, v247\n\t" \
    "v_cvt_pk_bf16_f32 v164, v248, v249\n\t" "v_cvt_pk_bf16_f32 v165, v250, v251\n\t" "v_cvt_pk_bf16_f32 v166, v252, v253\n\t" "v_cvt_pk_bf16_f32 v167, v254, v255\n\t" \
    "buffer_store_dwordx4 v[164:167], %[px], %[rs], %[tb] offen sc1\n\t" "buffer_store_dwordx2 v[162:163], %[pm], %[rs], %[tb] offen sc1\n\t" \
    "buffer_store_dwordx4 v[192:195], %[px], %[rs], %[tr] offen sc1\n\t" "buffer_store_dwordx2 v[192:193], %[pm], %[rs], %[tr] offen sc1\n\t" "s_waitcnt vmcnt(4)\n\t" \
    "v_readfirstlane_b32 s62, v240\n\t" "s_nop 3\n\t" "s_max_u32 %[sq], %[sq], s62\n\t" XT(96, 97) "s_branch 99f\n\t" "31:\n\t" "s_cmp_eq_u32 %[bd], 0\n\t" \
    "s_cbranch_scc1 36f\n\t" "s_sub_u32 %[bd], %[bd], 1\n\t" XCNT(100) "s_waitcnt lgkmcnt(0)\n\t" "s_nop 7\n\t" "s_branch 30b\n\t" "36:\n\t" "s_cmp_lg_u32 s60, 0\n\t" \
    "s_branch 32b\n\t" "34:\n\t" "s_cmp_eq_u32 %[bd], 0\n\t" "s_cbranch_scc1 35b\n\t" "s_sub_u32 %[bd], %[bd], 1\n\t" XCNT(101) "s_mov_b32 s60, 1\n\t" \
    "s_waitcnt lgkmcnt(0)\n\t" "s_nop 7\n\t" "s_branch 30b\n\t" "99:\n\t"
#define QK_STEP3 \
    "v_xor_b32 v216, 0x400, %[mo]\n\t" "s_mov_b32 s60, 0\n\t" XT(80, 81) "s_getpc_b64 s[62:63]\n\t" "48:\n\t" "s_memtime s[64:65]\n\t" "s_waitcnt lgkmcnt(0)\n\t" \
    "s_sub_u32 s61, %[tg], s64\n\t" "s_cmp_gt_i32 s61, 124\n\t" "s_cbranch_scc1 48b\n\t" "s_max_i32 s61, s61, 0\n\t" "s_and_b32 s61, s61, 124\n\t" \
    "s_sub_u32 s61, 49f-48b, s61\n\t" "s_add_u32 s62, s62, s61\n\t" "s_addc_u32 s63, s63, 0\n\t" "s_setpc_b64 s[62:63]\n\t" ".rept 31\n\t" "s_nop 0\n\t" ".endr\n\t" "49:\n\t" \
    XT(98, 99) "s_barrier\n\t" XT(82, 83) "40:\n\t" "buffer_load_dwordx4 v[240:243], %[mo], %[rs], %[sb] offen offset:0 sc1\n\t" "v_mov_b64 v[224:225], 0\n\t" \
    "v_mov_b64 v[226:227], 0\n\t" "v_mov_b64 v[228:229], 0\n\t" "v_mov_b64 v[230:231], 0\n\t" "v_mov_b64 v[232:233], 0\n\t" "v_mov_b64 v[234:235], 0\n\t" \
    "v_mov_b64 v[236:237], 0\n\t" "v_mov_b64 v[238:239], 0\n\t" "buffer_load_dwordx4 v[244:247], v216, %[rs], %[sb] offen offset:0 sc1\n\t" \
    "ds_read_b128 v[160:163], %[l0] offset:0\n\t" "ds_read_b128 v[164:167], %[l0] offset:1024\n\t" "ds_read_b128 v[168:171], %[l0] offset:2048\n\t" \
    "ds_read_b128 v[172:175], %[l0] offset:3072\n\t" "s_nop 3\n\t" "buffer_load_dwordx4 v[248:251], %[mo], %[rs], %[sb] offen offset:2048 sc1\n\t" \
    "ds_read_b128 v[176:179], %[l0] offset:4096\n\t" "ds_read_b128 v[180:183], %[l0] offset:5120\n\t" "ds_read_b128 v[184:187], %[l0] offset:6144\n\t" \
    "ds_read_b128 v[188:191], %[l0] offset:7168\n\t" "s_nop 3\n\t" "buffer_load_dwordx4 v[252:255], v216, %[rs], %[sb] offen offset:2048 sc1\n\t" "s_waitcnt vmcnt(3)\n\t" \
    XT(84, 85) "v_smfmac_f32_16x16x64_bf16 v[224:227], v[240:243], a[0:7], %[ix]\n\t" "ds_write_b128 %[l1], v[240:243] offset:12288\n\t" \
    "v_smfmac_f32_16x16x64_bf16 v[228:231], v[240:243], a[8:15], %[ix]\n\t" "v_max3_u32 v217, v240, v241, v242\n\t" \
    "v_smfmac_f32_16x16x64_bf16 v[232:235], v[240:243], v[32:39], %[ix]\n\t" "s_waitcnt lgkmcnt(7)\n\t" \
    "v_smfmac_f32_16x16x64_bf16 v[236:239], v[240:243], v[160:167], %[ix]\n\t" "ds_read_b128 v[160:163], %[l0] offset:8192\n\t" \
    "ds_read_b128 v[164:167], %[l0] offset:9216\n\t" "s_waitcnt vmcnt(2)\n\t" "v_smfmac_f32_16x16x64_bf16 v[224:227], v[244:247], a[16:23], %[ix]\n\t" \
    "ds_write_b128 %[l1], v[244:247] offset:13312\n\t" "v_max3_u32 v217, v217, v243, v244\n\t" "v_smfmac_f32_16x16x64_bf16 v[228:231], v[244:247], a[24:31], %[ix]\n\t" \
    "v_max3_u32 v217, v217, v245, v246\n\t" "v_max_u32 v217, v217, v247\n\t" "v_smfmac_f32_16x16x64_bf16 v[232:235], v[244:247], v[40:47], %[ix]\n\t" \
    "v_cmp_eq_u32 vcc, -1, v217\n\t" "s_cmp_lg_u32 s60, 0\n\t" "s_waitcnt lgkmcnt(8)\n\t" "v_smfmac_f32_16x16x64_bf16 v[236:239], v[244:247], v[168:175], %[ix]\n\t" \
    "s_cbranch_vccnz 41f\n\t" "42:\n\t" "ds_read_b128 v[168:171], %[l0] offset:10240\n\t" "s_waitcnt vmcnt(1)\n\t" \
    "v_smfmac_f32_16x16x64_bf16 v[224:227], v[248:251], a[32:39], %[ix]\n\t" "ds_write_b128 %[l1], v[248:251] offset:14336\n\t" "s_waitcnt lgkmcnt(2)\n\t" \
    "v_smfmac_f32_16x16x64_bf16 v[228:231], v[248:251], a[40:47], %[ix]\n\t" "s_cbranch_scc1 43f\n\t" "s_barrier\n\t" "43:\n\t" XT(86, 87) \
    "v_smfmac_f32_16x16x64_bf16 v[232:235], v[248:251], v[48:55], %[ix]\n\t" "ds_read_b128 v[192:195], %[l1] offset:0\n\t" "ds_read_b128 v[196:199], %[l1] offset:4096\n\t" \
    "v_smfmac_f32_16x16x64_bf16 v[236:239], v[248:251], v[176:183], %[ix]\n\t" "ds_read_b128 v[172:175], %[l0] offset:11264\n\t" \
    "ds_read_b128 v[176:179], %[l0] offset:12288\n\t" "s_waitcnt vmcnt(0)\n\t" "v_smfmac_f32_16x16x64_bf16 v[224:227], v[252:255], a[48:55], %[ix]\n\t" \
    "ds_write_b128 %[l1], v[252:255] offset:15360\n\t" "v_max3_u32 v217, v248, v249, v250\n\t" "v_smfmac_f32_16x16x64_bf16 v[228:231], v[252:255], a[56:63], %[ix]\n\t" \
    "ds_read_b128 v[180:183], %[l0] offset:13312\n\t" "v_max3_u32 v217, v217, v251, v252\n\t" "v_max3_u32 v217, v217, v253, v254\n\t" \
    "v_smfmac_f32_16x16x64_bf16 v[232:235], v[252:255], v[56:63], %[ix]\n\t" "v_max_u32 v217, v217, v255\n\t" "v_cmp_eq_u32 vcc, -1, v217\n\t" \
    "v_smfmac_f32_16x16x64_bf16 v[236:239], v[252:255], v[184:191], %[ix]\n\t" "s_cbranch_vccnz 44f\n\t" "45:\n\t" "ds_read_b128 v[240:243], %[l1] offset:8192\n\t" \
    "s_waitcnt lgkmcnt(6)\n\t" "v_smfmac_f32_16x16x64_bf16 v[224:227], v[192:195], a[64:71], %[ix]\n\t" "ds_read_b128 v[184:187], %[l0] offset:14336\n\t" \
    "ds_read_b128 v[188:191], %[l0] offset:15360\n\t" "v_smfmac_f32_16x16x64_bf16 v[228:231], v[192:195], a[72:79], %[ix]\n\t" \
    "ds_read_b128 v[244:247], %[l1] offset:1024\n\t" "s_waitcnt lgkmcnt(5)\n\t" "v_smfmac_f32_16x16x64_bf16 v[232:235], v[192:195], v[64:71], %[ix]\n\t" "s_barrier\n\t" \
    XT(88, 89) "s_memtime s[64:65]\n\t" "v_smfmac_f32_16x16x64_bf16 v[236:239], v[192:195], v[160:167], %[ix]\n\t" "ds_read_b128 v[160:163], %[l0] offset:16384\n\t" \
    "ds_read_b128 v[164:167], %[l0] offset:17408\n\t" "v_smfmac_f32_16x16x64_bf16 v[224:227], v[196:199], a[80:87], %[ix]\n\t" \
    "ds_read_b128 v[248:251], %[l1] offset:5120\n\t" "ds_read_b128 v[252:255], %[l1] offset:9216\n\t" "v_smfmac_f32_16x16x64_bf16 v[228:231], v[196:199], a[88:95], %[ix]\n\t" \
    "ds_read_b128 v[192:195], %[l1] offset:2048\n\t" "v_smfmac_f32_16x16x64_bf16 v[232:235], v[196:199], v[72:79], %[ix]\n\t" "s_sub_u32 s62, s64, %[ca]\n\t" \
    "s_mul_i32 s62, s62, %[k1]\n\t" "v_smfmac_f32_16x16x64_bf16 v[236:239], v[196:199], v[168:175], %[ix]\n\t" "s_lshr_b32 s62, s62, 16\n\t" "s_add_u32 s62, s62, %[ga]\n\t" \
    "s_waitcnt lgkmcnt(8)\n\t" "v_smfmac_f32_16x16x64_bf16 v[224:227], v[240:243], a[96:103], %[ix]\n\t" "s_bitset0_b32 s62, 0\n\t" "v_mov_b32 v218, s62\n\t" \
    "v_smfmac_f32_16x16x64_bf16 v[228:231], v[240:243], a[104:111], %[ix]\n\t" "buffer_store_dword v218, %[hq], %[rs], %[pb] offen sc1\n\t" \
    "ds_read_b128 v[168:171], %[l0] offset:18432\n\t" "v_smfmac_f32_16x16x64_bf16 v[232:235], v[240:243], v[80:87], %[ix]\n\t" \
    "ds_read_b128 v[172:175], %[l0] offset:19456\n\t" "ds_read_b128 v[196:199], %[l1] offset:6144\n\t" \
    "v_smfmac_f32_16x16x64_bf16 v[236:239], v[240:243], v[176:183], %[ix]\n\t" "ds_read_b128 v[176:179], %[l0] offset:20480\n\t" \
    "ds_read_b128 v[180:183], %[l0] offset:21504\n\t" "s_waitcnt lgkmcnt(10)\n\t" "v_smfmac_f32_16x16x64_bf16 v[224:227], v[244:247], a[112:119], %[ix]\n\t" \
    "ds_read_b128 v[240:243], %[l1] offset:10240\n\t" "v_smfmac_f32_16x16x64_bf16 v[228:231], v[244:247], a[120:127], %[ix]\n\t" \
    "v_smfmac_f32_16x16x64_bf16 v[232:235], v[244:247], v[88:95], %[ix]\n\t" "v_smfmac_f32_16x16x64_bf16 v[236:239], v[244:247], v[184:191], %[ix]\n\t" \
    "ds_read_b128 v[184:187], %[l0] offset:22528\n\t" "ds_read_b128 v[188:191], %[l0] offset:23552\n\t" "s_waitcnt lgkmcnt(10)\n\t" \
    "v_smfmac_f32_16x16x64_bf16 v[224:227], v[248:251], a[128:135], %[ix]\n\t" "ds_read_b128 v[244:247], %[l1] offset:3072\n\t" \
    "v_smfmac_f32_16x16x64_bf16 v[228:231], v[248:251], a[136:143], %[ix]\n\t" "v_smfmac_f32_16x16x64_bf16 v[232:235], v[248:251], v[96:103], %[ix]\n\t" \
    "v_smfmac_f32_16x16x64_bf16 v[236:239], v[248:251], v[160:167], %[ix]\n\t" "ds_read_b128 v[160:163], %[l0] offset:24576\n\t" \
    "ds_read_b128 v[164:167], %[l0] offset:25600\n\t" "s_waitcnt lgkmcnt(12)\n\t" "v_smfmac_f32_16x16x64_bf16 v[224:227], v[252:255], a[144:151], %[ix]\n\t" \
    "ds_read_b128 v[248:251], %[l1] offset:7168\n\t" "v_smfmac_f32_16x16x64_bf16 v[228:231], v[252:255], a[152:159], %[ix]\n\t" \
    "v_smfmac_f32_16x16x64_bf16 v[232:235], v[252:255], v[104:111], %[ix]\n\t" "s_waitcnt lgkmcnt(10)\n\t" \
    "v_smfmac_f32_16x16x64_bf16 v[236:239], v[252:255], v[168:175], %[ix]\n\t" "ds_read_b128 v[168:171], %[l0] offset:26624\n\t" \
    "ds_read_b128 v[172:175], %[l0] offset:27648\n\t" "v_smfmac_f32_16x16x64_bf16 v[224:227], v[192:195], a[160:167], %[ix]\n\t" \
    "ds_read_b128 v[252:255], %[l1] offset:11264\n\t" "v_smfmac_f32_16x16x64_bf16 v[228:231], v[192:195], a[168:175], %[ix]\n\t" "s_waitcnt lgkmcnt(12)\n\t" \
    "v_smfmac_f32_16x16x64_bf16 v[224:227], v[196:199], a[176:183], %[ix]\n\t" "v_smfmac_f32_16x16x64_bf16 v[228:231], v[196:199], a[184:191], %[ix]\n\t" \
    "s_waitcnt lgkmcnt(9)\n\t" "v_smfmac_f32_16x16x64_bf16 v[224:227], v[240:243], a[192:199], %[ix]\n\t" \
    "v_smfmac_f32_16x16x64_bf16 v[228:231], v[240:243], a[200:207], %[ix]\n\t" "s_waitcnt lgkmcnt(6)\n\t" \
    "v_smfmac_f32_16x16x64_bf16 v[224:227], v[244:247], a[208:215], %[ix]\n\t" "v_smfmac_f32_16x16x64_bf16 v[228:231], v[244:247], a[216:223], %[ix]\n\t" \
    "s_waitcnt lgkmcnt(3)\n\t" "v_smfmac_f32_16x16x64_bf16 v[224:227], v[248:251], a[224:231], %[ix]\n\t" \
    "v_smfmac_f32_16x16x64_bf16 v[228:231], v[248:251], a[232:239], %[ix]\n\t" "s_waitcnt lgkmcnt(0)\n\t" \
    "v_smfmac_f32_16x16x64_bf16 v[224:227], v[252:255], a[240:247], %[ix]\n\t" "s_nop 1\n\t" "v_smfmac_f32_16x16x64_bf16 v[228:231], v[252:255], a[248:255], %[ix]\n\t" \
    XT(90, 91) "s_nop 1\n\t" "v_smfmac_f32_16x16x64_bf16 v[232:235], v[192:195], v[112:119], %[ix]\n\t" "s_nop 1\n\t" \
    "v_smfmac_f32_16x16x64_bf16 v[236:239], v[192:195], v[176:183], %[ix]\n\t" "ds_read_b128 v[176:179], %[l0] offset:28672\n\t" "v_add_f32 v224, v224, v225\n\t" \
    "v_add_f32 v226, v226, v227\n\t" "v_smfmac_f32_16x16x64_bf16 v[232:235], v[196:199], v[120:127], %[ix]\n\t" "ds_read_b128 v[180:183], %[l0] offset:29696\n\t" \
    "v_add_f32 v228, v228, v229\n\t" "v_add_f32 v230, v230, v231\n\t" "v_smfmac_f32_16x16x64_bf16 v[236:239], v[196:199], v[184:191], %[ix]\n\t" \
    "ds_read_b128 v[184:187], %[l0] offset:30720\n\t" "v_cvt_pk_bf16_f32 v218, v224, v228\n\t" "v_cvt_pk_bf16_f32 v219, v226, v230\n\t" \
    "v_smfmac_f32_16x16x64_bf16 v[232:235], v[240:243], v[128:135], %[ix]\n\t" "ds_write2_b32 %[qw], v218, v219 offset1:8\n\t" \
    "ds_read_b128 v[188:191], %[l0] offset:31744\n\t" "v_smfmac_f32_16x16x64_bf16 v[236:239], v[240:243], v[160:167], %[ix]\n\t" \
    "v_smfmac_f32_16x16x64_bf16 v[232:235], v[244:247], v[136:143], %[ix]\n\t" "v_smfmac_f32_16x16x64_bf16 v[236:239], v[244:247], v[168:175], %[ix]\n\t" \
    "s_waitcnt lgkmcnt(1)\n\t" "s_barrier\n\t" "v_smfmac_f32_16x16x64_bf16 v[232:235], v[248:251], v[144:151], %[ix]\n\t" \
    "v_smfmac_f32_16x16x64_bf16 v[236:239], v[248:251], v[176:183], %[ix]\n\t" "v_smfmac_f32_16x16x64_bf16 v[232:235], v[252:255], v[152:159], %[ix]\n\t" \
    "s_waitcnt lgkmcnt(0)\n\t" "v_smfmac_f32_16x16x64_bf16 v[236:239], v[252:255], v[184:191], %[ix]\n\t" XT(92, 93) "s_nop 6\n\t" "v_add_f32 v232, v232, v233\n\t" \
    "v_add_f32 v234, v234, v235\n\t" "v_add_f32 v236, v236, v237\n\t" "v_add_f32 v238, v238, v239\n\t" "v_cvt_pk_bf16_f32 v232, v232, v236\n\t" \
    "v_cvt_pk_bf16_f32 v233, v234, v238\n\t" "ds_write2_b32 %[kw], v232, v233 offset1:68\n\t" "s_waitcnt lgkmcnt(0)\n\t" "s_barrier\n\t" XT(94, 95) XT(96, 97) \
    "s_branch 99f\n\t" "41:\n\t" "s_cmp_eq_u32 %[bd], 0\n\t" "s_cbranch_scc1 46f\n\t" "s_sub_u32 %[bd], %[bd], 1\n\t" XCNT(100) "s_waitcnt lgkmcnt(0)\n\t" "s_nop 7\n\t" \
    "s_branch 40b\n\t" "46:\n\t" "s_cmp_lg_u32 s60, 0\n\t" "s_branch 42b\n\t" "44:\n\t" "s_cmp_eq_u32 %[bd], 0\n\t" "s_cbranch_scc1 45b\n\t" "s_sub_u32 %[bd], %[bd], 1\n\t" \
    XCNT(101) "s_mov_b32 s60, 1\n\t" "s_waitcnt lgkmcnt(0)\n\t" "s_nop 7\n\t" "s_branch 40b\n\t" "99:\n\t" "mk_tail:\n\t"
#define VO_STEP \
    "v_xor_b32 v224, 0x400, %[mo]\n\t" "s_mov_b32 s60, 0\n\t" "s_cmp_eq_u32 %[w], 1\n\t" "s_cbranch_scc1 91f\n\t" "s_cmp_eq_u32 %[w], 2\n\t" "s_cbranch_scc1 92f\n\t" \
    "s_cmp_eq_u32 %[w], 3\n\t" "s_cbranch_scc1 93f\n\t" XT(80, 81) "s_getpc_b64 s[62:63]\n\t" "18:\n\t" "s_memtime s[64:65]\n\t" "s_waitcnt lgkmcnt(0)\n\t" \
    "s_sub_u32 s61, %[tg], s64\n\t" "s_cmp_gt_i32 s61, 124\n\t" "s_cbranch_scc1 18b\n\t" "s_max_i32 s61, s61, 0\n\t" "s_and_b32 s61, s61, 124\n\t" \
    "s_sub_u32 s61, 19f-18b, s61\n\t" "s_add_u32 s62, s62, s61\n\t" "s_addc_u32 s63, s63, 0\n\t" "s_setpc_b64 s[62:63]\n\t" ".rept 31\n\t" "s_nop 0\n\t" ".endr\n\t" "19:\n\t" \
    "s_barrier\n\t" XT(82, 83) "10:\n\t" "buffer_load_dwordx4 v[240:243], %[mo], %[rs], %[sb] offen offset:0 sc1\n\t" "v_mov_b64 v[232:233], 0\n\t" \
    "v_mov_b64 v[234:235], 0\n\t" "v_mov_b64 v[236:237], 0\n\t" "v_mov_b64 v[238:239], 0\n\t" "v_mov_b64 v[192:193], 0\n\t" "v_mov_b64 v[194:195], 0\n\t" \
    "v_mov_b64 v[196:197], 0\n\t" "v_mov_b64 v[198:199], 0\n\t" "buffer_load_dwordx4 v[244:247], v224, %[rs], %[sb] offen offset:0 sc1\n\t" \
    "ds_read_b128 v[160:163], %[l0] offset:0\n\t" "ds_read_b128 v[164:167], %[l0] offset:1024\n\t" "ds_read_b128 v[168:171], %[l0] offset:2048\n\t" \
    "ds_read_b128 v[172:175], %[l0] offset:3072\n\t" "s_nop 3\n\t" "buffer_load_dwordx4 v[248:251], %[mo], %[rs], %[sb] offen offset:2048 sc1\n\t" \
    "ds_read_b128 v[176:179], %[l0] offset:4096\n\t" "ds_read_b128 v[180:183], %[l0] offset:5120\n\t" "ds_read_b128 v[184:187], %[l0] offset:6144\n\t" \
    "ds_read_b128 v[188:191], %[l0] offset:7168\n\t" "s_nop 3\n\t" "buffer_load_dwordx4 v[252:255], v224, %[rs], %[sb] offen offset:2048 sc1\n\t" \
    "v_mov_b64 v[200:201], 0\n\t" "v_mov_b64 v[202:203], 0\n\t" "v_mov_b64 v[204:205], 0\n\t" "v_mov_b64 v[206:207], 0\n\t" "v_mov_b64 v[208:209], 0\n\t" \
    "v_mov_b64 v[210:211], 0\n\t" "v_mov_b64 v[212:213], 0\n\t" "v_mov_b64 v[214:215], 0\n\t" "v_mov_b64 v[216:217], 0\n\t" "v_mov_b64 v[218:219], 0\n\t" \
    "v_mov_b64 v[220:221], 0\n\t" "v_mov_b64 v[222:223], 0\n\t" "s_waitcnt vmcnt(3)\n\t" XT(84, 85) "v_smfmac_f32_16x16x64_bf16 v[232:235], v[240:243], a[0:7], %[ix]\n\t" \
    "ds_write_b128 %[l1], v[240:243] offset:0\n\t" "v_smfmac_f32_16x16x64_bf16 v[236:239], v[240:243], a[8:15], %[ix]\n\t" "v_max3_u32 v225, v240, v241, v242\n\t" \
    "s_waitcnt vmcnt(2)\n\t" "v_smfmac_f32_16x16x64_bf16 v[232:235], v[244:247], a[16:23], %[ix]\n\t" "ds_write_b128 %[l1], v[244:247] offset:1024\n\t" \
    "v_max3_u32 v225, v225, v243, v244\n\t" "s_waitcnt lgkmcnt(8)\n\t" "v_smfmac_f32_16x16x64_bf16 v[236:239], v[244:247], v[160:167], %[ix]\n\t" \
    "v_max3_u32 v225, v225, v245, v246\n\t" "v_max_u32 v225, v225, v247\n\t" "v_cmp_eq_u32 vcc, -1, v225\n\t" "s_cmp_lg_u32 s60, 0\n\t" "s_cbranch_vccnz 11f\n\t" "12:\n\t" \
    "s_waitcnt lgkmcnt(0)\n\t" "s_cbranch_scc1 13f\n\t" "s_barrier\n\t" "13:\n\t" XT(86, 87) "ds_read_b128 v[16:19], %[l1] offset:4096\n\t" \
    "ds_read_b128 v[20:23], %[l1] offset:8192\n\t" "ds_read_b128 v[24:27], %[l1] offset:12288\n\t" "s_waitcnt vmcnt(1)\n\t" \
    "v_smfmac_f32_16x16x64_bf16 v[232:235], v[248:251], a[24:31], %[ix]\n\t" "ds_write_b128 %[l1], v[248:251] offset:2048\n\t" "v_max3_u32 v225, v248, v249, v250\n\t" \
    "v_smfmac_f32_16x16x64_bf16 v[236:239], v[248:251], a[32:39], %[ix]\n\t" "ds_read_b128 v[28:31], %[l1] offset:5120\n\t" "ds_read_b128 v[240:243], %[l1] offset:9216\n\t" \
    "ds_read_b128 v[244:247], %[l1] offset:13312\n\t" "s_waitcnt vmcnt(0)\n\t" "v_smfmac_f32_16x16x64_bf16 v[232:235], v[252:255], a[40:47], %[ix]\n\t" \
    "ds_write_b128 %[l1], v[252:255] offset:3072\n\t" "v_max3_u32 v225, v225, v251, v252\n\t" "v_smfmac_f32_16x16x64_bf16 v[236:239], v[252:255], v[168:175], %[ix]\n\t" \
    "v_max3_u32 v225, v225, v253, v254\n\t" "v_max_u32 v225, v225, v255\n\t" "s_waitcnt lgkmcnt(7)\n\t" "v_smfmac_f32_16x16x64_bf16 v[232:235], v[16:19], a[48:55], %[ix]\n\t" \
    "v_cmp_eq_u32 vcc, -1, v225\n\t" "s_cbranch_vccnz 14f\n\t" "15:\n\t" "v_smfmac_f32_16x16x64_bf16 v[236:239], v[16:19], a[56:63], %[ix]\n\t" \
    "ds_read_b128 v[160:163], %[l0] offset:8192\n\t" "ds_read_b128 v[164:167], %[l0] offset:9216\n\t" "s_waitcnt lgkmcnt(8)\n\t" \
    "v_smfmac_f32_16x16x64_bf16 v[232:235], v[20:23], a[64:71], %[ix]\n\t" "ds_read_b128 v[168:171], %[l0] offset:10240\n\t" "ds_read_b128 v[172:175], %[l0] offset:11264\n\t" \
    "v_smfmac_f32_16x16x64_bf16 v[236:239], v[20:23], v[176:183], %[ix]\n\t" "s_waitcnt lgkmcnt(4)\n\t" "s_barrier\n\t" XT(88, 89) "s_memtime s[64:65]\n\t" \
    "v_smfmac_f32_16x16x64_bf16 v[232:235], v[24:27], a[72:79], %[ix]\n\t" "ds_read_b128 v[176:179], %[l0] offset:12288\n\t" "ds_read_b128 v[180:183], %[l0] offset:13312\n\t" \
    "v_smfmac_f32_16x16x64_bf16 v[236:239], v[24:27], a[80:87], %[ix]\n\t" "ds_read_b128 v[248:251], %[l1] offset:6144\n\t" "ds_read_b128 v[252:255], %[l1] offset:10240\n\t" \
    "v_smfmac_f32_16x16x64_bf16 v[232:235], v[28:31], a[88:95], %[ix]\n\t" "ds_read_b128 v[16:19], %[l1] offset:14336\n\t" "ds_read_b128 v[20:23], %[l1] offset:7168\n\t" \
    "v_smfmac_f32_16x16x64_bf16 v[236:239], v[28:31], v[184:191], %[ix]\n\t" "ds_read_b128 v[24:27], %[l1] offset:11264\n\t" "ds_read_b128 v[184:187], %[l0] offset:14336\n\t" \
    "v_smfmac_f32_16x16x64_bf16 v[232:235], v[240:243], a[96:103], %[ix]\n\t" "ds_read_b128 v[188:191], %[l0] offset:15360\n\t" \
    "ds_read_b128 v[28:31], %[l1] offset:15360\n\t" "v_smfmac_f32_16x16x64_bf16 v[236:239], v[240:243], a[104:111], %[ix]\n\t" "s_sub_u32 s62, s64, %[ca]\n\t" \
    "s_mul_i32 s62, s62, %[k1]\n\t" "v_smfmac_f32_16x16x64_bf16 v[232:235], v[244:247], a[112:119], %[ix]\n\t" "s_lshr_b32 s62, s62, 16\n\t" "s_add_u32 s62, s62, %[ga]\n\t" \
    "s_waitcnt lgkmcnt(12)\n\t" "v_smfmac_f32_16x16x64_bf16 v[236:239], v[244:247], v[160:167], %[ix]\n\t" "s_bitset0_b32 s62, 0\n\t" "v_mov_b32 v226, s62\n\t" \
    "s_waitcnt lgkmcnt(7)\n\t" "v_smfmac_f32_16x16x64_bf16 v[232:235], v[248:251], a[120:127], %[ix]\n\t" "buffer_store_dword v226, %[hq], %[rs], %[pb] offen sc1\n\t" \
    "ds_read_b128 v[160:163], %[l0] offset:16384\n\t" "v_smfmac_f32_16x16x64_bf16 v[236:239], v[248:251], a[128:135], %[ix]\n\t" \
    "ds_read_b128 v[164:167], %[l0] offset:17408\n\t" "s_waitcnt lgkmcnt(8)\n\t" "v_smfmac_f32_16x16x64_bf16 v[232:235], v[252:255], a[136:143], %[ix]\n\t" \
    "v_smfmac_f32_16x16x64_bf16 v[236:239], v[252:255], v[168:175], %[ix]\n\t" "ds_read_b128 v[168:171], %[l0] offset:18432\n\t" \
    "ds_read_b128 v[172:175], %[l0] offset:19456\n\t" "s_waitcnt lgkmcnt(9)\n\t" "v_smfmac_f32_16x16x64_bf16 v[232:235], v[16:19], a[144:151], %[ix]\n\t" \
    "v_smfmac_f32_16x16x64_bf16 v[236:239], v[16:19], a[152:159], %[ix]\n\t" "s_waitcnt lgkmcnt(8)\n\t" \
    "v_smfmac_f32_16x16x64_bf16 v[232:235], v[20:23], a[160:167], %[ix]\n\t" "v_smfmac_f32_16x16x64_bf16 v[236:239], v[20:23], v[176:183], %[ix]\n\t" \
    "ds_read_b128 v[176:179], %[l0] offset:20480\n\t" "ds_read_b128 v[180:183], %[l0] offset:21504\n\t" "s_waitcnt lgkmcnt(9)\n\t" \
    "v_smfmac_f32_16x16x64_bf16 v[232:235], v[24:27], a[168:175], %[ix]\n\t" "v_smfmac_f32_16x16x64_bf16 v[236:239], v[24:27], a[176:183], %[ix]\n\t" \
    "s_waitcnt lgkmcnt(6)\n\t" "v_smfmac_f32_16x16x64_bf16 v[232:235], v[28:31], a[184:191], %[ix]\n\t" \
    "v_smfmac_f32_16x16x64_bf16 v[236:239], v[28:31], v[184:191], %[ix]\n\t" "ds_read_b128 v[184:187], %[l0] offset:22528\n\t" \
    "ds_read_b128 v[188:191], %[l0] offset:23552\n\t" XT(90, 91) "s_nop 4\n\t" "v_add_f32 v232, v232, v233\n\t" "v_add_f32 v234, v234, v235\n\t" \
    "v_add_f32 v236, v236, v237\n\t" "v_add_f32 v238, v238, v239\n\t" "v_cvt_pk_bf16_f32 v232, v232, v236\n\t" "v_cvt_pk_bf16_f32 v233, v234, v238\n\t" \
    "ds_write2_b32 %[vw], v232, v233 offset1:68\n\t" "s_waitcnt lgkmcnt(0)\n\t" "s_barrier\n\t" XT(92, 93) "ds_read_b128 v[240:243], %[vr] offset:0\n\t" \
    "ds_read_b128 v[244:247], %[vr] offset:64\n\t" "ds_read_b128 v[248:251], %[vr] offset:128\n\t" "ds_read_b128 v[252:255], %[vr] offset:192\n\t" "s_mov_b32 s61, %[ub]\n\t" \
    "s_waitcnt lgkmcnt(3)\n\t" "v_smfmac_f32_16x16x64_bf16 v[192:195], v[240:243], v[32:39], %[ix]\n\t" \
    "v_smfmac_f32_16x16x64_bf16 v[196:199], v[240:243], v[48:55], %[ix]\n\t" "s_waitcnt lgkmcnt(2)\n\t" \
    "v_smfmac_f32_16x16x64_bf16 v[192:195], v[244:247], v[40:47], %[ix]\n\t" "v_smfmac_f32_16x16x64_bf16 v[196:199], v[244:247], v[56:63], %[ix]\n\t" \
    "s_waitcnt lgkmcnt(1)\n\t" "v_smfmac_f32_16x16x64_bf16 v[192:195], v[248:251], a[192:199], %[ix]\n\t" \
    "v_smfmac_f32_16x16x64_bf16 v[196:199], v[248:251], a[200:207], %[ix]\n\t" "s_waitcnt lgkmcnt(0)\n\t" \
    "v_smfmac_f32_16x16x64_bf16 v[192:195], v[252:255], v[160:167], %[ix]\n\t" "ds_read_b128 v[160:163], %[l0] offset:24576\n\t" \
    "ds_read_b128 v[164:167], %[l0] offset:25600\n\t" "v_smfmac_f32_16x16x64_bf16 v[196:199], v[252:255], v[168:175], %[ix]\n\t" \
    "ds_read_b128 v[168:171], %[l0] offset:26624\n\t" "ds_read_b128 v[172:175], %[l0] offset:27648\n\t" \
    "v_smfmac_f32_16x16x64_bf16 v[200:203], v[240:243], v[64:71], %[ix]\n\t" "s_nop 1\n\t" "v_smfmac_f32_16x16x64_bf16 v[204:207], v[240:243], v[80:87], %[ix]\n\t" \
    "v_cvt_pk_bf16_f32 v224, v192, v194\n\t" "v_cvt_pk_bf16_f32 v225, v193, v195\n\t" "v_smfmac_f32_16x16x64_bf16 v[200:203], v[244:247], v[72:79], %[ix]\n\t" \
    "v_cvt_pk_bf16_f32 v226, v196, v198\n\t" "v_cvt_pk_bf16_f32 v227, v197, v199\n\t" "v_smfmac_f32_16x16x64_bf16 v[204:207], v[244:247], v[88:95], %[ix]\n\t" "s_nop 0\n\t" \
    "v_permlane32_swap_b32 v224, v226\n\t" "v_smfmac_f32_16x16x64_bf16 v[200:203], v[248:251], a[208:215], %[ix]\n\t" "v_permlane32_swap_b32 v225, v227\n\t" "s_nop 0\n\t" \
    "v_smfmac_f32_16x16x64_bf16 v[204:207], v[248:251], a[216:223], %[ix]\n\t" "v_permlane16_swap_b32 v224, v225\n\t" "v_permlane16_swap_b32 v226, v227\n\t" \
    "v_smfmac_f32_16x16x64_bf16 v[200:203], v[252:255], v[176:183], %[ix]\n\t" "buffer_store_dwordx4 v[224:227], %[uo], %[rs], s61 offen sc1\n\t" \
    "s_add_u32 s61, s61, 0x10000\n\t" XT(94, 95) "v_smfmac_f32_16x16x64_bf16 v[204:207], v[252:255], v[184:191], %[ix]\n\t" "ds_read_b128 v[176:179], %[l0] offset:28672\n\t" \
    "ds_read_b128 v[180:183], %[l0] offset:29696\n\t" "v_smfmac_f32_16x16x64_bf16 v[208:211], v[240:243], v[96:103], %[ix]\n\t" \
    "ds_read_b128 v[184:187], %[l0] offset:30720\n\t" "ds_read_b128 v[188:191], %[l0] offset:31744\n\t" \
    "v_smfmac_f32_16x16x64_bf16 v[212:215], v[240:243], v[112:119], %[ix]\n\t" "v_cvt_pk_bf16_f32 v228, v200, v202\n\t" "v_cvt_pk_bf16_f32 v229, v201, v203\n\t" \
    "v_smfmac_f32_16x16x64_bf16 v[208:211], v[244:247], v[104:111], %[ix]\n\t" "v_cvt_pk_bf16_f32 v230, v204, v206\n\t" "v_cvt_pk_bf16_f32 v231, v205, v207\n\t" \
    "v_smfmac_f32_16x16x64_bf16 v[212:215], v[244:247], v[120:127], %[ix]\n\t" "s_nop 0\n\t" "v_permlane32_swap_b32 v228, v230\n\t" \
    "v_smfmac_f32_16x16x64_bf16 v[208:211], v[248:251], a[224:231], %[ix]\n\t" "v_permlane32_swap_b32 v229, v231\n\t" "s_nop 0\n\t" \
    "v_smfmac_f32_16x16x64_bf16 v[212:215], v[248:251], a[232:239], %[ix]\n\t" "v_permlane16_swap_b32 v228, v229\n\t" "v_permlane16_swap_b32 v230, v231\n\t" \
    "s_waitcnt lgkmcnt(6)\n\t" "v_smfmac_f32_16x16x64_bf16 v[208:211], v[252:255], v[160:167], %[ix]\n\t" "buffer_store_dwordx4 v[228:231], %[uo], %[rs], s61 offen sc1\n\t" \
    "s_add_u32 s61, s61, 0x10000\n\t" "s_waitcnt lgkmcnt(4)\n\t" "v_smfmac_f32_16x16x64_bf16 v[212:215], v[252:255], v[168:175], %[ix]\n\t" "s_nop 1\n\t" \
    "v_smfmac_f32_16x16x64_bf16 v[216:219], v[240:243], v[128:135], %[ix]\n\t" "s_nop 1\n\t" "v_smfmac_f32_16x16x64_bf16 v[220:223], v[240:243], v[144:151], %[ix]\n\t" \
    "v_cvt_pk_bf16_f32 v224, v208, v210\n\t" "v_cvt_pk_bf16_f32 v225, v209, v211\n\t" "v_smfmac_f32_16x16x64_bf16 v[216:219], v[244:247], v[136:143], %[ix]\n\t" \
    "v_cvt_pk_bf16_f32 v226, v212, v214\n\t" "v_cvt_pk_bf16_f32 v227, v213, v215\n\t" "v_smfmac_f32_16x16x64_bf16 v[220:223], v[244:247], v[152:159], %[ix]\n\t" "s_nop 0\n\t" \
    "v_permlane32_swap_b32 v224, v226\n\t" "v_smfmac_f32_16x16x64_bf16 v[216:219], v[248:251], a[240:247], %[ix]\n\t" "v_permlane32_swap_b32 v225, v227\n\t" "s_nop 0\n\t" \
    "v_smfmac_f32_16x16x64_bf16 v[220:223], v[248:251], a[248:255], %[ix]\n\t" "v_permlane16_swap_b32 v224, v225\n\t" "v_permlane16_swap_b32 v226, v227\n\t" \
    "s_waitcnt lgkmcnt(2)\n\t" "v_smfmac_f32_16x16x64_bf16 v[216:219], v[252:255], v[176:183], %[ix]\n\t" "buffer_store_dwordx4 v[224:227], %[uo], %[rs], s61 offen sc1\n\t" \
    "s_add_u32 s61, s61, 0x10000\n\t" "s_waitcnt lgkmcnt(0)\n\t" "v_smfmac_f32_16x16x64_bf16 v[220:223], v[252:255], v[184:191], %[ix]\n\t" "s_nop 5\n\t" \
    "v_cvt_pk_bf16_f32 v228, v216, v218\n\t" "v_cvt_pk_bf16_f32 v229, v217, v219\n\t" "v_cvt_pk_bf16_f32 v230, v220, v222\n\t" "v_cvt_pk_bf16_f32 v231, v221, v223\n\t" \
    "s_nop 0\n\t" "v_permlane32_swap_b32 v228, v230\n\t" "v_permlane32_swap_b32 v229, v231\n\t" "s_nop 1\n\t" "v_permlane16_swap_b32 v228, v229\n\t" \
    "v_permlane16_swap_b32 v230, v231\n\t" "buffer_store_dwordx4 v[228:231], %[uo], %[rs], s61 offen sc1\n\t" "s_nop 1\n\t" XT(96, 97) "s_branch 99f\n\t" "11:\n\t" \
    "s_cmp_eq_u32 %[bd], 0\n\t" "s_cbranch_scc1 16f\n\t" "s_sub_u32 %[bd], %[bd], 1\n\t" XCNT(100) "s_waitcnt lgkmcnt(0)\n\t" "s_nop 7\n\t" "s_branch 10b\n\t" "16:\n\t" \
    "s_cmp_lg_u32 s60, 0\n\t" "s_branch 12b\n\t" "14:\n\t" "s_cmp_eq_u32 %[bd], 0\n\t" "s_cbranch_scc1 15b\n\t" "s_sub_u32 %[bd], %[bd], 1\n\t" XCNT(101) \
    "s_mov_b32 s60, 1\n\t" "s_waitcnt lgkmcnt(0)\n\t" "s_nop 7\n\t" "s_branch 10b\n\t" "91:\n\t" XT(80, 81) "s_barrier\n\t" XT(82, 83) "20:\n\t" \
    "buffer_load_dwordx4 v[240:243], %[mo], %[rs], %[sb] offen offset:0 sc1\n\t" "v_mov_b64 v[232:233], 0\n\t" "v_mov_b64 v[234:235], 0\n\t" "v_mov_b64 v[236:237], 0\n\t" \
    "v_mov_b64 v[238:239], 0\n\t" "v_mov_b64 v[192:193], 0\n\t" "v_mov_b64 v[194:195], 0\n\t" "v_mov_b64 v[196:197], 0\n\t" "v_mov_b64 v[198:199], 0\n\t" \
    "buffer_load_dwordx4 v[244:247], v224, %[rs], %[sb] offen offset:0 sc1\n\t" "ds_read_b128 v[160:163], %[l0] offset:0\n\t" "ds_read_b128 v[164:167], %[l0] offset:1024\n\t" \
    "ds_read_b128 v[168:171], %[l0] offset:2048\n\t" "ds_read_b128 v[172:175], %[l0] offset:3072\n\t" "s_nop 3\n\t" \
    "buffer_load_dwordx4 v[248:251], %[mo], %[rs], %[sb] offen offset:2048 sc1\n\t" "ds_read_b128 v[176:179], %[l0] offset:4096\n\t" \
    "ds_read_b128 v[180:183], %[l0] offset:5120\n\t" "ds_read_b128 v[184:187], %[l0] offset:6144\n\t" "ds_read_b128 v[188:191], %[l0] offset:7168\n\t" "s_nop 3\n\t" \
    "buffer_load_dwordx4 v[252:255], v224, %[rs], %[sb] offen offset:2048 sc1\n\t" "v_mov_b64 v[200:201], 0\n\t" "v_mov_b64 v[202:203], 0\n\t" "v_mov_b64 v[204:205], 0\n\t" \
    "v_mov_b64 v[206:207], 0\n\t" "v_mov_b64 v[208:209], 0\n\t" "v_mov_b64 v[210:211], 0\n\t" "v_mov_b64 v[212:213], 0\n\t" "v_mov_b64 v[214:215], 0\n\t" \
    "v_mov_b64 v[216:217], 0\n\t" "v_mov_b64 v[218:219], 0\n\t" "v_mov_b64 v[220:221], 0\n\t" "v_mov_b64 v[222:223], 0\n\t" "s_waitcnt vmcnt(3)\n\t" XT(84, 85) \
    "v_smfmac_f32_16x16x64_bf16 v[232:235], v[240:243], a[0:7], %[ix]\n\t" "ds_write_b128 %[l1], v[240:243] offset:4096\n\t" \
    "v_smfmac_f32_16x16x64_bf16 v[236:239], v[240:243], a[8:15], %[ix]\n\t" "v_max3_u32 v225, v240, v241, v242\n\t" "s_waitcnt vmcnt(2)\n\t" \
    "v_smfmac_f32_16x16x64_bf16 v[232:235], v[244:247], a[16:23], %[ix]\n\t" "ds_write_b128 %[l1], v[244:247] offset:5120\n\t" "v_max3_u32 v225, v225, v243, v244\n\t" \
    "s_waitcnt lgkmcnt(8)\n\t" "v_smfmac_f32_16x16x64_bf16 v[236:239], v[244:247], v[160:167], %[ix]\n\t" "v_max3_u32 v225, v225, v245, v246\n\t" \
    "v_max_u32 v225, v225, v247\n\t" "v_cmp_eq_u32 vcc, -1, v225\n\t" "s_cmp_lg_u32 s60, 0\n\t" "s_cbranch_vccnz 21f\n\t" "22:\n\t" "s_waitcnt lgkmcnt(0)\n\t" \
    "s_cbranch_scc1 23f\n\t" "s_barrier\n\t" "23:\n\t" XT(86, 87) "ds_read_b128 v[16:19], %[l1] offset:8192\n\t" "ds_read_b128 v[20:23], %[l1] offset:12288\n\t" \
    "ds_read_b128 v[24:27], %[l1] offset:0\n\t" "s_waitcnt vmcnt(1)\n\t" "v_smfmac_f32_16x16x64_bf16 v[232:235], v[248:251], a[24:31], %[ix]\n\t" \
    "ds_write_b128 %[l1], v[248:251] offset:6144\n\t" "v_max3_u32 v225, v248, v249, v250\n\t" "v_smfmac_f32_16x16x64_bf16 v[236:239], v[248:251], a[32:39], %[ix]\n\t" \
    "ds_read_b128 v[28:31], %[l1] offset:9216\n\t" "ds_read_b128 v[240:243], %[l1] offset:13312\n\t" "ds_read_b128 v[244:247], %[l1] offset:1024\n\t" "s_waitcnt vmcnt(0)\n\t" \
    "v_smfmac_f32_16x16x64_bf16 v[232:235], v[252:255], a[40:47], %[ix]\n\t" "ds_write_b128 %[l1], v[252:255] offset:7168\n\t" "v_max3_u32 v225, v225, v251, v252\n\t" \
    "v_smfmac_f32_16x16x64_bf16 v[236:239], v[252:255], v[168:175], %[ix]\n\t" "v_max3_u32 v225, v225, v253, v254\n\t" "v_max_u32 v225, v225, v255\n\t" \
    "s_waitcnt lgkmcnt(7)\n\t" "v_smfmac_f32_16x16x64_bf16 v[232:235], v[16:19], a[48:55], %[ix]\n\t" "v_cmp_eq_u32 vcc, -1, v225\n\t" "s_cbranch_vccnz 24f\n\t" "25:\n\t" \
    "v_smfmac_f32_16x16x64_bf16 v[236:239], v[16:19], a[56:63], %[ix]\n\t" "ds_read_b128 v[160:163], %[l0] offset:8192\n\t" "ds_read_b128 v[164:167], %[l0] offset:9216\n\t" \
    "s_waitcnt lgkmcnt(8)\n\t" "v_smfmac_f32_16x16x64_bf16 v[232:235], v[20:23], a[64:71], %[ix]\n\t" "ds_read_b128 v[168:171], %[l0] offset:10240\n\t" \
    "ds_read_b128 v[172:175], %[l0] offset:11264\n\t" "v_smfmac_f32_16x16x64_bf16 v[236:239], v[20:23], v[176:183], %[ix]\n\t" "s_waitcnt lgkmcnt(4)\n\t" "s_barrier\n\t" \
    XT(88, 89) "v_smfmac_f32_16x16x64_bf16 v[232:235], v[24:27], a[72:79], %[ix]\n\t" "ds_read_b128 v[176:179], %[l0] offset:12288\n\t" \
    "ds_read_b128 v[180:183], %[l0] offset:13312\n\t" "v_smfmac_f32_16x16x64_bf16 v[236:239], v[24:27], a[80:87], %[ix]\n\t" "ds_read_b128 v[248:251], %[l1] offset:10240\n\t" \
    "ds_read_b128 v[252:255], %[l1] offset:14336\n\t" "v_smfmac_f32_16x16x64_bf16 v[232:235], v[28:31], a[88:95], %[ix]\n\t" "ds_read_b128 v[16:19], %[l1] offset:2048\n\t" \
    "ds_read_b128 v[20:23], %[l1] offset:11264\n\t" "v_smfmac_f32_16x16x64_bf16 v[236:239], v[28:31], v[184:191], %[ix]\n\t" "ds_read_b128 v[24:27], %[l1] offset:15360\n\t" \
    "ds_read_b128 v[184:187], %[l0] offset:14336\n\t" "v_smfmac_f32_16x16x64_bf16 v[232:235], v[240:243], a[96:103], %[ix]\n\t" \
    "ds_read_b128 v[188:191], %[l0] offset:15360\n\t" "ds_read_b128 v[28:31], %[l1] offset:3072\n\t" \
    "v_smfmac_f32_16x16x64_bf16 v[236:239], v[240:243], a[104:111], %[ix]\n\t" "v_smfmac_f32_16x16x64_bf16 v[232:235], v[244:247], a[112:119], %[ix]\n\t" \
    "s_waitcnt lgkmcnt(12)\n\t" "v_smfmac_f32_16x16x64_bf16 v[236:239], v[244:247], v[160:167], %[ix]\n\t" "ds_read_b128 v[160:163], %[l0] offset:16384\n\t" \
    "ds_read_b128 v[164:167], %[l0] offset:17408\n\t" "s_waitcnt lgkmcnt(9)\n\t" "v_smfmac_f32_16x16x64_bf16 v[232:235], v[248:251], a[120:127], %[ix]\n\t" \
    "v_smfmac_f32_16x16x64_bf16 v[236:239], v[248:251], a[128:135], %[ix]\n\t" "s_waitcnt lgkmcnt(8)\n\t" \
    "v_smfmac_f32_16x16x64_bf16 v[232:235], v[252:255], a[136:143], %[ix]\n\t" "v_smfmac_f32_16x16x64_bf16 v[236:239], v[252:255], v[168:175], %[ix]\n\t" \
    "ds_read_b128 v[168:171], %[l0] offset:18432\n\t" "ds_read_b128 v[172:175], %[l0] offset:19456\n\t" "s_waitcnt lgkmcnt(9)\n\t" \
    "v_smfmac_f32_16x16x64_bf16 v[232:235], v[16:19], a[144:151], %[ix]\n\t" "v_smfmac_f32_16x16x64_bf16 v[236:239], v[16:19], a[152:159], %[ix]\n\t" \
    "s_waitcnt lgkmcnt(8)\n\t" "v_smfmac_f32_16x16x64_bf16 v[232:235], v[20:23], a[160:167], %[ix]\n\t" \
    "v_smfmac_f32_16x16x64_bf16 v[236:239], v[20:23], v[176:183], %[ix]\n\t" "ds_read_b128 v[176:179], %[l0] offset:20480\n\t" \
    "ds_read_b128 v[180:183], %[l0] offset:21504\n\t" "s_waitcnt lgkmcnt(9)\n\t" "v_smfmac_f32_16x16x64_bf16 v[232:235], v[24:27], a[168:175], %[ix]\n\t" \
    "v_smfmac_f32_16x16x64_bf16 v[236:239], v[24:27], a[176:183], %[ix]\n\t" "s_waitcnt lgkmcnt(6)\n\t" \
    "v_smfmac_f32_16x16x64_bf16 v[232:235], v[28:31], a[184:191], %[ix]\n\t" "v_smfmac_f32_16x16x64_bf16 v[236:239], v[28:31], v[184:191], %[ix]\n\t" \
    "ds_read_b128 v[184:187], %[l0] offset:22528\n\t" "ds_read_b128 v[188:191], %[l0] offset:23552\n\t" XT(90, 91) "s_nop 4\n\t" "v_add_f32 v232, v232, v233\n\t" \
    "v_add_f32 v234, v234, v235\n\t" "v_add_f32 v236, v236, v237\n\t" "v_add_f32 v238, v238, v239\n\t" "v_cvt_pk_bf16_f32 v232, v232, v236\n\t" \
    "v_cvt_pk_bf16_f32 v233, v234, v238\n\t" "ds_write2_b32 %[vw], v232, v233 offset1:68\n\t" "s_waitcnt lgkmcnt(0)\n\t" "s_barrier\n\t" XT(92, 93) \
    "ds_read_b128 v[240:243], %[vr] offset:0\n\t" "ds_read_b128 v[244:247], %[vr] offset:64\n\t" "ds_read_b128 v[248:251], %[vr] offset:128\n\t" \
    "ds_read_b128 v[252:255], %[vr] offset:192\n\t" "s_mov_b32 s61, %[ub]\n\t" "s_waitcnt lgkmcnt(3)\n\t" \
    "v_smfmac_f32_16x16x64_bf16 v[192:195], v[240:243], v[32:39], %[ix]\n\t" "v_smfmac_f32_16x16x64_bf16 v[196:199], v[240:243], v[48:55], %[ix]\n\t" \
    "s_waitcnt lgkmcnt(2)\n\t" "v_smfmac_f32_16x16x64_bf16 v[192:195], v[244:247], v[40:47], %[ix]\n\t" \
    "v_smfmac_f32_16x16x64_bf16 v[196:199], v[244:247], v[56:63], %[ix]\n\t" "s_waitcnt lgkmcnt(1)\n\t" \
    "v_smfmac_f32_16x16x64_bf16 v[192:195], v[248:251], a[192:199], %[ix]\n\t" "v_smfmac_f32_16x16x64_bf16 v[196:199], v[248:251], a[200:207], %[ix]\n\t" \
    "s_waitcnt lgkmcnt(0)\n\t" "v_smfmac_f32_16x16x64_bf16 v[192:195], v[252:255], v[160:167], %[ix]\n\t" "ds_read_b128 v[160:163], %[l0] offset:24576\n\t" \
    "ds_read_b128 v[164:167], %[l0] offset:25600\n\t" "v_smfmac_f32_16x16x64_bf16 v[196:199], v[252:255], v[168:175], %[ix]\n\t" \
    "ds_read_b128 v[168:171], %[l0] offset:26624\n\t" "ds_read_b128 v[172:175], %[l0] offset:27648\n\t" \
    "v_smfmac_f32_16x16x64_bf16 v[200:203], v[240:243], v[64:71], %[ix]\n\t" "s_nop 1\n\t" "v_smfmac_f32_16x16x64_bf16 v[204:207], v[240:243], v[80:87], %[ix]\n\t" \
    "v_cvt_pk_bf16_f32 v224, v192, v194\n\t" "v_cvt_pk_bf16_f32 v225, v193, v195\n\t" "v_smfmac_f32_16x16x64_bf16 v[200:203], v[244:247], v[72:79], %[ix]\n\t" \
    "v_cvt_pk_bf16_f32 v226, v196, v198\n\t" "v_cvt_pk_bf16_f32 v227, v197, v199\n\t" "v_smfmac_f32_16x16x64_bf16 v[204:207], v[244:247], v[88:95], %[ix]\n\t" "s_nop 0\n\t" \
    "v_permlane32_swap_b32 v224, v226\n\t" "v_smfmac_f32_16x16x64_bf16 v[200:203], v[248:251], a[208:215], %[ix]\n\t" "v_permlane32_swap_b32 v225, v227\n\t" "s_nop 0\n\t" \
    "v_smfmac_f32_16x16x64_bf16 v[204:207], v[248:251], a[216:223], %[ix]\n\t" "v_permlane16_swap_b32 v224, v225\n\t" "v_permlane16_swap_b32 v226, v227\n\t" \
    "v_smfmac_f32_16x16x64_bf16 v[200:203], v[252:255], v[176:183], %[ix]\n\t" "buffer_store_dwordx4 v[224:227], %[uo], %[rs], s61 offen sc1\n\t" \
    "s_add_u32 s61, s61, 0x10000\n\t" XT(94, 95) "v_smfmac_f32_16x16x64_bf16 v[204:207], v[252:255], v[184:191], %[ix]\n\t" "ds_read_b128 v[176:179], %[l0] offset:28672\n\t" \
    "ds_read_b128 v[180:183], %[l0] offset:29696\n\t" "v_smfmac_f32_16x16x64_bf16 v[208:211], v[240:243], v[96:103], %[ix]\n\t" \
    "ds_read_b128 v[184:187], %[l0] offset:30720\n\t" "ds_read_b128 v[188:191], %[l0] offset:31744\n\t" \
    "v_smfmac_f32_16x16x64_bf16 v[212:215], v[240:243], v[112:119], %[ix]\n\t" "v_cvt_pk_bf16_f32 v228, v200, v202\n\t" "v_cvt_pk_bf16_f32 v229, v201, v203\n\t" \
    "v_smfmac_f32_16x16x64_bf16 v[208:211], v[244:247], v[104:111], %[ix]\n\t" "v_cvt_pk_bf16_f32 v230, v204, v206\n\t" "v_cvt_pk_bf16_f32 v231, v205, v207\n\t" \
    "v_smfmac_f32_16x16x64_bf16 v[212:215], v[244:247], v[120:127], %[ix]\n\t" "s_nop 0\n\t" "v_permlane32_swap_b32 v228, v230\n\t" \
    "v_smfmac_f32_16x16x64_bf16 v[208:211], v[248:251], a[224:231], %[ix]\n\t" "v_permlane32_swap_b32 v229, v231\n\t" "s_nop 0\n\t" \
    "v_smfmac_f32_16x16x64_bf16 v[212:215], v[248:251], a[232:239], %[ix]\n\t" "v_permlane16_swap_b32 v228, v229\n\t" "v_permlane16_swap_b32 v230, v231\n\t" \
    "s_waitcnt lgkmcnt(6)\n\t" "v_smfmac_f32_16x16x64_bf16 v[208:211], v[252:255], v[160:167], %[ix]\n\t" "buffer_store_dwordx4 v[228:231], %[uo], %[rs], s61 offen sc1\n\t" \
    "s_add_u32 s61, s61, 0x10000\n\t" "s_waitcnt lgkmcnt(4)\n\t" "v_smfmac_f32_16x16x64_bf16 v[212:215], v[252:255], v[168:175], %[ix]\n\t" "s_nop 1\n\t" \
    "v_smfmac_f32_16x16x64_bf16 v[216:219], v[240:243], v[128:135], %[ix]\n\t" "s_nop 1\n\t" "v_smfmac_f32_16x16x64_bf16 v[220:223], v[240:243], v[144:151], %[ix]\n\t" \
    "v_cvt_pk_bf16_f32 v224, v208, v210\n\t" "v_cvt_pk_bf16_f32 v225, v209, v211\n\t" "v_smfmac_f32_16x16x64_bf16 v[216:219], v[244:247], v[136:143], %[ix]\n\t" \
    "v_cvt_pk_bf16_f32 v226, v212, v214\n\t" "v_cvt_pk_bf16_f32 v227, v213, v215\n\t" "v_smfmac_f32_16x16x64_bf16 v[220:223], v[244:247], v[152:159], %[ix]\n\t" "s_nop 0\n\t" \
    "v_permlane32_swap_b32 v224, v226\n\t" "v_smfmac_f32_16x16x64_bf16 v[216:219], v[248:251], a[240:247], %[ix]\n\t" "v_permlane32_swap_b32 v225, v227\n\t" "s_nop 0\n\t" \
    "v_smfmac_f32_16x16x64_bf16 v[220:223], v[248:251], a[248:255], %[ix]\n\t" "v_permlane16_swap_b32 v224, v225\n\t" "v_permlane16_swap_b32 v226, v227\n\t" \
    "s_waitcnt lgkmcnt(2)\n\t" "v_smfmac_f32_16x16x64_bf16 v[216:219], v[252:255], v[176:183], %[ix]\n\t" "buffer_store_dwordx4 v[224:227], %[uo], %[rs], s61 offen sc1\n\t" \
    "s_add_u32 s61, s61, 0x10000\n\t" "s_waitcnt lgkmcnt(0)\n\t" "v_smfmac_f32_16x16x64_bf16 v[220:223], v[252:255], v[184:191], %[ix]\n\t" "s_nop 5\n\t" \
    "v_cvt_pk_bf16_f32 v228, v216, v218\n\t" "v_cvt_pk_bf16_f32 v229, v217, v219\n\t" "v_cvt_pk_bf16_f32 v230, v220, v222\n\t" "v_cvt_pk_bf16_f32 v231, v221, v223\n\t" \
    "s_nop 0\n\t" "v_permlane32_swap_b32 v228, v230\n\t" "v_permlane32_swap_b32 v229, v231\n\t" "s_nop 1\n\t" "v_permlane16_swap_b32 v228, v229\n\t" \
    "v_permlane16_swap_b32 v230, v231\n\t" "buffer_store_dwordx4 v[228:231], %[uo], %[rs], s61 offen sc1\n\t" "s_nop 1\n\t" XT(96, 97) "s_branch 99f\n\t" "21:\n\t" \
    "s_cmp_eq_u32 %[bd], 0\n\t" "s_cbranch_scc1 26f\n\t" "s_sub_u32 %[bd], %[bd], 1\n\t" XCNT(100) "s_waitcnt lgkmcnt(0)\n\t" "s_nop 7\n\t" "s_branch 20b\n\t" "26:\n\t" \
    "s_cmp_lg_u32 s60, 0\n\t" "s_branch 22b\n\t" "24:\n\t" "s_cmp_eq_u32 %[bd], 0\n\t" "s_cbranch_scc1 25b\n\t" "s_sub_u32 %[bd], %[bd], 1\n\t" XCNT(101) \
    "s_mov_b32 s60, 1\n\t" "s_waitcnt lgkmcnt(0)\n\t" "s_nop 7\n\t" "s_branch 20b\n\t" "92:\n\t" XT(80, 81) "s_barrier\n\t" XT(82, 83) "30:\n\t" \
    "buffer_load_dwordx4 v[240:243], %[mo], %[rs], %[sb] offen offset:0 sc1\n\t" "v_mov_b64 v[232:233], 0\n\t" "v_mov_b64 v[234:235], 0\n\t" "v_mov_b64 v[236:237], 0\n\t" \
    "v_mov_b64 v[238:239], 0\n\t" "v_mov_b64 v[192:193], 0\n\t" "v_mov_b64 v[194:195], 0\n\t" "v_mov_b64 v[196:197], 0\n\t" "v_mov_b64 v[198:199], 0\n\t" \
    "buffer_load_dwordx4 v[244:247], v224, %[rs], %[sb] offen offset:0 sc1\n\t" "ds_read_b128 v[160:163], %[l0] offset:0\n\t" "ds_read_b128 v[164:167], %[l0] offset:1024\n\t" \
    "ds_read_b128 v[168:171], %[l0] offset:2048\n\t" "ds_read_b128 v[172:175], %[l0] offset:3072\n\t" "s_nop 3\n\t" \
    "buffer_load_dwordx4 v[248:251], %[mo], %[rs], %[sb] offen offset:2048 sc1\n\t" "ds_read_b128 v[176:179], %[l0] offset:4096\n\t" \
    "ds_read_b128 v[180:183], %[l0] offset:5120\n\t" "ds_read_b128 v[184:187], %[l0] offset:6144\n\t" "ds_read_b128 v[188:191], %[l0] offset:7168\n\t" "s_nop 3\n\t" \
    "buffer_load_dwordx4 v[252:255], v224, %[rs], %[sb] offen offset:2048 sc1\n\t" "v_mov_b64 v[200:201], 0\n\t" "v_mov_b64 v[202:203], 0\n\t" "v_mov_b64 v[204:205], 0\n\t" \
    "v_mov_b64 v[206:207], 0\n\t" "v_mov_b64 v[208:209], 0\n\t" "v_mov_b64 v[210:211], 0\n\t" "v_mov_b64 v[212:213], 0\n\t" "v_mov_b64 v[214:215], 0\n\t" \
    "v_mov_b64 v[216:217], 0\n\t" "v_mov_b64 v[218:219], 0\n\t" "v_mov_b64 v[220:221], 0\n\t" "v_mov_b64 v[222:223], 0\n\t" "s_waitcnt vmcnt(3)\n\t" XT(84, 85) \
    "v_smfmac_f32_16x16x64_bf16 v[232:235], v[240:243], a[0:7], %[ix]\n\t" "ds_write_b128 %[l1], v[240:243] offset:8192\n\t" \
    "v_smfmac_f32_16x16x64_bf16 v[236:239], v[240:243], a[8:15], %[ix]\n\t" "v_max3_u32 v225, v240, v241, v242\n\t" "s_waitcnt vmcnt(2)\n\t" \
    "v_smfmac_f32_16x16x64_bf16 v[232:235], v[244:247], a[16:23], %[ix]\n\t" "ds_write_b128 %[l1], v[244:247] offset:9216\n\t" "v_max3_u32 v225, v225, v243, v244\n\t" \
    "s_waitcnt lgkmcnt(8)\n\t" "v_smfmac_f32_16x16x64_bf16 v[236:239], v[244:247], v[160:167], %[ix]\n\t" "v_max3_u32 v225, v225, v245, v246\n\t" \
    "v_max_u32 v225, v225, v247\n\t" "v_cmp_eq_u32 vcc, -1, v225\n\t" "s_cmp_lg_u32 s60, 0\n\t" "s_cbranch_vccnz 31f\n\t" "32:\n\t" "s_waitcnt lgkmcnt(0)\n\t" \
    "s_cbranch_scc1 33f\n\t" "s_barrier\n\t" "33:\n\t" XT(86, 87) "ds_read_b128 v[16:19], %[l1] offset:12288\n\t" "ds_read_b128 v[20:23], %[l1] offset:0\n\t" \
    "ds_read_b128 v[24:27], %[l1] offset:4096\n\t" "s_waitcnt vmcnt(1)\n\t" "v_smfmac_f32_16x16x64_bf16 v[232:235], v[248:251], a[24:31], %[ix]\n\t" \
    "ds_write_b128 %[l1], v[248:251] offset:10240\n\t" "v_max3_u32 v225, v248, v249, v250\n\t" "v_smfmac_f32_16x16x64_bf16 v[236:239], v[248:251], a[32:39], %[ix]\n\t" \
    "ds_read_b128 v[28:31], %[l1] offset:13312\n\t" "ds_read_b128 v[240:243], %[l1] offset:1024\n\t" "ds_read_b128 v[244:247], %[l1] offset:5120\n\t" "s_waitcnt vmcnt(0)\n\t" \
    "v_smfmac_f32_16x16x64_bf16 v[232:235], v[252:255], a[40:47], %[ix]\n\t" "ds_write_b128 %[l1], v[252:255] offset:11264\n\t" "v_max3_u32 v225, v225, v251, v252\n\t" \
    "v_smfmac_f32_16x16x64_bf16 v[236:239], v[252:255], v[168:175], %[ix]\n\t" "v_max3_u32 v225, v225, v253, v254\n\t" "v_max_u32 v225, v225, v255\n\t" \
    "s_waitcnt lgkmcnt(7)\n\t" "v_smfmac_f32_16x16x64_bf16 v[232:235], v[16:19], a[48:55], %[ix]\n\t" "v_cmp_eq_u32 vcc, -1, v225\n\t" "s_cbranch_vccnz 34f\n\t" "35:\n\t" \
    "v_smfmac_f32_16x16x64_bf16 v[236:239], v[16:19], a[56:63], %[ix]\n\t" "ds_read_b128 v[160:163], %[l0] offset:8192\n\t" "ds_read_b128 v[164:167], %[l0] offset:9216\n\t" \
    "s_waitcnt lgkmcnt(8)\n\t" "v_smfmac_f32_16x16x64_bf16 v[232:235], v[20:23], a[64:71], %[ix]\n\t" "ds_read_b128 v[168:171], %[l0] offset:10240\n\t" \
    "ds_read_b128 v[172:175], %[l0] offset:11264\n\t" "v_smfmac_f32_16x16x64_bf16 v[236:239], v[20:23], v[176:183], %[ix]\n\t" "s_waitcnt lgkmcnt(4)\n\t" "s_barrier\n\t" \
    XT(88, 89) "v_smfmac_f32_16x16x64_bf16 v[232:235], v[24:27], a[72:79], %[ix]\n\t" "ds_read_b128 v[176:179], %[l0] offset:12288\n\t" \
    "ds_read_b128 v[180:183], %[l0] offset:13312\n\t" "v_smfmac_f32_16x16x64_bf16 v[236:239], v[24:27], a[80:87], %[ix]\n\t" "ds_read_b128 v[248:251], %[l1] offset:14336\n\t" \
    "ds_read_b128 v[252:255], %[l1] offset:2048\n\t" "v_smfmac_f32_16x16x64_bf16 v[232:235], v[28:31], a[88:95], %[ix]\n\t" "ds_read_b128 v[16:19], %[l1] offset:6144\n\t" \
    "ds_read_b128 v[20:23], %[l1] offset:15360\n\t" "v_smfmac_f32_16x16x64_bf16 v[236:239], v[28:31], v[184:191], %[ix]\n\t" "ds_read_b128 v[24:27], %[l1] offset:3072\n\t" \
    "ds_read_b128 v[184:187], %[l0] offset:14336\n\t" "v_smfmac_f32_16x16x64_bf16 v[232:235], v[240:243], a[96:103], %[ix]\n\t" \
    "ds_read_b128 v[188:191], %[l0] offset:15360\n\t" "ds_read_b128 v[28:31], %[l1] offset:7168\n\t" \
    "v_smfmac_f32_16x16x64_bf16 v[236:239], v[240:243], a[104:111], %[ix]\n\t" "v_smfmac_f32_16x16x64_bf16 v[232:235], v[244:247], a[112:119], %[ix]\n\t" \
    "s_waitcnt lgkmcnt(12)\n\t" "v_smfmac_f32_16x16x64_bf16 v[236:239], v[244:247], v[160:167], %[ix]\n\t" "ds_read_b128 v[160:163], %[l0] offset:16384\n\t" \
    "ds_read_b128 v[164:167], %[l0] offset:17408\n\t" "s_waitcnt lgkmcnt(9)\n\t" "v_smfmac_f32_16x16x64_bf16 v[232:235], v[248:251], a[120:127], %[ix]\n\t" \
    "v_smfmac_f32_16x16x64_bf16 v[236:239], v[248:251], a[128:135], %[ix]\n\t" "s_waitcnt lgkmcnt(8)\n\t" \
    "v_smfmac_f32_16x16x64_bf16 v[232:235], v[252:255], a[136:143], %[ix]\n\t" "v_smfmac_f32_16x16x64_bf16 v[236:239], v[252:255], v[168:175], %[ix]\n\t" \
    "ds_read_b128 v[168:171], %[l0] offset:18432\n\t" "ds_read_b128 v[172:175], %[l0] offset:19456\n\t" "s_waitcnt lgkmcnt(9)\n\t" \
    "v_smfmac_f32_16x16x64_bf16 v[232:235], v[16:19], a[144:151], %[ix]\n\t" "v_smfmac_f32_16x16x64_bf16 v[236:239], v[16:19], a[152:159], %[ix]\n\t" \
    "s_waitcnt lgkmcnt(8)\n\t" "v_smfmac_f32_16x16x64_bf16 v[232:235], v[20:23], a[160:167], %[ix]\n\t" \
    "v_smfmac_f32_16x16x64_bf16 v[236:239], v[20:23], v[176:183], %[ix]\n\t" "ds_read_b128 v[176:179], %[l0] offset:20480\n\t" \
    "ds_read_b128 v[180:183], %[l0] offset:21504\n\t" "s_waitcnt lgkmcnt(9)\n\t" "v_smfmac_f32_16x16x64_bf16 v[232:235], v[24:27], a[168:175], %[ix]\n\t" \
    "v_smfmac_f32_16x16x64_bf16 v[236:239], v[24:27], a[176:183], %[ix]\n\t" "s_waitcnt lgkmcnt(6)\n\t" \
    "v_smfmac_f32_16x16x64_bf16 v[232:235], v[28:31], a[184:191], %[ix]\n\t" "v_smfmac_f32_16x16x64_bf16 v[236:239], v[28:31], v[184:191], %[ix]\n\t" \
    "ds_read_b128 v[184:187], %[l0] offset:22528\n\t" "ds_read_b128 v[188:191], %[l0] offset:23552\n\t" XT(90, 91) "s_nop 4\n\t" "v_add_f32 v232, v232, v233\n\t" \
    "v_add_f32 v234, v234, v235\n\t" "v_add_f32 v236, v236, v237\n\t" "v_add_f32 v238, v238, v239\n\t" "v_cvt_pk_bf16_f32 v232, v232, v236\n\t" \
    "v_cvt_pk_bf16_f32 v233, v234, v238\n\t" "ds_write2_b32 %[vw], v232, v233 offset1:68\n\t" "s_waitcnt lgkmcnt(0)\n\t" "s_barrier\n\t" XT(92, 93) \
    "ds_read_b128 v[240:243], %[vr] offset:0\n\t" "ds_read_b128 v[244:247], %[vr] offset:64\n\t" "ds_read_b128 v[248:251], %[vr] offset:128\n\t" \
    "ds_read_b128 v[252:255], %[vr] offset:192\n\t" "s_mov_b32 s61, %[ub]\n\t" "s_waitcnt lgkmcnt(3)\n\t" \
    "v_smfmac_f32_16x16x64_bf16 v[192:195], v[240:243], v[32:39], %[ix]\n\t" "v_smfmac_f32_16x16x64_bf16 v[196:199], v[240:243], v[48:55], %[ix]\n\t" \
    "s_waitcnt lgkmcnt(2)\n\t" "v_smfmac_f32_16x16x64_bf16 v[192:195], v[244:247], v[40:47], %[ix]\n\t" \
    "v_smfmac_f32_16x16x64_bf16 v[196:199], v[244:247], v[56:63], %[ix]\n\t" "s_waitcnt lgkmcnt(1)\n\t" \
    "v_smfmac_f32_16x16x64_bf16 v[192:195], v[248:251], a[192:199], %[ix]\n\t" "v_smfmac_f32_16x16x64_bf16 v[196:199], v[248:251], a[200:207], %[ix]\n\t" \
    "s_waitcnt lgkmcnt(0)\n\t" "v_smfmac_f32_16x16x64_bf16 v[192:195], v[252:255], v[160:167], %[ix]\n\t" "ds_read_b128 v[160:163], %[l0] offset:24576\n\t" \
    "ds_read_b128 v[164:167], %[l0] offset:25600\n\t" "v_smfmac_f32_16x16x64_bf16 v[196:199], v[252:255], v[168:175], %[ix]\n\t" \
    "ds_read_b128 v[168:171], %[l0] offset:26624\n\t" "ds_read_b128 v[172:175], %[l0] offset:27648\n\t" \
    "v_smfmac_f32_16x16x64_bf16 v[200:203], v[240:243], v[64:71], %[ix]\n\t" "s_nop 1\n\t" "v_smfmac_f32_16x16x64_bf16 v[204:207], v[240:243], v[80:87], %[ix]\n\t" \
    "v_cvt_pk_bf16_f32 v224, v192, v194\n\t" "v_cvt_pk_bf16_f32 v225, v193, v195\n\t" "v_smfmac_f32_16x16x64_bf16 v[200:203], v[244:247], v[72:79], %[ix]\n\t" \
    "v_cvt_pk_bf16_f32 v226, v196, v198\n\t" "v_cvt_pk_bf16_f32 v227, v197, v199\n\t" "v_smfmac_f32_16x16x64_bf16 v[204:207], v[244:247], v[88:95], %[ix]\n\t" "s_nop 0\n\t" \
    "v_permlane32_swap_b32 v224, v226\n\t" "v_smfmac_f32_16x16x64_bf16 v[200:203], v[248:251], a[208:215], %[ix]\n\t" "v_permlane32_swap_b32 v225, v227\n\t" "s_nop 0\n\t" \
    "v_smfmac_f32_16x16x64_bf16 v[204:207], v[248:251], a[216:223], %[ix]\n\t" "v_permlane16_swap_b32 v224, v225\n\t" "v_permlane16_swap_b32 v226, v227\n\t" \
    "v_smfmac_f32_16x16x64_bf16 v[200:203], v[252:255], v[176:183], %[ix]\n\t" "buffer_store_dwordx4 v[224:227], %[uo], %[rs], s61 offen sc1\n\t" \
    "s_add_u32 s61, s61, 0x10000\n\t" XT(94, 95) "v_smfmac_f32_16x16x64_bf16 v[204:207], v[252:255], v[184:191], %[ix]\n\t" "ds_read_b128 v[176:179], %[l0] offset:28672\n\t" \
    "ds_read_b128 v[180:183], %[l0] offset:29696\n\t" "v_smfmac_f32_16x16x64_bf16 v[208:211], v[240:243], v[96:103], %[ix]\n\t" \
    "ds_read_b128 v[184:187], %[l0] offset:30720\n\t" "ds_read_b128 v[188:191], %[l0] offset:31744\n\t" \
    "v_smfmac_f32_16x16x64_bf16 v[212:215], v[240:243], v[112:119], %[ix]\n\t" "v_cvt_pk_bf16_f32 v228, v200, v202\n\t" "v_cvt_pk_bf16_f32 v229, v201, v203\n\t" \
    "v_smfmac_f32_16x16x64_bf16 v[208:211], v[244:247], v[104:111], %[ix]\n\t" "v_cvt_pk_bf16_f32 v230, v204, v206\n\t" "v_cvt_pk_bf16_f32 v231, v205, v207\n\t" \
    "v_smfmac_f32_16x16x64_bf16 v[212:215], v[244:247], v[120:127], %[ix]\n\t" "s_nop 0\n\t" "v_permlane32_swap_b32 v228, v230\n\t" \
    "v_smfmac_f32_16x16x64_bf16 v[208:211], v[248:251], a[224:231], %[ix]\n\t" "v_permlane32_swap_b32 v229, v231\n\t" "s_nop 0\n\t" \
    "v_smfmac_f32_16x16x64_bf16 v[212:215], v[248:251], a[232:239], %[ix]\n\t" "v_permlane16_swap_b32 v228, v229\n\t" "v_permlane16_swap_b32 v230, v231\n\t" \
    "s_waitcnt lgkmcnt(6)\n\t" "v_smfmac_f32_16x16x64_bf16 v[208:211], v[252:255], v[160:167], %[ix]\n\t" "buffer_store_dwordx4 v[228:231], %[uo], %[rs], s61 offen sc1\n\t" \
    "s_add_u32 s61, s61, 0x10000\n\t" "s_waitcnt lgkmcnt(4)\n\t" "v_smfmac_f32_16x16x64_bf16 v[212:215], v[252:255], v[168:175], %[ix]\n\t" "s_nop 1\n\t" \
    "v_smfmac_f32_16x16x64_bf16 v[216:219], v[240:243], v[128:135], %[ix]\n\t" "s_nop 1\n\t" "v_smfmac_f32_16x16x64_bf16 v[220:223], v[240:243], v[144:151], %[ix]\n\t" \
    "v_cvt_pk_bf16_f32 v224, v208, v210\n\t" "v_cvt_pk_bf16_f32 v225, v209, v211\n\t" "v_smfmac_f32_16x16x64_bf16 v[216:219], v[244:247], v[136:143], %[ix]\n\t" \
    "v_cvt_pk_bf16_f32 v226, v212, v214\n\t" "v_cvt_pk_bf16_f32 v227, v213, v215\n\t" "v_smfmac_f32_16x16x64_bf16 v[220:223], v[244:247], v[152:159], %[ix]\n\t" "s_nop 0\n\t" \
    "v_permlane32_swap_b32 v224, v226\n\t" "v_smfmac_f32_16x16x64_bf16 v[216:219], v[248:251], a[240:247], %[ix]\n\t" "v_permlane32_swap_b32 v225, v227\n\t" "s_nop 0\n\t" \
    "v_smfmac_f32_16x16x64_bf16 v[220:223], v[248:251], a[248:255], %[ix]\n\t" "v_permlane16_swap_b32 v224, v225\n\t" "v_permlane16_swap_b32 v226, v227\n\t" \
    "s_waitcnt lgkmcnt(2)\n\t" "v_smfmac_f32_16x16x64_bf16 v[216:219], v[252:255], v[176:183], %[ix]\n\t" "buffer_store_dwordx4 v[224:227], %[uo], %[rs], s61 offen sc1\n\t" \
    "s_add_u32 s61, s61, 0x10000\n\t" "s_waitcnt lgkmcnt(0)\n\t" "v_smfmac_f32_16x16x64_bf16 v[220:223], v[252:255], v[184:191], %[ix]\n\t" "s_nop 5\n\t" \
    "v_cvt_pk_bf16_f32 v228, v216, v218\n\t" "v_cvt_pk_bf16_f32 v229, v217, v219\n\t" "v_cvt_pk_bf16_f32 v230, v220, v222\n\t" "v_cvt_pk_bf16_f32 v231, v221, v223\n\t" \
    "s_nop 0\n\t" "v_permlane32_swap_b32 v228, v230\n\t" "v_permlane32_swap_b32 v229, v231\n\t" "s_nop 1\n\t" "v_permlane16_swap_b32 v228, v229\n\t" \
    "v_permlane16_swap_b32 v230, v231\n\t" "buffer_store_dwordx4 v[228:231], %[uo], %[rs], s61 offen sc1\n\t" "s_nop 1\n\t" XT(96, 97) "s_branch 99f\n\t" "31:\n\t" \
    "s_cmp_eq_u32 %[bd], 0\n\t" "s_cbranch_scc1 36f\n\t" "s_sub_u32 %[bd], %[bd], 1\n\t" XCNT(100) "s_waitcnt lgkmcnt(0)\n\t" "s_nop 7\n\t" "s_branch 30b\n\t" "36:\n\t" \
    "s_cmp_lg_u32 s60, 0\n\t" "s_branch 32b\n\t" "34:\n\t" "s_cmp_eq_u32 %[bd], 0\n\t" "s_cbranch_scc1 35b\n\t" "s_sub_u32 %[bd], %[bd], 1\n\t" XCNT(101) \
    "s_mov_b32 s60, 1\n\t" "s_waitcnt lgkmcnt(0)\n\t" "s_nop 7\n\t" "s_branch 30b\n\t" "93:\n\t" XT(80, 81) "s_barrier\n\t" XT(82, 83) "40:\n\t" \
    "buffer_load_dwordx4 v[240:243], %[mo], %[rs], %[sb] offen offset:0 sc1\n\t" "v_mov_b64 v[232:233], 0\n\t" "v_mov_b64 v[234:235], 0\n\t" "v_mov_b64 v[236:237], 0\n\t" \
    "v_mov_b64 v[238:239], 0\n\t" "v_mov_b64 v[192:193], 0\n\t" "v_mov_b64 v[194:195], 0\n\t" "v_mov_b64 v[196:197], 0\n\t" "v_mov_b64 v[198:199], 0\n\t" \
    "buffer_load_dwordx4 v[244:247], v224, %[rs], %[sb] offen offset:0 sc1\n\t" "ds_read_b128 v[160:163], %[l0] offset:0\n\t" "ds_read_b128 v[164:167], %[l0] offset:1024\n\t" \
    "ds_read_b128 v[168:171], %[l0] offset:2048\n\t" "ds_read_b128 v[172:175], %[l0] offset:3072\n\t" "s_nop 3\n\t" \
    "buffer_load_dwordx4 v[248:251], %[mo], %[rs], %[sb] offen offset:2048 sc1\n\t" "ds_read_b128 v[176:179], %[l0] offset:4096\n\t" \
    "ds_read_b128 v[180:183], %[l0] offset:5120\n\t" "ds_read_b128 v[184:187], %[l0] offset:6144\n\t" "ds_read_b128 v[188:191], %[l0] offset:7168\n\t" "s_nop 3\n\t" \
    "buffer_load_dwordx4 v[252:255], v224, %[rs], %[sb] offen offset:2048 sc1\n\t" "v_mov_b64 v[200:201], 0\n\t" "v_mov_b64 v[202:203], 0\n\t" "v_mov_b64 v[204:205], 0\n\t" \
    "v_mov_b64 v[206:207], 0\n\t" "v_mov_b64 v[208:209], 0\n\t" "v_mov_b64 v[210:211], 0\n\t" "v_mov_b64 v[212:213], 0\n\t" "v_mov_b64 v[214:215], 0\n\t" \
    "v_mov_b64 v[216:217], 0\n\t" "v_mov_b64 v[218:219], 0\n\t" "v_mov_b64 v[220:221], 0\n\t" "v_mov_b64 v[222:223], 0\n\t" "s_waitcnt vmcnt(3)\n\t" XT(84, 85) \
    "v_smfmac_f32_16x16x64_bf16 v[232:235], v[240:243], a[0:7], %[ix]\n\t" "ds_write_b128 %[l1], v[240:243] offset:12288\n\t" \
    "v_smfmac_f32_16x16x64_bf16 v[236:239], v[240:243], a[8:15], %[ix]\n\t" "v_max3_u32 v225, v240, v241, v242\n\t" "s_waitcnt vmcnt(2)\n\t" \
    "v_smfmac_f32_16x16x64_bf16 v[232:235], v[244:247], a[16:23], %[ix]\n\t" "ds_write_b128 %[l1], v[244:247] offset:13312\n\t" "v_max3_u32 v225, v225, v243, v244\n\t" \
    "s_waitcnt lgkmcnt(8)\n\t" "v_smfmac_f32_16x16x64_bf16 v[236:239], v[244:247], v[160:167], %[ix]\n\t" "v_max3_u32 v225, v225, v245, v246\n\t" \
    "v_max_u32 v225, v225, v247\n\t" "v_cmp_eq_u32 vcc, -1, v225\n\t" "s_cmp_lg_u32 s60, 0\n\t" "s_cbranch_vccnz 41f\n\t" "42:\n\t" "s_waitcnt lgkmcnt(0)\n\t" \
    "s_cbranch_scc1 43f\n\t" "s_barrier\n\t" "43:\n\t" XT(86, 87) "ds_read_b128 v[16:19], %[l1] offset:0\n\t" "ds_read_b128 v[20:23], %[l1] offset:4096\n\t" \
    "ds_read_b128 v[24:27], %[l1] offset:8192\n\t" "s_waitcnt vmcnt(1)\n\t" "v_smfmac_f32_16x16x64_bf16 v[232:235], v[248:251], a[24:31], %[ix]\n\t" \
    "ds_write_b128 %[l1], v[248:251] offset:14336\n\t" "v_max3_u32 v225, v248, v249, v250\n\t" "v_smfmac_f32_16x16x64_bf16 v[236:239], v[248:251], a[32:39], %[ix]\n\t" \
    "ds_read_b128 v[28:31], %[l1] offset:1024\n\t" "ds_read_b128 v[240:243], %[l1] offset:5120\n\t" "ds_read_b128 v[244:247], %[l1] offset:9216\n\t" "s_waitcnt vmcnt(0)\n\t" \
    "v_smfmac_f32_16x16x64_bf16 v[232:235], v[252:255], a[40:47], %[ix]\n\t" "ds_write_b128 %[l1], v[252:255] offset:15360\n\t" "v_max3_u32 v225, v225, v251, v252\n\t" \
    "v_smfmac_f32_16x16x64_bf16 v[236:239], v[252:255], v[168:175], %[ix]\n\t" "v_max3_u32 v225, v225, v253, v254\n\t" "v_max_u32 v225, v225, v255\n\t" \
    "s_waitcnt lgkmcnt(7)\n\t" "v_smfmac_f32_16x16x64_bf16 v[232:235], v[16:19], a[48:55], %[ix]\n\t" "v_cmp_eq_u32 vcc, -1, v225\n\t" "s_cbranch_vccnz 44f\n\t" "45:\n\t" \
    "v_smfmac_f32_16x16x64_bf16 v[236:239], v[16:19], a[56:63], %[ix]\n\t" "ds_read_b128 v[160:163], %[l0] offset:8192\n\t" "ds_read_b128 v[164:167], %[l0] offset:9216\n\t" \
    "s_waitcnt lgkmcnt(8)\n\t" "v_smfmac_f32_16x16x64_bf16 v[232:235], v[20:23], a[64:71], %[ix]\n\t" "ds_read_b128 v[168:171], %[l0] offset:10240\n\t" \
    "ds_read_b128 v[172:175], %[l0] offset:11264\n\t" "v_smfmac_f32_16x16x64_bf16 v[236:239], v[20:23], v[176:183], %[ix]\n\t" "s_waitcnt lgkmcnt(4)\n\t" "s_barrier\n\t" \
    XT(88, 89) "v_smfmac_f32_16x16x64_bf16 v[232:235], v[24:27], a[72:79], %[ix]\n\t" "ds_read_b128 v[176:179], %[l0] offset:12288\n\t" \
    "ds_read_b128 v[180:183], %[l0] offset:13312\n\t" "v_smfmac_f32_16x16x64_bf16 v[236:239], v[24:27], a[80:87], %[ix]\n\t" "ds_read_b128 v[248:251], %[l1] offset:2048\n\t" \
    "ds_read_b128 v[252:255], %[l1] offset:6144\n\t" "v_smfmac_f32_16x16x64_bf16 v[232:235], v[28:31], a[88:95], %[ix]\n\t" "ds_read_b128 v[16:19], %[l1] offset:10240\n\t" \
    "ds_read_b128 v[20:23], %[l1] offset:3072\n\t" "v_smfmac_f32_16x16x64_bf16 v[236:239], v[28:31], v[184:191], %[ix]\n\t" "ds_read_b128 v[24:27], %[l1] offset:7168\n\t" \
    "ds_read_b128 v[184:187], %[l0] offset:14336\n\t" "v_smfmac_f32_16x16x64_bf16 v[232:235], v[240:243], a[96:103], %[ix]\n\t" \
    "ds_read_b128 v[188:191], %[l0] offset:15360\n\t" "ds_read_b128 v[28:31], %[l1] offset:11264\n\t" \
    "v_smfmac_f32_16x16x64_bf16 v[236:239], v[240:243], a[104:111], %[ix]\n\t" "v_smfmac_f32_16x16x64_bf16 v[232:235], v[244:247], a[112:119], %[ix]\n\t" \
    "s_waitcnt lgkmcnt(12)\n\t" "v_smfmac_f32_16x16x64_bf16 v[236:239], v[244:247], v[160:167], %[ix]\n\t" "ds_read_b128 v[160:163], %[l0] offset:16384\n\t" \
    "ds_read_b128 v[164:167], %[l0] offset:17408\n\t" "s_waitcnt lgkmcnt(9)\n\t" "v_smfmac_f32_16x16x64_bf16 v[232:235], v[248:251], a[120:127], %[ix]\n\t" \
    "v_smfmac_f32_16x16x64_bf16 v[236:239], v[248:251], a[128:135], %[ix]\n\t" "s_waitcnt lgkmcnt(8)\n\t" \
    "v_smfmac_f32_16x16x64_bf16 v[232:235], v[252:255], a[136:143], %[ix]\n\t" "v_smfmac_f32_16x16x64_bf16 v[236:239], v[252:255], v[168:175], %[ix]\n\t" \
    "ds_read_b128 v[168:171], %[l0] offset:18432\n\t" "ds_read_b128 v[172:175], %[l0] offset:19456\n\t" "s_waitcnt lgkmcnt(9)\n\t" \
    "v_smfmac_f32_16x16x64_bf16 v[232:235], v[16:19], a[144:151], %[ix]\n\t" "v_smfmac_f32_16x16x64_bf16 v[236:239], v[16:19], a[152:159], %[ix]\n\t" \
    "s_waitcnt lgkmcnt(8)\n\t" "v_smfmac_f32_16x16x64_bf16 v[232:235], v[20:23], a[160:167], %[ix]\n\t" \
    "v_smfmac_f32_16x16x64_bf16 v[236:239], v[20:23], v[176:183], %[ix]\n\t" "ds_read_b128 v[176:179], %[l0] offset:20480\n\t" \
    "ds_read_b128 v[180:183], %[l0] offset:21504\n\t" "s_waitcnt lgkmcnt(9)\n\t" "v_smfmac_f32_16x16x64_bf16 v[232:235], v[24:27], a[168:175], %[ix]\n\t" \
    "v_smfmac_f32_16x16x64_bf16 v[236:239], v[24:27], a[176:183], %[ix]\n\t" "s_waitcnt lgkmcnt(6)\n\t" \
    "v_smfmac_f32_16x16x64_bf16 v[232:235], v[28:31], a[184:191], %[ix]\n\t" "v_smfmac_f32_16x16x64_bf16 v[236:239], v[28:31], v[184:191], %[ix]\n\t" \
    "ds_read_b128 v[184:187], %[l0] offset:22528\n\t" "ds_read_b128 v[188:191], %[l0] offset:23552\n\t" XT(90, 91) "s_nop 4\n\t" "v_add_f32 v232, v232, v233\n\t" \
    "v_add_f32 v234, v234, v235\n\t" "v_add_f32 v236, v236, v237\n\t" "v_add_f32 v238, v238, v239\n\t" "v_cvt_pk_bf16_f32 v232, v232, v236\n\t" \
    "v_cvt_pk_bf16_f32 v233, v234, v238\n\t" "ds_write2_b32 %[vw], v232, v233 offset1:68\n\t" "s_waitcnt lgkmcnt(0)\n\t" "s_barrier\n\t" XT(92, 93) \
    "ds_read_b128 v[240:243], %[vr] offset:0\n\t" "ds_read_b128 v[244:247], %[vr] offset:64\n\t" "ds_read_b128 v[248:251], %[vr] offset:128\n\t" \
    "ds_read_b128 v[252:255], %[vr] offset:192\n\t" "s_mov_b32 s61, %[ub]\n\t" "s_waitcnt lgkmcnt(3)\n\t" \
    "v_smfmac_f32_16x16x64_bf16 v[192:195], v[240:243], v[32:39], %[ix]\n\t" "v_smfmac_f32_16x16x64_bf16 v[196:199], v[240:243], v[48:55], %[ix]\n\t" \
    "s_waitcnt lgkmcnt(2)\n\t" "v_smfmac_f32_16x16x64_bf16 v[192:195], v[244:247], v[40:47], %[ix]\n\t" \
    "v_smfmac_f32_16x16x64_bf16 v[196:199], v[244:247], v[56:63], %[ix]\n\t" "s_waitcnt lgkmcnt(1)\n\t" \
    "v_smfmac_f32_16x16x64_bf16 v[192:195], v[248:251], a[192:199], %[ix]\n\t" "v_smfmac_f32_16x16x64_bf16 v[196:199], v[248:251], a[200:207], %[ix]\n\t" \
    "s_waitcnt lgkmcnt(0)\n\t" "v_smfmac_f32_16x16x64_bf16 v[192:195], v[252:255], v[160:167], %[ix]\n\t" "ds_read_b128 v[160:163], %[l0] offset:24576\n\t" \
    "ds_read_b128 v[164:167], %[l0] offset:25600\n\t" "v_smfmac_f32_16x16x64_bf16 v[196:199], v[252:255], v[168:175], %[ix]\n\t" \
    "ds_read_b128 v[168:171], %[l0] offset:26624\n\t" "ds_read_b128 v[172:175], %[l0] offset:27648\n\t" \
    "v_smfmac_f32_16x16x64_bf16 v[200:203], v[240:243], v[64:71], %[ix]\n\t" "s_nop 1\n\t" "v_smfmac_f32_16x16x64_bf16 v[204:207], v[240:243], v[80:87], %[ix]\n\t" \
    "v_cvt_pk_bf16_f32 v224, v192, v194\n\t" "v_cvt_pk_bf16_f32 v225, v193, v195\n\t" "v_smfmac_f32_16x16x64_bf16 v[200:203], v[244:247], v[72:79], %[ix]\n\t" \
    "v_cvt_pk_bf16_f32 v226, v196, v198\n\t" "v_cvt_pk_bf16_f32 v227, v197, v199\n\t" "v_smfmac_f32_16x16x64_bf16 v[204:207], v[244:247], v[88:95], %[ix]\n\t" "s_nop 0\n\t" \
    "v_permlane32_swap_b32 v224, v226\n\t" "v_smfmac_f32_16x16x64_bf16 v[200:203], v[248:251], a[208:215], %[ix]\n\t" "v_permlane32_swap_b32 v225, v227\n\t" "s_nop 0\n\t" \
    "v_smfmac_f32_16x16x64_bf16 v[204:207], v[248:251], a[216:223], %[ix]\n\t" "v_permlane16_swap_b32 v224, v225\n\t" "v_permlane16_swap_b32 v226, v227\n\t" \
    "v_smfmac_f32_16x16x64_bf16 v[200:203], v[252:255], v[176:183], %[ix]\n\t" "buffer_store_dwordx4 v[224:227], %[uo], %[rs], s61 offen sc1\n\t" \
    "s_add_u32 s61, s61, 0x10000\n\t" XT(94, 95) "v_smfmac_f32_16x16x64_bf16 v[204:207], v[252:255], v[184:191], %[ix]\n\t" "ds_read_b128 v[176:179], %[l0] offset:28672\n\t" \
    "ds_read_b128 v[180:183], %[l0] offset:29696\n\t" "v_smfmac_f32_16x16x64_bf16 v[208:211], v[240:243], v[96:103], %[ix]\n\t" \
    "ds_read_b128 v[184:187], %[l0] offset:30720\n\t" "ds_read_b128 v[188:191], %[l0] offset:31744\n\t" \
    "v_smfmac_f32_16x16x64_bf16 v[212:215], v[240:243], v[112:119], %[ix]\n\t" "v_cvt_pk_bf16_f32 v228, v200, v202\n\t" "v_cvt_pk_bf16_f32 v229, v201, v203\n\t" \
    "v_smfmac_f32_16x16x64_bf16 v[208:211], v[244:247], v[104:111], %[ix]\n\t" "v_cvt_pk_bf16_f32 v230, v204, v206\n\t" "v_cvt_pk_bf16_f32 v231, v205, v207\n\t" \
    "v_smfmac_f32_16x16x64_bf16 v[212:215], v[244:247], v[120:127], %[ix]\n\t" "s_nop 0\n\t" "v_permlane32_swap_b32 v228, v230\n\t" \
    "v_smfmac_f32_16x16x64_bf16 v[208:211], v[248:251], a[224:231], %[ix]\n\t" "v_permlane32_swap_b32 v229, v231\n\t" "s_nop 0\n\t" \
    "v_smfmac_f32_16x16x64_bf16 v[212:215], v[248:251], a[232:239], %[ix]\n\t" "v_permlane16_swap_b32 v228, v229\n\t" "v_permlane16_swap_b32 v230, v231\n\t" \
    "s_waitcnt lgkmcnt(6)\n\t" "v_smfmac_f32_16x16x64_bf16 v[208:211], v[252:255], v[160:167], %[ix]\n\t" "buffer_store_dwordx4 v[228:231], %[uo], %[rs], s61 offen sc1\n\t" \
    "s_add_u32 s61, s61, 0x10000\n\t" "s_waitcnt lgkmcnt(4)\n\t" "v_smfmac_f32_16x16x64_bf16 v[212:215], v[252:255], v[168:175], %[ix]\n\t" "s_nop 1\n\t" \
    "v_smfmac_f32_16x16x64_bf16 v[216:219], v[240:243], v[128:135], %[ix]\n\t" "s_nop 1\n\t" "v_smfmac_f32_16x16x64_bf16 v[220:223], v[240:243], v[144:151], %[ix]\n\t" \
    "v_cvt_pk_bf16_f32 v224, v208, v210\n\t" "v_cvt_pk_bf16_f32 v225, v209, v211\n\t" "v_smfmac_f32_16x16x64_bf16 v[216:219], v[244:247], v[136:143], %[ix]\n\t" \
    "v_cvt_pk_bf16_f32 v226, v212, v214\n\t" "v_cvt_pk_bf16_f32 v227, v213, v215\n\t" "v_smfmac_f32_16x16x64_bf16 v[220:223], v[244:247], v[152:159], %[ix]\n\t" "s_nop 0\n\t" \
    "v_permlane32_swap_b32 v224, v226\n\t" "v_smfmac_f32_16x16x64_bf16 v[216:219], v[248:251], a[240:247], %[ix]\n\t" "v_permlane32_swap_b32 v225, v227\n\t" "s_nop 0\n\t" \
    "v_smfmac_f32_16x16x64_bf16 v[220:223], v[248:251], a[248:255], %[ix]\n\t" "v_permlane16_swap_b32 v224, v225\n\t" "v_permlane16_swap_b32 v226, v227\n\t" \
    "s_waitcnt lgkmcnt(2)\n\t" "v_smfmac_f32_16x16x64_bf16 v[216:219], v[252:255], v[176:183], %[ix]\n\t" "buffer_store_dwordx4 v[224:227], %[uo], %[rs], s61 offen sc1\n\t" \
    "s_add_u32 s61, s61, 0x10000\n\t" "s_waitcnt lgkmcnt(0)\n\t" "v_smfmac_f32_16x16x64_bf16 v[220:223], v[252:255], v[184:191], %[ix]\n\t" "s_nop 5\n\t" \
    "v_cvt_pk_bf16_f32 v228, v216, v218\n\t" "v_cvt_pk_bf16_f32 v229, v217, v219\n\t" "v_cvt_pk_bf16_f32 v230, v220, v222\n\t" "v_cvt_pk_bf16_f32 v231, v221, v223\n\t" \
    "s_nop 0\n\t" "v_permlane32_swap_b32 v228, v230\n\t" "v_permlane32_swap_b32 v229, v231\n\t" "s_nop 1\n\t" "v_permlane16_swap_b32 v228, v229\n\t" \
    "v_permlane16_swap_b32 v230, v231\n\t" "buffer_store_dwordx4 v[228:231], %[uo], %[rs], s61 offen sc1\n\t" "s_nop 1\n\t" XT(96, 97) "s_branch 99f\n\t" "41:\n\t" \
    "s_cmp_eq_u32 %[bd], 0\n\t" "s_cbranch_scc1 46f\n\t" "s_sub_u32 %[bd], %[bd], 1\n\t" XCNT(100) "s_waitcnt lgkmcnt(0)\n\t" "s_nop 7\n\t" "s_branch 40b\n\t" "46:\n\t" \
    "s_cmp_lg_u32 s60, 0\n\t" "s_branch 42b\n\t" "44:\n\t" "s_cmp_eq_u32 %[bd], 0\n\t" "s_cbranch_scc1 45b\n\t" "s_sub_u32 %[bd], %[bd], 1\n\t" XCNT(101) \
    "s_mov_b32 s60, 1\n\t" "s_waitcnt lgkmcnt(0)\n\t" "s_nop 7\n\t" "s_branch 40b\n\t" "99:\n\t"
#define Y_STEP \
    "s_getpc_b64 s[62:63]\n\t" "11:\n\t" "s_memtime s[64:65]\n\t" "s_waitcnt lgkmcnt(0)\n\t" "s_sub_u32 s61, %[tu], s64\n\t" "s_cmp_gt_i32 s61, 124\n\t" \
    "s_cbranch_scc1 11b\n\t" "s_max_i32 s61, s61, 0\n\t" "s_and_b32 s61, s61, 124\n\t" "s_sub_u32 s61, 12f-11b, s61\n\t" "s_add_u32 s62, s62, s61\n\t" \
    "s_addc_u32 s63, s63, 0\n\t" "s_setpc_b64 s[62:63]\n\t" ".rept 31\n\t" "s_nop 0\n\t" ".endr\n\t" "12:\n\t" XT(90, 91) \
    "buffer_load_dwordx4 v[144:147], %[u0], %[rs], %[ub] offen offset:0 sc1\n\t" "buffer_load_dwordx4 v[148:151], %[u0], %[rs], %[ub] offen offset:1024 sc1\n\t" \
    "buffer_load_dwordx4 v[152:155], %[u0], %[rs], %[ub] offen offset:2048 sc1\n\t" "buffer_load_dwordx4 v[156:159], %[u0], %[rs], %[ub] offen offset:3072 sc1\n\t" \
    "buffer_load_dwordx4 v[160:163], %[u1], %[rs], %[ub] offen offset:0 sc1\n\t" "buffer_load_dwordx4 v[164:167], %[u1], %[rs], %[ub] offen offset:1024 sc1\n\t" \
    "buffer_load_dwordx4 v[168:171], %[u1], %[rs], %[ub] offen offset:2048 sc1\n\t" "buffer_load_dwordx4 v[172:175], %[u1], %[rs], %[ub] offen offset:3072 sc1\n\t" \
    "s_cmp_lg_u32 %[late], 0\n\t" "s_cbranch_scc1 14f\n\t" "s_getpc_b64 s[62:63]\n\t" "13:\n\t" "s_memtime s[64:65]\n\t" "s_waitcnt lgkmcnt(0)\n\t" \
    "s_sub_u32 s61, %[tg], s64\n\t" "s_cmp_gt_i32 s61, 124\n\t" "s_cbranch_scc1 13b\n\t" "s_max_i32 s61, s61, 0\n\t" "s_and_b32 s61, s61, 124\n\t" \
    "s_sub_u32 s61, 14f-13b, s61\n\t" "s_add_u32 s62, s62, s61\n\t" "s_addc_u32 s63, s63, 0\n\t" "s_setpc_b64 s[62:63]\n\t" ".rept 31\n\t" "s_nop 0\n\t" ".endr\n\t" "14:\n\t" \
    XT(92, 93) "20:\n\t" "buffer_load_dwordx4 v[192:195], %[x0], %[rs], %[pb] offen offset:0 sc1\n\t" \
    "buffer_load_dwordx4 v[196:199], %[x0], %[rs], %[pb] offen offset:1024 sc1\n\t" "buffer_load_dwordx4 v[200:203], %[x0], %[rs], %[pb] offen offset:2048 sc1\n\t" \
    "buffer_load_dwordx4 v[204:207], %[x0], %[rs], %[pb] offen offset:3072 sc1\n\t" "buffer_load_dwordx4 v[208:211], %[x1], %[rs], %[pb] offen offset:0 sc1\n\t" \
    "buffer_load_dwordx4 v[212:215], %[x1], %[rs], %[pb] offen offset:1024 sc1\n\t" "buffer_load_dwordx4 v[216:219], %[x1], %[rs], %[pb] offen offset:2048 sc1\n\t" \
    "buffer_load_dwordx4 v[220:223], %[x1], %[rs], %[pb] offen offset:3072 sc1\n\t" "buffer_load_dwordx4 v[224:227], %[x2], %[rs], %[pb] offen offset:0 sc1\n\t" \
    "buffer_load_dwordx4 v[228:231], %[x2], %[rs], %[pb] offen offset:1024 sc1\n\t" "buffer_load_dwordx4 v[232:235], %[x2], %[rs], %[pb] offen offset:2048 sc1\n\t" \
    "buffer_load_dwordx4 v[236:239], %[x2], %[rs], %[pb] offen offset:3072 sc1\n\t" "buffer_load_dwordx4 v[240:243], %[x3], %[rs], %[pb] offen offset:0 sc1\n\t" \
    "buffer_load_dwordx4 v[244:247], %[x3], %[rs], %[pb] offen offset:1024 sc1\n\t" "buffer_load_dwordx4 v[248:251], %[x3], %[rs], %[pb] offen offset:2048 sc1\n\t" \
    "buffer_load_dwordx4 v[252:255], %[x3], %[rs], %[pb] offen offset:3072 sc1\n\t" "v_mov_b64 v[120:121], 0\n\t" "v_mov_b64 v[122:123], 0\n\t" \
    "buffer_load_dwordx2 v[176:177], %[pm], %[rs], %[pb] offen offset:0 sc1\n\t" "buffer_load_dwordx2 v[178:179], %[pm], %[rs], %[pb] offen offset:128 sc1\n\t" \
    "buffer_load_dwordx2 v[180:181], %[pm], %[rs], %[pb] offen offset:1024 sc1\n\t" "buffer_load_dwordx2 v[182:183], %[pm], %[rs], %[pb] offen offset:1152 sc1\n\t" \
    "buffer_load_dwordx2 v[184:185], %[pm], %[rs], %[pb] offen offset:2048 sc1\n\t" "buffer_load_dwordx2 v[186:187], %[pm], %[rs], %[pb] offen offset:2176 sc1\n\t" \
    "buffer_load_dwordx2 v[188:189], %[pm], %[rs], %[pb] offen offset:3072 sc1\n\t" "buffer_load_dwordx2 v[190:191], %[pm], %[rs], %[pb] offen offset:3200 sc1\n\t" \
    "buffer_load_dword %[sv], %[so], %[rs], %[pb] offen sc1\n\t" "v_mov_b32 v127, -1\n\t" "s_waitcnt vmcnt(24)\n\t" XT(94, 95) \
    "v_smfmac_f32_16x16x64_bf16 v[120:123], v[192:195], a[0:7], %[ix]\n\t" "s_waitcnt vmcnt(23)\n\t" "v_smfmac_f32_16x16x64_bf16 v[120:123], v[196:199], a[8:15], %[ix]\n\t" \
    "s_waitcnt vmcnt(22)\n\t" "v_smfmac_f32_16x16x64_bf16 v[120:123], v[200:203], a[16:23], %[ix]\n\t" "s_waitcnt vmcnt(21)\n\t" \
    "v_smfmac_f32_16x16x64_bf16 v[120:123], v[204:207], a[24:31], %[ix]\n\t" "s_waitcnt vmcnt(20)\n\t" \
    "v_smfmac_f32_16x16x64_bf16 v[120:123], v[208:211], a[32:39], %[ix]\n\t" "s_waitcnt vmcnt(19)\n\t" \
    "v_smfmac_f32_16x16x64_bf16 v[120:123], v[212:215], a[40:47], %[ix]\n\t" "s_waitcnt vmcnt(18)\n\t" \
    "v_smfmac_f32_16x16x64_bf16 v[120:123], v[216:219], a[48:55], %[ix]\n\t" "s_waitcnt vmcnt(17)\n\t" \
    "v_smfmac_f32_16x16x64_bf16 v[120:123], v[220:223], a[56:63], %[ix]\n\t" "s_waitcnt vmcnt(16)\n\t" \
    "v_smfmac_f32_16x16x64_bf16 v[120:123], v[224:227], a[64:71], %[ix]\n\t" "s_waitcnt vmcnt(15)\n\t" \
    "v_smfmac_f32_16x16x64_bf16 v[120:123], v[228:231], a[72:79], %[ix]\n\t" "s_waitcnt vmcnt(14)\n\t" \
    "v_smfmac_f32_16x16x64_bf16 v[120:123], v[232:235], a[80:87], %[ix]\n\t" "s_waitcnt vmcnt(13)\n\t" \
    "v_smfmac_f32_16x16x64_bf16 v[120:123], v[236:239], a[88:95], %[ix]\n\t" "s_waitcnt vmcnt(12)\n\t" \
    "v_smfmac_f32_16x16x64_bf16 v[120:123], v[240:243], a[96:103], %[ix]\n\t" "s_waitcnt vmcnt(11)\n\t" \
    "v_smfmac_f32_16x16x64_bf16 v[120:123], v[244:247], a[104:111], %[ix]\n\t" "s_waitcnt vmcnt(10)\n\t" \
    "v_smfmac_f32_16x16x64_bf16 v[120:123], v[248:251], a[112:119], %[ix]\n\t" "s_waitcnt vmcnt(9)\n\t" \
    "v_smfmac_f32_16x16x64_bf16 v[120:123], v[252:255], a[120:127], %[ix]\n\t" "s_waitcnt vmcnt(7)\n\t" \
    "v_smfmac_f32_16x16x64_bf16 v[120:123], v[176:179], v[144:151], %[ix]\n\t" "s_waitcnt vmcnt(5)\n\t" \
    "v_smfmac_f32_16x16x64_bf16 v[120:123], v[180:183], v[152:159], %[ix]\n\t" "s_waitcnt vmcnt(3)\n\t" \
    "v_smfmac_f32_16x16x64_bf16 v[120:123], v[184:187], v[160:167], %[ix]\n\t" "s_waitcnt vmcnt(1)\n\t" \
    "v_smfmac_f32_16x16x64_bf16 v[120:123], v[188:191], v[168:175], %[ix]\n\t" XT(82, 83) "s_nop 7\n\t" "v_add_f32 v120, v120, v121\n\t" "v_add_f32 v122, v122, v123\n\t" \
    "v_cvt_pk_bf16_f32 v124, v120, v122\n\t" "v_cmp_u_f32 vcc, v120, v122\n\t" "s_cbranch_vccnz 30f\n\t" "21:\n\t" \
    "v_mov_b32_dpp v125, v124 quad_perm:[1,0,3,2] row_mask:0xf bank_mask:0xf\n\t" "v_perm_b32 v124, v125, v124, %[ps]\n\t" "v_min_u32 v124, -2, v124\n\t" \
    "buffer_store_dword v124, %[mw], %[rs], %[mb] offen sc1\n\t" XT(84, 85) "buffer_store_dword v127, %[mw], %[rs], %[mr] offen sc1\n\t" "s_memtime s[64:65]\n\t" \
    "s_barrier\n\t" XT(98, 99) "s_waitcnt lgkmcnt(0)\n\t" "s_and_b32 %[bad], s64, -2\n\t" "s_branch 40f\n\t" "30:\n\t" "v_max_u32 v126, v144, v145\n\t" \
    "v_max3_u32 v126, v126, v146, v147\n\t" "v_max3_u32 v126, v126, v148, v149\n\t" "v_max3_u32 v126, v126, v150, v151\n\t" "v_max3_u32 v126, v126, v152, v153\n\t" \
    "v_max3_u32 v126, v126, v154, v155\n\t" "v_max3_u32 v126, v126, v156, v157\n\t" "v_max3_u32 v126, v126, v158, v159\n\t" "v_max3_u32 v126, v126, v160, v161\n\t" \
    "v_max3_u32 v126, v126, v162, v163\n\t" "v_max3_u32 v126, v126, v164, v165\n\t" "v_max3_u32 v126, v126, v166, v167\n\t" "v_max3_u32 v126, v126, v168, v169\n\t" \
    "v_max3_u32 v126, v126, v170, v171\n\t" "v_max3_u32 v126, v126, v172, v173\n\t" "v_max3_u32 v126, v126, v174, v175\n\t" UCNT "v_max3_u32 v126, v126, v176, v177\n\t" \
    "v_max3_u32 v126, v126, v178, v179\n\t" "v_max3_u32 v126, v126, v180, v181\n\t" "v_max3_u32 v126, v126, v182, v183\n\t" "v_max3_u32 v126, v126, v184, v185\n\t" \
    "v_max3_u32 v126, v126, v186, v187\n\t" "v_max3_u32 v126, v126, v188, v189\n\t" "v_max3_u32 v126, v126, v190, v191\n\t" "v_max3_u32 v126, v126, v192, v193\n\t" \
    "v_max3_u32 v126, v126, v194, v195\n\t" "v_max3_u32 v126, v126, v196, v197\n\t" "v_max3_u32 v126, v126, v198, v199\n\t" "v_max3_u32 v126, v126, v200, v201\n\t" \
    "v_max3_u32 v126, v126, v202, v203\n\t" "v_max3_u32 v126, v126, v204, v205\n\t" "v_max3_u32 v126, v126, v206, v207\n\t" "v_max3_u32 v126, v126, v208, v209\n\t" \
    "v_max3_u32 v126, v126, v210, v211\n\t" "v_max3_u32 v126, v126, v212, v213\n\t" "v_max3_u32 v126, v126, v214, v215\n\t" "v_max3_u32 v126, v126, v216, v217\n\t" \
    "v_max3_u32 v126, v126, v218, v219\n\t" "v_max3_u32 v126, v126, v220, v221\n\t" "v_max3_u32 v126, v126, v222, v223\n\t" "v_max3_u32 v126, v126, v224, v225\n\t" \
    "v_max3_u32 v126, v126, v226, v227\n\t" "v_max3_u32 v126, v126, v228, v229\n\t" "v_max3_u32 v126, v126, v230, v231\n\t" "v_max3_u32 v126, v126, v232, v233\n\t" \
    "v_max3_u32 v126, v126, v234, v235\n\t" "v_max3_u32 v126, v126, v236, v237\n\t" "v_max3_u32 v126, v126, v238, v239\n\t" "v_max3_u32 v126, v126, v240, v241\n\t" \
    "v_max3_u32 v126, v126, v242, v243\n\t" "v_max3_u32 v126, v126, v244, v245\n\t" "v_max3_u32 v126, v126, v246, v247\n\t" "v_max3_u32 v126, v126, v248, v249\n\t" \
    "v_max3_u32 v126, v126, v250, v251\n\t" "v_max3_u32 v126, v126, v252, v253\n\t" "v_max3_u32 v126, v126, v254, v255\n\t" "v_cmp_eq_u32 vcc, -1, v126\n\t" \
    "s_cbranch_vccz 21b\n\t" "s_cmp_eq_u32 %[bd], 0\n\t" "s_cbranch_scc1 21b\n\t" XCNT(100) "s_mov_b32 %[bad], 1\n\t" "s_waitcnt vmcnt(0)\n\t" "40:\n\t"
#define G_LOOP \
    "v_xor_b32 v72, 0, %[xo]\n\t" "v_add_u32 v76, 0x10000, v72\n\t" "v_xor_b32 v88, 0, %[xs]\n\t" "v_add_u32 v88, 0, v88\n\t" "v_xor_b32 v73, 64, %[xo]\n\t" \
    "v_add_u32 v77, 0x10000, v73\n\t" "v_xor_b32 v89, 16, %[xs]\n\t" "v_add_u32 v89, 2048, v89\n\t" "v_xor_b32 v74, 128, %[xo]\n\t" "v_add_u32 v78, 0x10000, v74\n\t" \
    "v_xor_b32 v90, 32, %[xs]\n\t" "v_add_u32 v90, 4096, v90\n\t" "v_xor_b32 v75, 192, %[xo]\n\t" "v_add_u32 v79, 0x10000, v75\n\t" "v_xor_b32 v91, 48, %[xs]\n\t" \
    "v_add_u32 v91, 6144, v91\n\t" "v_xor_b32 v92, 64, %[xs]\n\t" "v_add_u32 v92, 8192, v92\n\t" "v_xor_b32 v93, 80, %[xs]\n\t" "v_add_u32 v93, 10240, v93\n\t" \
    "v_xor_b32 v94, 96, %[xs]\n\t" "v_add_u32 v94, 12288, v94\n\t" "v_xor_b32 v95, 112, %[xs]\n\t" "v_add_u32 v95, 14336, v95\n\t" "v_mov_b32 v96, 2.0\n\t" \
    "s_mov_b32 s60, 0\n\t" "s_mov_b32 s62, 0\n\t" "s_waitcnt vmcnt(0)\n\t" "s_barrier\n\t" "ds_read_b128 v[100:103], v72 offset:0\n\t" \
    "ds_read_b128 v[104:107], v72 offset:32768\n\t" "ds_read_b128 v[108:111], v73 offset:0\n\t" "ds_read_b128 v[112:115], v73 offset:32768\n\t" \
    "ds_read_b128 v[116:119], v74 offset:0\n\t" "10:\n\t" XT(80, 81) "s_waitcnt lgkmcnt(4)\n\t" "v_mfma_f32_16x16x32_bf16 v[32:35], a[0:3], v[100:103], 0\n\t" \
    "ds_read_b128 v[120:123], v74 offset:32768\n\t" "s_add_u32 s61, s60, 1\n\t" "s_and_b32 s61, s61, 63\n\t" "v_mfma_f32_16x16x32_bf16 v[36:39], a[4:7], v[100:103], 0\n\t" \
    "s_lshl_b32 s61, s61, 16\n\t" "s_add_u32 s63, %[lw], 65536\n\t" "s_waitcnt lgkmcnt(4)\n\t" "v_mfma_f32_16x16x32_bf16 v[40:43], a[0:3], v[104:107], 0\n\t" \
    "ds_read_b128 v[124:127], v75 offset:0\n\t" "v_mfma_f32_16x16x32_bf16 v[44:47], a[4:7], v[104:107], 0\n\t" "s_waitcnt lgkmcnt(4)\n\t" \
    "v_mfma_f32_16x16x32_bf16 v[32:35], a[8:11], v[108:111], v[32:35]\n\t" "ds_read_b128 v[128:131], v75 offset:32768\n\t" "v_cvt_pk_bf16_f32 v64, v48, v49\n\t" \
    "v_mfma_f32_16x16x32_bf16 v[36:39], a[12:15], v[108:111], v[36:39]\n\t" "v_cvt_pk_bf16_f32 v65, v50, v51\n\t" "v_cvt_pk_bf16_f32 v66, v52, v53\n\t" \
    "s_waitcnt lgkmcnt(4)\n\t" "v_mfma_f32_16x16x32_bf16 v[40:43], a[8:11], v[112:115], v[40:43]\n\t" "ds_read_b128 v[100:103], v72 offset:256\n\t" \
    "v_cvt_pk_bf16_f32 v67, v54, v55\n\t" "v_mfma_f32_16x16x32_bf16 v[44:47], a[12:15], v[112:115], v[44:47]\n\t" "v_cvt_pk_bf16_f32 v68, v56, v57\n\t" \
    "v_cvt_pk_bf16_f32 v69, v58, v59\n\t" "s_waitcnt lgkmcnt(4)\n\t" "v_mfma_f32_16x16x32_bf16 v[32:35], a[16:19], v[116:119], v[32:35]\n\t" \
    "ds_read_b128 v[104:107], v72 offset:33024\n\t" "v_cvt_pk_bf16_f32 v70, v60, v61\n\t" "v_mfma_f32_16x16x32_bf16 v[36:39], a[20:23], v[116:119], v[36:39]\n\t" \
    "v_cvt_pk_bf16_f32 v71, v62, v63\n\t" "buffer_store_dwordx4 v[64:67], v96, %[rs], s62 offen offset:0 sc1\n\t" "s_waitcnt lgkmcnt(4)\n\t" \
    "v_mfma_f32_16x16x32_bf16 v[40:43], a[16:19], v[120:123], v[40:43]\n\t" "ds_read_b128 v[108:111], v73 offset:256\n\t" \
    "v_mfma_f32_16x16x32_bf16 v[44:47], a[20:23], v[120:123], v[44:47]\n\t" "s_waitcnt lgkmcnt(4)\n\t" "v_mfma_f32_16x16x32_bf16 v[32:35], a[24:27], v[124:127], v[32:35]\n\t" \
    "ds_read_b128 v[112:115], v73 offset:33024\n\t" "v_mfma_f32_16x16x32_bf16 v[36:39], a[28:31], v[124:127], v[36:39]\n\t" \
    "buffer_store_dwordx4 v[68:71], v96, %[rs], s62 offen offset:256 sc1\n\t" "s_waitcnt lgkmcnt(4)\n\t" \
    "v_mfma_f32_16x16x32_bf16 v[40:43], a[24:27], v[128:131], v[40:43]\n\t" "ds_read_b128 v[116:119], v74 offset:256\n\t" "s_mov_b32 m0, s63\n\t" \
    "v_mfma_f32_16x16x32_bf16 v[44:47], a[28:31], v[128:131], v[44:47]\n\t" "s_add_u32 s63, s63, 0x800\n\t" "buffer_load_dwordx4 v88, %[rx], s61 offen lds\n\t" \
    "s_waitcnt lgkmcnt(4)\n\t" "v_mfma_f32_16x16x32_bf16 v[32:35], a[32:35], v[100:103], v[32:35]\n\t" "ds_read_b128 v[120:123], v74 offset:33024\n\t" \
    "v_mfma_f32_16x16x32_bf16 v[36:39], a[36:39], v[100:103], v[36:39]\n\t" "buffer_load_dwordx4 v88, %[rx], s61 offen offset:1024 lds\n\t" "s_waitcnt lgkmcnt(4)\n\t" \
    "v_mfma_f32_16x16x32_bf16 v[40:43], a[32:35], v[104:107], v[40:43]\n\t" "ds_read_b128 v[124:127], v75 offset:256\n\t" "s_mov_b32 m0, s63\n\t" \
    "v_mfma_f32_16x16x32_bf16 v[44:47], a[36:39], v[104:107], v[44:47]\n\t" "s_add_u32 s63, s63, 0x800\n\t" "buffer_load_dwordx4 v89, %[rx], s61 offen lds\n\t" \
    "s_waitcnt lgkmcnt(4)\n\t" "v_mfma_f32_16x16x32_bf16 v[32:35], a[40:43], v[108:111], v[32:35]\n\t" "ds_read_b128 v[128:131], v75 offset:33024\n\t" \
    "v_mfma_f32_16x16x32_bf16 v[36:39], a[44:47], v[108:111], v[36:39]\n\t" "buffer_load_dwordx4 v89, %[rx], s61 offen offset:1024 lds\n\t" "s_waitcnt lgkmcnt(4)\n\t" \
    "v_mfma_f32_16x16x32_bf16 v[40:43], a[40:43], v[112:115], v[40:43]\n\t" "ds_read_b128 v[100:103], v72 offset:512\n\t" "s_mov_b32 m0, s63\n\t" \
    "v_mfma_f32_16x16x32_bf16 v[44:47], a[44:47], v[112:115], v[44:47]\n\t" "s_add_u32 s63, s63, 0x800\n\t" "buffer_load_dwordx4 v90, %[rx], s61 offen lds\n\t" \
    "s_waitcnt lgkmcnt(4)\n\t" "v_mfma_f32_16x16x32_bf16 v[32:35], a[48:51], v[116:119], v[32:35]\n\t" "ds_read_b128 v[104:107], v72 offset:33280\n\t" \
    "v_mfma_f32_16x16x32_bf16 v[36:39], a[52:55], v[116:119], v[36:39]\n\t" "buffer_load_dwordx4 v90, %[rx], s61 offen offset:1024 lds\n\t" "s_waitcnt lgkmcnt(4)\n\t" \
    "v_mfma_f32_16x16x32_bf16 v[40:43], a[48:51], v[120:123], v[40:43]\n\t" "ds_read_b128 v[108:111], v73 offset:512\n\t" "s_mov_b32 m0, s63\n\t" \
    "v_mfma_f32_16x16x32_bf16 v[44:47], a[52:55], v[120:123], v[44:47]\n\t" "s_add_u32 s63, s63, 0x800\n\t" "buffer_load_dwordx4 v91, %[rx], s61 offen lds\n\t" \
    "s_waitcnt lgkmcnt(4)\n\t" "v_mfma_f32_16x16x32_bf16 v[32:35], a[56:59], v[124:127], v[32:35]\n\t" "ds_read_b128 v[112:115], v73 offset:33280\n\t" \
    "v_mfma_f32_16x16x32_bf16 v[36:39], a[60:63], v[124:127], v[36:39]\n\t" "buffer_load_dwordx4 v91, %[rx], s61 offen offset:1024 lds\n\t" "s_waitcnt lgkmcnt(4)\n\t" \
    "v_mfma_f32_16x16x32_bf16 v[40:43], a[56:59], v[128:131], v[40:43]\n\t" "ds_read_b128 v[116:119], v74 offset:512\n\t" "s_mov_b32 m0, s63\n\t" \
    "v_mfma_f32_16x16x32_bf16 v[44:47], a[60:63], v[128:131], v[44:47]\n\t" "s_add_u32 s63, s63, 0x800\n\t" "buffer_load_dwordx4 v92, %[rx], s61 offen lds\n\t" \
    "s_waitcnt lgkmcnt(4)\n\t" "v_mfma_f32_16x16x32_bf16 v[32:35], a[64:67], v[100:103], v[32:35]\n\t" "ds_read_b128 v[120:123], v74 offset:33280\n\t" \
    "v_mfma_f32_16x16x32_bf16 v[36:39], a[68:71], v[100:103], v[36:39]\n\t" "buffer_load_dwordx4 v92, %[rx], s61 offen offset:1024 lds\n\t" "s_waitcnt lgkmcnt(4)\n\t" \
    "v_mfma_f32_16x16x32_bf16 v[40:43], a[64:67], v[104:107], v[40:43]\n\t" "ds_read_b128 v[124:127], v75 offset:512\n\t" "s_mov_b32 m0, s63\n\t" \
    "v_mfma_f32_16x16x32_bf16 v[44:47], a[68:71], v[104:107], v[44:47]\n\t" "s_add_u32 s63, s63, 0x800\n\t" "buffer_load_dwordx4 v93, %[rx], s61 offen lds\n\t" \
    "s_waitcnt lgkmcnt(4)\n\t" "v_mfma_f32_16x16x32_bf16 v[32:35], a[72:75], v[108:111], v[32:35]\n\t" "ds_read_b128 v[128:131], v75 offset:33280\n\t" \
    "v_mfma_f32_16x16x32_bf16 v[36:39], a[76:79], v[108:111], v[36:39]\n\t" "buffer_load_dwordx4 v93, %[rx], s61 offen offset:1024 lds\n\t" "s_waitcnt lgkmcnt(4)\n\t" \
    "v_mfma_f32_16x16x32_bf16 v[40:43], a[72:75], v[112:115], v[40:43]\n\t" "ds_read_b128 v[100:103], v72 offset:768\n\t" "s_mov_b32 m0, s63\n\t" \
    "v_mfma_f32_16x16x32_bf16 v[44:47], a[76:79], v[112:115], v[44:47]\n\t" "s_add_u32 s63, s63, 0x800\n\t" "buffer_load_dwordx4 v94, %[rx], s61 offen lds\n\t" \
    "s_waitcnt lgkmcnt(4)\n\t" "v_mfma_f32_16x16x32_bf16 v[32:35], a[80:83], v[116:119], v[32:35]\n\t" "ds_read_b128 v[104:107], v72 offset:33536\n\t" \
    "v_mfma_f32_16x16x32_bf16 v[36:39], a[84:87], v[116:119], v[36:39]\n\t" "buffer_load_dwordx4 v94, %[rx], s61 offen offset:1024 lds\n\t" "s_waitcnt lgkmcnt(4)\n\t" \
    "v_mfma_f32_16x16x32_bf16 v[40:43], a[80:83], v[120:123], v[40:43]\n\t" "ds_read_b128 v[108:111], v73 offset:768\n\t" "s_mov_b32 m0, s63\n\t" \
    "v_mfma_f32_16x16x32_bf16 v[44:47], a[84:87], v[120:123], v[44:47]\n\t" "s_add_u32 s63, s63, 0x800\n\t" "buffer_load_dwordx4 v95, %[rx], s61 offen lds\n\t" \
    "s_waitcnt lgkmcnt(4)\n\t" "v_mfma_f32_16x16x32_bf16 v[32:35], a[88:91], v[124:127], v[32:35]\n\t" "ds_read_b128 v[112:115], v73 offset:33536\n\t" \
    "v_mfma_f32_16x16x32_bf16 v[36:39], a[92:95], v[124:127], v[36:39]\n\t" "buffer_load_dwordx4 v95, %[rx], s61 offen offset:1024 lds\n\t" "s_waitcnt lgkmcnt(4)\n\t" \
    "v_mfma_f32_16x16x32_bf16 v[40:43], a[88:91], v[128:131], v[40:43]\n\t" "ds_read_b128 v[116:119], v74 offset:768\n\t" \
    "v_mfma_f32_16x16x32_bf16 v[44:47], a[92:95], v[128:131], v[44:47]\n\t" "s_waitcnt lgkmcnt(4)\n\t" "v_mfma_f32_16x16x32_bf16 v[32:35], a[96:99], v[100:103], v[32:35]\n\t" \
    "ds_read_b128 v[120:123], v74 offset:33536\n\t" "s_lshr_b32 s64, s60, 2\n\t" "s_xor_b32 s64, s64, %[xp]\n\t" \
    "v_mfma_f32_16x16x32_bf16 v[36:39], a[100:103], v[100:103], v[36:39]\n\t" "s_and_b32 s64, s64, 1\n\t" "s_lshl_b32 s64, s64, 13\n\t" "s_waitcnt lgkmcnt(4)\n\t" \
    "v_mfma_f32_16x16x32_bf16 v[40:43], a[96:99], v[104:107], v[40:43]\n\t" "ds_read_b128 v[124:127], v75 offset:768\n\t" "s_lshl_b32 s62, s60, 19\n\t" \
    "v_mfma_f32_16x16x32_bf16 v[44:47], a[100:103], v[104:107], v[44:47]\n\t" "s_add_u32 s62, s62, s64\n\t" "s_add_u32 s62, s62, %[ob]\n\t" "s_waitcnt lgkmcnt(4)\n\t" \
    "v_mfma_f32_16x16x32_bf16 v[32:35], a[104:107], v[108:111], v[32:35]\n\t" "ds_read_b128 v[128:131], v75 offset:33536\n\t" \
    "v_mfma_f32_16x16x32_bf16 v[36:39], a[108:111], v[108:111], v[36:39]\n\t" "s_waitcnt lgkmcnt(4)\n\t" \
    "v_mfma_f32_16x16x32_bf16 v[40:43], a[104:107], v[112:115], v[40:43]\n\t" "ds_read_b128 v[100:103], v72 offset:1024\n\t" \
    "v_mfma_f32_16x16x32_bf16 v[44:47], a[108:111], v[112:115], v[44:47]\n\t" "s_waitcnt lgkmcnt(4)\n\t" \
    "v_mfma_f32_16x16x32_bf16 v[32:35], a[112:115], v[116:119], v[32:35]\n\t" "ds_read_b128 v[104:107], v72 offset:33792\n\t" \
    "v_mfma_f32_16x16x32_bf16 v[36:39], a[116:119], v[116:119], v[36:39]\n\t" "s_waitcnt lgkmcnt(4)\n\t" \
    "v_mfma_f32_16x16x32_bf16 v[40:43], a[112:115], v[120:123], v[40:43]\n\t" "ds_read_b128 v[108:111], v73 offset:1024\n\t" \
    "v_mfma_f32_16x16x32_bf16 v[44:47], a[116:119], v[120:123], v[44:47]\n\t" "s_waitcnt lgkmcnt(4)\n\t" \
    "v_mfma_f32_16x16x32_bf16 v[32:35], a[120:123], v[124:127], v[32:35]\n\t" "ds_read_b128 v[112:115], v73 offset:33792\n\t" \
    "v_mfma_f32_16x16x32_bf16 v[36:39], a[124:127], v[124:127], v[36:39]\n\t" "s_waitcnt lgkmcnt(4)\n\t" \
    "v_mfma_f32_16x16x32_bf16 v[40:43], a[120:123], v[128:131], v[40:43]\n\t" "ds_read_b128 v[116:119], v74 offset:1024\n\t" \
    "v_mfma_f32_16x16x32_bf16 v[44:47], a[124:127], v[128:131], v[44:47]\n\t" "s_waitcnt lgkmcnt(4)\n\t" \
    "v_mfma_f32_16x16x32_bf16 v[32:35], a[128:131], v[100:103], v[32:35]\n\t" "ds_read_b128 v[120:123], v74 offset:33792\n\t" \
    "v_mfma_f32_16x16x32_bf16 v[36:39], a[132:135], v[100:103], v[36:39]\n\t" "s_waitcnt lgkmcnt(4)\n\t" \
    "v_mfma_f32_16x16x32_bf16 v[40:43], a[128:131], v[104:107], v[40:43]\n\t" "ds_read_b128 v[124:127], v75 offset:1024\n\t" \
    "v_mfma_f32_16x16x32_bf16 v[44:47], a[132:135], v[104:107], v[44:47]\n\t" "s_waitcnt lgkmcnt(4)\n\t" \
    "v_mfma_f32_16x16x32_bf16 v[32:35], a[136:139], v[108:111], v[32:35]\n\t" "ds_read_b128 v[128:131], v75 offset:33792\n\t" "s_waitcnt vmcnt(16)\n\t" "s_barrier\n\t" \
    "v_mfma_f32_16x16x32_bf16 v[36:39], a[140:143], v[108:111], v[36:39]\n\t" "v_mov_b32 v98, s60\n\t" "buffer_store_dword v98, %[fo], %[rc], %[fl] offen sc1\n\t" \
    "s_waitcnt lgkmcnt(4)\n\t" "v_mfma_f32_16x16x32_bf16 v[40:43], a[136:139], v[112:115], v[40:43]\n\t" "ds_read_b128 v[100:103], v72 offset:1280\n\t" \
    "v_mfma_f32_16x16x32_bf16 v[44:47], a[140:143], v[112:115], v[44:47]\n\t" "s_waitcnt lgkmcnt(4)\n\t" \
    "v_mfma_f32_16x16x32_bf16 v[32:35], a[144:147], v[116:119], v[32:35]\n\t" "ds_read_b128 v[104:107], v72 offset:34048\n\t" \
    "v_mfma_f32_16x16x32_bf16 v[36:39], a[148:151], v[116:119], v[36:39]\n\t" "s_waitcnt lgkmcnt(4)\n\t" \
    "v_mfma_f32_16x16x32_bf16 v[40:43], a[144:147], v[120:123], v[40:43]\n\t" "ds_read_b128 v[108:111], v73 offset:1280\n\t" \
    "v_mfma_f32_16x16x32_bf16 v[44:47], a[148:151], v[120:123], v[44:47]\n\t" "s_waitcnt lgkmcnt(4)\n\t" \
    "v_mfma_f32_16x16x32_bf16 v[32:35], a[152:155], v[124:127], v[32:35]\n\t" "ds_read_b128 v[112:115], v73 offset:34048\n\t" \
    "v_mfma_f32_16x16x32_bf16 v[36:39], a[156:159], v[124:127], v[36:39]\n\t" "s_waitcnt lgkmcnt(4)\n\t" \
    "v_mfma_f32_16x16x32_bf16 v[40:43], a[152:155], v[128:131], v[40:43]\n\t" "ds_read_b128 v[116:119], v74 offset:1280\n\t" \
    "v_mfma_f32_16x16x32_bf16 v[44:47], a[156:159], v[128:131], v[44:47]\n\t" "s_waitcnt lgkmcnt(4)\n\t" \
    "v_mfma_f32_16x16x32_bf16 v[32:35], a[160:163], v[100:103], v[32:35]\n\t" "ds_read_b128 v[120:123], v74 offset:34048\n\t" \
    "v_mfma_f32_16x16x32_bf16 v[36:39], a[164:167], v[100:103], v[36:39]\n\t" "s_waitcnt lgkmcnt(4)\n\t" \
    "v_mfma_f32_16x16x32_bf16 v[40:43], a[160:163], v[104:107], v[40:43]\n\t" "ds_read_b128 v[124:127], v75 offset:1280\n\t" \
    "v_mfma_f32_16x16x32_bf16 v[44:47], a[164:167], v[104:107], v[44:47]\n\t" "s_waitcnt lgkmcnt(4)\n\t" \
    "v_mfma_f32_16x16x32_bf16 v[32:35], a[168:171], v[108:111], v[32:35]\n\t" "ds_read_b128 v[128:131], v75 offset:34048\n\t" \
    "v_mfma_f32_16x16x32_bf16 v[36:39], a[172:175], v[108:111], v[36:39]\n\t" "s_waitcnt lgkmcnt(4)\n\t" \
    "v_mfma_f32_16x16x32_bf16 v[40:43], a[168:171], v[112:115], v[40:43]\n\t" "ds_read_b128 v[100:103], v72 offset:1536\n\t" \
    "v_mfma_f32_16x16x32_bf16 v[44:47], a[172:175], v[112:115], v[44:47]\n\t" "s_waitcnt lgkmcnt(4)\n\t" \
    "v_mfma_f32_16x16x32_bf16 v[32:35], a[176:179], v[116:119], v[32:35]\n\t" "ds_read_b128 v[104:107], v72 offset:34304\n\t" \
    "v_mfma_f32_16x16x32_bf16 v[36:39], a[180:183], v[116:119], v[36:39]\n\t" "s_waitcnt lgkmcnt(4)\n\t" \
    "v_mfma_f32_16x16x32_bf16 v[40:43], a[176:179], v[120:123], v[40:43]\n\t" "ds_read_b128 v[108:111], v73 offset:1536\n\t" \
    "v_mfma_f32_16x16x32_bf16 v[44:47], a[180:183], v[120:123], v[44:47]\n\t" "s_waitcnt lgkmcnt(4)\n\t" \
    "v_mfma_f32_16x16x32_bf16 v[32:35], a[184:187], v[124:127], v[32:35]\n\t" "ds_read_b128 v[112:115], v73 offset:34304\n\t" \
    "v_mfma_f32_16x16x32_bf16 v[36:39], a[188:191], v[124:127], v[36:39]\n\t" "s_waitcnt lgkmcnt(4)\n\t" \
    "v_mfma_f32_16x16x32_bf16 v[40:43], a[184:187], v[128:131], v[40:43]\n\t" "ds_read_b128 v[116:119], v74 offset:1536\n\t" \
    "v_mfma_f32_16x16x32_bf16 v[44:47], a[188:191], v[128:131], v[44:47]\n\t" "s_waitcnt lgkmcnt(4)\n\t" \
    "v_mfma_f32_16x16x32_bf16 v[32:35], a[192:195], v[100:103], v[32:35]\n\t" "ds_read_b128 v[120:123], v74 offset:34304\n\t" \
    "v_mfma_f32_16x16x32_bf16 v[36:39], a[196:199], v[100:103], v[36:39]\n\t" "s_waitcnt lgkmcnt(4)\n\t" \
    "v_mfma_f32_16x16x32_bf16 v[40:43], a[192:195], v[104:107], v[40:43]\n\t" "ds_read_b128 v[124:127], v75 offset:1536\n\t" \
    "v_mfma_f32_16x16x32_bf16 v[44:47], a[196:199], v[104:107], v[44:47]\n\t" "s_waitcnt lgkmcnt(4)\n\t" \
    "v_mfma_f32_16x16x32_bf16 v[32:35], a[200:203], v[108:111], v[32:35]\n\t" "ds_read_b128 v[128:131], v75 offset:34304\n\t" \
    "v_mfma_f32_16x16x32_bf16 v[36:39], a[204:207], v[108:111], v[36:39]\n\t" "s_waitcnt lgkmcnt(4)\n\t" \
    "v_mfma_f32_16x16x32_bf16 v[40:43], a[200:203], v[112:115], v[40:43]\n\t" "ds_read_b128 v[100:103], v72 offset:1792\n\t" \
    "v_mfma_f32_16x16x32_bf16 v[44:47], a[204:207], v[112:115], v[44:47]\n\t" "s_waitcnt lgkmcnt(4)\n\t" \
    "v_mfma_f32_16x16x32_bf16 v[32:35], a[208:211], v[116:119], v[32:35]\n\t" "ds_read_b128 v[104:107], v72 offset:34560\n\t" \
    "v_mfma_f32_16x16x32_bf16 v[36:39], a[212:215], v[116:119], v[36:39]\n\t" "s_waitcnt lgkmcnt(4)\n\t" \
    "v_mfma_f32_16x16x32_bf16 v[40:43], a[208:211], v[120:123], v[40:43]\n\t" "ds_read_b128 v[108:111], v73 offset:1792\n\t" \
    "v_mfma_f32_16x16x32_bf16 v[44:47], a[212:215], v[120:123], v[44:47]\n\t" "s_waitcnt lgkmcnt(4)\n\t" \
    "v_mfma_f32_16x16x32_bf16 v[32:35], a[216:219], v[124:127], v[32:35]\n\t" "ds_read_b128 v[112:115], v73 offset:34560\n\t" \
    "v_mfma_f32_16x16x32_bf16 v[36:39], a[220:223], v[124:127], v[36:39]\n\t" "s_waitcnt lgkmcnt(4)\n\t" \
    "v_mfma_f32_16x16x32_bf16 v[40:43], a[216:219], v[128:131], v[40:43]\n\t" "ds_read_b128 v[116:119], v74 offset:1792\n\t" \
    "v_mfma_f32_16x16x32_bf16 v[44:47], a[220:223], v[128:131], v[44:47]\n\t" "s_waitcnt lgkmcnt(4)\n\t" \
    "v_mfma_f32_16x16x32_bf16 v[32:35], a[224:227], v[100:103], v[32:35]\n\t" "ds_read_b128 v[120:123], v74 offset:34560\n\t" \
    "v_mfma_f32_16x16x32_bf16 v[36:39], a[228:231], v[100:103], v[36:39]\n\t" "s_waitcnt lgkmcnt(4)\n\t" \
    "v_mfma_f32_16x16x32_bf16 v[40:43], a[224:227], v[104:107], v[40:43]\n\t" "ds_read_b128 v[124:127], v75 offset:1792\n\t" \
    "v_mfma_f32_16x16x32_bf16 v[44:47], a[228:231], v[104:107], v[44:47]\n\t" "s_waitcnt lgkmcnt(4)\n\t" \
    "v_mfma_f32_16x16x32_bf16 v[32:35], a[232:235], v[108:111], v[32:35]\n\t" "ds_read_b128 v[128:131], v75 offset:34560\n\t" \
    "v_mfma_f32_16x16x32_bf16 v[36:39], a[236:239], v[108:111], v[36:39]\n\t" "s_waitcnt lgkmcnt(4)\n\t" \
    "v_mfma_f32_16x16x32_bf16 v[40:43], a[232:235], v[112:115], v[40:43]\n\t" "v_mfma_f32_16x16x32_bf16 v[44:47], a[236:239], v[112:115], v[44:47]\n\t" \
    "s_waitcnt lgkmcnt(3)\n\t" "v_mfma_f32_16x16x32_bf16 v[32:35], a[240:243], v[116:119], v[32:35]\n\t" \
    "v_mfma_f32_16x16x32_bf16 v[36:39], a[244:247], v[116:119], v[36:39]\n\t" "s_waitcnt vmcnt(1) lgkmcnt(0)\n\t" "s_barrier\n\t" XT(82, 83) \
    "v_mfma_f32_16x16x32_bf16 v[40:43], a[240:243], v[120:123], v[40:43]\n\t" "ds_read_b128 v[100:103], v76 offset:0\n\t" \
    "v_mfma_f32_16x16x32_bf16 v[44:47], a[244:247], v[120:123], v[44:47]\n\t" "v_mfma_f32_16x16x32_bf16 v[32:35], a[248:251], v[124:127], v[32:35]\n\t" \
    "ds_read_b128 v[104:107], v76 offset:32768\n\t" "v_mfma_f32_16x16x32_bf16 v[36:39], a[252:255], v[124:127], v[36:39]\n\t" "ds_read_b128 v[108:111], v77 offset:0\n\t" \
    "v_mfma_f32_16x16x32_bf16 v[40:43], a[248:251], v[128:131], v[40:43]\n\t" "ds_read_b128 v[112:115], v77 offset:32768\n\t" "ds_read_b128 v[116:119], v78 offset:0\n\t" \
    "v_mfma_f32_16x16x32_bf16 v[44:47], a[252:255], v[128:131], v[44:47]\n\t" "s_add_u32 s60, s60, 1\n\t" GSUM "s_cmp_eq_u32 s60, 1\n\t" "s_cbranch_scc1 20f\n\t" "11:\n\t" \
    XT(80, 81) "s_waitcnt lgkmcnt(4)\n\t" "v_mfma_f32_16x16x32_bf16 v[48:51], a[0:3], v[100:103], 0\n\t" "ds_read_b128 v[120:123], v78 offset:32768\n\t" \
    "s_add_u32 s61, s60, 1\n\t" "s_and_b32 s61, s61, 63\n\t" "v_mfma_f32_16x16x32_bf16 v[52:55], a[4:7], v[100:103], 0\n\t" "s_lshl_b32 s61, s61, 16\n\t" \
    "s_add_u32 s63, %[lw], 0\n\t" "s_waitcnt lgkmcnt(4)\n\t" "v_mfma_f32_16x16x32_bf16 v[56:59], a[0:3], v[104:107], 0\n\t" "ds_read_b128 v[124:127], v79 offset:0\n\t" \
    "v_mfma_f32_16x16x32_bf16 v[60:63], a[4:7], v[104:107], 0\n\t" "s_waitcnt lgkmcnt(4)\n\t" "v_mfma_f32_16x16x32_bf16 v[48:51], a[8:11], v[108:111], v[48:51]\n\t" \
    "ds_read_b128 v[128:131], v79 offset:32768\n\t" "v_cvt_pk_bf16_f32 v64, v32, v33\n\t" "v_mfma_f32_16x16x32_bf16 v[52:55], a[12:15], v[108:111], v[52:55]\n\t" \
    "v_cvt_pk_bf16_f32 v65, v34, v35\n\t" "v_cvt_pk_bf16_f32 v66, v36, v37\n\t" "s_waitcnt lgkmcnt(4)\n\t" \
    "v_mfma_f32_16x16x32_bf16 v[56:59], a[8:11], v[112:115], v[56:59]\n\t" "ds_read_b128 v[100:103], v76 offset:256\n\t" "v_cvt_pk_bf16_f32 v67, v38, v39\n\t" \
    "v_mfma_f32_16x16x32_bf16 v[60:63], a[12:15], v[112:115], v[60:63]\n\t" "v_cvt_pk_bf16_f32 v68, v40, v41\n\t" "v_cvt_pk_bf16_f32 v69, v42, v43\n\t" \
    "s_waitcnt lgkmcnt(4)\n\t" "v_mfma_f32_16x16x32_bf16 v[48:51], a[16:19], v[116:119], v[48:51]\n\t" "ds_read_b128 v[104:107], v76 offset:33024\n\t" \
    "v_cvt_pk_bf16_f32 v70, v44, v45\n\t" "v_mfma_f32_16x16x32_bf16 v[52:55], a[20:23], v[116:119], v[52:55]\n\t" "v_cvt_pk_bf16_f32 v71, v46, v47\n\t" \
    "buffer_store_dwordx4 v[64:67], v96, %[rs], s62 offen offset:0 sc1\n\t" "s_waitcnt lgkmcnt(4)\n\t" "v_mfma_f32_16x16x32_bf16 v[56:59], a[16:19], v[120:123], v[56:59]\n\t" \
    "ds_read_b128 v[108:111], v77 offset:256\n\t" "v_mfma_f32_16x16x32_bf16 v[60:63], a[20:23], v[120:123], v[60:63]\n\t" "s_waitcnt lgkmcnt(4)\n\t" \
    "v_mfma_f32_16x16x32_bf16 v[48:51], a[24:27], v[124:127], v[48:51]\n\t" "ds_read_b128 v[112:115], v77 offset:33024\n\t" \
    "v_mfma_f32_16x16x32_bf16 v[52:55], a[28:31], v[124:127], v[52:55]\n\t" "buffer_store_dwordx4 v[68:71], v96, %[rs], s62 offen offset:256 sc1\n\t" \
    "s_waitcnt lgkmcnt(4)\n\t" "v_mfma_f32_16x16x32_bf16 v[56:59], a[24:27], v[128:131], v[56:59]\n\t" "ds_read_b128 v[116:119], v78 offset:256\n\t" "s_mov_b32 m0, s63\n\t" \
    "v_mfma_f32_16x16x32_bf16 v[60:63], a[28:31], v[128:131], v[60:63]\n\t" "s_add_u32 s63, s63, 0x800\n\t" "buffer_load_dwordx4 v88, %[rx], s61 offen lds\n\t" \
    "s_waitcnt lgkmcnt(4)\n\t" "v_mfma_f32_16x16x32_bf16 v[48:51], a[32:35], v[100:103], v[48:51]\n\t" "ds_read_b128 v[120:123], v78 offset:33024\n\t" \
    "v_mfma_f32_16x16x32_bf16 v[52:55], a[36:39], v[100:103], v[52:55]\n\t" "buffer_load_dwordx4 v88, %[rx], s61 offen offset:1024 lds\n\t" "s_waitcnt lgkmcnt(4)\n\t" \
    "v_mfma_f32_16x16x32_bf16 v[56:59], a[32:35], v[104:107], v[56:59]\n\t" "ds_read_b128 v[124:127], v79 offset:256\n\t" "s_mov_b32 m0, s63\n\t" \
    "v_mfma_f32_16x16x32_bf16 v[60:63], a[36:39], v[104:107], v[60:63]\n\t" "s_add_u32 s63, s63, 0x800\n\t" "buffer_load_dwordx4 v89, %[rx], s61 offen lds\n\t" \
    "s_waitcnt lgkmcnt(4)\n\t" "v_mfma_f32_16x16x32_bf16 v[48:51], a[40:43], v[108:111], v[48:51]\n\t" "ds_read_b128 v[128:131], v79 offset:33024\n\t" \
    "v_mfma_f32_16x16x32_bf16 v[52:55], a[44:47], v[108:111], v[52:55]\n\t" "buffer_load_dwordx4 v89, %[rx], s61 offen offset:1024 lds\n\t" "s_waitcnt lgkmcnt(4)\n\t" \
    "v_mfma_f32_16x16x32_bf16 v[56:59], a[40:43], v[112:115], v[56:59]\n\t" "ds_read_b128 v[100:103], v76 offset:512\n\t" "s_mov_b32 m0, s63\n\t" \
    "v_mfma_f32_16x16x32_bf16 v[60:63], a[44:47], v[112:115], v[60:63]\n\t" "s_add_u32 s63, s63, 0x800\n\t" "buffer_load_dwordx4 v90, %[rx], s61 offen lds\n\t" \
    "s_waitcnt lgkmcnt(4)\n\t" "v_mfma_f32_16x16x32_bf16 v[48:51], a[48:51], v[116:119], v[48:51]\n\t" "ds_read_b128 v[104:107], v76 offset:33280\n\t" \
    "v_mfma_f32_16x16x32_bf16 v[52:55], a[52:55], v[116:119], v[52:55]\n\t" "buffer_load_dwordx4 v90, %[rx], s61 offen offset:1024 lds\n\t" "s_waitcnt lgkmcnt(4)\n\t" \
    "v_mfma_f32_16x16x32_bf16 v[56:59], a[48:51], v[120:123], v[56:59]\n\t" "ds_read_b128 v[108:111], v77 offset:512\n\t" "s_mov_b32 m0, s63\n\t" \
    "v_mfma_f32_16x16x32_bf16 v[60:63], a[52:55], v[120:123], v[60:63]\n\t" "s_add_u32 s63, s63, 0x800\n\t" "buffer_load_dwordx4 v91, %[rx], s61 offen lds\n\t" \
    "s_waitcnt lgkmcnt(4)\n\t" "v_mfma_f32_16x16x32_bf16 v[48:51], a[56:59], v[124:127], v[48:51]\n\t" "ds_read_b128 v[112:115], v77 offset:33280\n\t" \
    "v_mfma_f32_16x16x32_bf16 v[52:55], a[60:63], v[124:127], v[52:55]\n\t" "buffer_load_dwordx4 v91, %[rx], s61 offen offset:1024 lds\n\t" "s_waitcnt lgkmcnt(4)\n\t" \
    "v_mfma_f32_16x16x32_bf16 v[56:59], a[56:59], v[128:131], v[56:59]\n\t" "ds_read_b128 v[116:119], v78 offset:512\n\t" "s_mov_b32 m0, s63\n\t" \
    "v_mfma_f32_16x16x32_bf16 v[60:63], a[60:63], v[128:131], v[60:63]\n\t" "s_add_u32 s63, s63, 0x800\n\t" "buffer_load_dwordx4 v92, %[rx], s61 offen lds\n\t" \
    "s_waitcnt lgkmcnt(4)\n\t" "v_mfma_f32_16x16x32_bf16 v[48:51], a[64:67], v[100:103], v[48:51]\n\t" "ds_read_b128 v[120:123], v78 offset:33280\n\t" \
    "v_mfma_f32_16x16x32_bf16 v[52:55], a[68:71], v[100:103], v[52:55]\n\t" "buffer_load_dwordx4 v92, %[rx], s61 offen offset:1024 lds\n\t" "s_waitcnt lgkmcnt(4)\n\t" \
    "v_mfma_f32_16x16x32_bf16 v[56:59], a[64:67], v[104:107], v[56:59]\n\t" "ds_read_b128 v[124:127], v79 offset:512\n\t" "s_mov_b32 m0, s63\n\t" \
    "v_mfma_f32_16x16x32_bf16 v[60:63], a[68:71], v[104:107], v[60:63]\n\t" "s_add_u32 s63, s63, 0x800\n\t" "buffer_load_dwordx4 v93, %[rx], s61 offen lds\n\t" \
    "s_waitcnt lgkmcnt(4)\n\t" "v_mfma_f32_16x16x32_bf16 v[48:51], a[72:75], v[108:111], v[48:51]\n\t" "ds_read_b128 v[128:131], v79 offset:33280\n\t" \
    "v_mfma_f32_16x16x32_bf16 v[52:55], a[76:79], v[108:111], v[52:55]\n\t" "buffer_load_dwordx4 v93, %[rx], s61 offen offset:1024 lds\n\t" "s_waitcnt lgkmcnt(4)\n\t" \
    "v_mfma_f32_16x16x32_bf16 v[56:59], a[72:75], v[112:115], v[56:59]\n\t" "ds_read_b128 v[100:103], v76 offset:768\n\t" "s_mov_b32 m0, s63\n\t" \
    "v_mfma_f32_16x16x32_bf16 v[60:63], a[76:79], v[112:115], v[60:63]\n\t" "s_add_u32 s63, s63, 0x800\n\t" "buffer_load_dwordx4 v94, %[rx], s61 offen lds\n\t" \
    "s_waitcnt lgkmcnt(4)\n\t" "v_mfma_f32_16x16x32_bf16 v[48:51], a[80:83], v[116:119], v[48:51]\n\t" "ds_read_b128 v[104:107], v76 offset:33536\n\t" \
    "v_mfma_f32_16x16x32_bf16 v[52:55], a[84:87], v[116:119], v[52:55]\n\t" "buffer_load_dwordx4 v94, %[rx], s61 offen offset:1024 lds\n\t" "s_waitcnt lgkmcnt(4)\n\t" \
    "v_mfma_f32_16x16x32_bf16 v[56:59], a[80:83], v[120:123], v[56:59]\n\t" "ds_read_b128 v[108:111], v77 offset:768\n\t" "s_mov_b32 m0, s63\n\t" \
    "v_mfma_f32_16x16x32_bf16 v[60:63], a[84:87], v[120:123], v[60:63]\n\t" "s_add_u32 s63, s63, 0x800\n\t" "buffer_load_dwordx4 v95, %[rx], s61 offen lds\n\t" \
    "s_waitcnt lgkmcnt(4)\n\t" "v_mfma_f32_16x16x32_bf16 v[48:51], a[88:91], v[124:127], v[48:51]\n\t" "ds_read_b128 v[112:115], v77 offset:33536\n\t" \
    "v_mfma_f32_16x16x32_bf16 v[52:55], a[92:95], v[124:127], v[52:55]\n\t" "buffer_load_dwordx4 v95, %[rx], s61 offen offset:1024 lds\n\t" "s_waitcnt lgkmcnt(4)\n\t" \
    "v_mfma_f32_16x16x32_bf16 v[56:59], a[88:91], v[128:131], v[56:59]\n\t" "ds_read_b128 v[116:119], v78 offset:768\n\t" \
    "v_mfma_f32_16x16x32_bf16 v[60:63], a[92:95], v[128:131], v[60:63]\n\t" "s_waitcnt lgkmcnt(4)\n\t" "v_mfma_f32_16x16x32_bf16 v[48:51], a[96:99], v[100:103], v[48:51]\n\t" \
    "ds_read_b128 v[120:123], v78 offset:33536\n\t" "s_lshr_b32 s64, s60, 2\n\t" "s_xor_b32 s64, s64, %[xp]\n\t" \
    "v_mfma_f32_16x16x32_bf16 v[52:55], a[100:103], v[100:103], v[52:55]\n\t" "s_and_b32 s64, s64, 1\n\t" "s_lshl_b32 s64, s64, 13\n\t" "s_waitcnt lgkmcnt(4)\n\t" \
    "v_mfma_f32_16x16x32_bf16 v[56:59], a[96:99], v[104:107], v[56:59]\n\t" "ds_read_b128 v[124:127], v79 offset:768\n\t" "s_lshl_b32 s62, s60, 19\n\t" \
    "v_mfma_f32_16x16x32_bf16 v[60:63], a[100:103], v[104:107], v[60:63]\n\t" "s_add_u32 s62, s62, s64\n\t" "s_add_u32 s62, s62, %[ob]\n\t" "s_waitcnt lgkmcnt(4)\n\t" \
    "v_mfma_f32_16x16x32_bf16 v[48:51], a[104:107], v[108:111], v[48:51]\n\t" "ds_read_b128 v[128:131], v79 offset:33536\n\t" \
    "v_mfma_f32_16x16x32_bf16 v[52:55], a[108:111], v[108:111], v[52:55]\n\t" "s_waitcnt lgkmcnt(4)\n\t" \
    "v_mfma_f32_16x16x32_bf16 v[56:59], a[104:107], v[112:115], v[56:59]\n\t" "ds_read_b128 v[100:103], v76 offset:1024\n\t" \
    "v_mfma_f32_16x16x32_bf16 v[60:63], a[108:111], v[112:115], v[60:63]\n\t" "s_waitcnt lgkmcnt(4)\n\t" \
    "v_mfma_f32_16x16x32_bf16 v[48:51], a[112:115], v[116:119], v[48:51]\n\t" "ds_read_b128 v[104:107], v76 offset:33792\n\t" \
    "v_mfma_f32_16x16x32_bf16 v[52:55], a[116:119], v[116:119], v[52:55]\n\t" "s_waitcnt lgkmcnt(4)\n\t" \
    "v_mfma_f32_16x16x32_bf16 v[56:59], a[112:115], v[120:123], v[56:59]\n\t" "ds_read_b128 v[108:111], v77 offset:1024\n\t" \
    "v_mfma_f32_16x16x32_bf16 v[60:63], a[116:119], v[120:123], v[60:63]\n\t" "s_waitcnt lgkmcnt(4)\n\t" \
    "v_mfma_f32_16x16x32_bf16 v[48:51], a[120:123], v[124:127], v[48:51]\n\t" "ds_read_b128 v[112:115], v77 offset:33792\n\t" \
    "v_mfma_f32_16x16x32_bf16 v[52:55], a[124:127], v[124:127], v[52:55]\n\t" "s_waitcnt lgkmcnt(4)\n\t" \
    "v_mfma_f32_16x16x32_bf16 v[56:59], a[120:123], v[128:131], v[56:59]\n\t" "ds_read_b128 v[116:119], v78 offset:1024\n\t" \
    "v_mfma_f32_16x16x32_bf16 v[60:63], a[124:127], v[128:131], v[60:63]\n\t" "s_waitcnt lgkmcnt(4)\n\t" \
    "v_mfma_f32_16x16x32_bf16 v[48:51], a[128:131], v[100:103], v[48:51]\n\t" "ds_read_b128 v[120:123], v78 offset:33792\n\t" \
    "v_mfma_f32_16x16x32_bf16 v[52:55], a[132:135], v[100:103], v[52:55]\n\t" "s_waitcnt lgkmcnt(4)\n\t" \
    "v_mfma_f32_16x16x32_bf16 v[56:59], a[128:131], v[104:107], v[56:59]\n\t" "ds_read_b128 v[124:127], v79 offset:1024\n\t" \
    "v_mfma_f32_16x16x32_bf16 v[60:63], a[132:135], v[104:107], v[60:63]\n\t" "s_waitcnt lgkmcnt(4)\n\t" \
    "v_mfma_f32_16x16x32_bf16 v[48:51], a[136:139], v[108:111], v[48:51]\n\t" "ds_read_b128 v[128:131], v79 offset:33792\n\t" "s_waitcnt vmcnt(16)\n\t" "s_barrier\n\t" \
    "v_mfma_f32_16x16x32_bf16 v[52:55], a[140:143], v[108:111], v[52:55]\n\t" "v_mov_b32 v98, s60\n\t" "buffer_store_dword v98, %[fo], %[rc], %[fl] offen sc1\n\t" \
    "s_waitcnt lgkmcnt(4)\n\t" "v_mfma_f32_16x16x32_bf16 v[56:59], a[136:139], v[112:115], v[56:59]\n\t" "ds_read_b128 v[100:103], v76 offset:1280\n\t" \
    "v_mfma_f32_16x16x32_bf16 v[60:63], a[140:143], v[112:115], v[60:63]\n\t" "s_waitcnt lgkmcnt(4)\n\t" \
    "v_mfma_f32_16x16x32_bf16 v[48:51], a[144:147], v[116:119], v[48:51]\n\t" "ds_read_b128 v[104:107], v76 offset:34048\n\t" \
    "v_mfma_f32_16x16x32_bf16 v[52:55], a[148:151], v[116:119], v[52:55]\n\t" "s_waitcnt lgkmcnt(4)\n\t" \
    "v_mfma_f32_16x16x32_bf16 v[56:59], a[144:147], v[120:123], v[56:59]\n\t" "ds_read_b128 v[108:111], v77 offset:1280\n\t" \
    "v_mfma_f32_16x16x32_bf16 v[60:63], a[148:151], v[120:123], v[60:63]\n\t" "s_waitcnt lgkmcnt(4)\n\t" \
    "v_mfma_f32_16x16x32_bf16 v[48:51], a[152:155], v[124:127], v[48:51]\n\t" "ds_read_b128 v[112:115], v77 offset:34048\n\t" \
    "v_mfma_f32_16x16x32_bf16 v[52:55], a[156:159], v[124:127], v[52:55]\n\t" "s_waitcnt lgkmcnt(4)\n\t" \
    "v_mfma_f32_16x16x32_bf16 v[56:59], a[152:155], v[128:131], v[56:59]\n\t" "ds_read_b128 v[116:119], v78 offset:1280\n\t" \
    "v_mfma_f32_16x16x32_bf16 v[60:63], a[156:159], v[128:131], v[60:63]\n\t" "s_waitcnt lgkmcnt(4)\n\t" \
    "v_mfma_f32_16x16x32_bf16 v[48:51], a[160:163], v[100:103], v[48:51]\n\t" "ds_read_b128 v[120:123], v78 offset:34048\n\t" \
    "v_mfma_f32_16x16x32_bf16 v[52:55], a[164:167], v[100:103], v[52:55]\n\t" "s_waitcnt lgkmcnt(4)\n\t" \
    "v_mfma_f32_16x16x32_bf16 v[56:59], a[160:163], v[104:107], v[56:59]\n\t" "ds_read_b128 v[124:127], v79 offset:1280\n\t" \
    "v_mfma_f32_16x16x32_bf16 v[60:63], a[164:167], v[104:107], v[60:63]\n\t" "s_waitcnt lgkmcnt(4)\n\t" \
    "v_mfma_f32_16x16x32_bf16 v[48:51], a[168:171], v[108:111], v[48:51]\n\t" "ds_read_b128 v[128:131], v79 offset:34048\n\t" \
    "v_mfma_f32_16x16x32_bf16 v[52:55], a[172:175], v[108:111], v[52:55]\n\t" "s_waitcnt lgkmcnt(4)\n\t" \
    "v_mfma_f32_16x16x32_bf16 v[56:59], a[168:171], v[112:115], v[56:59]\n\t" "ds_read_b128 v[100:103], v76 offset:1536\n\t" \
    "v_mfma_f32_16x16x32_bf16 v[60:63], a[172:175], v[112:115], v[60:63]\n\t" "s_waitcnt lgkmcnt(4)\n\t" \
    "v_mfma_f32_16x16x32_bf16 v[48:51], a[176:179], v[116:119], v[48:51]\n\t" "ds_read_b128 v[104:107], v76 offset:34304\n\t" \
    "v_mfma_f32_16x16x32_bf16 v[52:55], a[180:183], v[116:119], v[52:55]\n\t" "s_waitcnt lgkmcnt(4)\n\t" \
    "v_mfma_f32_16x16x32_bf16 v[56:59], a[176:179], v[120:123], v[56:59]\n\t" "ds_read_b128 v[108:111], v77 offset:1536\n\t" \
    "v_mfma_f32_16x16x32_bf16 v[60:63], a[180:183], v[120:123], v[60:63]\n\t" "s_waitcnt lgkmcnt(4)\n\t" \
    "v_mfma_f32_16x16x32_bf16 v[48:51], a[184:187], v[124:127], v[48:51]\n\t" "ds_read_b128 v[112:115], v77 offset:34304\n\t" \
    "v_mfma_f32_16x16x32_bf16 v[52:55], a[188:191], v[124:127], v[52:55]\n\t" "s_waitcnt lgkmcnt(4)\n\t" \
    "v_mfma_f32_16x16x32_bf16 v[56:59], a[184:187], v[128:131], v[56:59]\n\t" "ds_read_b128 v[116:119], v78 offset:1536\n\t" \
    "v_mfma_f32_16x16x32_bf16 v[60:63], a[188:191], v[128:131], v[60:63]\n\t" "s_waitcnt lgkmcnt(4)\n\t" \
    "v_mfma_f32_16x16x32_bf16 v[48:51], a[192:195], v[100:103], v[48:51]\n\t" "ds_read_b128 v[120:123], v78 offset:34304\n\t" \
    "v_mfma_f32_16x16x32_bf16 v[52:55], a[196:199], v[100:103], v[52:55]\n\t" "s_waitcnt lgkmcnt(4)\n\t" \
    "v_mfma_f32_16x16x32_bf16 v[56:59], a[192:195], v[104:107], v[56:59]\n\t" "ds_read_b128 v[124:127], v79 offset:1536\n\t" \
    "v_mfma_f32_16x16x32_bf16 v[60:63], a[196:199], v[104:107], v[60:63]\n\t" "s_waitcnt lgkmcnt(4)\n\t" \
    "v_mfma_f32_16x16x32_bf16 v[48:51], a[200:203], v[108:111], v[48:51]\n\t" "ds_read_b128 v[128:131], v79 offset:34304\n\t" \
    "v_mfma_f32_16x16x32_bf16 v[52:55], a[204:207], v[108:111], v[52:55]\n\t" "s_waitcnt lgkmcnt(4)\n\t" \
    "v_mfma_f32_16x16x32_bf16 v[56:59], a[200:203], v[112:115], v[56:59]\n\t" "ds_read_b128 v[100:103], v76 offset:1792\n\t" \
    "v_mfma_f32_16x16x32_bf16 v[60:63], a[204:207], v[112:115], v[60:63]\n\t" "s_waitcnt lgkmcnt(4)\n\t" \
    "v_mfma_f32_16x16x32_bf16 v[48:51], a[208:211], v[116:119], v[48:51]\n\t" "ds_read_b128 v[104:107], v76 offset:34560\n\t" \
    "v_mfma_f32_16x16x32_bf16 v[52:55], a[212:215], v[116:119], v[52:55]\n\t" "s_waitcnt lgkmcnt(4)\n\t" \
    "v_mfma_f32_16x16x32_bf16 v[56:59], a[208:211], v[120:123], v[56:59]\n\t" "ds_read_b128 v[108:111], v77 offset:1792\n\t" \
    "v_mfma_f32_16x16x32_bf16 v[60:63], a[212:215], v[120:123], v[60:63]\n\t" "s_waitcnt lgkmcnt(4)\n\t" \
    "v_mfma_f32_16x16x32_bf16 v[48:51], a[216:219], v[124:127], v[48:51]\n\t" "ds_read_b128 v[112:115], v77 offset:34560\n\t" \
    "v_mfma_f32_16x16x32_bf16 v[52:55], a[220:223], v[124:127], v[52:55]\n\t" "s_waitcnt lgkmcnt(4)\n\t" \
    "v_mfma_f32_16x16x32_bf16 v[56:59], a[216:219], v[128:131], v[56:59]\n\t" "ds_read_b128 v[116:119], v78 offset:1792\n\t" \
    "v_mfma_f32_16x16x32_bf16 v[60:63], a[220:223], v[128:131], v[60:63]\n\t" "s_waitcnt lgkmcnt(4)\n\t" \
    "v_mfma_f32_16x16x32_bf16 v[48:51], a[224:227], v[100:103], v[48:51]\n\t" "ds_read_b128 v[120:123], v78 offset:34560\n\t" \
    "v_mfma_f32_16x16x32_bf16 v[52:55], a[228:231], v[100:103], v[52:55]\n\t" "s_waitcnt lgkmcnt(4)\n\t" \
    "v_mfma_f32_16x16x32_bf16 v[56:59], a[224:227], v[104:107], v[56:59]\n\t" "ds_read_b128 v[124:127], v79 offset:1792\n\t" \
    "v_mfma_f32_16x16x32_bf16 v[60:63], a[228:231], v[104:107], v[60:63]\n\t" "s_waitcnt lgkmcnt(4)\n\t" \
    "v_mfma_f32_16x16x32_bf16 v[48:51], a[232:235], v[108:111], v[48:51]\n\t" "ds_read_b128 v[128:131], v79 offset:34560\n\t" \
    "v_mfma_f32_16x16x32_bf16 v[52:55], a[236:239], v[108:111], v[52:55]\n\t" "s_waitcnt lgkmcnt(4)\n\t" \
    "v_mfma_f32_16x16x32_bf16 v[56:59], a[232:235], v[112:115], v[56:59]\n\t" "v_mfma_f32_16x16x32_bf16 v[60:63], a[236:239], v[112:115], v[60:63]\n\t" \
    "s_waitcnt lgkmcnt(3)\n\t" "v_mfma_f32_16x16x32_bf16 v[48:51], a[240:243], v[116:119], v[48:51]\n\t" \
    "v_mfma_f32_16x16x32_bf16 v[52:55], a[244:247], v[116:119], v[52:55]\n\t" "s_waitcnt vmcnt(1) lgkmcnt(0)\n\t" "s_barrier\n\t" XT(82, 83) \
    "v_mfma_f32_16x16x32_bf16 v[56:59], a[240:243], v[120:123], v[56:59]\n\t" "ds_read_b128 v[100:103], v72 offset:0\n\t" \
    "v_mfma_f32_16x16x32_bf16 v[60:63], a[244:247], v[120:123], v[60:63]\n\t" "v_mfma_f32_16x16x32_bf16 v[48:51], a[248:251], v[124:127], v[48:51]\n\t" \
    "ds_read_b128 v[104:107], v72 offset:32768\n\t" "v_mfma_f32_16x16x32_bf16 v[52:55], a[252:255], v[124:127], v[52:55]\n\t" "ds_read_b128 v[108:111], v73 offset:0\n\t" \
    "v_mfma_f32_16x16x32_bf16 v[56:59], a[248:251], v[128:131], v[56:59]\n\t" "ds_read_b128 v[112:115], v73 offset:32768\n\t" "ds_read_b128 v[116:119], v74 offset:0\n\t" \
    "v_mfma_f32_16x16x32_bf16 v[60:63], a[252:255], v[128:131], v[60:63]\n\t" "s_add_u32 s60, s60, 1\n\t" GSUM "s_cmp_lt_u32 s60, 64\n\t" "s_cbranch_scc1 10b\n\t" \
    "s_nop 7\n\t" "v_cvt_pk_bf16_f32 v64, v48, v49\n\t" "v_cvt_pk_bf16_f32 v65, v50, v51\n\t" "v_cvt_pk_bf16_f32 v66, v52, v53\n\t" "v_cvt_pk_bf16_f32 v67, v54, v55\n\t" \
    "v_cvt_pk_bf16_f32 v68, v56, v57\n\t" "v_cvt_pk_bf16_f32 v69, v58, v59\n\t" "v_cvt_pk_bf16_f32 v70, v60, v61\n\t" "v_cvt_pk_bf16_f32 v71, v62, v63\n\t" \
    "buffer_store_dwordx4 v[64:67], v96, %[rs], s62 offen offset:0 sc1\n\t" "buffer_store_dwordx4 v[68:71], v96, %[rs], s62 offen offset:256 sc1\n\t" "s_waitcnt vmcnt(0)\n\t" \
    "s_barrier\n\t" "v_mov_b32 v98, s60\n\t" "buffer_store_dword v98, %[fo], %[rc], %[fl] offen sc1\n\t" "s_waitcnt lgkmcnt(0)\n\t" "s_branch 30f\n\t" "20:\n\t" \
    "v_mov_b32 v96, %[so]\n\t" "s_nop 7\n\t" "v_cvt_pk_bf16_f32 v64, v32, v33\n\t" "v_cvt_pk_bf16_f32 v65, v34, v35\n\t" "v_cvt_pk_bf16_f32 v66, v36, v37\n\t" \
    "v_cvt_pk_bf16_f32 v67, v38, v39\n\t" "v_cvt_pk_bf16_f32 v68, v40, v41\n\t" "v_cvt_pk_bf16_f32 v69, v42, v43\n\t" "v_cvt_pk_bf16_f32 v70, v44, v45\n\t" \
    "v_cvt_pk_bf16_f32 v71, v46, v47\n\t" "buffer_store_dwordx4 v[64:67], v96, %[rs], s62 offen offset:0 sc1\n\t" \
    "buffer_store_dwordx4 v[68:71], v96, %[rs], s62 offen offset:256 sc1\n\t" "s_waitcnt vmcnt(0)\n\t" "s_barrier\n\t" "v_mov_b32 v98, s60\n\t" \
    "buffer_store_dword v98, %[fo], %[rc], %[fl] offen sc1\n\t" "s_branch 11b\n\t" "30:\n\t"
#define ZC_LOOP \
    "s_mov_b32 s60, 0\n\t" "s_mov_b32 s61, 0\n\t" "s_mov_b32 s62, 0\n\t" "s_mov_b32 s63, 0x7f000000\n\t" "s_cmp_ge_u32 s61, %[z0]\n\t" "s_cbranch_scc1 52f\n\t" "51:\n\t" \
    "buffer_load_dword v120, %[fo], %[rc], %[fv] offen sc1\n\t" "buffer_load_dword v121, %[fo], %[rc], %[vd] offen sc1\n\t" "s_waitcnt vmcnt(0)\n\t" \
    "v_readfirstlane_b32 s61, v120\n\t" "v_readfirstlane_b32 s66, v121\n\t" "s_nop 3\n\t" "s_cmp_ge_u32 s61, %[z0]\n\t" "s_cbranch_scc1 52f\n\t" "s_bitcmp1_b32 s66, 1\n\t" \
    "s_cbranch_scc1 90f\n\t" "s_cmp_eq_u32 %[bd], 0\n\t" "s_cbranch_scc1 52f\n\t" "s_sub_u32 %[bd], %[bd], 1\n\t" "s_sleep 2\n\t" "s_branch 51b\n\t" "52:\n\t" \
    "s_lshr_b32 s66, s60, 2\n\t" "s_xor_b32 s66, s66, %[xp]\n\t" "s_and_b32 s66, s66, 1\n\t" "s_lshl_b32 s66, s66, 13\n\t" "s_lshl_b32 s64, s60, 19\n\t" \
    "s_add_u32 s64, s64, s66\n\t" "s_add_u32 s64, s64, %[vb]\n\t" "buffer_load_dwordx4 v[16:19], %[ao], %[rs], s64 offen offset:0 sc1\n\t" \
    "buffer_load_dwordx4 v[20:23], %[ao], %[rs], s64 offen offset:256 sc1\n\t" "buffer_load_dwordx4 v[24:27], %[ao], %[rs], s64 offen offset:2048 sc1\n\t" \
    "buffer_load_dwordx4 v[28:31], %[ao], %[rs], s64 offen offset:2304 sc1\n\t" "buffer_load_dwordx4 v[32:35], %[a1], %[rs], s64 offen offset:0 sc1\n\t" \
    "buffer_load_dwordx4 v[36:39], %[a1], %[rs], s64 offen offset:256 sc1\n\t" "buffer_load_dwordx4 v[40:43], %[a1], %[rs], s64 offen offset:2048 sc1\n\t" \
    "buffer_load_dwordx4 v[44:47], %[a1], %[rs], s64 offen offset:2304 sc1\n\t" "s_waitcnt vmcnt(0)\n\t" "10:\n\t" XT(80, 81) "s_add_u32 s66, s62, %[rg]\n\t" \
    "s_cmp_lt_u32 s66, s60\n\t" "s_cbranch_scc1 60f\n\t" "63:\n\t" "v_mfma_f32_16x16x32_bf16 v[80:83], v[16:19], a[0:3], 0\n\t" \
    "v_mfma_f32_16x16x32_bf16 v[84:87], v[20:23], a[0:3], 0\n\t" "v_mfma_f32_16x16x32_bf16 v[80:83], v[24:27], a[4:7], v[80:83]\n\t" \
    "v_mfma_f32_16x16x32_bf16 v[84:87], v[28:31], a[4:7], v[84:87]\n\t" "v_mfma_f32_16x16x32_bf16 v[80:83], v[32:35], a[8:11], v[80:83]\n\t" \
    "v_cvt_pk_bf16_f32 v112, v96, v97\n\t" "v_mfma_f32_16x16x32_bf16 v[84:87], v[36:39], a[8:11], v[84:87]\n\t" "v_cvt_pk_bf16_f32 v113, v98, v99\n\t" \
    "v_mfma_f32_16x16x32_bf16 v[80:83], v[40:43], a[12:15], v[80:83]\n\t" "v_cvt_pk_bf16_f32 v114, v100, v101\n\t" \
    "v_mfma_f32_16x16x32_bf16 v[84:87], v[44:47], a[12:15], v[84:87]\n\t" "v_cvt_pk_bf16_f32 v115, v102, v103\n\t" \
    "v_mfma_f32_16x16x32_bf16 v[88:91], v[16:19], a[16:19], 0\n\t" "v_cvt_pk_bf16_f32 v116, v104, v105\n\t" "v_mfma_f32_16x16x32_bf16 v[92:95], v[20:23], a[16:19], 0\n\t" \
    "v_cvt_pk_bf16_f32 v117, v106, v107\n\t" "v_mfma_f32_16x16x32_bf16 v[88:91], v[24:27], a[20:23], v[88:91]\n\t" \
    "buffer_store_dwordx4 v[112:115], %[z1], %[rs], s63 offen sc1\n\t" "v_mfma_f32_16x16x32_bf16 v[92:95], v[28:31], a[20:23], v[92:95]\n\t" \
    "v_mfma_f32_16x16x32_bf16 v[88:91], v[32:35], a[24:27], v[88:91]\n\t" "v_cvt_pk_bf16_f32 v118, v108, v109\n\t" \
    "v_mfma_f32_16x16x32_bf16 v[92:95], v[36:39], a[24:27], v[92:95]\n\t" "v_cvt_pk_bf16_f32 v119, v110, v111\n\t" \
    "v_mfma_f32_16x16x32_bf16 v[88:91], v[40:43], a[28:31], v[88:91]\n\t" "buffer_store_dwordx4 v[116:119], %[z2], %[rs], s63 offen sc1\n\t" "s_and_b32 s63, s60, %[rm]\n\t" \
    "v_mfma_f32_16x16x32_bf16 v[92:95], v[44:47], a[28:31], v[92:95]\n\t" "s_lshl_b32 s63, s63, 22\n\t" "s_add_u32 s63, s63, %[zb]\n\t" \
    "v_mfma_f32_16x16x32_bf16 v[96:99], v[16:19], a[32:35], 0\n\t" "buffer_load_dword v120, %[fo], %[rc], %[fv] offen sc1\n\t" \
    "v_mfma_f32_16x16x32_bf16 v[100:103], v[20:23], a[32:35], 0\n\t" "v_mfma_f32_16x16x32_bf16 v[96:99], v[24:27], a[36:39], v[96:99]\n\t" \
    "buffer_load_dword v121, %[fo], %[rc], %[fs] offen sc1\n\t" "v_mfma_f32_16x16x32_bf16 v[100:103], v[28:31], a[36:39], v[100:103]\n\t" \
    "v_mfma_f32_16x16x32_bf16 v[96:99], v[32:35], a[40:43], v[96:99]\n\t" "v_cvt_pk_bf16_f32 v112, v80, v81\n\t" \
    "v_mfma_f32_16x16x32_bf16 v[100:103], v[36:39], a[40:43], v[100:103]\n\t" "v_cvt_pk_bf16_f32 v113, v82, v83\n\t" \
    "v_mfma_f32_16x16x32_bf16 v[96:99], v[40:43], a[44:47], v[96:99]\n\t" "v_cvt_pk_bf16_f32 v114, v84, v85\n\t" \
    "v_mfma_f32_16x16x32_bf16 v[100:103], v[44:47], a[44:47], v[100:103]\n\t" "v_cvt_pk_bf16_f32 v115, v86, v87\n\t" \
    "v_mfma_f32_16x16x32_bf16 v[104:107], v[16:19], a[48:51], 0\n\t" "v_cvt_pk_bf16_f32 v116, v88, v89\n\t" "v_mfma_f32_16x16x32_bf16 v[108:111], v[20:23], a[48:51], 0\n\t" \
    "v_cvt_pk_bf16_f32 v117, v90, v91\n\t" "v_mfma_f32_16x16x32_bf16 v[104:107], v[24:27], a[52:55], v[104:107]\n\t" \
    "buffer_store_dwordx4 v[112:115], %[z1], %[rs], s63 offen sc1\n\t" "v_mfma_f32_16x16x32_bf16 v[108:111], v[28:31], a[52:55], v[108:111]\n\t" \
    "v_mfma_f32_16x16x32_bf16 v[104:107], v[32:35], a[56:59], v[104:107]\n\t" "v_cvt_pk_bf16_f32 v118, v92, v93\n\t" \
    "v_mfma_f32_16x16x32_bf16 v[108:111], v[36:39], a[56:59], v[108:111]\n\t" "v_cvt_pk_bf16_f32 v119, v94, v95\n\t" \
    "v_mfma_f32_16x16x32_bf16 v[104:107], v[40:43], a[60:63], v[104:107]\n\t" "buffer_store_dwordx4 v[116:119], %[z2], %[rs], s63 offen sc1\n\t" \
    "v_mfma_f32_16x16x32_bf16 v[108:111], v[44:47], a[60:63], v[108:111]\n\t" "s_add_u32 s63, s63, 0x20000\n\t" "v_mfma_f32_16x16x32_bf16 v[80:83], v[16:19], a[64:67], 0\n\t" \
    "s_add_u32 s65, s60, 1\n\t" "s_lshr_b32 s66, s65, 2\n\t" "v_mfma_f32_16x16x32_bf16 v[84:87], v[20:23], a[64:67], 0\n\t" "s_xor_b32 s66, s66, %[xp]\n\t" \
    "s_and_b32 s66, s66, 1\n\t" "v_mfma_f32_16x16x32_bf16 v[80:83], v[24:27], a[68:71], v[80:83]\n\t" "s_lshl_b32 s66, s66, 13\n\t" "s_lshl_b32 s64, s65, 19\n\t" \
    "v_mfma_f32_16x16x32_bf16 v[84:87], v[28:31], a[68:71], v[84:87]\n\t" "s_add_u32 s64, s64, s66\n\t" "s_add_u32 s64, s64, %[vb]\n\t" \
    "v_mfma_f32_16x16x32_bf16 v[80:83], v[32:35], a[72:75], v[80:83]\n\t" "v_cvt_pk_bf16_f32 v112, v96, v97\n\t" \
    "v_mfma_f32_16x16x32_bf16 v[84:87], v[36:39], a[72:75], v[84:87]\n\t" "v_cvt_pk_bf16_f32 v113, v98, v99\n\t" \
    "v_mfma_f32_16x16x32_bf16 v[80:83], v[40:43], a[76:79], v[80:83]\n\t" "v_cvt_pk_bf16_f32 v114, v100, v101\n\t" \
    "v_mfma_f32_16x16x32_bf16 v[84:87], v[44:47], a[76:79], v[84:87]\n\t" "v_cvt_pk_bf16_f32 v115, v102, v103\n\t" \
    "v_mfma_f32_16x16x32_bf16 v[88:91], v[16:19], a[80:83], 0\n\t" "v_cvt_pk_bf16_f32 v116, v104, v105\n\t" "v_mfma_f32_16x16x32_bf16 v[92:95], v[20:23], a[80:83], 0\n\t" \
    "v_cvt_pk_bf16_f32 v117, v106, v107\n\t" "v_mfma_f32_16x16x32_bf16 v[88:91], v[24:27], a[84:87], v[88:91]\n\t" \
    "buffer_store_dwordx4 v[112:115], %[z1], %[rs], s63 offen sc1\n\t" "v_mfma_f32_16x16x32_bf16 v[92:95], v[28:31], a[84:87], v[92:95]\n\t" \
    "v_mfma_f32_16x16x32_bf16 v[88:91], v[32:35], a[88:91], v[88:91]\n\t" "v_cvt_pk_bf16_f32 v118, v108, v109\n\t" \
    "v_mfma_f32_16x16x32_bf16 v[92:95], v[36:39], a[88:91], v[92:95]\n\t" "v_cvt_pk_bf16_f32 v119, v110, v111\n\t" \
    "v_mfma_f32_16x16x32_bf16 v[88:91], v[40:43], a[92:95], v[88:91]\n\t" "buffer_store_dwordx4 v[116:119], %[z2], %[rs], s63 offen sc1\n\t" \
    "v_mfma_f32_16x16x32_bf16 v[92:95], v[44:47], a[92:95], v[92:95]\n\t" "s_add_u32 s63, s63, 0x20000\n\t" "v_mfma_f32_16x16x32_bf16 v[96:99], v[16:19], a[96:99], 0\n\t" \
    "v_mfma_f32_16x16x32_bf16 v[100:103], v[20:23], a[96:99], 0\n\t" "v_mfma_f32_16x16x32_bf16 v[96:99], v[24:27], a[100:103], v[96:99]\n\t" \
    "v_mfma_f32_16x16x32_bf16 v[100:103], v[28:31], a[100:103], v[100:103]\n\t" "v_mfma_f32_16x16x32_bf16 v[96:99], v[32:35], a[104:107], v[96:99]\n\t" \
    "v_cvt_pk_bf16_f32 v112, v80, v81\n\t" "v_mfma_f32_16x16x32_bf16 v[100:103], v[36:39], a[104:107], v[100:103]\n\t" "v_cvt_pk_bf16_f32 v113, v82, v83\n\t" \
    "v_mfma_f32_16x16x32_bf16 v[96:99], v[40:43], a[108:111], v[96:99]\n\t" "v_cvt_pk_bf16_f32 v114, v84, v85\n\t" \
    "v_mfma_f32_16x16x32_bf16 v[100:103], v[44:47], a[108:111], v[100:103]\n\t" "v_cvt_pk_bf16_f32 v115, v86, v87\n\t" \
    "v_mfma_f32_16x16x32_bf16 v[104:107], v[16:19], a[112:115], 0\n\t" "v_cvt_pk_bf16_f32 v116, v88, v89\n\t" \
    "v_mfma_f32_16x16x32_bf16 v[108:111], v[20:23], a[112:115], 0\n\t" "v_cvt_pk_bf16_f32 v117, v90, v91\n\t" \
    "v_mfma_f32_16x16x32_bf16 v[104:107], v[24:27], a[116:119], v[104:107]\n\t" "buffer_store_dwordx4 v[112:115], %[z1], %[rs], s63 offen sc1\n\t" \
    "v_mfma_f32_16x16x32_bf16 v[108:111], v[28:31], a[116:119], v[108:111]\n\t" "v_mfma_f32_16x16x32_bf16 v[104:107], v[32:35], a[120:123], v[104:107]\n\t" \
    "v_cvt_pk_bf16_f32 v118, v92, v93\n\t" "v_mfma_f32_16x16x32_bf16 v[108:111], v[36:39], a[120:123], v[108:111]\n\t" "v_cvt_pk_bf16_f32 v119, v94, v95\n\t" \
    "v_mfma_f32_16x16x32_bf16 v[104:107], v[40:43], a[124:127], v[104:107]\n\t" "buffer_store_dwordx4 v[116:119], %[z2], %[rs], s63 offen sc1\n\t" \
    "v_mfma_f32_16x16x32_bf16 v[108:111], v[44:47], a[124:127], v[108:111]\n\t" "s_add_u32 s63, s63, 0x20000\n\t" \
    "v_mfma_f32_16x16x32_bf16 v[80:83], v[16:19], a[128:131], 0\n\t" "s_waitcnt vmcnt(6)\n\t" "v_readfirstlane_b32 s66, v120\n\t" "v_readfirstlane_b32 s67, v121\n\t" \
    "v_mfma_f32_16x16x32_bf16 v[84:87], v[20:23], a[128:131], 0\n\t" "v_mov_b32 v122, s60\n\t" "buffer_store_dword v122, %[fo], %[rc], %[fz] offen sc1\n\t" \
    "v_mfma_f32_16x16x32_bf16 v[80:83], v[24:27], a[132:135], v[80:83]\n\t" "s_max_u32 s61, s61, s66\n\t" "s_max_u32 s62, s62, s67\n\t" "s_cmp_gt_u32 s61, s65\n\t" \
    "v_mfma_f32_16x16x32_bf16 v[84:87], v[28:31], a[132:135], v[84:87]\n\t" "s_cselect_b32 s65, 1, 0\n\t" "s_cselect_b32 s64, s64, 0x7f000000\n\t" \
    "v_mfma_f32_16x16x32_bf16 v[80:83], v[32:35], a[136:139], v[80:83]\n\t" "buffer_load_dwordx4 v[48:51], %[ao], %[rs], s64 offen offset:0 sc1\n\t" \
    "v_cvt_pk_bf16_f32 v112, v96, v97\n\t" "v_mfma_f32_16x16x32_bf16 v[84:87], v[36:39], a[136:139], v[84:87]\n\t" "v_cvt_pk_bf16_f32 v113, v98, v99\n\t" \
    "v_mfma_f32_16x16x32_bf16 v[80:83], v[40:43], a[140:143], v[80:83]\n\t" "buffer_load_dwordx4 v[52:55], %[ao], %[rs], s64 offen offset:256 sc1\n\t" \
    "v_cvt_pk_bf16_f32 v114, v100, v101\n\t" "v_mfma_f32_16x16x32_bf16 v[84:87], v[44:47], a[140:143], v[84:87]\n\t" "v_cvt_pk_bf16_f32 v115, v102, v103\n\t" \
    "v_mfma_f32_16x16x32_bf16 v[88:91], v[16:19], a[144:147], 0\n\t" "buffer_load_dwordx4 v[56:59], %[ao], %[rs], s64 offen offset:2048 sc1\n\t" \
    "v_cvt_pk_bf16_f32 v116, v104, v105\n\t" "v_mfma_f32_16x16x32_bf16 v[92:95], v[20:23], a[144:147], 0\n\t" "v_cvt_pk_bf16_f32 v117, v106, v107\n\t" \
    "v_mfma_f32_16x16x32_bf16 v[88:91], v[24:27], a[148:151], v[88:91]\n\t" "buffer_load_dwordx4 v[60:63], %[ao], %[rs], s64 offen offset:2304 sc1\n\t" \
    "v_mfma_f32_16x16x32_bf16 v[92:95], v[28:31], a[148:151], v[92:95]\n\t" "buffer_store_dwordx4 v[112:115], %[z1], %[rs], s63 offen sc1\n\t" \
    "v_mfma_f32_16x16x32_bf16 v[88:91], v[32:35], a[152:155], v[88:91]\n\t" "buffer_load_dwordx4 v[64:67], %[a1], %[rs], s64 offen offset:0 sc1\n\t" \
    "v_cvt_pk_bf16_f32 v118, v108, v109\n\t" "v_mfma_f32_16x16x32_bf16 v[92:95], v[36:39], a[152:155], v[92:95]\n\t" "v_cvt_pk_bf16_f32 v119, v110, v111\n\t" \
    "v_mfma_f32_16x16x32_bf16 v[88:91], v[40:43], a[156:159], v[88:91]\n\t" "buffer_load_dwordx4 v[68:71], %[a1], %[rs], s64 offen offset:256 sc1\n\t" \
    "buffer_store_dwordx4 v[116:119], %[z2], %[rs], s63 offen sc1\n\t" "v_mfma_f32_16x16x32_bf16 v[92:95], v[44:47], a[156:159], v[92:95]\n\t" \
    "s_add_u32 s63, s63, 0x20000\n\t" "v_mfma_f32_16x16x32_bf16 v[96:99], v[16:19], a[160:163], 0\n\t" \
    "buffer_load_dwordx4 v[72:75], %[a1], %[rs], s64 offen offset:2048 sc1\n\t" "v_mfma_f32_16x16x32_bf16 v[100:103], v[20:23], a[160:163], 0\n\t" \
    "v_mfma_f32_16x16x32_bf16 v[96:99], v[24:27], a[164:167], v[96:99]\n\t" "buffer_load_dwordx4 v[76:79], %[a1], %[rs], s64 offen offset:2304 sc1\n\t" \
    "v_mfma_f32_16x16x32_bf16 v[100:103], v[28:31], a[164:167], v[100:103]\n\t" "v_mfma_f32_16x16x32_bf16 v[96:99], v[32:35], a[168:171], v[96:99]\n\t" \
    "v_cvt_pk_bf16_f32 v112, v80, v81\n\t" "v_mfma_f32_16x16x32_bf16 v[100:103], v[36:39], a[168:171], v[100:103]\n\t" "v_cvt_pk_bf16_f32 v113, v82, v83\n\t" \
    "v_mfma_f32_16x16x32_bf16 v[96:99], v[40:43], a[172:175], v[96:99]\n\t" "v_cvt_pk_bf16_f32 v114, v84, v85\n\t" \
    "v_mfma_f32_16x16x32_bf16 v[100:103], v[44:47], a[172:175], v[100:103]\n\t" "v_cvt_pk_bf16_f32 v115, v86, v87\n\t" \
    "v_mfma_f32_16x16x32_bf16 v[104:107], v[16:19], a[176:179], 0\n\t" "v_cvt_pk_bf16_f32 v116, v88, v89\n\t" \
    "v_mfma_f32_16x16x32_bf16 v[108:111], v[20:23], a[176:179], 0\n\t" "v_cvt_pk_bf16_f32 v117, v90, v91\n\t" \
    "v_mfma_f32_16x16x32_bf16 v[104:107], v[24:27], a[180:183], v[104:107]\n\t" "buffer_store_dwordx4 v[112:115], %[z1], %[rs], s63 offen sc1\n\t" \
    "v_mfma_f32_16x16x32_bf16 v[108:111], v[28:31], a[180:183], v[108:111]\n\t" "v_mfma_f32_16x16x32_bf16 v[104:107], v[32:35], a[184:187], v[104:107]\n\t" \
    "v_cvt_pk_bf16_f32 v118, v92, v93\n\t" "v_mfma_f32_16x16x32_bf16 v[108:111], v[36:39], a[184:187], v[108:111]\n\t" "v_cvt_pk_bf16_f32 v119, v94, v95\n\t" \
    "v_mfma_f32_16x16x32_bf16 v[104:107], v[40:43], a[188:191], v[104:107]\n\t" "buffer_store_dwordx4 v[116:119], %[z2], %[rs], s63 offen sc1\n\t" \
    "v_mfma_f32_16x16x32_bf16 v[108:111], v[44:47], a[188:191], v[108:111]\n\t" "s_add_u32 s63, s63, 0x20000\n\t" \
    "v_mfma_f32_16x16x32_bf16 v[80:83], v[16:19], a[192:195], 0\n\t" "v_mfma_f32_16x16x32_bf16 v[84:87], v[20:23], a[192:195], 0\n\t" \
    "v_mfma_f32_16x16x32_bf16 v[80:83], v[24:27], a[196:199], v[80:83]\n\t" "v_mfma_f32_16x16x32_bf16 v[84:87], v[28:31], a[196:199], v[84:87]\n\t" \
    "v_mfma_f32_16x16x32_bf16 v[80:83], v[32:35], a[200:203], v[80:83]\n\t" "v_cvt_pk_bf16_f32 v112, v96, v97\n\t" \
    "v_mfma_f32_16x16x32_bf16 v[84:87], v[36:39], a[200:203], v[84:87]\n\t" "v_cvt_pk_bf16_f32 v113, v98, v99\n\t" \
    "v_mfma_f32_16x16x32_bf16 v[80:83], v[40:43], a[204:207], v[80:83]\n\t" "v_cvt_pk_bf16_f32 v114, v100, v101\n\t" \
    "v_mfma_f32_16x16x32_bf16 v[84:87], v[44:47], a[204:207], v[84:87]\n\t" "v_cvt_pk_bf16_f32 v115, v102, v103\n\t" \
    "v_mfma_f32_16x16x32_bf16 v[88:91], v[16:19], a[208:211], 0\n\t" "v_cvt_pk_bf16_f32 v116, v104, v105\n\t" "v_mfma_f32_16x16x32_bf16 v[92:95], v[20:23], a[208:211], 0\n\t" \
    "v_cvt_pk_bf16_f32 v117, v106, v107\n\t" "v_mfma_f32_16x16x32_bf16 v[88:91], v[24:27], a[212:215], v[88:91]\n\t" \
    "buffer_store_dwordx4 v[112:115], %[z1], %[rs], s63 offen sc1\n\t" "v_mfma_f32_16x16x32_bf16 v[92:95], v[28:31], a[212:215], v[92:95]\n\t" \
    "v_mfma_f32_16x16x32_bf16 v[88:91], v[32:35], a[216:219], v[88:91]\n\t" "v_cvt_pk_bf16_f32 v118, v108, v109\n\t" \
    "v_mfma_f32_16x16x32_bf16 v[92:95], v[36:39], a[216:219], v[92:95]\n\t" "v_cvt_pk_bf16_f32 v119, v110, v111\n\t" \
    "v_mfma_f32_16x16x32_bf16 v[88:91], v[40:43], a[220:223], v[88:91]\n\t" "buffer_store_dwordx4 v[116:119], %[z2], %[rs], s63 offen sc1\n\t" \
    "v_mfma_f32_16x16x32_bf16 v[92:95], v[44:47], a[220:223], v[92:95]\n\t" "s_add_u32 s63, s63, 0x20000\n\t" "v_mfma_f32_16x16x32_bf16 v[96:99], v[16:19], a[224:227], 0\n\t" \
    "v_mfma_f32_16x16x32_bf16 v[100:103], v[20:23], a[224:227], 0\n\t" "v_mfma_f32_16x16x32_bf16 v[96:99], v[24:27], a[228:231], v[96:99]\n\t" \
    "v_mfma_f32_16x16x32_bf16 v[100:103], v[28:31], a[228:231], v[100:103]\n\t" "v_mfma_f32_16x16x32_bf16 v[96:99], v[32:35], a[232:235], v[96:99]\n\t" \
    "v_cvt_pk_bf16_f32 v112, v80, v81\n\t" "v_mfma_f32_16x16x32_bf16 v[100:103], v[36:39], a[232:235], v[100:103]\n\t" "v_cvt_pk_bf16_f32 v113, v82, v83\n\t" \
    "v_mfma_f32_16x16x32_bf16 v[96:99], v[40:43], a[236:239], v[96:99]\n\t" "v_cvt_pk_bf16_f32 v114, v84, v85\n\t" \
    "v_mfma_f32_16x16x32_bf16 v[100:103], v[44:47], a[236:239], v[100:103]\n\t" "v_cvt_pk_bf16_f32 v115, v86, v87\n\t" \
    "v_mfma_f32_16x16x32_bf16 v[104:107], v[16:19], a[240:243], 0\n\t" "v_cvt_pk_bf16_f32 v116, v88, v89\n\t" \
    "v_mfma_f32_16x16x32_bf16 v[108:111], v[20:23], a[240:243], 0\n\t" "v_cvt_pk_bf16_f32 v117, v90, v91\n\t" \
    "v_mfma_f32_16x16x32_bf16 v[104:107], v[24:27], a[244:247], v[104:107]\n\t" "buffer_store_dwordx4 v[112:115], %[z1], %[rs], s63 offen sc1\n\t" \
    "v_mfma_f32_16x16x32_bf16 v[108:111], v[28:31], a[244:247], v[108:111]\n\t" "v_mfma_f32_16x16x32_bf16 v[104:107], v[32:35], a[248:251], v[104:107]\n\t" \
    "v_cvt_pk_bf16_f32 v118, v92, v93\n\t" "v_mfma_f32_16x16x32_bf16 v[108:111], v[36:39], a[248:251], v[108:111]\n\t" "v_cvt_pk_bf16_f32 v119, v94, v95\n\t" \
    "v_mfma_f32_16x16x32_bf16 v[104:107], v[40:43], a[252:255], v[104:107]\n\t" "buffer_store_dwordx4 v[116:119], %[z2], %[rs], s63 offen sc1\n\t" \
    "s_add_u32 s63, s63, 0x20000\n\t" "s_add_u32 s60, s60, 1\n\t" "v_mfma_f32_16x16x32_bf16 v[108:111], v[44:47], a[252:255], v[108:111]\n\t" "s_cmp_eq_u32 s65, 0\n\t" \
    "s_waitcnt vmcnt(6)\n\t" ZSUM "s_cbranch_scc1 20f\n\t" "23:\n\t" XT(80, 81) "s_add_u32 s66, s62, %[rg]\n\t" "s_cmp_lt_u32 s66, s60\n\t" "s_cbranch_scc1 70f\n\t" "73:\n\t" \
    "v_mfma_f32_16x16x32_bf16 v[80:83], v[48:51], a[0:3], 0\n\t" "v_mfma_f32_16x16x32_bf16 v[84:87], v[52:55], a[0:3], 0\n\t" \
    "v_mfma_f32_16x16x32_bf16 v[80:83], v[56:59], a[4:7], v[80:83]\n\t" "v_mfma_f32_16x16x32_bf16 v[84:87], v[60:63], a[4:7], v[84:87]\n\t" \
    "v_mfma_f32_16x16x32_bf16 v[80:83], v[64:67], a[8:11], v[80:83]\n\t" "v_cvt_pk_bf16_f32 v112, v96, v97\n\t" \
    "v_mfma_f32_16x16x32_bf16 v[84:87], v[68:71], a[8:11], v[84:87]\n\t" "v_cvt_pk_bf16_f32 v113, v98, v99\n\t" \
    "v_mfma_f32_16x16x32_bf16 v[80:83], v[72:75], a[12:15], v[80:83]\n\t" "v_cvt_pk_bf16_f32 v114, v100, v101\n\t" \
    "v_mfma_f32_16x16x32_bf16 v[84:87], v[76:79], a[12:15], v[84:87]\n\t" "v_cvt_pk_bf16_f32 v115, v102, v103\n\t" \
    "v_mfma_f32_16x16x32_bf16 v[88:91], v[48:51], a[16:19], 0\n\t" "v_cvt_pk_bf16_f32 v116, v104, v105\n\t" "v_mfma_f32_16x16x32_bf16 v[92:95], v[52:55], a[16:19], 0\n\t" \
    "v_cvt_pk_bf16_f32 v117, v106, v107\n\t" "v_mfma_f32_16x16x32_bf16 v[88:91], v[56:59], a[20:23], v[88:91]\n\t" \
    "buffer_store_dwordx4 v[112:115], %[z1], %[rs], s63 offen sc1\n\t" "v_mfma_f32_16x16x32_bf16 v[92:95], v[60:63], a[20:23], v[92:95]\n\t" \
    "v_mfma_f32_16x16x32_bf16 v[88:91], v[64:67], a[24:27], v[88:91]\n\t" "v_cvt_pk_bf16_f32 v118, v108, v109\n\t" \
    "v_mfma_f32_16x16x32_bf16 v[92:95], v[68:71], a[24:27], v[92:95]\n\t" "v_cvt_pk_bf16_f32 v119, v110, v111\n\t" \
    "v_mfma_f32_16x16x32_bf16 v[88:91], v[72:75], a[28:31], v[88:91]\n\t" "buffer_store_dwordx4 v[116:119], %[z2], %[rs], s63 offen sc1\n\t" "s_and_b32 s63, s60, %[rm]\n\t" \
    "v_mfma_f32_16x16x32_bf16 v[92:95], v[76:79], a[28:31], v[92:95]\n\t" "s_lshl_b32 s63, s63, 22\n\t" "s_add_u32 s63, s63, %[zb]\n\t" \
    "v_mfma_f32_16x16x32_bf16 v[96:99], v[48:51], a[32:35], 0\n\t" "buffer_load_dword v120, %[fo], %[rc], %[fv] offen sc1\n\t" \
    "v_mfma_f32_16x16x32_bf16 v[100:103], v[52:55], a[32:35], 0\n\t" "v_mfma_f32_16x16x32_bf16 v[96:99], v[56:59], a[36:39], v[96:99]\n\t" \
    "buffer_load_dword v121, %[fo], %[rc], %[fs] offen sc1\n\t" "v_mfma_f32_16x16x32_bf16 v[100:103], v[60:63], a[36:39], v[100:103]\n\t" \
    "v_mfma_f32_16x16x32_bf16 v[96:99], v[64:67], a[40:43], v[96:99]\n\t" "v_cvt_pk_bf16_f32 v112, v80, v81\n\t" \
    "v_mfma_f32_16x16x32_bf16 v[100:103], v[68:71], a[40:43], v[100:103]\n\t" "v_cvt_pk_bf16_f32 v113, v82, v83\n\t" \
    "v_mfma_f32_16x16x32_bf16 v[96:99], v[72:75], a[44:47], v[96:99]\n\t" "v_cvt_pk_bf16_f32 v114, v84, v85\n\t" \
    "v_mfma_f32_16x16x32_bf16 v[100:103], v[76:79], a[44:47], v[100:103]\n\t" "v_cvt_pk_bf16_f32 v115, v86, v87\n\t" \
    "v_mfma_f32_16x16x32_bf16 v[104:107], v[48:51], a[48:51], 0\n\t" "v_cvt_pk_bf16_f32 v116, v88, v89\n\t" "v_mfma_f32_16x16x32_bf16 v[108:111], v[52:55], a[48:51], 0\n\t" \
    "v_cvt_pk_bf16_f32 v117, v90, v91\n\t" "v_mfma_f32_16x16x32_bf16 v[104:107], v[56:59], a[52:55], v[104:107]\n\t" \
    "buffer_store_dwordx4 v[112:115], %[z1], %[rs], s63 offen sc1\n\t" "v_mfma_f32_16x16x32_bf16 v[108:111], v[60:63], a[52:55], v[108:111]\n\t" \
    "v_mfma_f32_16x16x32_bf16 v[104:107], v[64:67], a[56:59], v[104:107]\n\t" "v_cvt_pk_bf16_f32 v118, v92, v93\n\t" \
    "v_mfma_f32_16x16x32_bf16 v[108:111], v[68:71], a[56:59], v[108:111]\n\t" "v_cvt_pk_bf16_f32 v119, v94, v95\n\t" \
    "v_mfma_f32_16x16x32_bf16 v[104:107], v[72:75], a[60:63], v[104:107]\n\t" "buffer_store_dwordx4 v[116:119], %[z2], %[rs], s63 offen sc1\n\t" \
    "v_mfma_f32_16x16x32_bf16 v[108:111], v[76:79], a[60:63], v[108:111]\n\t" "s_add_u32 s63, s63, 0x20000\n\t" "v_mfma_f32_16x16x32_bf16 v[80:83], v[48:51], a[64:67], 0\n\t" \
    "s_add_u32 s65, s60, 1\n\t" "s_lshr_b32 s66, s65, 2\n\t" "v_mfma_f32_16x16x32_bf16 v[84:87], v[52:55], a[64:67], 0\n\t" "s_xor_b32 s66, s66, %[xp]\n\t" \
    "s_and_b32 s66, s66, 1\n\t" "v_mfma_f32_16x16x32_bf16 v[80:83], v[56:59], a[68:71], v[80:83]\n\t" "s_lshl_b32 s66, s66, 13\n\t" "s_lshl_b32 s64, s65, 19\n\t" \
    "v_mfma_f32_16x16x32_bf16 v[84:87], v[60:63], a[68:71], v[84:87]\n\t" "s_add_u32 s64, s64, s66\n\t" "s_add_u32 s64, s64, %[vb]\n\t" \
    "v_mfma_f32_16x16x32_bf16 v[80:83], v[64:67], a[72:75], v[80:83]\n\t" "v_cvt_pk_bf16_f32 v112, v96, v97\n\t" \
    "v_mfma_f32_16x16x32_bf16 v[84:87], v[68:71], a[72:75], v[84:87]\n\t" "v_cvt_pk_bf16_f32 v113, v98, v99\n\t" \
    "v_mfma_f32_16x16x32_bf16 v[80:83], v[72:75], a[76:79], v[80:83]\n\t" "v_cvt_pk_bf16_f32 v114, v100, v101\n\t" \
    "v_mfma_f32_16x16x32_bf16 v[84:87], v[76:79], a[76:79], v[84:87]\n\t" "v_cvt_pk_bf16_f32 v115, v102, v103\n\t" \
    "v_mfma_f32_16x16x32_bf16 v[88:91], v[48:51], a[80:83], 0\n\t" "v_cvt_pk_bf16_f32 v116, v104, v105\n\t" "v_mfma_f32_16x16x32_bf16 v[92:95], v[52:55], a[80:83], 0\n\t" \
    "v_cvt_pk_bf16_f32 v117, v106, v107\n\t" "v_mfma_f32_16x16x32_bf16 v[88:91], v[56:59], a[84:87], v[88:91]\n\t" \
    "buffer_store_dwordx4 v[112:115], %[z1], %[rs], s63 offen sc1\n\t" "v_mfma_f32_16x16x32_bf16 v[92:95], v[60:63], a[84:87], v[92:95]\n\t" \
    "v_mfma_f32_16x16x32_bf16 v[88:91], v[64:67], a[88:91], v[88:91]\n\t" "v_cvt_pk_bf16_f32 v118, v108, v109\n\t" \
    "v_mfma_f32_16x16x32_bf16 v[92:95], v[68:71], a[88:91], v[92:95]\n\t" "v_cvt_pk_bf16_f32 v119, v110, v111\n\t" \
    "v_mfma_f32_16x16x32_bf16 v[88:91], v[72:75], a[92:95], v[88:91]\n\t" "buffer_store_dwordx4 v[116:119], %[z2], %[rs], s63 offen sc1\n\t" \
    "v_mfma_f32_16x16x32_bf16 v[92:95], v[76:79], a[92:95], v[92:95]\n\t" "s_add_u32 s63, s63, 0x20000\n\t" "v_mfma_f32_16x16x32_bf16 v[96:99], v[48:51], a[96:99], 0\n\t" \
    "v_mfma_f32_16x16x32_bf16 v[100:103], v[52:55], a[96:99], 0\n\t" "v_mfma_f32_16x16x32_bf16 v[96:99], v[56:59], a[100:103], v[96:99]\n\t" \
    "v_mfma_f32_16x16x32_bf16 v[100:103], v[60:63], a[100:103], v[100:103]\n\t" "v_mfma_f32_16x16x32_bf16 v[96:99], v[64:67], a[104:107], v[96:99]\n\t" \
    "v_cvt_pk_bf16_f32 v112, v80, v81\n\t" "v_mfma_f32_16x16x32_bf16 v[100:103], v[68:71], a[104:107], v[100:103]\n\t" "v_cvt_pk_bf16_f32 v113, v82, v83\n\t" \
    "v_mfma_f32_16x16x32_bf16 v[96:99], v[72:75], a[108:111], v[96:99]\n\t" "v_cvt_pk_bf16_f32 v114, v84, v85\n\t" \
    "v_mfma_f32_16x16x32_bf16 v[100:103], v[76:79], a[108:111], v[100:103]\n\t" "v_cvt_pk_bf16_f32 v115, v86, v87\n\t" \
    "v_mfma_f32_16x16x32_bf16 v[104:107], v[48:51], a[112:115], 0\n\t" "v_cvt_pk_bf16_f32 v116, v88, v89\n\t" \
    "v_mfma_f32_16x16x32_bf16 v[108:111], v[52:55], a[112:115], 0\n\t" "v_cvt_pk_bf16_f32 v117, v90, v91\n\t" \
    "v_mfma_f32_16x16x32_bf16 v[104:107], v[56:59], a[116:119], v[104:107]\n\t" "buffer_store_dwordx4 v[112:115], %[z1], %[rs], s63 offen sc1\n\t" \
    "v_mfma_f32_16x16x32_bf16 v[108:111], v[60:63], a[116:119], v[108:111]\n\t" "v_mfma_f32_16x16x32_bf16 v[104:107], v[64:67], a[120:123], v[104:107]\n\t" \
    "v_cvt_pk_bf16_f32 v118, v92, v93\n\t" "v_mfma_f32_16x16x32_bf16 v[108:111], v[68:71], a[120:123], v[108:111]\n\t" "v_cvt_pk_bf16_f32 v119, v94, v95\n\t" \
    "v_mfma_f32_16x16x32_bf16 v[104:107], v[72:75], a[124:127], v[104:107]\n\t" "buffer_store_dwordx4 v[116:119], %[z2], %[rs], s63 offen sc1\n\t" \
    "v_mfma_f32_16x16x32_bf16 v[108:111], v[76:79], a[124:127], v[108:111]\n\t" "s_add_u32 s63, s63, 0x20000\n\t" \
    "v_mfma_f32_16x16x32_bf16 v[80:83], v[48:51], a[128:131], 0\n\t" "s_waitcnt vmcnt(6)\n\t" "v_readfirstlane_b32 s66, v120\n\t" "v_readfirstlane_b32 s67, v121\n\t" \
    "v_mfma_f32_16x16x32_bf16 v[84:87], v[52:55], a[128:131], 0\n\t" "v_mov_b32 v122, s60\n\t" "buffer_store_dword v122, %[fo], %[rc], %[fz] offen sc1\n\t" \
    "v_mfma_f32_16x16x32_bf16 v[80:83], v[56:59], a[132:135], v[80:83]\n\t" "s_max_u32 s61, s61, s66\n\t" "s_max_u32 s62, s62, s67\n\t" "s_cmp_gt_u32 s61, s65\n\t" \
    "v_mfma_f32_16x16x32_bf16 v[84:87], v[60:63], a[132:135], v[84:87]\n\t" "s_cselect_b32 s65, 1, 0\n\t" "s_cselect_b32 s64, s64, 0x7f000000\n\t" \
    "v_mfma_f32_16x16x32_bf16 v[80:83], v[64:67], a[136:139], v[80:83]\n\t" "buffer_load_dwordx4 v[16:19], %[ao], %[rs], s64 offen offset:0 sc1\n\t" \
    "v_cvt_pk_bf16_f32 v112, v96, v97\n\t" "v_mfma_f32_16x16x32_bf16 v[84:87], v[68:71], a[136:139], v[84:87]\n\t" "v_cvt_pk_bf16_f32 v113, v98, v99\n\t" \
    "v_mfma_f32_16x16x32_bf16 v[80:83], v[72:75], a[140:143], v[80:83]\n\t" "buffer_load_dwordx4 v[20:23], %[ao], %[rs], s64 offen offset:256 sc1\n\t" \
    "v_cvt_pk_bf16_f32 v114, v100, v101\n\t" "v_mfma_f32_16x16x32_bf16 v[84:87], v[76:79], a[140:143], v[84:87]\n\t" "v_cvt_pk_bf16_f32 v115, v102, v103\n\t" \
    "v_mfma_f32_16x16x32_bf16 v[88:91], v[48:51], a[144:147], 0\n\t" "buffer_load_dwordx4 v[24:27], %[ao], %[rs], s64 offen offset:2048 sc1\n\t" \
    "v_cvt_pk_bf16_f32 v116, v104, v105\n\t" "v_mfma_f32_16x16x32_bf16 v[92:95], v[52:55], a[144:147], 0\n\t" "v_cvt_pk_bf16_f32 v117, v106, v107\n\t" \
    "v_mfma_f32_16x16x32_bf16 v[88:91], v[56:59], a[148:151], v[88:91]\n\t" "buffer_load_dwordx4 v[28:31], %[ao], %[rs], s64 offen offset:2304 sc1\n\t" \
    "v_mfma_f32_16x16x32_bf16 v[92:95], v[60:63], a[148:151], v[92:95]\n\t" "buffer_store_dwordx4 v[112:115], %[z1], %[rs], s63 offen sc1\n\t" \
    "v_mfma_f32_16x16x32_bf16 v[88:91], v[64:67], a[152:155], v[88:91]\n\t" "buffer_load_dwordx4 v[32:35], %[a1], %[rs], s64 offen offset:0 sc1\n\t" \
    "v_cvt_pk_bf16_f32 v118, v108, v109\n\t" "v_mfma_f32_16x16x32_bf16 v[92:95], v[68:71], a[152:155], v[92:95]\n\t" "v_cvt_pk_bf16_f32 v119, v110, v111\n\t" \
    "v_mfma_f32_16x16x32_bf16 v[88:91], v[72:75], a[156:159], v[88:91]\n\t" "buffer_load_dwordx4 v[36:39], %[a1], %[rs], s64 offen offset:256 sc1\n\t" \
    "buffer_store_dwordx4 v[116:119], %[z2], %[rs], s63 offen sc1\n\t" "v_mfma_f32_16x16x32_bf16 v[92:95], v[76:79], a[156:159], v[92:95]\n\t" \
    "s_add_u32 s63, s63, 0x20000\n\t" "v_mfma_f32_16x16x32_bf16 v[96:99], v[48:51], a[160:163], 0\n\t" \
    "buffer_load_dwordx4 v[40:43], %[a1], %[rs], s64 offen offset:2048 sc1\n\t" "v_mfma_f32_16x16x32_bf16 v[100:103], v[52:55], a[160:163], 0\n\t" \
    "v_mfma_f32_16x16x32_bf16 v[96:99], v[56:59], a[164:167], v[96:99]\n\t" "buffer_load_dwordx4 v[44:47], %[a1], %[rs], s64 offen offset:2304 sc1\n\t" \
    "v_mfma_f32_16x16x32_bf16 v[100:103], v[60:63], a[164:167], v[100:103]\n\t" "v_mfma_f32_16x16x32_bf16 v[96:99], v[64:67], a[168:171], v[96:99]\n\t" \
    "v_cvt_pk_bf16_f32 v112, v80, v81\n\t" "v_mfma_f32_16x16x32_bf16 v[100:103], v[68:71], a[168:171], v[100:103]\n\t" "v_cvt_pk_bf16_f32 v113, v82, v83\n\t" \
    "v_mfma_f32_16x16x32_bf16 v[96:99], v[72:75], a[172:175], v[96:99]\n\t" "v_cvt_pk_bf16_f32 v114, v84, v85\n\t" \
    "v_mfma_f32_16x16x32_bf16 v[100:103], v[76:79], a[172:175], v[100:103]\n\t" "v_cvt_pk_bf16_f32 v115, v86, v87\n\t" \
    "v_mfma_f32_16x16x32_bf16 v[104:107], v[48:51], a[176:179], 0\n\t" "v_cvt_pk_bf16_f32 v116, v88, v89\n\t" \
    "v_mfma_f32_16x16x32_bf16 v[108:111], v[52:55], a[176:179], 0\n\t" "v_cvt_pk_bf16_f32 v117, v90, v91\n\t" \
    "v_mfma_f32_16x16x32_bf16 v[104:107], v[56:59], a[180:183], v[104:107]\n\t" "buffer_store_dwordx4 v[112:115], %[z1], %[rs], s63 offen sc1\n\t" \
    "v_mfma_f32_16x16x32_bf16 v[108:111], v[60:63], a[180:183], v[108:111]\n\t" "v_mfma_f32_16x16x32_bf16 v[104:107], v[64:67], a[184:187], v[104:107]\n\t" \
    "v_cvt_pk_bf16_f32 v118, v92, v93\n\t" "v_mfma_f32_16x16x32_bf16 v[108:111], v[68:71], a[184:187], v[108:111]\n\t" "v_cvt_pk_bf16_f32 v119, v94, v95\n\t" \
    "v_mfma_f32_16x16x32_bf16 v[104:107], v[72:75], a[188:191], v[104:107]\n\t" "buffer_store_dwordx4 v[116:119], %[z2], %[rs], s63 offen sc1\n\t" \
    "v_mfma_f32_16x16x32_bf16 v[108:111], v[76:79], a[188:191], v[108:111]\n\t" "s_add_u32 s63, s63, 0x20000\n\t" \
    "v_mfma_f32_16x16x32_bf16 v[80:83], v[48:51], a[192:195], 0\n\t" "v_mfma_f32_16x16x32_bf16 v[84:87], v[52:55], a[192:195], 0\n\t" \
    "v_mfma_f32_16x16x32_bf16 v[80:83], v[56:59], a[196:199], v[80:83]\n\t" "v_mfma_f32_16x16x32_bf16 v[84:87], v[60:63], a[196:199], v[84:87]\n\t" \
    "v_mfma_f32_16x16x32_bf16 v[80:83], v[64:67], a[200:203], v[80:83]\n\t" "v_cvt_pk_bf16_f32 v112, v96, v97\n\t" \
    "v_mfma_f32_16x16x32_bf16 v[84:87], v[68:71], a[200:203], v[84:87]\n\t" "v_cvt_pk_bf16_f32 v113, v98, v99\n\t" \
    "v_mfma_f32_16x16x32_bf16 v[80:83], v[72:75], a[204:207], v[80:83]\n\t" "v_cvt_pk_bf16_f32 v114, v100, v101\n\t" \
    "v_mfma_f32_16x16x32_bf16 v[84:87], v[76:79], a[204:207], v[84:87]\n\t" "v_cvt_pk_bf16_f32 v115, v102, v103\n\t" \
    "v_mfma_f32_16x16x32_bf16 v[88:91], v[48:51], a[208:211], 0\n\t" "v_cvt_pk_bf16_f32 v116, v104, v105\n\t" "v_mfma_f32_16x16x32_bf16 v[92:95], v[52:55], a[208:211], 0\n\t" \
    "v_cvt_pk_bf16_f32 v117, v106, v107\n\t" "v_mfma_f32_16x16x32_bf16 v[88:91], v[56:59], a[212:215], v[88:91]\n\t" \
    "buffer_store_dwordx4 v[112:115], %[z1], %[rs], s63 offen sc1\n\t" "v_mfma_f32_16x16x32_bf16 v[92:95], v[60:63], a[212:215], v[92:95]\n\t" \
    "v_mfma_f32_16x16x32_bf16 v[88:91], v[64:67], a[216:219], v[88:91]\n\t" "v_cvt_pk_bf16_f32 v118, v108, v109\n\t" \
    "v_mfma_f32_16x16x32_bf16 v[92:95], v[68:71], a[216:219], v[92:95]\n\t" "v_cvt_pk_bf16_f32 v119, v110, v111\n\t" \
    "v_mfma_f32_16x16x32_bf16 v[88:91], v[72:75], a[220:223], v[88:91]\n\t" "buffer_store_dwordx4 v[116:119], %[z2], %[rs], s63 offen sc1\n\t" \
    "v_mfma_f32_16x16x32_bf16 v[92:95], v[76:79], a[220:223], v[92:95]\n\t" "s_add_u32 s63, s63, 0x20000\n\t" "v_mfma_f32_16x16x32_bf16 v[96:99], v[48:51], a[224:227], 0\n\t" \
    "v_mfma_f32_16x16x32_bf16 v[100:103], v[52:55], a[224:227], 0\n\t" "v_mfma_f32_16x16x32_bf16 v[96:99], v[56:59], a[228:231], v[96:99]\n\t" \
    "v_mfma_f32_16x16x32_bf16 v[100:103], v[60:63], a[228:231], v[100:103]\n\t" "v_mfma_f32_16x16x32_bf16 v[96:99], v[64:67], a[232:235], v[96:99]\n\t" \
    "v_cvt_pk_bf16_f32 v112, v80, v81\n\t" "v_mfma_f32_16x16x32_bf16 v[100:103], v[68:71], a[232:235], v[100:103]\n\t" "v_cvt_pk_bf16_f32 v113, v82, v83\n\t" \
    "v_mfma_f32_16x16x32_bf16 v[96:99], v[72:75], a[236:239], v[96:99]\n\t" "v_cvt_pk_bf16_f32 v114, v84, v85\n\t" \
    "v_mfma_f32_16x16x32_bf16 v[100:103], v[76:79], a[236:239], v[100:103]\n\t" "v_cvt_pk_bf16_f32 v115, v86, v87\n\t" \
    "v_mfma_f32_16x16x32_bf16 v[104:107], v[48:51], a[240:243], 0\n\t" "v_cvt_pk_bf16_f32 v116, v88, v89\n\t" \
    "v_mfma_f32_16x16x32_bf16 v[108:111], v[52:55], a[240:243], 0\n\t" "v_cvt_pk_bf16_f32 v117, v90, v91\n\t" \
    "v_mfma_f32_16x16x32_bf16 v[104:107], v[56:59], a[244:247], v[104:107]\n\t" "buffer_store_dwordx4 v[112:115], %[z1], %[rs], s63 offen sc1\n\t" \
    "v_mfma_f32_16x16x32_bf16 v[108:111], v[60:63], a[244:247], v[108:111]\n\t" "v_mfma_f32_16x16x32_bf16 v[104:107], v[64:67], a[248:251], v[104:107]\n\t" \
    "v_cvt_pk_bf16_f32 v118, v92, v93\n\t" "v_mfma_f32_16x16x32_bf16 v[108:111], v[68:71], a[248:251], v[108:111]\n\t" "v_cvt_pk_bf16_f32 v119, v94, v95\n\t" \
    "v_mfma_f32_16x16x32_bf16 v[104:107], v[72:75], a[252:255], v[104:107]\n\t" "buffer_store_dwordx4 v[116:119], %[z2], %[rs], s63 offen sc1\n\t" \
    "s_add_u32 s63, s63, 0x20000\n\t" "s_add_u32 s60, s60, 1\n\t" "v_mfma_f32_16x16x32_bf16 v[108:111], v[76:79], a[252:255], v[108:111]\n\t" "s_cmp_eq_u32 s65, 0\n\t" \
    "s_waitcnt vmcnt(6)\n\t" ZSUM "s_cbranch_scc1 30f\n\t" "33:\n\t" "s_branch 10b\n\t" "20:\n\t" "s_nop 7\n\t" "v_cvt_pk_bf16_f32 v112, v96, v97\n\t" \
    "v_cvt_pk_bf16_f32 v113, v98, v99\n\t" "v_cvt_pk_bf16_f32 v114, v100, v101\n\t" "v_cvt_pk_bf16_f32 v115, v102, v103\n\t" "v_cvt_pk_bf16_f32 v116, v104, v105\n\t" \
    "v_cvt_pk_bf16_f32 v117, v106, v107\n\t" "v_cvt_pk_bf16_f32 v118, v108, v109\n\t" "v_cvt_pk_bf16_f32 v119, v110, v111\n\t" \
    "buffer_store_dwordx4 v[112:115], %[z1], %[rs], s63 offen sc1\n\t" "buffer_store_dwordx4 v[116:119], %[z2], %[rs], s63 offen sc1\n\t" \
    "buffer_load_dword v120, %[fo], %[rc], %[fv] offen sc1\n\t" "s_waitcnt vmcnt(0)\n\t" "v_readfirstlane_b32 s61, v120\n\t" "s_lshr_b32 s66, s60, 2\n\t" \
    "s_xor_b32 s66, s66, %[xp]\n\t" "s_and_b32 s66, s66, 1\n\t" "s_lshl_b32 s66, s66, 13\n\t" "s_lshl_b32 s64, s60, 19\n\t" "s_add_u32 s64, s64, s66\n\t" \
    "s_add_u32 s64, s64, %[vb]\n\t" "s_cmp_gt_u32 s61, s60\n\t" "s_cbranch_scc0 24f\n\t" "s_cmp_eq_u32 s60, 64\n\t" "s_cbranch_scc1 24f\n\t" \
    "buffer_load_dwordx4 v[48:51], %[ao], %[rs], s64 offen offset:0 sc1\n\t" "buffer_load_dwordx4 v[52:55], %[ao], %[rs], s64 offen offset:256 sc1\n\t" \
    "buffer_load_dwordx4 v[56:59], %[ao], %[rs], s64 offen offset:2048 sc1\n\t" "buffer_load_dwordx4 v[60:63], %[ao], %[rs], s64 offen offset:2304 sc1\n\t" \
    "buffer_load_dwordx4 v[64:67], %[a1], %[rs], s64 offen offset:0 sc1\n\t" "buffer_load_dwordx4 v[68:71], %[a1], %[rs], s64 offen offset:256 sc1\n\t" \
    "buffer_load_dwordx4 v[72:75], %[a1], %[rs], s64 offen offset:2048 sc1\n\t" "buffer_load_dwordx4 v[76:79], %[a1], %[rs], s64 offen offset:2304 sc1\n\t" \
    "v_mov_b32 v122, s60\n\t" "buffer_store_dword v122, %[fo], %[rc], %[fz] offen sc1\n\t" "s_waitcnt vmcnt(1)\n\t" "s_branch 23b\n\t" "24:\n\t" "v_mov_b32 v122, s60\n\t" \
    "buffer_store_dword v122, %[fo], %[rc], %[fz] offen sc1\n\t" "s_cmp_eq_u32 s60, 64\n\t" "s_cbranch_scc1 90f\n\t" "s_add_u32 s67, s60, 1\n\t" "s_cmp_ge_u32 s61, s67\n\t" \
    "s_cbranch_scc1 22f\n\t" "21:\n\t" "buffer_load_dword v120, %[fo], %[rc], %[fv] offen sc1\n\t" "buffer_load_dword v121, %[fo], %[rc], %[vd] offen sc1\n\t" \
    "s_waitcnt vmcnt(0)\n\t" "v_readfirstlane_b32 s61, v120\n\t" "v_readfirstlane_b32 s66, v121\n\t" "s_nop 3\n\t" "s_cmp_ge_u32 s61, s67\n\t" "s_cbranch_scc1 22f\n\t" \
    "s_bitcmp1_b32 s66, 1\n\t" "s_cbranch_scc1 90f\n\t" "s_cmp_eq_u32 %[bd], 0\n\t" "s_cbranch_scc1 22f\n\t" "s_sub_u32 %[bd], %[bd], 1\n\t" "s_sleep 2\n\t" \
    "s_branch 21b\n\t" "22:\n\t" "s_lshr_b32 s66, s60, 2\n\t" "s_xor_b32 s66, s66, %[xp]\n\t" "s_and_b32 s66, s66, 1\n\t" "s_lshl_b32 s66, s66, 13\n\t" \
    "s_lshl_b32 s64, s60, 19\n\t" "s_add_u32 s64, s64, s66\n\t" "s_add_u32 s64, s64, %[vb]\n\t" "buffer_load_dwordx4 v[48:51], %[ao], %[rs], s64 offen offset:0 sc1\n\t" \
    "buffer_load_dwordx4 v[52:55], %[ao], %[rs], s64 offen offset:256 sc1\n\t" "buffer_load_dwordx4 v[56:59], %[ao], %[rs], s64 offen offset:2048 sc1\n\t" \
    "buffer_load_dwordx4 v[60:63], %[ao], %[rs], s64 offen offset:2304 sc1\n\t" "buffer_load_dwordx4 v[64:67], %[a1], %[rs], s64 offen offset:0 sc1\n\t" \
    "buffer_load_dwordx4 v[68:71], %[a1], %[rs], s64 offen offset:256 sc1\n\t" "buffer_load_dwordx4 v[72:75], %[a1], %[rs], s64 offen offset:2048 sc1\n\t" \
    "buffer_load_dwordx4 v[76:79], %[a1], %[rs], s64 offen offset:2304 sc1\n\t" "s_waitcnt vmcnt(0)\n\t" "s_branch 23b\n\t" "60:\n\t" "s_sub_u32 s67, s60, %[rg]\n\t" \
    "s_cmp_ge_u32 s62, s67\n\t" "s_cbranch_scc1 62f\n\t" "61:\n\t" "buffer_load_dword v120, %[fo], %[rc], %[fs] offen sc1\n\t" \
    "buffer_load_dword v121, %[fo], %[rc], %[vd] offen sc1\n\t" "s_waitcnt vmcnt(0)\n\t" "v_readfirstlane_b32 s62, v120\n\t" "v_readfirstlane_b32 s66, v121\n\t" "s_nop 3\n\t" \
    "s_cmp_ge_u32 s62, s67\n\t" "s_cbranch_scc1 62f\n\t" "s_bitcmp1_b32 s66, 1\n\t" "s_cbranch_scc1 90f\n\t" "s_cmp_eq_u32 %[bd], 0\n\t" "s_cbranch_scc1 62f\n\t" \
    "s_sub_u32 %[bd], %[bd], 1\n\t" "s_sleep 2\n\t" "s_branch 61b\n\t" "62:\n\t" "s_branch 63b\n\t" "30:\n\t" "s_nop 7\n\t" "v_cvt_pk_bf16_f32 v112, v96, v97\n\t" \
    "v_cvt_pk_bf16_f32 v113, v98, v99\n\t" "v_cvt_pk_bf16_f32 v114, v100, v101\n\t" "v_cvt_pk_bf16_f32 v115, v102, v103\n\t" "v_cvt_pk_bf16_f32 v116, v104, v105\n\t" \
    "v_cvt_pk_bf16_f32 v117, v106, v107\n\t" "v_cvt_pk_bf16_f32 v118, v108, v109\n\t" "v_cvt_pk_bf16_f32 v119, v110, v111\n\t" \
    "buffer_store_dwordx4 v[112:115], %[z1], %[rs], s63 offen sc1\n\t" "buffer_store_dwordx4 v[116:119], %[z2], %[rs], s63 offen sc1\n\t" \
    "buffer_load_dword v120, %[fo], %[rc], %[fv] offen sc1\n\t" "s_waitcnt vmcnt(0)\n\t" "v_readfirstlane_b32 s61, v120\n\t" "s_lshr_b32 s66, s60, 2\n\t" \
    "s_xor_b32 s66, s66, %[xp]\n\t" "s_and_b32 s66, s66, 1\n\t" "s_lshl_b32 s66, s66, 13\n\t" "s_lshl_b32 s64, s60, 19\n\t" "s_add_u32 s64, s64, s66\n\t" \
    "s_add_u32 s64, s64, %[vb]\n\t" "s_cmp_gt_u32 s61, s60\n\t" "s_cbranch_scc0 34f\n\t" "s_cmp_eq_u32 s60, 64\n\t" "s_cbranch_scc1 34f\n\t" \
    "buffer_load_dwordx4 v[16:19], %[ao], %[rs], s64 offen offset:0 sc1\n\t" "buffer_load_dwordx4 v[20:23], %[ao], %[rs], s64 offen offset:256 sc1\n\t" \
    "buffer_load_dwordx4 v[24:27], %[ao], %[rs], s64 offen offset:2048 sc1\n\t" "buffer_load_dwordx4 v[28:31], %[ao], %[rs], s64 offen offset:2304 sc1\n\t" \
    "buffer_load_dwordx4 v[32:35], %[a1], %[rs], s64 offen offset:0 sc1\n\t" "buffer_load_dwordx4 v[36:39], %[a1], %[rs], s64 offen offset:256 sc1\n\t" \
    "buffer_load_dwordx4 v[40:43], %[a1], %[rs], s64 offen offset:2048 sc1\n\t" "buffer_load_dwordx4 v[44:47], %[a1], %[rs], s64 offen offset:2304 sc1\n\t" \
    "v_mov_b32 v122, s60\n\t" "buffer_store_dword v122, %[fo], %[rc], %[fz] offen sc1\n\t" "s_waitcnt vmcnt(1)\n\t" "s_branch 33b\n\t" "34:\n\t" "v_mov_b32 v122, s60\n\t" \
    "buffer_store_dword v122, %[fo], %[rc], %[fz] offen sc1\n\t" "s_cmp_eq_u32 s60, 64\n\t" "s_cbranch_scc1 90f\n\t" "s_add_u32 s67, s60, 1\n\t" "s_cmp_ge_u32 s61, s67\n\t" \
    "s_cbranch_scc1 32f\n\t" "31:\n\t" "buffer_load_dword v120, %[fo], %[rc], %[fv] offen sc1\n\t" "buffer_load_dword v121, %[fo], %[rc], %[vd] offen sc1\n\t" \
    "s_waitcnt vmcnt(0)\n\t" "v_readfirstlane_b32 s61, v120\n\t" "v_readfirstlane_b32 s66, v121\n\t" "s_nop 3\n\t" "s_cmp_ge_u32 s61, s67\n\t" "s_cbranch_scc1 32f\n\t" \
    "s_bitcmp1_b32 s66, 1\n\t" "s_cbranch_scc1 90f\n\t" "s_cmp_eq_u32 %[bd], 0\n\t" "s_cbranch_scc1 32f\n\t" "s_sub_u32 %[bd], %[bd], 1\n\t" "s_sleep 2\n\t" \
    "s_branch 31b\n\t" "32:\n\t" "s_lshr_b32 s66, s60, 2\n\t" "s_xor_b32 s66, s66, %[xp]\n\t" "s_and_b32 s66, s66, 1\n\t" "s_lshl_b32 s66, s66, 13\n\t" \
    "s_lshl_b32 s64, s60, 19\n\t" "s_add_u32 s64, s64, s66\n\t" "s_add_u32 s64, s64, %[vb]\n\t" "buffer_load_dwordx4 v[16:19], %[ao], %[rs], s64 offen offset:0 sc1\n\t" \
    "buffer_load_dwordx4 v[20:23], %[ao], %[rs], s64 offen offset:256 sc1\n\t" "buffer_load_dwordx4 v[24:27], %[ao], %[rs], s64 offen offset:2048 sc1\n\t" \
    "buffer_load_dwordx4 v[28:31], %[ao], %[rs], s64 offen offset:2304 sc1\n\t" "buffer_load_dwordx4 v[32:35], %[a1], %[rs], s64 offen offset:0 sc1\n\t" \
    "buffer_load_dwordx4 v[36:39], %[a1], %[rs], s64 offen offset:256 sc1\n\t" "buffer_load_dwordx4 v[40:43], %[a1], %[rs], s64 offen offset:2048 sc1\n\t" \
    "buffer_load_dwordx4 v[44:47], %[a1], %[rs], s64 offen offset:2304 sc1\n\t" "s_waitcnt vmcnt(0)\n\t" "s_branch 33b\n\t" "70:\n\t" "s_sub_u32 s67, s60, %[rg]\n\t" \
    "s_cmp_ge_u32 s62, s67\n\t" "s_cbranch_scc1 72f\n\t" "71:\n\t" "buffer_load_dword v120, %[fo], %[rc], %[fs] offen sc1\n\t" \
    "buffer_load_dword v121, %[fo], %[rc], %[vd] offen sc1\n\t" "s_waitcnt vmcnt(0)\n\t" "v_readfirstlane_b32 s62, v120\n\t" "v_readfirstlane_b32 s66, v121\n\t" "s_nop 3\n\t" \
    "s_cmp_ge_u32 s62, s67\n\t" "s_cbranch_scc1 72f\n\t" "s_bitcmp1_b32 s66, 1\n\t" "s_cbranch_scc1 90f\n\t" "s_cmp_eq_u32 %[bd], 0\n\t" "s_cbranch_scc1 72f\n\t" \
    "s_sub_u32 %[bd], %[bd], 1\n\t" "s_sleep 2\n\t" "s_branch 71b\n\t" "72:\n\t" "s_branch 73b\n\t" "90:\n\t"

#define T_DQ 1480
#define T_DV 1540
#define T_M1 150
#define T_DY 700
#define T_DX 1250
#define T_MIN 35
#define T_PEN 600
#define T_RETRY 1100
#define T_POLL 3
#define T_RST 12
#define T_U0 540
constexpr int T_OVH = 118;
constexpr unsigned NS(int ns) { return (unsigned)(ns * 64 / 10); }
struct Clk { unsigned cyc, g, k1, k2; };
DEV void anchor(Clk& k) {
  unsigned long long c, r[8];
  asm volatile("s_memtime %0\n\ts_memrealtime %1\n\ts_nop 2\n\ts_memrealtime %2\n\ts_nop 2\n\ts_memrealtime %3\n\ts_nop 2\n\ts_memrealtime %4\n\ts_nop 2\n\t"
               "s_memrealtime %5\n\ts_nop 2\n\ts_memrealtime %6\n\ts_nop 2\n\ts_memrealtime %7\n\ts_waitcnt lgkmcnt(0)"
               : "=s"(c), "=s"(r[0]), "=s"(r[1]), "=s"(r[2]), "=s"(r[3]), "=s"(r[4]), "=s"(r[5]), "=s"(r[6]) :: "memory");
  const unsigned a = (unsigned)r[0];
  unsigned n = 0;
#pragma unroll
  for (int i = 1; i < 7; ++i) asm("s_cmp_eq_u32 %1, %2\n\ts_addc_u32 %0, %0, 0" : "+s"(n) : "s"((unsigned)r[i]), "s"(a) : "scc");
  const unsigned nx = (a + 4u) << 6, cy = (unsigned)c + 12u + 16u * n;
  const float dc = (float)(cy - k.cyc), dg = (float)(nx - k.g);
  asm volatile("s_nop 0\n\tv_readfirstlane_b32 %0, %2\n\tv_readfirstlane_b32 %1, %3\n\ts_nop 3\n\ts_min_u32 %0, %0, 0x80000\n\ts_min_u32 %1, %1, 0x8000"
               : "=&s"(k.k1), "=&s"(k.k2) : "v"((unsigned)(dg * __builtin_amdgcn_rcpf(dc) * 65536.f)), "v"((unsigned)(dc * __builtin_amdgcn_rcpf(dg) * 65536.f)) : "scc");
  k.cyc = cy; k.g = nx;
}
DEV unsigned to_cyc(const Clk& k, unsigned g) {
  unsigned d;
  asm("s_max_i32 %0, %1, 0xffff8000\n\ts_min_i32 %0, %0, 0x8000\n\ts_mul_i32 %0, %0, %2\n\ts_ashr_i32 %0, %0, 16" : "=&s"(d) : "s"(g - k.g), "s"(k.k2) : "scc");
  return k.cyc + d - T_OVH;
}
DEV void wait_cyc(unsigned t) { asm volatile(TIMED_WAIT :: "s"(t) : CL_S); }
DEV bool slow_head(const u16* q, int h) { return ((((unsigned)(size_t)q >> 8) + h) & 3) == 1; }
DEV void hold(const Ctx& c, int ticks) {
#pragma unroll 1
  while ((unsigned)__builtin_amdgcn_s_memrealtime() - c.t0 < (unsigned)ticks) __builtin_amdgcn_s_sleep(4);
}
DEV int u_slot(int n, int par, int para) { return n == 2 ? R_U2 + par * 8192 : R_U + n * MB + para * 8192; }
DEV unsigned ask_stamps(Ctx& c, int par, int slot) { return __builtin_amdgcn_raw_buffer_load_b32(c.rs, amap(24576, par) + fresh_lane() * 128, slot, AUX_SC1); }
DEV unsigned latest(Ctx& c, const Clk& k, int par, int slot, unsigned v) {
  if (__builtin_expect(!allok(v), 0)) {
#pragma unroll 1
    do {
      __builtin_amdgcn_s_sleep(T_POLL);
      asm volatile("" ::: "memory");
      const unsigned t = __builtin_amdgcn_raw_buffer_load_b32(c.rs, v == SENT ? amap(24576, par) + fresh_lane() * 128 : 1 << 30, slot, AUX_SC1);
      v = v == SENT ? t : v;
    } while (!allok(v) && !spend(c));
  }
  int d = (int)(v - k.g);
  d = max(d, __builtin_amdgcn_update_dpp(d, d, 0x128, 0xf, 0xf, true));
  d = max(d, __builtin_amdgcn_update_dpp(d, d, 0x124, 0xf, 0xf, true));
  d = max(d, __builtin_amdgcn_update_dpp(d, d, 0x122, 0xf, 0xf, true));
  d = max(d, __builtin_amdgcn_update_dpp(d, d, 0x121, 0xf, 0xf, true));
  unsigned m;
  asm("s_max_i32 %0, %1, %2\n\ts_max_i32 %0, %0, %3\n\ts_max_i32 %0, %0, %4" : "=&s"(m)
      : "s"(__builtin_amdgcn_readlane(d, 0)), "s"(__builtin_amdgcn_readlane(d, 16)), "s"(__builtin_amdgcn_readlane(d, 32)), "s"(__builtin_amdgcn_readlane(d, 48)) : "scc");
  return k.g + m;
}
struct Plan { unsigned gx, dx; };
DEV unsigned smax(unsigned a, unsigned b) { return (int)(a - b) > 0 ? a : b; }
DEV unsigned second_try(const Plan& t, unsigned last) { return smax(last + NS(T_PEN), t.gx + t.dx + NS(T_M1 + T_RETRY)); }
DEV void advance(Plan& t, unsigned last) {
  const unsigned gy = t.gx + t.dx + NS(T_M1), dur = last - t.gx;
  const bool missed = (int)(last + NS(T_MIN) - gy) > 0;
  const unsigned nx = (missed ? second_try(t, last) : gy) + NS(T_DY);
  t.dx = dur < t.dx + NS(60) ? (3u * t.dx + dur) >> 2 : t.dx + NS(8);
  t.gx = nx;
}
DEV unsigned next_go(Ctx& c, Clk& k, Plan& t, int par, int slot, unsigned v) {
  SCHED();
  anchor(k);
  SCHED();
  advance(t, latest(c, k, par, slot, v));
  return to_cyc(k, t.gx);
}

DEV constexpr int qk_r(int j) { return j < 4 ? 0 : 1 + (j - 4) % 3; }
DEV constexpr int qk_i(int j) { return j < 4 ? j : (j - 4) / 3; }
DEV constexpr int qk_upto(int u) { return u < 6 ? u + 7 : u < 10 ? 12 + (u - 4) / 2 : u < 12 ? 15 : 16; }
DEV void qk_role(const Params& p, Ctx& c, int h) {
  constexpr int WL = 0, MEMS = 131072, KXL = 147456, QKX = 155648, QROW = 272;
  c.budget = SPIN_CAP;
  const int w = c.w;
  Clk k = {0u, 0u, 0u, 0u};
  anchor(k);
  v8u wa[32], wh[16];
  claim(c, 0);
  hold(c, QK_AT - (slow_head(p.Wq, h) ? SLOW_LEAD : 0));
  {
    const int lane = fresh_lane(), g = lane >> 4, cc = lane & 15;
    const rsrc_t rq = mkrs(p.Wq, 1024 * 4096 * 2), rk = mkrs(p.Wk, 1024 * 4096 * 2);
    const int vo0 = opq((256 * w + 64 * (h & 1) + krow_lane(g)) * 8192 + (h * 128 + 8 * cc) * 2), vo1 = opq(vo0 ^ (64 * 8192));
    v4u row[16][8];
    auto request = [&](int u) {
#pragma unroll
      for (int r = 0; r < 8; ++r) row[u][r] = ld16w(u < 8 ? rq : rk, (u & 2) ? vo1 : vo0, (32 * (u & 4) + krow(4 * (u & 1) + (r >> 1), r & 1)) * 8192);
    };
#pragma unroll
    for (int u = 0; u < qk_upto(-1); ++u) request(u);
    SCHED();
    const int sw = MEMS + w * 8192 + g * 256, lw = opq(WL + w * 32768 + lane * 16);
    int sr[2];
#pragma unroll
    for (int t = 0; t < 2; ++t) { const int i = (2 * cc + t) & 7; sr[t] = opq(i * 1024 + g * 256 + ((4 * w + (cc >> 2) + 2 * i) & 15) * 16); }
    sfor<16>([&](auto U) __attribute__((always_inline)) {
      constexpr int u = U.value;
#pragma unroll
      for (int i = 0; i < 8; ++i) {
        v4u f;
#pragma unroll
        for (int r = 0; r < 4; ++r) f[r] = prm(row[u][2 * r + 1][i >> 1], row[u][2 * r][i >> 1], i & 1);
        sts16(c.L, sw + i * 1024 + ((cc + 2 * i) & 15) * 16, f);
      }
      SCHED();
      if (qk_upto(u) > qk_upto(u - 1)) request(qk_upto(u - 1));
      BAR();
      v4u in[4][2];
#pragma unroll
      for (int t = 1; t >= 0; --t)
#pragma unroll
        for (int r = 0; r < 4; ++r) in[r][t] = lds16(c.L, sr[t] + MEMS + ((w + r) & 3) * 8192);
      SCHED();
#pragma unroll
      for (int j = 0; j < 16; ++j) {
        if (qk_i(j) != ((u >> 1) & 3)) continue;
#pragma unroll
        for (int t = 1; t >= 0; --t) {
          const v4u o = in[qk_r(j)][t];
          if (u >= 8 && t == 1) sts16(c.L, lw + j * 2048 + (u & 1) * 1024, o);
          else {
            v8u& d = u < 8 ? wa[2 * j + t] : wh[j];
#pragma unroll
            for (int r = 0; r < 4; ++r) d[4 * (u & 1) + r] = o[r];
#define M_(r) asm volatile("" : "+" r(d))
            if (u & 1) { if (u < 8) AT_ACC(M_, 2 * j + t); else AT_HOME(M_, j); }
#undef M_
          }
        }
      }
      BAR();
    });
  }
  STAMP(0);
  BAR();
  const unsigned vote = wait_verdict(c);
  const int par = vote_b(vote), para = vote_a(vote);
  const int lane = fresh_lane(), g = lane >> 4, cc = lane & 15;
  const int moff = opq(amap(w * 4096 + lane * 16, par) + (h & 1) * 1024);
  const int idx = opq((lane & 1) ? 0xEEEE : 0x4444);
  TICK_DECL;
  const int l0 = opq(WL + w * 32768 + lane * 16), l1 = opq(MEMS + lane * 16);
  const int qw = opq(QKX + (2 * w + (cc >> 3)) * 256 + g * 64 + (cc & 1) * 16 + ((cc >> 1) & 3) * 4), kw = opq(QKX + (8 + 2 * g) * QROW + (32 * w + 2 * cc) * 2);
#define QK_PINS PIN_ACC0(wa); PIN_ACC1(wa); PIN_HOME(wh)
#define QK_IN [l0] "v"(l0), [l1] "v"(l1), [mo] "v"(moff), [ix] "v"(idx), [qw] "v"(qw), [kw] "v"(kw), [rs] "s"(c.rs), [sb] "s"(R_MEM + (s & 3) * MEM_SLOT)
#define QK_CL CL_S, CL_16_32, CL_160_200, CL_200_216, CL_216_256
  if (w == 3) {
    const int hq = opq(lane ? 1 << 30 : amap(24576 + h * 128, par));
    anchor(k);
    unsigned tg = k.cyc;
    Plan tt = {k.g - NS(100000), NS(T_DX)};
    for (int s = 0; s < NSEG; ++s) {
      const int pb = R_P + (s & 3) * P_SLOT;
      QK_PINS;
      asm volatile(QK_STEP3 : [bd] "+s"(c.budget) : QK_IN, [hq] "v"(hq), [pb] "s"(pb), [tg] "s"(tg), [ca] "s"(k.cyc - T_DQ), [k1] "s"(k.k1), [ga] "s"(k.g) : QK_CL);
      QK_PINS;
      tg = next_go(c, k, tt, par, pb, ask_stamps(c, par, pb));
      st4s(c.rs, hq, R_P + ((s + 2) & 3) * P_SLOT, fresh_sent());
      st4s(mkrs((const void*)c.ctrl, CTRL_BYTES), hq & (1 << 30), C_FLAG + (F_S + h) * 128, (unsigned)s + 1u);
      TICK_SUM(10);
    }
  } else {
    const int kx0 = opq(KXL + g * 512 + cc * 16), kmrd = opq(QKX + g * 16 + (8 + (cc & 7)) * QROW);
    unsigned seen = NOFLAG;
    if (w == 0) {
      const int prow = (2 * g + (cc & 1)) * 2 + ((cc >> 1) & 1);
      const int px = opq(amap((h >> 1) * 1024 + ((2 * (h & 1) + (cc >> 3)) * 16 + prow) * 16 + ((cc >> 2) & 1) * 8, par));
      const int pm = opq(cc < 8 ? amap(16384 + h * 128 + prow * 8 + (cc >> 2) * 4, par) : 1 << 30);
      const int ps = opq((cc & 1) ? 0x03020706 : 0x05040100);
      for (int s = 0; s < NSEG; ++s) {
        if (seen < (unsigned)s + 1u) seen = wait_flag(c, F_K + h, s + 1);
        QK_PINS;
        asm volatile(QK_STEP0 : [bd] "+s"(c.budget)
                     : QK_IN, [kx] "v"(kx0), [km] "v"(kmrd), [px] "v"(px), [pm] "v"(pm), [ps] "v"(ps), [pb] "s"(R_P + (s & 3) * P_SLOT), [pr] "s"(R_P + ((s + 1) & 3) * P_SLOT),
                       [kb] "s"(R_KX - MEMS + xblk(s, h, para)) : QK_CL);
        QK_PINS;
        TICK_SUM(10);
      }
    } else {
      const int qt = w - 1;
      const int px = opq(amap(qt ? 16384 + h * 1024 + lane * 16 : h * 512 + (g >> 1) * 256 + cc * 16 + (g & 1) * 8, para));
      const int pm = opq(g < 2 ? amap(49152 + (qt * 32 + h) * 256 + cc * 16 + g * 8, para) : 1 << 30);
      const int ps = opq(cc - 4 * g), hq = opq(g * 512 + (16 * qt + cc) * 16);
#define QK_TOK(STEP) for (int s = 0; s < NSEG; ++s) { QK_PINS; \
        asm volatile(STEP : [bd] "+s"(c.budget), [sq] "+s"(seen) \
                     : QK_IN, [kx] "v"(kx0), [km] "v"(kmrd), [px] "v"(px), [pm] "v"(pm), [ps] "v"(ps), [hq] "v"(hq), [qb] "s"(R_QX + xblk(s, h, para)), [tb] "s"(R_PT + (s & 7) * PT_SLOT), \
                       [tr] "s"(R_PT + ((s + 3) & 7) * PT_SLOT), [sn] "s"(s + 1), [fq] "s"(c.ctrl + (C_FLAG + (F_Q + h) * 128) / 4) : QK_CL); \
        QK_PINS; TICK_SUM(10); }
      if (w == 1) { QK_TOK(QK_STEP1) } else { QK_TOK(QK_STEP2) }
    }
  }
  TICK_OUT();
  if (c.budget == 0u) __hip_atomic_store(c.ctrl + C_TMO / 4, 1u, RLX_AGENT);
}

DEV void shuffle_cols(Ctx& c, int scratch, const v4u (&rows)[8], v4u (&o)[8]) {
  const int g = c.lane >> 4, cc = c.lane & 15;
#pragma unroll
  for (int i = 0; i < 8; ++i) {
    v4u f;
#pragma unroll
    for (int r = 0; r < 4; ++r) f[r] = prm(rows[2 * r + 1][i >> 1], rows[2 * r][i >> 1], i & 1);
    sts16(c.L, scratch + (8 * cc + i) * 64 + ((g ^ (cc & 3)) << 4), f);
  }
  asm volatile("s_waitcnt lgkmcnt(0)" ::: "memory");
#pragma unroll
  for (int i = 0; i < 8; ++i) { const int col = 16 * i + cc; o[i] = lds16(c.L, scratch + col * 64 + ((g ^ ((col >> 3) & 3)) << 4)); }
  asm volatile("s_waitcnt lgkmcnt(0)" ::: "memory");
}

DEV void vo_role(const Params& p, Ctx& c, int h) {
  constexpr int WL = 0, MEMS = 131072, VM = 147456, VROW = 272;
  c.budget = SPIN_CAP;
  const int w = c.w, lane = c.lane = fresh_lane(), g = lane >> 4, cc = lane & 15;
  Clk k = {0u, 0u, 0u, 0u};
  anchor(k);
  v8u wa[32], wh[16];
  claim(c, 0);
  hold(c, VO_AT);
  const rsrc_t rwo = mkrs(p.Wo, 4096 * 1024 * 2);
  const int voo = (h * 128 + 4 * g) * 2048 + (256 * w + 8 * cc) * 2;
  v4u buf[3][8];
  auto request_o = [&](int u) {
#pragma unroll
    for (int r = 0; r < 8; ++r) buf[u % 3][r] = ld16w(rwo, voo, (32 * (u & 3) + 16 * (r >> 2) + (r & 3)) * 2048 + (u >> 2) * 256);
  };
  {
    const rsrc_t rw = mkrs(p.Wv, 1024 * 4096 * 2);
    const int vo = (256 * w + krow_lane(g)) * 8192 + (h * 128 + 8 * cc) * 2;
    v4u vrow[8][8];
    auto request = [&](int u) {
#pragma unroll
      for (int r = 0; r < 8; ++r) vrow[u][r] = ld16w(rw, vo, (64 * ((u >> 1) ^ (h & 1)) + krow(4 * (u & 1) + (r >> 1), r & 1)) * 8192);
    };
#pragma unroll
    for (int u = 0; u < 5; ++u) request(u);
    SCHED();
    const int wr_ = MEMS + w * 8192 + g * 256;
    int sr[2];
#pragma unroll
    for (int t = 0; t < 2; ++t) { const int i = (2 * cc + t) & 7; sr[t] = opq(i * 1024 + g * 256 + ((4 * w + (cc >> 2) + 2 * i) & 15) * 16); }
#pragma unroll
    for (int u = 0; u < 8; ++u) {
#pragma unroll
      for (int i = 0; i < 8; ++i) {
        v4u f;
#pragma unroll
        for (int r = 0; r < 4; ++r) f[r] = prm(vrow[u][2 * r + 1][i >> 1], vrow[u][2 * r][i >> 1], i & 1);
        sts16(c.L, wr_ + i * 1024 + ((cc + 2 * i) & 15) * 16, f);
      }
      SCHED();
      if (u + 5 < 8) request(u + 5); else if (u < 6) request_o(u - 3);
      BAR();
      v4u in[4][2];
#pragma unroll
      for (int t = 1; t >= 0; --t)
#pragma unroll
        for (int r = 0; r < 4; ++r) in[r][t] = lds16(c.L, sr[t] + MEMS + ((w + r) & 3) * 8192);
      SCHED();
#pragma unroll
      for (int j = 0; j < 16; ++j) {
        if (qk_i(j) != (u >> 1)) continue;
#pragma unroll
        for (int t = 1; t >= 0; --t) {
          const v4u o = in[qk_r(j)][t];
          if (t == 1 && (j & 1)) sts16(c.L, WL + (w * 16 + (j >> 1)) * 2048 + (u & 1) * 1024 + lane * 16, o);
          else {
            const int n = 3 * (j >> 1) + ((j & 1) ? 2 : t);
#pragma unroll
            for (int r = 0; r < 4; ++r) wa[n][4 * (u & 1) + r] = o[r];
#define M_(r) asm volatile("" : "+" r(wa[n]))
            if (u & 1) AT_ACC(M_, n);
#undef M_
          }
        }
      }
      BAR();
    }
  }
#pragma unroll
  for (int u = 0; u < 8; ++u) {
    v4u o[8];
    shuffle_cols(c, MEMS + w * 8192, buf[u % 3], o);
    SCHED();
    if (u + 3 < 8) request_o(u + 3);
#pragma unroll
    for (int i = 0; i < 4; ++i) {
      const int pr = 4 * (u >> 2) + i, ch = u & 3;
      const v8u f = {o[2 * i][0], o[2 * i + 1][0], o[2 * i][1], o[2 * i + 1][1], o[2 * i][2], o[2 * i + 1][2], o[2 * i][3], o[2 * i + 1][3]};
      if (ch < 2) {
        wh[2 * pr + ch] = f;
#define M_(r) asm volatile("" : "+" r(wh[2 * pr + ch]))
        AT_HOME(M_, 2 * pr + ch);
#undef M_
      } else if (ch == 2) {
        wa[24 + pr] = f;
#define M_(r) asm volatile("" : "+" r(wa[24 + pr]))
        AT_ACC(M_, 24 + pr);
#undef M_
      } else {
        sts16(c.L, WL + (w * 16 + 8 + pr) * 2048 + lane * 16, lo4(f));
        sts16(c.L, WL + (w * 16 + 8 + pr) * 2048 + 1024 + lane * 16, hi4(f));
      }
    }
    SCHED();
  }
  BAR();
  STAMP(0);
  const unsigned vote = wait_verdict(c);
  const int par = vote_b(vote), para = vote_a(vote);
  const int moff = opq(amap(w * 4096 + lane * 16, par) + (h & 1) * 1024);
  const int idx = opq((lane & 1) ? 0xEEEE : 0x4444);
  const int u_off = opq((16 * w + g) * 16384 + h * 256 + cc * 16);
  const int hq = opq((lane | w) ? 1 << 30 : amap(24576 + (32 + h) * 128, par));
  const int l0 = opq(WL + w * 32768 + lane * 16), l1 = opq(MEMS + lane * 16);
  const int vmw = opq(VM + 2 * g * VROW + (32 * w + 2 * cc) * 2);
  const int vmr = opq(VM + (cc >> 1) * VROW + g * 16);
  TICK_DECL;
  anchor(k);
  unsigned tg = k.cyc;
  Plan tt = {k.g - NS(100000), NS(T_DX)};
  int u3 = 0;
  for (int s = 0; s < NSEG; ++s) {
    const int pb = R_P + (s & 3) * P_SLOT;
    PIN_ACC0(wa); PIN_ACC1(wa); PIN_HOME(wh);
    asm volatile(VO_STEP
        : [bd] "+s"(c.budget)
        : [l0] "v"(l0), [l1] "v"(l1), [mo] "v"(moff), [ix] "v"(idx), [vw] "v"(vmw), [vr] "v"(vmr), [uo] "v"(u_off), [w] "s"(w), [rs] "s"(c.rs),
          [sb] "s"(R_MEM + (s & 3) * MEM_SLOT), [ub] "s"(u_slot(u3, par, para)), [tg] "s"(tg), [hq] "v"(hq), [pb] "s"(pb),
          [ca] "s"(k.cyc - T_DV), [k1] "s"(k.k1), [ga] "s"(k.g)
        : CL_S, CL_16_32, CL_160_200, CL_200_216, CL_216_256);
    PIN_ACC0(wa); PIN_ACC1(wa); PIN_HOME(wh);
    if (w == 0) tg = next_go(c, k, tt, par, pb, ask_stamps(c, par, pb)); else __builtin_amdgcn_s_sleep(T_RST);
    {
      const int ur = u_slot(u3 ? u3 - 1 : 2, par, para);
      const v4u se = {fresh_sent(), fresh_sent(), fresh_sent(), fresh_sent()};
#pragma unroll
      for (int i = 0; i < 4; ++i) st16s(c.rs, u_off, ur + i * 65536, se);
      st4s(c.rs, hq, R_P + ((s + 2) & 3) * P_SLOT, se[0]);
      u3 = u3 == 2 ? 0 : u3 + 1;
    }
    TICK_SUM(10);
  }
  TICK_OUT();
  if (c.budget == 0u) __hip_atomic_store(c.ctrl + C_TMO / 4, 1u, RLX_AGENT);
}

DEV void y_role(const Params& p, Ctx& c, int y) {
  constexpr int ZR = 0, UL = 131072, CW = 155648;
  c.budget = SPIN_CAP;
  const int w = c.w, lane = c.lane = fresh_lane(), g = lane >> 4, cc = lane & 15;
  const v4u sent4 = {SENT, SENT, SENT, SENT};
  STAMP(2);
  Clk k = {0u, 0u, 0u, 0u};
  anchor(k);
  unsigned m0 = 0;
  if (w == 0) {
    unsigned best[2] = {~0u, ~0u};
    st4s(c.rs, y * 16384 + (lane >> 5) * 8192 + (lane & 31) * 4, R_U, SENT);
#pragma unroll 1
    for (int n = 0; n < 12; ++n) {
#pragma unroll
      for (int r = 0; r < 2; ++r) {
        VMWAIT(0);
        const unsigned t0 = (unsigned)__builtin_amdgcn_s_memtime();
        LGKM(0);
        st4s(c.rs, y * 16384 + (lane & 31) * 4, R_U + r * 8192, SENT);
        VMWAIT(0);
        const unsigned t1 = (unsigned)__builtin_amdgcn_s_memtime();
        LGKM(0);
        best[r] = min(best[r], t1 - t0);
      }
      if (n + 1 >= PROBES && min(best[0], best[1]) < 600u && max(best[0], best[1]) > min(best[0], best[1]) + 128u) break;
    }
    STAMP(5);
    claim(c, CL_VOTED | (best[1] < best[0] ? CL_A : 0u));
    m0 = *(const GAS unsigned*)(p.mem0 + (lane >> 3) * 1024 + 16 * y + 2 * (lane & 7));
    m0 = m0 < SENT - 1u ? m0 : SENT - 1u;
    if (lane < 8) *(LAS unsigned*)(c.L + CW + lane * 4) = 0u;
    const unsigned v = wait_vote(c);
    STAMP(7);
    const int oa = y * 16384 + vote_a(v) * 8192 + lane * 16, ob = amap(y * 3072 + lane * 16, vote_b(v));
#pragma unroll
    for (int i = 0; i < 16; ++i) st16s(c.rs, oa + (i & 3) * 1024, R_U + (i >> 3) * MB + ((i >> 2) & 1) * 4096, sent4);
#pragma unroll
    for (int i = 0; i < 3; ++i) st16s(c.rs, amap(y * 3072 + i * 1024 + lane * 16, vote_b(v)), R_MEM, sent4);
#pragma unroll
    for (int i = 0; i < 8; ++i) st16s(c.rs, oa + (i & 3) * 1024, R_PT + (i >> 2) * 4096, sent4);
    (void)ob;
    VMWAIT(0);
    if (lane == 0) __hip_atomic_fetch_add(c.ctrl + (C_CLAIM + c.att * 1024) / 4 + c.slot, CL_FILLED, RLX_AGENT);
    STAMP(6);
  }
  BAR();
  const unsigned vote = wait_verdict(c);
  const int par = vote_b(vote), para = vote_a(vote);
  const int mo = amap(y * 256 + lane * 4, par);
  if (w == 0) st4s(c.rs, mo, R_MEM, m0);
  STAMP(0);
  volatile LAS unsigned* const GO = (volatile LAS unsigned*)(c.L + CW);
  TICK_DECL;
  if (w == 0) {
    int xo[4];
#pragma unroll
    for (int i = 0; i < 4; ++i) xo[i] = amap(i * 4096 + lane * 16, par);
    const int pmo = amap(16384 + 2 * g * 128 + cc * 8, par), psel = (cc & 1) ? 0x03020706 : 0x05040100;
    const int lead = T_U0 - 128 * ((y >> 2) & 3), late = __builtin_amdgcn_readfirstlane(opq(lead < 264));
    anchor(k);
    Plan tt = {k.g - NS(100000), NS(T_DX)};
    unsigned done = k.cyc;
    const int so = amap(24576, par) + lane * 128;
    const int idx = opq((lane & 1) ? 0xEEEE : 0x4444);
    const int uo = y * 16384 + lane * 16;
    int u3 = 0;
    const int mw = amap(y * 256 + (2 * g + (cc & 1)) * 32 + (cc >> 1) * 4, par);
    for (int s = 0; s < NSEG - 1; ++s) {
      if (s == 0) RAWBAR();
      v32u z0, z1, z2, z3;
      asm volatile(
          "ds_read_b128 a[0:3], %[za] offset:0\n\t" "ds_read_b128 a[4:7], %[za] offset:1024\n\t" "ds_read_b128 a[8:11], %[za] offset:2048\n\t" "ds_read_b128 a[12:15], %[za] offset:3072\n\t"
          "ds_read_b128 a[16:19], %[za] offset:4096\n\t" "ds_read_b128 a[20:23], %[za] offset:5120\n\t" "ds_read_b128 a[24:27], %[za] offset:6144\n\t" "ds_read_b128 a[28:31], %[za] offset:7168\n\t"
          "ds_read_b128 a[32:35], %[za] offset:8192\n\t" "ds_read_b128 a[36:39], %[za] offset:9216\n\t" "ds_read_b128 a[40:43], %[za] offset:10240\n\t" "ds_read_b128 a[44:47], %[za] offset:11264\n\t"
          "ds_read_b128 a[48:51], %[za] offset:12288\n\t" "ds_read_b128 a[52:55], %[za] offset:13312\n\t" "ds_read_b128 a[56:59], %[za] offset:14336\n\t" "ds_read_b128 a[60:63], %[za] offset:15360\n\t"
          "ds_read_b128 a[64:67], %[za] offset:16384\n\t" "ds_read_b128 a[68:71], %[za] offset:17408\n\t" "ds_read_b128 a[72:75], %[za] offset:18432\n\t" "ds_read_b128 a[76:79], %[za] offset:19456\n\t"
          "ds_read_b128 a[80:83], %[za] offset:20480\n\t" "ds_read_b128 a[84:87], %[za] offset:21504\n\t" "ds_read_b128 a[88:91], %[za] offset:22528\n\t" "ds_read_b128 a[92:95], %[za] offset:23552\n\t"
          "ds_read_b128 a[96:99], %[za] offset:24576\n\t" "ds_read_b128 a[100:103], %[za] offset:25600\n\t" "ds_read_b128 a[104:107], %[za] offset:26624\n\t" "ds_read_b128 a[108:111], %[za] offset:27648\n\t"
          "ds_read_b128 a[112:115], %[za] offset:28672\n\t" "ds_read_b128 a[116:119], %[za] offset:29696\n\t" "ds_read_b128 a[120:123], %[za] offset:30720\n\t" "ds_read_b128 a[124:127], %[za] offset:31744\n\t"
          "s_waitcnt lgkmcnt(0)"
          : "={a[0:31]}"(z0), "={a[32:63]}"(z1), "={a[64:95]}"(z2), "={a[96:127]}"(z3) : [za] "v"(ZR + (s & 3) * 32768 + lane * 16) : "memory");
      const int pb = R_P + (s & 3) * P_SLOT;
      v32u uu;
      const int ub = u_slot(u3, par, para);
      TICK(0);
      anchor(k);
      TICK(8);
      unsigned tg = to_cyc(k, tt.gx + tt.dx + NS(T_M1)), lt = late, sv = SENT;
      unsigned bad = (int)(done - to_cyc(k, tt.gx)) > T_OVH;
      TICK(4);
#pragma unroll 1
      for (;;) {
        if (bad) {
          const bool all = allok(sv);
          const unsigned last = latest(c, k, par, pb, sv);
          if (!all) anchor(k);
          tg = to_cyc(k, second_try(tt, last));
          lt = 1;
        }
        asm volatile(Y_STEP
          : "=&{v[144:175]}"(uu), [bad] "=&s"(bad), [sv] "=&v"(sv)
          : [x0] "v"(xo[0]), [x1] "v"(xo[1]), [x2] "v"(xo[2]), [x3] "v"(xo[3]), [pm] "v"(pmo), [u0] "v"(uo), [u1] "v"(uo + 4096), [mw] "v"(mw), [ps] "v"(psel), [ix] "v"(idx),
            [rs] "s"(c.rs), [pb] "s"(pb), [ub] "s"(ub), [mb] "s"(R_MEM + ((s + 1) & 3) * MEM_SLOT), [mr] "s"(R_MEM + ((s + 3) & 3) * MEM_SLOT), [tu] "s"(tg - (lt ? 128 : lead)), [tg] "s"(tg), [late] "s"(lt),
            [so] "v"(so), [bd] "s"(c.budget),
            "{a[0:31]}"(z0), "{a[32:63]}"(z1), "{a[64:95]}"(z2), "{a[96:127]}"(z3)
          : CL_S, CL_120_128, CL_176_200, CL_200_216, CL_216_256);
        bad = __builtin_amdgcn_readfirstlane(bad);
        if (bad != 1u) break;
        spend(c);
      }
      done = bad;
      SCHED();
      const int ul = opq(UL + lane * 16);
      u3 = u3 == 2 ? 0 : u3 + 1;
#pragma unroll
      for (int i = 0; i < 8; ++i) sts16(c.L, ul + i * 1024, v4u{uu[4 * i], uu[4 * i + 1], uu[4 * i + 2], uu[4 * i + 3]});
      asm volatile("" ::: "memory");
      *GO = s + 1;
      asm volatile("s_waitcnt vmcnt(2)" : "+v"(sv));
      advance(tt, latest(c, k, par, pb, sv));
      TICK(3);
      TICK_LDS(CW + 256);
    }
    {
      anchor(k);
      wait_cyc(to_cyc(k, tt.gx + tt.dx + NS(T_M1)));
      const int ub = u_slot(u3, par, para);
      v4u u[8];
#pragma unroll 1
      for (;;) {
        asm volatile("" ::: "memory");
#pragma unroll
        for (int i = 0; i < 8; ++i) u[i] = ld16s(c.rs, uo + (i & 3) * 1024, ub + (i >> 2) * 4096);
        SCHED();
        unsigned t = 0;
#pragma unroll
        for (int i = 0; i < 8; ++i) t = umax(t, top(u[i]));
        if (allok(t) || spend(c)) break;
      }
      RAWBAR();
#pragma unroll
      for (int i = 0; i < 8; ++i) sts16(c.L, UL + i * 1024 + lane * 16, u[i]);
      asm volatile("" ::: "memory");
      *GO = NSEG;
    }
    TICK_DUMP(CW + 256);
  } else if (w < 3) {
    const rsrc_t ro = mkrs(p.out, 2048 * 2048 * 2);
    auto tokens = [&](auto QT) {
      constexpr int qt = QT.value, NX = qt ? 32 : 16;
      RAWBAR();
      for (int s = 0; s < NSEG; ++s) {
        const int tb = R_PT + (s & 7) * PT_SLOT + para * 8192;
        const int ua = opq(UL + lane * 16);
        const int za = opq(ZR + (s & 3) * 32768 + (qt ? lane * 16 : (g >> 1) * 1024 + (g & 1) * 512 + cc * 16));
        SCHED();
        RAWBAR();
        TICK(0);
        f32x4 a;
#pragma unroll 1
        for (;;) {
          asm volatile("" ::: "memory");
          v4u pt[NX + 8];
#pragma unroll
          for (int i = 0; i < NX; ++i) pt[i] = ld16s(c.rs, (i & 3) * 1024 + lane * 16, tb + amap(qt * 16384 + (i >> 2) * 4096, 0));
#pragma unroll
          for (int i = 0; i < 8; ++i) pt[NX + i] = ld16s(c.rs, (i & 3) * 1024 + lane * 16, tb + amap(49152 + qt * 8192 + (i >> 2) * 4096, 0));
          SCHED();
          TICK(1);
#pragma unroll 1
          while (__builtin_amdgcn_readfirstlane(*GO) < (unsigned)s + 1u) { if (spend(c)) break; }
          asm volatile("" ::: "memory");
          f32x4 d[2] = {f32x4{0.f, 0.f, 0.f, 0.f}, f32x4{0.f, 0.f, 0.f, 0.f}};
          v4u fa[8];
          auto frag = [&](int i) {
            if (i >= NX) fa[i & 7] = lds16(c.L, ua + (i - NX) * 1024);
            else if (qt) fa[i & 7] = lds16(c.L, za + i * 1024);
            else {
              const v2u lo = *(LAS v2u*)(c.L + za + i * 2048), hi = *(LAS v2u*)(c.L + za + i * 2048 + 256);
              fa[i & 7] = v4u{lo.x, lo.y, hi.x, hi.y};
            }
          };
#pragma unroll
          for (int i = 0; i < 7; ++i) frag(i);
          SCHED();
#pragma unroll
          for (int i = 0; i < NX + 8; ++i) {
            if (i + 7 < NX + 8) frag(i + 7);
            d[i & 1] = mfma16(fa[i & 7], pt[i], d[i & 1]);
            SCHED();
          }
          a = d[0] + d[1];
          if (!__any(a[0] != a[0])) break;
          unsigned t = 0;
#pragma unroll
          for (int i = 0; i < NX + 8; ++i) t = umax(t, top(pt[i]));
          if (allok(t) || spend(c)) break;
        }
        __builtin_amdgcn_raw_buffer_store_b64(v2u{pk(a[0], a[1]), pk(a[2], a[3])}, ro, (16 * qt + cc) * 4096 + (16 * y + 4 * g) * 2, s * 32 * 4096, AUX_NT);
        TICK(2);
        TICK_SUM(3);
      }
    };
    if (w == 1) tokens(IC<0>{}); else tokens(IC<1>{});
  } else {
    unsigned seenZ = NOFLAG;
    const rsrc_t rc = mkrs((const void*)c.ctrl, CTRL_BYTES);
    const int fo = lane < 32 ? lane * 128 : 1 << 30;
    __builtin_amdgcn_s_setprio(3);
    auto least = [&](unsigned v) {
      v |= lane < 32 ? 0u : ~0u;
#pragma unroll
      for (int o = 1; o < 32; o <<= 1) v = min(v, (unsigned)__shfl_xor((int)v, o));
      seenZ = umax(seenZ, __builtin_amdgcn_readfirstlane(v));
    };
    auto await = [&](unsigned need) {
#pragma unroll 1
      while (seenZ < need) {
        asm volatile("" ::: "memory");
        least(__builtin_amdgcn_raw_buffer_load_b32(rc, fo, C_FLAG + (F_Z + (y >> 4) * 32) * 128, AUX_SC1));
        if (seenZ >= need || spend(c)) break;
        __builtin_amdgcn_s_sleep(4);
      }
      __builtin_amdgcn_fence(__ATOMIC_ACQUIRE, "wavefront");
    };
    for (int n = 0; n < NSEG; ++n) {
      TICK(0);
      await(n + 1);
      TICK(1);
      v4u z[32];
#pragma unroll
      for (int i = 0; i < 32; ++i) {
        z[i] = ld16s(c.rs, (i & 3) * 1024 + lane * 16, R_Z + (n & (ZRING - 1)) * Z_SLOT + y * 65536 + ((para ^ (y >> 5)) & 1) * 8192 + amap((i >> 2) * 4096, 0));
        SCHED();
        asm volatile("s_nop " STR(Z_GAP));
        SCHED();
      }
      const unsigned look = __builtin_amdgcn_raw_buffer_load_b32(rc, seenZ < (unsigned)n + 2u ? fo : 1 << 30, C_FLAG + (F_Z + (y >> 4) * 32) * 128, AUX_SC1);
      SCHED();
      TICK(4);
      const int za = opq(ZR + (n & 3) * 32768 + lane * 16);
#pragma unroll
      for (int i = 0; i < 32; ++i) {
        sts16(c.L, za + i * 1024, z[i]);
        if (i == 30) { TICK(3); }
      }
      least(look);
      if (n == 0) await(PRIME);
      TICK(2);
      { const int s = n; (void)s; TICK_SUM(3); }
      BAR();
    }
    RAWBAR();
  }
  TICK_OUT();
  if (c.budget == 0u) __hip_atomic_store(c.ctrl + C_TMO / 4, 1u, RLX_AGENT);
}

#define MF1A(d, a, b) asm volatile(MF16 "%0, %1, %2, %0" : "+v"(d) : "a"(a), "v"(b))
#define MF32A(d, a, b) asm volatile("v_mfma_f32_32x32x16_bf16 %0, %1, %2, %0" : "+v"(d) : "a"(a), "v"(b))
#define MF32AZ(d, a, b) asm volatile("v_mfma_f32_32x32x16_bf16 %0, %1, %2, 0" : "=&v"(d) : "a"(a), "v"(b))
#define MF1B(d, a, b) asm volatile(MF16 "%0, %1, %2, %0" : "+v"(d) : "v"(a), "a"(b))
#define MF1BZ(d, a, b) asm volatile(MF16 "%0, %1, %2, 0" : "=&v"(d) : "v"(a), "a"(b))
#define MF1AZ(d, a, b) asm volatile(MF16 "%0, %1, %2, 0" : "=&v"(d) : "a"(a), "v"(b))
DEV void g_role(const Params& p, Ctx& c, int mat, int h, int gi) {
  constexpr int EX = 131072;
  c.budget = SPIN_CAP;
  const int w = c.w, lane = c.lane = fresh_lane(), g = lane >> 4, cc = lane & 15;
  if (mat < 2) {
    unsigned t = 0;
#pragma unroll
    for (int i = 0; i < 3; ++i) t += __builtin_amdgcn_raw_buffer_load_b32(mkrs(p.Wv, 8 * MB), (256 * w + (lane >> 1)) * 8192 + h * 256 + (lane & 1) * 128, (2 + 2 * i + mat) * 32 * 8192, 0);
    if (mat == 0) {
      hold(c, X_AT);
#pragma unroll
      for (int i = 0; i < 4; ++i) t += __builtin_amdgcn_raw_buffer_load_b32(mkrs(p.x, 4 * MB), ((4 * h + w) * 64 + lane) * 128, i * MB, 0);
    }
    asm volatile("" :: "v"(t));
    claim(c, 0);
    hold(c, (mat ? GK_AT : GQ_AT) - (slow_head(p.Wq, h) ? SLOW_LEAD : 0));
  }
  const rsrc_t rx = mkrs(p.x, 2048 * 2048);
  const int xs = w * 16384 + ((lane * 16) ^ ((w & 1) << 7));

  v32u wa[8];
  {
    const rsrc_t rw = mkrs(mat == 0 ? p.Wq : mat == 1 ? p.Wk : p.Wv, 1024 * 4096 * 2);
    const int vo = (256 * w + 8 * g) * 8192 + (h * 128 + 8 * cc) * 2;
    v4u row[8][8];
    auto request = [&](int u) {
#pragma unroll
      for (int r = 0; r < 8; ++r) row[u][r] = ld16w(rw, vo, (32 * u + r) * 8192);
    };
#pragma unroll
    for (int u = 0; u < 2; ++u) request(u);
#pragma unroll
    for (int u = 0; u < 16; ++u) dma16(rx, c.L + w * 16384 + u * 1024, (xs ^ ((u >> 1) << 4)) + u * 1024);
#pragma unroll
    for (int u = 2; u < 6; ++u) request(u);
    SCHED();
    const int sw_ = EX + w * 8192 + g * 256;
    const int sr0 = opq(EX + (cc & 3) * 1024 + g * 256 + ((4 * w + (cc >> 2) + 4 * cc) & 15) * 16), sr1 = sr0 + 4096;
#pragma unroll
    for (int u = 0; u < 8; ++u) {
#pragma unroll
      for (int i = 0; i < 8; ++i) {
        v4u f;
#pragma unroll
        for (int r = 0; r < 4; ++r) f[r] = prm(row[u][2 * r + 1][i >> 1], row[u][2 * r][i >> 1], i & 1);
        sts16(c.L, sw_ + i * 1024 + ((cc + 4 * i) & 15) * 16, f);
      }
      SCHED();
      if (u + 6 < 8) request(u + 6);
      if (u == 7 && mat == 2) claim(c, 0);
      BAR();
      v8u in[4];
#pragma unroll
      for (int q = 0; q < 4; ++q) in[q] = cat(lds16(c.L, sr0 + q * 8192), lds16(c.L, sr1 + q * 8192));
      SCHED();
#pragma unroll
      for (int q = 0; q < 4; ++q) {
        v8u f = in[q];
#define M_(r) asm volatile("" : "+" r(f))
        AT_ACC(M_, 8 * q + u);
#undef M_
#pragma unroll
        for (int r = 0; r < 8; ++r) wa[2 * q + (u >> 2)][8 * (u & 3) + r] = f[r];
      }
      BAR();
    }
  }
  STAMP(0);
  const int xpar = vote_a(wait_vote(c)) ^ (mat >> 1);
  TICK_DECL;
  asm volatile(G_LOOP
      :: [xo] "v"(cc * 2048 + ((g ^ cc) << 4)), [xs] "v"(xs), [so] "v"((4 * w + g) * 512 + cc * 16), [fo] "v"((w | lane) ? 1 << 30 : 0),
         [rs] "s"(c.rs), [rx] "s"(rx), [rc] "s"(mkrs((const void*)c.ctrl, CTRL_BYTES)), [fl] "s"(C_FLAG + (mat * 32 + h) * 128), [xp] "s"(xpar),
         [ob] "s"((mat == 1 ? R_KX : R_QX) + h * 16384), [lw] "s"(w * 16384), GTRACE ACC_IN(wa)
      : CL_S, CL_16_32, CL_32_100);
}

DEV void zc_role(const Params& p, Ctx& c, int h) {
  c.budget = SPIN_CAP;
  const int w = c.w, lane = c.lane = fresh_lane(), g = lane >> 4, cc = lane & 15, h2 = lane >> 5, r32 = lane & 31;
  v32u wa[8];
  hold(c, ZC_AT);
  {
    const rsrc_t rw = mkrs(p.Wo, 4096 * 1024 * 2);
    const int vo = (h * 128 + 8 * g) * 2048 + (256 * w + 8 * cc) * 2;
    v4u row[8][8];
    auto request = [&](int u) {
#pragma unroll
      for (int r = 0; r < 8; ++r) row[u][r] = ld16w(rw, vo, (32 * (u & 3) + r) * 2048 + (u >> 2) * 256);
    };
#pragma unroll
    for (int u = 0; u < 2; ++u) request(u);
    SCHED();
    claim(c, 0);
    SCHED();
#pragma unroll
    for (int u = 2; u < 6; ++u) request(u);
    const int sw_ = w * 8192 + g * 256, sr_ = w * 8192 + (cc & 7) * 1024 + g * 256;
    v8u two[8];
#pragma unroll
    for (int u = 0; u < 8; ++u) {
#pragma unroll
      for (int i = 0; i < 8; ++i) {
        v4u f;
#pragma unroll
        for (int r = 0; r < 4; ++r) f[r] = prm(row[u][2 * r + 1][i >> 1], row[u][2 * r][i >> 1], i & 1);
        sts16(c.L, sw_ + i * 1024 + ((cc + 2 * i) & 15) * 16, f);
      }
      SCHED();
      if (u + 6 < 8) request(u + 6);
      LGKM(0);
#pragma unroll
      for (int q = 0; q < 8; ++q) {
        const v4u o = lds16(c.L, sr_ + ((2 * q + (cc >> 3) + 2 * cc) & 15) * 16);
#pragma unroll
        for (int r = 0; r < 4; ++r) two[q][4 * (u & 1) + r] = o[r];
      }
      SCHED();
      if (u & 1) {
#pragma unroll
        for (int q = 0; q < 8; ++q) {
          const int n = 2 * (8 * (u >> 2) + q) + ((u >> 1) & 1);
#define M_(r) asm volatile("" : "+" r(two[q]))
          AT_ACC(M_, n);
#undef M_
#pragma unroll
          for (int r = 0; r < 8; ++r) wa[n >> 2][8 * (n & 3) + r] = two[q][r];
        }
      }
      LGKM(0);
      SCHED();
    }
  }
  STAMP(0);
  BAR();
  const int para = vote_a(wait_vote(c));
  const int ao = g * 512 + cc * 16, zo = 16 * w * 65536 + lane * 16 + ((para ^ (w >> 1)) & 1) * 8192 + amap(h * 1024, 0);
  TICK_DECL;
  asm volatile(ZC_LOOP
      : [bd] "+s"(c.budget)
      : [ao] "v"(ao), [a1] "v"(ao + 4096), [z1] "v"(zo), [z2] "v"(zo + 65536), [fo] "v"(lane ? 1 << 30 : 0),
        [rs] "s"(c.rs), [rc] "s"(mkrs((const void*)c.ctrl, CTRL_BYTES)), [fv] "s"(C_FLAG + (F_V + h) * 128), [fs] "s"(C_FLAG + (F_S + h) * 128),
        [fz] "s"(C_FLAG + (F_Z + w * 32 + h) * 128), [xp] "s"(para ^ 1), [vb] "s"(R_QX + h * 16384), [vd] "s"(C_VERDICT + c.att * 128), [z0] "n"(ZC_PRIME), [zb] "n"(R_Z), [rg] "n"(ZRING - 2), [rm] "n"(ZRING - 1), GTRACE ACC_IN(wa)
      : CL_S, CL_16_32, CL_32_100);
  if (c.budget == 0u) __hip_atomic_store(c.ctrl + C_TMO / 4, 1u, RLX_AGENT);
}

DEV void conv_role(const Params& p, Ctx& c, int cg, int far) {
  constexpr int CH = 5200, U8 = 0;
  constexpr int CP = 4160, FC = 41600;
  constexpr int YS = FC + 16 * CP, RED = YS + 32768;
  static_assert(RED + 8192 <= LDS_BYTES - 16, "lds");
  c.budget = SPIN_CAP;
  const int w = c.w, lane = c.lane = fresh_lane(), tid = w * 64 + lane;
  const int d0 = 8 * cg;
  const int pg = ((far ^ (cg >> 5)) & 1) * 8192;
  auto crow = [](int ch) { return (ch >> 1) * 16384 + (ch & 1) * 4096; };
  const int de = (cg >> 3) * 64 + (tid & 7) * 8, t0 = (cg & 7) * 256 + (tid >> 3) * 8;
  auto group_sync = [&](int phase) {
    gu32* cnt = c.ctrl + (C_CONV + (cg >> 3) * 16 + phase * 8) / 4;
    VMWAIT(0);
    BAR();
    if (w == 0) {
      if (lane == 0) __hip_atomic_fetch_add(cnt, 1u, RLX_AGENT);
#pragma unroll 1
      while (__builtin_amdgcn_readfirstlane(__hip_atomic_load(cnt, RLX_AGENT)) < 8u) { if (spend(c)) break; __builtin_amdgcn_s_sleep(4); }
    }
    BAR();
  };
  v4u kv[8];
  BAR();
  {
    const rsrc_t rk = mkrs(p.bk, 2048 * 2048), rv = mkrs(p.bv, 2048 * 2048);
    v4u kr[11], vr[8], fr[8];
#pragma unroll
    for (int i = 0; i < 11; ++i) kr[i] = ld16c(rk, (t0 + i - 3) * 2048 + de * 2, 0);
#pragma unroll
    for (int i = 0; i < 8; ++i) vr[i] = ld16c(rv, (t0 + i) * 2048 + de * 2, 0);
    v4u sh[4];
#pragma unroll
    for (int i = 0; i < 4; ++i) sh[i] = *(const GAS v4u*)(p.shift + de * 4 + i * 8);
#pragma unroll
    for (int i = 0; i < 8; ++i) fr[i] = ld16c(mkrs(p.ssmk, 4 * MB), tid * 16, (d0 + i) * 4096);
    if (tid < 40) sts16(c.L, U8 + (tid / 5) * CH + (tid % 5) * 16, v4u{0, 0, 0, 0});
    if (tid < 64) sts16(c.L, FC + (tid >> 2) * CP + 4096 + (tid & 3) * 16, v4u{0, 0, 0, 0});
#pragma unroll
    for (int ch = 0; ch < 8; ++ch) {
      const unsigned sa = sh[ch >> 1][(ch & 1) * 2], sb = sh[ch >> 1][(ch & 1) * 2 + 1];
      const float s0 = blo(sa), s1 = bhi(sa), s2 = blo(sb), s3 = bhi(sb);
      float kk[11], o[8];
#pragma unroll
      for (int i = 0; i < 11; ++i) kk[i] = (ch & 1) ? bhi(kr[i][ch >> 1]) : blo(kr[i][ch >> 1]);
#pragma unroll
      for (int i = 0; i < 8; ++i) {
        float a = rb(kk[i] * s3);
        a = rb(a + rb(kk[i + 1] * s2));
        a = rb(a + rb(kk[i + 2] * s1));
        a = rb(a + rb(kk[i + 3] * s0));
        o[i] = a * ((ch & 1) ? bhi(vr[i][ch >> 1]) : blo(vr[i][ch >> 1]));
      }
      kv[ch] = v4u{pk(o[0], o[1]), pk(o[2], o[3]), pk(o[4], o[5]), pk(o[6], o[7])};
      st16s(c.rs, de * 8192 + t0 * 2, R_KVT + pg + crow(ch), kv[ch]);
    }
#pragma unroll
    for (int i = 0; i < 8; ++i) sts16(c.L, YS + i * 4096 + tid * 16, fr[i]);
    group_sync(0);
#pragma unroll
    for (int ch = 0; ch < 8; ++ch) fr[ch] = ld16s(c.rs, tid * 16, R_KVT + pg + d0 * 8192 + crow(ch));
#pragma unroll
    for (int ch = 0; ch < 8; ++ch) sts16(c.L, U8 + ch * CH + 80 + (tid >> 2) * 80 + (tid & 3) * 16, fr[ch]);
  }
  BAR();
  STAMP(5);
  auto build = [&](int ch, int q0, int q1) {
    for (int q = q0; q < q1; ++q) {
      const int m = lane + 64 * q;
      const v4u hi = lds16(c.L, YS + ch * 4096 + (255 - m) * 16);
      v4u lo = lds16(c.L, YS + ch * 4096 + (m < 255 ? 254 - m : 0) * 16);
      if (m == 255) lo = v4u{0, 0, 0, 0};
      unsigned G[8];
#pragma unroll
      for (int i = 0; i < 4; ++i) { G[i] = __builtin_amdgcn_alignbyte(hi[3 - i], hi[3 - i], 2); G[4 + i] = __builtin_amdgcn_alignbyte(lo[3 - i], lo[3 - i], 2); }
#pragma unroll
      for (int sg = 0; sg < 8; ++sg) {
        v4u o;
#pragma unroll
        for (int i = 0; i < 4; ++i) o[i] = (sg & 1) ? __builtin_amdgcn_alignbyte(G[(sg >> 1) + i + 1], G[(sg >> 1) + i], 2) : G[(sg >> 1) + i];
        sts16(c.L, FC + ((ch & 1) * 8 + sg) * CP + m * 16, o);
      }
    }
  };
  build(0, w, w + 1);
  BAR();
  const int h2 = lane >> 5, r = lane & 31;
  for (int ch = 0; ch < 8; ++ch) {
    f32x16 acc;
    if (w == 3) {
      if (ch + 1 < 8) build(ch + 1, 0, 4);
    } else {
#pragma unroll
      for (int i = 0; i < 16; ++i) acc[i] = 0.f;
      const int dl0 = w == 2 ? 32 : 0, i0 = w == 0 ? 0 : 32;
      const int sg = (7 - r) & 7;
      int fo = FC + ((ch & 1) * 8 + sg) * CP + (2047 - 32 * dl0 - r + 8 * h2 - sg) * 2;
      const int ub = (i0 + r - dl0) * 80;
      const int ubase = U8 + ch * CH + 80 + h2 * 16;
      v4u fa[3][2], fb[3][2];
      fo = opq(fo - 31 * 64);
#pragma unroll
      for (int dl = -2; dl < 32; ++dl) {
        const int dn = dl + 2;
        if (dn < 32) {
          const int uo = ubase + max(ub - dn * 80, -80);
          fa[dn % 3][0] = lds16(c.L, fo + (31 - dn) * 64); fb[dn % 3][0] = lds16(c.L, uo);
          fa[dn % 3][1] = lds16(c.L, fo + (31 - dn) * 64 + 32); fb[dn % 3][1] = lds16(c.L, uo + 32);
        }
        SCHED();
        if (dl >= 0) {
          acc = mfma32(fa[dl % 3][0], fb[dl % 3][0], acc);
          acc = mfma32(fa[dl % 3][1], fb[dl % 3][1], acc);
        }
        SCHED();
      }
      if (w == 2) {
#pragma unroll
        for (int m = 0; m < 4; ++m) sts16(c.L, RED + (ch & 1) * 4096 + m * 1024 + lane * 16, v4u{fu(acc[4 * m]), fu(acc[4 * m + 1]), fu(acc[4 * m + 2]), fu(acc[4 * m + 3])});
      }
    }
    BAR();
    if (w < 2) {
#pragma unroll
      for (int m = 0; m < 4; ++m) {
        f32x4 v = {acc[4 * m], acc[4 * m + 1], acc[4 * m + 2], acc[4 * m + 3]};
        if (w == 1) v += __builtin_bit_cast(f32x4, lds16(c.L, RED + (ch & 1) * 4096 + m * 1024 + lane * 16));
        sts8(c.L, YS + ch * 4096 + (32 * (32 * w + r) + 8 * m + 4 * h2) * 2, v2u{pk(v[0], v[1]), pk(v[2], v[3])});
      }
    }
  }
  BAR();
  STAMP(6);
  {
    const rsrc_t ro = mkrs(p.out, 2048 * 2048 * 2);
    v4u qr[8], yv[8];
#pragma unroll
    for (int ch = 0; ch < 8; ++ch) st16s(c.rs, tid * 16, R_YT + pg + d0 * 8192 + crow(ch), lds16(c.L, YS + ch * 4096 + tid * 16));
#pragma unroll
    for (int i = 0; i < 8; ++i) qr[i] = ld16c(mkrs(p.bq, 4 * MB), (t0 + i) * 2048 + de * 2, 0);
    const v4u dd = *(const GAS v4u*)(p.ssmD + de);
    group_sync(1);
#pragma unroll
    for (int ch = 0; ch < 8; ++ch) yv[ch] = ld16s(c.rs, de * 8192 + t0 * 2, R_YT + pg + crow(ch));
#pragma unroll
    for (int i = 0; i < 8; ++i) {
      float o[8];
#pragma unroll
      for (int ch = 0; ch < 8; ++ch) {
        const float yy = (i & 1) ? bhi(yv[ch][i >> 1]) : blo(yv[ch][i >> 1]);
        const float uu = (i & 1) ? bhi(kv[ch][i >> 1]) : blo(kv[ch][i >> 1]);
        const float dv = (ch & 1) ? bhi(dd[ch >> 1]) : blo(dd[ch >> 1]);
        const float qq = (ch & 1) ? bhi(qr[i][ch >> 1]) : blo(qr[i][ch >> 1]);
        o[ch] = qq * rb(yy + rb(uu * dv));
      }
      __builtin_amdgcn_raw_buffer_store_b128(v4u{pk(o[0], o[1]), pk(o[2], o[3]), pk(o[4], o[5]), pk(o[6], o[7])}, ro, (t0 + i) * 4096 + (1024 + de) * 2, 0, AUX_NT);
    }
  }
  if (c.budget == 0u) __hip_atomic_store(c.ctrl + C_TMO / 4, 1u, RLX_AGENT);
}

extern "C" __global__ void __launch_bounds__(256, 1) mk_mega(Params p) {
  extern __shared__ __attribute__((aligned(16))) unsigned char lds_raw[];
  Ctx c;
  c.ctrl = (gu32*)p.ctrl;
  c.rs = mkrs(p.data, R_END);
  c.L = (lds_t)lds_raw;
  c.w = __builtin_amdgcn_readfirstlane(threadIdx.x >> 6);
  c.lane = threadIdx.x & 63;
  c.t0 = (unsigned)__builtin_amdgcn_s_memrealtime();
  c.b = (int)(blockIdx.x & 255u);
  c.att = (int)(blockIdx.x >> 8);
  c.ask = 0;
  if (c.w == 1) {
    unsigned long long base; unsigned len, t;
    asm volatile("s_getpc_b64 s[60:61]\n1:\n\ts_sub_u32 s60, s60, 1b-mk_mega\n\ts_subb_u32 s61, s61, 0\n\ts_mov_b64 %0, s[60:61]\n\ts_mov_b32 %1, mk_tail-mk_mega"
                 : "=s"(base), "=s"(len) :: "s60", "s61", "scc");
    t = __builtin_amdgcn_raw_buffer_load_b32(mkrs((const void*)base, len), (c.lane * 32 + (c.b >> 3)) * 128, 0, 0);
    asm volatile("" :: "v"(t));
  }
  if (c.w == 0 && c.att == 0) {
    c.ask = __builtin_amdgcn_readfirstlane(__hip_atomic_load(c.ctrl + C_ECHO / 4 + c.b, RLX_AGENT)) + 1u;
    if (c.lane == 0) __hip_atomic_store(c.ctrl + (C_ASK + c.b * 64) / 4, c.ask, RLX_AGENT);
  }
  c.slot = (c.att ? (c.b & 7) : (int)((unsigned)__builtin_amdgcn_s_getreg(20 | (3 << 11)) & 7u)) * 32 + (c.b >> 3);
  if (c.b == SERVER && c.w == 3) serve(c);
  const int x = c.slot >> 5, t = c.slot & 31;
  if (x < 4) {
    if (t < 8) qk_role(p, c, x * 8 + t);
    else if (t < 16) vo_role(p, c, x * 8 + t - 8);
    else y_role(p, c, x * 16 + t - 16);
  } else {
    if (t < 24) g_role(p, c, t >> 3, (x - 4) * 8 + (t & 7), t);
    else zc_role(p, c, (x - 4) * 8 + t - 24);
    STAMP(2);
    c.budget = SPIN_CAP;
    conv_role(p, c, (x - 4) * 32 + t, vote_a(wait_verdict(c)) ^ 1);
  }
  STAMP(3);
}

}

extern "C" void kernel_launch(void* const* d_in, const int* in_sizes, int n_in, void* d_out, int out_size, void* d_ws, size_t ws_size,
                              hipStream_t stream) {
  using namespace mk;
  (void)in_sizes; (void)n_in; (void)out_size; (void)ws_size;
  Params p;
  p.x = (const u16*)d_in[0]; p.mem0 = (const u16*)d_in[1];
  p.Wq = (const u16*)d_in[2]; p.Wk = (const u16*)d_in[3]; p.Wv = (const u16*)d_in[4]; p.Wo = (const u16*)d_in[5];
  p.bq = (const u16*)d_in[7]; p.bk = (const u16*)d_in[8]; p.bv = (const u16*)d_in[9];
  p.shift = (const u16*)d_in[10]; p.ssmk = (const u16*)d_in[11]; p.ssmD = (const u16*)d_in[12];
  p.out = (u16*)d_out;
  p.ctrl = (unsigned char*)d_ws;
  p.data = (unsigned char*)(((size_t)d_ws + CTRL_BYTES + (2u << 20) - 1) & ~(size_t)((2u << 20) - 1));
  (void)hipFuncSetAttribute((const void*)mk_mega, hipFuncAttributeMaxDynamicSharedMemorySize, LDS_BYTES);
  hipLaunchKernelGGL(mk_mega, dim3(256), dim3(256), LDS_BYTES, stream, p);
}
```

```cpp
#include <hip/hip_runtime.h>
#include <hip/hip_bf16.h>

namespace mk {

#define GAS __attribute__((address_space(1)))
#define LAS __attribute__((address_space(3)))
#define DEV __device__ __forceinline__
#define RLX_AGENT __ATOMIC_RELAXED, __HIP_MEMORY_SCOPE_AGENT
typedef unsigned short u16;
typedef unsigned v2u __attribute__((ext_vector_type(2)));
typedef unsigned v4u __attribute__((ext_vector_type(4)));
typedef unsigned v8u __attribute__((ext_vector_type(8)));
typedef unsigned v16u __attribute__((ext_vector_type(16)));
typedef unsigned v32u __attribute__((ext_vector_type(32)));
typedef float f32x2 __attribute__((ext_vector_type(2)));
typedef float f32x4 __attribute__((ext_vector_type(4)));
typedef float f32x16 __attribute__((ext_vector_type(16)));
typedef __bf16 bf16x2 __attribute__((ext_vector_type(2)));
typedef __bf16 bf16x8 __attribute__((ext_vector_type(8)));
typedef __bf16 bf16x16 __attribute__((ext_vector_type(16)));
typedef GAS unsigned gu32;
typedef __amdgpu_buffer_rsrc_t rsrc_t;
typedef LAS unsigned char* lds_t;

#define XCNT(r) ""
#define XT(a, b) ""
#define UCNT ""
#define CL_T
#define GSUM ""
#define ZSUM ""
#define GTRACE
#define MIXED 1
#define NOFLAG 0
#define TOK_KICK 1
#define ZRING 16
#define Z_AT 16
#define Z_GAP 10
#define STR_(x) #x
#define STR(x) STR_(x)
#define GQ_AT 600
#define GK_AT 300
#define PULL_AT 800
#define QK_AT 100
#define SERVER 194
#define SLOW_LEAD 100
#define PULL_DEPTH 1
#define PRIME 2
#define X_AT 150
#define VO_AT 250
#define ZC_AT 0
#define CONV_AUX 2
#define W_AUX 0
#define ZC_PRIME 1
#define CODE_PULL 1
#define PROBES 2
constexpr int NSEG = 64;
constexpr unsigned SENT = 0xFFFFFFFFu;
constexpr int AUX_SC1 = 16, AUX_NT = 2;
constexpr int T_LAG = 61;
constexpr int G_AHEAD = 4;
constexpr unsigned SPIN_CAP = 1u << 21;
constexpr int LDS_BYTES = 163840;

constexpr int C_ECHO = 0, C_CLAIM = 1024, C_VERDICT = 3072, C_TMO = 3328, C_CONV = 3456, C_VOTE = 3840, C_FLAG = 4096, C_ASK = C_FLAG + 256 * 128;
constexpr int F_Q = 0, F_K = 32, F_V = 64, F_S = 96, F_Z = 128;
constexpr int CTRL_BYTES = C_ASK + 256 * 64;
constexpr int MB = 1 << 20;
constexpr int R_U = 0, R_U2 = 3 * MB;
constexpr int R_MEM = 2 * MB, MEM_SLOT = 32768;
constexpr int R_P = R_MEM + 4 * MEM_SLOT, P_SLOT = 65536;
constexpr int R_PT = 4 * MB, PT_SLOT = 131072;
constexpr int R_QX = 8 * MB, R_KX = 40 * MB, R_Z = 72 * MB, Z_SLOT = 4 * MB, R_KVT = R_KX, R_YT = R_KX + 8 * MB, R_END = 208 * MB;

struct Params {
  const u16 *x, *mem0, *Wq, *Wk, *Wv, *Wo, *bq, *bk, *bv, *shift, *ssmk, *ssmD;
  u16* out;
  unsigned char* ctrl;
  unsigned char* data;
};

DEV rsrc_t mkrs(const void* p, unsigned bytes) { return __builtin_amdgcn_make_buffer_rsrc((void*)p, 0, bytes, 0x00020000); }
DEV v4u ld16s(rsrc_t rs, int v, int s) { return __builtin_amdgcn_raw_buffer_load_b128(rs, v, s, AUX_SC1); }
DEV v2u ld8s(rsrc_t rs, int v, int s) { return __builtin_amdgcn_raw_buffer_load_b64(rs, v, s, AUX_SC1); }
DEV v4u ld16(rsrc_t rs, int v, int s) { return __builtin_amdgcn_raw_buffer_load_b128(rs, v, s, 0); }
DEV v4u ld16c(rsrc_t rs, int v, int s) { return __builtin_amdgcn_raw_buffer_load_b128(rs, v, s, CONV_AUX); }
DEV v4u ld16w(rsrc_t rs, int v, int s) { return __builtin_amdgcn_raw_buffer_load_b128(rs, v, s, W_AUX); }
DEV v2u ld8(rsrc_t rs, int v, int s) { return __builtin_amdgcn_raw_buffer_load_b64(rs, v, s, 0); }
DEV void dma16(rsrc_t rs, lds_t l, int v) {
#if __HIP_DEVICE_COMPILE__
  __builtin_amdgcn_raw_ptr_buffer_load_lds(rs, (LAS void*)l, 16, v, 0, 0, 0);
#endif
}
DEV void st16s(rsrc_t rs, int v, int s, v4u d) { __builtin_amdgcn_raw_buffer_store_b128(d, rs, v, s, AUX_SC1); }
DEV void st8s(rsrc_t rs, int v, int s, v2u d) { __builtin_amdgcn_raw_buffer_store_b64(d, rs, v, s, AUX_SC1); }
DEV void st4s(rsrc_t rs, int v, int s, unsigned d) { __builtin_amdgcn_raw_buffer_store_b32(d, rs, v, s, AUX_SC1); }
DEV int amap(int o, int par) { return ((o >> 13) << 14) | (par << 13) | (o & 8191); }
DEV int xblk(int s, int h, int par) { return (s * 32 + h) * 16384 + ((par ^ (s >> 2)) & 1) * 8192; }
DEV unsigned umax(unsigned a, unsigned b) { return a > b ? a : b; }
DEV unsigned top(v4u v) { return umax(umax(v.x, v.y), umax(v.z, v.w)); }
DEV unsigned pk(float a, float b) { bf16x2 v = {(__bf16)a, (__bf16)b}; return __builtin_bit_cast(unsigned, v); }
DEV unsigned pks(float a, float b) { unsigned u = pk(a, b); return u < SENT - 1u ? u : SENT - 1u; }
DEV float blo(unsigned u) { return __builtin_bit_cast(float, u << 16); }
DEV float bhi(unsigned u) { return __builtin_bit_cast(float, u & 0xFFFF0000u); }
DEV float rb(float a) { return blo(pk(a, a)); }
DEV unsigned fu(float f) { return __builtin_bit_cast(unsigned, f); }
DEV float uf(unsigned u) { return __builtin_bit_cast(float, u); }
DEV f32x4 mfma16(v4u a, v4u b, f32x4 c) {
  return __builtin_amdgcn_mfma_f32_16x16x32_bf16(__builtin_bit_cast(bf16x8, a), __builtin_bit_cast(bf16x8, b), c, 0, 0, 0);
}
DEV f32x16 mfma32(v4u a, v4u b, f32x16 c) {
  return __builtin_amdgcn_mfma_f32_32x32x16_bf16(__builtin_bit_cast(bf16x8, a), __builtin_bit_cast(bf16x8, b), c, 0, 0, 0);
}
DEV f32x4 smf(v4u a, v8u b, f32x4 c, int idx) {
  return __builtin_amdgcn_smfmac_f32_16x16x64_bf16(__builtin_bit_cast(bf16x8, a), __builtin_bit_cast(bf16x16, b), c, idx, 0, 0);
}
DEV v8u cat(v4u a, v4u b) { return __builtin_shufflevector(a, b, 0, 1, 2, 3, 4, 5, 6, 7); }
DEV v4u lo4(v8u a) { return __builtin_shufflevector(a, a, 0, 1, 2, 3); }
DEV v4u hi4(v8u a) { return __builtin_shufflevector(a, a, 4, 5, 6, 7); }
DEV v4u lds16(lds_t L, int o) { return *(LAS v4u*)(L + o); }
DEV void sts16(lds_t L, int o, v4u v) { *(LAS v4u*)(L + o) = v; }
DEV void sts8(lds_t L, int o, v2u v) { *(LAS v2u*)(L + o) = v; }
#define SCHED() __builtin_amdgcn_sched_barrier(0)
#define PIN_A(x) asm volatile("" : "+a"(x))
#define PIN_V(x) asm volatile("" : "+v"(x))
DEV int opq(int v) { asm volatile("" : "+v"(v)); return v; }
DEV int fresh_lane() {
  int l; asm volatile("v_mbcnt_lo_u32_b32 %0, -1, 0\n\tv_mbcnt_hi_u32_b32 %0, -1, %0" : "=v"(l)); return l;
}
DEV f32x4 fresh_zero() {
  f32x2 a, b;
  asm volatile("v_mov_b64 %0, 0" : "=v"(a));
  asm volatile("v_mov_b64 %0, 0" : "=v"(b));
  return __builtin_shufflevector(a, b, 0, 1, 2, 3);
}
DEV unsigned fresh_sent() { unsigned z; asm volatile("v_mov_b32 %0, -1" : "=v"(z)); return z; }
#define SMF "v_smfmac_f32_16x16x64_bf16 "
#define MF16 "v_mfma_f32_16x16x32_bf16 "
#define SMF1(cls, d, a, b) asm volatile(SMF "%0, %1, %2, %3" : "+v"(d) : "v"(a), cls(b), "v"(idx))
#define SMF1F(cls, lo, hi, d, a, b) asm volatile(SMF "v[" #lo ":" #hi "], %1, %2, %3" : "+{v[" #lo ":" #hi "]}"(d) : "v"(a), cls(b), "v"(idx))
#define AT_HOME(M, j) do { const int j_ = (j); if (j_ == 0) M("{v[32:39]}"); if (j_ == 1) M("{v[40:47]}"); if (j_ == 2) M("{v[48:55]}"); if (j_ == 3) M("{v[56:63]}"); if (j_ == 4) M("{v[64:71]}"); if (j_ == 5) M("{v[72:79]}"); if (j_ == 6) M("{v[80:87]}"); if (j_ == 7) M("{v[88:95]}"); if (j_ == 8) M("{v[96:103]}"); if (j_ == 9) M("{v[104:111]}"); if (j_ == 10) M("{v[112:119]}"); if (j_ == 11) M("{v[120:127]}"); if (j_ == 12) M("{v[128:135]}"); if (j_ == 13) M("{v[136:143]}"); if (j_ == 14) M("{v[144:151]}"); if (j_ == 15) M("{v[152:159]}"); } while (0)
#define MF1(d, a, b) asm volatile(MF16 "%0, %1, %2, %0" : "+v"(d) : "v"(a), "v"(b))
#define MF1Z(d, a, b) asm volatile(MF16 "%0, %1, %2, 0" : "=&v"(d) : "v"(a), "v"(b))
#define LGKM(n) asm volatile("s_waitcnt lgkmcnt(" #n ")" ::: "memory")
#define RAWBAR() do { __builtin_amdgcn_s_barrier(); asm volatile("" ::: "memory"); } while (0)
DEV void settle3(f32x4& s0, f32x4& s1, f32x4& s2) { asm volatile("s_nop 7" : "+v"(s0), "+v"(s1), "+v"(s2)); }
DEV void settle4(f32x4 (&d)[4]) { asm volatile("s_nop 7" : "+v"(d[0]), "+v"(d[1]), "+v"(d[2]), "+v"(d[3])); }
#define BAR() do { asm volatile("s_waitcnt lgkmcnt(0)" ::: "memory"); __builtin_amdgcn_s_barrier(); asm volatile("" ::: "memory"); } while (0)
#define VMWAIT(n) asm volatile("s_waitcnt vmcnt(" #n ")" ::: "memory")
DEV float add1(float a, float b) {
  float o; asm("v_add_f32 %0, %1, %2" : "=v"(o) : "v"(a), "v"(b)); return o;
}
DEV float xmax(float v) {
  auto r = __builtin_amdgcn_permlane32_swap(fu(v), fu(v), false, false); v = fmaxf(uf(r[0]), uf(r[1]));
  auto q = __builtin_amdgcn_permlane16_swap(fu(v), fu(v), false, false); return fmaxf(uf(q[0]), uf(q[1]));
}
DEV float xsum(float v) {
  auto r = __builtin_amdgcn_permlane32_swap(fu(v), fu(v), false, false); v = uf(r[0]) + uf(r[1]);
  auto q = __builtin_amdgcn_permlane16_swap(fu(v), fu(v), false, false); return uf(q[0]) + uf(q[1]);
}

DEV float ror8(float v) { return uf(__builtin_amdgcn_update_dpp(0u, fu(v), 0x128, 0xf, 0xf, true)); }
DEV float fmax2(float a, float b) { return __builtin_amdgcn_fmed3f(a, b, __builtin_inff()); }
DEV float fmax3(float a, float b, float c) { float r; asm("v_max3_f32 %0, %1, %2, %3" : "=v"(r) : "v"(a), "v"(b), "v"(c)); return r; }
DEV bool allok(unsigned t) { return __all(t != SENT); }

template <int N> struct IC { static constexpr int value = N; };
template <int N, class F> DEV void sfor(F&& f) { if constexpr (N > 0) { sfor<N - 1>(f); f(IC<N - 1>{}); } }
struct Ctx {
  gu32* ctrl;
  rsrc_t rs;
  lds_t L;
  int w, lane;
  int b, slot, att;
  unsigned ask;
  unsigned t0;
  unsigned budget;
};
DEV bool spend(Ctx& c) {
  const unsigned old = c.budget;
  asm volatile("s_cmp_lg_u32 %0, 0\n\ts_subb_u32 %0, %0, 0" : "+s"(c.budget) :: "scc");
  return old == 0u;
}
DEV unsigned wait_flag(Ctx& c, int flag, unsigned need) {
  gu32* f = c.ctrl + (C_FLAG + flag * 128) / 4;
  unsigned v;
#pragma unroll 1
  for (;;) {
    v = __builtin_amdgcn_readfirstlane(__hip_atomic_load(f, RLX_AGENT));
    if (v >= need || spend(c)) break;
    __builtin_amdgcn_s_sleep(2);
  }
  __builtin_amdgcn_fence(__ATOMIC_ACQUIRE, "wavefront");
  return v;
}
DEV void post_flag(Ctx& c, int flag, unsigned v, int off = 0) {
  st4s(mkrs((const void*)c.ctrl, CTRL_BYTES), (c.w | off | fresh_lane()) ? 1 << 30 : 0, C_FLAG + flag * 128, v);
}
constexpr unsigned CL_A = 1u << 16, CL_VOTED = 1u << 24, CL_FILLED = 1u << 28;
DEV void claim(Ctx& c, unsigned votes) {
  if (c.w != 0) return;
  if (c.att == 0) {
#pragma unroll 1
    while (__builtin_amdgcn_readfirstlane(__hip_atomic_load(c.ctrl + C_ECHO / 4 + c.b, RLX_AGENT)) != c.ask) {
      if (spend(c)) break;
      __builtin_amdgcn_s_sleep(2);
    }
  }
  if (fresh_lane() == 0) __hip_atomic_fetch_add(c.ctrl + (C_CLAIM + c.att * 1024) / 4 + c.slot, 1u + votes, RLX_AGENT);
}
DEV unsigned wait_verdict(Ctx& c) {
  unsigned v;
#pragma unroll 1
  for (;;) {
    v = __builtin_amdgcn_readfirstlane(__hip_atomic_load(c.ctrl + (C_VERDICT + c.att * 128) / 4, RLX_AGENT));
    if ((v & 1u) || spend(c)) break;
    __builtin_amdgcn_s_sleep(2);
  }
  __builtin_amdgcn_fence(__ATOMIC_ACQUIRE, "wavefront");
  if ((v & 3u) == 3u && c.att == 0) {
    asm volatile("s_waitcnt vmcnt(0) lgkmcnt(0)\n\ts_barrier\n\ts_mov_b64 s[0:1], %0\n\ts_mov_b32 s2, %1\n\tv_mov_b32 v0, %2\n\t"
                 "s_getpc_b64 s[4:5]\n1:\n\ts_sub_u32 s4, s4, 1b-mk_mega\n\ts_subb_u32 s5, s5, 0\n\ts_setpc_b64 s[4:5]"
                 :: "s"(__builtin_amdgcn_kernarg_segment_ptr()), "s"(c.b + 256), "v"(c.w * 64 + fresh_lane()) : "s0", "s1", "s2", "s4", "s5", "v0", "scc", "memory");
    __builtin_unreachable();
  }
  return v;
}
DEV unsigned wait_vote(Ctx& c) {
  unsigned v;
#pragma unroll 1
  for (;;) {
    v = __builtin_amdgcn_readfirstlane(__hip_atomic_load(c.ctrl + C_VOTE / 4, RLX_AGENT));
    if ((v & 1u) || spend(c)) break;
    __builtin_amdgcn_s_sleep(2);
  }
  return v;
}
DEV void serve(Ctx& c) {
  c.budget = SPIN_CAP;
  const int lane = fresh_lane();
  const rsrc_t rc = mkrs((const void*)c.ctrl, CTRL_BYTES);
  if (c.att == 0) {
#pragma unroll
    for (int i = C_CLAIM / 1024; i < C_ASK / 1024; ++i) st16s(rc, lane * 16, i * 1024, v4u{0, 0, 0, 0});
    VMWAIT(0);
  }
  unsigned vote = c.att ? wait_vote(c) : 0u;
  bool echo = c.att == 0, first = true;
  v4u last = {0u, 0u, 0u, 0u};
#pragma unroll 1
  for (;;) {
    asm volatile("" ::: "memory");
    const v4u cl = ld16s(rc, lane * 16, C_CLAIM + c.att * 1024);
    if (echo) {
      v4u e;
#pragma unroll
      for (int i = 0; i < 4; ++i) e[i] = __builtin_amdgcn_raw_buffer_load_b32(rc, (lane * 4 + i) * 64, C_ASK, AUX_SC1);
      if (first || __any(e[0] != last[0] || e[1] != last[1] || e[2] != last[2] || e[3] != last[3])) st16s(rc, lane * 16, C_ECHO, e);
      last = e; first = false;
    }
    unsigned n = 0, one = 1;
    int va = 0, nv = 0, nf = 0;
#pragma unroll
    for (int i = 0; i < 4; ++i) {
      n += cl[i] & 0xFFFFu; one &= (cl[i] & 0xFFFFu) == 1u;
      va += __popcll(__ballot(cl[i] & CL_A)); nv += __popcll(__ballot(cl[i] & CL_VOTED)); nf += __popcll(__ballot(cl[i] & CL_FILLED));
    }
#pragma unroll
    for (int o = 1; o < 64; o <<= 1) n += (unsigned)__shfl_xor((int)n, o);
    const bool all = __builtin_amdgcn_readfirstlane(n) >= 256u, good = __all(one);
    echo = echo && !all;
    if (!vote && (all || max(va, nv - va) > 32)) {
      vote = va > 32 ? 5u : 9u;
      if (lane == 0) __hip_atomic_store(c.ctrl + C_VOTE / 4, vote, RLX_AGENT);
    }
    if (all && (!good || nf >= 64)) {
      if (lane == 0) __hip_atomic_store(c.ctrl + (C_VERDICT + c.att * 128) / 4, vote | (good ? 0u : 2u), RLX_AGENT);
      break;
    }
    if (spend(c)) break;
  }
}
DEV int vote_a(unsigned v) { return (int)((v >> 2) & 1u); }
DEV int vote_b(unsigned v) { return (int)((v >> 3) & 1u); }

#define STAMP(slot) do {} while (0)
#define TICK_DECL
#define TICK(i) do {} while (0)
#define TICK_SUM(n) do {} while (0)
#define TICK_LDS(b) do {} while (0)
#define TICK_DUMP(b) do {} while (0)
#define TICK_OUT() do {} while (0)

DEV constexpr int krow(int rho8, int i) { return 32 * (rho8 >> 2) + 8 * (rho8 & 1) + 2 * ((rho8 >> 1) & 1) + i; }
DEV int krow_lane(int g) { return 16 * (g >> 1) + 4 * (g & 1); }
DEV unsigned prm(unsigned hi, unsigned lo, int odd) { return __builtin_amdgcn_perm(hi, lo, odd ? 0x07060302u : 0x05040100u); }

#define AT_ACC(M, n) do { const int n_ = (n); if (n_ == 0) M("{a[0:7]}"); if (n_ == 1) M("{a[8:15]}"); if (n_ == 2) M("{a[16:23]}"); if (n_ == 3) M("{a[24:31]}"); if (n_ == 4) M("{a[32:39]}"); if (n_ == 5) M("{a[40:47]}"); if (n_ == 6) M("{a[48:55]}"); if (n_ == 7) M("{a[56:63]}"); if (n_ == 8) M("{a[64:71]}"); if (n_ == 9) M("{a[72:79]}"); if (n_ == 10) M("{a[80:87]}"); if (n_ == 11) M("{a[88:95]}"); if (n_ == 12) M("{a[96:103]}"); if (n_ == 13) M("{a[104:111]}"); if (n_ == 14) M("{a[112:119]}"); if (n_ == 15) M("{a[120:127]}"); if (n_ == 16) M("{a[128:135]}"); if (n_ == 17) M("{a[136:143]}"); if (n_ == 18) M("{a[144:151]}"); if (n_ == 19) M("{a[152:159]}"); if (n_ == 20) M("{a[160:167]}"); if (n_ == 21) M("{a[168:175]}"); if (n_ == 22) M("{a[176:183]}"); if (n_ == 23) M("{a[184:191]}"); if (n_ == 24) M("{a[192:199]}"); if (n_ == 25) M("{a[200:207]}"); if (n_ == 26) M("{a[208:215]}"); if (n_ == 27) M("{a[216:223]}"); if (n_ == 28) M("{a[224:231]}"); if (n_ == 29) M("{a[232:239]}"); if (n_ == 30) M("{a[240:247]}"); if (n_ == 31) M("{a[248:255]}"); } while (0)
#define PIN_ACC0(x) asm volatile("" :: "{a[0:7]}"(x[0]), "{a[8:15]}"(x[1]), "{a[16:23]}"(x[2]), "{a[24:31]}"(x[3]), "{a[32:39]}"(x[4]), "{a[40:47]}"(x[5]), "{a[48:55]}"(x[6]), "{a[56:63]}"(x[7]), "{a[64:71]}"(x[8]), "{a[72:79]}"(x[9]), "{a[80:87]}"(x[10]), "{a[88:95]}"(x[11]), "{a[96:103]}"(x[12]), "{a[104:111]}"(x[13]), "{a[112:119]}"(x[14]), "{a[120:127]}"(x[15]))
#define PIN_ACC1(x) asm volatile("" :: "{a[128:135]}"(x[16]), "{a[136:143]}"(x[17]), "{a[144:151]}"(x[18]), "{a[152:159]}"(x[19]), "{a[160:167]}"(x[20]), "{a[168:175]}"(x[21]), "{a[176:183]}"(x[22]), "{a[184:191]}"(x[23]), "{a[192:199]}"(x[24]), "{a[200:207]}"(x[25]), "{a[208:215]}"(x[26]), "{a[216:223]}"(x[27]), "{a[224:231]}"(x[28]), "{a[232:239]}"(x[29]), "{a[240:247]}"(x[30]), "{a[248:255]}"(x[31]))
#define PIN_HOME(x) asm volatile("" :: "{v[32:39]}"(x[0]), "{v[40:47]}"(x[1]), "{v[48:55]}"(x[2]), "{v[56:63]}"(x[3]), "{v[64:71]}"(x[4]), "{v[72:79]}"(x[5]), "{v[80:87]}"(x[6]), "{v[88:95]}"(x[7]), "{v[96:103]}"(x[8]), "{v[104:111]}"(x[9]), "{v[112:119]}"(x[10]), "{v[120:127]}"(x[11]), "{v[128:135]}"(x[12]), "{v[136:143]}"(x[13]), "{v[144:151]}"(x[14]), "{v[152:159]}"(x[15]))
#define CL_32_100 "v32", "v33", "v34", "v35", "v36", "v37", "v38", "v39", "v40", "v41", "v42", "v43", "v44", "v45", "v46", "v47", "v48", "v49", "v50", "v51", "v52", "v53", "v54", "v55", "v56", "v57", "v58", "v59", "v60", "v61", "v62", "v63", "v64", "v65", "v66", "v67", "v68", "v69", "v70", "v71", "v72", "v73", "v74", "v75", "v76", "v77", "v78", "v79", "v80", "v81", "v82", "v83", "v84", "v85", "v86", "v87", "v88", "v89", "v90", "v91", "v92", "v93", "v94", "v95", "v96", "v97", "v98", "v99", "v100", "v101", "v102", "v103", "v104", "v105", "v106", "v107", "v108", "v109", "v110", "v111", "v112", "v113", "v114", "v115", "v116", "v117", "v118", "v119", "v120", "v121", "v122", "v123", "v124", "v125", "v126", "v127", "v128", "v129", "v130", "v131"
#define ACC_IN(x) "{a[0:31]}"(x[0]), "{a[32:63]}"(x[1]), "{a[64:95]}"(x[2]), "{a[96:127]}"(x[3]), "{a[128:159]}"(x[4]), "{a[160:191]}"(x[5]), "{a[192:223]}"(x[6]), "{a[224:255]}"(x[7])
#define CL_16_32 "v16", "v17", "v18", "v19", "v20", "v21", "v22", "v23", "v24", "v25", "v26", "v27", "v28", "v29", "v30", "v31"
#define CL_120_128 "v120", "v121", "v122", "v123", "v124", "v125", "v126", "v127"
#define CL_160_200 "v160", "v161", "v162", "v163", "v164", "v165", "v166", "v167", "v168", "v169", "v170", "v171", "v172", "v173", "v174", "v175", "v176", "v177", "v178", "v179", "v180", "v181", "v182", "v183", "v184", "v185", "v186", "v187", "v188", "v189", "v190", "v191", "v192", "v193", "v194", "v195", "v196", "v197", "v198", "v199"
#define CL_176_200 "v176", "v177", "v178", "v179", "v180", "v181", "v182", "v183", "v184", "v185", "v186", "v187", "v188", "v189", "v190", "v191", "v192", "v193", "v194", "v195", "v196", "v197", "v198", "v199"
#define CL_200_216 "v200", "v201", "v202", "v203", "v204", "v205", "v206", "v207", "v208", "v209", "v210", "v211", "v212", "v213", "v214", "v215"
#define CL_216_256 "v216", "v217", "v218", "v219", "v220", "v221", "v222", "v223", "v224", "v225", "v226", "v227", "v228", "v229", "v230", "v231", "v232", "v233", "v234", "v235", "v236", "v237", "v238", "v239", "v240", "v241", "v242", "v243", "v244", "v245", "v246", "v247", "v248", "v249", "v250", "v251", "v252", "v253", "v254", "v255"
#define CL_S "memory", "vcc", "scc", "s60", "s61", "s62", "s63", "s64", "s65", "s66", "s67" CL_T
#define TIMED_WAIT \
    "s_getpc_b64 s[62:63]\n\t" "1:\n\t" "s_memtime s[64:65]\n\t" "s_waitcnt lgkmcnt(0)\n\t" "s_sub_u32 s61, %0, s64\n\t" "s_cmp_gt_i32 s61, 124\n\t" "s_cbranch_scc1 1b\n\t" \
    "s_max_i32 s61, s61, 0\n\t" "s_and_b32 s61, s61, 124\n\t" "s_sub_u32 s61, 2f-1b, s61\n\t" "s_add_u32 s62, s62, s61\n\t" "s_addc_u32 s63, s63, 0\n\t" \
    "s_setpc_b64 s[62:63]\n\t" ".rept 31\n\t" "s_nop 0\n\t" ".endr\n\t" "2:\n\t"
#define QK_STEP0 \
    "v_xor_b32 v216, 0x400, %[mo]\n\t" "s_mov_b32 s60, 0\n\t" "s_mov_b32 s66, 0x3e0293ee\n\t" "s_mov_b32 s67, 0x3e0293ee\n\t" XT(80, 81) "s_add_u32 s61, %[kb], 0x1000\n\t" \
    "buffer_load_dwordx4 v[16:19], %[l1], %[rs], %[kb] offen offset:0 sc1\n\t" "buffer_load_dwordx4 v[20:23], %[l1], %[rs], %[kb] offen offset:1024 sc1\n\t" \
    "buffer_load_dwordx4 v[24:27], %[l1], %[rs], %[kb] offen offset:2048 sc1\n\t" "buffer_load_dwordx4 v[28:31], %[l1], %[rs], %[kb] offen offset:3072 sc1\n\t" \
    "buffer_load_dwordx4 v[200:203], %[l1], %[rs], s61 offen offset:0 sc1\n\t" "buffer_load_dwordx4 v[204:207], %[l1], %[rs], s61 offen offset:1024 sc1\n\t" \
    "buffer_load_dwordx4 v[208:211], %[l1], %[rs], s61 offen offset:2048 sc1\n\t" "buffer_load_dwordx4 v[212:215], %[l1], %[rs], s61 offen offset:3072 sc1\n\t" \
    "v_mov_b64 v[218:219], -1\n\t" "buffer_store_dwordx2 v[218:219], %[px], %[rs], %[pr] offen sc1\n\t" "buffer_store_dword v218, %[pm], %[rs], %[pr] offen sc1\n\t" \
    "s_waitcnt vmcnt(2)\n\t" XT(98, 99) "s_barrier\n\t" XT(82, 83) "10:\n\t" "buffer_load_dwordx4 v[240:243], %[mo], %[rs], %[sb] offen offset:0 sc1\n\t" \
    "v_mov_b64 v[224:225], 0\n\t" "v_mov_b64 v[226:227], 0\n\t" "v_mov_b64 v[228:229], 0\n\t" "v_mov_b64 v[230:231], 0\n\t" "v_mov_b64 v[232:233], 0\n\t" \
    "v_mov_b64 v[234:235], 0\n\t" "v_mov_b64 v[236:237], 0\n\t" "v_mov_b64 v[238:239], 0\n\t" "buffer_load_dwordx4 v[244:247], v216, %[rs], %[sb] offen offset:0 sc1\n\t" \
    "ds_read_b128 v[160:163], %[l0] offset:0\n\t" "ds_read_b128 v[164:167], %[l0] offset:1024\n\t" "ds_read_b128 v[168:171], %[l0] offset:2048\n\t" \
    "ds_read_b128 v[172:175], %[l0] offset:3072\n\t" "s_nop 3\n\t" "buffer_load_dwordx4 v[248:251], %[mo], %[rs], %[sb] offen offset:2048 sc1\n\t" \
    "ds_read_b128 v[176:179], %[l0] offset:4096\n\t" "ds_read_b128 v[180:183], %[l0] offset:5120\n\t" "ds_read_b128 v[184:187], %[l0] offset:6144\n\t" \
    "ds_read_b128 v[188:191], %[l0] offset:7168\n\t" "s_nop 3\n\t" "buffer_load_dwordx4 v[252:255], v216, %[rs], %[sb] offen offset:2048 sc1\n\t" "s_cmp_lg_u32 s61, 0\n\t" \
    "s_cbranch_scc0 177f\n\t" "ds_write_b128 %[l1], v[16:19] offset:16384\n\t" "ds_write_b128 %[l1], v[20:23] offset:17408\n\t" \
    "ds_write_b128 %[l1], v[24:27] offset:18432\n\t" "ds_write_b128 %[l1], v[28:31] offset:19456\n\t" "ds_write_b128 %[l1], v[200:203] offset:20480\n\t" \
    "ds_write_b128 %[l1], v[204:207] offset:21504\n\t" "ds_write_b128 %[l1], v[208:211] offset:22528\n\t" "ds_write_b128 %[l1], v[212:215] offset:23552\n\t" \
    "s_mov_b32 s61, 0\n\t" "s_waitcnt lgkmcnt(0)\n\t" "177:\n\t" "v_mov_b64 v[208:209], 0\n\t" "v_mov_b64 v[210:211], 0\n\t" "v_mov_b64 v[212:213], 0\n\t" \
    "v_mov_b64 v[214:215], 0\n\t" "s_waitcnt vmcnt(3)\n\t" XT(84, 85) "v_smfmac_f32_16x16x64_bf16 v[224:227], v[240:243], a[0:7], %[ix]\n\t" \
    "ds_write_b128 %[l1], v[240:243] offset:0\n\t" "v_smfmac_f32_16x16x64_bf16 v[228:231], v[240:243], a[8:15], %[ix]\n\t" "v_max3_u32 v217, v240, v241, v242\n\t" \
    "v_smfmac_f32_16x16x64_bf16 v[232:235], v[240:243], v[32:39], %[ix]\n\t" "s_waitcnt lgkmcnt(7)\n\t" \
    "v_smfmac_f32_16x16x64_bf16 v[236:239], v[240:243], v[160:167], %[ix]\n\t" "ds_read_b128 v[160:163], %[l0] offset:8192\n\t" \
    "ds_read_b128 v[164:167], %[l0] offset:9216\n\t" "s_waitcnt vmcnt(2)\n\t" "v_smfmac_f32_16x16x64_bf16 v[224:227], v[244:247], a[16:23], %[ix]\n\t" \
    "ds_write_b128 %[l1], v[244:247] offset:1024\n\t" "v_max3_u32 v217, v217, v243, v244\n\t" "v_smfmac_f32_16x16x64_bf16 v[228:231], v[244:247], a[24:31], %[ix]\n\t" \
    "v_max3_u32 v217, v217, v245, v246\n\t" "v_max_u32 v217, v217, v247\n\t" "v_smfmac_f32_16x16x64_bf16 v[232:235], v[244:247], v[40:47], %[ix]\n\t" \
    "v_cmp_eq_u32 vcc, -1, v217\n\t" "s_cmp_lg_u32 s60, 0\n\t" "s_waitcnt lgkmcnt(8)\n\t" "v_smfmac_f32_16x16x64_bf16 v[236:239], v[244:247], v[168:175], %[ix]\n\t" \
    "s_cbranch_vccnz 11f\n\t" "12:\n\t" "ds_read_b128 v[168:171], %[l0] offset:10240\n\t" "s_waitcnt vmcnt(1)\n\t" \
    "v_smfmac_f32_16x16x64_bf16 v[224:227], v[248:251], a[32:39], %[ix]\n\t" "ds_write_b128 %[l1], v[248:251] offset:2048\n\t" "s_waitcnt lgkmcnt(2)\n\t" \
    "v_smfmac_f32_16x16x64_bf16 v[228:231], v[248:251], a[40:47], %[ix]\n\t" "s_cbranch_scc1 13f\n\t" "s_barrier\n\t" "13:\n\t" XT(86, 87) \
    "v_smfmac_f32_16x16x64_bf16 v[232:235], v[248:251], v[48:55], %[ix]\n\t" "ds_read_b128 v[192:195], %[l1] offset:4096\n\t" "ds_read_b128 v[196:199], %[l1] offset:8192\n\t" \
    "v_smfmac_f32_16x16x64_bf16 v[236:239], v[248:251], v[176:183], %[ix]\n\t" "ds_read_b128 v[172:175], %[l0] offset:11264\n\t" \
    "ds_read_b128 v[176:179], %[l0] offset:12288\n\t" "s_waitcnt vmcnt(0)\n\t" "v_smfmac_f32_16x16x64_bf16 v[224:227], v[252:255], a[48:55], %[ix]\n\t" \
    "ds_write_b128 %[l1], v[252:255] offset:3072\n\t" "v_max3_u32 v217, v248, v249, v250\n\t" "v_smfmac_f32_16x16x64_bf16 v[228:231], v[252:255], a[56:63], %[ix]\n\t" \
    "ds_read_b128 v[180:183], %[l0] offset:13312\n\t" "v_max3_u32 v217, v217, v251, v252\n\t" "v_max3_u32 v217, v217, v253, v254\n\t" \
    "v_smfmac_f32_16x16x64_bf16 v[232:235], v[252:255], v[56:63], %[ix]\n\t" "v_max_u32 v217, v217, v255\n\t" "v_cmp_eq_u32 vcc, -1, v217\n\t" \
    "v_smfmac_f32_16x16x64_bf16 v[236:239], v[252:255], v[184:191], %[ix]\n\t" "s_cbranch_vccnz 14f\n\t" "15:\n\t" "ds_read_b128 v[240:243], %[l1] offset:12288\n\t" \
    "s_waitcnt lgkmcnt(6)\n\t" "v_smfmac_f32_16x16x64_bf16 v[224:227], v[192:195], a[64:71], %[ix]\n\t" "ds_read_b128 v[184:187], %[l0] offset:14336\n\t" \
    "ds_read_b128 v[188:191], %[l0] offset:15360\n\t" "v_smfmac_f32_16x16x64_bf16 v[228:231], v[192:195], a[72:79], %[ix]\n\t" \
    "ds_read_b128 v[244:247], %[l1] offset:5120\n\t" "s_waitcnt lgkmcnt(5)\n\t" "v_smfmac_f32_16x16x64_bf16 v[232:235], v[192:195], v[64:71], %[ix]\n\t" "s_barrier\n\t" \
    XT(88, 89) "v_smfmac_f32_16x16x64_bf16 v[236:239], v[192:195], v[160:167], %[ix]\n\t" "ds_read_b128 v[160:163], %[l0] offset:16384\n\t" \
    "ds_read_b128 v[164:167], %[l0] offset:17408\n\t" "v_smfmac_f32_16x16x64_bf16 v[224:227], v[196:199], a[80:87], %[ix]\n\t" \
    "ds_read_b128 v[248:251], %[l1] offset:9216\n\t" "ds_read_b128 v[252:255], %[l1] offset:13312\n\t" \
    "v_smfmac_f32_16x16x64_bf16 v[228:231], v[196:199], a[88:95], %[ix]\n\t" "ds_read_b128 v[192:195], %[l1] offset:6144\n\t" \
    "v_smfmac_f32_16x16x64_bf16 v[232:235], v[196:199], v[72:79], %[ix]\n\t" "ds_read_b128 v[16:19], %[kx] offset:0\n\t" \
    "v_smfmac_f32_16x16x64_bf16 v[236:239], v[196:199], v[168:175], %[ix]\n\t" "ds_read_b128 v[168:171], %[l0] offset:18432\n\t" \
    "ds_read_b128 v[172:175], %[l0] offset:19456\n\t" "s_waitcnt lgkmcnt(11)\n\t" "v_smfmac_f32_16x16x64_bf16 v[224:227], v[240:243], a[96:103], %[ix]\n\t" \
    "ds_read_b128 v[196:199], %[l1] offset:10240\n\t" "v_smfmac_f32_16x16x64_bf16 v[228:231], v[240:243], a[104:111], %[ix]\n\t" \
    "ds_read_b128 v[20:23], %[kx] offset:2048\n\t" "v_smfmac_f32_16x16x64_bf16 v[232:235], v[240:243], v[80:87], %[ix]\n\t" \
    "v_smfmac_f32_16x16x64_bf16 v[236:239], v[240:243], v[176:183], %[ix]\n\t" "ds_read_b128 v[176:179], %[l0] offset:20480\n\t" \
    "ds_read_b128 v[180:183], %[l0] offset:21504\n\t" "s_waitcnt lgkmcnt(12)\n\t" "v_smfmac_f32_16x16x64_bf16 v[224:227], v[244:247], a[112:119], %[ix]\n\t" \
    "ds_read_b128 v[240:243], %[l1] offset:14336\n\t" "ds_read_b128 v[24:27], %[kx] offset:4096\n\t" \
    "v_smfmac_f32_16x16x64_bf16 v[228:231], v[244:247], a[120:127], %[ix]\n\t" "v_smfmac_f32_16x16x64_bf16 v[232:235], v[244:247], v[88:95], %[ix]\n\t" \
    "v_smfmac_f32_16x16x64_bf16 v[236:239], v[244:247], v[184:191], %[ix]\n\t" "ds_read_b128 v[184:187], %[l0] offset:22528\n\t" \
    "ds_read_b128 v[188:191], %[l0] offset:23552\n\t" "s_waitcnt lgkmcnt(13)\n\t" "v_smfmac_f32_16x16x64_bf16 v[224:227], v[248:251], a[128:135], %[ix]\n\t" \
    "ds_read_b128 v[244:247], %[l1] offset:7168\n\t" "ds_read_b128 v[28:31], %[kx] offset:6144\n\t" "v_smfmac_f32_16x16x64_bf16 v[228:231], v[248:251], a[136:143], %[ix]\n\t" \
    "v_smfmac_f32_16x16x64_bf16 v[232:235], v[248:251], v[96:103], %[ix]\n\t" "ds_read_b128 v[200:203], %[kx] offset:256\n\t" \
    "v_smfmac_f32_16x16x64_bf16 v[236:239], v[248:251], v[160:167], %[ix]\n\t" "ds_read_b128 v[160:163], %[l0] offset:24576\n\t" \
    "ds_read_b128 v[164:167], %[l0] offset:25600\n\t" "s_waitcnt lgkmcnt(14)\n\t" "v_smfmac_f32_16x16x64_bf16 v[224:227], v[252:255], a[144:151], %[ix]\n\t" \
    "ds_read_b128 v[248:251], %[l1] offset:11264\n\t" "v_smfmac_f32_16x16x64_bf16 v[228:231], v[252:255], a[152:159], %[ix]\n\t" \
    "ds_read_b128 v[204:207], %[kx] offset:2304\n\t" "v_smfmac_f32_16x16x64_bf16 v[232:235], v[252:255], v[104:111], %[ix]\n\t" "s_waitcnt lgkmcnt(14)\n\t" \
    "v_smfmac_f32_16x16x64_bf16 v[236:239], v[252:255], v[168:175], %[ix]\n\t" "ds_read_b128 v[168:171], %[l0] offset:26624\n\t" \
    "ds_read_b128 v[172:175], %[l0] offset:27648\n\t" "v_smfmac_f32_16x16x64_bf16 v[224:227], v[192:195], a[160:167], %[ix]\n\t" \
    "ds_read_b128 v[252:255], %[l1] offset:15360\n\t" "v_smfmac_f32_16x16x64_bf16 v[228:231], v[192:195], a[168:175], %[ix]\n\t" \
    "v_smfmac_f32_16x16x64_bf16 v[224:227], v[196:199], a[176:183], %[ix]\n\t" "v_smfmac_f32_16x16x64_bf16 v[228:231], v[196:199], a[184:191], %[ix]\n\t" \
    "s_waitcnt lgkmcnt(13)\n\t" "v_smfmac_f32_16x16x64_bf16 v[224:227], v[240:243], a[192:199], %[ix]\n\t" \
    "v_smfmac_f32_16x16x64_bf16 v[228:231], v[240:243], a[200:207], %[ix]\n\t" "s_waitcnt lgkmcnt(9)\n\t" \
    "v_smfmac_f32_16x16x64_bf16 v[224:227], v[244:247], a[208:215], %[ix]\n\t" "v_smfmac_f32_16x16x64_bf16 v[228:231], v[244:247], a[216:223], %[ix]\n\t" \
    "s_waitcnt lgkmcnt(4)\n\t" "v_smfmac_f32_16x16x64_bf16 v[224:227], v[248:251], a[224:231], %[ix]\n\t" \
    "v_smfmac_f32_16x16x64_bf16 v[228:231], v[248:251], a[232:239], %[ix]\n\t" "s_waitcnt lgkmcnt(0)\n\t" \
    "v_smfmac_f32_16x16x64_bf16 v[224:227], v[252:255], a[240:247], %[ix]\n\t" "s_nop 1\n\t" "v_smfmac_f32_16x16x64_bf16 v[228:231], v[252:255], a[248:255], %[ix]\n\t" \
    XT(90, 91) "s_nop 1\n\t" "v_smfmac_f32_16x16x64_bf16 v[232:235], v[192:195], v[112:119], %[ix]\n\t" "s_nop 1\n\t" \
    "v_smfmac_f32_16x16x64_bf16 v[236:239], v[192:195], v[176:183], %[ix]\n\t" "ds_read_b128 v[176:179], %[l0] offset:28672\n\t" "v_add_f32 v224, v224, v225\n\t" \
    "v_add_f32 v226, v226, v227\n\t" "v_smfmac_f32_16x16x64_bf16 v[232:235], v[196:199], v[120:127], %[ix]\n\t" "ds_read_b128 v[180:183], %[l0] offset:29696\n\t" \
    "v_add_f32 v228, v228, v229\n\t" "v_add_f32 v230, v230, v231\n\t" "v_smfmac_f32_16x16x64_bf16 v[236:239], v[196:199], v[184:191], %[ix]\n\t" \
    "ds_read_b128 v[184:187], %[l0] offset:30720\n\t" "v_cvt_pk_bf16_f32 v218, v224, v228\n\t" "v_cvt_pk_bf16_f32 v219, v226, v230\n\t" \
    "v_smfmac_f32_16x16x64_bf16 v[232:235], v[240:243], v[128:135], %[ix]\n\t" "ds_write2_b32 %[qw], v218, v219 offset1:8\n\t" \
    "ds_read_b128 v[188:191], %[l0] offset:31744\n\t" "v_smfmac_f32_16x16x64_bf16 v[236:239], v[240:243], v[160:167], %[ix]\n\t" \
    "ds_read_b128 v[192:195], %[kx] offset:4352\n\t" "ds_read_b128 v[196:199], %[kx] offset:6400\n\t" \
    "v_smfmac_f32_16x16x64_bf16 v[232:235], v[244:247], v[136:143], %[ix]\n\t" "v_smfmac_f32_16x16x64_bf16 v[236:239], v[244:247], v[168:175], %[ix]\n\t" \
    "s_waitcnt lgkmcnt(3)\n\t" "s_barrier\n\t" "v_smfmac_f32_16x16x64_bf16 v[232:235], v[248:251], v[144:151], %[ix]\n\t" "ds_read_b128 v[216:219], %[l1] offset:24576\n\t" \
    "ds_read_b128 v[220:223], %[l1] offset:25600\n\t" "v_smfmac_f32_16x16x64_bf16 v[236:239], v[248:251], v[176:183], %[ix]\n\t" \
    "v_smfmac_f32_16x16x64_bf16 v[232:235], v[252:255], v[152:159], %[ix]\n\t" "s_waitcnt lgkmcnt(4)\n\t" \
    "v_smfmac_f32_16x16x64_bf16 v[236:239], v[252:255], v[184:191], %[ix]\n\t" XT(92, 93) "s_waitcnt lgkmcnt(1)\n\t" \
    "v_smfmac_f32_16x16x64_bf16 v[208:211], v[216:219], v[16:23], %[ix]\n\t" "s_nop 1\n\t" "v_smfmac_f32_16x16x64_bf16 v[212:215], v[216:219], v[200:207], %[ix]\n\t" \
    "s_waitcnt lgkmcnt(0)\n\t" "s_nop 0\n\t" "v_add_f32 v232, v232, v233\n\t" "v_add_f32 v234, v234, v235\n\t" "v_add_f32 v236, v236, v237\n\t" \
    "v_add_f32 v238, v238, v239\n\t" "v_cvt_pk_bf16_f32 v232, v232, v236\n\t" "v_cvt_pk_bf16_f32 v233, v234, v238\n\t" "ds_write2_b32 %[kw], v232, v233 offset1:68\n\t" \
    "v_smfmac_f32_16x16x64_bf16 v[208:211], v[220:223], v[24:31], %[ix]\n\t" "v_smfmac_f32_16x16x64_bf16 v[212:215], v[220:223], v[192:199], %[ix]\n\t" \
    "v_mov_b64 v[224:225], 0\n\t" "v_mov_b64 v[226:227], 0\n\t" "s_waitcnt lgkmcnt(0)\n\t" "s_barrier\n\t" XT(94, 95) "ds_read_b128 v[160:163], %[km] offset:0\n\t" \
    "ds_read_b128 v[164:167], %[km] offset:64\n\t" "ds_read_b128 v[168:171], %[km] offset:128\n\t" "ds_read_b128 v[172:175], %[km] offset:192\n\t" \
    "v_add_f32 v178, v208, v209\n\t" "v_add_f32 v179, v210, v211\n\t" "v_add_f32 v180, v212, v213\n\t" "v_add_f32 v181, v214, v215\n\t" "v_max_f32 v182, v178, v180\n\t" \
    "v_max_f32 v183, v179, v181\n\t" "s_nop 0\n\t" "v_max_f32_dpp v182, v182, v182 row_ror:8 row_mask:0xf bank_mask:0xf\n\t" \
    "v_max_f32_dpp v183, v183, v183 row_ror:8 row_mask:0xf bank_mask:0xf\n\t" "s_nop 0\n\t" "v_max_f32_dpp v182, v182, v182 row_ror:4 row_mask:0xf bank_mask:0xf\n\t" \
    "v_max_f32_dpp v183, v183, v183 row_ror:4 row_mask:0xf bank_mask:0xf\n\t" "s_nop 0\n\t" "v_max_f32_dpp v182, v182, v182 row_ror:2 row_mask:0xf bank_mask:0xf\n\t" \
    "v_max_f32_dpp v183, v183, v183 row_ror:2 row_mask:0xf bank_mask:0xf\n\t" "s_nop 0\n\t" "v_max_f32_dpp v182, v182, v182 row_ror:1 row_mask:0xf bank_mask:0xf\n\t" \
    "v_max_f32_dpp v183, v183, v183 row_ror:1 row_mask:0xf bank_mask:0xf\n\t" "s_waitcnt lgkmcnt(2)\n\t" \
    "v_smfmac_f32_16x16x64_bf16 v[224:227], v[216:219], v[160:167], %[ix]\n\t" "s_waitcnt lgkmcnt(0)\n\t" \
    "v_smfmac_f32_16x16x64_bf16 v[224:227], v[220:223], v[168:175], %[ix]\n\t" "s_nop 7\n\t" "v_add_f32 v176, v224, v225\n\t" "v_add_f32 v177, v226, v227\n\t" \
    "v_max_f32 v182, v176, v182\n\t" "v_max_f32 v183, v177, v183\n\t" "s_nop 0\n\t" "v_max_f32_dpp v182, v182, v182 row_ror:4 row_mask:0xf bank_mask:0xf\n\t" \
    "v_max_f32_dpp v183, v183, v183 row_ror:4 row_mask:0xf bank_mask:0xf\n\t" "s_nop 0\n\t" "v_max_f32_dpp v182, v182, v182 row_ror:2 row_mask:0xf bank_mask:0xf\n\t" \
    "v_max_f32_dpp v183, v183, v183 row_ror:2 row_mask:0xf bank_mask:0xf\n\t" "s_nop 0\n\t" "v_max_f32_dpp v182, v182, v182 row_ror:1 row_mask:0xf bank_mask:0xf\n\t" \
    "v_max_f32_dpp v183, v183, v183 row_ror:1 row_mask:0xf bank_mask:0xf\n\t" "v_pk_add_f32 v[178:179], v[178:179], v[182:183] neg_lo:[0,1] neg_hi:[0,1]\n\t" \
    "v_pk_add_f32 v[180:181], v[180:181], v[182:183] neg_lo:[0,1] neg_hi:[0,1]\n\t" "v_pk_add_f32 v[176:177], v[176:177], v[182:183] neg_lo:[0,1] neg_hi:[0,1]\n\t" \
    "v_pk_mul_f32 v[178:179], v[178:179], s[66:67]\n\t" "v_pk_mul_f32 v[180:181], v[180:181], s[66:67]\n\t" "v_pk_mul_f32 v[176:177], v[176:177], s[66:67]\n\t" \
    "v_exp_f32 v178, v178\n\t" "v_exp_f32 v179, v179\n\t" "v_exp_f32 v180, v180\n\t" "v_exp_f32 v181, v181\n\t" "v_exp_f32 v176, v176\n\t" \
    "v_pk_add_f32 v[182:183], v[178:179], v[180:181]\n\t" "v_exp_f32 v177, v177\n\t" "s_nop 0\n\t" "v_add_f32_dpp v182, v182, v182 row_ror:8 row_mask:0xf bank_mask:0xf\n\t" \
    "v_add_f32_dpp v183, v183, v183 row_ror:8 row_mask:0xf bank_mask:0xf\n\t" "v_add_f32 v182, v182, v176\n\t" "v_add_f32 v183, v183, v177\n\t" "s_nop 0\n\t" \
    "v_add_f32_dpp v182, v182, v182 row_ror:4 row_mask:0xf bank_mask:0xf\n\t" "v_add_f32_dpp v183, v183, v183 row_ror:4 row_mask:0xf bank_mask:0xf\n\t" "s_nop 0\n\t" \
    "v_add_f32_dpp v182, v182, v182 row_ror:2 row_mask:0xf bank_mask:0xf\n\t" "v_add_f32_dpp v183, v183, v183 row_ror:2 row_mask:0xf bank_mask:0xf\n\t" "s_nop 0\n\t" \
    "v_add_f32_dpp v182, v182, v182 row_ror:1 row_mask:0xf bank_mask:0xf\n\t" "v_add_f32_dpp v183, v183, v183 row_ror:1 row_mask:0xf bank_mask:0xf\n\t" \
    "v_rcp_f32 v182, v182\n\t" "v_rcp_f32 v183, v183\n\t" "s_nop 0\n\t" "v_pk_mul_f32 v[176:177], v[176:177], v[182:183]\n\t" \
    "v_pk_mul_f32 v[178:179], v[178:179], v[182:183]\n\t" "v_pk_mul_f32 v[180:181], v[180:181], v[182:183]\n\t" "v_cvt_pk_bf16_f32 v176, v176, v177\n\t" \
    "v_cvt_pk_bf16_f32 v178, v178, v179\n\t" "v_cvt_pk_bf16_f32 v180, v180, v181\n\t" "v_mov_b32_dpp v177, v176 quad_perm:[1,0,3,2] row_mask:0xf bank_mask:0xf\n\t" \
    "v_mov_b32_dpp v179, v178 quad_perm:[1,0,3,2] row_mask:0xf bank_mask:0xf\n\t" "v_mov_b32_dpp v181, v180 quad_perm:[1,0,3,2] row_mask:0xf bank_mask:0xf\n\t" \
    "v_perm_b32 v184, v179, v178, %[ps]\n\t" "v_perm_b32 v185, v181, v180, %[ps]\n\t" "buffer_store_dwordx2 v[184:185], %[px], %[rs], %[pb] offen sc1\n\t" \
    "v_perm_b32 v186, v177, v176, %[ps]\n\t" "buffer_store_dword v186, %[pm], %[rs], %[pb] offen sc1\n\t" XT(96, 97) "s_branch 99f\n\t" "11:\n\t" "s_cmp_eq_u32 %[bd], 0\n\t" \
    "s_cbranch_scc1 16f\n\t" "s_sub_u32 %[bd], %[bd], 1\n\t" XCNT(100) "s_waitcnt lgkmcnt(0)\n\t" "s_nop 7\n\t" "s_branch 10b\n\t" "16:\n\t" "s_cmp_lg_u32 s60, 0\n\t" \
    "s_branch 12b\n\t" "14:\n\t" "s_cmp_eq_u32 %[bd], 0\n\t" "s_cbranch_scc1 15b\n\t" "s_sub_u32 %[bd], %[bd], 1\n\t" XCNT(101) "s_mov_b32 s60, 1\n\t" \
    "s_waitcnt lgkmcnt(0)\n\t" "s_nop 7\n\t" "s_branch 10b\n\t" "99:\n\t"
#define QK_STEP1 \
    "v_xor_b32 v216, 0x400, %[mo]\n\t" "s_mov_b32 s60, 0\n\t" "s_add_u32 s61, %[qb], 0x1000\n\t" "s_mov_b32 s66, 0x3e0293ee\n\t" "s_mov_b32 s67, 0x3e0293ee\n\t" XT(80, 81) \
    XT(98, 99) "s_barrier\n\t" XT(82, 83) "20:\n\t" "buffer_load_dwordx4 v[240:243], %[mo], %[rs], %[sb] offen offset:0 sc1\n\t" "v_mov_b64 v[224:225], 0\n\t" \
    "v_mov_b64 v[226:227], 0\n\t" "v_mov_b64 v[228:229], 0\n\t" "v_mov_b64 v[230:231], 0\n\t" "v_mov_b64 v[232:233], 0\n\t" "v_mov_b64 v[234:235], 0\n\t" \
    "v_mov_b64 v[236:237], 0\n\t" "v_mov_b64 v[238:239], 0\n\t" "buffer_load_dwordx4 v[244:247], v216, %[rs], %[sb] offen offset:0 sc1\n\t" \
    "ds_read_b128 v[160:163], %[l0] offset:0\n\t" "ds_read_b128 v[164:167], %[l0] offset:1024\n\t" "ds_read_b128 v[168:171], %[l0] offset:2048\n\t" \
    "ds_read_b128 v[172:175], %[l0] offset:3072\n\t" "s_nop 3\n\t" "buffer_load_dwordx4 v[248:251], %[mo], %[rs], %[sb] offen offset:2048 sc1\n\t" \
    "ds_read_b128 v[176:179], %[l0] offset:4096\n\t" "ds_read_b128 v[180:183], %[l0] offset:5120\n\t" "ds_read_b128 v[184:187], %[l0] offset:6144\n\t" \
    "ds_read_b128 v[188:191], %[l0] offset:7168\n\t" "s_nop 3\n\t" "buffer_load_dwordx4 v[252:255], v216, %[rs], %[sb] offen offset:2048 sc1\n\t" \
    "v_mov_b32 v208, 0xd028965f\n\t" "v_mov_b32 v209, 0xff800000\n\t" "s_mov_b32 s62, 0\n\t" "s_mov_b32 s63, -1\n\t" "v_mov_b32 v210, 0\n\t" "v_cmp_gt_i32 vcc, 0, %[ps]\n\t" \
    "v_cndmask_b32 v204, 0, v209, s[62:63]\n\t" "s_nop 0\n\t" "v_cndmask_b32 v200, 0, v208, vcc\n\t" "v_cmp_gt_i32 vcc, 1, %[ps]\n\t" \
    "v_cndmask_b32 v205, 0, v209, s[62:63]\n\t" "s_nop 0\n\t" "v_cndmask_b32 v201, 0, v208, vcc\n\t" "v_cmp_gt_i32 vcc, 2, %[ps]\n\t" \
    "v_cndmask_b32 v206, 0, v209, s[62:63]\n\t" "s_nop 0\n\t" "v_cndmask_b32 v202, 0, v208, vcc\n\t" "v_cmp_gt_i32 vcc, 3, %[ps]\n\t" \
    "v_cndmask_b32 v207, 0, v209, s[62:63]\n\t" "s_nop 0\n\t" "v_cndmask_b32 v203, 0, v208, vcc\n\t" "s_waitcnt vmcnt(3)\n\t" XT(84, 85) \
    "v_smfmac_f32_16x16x64_bf16 v[224:227], v[240:243], a[0:7], %[ix]\n\t" "ds_write_b128 %[l1], v[240:243] offset:4096\n\t" \
    "v_smfmac_f32_16x16x64_bf16 v[228:231], v[240:243], a[8:15], %[ix]\n\t" "v_max3_u32 v217, v240, v241, v242\n\t" \
    "v_smfmac_f32_16x16x64_bf16 v[232:235], v[240:243], v[32:39], %[ix]\n\t" "s_waitcnt lgkmcnt(7)\n\t" \
    "v_smfmac_f32_16x16x64_bf16 v[236:239], v[240:243], v[160:167], %[ix]\n\t" "ds_read_b128 v[160:163], %[l0] offset:8192\n\t" \
    "ds_read_b128 v[164:167], %[l0] offset:9216\n\t" "s_waitcnt vmcnt(2)\n\t" "v_smfmac_f32_16x16x64_bf16 v[224:227], v[244:247], a[16:23], %[ix]\n\t" \
    "ds_write_b128 %[l1], v[244:247] offset:5120\n\t" "v_max3_u32 v217, v217, v243, v244\n\t" "v_smfmac_f32_16x16x64_bf16 v[228:231], v[244:247], a[24:31], %[ix]\n\t" \
    "v_max3_u32 v217, v217, v245, v246\n\t" "v_max_u32 v217, v217, v247\n\t" "v_smfmac_f32_16x16x64_bf16 v[232:235], v[244:247], v[40:47], %[ix]\n\t" \
    "v_cmp_eq_u32 vcc, -1, v217\n\t" "s_cmp_lg_u32 s60, 0\n\t" "s_waitcnt lgkmcnt(8)\n\t" "v_smfmac_f32_16x16x64_bf16 v[236:239], v[244:247], v[168:175], %[ix]\n\t" \
    "s_cbranch_vccnz 21f\n\t" "22:\n\t" "ds_read_b128 v[168:171], %[l0] offset:10240\n\t" "s_waitcnt vmcnt(1)\n\t" \
    "v_smfmac_f32_16x16x64_bf16 v[224:227], v[248:251], a[32:39], %[ix]\n\t" "ds_write_b128 %[l1], v[248:251] offset:6144\n\t" "s_waitcnt lgkmcnt(2)\n\t" \
    "v_smfmac_f32_16x16x64_bf16 v[228:231], v[248:251], a[40:47], %[ix]\n\t" "s_cbranch_scc1 23f\n\t" "s_barrier\n\t" "23:\n\t" XT(86, 87) \
    "v_smfmac_f32_16x16x64_bf16 v[232:235], v[248:251], v[48:55], %[ix]\n\t" "ds_read_b128 v[192:195], %[l1] offset:8192\n\t" \
    "ds_read_b128 v[196:199], %[l1] offset:12288\n\t" "v_smfmac_f32_16x16x64_bf16 v[236:239], v[248:251], v[176:183], %[ix]\n\t" \
    "ds_read_b128 v[172:175], %[l0] offset:11264\n\t" "ds_read_b128 v[176:179], %[l0] offset:12288\n\t" "s_waitcnt vmcnt(0)\n\t" \
    "v_smfmac_f32_16x16x64_bf16 v[224:227], v[252:255], a[48:55], %[ix]\n\t" "ds_write_b128 %[l1], v[252:255] offset:7168\n\t" "v_max3_u32 v217, v248, v249, v250\n\t" \
    "v_smfmac_f32_16x16x64_bf16 v[228:231], v[252:255], a[56:63], %[ix]\n\t" "ds_read_b128 v[180:183], %[l0] offset:13312\n\t" "v_max3_u32 v217, v217, v251, v252\n\t" \
    "v_max3_u32 v217, v217, v253, v254\n\t" "v_smfmac_f32_16x16x64_bf16 v[232:235], v[252:255], v[56:63], %[ix]\n\t" "v_max_u32 v217, v217, v255\n\t" \
    "v_cmp_eq_u32 vcc, -1, v217\n\t" "v_smfmac_f32_16x16x64_bf16 v[236:239], v[252:255], v[184:191], %[ix]\n\t" "s_cbranch_vccnz 24f\n\t" "25:\n\t" \
    "ds_read_b128 v[240:243], %[l1] offset:0\n\t" "s_waitcnt lgkmcnt(6)\n\t" "v_smfmac_f32_16x16x64_bf16 v[224:227], v[192:195], a[64:71], %[ix]\n\t" \
    "ds_read_b128 v[184:187], %[l0] offset:14336\n\t" "ds_read_b128 v[188:191], %[l0] offset:15360\n\t" \
    "v_smfmac_f32_16x16x64_bf16 v[228:231], v[192:195], a[72:79], %[ix]\n\t" "ds_read_b128 v[244:247], %[l1] offset:9216\n\t" "s_waitcnt lgkmcnt(5)\n\t" \
    "v_smfmac_f32_16x16x64_bf16 v[232:235], v[192:195], v[64:71], %[ix]\n\t" "s_barrier\n\t" XT(88, 89) \
    "v_smfmac_f32_16x16x64_bf16 v[236:239], v[192:195], v[160:167], %[ix]\n\t" "ds_read_b128 v[160:163], %[l0] offset:16384\n\t" \
    "ds_read_b128 v[164:167], %[l0] offset:17408\n\t" "v_smfmac_f32_16x16x64_bf16 v[224:227], v[196:199], a[80:87], %[ix]\n\t" \
    "ds_read_b128 v[248:251], %[l1] offset:13312\n\t" "ds_read_b128 v[252:255], %[l1] offset:1024\n\t" \
    "v_smfmac_f32_16x16x64_bf16 v[228:231], v[196:199], a[88:95], %[ix]\n\t" "ds_read_b128 v[192:195], %[l1] offset:10240\n\t" \
    "v_smfmac_f32_16x16x64_bf16 v[232:235], v[196:199], v[72:79], %[ix]\n\t" "buffer_load_dwordx4 v[16:19], %[hq], %[rs], %[qb] offen offset:0 sc1\n\t" \
    "v_smfmac_f32_16x16x64_bf16 v[236:239], v[196:199], v[168:175], %[ix]\n\t" "ds_read_b128 v[168:171], %[l0] offset:18432\n\t" \
    "ds_read_b128 v[172:175], %[l0] offset:19456\n\t" "s_waitcnt lgkmcnt(10)\n\t" "v_smfmac_f32_16x16x64_bf16 v[224:227], v[240:243], a[96:103], %[ix]\n\t" \
    "ds_read_b128 v[196:199], %[l1] offset:14336\n\t" "v_smfmac_f32_16x16x64_bf16 v[228:231], v[240:243], a[104:111], %[ix]\n\t" \
    "v_smfmac_f32_16x16x64_bf16 v[232:235], v[240:243], v[80:87], %[ix]\n\t" "buffer_load_dwordx4 v[20:23], %[hq], %[rs], %[qb] offen offset:2048 sc1\n\t" \
    "v_smfmac_f32_16x16x64_bf16 v[236:239], v[240:243], v[176:183], %[ix]\n\t" "ds_read_b128 v[176:179], %[l0] offset:20480\n\t" \
    "ds_read_b128 v[180:183], %[l0] offset:21504\n\t" "s_waitcnt lgkmcnt(10)\n\t" "v_smfmac_f32_16x16x64_bf16 v[224:227], v[244:247], a[112:119], %[ix]\n\t" \
    "ds_read_b128 v[240:243], %[l1] offset:2048\n\t" "v_smfmac_f32_16x16x64_bf16 v[228:231], v[244:247], a[120:127], %[ix]\n\t" \
    "v_smfmac_f32_16x16x64_bf16 v[232:235], v[244:247], v[88:95], %[ix]\n\t" "buffer_load_dwordx4 v[24:27], %[hq], %[rs], s61 offen offset:0 sc1\n\t" \
    "v_smfmac_f32_16x16x64_bf16 v[236:239], v[244:247], v[184:191], %[ix]\n\t" "ds_read_b128 v[184:187], %[l0] offset:22528\n\t" \
    "ds_read_b128 v[188:191], %[l0] offset:23552\n\t" "s_waitcnt lgkmcnt(10)\n\t" "v_smfmac_f32_16x16x64_bf16 v[224:227], v[248:251], a[128:135], %[ix]\n\t" \
    "ds_read_b128 v[244:247], %[l1] offset:11264\n\t" "v_smfmac_f32_16x16x64_bf16 v[228:231], v[248:251], a[136:143], %[ix]\n\t" \
    "v_smfmac_f32_16x16x64_bf16 v[232:235], v[248:251], v[96:103], %[ix]\n\t" "buffer_load_dwordx4 v[28:31], %[hq], %[rs], s61 offen offset:2048 sc1\n\t" \
    "v_smfmac_f32_16x16x64_bf16 v[236:239], v[248:251], v[160:167], %[ix]\n\t" "ds_read_b128 v[160:163], %[l0] offset:24576\n\t" \
    "ds_read_b128 v[164:167], %[l0] offset:25600\n\t" "s_waitcnt lgkmcnt(12)\n\t" "v_smfmac_f32_16x16x64_bf16 v[224:227], v[252:255], a[144:151], %[ix]\n\t" \
    "ds_read_b128 v[248:251], %[l1] offset:15360\n\t" "v_smfmac_f32_16x16x64_bf16 v[228:231], v[252:255], a[152:159], %[ix]\n\t" \
    "v_smfmac_f32_16x16x64_bf16 v[232:235], v[252:255], v[104:111], %[ix]\n\t" "s_waitcnt lgkmcnt(10)\n\t" \
    "v_smfmac_f32_16x16x64_bf16 v[236:239], v[252:255], v[168:175], %[ix]\n\t" "ds_read_b128 v[168:171], %[l0] offset:26624\n\t" \
    "ds_read_b128 v[172:175], %[l0] offset:27648\n\t" "v_smfmac_f32_16x16x64_bf16 v[224:227], v[192:195], a[160:167], %[ix]\n\t" \
    "ds_read_b128 v[252:255], %[l1] offset:3072\n\t" "v_smfmac_f32_16x16x64_bf16 v[228:231], v[192:195], a[168:175], %[ix]\n\t" "s_waitcnt lgkmcnt(12)\n\t" \
    "v_smfmac_f32_16x16x64_bf16 v[224:227], v[196:199], a[176:183], %[ix]\n\t" "v_smfmac_f32_16x16x64_bf16 v[228:231], v[196:199], a[184:191], %[ix]\n\t" \
    "s_waitcnt lgkmcnt(9)\n\t" "v_smfmac_f32_16x16x64_bf16 v[224:227], v[240:243], a[192:199], %[ix]\n\t" \
    "v_smfmac_f32_16x16x64_bf16 v[228:231], v[240:243], a[200:207], %[ix]\n\t" "s_waitcnt lgkmcnt(6)\n\t" \
    "v_smfmac_f32_16x16x64_bf16 v[224:227], v[244:247], a[208:215], %[ix]\n\t" "v_smfmac_f32_16x16x64_bf16 v[228:231], v[244:247], a[216:223], %[ix]\n\t" \
    "s_waitcnt lgkmcnt(3)\n\t" "v_smfmac_f32_16x16x64_bf16 v[224:227], v[248:251], a[224:231], %[ix]\n\t" \
    "v_smfmac_f32_16x16x64_bf16 v[228:231], v[248:251], a[232:239], %[ix]\n\t" "s_waitcnt lgkmcnt(0)\n\t" \
    "v_smfmac_f32_16x16x64_bf16 v[224:227], v[252:255], a[240:247], %[ix]\n\t" "s_nop 1\n\t" "v_smfmac_f32_16x16x64_bf16 v[228:231], v[252:255], a[248:255], %[ix]\n\t" \
    XT(90, 91) "s_nop 1\n\t" "v_smfmac_f32_16x16x64_bf16 v[232:235], v[192:195], v[112:119], %[ix]\n\t" "s_nop 1\n\t" \
    "v_smfmac_f32_16x16x64_bf16 v[236:239], v[192:195], v[176:183], %[ix]\n\t" "ds_read_b128 v[176:179], %[l0] offset:28672\n\t" "v_add_f32 v224, v224, v225\n\t" \
    "v_add_f32 v226, v226, v227\n\t" "v_smfmac_f32_16x16x64_bf16 v[232:235], v[196:199], v[120:127], %[ix]\n\t" "ds_read_b128 v[180:183], %[l0] offset:29696\n\t" \
    "v_add_f32 v228, v228, v229\n\t" "v_add_f32 v230, v230, v231\n\t" "v_smfmac_f32_16x16x64_bf16 v[236:239], v[196:199], v[184:191], %[ix]\n\t" \
    "ds_read_b128 v[184:187], %[l0] offset:30720\n\t" "v_cvt_pk_bf16_f32 v218, v224, v228\n\t" "v_cvt_pk_bf16_f32 v219, v226, v230\n\t" \
    "v_smfmac_f32_16x16x64_bf16 v[232:235], v[240:243], v[128:135], %[ix]\n\t" "ds_write2_b32 %[qw], v218, v219 offset1:8\n\t" \
    "ds_read_b128 v[188:191], %[l0] offset:31744\n\t" "v_smfmac_f32_16x16x64_bf16 v[236:239], v[240:243], v[160:167], %[ix]\n\t" \
    "v_smfmac_f32_16x16x64_bf16 v[232:235], v[244:247], v[136:143], %[ix]\n\t" "v_smfmac_f32_16x16x64_bf16 v[236:239], v[244:247], v[168:175], %[ix]\n\t" \
    "s_waitcnt lgkmcnt(1)\n\t" "s_barrier\n\t" "v_smfmac_f32_16x16x64_bf16 v[232:235], v[248:251], v[144:151], %[ix]\n\t" \
    "v_smfmac_f32_16x16x64_bf16 v[236:239], v[248:251], v[176:183], %[ix]\n\t" "v_smfmac_f32_16x16x64_bf16 v[232:235], v[252:255], v[152:159], %[ix]\n\t" \
    "s_waitcnt lgkmcnt(0)\n\t" "v_smfmac_f32_16x16x64_bf16 v[236:239], v[252:255], v[184:191], %[ix]\n\t" XT(92, 93) "s_nop 6\n\t" "v_add_f32 v232, v232, v233\n\t" \
    "v_add_f32 v234, v234, v235\n\t" "v_add_f32 v236, v236, v237\n\t" "v_add_f32 v238, v238, v239\n\t" "v_cvt_pk_bf16_f32 v232, v232, v236\n\t" \
    "v_cvt_pk_bf16_f32 v233, v234, v238\n\t" "ds_write2_b32 %[kw], v232, v233 offset1:68\n\t" "s_waitcnt lgkmcnt(0)\n\t" "s_barrier\n\t" XT(94, 95) \
    "s_cmp_ge_u32 %[sq], %[sn]\n\t" "s_cbranch_scc1 277f\n\t" "275:\n\t" "global_load_dword v240, v210, %[fq] sc1\n\t" "s_waitcnt vmcnt(0)\n\t" \
    "v_readfirstlane_b32 %[sq], v240\n\t" "s_nop 3\n\t" "s_cmp_ge_u32 %[sq], %[sn]\n\t" "s_cbranch_scc1 276f\n\t" "s_cmp_eq_u32 %[bd], 0\n\t" "s_cbranch_scc1 276f\n\t" \
    "s_sub_u32 %[bd], %[bd], 1\n\t" "s_sleep 2\n\t" "s_branch 275b\n\t" "276:\n\t" "buffer_load_dwordx4 v[16:19], %[hq], %[rs], %[qb] offen offset:0 sc1\n\t" \
    "buffer_load_dwordx4 v[20:23], %[hq], %[rs], %[qb] offen offset:2048 sc1\n\t" "buffer_load_dwordx4 v[24:27], %[hq], %[rs], s61 offen offset:0 sc1\n\t" \
    "buffer_load_dwordx4 v[28:31], %[hq], %[rs], s61 offen offset:2048 sc1\n\t" "277:\n\t" "s_nop 3\n\t" "ds_read_b128 v[160:163], %[kx] offset:0\n\t" \
    "ds_read_b128 v[164:167], %[kx] offset:2048\n\t" "ds_read_b128 v[168:171], %[kx] offset:4096\n\t" "ds_read_b128 v[172:175], %[kx] offset:6144\n\t" \
    "ds_read_b128 v[224:227], %[km] offset:0\n\t" "ds_read_b128 v[228:231], %[km] offset:64\n\t" "ds_read_b128 v[232:235], %[km] offset:128\n\t" \
    "ds_read_b128 v[236:239], %[km] offset:192\n\t" "s_waitcnt vmcnt(0)\n\t" "global_load_dword v240, v210, %[fq] sc1\n\t" "s_waitcnt lgkmcnt(7)\n\t" \
    "v_mfma_f32_16x16x32_bf16 v[248:251], v[160:163], v[16:19], v[200:203]\n\t" "s_waitcnt lgkmcnt(6)\n\t" \
    "v_mfma_f32_16x16x32_bf16 v[248:251], v[164:167], v[20:23], v[248:251]\n\t" "s_waitcnt lgkmcnt(5)\n\t" \
    "v_mfma_f32_16x16x32_bf16 v[248:251], v[168:171], v[24:27], v[248:251]\n\t" "s_waitcnt lgkmcnt(4)\n\t" \
    "v_mfma_f32_16x16x32_bf16 v[248:251], v[172:175], v[28:31], v[248:251]\n\t" "s_waitcnt lgkmcnt(3)\n\t" \
    "v_mfma_f32_16x16x32_bf16 v[244:247], v[224:227], v[16:19], v[204:207]\n\t" "s_waitcnt lgkmcnt(2)\n\t" \
    "v_mfma_f32_16x16x32_bf16 v[244:247], v[228:231], v[20:23], v[244:247]\n\t" "s_waitcnt lgkmcnt(1)\n\t" \
    "v_mfma_f32_16x16x32_bf16 v[244:247], v[232:235], v[24:27], v[244:247]\n\t" "s_waitcnt lgkmcnt(0)\n\t" \
    "v_mfma_f32_16x16x32_bf16 v[244:247], v[236:239], v[28:31], v[244:247]\n\t" "v_mov_b32 v192, -1\n\t" "v_mov_b32 v193, -1\n\t" "v_mov_b32 v194, -1\n\t" \
    "v_mov_b32 v195, -1\n\t" "v_max3_f32 v242, v248, v249, v250\n\t" "v_max_f32 v242, v242, v251\n\t" "s_nop 1\n\t" "v_max3_f32 v242, v242, v244, v245\n\t" \
    "v_max3_f32 v242, v242, v246, v247\n\t" "v_mov_b32 v243, v242\n\t" "s_nop 1\n\t" "v_permlane32_swap_b32 v242, v243\n\t" "v_max_f32 v242, v242, v243\n\t" \
    "v_mov_b32 v243, v242\n\t" "s_nop 1\n\t" "v_permlane16_swap_b32 v242, v243\n\t" "v_max_f32 v242, v242, v243\n\t" \
    "v_pk_add_f32 v[244:245], v[244:245], v[242:243] op_sel_hi:[1,0] neg_lo:[0,1] neg_hi:[0,1]\n\t" \
    "v_pk_add_f32 v[246:247], v[246:247], v[242:243] op_sel_hi:[1,0] neg_lo:[0,1] neg_hi:[0,1]\n\t" \
    "v_pk_add_f32 v[248:249], v[248:249], v[242:243] op_sel_hi:[1,0] neg_lo:[0,1] neg_hi:[0,1]\n\t" \
    "v_pk_add_f32 v[250:251], v[250:251], v[242:243] op_sel_hi:[1,0] neg_lo:[0,1] neg_hi:[0,1]\n\t" "v_pk_mul_f32 v[244:245], v[244:245], s[66:67]\n\t" \
    "v_pk_mul_f32 v[246:247], v[246:247], s[66:67]\n\t" "v_pk_mul_f32 v[248:249], v[248:249], s[66:67]\n\t" "v_pk_mul_f32 v[250:251], v[250:251], s[66:67]\n\t" \
    "v_exp_f32 v244, v244\n\t" "v_exp_f32 v245, v245\n\t" "v_exp_f32 v246, v246\n\t" "v_exp_f32 v247, v247\n\t" "v_exp_f32 v248, v248\n\t" "v_exp_f32 v249, v249\n\t" \
    "v_exp_f32 v250, v250\n\t" "v_exp_f32 v251, v251\n\t" "v_pk_add_f32 v[242:243], v[244:245], v[246:247]\n\t" "v_pk_add_f32 v[242:243], v[242:243], v[248:249]\n\t" \
    "v_pk_add_f32 v[242:243], v[242:243], v[250:251]\n\t" "v_add_f32 v242, v242, v243\n\t" "v_mov_b32 v243, v242\n\t" "s_nop 1\n\t" "v_permlane32_swap_b32 v242, v243\n\t" \
    "v_add_f32 v242, v242, v243\n\t" "v_mov_b32 v243, v242\n\t" "s_nop 1\n\t" "v_permlane16_swap_b32 v242, v243\n\t" "v_add_f32 v242, v242, v243\n\t" \
    "v_rcp_f32 v242, v242\n\t" "s_nop 0\n\t" "v_pk_mul_f32 v[244:245], v[244:245], v[242:243] op_sel_hi:[1,0]\n\t" \
    "v_pk_mul_f32 v[246:247], v[246:247], v[242:243] op_sel_hi:[1,0]\n\t" "v_pk_mul_f32 v[248:249], v[248:249], v[242:243] op_sel_hi:[1,0]\n\t" \
    "v_pk_mul_f32 v[250:251], v[250:251], v[242:243] op_sel_hi:[1,0]\n\t" "v_cvt_pk_bf16_f32 v162, v244, v245\n\t" "v_cvt_pk_bf16_f32 v163, v246, v247\n\t" \
    "v_cvt_pk_bf16_f32 v164, v248, v249\n\t" "v_cvt_pk_bf16_f32 v165, v250, v251\n\t" "buffer_store_dwordx2 v[164:165], %[px], %[rs], %[tb] offen sc1\n\t" \
    "buffer_store_dwordx2 v[162:163], %[pm], %[rs], %[tb] offen sc1\n\t" "buffer_store_dwordx2 v[192:193], %[px], %[rs], %[tr] offen sc1\n\t" \
    "buffer_store_dwordx2 v[192:193], %[pm], %[rs], %[tr] offen sc1\n\t" "s_waitcnt vmcnt(4)\n\t" "v_readfirstlane_b32 s62, v240\n\t" "s_nop 3\n\t" \
    "s_max_u32 %[sq], %[sq], s62\n\t" XT(96, 97) "s_branch 99f\n\t" "21:\n\t" "s_cmp_eq_u32 %[bd], 0\n\t" "s_cbranch_scc1 26f\n\t" "s_sub_u32 %[bd], %[bd], 1\n\t" XCNT(100) \
    "s_waitcnt lgkmcnt(0)\n\t" "s_nop 7\n\t" "s_branch 20b\n\t" "26:\n\t" "s_cmp_lg_u32 s60, 0\n\t" "s_branch 22b\n\t" "24:\n\t" "s_cmp_eq_u32 %[bd], 0\n\t" \
    "s_cbranch_scc1 25b\n\t" "s_sub_u32 %[bd], %[bd], 1\n\t" XCNT(101) "s_mov_b32 s60, 1\n\t" "s_waitcnt lgkmcnt(0)\n\t" "s_nop 7\n\t" "s_branch 20b\n\t" "99:\n\t"
#define QK_STEP2 \
    "v_xor_b32 v216, 0x400, %[mo]\n\t" "s_mov_b32 s60, 0\n\t" "s_add_u32 s61, %[qb], 0x1000\n\t" "s_mov_b32 s66, 0x3e0293ee\n\t" "s_mov_b32 s67, 0x3e0293ee\n\t" XT(80, 81) \
    XT(98, 99) "s_barrier\n\t" XT(82, 83) "30:\n\t" "buffer_load_dwordx4 v[240:243], %[mo], %[rs], %[sb] offen offset:0 sc1\n\t" "v_mov_b64 v[224:225], 0\n\t" \
    "v_mov_b64 v[226:227], 0\n\t" "v_mov_b64 v[228:229], 0\n\t" "v_mov_b64 v[230:231], 0\n\t" "v_mov_b64 v[232:233], 0\n\t" "v_mov_b64 v[234:235], 0\n\t" \
    "v_mov_b64 v[236:237], 0\n\t" "v_mov_b64 v[238:239], 0\n\t" "buffer_load_dwordx4 v[244:247], v216, %[rs], %[sb] offen offset:0 sc1\n\t" \
    "ds_read_b128 v[160:163], %[l0] offset:0\n\t" "ds_read_b128 v[164:167], %[l0] offset:1024\n\t" "ds_read_b128 v[168:171], %[l0] offset:2048\n\t" \
    "ds_read_b128 v[172:175], %[l0] offset:3072\n\t" "s_nop 3\n\t" "buffer_load_dwordx4 v[248:251], %[mo], %[rs], %[sb] offen offset:2048 sc1\n\t" \
    "ds_read_b128 v[176:179], %[l0] offset:4096\n\t" "ds_read_b128 v[180:183], %[l0] offset:5120\n\t" "ds_read_b128 v[184:187], %[l0] offset:6144\n\t" \
    "ds_read_b128 v[188:191], %[l0] offset:7168\n\t" "s_nop 3\n\t" "buffer_load_dwordx4 v[252:255], v216, %[rs], %[sb] offen offset:2048 sc1\n\t" \
    "v_mov_b32 v208, 0xd028965f\n\t" "v_mov_b32 v209, 0xff800000\n\t" "s_mov_b32 s62, 0\n\t" "s_mov_b32 s63, -1\n\t" "v_mov_b32 v210, 0\n\t" "v_cmp_gt_i32 vcc, 0, %[ps]\n\t" \
    "v_cndmask_b32 v204, 0, v209, s[62:63]\n\t" "s_nop 0\n\t" "v_cndmask_b32 v200, 0, v208, vcc\n\t" "v_cmp_gt_i32 vcc, 1, %[ps]\n\t" \
    "v_cndmask_b32 v205, 0, v209, s[62:63]\n\t" "s_nop 0\n\t" "v_cndmask_b32 v201, 0, v208, vcc\n\t" "v_cmp_gt_i32 vcc, 2, %[ps]\n\t" \
    "v_cndmask_b32 v206, 0, v209, s[62:63]\n\t" "s_nop 0\n\t" "v_cndmask_b32 v202, 0, v208, vcc\n\t" "v_cmp_gt_i32 vcc, 3, %[ps]\n\t" \
    "v_cndmask_b32 v207, 0, v209, s[62:63]\n\t" "s_nop 0\n\t" "v_cndmask_b32 v203, 0, v208, vcc\n\t" "s_waitcnt vmcnt(3)\n\t" XT(84, 85) \
    "v_smfmac_f32_16x16x64_bf16 v[224:227], v[240:243], a[0:7], %[ix]\n\t" "ds_write_b128 %[l1], v[240:243] offset:8192\n\t" \
    "v_smfmac_f32_16x16x64_bf16 v[228:231], v[240:243], a[8:15], %[ix]\n\t" "v_max3_u32 v217, v240, v241, v242\n\t" \
    "v_smfmac_f32_16x16x64_bf16 v[232:235], v[240:243], v[32:39], %[ix]\n\t" "s_waitcnt lgkmcnt(7)\n\t" \
    "v_smfmac_f32_16x16x64_bf16 v[236:239], v[240:243], v[160:167], %[ix]\n\t" "ds_read_b128 v[160:163], %[l0] offset:8192\n\t" \
    "ds_read_b128 v[164:167], %[l0] offset:9216\n\t" "s_waitcnt vmcnt(2)\n\t" "v_smfmac_f32_16x16x64_bf16 v[224:227], v[244:247], a[16:23], %[ix]\n\t" \
    "ds_write_b128 %[l1], v[244:247] offset:9216\n\t" "v_max3_u32 v217, v217, v243, v244\n\t" "v_smfmac_f32_16x16x64_bf16 v[228:231], v[244:247], a[24:31], %[ix]\n\t" \
    "v_max3_u32 v217, v217, v245, v246\n\t" "v_max_u32 v217, v217, v247\n\t" "v_smfmac_f32_16x16x64_bf16 v[232:235], v[244:247], v[40:47], %[ix]\n\t" \
    "v_cmp_eq_u32 vcc, -1, v217\n\t" "s_cmp_lg_u32 s60, 0\n\t" "s_waitcnt lgkmcnt(8)\n\t" "v_smfmac_f32_16x16x64_bf16 v[236:239], v[244:247], v[168:175], %[ix]\n\t" \
    "s_cbranch_vccnz 31f\n\t" "32:\n\t" "ds_read_b128 v[168:171], %[l0] offset:10240\n\t" "s_waitcnt vmcnt(1)\n\t" \
    "v_smfmac_f32_16x16x64_bf16 v[224:227], v[248:251], a[32:39], %[ix]\n\t" "ds_write_b128 %[l1], v[248:251] offset:10240\n\t" "s_waitcnt lgkmcnt(2)\n\t" \
    "v_smfmac_f32_16x16x64_bf16 v[228:231], v[248:251], a[40:47], %[ix]\n\t" "s_cbranch_scc1 33f\n\t" "s_barrier\n\t" "33:\n\t" XT(86, 87) \
    "v_smfmac_f32_16x16x64_bf16 v[232:235], v[248:251], v[48:55], %[ix]\n\t" "ds_read_b128 v[192:195], %[l1] offset:12288\n\t" "ds_read_b128 v[196:199], %[l1] offset:0\n\t" \
    "v_smfmac_f32_16x16x64_bf16 v[236:239], v[248:251], v[176:183], %[ix]\n\t" "ds_read_b128 v[172:175], %[l0] offset:11264\n\t" \
    "ds_read_b128 v[176:179], %[l0] offset:12288\n\t" "s_waitcnt vmcnt(0)\n\t" "v_smfmac_f32_16x16x64_bf16 v[224:227], v[252:255], a[48:55], %[ix]\n\t" \
    "ds_write_b128 %[l1], v[252:255] offset:11264\n\t" "v_max3_u32 v217, v248, v249, v250\n\t" "v_smfmac_f32_16x16x64_bf16 v[228:231], v[252:255], a[56:63], %[ix]\n\t" \
    "ds_read_b128 v[180:183], %[l0] offset:13312\n\t" "v_max3_u32 v217, v217, v251, v252\n\t" "v_max3_u32 v217, v217, v253, v254\n\t" \
    "v_smfmac_f32_16x16x64_bf16 v[232:235], v[252:255], v[56:63], %[ix]\n\t" "v_max_u32 v217, v217, v255\n\t" "v_cmp_eq_u32 vcc, -1, v217\n\t" \
    "v_smfmac_f32_16x16x64_bf16 v[236:239], v[252:255], v[184:191], %[ix]\n\t" "s_cbranch_vccnz 34f\n\t" "35:\n\t" "ds_read_b128 v[240:243], %[l1] offset:4096\n\t" \
    "s_waitcnt lgkmcnt(6)\n\t" "v_smfmac_f32_16x16x64_bf16 v[224:227], v[192:195], a[64:71], %[ix]\n\t" "ds_read_b128 v[184:187], %[l0] offset:14336\n\t" \
    "ds_read_b128 v[188:191], %[l0] offset:15360\n\t" "v_smfmac_f32_16x16x64_bf16 v[228:231], v[192:195], a[72:79], %[ix]\n\t" \
    "ds_read_b128 v[244:247], %[l1] offset:13312\n\t" "s_waitcnt lgkmcnt(5)\n\t" "v_smfmac_f32_16x16x64_bf16 v[232:235], v[192:195], v[64:71], %[ix]\n\t" "s_barrier\n\t" \
    XT(88, 89) "v_smfmac_f32_16x16x64_bf16 v[236:239], v[192:195], v[160:167], %[ix]\n\t" "ds_read_b128 v[160:163], %[l0] offset:16384\n\t" \
    "ds_read_b128 v[164:167], %[l0] offset:17408\n\t" "v_smfmac_f32_16x16x64_bf16 v[224:227], v[196:199], a[80:87], %[ix]\n\t" \
    "ds_read_b128 v[248:251], %[l1] offset:1024\n\t" "ds_read_b128 v[252:255], %[l1] offset:5120\n\t" "v_smfmac_f32_16x16x64_bf16 v[228:231], v[196:199], a[88:95], %[ix]\n\t" \
    "ds_read_b128 v[192:195], %[l1] offset:14336\n\t" "v_smfmac_f32_16x16x64_bf16 v[232:235], v[196:199], v[72:79], %[ix]\n\t" \
    "buffer_load_dwordx4 v[16:19], %[hq], %[rs], %[qb] offen offset:0 sc1\n\t" "v_smfmac_f32_16x16x64_bf16 v[236:239], v[196:199], v[168:175], %[ix]\n\t" \
    "ds_read_b128 v[168:171], %[l0] offset:18432\n\t" "ds_read_b128 v[172:175], %[l0] offset:19456\n\t" "s_waitcnt lgkmcnt(10)\n\t" \
    "v_smfmac_f32_16x16x64_bf16 v[224:227], v[240:243], a[96:103], %[ix]\n\t" "ds_read_b128 v[196:199], %[l1] offset:2048\n\t" \
    "v_smfmac_f32_16x16x64_bf16 v[228:231], v[240:243], a[104:111], %[ix]\n\t" "v_smfmac_f32_16x16x64_bf16 v[232:235], v[240:243], v[80:87], %[ix]\n\t" \
    "buffer_load_dwordx4 v[20:23], %[hq], %[rs], %[qb] offen offset:2048 sc1\n\t" "v_smfmac_f32_16x16x64_bf16 v[236:239], v[240:243], v[176:183], %[ix]\n\t" \
    "ds_read_b128 v[176:179], %[l0] offset:20480\n\t" "ds_read_b128 v[180:183], %[l0] offset:21504\n\t" "s_waitcnt lgkmcnt(10)\n\t" \
    "v_smfmac_f32_16x16x64_bf16 v[224:227], v[244:247], a[112:119], %[ix]\n\t" "ds_read_b128 v[240:243], %[l1] offset:6144\n\t" \
    "v_smfmac_f32_16x16x64_bf16 v[228:231], v[244:247], a[120:127], %[ix]\n\t" "v_smfmac_f32_16x16x64_bf16 v[232:235], v[244:247], v[88:95], %[ix]\n\t" \
    "buffer_load_dwordx4 v[24:27], %[hq], %[rs], s61 offen offset:0 sc1\n\t" "v_smfmac_f32_16x16x64_bf16 v[236:239], v[244:247], v[184:191], %[ix]\n\t" \
    "ds_read_b128 v[184:187], %[l0] offset:22528\n\t" "ds_read_b128 v[188:191], %[l0] offset:23552\n\t" "s_waitcnt lgkmcnt(10)\n\t" \
    "v_smfmac_f32_16x16x64_bf16 v[224:227], v[248:251], a[128:135], %[ix]\n\t" "ds_read_b128 v[244:247], %[l1] offset:15360\n\t" \
    "v_smfmac_f32_16x16x64_bf16 v[228:231], v[248:251], a[136:143], %[ix]\n\t" "v_smfmac_f32_16x16x64_bf16 v[232:235], v[248:251], v[96:103], %[ix]\n\t" \
    "buffer_load_dwordx4 v[28:31], %[hq], %[rs], s61 offen offset:2048 sc1\n\t" "v_smfmac_f32_16x16x64_bf16 v[236:239], v[248:251], v[160:167], %[ix]\n\t" \
    "ds_read_b128 v[160:163], %[l0] offset:24576\n\t" "ds_read_b128 v[164:167], %[l0] offset:25600\n\t" "s_waitcnt lgkmcnt(12)\n\t" \
    "v_smfmac_f32_16x16x64_bf16 v[224:227], v[252:255], a[144:151], %[ix]\n\t" "ds_read_b128 v[248:251], %[l1] offset:3072\n\t" \
    "v_smfmac_f32_16x16x64_bf16 v[228:231], v[252:255], a[152:159], %[ix]\n\t" "v_smfmac_f32_16x16x64_bf16 v[232:235], v[252:255], v[104:111], %[ix]\n\t" \
    "s_waitcnt lgkmcnt(10)\n\t" "v_smfmac_f32_16x16x64_bf16 v[236:239], v[252:255], v[168:175], %[ix]\n\t" "ds_read_b128 v[168:171], %[l0] offset:26624\n\t" \
    "ds_read_b128 v[172:175], %[l0] offset:27648\n\t" "v_smfmac_f32_16x16x64_bf16 v[224:227], v[192:195], a[160:167], %[ix]\n\t" \
    "ds_read_b128 v[252:255], %[l1] offset:7168\n\t" "v_smfmac_f32_16x16x64_bf16 v[228:231], v[192:195], a[168:175], %[ix]\n\t" "s_waitcnt lgkmcnt(12)\n\t" \
    "v_smfmac_f32_16x16x64_bf16 v[224:227], v[196:199], a[176:183], %[ix]\n\t" "v_smfmac_f32_16x16x64_bf16 v[228:231], v[196:199], a[184:191], %[ix]\n\t" \
    "s_waitcnt lgkmcnt(9)\n\t" "v_smfmac_f32_16x16x64_bf16 v[224:227], v[240:243], a[192:199], %[ix]\n\t" \
    "v_smfmac_f32_16x16x64_bf16 v[228:231], v[240:243], a[200:207], %[ix]\n\t" "s_waitcnt lgkmcnt(6)\n\t" \
    "v_smfmac_f32_16x16x64_bf16 v[224:227], v[244:247], a[208:215], %[ix]\n\t" "v_smfmac_f32_16x16x64_bf16 v[228:231], v[244:247], a[216:223], %[ix]\n\t" \
    "s_waitcnt lgkmcnt(3)\n\t" "v_smfmac_f32_16x16x64_bf16 v[224:227], v[248:251], a[224:231], %[ix]\n\t" \
    "v_smfmac_f32_16x16x64_bf16 v[228:231], v[248:251], a[232:239], %[ix]\n\t" "s_waitcnt lgkmcnt(0)\n\t" \
    "v_smfmac_f32_16x16x64_bf16 v[224:227], v[252:255], a[240:247], %[ix]\n\t" "s_nop 1\n\t" "v_smfmac_f32_16x16x64_bf16 v[228:231], v[252:255], a[248:255], %[ix]\n\t" \
    XT(90, 91) "s_nop 1\n\t" "v_smfmac_f32_16x16x64_bf16 v[232:235], v[192:195], v[112:119], %[ix]\n\t" "s_nop 1\n\t" \
    "v_smfmac_f32_16x16x64_bf16 v[236:239], v[192:195], v[176:183], %[ix]\n\t" "ds_read_b128 v[176:179], %[l0] offset:28672\n\t" "v_add_f32 v224, v224, v225\n\t" \
    "v_add_f32 v226, v226, v227\n\t" "v_smfmac_f32_16x16x64_bf16 v[232:235], v[196:199], v[120:127], %[ix]\n\t" "ds_read_b128 v[180:183], %[l0] offset:29696\n\t" \
    "v_add_f32 v228, v228, v229\n\t" "v_add_f32 v230, v230, v231\n\t" "v_smfmac_f32_16x16x64_bf16 v[236:239], v[196:199], v[184:191], %[ix]\n\t" \
    "ds_read_b128 v[184:187], %[l0] offset:30720\n\t" "v_cvt_pk_bf16_f32 v218, v224, v228\n\t" "v_cvt_pk_bf16_f32 v219, v226, v230\n\t" \
    "v_smfmac_f32_16x16x64_bf16 v[232:235], v[240:243], v[128:135], %[ix]\n\t" "ds_write2_b32 %[qw], v218, v219 offset1:8\n\t" \
    "ds_read_b128 v[188:191], %[l0] offset:31744\n\t" "v_smfmac_f32_16x16x64_bf16 v[236:239], v[240:243], v[160:167], %[ix]\n\t" \
    "v_smfmac_f32_16x16x64_bf16 v[232:235], v[244:247], v[136:143], %[ix]\n\t" "v_smfmac_f32_16x16x64_bf16 v[236:239], v[244:247], v[168:175], %[ix]\n\t" \
    "s_waitcnt lgkmcnt(1)\n\t" "s_barrier\n\t" "v_smfmac_f32_16x16x64_bf16 v[232:235], v[248:251], v[144:151], %[ix]\n\t" \
    "v_smfmac_f32_16x16x64_bf16 v[236:239], v[248:251], v[176:183], %[ix]\n\t" "v_smfmac_f32_16x16x64_bf16 v[232:235], v[252:255], v[152:159], %[ix]\n\t" \
    "s_waitcnt lgkmcnt(0)\n\t" "v_smfmac_f32_16x16x64_bf16 v[236:239], v[252:255], v[184:191], %[ix]\n\t" XT(92, 93) "s_nop 6\n\t" "v_add_f32 v232, v232, v233\n\t" \
    "v_add_f32 v234, v234, v235\n\t" "v_add_f32 v236, v236, v237\n\t" "v_add_f32 v238, v238, v239\n\t" "v_cvt_pk_bf16_f32 v232, v232, v236\n\t" \
    "v_cvt_pk_bf16_f32 v233, v234, v238\n\t" "ds_write2_b32 %[kw], v232, v233 offset1:68\n\t" "s_waitcnt lgkmcnt(0)\n\t" "s_barrier\n\t" XT(94, 95) \
    "s_cmp_ge_u32 %[sq], %[sn]\n\t" "s_cbranch_scc1 377f\n\t" "375:\n\t" "global_load_dword v240, v210, %[fq] sc1\n\t" "s_waitcnt vmcnt(0)\n\t" \
    "v_readfirstlane_b32 %[sq], v240\n\t" "s_nop 3\n\t" "s_cmp_ge_u32 %[sq], %[sn]\n\t" "s_cbranch_scc1 376f\n\t" "s_cmp_eq_u32 %[bd], 0\n\t" "s_cbranch_scc1 376f\n\t" \
    "s_sub_u32 %[bd], %[bd], 1\n\t" "s_sleep 2\n\t" "s_branch 375b\n\t" "376:\n\t" "buffer_load_dwordx4 v[16:19], %[hq], %[rs], %[qb] offen offset:0 sc1\n\t" \
    "buffer_load_dwordx4 v[20:23], %[hq], %[rs], %[qb] offen offset:2048 sc1\n\t" "buffer_load_dwordx4 v[24:27], %[hq], %[rs], s61 offen offset:0 sc1\n\t" \
    "buffer_load_dwordx4 v[28:31], %[hq], %[rs], s61 offen offset:2048 sc1\n\t" "377:\n\t" "s_nop 3\n\t" "ds_read_b128 v[160:163], %[kx] offset:0\n\t" \
    "ds_read_b128 v[164:167], %[kx] offset:2048\n\t" "ds_read_b128 v[168:171], %[kx] offset:4096\n\t" "ds_read_b128 v[172:175], %[kx] offset:6144\n\t" \
    "ds_read_b128 v[176:179], %[kx] offset:256\n\t" "ds_read_b128 v[180:183], %[kx] offset:2304\n\t" "ds_read_b128 v[184:187], %[kx] offset:4352\n\t" \
    "ds_read_b128 v[188:191], %[kx] offset:6400\n\t" "ds_read_b128 v[224:227], %[km] offset:0\n\t" "ds_read_b128 v[228:231], %[km] offset:64\n\t" \
    "ds_read_b128 v[232:235], %[km] offset:128\n\t" "ds_read_b128 v[236:239], %[km] offset:192\n\t" "s_waitcnt vmcnt(0)\n\t" "global_load_dword v240, v210, %[fq] sc1\n\t" \
    "s_waitcnt lgkmcnt(11)\n\t" "v_mfma_f32_16x16x32_bf16 v[248:251], v[160:163], v[16:19], 0\n\t" "s_waitcnt lgkmcnt(10)\n\t" \
    "v_mfma_f32_16x16x32_bf16 v[248:251], v[164:167], v[20:23], v[248:251]\n\t" "s_waitcnt lgkmcnt(9)\n\t" \
    "v_mfma_f32_16x16x32_bf16 v[248:251], v[168:171], v[24:27], v[248:251]\n\t" "s_waitcnt lgkmcnt(8)\n\t" \
    "v_mfma_f32_16x16x32_bf16 v[248:251], v[172:175], v[28:31], v[248:251]\n\t" "s_waitcnt lgkmcnt(7)\n\t" \
    "v_mfma_f32_16x16x32_bf16 v[252:255], v[176:179], v[16:19], v[200:203]\n\t" "s_waitcnt lgkmcnt(6)\n\t" \
    "v_mfma_f32_16x16x32_bf16 v[252:255], v[180:183], v[20:23], v[252:255]\n\t" "s_waitcnt lgkmcnt(5)\n\t" \
    "v_mfma_f32_16x16x32_bf16 v[252:255], v[184:187], v[24:27], v[252:255]\n\t" "s_waitcnt lgkmcnt(4)\n\t" \
    "v_mfma_f32_16x16x32_bf16 v[252:255], v[188:191], v[28:31], v[252:255]\n\t" "s_waitcnt lgkmcnt(3)\n\t" \
    "v_mfma_f32_16x16x32_bf16 v[244:247], v[224:227], v[16:19], v[204:207]\n\t" "s_waitcnt lgkmcnt(2)\n\t" \
    "v_mfma_f32_16x16x32_bf16 v[244:247], v[228:231], v[20:23], v[244:247]\n\t" "s_waitcnt lgkmcnt(1)\n\t" \
    "v_mfma_f32_16x16x32_bf16 v[244:247], v[232:235], v[24:27], v[244:247]\n\t" "s_waitcnt lgkmcnt(0)\n\t" \
    "v_mfma_f32_16x16x32_bf16 v[244:247], v[236:239], v[28:31], v[244:247]\n\t" "v_mov_b32 v192, -1\n\t" "v_mov_b32 v193, -1\n\t" "v_mov_b32 v194, -1\n\t" \
    "v_mov_b32 v195, -1\n\t" "v_max3_f32 v242, v248, v249, v250\n\t" "v_max3_f32 v242, v242, v251, v252\n\t" "v_max3_f32 v242, v242, v253, v254\n\t" \
    "v_max_f32 v242, v242, v255\n\t" "v_max3_f32 v242, v242, v244, v245\n\t" "v_max3_f32 v242, v242, v246, v247\n\t" "v_mov_b32 v243, v242\n\t" "s_nop 1\n\t" \
    "v_permlane32_swap_b32 v242, v243\n\t" "v_max_f32 v242, v242, v243\n\t" "v_mov_b32 v243, v242\n\t" "s_nop 1\n\t" "v_permlane16_swap_b32 v242, v243\n\t" \
    "v_max_f32 v242, v242, v243\n\t" "v_pk_add_f32 v[244:245], v[244:245], v[242:243] op_sel_hi:[1,0] neg_lo:[0,1] neg_hi:[0,1]\n\t" \
    "v_pk_add_f32 v[246:247], v[246:247], v[242:243] op_sel_hi:[1,0] neg_lo:[0,1] neg_hi:[0,1]\n\t" \
    "v_pk_add_f32 v[248:249], v[248:249], v[242:243] op_sel_hi:[1,0] neg_lo:[0,1] neg_hi:[0,1]\n\t" \
    "v_pk_add_f32 v[250:251], v[250:251], v[242:243] op_sel_hi:[1,0] neg_lo:[0,1] neg_hi:[0,1]\n\t" \
    "v_pk_add_f32 v[252:253], v[252:253], v[242:243] op_sel_hi:[1,0] neg_lo:[0,1] neg_hi:[0,1]\n\t" \
    "v_pk_add_f32 v[254:255], v[254:255], v[242:243] op_sel_hi:[1,0] neg_lo:[0,1] neg_hi:[0,1]\n\t" "v_pk_mul_f32 v[244:245], v[244:245], s[66:67]\n\t" \
    "v_pk_mul_f32 v[246:247], v[246:247], s[66:67]\n\t" "v_pk_mul_f32 v[248:249], v[248:249], s[66:67]\n\t" "v_pk_mul_f32 v[250:251], v[250:251], s[66:67]\n\t" \
    "v_pk_mul_f32 v[252:253], v[252:253], s[66:67]\n\t" "v_pk_mul_f32 v[254:255], v[254:255], s[66:67]\n\t" "v_exp_f32 v244, v244\n\t" "v_exp_f32 v245, v245\n\t" \
    "v_exp_f32 v246, v246\n\t" "v_exp_f32 v247, v247\n\t" "v_exp_f32 v248, v248\n\t" "v_exp_f32 v249, v249\n\t" "v_exp_f32 v250, v250\n\t" "v_exp_f32 v251, v251\n\t" \
    "v_exp_f32 v252, v252\n\t" "v_exp_f32 v253, v253\n\t" "v_exp_f32 v254, v254\n\t" "v_exp_f32 v255, v255\n\t" "v_pk_add_f32 v[242:243], v[244:245], v[246:247]\n\t" \
    "v_pk_add_f32 v[242:243], v[242:243], v[248:249]\n\t" "v_pk_add_f32 v[242:243], v[242:243], v[250:251]\n\t" "v_pk_add_f32 v[242:243], v[242:243], v[252:253]\n\t" \
    "v_pk_add_f32 v[242:243], v[242:243], v[254:255]\n\t" "v_add_f32 v242, v242, v243\n\t" "v_mov_b32 v243, v242\n\t" "s_nop 1\n\t" "v_permlane32_swap_b32 v242, v243\n\t" \
    "v_add_f32 v242, v242, v243\n\t" "v_mov_b32 v243, v242\n\t" "s_nop 1\n\t" "v_permlane16_swap_b32 v242, v243\n\t" "v_add_f32 v242, v242, v243\n\t" \
    "v_rcp_f32 v242, v242\n\t" "s_nop 0\n\t" "v_pk_mul_f32 v[244:245], v[244:245], v[242:243] op_sel_hi:[1,0]\n\t" \
    "v_pk_mul_f32 v[246:247], v[246:247], v[242:243] op_sel_hi:[1,0]\n\t" "v_pk_mul_f32 v[248:249], v[248:249], v[242:243] op_sel_hi:[1,0]\n\t" \
    "v_pk_mul_f32 v[250:251], v[250:251], v[242:243] op_sel_hi:[1,0]\n\t" "v_pk_mul_f32 v[252:253], v[252:253], v[242:243] op_sel_hi:[1,0]\n\t" \
    "v_pk_mul_f32 v[254:255], v[254:255], v[242:243] op_sel_hi:[1,0]\n\t" "v_cvt_pk_bf16_f32 v162, v244, v245\n\t" "v_cvt_pk_bf16_f32 v163, v246, v247\n\t" \
    "v_cvt_pk_bf16_f32 v164, v248, v249\n\t" "v_cvt_pk_bf16_f32 v165, v250, v251\n\t" "v_cvt_pk_bf16_f32 v166, v252, v253\n\t" "v_cvt_pk_bf16_f32 v167, v254, v255\n\t" \
    "buffer_store_dwordx4 v[164:167], %[px], %[rs], %[tb] offen sc1\n\t" "buffer_store_dwordx2 v[162:163], %[pm], %[rs], %[tb] offen sc1\n\t" \
    "buffer_store_dwordx4 v[192:195], %[px], %[rs], %[tr] offen sc1\n\t" "buffer_store_dwordx2 v[192:193], %[pm], %[rs], %[tr] offen sc1\n\t" "s_waitcnt vmcnt(4)\n\t" \
    "v_readfirstlane_b32 s62, v240\n\t" "s_nop 3\n\t" "s_max_u32 %[sq], %[sq], s62\n\t" XT(96, 97) "s_branch 99f\n\t" "31:\n\t" "s_cmp_eq_u32 %[bd], 0\n\t" \
    "s_cbranch_scc1 36f\n\t" "s_sub_u32 %[bd], %[bd], 1\n\t" XCNT(100) "s_waitcnt lgkmcnt(0)\n\t" "s_nop 7\n\t" "s_branch 30b\n\t" "36:\n\t" "s_cmp_lg_u32 s60, 0\n\t" \
    "s_branch 32b\n\t" "34:\n\t" "s_cmp_eq_u32 %[bd], 0\n\t" "s_cbranch_scc1 35b\n\t" "s_sub_u32 %[bd], %[bd], 1\n\t" XCNT(101) "s_mov_b32 s60, 1\n\t" \
    "s_waitcnt lgkmcnt(0)\n\t" "s_nop 7\n\t" "s_branch 30b\n\t" "99:\n\t"
#define QK_STEP3 \
    "v_xor_b32 v216, 0x400, %[mo]\n\t" "s_mov_b32 s60, 0\n\t" XT(80, 81) "s_getpc_b64 s[62:63]\n\t" "48:\n\t" "s_memtime s[64:65]\n\t" "s_waitcnt lgkmcnt(0)\n\t" \
    "s_sub_u32 s61, %[tg], s64\n\t" "s_cmp_gt_i32 s61, 124\n\t" "s_cbranch_scc1 48b\n\t" "s_max_i32 s61, s61, 0\n\t" "s_and_b32 s61, s61, 124\n\t" \
    "s_sub_u32 s61, 49f-48b, s61\n\t" "s_add_u32 s62, s62, s61\n\t" "s_addc_u32 s63, s63, 0\n\t" "s_setpc_b64 s[62:63]\n\t" ".rept 31\n\t" "s_nop 0\n\t" ".endr\n\t" "49:\n\t" \
    XT(98, 99) "s_barrier\n\t" XT(82, 83) "40:\n\t" "buffer_load_dwordx4 v[240:243], %[mo], %[rs], %[sb] offen offset:0 sc1\n\t" "v_mov_b64 v[224:225], 0\n\t" \
    "v_mov_b64 v[226:227], 0\n\t" "v_mov_b64 v[228:229], 0\n\t" "v_mov_b64 v[230:231], 0\n\t" "v_mov_b64 v[232:233], 0\n\t" "v_mov_b64 v[234:235], 0\n\t" \
    "v_mov_b64 v[236:237], 0\n\t" "v_mov_b64 v[238:239], 0\n\t" "buffer_load_dwordx4 v[244:247], v216, %[rs], %[sb] offen offset:0 sc1\n\t" \
    "ds_read_b128 v[160:163], %[l0] offset:0\n\t" "ds_read_b128 v[164:167], %[l0] offset:1024\n\t" "ds_read_b128 v[168:171], %[l0] offset:2048\n\t" \
    "ds_read_b128 v[172:175], %[l0] offset:3072\n\t" "s_nop 3\n\t" "buffer_load_dwordx4 v[248:251], %[mo], %[rs], %[sb] offen offset:2048 sc1\n\t" \
    "ds_read_b128 v[176:179], %[l0] offset:4096\n\t" "ds_read_b128 v[180:183], %[l0] offset:5120\n\t" "ds_read_b128 v[184:187], %[l0] offset:6144\n\t" \
    "ds_read_b128 v[188:191], %[l0] offset:7168\n\t" "s_nop 3\n\t" "buffer_load_dwordx4 v[252:255], v216, %[rs], %[sb] offen offset:2048 sc1\n\t" "s_waitcnt vmcnt(3)\n\t" \
    XT(84, 85) "v_smfmac_f32_16x16x64_bf16 v[224:227], v[240:243], a[0:7], %[ix]\n\t" "ds_write_b128 %[l1], v[240:243] offset:12288\n\t" \
    "v_smfmac_f32_16x16x64_bf16 v[228:231], v[240:243], a[8:15], %[ix]\n\t" "v_max3_u32 v217, v240, v241, v242\n\t" \
    "v_smfmac_f32_16x16x64_bf16 v[232:235], v[240:243], v[32:39], %[ix]\n\t" "s_waitcnt lgkmcnt(7)\n\t" \
    "v_smfmac_f32_16x16x64_bf16 v[236:239], v[240:243], v[160:167], %[ix]\n\t" "ds_read_b128 v[160:163], %[l0] offset:8192\n\t" \
    "ds_read_b128 v[164:167], %[l0] offset:9216\n\t" "s_waitcnt vmcnt(2)\n\t" "v_smfmac_f32_16x16x64_bf16 v[224:227], v[244:247], a[16:23], %[ix]\n\t" \
    "ds_write_b128 %[l1], v[244:247] offset:13312\n\t" "v_max3_u32 v217, v217, v243, v244\n\t" "v_smfmac_f32_16x16x64_bf16 v[228:231], v[244:247], a[24:31], %[ix]\n\t" \
    "v_max3_u32 v217, v217, v245, v246\n\t" "v_max_u32 v217, v217, v247\n\t" "v_smfmac_f32_16x16x64_bf16 v[232:235], v[244:247], v[40:47], %[ix]\n\t" \
    "v_cmp_eq_u32 vcc, -1, v217\n\t" "s_cmp_lg_u32 s60, 0\n\t" "s_waitcnt lgkmcnt(8)\n\t" "v_smfmac_f32_16x16x64_bf16 v[236:239], v[244:247], v[168:175], %[ix]\n\t" \
    "s_cbranch_vccnz 41f\n\t" "42:\n\t" "ds_read_b128 v[168:171], %[l0] offset:10240\n\t" "s_waitcnt vmcnt(1)\n\t" \
    "v_smfmac_f32_16x16x64_bf16 v[224:227], v[248:251], a[32:39], %[ix]\n\t" "ds_write_b128 %[l1], v[248:251] offset:14336\n\t" "s_waitcnt lgkmcnt(2)\n\t" \
    "v_smfmac_f32_16x16x64_bf16 v[228:231], v[248:251], a[40:47], %[ix]\n\t" "s_cbranch_scc1 43f\n\t" "s_barrier\n\t" "43:\n\t" XT(86, 87) \
    "v_smfmac_f32_16x16x64_bf16 v[232:235], v[248:251], v[48:55], %[ix]\n\t" "ds_read_b128 v[192:195], %[l1] offset:0\n\t" "ds_read_b128 v[196:199], %[l1] offset:4096\n\t" \
    "v_smfmac_f32_16x16x64_bf16 v[236:239], v[248:251], v[176:183], %[ix]\n\t" "ds_read_b128 v[172:175], %[l0] offset:11264\n\t" \
    "ds_read_b128 v[176:179], %[l0] offset:12288\n\t" "s_waitcnt vmcnt(0)\n\t" "v_smfmac_f32_16x16x64_bf16 v[224:227], v[252:255], a[48:55], %[ix]\n\t" \
    "ds_write_b128 %[l1], v[252:255] offset:15360\n\t" "v_max3_u32 v217, v248, v249, v250\n\t" "v_smfmac_f32_16x16x64_bf16 v[228:231], v[252:255], a[56:63], %[ix]\n\t" \
    "ds_read_b128 v[180:183], %[l0] offset:13312\n\t" "v_max3_u32 v217, v217, v251, v252\n\t" "v_max3_u32 v217, v217, v253, v254\n\t" \
    "v_smfmac_f32_16x16x64_bf16 v[232:235], v[252:255], v[56:63], %[ix]\n\t" "v_max_u32 v217, v217, v255\n\t" "v_cmp_eq_u32 vcc, -1, v217\n\t" \
    "v_smfmac_f32_16x16x64_bf16 v[236:239], v[252:255], v[184:191], %[ix]\n\t" "s_cbranch_vccnz 44f\n\t" "45:\n\t" "ds_read_b128 v[240:243], %[l1] offset:8192\n\t" \
    "s_waitcnt lgkmcnt(6)\n\t" "v_smfmac_f32_16x16x64_bf16 v[224:227], v[192:195], a[64:71], %[ix]\n\t" "ds_read_b128 v[184:187], %[l0] offset:14336\n\t" \
    "ds_read_b128 v[188:191], %[l0] offset:15360\n\t" "v_smfmac_f32_16x16x64_bf16 v[228:231], v[192:195], a[72:79], %[ix]\n\t" \
    "ds_read_b128 v[244:247], %[l1] offset:1024\n\t" "s_waitcnt lgkmcnt(5)\n\t" "v_smfmac_f32_16x16x64_bf16 v[232:235], v[192:195], v[64:71], %[ix]\n\t" "s_barrier\n\t" \
    XT(88, 89) "s_memtime s[64:65]\n\t" "v_smfmac_f32_16x16x64_bf16 v[236:239], v[192:195], v[160:167], %[ix]\n\t" "ds_read_b128 v[160:163], %[l0] offset:16384\n\t" \
    "ds_read_b128 v[164:167], %[l0] offset:17408\n\t" "v_smfmac_f32_16x16x64_bf16 v[224:227], v[196:199], a[80:87], %[ix]\n\t" \
    "ds_read_b128 v[248:251], %[l1] offset:5120\n\t" "ds_read_b128 v[252:255], %[l1] offset:9216\n\t" "v_smfmac_f32_16x16x64_bf16 v[228:231], v[196:199], a[88:95], %[ix]\n\t" \
    "ds_read_b128 v[192:195], %[l1] offset:2048\n\t" "v_smfmac_f32_16x16x64_bf16 v[232:235], v[196:199], v[72:79], %[ix]\n\t" "s_sub_u32 s62, s64, %[ca]\n\t" \
    "s_mul_i32 s62, s62, %[k1]\n\t" "v_smfmac_f32_16x16x64_bf16 v[236:239], v[196:199], v[168:175], %[ix]\n\t" "s_lshr_b32 s62, s62, 16\n\t" "s_add_u32 s62, s62, %[ga]\n\t" \
    "s_waitcnt lgkmcnt(8)\n\t" "v_smfmac_f32_16x16x64_bf16 v[224:227], v[240:243], a[96:103], %[ix]\n\t" "s_bitset0_b32 s62, 0\n\t" "v_mov_b32 v218, s62\n\t" \
    "v_smfmac_f32_16x16x64_bf16 v[228:231], v[240:243], a[104:111], %[ix]\n\t" "buffer_store_dword v218, %[hq], %[rs], %[pb] offen sc1\n\t" \
    "ds_read_b128 v[168:171], %[l0] offset:18432\n\t" "v_smfmac_f32_16x16x64_bf16 v[232:235], v[240:243], v[80:87], %[ix]\n\t" \
    "ds_read_b128 v[172:175], %[l0] offset:19456\n\t" "ds_read_b128 v[196:199], %[l1] offset:6144\n\t" \
    "v_smfmac_f32_16x16x64_bf16 v[236:239], v[240:243], v[176:183], %[ix]\n\t" "ds_read_b128 v[176:179], %[l0] offset:20480\n\t" \
    "ds_read_b128 v[180:183], %[l0] offset:21504\n\t" "s_waitcnt lgkmcnt(10)\n\t" "v_smfmac_f32_16x16x64_bf16 v[224:227], v[244:247], a[112:119], %[ix]\n\t" \
    "ds_read_b128 v[240:243], %[l1] offset:10240\n\t" "v_smfmac_f32_16x16x64_bf16 v[228:231], v[244:247], a[120:127], %[ix]\n\t" \
    "v_smfmac_f32_16x16x64_bf16 v[232:235], v[244:247], v[88:95], %[ix]\n\t" "v_smfmac_f32_16x16x64_bf16 v[236:239], v[244:247], v[184:191], %[ix]\n\t" \
    "ds_read_b128 v[184:187], %[l0] offset:22528\n\t" "ds_read_b128 v[188:191], %[l0] offset:23552\n\t" "s_waitcnt lgkmcnt(10)\n\t" \
    "v_smfmac_f32_16x16x64_bf16 v[224:227], v[248:251], a[128:135], %[ix]\n\t" "ds_read_b128 v[244:247], %[l1] offset:3072\n\t" \
    "v_smfmac_f32_16x16x64_bf16 v[228:231], v[248:251], a[136:143], %[ix]\n\t" "v_smfmac_f32_16x16x64_bf16 v[232:235], v[248:251], v[96:103], %[ix]\n\t" \
    "v_smfmac_f32_16x16x64_bf16 v[236:239], v[248:251], v[160:167], %[ix]\n\t" "ds_read_b128 v[160:163], %[l0] offset:24576\n\t" \
    "ds_read_b128 v[164:167], %[l0] offset:25600\n\t" "s_waitcnt lgkmcnt(12)\n\t" "v_smfmac_f32_16x16x64_bf16 v[224:227], v[252:255], a[144:151], %[ix]\n\t" \
    "ds_read_b128 v[248:251], %[l1] offset:7168\n\t" "v_smfmac_f32_16x16x64_bf16 v[228:231], v[252:255], a[152:159], %[ix]\n\t" \
    "v_smfmac_f32_16x16x64_bf16 v[232:235], v[252:255], v[104:111], %[ix]\n\t" "s_waitcnt lgkmcnt(10)\n\t" \
    "v_smfmac_f32_16x16x64_bf16 v[236:239], v[252:255], v[168:175], %[ix]\n\t" "ds_read_b128 v[168:171], %[l0] offset:26624\n\t" \
    "ds_read_b128 v[172:175], %[l0] offset:27648\n\t" "v_smfmac_f32_16x16x64_bf16 v[224:227], v[192:195], a[160:167], %[ix]\n\t" \
    "ds_read_b128 v[252:255], %[l1] offset:11264\n\t" "v_smfmac_f32_16x16x64_bf16 v[228:231], v[192:195], a[168:175], %[ix]\n\t" "s_waitcnt lgkmcnt(12)\n\t" \
    "v_smfmac_f32_16x16x64_bf16 v[224:227], v[196:199], a[176:183], %[ix]\n\t" "v_smfmac_f32_16x16x64_bf16 v[228:231], v[196:199], a[184:191], %[ix]\n\t" \
    "s_waitcnt lgkmcnt(9)\n\t" "v_smfmac_f32_16x16x64_bf16 v[224:227], v[240:243], a[192:199], %[ix]\n\t" \
    "v_smfmac_f32_16x16x64_bf16 v[228:231], v[240:243], a[200:207], %[ix]\n\t" "s_waitcnt lgkmcnt(6)\n\t" \
    "v_smfmac_f32_16x16x64_bf16 v[224:227], v[244:247], a[208:215], %[ix]\n\t" "v_smfmac_f32_16x16x64_bf16 v[228:231], v[244:247], a[216:223], %[ix]\n\t" \
    "s_waitcnt lgkmcnt(3)\n\t" "v_smfmac_f32_16x16x64_bf16 v[224:227], v[248:251], a[224:231], %[ix]\n\t" \
    "v_smfmac_f32_16x16x64_bf16 v[228:231], v[248:251], a[232:239], %[ix]\n\t" "s_waitcnt lgkmcnt(0)\n\t" \
    "v_smfmac_f32_16x16x64_bf16 v[224:227], v[252:255], a[240:247], %[ix]\n\t" "s_nop 1\n\t" "v_smfmac_f32_16x16x64_bf16 v[228:231], v[252:255], a[248:255], %[ix]\n\t" \
    XT(90, 91) "s_nop 1\n\t" "v_smfmac_f32_16x16x64_bf16 v[232:235], v[192:195], v[112:119], %[ix]\n\t" "s_nop 1\n\t" \
    "v_smfmac_f32_16x16x64_bf16 v[236:239], v[192:195], v[176:183], %[ix]\n\t" "ds_read_b128 v[176:179], %[l0] offset:28672\n\t" "v_add_f32 v224, v224, v225\n\t" \
    "v_add_f32 v226, v226, v227\n\t" "v_smfmac_f32_16x16x64_bf16 v[232:235], v[196:199], v[120:127], %[ix]\n\t" "ds_read_b128 v[180:183], %[l0] offset:29696\n\t" \
    "v_add_f32 v228, v228, v229\n\t" "v_add_f32 v230, v230, v231\n\t" "v_smfmac_f32_16x16x64_bf16 v[236:239], v[196:199], v[184:191], %[ix]\n\t" \
    "ds_read_b128 v[184:187], %[l0] offset:30720\n\t" "v_cvt_pk_bf16_f32 v218, v224, v228\n\t" "v_cvt_pk_bf16_f32 v219, v226, v230\n\t" \
    "v_smfmac_f32_16x16x64_bf16 v[232:235], v[240:243], v[128:135], %[ix]\n\t" "ds_write2_b32 %[qw], v218, v219 offset1:8\n\t" \
    "ds_read_b128 v[188:191], %[l0] offset:31744\n\t" "v_smfmac_f32_16x16x64_bf16 v[236:239], v[240:243], v[160:167], %[ix]\n\t" \
    "v_smfmac_f32_16x16x64_bf16 v[232:235], v[244:247], v[136:143], %[ix]\n\t" "v_smfmac_f32_16x16x64_bf16 v[236:239], v[244:247], v[168:175], %[ix]\n\t" \
    "s_waitcnt lgkmcnt(1)\n\t" "s_barrier\n\t" "v_smfmac_f32_16x16x64_bf16 v[232:235], v[248:251], v[144:151], %[ix]\n\t" \
    "v_smfmac_f32_16x16x64_bf16 v[236:239], v[248:251], v[176:183], %[ix]\n\t" "v_smfmac_f32_16x16x64_bf16 v[232:235], v[252:255], v[152:159], %[ix]\n\t" \
    "s_waitcnt lgkmcnt(0)\n\t" "v_smfmac_f32_16x16x64_bf16 v[236:239], v[252:255], v[184:191], %[ix]\n\t" XT(92, 93) "s_nop 6\n\t" "v_add_f32 v232, v232, v233\n\t" \
    "v_add_f32 v234, v234, v235\n\t" "v_add_f32 v236, v236, v237\n\t" "v_add_f32 v238, v238, v239\n\t" "v_cvt_pk_bf16_f32 v232, v232, v236\n\t" \
    "v_cvt_pk_bf16_f32 v233, v234, v238\n\t" "ds_write2_b32 %[kw], v232, v233 offset1:68\n\t" "s_waitcnt lgkmcnt(0)\n\t" "s_barrier\n\t" XT(94, 95) XT(96, 97) \
    "s_branch 99f\n\t" "41:\n\t" "s_cmp_eq_u32 %[bd], 0\n\t" "s_cbranch_scc1 46f\n\t" "s_sub_u32 %[bd], %[bd], 1\n\t" XCNT(100) "s_waitcnt lgkmcnt(0)\n\t" "s_nop 7\n\t" \
    "s_branch 40b\n\t" "46:\n\t" "s_cmp_lg_u32 s60, 0\n\t" "s_branch 42b\n\t" "44:\n\t" "s_cmp_eq_u32 %[bd], 0\n\t" "s_cbranch_scc1 45b\n\t" "s_sub_u32 %[bd], %[bd], 1\n\t" \
    XCNT(101) "s_mov_b32 s60, 1\n\t" "s_waitcnt lgkmcnt(0)\n\t" "s_nop 7\n\t" "s_branch 40b\n\t" "99:\n\t" "mk_tail:\n\t"
#define VO_STEP \
    "v_xor_b32 v224, 0x400, %[mo]\n\t" "s_mov_b32 s60, 0\n\t" "s_cmp_eq_u32 %[w], 1\n\t" "s_cbranch_scc1 91f\n\t" "s_cmp_eq_u32 %[w], 2\n\t" "s_cbranch_scc1 92f\n\t" \
    "s_cmp_eq_u32 %[w], 3\n\t" "s_cbranch_scc1 93f\n\t" XT(80, 81) "s_getpc_b64 s[62:63]\n\t" "18:\n\t" "s_memtime s[64:65]\n\t" "s_waitcnt lgkmcnt(0)\n\t" \
    "s_sub_u32 s61, %[tg], s64\n\t" "s_cmp_gt_i32 s61, 124\n\t" "s_cbranch_scc1 18b\n\t" "s_max_i32 s61, s61, 0\n\t" "s_and_b32 s61, s61, 124\n\t" \
    "s_sub_u32 s61, 19f-18b, s61\n\t" "s_add_u32 s62, s62, s61\n\t" "s_addc_u32 s63, s63, 0\n\t" "s_setpc_b64 s[62:63]\n\t" ".rept 31\n\t" "s_nop 0\n\t" ".endr\n\t" "19:\n\t" \
    "s_barrier\n\t" XT(82, 83) "10:\n\t" "buffer_load_dwordx4 v[240:243], %[mo], %[rs], %[sb] offen offset:0 sc1\n\t" "v_mov_b64 v[232:233], 0\n\t" \
    "v_mov_b64 v[234:235], 0\n\t" "v_mov_b64 v[236:237], 0\n\t" "v_mov_b64 v[238:239], 0\n\t" "v_mov_b64 v[192:193], 0\n\t" "v_mov_b64 v[194:195], 0\n\t" \
    "v_mov_b64 v[196:197], 0\n\t" "v_mov_b64 v[198:199], 0\n\t" "buffer_load_dwordx4 v[244:247], v224, %[rs], %[sb] offen offset:0 sc1\n\t" \
    "ds_read_b128 v[160:163], %[l0] offset:0\n\t" "ds_read_b128 v[164:167], %[l0] offset:1024\n\t" "ds_read_b128 v[168:171], %[l0] offset:2048\n\t" \
    "ds_read_b128 v[172:175], %[l0] offset:3072\n\t" "s_nop 3\n\t" "buffer_load_dwordx4 v[248:251], %[mo], %[rs], %[sb] offen offset:2048 sc1\n\t" \
    "ds_read_b128 v[176:179], %[l0] offset:4096\n\t" "ds_read_b128 v[180:183], %[l0] offset:5120\n\t" "ds_read_b128 v[184:187], %[l0] offset:6144\n\t" \
    "ds_read_b128 v[188:191], %[l0] offset:7168\n\t" "s_nop 3\n\t" "buffer_load_dwordx4 v[252:255], v224, %[rs], %[sb] offen offset:2048 sc1\n\t" \
    "v_mov_b64 v[200:201], 0\n\t" "v_mov_b64 v[202:203], 0\n\t" "v_mov_b64 v[204:205], 0\n\t" "v_mov_b64 v[206:207], 0\n\t" "v_mov_b64 v[208:209], 0\n\t" \
    "v_mov_b64 v[210:211], 0\n\t" "v_mov_b64 v[212:213], 0\n\t" "v_mov_b64 v[214:215], 0\n\t" "v_mov_b64 v[216:217], 0\n\t" "v_mov_b64 v[218:219], 0\n\t" \
    "v_mov_b64 v[220:221], 0\n\t" "v_mov_b64 v[222:223], 0\n\t" "s_waitcnt vmcnt(3)\n\t" XT(84, 85) "v_smfmac_f32_16x16x64_bf16 v[232:235], v[240:243], a[0:7], %[ix]\n\t" \
    "ds_write_b128 %[l1], v[240:243] offset:0\n\t" "v_smfmac_f32_16x16x64_bf16 v[236:239], v[240:243], a[8:15], %[ix]\n\t" "v_max3_u32 v225, v240, v241, v242\n\t" \
    "s_waitcnt vmcnt(2)\n\t" "v_smfmac_f32_16x16x64_bf16 v[232:235], v[244:247], a[16:23], %[ix]\n\t" "ds_write_b128 %[l1], v[244:247] offset:1024\n\t" \
    "v_max3_u32 v225, v225, v243, v244\n\t" "s_waitcnt lgkmcnt(8)\n\t" "v_smfmac_f32_16x16x64_bf16 v[236:239], v[244:247], v[160:167], %[ix]\n\t" \
    "v_max3_u32 v225, v225, v245, v246\n\t" "v_max_u32 v225, v225, v247\n\t" "v_cmp_eq_u32 vcc, -1, v225\n\t" "s_cmp_lg_u32 s60, 0\n\t" "s_cbranch_vccnz 11f\n\t" "12:\n\t" \
    "s_waitcnt lgkmcnt(0)\n\t" "s_cbranch_scc1 13f\n\t" "s_barrier\n\t" "13:\n\t" XT(86, 87) "ds_read_b128 v[16:19], %[l1] offset:4096\n\t" \
    "ds_read_b128 v[20:23], %[l1] offset:8192\n\t" "ds_read_b128 v[24:27], %[l1] offset:12288\n\t" "s_waitcnt vmcnt(1)\n\t" \
    "v_smfmac_f32_16x16x64_bf16 v[232:235], v[248:251], a[24:31], %[ix]\n\t" "ds_write_b128 %[l1], v[248:251] offset:2048\n\t" "v_max3_u32 v225, v248, v249, v250\n\t" \
    "v_smfmac_f32_16x16x64_bf16 v[236:239], v[248:251], a[32:39], %[ix]\n\t" "ds_read_b128 v[28:31], %[l1] offset:5120\n\t" "ds_read_b128 v[240:243], %[l1] offset:9216\n\t" \
    "ds_read_b128 v[244:247], %[l1] offset:13312\n\t" "s_waitcnt vmcnt(0)\n\t" "v_smfmac_f32_16x16x64_bf16 v[232:235], v[252:255], a[40:47], %[ix]\n\t" \
    "ds_write_b128 %[l1], v[252:255] offset:3072\n\t" "v_max3_u32 v225, v225, v251, v252\n\t" "v_smfmac_f32_16x16x64_bf16 v[236:239], v[252:255], v[168:175], %[ix]\n\t" \
    "v_max3_u32 v225, v225, v253, v254\n\t" "v_max_u32 v225, v225, v255\n\t" "s_waitcnt lgkmcnt(7)\n\t" "v_smfmac_f32_16x16x64_bf16 v[232:235], v[16:19], a[48:55], %[ix]\n\t" \
    "v_cmp_eq_u32 vcc, -1, v225\n\t" "s_cbranch_vccnz 14f\n\t" "15:\n\t" "v_smfmac_f32_16x16x64_bf16 v[236:239], v[16:19], a[56:63], %[ix]\n\t" \
    "ds_read_b128 v[160:163], %[l0] offset:8192\n\t" "ds_read_b128 v[164:167], %[l0] offset:9216\n\t" "s_waitcnt lgkmcnt(8)\n\t" \
    "v_smfmac_f32_16x16x64_bf16 v[232:235], v[20:23], a[64:71], %[ix]\n\t" "ds_read_b128 v[168:171], %[l0] offset:10240\n\t" "ds_read_b128 v[172:175], %[l0] offset:11264\n\t" \
    "v_smfmac_f32_16x16x64_bf16 v[236:239], v[20:23], v[176:183], %[ix]\n\t" "s_waitcnt lgkmcnt(4)\n\t" "s_barrier\n\t" XT(88, 89) "s_memtime s[64:65]\n\t" \
    "v_smfmac_f32_16x16x64_bf16 v[232:235], v[24:27], a[72:79], %[ix]\n\t" "ds_read_b128 v[176:179], %[l0] offset:12288\n\t" "ds_read_b128 v[180:183], %[l0] offset:13312\n\t" \
    "v_smfmac_f32_16x16x64_bf16 v[236:239], v[24:27], a[80:87], %[ix]\n\t" "ds_read_b128 v[248:251], %[l1] offset:6144\n\t" "ds_read_b128 v[252:255], %[l1] offset:10240\n\t" \
    "v_smfmac_f32_16x16x64_bf16 v[232:235], v[28:31], a[88:95], %[ix]\n\t" "ds_read_b128 v[16:19], %[l1] offset:14336\n\t" "ds_read_b128 v[20:23], %[l1] offset:7168\n\t" \
    "v_smfmac_f32_16x16x64_bf16 v[236:239], v[28:31], v[184:191], %[ix]\n\t" "ds_read_b128 v[24:27], %[l1] offset:11264\n\t" "ds_read_b128 v[184:187], %[l0] offset:14336\n\t" \
    "v_smfmac_f32_16x16x64_bf16 v[232:235], v[240:243], a[96:103], %[ix]\n\t" "ds_read_b128 v[188:191], %[l0] offset:15360\n\t" \
    "ds_read_b128 v[28:31], %[l1] offset:15360\n\t" "v_smfmac_f32_16x16x64_bf16 v[236:239], v[240:243], a[104:111], %[ix]\n\t" "s_sub_u32 s62, s64, %[ca]\n\t" \
    "s_mul_i32 s62, s62, %[k1]\n\t" "v_smfmac_f32_16x16x64_bf16 v[232:235], v[244:247], a[112:119], %[ix]\n\t" "s_lshr_b32 s62, s62, 16\n\t" "s_add_u32 s62, s62, %[ga]\n\t" \
    "s_waitcnt lgkmcnt(12)\n\t" "v_smfmac_f32_16x16x64_bf16 v[236:239], v[244:247], v[160:167], %[ix]\n\t" "s_bitset0_b32 s62, 0\n\t" "v_mov_b32 v226, s62\n\t" \
    "s_waitcnt lgkmcnt(7)\n\t" "v_smfmac_f32_16x16x64_bf16 v[232:235], v[248:251], a[120:127], %[ix]\n\t" "buffer_store_dword v226, %[hq], %[rs], %[pb] offen sc1\n\t" \
    "ds_read_b128 v[160:163], %[l0] offset:16384\n\t" "v_smfmac_f32_16x16x64_bf16 v[236:239], v[248:251], a[128:135], %[ix]\n\t" \
    "ds_read_b128 v[164:167], %[l0] offset:17408\n\t" "s_waitcnt lgkmcnt(8)\n\t" "v_smfmac_f32_16x16x64_bf16 v[232:235], v[252:255], a[136:143], %[ix]\n\t" \
    "v_smfmac_f32_16x16x64_bf16 v[236:239], v[252:255], v[168:175], %[ix]\n\t" "ds_read_b128 v[168:171], %[l0] offset:18432\n\t" \
    "ds_read_b128 v[172:175], %[l0] offset:19456\n\t" "s_waitcnt lgkmcnt(9)\n\t" "v_smfmac_f32_16x16x64_bf16 v[232:235], v[16:19], a[144:151], %[ix]\n\t" \
    "v_smfmac_f32_16x16x64_bf16 v[236:239], v[16:19], a[152:159], %[ix]\n\t" "s_waitcnt lgkmcnt(8)\n\t" \
    "v_smfmac_f32_16x16x64_bf16 v[232:235], v[20:23], a[160:167], %[ix]\n\t" "v_smfmac_f32_16x16x64_bf16 v[236:239], v[20:23], v[176:183], %[ix]\n\t" \
    "ds_read_b128 v[176:179], %[l0] offset:20480\n\t" "ds_read_b128 v[180:183], %[l0] offset:21504\n\t" "s_waitcnt lgkmcnt(9)\n\t" \
    "v_smfmac_f32_16x16x64_bf16 v[232:235], v[24:27], a[168:175], %[ix]\n\t" "v_smfmac_f32_16x16x64_bf16 v[236:239], v[24:27], a[176:183], %[ix]\n\t" \
    "s_waitcnt lgkmcnt(6)\n\t" "v_smfmac_f32_16x16x64_bf16 v[232:235], v[28:31], a[184:191], %[ix]\n\t" \
    "v_smfmac_f32_16x16x64_bf16 v[236:239], v[28:31], v[184:191], %[ix]\n\t" "ds_read_b128 v[184:187], %[l0] offset:22528\n\t" \
    "ds_read_b128 v[188:191], %[l0] offset:23552\n\t" XT(90, 91) "s_nop 4\n\t" "v_add_f32 v232, v232, v233\n\t" "v_add_f32 v234, v234, v235\n\t" \
    "v_add_f32 v236, v236, v237\n\t" "v_add_f32 v238, v238, v239\n\t" "v_cvt_pk_bf16_f32 v232, v232, v236\n\t" "v_cvt_pk_bf16_f32 v233, v234, v238\n\t" \
    "ds_write2_b32 %[vw], v232, v233 offset1:68\n\t" "s_waitcnt lgkmcnt(0)\n\t" "s_barrier\n\t" XT(92, 93) "ds_read_b128 v[240:243], %[vr] offset:0\n\t" \
    "ds_read_b128 v[244:247], %[vr] offset:64\n\t" "ds_read_b128 v[248:251], %[vr] offset:128\n\t" "ds_read_b128 v[252:255], %[vr] offset:192\n\t" "s_mov_b32 s61, %[ub]\n\t" \
    "s_waitcnt lgkmcnt(3)\n\t" "v_smfmac_f32_16x16x64_bf16 v[192:195], v[240:243], v[32:39], %[ix]\n\t" \
    "v_smfmac_f32_16x16x64_bf16 v[196:199], v[240:243], v[48:55], %[ix]\n\t" "s_waitcnt lgkmcnt(2)\n\t" \
    "v_smfmac_f32_16x16x64_bf16 v[192:195], v[244:247], v[40:47], %[ix]\n\t" "v_smfmac_f32_16x16x64_bf16 v[196:199], v[244:247], v[56:63], %[ix]\n\t" \
    "s_waitcnt lgkmcnt(1)\n\t" "v_smfmac_f32_16x16x64_bf16 v[192:195], v[248:251], a[192:199], %[ix]\n\t" \
    "v_smfmac_f32_16x16x64_bf16 v[196:199], v[248:251], a[200:207], %[ix]\n\t" "s_waitcnt lgkmcnt(0)\n\t" \
    "v_smfmac_f32_16x16x64_bf16 v[192:195], v[252:255], v[160:167], %[ix]\n\t" "ds_read_b128 v[160:163], %[l0] offset:24576\n\t" \
    "ds_read_b128 v[164:167], %[l0] offset:25600\n\t" "v_smfmac_f32_16x16x64_bf16 v[196:199], v[252:255], v[168:175], %[ix]\n\t" \
    "ds_read_b128 v[168:171], %[l0] offset:26624\n\t" "ds_read_b128 v[172:175], %[l0] offset:27648\n\t" \
    "v_smfmac_f32_16x16x64_bf16 v[200:203], v[240:243], v[64:71], %[ix]\n\t" "s_nop 1\n\t" "v_smfmac_f32_16x16x64_bf16 v[204:207], v[240:243], v[80:87], %[ix]\n\t" \
    "v_cvt_pk_bf16_f32 v224, v192, v194\n\t" "v_cvt_pk_bf16_f32 v225, v193, v195\n\t" "v_smfmac_f32_16x16x64_bf16 v[200:203], v[244:247], v[72:79], %[ix]\n\t" \
    "v_cvt_pk_bf16_f32 v226, v196, v198\n\t" "v_cvt_pk_bf16_f32 v227, v197, v199\n\t" "v_smfmac_f32_16x16x64_bf16 v[204:207], v[244:247], v[88:95], %[ix]\n\t" "s_nop 0\n\t" \
    "v_permlane32_swap_b32 v224, v226\n\t" "v_smfmac_f32_16x16x64_bf16 v[200:203], v[248:251], a[208:215], %[ix]\n\t" "v_permlane32_swap_b32 v225, v227\n\t" "s_nop 0\n\t" \
    "v_smfmac_f32_16x16x64_bf16 v[204:207], v[248:251], a[216:223], %[ix]\n\t" "v_permlane16_swap_b32 v224, v225\n\t" "v_permlane16_swap_b32 v226, v227\n\t" \
    "v_smfmac_f32_16x16x64_bf16 v[200:203], v[252:255], v[176:183], %[ix]\n\t" "buffer_store_dwordx4 v[224:227], %[uo], %[rs], s61 offen sc1\n\t" \
    "s_add_u32 s61, s61, 0x10000\n\t" XT(94, 95) "v_smfmac_f32_16x16x64_bf16 v[204:207], v[252:255], v[184:191], %[ix]\n\t" "ds_read_b128 v[176:179], %[l0] offset:28672\n\t" \
    "ds_read_b128 v[180:183], %[l0] offset:29696\n\t" "v_smfmac_f32_16x16x64_bf16 v[208:211], v[240:243], v[96:103], %[ix]\n\t" \
    "ds_read_b128 v[184:187], %[l0] offset:30720\n\t" "ds_read_b128 v[188:191], %[l0] offset:31744\n\t" \
    "v_smfmac_f32_16x16x64_bf16 v[212:215], v[240:243], v[112:119], %[ix]\n\t" "v_cvt_pk_bf16_f32 v228, v200, v202\n\t" "v_cvt_pk_bf16_f32 v229, v201, v203\n\t" \
    "v_smfmac_f32_16x16x64_bf16 v[208:211], v[244:247], v[104:111], %[ix]\n\t" "v_cvt_pk_bf16_f32 v230, v204, v206\n\t" "v_cvt_pk_bf16_f32 v231, v205, v207\n\t" \
    "v_smfmac_f32_16x16x64_bf16 v[212:215], v[244:247], v[120:127], %[ix]\n\t" "s_nop 0\n\t" "v_permlane32_swap_b32 v228, v230\n\t" \
    "v_smfmac_f32_16x16x64_bf16 v[208:211], v[248:251], a[224:231], %[ix]\n\t" "v_permlane32_swap_b32 v229, v231\n\t" "s_nop 0\n\t" \
    "v_smfmac_f32_16x16x64_bf16 v[212:215], v[248:251], a[232:239], %[ix]\n\t" "v_permlane16_swap_b32 v228, v229\n\t" "v_permlane16_swap_b32 v230, v231\n\t" \
    "s_waitcnt lgkmcnt(6)\n\t" "v_smfmac_f32_16x16x64_bf16 v[208:211], v[252:255], v[160:167], %[ix]\n\t" "buffer_store_dwordx4 v[228:231], %[uo], %[rs], s61 offen sc1\n\t" \
    "s_add_u32 s61, s61, 0x10000\n\t" "s_waitcnt lgkmcnt(4)\n\t" "v_smfmac_f32_16x16x64_bf16 v[212:215], v[252:255], v[168:175], %[ix]\n\t" "s_nop 1\n\t" \
    "v_smfmac_f32_16x16x64_bf16 v[216:219], v[240:243], v[128:135], %[ix]\n\t" "s_nop 1\n\t" "v_smfmac_f32_16x16x64_bf16 v[220:223], v[240:243], v[144:151], %[ix]\n\t" \
    "v_cvt_pk_bf16_f32 v224, v208, v210\n\t" "v_cvt_pk_bf16_f32 v225, v209, v211\n\t" "v_smfmac_f32_16x16x64_bf16 v[216:219], v[244:247], v[136:143], %[ix]\n\t" \
    "v_cvt_pk_bf16_f32 v226, v212, v214\n\t" "v_cvt_pk_bf16_f32 v227, v213, v215\n\t" "v_smfmac_f32_16x16x64_bf16 v[220:223], v[244:247], v[152:159], %[ix]\n\t" "s_nop 0\n\t" \
    "v_permlane32_swap_b32 v224, v226\n\t" "v_smfmac_f32_16x16x64_bf16 v[216:219], v[248:251], a[240:247], %[ix]\n\t" "v_permlane32_swap_b32 v225, v227\n\t" "s_nop 0\n\t" \
    "v_smfmac_f32_16x16x64_bf16 v[220:223], v[248:251], a[248:255], %[ix]\n\t" "v_permlane16_swap_b32 v224, v225\n\t" "v_permlane16_swap_b32 v226, v227\n\t" \
    "s_waitcnt lgkmcnt(2)\n\t" "v_smfmac_f32_16x16x64_bf16 v[216:219], v[252:255], v[176:183], %[ix]\n\t" "buffer_store_dwordx4 v[224:227], %[uo], %[rs], s61 offen sc1\n\t" \
    "s_add_u32 s61, s61, 0x10000\n\t" "s_waitcnt lgkmcnt(0)\n\t" "v_smfmac_f32_16x16x64_bf16 v[220:223], v[252:255], v[184:191], %[ix]\n\t" "s_nop 5\n\t" \
    "v_cvt_pk_bf16_f32 v228, v216, v218\n\t" "v_cvt_pk_bf16_f32 v229, v217, v219\n\t" "v_cvt_pk_bf16_f32 v230, v220, v222\n\t" "v_cvt_pk_bf16_f32 v231, v221, v223\n\t" \
    "s_nop 0\n\t" "v_permlane32_swap_b32 v228, v230\n\t" "v_permlane32_swap_b32 v229, v231\n\t" "s_nop 1\n\t" "v_permlane16_swap_b32 v228, v229\n\t" \
    "v_permlane16_swap_b32 v230, v231\n\t" "buffer_store_dwordx4 v[228:231], %[uo], %[rs], s61 offen sc1\n\t" "s_nop 1\n\t" XT(96, 97) "s_branch 99f\n\t" "11:\n\t" \
    "s_cmp_eq_u32 %[bd], 0\n\t" "s_cbranch_scc1 16f\n\t" "s_sub_u32 %[bd], %[bd], 1\n\t" XCNT(100) "s_waitcnt lgkmcnt(0)\n\t" "s_nop 7\n\t" "s_branch 10b\n\t" "16:\n\t" \
    "s_cmp_lg_u32 s60, 0\n\t" "s_branch 12b\n\t" "14:\n\t" "s_cmp_eq_u32 %[bd], 0\n\t" "s_cbranch_scc1 15b\n\t" "s_sub_u32 %[bd], %[bd], 1\n\t" XCNT(101) \
    "s_mov_b32 s60, 1\n\t" "s_waitcnt lgkmcnt(0)\n\t" "s_nop 7\n\t" "s_branch 10b\n\t" "91:\n\t" XT(80, 81) "s_barrier\n\t" XT(82, 83) "20:\n\t" \
    "buffer_load_dwordx4 v[240:243], %[mo], %[rs], %[sb] offen offset:0 sc1\n\t" "v_mov_b64 v[232:233], 0\n\t" "v_mov_b64 v[234:235], 0\n\t" "v_mov_b64 v[236:237], 0\n\t" \
    "v_mov_b64 v[238:239], 0\n\t" "v_mov_b64 v[192:193], 0\n\t" "v_mov_b64 v[194:195], 0\n\t" "v_mov_b64 v[196:197], 0\n\t" "v_mov_b64 v[198:199], 0\n\t" \
    "buffer_load_dwordx4 v[244:247], v224, %[rs], %[sb] offen offset:0 sc1\n\t" "ds_read_b128 v[160:163], %[l0] offset:0\n\t" "ds_read_b128 v[164:167], %[l0] offset:1024\n\t" \
    "ds_read_b128 v[168:171], %[l0] offset:2048\n\t" "ds_read_b128 v[172:175], %[l0] offset:3072\n\t" "s_nop 3\n\t" \
    "buffer_load_dwordx4 v[248:251], %[mo], %[rs], %[sb] offen offset:2048 sc1\n\t" "ds_read_b128 v[176:179], %[l0] offset:4096\n\t" \
    "ds_read_b128 v[180:183], %[l0] offset:5120\n\t" "ds_read_b128 v[184:187], %[l0] offset:6144\n\t" "ds_read_b128 v[188:191], %[l0] offset:7168\n\t" "s_nop 3\n\t" \
    "buffer_load_dwordx4 v[252:255], v224, %[rs], %[sb] offen offset:2048 sc1\n\t" "v_mov_b64 v[200:201], 0\n\t" "v_mov_b64 v[202:203], 0\n\t" "v_mov_b64 v[204:205], 0\n\t" \
    "v_mov_b64 v[206:207], 0\n\t" "v_mov_b64 v[208:209], 0\n\t" "v_mov_b64 v[210:211], 0\n\t" "v_mov_b64 v[212:213], 0\n\t" "v_mov_b64 v[214:215], 0\n\t" \
    "v_mov_b64 v[216:217], 0\n\t" "v_mov_b64 v[218:219], 0\n\t" "v_mov_b64 v[220:221], 0\n\t" "v_mov_b64 v[222:223], 0\n\t" "s_waitcnt vmcnt(3)\n\t" XT(84, 85) \
    "v_smfmac_f32_16x16x64_bf16 v[232:235], v[240:243], a[0:7], %[ix]\n\t" "ds_write_b128 %[l1], v[240:243] offset:4096\n\t" \
    "v_smfmac_f32_16x16x64_bf16 v[236:239], v[240:243], a[8:15], %[ix]\n\t" "v_max3_u32 v225, v240, v241, v242\n\t" "s_waitcnt vmcnt(2)\n\t" \
    "v_smfmac_f32_16x16x64_bf16 v[232:235], v[244:247], a[16:23], %[ix]\n\t" "ds_write_b128 %[l1], v[244:247] offset:5120\n\t" "v_max3_u32 v225, v225, v243, v244\n\t" \
    "s_waitcnt lgkmcnt(8)\n\t" "v_smfmac_f32_16x16x64_bf16 v[236:239], v[244:247], v[160:167], %[ix]\n\t" "v_max3_u32 v225, v225, v245, v246\n\t" \
    "v_max_u32 v225, v225, v247\n\t" "v_cmp_eq_u32 vcc, -1, v225\n\t" "s_cmp_lg_u32 s60, 0\n\t" "s_cbranch_vccnz 21f\n\t" "22:\n\t" "s_waitcnt lgkmcnt(0)\n\t" \
    "s_cbranch_scc1 23f\n\t" "s_barrier\n\t" "23:\n\t" XT(86, 87) "ds_read_b128 v[16:19], %[l1] offset:8192\n\t" "ds_read_b128 v[20:23], %[l1] offset:12288\n\t" \
    "ds_read_b128 v[24:27], %[l1] offset:0\n\t" "s_waitcnt vmcnt(1)\n\t" "v_smfmac_f32_16x16x64_bf16 v[232:235], v[248:251], a[24:31], %[ix]\n\t" \
    "ds_write_b128 %[l1], v[248:251] offset:6144\n\t" "v_max3_u32 v225, v248, v249, v250\n\t" "v_smfmac_f32_16x16x64_bf16 v[236:239], v[248:251], a[32:39], %[ix]\n\t" \
    "ds_read_b128 v[28:31], %[l1] offset:9216\n\t" "ds_read_b128 v[240:243], %[l1] offset:13312\n\t" "ds_read_b128 v[244:247], %[l1] offset:1024\n\t" "s_waitcnt vmcnt(0)\n\t" \
    "v_smfmac_f32_16x16x64_bf16 v[232:235], v[252:255], a[40:47], %[ix]\n\t" "ds_write_b128 %[l1], v[252:255] offset:7168\n\t" "v_max3_u32 v225, v225, v251, v252\n\t" \
    "v_smfmac_f32_16x16x64_bf16 v[236:239], v[252:255], v[168:175], %[ix]\n\t" "v_max3_u32 v225, v225, v253, v254\n\t" "v_max_u32 v225, v225, v255\n\t" \
    "s_waitcnt lgkmcnt(7)\n\t" "v_smfmac_f32_16x16x64_bf16 v[232:235], v[16:19], a[48:55], %[ix]\n\t" "v_cmp_eq_u32 vcc, -1, v225\n\t" "s_cbranch_vccnz 24f\n\t" "25:\n\t" \
    "v_smfmac_f32_16x16x64_bf16 v[236:239], v[16:19], a[56:63], %[ix]\n\t" "ds_read_b128 v[160:163], %[l0] offset:8192\n\t" "ds_read_b128 v[164:167], %[l0] offset:9216\n\t" \
    "s_waitcnt lgkmcnt(8)\n\t" "v_smfmac_f32_16x16x64_bf16 v[232:235], v[20:23], a[64:71], %[ix]\n\t" "ds_read_b128 v[168:171], %[l0] offset:10240\n\t" \
    "ds_read_b128 v[172:175], %[l0] offset:11264\n\t" "v_smfmac_f32_16x16x64_bf16 v[236:239], v[20:23], v[176:183], %[ix]\n\t" "s_waitcnt lgkmcnt(4)\n\t" "s_barrier\n\t" \
    XT(88, 89) "v_smfmac_f32_16x16x64_bf16 v[232:235], v[24:27], a[72:79], %[ix]\n\t" "ds_read_b128 v[176:179], %[l0] offset:12288\n\t" \
    "ds_read_b128 v[180:183], %[l0] offset:13312\n\t" "v_smfmac_f32_16x16x64_bf16 v[236:239], v[24:27], a[80:87], %[ix]\n\t" "ds_read_b128 v[248:251], %[l1] offset:10240\n\t" \
    "ds_read_b128 v[252:255], %[l1] offset:14336\n\t" "v_smfmac_f32_16x16x64_bf16 v[232:235], v[28:31], a[88:95], %[ix]\n\t" "ds_read_b128 v[16:19], %[l1] offset:2048\n\t" \
    "ds_read_b128 v[20:23], %[l1] offset:11264\n\t" "v_smfmac_f32_16x16x64_bf16 v[236:239], v[28:31], v[184:191], %[ix]\n\t" "ds_read_b128 v[24:27], %[l1] offset:15360\n\t" \
    "ds_read_b128 v[184:187], %[l0] offset:14336\n\t" "v_smfmac_f32_16x16x64_bf16 v[232:235], v[240:243], a[96:103], %[ix]\n\t" \
    "ds_read_b128 v[188:191], %[l0] offset:15360\n\t" "ds_read_b128 v[28:31], %[l1] offset:3072\n\t" \
    "v_smfmac_f32_16x16x64_bf16 v[236:239], v[240:243], a[104:111], %[ix]\n\t" "v_smfmac_f32_16x16x64_bf16 v[232:235], v[244:247], a[112:119], %[ix]\n\t" \
    "s_waitcnt lgkmcnt(12)\n\t" "v_smfmac_f32_16x16x64_bf16 v[236:239], v[244:247], v[160:167], %[ix]\n\t" "ds_read_b128 v[160:163], %[l0] offset:16384\n\t" \
    "ds_read_b128 v[164:167], %[l0] offset:17408\n\t" "s_waitcnt lgkmcnt(9)\n\t" "v_smfmac_f32_16x16x64_bf16 v[232:235], v[248:251], a[120:127], %[ix]\n\t" \
    "v_smfmac_f32_16x16x64_bf16 v[236:239], v[248:251], a[128:135], %[ix]\n\t" "s_waitcnt lgkmcnt(8)\n\t" \
    "v_smfmac_f32_16x16x64_bf16 v[232:235], v[252:255], a[136:143], %[ix]\n\t" "v_smfmac_f32_16x16x64_bf16 v[236:239], v[252:255], v[168:175], %[ix]\n\t" \
    "ds_read_b128 v[168:171], %[l0] offset:18432\n\t" "ds_read_b128 v[172:175], %[l0] offset:19456\n\t" "s_waitcnt lgkmcnt(9)\n\t" \
    "v_smfmac_f32_16x16x64_bf16 v[232:235], v[16:19], a[144:151], %[ix]\n\t" "v_smfmac_f32_16x16x64_bf16 v[236:239], v[16:19], a[152:159], %[ix]\n\t" \
    "s_waitcnt lgkmcnt(8)\n\t" "v_smfmac_f32_16x16x64_bf16 v[232:235], v[20:23], a[160:167], %[ix]\n\t" \
    "v_smfmac_f32_16x16x64_bf16 v[236:239], v[20:23], v[176:183], %[ix]\n\t" "ds_read_b128 v[176:179], %[l0] offset:20480\n\t" \
    "ds_read_b128 v[180:183], %[l0] offset:21504\n\t" "s_waitcnt lgkmcnt(9)\n\t" "v_smfmac_f32_16x16x64_bf16 v[232:235], v[24:27], a[168:175], %[ix]\n\t" \
    "v_smfmac_f32_16x16x64_bf16 v[236:239], v[24:27], a[176:183], %[ix]\n\t" "s_waitcnt lgkmcnt(6)\n\t" \
    "v_smfmac_f32_16x16x64_bf16 v[232:235], v[28:31], a[184:191], %[ix]\n\t" "v_smfmac_f32_16x16x64_bf16 v[236:239], v[28:31], v[184:191], %[ix]\n\t" \
    "ds_read_b128 v[184:187], %[l0] offset:22528\n\t" "ds_read_b128 v[188:191], %[l0] offset:23552\n\t" XT(90, 91) "s_nop 4\n\t" "v_add_f32 v232, v232, v233\n\t" \
    "v_add_f32 v234, v234, v235\n\t" "v_add_f32 v236, v236, v237\n\t" "v_add_f32 v238, v238, v239\n\t" "v_cvt_pk_bf16_f32 v232, v232, v236\n\t" \
    "v_cvt_pk_bf16_f32 v233, v234, v238\n\t" "ds_write2_b32 %[vw], v232, v233 offset1:68\n\t" "s_waitcnt lgkmcnt(0)\n\t" "s_barrier\n\t" XT(92, 93) \
    "ds_read_b128 v[240:243], %[vr] offset:0\n\t" "ds_read_b128 v[244:247], %[vr] offset:64\n\t" "ds_read_b128 v[248:251], %[vr] offset:128\n\t" \
    "ds_read_b128 v[252:255], %[vr] offset:192\n\t" "s_mov_b32 s61, %[ub]\n\t" "s_waitcnt lgkmcnt(3)\n\t" \
    "v_smfmac_f32_16x16x64_bf16 v[192:195], v[240:243], v[32:39], %[ix]\n\t" "v_smfmac_f32_16x16x64_bf16 v[196:199], v[240:243], v[48:55], %[ix]\n\t" \
    "s_waitcnt lgkmcnt(2)\n\t" "v_smfmac_f32_16x16x64_bf16 v[192:195], v[244:247], v[40:47], %[ix]\n\t" \
    "v_smfmac_f32_16x16x64_bf16 v[196:199], v[244:247], v[56:63], %[ix]\n\t" "s_waitcnt lgkmcnt(1)\n\t" \
    "v_smfmac_f32_16x16x64_bf16 v[192:195], v[248:251], a[192:199], %[ix]\n\t" "v_smfmac_f32_16x16x64_bf16 v[196:199], v[248:251], a[200:207], %[ix]\n\t" \
    "s_waitcnt lgkmcnt(0)\n\t" "v_smfmac_f32_16x16x64_bf16 v[192:195], v[252:255], v[160:167], %[ix]\n\t" "ds_read_b128 v[160:163], %[l0] offset:24576\n\t" \
    "ds_read_b128 v[164:167], %[l0] offset:25600\n\t" "v_smfmac_f32_16x16x64_bf16 v[196:199], v[252:255], v[168:175], %[ix]\n\t" \
    "ds_read_b128 v[168:171], %[l0] offset:26624\n\t" "ds_read_b128 v[172:175], %[l0] offset:27648\n\t" \
    "v_smfmac_f32_16x16x64_bf16 v[200:203], v[240:243], v[64:71], %[ix]\n\t" "s_nop 1\n\t" "v_smfmac_f32_16x16x64_bf16 v[204:207], v[240:243], v[80:87], %[ix]\n\t" \
    "v_cvt_pk_bf16_f32 v224, v192, v194\n\t" "v_cvt_pk_bf16_f32 v225, v193, v195\n\t" "v_smfmac_f32_16x16x64_bf16 v[200:203], v[244:247], v[72:79], %[ix]\n\t" \
    "v_cvt_pk_bf16_f32 v226, v196, v198\n\t" "v_cvt_pk_bf16_f32 v227, v197, v199\n\t" "v_smfmac_f32_16x16x64_bf16 v[204:207], v[244:247], v[88:95], %[ix]\n\t" "s_nop 0\n\t" \
    "v_permlane32_swap_b32 v224, v226\n\t" "v_smfmac_f32_16x16x64_bf16 v[200:203], v[248:251], a[208:215], %[ix]\n\t" "v_permlane32_swap_b32 v225, v227\n\t" "s_nop 0\n\t" \
    "v_smfmac_f32_16x16x64_bf16 v[204:207], v[248:251], a[216:223], %[ix]\n\t" "v_permlane16_swap_b32 v224, v225\n\t" "v_permlane16_swap_b32 v226, v227\n\t" \
    "v_smfmac_f32_16x16x64_bf16 v[200:203], v[252:255], v[176:183], %[ix]\n\t" "buffer_store_dwordx4 v[224:227], %[uo], %[rs], s61 offen sc1\n\t" \
    "s_add_u32 s61, s61, 0x10000\n\t" XT(94, 95) "v_smfmac_f32_16x16x64_bf16 v[204:207], v[252:255], v[184:191], %[ix]\n\t" "ds_read_b128 v[176:179], %[l0] offset:28672\n\t" \
    "ds_read_b128 v[180:183], %[l0] offset:29696\n\t" "v_smfmac_f32_16x16x64_bf16 v[208:211], v[240:243], v[96:103], %[ix]\n\t" \
    "ds_read_b128 v[184:187], %[l0] offset:30720\n\t" "ds_read_b128 v[188:191], %[l0] offset:31744\n\t" \
    "v_smfmac_f32_16x16x64_bf16 v[212:215], v[240:243], v[112:119], %[ix]\n\t" "v_cvt_pk_bf16_f32 v228, v200, v202\n\t" "v_cvt_pk_bf16_f32 v229, v201, v203\n\t" \
    "v_smfmac_f32_16x16x64_bf16 v[208:211], v[244:247], v[104:111], %[ix]\n\t" "v_cvt_pk_bf16_f32 v230, v204, v206\n\t" "v_cvt_pk_bf16_f32 v231, v205, v207\n\t" \
    "v_smfmac_f32_16x16x64_bf16 v[212:215], v[244:247], v[120:127], %[ix]\n\t" "s_nop 0\n\t" "v_permlane32_swap_b32 v228, v230\n\t" \
    "v_smfmac_f32_16x16x64_bf16 v[208:211], v[248:251], a[224:231], %[ix]\n\t" "v_permlane32_swap_b32 v229, v231\n\t" "s_nop 0\n\t" \
    "v_smfmac_f32_16x16x64_bf16 v[212:215], v[248:251], a[232:239], %[ix]\n\t" "v_permlane16_swap_b32 v228, v229\n\t" "v_permlane16_swap_b32 v230, v231\n\t" \
    "s_waitcnt lgkmcnt(6)\n\t" "v_smfmac_f32_16x16x64_bf16 v[208:211], v[252:255], v[160:167], %[ix]\n\t" "buffer_store_dwordx4 v[228:231], %[uo], %[rs], s61 offen sc1\n\t" \
    "s_add_u32 s61, s61, 0x10000\n\t" "s_waitcnt lgkmcnt(4)\n\t" "v_smfmac_f32_16x16x64_bf16 v[212:215], v[252:255], v[168:175], %[ix]\n\t" "s_nop 1\n\t" \
    "v_smfmac_f32_16x16x64_bf16 v[216:219], v[240:243], v[128:135], %[ix]\n\t" "s_nop 1\n\t" "v_smfmac_f32_16x16x64_bf16 v[220:223], v[240:243], v[144:151], %[ix]\n\t" \
    "v_cvt_pk_bf16_f32 v224, v208, v210\n\t" "v_cvt_pk_bf16_f32 v225, v209, v211\n\t" "v_smfmac_f32_16x16x64_bf16 v[216:219], v[244:247], v[136:143], %[ix]\n\t" \
    "v_cvt_pk_bf16_f32 v226, v212, v214\n\t" "v_cvt_pk_bf16_f32 v227, v213, v215\n\t" "v_smfmac_f32_16x16x64_bf16 v[220:223], v[244:247], v[152:159], %[ix]\n\t" "s_nop 0\n\t" \
    "v_permlane32_swap_b32 v224, v226\n\t" "v_smfmac_f32_16x16x64_bf16 v[216:219], v[248:251], a[240:247], %[ix]\n\t" "v_permlane32_swap_b32 v225, v227\n\t" "s_nop 0\n\t" \
    "v_smfmac_f32_16x16x64_bf16 v[220:223], v[248:251], a[248:255], %[ix]\n\t" "v_permlane16_swap_b32 v224, v225\n\t" "v_permlane16_swap_b32 v226, v227\n\t" \
    "s_waitcnt lgkmcnt(2)\n\t" "v_smfmac_f32_16x16x64_bf16 v[216:219], v[252:255], v[176:183], %[ix]\n\t" "buffer_store_dwordx4 v[224:227], %[uo], %[rs], s61 offen sc1\n\t" \
    "s_add_u32 s61, s61, 0x10000\n\t" "s_waitcnt lgkmcnt(0)\n\t" "v_smfmac_f32_16x16x64_bf16 v[220:223], v[252:255], v[184:191], %[ix]\n\t" "s_nop 5\n\t" \
    "v_cvt_pk_bf16_f32 v228, v216, v218\n\t" "v_cvt_pk_bf16_f32 v229, v217, v219\n\t" "v_cvt_pk_bf16_f32 v230, v220, v222\n\t" "v_cvt_pk_bf16_f32 v231, v221, v223\n\t" \
    "s_nop 0\n\t" "v_permlane32_swap_b32 v228, v230\n\t" "v_permlane32_swap_b32 v229, v231\n\t" "s_nop 1\n\t" "v_permlane16_swap_b32 v228, v229\n\t" \
    "v_permlane16_swap_b32 v230, v231\n\t" "buffer_store_dwordx4 v[228:231], %[uo], %[rs], s61 offen sc1\n\t" "s_nop 1\n\t" XT(96, 97) "s_branch 99f\n\t" "21:\n\t" \
    "s_cmp_eq_u32 %[bd], 0\n\t" "s_cbranch_scc1 26f\n\t" "s_sub_u32 %[bd], %[bd], 1\n\t" XCNT(100) "s_waitcnt lgkmcnt(0)\n\t" "s_nop 7\n\t" "s_branch 20b\n\t" "26:\n\t" \
    "s_cmp_lg_u32 s60, 0\n\t" "s_branch 22b\n\t" "24:\n\t" "s_cmp_eq_u32 %[bd], 0\n\t" "s_cbranch_scc1 25b\n\t" "s_sub_u32 %[bd], %[bd], 1\n\t" XCNT(101) \
    "s_mov_b32 s60, 1\n\t" "s_waitcnt lgkmcnt(0)\n\t" "s_nop 7\n\t" "s_branch 20b\n\t" "92:\n\t" XT(80, 81) "s_barrier\n\t" XT(82, 83) "30:\n\t" \
    "buffer_load_dwordx4 v[240:243], %[mo], %[rs], %[sb] offen offset:0 sc1\n\t" "v_mov_b64 v[232:233], 0\n\t" "v_mov_b64 v[234:235], 0\n\t" "v_mov_b64 v[236:237], 0\n\t" \
    "v_mov_b64 v[238:239], 0\n\t" "v_mov_b64 v[192:193], 0\n\t" "v_mov_b64 v[194:195], 0\n\t" "v_mov_b64 v[196:197], 0\n\t" "v_mov_b64 v[198:199], 0\n\t" \
    "buffer_load_dwordx4 v[244:247], v224, %[rs], %[sb] offen offset:0 sc1\n\t" "ds_read_b128 v[160:163], %[l0] offset:0\n\t" "ds_read_b128 v[164:167], %[l0] offset:1024\n\t" \
    "ds_read_b128 v[168:171], %[l0] offset:2048\n\t" "ds_read_b128 v[172:175], %[l0] offset:3072\n\t" "s_nop 3\n\t" \
    "buffer_load_dwordx4 v[248:251], %[mo], %[rs], %[sb] offen offset:2048 sc1\n\t" "ds_read_b128 v[176:179], %[l0] offset:4096\n\t" \
    "ds_read_b128 v[180:183], %[l0] offset:5120\n\t" "ds_read_b128 v[184:187], %[l0] offset:6144\n\t" "ds_read_b128 v[188:191], %[l0] offset:7168\n\t" "s_nop 3\n\t" \
    "buffer_load_dwordx4 v[252:255], v224, %[rs], %[sb] offen offset:2048 sc1\n\t" "v_mov_b64 v[200:201], 0\n\t" "v_mov_b64 v[202:203], 0\n\t" "v_mov_b64 v[204:205], 0\n\t" \
    "v_mov_b64 v[206:207], 0\n\t" "v_mov_b64 v[208:209], 0\n\t" "v_mov_b64 v[210:211], 0\n\t" "v_mov_b64 v[212:213], 0\n\t" "v_mov_b64 v[214:215], 0\n\t" \
    "v_mov_b64 v[216:217], 0\n\t" "v_mov_b64 v[218:219], 0\n\t" "v_mov_b64 v[220:221], 0\n\t" "v_mov_b64 v[222:223], 0\n\t" "s_waitcnt vmcnt(3)\n\t" XT(84, 85) \
    "v_smfmac_f32_16x16x64_bf16 v[232:235], v[240:243], a[0:7], %[ix]\n\t" "ds_write_b128 %[l1], v[240:243] offset:8192\n\t" \
    "v_smfmac_f32_16x16x64_bf16 v[236:239], v[240:243], a[8:15], %[ix]\n\t" "v_max3_u32 v225, v240, v241, v242\n\t" "s_waitcnt vmcnt(2)\n\t" \
    "v_smfmac_f32_16x16x64_bf16 v[232:235], v[244:247], a[16:23], %[ix]\n\t" "ds_write_b128 %[l1], v[244:247] offset:9216\n\t" "v_max3_u32 v225, v225, v243, v244\n\t" \
    "s_waitcnt lgkmcnt(8)\n\t" "v_smfmac_f32_16x16x64_bf16 v[236:239], v[244:247], v[160:167], %[ix]\n\t" "v_max3_u32 v225, v225, v245, v246\n\t" \
    "v_max_u32 v225, v225, v247\n\t" "v_cmp_eq_u32 vcc, -1, v225\n\t" "s_cmp_lg_u32 s60, 0\n\t" "s_cbranch_vccnz 31f\n\t" "32:\n\t" "s_waitcnt lgkmcnt(0)\n\t" \
    "s_cbranch_scc1 33f\n\t" "s_barrier\n\t" "33:\n\t" XT(86, 87) "ds_read_b128 v[16:19], %[l1] offset:12288\n\t" "ds_read_b128 v[20:23], %[l1] offset:0\n\t" \
    "ds_read_b128 v[24:27], %[l1] offset:4096\n\t" "s_waitcnt vmcnt(1)\n\t" "v_smfmac_f32_16x16x64_bf16 v[232:235], v[248:251], a[24:31], %[ix]\n\t" \
    "ds_write_b128 %[l1], v[248:251] offset:10240\n\t" "v_max3_u32 v225, v248, v249, v250\n\t" "v_smfmac_f32_16x16x64_bf16 v[236:239], v[248:251], a[32:39], %[ix]\n\t" \
    "ds_read_b128 v[28:31], %[l1] offset:13312\n\t" "ds_read_b128 v[240:243], %[l1] offset:1024\n\t" "ds_read_b128 v[244:247], %[l1] offset:5120\n\t" "s_waitcnt vmcnt(0)\n\t" \
    "v_smfmac_f32_16x16x64_bf16 v[232:235], v[252:255], a[40:47], %[ix]\n\t" "ds_write_b128 %[l1], v[252:255] offset:11264\n\t" "v_max3_u32 v225, v225, v251, v252\n\t" \
    "v_smfmac_f32_16x16x64_bf16 v[236:239], v[252:255], v[168:175], %[ix]\n\t" "v_max3_u32 v225, v225, v253, v254\n\t" "v_max_u32 v225, v225, v255\n\t" \
    "s_waitcnt lgkmcnt(7)\n\t" "v_smfmac_f32_16x16x64_bf16 v[232:235], v[16:19], a[48:55], %[ix]\n\t" "v_cmp_eq_u32 vcc, -1, v225\n\t" "s_cbranch_vccnz 34f\n\t" "35:\n\t" \
    "v_smfmac_f32_16x16x64_bf16 v[236:239], v[16:19], a[56:63], %[ix]\n\t" "ds_read_b128 v[160:163], %[l0] offset:8192\n\t" "ds_read_b128 v[164:167], %[l0] offset:9216\n\t" \
    "s_waitcnt lgkmcnt(8)\n\t" "v_smfmac_f32_16x16x64_bf16 v[232:235], v[20:23], a[64:71], %[ix]\n\t" "ds_read_b128 v[168:171], %[l0] offset:10240\n\t" \
    "ds_read_b128 v[172:175], %[l0] offset:11264\n\t" "v_smfmac_f32_16x16x64_bf16 v[236:239], v[20:23], v[176:183], %[ix]\n\t" "s_waitcnt lgkmcnt(4)\n\t" "s_barrier\n\t" \
    XT(88, 89) "v_smfmac_f32_16x16x64_bf16 v[232:235], v[24:27], a[72:79], %[ix]\n\t" "ds_read_b128 v[176:179], %[l0] offset:12288\n\t" \
    "ds_read_b128 v[180:183], %[l0] offset:13312\n\t" "v_smfmac_f32_16x16x64_bf16 v[236:239], v[24:27], a[80:87], %[ix]\n\t" "ds_read_b128 v[248:251], %[l1] offset:14336\n\t" \
    "ds_read_b128 v[252:255], %[l1] offset:2048\n\t" "v_smfmac_f32_16x16x64_bf16 v[232:235], v[28:31], a[88:95], %[ix]\n\t" "ds_read_b128 v[16:19], %[l1] offset:6144\n\t" \
    "ds_read_b128 v[20:23], %[l1] offset:15360\n\t" "v_smfmac_f32_16x16x64_bf16 v[236:239], v[28:31], v[184:191], %[ix]\n\t" "ds_read_b128 v[24:27], %[l1] offset:3072\n\t" \
    "ds_read_b128 v[184:187], %[l0] offset:14336\n\t" "v_smfmac_f32_16x16x64_bf16 v[232:235], v[240:243], a[96:103], %[ix]\n\t" \
    "ds_read_b128 v[188:191], %[l0] offset:15360\n\t" "ds_read_b128 v[28:31], %[l1] offset:7168\n\t" \
    "v_smfmac_f32_16x16x64_bf16 v[236:239], v[240:243], a[104:111], %[ix]\n\t" "v_smfmac_f32_16x16x64_bf16 v[232:235], v[244:247], a[112:119], %[ix]\n\t" \
    "s_waitcnt lgkmcnt(12)\n\t" "v_smfmac_f32_16x16x64_bf16 v[236:239], v[244:247], v[160:167], %[ix]\n\t" "ds_read_b128 v[160:163], %[l0] offset:16384\n\t" \
    "ds_read_b128 v[164:167], %[l0] offset:17408\n\t" "s_waitcnt lgkmcnt(9)\n\t" "v_smfmac_f32_16x16x64_bf16 v[232:235], v[248:251], a[120:127], %[ix]\n\t" \
    "v_smfmac_f32_16x16x64_bf16 v[236:239], v[248:251], a[128:135], %[ix]\n\t" "s_waitcnt lgkmcnt(8)\n\t" \
    "v_smfmac_f32_16x16x64_bf16 v[232:235], v[252:255], a[136:143], %[ix]\n\t" "v_smfmac_f32_16x16x64_bf16 v[236:239], v[252:255], v[168:175], %[ix]\n\t" \
    "ds_read_b128 v[168:171], %[l0] offset:18432\n\t" "ds_read_b128 v[172:175], %[l0] offset:19456\n\t" "s_waitcnt lgkmcnt(9)\n\t" \
    "v_smfmac_f32_16x16x64_bf16 v[232:235], v[16:19], a[144:151], %[ix]\n\t" "v_smfmac_f32_16x16x64_bf16 v[236:239], v[16:19], a[152:159], %[ix]\n\t" \
    "s_waitcnt lgkmcnt(8)\n\t" "v_smfmac_f32_16x16x64_bf16 v[232:235], v[20:23], a[160:167], %[ix]\n\t" \
    "v_smfmac_f32_16x16x64_bf16 v[236:239], v[20:23], v[176:183], %[ix]\n\t" "ds_read_b128 v[176:179], %[l0] offset:20480\n\t" \
    "ds_read_b128 v[180:183], %[l0] offset:21504\n\t" "s_waitcnt lgkmcnt(9)\n\t" "v_smfmac_f32_16x16x64_bf16 v[232:235], v[24:27], a[168:175], %[ix]\n\t" \
    "v_smfmac_f32_16x16x64_bf16 v[236:239], v[24:27], a[176:183], %[ix]\n\t" "s_waitcnt lgkmcnt(6)\n\t" \
    "v_smfmac_f32_16x16x64_bf16 v[232:235], v[28:31], a[184:191], %[ix]\n\t" "v_smfmac_f32_16x16x64_bf16 v[236:239], v[28:31], v[184:191], %[ix]\n\t" \
    "ds_read_b128 v[184:187], %[l0] offset:22528\n\t" "ds_read_b128 v[188:191], %[l0] offset:23552\n\t" XT(90, 91) "s_nop 4\n\t" "v_add_f32 v232, v232, v233\n\t" \
    "v_add_f32 v234, v234, v235\n\t" "v_add_f32 v236, v236, v237\n\t" "v_add_f32 v238, v238, v239\n\t" "v_cvt_pk_bf16_f32 v232, v232, v236\n\t" \
    "v_cvt_pk_bf16_f32 v233, v234, v238\n\t" "ds_write2_b32 %[vw], v232, v233 offset1:68\n\t" "s_waitcnt lgkmcnt(0)\n\t" "s_barrier\n\t" XT(92, 93) \
    "ds_read_b128 v[240:243], %[vr] offset:0\n\t" "ds_read_b128 v[244:247], %[vr] offset:64\n\t" "ds_read_b128 v[248:251], %[vr] offset:128\n\t" \
    "ds_read_b128 v[252:255], %[vr] offset:192\n\t" "s_mov_b32 s61, %[ub]\n\t" "s_waitcnt lgkmcnt(3)\n\t" \
    "v_smfmac_f32_16x16x64_bf16 v[192:195], v[240:243], v[32:39], %[ix]\n\t" "v_smfmac_f32_16x16x64_bf16 v[196:199], v[240:243], v[48:55], %[ix]\n\t" \
    "s_waitcnt lgkmcnt(2)\n\t" "v_smfmac_f32_16x16x64_bf16 v[192:195], v[244:247], v[40:47], %[ix]\n\t" \
    "v_smfmac_f32_16x16x64_bf16 v[196:199], v[244:247], v[56:63], %[ix]\n\t" "s_waitcnt lgkmcnt(1)\n\t" \
    "v_smfmac_f32_16x16x64_bf16 v[192:195], v[248:251], a[192:199], %[ix]\n\t" "v_smfmac_f32_16x16x64_bf16 v[196:199], v[248:251], a[200:207], %[ix]\n\t" \
    "s_waitcnt lgkmcnt(0)\n\t" "v_smfmac_f32_16x16x64_bf16 v[192:195], v[252:255], v[160:167], %[ix]\n\t" "ds_read_b128 v[160:163], %[l0] offset:24576\n\t" \
    "ds_read_b128 v[164:167], %[l0] offset:25600\n\t" "v_smfmac_f32_16x16x64_bf16 v[196:199], v[252:255], v[168:175], %[ix]\n\t" \
    "ds_read_b128 v[168:171], %[l0] offset:26624\n\t" "ds_read_b128 v[172:175], %[l0] offset:27648\n\t" \
    "v_smfmac_f32_16x16x64_bf16 v[200:203], v[240:243], v[64:71], %[ix]\n\t" "s_nop 1\n\t" "v_smfmac_f32_16x16x64_bf16 v[204:207], v[240:243], v[80:87], %[ix]\n\t" \
    "v_cvt_pk_bf16_f32 v224, v192, v194\n\t" "v_cvt_pk_bf16_f32 v225, v193, v195\n\t" "v_smfmac_f32_16x16x64_bf16 v[200:203], v[244:247], v[72:79], %[ix]\n\t" \
    "v_cvt_pk_bf16_f32 v226, v196, v198\n\t" "v_cvt_pk_bf16_f32 v227, v197, v199\n\t" "v_smfmac_f32_16x16x64_bf16 v[204:207], v[244:247], v[88:95], %[ix]\n\t" "s_nop 0\n\t" \
    "v_permlane32_swap_b32 v224, v226\n\t" "v_smfmac_f32_16x16x64_bf16 v[200:203], v[248:251], a[208:215], %[ix]\n\t" "v_permlane32_swap_b32 v225, v227\n\t" "s_nop 0\n\t" \
    "v_smfmac_f32_16x16x64_bf16 v[204:207], v[248:251], a[216:223], %[ix]\n\t" "v_permlane16_swap_b32 v224, v225\n\t" "v_permlane16_swap_b32 v226, v227\n\t" \
    "v_smfmac_f32_16x16x64_bf16 v[200:203], v[252:255], v[176:183], %[ix]\n\t" "buffer_store_dwordx4 v[224:227], %[uo], %[rs], s61 offen sc1\n\t" \
    "s_add_u32 s61, s61, 0x10000\n\t" XT(94, 95) "v_smfmac_f32_16x16x64_bf16 v[204:207], v[252:255], v[184:191], %[ix]\n\t" "ds_read_b128 v[176:179], %[l0] offset:28672\n\t" \
    "ds_read_b128 v[180:183], %[l0] offset:29696\n\t" "v_smfmac_f32_16x16x64_bf16 v[208:211], v[240:243], v[96:103], %[ix]\n\t" \
    "ds_read_b128 v[184:187], %[l0] offset:30720\n\t" "ds_read_b128 v[188:191], %[l0] offset:31744\n\t" \
    "v_smfmac_f32_16x16x64_bf16 v[212:215], v[240:243], v[112:119], %[ix]\n\t" "v_cvt_pk_bf16_f32 v228, v200, v202\n\t" "v_cvt_pk_bf16_f32 v229, v201, v203\n\t" \
    "v_smfmac_f32_16x16x64_bf16 v[208:211], v[244:247], v[104:111], %[ix]\n\t" "v_cvt_pk_bf16_f32 v230, v204, v206\n\t" "v_cvt_pk_bf16_f32 v231, v205, v207\n\t" \
    "v_smfmac_f32_16x16x64_bf16 v[212:215], v[244:247], v[120:127], %[ix]\n\t" "s_nop 0\n\t" "v_permlane32_swap_b32 v228, v230\n\t" \
    "v_smfmac_f32_16x16x64_bf16 v[208:211], v[248:251], a[224:231], %[ix]\n\t" "v_permlane32_swap_b32 v229, v231\n\t" "s_nop 0\n\t" \
    "v_smfmac_f32_16x16x64_bf16 v[212:215], v[248:251], a[232:239], %[ix]\n\t" "v_permlane16_swap_b32 v228, v229\n\t" "v_permlane16_swap_b32 v230, v231\n\t" \
    "s_waitcnt lgkmcnt(6)\n\t" "v_smfmac_f32_16x16x64_bf16 v[208:211], v[252:255], v[160:167], %[ix]\n\t" "buffer_store_dwordx4 v[228:231], %[uo], %[rs], s61 offen sc1\n\t" \
    "s_add_u32 s61, s61, 0x10000\n\t" "s_waitcnt lgkmcnt(4)\n\t" "v_smfmac_f32_16x16x64_bf16 v[212:215], v[252:255], v[168:175], %[ix]\n\t" "s_nop 1\n\t" \
    "v_smfmac_f32_16x16x64_bf16 v[216:219], v[240:243], v[128:135], %[ix]\n\t" "s_nop 1\n\t" "v_smfmac_f32_16x16x64_bf16 v[220:223], v[240:243], v[144:151], %[ix]\n\t" \
    "v_cvt_pk_bf16_f32 v224, v208, v210\n\t" "v_cvt_pk_bf16_f32 v225, v209, v211\n\t" "v_smfmac_f32_16x16x64_bf16 v[216:219], v[244:247], v[136:143], %[ix]\n\t" \
    "v_cvt_pk_bf16_f32 v226, v212, v214\n\t" "v_cvt_pk_bf16_f32 v227, v213, v215\n\t" "v_smfmac_f32_16x16x64_bf16 v[220:223], v[244:247], v[152:159], %[ix]\n\t" "s_nop 0\n\t" \
    "v_permlane32_swap_b32 v224, v226\n\t" "v_smfmac_f32_16x16x64_bf16 v[216:219], v[248:251], a[240:247], %[ix]\n\t" "v_permlane32_swap_b32 v225, v227\n\t" "s_nop 0\n\t" \
    "v_smfmac_f32_16x16x64_bf16 v[220:223], v[248:251], a[248:255], %[ix]\n\t" "v_permlane16_swap_b32 v224, v225\n\t" "v_permlane16_swap_b32 v226, v227\n\t" \
    "s_waitcnt lgkmcnt(2)\n\t" "v_smfmac_f32_16x16x64_bf16 v[216:219], v[252:255], v[176:183], %[ix]\n\t" "buffer_store_dwordx4 v[224:227], %[uo], %[rs], s61 offen sc1\n\t" \
    "s_add_u32 s61, s61, 0x10000\n\t" "s_waitcnt lgkmcnt(0)\n\t" "v_smfmac_f32_16x16x64_bf16 v[220:223], v[252:255], v[184:191], %[ix]\n\t" "s_nop 5\n\t" \
    "v_cvt_pk_bf16_f32 v228, v216, v218\n\t" "v_cvt_pk_bf16_f32 v229, v217, v219\n\t" "v_cvt_pk_bf16_f32 v230, v220, v222\n\t" "v_cvt_pk_bf16_f32 v231, v221, v223\n\t" \
    "s_nop 0\n\t" "v_permlane32_swap_b32 v228, v230\n\t" "v_permlane32_swap_b32 v229, v231\n\t" "s_nop 1\n\t" "v_permlane16_swap_b32 v228, v229\n\t" \
    "v_permlane16_swap_b32 v230, v231\n\t" "buffer_store_dwordx4 v[228:231], %[uo], %[rs], s61 offen sc1\n\t" "s_nop 1\n\t" XT(96, 97) "s_branch 99f\n\t" "31:\n\t" \
    "s_cmp_eq_u32 %[bd], 0\n\t" "s_cbranch_scc1 36f\n\t" "s_sub_u32 %[bd], %[bd], 1\n\t" XCNT(100) "s_waitcnt lgkmcnt(0)\n\t" "s_nop 7\n\t" "s_branch 30b\n\t" "36:\n\t" \
    "s_cmp_lg_u32 s60, 0\n\t" "s_branch 32b\n\t" "34:\n\t" "s_cmp_eq_u32 %[bd], 0\n\t" "s_cbranch_scc1 35b\n\t" "s_sub_u32 %[bd], %[bd], 1\n\t" XCNT(101) \
    "s_mov_b32 s60, 1\n\t" "s_waitcnt lgkmcnt(0)\n\t" "s_nop 7\n\t" "s_branch 30b\n\t" "93:\n\t" XT(80, 81) "s_barrier\n\t" XT(82, 83) "40:\n\t" \
    "buffer_load_dwordx4 v[240:243], %[mo], %[rs], %[sb] offen offset:0 sc1\n\t" "v_mov_b64 v[232:233], 0\n\t" "v_mov_b64 v[234:235], 0\n\t" "v_mov_b64 v[236:237], 0\n\t" \
    "v_mov_b64 v[238:239], 0\n\t" "v_mov_b64 v[192:193], 0\n\t" "v_mov_b64 v[194:195], 0\n\t" "v_mov_b64 v[196:197], 0\n\t" "v_mov_b64 v[198:199], 0\n\t" \
    "buffer_load_dwordx4 v[244:247], v224, %[rs], %[sb] offen offset:0 sc1\n\t" "ds_read_b128 v[160:163], %[l0] offset:0\n\t" "ds_read_b128 v[164:167], %[l0] offset:1024\n\t" \
    "ds_read_b128 v[168:171], %[l0] offset:2048\n\t" "ds_read_b128 v[172:175], %[l0] offset:3072\n\t" "s_nop 3\n\t" \
    "buffer_load_dwordx4 v[248:251], %[mo], %[rs], %[sb] offen offset:2048 sc1\n\t" "ds_read_b128 v[176:179], %[l0] offset:4096\n\t" \
    "ds_read_b128 v[180:183], %[l0] offset:5120\n\t" "ds_read_b128 v[184:187], %[l0] offset:6144\n\t" "ds_read_b128 v[188:191], %[l0] offset:7168\n\t" "s_nop 3\n\t" \
    "buffer_load_dwordx4 v[252:255], v224, %[rs], %[sb] offen offset:2048 sc1\n\t" "v_mov_b64 v[200:201], 0\n\t" "v_mov_b64 v[202:203], 0\n\t" "v_mov_b64 v[204:205], 0\n\t" \
    "v_mov_b64 v[206:207], 0\n\t" "v_mov_b64 v[208:209], 0\n\t" "v_mov_b64 v[210:211], 0\n\t" "v_mov_b64 v[212:213], 0\n\t" "v_mov_b64 v[214:215], 0\n\t" \
    "v_mov_b64 v[216:217], 0\n\t" "v_mov_b64 v[218:219], 0\n\t" "v_mov_b64 v[220:221], 0\n\t" "v_mov_b64 v[222:223], 0\n\t" "s_waitcnt vmcnt(3)\n\t" XT(84, 85) \
    "v_smfmac_f32_16x16x64_bf16 v[232:235], v[240:243], a[0:7], %[ix]\n\t" "ds_write_b128 %[l1], v[240:243] offset:12288\n\t" \
    "v_smfmac_f32_16x16x64_bf16 v[236:239], v[240:243], a[8:15], %[ix]\n\t" "v_max3_u32 v225, v240, v241, v242\n\t" "s_waitcnt vmcnt(2)\n\t" \
    "v_smfmac_f32_16x16x64_bf16 v[232:235], v[244:247], a[16:23], %[ix]\n\t" "ds_write_b128 %[l1], v[244:247] offset:13312\n\t" "v_max3_u32 v225, v225, v243, v244\n\t" \
    "s_waitcnt lgkmcnt(8)\n\t" "v_smfmac_f32_16x16x64_bf16 v[236:239], v[244:247], v[160:167], %[ix]\n\t" "v_max3_u32 v225, v225, v245, v246\n\t" \
    "v_max_u32 v225, v225, v247\n\t" "v_cmp_eq_u32 vcc, -1, v225\n\t" "s_cmp_lg_u32 s60, 0\n\t" "s_cbranch_vccnz 41f\n\t" "42:\n\t" "s_waitcnt lgkmcnt(0)\n\t" \
    "s_cbranch_scc1 43f\n\t" "s_barrier\n\t" "43:\n\t" XT(86, 87) "ds_read_b128 v[16:19], %[l1] offset:0\n\t" "ds_read_b128 v[20:23], %[l1] offset:4096\n\t" \
    "ds_read_b128 v[24:27], %[l1] offset:8192\n\t" "s_waitcnt vmcnt(1)\n\t" "v_smfmac_f32_16x16x64_bf16 v[232:235], v[248:251], a[24:31], %[ix]\n\t" \
    "ds_write_b128 %[l1], v[248:251] offset:14336\n\t" "v_max3_u32 v225, v248, v249, v250\n\t" "v_smfmac_f32_16x16x64_bf16 v[236:239], v[248:251], a[32:39], %[ix]\n\t" \
    "ds_read_b128 v[28:31], %[l1] offset:1024\n\t" "ds_read_b128 v[240:243], %[l1] offset:5120\n\t" "ds_read_b128 v[244:247], %[l1] offset:9216\n\t" "s_waitcnt vmcnt(0)\n\t" \
    "v_smfmac_f32_16x16x64_bf16 v[232:235], v[252:255], a[40:47], %[ix]\n\t" "ds_write_b128 %[l1], v[252:255] offset:15360\n\t" "v_max3_u32 v225, v225, v251, v252\n\t" \
    "v_smfmac_f32_16x16x64_bf16 v[236:239], v[252:255], v[168:175], %[ix]\n\t" "v_max3_u32 v225, v225, v253, v254\n\t" "v_max_u32 v225, v225, v255\n\t" \
    "s_waitcnt lgkmcnt(7)\n\t" "v_smfmac_f32_16x16x64_bf16 v[232:235], v[16:19], a[48:55], %[ix]\n\t" "v_cmp_eq_u32 vcc, -1, v225\n\t" "s_cbranch_vccnz 44f\n\t" "45:\n\t" \
    "v_smfmac_f32_16x16x64_bf16 v[236:239], v[16:19], a[56:63], %[ix]\n\t" "ds_read_b128 v[160:163], %[l0] offset:8192\n\t" "ds_read_b128 v[164:167], %[l0] offset:9216\n\t" \
    "s_waitcnt lgkmcnt(8)\n\t" "v_smfmac_f32_16x16x64_bf16 v[232:235], v[20:23], a[64:71], %[ix]\n\t" "ds_read_b128 v[168:171], %[l0] offset:10240\n\t" \
    "ds_read_b128 v[172:175], %[l0] offset:11264\n\t" "v_smfmac_f32_16x16x64_bf16 v[236:239], v[20:23], v[176:183], %[ix]\n\t" "s_waitcnt lgkmcnt(4)\n\t" "s_barrier\n\t" \
    XT(88, 89) "v_smfmac_f32_16x16x64_bf16 v[232:235], v[24:27], a[72:79], %[ix]\n\t" "ds_read_b128 v[176:179], %[l0] offset:12288\n\t" \
    "ds_read_b128 v[180:183], %[l0] offset:13312\n\t" "v_smfmac_f32_16x16x64_bf16 v[236:239], v[24:27], a[80:87], %[ix]\n\t" "ds_read_b128 v[248:251], %[l1] offset:2048\n\t" \
    "ds_read_b128 v[252:255], %[l1] offset:6144\n\t" "v_smfmac_f32_16x16x64_bf16 v[232:235], v[28:31], a[88:95], %[ix]\n\t" "ds_read_b128 v[16:19], %[l1] offset:10240\n\t" \
    "ds_read_b128 v[20:23], %[l1] offset:3072\n\t" "v_smfmac_f32_16x16x64_bf16 v[236:239], v[28:31], v[184:191], %[ix]\n\t" "ds_read_b128 v[24:27], %[l1] offset:7168\n\t" \
    "ds_read_b128 v[184:187], %[l0] offset:14336\n\t" "v_smfmac_f32_16x16x64_bf16 v[232:235], v[240:243], a[96:103], %[ix]\n\t" \
    "ds_read_b128 v[188:191], %[l0] offset:15360\n\t" "ds_read_b128 v[28:31], %[l1] offset:11264\n\t" \
    "v_smfmac_f32_16x16x64_bf16 v[236:239], v[240:243], a[104:111], %[ix]\n\t" "v_smfmac_f32_16x16x64_bf16 v[232:235], v[244:247], a[112:119], %[ix]\n\t" \
    "s_waitcnt lgkmcnt(12)\n\t" "v_smfmac_f32_16x16x64_bf16 v[236:239], v[244:247], v[160:167], %[ix]\n\t" "ds_read_b128 v[160:163], %[l0] offset:16384\n\t" \
    "ds_read_b128 v[164:167], %[l0] offset:17408\n\t" "s_waitcnt lgkmcnt(9)\n\t" "v_smfmac_f32_16x16x64_bf16 v[232:235], v[248:251], a[120:127], %[ix]\n\t" \
    "v_smfmac_f32_16x16x64_bf16 v[236:239], v[248:251], a[128:135], %[ix]\n\t" "s_waitcnt lgkmcnt(8)\n\t" \
    "v_smfmac_f32_16x16x64_bf16 v[232:235], v[252:255], a[136:143], %[ix]\n\t" "v_smfmac_f32_16x16x64_bf16 v[236:239], v[252:255], v[168:175], %[ix]\n\t" \
    "ds_read_b128 v[168:171], %[l0] offset:18432\n\t" "ds_read_b128 v[172:175], %[l0] offset:19456\n\t" "s_waitcnt lgkmcnt(9)\n\t" \
    "v_smfmac_f32_16x16x64_bf16 v[232:235], v[16:19], a[144:151], %[ix]\n\t" "v_smfmac_f32_16x16x64_bf16 v[236:239], v[16:19], a[152:159], %[ix]\n\t" \
    "s_waitcnt lgkmcnt(8)\n\t" "v_smfmac_f32_16x16x64_bf16 v[232:235], v[20:23], a[160:167], %[ix]\n\t" \
    "v_smfmac_f32_16x16x64_bf16 v[236:239], v[20:23], v[176:183], %[ix]\n\t" "ds_read_b128 v[176:179], %[l0] offset:20480\n\t" \
    "ds_read_b128 v[180:183], %[l0] offset:21504\n\t" "s_waitcnt lgkmcnt(9)\n\t" "v_smfmac_f32_16x16x64_bf16 v[232:235], v[24:27], a[168:175], %[ix]\n\t" \
    "v_smfmac_f32_16x16x64_bf16 v[236:239], v[24:27], a[176:183], %[ix]\n\t" "s_waitcnt lgkmcnt(6)\n\t" \
    "v_smfmac_f32_16x16x64_bf16 v[232:235], v[28:31], a[184:191], %[ix]\n\t" "v_smfmac_f32_16x16x64_bf16 v[236:239], v[28:31], v[184:191], %[ix]\n\t" \
    "ds_read_b128 v[184:187], %[l0] offset:22528\n\t" "ds_read_b128 v[188:191], %[l0] offset:23552\n\t" XT(90, 91) "s_nop 4\n\t" "v_add_f32 v232, v232, v233\n\t" \
    "v_add_f32 v234, v234, v235\n\t" "v_add_f32 v236, v236, v237\n\t" "v_add_f32 v238, v238, v239\n\t" "v_cvt_pk_bf16_f32 v232, v232, v236\n\t" \
    "v_cvt_pk_bf16_f32 v233, v234, v238\n\t" "ds_write2_b32 %[vw], v232, v233 offset1:68\n\t" "s_waitcnt lgkmcnt(0)\n\t" "s_barrier\n\t" XT(92, 93) \
    "ds_read_b128 v[240:243], %[vr] offset:0\n\t" "ds_read_b128 v[244:247], %[vr] offset:64\n\t" "ds_read_b128 v[248:251], %[vr] offset:128\n\t" \
    "ds_read_b128 v[252:255], %[vr] offset:192\n\t" "s_mov_b32 s61, %[ub]\n\t" "s_waitcnt lgkmcnt(3)\n\t" \
    "v_smfmac_f32_16x16x64_bf16 v[192:195], v[240:243], v[32:39], %[ix]\n\t" "v_smfmac_f32_16x16x64_bf16 v[196:199], v[240:243], v[48:55], %[ix]\n\t" \
    "s_waitcnt lgkmcnt(2)\n\t" "v_smfmac_f32_16x16x64_bf16 v[192:195], v[244:247], v[40:47], %[ix]\n\t" \
    "v_smfmac_f32_16x16x64_bf16 v[196:199], v[244:247], v[56:63], %[ix]\n\t" "s_waitcnt lgkmcnt(1)\n\t" \
    "v_smfmac_f32_16x16x64_bf16 v[192:195], v[248:251], a[192:199], %[ix]\n\t" "v_smfmac_f32_16x16x64_bf16 v[196:199], v[248:251], a[200:207], %[ix]\n\t" \
    "s_waitcnt lgkmcnt(0)\n\t" "v_smfmac_f32_16x16x64_bf16 v[192:195], v[252:255], v[160:167], %[ix]\n\t" "ds_read_b128 v[160:163], %[l0] offset:24576\n\t" \
    "ds_read_b128 v[164:167], %[l0] offset:25600\n\t" "v_smfmac_f32_16x16x64_bf16 v[196:199], v[252:255], v[168:175], %[ix]\n\t" \
    "ds_read_b128 v[168:171], %[l0] offset:26624\n\t" "ds_read_b128 v[172:175], %[l0] offset:27648\n\t" \
    "v_smfmac_f32_16x16x64_bf16 v[200:203], v[240:243], v[64:71], %[ix]\n\t" "s_nop 1\n\t" "v_smfmac_f32_16x16x64_bf16 v[204:207], v[240:243], v[80:87], %[ix]\n\t" \
    "v_cvt_pk_bf16_f32 v224, v192, v194\n\t" "v_cvt_pk_bf16_f32 v225, v193, v195\n\t" "v_smfmac_f32_16x16x64_bf16 v[200:203], v[244:247], v[72:79], %[ix]\n\t" \
    "v_cvt_pk_bf16_f32 v226, v196, v198\n\t" "v_cvt_pk_bf16_f32 v227, v197, v199\n\t" "v_smfmac_f32_16x16x64_bf16 v[204:207], v[244:247], v[88:95], %[ix]\n\t" "s_nop 0\n\t" \
    "v_permlane32_swap_b32 v224, v226\n\t" "v_smfmac_f32_16x16x64_bf16 v[200:203], v[248:251], a[208:215], %[ix]\n\t" "v_permlane32_swap_b32 v225, v227\n\t" "s_nop 0\n\t" \
    "v_smfmac_f32_16x16x64_bf16 v[204:207], v[248:251], a[216:223], %[ix]\n\t" "v_permlane16_swap_b32 v224, v225\n\t" "v_permlane16_swap_b32 v226, v227\n\t" \
    "v_smfmac_f32_16x16x64_bf16 v[200:203], v[252:255], v[176:183], %[ix]\n\t" "buffer_store_dwordx4 v[224:227], %[uo], %[rs], s61 offen sc1\n\t" \
    "s_add_u32 s61, s61, 0x10000\n\t" XT(94, 95) "v_smfmac_f32_16x16x64_bf16 v[204:207], v[252:255], v[184:191], %[ix]\n\t" "ds_read_b128 v[176:179], %[l0] offset:28672\n\t" \
    "ds_read_b128 v[180:183], %[l0] offset:29696\n\t" "v_smfmac_f32_16x16x64_bf16 v[208:211], v[240:243], v[96:103], %[ix]\n\t" \
    "ds_read_b128 v[184:187], %[l0] offset:30720\n\t" "ds_read_b128 v[188:191], %[l0] offset:31744\n\t" \
    "v_smfmac_f32_16x16x64_bf16 v[212:215], v[240:243], v[112:119], %[ix]\n\t" "v_cvt_pk_bf16_f32 v228, v200, v202\n\t" "v_cvt_pk_bf16_f32 v229, v201, v203\n\t" \
    "v_smfmac_f32_16x16x64_bf16 v[208:211], v[244:247], v[104:111], %[ix]\n\t" "v_cvt_pk_bf16_f32 v230, v204, v206\n\t" "v_cvt_pk_bf16_f32 v231, v205, v207\n\t" \
    "v_smfmac_f32_16x16x64_bf16 v[212:215], v[244:247], v[120:127], %[ix]\n\t" "s_nop 0\n\t" "v_permlane32_swap_b32 v228, v230\n\t" \
    "v_smfmac_f32_16x16x64_bf16 v[208:211], v[248:251], a[224:231], %[ix]\n\t" "v_permlane32_swap_b32 v229, v231\n\t" "s_nop 0\n\t" \
    "v_smfmac_f32_16x16x64_bf16 v[212:215], v[248:251], a[232:239], %[ix]\n\t" "v_permlane16_swap_b32 v228, v229\n\t" "v_permlane16_swap_b32 v230, v231\n\t" \
    "s_waitcnt lgkmcnt(6)\n\t" "v_smfmac_f32_16x16x64_bf16 v[208:211], v[252:255], v[160:167], %[ix]\n\t" "buffer_store_dwordx4 v[228:231], %[uo], %[rs], s61 offen sc1\n\t" \
    "s_add_u32 s61, s61, 0x10000\n\t" "s_waitcnt lgkmcnt(4)\n\t" "v_smfmac_f32_16x16x64_bf16 v[212:215], v[252:255], v[168:175], %[ix]\n\t" "s_nop 1\n\t" \
    "v_smfmac_f32_16x16x64_bf16 v[216:219], v[240:243], v[128:135], %[ix]\n\t" "s_nop 1\n\t" "v_smfmac_f32_16x16x64_bf16 v[220:223], v[240:243], v[144:151], %[ix]\n\t" \
    "v_cvt_pk_bf16_f32 v224, v208, v210\n\t" "v_cvt_pk_bf16_f32 v225, v209, v211\n\t" "v_smfmac_f32_16x16x64_bf16 v[216:219], v[244:247], v[136:143], %[ix]\n\t" \
    "v_cvt_pk_bf16_f32 v226, v212, v214\n\t" "v_cvt_pk_bf16_f32 v227, v213, v215\n\t" "v_smfmac_f32_16x16x64_bf16 v[220:223], v[244:247], v[152:159], %[ix]\n\t" "s_nop 0\n\t" \
    "v_permlane32_swap_b32 v224, v226\n\t" "v_smfmac_f32_16x16x64_bf16 v[216:219], v[248:251], a[240:247], %[ix]\n\t" "v_permlane32_swap_b32 v225, v227\n\t" "s_nop 0\n\t" \
    "v_smfmac_f32_16x16x64_bf16 v[220:223], v[248:251], a[248:255], %[ix]\n\t" "v_permlane16_swap_b32 v224, v225\n\t" "v_permlane16_swap_b32 v226, v227\n\t" \
    "s_waitcnt lgkmcnt(2)\n\t" "v_smfmac_f32_16x16x64_bf16 v[216:219], v[252:255], v[176:183], %[ix]\n\t" "buffer_store_dwordx4 v[224:227], %[uo], %[rs], s61 offen sc1\n\t" \
    "s_add_u32 s61, s61, 0x10000\n\t" "s_waitcnt lgkmcnt(0)\n\t" "v_smfmac_f32_16x16x64_bf16 v[220:223], v[252:255], v[184:191], %[ix]\n\t" "s_nop 5\n\t" \
    "v_cvt_pk_bf16_f32 v228, v216, v218\n\t" "v_cvt_pk_bf16_f32 v229, v217, v219\n\t" "v_cvt_pk_bf16_f32 v230, v220, v222\n\t" "v_cvt_pk_bf16_f32 v231, v221, v223\n\t" \
    "s_nop 0\n\t" "v_permlane32_swap_b32 v228, v230\n\t" "v_permlane32_swap_b32 v229, v231\n\t" "s_nop 1\n\t" "v_permlane16_swap_b32 v228, v229\n\t" \
    "v_permlane16_swap_b32 v230, v231\n\t" "buffer_store_dwordx4 v[228:231], %[uo], %[rs], s61 offen sc1\n\t" "s_nop 1\n\t" XT(96, 97) "s_branch 99f\n\t" "41:\n\t" \
    "s_cmp_eq_u32 %[bd], 0\n\t" "s_cbranch_scc1 46f\n\t" "s_sub_u32 %[bd], %[bd], 1\n\t" XCNT(100) "s_waitcnt lgkmcnt(0)\n\t" "s_nop 7\n\t" "s_branch 40b\n\t" "46:\n\t" \
    "s_cmp_lg_u32 s60, 0\n\t" "s_branch 42b\n\t" "44:\n\t" "s_cmp_eq_u32 %[bd], 0\n\t" "s_cbranch_scc1 45b\n\t" "s_sub_u32 %[bd], %[bd], 1\n\t" XCNT(101) \
    "s_mov_b32 s60, 1\n\t" "s_waitcnt lgkmcnt(0)\n\t" "s_nop 7\n\t" "s_branch 40b\n\t" "99:\n\t"
#define Y_STEP \
    "s_getpc_b64 s[62:63]\n\t" "11:\n\t" "s_memtime s[64:65]\n\t" "s_waitcnt lgkmcnt(0)\n\t" "s_sub_u32 s61, %[tu], s64\n\t" "s_cmp_gt_i32 s61, 124\n\t" \
    "s_cbranch_scc1 11b\n\t" "s_max_i32 s61, s61, 0\n\t" "s_and_b32 s61, s61, 124\n\t" "s_sub_u32 s61, 12f-11b, s61\n\t" "s_add_u32 s62, s62, s61\n\t" \
    "s_addc_u32 s63, s63, 0\n\t" "s_setpc_b64 s[62:63]\n\t" ".rept 31\n\t" "s_nop 0\n\t" ".endr\n\t" "12:\n\t" XT(90, 91) \
    "buffer_load_dwordx4 v[144:147], %[u0], %[rs], %[ub] offen offset:0 sc1\n\t" "buffer_load_dwordx4 v[148:151], %[u0], %[rs], %[ub] offen offset:1024 sc1\n\t" \
    "buffer_load_dwordx4 v[152:155], %[u0], %[rs], %[ub] offen offset:2048 sc1\n\t" "buffer_load_dwordx4 v[156:159], %[u0], %[rs], %[ub] offen offset:3072 sc1\n\t" \
    "buffer_load_dwordx4 v[160:163], %[u1], %[rs], %[ub] offen offset:0 sc1\n\t" "buffer_load_dwordx4 v[164:167], %[u1], %[rs], %[ub] offen offset:1024 sc1\n\t" \
    "buffer_load_dwordx4 v[168:171], %[u1], %[rs], %[ub] offen offset:2048 sc1\n\t" "buffer_load_dwordx4 v[172:175], %[u1], %[rs], %[ub] offen offset:3072 sc1\n\t" \
    "s_cmp_lg_u32 %[late], 0\n\t" "s_cbranch_scc1 14f\n\t" "s_getpc_b64 s[62:63]\n\t" "13:\n\t" "s_memtime s[64:65]\n\t" "s_waitcnt lgkmcnt(0)\n\t" \
    "s_sub_u32 s61, %[tg], s64\n\t" "s_cmp_gt_i32 s61, 124\n\t" "s_cbranch_scc1 13b\n\t" "s_max_i32 s61, s61, 0\n\t" "s_and_b32 s61, s61, 124\n\t" \
    "s_sub_u32 s61, 14f-13b, s61\n\t" "s_add_u32 s62, s62, s61\n\t" "s_addc_u32 s63, s63, 0\n\t" "s_setpc_b64 s[62:63]\n\t" ".rept 31\n\t" "s_nop 0\n\t" ".endr\n\t" "14:\n\t" \
    XT(92, 93) "20:\n\t" "buffer_load_dwordx4 v[192:195], %[x0], %[rs], %[pb] offen offset:0 sc1\n\t" \
    "buffer_load_dwordx4 v[196:199], %[x0], %[rs], %[pb] offen offset:1024 sc1\n\t" "buffer_load_dwordx4 v[200:203], %[x0], %[rs], %[pb] offen offset:2048 sc1\n\t" \
    "buffer_load_dwordx4 v[204:207], %[x0], %[rs], %[pb] offen offset:3072 sc1\n\t" "buffer_load_dwordx4 v[208:211], %[x1], %[rs], %[pb] offen offset:0 sc1\n\t" \
    "buffer_load_dwordx4 v[212:215], %[x1], %[rs], %[pb] offen offset:1024 sc1\n\t" "buffer_load_dwordx4 v[216:219], %[x1], %[rs], %[pb] offen offset:2048 sc1\n\t" \
    "buffer_load_dwordx4 v[220:223], %[x1], %[rs], %[pb] offen offset:3072 sc1\n\t" "buffer_load_dwordx4 v[224:227], %[x2], %[rs], %[pb] offen offset:0 sc1\n\t" \
    "buffer_load_dwordx4 v[228:231], %[x2], %[rs], %[pb] offen offset:1024 sc1\n\t" "buffer_load_dwordx4 v[232:235], %[x2], %[rs], %[pb] offen offset:2048 sc1\n\t" \
    "buffer_load_dwordx4 v[236:239], %[x2], %[rs], %[pb] offen offset:3072 sc1\n\t" "buffer_load_dwordx4 v[240:243], %[x3], %[rs], %[pb] offen offset:0 sc1\n\t" \
    "buffer_load_dwordx4 v[244:247], %[x3], %[rs], %[pb] offen offset:1024 sc1\n\t" "buffer_load_dwordx4 v[248:251], %[x3], %[rs], %[pb] offen offset:2048 sc1\n\t" \
    "buffer_load_dwordx4 v[252:255], %[x3], %[rs], %[pb] offen offset:3072 sc1\n\t" "v_mov_b64 v[120:121], 0\n\t" "v_mov_b64 v[122:123], 0\n\t" \
    "buffer_load_dwordx2 v[176:177], %[pm], %[rs], %[pb] offen offset:0 sc1\n\t" "buffer_load_dwordx2 v[178:179], %[pm], %[rs], %[pb] offen offset:128 sc1\n\t" \
    "buffer_load_dwordx2 v[180:181], %[pm], %[rs], %[pb] offen offset:1024 sc1\n\t" "buffer_load_dwordx2 v[182:183], %[pm], %[rs], %[pb] offen offset:1152 sc1\n\t" \
    "buffer_load_dwordx2 v[184:185], %[pm], %[rs], %[pb] offen offset:2048 sc1\n\t" "buffer_load_dwordx2 v[186:187], %[pm], %[rs], %[pb] offen offset:2176 sc1\n\t" \
    "buffer_load_dwordx2 v[188:189], %[pm], %[rs], %[pb] offen offset:3072 sc1\n\t" "buffer_load_dwordx2 v[190:191], %[pm], %[rs], %[pb] offen offset:3200 sc1\n\t" \
    "buffer_load_dword %[sv], %[so], %[rs], %[pb] offen sc1\n\t" "v_mov_b32 v127, -1\n\t" "s_waitcnt vmcnt(24)\n\t" XT(94, 95) \
    "v_smfmac_f32_16x16x64_bf16 v[120:123], v[192:195], a[0:7], %[ix]\n\t" "s_waitcnt vmcnt(23)\n\t" "v_smfmac_f32_16x16x64_bf16 v[120:123], v[196:199], a[8:15], %[ix]\n\t" \
    "s_waitcnt vmcnt(22)\n\t" "v_smfmac_f32_16x16x64_bf16 v[120:123], v[200:203], a[16:23], %[ix]\n\t" "s_waitcnt vmcnt(21)\n\t" \
    "v_smfmac_f32_16x16x64_bf16 v[120:123], v[204:207], a[24:31], %[ix]\n\t" "s_waitcnt vmcnt(20)\n\t" \
    "v_smfmac_f32_16x16x64_bf16 v[120:123], v[208:211], a[32:39], %[ix]\n\t" "s_waitcnt vmcnt(19)\n\t" \
    "v_smfmac_f32_16x16x64_bf16 v[120:123], v[212:215], a[40:47], %[ix]\n\t" "s_waitcnt vmcnt(18)\n\t" \
    "v_smfmac_f32_16x16x64_bf16 v[120:123], v[216:219], a[48:55], %[ix]\n\t" "s_waitcnt vmcnt(17)\n\t" \
    "v_smfmac_f32_16x16x64_bf16 v[120:123], v[220:223], a[56:63], %[ix]\n\t" "s_waitcnt vmcnt(16)\n\t" \
    "v_smfmac_f32_16x16x64_bf16 v[120:123], v[224:227], a[64:71], %[ix]\n\t" "s_waitcnt vmcnt(15)\n\t" \
    "v_smfmac_f32_16x16x64_bf16 v[120:123], v[228:231], a[72:79], %[ix]\n\t" "s_waitcnt vmcnt(14)\n\t" \
    "v_smfmac_f32_16x16x64_bf16 v[120:123], v[232:235], a[80:87], %[ix]\n\t" "s_waitcnt vmcnt(13)\n\t" \
    "v_smfmac_f32_16x16x64_bf16 v[120:123], v[236:239], a[88:95], %[ix]\n\t" "s_waitcnt vmcnt(12)\n\t" \
    "v_smfmac_f32_16x16x64_bf16 v[120:123], v[240:243], a[96:103], %[ix]\n\t" "s_waitcnt vmcnt(11)\n\t" \
    "v_smfmac_f32_16x16x64_bf16 v[120:123], v[244:247], a[104:111], %[ix]\n\t" "s_waitcnt vmcnt(10)\n\t" \
    "v_smfmac_f32_16x16x64_bf16 v[120:123], v[248:251], a[112:119], %[ix]\n\t" "s_waitcnt vmcnt(9)\n\t" \
    "v_smfmac_f32_16x16x64_bf16 v[120:123], v[252:255], a[120:127], %[ix]\n\t" "s_waitcnt vmcnt(7)\n\t" \
    "v_smfmac_f32_16x16x64_bf16 v[120:123], v[176:179], v[144:151], %[ix]\n\t" "s_waitcnt vmcnt(5)\n\t" \
    "v_smfmac_f32_16x16x64_bf16 v[120:123], v[180:183], v[152:159], %[ix]\n\t" "s_waitcnt vmcnt(3)\n\t" \
    "v_smfmac_f32_16x16x64_bf16 v[120:123], v[184:187], v[160:167], %[ix]\n\t" "s_waitcnt vmcnt(1)\n\t" \
    "v_smfmac_f32_16x16x64_bf16 v[120:123], v[188:191], v[168:175], %[ix]\n\t" XT(82, 83) "s_nop 7\n\t" "v_add_f32 v120, v120, v121\n\t" "v_add_f32 v122, v122, v123\n\t" \
    "v_cvt_pk_bf16_f32 v124, v120, v122\n\t" "v_cmp_u_f32 vcc, v120, v122\n\t" "s_cbranch_vccnz 30f\n\t" "21:\n\t" \
    "v_mov_b32_dpp v125, v124 quad_perm:[1,0,3,2] row_mask:0xf bank_mask:0xf\n\t" "v_perm_b32 v124, v125, v124, %[ps]\n\t" "v_min_u32 v124, -2, v124\n\t" \
    "buffer_store_dword v124, %[mw], %[rs], %[mb] offen sc1\n\t" XT(84, 85) "buffer_store_dword v127, %[mw], %[rs], %[mr] offen sc1\n\t" "s_memtime s[64:65]\n\t" \
    "s_barrier\n\t" XT(98, 99) "s_waitcnt lgkmcnt(0)\n\t" "s_and_b32 %[bad], s64, -2\n\t" "s_branch 40f\n\t" "30:\n\t" "v_max_u32 v126, v144, v145\n\t" \
    "v_max3_u32 v126, v126, v146, v147\n\t" "v_max3_u32 v126, v126, v148, v149\n\t" "v_max3_u32 v126, v126, v150, v151\n\t" "v_max3_u32 v126, v126, v152, v153\n\t" \
    "v_max3_u32 v126, v126, v154, v155\n\t" "v_max3_u32 v126, v126, v156, v157\n\t" "v_max3_u32 v126, v126, v158, v159\n\t" "v_max3_u32 v126, v126, v160, v161\n\t" \
    "v_max3_u32 v126, v126, v162, v163\n\t" "v_max3_u32 v126, v126, v164, v165\n\t" "v_max3_u32 v126, v126, v166, v167\n\t" "v_max3_u32 v126, v126, v168, v169\n\t" \
    "v_max3_u32 v126, v126, v170, v171\n\t" "v_max3_u32 v126, v126, v172, v173\n\t" "v_max3_u32 v126, v126, v174, v175\n\t" UCNT "v_max3_u32 v126, v126, v176, v177\n\t" \
    "v_max3_u32 v126, v126, v178, v179\n\t" "v_max3_u32 v126, v126, v180, v181\n\t" "v_max3_u32 v126, v126, v182, v183\n\t" "v_max3_u32 v126, v126, v184, v185\n\t" \
    "v_max3_u32 v126, v126, v186, v187\n\t" "v_max3_u32 v126, v126, v188, v189\n\t" "v_max3_u32 v126, v126, v190, v191\n\t" "v_max3_u32 v126, v126, v192, v193\n\t" \
    "v_max3_u32 v126, v126, v194, v195\n\t" "v_max3_u32 v126, v126, v196, v197\n\t" "v_max3_u32 v126, v126, v198, v199\n\t" "v_max3_u32 v126, v126, v200, v201\n\t" \
    "v_max3_u32 v126, v126, v202, v203\n\t" "v_max3_u32 v126, v126, v204, v205\n\t" "v_max3_u32 v126, v126, v206, v207\n\t" "v_max3_u32 v126, v126, v208, v209\n\t" \
    "v_max3_u32 v126, v126, v210, v211\n\t" "v_max3_u32 v126, v126, v212, v213\n\t" "v_max3_u32 v126, v126, v214, v215\n\t" "v_max3_u32 v126, v126, v216, v217\n\t" \
    "v_max3_u32 v126, v126, v218, v219\n\t" "v_max3_u32 v126, v126, v220, v221\n\t" "v_max3_u32 v126, v126, v222, v223\n\t" "v_max3_u32 v126, v126, v224, v225\n\t" \
    "v_max3_u32 v126, v126, v226, v227\n\t" "v_max3_u32 v126, v126, v228, v229\n\t" "v_max3_u32 v126, v126, v230, v231\n\t" "v_max3_u32 v126, v126, v232, v233\n\t" \
    "v_max3_u32 v126, v126, v234, v235\n\t" "v_max3_u32 v126, v126, v236, v237\n\t" "v_max3_u32 v126, v126, v238, v239\n\t" "v_max3_u32 v126, v126, v240, v241\n\t" \
    "v_max3_u32 v126, v126, v242, v243\n\t" "v_max3_u32 v126, v126, v244, v245\n\t" "v_max3_u32 v126, v126, v246, v247\n\t" "v_max3_u32 v126, v126, v248, v249\n\t" \
    "v_max3_u32 v126, v126, v250, v251\n\t" "v_max3_u32 v126, v126, v252, v253\n\t" "v_max3_u32 v126, v126, v254, v255\n\t" "v_cmp_eq_u32 vcc, -1, v126\n\t" \
    "s_cbranch_vccz 21b\n\t" "s_cmp_eq_u32 %[bd], 0\n\t" "s_cbranch_scc1 21b\n\t" XCNT(100) "s_mov_b32 %[bad], 1\n\t" "s_waitcnt vmcnt(0)\n\t" "40:\n\t"
#define G_LOOP \
    "v_xor_b32 v72, 0, %[xo]\n\t" "v_add_u32 v76, 0x10000, v72\n\t" "v_xor_b32 v88, 0, %[xs]\n\t" "v_add_u32 v88, 0, v88\n\t" "v_xor_b32 v73, 64, %[xo]\n\t" \
    "v_add_u32 v77, 0x10000, v73\n\t" "v_xor_b32 v89, 16, %[xs]\n\t" "v_add_u32 v89, 2048, v89\n\t" "v_xor_b32 v74, 128, %[xo]\n\t" "v_add_u32 v78, 0x10000, v74\n\t" \
    "v_xor_b32 v90, 32, %[xs]\n\t" "v_add_u32 v90, 4096, v90\n\t" "v_xor_b32 v75, 192, %[xo]\n\t" "v_add_u32 v79, 0x10000, v75\n\t" "v_xor_b32 v91, 48, %[xs]\n\t" \
    "v_add_u32 v91, 6144, v91\n\t" "v_xor_b32 v92, 64, %[xs]\n\t" "v_add_u32 v92, 8192, v92\n\t" "v_xor_b32 v93, 80, %[xs]\n\t" "v_add_u32 v93, 10240, v93\n\t" \
    "v_xor_b32 v94, 96, %[xs]\n\t" "v_add_u32 v94, 12288, v94\n\t" "v_xor_b32 v95, 112, %[xs]\n\t" "v_add_u32 v95, 14336, v95\n\t" "v_mov_b32 v96, 2.0\n\t" \
    "s_mov_b32 s60, 0\n\t" "s_mov_b32 s62, 0\n\t" "s_waitcnt vmcnt(0)\n\t" "s_barrier\n\t" "ds_read_b128 v[100:103], v72 offset:0\n\t" \
    "ds_read_b128 v[104:107], v72 offset:32768\n\t" "ds_read_b128 v[108:111], v73 offset:0\n\t" "ds_read_b128 v[112:115], v73 offset:32768\n\t" \
    "ds_read_b128 v[116:119], v74 offset:0\n\t" "10:\n\t" XT(80, 81) "s_waitcnt lgkmcnt(4)\n\t" "v_mfma_f32_16x16x32_bf16 v[32:35], a[0:3], v[100:103], 0\n\t" \
    "ds_read_b128 v[120:123], v74 offset:32768\n\t" "s_add_u32 s61, s60, 1\n\t" "s_and_b32 s61, s61, 63\n\t" "v_mfma_f32_16x16x32_bf16 v[36:39], a[4:7], v[100:103], 0\n\t" \
    "s_lshl_b32 s61, s61, 16\n\t" "s_add_u32 s63, %[lw], 65536\n\t" "s_waitcnt lgkmcnt(4)\n\t" "v_mfma_f32_16x16x32_bf16 v[40:43], a[0:3], v[104:107], 0\n\t" \
    "ds_read_b128 v[124:127], v75 offset:0\n\t" "v_mfma_f32_16x16x32_bf16 v[44:47], a[4:7], v[104:107], 0\n\t" "s_waitcnt lgkmcnt(4)\n\t" \
    "v_mfma_f32_16x16x32_bf16 v[32:35], a[8:11], v[108:111], v[32:35]\n\t" "ds_read_b128 v[128:131], v75 offset:32768\n\t" "v_cvt_pk_bf16_f32 v64, v48, v49\n\t" \
    "v_mfma_f32_16x16x32_bf16 v[36:39], a[12:15], v[108:111], v[36:39]\n\t" "v_cvt_pk_bf16_f32 v65, v50, v51\n\t" "v_cvt_pk_bf16_f32 v66, v52, v53\n\t" \
    "s_waitcnt lgkmcnt(4)\n\t" "v_mfma_f32_16x16x32_bf16 v[40:43], a[8:11], v[112:115], v[40:43]\n\t" "ds_read_b128 v[100:103], v72 offset:256\n\t" \
    "v_cvt_pk_bf16_f32 v67, v54, v55\n\t" "v_mfma_f32_16x16x32_bf16 v[44:47], a[12:15], v[112:115], v[44:47]\n\t" "v_cvt_pk_bf16_f32 v68, v56, v57\n\t" \
    "v_cvt_pk_bf16_f32 v69, v58, v59\n\t" "s_waitcnt lgkmcnt(4)\n\t" "v_mfma_f32_16x16x32_bf16 v[32:35], a[16:19], v[116:119], v[32:35]\n\t" \
    "ds_read_b128 v[104:107], v72 offset:33024\n\t" "v_cvt_pk_bf16_f32 v70, v60, v61\n\t" "v_mfma_f32_16x16x32_bf16 v[36:39], a[20:23], v[116:119], v[36:39]\n\t" \
    "v_cvt_pk_bf16_f32 v71, v62, v63\n\t" "buffer_store_dwordx4 v[64:67], v96, %[rs], s62 offen offset:0 sc1\n\t" "s_waitcnt lgkmcnt(4)\n\t" \
    "v_mfma_f32_16x16x32_bf16 v[40:43], a[16:19], v[120:123], v[40:43]\n\t" "ds_read_b128 v[108:111], v73 offset:256\n\t" \
    "v_mfma_f32_16x16x32_bf16 v[44:47], a[20:23], v[120:123], v[44:47]\n\t" "s_waitcnt lgkmcnt(4)\n\t" "v_mfma_f32_16x16x32_bf16 v[32:35], a[24:27], v[124:127], v[32:35]\n\t" \
    "ds_read_b128 v[112:115], v73 offset:33024\n\t" "v_mfma_f32_16x16x32_bf16 v[36:39], a[28:31], v[124:127], v[36:39]\n\t" \
    "buffer_store_dwordx4 v[68:71], v96, %[rs], s62 offen offset:256 sc1\n\t" "s_waitcnt lgkmcnt(4)\n\t" \
    "v_mfma_f32_16x16x32_bf16 v[40:43], a[24:27], v[128:131], v[40:43]\n\t" "ds_read_b128 v[116:119], v74 offset:256\n\t" "s_mov_b32 m0, s63\n\t" \
    "v_mfma_f32_16x16x32_bf16 v[44:47], a[28:31], v[128:131], v[44:47]\n\t" "s_add_u32 s63, s63, 0x800\n\t" "buffer_load_dwordx4 v88, %[rx], s61 offen lds\n\t" \
    "s_waitcnt lgkmcnt(4)\n\t" "v_mfma_f32_16x16x32_bf16 v[32:35], a[32:35], v[100:103], v[32:35]\n\t" "ds_read_b128 v[120:123], v74 offset:33024\n\t" \
    "v_mfma_f32_16x16x32_bf16 v[36:39], a[36:39], v[100:103], v[36:39]\n\t" "buffer_load_dwordx4 v88, %[rx], s61 offen offset:1024 lds\n\t" "s_waitcnt lgkmcnt(4)\n\t" \
    "v_mfma_f32_16x16x32_bf16 v[40:43], a[32:35], v[104:107], v[40:43]\n\t" "ds_read_b128 v[124:127], v75 offset:256\n\t" "s_mov_b32 m0, s63\n\t" \
    "v_mfma_f32_16x16x32_bf16 v[44:47], a[36:39], v[104:107], v[44:47]\n\t" "s_add_u32 s63, s63, 0x800\n\t" "buffer_load_dwordx4 v89, %[rx], s61 offen lds\n\t" \
    "s_waitcnt lgkmcnt(4)\n\t" "v_mfma_f32_16x16x32_bf16 v[32:35], a[40:43], v[108:111], v[32:35]\n\t" "ds_read_b128 v[128:131], v75 offset:33024\n\t" \
    "v_mfma_f32_16x16x32_bf16 v[36:39], a[44:47], v[108:111], v[36:39]\n\t" "buffer_load_dwordx4 v89, %[rx], s61 offen offset:1024 lds\n\t" "s_waitcnt lgkmcnt(4)\n\t" \
    "v_mfma_f32_16x16x32_bf16 v[40:43], a[40:43], v[112:115], v[40:43]\n\t" "ds_read_b128 v[100:103], v72 offset:512\n\t" "s_mov_b32 m0, s63\n\t" \
    "v_mfma_f32_16x16x32_bf16 v[44:47], a[44:47], v[112:115], v[44:47]\n\t" "s_add_u32 s63, s63, 0x800\n\t" "buffer_load_dwordx4 v90, %[rx], s61 offen lds\n\t" \
    "s_waitcnt lgkmcnt(4)\n\t" "v_mfma_f32_16x16x32_bf16 v[32:35], a[48:51], v[116:119], v[32:35]\n\t" "ds_read_b128 v[104:107], v72 offset:33280\n\t" \
    "v_mfma_f32_16x16x32_bf16 v[36:39], a[52:55], v[116:119], v[36:39]\n\t" "buffer_load_dwordx4 v90, %[rx], s61 offen offset:1024 lds\n\t" "s_waitcnt lgkmcnt(4)\n\t" \
    "v_mfma_f32_16x16x32_bf16 v[40:43], a[48:51], v[120:123], v[40:43]\n\t" "ds_read_b128 v[108:111], v73 offset:512\n\t" "s_mov_b32 m0, s63\n\t" \
    "v_mfma_f32_16x16x32_bf16 v[44:47], a[52:55], v[120:123], v[44:47]\n\t" "s_add_u32 s63, s63, 0x800\n\t" "buffer_load_dwordx4 v91, %[rx], s61 offen lds\n\t" \
    "s_waitcnt lgkmcnt(4)\n\t" "v_mfma_f32_16x16x32_bf16 v[32:35], a[56:59], v[124:127], v[32:35]\n\t" "ds_read_b128 v[112:115], v73 offset:33280\n\t" \
    "v_mfma_f32_16x16x32_bf16 v[36:39], a[60:63], v[124:127], v[36:39]\n\t" "buffer_load_dwordx4 v91, %[rx], s61 offen offset:1024 lds\n\t" "s_waitcnt lgkmcnt(4)\n\t" \
    "v_mfma_f32_16x16x32_bf16 v[40:43], a[56:59], v[128:131], v[40:43]\n\t" "ds_read_b128 v[116:119], v74 offset:512\n\t" "s_mov_b32 m0, s63\n\t" \
    "v_mfma_f32_16x16x32_bf16 v[44:47], a[60:63], v[128:131], v[44:47]\n\t" "s_add_u32 s63, s63, 0x800\n\t" "buffer_load_dwordx4 v92, %[rx], s61 offen lds\n\t" \
    "s_waitcnt lgkmcnt(4)\n\t" "v_mfma_f32_16x16x32_bf16 v[32:35], a[64:67], v[100:103], v[32:35]\n\t" "ds_read_b128 v[120:123], v74 offset:33280\n\t" \
    "v_mfma_f32_16x16x32_bf16 v[36:39], a[68:71], v[100:103], v[36:39]\n\t" "buffer_load_dwordx4 v92, %[rx], s61 offen offset:1024 lds\n\t" "s_waitcnt lgkmcnt(4)\n\t" \
    "v_mfma_f32_16x16x32_bf16 v[40:43], a[64:67], v[104:107], v[40:43]\n\t" "ds_read_b128 v[124:127], v75 offset:512\n\t" "s_mov_b32 m0, s63\n\t" \
    "v_mfma_f32_16x16x32_bf16 v[44:47], a[68:71], v[104:107], v[44:47]\n\t" "s_add_u32 s63, s63, 0x800\n\t" "buffer_load_dwordx4 v93, %[rx], s61 offen lds\n\t" \
    "s_waitcnt lgkmcnt(4)\n\t" "v_mfma_f32_16x16x32_bf16 v[32:35], a[72:75], v[108:111], v[32:35]\n\t" "ds_read_b128 v[128:131], v75 offset:33280\n\t" \
    "v_mfma_f32_16x16x32_bf16 v[36:39], a[76:79], v[108:111], v[36:39]\n\t" "buffer_load_dwordx4 v93, %[rx], s61 offen offset:1024 lds\n\t" "s_waitcnt lgkmcnt(4)\n\t" \
    "v_mfma_f32_16x16x32_bf16 v[40:43], a[72:75], v[112:115], v[40:43]\n\t" "ds_read_b128 v[100:103], v72 offset:768\n\t" "s_mov_b32 m0, s63\n\t" \
    "v_mfma_f32_16x16x32_bf16 v[44:47], a[76:79], v[112:115], v[44:47]\n\t" "s_add_u32 s63, s63, 0x800\n\t" "buffer_load_dwordx4 v94, %[rx], s61 offen lds\n\t" \
    "s_waitcnt lgkmcnt(4)\n\t" "v_mfma_f32_16x16x32_bf16 v[32:35], a[80:83], v[116:119], v[32:35]\n\t" "ds_read_b128 v[104:107], v72 offset:33536\n\t" \
    "v_mfma_f32_16x16x32_bf16 v[36:39], a[84:87], v[116:119], v[36:39]\n\t" "buffer_load_dwordx4 v94, %[rx], s61 offen offset:1024 lds\n\t" "s_waitcnt lgkmcnt(4)\n\t" \
    "v_mfma_f32_16x16x32_bf16 v[40:43], a[80:83], v[120:123], v[40:43]\n\t" "ds_read_b128 v[108:111], v73 offset:768\n\t" "s_mov_b32 m0, s63\n\t" \
    "v_mfma_f32_16x16x32_bf16 v[44:47], a[84:87], v[120:123], v[44:47]\n\t" "s_add_u32 s63, s63, 0x800\n\t" "buffer_load_dwordx4 v95, %[rx], s61 offen lds\n\t" \
    "s_waitcnt lgkmcnt(4)\n\t" "v_mfma_f32_16x16x32_bf16 v[32:35], a[88:91], v[124:127], v[32:35]\n\t" "ds_read_b128 v[112:115], v73 offset:33536\n\t" \
    "v_mfma_f32_16x16x32_bf16 v[36:39], a[92:95], v[124:127], v[36:39]\n\t" "buffer_load_dwordx4 v95, %[rx], s61 offen offset:1024 lds\n\t" "s_waitcnt lgkmcnt(4)\n\t" \
    "v_mfma_f32_16x16x32_bf16 v[40:43], a[88:91], v[128:131], v[40:43]\n\t" "ds_read_b128 v[116:119], v74 offset:768\n\t" \
    "v_mfma_f32_16x16x32_bf16 v[44:47], a[92:95], v[128:131], v[44:47]\n\t" "s_waitcnt lgkmcnt(4)\n\t" "v_mfma_f32_16x16x32_bf16 v[32:35], a[96:99], v[100:103], v[32:35]\n\t" \
    "ds_read_b128 v[120:123], v74 offset:33536\n\t" "s_lshr_b32 s64, s60, 2\n\t" "s_xor_b32 s64, s64, %[xp]\n\t" \
    "v_mfma_f32_16x16x32_bf16 v[36:39], a[100:103], v[100:103], v[36:39]\n\t" "s_and_b32 s64, s64, 1\n\t" "s_lshl_b32 s64, s64, 13\n\t" "s_waitcnt lgkmcnt(4)\n\t" \
    "v_mfma_f32_16x16x32_bf16 v[40:43], a[96:99], v[104:107], v[40:43]\n\t" "ds_read_b128 v[124:127], v75 offset:768\n\t" "s_lshl_b32 s62, s60, 19\n\t" \
    "v_mfma_f32_16x16x32_bf16 v[44:47], a[100:103], v[104:107], v[44:47]\n\t" "s_add_u32 s62, s62, s64\n\t" "s_add_u32 s62, s62, %[ob]\n\t" "s_waitcnt lgkmcnt(4)\n\t" \
    "v_mfma_f32_16x16x32_bf16 v[32:35], a[104:107], v[108:111], v[32:35]\n\t" "ds_read_b128 v[128:131], v75 offset:33536\n\t" \
    "v_mfma_f32_16x16x32_bf16 v[36:39], a[108:111], v[108:111], v[36:39]\n\t" "s_waitcnt lgkmcnt(4)\n\t" \
    "v_mfma_f32_16x16x32_bf16 v[40:43], a[104:107], v[112:115], v[40:43]\n\t" "ds_read_b128 v[100:103], v72 offset:1024\n\t" \
    "v_mfma_f32_16x16x32_bf16 v[44:47], a[108:111], v[112:115], v[44:47]\n\t" "s_waitcnt lgkmcnt(4)\n\t" \
    "v_mfma_f32_16x16x32_bf16 v[32:35], a[112:115], v[116:119], v[32:35]\n\t" "ds_read_b128 v[104:107], v72 offset:33792\n\t" \
    "v_mfma_f32_16x16x32_bf16 v[36:39], a[116:119], v[116:119], v[36:39]\n\t" "s_waitcnt lgkmcnt(4)\n\t" \
    "v_mfma_f32_16x16x32_bf16 v[40:43], a[112:115], v[120:123], v[40:43]\n\t" "ds_read_b128 v[108:111], v73 offset:1024\n\t" \
    "v_mfma_f32_16x16x32_bf16 v[44:47], a[116:119], v[120:123], v[44:47]\n\t" "s_waitcnt lgkmcnt(4)\n\t" \
    "v_mfma_f32_16x16x32_bf16 v[32:35], a[120:123], v[124:127], v[32:35]\n\t" "ds_read_b128 v[112:115], v73 offset:33792\n\t" \
    "v_mfma_f32_16x16x32_bf16 v[36:39], a[124:127], v[124:127], v[36:39]\n\t" "s_waitcnt lgkmcnt(4)\n\t" \
    "v_mfma_f32_16x16x32_bf16 v[40:43], a[120:123], v[128:131], v[40:43]\n\t" "ds_read_b128 v[116:119], v74 offset:1024\n\t" \
    "v_mfma_f32_16x16x32_bf16 v[44:47], a[124:127], v[128:131], v[44:47]\n\t" "s_waitcnt lgkmcnt(4)\n\t" \
    "v_mfma_f32_16x16x32_bf16 v[32:35], a[128:131], v[100:103], v[32:35]\n\t" "ds_read_b128 v[120:123], v74 offset:33792\n\t" \
    "v_mfma_f32_16x16x32_bf16 v[36:39], a[132:135], v[100:103], v[36:39]\n\t" "s_waitcnt lgkmcnt(4)\n\t" \
    "v_mfma_f32_16x16x32_bf16 v[40:43], a[128:131], v[104:107], v[40:43]\n\t" "ds_read_b128 v[124:127], v75 offset:1024\n\t" \
    "v_mfma_f32_16x16x32_bf16 v[44:47], a[132:135], v[104:107], v[44:47]\n\t" "s_waitcnt lgkmcnt(4)\n\t" \
    "v_mfma_f32_16x16x32_bf16 v[32:35], a[136:139], v[108:111], v[32:35]\n\t" "ds_read_b128 v[128:131], v75 offset:33792\n\t" "s_waitcnt vmcnt(16)\n\t" "s_barrier\n\t" \
    "v_mfma_f32_16x16x32_bf16 v[36:39], a[140:143], v[108:111], v[36:39]\n\t" "v_mov_b32 v98, s60\n\t" "buffer_store_dword v98, %[fo], %[rc], %[fl] offen sc1\n\t" \
    "s_waitcnt lgkmcnt(4)\n\t" "v_mfma_f32_16x16x32_bf16 v[40:43], a[136:139], v[112:115], v[40:43]\n\t" "ds_read_b128 v[100:103], v72 offset:1280\n\t" \
    "v_mfma_f32_16x16x32_bf16 v[44:47], a[140:143], v[112:115], v[44:47]\n\t" "s_waitcnt lgkmcnt(4)\n\t" \
    "v_mfma_f32_16x16x32_bf16 v[32:35], a[144:147], v[116:119], v[32:35]\n\t" "ds_read_b128 v[104:107], v72 offset:34048\n\t" \
    "v_mfma_f32_16x16x32_bf16 v[36:39], a[148:151], v[116:119], v[36:39]\n\t" "s_waitcnt lgkmcnt(4)\n\t" \
    "v_mfma_f32_16x16x32_bf16 v[40:43], a[144:147], v[120:123], v[40:43]\n\t" "ds_read_b128 v[108:111], v73 offset:1280\n\t" \
    "v_mfma_f32_16x16x32_bf16 v[44:47], a[148:151], v[120:123], v[44:47]\n\t" "s_waitcnt lgkmcnt(4)\n\t" \
    "v_mfma_f32_16x16x32_bf16 v[32:35], a[152:155], v[124:127], v[32:35]\n\t" "ds_read_b128 v[112:115], v73 offset:34048\n\t" \
    "v_mfma_f32_16x16x32_bf16 v[36:39], a[156:159], v[124:127], v[36:39]\n\t" "s_waitcnt lgkmcnt(4)\n\t" \
    "v_mfma_f32_16x16x32_bf16 v[40:43], a[152:155], v[128:131], v[40:43]\n\t" "ds_read_b128 v[116:119], v74 offset:1280\n\t" \
    "v_mfma_f32_16x16x32_bf16 v[44:47], a[156:159], v[128:131], v[44:47]\n\t" "s_waitcnt lgkmcnt(4)\n\t" \
    "v_mfma_f32_16x16x32_bf16 v[32:35], a[160:163], v[100:103], v[32:35]\n\t" "ds_read_b128 v[120:123], v74 offset:34048\n\t" \
    "v_mfma_f32_16x16x32_bf16 v[36:39], a[164:167], v[100:103], v[36:39]\n\t" "s_waitcnt lgkmcnt(4)\n\t" \
    "v_mfma_f32_16x16x32_bf16 v[40:43], a[160:163], v[104:107], v[40:43]\n\t" "ds_read_b128 v[124:127], v75 offset:1280\n\t" \
    "v_mfma_f32_16x16x32_bf16 v[44:47], a[164:167], v[104:107], v[44:47]\n\t" "s_waitcnt lgkmcnt(4)\n\t" \
    "v_mfma_f32_16x16x32_bf16 v[32:35], a[168:171], v[108:111], v[32:35]\n\t" "ds_read_b128 v[128:131], v75 offset:34048\n\t" \
    "v_mfma_f32_16x16x32_bf16 v[36:39], a[172:175], v[108:111], v[36:39]\n\t" "s_waitcnt lgkmcnt(4)\n\t" \
    "v_mfma_f32_16x16x32_bf16 v[40:43], a[168:171], v[112:115], v[40:43]\n\t" "ds_read_b128 v[100:103], v72 offset:1536\n\t" \
    "v_mfma_f32_16x16x32_bf16 v[44:47], a[172:175], v[112:115], v[44:47]\n\t" "s_waitcnt lgkmcnt(4)\n\t" \
    "v_mfma_f32_16x16x32_bf16 v[32:35], a[176:179], v[116:119], v[32:35]\n\t" "ds_read_b128 v[104:107], v72 offset:34304\n\t" \
    "v_mfma_f32_16x16x32_bf16 v[36:39], a[180:183], v[116:119], v[36:39]\n\t" "s_waitcnt lgkmcnt(4)\n\t" \
    "v_mfma_f32_16x16x32_bf16 v[40:43], a[176:179], v[120:123], v[40:43]\n\t" "ds_read_b128 v[108:111], v73 offset:1536\n\t" \
    "v_mfma_f32_16x16x32_bf16 v[44:47], a[180:183], v[120:123], v[44:47]\n\t" "s_waitcnt lgkmcnt(4)\n\t" \
    "v_mfma_f32_16x16x32_bf16 v[32:35], a[184:187], v[124:127], v[32:35]\n\t" "ds_read_b128 v[112:115], v73 offset:34304\n\t" \
    "v_mfma_f32_16x16x32_bf16 v[36:39], a[188:191], v[124:127], v[36:39]\n\t" "s_waitcnt lgkmcnt(4)\n\t" \
    "v_mfma_f32_16x16x32_bf16 v[40:43], a[184:187], v[128:131], v[40:43]\n\t" "ds_read_b128 v[116:119], v74 offset:1536\n\t" \
    "v_mfma_f32_16x16x32_bf16 v[44:47], a[188:191], v[128:131], v[44:47]\n\t" "s_waitcnt lgkmcnt(4)\n\t" \
    "v_mfma_f32_16x16x32_bf16 v[32:35], a[192:195], v[100:103], v[32:35]\n\t" "ds_read_b128 v[120:123], v74 offset:34304\n\t" \
    "v_mfma_f32_16x16x32_bf16 v[36:39], a[196:199], v[100:103], v[36:39]\n\t" "s_waitcnt lgkmcnt(4)\n\t" \
    "v_mfma_f32_16x16x32_bf16 v[40:43], a[192:195], v[104:107], v[40:43]\n\t" "ds_read_b128 v[124:127], v75 offset:1536\n\t" \
    "v_mfma_f32_16x16x32_bf16 v[44:47], a[196:199], v[104:107], v[44:47]\n\t" "s_waitcnt lgkmcnt(4)\n\t" \
    "v_mfma_f32_16x16x32_bf16 v[32:35], a[200:203], v[108:111], v[32:35]\n\t" "ds_read_b128 v[128:131], v75 offset:34304\n\t" \
    "v_mfma_f32_16x16x32_bf16 v[36:39], a[204:207], v[108:111], v[36:39]\n\t" "s_waitcnt lgkmcnt(4)\n\t" \
    "v_mfma_f32_16x16x32_bf16 v[40:43], a[200:203], v[112:115], v[40:43]\n\t" "ds_read_b128 v[100:103], v72 offset:1792\n\t" \
    "v_mfma_f32_16x16x32_bf16 v[44:47], a[204:207], v[112:115], v[44:47]\n\t" "s_waitcnt lgkmcnt(4)\n\t" \
    "v_mfma_f32_16x16x32_bf16 v[32:35], a[208:211], v[116:119], v[32:35]\n\t" "ds_read_b128 v[104:107], v72 offset:34560\n\t" \
    "v_mfma_f32_16x16x32_bf16 v[36:39], a[212:215], v[116:119], v[36:39]\n\t" "s_waitcnt lgkmcnt(4)\n\t" \
    "v_mfma_f32_16x16x32_bf16 v[40:43], a[208:211], v[120:123], v[40:43]\n\t" "ds_read_b128 v[108:111], v73 offset:1792\n\t" \
    "v_mfma_f32_16x16x32_bf16 v[44:47], a[212:215], v[120:123], v[44:47]\n\t" "s_waitcnt lgkmcnt(4)\n\t" \
    "v_mfma_f32_16x16x32_bf16 v[32:35], a[216:219], v[124:127], v[32:35]\n\t" "ds_read_b128 v[112:115], v73 offset:34560\n\t" \
    "v_mfma_f32_16x16x32_bf16 v[36:39], a[220:223], v[124:127], v[36:39]\n\t" "s_waitcnt lgkmcnt(4)\n\t" \
    "v_mfma_f32_16x16x32_bf16 v[40:43], a[216:219], v[128:131], v[40:43]\n\t" "ds_read_b128 v[116:119], v74 offset:1792\n\t" \
    "v_mfma_f32_16x16x32_bf16 v[44:47], a[220:223], v[128:131], v[44:47]\n\t" "s_waitcnt lgkmcnt(4)\n\t" \
    "v_mfma_f32_16x16x32_bf16 v[32:35], a[224:227], v[100:103], v[32:35]\n\t" "ds_read_b128 v[120:123], v74 offset:34560\n\t" \
    "v_mfma_f32_16x16x32_bf16 v[36:39], a[228:231], v[100:103], v[36:39]\n\t" "s_waitcnt lgkmcnt(4)\n\t" \
    "v_mfma_f32_16x16x32_bf16 v[40:43], a[224:227], v[104:107], v[40:43]\n\t" "ds_read_b128 v[124:127], v75 offset:1792\n\t" \
    "v_mfma_f32_16x16x32_bf16 v[44:47], a[228:231], v[104:107], v[44:47]\n\t" "s_waitcnt lgkmcnt(4)\n\t" \
    "v_mfma_f32_16x16x32_bf16 v[32:35], a[232:235], v[108:111], v[32:35]\n\t" "ds_read_b128 v[128:131], v75 offset:34560\n\t" \
    "v_mfma_f32_16x16x32_bf16 v[36:39], a[236:239], v[108:111], v[36:39]\n\t" "s_waitcnt lgkmcnt(4)\n\t" \
    "v_mfma_f32_16x16x32_bf16 v[40:43], a[232:235], v[112:115], v[40:43]\n\t" "v_mfma_f32_16x16x32_bf16 v[44:47], a[236:239], v[112:115], v[44:47]\n\t" \
    "s_waitcnt lgkmcnt(3)\n\t" "v_mfma_f32_16x16x32_bf16 v[32:35], a[240:243], v[116:119], v[32:35]\n\t" \
    "v_mfma_f32_16x16x32_bf16 v[36:39], a[244:247], v[116:119], v[36:39]\n\t" "s_waitcnt vmcnt(1) lgkmcnt(0)\n\t" "s_barrier\n\t" XT(82, 83) \
    "v_mfma_f32_16x16x32_bf16 v[40:43], a[240:243], v[120:123], v[40:43]\n\t" "ds_read_b128 v[100:103], v76 offset:0\n\t" \
    "v_mfma_f32_16x16x32_bf16 v[44:47], a[244:247], v[120:123], v[44:47]\n\t" "v_mfma_f32_16x16x32_bf16 v[32:35], a[248:251], v[124:127], v[32:35]\n\t" \
    "ds_read_b128 v[104:107], v76 offset:32768\n\t" "v_mfma_f32_16x16x32_bf16 v[36:39], a[252:255], v[124:127], v[36:39]\n\t" "ds_read_b128 v[108:111], v77 offset:0\n\t" \
    "v_mfma_f32_16x16x32_bf16 v[40:43], a[248:251], v[128:131], v[40:43]\n\t" "ds_read_b128 v[112:115], v77 offset:32768\n\t" "ds_read_b128 v[116:119], v78 offset:0\n\t" \
    "v_mfma_f32_16x16x32_bf16 v[44:47], a[252:255], v[128:131], v[44:47]\n\t" "s_add_u32 s60, s60, 1\n\t" GSUM "s_cmp_eq_u32 s60, 1\n\t" "s_cbranch_scc1 20f\n\t" "11:\n\t" \
    XT(80, 81) "s_waitcnt lgkmcnt(4)\n\t" "v_mfma_f32_16x16x32_bf16 v[48:51], a[0:3], v[100:103], 0\n\t" "ds_read_b128 v[120:123], v78 offset:32768\n\t" \
    "s_add_u32 s61, s60, 1\n\t" "s_and_b32 s61, s61, 63\n\t" "v_mfma_f32_16x16x32_bf16 v[52:55], a[4:7], v[100:103], 0\n\t" "s_lshl_b32 s61, s61, 16\n\t" \
    "s_add_u32 s63, %[lw], 0\n\t" "s_waitcnt lgkmcnt(4)\n\t" "v_mfma_f32_16x16x32_bf16 v[56:59], a[0:3], v[104:107], 0\n\t" "ds_read_b128 v[124:127], v79 offset:0\n\t" \
    "v_mfma_f32_16x16x32_bf16 v[60:63], a[4:7], v[104:107], 0\n\t" "s_waitcnt lgkmcnt(4)\n\t" "v_mfma_f32_16x16x32_bf16 v[48:51], a[8:11], v[108:111], v[48:51]\n\t" \
    "ds_read_b128 v[128:131], v79 offset:32768\n\t" "v_cvt_pk_bf16_f32 v64, v32, v33\n\t" "v_mfma_f32_16x16x32_bf16 v[52:55], a[12:15], v[108:111], v[52:55]\n\t" \
    "v_cvt_pk_bf16_f32 v65, v34, v35\n\t" "v_cvt_pk_bf16_f32 v66, v36, v37\n\t" "s_waitcnt lgkmcnt(4)\n\t" \
    "v_mfma_f32_16x16x32_bf16 v[56:59], a[8:11], v[112:115], v[56:59]\n\t" "ds_read_b128 v[100:103], v76 offset:256\n\t" "v_cvt_pk_bf16_f32 v67, v38, v39\n\t" \
    "v_mfma_f32_16x16x32_bf16 v[60:63], a[12:15], v[112:115], v[60:63]\n\t" "v_cvt_pk_bf16_f32 v68, v40, v41\n\t" "v_cvt_pk_bf16_f32 v69, v42, v43\n\t" \
    "s_waitcnt lgkmcnt(4)\n\t" "v_mfma_f32_16x16x32_bf16 v[48:51], a[16:19], v[116:119], v[48:51]\n\t" "ds_read_b128 v[104:107], v76 offset:33024\n\t" \
    "v_cvt_pk_bf16_f32 v70, v44, v45\n\t" "v_mfma_f32_16x16x32_bf16 v[52:55], a[20:23], v[116:119], v[52:55]\n\t" "v_cvt_pk_bf16_f32 v71, v46, v47\n\t" \
    "buffer_store_dwordx4 v[64:67], v96, %[rs], s62 offen offset:0 sc1\n\t" "s_waitcnt lgkmcnt(4)\n\t" "v_mfma_f32_16x16x32_bf16 v[56:59], a[16:19], v[120:123], v[56:59]\n\t" \
    "ds_read_b128 v[108:111], v77 offset:256\n\t" "v_mfma_f32_16x16x32_bf16 v[60:63], a[20:23], v[120:123], v[60:63]\n\t" "s_waitcnt lgkmcnt(4)\n\t" \
    "v_mfma_f32_16x16x32_bf16 v[48:51], a[24:27], v[124:127], v[48:51]\n\t" "ds_read_b128 v[112:115], v77 offset:33024\n\t" \
    "v_mfma_f32_16x16x32_bf16 v[52:55], a[28:31], v[124:127], v[52:55]\n\t" "buffer_store_dwordx4 v[68:71], v96, %[rs], s62 offen offset:256 sc1\n\t" \
    "s_waitcnt lgkmcnt(4)\n\t" "v_mfma_f32_16x16x32_bf16 v[56:59], a[24:27], v[128:131], v[56:59]\n\t" "ds_read_b128 v[116:119], v78 offset:256\n\t" "s_mov_b32 m0, s63\n\t" \
    "v_mfma_f32_16x16x32_bf16 v[60:63], a[28:31], v[128:131], v[60:63]\n\t" "s_add_u32 s63, s63, 0x800\n\t" "buffer_load_dwordx4 v88, %[rx], s61 offen lds\n\t" \
    "s_waitcnt lgkmcnt(4)\n\t" "v_mfma_f32_16x16x32_bf16 v[48:51], a[32:35], v[100:103], v[48:51]\n\t" "ds_read_b128 v[120:123], v78 offset:33024\n\t" \
    "v_mfma_f32_16x16x32_bf16 v[52:55], a[36:39], v[100:103], v[52:55]\n\t" "buffer_load_dwordx4 v88, %[rx], s61 offen offset:1024 lds\n\t" "s_waitcnt lgkmcnt(4)\n\t" \
    "v_mfma_f32_16x16x32_bf16 v[56:59], a[32:35], v[104:107], v[56:59]\n\t" "ds_read_b128 v[124:127], v79 offset:256\n\t" "s_mov_b32 m0, s63\n\t" \
    "v_mfma_f32_16x16x32_bf16 v[60:63], a[36:39], v[104:107], v[60:63]\n\t" "s_add_u32 s63, s63, 0x800\n\t" "buffer_load_dwordx4 v89, %[rx], s61 offen lds\n\t" \
    "s_waitcnt lgkmcnt(4)\n\t" "v_mfma_f32_16x16x32_bf16 v[48:51], a[40:43], v[108:111], v[48:51]\n\t" "ds_read_b128 v[128:131], v79 offset:33024\n\t" \
    "v_mfma_f32_16x16x32_bf16 v[52:55], a[44:47], v[108:111], v[52:55]\n\t" "buffer_load_dwordx4 v89, %[rx], s61 offen offset:1024 lds\n\t" "s_waitcnt lgkmcnt(4)\n\t" \
    "v_mfma_f32_16x16x32_bf16 v[56:59], a[40:43], v[112:115], v[56:59]\n\t" "ds_read_b128 v[100:103], v76 offset:512\n\t" "s_mov_b32 m0, s63\n\t" \
    "v_mfma_f32_16x16x32_bf16 v[60:63], a[44:47], v[112:115], v[60:63]\n\t" "s_add_u32 s63, s63, 0x800\n\t" "buffer_load_dwordx4 v90, %[rx], s61 offen lds\n\t" \
    "s_waitcnt lgkmcnt(4)\n\t" "v_mfma_f32_16x16x32_bf16 v[48:51], a[48:51], v[116:119], v[48:51]\n\t" "ds_read_b128 v[104:107], v76 offset:33280\n\t" \
    "v_mfma_f32_16x16x32_bf16 v[52:55], a[52:55], v[116:119], v[52:55]\n\t" "buffer_load_dwordx4 v90, %[rx], s61 offen offset:1024 lds\n\t" "s_waitcnt lgkmcnt(4)\n\t" \
    "v_mfma_f32_16x16x32_bf16 v[56:59], a[48:51], v[120:123], v[56:59]\n\t" "ds_read_b128 v[108:111], v77 offset:512\n\t" "s_mov_b32 m0, s63\n\t" \
    "v_mfma_f32_16x16x32_bf16 v[60:63], a[52:55], v[120:123], v[60:63]\n\t" "s_add_u32 s63, s63, 0x800\n\t" "buffer_load_dwordx4 v91, %[rx], s61 offen lds\n\t" \
    "s_waitcnt lgkmcnt(4)\n\t" "v_mfma_f32_16x16x32_bf16 v[48:51], a[56:59], v[124:127], v[48:51]\n\t" "ds_read_b128 v[112:115], v77 offset:33280\n\t" \
    "v_mfma_f32_16x16x32_bf16 v[52:55], a[60:63], v[124:127], v[52:55]\n\t" "buffer_load_dwordx4 v91, %[rx], s61 offen offset:1024 lds\n\t" "s_waitcnt lgkmcnt(4)\n\t" \
    "v_mfma_f32_16x16x32_bf16 v[56:59], a[56:59], v[128:131], v[56:59]\n\t" "ds_read_b128 v[116:119], v78 offset:512\n\t" "s_mov_b32 m0, s63\n\t" \
    "v_mfma_f32_16x16x32_bf16 v[60:63], a[60:63], v[128:131], v[60:63]\n\t" "s_add_u32 s63, s63, 0x800\n\t" "buffer_load_dwordx4 v92, %[rx], s61 offen lds\n\t" \
    "s_waitcnt lgkmcnt(4)\n\t" "v_mfma_f32_16x16x32_bf16 v[48:51], a[64:67], v[100:103], v[48:51]\n\t" "ds_read_b128 v[120:123], v78 offset:33280\n\t" \
    "v_mfma_f32_16x16x32_bf16 v[52:55], a[68:71], v[100:103], v[52:55]\n\t" "buffer_load_dwordx4 v92, %[rx], s61 offen offset:1024 lds\n\t" "s_waitcnt lgkmcnt(4)\n\t" \
    "v_mfma_f32_16x16x32_bf16 v[56:59], a[64:67], v[104:107], v[56:59]\n\t" "ds_read_b128 v[124:127], v79 offset:512\n\t" "s_mov_b32 m0, s63\n\t" \
    "v_mfma_f32_16x16x32_bf16 v[60:63], a[68:71], v[104:107], v[60:63]\n\t" "s_add_u32 s63, s63, 0x800\n\t" "buffer_load_dwordx4 v93, %[rx], s61 offen lds\n\t" \
    "s_waitcnt lgkmcnt(4)\n\t" "v_mfma_f32_16x16x32_bf16 v[48:51], a[72:75], v[108:111], v[48:51]\n\t" "ds_read_b128 v[128:131], v79 offset:33280\n\t" \
    "v_mfma_f32_16x16x32_bf16 v[52:55], a[76:79], v[108:111], v[52:55]\n\t" "buffer_load_dwordx4 v93, %[rx], s61 offen offset:1024 lds\n\t" "s_waitcnt lgkmcnt(4)\n\t" \
    "v_mfma_f32_16x16x32_bf16 v[56:59], a[72:75], v[112:115], v[56:59]\n\t" "ds_read_b128 v[100:103], v76 offset:768\n\t" "s_mov_b32 m0, s63\n\t" \
    "v_mfma_f32_16x16x32_bf16 v[60:63], a[76:79], v[112:115], v[60:63]\n\t" "s_add_u32 s63, s63, 0x800\n\t" "buffer_load_dwordx4 v94, %[rx], s61 offen lds\n\t" \
    "s_waitcnt lgkmcnt(4)\n\t" "v_mfma_f32_16x16x32_bf16 v[48:51], a[80:83], v[116:119], v[48:51]\n\t" "ds_read_b128 v[104:107], v76 offset:33536\n\t" \
    "v_mfma_f32_16x16x32_bf16 v[52:55], a[84:87], v[116:119], v[52:55]\n\t" "buffer_load_dwordx4 v94, %[rx], s61 offen offset:1024 lds\n\t" "s_waitcnt lgkmcnt(4)\n\t" \
    "v_mfma_f32_16x16x32_bf16 v[56:59], a[80:83], v[120:123], v[56:59]\n\t" "ds_read_b128 v[108:111], v77 offset:768\n\t" "s_mov_b32 m0, s63\n\t" \
    "v_mfma_f32_16x16x32_bf16 v[60:63], a[84:87], v[120:123], v[60:63]\n\t" "s_add_u32 s63, s63, 0x800\n\t" "buffer_load_dwordx4 v95, %[rx], s61 offen lds\n\t" \
    "s_waitcnt lgkmcnt(4)\n\t" "v_mfma_f32_16x16x32_bf16 v[48:51], a[88:91], v[124:127], v[48:51]\n\t" "ds_read_b128 v[112:115], v77 offset:33536\n\t" \
    "v_mfma_f32_16x16x32_bf16 v[52:55], a[92:95], v[124:127], v[52:55]\n\t" "buffer_load_dwordx4 v95, %[rx], s61 offen offset:1024 lds\n\t" "s_waitcnt lgkmcnt(4)\n\t" \
    "v_mfma_f32_16x16x32_bf16 v[56:59], a[88:91], v[128:131], v[56:59]\n\t" "ds_read_b128 v[116:119], v78 offset:768\n\t" \
    "v_mfma_f32_16x16x32_bf16 v[60:63], a[92:95], v[128:131], v[60:63]\n\t" "s_waitcnt lgkmcnt(4)\n\t" "v_mfma_f32_16x16x32_bf16 v[48:51], a[96:99], v[100:103], v[48:51]\n\t" \
    "ds_read_b128 v[120:123], v78 offset:33536\n\t" "s_lshr_b32 s64, s60, 2\n\t" "s_xor_b32 s64, s64, %[xp]\n\t" \
    "v_mfma_f32_16x16x32_bf16 v[52:55], a[100:103], v[100:103], v[52:55]\n\t" "s_and_b32 s64, s64, 1\n\t" "s_lshl_b32 s64, s64, 13\n\t" "s_waitcnt lgkmcnt(4)\n\t" \
    "v_mfma_f32_16x16x32_bf16 v[56:59], a[96:99], v[104:107], v[56:59]\n\t" "ds_read_b128 v[124:127], v79 offset:768\n\t" "s_lshl_b32 s62, s60, 19\n\t" \
    "v_mfma_f32_16x16x32_bf16 v[60:63], a[100:103], v[104:107], v[60:63]\n\t" "s_add_u32 s62, s62, s64\n\t" "s_add_u32 s62, s62, %[ob]\n\t" "s_waitcnt lgkmcnt(4)\n\t" \
    "v_mfma_f32_16x16x32_bf16 v[48:51], a[104:107], v[108:111], v[48:51]\n\t" "ds_read_b128 v[128:131], v79 offset:33536\n\t" \
    "v_mfma_f32_16x16x32_bf16 v[52:55], a[108:111], v[108:111], v[52:55]\n\t" "s_waitcnt lgkmcnt(4)\n\t" \
    "v_mfma_f32_16x16x32_bf16 v[56:59], a[104:107], v[112:115], v[56:59]\n\t" "ds_read_b128 v[100:103], v76 offset:1024\n\t" \
    "v_mfma_f32_16x16x32_bf16 v[60:63], a[108:111], v[112:115], v[60:63]\n\t" "s_waitcnt lgkmcnt(4)\n\t" \
    "v_mfma_f32_16x16x32_bf16 v[48:51], a[112:115], v[116:119], v[48:51]\n\t" "ds_read_b128 v[104:107], v76 offset:33792\n\t" \
    "v_mfma_f32_16x16x32_bf16 v[52:55], a[116:119], v[116:119], v[52:55]\n\t" "s_waitcnt lgkmcnt(4)\n\t" \
    "v_mfma_f32_16x16x32_bf16 v[56:59], a[112:115], v[120:123], v[56:59]\n\t" "ds_read_b128 v[108:111], v77 offset:1024\n\t" \
    "v_mfma_f32_16x16x32_bf16 v[60:63], a[116:119], v[120:123], v[60:63]\n\t" "s_waitcnt lgkmcnt(4)\n\t" \
    "v_mfma_f32_16x16x32_bf16 v[48:51], a[120:123], v[124:127], v[48:51]\n\t" "ds_read_b128 v[112:115], v77 offset:33792\n\t" \
    "v_mfma_f32_16x16x32_bf16 v[52:55], a[124:127], v[124:127], v[52:55]\n\t" "s_waitcnt lgkmcnt(4)\n\t" \
    "v_mfma_f32_16x16x32_bf16 v[56:59], a[120:123], v[128:131], v[56:59]\n\t" "ds_read_b128 v[116:119], v78 offset:1024\n\t" \
    "v_mfma_f32_16x16x32_bf16 v[60:63], a[124:127], v[128:131], v[60:63]\n\t" "s_waitcnt lgkmcnt(4)\n\t" \
    "v_mfma_f32_16x16x32_bf16 v[48:51], a[128:131], v[100:103], v[48:51]\n\t" "ds_read_b128 v[120:123], v78 offset:33792\n\t" \
    "v_mfma_f32_16x16x32_bf16 v[52:55], a[132:135], v[100:103], v[52:55]\n\t" "s_waitcnt lgkmcnt(4)\n\t" \
    "v_mfma_f32_16x16x32_bf16 v[56:59], a[128:131], v[104:107], v[56:59]\n\t" "ds_read_b128 v[124:127], v79 offset:1024\n\t" \
    "v_mfma_f32_16x16x32_bf16 v[60:63], a[132:135], v[104:107], v[60:63]\n\t" "s_waitcnt lgkmcnt(4)\n\t" \
    "v_mfma_f32_16x16x32_bf16 v[48:51], a[136:139], v[108:111], v[48:51]\n\t" "ds_read_b128 v[128:131], v79 offset:33792\n\t" "s_waitcnt vmcnt(16)\n\t" "s_barrier\n\t" \
    "v_mfma_f32_16x16x32_bf16 v[52:55], a[140:143], v[108:111], v[52:55]\n\t" "v_mov_b32 v98, s60\n\t" "buffer_store_dword v98, %[fo], %[rc], %[fl] offen sc1\n\t" \
    "s_waitcnt lgkmcnt(4)\n\t" "v_mfma_f32_16x16x32_bf16 v[56:59], a[136:139], v[112:115], v[56:59]\n\t" "ds_read_b128 v[100:103], v76 offset:1280\n\t" \
    "v_mfma_f32_16x16x32_bf16 v[60:63], a[140:143], v[112:115], v[60:63]\n\t" "s_waitcnt lgkmcnt(4)\n\t" \
    "v_mfma_f32_16x16x32_bf16 v[48:51], a[144:147], v[116:119], v[48:51]\n\t" "ds_read_b128 v[104:107], v76 offset:34048\n\t" \
    "v_mfma_f32_16x16x32_bf16 v[52:55], a[148:151], v[116:119], v[52:55]\n\t" "s_waitcnt lgkmcnt(4)\n\t" \
    "v_mfma_f32_16x16x32_bf16 v[56:59], a[144:147], v[120:123], v[56:59]\n\t" "ds_read_b128 v[108:111], v77 offset:1280\n\t" \
    "v_mfma_f32_16x16x32_bf16 v[60:63], a[148:151], v[120:123], v[60:63]\n\t" "s_waitcnt lgkmcnt(4)\n\t" \
    "v_mfma_f32_16x16x32_bf16 v[48:51], a[152:155], v[124:127], v[48:51]\n\t" "ds_read_b128 v[112:115], v77 offset:34048\n\t" \
    "v_mfma_f32_16x16x32_bf16 v[52:55], a[156:159], v[124:127], v[52:55]\n\t" "s_waitcnt lgkmcnt(4)\n\t" \
    "v_mfma_f32_16x16x32_bf16 v[56:59], a[152:155], v[128:131], v[56:59]\n\t" "ds_read_b128 v[116:119], v78 offset:1280\n\t" \
    "v_mfma_f32_16x16x32_bf16 v[60:63], a[156:159], v[128:131], v[60:63]\n\t" "s_waitcnt lgkmcnt(4)\n\t" \
    "v_mfma_f32_16x16x32_bf16 v[48:51], a[160:163], v[100:103], v[48:51]\n\t" "ds_read_b128 v[120:123], v78 offset:34048\n\t" \
    "v_mfma_f32_16x16x32_bf16 v[52:55], a[164:167], v[100:103], v[52:55]\n\t" "s_waitcnt lgkmcnt(4)\n\t" \
    "v_mfma_f32_16x16x32_bf16 v[56:59], a[160:163], v[104:107], v[56:59]\n\t" "ds_read_b128 v[124:127], v79 offset:1280\n\t" \
    "v_mfma_f32_16x16x32_bf16 v[60:63], a[164:167], v[104:107], v[60:63]\n\t" "s_waitcnt lgkmcnt(4)\n\t" \
    "v_mfma_f32_16x16x32_bf16 v[48:51], a[168:171], v[108:111], v[48:51]\n\t" "ds_read_b128 v[128:131], v79 offset:34048\n\t" \
    "v_mfma_f32_16x16x32_bf16 v[52:55], a[172:175], v[108:111], v[52:55]\n\t" "s_waitcnt lgkmcnt(4)\n\t" \
    "v_mfma_f32_16x16x32_bf16 v[56:59], a[168:171], v[112:115], v[56:59]\n\t" "ds_read_b128 v[100:103], v76 offset:1536\n\t" \
    "v_mfma_f32_16x16x32_bf16 v[60:63], a[172:175], v[112:115], v[60:63]\n\t" "s_waitcnt lgkmcnt(4)\n\t" \
    "v_mfma_f32_16x16x32_bf16 v[48:51], a[176:179], v[116:119], v[48:51]\n\t" "ds_read_b128 v[104:107], v76 offset:34304\n\t" \
    "v_mfma_f32_16x16x32_bf16 v[52:55], a[180:183], v[116:119], v[52:55]\n\t" "s_waitcnt lgkmcnt(4)\n\t" \
    "v_mfma_f32_16x16x32_bf16 v[56:59], a[176:179], v[120:123], v[56:59]\n\t" "ds_read_b128 v[108:111], v77 offset:1536\n\t" \
    "v_mfma_f32_16x16x32_bf16 v[60:63], a[180:183], v[120:123], v[60:63]\n\t" "s_waitcnt lgkmcnt(4)\n\t" \
    "v_mfma_f32_16x16x32_bf16 v[48:51], a[184:187], v[124:127], v[48:51]\n\t" "ds_read_b128 v[112:115], v77 offset:34304\n\t" \
    "v_mfma_f32_16x16x32_bf16 v[52:55], a[188:191], v[124:127], v[52:55]\n\t" "s_waitcnt lgkmcnt(4)\n\t" \
    "v_mfma_f32_16x16x32_bf16 v[56:59], a[184:187], v[128:131], v[56:59]\n\t" "ds_read_b128 v[116:119], v78 offset:1536\n\t" \
    "v_mfma_f32_16x16x32_bf16 v[60:63], a[188:191], v[128:131], v[60:63]\n\t" "s_waitcnt lgkmcnt(4)\n\t" \
    "v_mfma_f32_16x16x32_bf16 v[48:51], a[192:195], v[100:103], v[48:51]\n\t" "ds_read_b128 v[120:123], v78 offset:34304\n\t" \
    "v_mfma_f32_16x16x32_bf16 v[52:55], a[196:199], v[100:103], v[52:55]\n\t" "s_waitcnt lgkmcnt(4)\n\t" \
    "v_mfma_f32_16x16x32_bf16 v[56:59], a[192:195], v[104:107], v[56:59]\n\t" "ds_read_b128 v[124:127], v79 offset:1536\n\t" \
    "v_mfma_f32_16x16x32_bf16 v[60:63], a[196:199], v[104:107], v[60:63]\n\t" "s_waitcnt lgkmcnt(4)\n\t" \
    "v_mfma_f32_16x16x32_bf16 v[48:51], a[200:203], v[108:111], v[48:51]\n\t" "ds_read_b128 v[128:131], v79 offset:34304\n\t" \
    "v_mfma_f32_16x16x32_bf16 v[52:55], a[204:207], v[108:111], v[52:55]\n\t" "s_waitcnt lgkmcnt(4)\n\t" \
    "v_mfma_f32_16x16x32_bf16 v[56:59], a[200:203], v[112:115], v[56:59]\n\t" "ds_read_b128 v[100:103], v76 offset:1792\n\t" \
    "v_mfma_f32_16x16x32_bf16 v[60:63], a[204:207], v[112:115], v[60:63]\n\t" "s_waitcnt lgkmcnt(4)\n\t" \
    "v_mfma_f32_16x16x32_bf16 v[48:51], a[208:211], v[116:119], v[48:51]\n\t" "ds_read_b128 v[104:107], v76 offset:34560\n\t" \
    "v_mfma_f32_16x16x32_bf16 v[52:55], a[212:215], v[116:119], v[52:55]\n\t" "s_waitcnt lgkmcnt(4)\n\t" \
    "v_mfma_f32_16x16x32_bf16 v[56:59], a[208:211], v[120:123], v[56:59]\n\t" "ds_read_b128 v[108:111], v77 offset:1792\n\t" \
    "v_mfma_f32_16x16x32_bf16 v[60:63], a[212:215], v[120:123], v[60:63]\n\t" "s_waitcnt lgkmcnt(4)\n\t" \
    "v_mfma_f32_16x16x32_bf16 v[48:51], a[216:219], v[124:127], v[48:51]\n\t" "ds_read_b128 v[112:115], v77 offset:34560\n\t" \
    "v_mfma_f32_16x16x32_bf16 v[52:55], a[220:223], v[124:127], v[52:55]\n\t" "s_waitcnt lgkmcnt(4)\n\t" \
    "v_mfma_f32_16x16x32_bf16 v[56:59], a[216:219], v[128:131], v[56:59]\n\t" "ds_read_b128 v[116:119], v78 offset:1792\n\t" \
    "v_mfma_f32_16x16x32_bf16 v[60:63], a[220:223], v[128:131], v[60:63]\n\t" "s_waitcnt lgkmcnt(4)\n\t" \
    "v_mfma_f32_16x16x32_bf16 v[48:51], a[224:227], v[100:103], v[48:51]\n\t" "ds_read_b128 v[120:123], v78 offset:34560\n\t" \
    "v_mfma_f32_16x16x32_bf16 v[52:55], a[228:231], v[100:103], v[52:55]\n\t" "s_waitcnt lgkmcnt(4)\n\t" \
    "v_mfma_f32_16x16x32_bf16 v[56:59], a[224:227], v[104:107], v[56:59]\n\t" "ds_read_b128 v[124:127], v79 offset:1792\n\t" \
    "v_mfma_f32_16x16x32_bf16 v[60:63], a[228:231], v[104:107], v[60:63]\n\t" "s_waitcnt lgkmcnt(4)\n\t" \
    "v_mfma_f32_16x16x32_bf16 v[48:51], a[232:235], v[108:111], v[48:51]\n\t" "ds_read_b128 v[128:131], v79 offset:34560\n\t" \
    "v_mfma_f32_16x16x32_bf16 v[52:55], a[236:239], v[108:111], v[52:55]\n\t" "s_waitcnt lgkmcnt(4)\n\t" \
    "v_mfma_f32_16x16x32_bf16 v[56:59], a[232:235], v[112:115], v[56:59]\n\t" "v_mfma_f32_16x16x32_bf16 v[60:63], a[236:239], v[112:115], v[60:63]\n\t" \
    "s_waitcnt lgkmcnt(3)\n\t" "v_mfma_f32_16x16x32_bf16 v[48:51], a[240:243], v[116:119], v[48:51]\n\t" \
    "v_mfma_f32_16x16x32_bf16 v[52:55], a[244:247], v[116:119], v[52:55]\n\t" "s_waitcnt vmcnt(1) lgkmcnt(0)\n\t" "s_barrier\n\t" XT(82, 83) \
    "v_mfma_f32_16x16x32_bf16 v[56:59], a[240:243], v[120:123], v[56:59]\n\t" "ds_read_b128 v[100:103], v72 offset:0\n\t" \
    "v_mfma_f32_16x16x32_bf16 v[60:63], a[244:247], v[120:123], v[60:63]\n\t" "v_mfma_f32_16x16x32_bf16 v[48:51], a[248:251], v[124:127], v[48:51]\n\t" \
    "ds_read_b128 v[104:107], v72 offset:32768\n\t" "v_mfma_f32_16x16x32_bf16 v[52:55], a[252:255], v[124:127], v[52:55]\n\t" "ds_read_b128 v[108:111], v73 offset:0\n\t" \
    "v_mfma_f32_16x16x32_bf16 v[56:59], a[248:251], v[128:131], v[56:59]\n\t" "ds_read_b128 v[112:115], v73 offset:32768\n\t" "ds_read_b128 v[116:119], v74 offset:0\n\t" \
    "v_mfma_f32_16x16x32_bf16 v[60:63], a[252:255], v[128:131], v[60:63]\n\t" "s_add_u32 s60, s60, 1\n\t" GSUM "s_cmp_lt_u32 s60, 64\n\t" "s_cbranch_scc1 10b\n\t" \
    "s_nop 7\n\t" "v_cvt_pk_bf16_f32 v64, v48, v49\n\t" "v_cvt_pk_bf16_f32 v65, v50, v51\n\t" "v_cvt_pk_bf16_f32 v66, v52, v53\n\t" "v_cvt_pk_bf16_f32 v67, v54, v55\n\t" \
    "v_cvt_pk_bf16_f32 v68, v56, v57\n\t" "v_cvt_pk_bf16_f32 v69, v58, v59\n\t" "v_cvt_pk_bf16_f32 v70, v60, v61\n\t" "v_cvt_pk_bf16_f32 v71, v62, v63\n\t" \
    "buffer_store_dwordx4 v[64:67], v96, %[rs], s62 offen offset:0 sc1\n\t" "buffer_store_dwordx4 v[68:71], v96, %[rs], s62 offen offset:256 sc1\n\t" "s_waitcnt vmcnt(0)\n\t" \
    "s_barrier\n\t" "v_mov_b32 v98, s60\n\t" "buffer_store_dword v98, %[fo], %[rc], %[fl] offen sc1\n\t" "s_waitcnt lgkmcnt(0)\n\t" "s_branch 30f\n\t" "20:\n\t" \
    "v_mov_b32 v96, %[so]\n\t" "s_nop 7\n\t" "v_cvt_pk_bf16_f32 v64, v32, v33\n\t" "v_cvt_pk_bf16_f32 v65, v34, v35\n\t" "v_cvt_pk_bf16_f32 v66, v36, v37\n\t" \
    "v_cvt_pk_bf16_f32 v67, v38, v39\n\t" "v_cvt_pk_bf16_f32 v68, v40, v41\n\t" "v_cvt_pk_bf16_f32 v69, v42, v43\n\t" "v_cvt_pk_bf16_f32 v70, v44, v45\n\t" \
    "v_cvt_pk_bf16_f32 v71, v46, v47\n\t" "buffer_store_dwordx4 v[64:67], v96, %[rs], s62 offen offset:0 sc1\n\t" \
    "buffer_store_dwordx4 v[68:71], v96, %[rs], s62 offen offset:256 sc1\n\t" "s_waitcnt vmcnt(0)\n\t" "s_barrier\n\t" "v_mov_b32 v98, s60\n\t" \
    "buffer_store_dword v98, %[fo], %[rc], %[fl] offen sc1\n\t" "s_branch 11b\n\t" "30:\n\t"
#define ZC_LOOP \
    "s_mov_b32 s60, 0\n\t" "s_mov_b32 s61, 0\n\t" "s_mov_b32 s62, 0\n\t" "s_mov_b32 s63, 0x7f000000\n\t" "s_cmp_ge_u32 s61, %[z0]\n\t" "s_cbranch_scc1 52f\n\t" "51:\n\t" \
    "buffer_load_dword v120, %[fo], %[rc], %[fv] offen sc1\n\t" "buffer_load_dword v121, %[fo], %[rc], %[vd] offen sc1\n\t" "s_waitcnt vmcnt(0)\n\t" \
    "v_readfirstlane_b32 s61, v120\n\t" "v_readfirstlane_b32 s66, v121\n\t" "s_nop 3\n\t" "s_cmp_ge_u32 s61, %[z0]\n\t" "s_cbranch_scc1 52f\n\t" "s_bitcmp1_b32 s66, 1\n\t" \
    "s_cbranch_scc1 90f\n\t" "s_cmp_eq_u32 %[bd], 0\n\t" "s_cbranch_scc1 52f\n\t" "s_sub_u32 %[bd], %[bd], 1\n\t" "s_sleep 2\n\t" "s_branch 51b\n\t" "52:\n\t" \
    "s_lshr_b32 s66, s60, 2\n\t" "s_xor_b32 s66, s66, %[xp]\n\t" "s_and_b32 s66, s66, 1\n\t" "s_lshl_b32 s66, s66, 13\n\t" "s_lshl_b32 s64, s60, 19\n\t" \
    "s_add_u32 s64, s64, s66\n\t" "s_add_u32 s64, s64, %[vb]\n\t" "buffer_load_dwordx4 v[16:19], %[ao], %[rs], s64 offen offset:0 sc1\n\t" \
    "buffer_load_dwordx4 v[20:23], %[ao], %[rs], s64 offen offset:256 sc1\n\t" "buffer_load_dwordx4 v[24:27], %[ao], %[rs], s64 offen offset:2048 sc1\n\t" \
    "buffer_load_dwordx4 v[28:31], %[ao], %[rs], s64 offen offset:2304 sc1\n\t" "buffer_load_dwordx4 v[32:35], %[a1], %[rs], s64 offen offset:0 sc1\n\t" \
    "buffer_load_dwordx4 v[36:39], %[a1], %[rs], s64 offen offset:256 sc1\n\t" "buffer_load_dwordx4 v[40:43], %[a1], %[rs], s64 offen offset:2048 sc1\n\t" \
    "buffer_load_dwordx4 v[44:47], %[a1], %[rs], s64 offen offset:2304 sc1\n\t" "s_waitcnt vmcnt(0)\n\t" "10:\n\t" XT(80, 81) "s_add_u32 s66, s62, %[rg]\n\t" \
    "s_cmp_lt_u32 s66, s60\n\t" "s_cbranch_scc1 60f\n\t" "63:\n\t" "v_mfma_f32_16x16x32_bf16 v[80:83], v[16:19], a[0:3], 0\n\t" \
    "v_mfma_f32_16x16x32_bf16 v[84:87], v[20:23], a[0:3], 0\n\t" "v_mfma_f32_16x16x32_bf16 v[80:83], v[24:27], a[4:7], v[80:83]\n\t" \
    "v_mfma_f32_16x16x32_bf16 v[84:87], v[28:31], a[4:7], v[84:87]\n\t" "v_mfma_f32_16x16x32_bf16 v[80:83], v[32:35], a[8:11], v[80:83]\n\t" \
    "v_cvt_pk_bf16_f32 v112, v96, v97\n\t" "v_mfma_f32_16x16x32_bf16 v[84:87], v[36:39], a[8:11], v[84:87]\n\t" "v_cvt_pk_bf16_f32 v113, v98, v99\n\t" \
    "v_mfma_f32_16x16x32_bf16 v[80:83], v[40:43], a[12:15], v[80:83]\n\t" "v_cvt_pk_bf16_f32 v114, v100, v101\n\t" \
    "v_mfma_f32_16x16x32_bf16 v[84:87], v[44:47], a[12:15], v[84:87]\n\t" "v_cvt_pk_bf16_f32 v115, v102, v103\n\t" \
    "v_mfma_f32_16x16x32_bf16 v[88:91], v[16:19], a[16:19], 0\n\t" "v_cvt_pk_bf16_f32 v116, v104, v105\n\t" "v_mfma_f32_16x16x32_bf16 v[92:95], v[20:23], a[16:19], 0\n\t" \
    "v_cvt_pk_bf16_f32 v117, v106, v107\n\t" "v_mfma_f32_16x16x32_bf16 v[88:91], v[24:27], a[20:23], v[88:91]\n\t" \
    "buffer_store_dwordx4 v[112:115], %[z1], %[rs], s63 offen sc1\n\t" "v_mfma_f32_16x16x32_bf16 v[92:95], v[28:31], a[20:23], v[92:95]\n\t" \
    "v_mfma_f32_16x16x32_bf16 v[88:91], v[32:35], a[24:27], v[88:91]\n\t" "v_cvt_pk_bf16_f32 v118, v108, v109\n\t" \
    "v_mfma_f32_16x16x32_bf16 v[92:95], v[36:39], a[24:27], v[92:95]\n\t" "v_cvt_pk_bf16_f32 v119, v110, v111\n\t" \
    "v_mfma_f32_16x16x32_bf16 v[88:91], v[40:43], a[28:31], v[88:91]\n\t" "buffer_store_dwordx4 v[116:119], %[z2], %[rs], s63 offen sc1\n\t" "s_and_b32 s63, s60, %[rm]\n\t" \
    "v_mfma_f32_16x16x32_bf16 v[92:95], v[44:47], a[28:31], v[92:95]\n\t" "s_lshl_b32 s63, s63, 22\n\t" "s_add_u32 s63, s63, %[zb]\n\t" \
    "v_mfma_f32_16x16x32_bf16 v[96:99], v[16:19], a[32:35], 0\n\t" "buffer_load_dword v120, %[fo], %[rc], %[fv] offen sc1\n\t" \
    "v_mfma_f32_16x16x32_bf16 v[100:103], v[20:23], a[32:35], 0\n\t" "v_mfma_f32_16x16x32_bf16 v[96:99], v[24:27], a[36:39], v[96:99]\n\t" \
    "buffer_load_dword v121, %[fo], %[rc], %[fs] offen sc1\n\t" "v_mfma_f32_16x16x32_bf16 v[100:103], v[28:31], a[36:39], v[100:103]\n\t" \
    "v_mfma_f32_16x16x32_bf16 v[96:99], v[32:35], a[40:43], v[96:99]\n\t" "v_cvt_pk_bf16_f32 v112, v80, v81\n\t" \
    "v_mfma_f32_16x16x32_bf16 v[100:103], v[36:39], a[40:43], v[100:103]\n\t" "v_cvt_pk_bf16_f32 v113, v82, v83\n\t" \
    "v_mfma_f32_16x16x32_bf16 v[96:99], v[40:43], a[44:47], v[96:99]\n\t" "v_cvt_pk_bf16_f32 v114, v84, v85\n\t" \
    "v_mfma_f32_16x16x32_bf16 v[100:103], v[44:47], a[44:47], v[100:103]\n\t" "v_cvt_pk_bf16_f32 v115, v86, v87\n\t" \
    "v_mfma_f32_16x16x32_bf16 v[104:107], v[16:19], a[48:51], 0\n\t" "v_cvt_pk_bf16_f32 v116, v88, v89\n\t" "v_mfma_f32_16x16x32_bf16 v[108:111], v[20:23], a[48:51], 0\n\t" \
    "v_cvt_pk_bf16_f32 v117, v90, v91\n\t" "v_mfma_f32_16x16x32_bf16 v[104:107], v[24:27], a[52:55], v[104:107]\n\t" \
    "buffer_store_dwordx4 v[112:115], %[z1], %[rs], s63 offen sc1\n\t" "v_mfma_f32_16x16x32_bf16 v[108:111], v[28:31], a[52:55], v[108:111]\n\t" \
    "v_mfma_f32_16x16x32_bf16 v[104:107], v[32:35], a[56:59], v[104:107]\n\t" "v_cvt_pk_bf16_f32 v118, v92, v93\n\t" \
    "v_mfma_f32_16x16x32_bf16 v[108:111], v[36:39], a[56:59], v[108:111]\n\t" "v_cvt_pk_bf16_f32 v119, v94, v95\n\t" \
    "v_mfma_f32_16x16x32_bf16 v[104:107], v[40:43], a[60:63], v[104:107]\n\t" "buffer_store_dwordx4 v[116:119], %[z2], %[rs], s63 offen sc1\n\t" \
    "v_mfma_f32_16x16x32_bf16 v[108:111], v[44:47], a[60:63], v[108:111]\n\t" "s_add_u32 s63, s63, 0x20000\n\t" "v_mfma_f32_16x16x32_bf16 v[80:83], v[16:19], a[64:67], 0\n\t" \
    "s_add_u32 s65, s60, 1\n\t" "s_lshr_b32 s66, s65, 2\n\t" "v_mfma_f32_16x16x32_bf16 v[84:87], v[20:23], a[64:67], 0\n\t" "s_xor_b32 s66, s66, %[xp]\n\t" \
    "s_and_b32 s66, s66, 1\n\t" "v_mfma_f32_16x16x32_bf16 v[80:83], v[24:27], a[68:71], v[80:83]\n\t" "s_lshl_b32 s66, s66, 13\n\t" "s_lshl_b32 s64, s65, 19\n\t" \
    "v_mfma_f32_16x16x32_bf16 v[84:87], v[28:31], a[68:71], v[84:87]\n\t" "s_add_u32 s64, s64, s66\n\t" "s_add_u32 s64, s64, %[vb]\n\t" \
    "v_mfma_f32_16x16x32_bf16 v[80:83], v[32:35], a[72:75], v[80:83]\n\t" "v_cvt_pk_bf16_f32 v112, v96, v97\n\t" \
    "v_mfma_f32_16x16x32_bf16 v[84:87], v[36:39], a[72:75], v[84:87]\n\t" "v_cvt_pk_bf16_f32 v113, v98, v99\n\t" \
    "v_mfma_f32_16x16x32_bf16 v[80:83], v[40:43], a[76:79], v[80:83]\n\t" "v_cvt_pk_bf16_f32 v114, v100, v101\n\t" \
    "v_mfma_f32_16x16x32_bf16 v[84:87], v[44:47], a[76:79], v[84:87]\n\t" "v_cvt_pk_bf16_f32 v115, v102, v103\n\t" \
    "v_mfma_f32_16x16x32_bf16 v[88:91], v[16:19], a[80:83], 0\n\t" "v_cvt_pk_bf16_f32 v116, v104, v105\n\t" "v_mfma_f32_16x16x32_bf16 v[92:95], v[20:23], a[80:83], 0\n\t" \
    "v_cvt_pk_bf16_f32 v117, v106, v107\n\t" "v_mfma_f32_16x16x32_bf16 v[88:91], v[24:27], a[84:87], v[88:91]\n\t" \
    "buffer_store_dwordx4 v[112:115], %[z1], %[rs], s63 offen sc1\n\t" "v_mfma_f32_16x16x32_bf16 v[92:95], v[28:31], a[84:87], v[92:95]\n\t" \
    "v_mfma_f32_16x16x32_bf16 v[88:91], v[32:35], a[88:91], v[88:91]\n\t" "v_cvt_pk_bf16_f32 v118, v108, v109\n\t" \
    "v_mfma_f32_16x16x32_bf16 v[92:95], v[36:39], a[88:91], v[92:95]\n\t" "v_cvt_pk_bf16_f32 v119, v110, v111\n\t" \
    "v_mfma_f32_16x16x32_bf16 v[88:91], v[40:43], a[92:95], v[88:91]\n\t" "buffer_store_dwordx4 v[116:119], %[z2], %[rs], s63 offen sc1\n\t" \
    "v_mfma_f32_16x16x32_bf16 v[92:95], v[44:47], a[92:95], v[92:95]\n\t" "s_add_u32 s63, s63, 0x20000\n\t" "v_mfma_f32_16x16x32_bf16 v[96:99], v[16:19], a[96:99], 0\n\t" \
    "v_mfma_f32_16x16x32_bf16 v[100:103], v[20:23], a[96:99], 0\n\t" "v_mfma_f32_16x16x32_bf16 v[96:99], v[24:27], a[100:103], v[96:99]\n\t" \
    "v_mfma_f32_16x16x32_bf16 v[100:103], v[28:31], a[100:103], v[100:103]\n\t" "v_mfma_f32_16x16x32_bf16 v[96:99], v[32:35], a[104:107], v[96:99]\n\t" \
    "v_cvt_pk_bf16_f32 v112, v80, v81\n\t" "v_mfma_f32_16x16x32_bf16 v[100:103], v[36:39], a[104:107], v[100:103]\n\t" "v_cvt_pk_bf16_f32 v113, v82, v83\n\t" \
    "v_mfma_f32_16x16x32_bf16 v[96:99], v[40:43], a[108:111], v[96:99]\n\t" "v_cvt_pk_bf16_f32 v114, v84, v85\n\t" \
    "v_mfma_f32_16x16x32_bf16 v[100:103], v[44:47], a[108:111], v[100:103]\n\t" "v_cvt_pk_bf16_f32 v115, v86, v87\n\t" \
    "v_mfma_f32_16x16x32_bf16 v[104:107], v[16:19], a[112:115], 0\n\t" "v_cvt_pk_bf16_f32 v116, v88, v89\n\t" \
    "v_mfma_f32_16x16x32_bf16 v[108:111], v[20:23], a[112:115], 0\n\t" "v_cvt_pk_bf16_f32 v117, v90, v91\n\t" \
    "v_mfma_f32_16x16x32_bf16 v[104:107], v[24:27], a[116:119], v[104:107]\n\t" "buffer_store_dwordx4 v[112:115], %[z1], %[rs], s63 offen sc1\n\t" \
    "v_mfma_f32_16x16x32_bf16 v[108:111], v[28:31], a[116:119], v[108:111]\n\t" "v_mfma_f32_16x16x32_bf16 v[104:107], v[32:35], a[120:123], v[104:107]\n\t" \
    "v_cvt_pk_bf16_f32 v118, v92, v93\n\t" "v_mfma_f32_16x16x32_bf16 v[108:111], v[36:39], a[120:123], v[108:111]\n\t" "v_cvt_pk_bf16_f32 v119, v94, v95\n\t" \
    "v_mfma_f32_16x16x32_bf16 v[104:107], v[40:43], a[124:127], v[104:107]\n\t" "buffer_store_dwordx4 v[116:119], %[z2], %[rs], s63 offen sc1\n\t" \
    "v_mfma_f32_16x16x32_bf16 v[108:111], v[44:47], a[124:127], v[108:111]\n\t" "s_add_u32 s63, s63, 0x20000\n\t" \
    "v_mfma_f32_16x16x32_bf16 v[80:83], v[16:19], a[128:131], 0\n\t" "s_waitcnt vmcnt(6)\n\t" "v_readfirstlane_b32 s66, v120\n\t" "v_readfirstlane_b32 s67, v121\n\t" \
    "v_mfma_f32_16x16x32_bf16 v[84:87], v[20:23], a[128:131], 0\n\t" "v_mov_b32 v122, s60\n\t" "buffer_store_dword v122, %[fo], %[rc], %[fz] offen sc1\n\t" \
    "v_mfma_f32_16x16x32_bf16 v[80:83], v[24:27], a[132:135], v[80:83]\n\t" "s_max_u32 s61, s61, s66\n\t" "s_max_u32 s62, s62, s67\n\t" "s_cmp_gt_u32 s61, s65\n\t" \
    "v_mfma_f32_16x16x32_bf16 v[84:87], v[28:31], a[132:135], v[84:87]\n\t" "s_cselect_b32 s65, 1, 0\n\t" "s_cselect_b32 s64, s64, 0x7f000000\n\t" \
    "v_mfma_f32_16x16x32_bf16 v[80:83], v[32:35], a[136:139], v[80:83]\n\t" "buffer_load_dwordx4 v[48:51], %[ao], %[rs], s64 offen offset:0 sc1\n\t" \
    "v_cvt_pk_bf16_f32 v112, v96, v97\n\t" "v_mfma_f32_16x16x32_bf16 v[84:87], v[36:39], a[136:139], v[84:87]\n\t" "v_cvt_pk_bf16_f32 v113, v98, v99\n\t" \
    "v_mfma_f32_16x16x32_bf16 v[80:83], v[40:43], a[140:143], v[80:83]\n\t" "buffer_load_dwordx4 v[52:55], %[ao], %[rs], s64 offen offset:256 sc1\n\t" \
    "v_cvt_pk_bf16_f32 v114, v100, v101\n\t" "v_mfma_f32_16x16x32_bf16 v[84:87], v[44:47], a[140:143], v[84:87]\n\t" "v_cvt_pk_bf16_f32 v115, v102, v103\n\t" \
    "v_mfma_f32_16x16x32_bf16 v[88:91], v[16:19], a[144:147], 0\n\t" "buffer_load_dwordx4 v[56:59], %[ao], %[rs], s64 offen offset:2048 sc1\n\t" \
    "v_cvt_pk_bf16_f32 v116, v104, v105\n\t" "v_mfma_f32_16x16x32_bf16 v[92:95], v[20:23], a[144:147], 0\n\t" "v_cvt_pk_bf16_f32 v117, v106, v107\n\t" \
    "v_mfma_f32_16x16x32_bf16 v[88:91], v[24:27], a[148:151], v[88:91]\n\t" "buffer_load_dwordx4 v[60:63], %[ao], %[rs], s64 offen offset:2304 sc1\n\t" \
    "v_mfma_f32_16x16x32_bf16 v[92:95], v[28:31], a[148:151], v[92:95]\n\t" "buffer_store_dwordx4 v[112:115], %[z1], %[rs], s63 offen sc1\n\t" \
    "v_mfma_f32_16x16x32_bf16 v[88:91], v[32:35], a[152:155], v[88:91]\n\t" "buffer_load_dwordx4 v[64:67], %[a1], %[rs], s64 offen offset:0 sc1\n\t" \
    "v_cvt_pk_bf16_f32 v118, v108, v109\n\t" "v_mfma_f32_16x16x32_bf16 v[92:95], v[36:39], a[152:155], v[92:95]\n\t" "v_cvt_pk_bf16_f32 v119, v110, v111\n\t" \
    "v_mfma_f32_16x16x32_bf16 v[88:91], v[40:43], a[156:159], v[88:91]\n\t" "buffer_load_dwordx4 v[68:71], %[a1], %[rs], s64 offen offset:256 sc1\n\t" \
    "buffer_store_dwordx4 v[116:119], %[z2], %[rs], s63 offen sc1\n\t" "v_mfma_f32_16x16x32_bf16 v[92:95], v[44:47], a[156:159], v[92:95]\n\t" \
    "s_add_u32 s63, s63, 0x20000\n\t" "v_mfma_f32_16x16x32_bf16 v[96:99], v[16:19], a[160:163], 0\n\t" \
    "buffer_load_dwordx4 v[72:75], %[a1], %[rs], s64 offen offset:2048 sc1\n\t" "v_mfma_f32_16x16x32_bf16 v[100:103], v[20:23], a[160:163], 0\n\t" \
    "v_mfma_f32_16x16x32_bf16 v[96:99], v[24:27], a[164:167], v[96:99]\n\t" "buffer_load_dwordx4 v[76:79], %[a1], %[rs], s64 offen offset:2304 sc1\n\t" \
    "v_mfma_f32_16x16x32_bf16 v[100:103], v[28:31], a[164:167], v[100:103]\n\t" "v_mfma_f32_16x16x32_bf16 v[96:99], v[32:35], a[168:171], v[96:99]\n\t" \
    "v_cvt_pk_bf16_f32 v112, v80, v81\n\t" "v_mfma_f32_16x16x32_bf16 v[100:103], v[36:39], a[168:171], v[100:103]\n\t" "v_cvt_pk_bf16_f32 v113, v82, v83\n\t" \
    "v_mfma_f32_16x16x32_bf16 v[96:99], v[40:43], a[172:175], v[96:99]\n\t" "v_cvt_pk_bf16_f32 v114, v84, v85\n\t" \
    "v_mfma_f32_16x16x32_bf16 v[100:103], v[44:47], a[172:175], v[100:103]\n\t" "v_cvt_pk_bf16_f32 v115, v86, v87\n\t" \
    "v_mfma_f32_16x16x32_bf16 v[104:107], v[16:19], a[176:179], 0\n\t" "v_cvt_pk_bf16_f32 v116, v88, v89\n\t" \
    "v_mfma_f32_16x16x32_bf16 v[108:111], v[20:23], a[176:179], 0\n\t" "v_cvt_pk_bf16_f32 v117, v90, v91\n\t" \
    "v_mfma_f32_16x16x32_bf16 v[104:107], v[24:27], a[180:183], v[104:107]\n\t" "buffer_store_dwordx4 v[112:115], %[z1], %[rs], s63 offen sc1\n\t" \
    "v_mfma_f32_16x16x32_bf16 v[108:111], v[28:31], a[180:183], v[108:111]\n\t" "v_mfma_f32_16x16x32_bf16 v[104:107], v[32:35], a[184:187], v[104:107]\n\t" \
    "v_cvt_pk_bf16_f32 v118, v92, v93\n\t" "v_mfma_f32_16x16x32_bf16 v[108:111], v[36:39], a[184:187], v[108:111]\n\t" "v_cvt_pk_bf16_f32 v119, v94, v95\n\t" \
    "v_mfma_f32_16x16x32_bf16 v[104:107], v[40:43], a[188:191], v[104:107]\n\t" "buffer_store_dwordx4 v[116:119], %[z2], %[rs], s63 offen sc1\n\t" \
    "v_mfma_f32_16x16x32_bf16 v[108:111], v[44:47], a[188:191], v[108:111]\n\t" "s_add_u32 s63, s63, 0x20000\n\t" \
    "v_mfma_f32_16x16x32_bf16 v[80:83], v[16:19], a[192:195], 0\n\t" "v_mfma_f32_16x16x32_bf16 v[84:87], v[20:23], a[192:195], 0\n\t" \
    "v_mfma_f32_16x16x32_bf16 v[80:83], v[24:27], a[196:199], v[80:83]\n\t" "v_mfma_f32_16x16x32_bf16 v[84:87], v[28:31], a[196:199], v[84:87]\n\t" \
    "v_mfma_f32_16x16x32_bf16 v[80:83], v[32:35], a[200:203], v[80:83]\n\t" "v_cvt_pk_bf16_f32 v112, v96, v97\n\t" \
    "v_mfma_f32_16x16x32_bf16 v[84:87], v[36:39], a[200:203], v[84:87]\n\t" "v_cvt_pk_bf16_f32 v113, v98, v99\n\t" \
    "v_mfma_f32_16x16x32_bf16 v[80:83], v[40:43], a[204:207], v[80:83]\n\t" "v_cvt_pk_bf16_f32 v114, v100, v101\n\t" \
    "v_mfma_f32_16x16x32_bf16 v[84:87], v[44:47], a[204:207], v[84:87]\n\t" "v_cvt_pk_bf16_f32 v115, v102, v103\n\t" \
    "v_mfma_f32_16x16x32_bf16 v[88:91], v[16:19], a[208:211], 0\n\t" "v_cvt_pk_bf16_f32 v116, v104, v105\n\t" "v_mfma_f32_16x16x32_bf16 v[92:95], v[20:23], a[208:211], 0\n\t" \
    "v_cvt_pk_bf16_f32 v117, v106, v107\n\t" "v_mfma_f32_16x16x32_bf16 v[88:91], v[24:27], a[212:215], v[88:91]\n\t" \
    "buffer_store_dwordx4 v[112:115], %[z1], %[rs], s63 offen sc1\n\t" "v_mfma_f32_16x16x32_bf16 v[92:95], v[28:31], a[212:215], v[92:95]\n\t" \
    "v_mfma_f32_16x16x32_bf16 v[88:91], v[32:35], a[216:219], v[88:91]\n\t" "v_cvt_pk_bf16_f32 v118, v108, v109\n\t" \
    "v_mfma_f32_16x16x32_bf16 v[92:95], v[36:39], a[216:219], v[92:95]\n\t" "v_cvt_pk_bf16_f32 v119, v110, v111\n\t" \
    "v_mfma_f32_16x16x32_bf16 v[88:91], v[40:43], a[220:223], v[88:91]\n\t" "buffer_store_dwordx4 v[116:119], %[z2], %[rs], s63 offen sc1\n\t" \
    "v_mfma_f32_16x16x32_bf16 v[92:95], v[44:47], a[220:223], v[92:95]\n\t" "s_add_u32 s63, s63, 0x20000\n\t" "v_mfma_f32_16x16x32_bf16 v[96:99], v[16:19], a[224:227], 0\n\t" \
    "v_mfma_f32_16x16x32_bf16 v[100:103], v[20:23], a[224:227], 0\n\t" "v_mfma_f32_16x16x32_bf16 v[96:99], v[24:27], a[228:231], v[96:99]\n\t" \
    "v_mfma_f32_16x16x32_bf16 v[100:103], v[28:31], a[228:231], v[100:103]\n\t" "v_mfma_f32_16x16x32_bf16 v[96:99], v[32:35], a[232:235], v[96:99]\n\t" \
    "v_cvt_pk_bf16_f32 v112, v80, v81\n\t" "v_mfma_f32_16x16x32_bf16 v[100:103], v[36:39], a[232:235], v[100:103]\n\t" "v_cvt_pk_bf16_f32 v113, v82, v83\n\t" \
    "v_mfma_f32_16x16x32_bf16 v[96:99], v[40:43], a[236:239], v[96:99]\n\t" "v_cvt_pk_bf16_f32 v114, v84, v85\n\t" \
    "v_mfma_f32_16x16x32_bf16 v[100:103], v[44:47], a[236:239], v[100:103]\n\t" "v_cvt_pk_bf16_f32 v115, v86, v87\n\t" \
    "v_mfma_f32_16x16x32_bf16 v[104:107], v[16:19], a[240:243], 0\n\t" "v_cvt_pk_bf16_f32 v116, v88, v89\n\t" \
    "v_mfma_f32_16x16x32_bf16 v[108:111], v[20:23], a[240:243], 0\n\t" "v_cvt_pk_bf16_f32 v117, v90, v91\n\t" \
    "v_mfma_f32_16x16x32_bf16 v[104:107], v[24:27], a[244:247], v[104:107]\n\t" "buffer_store_dwordx4 v[112:115], %[z1], %[rs], s63 offen sc1\n\t" \
    "v_mfma_f32_16x16x32_bf16 v[108:111], v[28:31], a[244:247], v[108:111]\n\t" "v_mfma_f32_16x16x32_bf16 v[104:107], v[32:35], a[248:251], v[104:107]\n\t" \
    "v_cvt_pk_bf16_f32 v118, v92, v93\n\t" "v_mfma_f32_16x16x32_bf16 v[108:111], v[36:39], a[248:251], v[108:111]\n\t" "v_cvt_pk_bf16_f32 v119, v94, v95\n\t" \
    "v_mfma_f32_16x16x32_bf16 v[104:107], v[40:43], a[252:255], v[104:107]\n\t" "buffer_store_dwordx4 v[116:119], %[z2], %[rs], s63 offen sc1\n\t" \
    "s_add_u32 s63, s63, 0x20000\n\t" "s_add_u32 s60, s60, 1\n\t" "v_mfma_f32_16x16x32_bf16 v[108:111], v[44:47], a[252:255], v[108:111]\n\t" "s_cmp_eq_u32 s65, 0\n\t" \
    "s_waitcnt vmcnt(6)\n\t" ZSUM "s_cbranch_scc1 20f\n\t" "23:\n\t" XT(80, 81) "s_add_u32 s66, s62, %[rg]\n\t" "s_cmp_lt_u32 s66, s60\n\t" "s_cbranch_scc1 70f\n\t" "73:\n\t" \
    "v_mfma_f32_16x16x32_bf16 v[80:83], v[48:51], a[0:3], 0\n\t" "v_mfma_f32_16x16x32_bf16 v[84:87], v[52:55], a[0:3], 0\n\t" \
    "v_mfma_f32_16x16x32_bf16 v[80:83], v[56:59], a[4:7], v[80:83]\n\t" "v_mfma_f32_16x16x32_bf16 v[84:87], v[60:63], a[4:7], v[84:87]\n\t" \
    "v_mfma_f32_16x16x32_bf16 v[80:83], v[64:67], a[8:11], v[80:83]\n\t" "v_cvt_pk_bf16_f32 v112, v96, v97\n\t" \
    "v_mfma_f32_16x16x32_bf16 v[84:87], v[68:71], a[8:11], v[84:87]\n\t" "v_cvt_pk_bf16_f32 v113, v98, v99\n\t" \
    "v_mfma_f32_16x16x32_bf16 v[80:83], v[72:75], a[12:15], v[80:83]\n\t" "v_cvt_pk_bf16_f32 v114, v100, v101\n\t" \
    "v_mfma_f32_16x16x32_bf16 v[84:87], v[76:79], a[12:15], v[84:87]\n\t" "v_cvt_pk_bf16_f32 v115, v102, v103\n\t" \
    "v_mfma_f32_16x16x32_bf16 v[88:91], v[48:51], a[16:19], 0\n\t" "v_cvt_pk_bf16_f32 v116, v104, v105\n\t" "v_mfma_f32_16x16x32_bf16 v[92:95], v[52:55], a[16:19], 0\n\t" \
    "v_cvt_pk_bf16_f32 v117, v106, v107\n\t" "v_mfma_f32_16x16x32_bf16 v[88:91], v[56:59], a[20:23], v[88:91]\n\t" \
    "buffer_store_dwordx4 v[112:115], %[z1], %[rs], s63 offen sc1\n\t" "v_mfma_f32_16x16x32_bf16 v[92:95], v[60:63], a[20:23], v[92:95]\n\t" \
    "v_mfma_f32_16x16x32_bf16 v[88:91], v[64:67], a[24:27], v[88:91]\n\t" "v_cvt_pk_bf16_f32 v118, v108, v109\n\t" \
    "v_mfma_f32_16x16x32_bf16 v[92:95], v[68:71], a[24:27], v[92:95]\n\t" "v_cvt_pk_bf16_f32 v119, v110, v111\n\t" \
    "v_mfma_f32_16x16x32_bf16 v[88:91], v[72:75], a[28:31], v[88:91]\n\t" "buffer_store_dwordx4 v[116:119], %[z2], %[rs], s63 offen sc1\n\t" "s_and_b32 s63, s60, %[rm]\n\t" \
    "v_mfma_f32_16x16x32_bf16 v[92:95], v[76:79], a[28:31], v[92:95]\n\t" "s_lshl_b32 s63, s63, 22\n\t" "s_add_u32 s63, s63, %[zb]\n\t" \
    "v_mfma_f32_16x16x32_bf16 v[96:99], v[48:51], a[32:35], 0\n\t" "buffer_load_dword v120, %[fo], %[rc], %[fv] offen sc1\n\t" \
    "v_mfma_f32_16x16x32_bf16 v[100:103], v[52:55], a[32:35], 0\n\t" "v_mfma_f32_16x16x32_bf16 v[96:99], v[56:59], a[36:39], v[96:99]\n\t" \
    "buffer_load_dword v121, %[fo], %[rc], %[fs] offen sc1\n\t" "v_mfma_f32_16x16x32_bf16 v[100:103], v[60:63], a[36:39], v[100:103]\n\t" \
    "v_mfma_f32_16x16x32_bf16 v[96:99], v[64:67], a[40:43], v[96:99]\n\t" "v_cvt_pk_bf16_f32 v112, v80, v81\n\t" \
    "v_mfma_f32_16x16x32_bf16 v[100:103], v[68:71], a[40:43], v[100:103]\n\t" "v_cvt_pk_bf16_f32 v113, v82, v83\n\t" \
    "v_mfma_f32_16x16x32_bf16 v[96:99], v[72:75], a[44:47], v[96:99]\n\t" "v_cvt_pk_bf16_f32 v114, v84, v85\n\t" \
    "v_mfma_f32_16x16x32_bf16 v[100:103], v[76:79], a[44:47], v[100:103]\n\t" "v_cvt_pk_bf16_f32 v115, v86, v87\n\t" \
    "v_mfma_f32_16x16x32_bf16 v[104:107], v[48:51], a[48:51], 0\n\t" "v_cvt_pk_bf16_f32 v116, v88, v89\n\t" "v_mfma_f32_16x16x32_bf16 v[108:111], v[52:55], a[48:51], 0\n\t" \
    "v_cvt_pk_bf16_f32 v117, v90, v91\n\t" "v_mfma_f32_16x16x32_bf16 v[104:107], v[56:59], a[52:55], v[104:107]\n\t" \
    "buffer_store_dwordx4 v[112:115], %[z1], %[rs], s63 offen sc1\n\t" "v_mfma_f32_16x16x32_bf16 v[108:111], v[60:63], a[52:55], v[108:111]\n\t" \
    "v_mfma_f32_16x16x32_bf16 v[104:107], v[64:67], a[56:59], v[104:107]\n\t" "v_cvt_pk_bf16_f32 v118, v92, v93\n\t" \
    "v_mfma_f32_16x16x32_bf16 v[108:111], v[68:71], a[56:59], v[108:111]\n\t" "v_cvt_pk_bf16_f32 v119, v94, v95\n\t" \
    "v_mfma_f32_16x16x32_bf16 v[104:107], v[72:75], a[60:63], v[104:107]\n\t" "buffer_store_dwordx4 v[116:119], %[z2], %[rs], s63 offen sc1\n\t" \
    "v_mfma_f32_16x16x32_bf16 v[108:111], v[76:79], a[60:63], v[108:111]\n\t" "s_add_u32 s63, s63, 0x20000\n\t" "v_mfma_f32_16x16x32_bf16 v[80:83], v[48:51], a[64:67], 0\n\t" \
    "s_add_u32 s65, s60, 1\n\t" "s_lshr_b32 s66, s65, 2\n\t" "v_mfma_f32_16x16x32_bf16 v[84:87], v[52:55], a[64:67], 0\n\t" "s_xor_b32 s66, s66, %[xp]\n\t" \
    "s_and_b32 s66, s66, 1\n\t" "v_mfma_f32_16x16x32_bf16 v[80:83], v[56:59], a[68:71], v[80:83]\n\t" "s_lshl_b32 s66, s66, 13\n\t" "s_lshl_b32 s64, s65, 19\n\t" \
    "v_mfma_f32_16x16x32_bf16 v[84:87], v[60:63], a[68:71], v[84:87]\n\t" "s_add_u32 s64, s64, s66\n\t" "s_add_u32 s64, s64, %[vb]\n\t" \
    "v_mfma_f32_16x16x32_bf16 v[80:83], v[64:67], a[72:75], v[80:83]\n\t" "v_cvt_pk_bf16_f32 v112, v96, v97\n\t" \
    "v_mfma_f32_16x16x32_bf16 v[84:87], v[68:71], a[72:75], v[84:87]\n\t" "v_cvt_pk_bf16_f32 v113, v98, v99\n\t" \
    "v_mfma_f32_16x16x32_bf16 v[80:83], v[72:75], a[76:79], v[80:83]\n\t" "v_cvt_pk_bf16_f32 v114, v100, v101\n\t" \
    "v_mfma_f32_16x16x32_bf16 v[84:87], v[76:79], a[76:79], v[84:87]\n\t" "v_cvt_pk_bf16_f32 v115, v102, v103\n\t" \
    "v_mfma_f32_16x16x32_bf16 v[88:91], v[48:51], a[80:83], 0\n\t" "v_cvt_pk_bf16_f32 v116, v104, v105\n\t" "v_mfma_f32_16x16x32_bf16 v[92:95], v[52:55], a[80:83], 0\n\t" \
    "v_cvt_pk_bf16_f32 v117, v106, v107\n\t" "v_mfma_f32_16x16x32_bf16 v[88:91], v[56:59], a[84:87], v[88:91]\n\t" \
    "buffer_store_dwordx4 v[112:115], %[z1], %[rs], s63 offen sc1\n\t" "v_mfma_f32_16x16x32_bf16 v[92:95], v[60:63], a[84:87], v[92:95]\n\t" \
    "v_mfma_f32_16x16x32_bf16 v[88:91], v[64:67], a[88:91], v[88:91]\n\t" "v_cvt_pk_bf16_f32 v118, v108, v109\n\t" \
    "v_mfma_f32_16x16x32_bf16 v[92:95], v[68:71], a[88:91], v[92:95]\n\t" "v_cvt_pk_bf16_f32 v119, v110, v111\n\t" \
    "v_mfma_f32_16x16x32_bf16 v[88:91], v[72:75], a[92:95], v[88:91]\n\t" "buffer_store_dwordx4 v[116:119], %[z2], %[rs], s63 offen sc1\n\t" \
    "v_mfma_f32_16x16x32_bf16 v[92:95], v[76:79], a[92:95], v[92:95]\n\t" "s_add_u32 s63, s63, 0x20000\n\t" "v_mfma_f32_16x16x32_bf16 v[96:99], v[48:51], a[96:99], 0\n\t" \
    "v_mfma_f32_16x16x32_bf16 v[100:103], v[52:55], a[96:99], 0\n\t" "v_mfma_f32_16x16x32_bf16 v[96:99], v[56:59], a[100:103], v[96:99]\n\t" \
    "v_mfma_f32_16x16x32_bf16 v[100:103], v[60:63], a[100:103], v[100:103]\n\t" "v_mfma_f32_16x16x32_bf16 v[96:99], v[64:67], a[104:107], v[96:99]\n\t" \
    "v_cvt_pk_bf16_f32 v112, v80, v81\n\t" "v_mfma_f32_16x16x32_bf16 v[100:103], v[68:71], a[104:107], v[100:103]\n\t" "v_cvt_pk_bf16_f32 v113, v82, v83\n\t" \
    "v_mfma_f32_16x16x32_bf16 v[96:99], v[72:75], a[108:111], v[96:99]\n\t" "v_cvt_pk_bf16_f32 v114, v84, v85\n\t" \
    "v_mfma_f32_16x16x32_bf16 v[100:103], v[76:79], a[108:111], v[100:103]\n\t" "v_cvt_pk_bf16_f32 v115, v86, v87\n\t" \
    "v_mfma_f32_16x16x32_bf16 v[104:107], v[48:51], a[112:115], 0\n\t" "v_cvt_pk_bf16_f32 v116, v88, v89\n\t" \
    "v_mfma_f32_16x16x32_bf16 v[108:111], v[52:55], a[112:115], 0\n\t" "v_cvt_pk_bf16_f32 v117, v90, v91\n\t" \
    "v_mfma_f32_16x16x32_bf16 v[104:107], v[56:59], a[116:119], v[104:107]\n\t" "buffer_store_dwordx4 v[112:115], %[z1], %[rs], s63 offen sc1\n\t" \
    "v_mfma_f32_16x16x32_bf16 v[108:111], v[60:63], a[116:119], v[108:111]\n\t" "v_mfma_f32_16x16x32_bf16 v[104:107], v[64:67], a[120:123], v[104:107]\n\t" \
    "v_cvt_pk_bf16_f32 v118, v92, v93\n\t" "v_mfma_f32_16x16x32_bf16 v[108:111], v[68:71], a[120:123], v[108:111]\n\t" "v_cvt_pk_bf16_f32 v119, v94, v95\n\t" \
    "v_mfma_f32_16x16x32_bf16 v[104:107], v[72:75], a[124:127], v[104:107]\n\t" "buffer_store_dwordx4 v[116:119], %[z2], %[rs], s63 offen sc1\n\t" \
    "v_mfma_f32_16x16x32_bf16 v[108:111], v[76:79], a[124:127], v[108:111]\n\t" "s_add_u32 s63, s63, 0x20000\n\t" \
    "v_mfma_f32_16x16x32_bf16 v[80:83], v[48:51], a[128:131], 0\n\t" "s_waitcnt vmcnt(6)\n\t" "v_readfirstlane_b32 s66, v120\n\t" "v_readfirstlane_b32 s67, v121\n\t" \
    "v_mfma_f32_16x16x32_bf16 v[84:87], v[52:55], a[128:131], 0\n\t" "v_mov_b32 v122, s60\n\t" "buffer_store_dword v122, %[fo], %[rc], %[fz] offen sc1\n\t" \
    "v_mfma_f32_16x16x32_bf16 v[80:83], v[56:59], a[132:135], v[80:83]\n\t" "s_max_u32 s61, s61, s66\n\t" "s_max_u32 s62, s62, s67\n\t" "s_cmp_gt_u32 s61, s65\n\t" \
    "v_mfma_f32_16x16x32_bf16 v[84:87], v[60:63], a[132:135], v[84:87]\n\t" "s_cselect_b32 s65, 1, 0\n\t" "s_cselect_b32 s64, s64, 0x7f000000\n\t" \
    "v_mfma_f32_16x16x32_bf16 v[80:83], v[64:67], a[136:139], v[80:83]\n\t" "buffer_load_dwordx4 v[16:19], %[ao], %[rs], s64 offen offset:0 sc1\n\t" \
    "v_cvt_pk_bf16_f32 v112, v96, v97\n\t" "v_mfma_f32_16x16x32_bf16 v[84:87], v[68:71], a[136:139], v[84:87]\n\t" "v_cvt_pk_bf16_f32 v113, v98, v99\n\t" \
    "v_mfma_f32_16x16x32_bf16 v[80:83], v[72:75], a[140:143], v[80:83]\n\t" "buffer_load_dwordx4 v[20:23], %[ao], %[rs], s64 offen offset:256 sc1\n\t" \
    "v_cvt_pk_bf16_f32 v114, v100, v101\n\t" "v_mfma_f32_16x16x32_bf16 v[84:87], v[76:79], a[140:143], v[84:87]\n\t" "v_cvt_pk_bf16_f32 v115, v102, v103\n\t" \
    "v_mfma_f32_16x16x32_bf16 v[88:91], v[48:51], a[144:147], 0\n\t" "buffer_load_dwordx4 v[24:27], %[ao], %[rs], s64 offen offset:2048 sc1\n\t" \
    "v_cvt_pk_bf16_f32 v116, v104, v105\n\t" "v_mfma_f32_16x16x32_bf16 v[92:95], v[52:55], a[144:147], 0\n\t" "v_cvt_pk_bf16_f32 v117, v106, v107\n\t" \
    "v_mfma_f32_16x16x32_bf16 v[88:91], v[56:59], a[148:151], v[88:91]\n\t" "buffer_load_dwordx4 v[28:31], %[ao], %[rs], s64 offen offset:2304 sc1\n\t" \
    "v_mfma_f32_16x16x32_bf16 v[92:95], v[60:63], a[148:151], v[92:95]\n\t" "buffer_store_dwordx4 v[112:115], %[z1], %[rs], s63 offen sc1\n\t" \
    "v_mfma_f32_16x16x32_bf16 v[88:91], v[64:67], a[152:155], v[88:91]\n\t" "buffer_load_dwordx4 v[32:35], %[a1], %[rs], s64 offen offset:0 sc1\n\t" \
    "v_cvt_pk_bf16_f32 v118, v108, v109\n\t" "v_mfma_f32_16x16x32_bf16 v[92:95], v[68:71], a[152:155], v[92:95]\n\t" "v_cvt_pk_bf16_f32 v119, v110, v111\n\t" \
    "v_mfma_f32_16x16x32_bf16 v[88:91], v[72:75], a[156:159], v[88:91]\n\t" "buffer_load_dwordx4 v[36:39], %[a1], %[rs], s64 offen offset:256 sc1\n\t" \
    "buffer_store_dwordx4 v[116:119], %[z2], %[rs], s63 offen sc1\n\t" "v_mfma_f32_16x16x32_bf16 v[92:95], v[76:79], a[156:159], v[92:95]\n\t" \
    "s_add_u32 s63, s63, 0x20000\n\t" "v_mfma_f32_16x16x32_bf16 v[96:99], v[48:51], a[160:163], 0\n\t" \
    "buffer_load_dwordx4 v[40:43], %[a1], %[rs], s64 offen offset:2048 sc1\n\t" "v_mfma_f32_16x16x32_bf16 v[100:103], v[52:55], a[160:163], 0\n\t" \
    "v_mfma_f32_16x16x32_bf16 v[96:99], v[56:59], a[164:167], v[96:99]\n\t" "buffer_load_dwordx4 v[44:47], %[a1], %[rs], s64 offen offset:2304 sc1\n\t" \
    "v_mfma_f32_16x16x32_bf16 v[100:103], v[60:63], a[164:167], v[100:103]\n\t" "v_mfma_f32_16x16x32_bf16 v[96:99], v[64:67], a[168:171], v[96:99]\n\t" \
    "v_cvt_pk_bf16_f32 v112, v80, v81\n\t" "v_mfma_f32_16x16x32_bf16 v[100:103], v[68:71], a[168:171], v[100:103]\n\t" "v_cvt_pk_bf16_f32 v113, v82, v83\n\t" \
    "v_mfma_f32_16x16x32_bf16 v[96:99], v[72:75], a[172:175], v[96:99]\n\t" "v_cvt_pk_bf16_f32 v114, v84, v85\n\t" \
    "v_mfma_f32_16x16x32_bf16 v[100:103], v[76:79], a[172:175], v[100:103]\n\t" "v_cvt_pk_bf16_f32 v115, v86, v87\n\t" \
    "v_mfma_f32_16x16x32_bf16 v[104:107], v[48:51], a[176:179], 0\n\t" "v_cvt_pk_bf16_f32 v116, v88, v89\n\t" \
    "v_mfma_f32_16x16x32_bf16 v[108:111], v[52:55], a[176:179], 0\n\t" "v_cvt_pk_bf16_f32 v117, v90, v91\n\t" \
    "v_mfma_f32_16x16x32_bf16 v[104:107], v[56:59], a[180:183], v[104:107]\n\t" "buffer_store_dwordx4 v[112:115], %[z1], %[rs], s63 offen sc1\n\t" \
    "v_mfma_f32_16x16x32_bf16 v[108:111], v[60:63], a[180:183], v[108:111]\n\t" "v_mfma_f32_16x16x32_bf16 v[104:107], v[64:67], a[184:187], v[104:107]\n\t" \
    "v_cvt_pk_bf16_f32 v118, v92, v93\n\t" "v_mfma_f32_16x16x32_bf16 v[108:111], v[68:71], a[184:187], v[108:111]\n\t" "v_cvt_pk_bf16_f32 v119, v94, v95\n\t" \
    "v_mfma_f32_16x16x32_bf16 v[104:107], v[72:75], a[188:191], v[104:107]\n\t" "buffer_store_dwordx4 v[116:119], %[z2], %[rs], s63 offen sc1\n\t" \
    "v_mfma_f32_16x16x32_bf16 v[108:111], v[76:79], a[188:191], v[108:111]\n\t" "s_add_u32 s63, s63, 0x20000\n\t" \
    "v_mfma_f32_16x16x32_bf16 v[80:83], v[48:51], a[192:195], 0\n\t" "v_mfma_f32_16x16x32_bf16 v[84:87], v[52:55], a[192:195], 0\n\t" \
    "v_mfma_f32_16x16x32_bf16 v[80:83], v[56:59], a[196:199], v[80:83]\n\t" "v_mfma_f32_16x16x32_bf16 v[84:87], v[60:63], a[196:199], v[84:87]\n\t" \
    "v_mfma_f32_16x16x32_bf16 v[80:83], v[64:67], a[200:203], v[80:83]\n\t" "v_cvt_pk_bf16_f32 v112, v96, v97\n\t" \
    "v_mfma_f32_16x16x32_bf16 v[84:87], v[68:71], a[200:203], v[84:87]\n\t" "v_cvt_pk_bf16_f32 v113, v98, v99\n\t" \
    "v_mfma_f32_16x16x32_bf16 v[80:83], v[72:75], a[204:207], v[80:83]\n\t" "v_cvt_pk_bf16_f32 v114, v100, v101\n\t" \
    "v_mfma_f32_16x16x32_bf16 v[84:87], v[76:79], a[204:207], v[84:87]\n\t" "v_cvt_pk_bf16_f32 v115, v102, v103\n\t" \
    "v_mfma_f32_16x16x32_bf16 v[88:91], v[48:51], a[208:211], 0\n\t" "v_cvt_pk_bf16_f32 v116, v104, v105\n\t" "v_mfma_f32_16x16x32_bf16 v[92:95], v[52:55], a[208:211], 0\n\t" \
    "v_cvt_pk_bf16_f32 v117, v106, v107\n\t" "v_mfma_f32_16x16x32_bf16 v[88:91], v[56:59], a[212:215], v[88:91]\n\t" \
    "buffer_store_dwordx4 v[112:115], %[z1], %[rs], s63 offen sc1\n\t" "v_mfma_f32_16x16x32_bf16 v[92:95], v[60:63], a[212:215], v[92:95]\n\t" \
    "v_mfma_f32_16x16x32_bf16 v[88:91], v[64:67], a[216:219], v[88:91]\n\t" "v_cvt_pk_bf16_f32 v118, v108, v109\n\t" \
    "v_mfma_f32_16x16x32_bf16 v[92:95], v[68:71], a[216:219], v[92:95]\n\t" "v_cvt_pk_bf16_f32 v119, v110, v111\n\t" \
    "v_mfma_f32_16x16x32_bf16 v[88:91], v[72:75], a[220:223], v[88:91]\n\t" "buffer_store_dwordx4 v[116:119], %[z2], %[rs], s63 offen sc1\n\t" \
    "v_mfma_f32_16x16x32_bf16 v[92:95], v[76:79], a[220:223], v[92:95]\n\t" "s_add_u32 s63, s63, 0x20000\n\t" "v_mfma_f32_16x16x32_bf16 v[96:99], v[48:51], a[224:227], 0\n\t" \
    "v_mfma_f32_16x16x32_bf16 v[100:103], v[52:55], a[224:227], 0\n\t" "v_mfma_f32_16x16x32_bf16 v[96:99], v[56:59], a[228:231], v[96:99]\n\t" \
    "v_mfma_f32_16x16x32_bf16 v[100:103], v[60:63], a[228:231], v[100:103]\n\t" "v_mfma_f32_16x16x32_bf16 v[96:99], v[64:67], a[232:235], v[96:99]\n\t" \
    "v_cvt_pk_bf16_f32 v112, v80, v81\n\t" "v_mfma_f32_16x16x32_bf16 v[100:103], v[68:71], a[232:235], v[100:103]\n\t" "v_cvt_pk_bf16_f32 v113, v82, v83\n\t" \
    "v_mfma_f32_16x16x32_bf16 v[96:99], v[72:75], a[236:239], v[96:99]\n\t" "v_cvt_pk_bf16_f32 v114, v84, v85\n\t" \
    "v_mfma_f32_16x16x32_bf16 v[100:103], v[76:79], a[236:239], v[100:103]\n\t" "v_cvt_pk_bf16_f32 v115, v86, v87\n\t" \
    "v_mfma_f32_16x16x32_bf16 v[104:107], v[48:51], a[240:243], 0\n\t" "v_cvt_pk_bf16_f32 v116, v88, v89\n\t" \
    "v_mfma_f32_16x16x32_bf16 v[108:111], v[52:55], a[240:243], 0\n\t" "v_cvt_pk_bf16_f32 v117, v90, v91\n\t" \
    "v_mfma_f32_16x16x32_bf16 v[104:107], v[56:59], a[244:247], v[104:107]\n\t" "buffer_store_dwordx4 v[112:115], %[z1], %[rs], s63 offen sc1\n\t" \
    "v_mfma_f32_16x16x32_bf16 v[108:111], v[60:63], a[244:247], v[108:111]\n\t" "v_mfma_f32_16x16x32_bf16 v[104:107], v[64:67], a[248:251], v[104:107]\n\t" \
    "v_cvt_pk_bf16_f32 v118, v92, v93\n\t" "v_mfma_f32_16x16x32_bf16 v[108:111], v[68:71], a[248:251], v[108:111]\n\t" "v_cvt_pk_bf16_f32 v119, v94, v95\n\t" \
    "v_mfma_f32_16x16x32_bf16 v[104:107], v[72:75], a[252:255], v[104:107]\n\t" "buffer_store_dwordx4 v[116:119], %[z2], %[rs], s63 offen sc1\n\t" \
    "s_add_u32 s63, s63, 0x20000\n\t" "s_add_u32 s60, s60, 1\n\t" "v_mfma_f32_16x16x32_bf16 v[108:111], v[76:79], a[252:255], v[108:111]\n\t" "s_cmp_eq_u32 s65, 0\n\t" \
    "s_waitcnt vmcnt(6)\n\t" ZSUM "s_cbranch_scc1 30f\n\t" "33:\n\t" "s_branch 10b\n\t" "20:\n\t" "s_nop 7\n\t" "v_cvt_pk_bf16_f32 v112, v96, v97\n\t" \
    "v_cvt_pk_bf16_f32 v113, v98, v99\n\t" "v_cvt_pk_bf16_f32 v114, v100, v101\n\t" "v_cvt_pk_bf16_f32 v115, v102, v103\n\t" "v_cvt_pk_bf16_f32 v116, v104, v105\n\t" \
    "v_cvt_pk_bf16_f32 v117, v106, v107\n\t" "v_cvt_pk_bf16_f32 v118, v108, v109\n\t" "v_cvt_pk_bf16_f32 v119, v110, v111\n\t" \
    "buffer_store_dwordx4 v[112:115], %[z1], %[rs], s63 offen sc1\n\t" "buffer_store_dwordx4 v[116:119], %[z2], %[rs], s63 offen sc1\n\t" \
    "buffer_load_dword v120, %[fo], %[rc], %[fv] offen sc1\n\t" "s_waitcnt vmcnt(0)\n\t" "v_readfirstlane_b32 s61, v120\n\t" "s_lshr_b32 s66, s60, 2\n\t" \
    "s_xor_b32 s66, s66, %[xp]\n\t" "s_and_b32 s66, s66, 1\n\t" "s_lshl_b32 s66, s66, 13\n\t" "s_lshl_b32 s64, s60, 19\n\t" "s_add_u32 s64, s64, s66\n\t" \
    "s_add_u32 s64, s64, %[vb]\n\t" "s_cmp_gt_u32 s61, s60\n\t" "s_cbranch_scc0 24f\n\t" "s_cmp_eq_u32 s60, 64\n\t" "s_cbranch_scc1 24f\n\t" \
    "buffer_load_dwordx4 v[48:51], %[ao], %[rs], s64 offen offset:0 sc1\n\t" "buffer_load_dwordx4 v[52:55], %[ao], %[rs], s64 offen offset:256 sc1\n\t" \
    "buffer_load_dwordx4 v[56:59], %[ao], %[rs], s64 offen offset:2048 sc1\n\t" "buffer_load_dwordx4 v[60:63], %[ao], %[rs], s64 offen offset:2304 sc1\n\t" \
    "buffer_load_dwordx4 v[64:67], %[a1], %[rs], s64 offen offset:0 sc1\n\t" "buffer_load_dwordx4 v[68:71], %[a1], %[rs], s64 offen offset:256 sc1\n\t" \
    "buffer_load_dwordx4 v[72:75], %[a1], %[rs], s64 offen offset:2048 sc1\n\t" "buffer_load_dwordx4 v[76:79], %[a1], %[rs], s64 offen offset:2304 sc1\n\t" \
    "v_mov_b32 v122, s60\n\t" "buffer_store_dword v122, %[fo], %[rc], %[fz] offen sc1\n\t" "s_waitcnt vmcnt(1)\n\t" "s_branch 23b\n\t" "24:\n\t" "v_mov_b32 v122, s60\n\t" \
    "buffer_store_dword v122, %[fo], %[rc], %[fz] offen sc1\n\t" "s_cmp_eq_u32 s60, 64\n\t" "s_cbranch_scc1 90f\n\t" "s_add_u32 s67, s60, 1\n\t" "s_cmp_ge_u32 s61, s67\n\t" \
    "s_cbranch_scc1 22f\n\t" "21:\n\t" "buffer_load_dword v120, %[fo], %[rc], %[fv] offen sc1\n\t" "buffer_load_dword v121, %[fo], %[rc], %[vd] offen sc1\n\t" \
    "s_waitcnt vmcnt(0)\n\t" "v_readfirstlane_b32 s61, v120\n\t" "v_readfirstlane_b32 s66, v121\n\t" "s_nop 3\n\t" "s_cmp_ge_u32 s61, s67\n\t" "s_cbranch_scc1 22f\n\t" \
    "s_bitcmp1_b32 s66, 1\n\t" "s_cbranch_scc1 90f\n\t" "s_cmp_eq_u32 %[bd], 0\n\t" "s_cbranch_scc1 22f\n\t" "s_sub_u32 %[bd], %[bd], 1\n\t" "s_sleep 2\n\t" \
    "s_branch 21b\n\t" "22:\n\t" "s_lshr_b32 s66, s60, 2\n\t" "s_xor_b32 s66, s66, %[xp]\n\t" "s_and_b32 s66, s66, 1\n\t" "s_lshl_b32 s66, s66, 13\n\t" \
    "s_lshl_b32 s64, s60, 19\n\t" "s_add_u32 s64, s64, s66\n\t" "s_add_u32 s64, s64, %[vb]\n\t" "buffer_load_dwordx4 v[48:51], %[ao], %[rs], s64 offen offset:0 sc1\n\t" \
    "buffer_load_dwordx4 v[52:55], %[ao], %[rs], s64 offen offset:256 sc1\n\t" "buffer_load_dwordx4 v[56:59], %[ao], %[rs], s64 offen offset:2048 sc1\n\t" \
    "buffer_load_dwordx4 v[60:63], %[ao], %[rs], s64 offen offset:2304 sc1\n\t" "buffer_load_dwordx4 v[64:67], %[a1], %[rs], s64 offen offset:0 sc1\n\t" \
    "buffer_load_dwordx4 v[68:71], %[a1], %[rs], s64 offen offset:256 sc1\n\t" "buffer_load_dwordx4 v[72:75], %[a1], %[rs], s64 offen offset:2048 sc1\n\t" \
    "buffer_load_dwordx4 v[76:79], %[a1], %[rs], s64 offen offset:2304 sc1\n\t" "s_waitcnt vmcnt(0)\n\t" "s_branch 23b\n\t" "60:\n\t" "s_sub_u32 s67, s60, %[rg]\n\t" \
    "s_cmp_ge_u32 s62, s67\n\t" "s_cbranch_scc1 62f\n\t" "61:\n\t" "buffer_load_dword v120, %[fo], %[rc], %[fs] offen sc1\n\t" \
    "buffer_load_dword v121, %[fo], %[rc], %[vd] offen sc1\n\t" "s_waitcnt vmcnt(0)\n\t" "v_readfirstlane_b32 s62, v120\n\t" "v_readfirstlane_b32 s66, v121\n\t" "s_nop 3\n\t" \
    "s_cmp_ge_u32 s62, s67\n\t" "s_cbranch_scc1 62f\n\t" "s_bitcmp1_b32 s66, 1\n\t" "s_cbranch_scc1 90f\n\t" "s_cmp_eq_u32 %[bd], 0\n\t" "s_cbranch_scc1 62f\n\t" \
    "s_sub_u32 %[bd], %[bd], 1\n\t" "s_sleep 2\n\t" "s_branch 61b\n\t" "62:\n\t" "s_branch 63b\n\t" "30:\n\t" "s_nop 7\n\t" "v_cvt_pk_bf16_f32 v112, v96, v97\n\t" \
    "v_cvt_pk_bf16_f32 v113, v98, v99\n\t" "v_cvt_pk_bf16_f32 v114, v100, v101\n\t" "v_cvt_pk_bf16_f32 v115, v102, v103\n\t" "v_cvt_pk_bf16_f32 v116, v104, v105\n\t" \
    "v_cvt_pk_bf16_f32 v117, v106, v107\n\t" "v_cvt_pk_bf16_f32 v118, v108, v109\n\t" "v_cvt_pk_bf16_f32 v119, v110, v111\n\t" \
    "buffer_store_dwordx4 v[112:115], %[z1], %[rs], s63 offen sc1\n\t" "buffer_store_dwordx4 v[116:119], %[z2], %[rs], s63 offen sc1\n\t" \
    "buffer_load_dword v120, %[fo], %[rc], %[fv] offen sc1\n\t" "s_waitcnt vmcnt(0)\n\t" "v_readfirstlane_b32 s61, v120\n\t" "s_lshr_b32 s66, s60, 2\n\t" \
    "s_xor_b32 s66, s66, %[xp]\n\t" "s_and_b32 s66, s66, 1\n\t" "s_lshl_b32 s66, s66, 13\n\t" "s_lshl_b32 s64, s60, 19\n\t" "s_add_u32 s64, s64, s66\n\t" \
    "s_add_u32 s64, s64, %[vb]\n\t" "s_cmp_gt_u32 s61, s60\n\t" "s_cbranch_scc0 34f\n\t" "s_cmp_eq_u32 s60, 64\n\t" "s_cbranch_scc1 34f\n\t" \
    "buffer_load_dwordx4 v[16:19], %[ao], %[rs], s64 offen offset:0 sc1\n\t" "buffer_load_dwordx4 v[20:23], %[ao], %[rs], s64 offen offset:256 sc1\n\t" \
    "buffer_load_dwordx4 v[24:27], %[ao], %[rs], s64 offen offset:2048 sc1\n\t" "buffer_load_dwordx4 v[28:31], %[ao], %[rs], s64 offen offset:2304 sc1\n\t" \
    "buffer_load_dwordx4 v[32:35], %[a1], %[rs], s64 offen offset:0 sc1\n\t" "buffer_load_dwordx4 v[36:39], %[a1], %[rs], s64 offen offset:256 sc1\n\t" \
    "buffer_load_dwordx4 v[40:43], %[a1], %[rs], s64 offen offset:2048 sc1\n\t" "buffer_load_dwordx4 v[44:47], %[a1], %[rs], s64 offen offset:2304 sc1\n\t" \
    "v_mov_b32 v122, s60\n\t" "buffer_store_dword v122, %[fo], %[rc], %[fz] offen sc1\n\t" "s_waitcnt vmcnt(1)\n\t" "s_branch 33b\n\t" "34:\n\t" "v_mov_b32 v122, s60\n\t" \
    "buffer_store_dword v122, %[fo], %[rc], %[fz] offen sc1\n\t" "s_cmp_eq_u32 s60, 64\n\t" "s_cbranch_scc1 90f\n\t" "s_add_u32 s67, s60, 1\n\t" "s_cmp_ge_u32 s61, s67\n\t" \
    "s_cbranch_scc1 32f\n\t" "31:\n\t" "buffer_load_dword v120, %[fo], %[rc], %[fv] offen sc1\n\t" "buffer_load_dword v121, %[fo], %[rc], %[vd] offen sc1\n\t" \
    "s_waitcnt vmcnt(0)\n\t" "v_readfirstlane_b32 s61, v120\n\t" "v_readfirstlane_b32 s66, v121\n\t" "s_nop 3\n\t" "s_cmp_ge_u32 s61, s67\n\t" "s_cbranch_scc1 32f\n\t" \
    "s_bitcmp1_b32 s66, 1\n\t" "s_cbranch_scc1 90f\n\t" "s_cmp_eq_u32 %[bd], 0\n\t" "s_cbranch_scc1 32f\n\t" "s_sub_u32 %[bd], %[bd], 1\n\t" "s_sleep 2\n\t" \
    "s_branch 31b\n\t" "32:\n\t" "s_lshr_b32 s66, s60, 2\n\t" "s_xor_b32 s66, s66, %[xp]\n\t" "s_and_b32 s66, s66, 1\n\t" "s_lshl_b32 s66, s66, 13\n\t" \
    "s_lshl_b32 s64, s60, 19\n\t" "s_add_u32 s64, s64, s66\n\t" "s_add_u32 s64, s64, %[vb]\n\t" "buffer_load_dwordx4 v[16:19], %[ao], %[rs], s64 offen offset:0 sc1\n\t" \
    "buffer_load_dwordx4 v[20:23], %[ao], %[rs], s64 offen offset:256 sc1\n\t" "buffer_load_dwordx4 v[24:27], %[ao], %[rs], s64 offen offset:2048 sc1\n\t" \
    "buffer_load_dwordx4 v[28:31], %[ao], %[rs], s64 offen offset:2304 sc1\n\t" "buffer_load_dwordx4 v[32:35], %[a1], %[rs], s64 offen offset:0 sc1\n\t" \
    "buffer_load_dwordx4 v[36:39], %[a1], %[rs], s64 offen offset:256 sc1\n\t" "buffer_load_dwordx4 v[40:43], %[a1], %[rs], s64 offen offset:2048 sc1\n\t" \
    "buffer_load_dwordx4 v[44:47], %[a1], %[rs], s64 offen offset:2304 sc1\n\t" "s_waitcnt vmcnt(0)\n\t" "s_branch 33b\n\t" "70:\n\t" "s_sub_u32 s67, s60, %[rg]\n\t" \
    "s_cmp_ge_u32 s62, s67\n\t" "s_cbranch_scc1 72f\n\t" "71:\n\t" "buffer_load_dword v120, %[fo], %[rc], %[fs] offen sc1\n\t" \
    "buffer_load_dword v121, %[fo], %[rc], %[vd] offen sc1\n\t" "s_waitcnt vmcnt(0)\n\t" "v_readfirstlane_b32 s62, v120\n\t" "v_readfirstlane_b32 s66, v121\n\t" "s_nop 3\n\t" \
    "s_cmp_ge_u32 s62, s67\n\t" "s_cbranch_scc1 72f\n\t" "s_bitcmp1_b32 s66, 1\n\t" "s_cbranch_scc1 90f\n\t" "s_cmp_eq_u32 %[bd], 0\n\t" "s_cbranch_scc1 72f\n\t" \
    "s_sub_u32 %[bd], %[bd], 1\n\t" "s_sleep 2\n\t" "s_branch 71b\n\t" "72:\n\t" "s_branch 73b\n\t" "90:\n\t"

#define T_DQ 1480
#define T_DV 1540
#define T_M1 150
#define T_DY 700
#define T_DX 1250
#define T_MIN 35
#define T_PEN 600
#define T_RETRY 1100
#define T_POLL 3
#define T_RST 12
#define T_U0 540
constexpr int T_OVH = 118;
constexpr unsigned NS(int ns) { return (unsigned)(ns * 64 / 10); }
struct Clk { unsigned cyc, g, k1, k2; };
DEV void anchor(Clk& k) {
  unsigned long long c, r[8];
  asm volatile("s_memtime %0\n\ts_memrealtime %1\n\ts_nop 2\n\ts_memrealtime %2\n\ts_nop 2\n\ts_memrealtime %3\n\ts_nop 2\n\ts_memrealtime %4\n\ts_nop 2\n\t"
               "s_memrealtime %5\n\ts_nop 2\n\ts_memrealtime %6\n\ts_nop 2\n\ts_memrealtime %7\n\ts_waitcnt lgkmcnt(0)"
               : "=s"(c), "=s"(r[0]), "=s"(r[1]), "=s"(r[2]), "=s"(r[3]), "=s"(r[4]), "=s"(r[5]), "=s"(r[6]) :: "memory");
  const unsigned a = (unsigned)r[0];
  unsigned n = 0;
#pragma unroll
  for (int i = 1; i < 7; ++i) asm("s_cmp_eq_u32 %1, %2\n\ts_addc_u32 %0, %0, 0" : "+s"(n) : "s"((unsigned)r[i]), "s"(a) : "scc");
  const unsigned nx = (a + 4u) << 6, cy = (unsigned)c + 12u + 16u * n;
  const float dc = (float)(cy - k.cyc), dg = (float)(nx - k.g);
  asm volatile("s_nop 0\n\tv_readfirstlane_b32 %0, %2\n\tv_readfirstlane_b32 %1, %3\n\ts_nop 3\n\ts_min_u32 %0, %0, 0x80000\n\ts_min_u32 %1, %1, 0x8000"
               : "=&s"(k.k1), "=&s"(k.k2) : "v"((unsigned)(dg * __builtin_amdgcn_rcpf(dc) * 65536.f)), "v"((unsigned)(dc * __builtin_amdgcn_rcpf(dg) * 65536.f)) : "scc");
  k.cyc = cy; k.g = nx;
}
DEV unsigned to_cyc(const Clk& k, unsigned g) {
  unsigned d;
  asm("s_max_i32 %0, %1, 0xffff8000\n\ts_min_i32 %0, %0, 0x8000\n\ts_mul_i32 %0, %0, %2\n\ts_ashr_i32 %0, %0, 16" : "=&s"(d) : "s"(g - k.g), "s"(k.k2) : "scc");
  return k.cyc + d - T_OVH;
}
DEV void wait_cyc(unsigned t) { asm volatile(TIMED_WAIT :: "s"(t) : CL_S); }
DEV bool slow_head(const u16* q, int h) { return ((((unsigned)(size_t)q >> 8) + h) & 3) == 1; }
DEV void hold(const Ctx& c, int ticks) {
#pragma unroll 1
  while ((unsigned)__builtin_amdgcn_s_memrealtime() - c.t0 < (unsigned)ticks) __builtin_amdgcn_s_sleep(4);
}
DEV int u_slot(int n, int par, int para) { return n == 2 ? R_U2 + par * 8192 : R_U + n * MB + para * 8192; }
DEV unsigned ask_stamps(Ctx& c, int par, int slot) { return __builtin_amdgcn_raw_buffer_load_b32(c.rs, amap(24576, par) + fresh_lane() * 128, slot, AUX_SC1); }
DEV unsigned latest(Ctx& c, const Clk& k, int par, int slot, unsigned v) {
  if (__builtin_expect(!allok(v), 0)) {
#pragma unroll 1
    do {
      __builtin_amdgcn_s_sleep(T_POLL);
      asm volatile("" ::: "memory");
      const unsigned t = __builtin_amdgcn_raw_buffer_load_b32(c.rs, v == SENT ? amap(24576, par) + fresh_lane() * 128 : 1 << 30, slot, AUX_SC1);
      v = v == SENT ? t : v;
    } while (!allok(v) && !spend(c));
  }
  int d = (int)(v - k.g);
  d = max(d, __builtin_amdgcn_update_dpp(d, d, 0x128, 0xf, 0xf, true));
  d = max(d, __builtin_amdgcn_update_dpp(d, d, 0x124, 0xf, 0xf, true));
  d = max(d, __builtin_amdgcn_update_dpp(d, d, 0x122, 0xf, 0xf, true));
  d = max(d, __builtin_amdgcn_update_dpp(d, d, 0x121, 0xf, 0xf, true));
  unsigned m;
  asm("s_max_i32 %0, %1, %2\n\ts_max_i32 %0, %0, %3\n\ts_max_i32 %0, %0, %4" : "=&s"(m)
      : "s"(__builtin_amdgcn_readlane(d, 0)), "s"(__builtin_amdgcn_readlane(d, 16)), "s"(__builtin_amdgcn_readlane(d, 32)), "s"(__builtin_amdgcn_readlane(d, 48)) : "scc");
  return k.g + m;
}
struct Plan { unsigned gx, dx; };
DEV unsigned smax(unsigned a, unsigned b) { return (int)(a - b) > 0 ? a : b; }
DEV unsigned second_try(const Plan& t, unsigned last) { return smax(last + NS(T_PEN), t.gx + t.dx + NS(T_M1 + T_RETRY)); }
DEV void advance(Plan& t, unsigned last) {
  const unsigned gy = t.gx + t.dx + NS(T_M1), dur = last - t.gx;
  const bool missed = (int)(last + NS(T_MIN) - gy) > 0;
  const unsigned nx = (missed ? second_try(t, last) : gy) + NS(T_DY);
  t.dx = dur < t.dx + NS(60) ? (3u * t.dx + dur) >> 2 : t.dx + NS(8);
  t.gx = nx;
}
DEV unsigned next_go(Ctx& c, Clk& k, Plan& t, int par, int slot, unsigned v) {
  SCHED();
  anchor(k);
  SCHED();
  advance(t, latest(c, k, par, slot, v));
  return to_cyc(k, t.gx);
}

DEV constexpr int qk_r(int j) { return j < 4 ? 0 : 1 + (j - 4) % 3; }
DEV constexpr int qk_i(int j) { return j < 4 ? j : (j - 4) / 3; }
DEV constexpr int qk_upto(int u) { return u < 6 ? u + 7 : u < 10 ? 12 + (u - 4) / 2 : u < 12 ? 15 : 16; }
DEV void qk_role(const Params& p, Ctx& c, int h) {
  constexpr int WL = 0, MEMS = 131072, KXL = 147456, QKX = 155648, QROW = 272;
  c.budget = SPIN_CAP;
  const int w = c.w;
  Clk k = {0u, 0u, 0u, 0u};
  anchor(k);
  v8u wa[32], wh[16];
  claim(c, 0);
  hold(c, QK_AT - (slow_head(p.Wq, h) ? SLOW_LEAD : 0));
  {
    const int lane = fresh_lane(), g = lane >> 4, cc = lane & 15;
    const rsrc_t rq = mkrs(p.Wq, 1024 * 4096 * 2), rk = mkrs(p.Wk, 1024 * 4096 * 2);
    const int vo0 = opq((256 * w + 64 * (h & 1) + krow_lane(g)) * 8192 + (h * 128 + 8 * cc) * 2), vo1 = opq(vo0 ^ (64 * 8192));
    v4u row[16][8];
    auto request = [&](int u) {
#pragma unroll
      for (int r = 0; r < 8; ++r) row[u][r] = ld16w(u < 8 ? rq : rk, (u & 2) ? vo1 : vo0, (32 * (u & 4) + krow(4 * (u & 1) + (r >> 1), r & 1)) * 8192);
    };
#pragma unroll
    for (int u = 0; u < qk_upto(-1); ++u) request(u);
    SCHED();
    const int sw = MEMS + w * 8192 + g * 256, lw = opq(WL + w * 32768 + lane * 16);
    int sr[2];
#pragma unroll
    for (int t = 0; t < 2; ++t) { const int i = (2 * cc + t) & 7; sr[t] = opq(i * 1024 + g * 256 + ((4 * w + (cc >> 2) + 2 * i) & 15) * 16); }
    sfor<16>([&](auto U) __attribute__((always_inline)) {
      constexpr int u = U.value;
#pragma unroll
      for (int i = 0; i < 8; ++i) {
        v4u f;
#pragma unroll
        for (int r = 0; r < 4; ++r) f[r] = prm(row[u][2 * r + 1][i >> 1], row[u][2 * r][i >> 1], i & 1);
        sts16(c.L, sw + i * 1024 + ((cc + 2 * i) & 15) * 16, f);
      }
      SCHED();
      if (qk_upto(u) > qk_upto(u - 1)) request(qk_upto(u - 1));
      BAR();
      v4u in[4][2];
#pragma unroll
      for (int t = 1; t >= 0; --t)
#pragma unroll
        for (int r = 0; r < 4; ++r) in[r][t] = lds16(c.L, sr[t] + MEMS + ((w + r) & 3) * 8192);
      SCHED();
#pragma unroll
      for (int j = 0; j < 16; ++j) {
        if (qk_i(j) != ((u >> 1) & 3)) continue;
#pragma unroll
        for (int t = 1; t >= 0; --t) {
          const v4u o = in[qk_r(j)][t];
          if (u >= 8 && t == 1) sts16(c.L, lw + j * 2048 + (u & 1) * 1024, o);
          else {
            v8u& d = u < 8 ? wa[2 * j + t] : wh[j];
#pragma unroll
            for (int r = 0; r < 4; ++r) d[4 * (u & 1) + r] = o[r];
#define M_(r) asm volatile("" : "+" r(d))
            if (u & 1) { if (u < 8) AT_ACC(M_, 2 * j + t); else AT_HOME(M_, j); }
#undef M_
          }
        }
      }
      BAR();
    });
  }
  STAMP(0);
  BAR();
  const unsigned vote = wait_verdict(c);
  const int par = vote_b(vote), para = vote_a(vote);
  const int lane = fresh_lane(), g = lane >> 4, cc = lane & 15;
  const int moff = opq(amap(w * 4096 + lane * 16, par) + (h & 1) * 1024);
  const int idx = opq((lane & 1) ? 0xEEEE : 0x4444);
  TICK_DECL;
  const int l0 = opq(WL + w * 32768 + lane * 16), l1 = opq(MEMS + lane * 16);
  const int qw = opq(QKX + (2 * w + (cc >> 3)) * 256 + g * 64 + (cc & 1) * 16 + ((cc >> 1) & 3) * 4), kw = opq(QKX + (8 + 2 * g) * QROW + (32 * w + 2 * cc) * 2);
#define QK_PINS PIN_ACC0(wa); PIN_ACC1(wa); PIN_HOME(wh)
#define QK_IN [l0] "v"(l0), [l1] "v"(l1), [mo] "v"(moff), [ix] "v"(idx), [qw] "v"(qw), [kw] "v"(kw), [rs] "s"(c.rs), [sb] "s"(R_MEM + (s & 3) * MEM_SLOT)
#define QK_CL CL_S, CL_16_32, CL_160_200, CL_200_216, CL_216_256
  if (w == 3) {
    const int hq = opq(lane ? 1 << 30 : amap(24576 + h * 128, par));
    anchor(k);
    unsigned tg = k.cyc;
    Plan tt = {k.g - NS(100000), NS(T_DX)};
    for (int s = 0; s < NSEG; ++s) {
      const int pb = R_P + (s & 3) * P_SLOT;
      QK_PINS;
      asm volatile(QK_STEP3 : [bd] "+s"(c.budget) : QK_IN, [hq] "v"(hq), [pb] "s"(pb), [tg] "s"(tg), [ca] "s"(k.cyc - T_DQ), [k1] "s"(k.k1), [ga] "s"(k.g) : QK_CL);
      QK_PINS;
      tg = next_go(c, k, tt, par, pb, ask_stamps(c, par, pb));
      st4s(c.rs, hq, R_P + ((s + 2) & 3) * P_SLOT, fresh_sent());
      st4s(mkrs((const void*)c.ctrl, CTRL_BYTES), hq & (1 << 30), C_FLAG + (F_S + h) * 128, (unsigned)s + 1u);
      TICK_SUM(10);
    }
  } else {
    const int kx0 = opq(KXL + g * 512 + cc * 16), kmrd = opq(QKX + g * 16 + (8 + (cc & 7)) * QROW);
    unsigned seen = NOFLAG;
    if (w == 0) {
      const int prow = (2 * g + (cc & 1)) * 2 + ((cc >> 1) & 1);
      const int px = opq(amap((h >> 1) * 1024 + ((2 * (h & 1) + (cc >> 3)) * 16 + prow) * 16 + ((cc >> 2) & 1) * 8, par));
      const int pm = opq(cc < 8 ? amap(16384 + h * 128 + prow * 8 + (cc >> 2) * 4, par) : 1 << 30);
      const int ps = opq((cc & 1) ? 0x03020706 : 0x05040100);
      for (int s = 0; s < NSEG; ++s) {
        if (seen < (unsigned)s + 1u) seen = wait_flag(c, F_K + h, s + 1);
        QK_PINS;
        asm volatile(QK_STEP0 : [bd] "+s"(c.budget)
                     : QK_IN, [kx] "v"(kx0), [km] "v"(kmrd), [px] "v"(px), [pm] "v"(pm), [ps] "v"(ps), [pb] "s"(R_P + (s & 3) * P_SLOT), [pr] "s"(R_P + ((s + 1) & 3) * P_SLOT),
                       [kb] "s"(R_KX - MEMS + xblk(s, h, para)) : QK_CL);
        QK_PINS;
        TICK_SUM(10);
      }
    } else {
      const int qt = w - 1;
      const int px = opq(amap(qt ? 16384 + h * 1024 + lane * 16 : h * 512 + (g >> 1) * 256 + cc * 16 + (g & 1) * 8, para));
      const int pm = opq(g < 2 ? amap(49152 + (qt * 32 + h) * 256 + cc * 16 + g * 8, para) : 1 << 30);
      const int ps = opq(cc - 4 * g), hq = opq(g * 512 + (16 * qt + cc) * 16);
#define QK_TOK(STEP) for (int s = 0; s < NSEG; ++s) { QK_PINS; \
        asm volatile(STEP : [bd] "+s"(c.budget), [sq] "+s"(seen) \
                     : QK_IN, [kx] "v"(kx0), [km] "v"(kmrd), [px] "v"(px), [pm] "v"(pm), [ps] "v"(ps), [hq] "v"(hq), [qb] "s"(R_QX + xblk(s, h, para)), [tb] "s"(R_PT + (s & 7) * PT_SLOT), \
                       [tr] "s"(R_PT + ((s + 3) & 7) * PT_SLOT), [sn] "s"(s + 1), [fq] "s"(c.ctrl + (C_FLAG + (F_Q + h) * 128) / 4) : QK_CL); \
        QK_PINS; TICK_SUM(10); }
      if (w == 1) { QK_TOK(QK_STEP1) } else { QK_TOK(QK_STEP2) }
    }
  }
  TICK_OUT();
  if (c.budget == 0u) __hip_atomic_store(c.ctrl + C_TMO / 4, 1u, RLX_AGENT);
}

DEV void shuffle_cols(Ctx& c, int scratch, const v4u (&rows)[8], v4u (&o)[8]) {
  const int g = c.lane >> 4, cc = c.lane & 15;
#pragma unroll
  for (int i = 0; i < 8; ++i) {
    v4u f;
#pragma unroll
    for (int r = 0; r < 4; ++r) f[r] = prm(rows[2 * r + 1][i >> 1], rows[2 * r][i >> 1], i & 1);
    sts16(c.L, scratch + (8 * cc + i) * 64 + ((g ^ (cc & 3)) << 4), f);
  }
  asm volatile("s_waitcnt lgkmcnt(0)" ::: "memory");
#pragma unroll
  for (int i = 0; i < 8; ++i) { const int col = 16 * i + cc; o[i] = lds16(c.L, scratch + col * 64 + ((g ^ ((col >> 3) & 3)) << 4)); }
  asm volatile("s_waitcnt lgkmcnt(0)" ::: "memory");
}

DEV void vo_role(const Params& p, Ctx& c, int h) {
  constexpr int WL = 0, MEMS = 131072, VM = 147456, VROW = 272;
  c.budget = SPIN_CAP;
  const int w = c.w, lane = c.lane = fresh_lane(), g = lane >> 4, cc = lane & 15;
  Clk k = {0u, 0u, 0u, 0u};
  anchor(k);
  v8u wa[32], wh[16];
  claim(c, 0);
  hold(c, VO_AT);
  const rsrc_t rwo = mkrs(p.Wo, 4096 * 1024 * 2);
  const int voo = (h * 128 + 4 * g) * 2048 + (256 * w + 8 * cc) * 2;
  v4u buf[3][8];
  auto request_o = [&](int u) {
#pragma unroll
    for (int r = 0; r < 8; ++r) buf[u % 3][r] = ld16w(rwo, voo, (32 * (u & 3) + 16 * (r >> 2) + (r & 3)) * 2048 + (u >> 2) * 256);
  };
  {
    const rsrc_t rw = mkrs(p.Wv, 1024 * 4096 * 2);
    const int vo = (256 * w + krow_lane(g)) * 8192 + (h * 128 + 8 * cc) * 2;
    v4u vrow[8][8];
    auto request = [&](int u) {
#pragma unroll
      for (int r = 0; r < 8; ++r) vrow[u][r] = ld16w(rw, vo, (64 * ((u >> 1) ^ (h & 1)) + krow(4 * (u & 1) + (r >> 1), r & 1)) * 8192);
    };
#pragma unroll
    for (int u = 0; u < 5; ++u) request(u);
    SCHED();
    const int wr_ = MEMS + w * 8192 + g * 256;
    int sr[2];
#pragma unroll
    for (int t = 0; t < 2; ++t) { const int i = (2 * cc + t) & 7; sr[t] = opq(i * 1024 + g * 256 + ((4 * w + (cc >> 2) + 2 * i) & 15) * 16); }
#pragma unroll
    for (int u = 0; u < 8; ++u) {
#pragma unroll
      for (int i = 0; i < 8; ++i) {
        v4u f;
#pragma unroll
        for (int r = 0; r < 4; ++r) f[r] = prm(vrow[u][2 * r + 1][i >> 1], vrow[u][2 * r][i >> 1], i & 1);
        sts16(c.L, wr_ + i * 1024 + ((cc + 2 * i) & 15) * 16, f);
      }
      SCHED();
      if (u + 5 < 8) request(u + 5); else if (u < 6) request_o(u - 3);
      BAR();
      v4u in[4][2];
#pragma unroll
      for (int t = 1; t >= 0; --t)
#pragma unroll
        for (int r = 0; r < 4; ++r) in[r][t] = lds16(c.L, sr[t] + MEMS + ((w + r) & 3) * 8192);
      SCHED();
#pragma unroll
      for (int j = 0; j < 16; ++j) {
        if (qk_i(j) != (u >> 1)) continue;
#pragma unroll
        for (int t = 1; t >= 0; --t) {
          const v4u o = in[qk_r(j)][t];
          if (t == 1 && (j & 1)) sts16(c.L, WL + (w * 16 + (j >> 1)) * 2048 + (u & 1) * 1024 + lane * 16, o);
          else {
            const int n = 3 * (j >> 1) + ((j & 1) ? 2 : t);
#pragma unroll
            for (int r = 0; r < 4; ++r) wa[n][4 * (u & 1) + r] = o[r];
#define M_(r) asm volatile("" : "+" r(wa[n]))
            if (u & 1) AT_ACC(M_, n);
#undef M_
          }
        }
      }
      BAR();
    }
  }
#pragma unroll
  for (int u = 0; u < 8; ++u) {
    v4u o[8];
    shuffle_cols(c, MEMS + w * 8192, buf[u % 3], o);
    SCHED();
    if (u + 3 < 8) request_o(u + 3);
#pragma unroll
    for (int i = 0; i < 4; ++i) {
      const int pr = 4 * (u >> 2) + i, ch = u & 3;
      const v8u f = {o[2 * i][0], o[2 * i + 1][0], o[2 * i][1], o[2 * i + 1][1], o[2 * i][2], o[2 * i + 1][2], o[2 * i][3], o[2 * i + 1][3]};
      if (ch < 2) {
        wh[2 * pr + ch] = f;
#define M_(r) asm volatile("" : "+" r(wh[2 * pr + ch]))
        AT_HOME(M_, 2 * pr + ch);
#undef M_
      } else if (ch == 2) {
        wa[24 + pr] = f;
#define M_(r) asm volatile("" : "+" r(wa[24 + pr]))
        AT_ACC(M_, 24 + pr);
#undef M_
      } else {
        sts16(c.L, WL + (w * 16 + 8 + pr) * 2048 + lane * 16, lo4(f));
        sts16(c.L, WL + (w * 16 + 8 + pr) * 2048 + 1024 + lane * 16, hi4(f));
      }
    }
    SCHED();
  }
  BAR();
  STAMP(0);
  const unsigned vote = wait_verdict(c);
  const int par = vote_b(vote), para = vote_a(vote);
  const int moff = opq(amap(w * 4096 + lane * 16, par) + (h & 1) * 1024);
  const int idx = opq((lane & 1) ? 0xEEEE : 0x4444);
  const int u_off = opq((16 * w + g) * 16384 + h * 256 + cc * 16);
  const int hq = opq((lane | w) ? 1 << 30 : amap(24576 + (32 + h) * 128, par));
  const int l0 = opq(WL + w * 32768 + lane * 16), l1 = opq(MEMS + lane * 16);
  const int vmw = opq(VM + 2 * g * VROW + (32 * w + 2 * cc) * 2);
  const int vmr = opq(VM + (cc >> 1) * VROW + g * 16);
  TICK_DECL;
  anchor(k);
  unsigned tg = k.cyc;
  Plan tt = {k.g - NS(100000), NS(T_DX)};
  int u3 = 0;
  for (int s = 0; s < NSEG; ++s) {
    const int pb = R_P + (s & 3) * P_SLOT;
    PIN_ACC0(wa); PIN_ACC1(wa); PIN_HOME(wh);
    asm volatile(VO_STEP
        : [bd] "+s"(c.budget)
        : [l0] "v"(l0), [l1] "v"(l1), [mo] "v"(moff), [ix] "v"(idx), [vw] "v"(vmw), [vr] "v"(vmr), [uo] "v"(u_off), [w] "s"(w), [rs] "s"(c.rs),
          [sb] "s"(R_MEM + (s & 3) * MEM_SLOT), [ub] "s"(u_slot(u3, par, para)), [tg] "s"(tg), [hq] "v"(hq), [pb] "s"(pb),
          [ca] "s"(k.cyc - T_DV), [k1] "s"(k.k1), [ga] "s"(k.g)
        : CL_S, CL_16_32, CL_160_200, CL_200_216, CL_216_256);
    PIN_ACC0(wa); PIN_ACC1(wa); PIN_HOME(wh);
    if (w == 0) tg = next_go(c, k, tt, par, pb, ask_stamps(c, par, pb)); else __builtin_amdgcn_s_sleep(T_RST);
    {
      const int ur = u_slot(u3 ? u3 - 1 : 2, par, para);
      const v4u se = {fresh_sent(), fresh_sent(), fresh_sent(), fresh_sent()};
#pragma unroll
      for (int i = 0; i < 4; ++i) st16s(c.rs, u_off, ur + i * 65536, se);
      st4s(c.rs, hq, R_P + ((s + 2) & 3) * P_SLOT, se[0]);
      u3 = u3 == 2 ? 0 : u3 + 1;
    }
    TICK_SUM(10);
  }
  TICK_OUT();
  if (c.budget == 0u) __hip_atomic_store(c.ctrl + C_TMO / 4, 1u, RLX_AGENT);
}

DEV void y_role(const Params& p, Ctx& c, int y) {
  constexpr int ZR = 0, UL = 131072, CW = 155648;
  c.budget = SPIN_CAP;
  const int w = c.w, lane = c.lane = fresh_lane(), g = lane >> 4, cc = lane & 15;
  const v4u sent4 = {SENT, SENT, SENT, SENT};
  STAMP(2);
  Clk k = {0u, 0u, 0u, 0u};
  anchor(k);
  unsigned m0 = 0;
  if (w == 0) {
    unsigned best[2] = {~0u, ~0u};
    st4s(c.rs, y * 16384 + (lane >> 5) * 8192 + (lane & 31) * 4, R_U, SENT);
#pragma unroll 1
    for (int n = 0; n < 12; ++n) {
#pragma unroll
      for (int r = 0; r < 2; ++r) {
        VMWAIT(0);
        const unsigned t0 = (unsigned)__builtin_amdgcn_s_memtime();
        LGKM(0);
        st4s(c.rs, y * 16384 + (lane & 31) * 4, R_U + r * 8192, SENT);
        VMWAIT(0);
        const unsigned t1 = (unsigned)__builtin_amdgcn_s_memtime();
        LGKM(0);
        best[r] = min(best[r], t1 - t0);
      }
      if (n + 1 >= PROBES && min(best[0], best[1]) < 600u && max(best[0], best[1]) > min(best[0], best[1]) + 128u) break;
    }
    STAMP(5);
    claim(c, CL_VOTED | (best[1] < best[0] ? CL_A : 0u));
    m0 = *(const GAS unsigned*)(p.mem0 + (lane >> 3) * 1024 + 16 * y + 2 * (lane & 7));
    m0 = m0 < SENT - 1u ? m0 : SENT - 1u;
    if (lane < 8) *(LAS unsigned*)(c.L + CW + lane * 4) = 0u;
    const unsigned v = wait_vote(c);
    STAMP(7);
    const int oa = y * 16384 + vote_a(v) * 8192 + lane * 16, ob = amap(y * 3072 + lane * 16, vote_b(v));
#pragma unroll
    for (int i = 0; i < 16; ++i) st16s(c.rs, oa + (i & 3) * 1024, R_U + (i >> 3) * MB + ((i >> 2) & 1) * 4096, sent4);
#pragma unroll
    for (int i = 0; i < 3; ++i) st16s(c.rs, amap(y * 3072 + i * 1024 + lane * 16, vote_b(v)), R_MEM, sent4);
#pragma unroll
    for (int i = 0; i < 8; ++i) st16s(c.rs, oa + (i & 3) * 1024, R_PT + (i >> 2) * 4096, sent4);
    (void)ob;
    VMWAIT(0);
    if (lane == 0) __hip_atomic_fetch_add(c.ctrl + (C_CLAIM + c.att * 1024) / 4 + c.slot, CL_FILLED, RLX_AGENT);
    STAMP(6);
  }
  BAR();
  const unsigned vote = wait_verdict(c);
  const int par = vote_b(vote), para = vote_a(vote);
  const int mo = amap(y * 256 + lane * 4, par);
  if (w == 0) st4s(c.rs, mo, R_MEM, m0);
  STAMP(0);
  volatile LAS unsigned* const GO = (volatile LAS unsigned*)(c.L + CW);
  TICK_DECL;
  if (w == 0) {
    int xo[4];
#pragma unroll
    for (int i = 0; i < 4; ++i) xo[i] = amap(i * 4096 + lane * 16, par);
    const int pmo = amap(16384 + 2 * g * 128 + cc * 8, par), psel = (cc & 1) ? 0x03020706 : 0x05040100;
    const int lead = T_U0 - 128 * ((y >> 2) & 3), late = __builtin_amdgcn_readfirstlane(opq(lead < 264));
    anchor(k);
    Plan tt = {k.g - NS(100000), NS(T_DX)};
    unsigned done = k.cyc;
    const int so = amap(24576, par) + lane * 128;
    const int idx = opq((lane & 1) ? 0xEEEE : 0x4444);
    const int uo = y * 16384 + lane * 16;
    int u3 = 0;
    const int mw = amap(y * 256 + (2 * g + (cc & 1)) * 32 + (cc >> 1) * 4, par);
    for (int s = 0; s < NSEG - 1; ++s) {
      if (s == 0) RAWBAR();
      v32u z0, z1, z2, z3;
      asm volatile(
          "ds_read_b128 a[0:3], %[za] offset:0\n\t" "ds_read_b128 a[4:7], %[za] offset:1024\n\t" "ds_read_b128 a[8:11], %[za] offset:2048\n\t" "ds_read_b128 a[12:15], %[za] offset:3072\n\t"
          "ds_read_b128 a[16:19], %[za] offset:4096\n\t" "ds_read_b128 a[20:23], %[za] offset:5120\n\t" "ds_read_b128 a[24:27], %[za] offset:6144\n\t" "ds_read_b128 a[28:31], %[za] offset:7168\n\t"
          "ds_read_b128 a[32:35], %[za] offset:8192\n\t" "ds_read_b128 a[36:39], %[za] offset:9216\n\t" "ds_read_b128 a[40:43], %[za] offset:10240\n\t" "ds_read_b128 a[44:47], %[za] offset:11264\n\t"
          "ds_read_b128 a[48:51], %[za] offset:12288\n\t" "ds_read_b128 a[52:55], %[za] offset:13312\n\t" "ds_read_b128 a[56:59], %[za] offset:14336\n\t" "ds_read_b128 a[60:63], %[za] offset:15360\n\t"
          "ds_read_b128 a[64:67], %[za] offset:16384\n\t" "ds_read_b128 a[68:71], %[za] offset:17408\n\t" "ds_read_b128 a[72:75], %[za] offset:18432\n\t" "ds_read_b128 a[76:79], %[za] offset:19456\n\t"
          "ds_read_b128 a[80:83], %[za] offset:20480\n\t" "ds_read_b128 a[84:87], %[za] offset:21504\n\t" "ds_read_b128 a[88:91], %[za] offset:22528\n\t" "ds_read_b128 a[92:95], %[za] offset:23552\n\t"
          "ds_read_b128 a[96:99], %[za] offset:24576\n\t" "ds_read_b128 a[100:103], %[za] offset:25600\n\t" "ds_read_b128 a[104:107], %[za] offset:26624\n\t" "ds_read_b128 a[108:111], %[za] offset:27648\n\t"
          "ds_read_b128 a[112:115], %[za] offset:28672\n\t" "ds_read_b128 a[116:119], %[za] offset:29696\n\t" "ds_read_b128 a[120:123], %[za] offset:30720\n\t" "ds_read_b128 a[124:127], %[za] offset:31744\n\t"
          "s_waitcnt lgkmcnt(0)"
          : "={a[0:31]}"(z0), "={a[32:63]}"(z1), "={a[64:95]}"(z2), "={a[96:127]}"(z3) : [za] "v"(ZR + (s & 3) * 32768 + lane * 16) : "memory");
      const int pb = R_P + (s & 3) * P_SLOT;
      v32u uu;
      const int ub = u_slot(u3, par, para);
      TICK(0);
      anchor(k);
      TICK(8);
      unsigned tg = to_cyc(k, tt.gx + tt.dx + NS(T_M1)), lt = late, sv = SENT;
      unsigned bad = (int)(done - to_cyc(k, tt.gx)) > T_OVH;
      TICK(4);
#pragma unroll 1
      for (;;) {
        if (bad) {
          const bool all = allok(sv);
          const unsigned last = latest(c, k, par, pb, sv);
          if (!all) anchor(k);
          tg = to_cyc(k, second_try(tt, last));
          lt = 1;
        }
        asm volatile(Y_STEP
          : "=&{v[144:175]}"(uu), [bad] "=&s"(bad), [sv] "=&v"(sv)
          : [x0] "v"(xo[0]), [x1] "v"(xo[1]), [x2] "v"(xo[2]), [x3] "v"(xo[3]), [pm] "v"(pmo), [u0] "v"(uo), [u1] "v"(uo + 4096), [mw] "v"(mw), [ps] "v"(psel), [ix] "v"(idx),
            [rs] "s"(c.rs), [pb] "s"(pb), [ub] "s"(ub), [mb] "s"(R_MEM + ((s + 1) & 3) * MEM_SLOT), [mr] "s"(R_MEM + ((s + 3) & 3) * MEM_SLOT), [tu] "s"(tg - (lt ? 128 : lead)), [tg] "s"(tg), [late] "s"(lt),
            [so] "v"(so), [bd] "s"(c.budget),
            "{a[0:31]}"(z0), "{a[32:63]}"(z1), "{a[64:95]}"(z2), "{a[96:127]}"(z3)
          : CL_S, CL_120_128, CL_176_200, CL_200_216, CL_216_256);
        bad = __builtin_amdgcn_readfirstlane(bad);
        if (bad != 1u) break;
        spend(c);
      }
      done = bad;
      SCHED();
      const int ul = opq(UL + lane * 16);
      u3 = u3 == 2 ? 0 : u3 + 1;
#pragma unroll
      for (int i = 0; i < 8; ++i) sts16(c.L, ul + i * 1024, v4u{uu[4 * i], uu[4 * i + 1], uu[4 * i + 2], uu[4 * i + 3]});
      asm volatile("" ::: "memory");
      *GO = s + 1;
      asm volatile("s_waitcnt vmcnt(2)" : "+v"(sv));
      advance(tt, latest(c, k, par, pb, sv));
      TICK(3);
      TICK_LDS(CW + 256);
    }
    {
      anchor(k);
      wait_cyc(to_cyc(k, tt.gx + tt.dx + NS(T_M1)));
      const int ub = u_slot(u3, par, para);
      v4u u[8];
#pragma unroll 1
      for (;;) {
        asm volatile("" ::: "memory");
#pragma unroll
        for (int i = 0; i < 8; ++i) u[i] = ld16s(c.rs, uo + (i & 3) * 1024, ub + (i >> 2) * 4096);
        SCHED();
        unsigned t = 0;
#pragma unroll
        for (int i = 0; i < 8; ++i) t = umax(t, top(u[i]));
        if (allok(t) || spend(c)) break;
      }
      RAWBAR();
#pragma unroll
      for (int i = 0; i < 8; ++i) sts16(c.L, UL + i * 1024 + lane * 16, u[i]);
      asm volatile("" ::: "memory");
      *GO = NSEG;
    }
    TICK_DUMP(CW + 256);
  } else if (w < 3) {
    const rsrc_t ro = mkrs(p.out, 2048 * 2048 * 2);
    auto tokens = [&](auto QT) {
      constexpr int qt = QT.value, NX = qt ? 32 : 16;
      RAWBAR();
      for (int s = 0; s < NSEG; ++s) {
        const int tb = R_PT + (s & 7) * PT_SLOT + para * 8192;
        const int ua = opq(UL + lane * 16);
        const int za = opq(ZR + (s & 3) * 32768 + (qt ? lane * 16 : (g >> 1) * 1024 + (g & 1) * 512 + cc * 16));
        SCHED();
        RAWBAR();
        TICK(0);
        f32x4 a;
#pragma unroll 1
        for (;;) {
          asm volatile("" ::: "memory");
          v4u pt[NX + 8];
#pragma unroll
          for (int i = 0; i < NX; ++i) pt[i] = ld16s(c.rs, (i & 3) * 1024 + lane * 16, tb + amap(qt * 16384 + (i >> 2) * 4096, 0));
#pragma unroll
          for (int i = 0; i < 8; ++i) pt[NX + i] = ld16s(c.rs, (i & 3) * 1024 + lane * 16, tb + amap(49152 + qt * 8192 + (i >> 2) * 4096, 0));
          SCHED();
          TICK(1);
#pragma unroll 1
          while (__builtin_amdgcn_readfirstlane(*GO) < (unsigned)s + 1u) { if (spend(c)) break; }
          asm volatile("" ::: "memory");
          f32x4 d[2] = {f32x4{0.f, 0.f, 0.f, 0.f}, f32x4{0.f, 0.f, 0.f, 0.f}};
          v4u fa[8];
          auto frag = [&](int i) {
            if (i >= NX) fa[i & 7] = lds16(c.L, ua + (i - NX) * 1024);
            else if (qt) fa[i & 7] = lds16(c.L, za + i * 1024);
            else {
              const v2u lo = *(LAS v2u*)(c.L + za + i * 2048), hi = *(LAS v2u*)(c.L + za + i * 2048 + 256);
              fa[i & 7] = v4u{lo.x, lo.y, hi.x, hi.y};
            }
          };
#pragma unroll
          for (int i = 0; i < 7; ++i) frag(i);
          SCHED();
#pragma unroll
          for (int i = 0; i < NX + 8; ++i) {
            if (i + 7 < NX + 8) frag(i + 7);
            d[i & 1] = mfma16(fa[i & 7], pt[i], d[i & 1]);
            SCHED();
          }
          a = d[0] + d[1];
          if (!__any(a[0] != a[0])) break;
          unsigned t = 0;
#pragma unroll
          for (int i = 0; i < NX + 8; ++i) t = umax(t, top(pt[i]));
          if (allok(t) || spend(c)) break;
        }
        __builtin_amdgcn_raw_buffer_store_b64(v2u{pk(a[0], a[1]), pk(a[2], a[3])}, ro, (16 * qt + cc) * 4096 + (16 * y + 4 * g) * 2, s * 32 * 4096, AUX_NT);
        TICK(2);
        TICK_SUM(3);
      }
    };
    if (w == 1) tokens(IC<0>{}); else tokens(IC<1>{});
  } else {
    unsigned seenZ = NOFLAG;
    const rsrc_t rc = mkrs((const void*)c.ctrl, CTRL_BYTES);
    const int fo = lane < 32 ? lane * 128 : 1 << 30;
    __builtin_amdgcn_s_setprio(3);
    auto least = [&](unsigned v) {
      v |= lane < 32 ? 0u : ~0u;
#pragma unroll
      for (int o = 1; o < 32; o <<= 1) v = min(v, (unsigned)__shfl_xor((int)v, o));
      seenZ = umax(seenZ, __builtin_amdgcn_readfirstlane(v));
    };
    auto await = [&](unsigned need) {
#pragma unroll 1
      while (seenZ < need) {
        asm volatile("" ::: "memory");
        least(__builtin_amdgcn_raw_buffer_load_b32(rc, fo, C_FLAG + (F_Z + (y >> 4) * 32) * 128, AUX_SC1));
        if (seenZ >= need || spend(c)) break;
        __builtin_amdgcn_s_sleep(4);
      }
      __builtin_amdgcn_fence(__ATOMIC_ACQUIRE, "wavefront");
    };
    for (int n = 0; n < NSEG; ++n) {
      TICK(0);
      await(n + 1);
      TICK(1);
      v4u z[32];
#pragma unroll
      for (int i = 0; i < 32; ++i) {
        z[i] = ld16s(c.rs, (i & 3) * 1024 + lane * 16, R_Z + (n & (ZRING - 1)) * Z_SLOT + y * 65536 + ((para ^ (y >> 5)) & 1) * 8192 + amap((i >> 2) * 4096, 0));
        SCHED();
        asm volatile("s_nop " STR(Z_GAP));
        SCHED();
      }
      const unsigned look = __builtin_amdgcn_raw_buffer_load_b32(rc, seenZ < (unsigned)n + 2u ? fo : 1 << 30, C_FLAG + (F_Z + (y >> 4) * 32) * 128, AUX_SC1);
      SCHED();
      TICK(4);
      const int za = opq(ZR + (n & 3) * 32768 + lane * 16);
#pragma unroll
      for (int i = 0; i < 32; ++i) {
        sts16(c.L, za + i * 1024, z[i]);
        if (i == 30) { TICK(3); }
      }
      least(look);
      if (n == 0) await(PRIME);
      TICK(2);
      { const int s = n; (void)s; TICK_SUM(3); }
      BAR();
    }
    RAWBAR();
  }
  TICK_OUT();
  if (c.budget == 0u) __hip_atomic_store(c.ctrl + C_TMO / 4, 1u, RLX_AGENT);
}

#define MF1A(d, a, b) asm volatile(MF16 "%0, %1, %2, %0" : "+v"(d) : "a"(a), "v"(b))
#define MF32A(d, a, b) asm volatile("v_mfma_f32_32x32x16_bf16 %0, %1, %2, %0" : "+v"(d) : "a"(a), "v"(b))
#define MF32AZ(d, a, b) asm volatile("v_mfma_f32_32x32x16_bf16 %0, %1, %2, 0" : "=&v"(d) : "a"(a), "v"(b))
#define MF1B(d, a, b) asm volatile(MF16 "%0, %1, %2, %0" : "+v"(d) : "v"(a), "a"(b))
#define MF1BZ(d, a, b) asm volatile(MF16 "%0, %1, %2, 0" : "=&v"(d) : "v"(a), "a"(b))
#define MF1AZ(d, a, b) asm volatile(MF16 "%0, %1, %2, 0" : "=&v"(d) : "a"(a), "v"(b))
DEV void g_role(const Params& p, Ctx& c, int mat, int h, int gi) {
  constexpr int EX = 131072;
  c.budget = SPIN_CAP;
  const int w = c.w, lane = c.lane = fresh_lane(), g = lane >> 4, cc = lane & 15;
  if (mat < 2) {
    unsigned t = 0;
#pragma unroll
    for (int i = 0; i < 3; ++i) t += __builtin_amdgcn_raw_buffer_load_b32(mkrs(p.Wv, 8 * MB), (256 * w + (lane >> 1)) * 8192 + h * 256 + (lane & 1) * 128, (2 + 2 * i + mat) * 32 * 8192, 0);
    if (mat == 0) {
      hold(c, X_AT);
#pragma unroll
      for (int i = 0; i < 4; ++i) t += __builtin_amdgcn_raw_buffer_load_b32(mkrs(p.x, 4 * MB), ((4 * h + w) * 64 + lane) * 128, i * MB, 0);
    }
    asm volatile("" :: "v"(t));
    claim(c, 0);
    hold(c, (mat ? GK_AT : GQ_AT) - (slow_head(p.Wq, h) ? SLOW_LEAD : 0));
  }
  const rsrc_t rx = mkrs(p.x, 2048 * 2048);
  const int xs = w * 16384 + ((lane * 16) ^ ((w & 1) << 7));

  v32u wa[8];
  {
    const rsrc_t rw = mkrs(mat == 0 ? p.Wq : mat == 1 ? p.Wk : p.Wv, 1024 * 4096 * 2);
    const int vo = (256 * w + 8 * g) * 8192 + (h * 128 + 8 * cc) * 2;
    v4u row[8][8];
    auto request = [&](int u) {
#pragma unroll
      for (int r = 0; r < 8; ++r) row[u][r] = ld16w(rw, vo, (32 * u + r) * 8192);
    };
#pragma unroll
    for (int u = 0; u < 2; ++u) request(u);
#pragma unroll
    for (int u = 0; u < 16; ++u) dma16(rx, c.L + w * 16384 + u * 1024, (xs ^ ((u >> 1) << 4)) + u * 1024);
#pragma unroll
    for (int u = 2; u < 6; ++u) request(u);
    SCHED();
    const int sw_ = EX + w * 8192 + g * 256;
    const int sr0 = opq(EX + (cc & 3) * 1024 + g * 256 + ((4 * w + (cc >> 2) + 4 * cc) & 15) * 16), sr1 = sr0 + 4096;
#pragma unroll
    for (int u = 0; u < 8; ++u) {
#pragma unroll
      for (int i = 0; i < 8; ++i) {
        v4u f;
#pragma unroll
        for (int r = 0; r < 4; ++r) f[r] = prm(row[u][2 * r + 1][i >> 1], row[u][2 * r][i >> 1], i & 1);
        sts16(c.L, sw_ + i * 1024 + ((cc + 4 * i) & 15) * 16, f);
      }
      SCHED();
      if (u + 6 < 8) request(u + 6);
      if (u == 7 && mat == 2) claim(c, 0);
      BAR();
      v8u in[4];
#pragma unroll
      for (int q = 0; q < 4; ++q) in[q] = cat(lds16(c.L, sr0 + q * 8192), lds16(c.L, sr1 + q * 8192));
      SCHED();
#pragma unroll
      for (int q = 0; q < 4; ++q) {
        v8u f = in[q];
#define M_(r) asm volatile("" : "+" r(f))
        AT_ACC(M_, 8 * q + u);
#undef M_
#pragma unroll
        for (int r = 0; r < 8; ++r) wa[2 * q + (u >> 2)][8 * (u & 3) + r] = f[r];
      }
      BAR();
    }
  }
  STAMP(0);
  const int xpar = vote_a(wait_vote(c)) ^ (mat >> 1);
  TICK_DECL;
  asm volatile(G_LOOP
      :: [xo] "v"(cc * 2048 + ((g ^ cc) << 4)), [xs] "v"(xs), [so] "v"((4 * w + g) * 512 + cc * 16), [fo] "v"((w | lane) ? 1 << 30 : 0),
         [rs] "s"(c.rs), [rx] "s"(rx), [rc] "s"(mkrs((const void*)c.ctrl, CTRL_BYTES)), [fl] "s"(C_FLAG + (mat * 32 + h) * 128), [xp] "s"(xpar),
         [ob] "s"((mat == 1 ? R_KX : R_QX) + h * 16384), [lw] "s"(w * 16384), GTRACE ACC_IN(wa)
      : CL_S, CL_16_32, CL_32_100);
}

DEV void zc_role(const Params& p, Ctx& c, int h) {
  c.budget = SPIN_CAP;
  const int w = c.w, lane = c.lane = fresh_lane(), g = lane >> 4, cc = lane & 15, h2 = lane >> 5, r32 = lane & 31;
  v32u wa[8];
  hold(c, ZC_AT);
  {
    const rsrc_t rw = mkrs(p.Wo, 4096 * 1024 * 2);
    const int vo = (h * 128 + 8 * g) * 2048 + (256 * w + 8 * cc) * 2;
    v4u row[8][8];
    auto request = [&](int u) {
#pragma unroll
      for (int r = 0; r < 8; ++r) row[u][r] = ld16w(rw, vo, (32 * (u & 3) + r) * 2048 + (u >> 2) * 256);
    };
#pragma unroll
    for (int u = 0; u < 2; ++u) request(u);
    SCHED();
    claim(c, 0);
    SCHED();
#pragma unroll
    for (int u = 2; u < 6; ++u) request(u);
    const int sw_ = w * 8192 + g * 256, sr_ = w * 8192 + (cc & 7) * 1024 + g * 256;
    v8u two[8];
#pragma unroll
    for (int u = 0; u < 8; ++u) {
#pragma unroll
      for (int i = 0; i < 8; ++i) {
        v4u f;
#pragma unroll
        for (int r = 0; r < 4; ++r) f[r] = prm(row[u][2 * r + 1][i >> 1], row[u][2 * r][i >> 1], i & 1);
        sts16(c.L, sw_ + i * 1024 + ((cc + 2 * i) & 15) * 16, f);
      }
      SCHED();
      if (u + 6 < 8) request(u + 6);
      LGKM(0);
#pragma unroll
      for (int q = 0; q < 8; ++q) {
        const v4u o = lds16(c.L, sr_ + ((2 * q + (cc >> 3) + 2 * cc) & 15) * 16);
#pragma unroll
        for (int r = 0; r < 4; ++r) two[q][4 * (u & 1) + r] = o[r];
      }
      SCHED();
      if (u & 1) {
#pragma unroll
        for (int q = 0; q < 8; ++q) {
          const int n = 2 * (8 * (u >> 2) + q) + ((u >> 1) & 1);
#define M_(r) asm volatile("" : "+" r(two[q]))
          AT_ACC(M_, n);
#undef M_
#pragma unroll
          for (int r = 0; r < 8; ++r) wa[n >> 2][8 * (n & 3) + r] = two[q][r];
        }
      }
      LGKM(0);
      SCHED();
    }
  }
  STAMP(0);
  BAR();
  const int para = vote_a(wait_vote(c));
  const int ao = g * 512 + cc * 16, zo = 16 * w * 65536 + lane * 16 + ((para ^ (w >> 1)) & 1) * 8192 + amap(h * 1024, 0);
  TICK_DECL;
  asm volatile(ZC_LOOP
      : [bd] "+s"(c.budget)
      : [ao] "v"(ao), [a1] "v"(ao + 4096), [z1] "v"(zo), [z2] "v"(zo + 65536), [fo] "v"(lane ? 1 << 30 : 0),
        [rs] "s"(c.rs), [rc] "s"(mkrs((const void*)c.ctrl, CTRL_BYTES)), [fv] "s"(C_FLAG + (F_V + h) * 128), [fs] "s"(C_FLAG + (F_S + h) * 128),
        [fz] "s"(C_FLAG + (F_Z + w * 32 + h) * 128), [xp] "s"(para ^ 1), [vb] "s"(R_QX + h * 16384), [vd] "s"(C_VERDICT + c.att * 128), [z0] "n"(ZC_PRIME), [zb] "n"(R_Z), [rg] "n"(ZRING - 2), [rm] "n"(ZRING - 1), GTRACE ACC_IN(wa)
      : CL_S, CL_16_32, CL_32_100);
  if (c.budget == 0u) __hip_atomic_store(c.ctrl + C_TMO / 4, 1u, RLX_AGENT);
}

DEV void conv_role(const Params& p, Ctx& c, int cg, int far) {
  constexpr int FS = 4160, PL = 1280, UO = 4 * FS, WS = UO + 8 * PL;
  const int w = c.w, lane = c.lane = fresh_lane(), tid = w * 64 + lane, g = lane >> 4, cc = lane & 15;
  const int d0 = 8 * cg + 2 * w;
  const int pg = ((far ^ (cg >> 5)) & 1) * 8192;
  auto crow = [](int ch) { return (ch >> 1) * 16384 + (ch & 1) * 4096; };
  const int de = (cg >> 3) * 64 + (tid & 7) * 8, t0 = (cg & 7) * 256 + (tid >> 3) * 8;
  auto group_sync = [&](int phase) {
    gu32* cnt = c.ctrl + (C_CONV + (cg >> 3) * 16 + phase * 8) / 4;
    VMWAIT(0);
    BAR();
    if (w == 0) {
      if (lane == 0) __hip_atomic_fetch_add(cnt, 1u, RLX_AGENT);
#pragma unroll 1
      while (__builtin_amdgcn_readfirstlane(__hip_atomic_load(cnt, RLX_AGENT)) < 8u) { if (spend(c)) break; __builtin_amdgcn_s_sleep(2); }
    }
    BAR();
  };
  v4u fr[2][4], ur[2][4], qr[8];
  unsigned up[2][4];
  const int so = cc * 64 + g * 16;
  const unsigned dd = __builtin_amdgcn_raw_buffer_load_b32(mkrs(p.ssmD, 2048), d0 * 2, 0, CONV_AUX);
  BAR();
  {
    const rsrc_t rk = mkrs(p.bk, 2048 * 2048), rv = mkrs(p.bv, 2048 * 2048);
    v4u kr[11], vr[8], sh[4];
#pragma unroll
    for (int i = 0; i < 4; ++i) sh[i] = ld16c(mkrs(p.shift, 8192), de * 8 + i * 16, 0);
#pragma unroll
    for (int i = 0; i < 11; ++i) kr[i] = ld16c(rk, (t0 + i - 3) * 2048 + de * 2, 0);
#pragma unroll
    for (int i = 0; i < 8; ++i) vr[i] = ld16c(rv, (t0 + i) * 2048 + de * 2, 0);
#pragma unroll
    for (int i = 0; i < 8; ++i) fr[i >> 2][i & 3] = ld16c(mkrs(p.ssmk, 4 * MB), lane * 16 + (i & 3) * 1024, (d0 + (i >> 2)) * 4096);
    SCHED();
    if (lane < 16) sts16(c.L, w * WS + (lane >> 2) * FS + (lane & 3) * 16, v4u{0, 0, 0, 0});
#pragma unroll
    for (int i = 0; i < 2; ++i) sts16(c.L, w * WS + UO + (4 * i + g) * PL + cc * 16, v4u{0, 0, 0, 0});
#pragma unroll
    for (int ch = 0; ch < 8; ++ch) {
      const unsigned sa = sh[ch >> 1][(ch & 1) * 2], sb = sh[ch >> 1][(ch & 1) * 2 + 1];
      const float s0 = blo(sa), s1 = bhi(sa), s2 = blo(sb), s3 = bhi(sb);
      float kk[11], o[8];
#pragma unroll
      for (int i = 0; i < 11; ++i) kk[i] = (ch & 1) ? bhi(kr[i][ch >> 1]) : blo(kr[i][ch >> 1]);
#pragma unroll
      for (int i = 0; i < 8; ++i) o[i] = fmaf(kk[i + 3], s0, fmaf(kk[i + 2], s1, fmaf(kk[i + 1], s2, kk[i] * s3))) * ((ch & 1) ? bhi(vr[i][ch >> 1]) : blo(vr[i][ch >> 1]));
      st16s(c.rs, de * 8192 + t0 * 2, R_KVT + pg + crow(ch), v4u{pk(o[0], o[1]), pk(o[2], o[3]), pk(o[4], o[5]), pk(o[6], o[7])});
    }
    group_sync(0);
#pragma unroll
    for (int i = 0; i < 8; ++i) {
      const int b = R_KVT + pg + d0 * 8192 + (i >> 2) * 4096 + (i & 3) * 1024;
      ur[i >> 2][i & 3] = ld16s(c.rs, so, b);
      up[i >> 2][i & 3] = __builtin_amdgcn_raw_buffer_load_b32(c.rs, (i & 3) == 0 && lane == 0 ? 1 << 30 : so, b - 4, AUX_SC1);
    }
#pragma unroll
    for (int i = 0; i < 8; ++i) qr[i] = ld16c(mkrs(p.bq, 4 * MB), (t0 + i) * 2048 + de * 2, 0);
  }
  STAMP(5);
  const int fw = opq(w * WS + 48 + lane * 16), uw = opq(w * WS + UO + g * PL + cc * 16);
  const int fa = opq(w * WS + (cc & 3) * FS + (3 + (cc >> 2) - g) * 16);
  sfor<2>([&](auto CH) __attribute__((always_inline)) {
    constexpr int ch = CH.value;
    SCHED();
#pragma unroll
    for (int i = 0; i < 4; ++i) {
      const v4u f = fr[ch][i], a = ur[ch][i];
      sts16(c.L, fw + 3 * FS + i * 1024, f);
      sts8(c.L, fw + FS + i * 1024 + 8, v2u{f[0], f[1]}); sts8(c.L, fw + FS + i * 1024 + 16, v2u{f[2], f[3]});
#pragma unroll
      for (int r = 0; r < 2; ++r) {
        const int o = fw + 2 * r * FS + i * 1024 + 12 - 8 * r;
        *(LAS unsigned*)(c.L + o) = f[0]; sts8(c.L, o + 4, v2u{f[1], f[2]}); *(LAS unsigned*)(c.L + o + 12) = f[3];
      }
      v4u r0, r1;
#pragma unroll
      for (int r = 0; r < 4; ++r) {
        r0[r] = __builtin_amdgcn_alignbyte(a[3 - r], a[3 - r], 2);
        r1[r] = __builtin_amdgcn_perm(r < 3 ? a[2 - r] : up[ch][i], a[3 - r], 0x07060100u);
      }
      sts16(c.L, uw + (16 + 16 * i) * 16, r0);
      sts16(c.L, uw + 4 * PL + (16 + 16 * i) * 16, r1);
    }
    f32x4 acc[4][2];
#pragma unroll
    for (int i = 0; i < 8; ++i) acc[i >> 1][i & 1] = f32x4{0.f, 0.f, 0.f, 0.f};
    v4u A[2][4], B[2][8];
    auto frag = [&](int d, int n) {
      constexpr int order[12] = {0, 4, 5, 6, 7, 8, 9, 10, 11, 1, 2, 3};
      const int x = order[n];
      if (x < 4) A[d & 1][x] = lds16(c.L, fa + (d + 16 * x) * 64);
      else B[d & 1][x - 4] = lds16(c.L, uw + ((x - 4) & 1) * 4 * PL + (16 + 16 * ((x - 4) >> 1) - d) * 16);
    };
    asm volatile("" ::: "memory");
#pragma unroll
    for (int n = 0; n < 12; ++n) frag(0, n);
    SCHED();
    sfor<16>([&](auto D) __attribute__((always_inline)) {
      constexpr int d = D.value;
      int n = 0;
#pragma unroll
      for (int k = 0; k < 4; ++k)
#pragma unroll
        for (int m = 0; m + k < 4; ++m)
#pragma unroll
          for (int e = 0; e < 2; ++e) {
            acc[m + k][e] = mfma16(A[d & 1][k], B[d & 1][2 * m + e], acc[m + k][e]);
            if (d < 15 && n < 12) frag(d + 1, n);
            ++n;
            SCHED();
          }
    });
    const float dv = ch ? bhi(dd) : blo(dd);
#pragma unroll
    for (int q = 0; q < 4; ++q) {
      v4u z;
#pragma unroll
      for (int i = 0; i < 4; ++i) {
        const unsigned yy = pk(acc[q][1][i], acc[q][0][i]), ud = pk(blo(ur[ch][q][i]) * dv, bhi(ur[ch][q][i]) * dv);
        z[i] = pk(blo(yy) + blo(ud), bhi(yy) + bhi(ud));
      }
      st16s(c.rs, so + q * 1024, R_YT + pg + d0 * 8192 + ch * 4096, z);
    }
  });
  STAMP(6);
  {
    const rsrc_t ro = mkrs(p.out, 2048 * 2048 * 2);
    v4u yv[8];
    group_sync(1);
#pragma unroll
    for (int ch = 0; ch < 8; ++ch) yv[ch] = ld16s(c.rs, de * 8192 + t0 * 2, R_YT + pg + crow(ch));
#pragma unroll
    for (int i = 0; i < 8; ++i) {
      float o[8];
#pragma unroll
      for (int ch = 0; ch < 8; ++ch) o[ch] = ((ch & 1) ? bhi(qr[i][ch >> 1]) : blo(qr[i][ch >> 1])) * ((i & 1) ? bhi(yv[ch][i >> 1]) : blo(yv[ch][i >> 1]));
      __builtin_amdgcn_raw_buffer_store_b128(v4u{pk(o[0], o[1]), pk(o[2], o[3]), pk(o[4], o[5]), pk(o[6], o[7])}, ro, (t0 + i) * 4096 + (1024 + de) * 2, 0, AUX_NT);
    }
  }
  if (c.budget == 0u) __hip_atomic_store(c.ctrl + C_TMO / 4, 1u, RLX_AGENT);
}

extern "C" __global__ void __launch_bounds__(256, 1) mk_mega(Params p) {
  extern __shared__ __attribute__((aligned(16))) unsigned char lds_raw[];
  Ctx c;
  c.ctrl = (gu32*)p.ctrl;
  c.rs = mkrs(p.data, R_END);
  c.L = (lds_t)lds_raw;
  c.w = __builtin_amdgcn_readfirstlane(threadIdx.x >> 6);
  c.lane = threadIdx.x & 63;
  c.t0 = (unsigned)__builtin_amdgcn_s_memrealtime();
  c.b = (int)(blockIdx.x & 255u);
  c.att = (int)(blockIdx.x >> 8);
  c.ask = 0;
  if (c.w == 1) {
    unsigned long long base; unsigned len, t;
    asm volatile("s_getpc_b64 s[60:61]\n1:\n\ts_sub_u32 s60, s60, 1b-mk_mega\n\ts_subb_u32 s61, s61, 0\n\ts_mov_b64 %0, s[60:61]\n\ts_mov_b32 %1, mk_tail-mk_mega"
                 : "=s"(base), "=s"(len) :: "s60", "s61", "scc");
    t = __builtin_amdgcn_raw_buffer_load_b32(mkrs((const void*)base, len), (c.lane * 32 + (c.b >> 3)) * 128, 0, 0);
    asm volatile("" :: "v"(t));
  }
  if (c.w == 0 && c.att == 0) {
    c.ask = __builtin_amdgcn_readfirstlane(__hip_atomic_load(c.ctrl + C_ECHO / 4 + c.b, RLX_AGENT)) + 1u;
    if (c.lane == 0) __hip_atomic_store(c.ctrl + (C_ASK + c.b * 64) / 4, c.ask, RLX_AGENT);
  }
  c.slot = (c.att ? (c.b & 7) : (int)((unsigned)__builtin_amdgcn_s_getreg(20 | (3 << 11)) & 7u)) * 32 + (c.b >> 3);
  if (c.b == SERVER && c.w == 3) serve(c);
  const int x = c.slot >> 5, t = c.slot & 31;
  if (x < 4) {
    if (t < 8) qk_role(p, c, x * 8 + t);
    else if (t < 16) vo_role(p, c, x * 8 + t - 8);
    else y_role(p, c, x * 16 + t - 16);
  } else {
    if (t < 24) g_role(p, c, t >> 3, (x - 4) * 8 + (t & 7), t);
    else zc_role(p, c, (x - 4) * 8 + t - 24);
    STAMP(2);
    c.budget = SPIN_CAP;
    conv_role(p, c, (x - 4) * 32 + t, vote_a(wait_verdict(c)) ^ 1);
  }
  STAMP(3);
}

}

extern "C" void kernel_launch(void* const* d_in, const int* in_sizes, int n_in, void* d_out, int out_size, void* d_ws, size_t ws_size,
                              hipStream_t stream) {
  using namespace mk;
  (void)in_sizes; (void)n_in; (void)out_size; (void)ws_size;
  Params p;
  p.x = (const u16*)d_in[0]; p.mem0 = (const u16*)d_in[1];
  p.Wq = (const u16*)d_in[2]; p.Wk = (const u16*)d_in[3]; p.Wv = (const u16*)d_in[4]; p.Wo = (const u16*)d_in[5];
  p.bq = (const u16*)d_in[7]; p.bk = (const u16*)d_in[8]; p.bv = (const u16*)d_in[9];
  p.shift = (const u16*)d_in[10]; p.ssmk = (const u16*)d_in[11]; p.ssmD = (const u16*)d_in[12];
  p.out = (u16*)d_out;
  p.ctrl = (unsigned char*)d_ws;
  p.data = (unsigned char*)(((size_t)d_ws + CTRL_BYTES + (2u << 20) - 1) & ~(size_t)((2u << 20) - 1));
  (void)hipFuncSetAttribute((const void*)mk_mega, hipFuncAttributeMaxDynamicSharedMemorySize, LDS_BYTES);
  hipLaunchKernelGGL(mk_mega, dim3(256), dim3(256), LDS_BYTES, stream, p);
}
```

```cpp
#include <hip/hip_runtime.h>
#include <hip/hip_bf16.h>

namespace mk {

#define GAS __attribute__((address_space(1)))
#define LAS __attribute__((address_space(3)))
#define DEV __device__ __forceinline__
#define RLX_AGENT __ATOMIC_RELAXED, __HIP_MEMORY_SCOPE_AGENT
typedef unsigned short u16;
typedef unsigned v2u __attribute__((ext_vector_type(2)));
typedef unsigned v4u __attribute__((ext_vector_type(4)));
typedef unsigned v8u __attribute__((ext_vector_type(8)));
typedef unsigned v16u __attribute__((ext_vector_type(16)));
typedef unsigned v32u __attribute__((ext_vector_type(32)));
typedef float f32x2 __attribute__((ext_vector_type(2)));
typedef float f32x4 __attribute__((ext_vector_type(4)));
typedef float f32x16 __attribute__((ext_vector_type(16)));
typedef __bf16 bf16x2 __attribute__((ext_vector_type(2)));
typedef __bf16 bf16x8 __attribute__((ext_vector_type(8)));
typedef __bf16 bf16x16 __attribute__((ext_vector_type(16)));
typedef GAS unsigned gu32;
typedef __amdgpu_buffer_rsrc_t rsrc_t;
typedef LAS unsigned char* lds_t;

#define XCNT(r) ""
#define XT(a, b) ""
#define UCNT ""
#define CL_T
#define GSUM ""
#define ZSUM ""
#define GTRACE
#define MIXED 1
#define NOFLAG 0
#define TOK_KICK 1
#define ZRING 16
#define Z_AT 16
#define Z_GAP 10
#define STR_(x) #x
#define STR(x) STR_(x)
#define GQ_AT 600
#define GK_AT 300
#define PULL_AT 800
#define QK_AT 100
#define SERVER 194
#define SLOW_LEAD 100
#define PULL_DEPTH 1
#define PRIME 2
#define X_AT 150
#define VO_AT 250
#define ZC_AT 0
#define CONV_AUX 2
#define W_AUX 0
#define ZC_PRIME 1
#define CODE_PULL 1
#define PROBES 2
constexpr int NSEG = 64;
constexpr unsigned SENT = 0xFFFFFFFFu;
constexpr int AUX_SC1 = 16, AUX_NT = 2;
constexpr int T_LAG = 61;
constexpr int G_AHEAD = 4;
constexpr unsigned SPIN_CAP = 1u << 21;
constexpr int LDS_BYTES = 163840;

constexpr int C_ECHO = 0, C_CLAIM = 1024, C_VERDICT = 3072, C_TMO = 3328, C_CONV = 3456, C_VOTE = 3840, C_FLAG = 4096, C_ASK = C_FLAG + 256 * 128;
constexpr int F_Q = 0, F_K = 32, F_V = 64, F_S = 96, F_Z = 128;
constexpr int CTRL_BYTES = C_ASK + 256 * 64;
constexpr int MB = 1 << 20;
constexpr int R_U = 0, R_U2 = 3 * MB;
constexpr int R_MEM = 2 * MB, MEM_SLOT = 32768;
constexpr int R_P = R_MEM + 4 * MEM_SLOT, P_SLOT = 65536;
constexpr int R_PT = 4 * MB, PT_SLOT = 131072;
constexpr int R_QX = 8 * MB, R_KX = 40 * MB, R_Z = 72 * MB, Z_SLOT = 4 * MB, R_KVT = R_KX, R_YT = R_KX + 8 * MB, R_END = 208 * MB;

struct Params {
  const u16 *x, *mem0, *Wq, *Wk, *Wv, *Wo, *bq, *bk, *bv, *shift, *ssmk, *ssmD;
  u16* out;
  unsigned char* ctrl;
  unsigned char* data;
};

DEV rsrc_t mkrs(const void* p, unsigned bytes) { return __builtin_amdgcn_make_buffer_rsrc((void*)p, 0, bytes, 0x00020000); }
DEV v4u ld16s(rsrc_t rs, int v, int s) { return __builtin_amdgcn_raw_buffer_load_b128(rs, v, s, AUX_SC1); }
DEV v2u ld8s(rsrc_t rs, int v, int s) { return __builtin_amdgcn_raw_buffer_load_b64(rs, v, s, AUX_SC1); }
DEV v4u ld16(rsrc_t rs, int v, int s) { return __builtin_amdgcn_raw_buffer_load_b128(rs, v, s, 0); }
DEV v4u ld16c(rsrc_t rs, int v, int s) { return __builtin_amdgcn_raw_buffer_load_b128(rs, v, s, CONV_AUX); }
DEV v4u ld16w(rsrc_t rs, int v, int s) { return __builtin_amdgcn_raw_buffer_load_b128(rs, v, s, W_AUX); }
DEV v2u ld8(rsrc_t rs, int v, int s) { return __builtin_amdgcn_raw_buffer_load_b64(rs, v, s, 0); }
DEV void dma16(rsrc_t rs, lds_t l, int v) {
#if __HIP_DEVICE_COMPILE__
  __builtin_amdgcn_raw_ptr_buffer_load_lds(rs, (LAS void*)l, 16, v, 0, 0, 0);
#endif
}
DEV void st16s(rsrc_t rs, int v, int s, v4u d) { __builtin_amdgcn_raw_buffer_store_b128(d, rs, v, s, AUX_SC1); }
DEV void st8s(rsrc_t rs, int v, int s, v2u d) { __builtin_amdgcn_raw_buffer_store_b64(d, rs, v, s, AUX_SC1); }
DEV void st4s(rsrc_t rs, int v, int s, unsigned d) { __builtin_amdgcn_raw_buffer_store_b32(d, rs, v, s, AUX_SC1); }
DEV int amap(int o, int par) { return ((o >> 13) << 14) | (par << 13) | (o & 8191); }
DEV int xblk(int s, int h, int par) { return (s * 32 + h) * 16384 + ((par ^ (s >> 2)) & 1) * 8192; }
DEV unsigned umax(unsigned a, unsigned b) { return a > b ? a : b; }
DEV unsigned top(v4u v) { return umax(umax(v.x, v.y), umax(v.z, v.w)); }
DEV unsigned pk(float a, float b) { bf16x2 v = {(__bf16)a, (__bf16)b}; return __builtin_bit_cast(unsigned, v); }
DEV unsigned pks(float a, float b) { unsigned u = pk(a, b); return u < SENT - 1u ? u : SENT - 1u; }
DEV float blo(unsigned u) { return __builtin_bit_cast(float, u << 16); }
DEV float bhi(unsigned u) { return __builtin_bit_cast(float, u & 0xFFFF0000u); }
DEV float rb(float a) { return blo(pk(a, a)); }
DEV unsigned fu(float f) { return __builtin_bit_cast(unsigned, f); }
DEV float uf(unsigned u) { return __builtin_bit_cast(float, u); }
DEV f32x4 mfma16(v4u a, v4u b, f32x4 c) {
  return __builtin_amdgcn_mfma_f32_16x16x32_bf16(__builtin_bit_cast(bf16x8, a), __builtin_bit_cast(bf16x8, b), c, 0, 0, 0);
}
DEV f32x16 mfma32(v4u a, v4u b, f32x16 c) {
  return __builtin_amdgcn_mfma_f32_32x32x16_bf16(__builtin_bit_cast(bf16x8, a), __builtin_bit_cast(bf16x8, b), c, 0, 0, 0);
}
DEV f32x4 smf(v4u a, v8u b, f32x4 c, int idx) {
  return __builtin_amdgcn_smfmac_f32_16x16x64_bf16(__builtin_bit_cast(bf16x8, a), __builtin_bit_cast(bf16x16, b), c, idx, 0, 0);
}
DEV v8u cat(v4u a, v4u b) { return __builtin_shufflevector(a, b, 0, 1, 2, 3, 4, 5, 6, 7); }
DEV v4u lo4(v8u a) { return __builtin_shufflevector(a, a, 0, 1, 2, 3); }
DEV v4u hi4(v8u a) { return __builtin_shufflevector(a, a, 4, 5, 6, 7); }
DEV v4u lds16(lds_t L, int o) { return *(LAS v4u*)(L + o); }
DEV void sts16(lds_t L, int o, v4u v) { *(LAS v4u*)(L + o) = v; }
DEV void sts8(lds_t L, int o, v2u v) { *(LAS v2u*)(L + o) = v; }
#define SCHED() __builtin_amdgcn_sched_barrier(0)
#define PIN_A(x) asm volatile("" : "+a"(x))
#define PIN_V(x) asm volatile("" : "+v"(x))
DEV int opq(int v) { asm volatile("" : "+v"(v)); return v; }
DEV int fresh_lane() {
  int l; asm volatile("v_mbcnt_lo_u32_b32 %0, -1, 0\n\tv_mbcnt_hi_u32_b32 %0, -1, %0" : "=v"(l)); return l;
}
DEV f32x4 fresh_zero() {
  f32x2 a, b;
  asm volatile("v_mov_b64 %0, 0" : "=v"(a));
  asm volatile("v_mov_b64 %0, 0" : "=v"(b));
  return __builtin_shufflevector(a, b, 0, 1, 2, 3);
}
DEV unsigned fresh_sent() { unsigned z; asm volatile("v_mov_b32 %0, -1" : "=v"(z)); return z; }
#define SMF "v_smfmac_f32_16x16x64_bf16 "
#define MF16 "v_mfma_f32_16x16x32_bf16 "
#define SMF1(cls, d, a, b) asm volatile(SMF "%0, %1, %2, %3" : "+v"(d) : "v"(a), cls(b), "v"(idx))
#define SMF1F(cls, lo, hi, d, a, b) asm volatile(SMF "v[" #lo ":" #hi "], %1, %2, %3" : "+{v[" #lo ":" #hi "]}"(d) : "v"(a), cls(b), "v"(idx))
#define AT_HOME(M, j) do { const int j_ = (j); if (j_ == 0) M("{v[32:39]}"); if (j_ == 1) M("{v[40:47]}"); if (j_ == 2) M("{v[48:55]}"); if (j_ == 3) M("{v[56:63]}"); if (j_ == 4) M("{v[64:71]}"); if (j_ == 5) M("{v[72:79]}"); if (j_ == 6) M("{v[80:87]}"); if (j_ == 7) M("{v[88:95]}"); if (j_ == 8) M("{v[96:103]}"); if (j_ == 9) M("{v[104:111]}"); if (j_ == 10) M("{v[112:119]}"); if (j_ == 11) M("{v[120:127]}"); if (j_ == 12) M("{v[128:135]}"); if (j_ == 13) M("{v[136:143]}"); if (j_ == 14) M("{v[144:151]}"); if (j_ == 15) M("{v[152:159]}"); } while (0)
#define MF1(d, a, b) asm volatile(MF16 "%0, %1, %2, %0" : "+v"(d) : "v"(a), "v"(b))
#define MF1Z(d, a, b) asm volatile(MF16 "%0, %1, %2, 0" : "=&v"(d) : "v"(a), "v"(b))
#define LGKM(n) asm volatile("s_waitcnt lgkmcnt(" #n ")" ::: "memory")
#define RAWBAR() do { __builtin_amdgcn_s_barrier(); asm volatile("" ::: "memory"); } while (0)
DEV void settle3(f32x4& s0, f32x4& s1, f32x4& s2) { asm volatile("s_nop 7" : "+v"(s0), "+v"(s1), "+v"(s2)); }
DEV void settle4(f32x4 (&d)[4]) { asm volatile("s_nop 7" : "+v"(d[0]), "+v"(d[1]), "+v"(d[2]), "+v"(d[3])); }
#define BAR() do { asm volatile("s_waitcnt lgkmcnt(0)" ::: "memory"); __builtin_amdgcn_s_barrier(); asm volatile("" ::: "memory"); } while (0)
#define VMWAIT(n) asm volatile("s_waitcnt vmcnt(" #n ")" ::: "memory")
DEV float add1(float a, float b) {
  float o; asm("v_add_f32 %0, %1, %2" : "=v"(o) : "v"(a), "v"(b)); return o;
}
DEV float xmax(float v) {
  auto r = __builtin_amdgcn_permlane32_swap(fu(v), fu(v), false, false); v = fmaxf(uf(r[0]), uf(r[1]));
  auto q = __builtin_amdgcn_permlane16_swap(fu(v), fu(v), false, false); return fmaxf(uf(q[0]), uf(q[1]));
}
DEV float xsum(float v) {
  auto r = __builtin_amdgcn_permlane32_swap(fu(v), fu(v), false, false); v = uf(r[0]) + uf(r[1]);
  auto q = __builtin_amdgcn_permlane16_swap(fu(v), fu(v), false, false); return uf(q[0]) + uf(q[1]);
}

DEV float ror8(float v) { return uf(__builtin_amdgcn_update_dpp(0u, fu(v), 0x128, 0xf, 0xf, true)); }
DEV float fmax2(float a, float b) { return __builtin_amdgcn_fmed3f(a, b, __builtin_inff()); }
DEV float fmax3(float a, float b, float c) { float r; asm("v_max3_f32 %0, %1, %2, %3" : "=v"(r) : "v"(a), "v"(b), "v"(c)); return r; }
DEV bool allok(unsigned t) { return __all(t != SENT); }

template <int N> struct IC { static constexpr int value = N; };
template <int N, class F> DEV void sfor(F&& f) { if constexpr (N > 0) { sfor<N - 1>(f); f(IC<N - 1>{}); } }
struct Ctx {
  gu32* ctrl;
  rsrc_t rs;
  lds_t L;
  int w, lane;
  int b, slot, att;
  unsigned ask;
  unsigned t0;
  unsigned budget;
};
DEV bool spend(Ctx& c) {
  const unsigned old = c.budget;
  asm volatile("s_cmp_lg_u32 %0, 0\n\ts_subb_u32 %0, %0, 0" : "+s"(c.budget) :: "scc");
  return old == 0u;
}
DEV unsigned wait_flag(Ctx& c, int flag, unsigned need) {
  gu32* f = c.ctrl + (C_FLAG + flag * 128) / 4;
  unsigned v;
#pragma unroll 1
  for (;;) {
    v = __builtin_amdgcn_readfirstlane(__hip_atomic_load(f, RLX_AGENT));
    if (v >= need || spend(c)) break;
    __builtin_amdgcn_s_sleep(2);
  }
  __builtin_amdgcn_fence(__ATOMIC_ACQUIRE, "wavefront");
  return v;
}
DEV void post_flag(Ctx& c, int flag, unsigned v, int off = 0) {
  st4s(mkrs((const void*)c.ctrl, CTRL_BYTES), (c.w | off | fresh_lane()) ? 1 << 30 : 0, C_FLAG + flag * 128, v);
}
constexpr unsigned CL_A = 1u << 16, CL_VOTED = 1u << 24, CL_FILLED = 1u << 28;
DEV void claim(Ctx& c, unsigned votes) {
  if (c.w != 0) return;
  if (c.att == 0) {
#pragma unroll 1
    while (__builtin_amdgcn_readfirstlane(__hip_atomic_load(c.ctrl + C_ECHO / 4 + c.b, RLX_AGENT)) != c.ask) {
      if (spend(c)) break;
      __builtin_amdgcn_s_sleep(2);
    }
  }
  if (fresh_lane() == 0) __hip_atomic_fetch_add(c.ctrl + (C_CLAIM + c.att * 1024) / 4 + c.slot, 1u + votes, RLX_AGENT);
}
DEV unsigned wait_verdict(Ctx& c) {
  unsigned v;
#pragma unroll 1
  for (;;) {
    v = __builtin_amdgcn_readfirstlane(__hip_atomic_load(c.ctrl + (C_VERDICT + c.att * 128) / 4, RLX_AGENT));
    if ((v & 1u) || spend(c)) break;
    __builtin_amdgcn_s_sleep(2);
  }
  __builtin_amdgcn_fence(__ATOMIC_ACQUIRE, "wavefront");
  if ((v & 3u) == 3u && c.att == 0) {
    asm volatile("s_waitcnt vmcnt(0) lgkmcnt(0)\n\ts_barrier\n\ts_mov_b64 s[0:1], %0\n\ts_mov_b32 s2, %1\n\tv_mov_b32 v0, %2\n\t"
                 "s_getpc_b64 s[4:5]\n1:\n\ts_sub_u32 s4, s4, 1b-mk_mega\n\ts_subb_u32 s5, s5, 0\n\ts_setpc_b64 s[4:5]"
                 :: "s"(__builtin_amdgcn_kernarg_segment_ptr()), "s"(c.b + 256), "v"(c.w * 64 + fresh_lane()) : "s0", "s1", "s2", "s4", "s5", "v0", "scc", "memory");
    __builtin_unreachable();
  }
  return v;
}
DEV unsigned wait_vote(Ctx& c) {
  unsigned v;
#pragma unroll 1
  for (;;) {
    v = __builtin_amdgcn_readfirstlane(__hip_atomic_load(c.ctrl + C_VOTE / 4, RLX_AGENT));
    if ((v & 1u) || spend(c)) break;
    __builtin_amdgcn_s_sleep(2);
  }
  return v;
}
DEV void serve(Ctx& c) {
  c.budget = SPIN_CAP;
  const int lane = fresh_lane();
  const rsrc_t rc = mkrs((const void*)c.ctrl, CTRL_BYTES);
  if (c.att == 0) {
#pragma unroll
    for (int i = C_CLAIM / 1024; i < C_ASK / 1024; ++i) st16s(rc, lane * 16, i * 1024, v4u{0, 0, 0, 0});
    VMWAIT(0);
  }
  unsigned vote = c.att ? wait_vote(c) : 0u;
  bool echo = c.att == 0, first = true;
  v4u last = {0u, 0u, 0u, 0u};
#pragma unroll 1
  for (;;) {
    asm volatile("" ::: "memory");
    const v4u cl = ld16s(rc, lane * 16, C_CLAIM + c.att * 1024);
    if (echo) {
      v4u e;
#pragma unroll
      for (int i = 0; i < 4; ++i) e[i] = __builtin_amdgcn_raw_buffer_load_b32(rc, (lane * 4 + i) * 64, C_ASK, AUX_SC1);
      if (first || __any(e[0] != last[0] || e[1] != last[1] || e[2] != last[2] || e[3] != last[3])) st16s(rc, lane * 16, C_ECHO, e);
      last = e; first = false;
    }
    unsigned n = 0, one = 1;
    int va = 0, nv = 0, nf = 0;
#pragma unroll
    for (int i = 0; i < 4; ++i) {
      n += cl[i] & 0xFFFFu; one &= (cl[i] & 0xFFFFu) == 1u;
      va += __popcll(__ballot(cl[i] & CL_A)); nv += __popcll(__ballot(cl[i] & CL_VOTED)); nf += __popcll(__ballot(cl[i] & CL_FILLED));
    }
#pragma unroll
    for (int o = 1; o < 64; o <<= 1) n += (unsigned)__shfl_xor((int)n, o);
    const bool all = __builtin_amdgcn_readfirstlane(n) >= 256u, good = __all(one);
    echo = echo && !all;
    if (!vote && (all || max(va, nv - va) > 32)) {
      vote = va > 32 ? 5u : 9u;
      if (lane == 0) __hip_atomic_store(c.ctrl + C_VOTE / 4, vote, RLX_AGENT);
    }
    if (all && (!good || nf >= 64)) {
      if (lane == 0) __hip_atomic_store(c.ctrl + (C_VERDICT + c.att * 128) / 4, vote | (good ? 0u : 2u), RLX_AGENT);
      break;
    }
    if (spend(c)) break;
  }
}
DEV int vote_a(unsigned v) { return (int)((v >> 2) & 1u); }
DEV int vote_b(unsigned v) { return (int)((v >> 3) & 1u); }

#define STAMP(slot) do {} while (0)
#define TICK_DECL
#define TICK(i) do {} while (0)
#define TICK_SUM(n) do {} while (0)
#define TICK_LDS(b) do {} while (0)
#define TICK_DUMP(b) do {} while (0)
#define TICK_OUT() do {} while (0)

DEV constexpr int krow(int rho8, int i) { return 32 * (rho8 >> 2) + 8 * (rho8 & 1) + 2 * ((rho8 >> 1) & 1) + i; }
DEV int krow_lane(int g) { return 16 * (g >> 1) + 4 * (g & 1); }
DEV unsigned prm(unsigned hi, unsigned lo, int odd) { return __builtin_amdgcn_perm(hi, lo, odd ? 0x07060302u : 0x05040100u); }

#define AT_ACC(M, n) do { const int n_ = (n); if (n_ == 0) M("{a[0:7]}"); if (n_ == 1) M("{a[8:15]}"); if (n_ == 2) M("{a[16:23]}"); if (n_ == 3) M("{a[24:31]}"); if (n_ == 4) M("{a[32:39]}"); if (n_ == 5) M("{a[40:47]}"); if (n_ == 6) M("{a[48:55]}"); if (n_ == 7) M("{a[56:63]}"); if (n_ == 8) M("{a[64:71]}"); if (n_ == 9) M("{a[72:79]}"); if (n_ == 10) M("{a[80:87]}"); if (n_ == 11) M("{a[88:95]}"); if (n_ == 12) M("{a[96:103]}"); if (n_ == 13) M("{a[104:111]}"); if (n_ == 14) M("{a[112:119]}"); if (n_ == 15) M("{a[120:127]}"); if (n_ == 16) M("{a[128:135]}"); if (n_ == 17) M("{a[136:143]}"); if (n_ == 18) M("{a[144:151]}"); if (n_ == 19) M("{a[152:159]}"); if (n_ == 20) M("{a[160:167]}"); if (n_ == 21) M("{a[168:175]}"); if (n_ == 22) M("{a[176:183]}"); if (n_ == 23) M("{a[184:191]}"); if (n_ == 24) M("{a[192:199]}"); if (n_ == 25) M("{a[200:207]}"); if (n_ == 26) M("{a[208:215]}"); if (n_ == 27) M("{a[216:223]}"); if (n_ == 28) M("{a[224:231]}"); if (n_ == 29) M("{a[232:239]}"); if (n_ == 30) M("{a[240:247]}"); if (n_ == 31) M("{a[248:255]}"); } while (0)
#define PIN_ACC0(x) asm volatile("" :: "{a[0:7]}"(x[0]), "{a[8:15]}"(x[1]), "{a[16:23]}"(x[2]), "{a[24:31]}"(x[3]), "{a[32:39]}"(x[4]), "{a[40:47]}"(x[5]), "{a[48:55]}"(x[6]), "{a[56:63]}"(x[7]), "{a[64:71]}"(x[8]), "{a[72:79]}"(x[9]), "{a[80:87]}"(x[10]), "{a[88:95]}"(x[11]), "{a[96:103]}"(x[12]), "{a[104:111]}"(x[13]), "{a[112:119]}"(x[14]), "{a[120:127]}"(x[15]))
#define PIN_ACC1(x) asm volatile("" :: "{a[128:135]}"(x[16]), "{a[136:143]}"(x[17]), "{a[144:151]}"(x[18]), "{a[152:159]}"(x[19]), "{a[160:167]}"(x[20]), "{a[168:175]}"(x[21]), "{a[176:183]}"(x[22]), "{a[184:191]}"(x[23]), "{a[192:199]}"(x[24]), "{a[200:207]}"(x[25]), "{a[208:215]}"(x[26]), "{a[216:223]}"(x[27]), "{a[224:231]}"(x[28]), "{a[232:239]}"(x[29]), "{a[240:247]}"(x[30]), "{a[248:255]}"(x[31]))
#define PIN_HOME(x) asm volatile("" :: "{v[32:39]}"(x[0]), "{v[40:47]}"(x[1]), "{v[48:55]}"(x[2]), "{v[56:63]}"(x[3]), "{v[64:71]}"(x[4]), "{v[72:79]}"(x[5]), "{v[80:87]}"(x[6]), "{v[88:95]}"(x[7]), "{v[96:103]}"(x[8]), "{v[104:111]}"(x[9]), "{v[112:119]}"(x[10]), "{v[120:127]}"(x[11]), "{v[128:135]}"(x[12]), "{v[136:143]}"(x[13]), "{v[144:151]}"(x[14]), "{v[152:159]}"(x[15]))
#define CL_32_100 "v32", "v33", "v34", "v35", "v36", "v37", "v38", "v39", "v40", "v41", "v42", "v43", "v44", "v45", "v46", "v47", "v48", "v49", "v50", "v51", "v52", "v53", "v54", "v55", "v56", "v57", "v58", "v59", "v60", "v61", "v62", "v63", "v64", "v65", "v66", "v67", "v68", "v69", "v70", "v71", "v72", "v73", "v74", "v75", "v76", "v77", "v78", "v79", "v80", "v81", "v82", "v83", "v84", "v85", "v86", "v87", "v88", "v89", "v90", "v91", "v92", "v93", "v94", "v95", "v96", "v97", "v98", "v99", "v100", "v101", "v102", "v103", "v104", "v105", "v106", "v107", "v108", "v109", "v110", "v111", "v112", "v113", "v114", "v115", "v116", "v117", "v118", "v119", "v120", "v121", "v122", "v123", "v124", "v125", "v126", "v127", "v128", "v129", "v130", "v131"
#define ACC_IN(x) "{a[0:31]}"(x[0]), "{a[32:63]}"(x[1]), "{a[64:95]}"(x[2]), "{a[96:127]}"(x[3]), "{a[128:159]}"(x[4]), "{a[160:191]}"(x[5]), "{a[192:223]}"(x[6]), "{a[224:255]}"(x[7])
#define CL_16_32 "v16", "v17", "v18", "v19", "v20", "v21", "v22", "v23", "v24", "v25", "v26", "v27", "v28", "v29", "v30", "v31"
#define CL_120_128 "v120", "v121", "v122", "v123", "v124", "v125", "v126", "v127", "v128", "v129", "v130", "v131", "v132", "v133", "v134", "v135"
#define CL_160_200 "v160", "v161", "v162", "v163", "v164", "v165", "v166", "v167", "v168", "v169", "v170", "v171", "v172", "v173", "v174", "v175", "v176", "v177", "v178", "v179", "v180", "v181", "v182", "v183", "v184", "v185", "v186", "v187", "v188", "v189", "v190", "v191", "v192", "v193", "v194", "v195", "v196", "v197", "v198", "v199"
#define CL_176_200 "v176", "v177", "v178", "v179", "v180", "v181", "v182", "v183", "v184", "v185", "v186", "v187", "v188", "v189", "v190", "v191", "v192", "v193", "v194", "v195", "v196", "v197", "v198", "v199"
#define CL_200_216 "v200", "v201", "v202", "v203", "v204", "v205", "v206", "v207", "v208", "v209", "v210", "v211", "v212", "v213", "v214", "v215"
#define CL_216_256 "v216", "v217", "v218", "v219", "v220", "v221", "v222", "v223", "v224", "v225", "v226", "v227", "v228", "v229", "v230", "v231", "v232", "v233", "v234", "v235", "v236", "v237", "v238", "v239", "v240", "v241", "v242", "v243", "v244", "v245", "v246", "v247", "v248", "v249", "v250", "v251", "v252", "v253", "v254", "v255"
#define CL_S "memory", "vcc", "scc", "s60", "s61", "s62", "s63", "s64", "s65", "s66", "s67" CL_T
#define TIMED_WAIT \
    "s_getpc_b64 s[62:63]\n\t" "1:\n\t" "s_memtime s[64:65]\n\t" "s_waitcnt lgkmcnt(0)\n\t" "s_sub_u32 s61, %0, s64\n\t" "s_cmp_gt_i32 s61, 124\n\t" "s_cbranch_scc1 1b\n\t" \
    "s_max_i32 s61, s61, 0\n\t" "s_and_b32 s61, s61, 124\n\t" "s_sub_u32 s61, 2f-1b, s61\n\t" "s_add_u32 s62, s62, s61\n\t" "s_addc_u32 s63, s63, 0\n\t" \
    "s_setpc_b64 s[62:63]\n\t" ".rept 31\n\t" "s_nop 0\n\t" ".endr\n\t" "2:\n\t"
#define QK_STEP0 \
    "v_xor_b32 v216, 0x400, %[mo]\n\t" "s_xor_b32 s60, %[sb], 0x800\n\t" "s_mov_b32 s66, 0x3e0293ee\n\t" "s_mov_b32 s67, 0x3e0293ee\n\t" XT(80, 81) \
    "s_add_u32 s61, %[kb], 0x1000\n\t" "buffer_load_dwordx4 v[16:19], %[l1], %[rs], %[kb] offen offset:0 sc1\n\t" \
    "buffer_load_dwordx4 v[20:23], %[l1], %[rs], %[kb] offen offset:1024 sc1\n\t" "buffer_load_dwordx4 v[24:27], %[l1], %[rs], %[kb] offen offset:2048 sc1\n\t" \
    "buffer_load_dwordx4 v[28:31], %[l1], %[rs], %[kb] offen offset:3072 sc1\n\t" "buffer_load_dwordx4 v[200:203], %[l1], %[rs], s61 offen offset:0 sc1\n\t" \
    "buffer_load_dwordx4 v[204:207], %[l1], %[rs], s61 offen offset:1024 sc1\n\t" "buffer_load_dwordx4 v[208:211], %[l1], %[rs], s61 offen offset:2048 sc1\n\t" \
    "buffer_load_dwordx4 v[212:215], %[l1], %[rs], s61 offen offset:3072 sc1\n\t" "v_mov_b64 v[218:219], -1\n\t" \
    "buffer_store_dwordx2 v[218:219], %[px], %[rs], %[pr] offen sc1\n\t" "buffer_store_dword v218, %[pm], %[rs], %[pr] offen sc1\n\t" "s_waitcnt vmcnt(2)\n\t" XT(98, 99) \
    "s_barrier\n\t" XT(82, 83) "10:\n\t" "buffer_load_dwordx4 v[240:243], %[mo], %[rs], %[sb] offen sc1\n\t" "v_mov_b64 v[224:225], 0\n\t" "v_mov_b64 v[226:227], 0\n\t" \
    "v_mov_b64 v[228:229], 0\n\t" "v_mov_b64 v[230:231], 0\n\t" "v_mov_b64 v[232:233], 0\n\t" "v_mov_b64 v[234:235], 0\n\t" "v_mov_b64 v[236:237], 0\n\t" \
    "v_mov_b64 v[238:239], 0\n\t" "buffer_load_dwordx4 v[244:247], v216, %[rs], %[sb] offen sc1\n\t" "ds_read_b128 v[160:163], %[l0] offset:0\n\t" \
    "ds_read_b128 v[164:167], %[l0] offset:1024\n\t" "ds_read_b128 v[168:171], %[l0] offset:2048\n\t" "ds_read_b128 v[172:175], %[l0] offset:3072\n\t" "s_nop 3\n\t" \
    "buffer_load_dwordx4 v[248:251], %[mo], %[rs], s60 offen sc1\n\t" "ds_read_b128 v[176:179], %[l0] offset:4096\n\t" "ds_read_b128 v[180:183], %[l0] offset:5120\n\t" \
    "ds_read_b128 v[184:187], %[l0] offset:6144\n\t" "ds_read_b128 v[188:191], %[l0] offset:7168\n\t" "s_nop 3\n\t" \
    "buffer_load_dwordx4 v[252:255], v216, %[rs], s60 offen sc1\n\t" "s_cmp_lg_u32 s61, 0\n\t" "s_cbranch_scc0 177f\n\t" "ds_write_b128 %[l1], v[16:19] offset:16384\n\t" \
    "ds_write_b128 %[l1], v[20:23] offset:17408\n\t" "ds_write_b128 %[l1], v[24:27] offset:18432\n\t" "ds_write_b128 %[l1], v[28:31] offset:19456\n\t" \
    "ds_write_b128 %[l1], v[200:203] offset:20480\n\t" "ds_write_b128 %[l1], v[204:207] offset:21504\n\t" "ds_write_b128 %[l1], v[208:211] offset:22528\n\t" \
    "ds_write_b128 %[l1], v[212:215] offset:23552\n\t" "s_mov_b32 s61, 0\n\t" "s_waitcnt lgkmcnt(0)\n\t" "177:\n\t" "v_mov_b64 v[208:209], 0\n\t" \
    "v_mov_b64 v[210:211], 0\n\t" "v_mov_b64 v[212:213], 0\n\t" "v_mov_b64 v[214:215], 0\n\t" "s_waitcnt vmcnt(3)\n\t" XT(84, 85) \
    "v_smfmac_f32_16x16x64_bf16 v[224:227], v[240:243], a[0:7], %[ix]\n\t" "ds_write_b128 %[l1], v[240:243] offset:0\n\t" \
    "v_smfmac_f32_16x16x64_bf16 v[228:231], v[240:243], a[8:15], %[ix]\n\t" "v_smfmac_f32_16x16x64_bf16 v[232:235], v[240:243], v[32:39], %[ix]\n\t" \
    "s_waitcnt lgkmcnt(7)\n\t" "v_smfmac_f32_16x16x64_bf16 v[236:239], v[240:243], v[160:167], %[ix]\n\t" "ds_read_b128 v[160:163], %[l0] offset:8192\n\t" \
    "ds_read_b128 v[164:167], %[l0] offset:9216\n\t" "s_waitcnt vmcnt(2)\n\t" "v_smfmac_f32_16x16x64_bf16 v[224:227], v[244:247], a[16:23], %[ix]\n\t" \
    "ds_write_b128 %[l1], v[244:247] offset:1024\n\t" "v_smfmac_f32_16x16x64_bf16 v[228:231], v[244:247], a[24:31], %[ix]\n\t" \
    "v_smfmac_f32_16x16x64_bf16 v[232:235], v[244:247], v[40:47], %[ix]\n\t" "s_waitcnt lgkmcnt(8)\n\t" \
    "v_smfmac_f32_16x16x64_bf16 v[236:239], v[244:247], v[168:175], %[ix]\n\t" "ds_read_b128 v[168:171], %[l0] offset:10240\n\t" \
    "ds_read_b128 v[172:175], %[l0] offset:11264\n\t" "s_waitcnt vmcnt(1)\n\t" "v_smfmac_f32_16x16x64_bf16 v[224:227], v[248:251], a[32:39], %[ix]\n\t" \
    "ds_write_b128 %[l1], v[248:251] offset:2048\n\t" "v_smfmac_f32_16x16x64_bf16 v[228:231], v[248:251], a[40:47], %[ix]\n\t" "s_waitcnt lgkmcnt(3)\n\t" "s_barrier\n\t" \
    XT(86, 87) "v_smfmac_f32_16x16x64_bf16 v[232:235], v[248:251], v[48:55], %[ix]\n\t" "ds_read_b128 v[192:195], %[l1] offset:4096\n\t" \
    "ds_read_b128 v[196:199], %[l1] offset:8192\n\t" "v_smfmac_f32_16x16x64_bf16 v[236:239], v[248:251], v[176:183], %[ix]\n\t" \
    "ds_read_b128 v[176:179], %[l0] offset:12288\n\t" "ds_read_b128 v[180:183], %[l0] offset:13312\n\t" "s_waitcnt vmcnt(0)\n\t" \
    "v_smfmac_f32_16x16x64_bf16 v[224:227], v[252:255], a[48:55], %[ix]\n\t" "ds_write_b128 %[l1], v[252:255] offset:3072\n\t" \
    "v_smfmac_f32_16x16x64_bf16 v[228:231], v[252:255], a[56:63], %[ix]\n\t" "ds_read_b128 v[240:243], %[l1] offset:12288\n\t" \
    "ds_read_b128 v[244:247], %[l1] offset:5120\n\t" "v_smfmac_f32_16x16x64_bf16 v[232:235], v[252:255], v[56:63], %[ix]\n\t" \
    "v_smfmac_f32_16x16x64_bf16 v[236:239], v[252:255], v[184:191], %[ix]\n\t" "ds_read_b128 v[184:187], %[l0] offset:14336\n\t" \
    "ds_read_b128 v[188:191], %[l0] offset:15360\n\t" "s_waitcnt lgkmcnt(8)\n\t" "v_smfmac_f32_16x16x64_bf16 v[224:227], v[192:195], a[64:71], %[ix]\n\t" \
    "v_smfmac_f32_16x16x64_bf16 v[228:231], v[192:195], a[72:79], %[ix]\n\t" "v_smfmac_f32_16x16x64_bf16 v[232:235], v[192:195], v[64:71], %[ix]\n\t" \
    "s_waitcnt lgkmcnt(4)\n\t" "s_barrier\n\t" XT(88, 89) "v_smfmac_f32_16x16x64_bf16 v[236:239], v[192:195], v[160:167], %[ix]\n\t" \
    "ds_read_b128 v[160:163], %[l0] offset:16384\n\t" "ds_read_b128 v[164:167], %[l0] offset:17408\n\t" \
    "v_smfmac_f32_16x16x64_bf16 v[224:227], v[196:199], a[80:87], %[ix]\n\t" "ds_read_b128 v[248:251], %[l1] offset:9216\n\t" \
    "ds_read_b128 v[252:255], %[l1] offset:13312\n\t" "v_smfmac_f32_16x16x64_bf16 v[228:231], v[196:199], a[88:95], %[ix]\n\t" \
    "ds_read_b128 v[192:195], %[l1] offset:6144\n\t" "v_smfmac_f32_16x16x64_bf16 v[232:235], v[196:199], v[72:79], %[ix]\n\t" "ds_read_b128 v[16:19], %[kx] offset:0\n\t" \
    "v_smfmac_f32_16x16x64_bf16 v[236:239], v[196:199], v[168:175], %[ix]\n\t" "ds_read_b128 v[168:171], %[l0] offset:18432\n\t" \
    "ds_read_b128 v[172:175], %[l0] offset:19456\n\t" "s_waitcnt lgkmcnt(11)\n\t" "v_smfmac_f32_16x16x64_bf16 v[224:227], v[240:243], a[96:103], %[ix]\n\t" \
    "ds_read_b128 v[196:199], %[l1] offset:10240\n\t" "v_smfmac_f32_16x16x64_bf16 v[228:231], v[240:243], a[104:111], %[ix]\n\t" \
    "ds_read_b128 v[20:23], %[kx] offset:2048\n\t" "v_smfmac_f32_16x16x64_bf16 v[232:235], v[240:243], v[80:87], %[ix]\n\t" \
    "v_smfmac_f32_16x16x64_bf16 v[236:239], v[240:243], v[176:183], %[ix]\n\t" "ds_read_b128 v[176:179], %[l0] offset:20480\n\t" \
    "ds_read_b128 v[180:183], %[l0] offset:21504\n\t" "s_waitcnt lgkmcnt(14)\n\t" "v_smfmac_f32_16x16x64_bf16 v[224:227], v[244:247], a[112:119], %[ix]\n\t" \
    "ds_read_b128 v[240:243], %[l1] offset:14336\n\t" "ds_read_b128 v[24:27], %[kx] offset:4096\n\t" \
    "v_smfmac_f32_16x16x64_bf16 v[228:231], v[244:247], a[120:127], %[ix]\n\t" "v_smfmac_f32_16x16x64_bf16 v[232:235], v[244:247], v[88:95], %[ix]\n\t" \
    "s_waitcnt lgkmcnt(14)\n\t" "v_smfmac_f32_16x16x64_bf16 v[236:239], v[244:247], v[184:191], %[ix]\n\t" "ds_read_b128 v[184:187], %[l0] offset:22528\n\t" \
    "ds_read_b128 v[188:191], %[l0] offset:23552\n\t" "s_waitcnt lgkmcnt(13)\n\t" "v_smfmac_f32_16x16x64_bf16 v[224:227], v[248:251], a[128:135], %[ix]\n\t" \
    "ds_read_b128 v[244:247], %[l1] offset:7168\n\t" "ds_read_b128 v[28:31], %[kx] offset:6144\n\t" "v_smfmac_f32_16x16x64_bf16 v[228:231], v[248:251], a[136:143], %[ix]\n\t" \
    "v_smfmac_f32_16x16x64_bf16 v[232:235], v[248:251], v[96:103], %[ix]\n\t" "ds_read_b128 v[200:203], %[kx] offset:256\n\t" \
    "v_smfmac_f32_16x16x64_bf16 v[236:239], v[248:251], v[160:167], %[ix]\n\t" "ds_read_b128 v[160:163], %[l0] offset:24576\n\t" \
    "ds_read_b128 v[164:167], %[l0] offset:25600\n\t" "s_waitcnt lgkmcnt(14)\n\t" "v_smfmac_f32_16x16x64_bf16 v[224:227], v[252:255], a[144:151], %[ix]\n\t" \
    "ds_read_b128 v[248:251], %[l1] offset:11264\n\t" "v_smfmac_f32_16x16x64_bf16 v[228:231], v[252:255], a[152:159], %[ix]\n\t" \
    "ds_read_b128 v[204:207], %[kx] offset:2304\n\t" "v_smfmac_f32_16x16x64_bf16 v[232:235], v[252:255], v[104:111], %[ix]\n\t" "s_waitcnt lgkmcnt(14)\n\t" \
    "v_smfmac_f32_16x16x64_bf16 v[236:239], v[252:255], v[168:175], %[ix]\n\t" "ds_read_b128 v[168:171], %[l0] offset:26624\n\t" \
    "ds_read_b128 v[172:175], %[l0] offset:27648\n\t" "v_smfmac_f32_16x16x64_bf16 v[224:227], v[192:195], a[160:167], %[ix]\n\t" \
    "ds_read_b128 v[252:255], %[l1] offset:15360\n\t" "v_smfmac_f32_16x16x64_bf16 v[228:231], v[192:195], a[168:175], %[ix]\n\t" \
    "v_smfmac_f32_16x16x64_bf16 v[224:227], v[196:199], a[176:183], %[ix]\n\t" "v_smfmac_f32_16x16x64_bf16 v[228:231], v[196:199], a[184:191], %[ix]\n\t" \
    "s_waitcnt lgkmcnt(13)\n\t" "v_smfmac_f32_16x16x64_bf16 v[224:227], v[240:243], a[192:199], %[ix]\n\t" \
    "v_smfmac_f32_16x16x64_bf16 v[228:231], v[240:243], a[200:207], %[ix]\n\t" "s_waitcnt lgkmcnt(9)\n\t" \
    "v_smfmac_f32_16x16x64_bf16 v[224:227], v[244:247], a[208:215], %[ix]\n\t" "v_smfmac_f32_16x16x64_bf16 v[228:231], v[244:247], a[216:223], %[ix]\n\t" \
    "s_waitcnt lgkmcnt(4)\n\t" "v_smfmac_f32_16x16x64_bf16 v[224:227], v[248:251], a[224:231], %[ix]\n\t" \
    "v_smfmac_f32_16x16x64_bf16 v[228:231], v[248:251], a[232:239], %[ix]\n\t" "s_waitcnt lgkmcnt(0)\n\t" \
    "v_smfmac_f32_16x16x64_bf16 v[224:227], v[252:255], a[240:247], %[ix]\n\t" "s_nop 1\n\t" "v_smfmac_f32_16x16x64_bf16 v[228:231], v[252:255], a[248:255], %[ix]\n\t" \
    XT(90, 91) "s_nop 1\n\t" "v_smfmac_f32_16x16x64_bf16 v[232:235], v[192:195], v[112:119], %[ix]\n\t" "s_nop 1\n\t" \
    "v_smfmac_f32_16x16x64_bf16 v[236:239], v[192:195], v[176:183], %[ix]\n\t" "ds_read_b128 v[176:179], %[l0] offset:28672\n\t" "v_add_f32 v224, v224, v225\n\t" \
    "v_add_f32 v226, v226, v227\n\t" "v_smfmac_f32_16x16x64_bf16 v[232:235], v[196:199], v[120:127], %[ix]\n\t" "ds_read_b128 v[180:183], %[l0] offset:29696\n\t" \
    "v_add_f32 v228, v228, v229\n\t" "v_add_f32 v230, v230, v231\n\t" "v_cmp_u_f32 vcc, v224, v226\n\t" \
    "v_smfmac_f32_16x16x64_bf16 v[236:239], v[196:199], v[184:191], %[ix]\n\t" "ds_read_b128 v[184:187], %[l0] offset:30720\n\t" "s_cbranch_vccnz 11f\n\t" "12:\n\t" \
    "v_cvt_pk_bf16_f32 v218, v224, v228\n\t" "v_cvt_pk_bf16_f32 v219, v226, v230\n\t" "v_smfmac_f32_16x16x64_bf16 v[232:235], v[240:243], v[128:135], %[ix]\n\t" \
    "ds_write2_b32 %[qw], v218, v219 offset1:8\n\t" "ds_read_b128 v[188:191], %[l0] offset:31744\n\t" \
    "v_smfmac_f32_16x16x64_bf16 v[236:239], v[240:243], v[160:167], %[ix]\n\t" "ds_read_b128 v[192:195], %[kx] offset:4352\n\t" \
    "ds_read_b128 v[196:199], %[kx] offset:6400\n\t" "v_smfmac_f32_16x16x64_bf16 v[232:235], v[244:247], v[136:143], %[ix]\n\t" \
    "v_smfmac_f32_16x16x64_bf16 v[236:239], v[244:247], v[168:175], %[ix]\n\t" "s_waitcnt lgkmcnt(3)\n\t" "s_barrier\n\t" \
    "v_smfmac_f32_16x16x64_bf16 v[232:235], v[248:251], v[144:151], %[ix]\n\t" "ds_read_b128 v[216:219], %[l1] offset:24576\n\t" \
    "ds_read_b128 v[220:223], %[l1] offset:25600\n\t" "v_smfmac_f32_16x16x64_bf16 v[236:239], v[248:251], v[176:183], %[ix]\n\t" \
    "v_smfmac_f32_16x16x64_bf16 v[232:235], v[252:255], v[152:159], %[ix]\n\t" "s_waitcnt lgkmcnt(4)\n\t" \
    "v_smfmac_f32_16x16x64_bf16 v[236:239], v[252:255], v[184:191], %[ix]\n\t" XT(92, 93) "s_waitcnt lgkmcnt(1)\n\t" \
    "v_smfmac_f32_16x16x64_bf16 v[208:211], v[216:219], v[16:23], %[ix]\n\t" "s_nop 1\n\t" "v_smfmac_f32_16x16x64_bf16 v[212:215], v[216:219], v[200:207], %[ix]\n\t" \
    "s_waitcnt lgkmcnt(0)\n\t" "s_nop 0\n\t" "v_add_f32 v232, v232, v233\n\t" "v_add_f32 v234, v234, v235\n\t" "v_add_f32 v236, v236, v237\n\t" \
    "v_add_f32 v238, v238, v239\n\t" "v_cvt_pk_bf16_f32 v232, v232, v236\n\t" "v_cvt_pk_bf16_f32 v233, v234, v238\n\t" "ds_write2_b32 %[kw], v232, v233 offset1:68\n\t" \
    "v_smfmac_f32_16x16x64_bf16 v[208:211], v[220:223], v[24:31], %[ix]\n\t" "v_smfmac_f32_16x16x64_bf16 v[212:215], v[220:223], v[192:199], %[ix]\n\t" \
    "v_mov_b64 v[224:225], 0\n\t" "v_mov_b64 v[226:227], 0\n\t" "s_waitcnt lgkmcnt(0)\n\t" "s_barrier\n\t" XT(94, 95) "ds_read_b128 v[160:163], %[km] offset:0\n\t" \
    "ds_read_b128 v[164:167], %[km] offset:64\n\t" "ds_read_b128 v[168:171], %[km] offset:128\n\t" "ds_read_b128 v[172:175], %[km] offset:192\n\t" \
    "v_add_f32 v178, v208, v209\n\t" "v_add_f32 v179, v210, v211\n\t" "v_add_f32 v180, v212, v213\n\t" "v_add_f32 v181, v214, v215\n\t" "v_max_f32 v182, v178, v180\n\t" \
    "v_max_f32 v183, v179, v181\n\t" "s_nop 0\n\t" "v_max_f32_dpp v182, v182, v182 row_ror:8 row_mask:0xf bank_mask:0xf\n\t" \
    "v_max_f32_dpp v183, v183, v183 row_ror:8 row_mask:0xf bank_mask:0xf\n\t" "s_nop 0\n\t" "v_max_f32_dpp v182, v182, v182 row_ror:4 row_mask:0xf bank_mask:0xf\n\t" \
    "v_max_f32_dpp v183, v183, v183 row_ror:4 row_mask:0xf bank_mask:0xf\n\t" "s_nop 0\n\t" "v_max_f32_dpp v182, v182, v182 row_ror:2 row_mask:0xf bank_mask:0xf\n\t" \
    "v_max_f32_dpp v183, v183, v183 row_ror:2 row_mask:0xf bank_mask:0xf\n\t" "s_nop 0\n\t" "v_max_f32_dpp v182, v182, v182 row_ror:1 row_mask:0xf bank_mask:0xf\n\t" \
    "v_max_f32_dpp v183, v183, v183 row_ror:1 row_mask:0xf bank_mask:0xf\n\t" "s_waitcnt lgkmcnt(2)\n\t" \
    "v_smfmac_f32_16x16x64_bf16 v[224:227], v[216:219], v[160:167], %[ix]\n\t" "s_waitcnt lgkmcnt(0)\n\t" \
    "v_smfmac_f32_16x16x64_bf16 v[224:227], v[220:223], v[168:175], %[ix]\n\t" "s_nop 7\n\t" "v_add_f32 v176, v224, v225\n\t" "v_add_f32 v177, v226, v227\n\t" \
    "v_max_f32 v182, v176, v182\n\t" "v_max_f32 v183, v177, v183\n\t" "s_nop 0\n\t" "v_max_f32_dpp v182, v182, v182 row_ror:4 row_mask:0xf bank_mask:0xf\n\t" \
    "v_max_f32_dpp v183, v183, v183 row_ror:4 row_mask:0xf bank_mask:0xf\n\t" "s_nop 0\n\t" "v_max_f32_dpp v182, v182, v182 row_ror:2 row_mask:0xf bank_mask:0xf\n\t" \
    "v_max_f32_dpp v183, v183, v183 row_ror:2 row_mask:0xf bank_mask:0xf\n\t" "s_nop 0\n\t" "v_max_f32_dpp v182, v182, v182 row_ror:1 row_mask:0xf bank_mask:0xf\n\t" \
    "v_max_f32_dpp v183, v183, v183 row_ror:1 row_mask:0xf bank_mask:0xf\n\t" "v_pk_add_f32 v[178:179], v[178:179], v[182:183] neg_lo:[0,1] neg_hi:[0,1]\n\t" \
    "v_pk_add_f32 v[180:181], v[180:181], v[182:183] neg_lo:[0,1] neg_hi:[0,1]\n\t" "v_pk_add_f32 v[176:177], v[176:177], v[182:183] neg_lo:[0,1] neg_hi:[0,1]\n\t" \
    "v_pk_mul_f32 v[178:179], v[178:179], s[66:67]\n\t" "v_pk_mul_f32 v[180:181], v[180:181], s[66:67]\n\t" "v_pk_mul_f32 v[176:177], v[176:177], s[66:67]\n\t" \
    "v_exp_f32 v178, v178\n\t" "v_exp_f32 v179, v179\n\t" "v_exp_f32 v180, v180\n\t" "v_exp_f32 v181, v181\n\t" "v_exp_f32 v176, v176\n\t" \
    "v_pk_add_f32 v[182:183], v[178:179], v[180:181]\n\t" "v_exp_f32 v177, v177\n\t" "s_nop 0\n\t" "v_add_f32_dpp v182, v182, v182 row_ror:8 row_mask:0xf bank_mask:0xf\n\t" \
    "v_add_f32_dpp v183, v183, v183 row_ror:8 row_mask:0xf bank_mask:0xf\n\t" "v_add_f32 v182, v182, v176\n\t" "v_add_f32 v183, v183, v177\n\t" "s_nop 0\n\t" \
    "v_add_f32_dpp v182, v182, v182 row_ror:4 row_mask:0xf bank_mask:0xf\n\t" "v_add_f32_dpp v183, v183, v183 row_ror:4 row_mask:0xf bank_mask:0xf\n\t" "s_nop 0\n\t" \
    "v_add_f32_dpp v182, v182, v182 row_ror:2 row_mask:0xf bank_mask:0xf\n\t" "v_add_f32_dpp v183, v183, v183 row_ror:2 row_mask:0xf bank_mask:0xf\n\t" "s_nop 0\n\t" \
    "v_add_f32_dpp v182, v182, v182 row_ror:1 row_mask:0xf bank_mask:0xf\n\t" "v_add_f32_dpp v183, v183, v183 row_ror:1 row_mask:0xf bank_mask:0xf\n\t" \
    "v_rcp_f32 v182, v182\n\t" "v_rcp_f32 v183, v183\n\t" "s_nop 0\n\t" "v_pk_mul_f32 v[176:177], v[176:177], v[182:183]\n\t" \
    "v_pk_mul_f32 v[178:179], v[178:179], v[182:183]\n\t" "v_pk_mul_f32 v[180:181], v[180:181], v[182:183]\n\t" "v_cvt_pk_bf16_f32 v176, v176, v177\n\t" \
    "v_cvt_pk_bf16_f32 v178, v178, v179\n\t" "v_cvt_pk_bf16_f32 v180, v180, v181\n\t" "v_mov_b32_dpp v177, v176 quad_perm:[1,0,3,2] row_mask:0xf bank_mask:0xf\n\t" \
    "v_mov_b32_dpp v179, v178 quad_perm:[1,0,3,2] row_mask:0xf bank_mask:0xf\n\t" "v_mov_b32_dpp v181, v180 quad_perm:[1,0,3,2] row_mask:0xf bank_mask:0xf\n\t" \
    "v_perm_b32 v184, v179, v178, %[ps]\n\t" "v_perm_b32 v185, v181, v180, %[ps]\n\t" "buffer_store_dwordx2 v[184:185], %[px], %[rs], %[pb] offen sc1\n\t" \
    "v_perm_b32 v186, v177, v176, %[ps]\n\t" "buffer_store_dword v186, %[pm], %[rs], %[pb] offen sc1\n\t" XT(96, 97) "s_branch 99f\n\t" "11:\n\t" "s_mov_b64 s[62:63], 0\n\t" \
    "ds_read_b128 v[208:211], %[l1] offset:0\n\t" "ds_read_b128 v[212:215], %[l1] offset:1024\n\t" "ds_read_b128 v[216:219], %[l1] offset:2048\n\t" \
    "ds_read_b128 v[220:223], %[l1] offset:3072\n\t" "s_waitcnt lgkmcnt(3)\n\t" "v_max3_u32 v208, v208, v209, v210\n\t" "v_max_u32 v208, v208, v211\n\t" \
    "v_cmp_eq_u32 vcc, -1, v208\n\t" "ds_read_b128 v[208:211], %[l1] offset:4096\n\t" "s_or_b64 s[62:63], s[62:63], vcc\n\t" "s_waitcnt lgkmcnt(3)\n\t" \
    "v_max3_u32 v212, v212, v213, v214\n\t" "v_max_u32 v212, v212, v215\n\t" "v_cmp_eq_u32 vcc, -1, v212\n\t" "ds_read_b128 v[212:215], %[l1] offset:5120\n\t" \
    "s_or_b64 s[62:63], s[62:63], vcc\n\t" "s_waitcnt lgkmcnt(3)\n\t" "v_max3_u32 v216, v216, v217, v218\n\t" "v_max_u32 v216, v216, v219\n\t" \
    "v_cmp_eq_u32 vcc, -1, v216\n\t" "ds_read_b128 v[216:219], %[l1] offset:6144\n\t" "s_or_b64 s[62:63], s[62:63], vcc\n\t" "s_waitcnt lgkmcnt(3)\n\t" \
    "v_max3_u32 v220, v220, v221, v222\n\t" "v_max_u32 v220, v220, v223\n\t" "v_cmp_eq_u32 vcc, -1, v220\n\t" "ds_read_b128 v[220:223], %[l1] offset:7168\n\t" \
    "s_or_b64 s[62:63], s[62:63], vcc\n\t" "s_waitcnt lgkmcnt(3)\n\t" "v_max3_u32 v208, v208, v209, v210\n\t" "v_max_u32 v208, v208, v211\n\t" \
    "v_cmp_eq_u32 vcc, -1, v208\n\t" "ds_read_b128 v[208:211], %[l1] offset:8192\n\t" "s_or_b64 s[62:63], s[62:63], vcc\n\t" "s_waitcnt lgkmcnt(3)\n\t" \
    "v_max3_u32 v212, v212, v213, v214\n\t" "v_max_u32 v212, v212, v215\n\t" "v_cmp_eq_u32 vcc, -1, v212\n\t" "ds_read_b128 v[212:215], %[l1] offset:9216\n\t" \
    "s_or_b64 s[62:63], s[62:63], vcc\n\t" "s_waitcnt lgkmcnt(3)\n\t" "v_max3_u32 v216, v216, v217, v218\n\t" "v_max_u32 v216, v216, v219\n\t" \
    "v_cmp_eq_u32 vcc, -1, v216\n\t" "ds_read_b128 v[216:219], %[l1] offset:10240\n\t" "s_or_b64 s[62:63], s[62:63], vcc\n\t" "s_waitcnt lgkmcnt(3)\n\t" \
    "v_max3_u32 v220, v220, v221, v222\n\t" "v_max_u32 v220, v220, v223\n\t" "v_cmp_eq_u32 vcc, -1, v220\n\t" "ds_read_b128 v[220:223], %[l1] offset:11264\n\t" \
    "s_or_b64 s[62:63], s[62:63], vcc\n\t" "s_waitcnt lgkmcnt(3)\n\t" "v_max3_u32 v208, v208, v209, v210\n\t" "v_max_u32 v208, v208, v211\n\t" \
    "v_cmp_eq_u32 vcc, -1, v208\n\t" "ds_read_b128 v[208:211], %[l1] offset:12288\n\t" "s_or_b64 s[62:63], s[62:63], vcc\n\t" "s_waitcnt lgkmcnt(3)\n\t" \
    "v_max3_u32 v212, v212, v213, v214\n\t" "v_max_u32 v212, v212, v215\n\t" "v_cmp_eq_u32 vcc, -1, v212\n\t" "ds_read_b128 v[212:215], %[l1] offset:13312\n\t" \
    "s_or_b64 s[62:63], s[62:63], vcc\n\t" "s_waitcnt lgkmcnt(3)\n\t" "v_max3_u32 v216, v216, v217, v218\n\t" "v_max_u32 v216, v216, v219\n\t" \
    "v_cmp_eq_u32 vcc, -1, v216\n\t" "ds_read_b128 v[216:219], %[l1] offset:14336\n\t" "s_or_b64 s[62:63], s[62:63], vcc\n\t" "s_waitcnt lgkmcnt(3)\n\t" \
    "v_max3_u32 v220, v220, v221, v222\n\t" "v_max_u32 v220, v220, v223\n\t" "v_cmp_eq_u32 vcc, -1, v220\n\t" "ds_read_b128 v[220:223], %[l1] offset:15360\n\t" \
    "s_or_b64 s[62:63], s[62:63], vcc\n\t" "s_waitcnt lgkmcnt(3)\n\t" "v_max3_u32 v208, v208, v209, v210\n\t" "v_max_u32 v208, v208, v211\n\t" \
    "v_cmp_eq_u32 vcc, -1, v208\n\t" "s_or_b64 s[62:63], s[62:63], vcc\n\t" "s_waitcnt lgkmcnt(2)\n\t" "v_max3_u32 v212, v212, v213, v214\n\t" \
    "v_max_u32 v212, v212, v215\n\t" "v_cmp_eq_u32 vcc, -1, v212\n\t" "s_or_b64 s[62:63], s[62:63], vcc\n\t" "s_waitcnt lgkmcnt(1)\n\t" \
    "v_max3_u32 v216, v216, v217, v218\n\t" "v_max_u32 v216, v216, v219\n\t" "v_cmp_eq_u32 vcc, -1, v216\n\t" "s_or_b64 s[62:63], s[62:63], vcc\n\t" \
    "s_waitcnt lgkmcnt(0)\n\t" "v_max3_u32 v220, v220, v221, v222\n\t" "v_max_u32 v220, v220, v223\n\t" "v_cmp_eq_u32 vcc, -1, v220\n\t" \
    "s_or_b64 s[62:63], s[62:63], vcc\n\t" "v_mov_b64 v[208:209], 0\n\t" "v_mov_b64 v[210:211], 0\n\t" "v_mov_b64 v[212:213], 0\n\t" "v_mov_b64 v[214:215], 0\n\t" \
    "s_cmp_eq_u64 s[62:63], 0\n\t" "s_cbranch_scc1 12b\n\t" "s_cmp_eq_u32 %[rt], 0\n\t" "s_cbranch_scc1 12b\n\t" "s_sub_u32 %[rt], %[rt], 1\n\t" \
    "v_xor_b32 v216, 0x400, %[mo]\n\t" XCNT(100) "14:\n\t" "buffer_load_dwordx4 v[240:243], %[mo], %[rs], %[sb] offen sc1\n\t" \
    "buffer_load_dwordx4 v[244:247], v216, %[rs], %[sb] offen sc1\n\t" "buffer_load_dwordx4 v[248:251], %[mo], %[rs], s60 offen sc1\n\t" \
    "buffer_load_dwordx4 v[252:255], v216, %[rs], s60 offen sc1\n\t" "s_waitcnt vmcnt(0)\n\t" "v_max_u32 v220, v240, v241\n\t" "v_max3_u32 v220, v220, v242, v243\n\t" \
    "v_max3_u32 v220, v220, v244, v245\n\t" "v_max3_u32 v220, v220, v246, v247\n\t" "v_max3_u32 v220, v220, v248, v249\n\t" "v_max3_u32 v220, v220, v250, v251\n\t" \
    "v_max3_u32 v220, v220, v252, v253\n\t" "v_max3_u32 v220, v220, v254, v255\n\t" "v_cmp_eq_u32 vcc, -1, v220\n\t" "s_cbranch_vccz 15f\n\t" "s_cmp_eq_u32 %[bd], 0\n\t" \
    "s_cbranch_scc1 15f\n\t" "s_sub_u32 %[bd], %[bd], 1\n\t" XCNT(101) "s_sleep 1\n\t" "s_branch 14b\n\t" "15:\n\t" "s_barrier\n\t" "s_branch 10b\n\t" "99:\n\t"
#define QK_STEP1 \
    "v_xor_b32 v216, 0x400, %[mo]\n\t" "s_xor_b32 s60, %[sb], 0x800\n\t" "s_add_u32 s61, %[qb], 0x1000\n\t" "s_mov_b32 s66, 0x3e0293ee\n\t" "s_mov_b32 s67, 0x3e0293ee\n\t" \
    XT(80, 81) XT(98, 99) "s_barrier\n\t" XT(82, 83) "20:\n\t" "buffer_load_dwordx4 v[240:243], %[mo], %[rs], %[sb] offen sc1\n\t" "v_mov_b64 v[224:225], 0\n\t" \
    "v_mov_b64 v[226:227], 0\n\t" "v_mov_b64 v[228:229], 0\n\t" "v_mov_b64 v[230:231], 0\n\t" "v_mov_b64 v[232:233], 0\n\t" "v_mov_b64 v[234:235], 0\n\t" \
    "v_mov_b64 v[236:237], 0\n\t" "v_mov_b64 v[238:239], 0\n\t" "buffer_load_dwordx4 v[244:247], v216, %[rs], %[sb] offen sc1\n\t" \
    "ds_read_b128 v[160:163], %[l0] offset:0\n\t" "ds_read_b128 v[164:167], %[l0] offset:1024\n\t" "ds_read_b128 v[168:171], %[l0] offset:2048\n\t" \
    "ds_read_b128 v[172:175], %[l0] offset:3072\n\t" "s_nop 3\n\t" "buffer_load_dwordx4 v[248:251], %[mo], %[rs], s60 offen sc1\n\t" \
    "ds_read_b128 v[176:179], %[l0] offset:4096\n\t" "ds_read_b128 v[180:183], %[l0] offset:5120\n\t" "ds_read_b128 v[184:187], %[l0] offset:6144\n\t" \
    "ds_read_b128 v[188:191], %[l0] offset:7168\n\t" "s_nop 3\n\t" "buffer_load_dwordx4 v[252:255], v216, %[rs], s60 offen sc1\n\t" "v_mov_b32 v208, 0xd028965f\n\t" \
    "v_mov_b32 v209, 0xff800000\n\t" "s_mov_b32 s62, 0\n\t" "s_mov_b32 s63, -1\n\t" "v_mov_b32 v210, 0\n\t" "v_cmp_gt_i32 vcc, 0, %[ps]\n\t" \
    "v_cndmask_b32 v204, 0, v209, s[62:63]\n\t" "s_nop 0\n\t" "v_cndmask_b32 v200, 0, v208, vcc\n\t" "v_cmp_gt_i32 vcc, 1, %[ps]\n\t" \
    "v_cndmask_b32 v205, 0, v209, s[62:63]\n\t" "s_nop 0\n\t" "v_cndmask_b32 v201, 0, v208, vcc\n\t" "v_cmp_gt_i32 vcc, 2, %[ps]\n\t" \
    "v_cndmask_b32 v206, 0, v209, s[62:63]\n\t" "s_nop 0\n\t" "v_cndmask_b32 v202, 0, v208, vcc\n\t" "v_cmp_gt_i32 vcc, 3, %[ps]\n\t" \
    "v_cndmask_b32 v207, 0, v209, s[62:63]\n\t" "s_nop 0\n\t" "v_cndmask_b32 v203, 0, v208, vcc\n\t" "s_waitcnt vmcnt(3)\n\t" XT(84, 85) \
    "v_smfmac_f32_16x16x64_bf16 v[224:227], v[240:243], a[0:7], %[ix]\n\t" "ds_write_b128 %[l1], v[240:243] offset:4096\n\t" \
    "v_smfmac_f32_16x16x64_bf16 v[228:231], v[240:243], a[8:15], %[ix]\n\t" "v_smfmac_f32_16x16x64_bf16 v[232:235], v[240:243], v[32:39], %[ix]\n\t" \
    "s_waitcnt lgkmcnt(7)\n\t" "v_smfmac_f32_16x16x64_bf16 v[236:239], v[240:243], v[160:167], %[ix]\n\t" "ds_read_b128 v[160:163], %[l0] offset:8192\n\t" \
    "ds_read_b128 v[164:167], %[l0] offset:9216\n\t" "s_waitcnt vmcnt(2)\n\t" "v_smfmac_f32_16x16x64_bf16 v[224:227], v[244:247], a[16:23], %[ix]\n\t" \
    "ds_write_b128 %[l1], v[244:247] offset:5120\n\t" "v_smfmac_f32_16x16x64_bf16 v[228:231], v[244:247], a[24:31], %[ix]\n\t" \
    "v_smfmac_f32_16x16x64_bf16 v[232:235], v[244:247], v[40:47], %[ix]\n\t" "s_waitcnt lgkmcnt(8)\n\t" \
    "v_smfmac_f32_16x16x64_bf16 v[236:239], v[244:247], v[168:175], %[ix]\n\t" "ds_read_b128 v[168:171], %[l0] offset:10240\n\t" \
    "ds_read_b128 v[172:175], %[l0] offset:11264\n\t" "s_waitcnt vmcnt(1)\n\t" "v_smfmac_f32_16x16x64_bf16 v[224:227], v[248:251], a[32:39], %[ix]\n\t" \
    "ds_write_b128 %[l1], v[248:251] offset:6144\n\t" "v_smfmac_f32_16x16x64_bf16 v[228:231], v[248:251], a[40:47], %[ix]\n\t" "s_waitcnt lgkmcnt(3)\n\t" "s_barrier\n\t" \
    XT(86, 87) "v_smfmac_f32_16x16x64_bf16 v[232:235], v[248:251], v[48:55], %[ix]\n\t" "ds_read_b128 v[192:195], %[l1] offset:8192\n\t" \
    "ds_read_b128 v[196:199], %[l1] offset:12288\n\t" "v_smfmac_f32_16x16x64_bf16 v[236:239], v[248:251], v[176:183], %[ix]\n\t" \
    "ds_read_b128 v[176:179], %[l0] offset:12288\n\t" "ds_read_b128 v[180:183], %[l0] offset:13312\n\t" "s_waitcnt vmcnt(0)\n\t" \
    "v_smfmac_f32_16x16x64_bf16 v[224:227], v[252:255], a[48:55], %[ix]\n\t" "ds_write_b128 %[l1], v[252:255] offset:7168\n\t" \
    "v_smfmac_f32_16x16x64_bf16 v[228:231], v[252:255], a[56:63], %[ix]\n\t" "ds_read_b128 v[240:243], %[l1] offset:0\n\t" "ds_read_b128 v[244:247], %[l1] offset:9216\n\t" \
    "v_smfmac_f32_16x16x64_bf16 v[232:235], v[252:255], v[56:63], %[ix]\n\t" "v_smfmac_f32_16x16x64_bf16 v[236:239], v[252:255], v[184:191], %[ix]\n\t" \
    "ds_read_b128 v[184:187], %[l0] offset:14336\n\t" "ds_read_b128 v[188:191], %[l0] offset:15360\n\t" "s_waitcnt lgkmcnt(8)\n\t" \
    "v_smfmac_f32_16x16x64_bf16 v[224:227], v[192:195], a[64:71], %[ix]\n\t" "v_smfmac_f32_16x16x64_bf16 v[228:231], v[192:195], a[72:79], %[ix]\n\t" \
    "v_smfmac_f32_16x16x64_bf16 v[232:235], v[192:195], v[64:71], %[ix]\n\t" "s_waitcnt lgkmcnt(4)\n\t" "s_barrier\n\t" XT(88, 89) \
    "v_smfmac_f32_16x16x64_bf16 v[236:239], v[192:195], v[160:167], %[ix]\n\t" "ds_read_b128 v[160:163], %[l0] offset:16384\n\t" \
    "ds_read_b128 v[164:167], %[l0] offset:17408\n\t" "v_smfmac_f32_16x16x64_bf16 v[224:227], v[196:199], a[80:87], %[ix]\n\t" \
    "ds_read_b128 v[248:251], %[l1] offset:13312\n\t" "ds_read_b128 v[252:255], %[l1] offset:1024\n\t" \
    "v_smfmac_f32_16x16x64_bf16 v[228:231], v[196:199], a[88:95], %[ix]\n\t" "ds_read_b128 v[192:195], %[l1] offset:10240\n\t" \
    "v_smfmac_f32_16x16x64_bf16 v[232:235], v[196:199], v[72:79], %[ix]\n\t" "buffer_load_dwordx4 v[16:19], %[hq], %[rs], %[qb] offen offset:0 sc1\n\t" \
    "v_smfmac_f32_16x16x64_bf16 v[236:239], v[196:199], v[168:175], %[ix]\n\t" "ds_read_b128 v[168:171], %[l0] offset:18432\n\t" \
    "ds_read_b128 v[172:175], %[l0] offset:19456\n\t" "s_waitcnt lgkmcnt(10)\n\t" "v_smfmac_f32_16x16x64_bf16 v[224:227], v[240:243], a[96:103], %[ix]\n\t" \
    "ds_read_b128 v[196:199], %[l1] offset:14336\n\t" "v_smfmac_f32_16x16x64_bf16 v[228:231], v[240:243], a[104:111], %[ix]\n\t" \
    "v_smfmac_f32_16x16x64_bf16 v[232:235], v[240:243], v[80:87], %[ix]\n\t" "buffer_load_dwordx4 v[20:23], %[hq], %[rs], %[qb] offen offset:2048 sc1\n\t" \
    "v_smfmac_f32_16x16x64_bf16 v[236:239], v[240:243], v[176:183], %[ix]\n\t" "ds_read_b128 v[176:179], %[l0] offset:20480\n\t" \
    "ds_read_b128 v[180:183], %[l0] offset:21504\n\t" "s_waitcnt lgkmcnt(12)\n\t" "v_smfmac_f32_16x16x64_bf16 v[224:227], v[244:247], a[112:119], %[ix]\n\t" \
    "ds_read_b128 v[240:243], %[l1] offset:2048\n\t" "v_smfmac_f32_16x16x64_bf16 v[228:231], v[244:247], a[120:127], %[ix]\n\t" \
    "v_smfmac_f32_16x16x64_bf16 v[232:235], v[244:247], v[88:95], %[ix]\n\t" "buffer_load_dwordx4 v[24:27], %[hq], %[rs], s61 offen offset:0 sc1\n\t" \
    "s_waitcnt lgkmcnt(11)\n\t" "v_smfmac_f32_16x16x64_bf16 v[236:239], v[244:247], v[184:191], %[ix]\n\t" "ds_read_b128 v[184:187], %[l0] offset:22528\n\t" \
    "ds_read_b128 v[188:191], %[l0] offset:23552\n\t" "s_waitcnt lgkmcnt(10)\n\t" "v_smfmac_f32_16x16x64_bf16 v[224:227], v[248:251], a[128:135], %[ix]\n\t" \
    "ds_read_b128 v[244:247], %[l1] offset:11264\n\t" "v_smfmac_f32_16x16x64_bf16 v[228:231], v[248:251], a[136:143], %[ix]\n\t" \
    "v_smfmac_f32_16x16x64_bf16 v[232:235], v[248:251], v[96:103], %[ix]\n\t" "buffer_load_dwordx4 v[28:31], %[hq], %[rs], s61 offen offset:2048 sc1\n\t" \
    "v_smfmac_f32_16x16x64_bf16 v[236:239], v[248:251], v[160:167], %[ix]\n\t" "ds_read_b128 v[160:163], %[l0] offset:24576\n\t" \
    "ds_read_b128 v[164:167], %[l0] offset:25600\n\t" "s_waitcnt lgkmcnt(12)\n\t" "v_smfmac_f32_16x16x64_bf16 v[224:227], v[252:255], a[144:151], %[ix]\n\t" \
    "ds_read_b128 v[248:251], %[l1] offset:15360\n\t" "v_smfmac_f32_16x16x64_bf16 v[228:231], v[252:255], a[152:159], %[ix]\n\t" \
    "v_smfmac_f32_16x16x64_bf16 v[232:235], v[252:255], v[104:111], %[ix]\n\t" "s_waitcnt lgkmcnt(10)\n\t" \
    "v_smfmac_f32_16x16x64_bf16 v[236:239], v[252:255], v[168:175], %[ix]\n\t" "ds_read_b128 v[168:171], %[l0] offset:26624\n\t" \
    "ds_read_b128 v[172:175], %[l0] offset:27648\n\t" "v_smfmac_f32_16x16x64_bf16 v[224:227], v[192:195], a[160:167], %[ix]\n\t" \
    "ds_read_b128 v[252:255], %[l1] offset:3072\n\t" "v_smfmac_f32_16x16x64_bf16 v[228:231], v[192:195], a[168:175], %[ix]\n\t" "s_waitcnt lgkmcnt(12)\n\t" \
    "v_smfmac_f32_16x16x64_bf16 v[224:227], v[196:199], a[176:183], %[ix]\n\t" "v_smfmac_f32_16x16x64_bf16 v[228:231], v[196:199], a[184:191], %[ix]\n\t" \
    "s_waitcnt lgkmcnt(9)\n\t" "v_smfmac_f32_16x16x64_bf16 v[224:227], v[240:243], a[192:199], %[ix]\n\t" \
    "v_smfmac_f32_16x16x64_bf16 v[228:231], v[240:243], a[200:207], %[ix]\n\t" "s_waitcnt lgkmcnt(6)\n\t" \
    "v_smfmac_f32_16x16x64_bf16 v[224:227], v[244:247], a[208:215], %[ix]\n\t" "v_smfmac_f32_16x16x64_bf16 v[228:231], v[244:247], a[216:223], %[ix]\n\t" \
    "s_waitcnt lgkmcnt(3)\n\t" "v_smfmac_f32_16x16x64_bf16 v[224:227], v[248:251], a[224:231], %[ix]\n\t" \
    "v_smfmac_f32_16x16x64_bf16 v[228:231], v[248:251], a[232:239], %[ix]\n\t" "s_waitcnt lgkmcnt(0)\n\t" \
    "v_smfmac_f32_16x16x64_bf16 v[224:227], v[252:255], a[240:247], %[ix]\n\t" "s_nop 1\n\t" "v_smfmac_f32_16x16x64_bf16 v[228:231], v[252:255], a[248:255], %[ix]\n\t" \
    XT(90, 91) "s_nop 1\n\t" "v_smfmac_f32_16x16x64_bf16 v[232:235], v[192:195], v[112:119], %[ix]\n\t" "s_nop 1\n\t" \
    "v_smfmac_f32_16x16x64_bf16 v[236:239], v[192:195], v[176:183], %[ix]\n\t" "ds_read_b128 v[176:179], %[l0] offset:28672\n\t" "v_add_f32 v224, v224, v225\n\t" \
    "v_add_f32 v226, v226, v227\n\t" "v_smfmac_f32_16x16x64_bf16 v[232:235], v[196:199], v[120:127], %[ix]\n\t" "ds_read_b128 v[180:183], %[l0] offset:29696\n\t" \
    "v_add_f32 v228, v228, v229\n\t" "v_add_f32 v230, v230, v231\n\t" "v_cmp_u_f32 vcc, v224, v226\n\t" \
    "v_smfmac_f32_16x16x64_bf16 v[236:239], v[196:199], v[184:191], %[ix]\n\t" "ds_read_b128 v[184:187], %[l0] offset:30720\n\t" "s_cbranch_vccnz 21f\n\t" "22:\n\t" \
    "v_cvt_pk_bf16_f32 v218, v224, v228\n\t" "v_cvt_pk_bf16_f32 v219, v226, v230\n\t" "v_smfmac_f32_16x16x64_bf16 v[232:235], v[240:243], v[128:135], %[ix]\n\t" \
    "ds_write2_b32 %[qw], v218, v219 offset1:8\n\t" "ds_read_b128 v[188:191], %[l0] offset:31744\n\t" \
    "v_smfmac_f32_16x16x64_bf16 v[236:239], v[240:243], v[160:167], %[ix]\n\t" "v_smfmac_f32_16x16x64_bf16 v[232:235], v[244:247], v[136:143], %[ix]\n\t" \
    "v_smfmac_f32_16x16x64_bf16 v[236:239], v[244:247], v[168:175], %[ix]\n\t" "s_waitcnt lgkmcnt(1)\n\t" "s_barrier\n\t" \
    "v_smfmac_f32_16x16x64_bf16 v[232:235], v[248:251], v[144:151], %[ix]\n\t" "v_smfmac_f32_16x16x64_bf16 v[236:239], v[248:251], v[176:183], %[ix]\n\t" \
    "v_smfmac_f32_16x16x64_bf16 v[232:235], v[252:255], v[152:159], %[ix]\n\t" "s_waitcnt lgkmcnt(0)\n\t" \
    "v_smfmac_f32_16x16x64_bf16 v[236:239], v[252:255], v[184:191], %[ix]\n\t" XT(92, 93) "s_nop 6\n\t" "v_add_f32 v232, v232, v233\n\t" "v_add_f32 v234, v234, v235\n\t" \
    "v_add_f32 v236, v236, v237\n\t" "v_add_f32 v238, v238, v239\n\t" "v_cvt_pk_bf16_f32 v232, v232, v236\n\t" "v_cvt_pk_bf16_f32 v233, v234, v238\n\t" \
    "ds_write2_b32 %[kw], v232, v233 offset1:68\n\t" "s_waitcnt lgkmcnt(0)\n\t" "s_barrier\n\t" XT(94, 95) "s_cmp_ge_u32 %[sq], %[sn]\n\t" "s_cbranch_scc1 277f\n\t" \
    "275:\n\t" "global_load_dword v240, v210, %[fq] sc1\n\t" "s_waitcnt vmcnt(0)\n\t" "v_readfirstlane_b32 %[sq], v240\n\t" "s_nop 3\n\t" "s_cmp_ge_u32 %[sq], %[sn]\n\t" \
    "s_cbranch_scc1 276f\n\t" "s_cmp_eq_u32 %[bd], 0\n\t" "s_cbranch_scc1 276f\n\t" "s_sub_u32 %[bd], %[bd], 1\n\t" "s_sleep 2\n\t" "s_branch 275b\n\t" "276:\n\t" \
    "buffer_load_dwordx4 v[16:19], %[hq], %[rs], %[qb] offen offset:0 sc1\n\t" "buffer_load_dwordx4 v[20:23], %[hq], %[rs], %[qb] offen offset:2048 sc1\n\t" \
    "buffer_load_dwordx4 v[24:27], %[hq], %[rs], s61 offen offset:0 sc1\n\t" "buffer_load_dwordx4 v[28:31], %[hq], %[rs], s61 offen offset:2048 sc1\n\t" "277:\n\t" \
    "s_nop 3\n\t" "ds_read_b128 v[160:163], %[kx] offset:0\n\t" "ds_read_b128 v[164:167], %[kx] offset:2048\n\t" "ds_read_b128 v[168:171], %[kx] offset:4096\n\t" \
    "ds_read_b128 v[172:175], %[kx] offset:6144\n\t" "ds_read_b128 v[224:227], %[km] offset:0\n\t" "ds_read_b128 v[228:231], %[km] offset:64\n\t" \
    "ds_read_b128 v[232:235], %[km] offset:128\n\t" "ds_read_b128 v[236:239], %[km] offset:192\n\t" "s_waitcnt vmcnt(0)\n\t" "global_load_dword v240, v210, %[fq] sc1\n\t" \
    "s_waitcnt lgkmcnt(7)\n\t" "v_mfma_f32_16x16x32_bf16 v[248:251], v[160:163], v[16:19], v[200:203]\n\t" "s_waitcnt lgkmcnt(6)\n\t" \
    "v_mfma_f32_16x16x32_bf16 v[248:251], v[164:167], v[20:23], v[248:251]\n\t" "s_waitcnt lgkmcnt(5)\n\t" \
    "v_mfma_f32_16x16x32_bf16 v[248:251], v[168:171], v[24:27], v[248:251]\n\t" "s_waitcnt lgkmcnt(4)\n\t" \
    "v_mfma_f32_16x16x32_bf16 v[248:251], v[172:175], v[28:31], v[248:251]\n\t" "s_waitcnt lgkmcnt(3)\n\t" \
    "v_mfma_f32_16x16x32_bf16 v[244:247], v[224:227], v[16:19], v[204:207]\n\t" "s_waitcnt lgkmcnt(2)\n\t" \
    "v_mfma_f32_16x16x32_bf16 v[244:247], v[228:231], v[20:23], v[244:247]\n\t" "s_waitcnt lgkmcnt(1)\n\t" \
    "v_mfma_f32_16x16x32_bf16 v[244:247], v[232:235], v[24:27], v[244:247]\n\t" "s_waitcnt lgkmcnt(0)\n\t" \
    "v_mfma_f32_16x16x32_bf16 v[244:247], v[236:239], v[28:31], v[244:247]\n\t" "v_mov_b32 v192, -1\n\t" "v_mov_b32 v193, -1\n\t" "v_mov_b32 v194, -1\n\t" \
    "v_mov_b32 v195, -1\n\t" "v_max3_f32 v242, v248, v249, v250\n\t" "v_max_f32 v242, v242, v251\n\t" "s_nop 1\n\t" "v_max3_f32 v242, v242, v244, v245\n\t" \
    "v_max3_f32 v242, v242, v246, v247\n\t" "v_mov_b32 v243, v242\n\t" "s_nop 1\n\t" "v_permlane32_swap_b32 v242, v243\n\t" "v_max_f32 v242, v242, v243\n\t" \
    "v_mov_b32 v243, v242\n\t" "s_nop 1\n\t" "v_permlane16_swap_b32 v242, v243\n\t" "v_max_f32 v242, v242, v243\n\t" \
    "v_pk_add_f32 v[244:245], v[244:245], v[242:243] op_sel_hi:[1,0] neg_lo:[0,1] neg_hi:[0,1]\n\t" \
    "v_pk_add_f32 v[246:247], v[246:247], v[242:243] op_sel_hi:[1,0] neg_lo:[0,1] neg_hi:[0,1]\n\t" \
    "v_pk_add_f32 v[248:249], v[248:249], v[242:243] op_sel_hi:[1,0] neg_lo:[0,1] neg_hi:[0,1]\n\t" \
    "v_pk_add_f32 v[250:251], v[250:251], v[242:243] op_sel_hi:[1,0] neg_lo:[0,1] neg_hi:[0,1]\n\t" "v_pk_mul_f32 v[244:245], v[244:245], s[66:67]\n\t" \
    "v_pk_mul_f32 v[246:247], v[246:247], s[66:67]\n\t" "v_pk_mul_f32 v[248:249], v[248:249], s[66:67]\n\t" "v_pk_mul_f32 v[250:251], v[250:251], s[66:67]\n\t" \
    "v_exp_f32 v244, v244\n\t" "v_exp_f32 v245, v245\n\t" "v_exp_f32 v246, v246\n\t" "v_exp_f32 v247, v247\n\t" "v_exp_f32 v248, v248\n\t" "v_exp_f32 v249, v249\n\t" \
    "v_exp_f32 v250, v250\n\t" "v_exp_f32 v251, v251\n\t" "v_pk_add_f32 v[242:243], v[244:245], v[246:247]\n\t" "v_pk_add_f32 v[242:243], v[242:243], v[248:249]\n\t" \
    "v_pk_add_f32 v[242:243], v[242:243], v[250:251]\n\t" "v_add_f32 v242, v242, v243\n\t" "v_mov_b32 v243, v242\n\t" "s_nop 1\n\t" "v_permlane32_swap_b32 v242, v243\n\t" \
    "v_add_f32 v242, v242, v243\n\t" "v_mov_b32 v243, v242\n\t" "s_nop 1\n\t" "v_permlane16_swap_b32 v242, v243\n\t" "v_add_f32 v242, v242, v243\n\t" \
    "v_rcp_f32 v242, v242\n\t" "s_nop 0\n\t" "v_pk_mul_f32 v[244:245], v[244:245], v[242:243] op_sel_hi:[1,0]\n\t" \
    "v_pk_mul_f32 v[246:247], v[246:247], v[242:243] op_sel_hi:[1,0]\n\t" "v_pk_mul_f32 v[248:249], v[248:249], v[242:243] op_sel_hi:[1,0]\n\t" \
    "v_pk_mul_f32 v[250:251], v[250:251], v[242:243] op_sel_hi:[1,0]\n\t" "v_cvt_pk_bf16_f32 v162, v244, v245\n\t" "v_cvt_pk_bf16_f32 v163, v246, v247\n\t" \
    "v_cvt_pk_bf16_f32 v164, v248, v249\n\t" "v_cvt_pk_bf16_f32 v165, v250, v251\n\t" "buffer_store_dwordx2 v[164:165], %[px], %[rs], %[tb] offen sc1\n\t" \
    "buffer_store_dwordx2 v[162:163], %[pm], %[rs], %[tb] offen sc1\n\t" "buffer_store_dwordx2 v[192:193], %[px], %[rs], %[tr] offen sc1\n\t" \
    "buffer_store_dwordx2 v[192:193], %[pm], %[rs], %[tr] offen sc1\n\t" "s_waitcnt vmcnt(4)\n\t" "v_readfirstlane_b32 s62, v240\n\t" "s_nop 3\n\t" \
    "s_max_u32 %[sq], %[sq], s62\n\t" XT(96, 97) "s_branch 99f\n\t" "21:\n\t" "s_mov_b64 s[62:63], 0\n\t" "ds_read_b128 v[208:211], %[l1] offset:0\n\t" \
    "ds_read_b128 v[212:215], %[l1] offset:1024\n\t" "ds_read_b128 v[216:219], %[l1] offset:2048\n\t" "ds_read_b128 v[220:223], %[l1] offset:3072\n\t" \
    "s_waitcnt lgkmcnt(3)\n\t" "v_max3_u32 v208, v208, v209, v210\n\t" "v_max_u32 v208, v208, v211\n\t" "v_cmp_eq_u32 vcc, -1, v208\n\t" \
    "ds_read_b128 v[208:211], %[l1] offset:4096\n\t" "s_or_b64 s[62:63], s[62:63], vcc\n\t" "s_waitcnt lgkmcnt(3)\n\t" "v_max3_u32 v212, v212, v213, v214\n\t" \
    "v_max_u32 v212, v212, v215\n\t" "v_cmp_eq_u32 vcc, -1, v212\n\t" "ds_read_b128 v[212:215], %[l1] offset:5120\n\t" "s_or_b64 s[62:63], s[62:63], vcc\n\t" \
    "s_waitcnt lgkmcnt(3)\n\t" "v_max3_u32 v216, v216, v217, v218\n\t" "v_max_u32 v216, v216, v219\n\t" "v_cmp_eq_u32 vcc, -1, v216\n\t" \
    "ds_read_b128 v[216:219], %[l1] offset:6144\n\t" "s_or_b64 s[62:63], s[62:63], vcc\n\t" "s_waitcnt lgkmcnt(3)\n\t" "v_max3_u32 v220, v220, v221, v222\n\t" \
    "v_max_u32 v220, v220, v223\n\t" "v_cmp_eq_u32 vcc, -1, v220\n\t" "ds_read_b128 v[220:223], %[l1] offset:7168\n\t" "s_or_b64 s[62:63], s[62:63], vcc\n\t" \
    "s_waitcnt lgkmcnt(3)\n\t" "v_max3_u32 v208, v208, v209, v210\n\t" "v_max_u32 v208, v208, v211\n\t" "v_cmp_eq_u32 vcc, -1, v208\n\t" \
    "ds_read_b128 v[208:211], %[l1] offset:8192\n\t" "s_or_b64 s[62:63], s[62:63], vcc\n\t" "s_waitcnt lgkmcnt(3)\n\t" "v_max3_u32 v212, v212, v213, v214\n\t" \
    "v_max_u32 v212, v212, v215\n\t" "v_cmp_eq_u32 vcc, -1, v212\n\t" "ds_read_b128 v[212:215], %[l1] offset:9216\n\t" "s_or_b64 s[62:63], s[62:63], vcc\n\t" \
    "s_waitcnt lgkmcnt(3)\n\t" "v_max3_u32 v216, v216, v217, v218\n\t" "v_max_u32 v216, v216, v219\n\t" "v_cmp_eq_u32 vcc, -1, v216\n\t" \
    "ds_read_b128 v[216:219], %[l1] offset:10240\n\t" "s_or_b64 s[62:63], s[62:63], vcc\n\t" "s_waitcnt lgkmcnt(3)\n\t" "v_max3_u32 v220, v220, v221, v222\n\t" \
    "v_max_u32 v220, v220, v223\n\t" "v_cmp_eq_u32 vcc, -1, v220\n\t" "ds_read_b128 v[220:223], %[l1] offset:11264\n\t" "s_or_b64 s[62:63], s[62:63], vcc\n\t" \
    "s_waitcnt lgkmcnt(3)\n\t" "v_max3_u32 v208, v208, v209, v210\n\t" "v_max_u32 v208, v208, v211\n\t" "v_cmp_eq_u32 vcc, -1, v208\n\t" \
    "ds_read_b128 v[208:211], %[l1] offset:12288\n\t" "s_or_b64 s[62:63], s[62:63], vcc\n\t" "s_waitcnt lgkmcnt(3)\n\t" "v_max3_u32 v212, v212, v213, v214\n\t" \
    "v_max_u32 v212, v212, v215\n\t" "v_cmp_eq_u32 vcc, -1, v212\n\t" "ds_read_b128 v[212:215], %[l1] offset:13312\n\t" "s_or_b64 s[62:63], s[62:63], vcc\n\t" \
    "s_waitcnt lgkmcnt(3)\n\t" "v_max3_u32 v216, v216, v217, v218\n\t" "v_max_u32 v216, v216, v219\n\t" "v_cmp_eq_u32 vcc, -1, v216\n\t" \
    "ds_read_b128 v[216:219], %[l1] offset:14336\n\t" "s_or_b64 s[62:63], s[62:63], vcc\n\t" "s_waitcnt lgkmcnt(3)\n\t" "v_max3_u32 v220, v220, v221, v222\n\t" \
    "v_max_u32 v220, v220, v223\n\t" "v_cmp_eq_u32 vcc, -1, v220\n\t" "ds_read_b128 v[220:223], %[l1] offset:15360\n\t" "s_or_b64 s[62:63], s[62:63], vcc\n\t" \
    "s_waitcnt lgkmcnt(3)\n\t" "v_max3_u32 v208, v208, v209, v210\n\t" "v_max_u32 v208, v208, v211\n\t" "v_cmp_eq_u32 vcc, -1, v208\n\t" \
    "s_or_b64 s[62:63], s[62:63], vcc\n\t" "s_waitcnt lgkmcnt(2)\n\t" "v_max3_u32 v212, v212, v213, v214\n\t" "v_max_u32 v212, v212, v215\n\t" \
    "v_cmp_eq_u32 vcc, -1, v212\n\t" "s_or_b64 s[62:63], s[62:63], vcc\n\t" "s_waitcnt lgkmcnt(1)\n\t" "v_max3_u32 v216, v216, v217, v218\n\t" \
    "v_max_u32 v216, v216, v219\n\t" "v_cmp_eq_u32 vcc, -1, v216\n\t" "s_or_b64 s[62:63], s[62:63], vcc\n\t" "s_waitcnt lgkmcnt(0)\n\t" \
    "v_max3_u32 v220, v220, v221, v222\n\t" "v_max_u32 v220, v220, v223\n\t" "v_cmp_eq_u32 vcc, -1, v220\n\t" "s_or_b64 s[62:63], s[62:63], vcc\n\t" \
    "v_mov_b64 v[208:209], 0\n\t" "v_mov_b64 v[210:211], 0\n\t" "v_mov_b64 v[212:213], 0\n\t" "v_mov_b64 v[214:215], 0\n\t" "s_cmp_eq_u64 s[62:63], 0\n\t" \
    "s_cbranch_scc1 22b\n\t" "s_cmp_eq_u32 %[rt], 0\n\t" "s_cbranch_scc1 22b\n\t" "s_sub_u32 %[rt], %[rt], 1\n\t" "v_xor_b32 v216, 0x400, %[mo]\n\t" XCNT(100) "24:\n\t" \
    "buffer_load_dwordx4 v[240:243], %[mo], %[rs], %[sb] offen sc1\n\t" "buffer_load_dwordx4 v[244:247], v216, %[rs], %[sb] offen sc1\n\t" \
    "buffer_load_dwordx4 v[248:251], %[mo], %[rs], s60 offen sc1\n\t" "buffer_load_dwordx4 v[252:255], v216, %[rs], s60 offen sc1\n\t" "s_waitcnt vmcnt(0)\n\t" \
    "v_max_u32 v220, v240, v241\n\t" "v_max3_u32 v220, v220, v242, v243\n\t" "v_max3_u32 v220, v220, v244, v245\n\t" "v_max3_u32 v220, v220, v246, v247\n\t" \
    "v_max3_u32 v220, v220, v248, v249\n\t" "v_max3_u32 v220, v220, v250, v251\n\t" "v_max3_u32 v220, v220, v252, v253\n\t" "v_max3_u32 v220, v220, v254, v255\n\t" \
    "v_cmp_eq_u32 vcc, -1, v220\n\t" "s_cbranch_vccz 25f\n\t" "s_cmp_eq_u32 %[bd], 0\n\t" "s_cbranch_scc1 25f\n\t" "s_sub_u32 %[bd], %[bd], 1\n\t" XCNT(101) "s_sleep 1\n\t" \
    "s_branch 24b\n\t" "25:\n\t" "s_barrier\n\t" "s_branch 20b\n\t" "99:\n\t"
#define QK_STEP2 \
    "v_xor_b32 v216, 0x400, %[mo]\n\t" "s_xor_b32 s60, %[sb], 0x800\n\t" "s_add_u32 s61, %[qb], 0x1000\n\t" "s_mov_b32 s66, 0x3e0293ee\n\t" "s_mov_b32 s67, 0x3e0293ee\n\t" \
    XT(80, 81) XT(98, 99) "s_barrier\n\t" XT(82, 83) "30:\n\t" "buffer_load_dwordx4 v[240:243], %[mo], %[rs], %[sb] offen sc1\n\t" "v_mov_b64 v[224:225], 0\n\t" \
    "v_mov_b64 v[226:227], 0\n\t" "v_mov_b64 v[228:229], 0\n\t" "v_mov_b64 v[230:231], 0\n\t" "v_mov_b64 v[232:233], 0\n\t" "v_mov_b64 v[234:235], 0\n\t" \
    "v_mov_b64 v[236:237], 0\n\t" "v_mov_b64 v[238:239], 0\n\t" "buffer_load_dwordx4 v[244:247], v216, %[rs], %[sb] offen sc1\n\t" \
    "ds_read_b128 v[160:163], %[l0] offset:0\n\t" "ds_read_b128 v[164:167], %[l0] offset:1024\n\t" "ds_read_b128 v[168:171], %[l0] offset:2048\n\t" \
    "ds_read_b128 v[172:175], %[l0] offset:3072\n\t" "s_nop 3\n\t" "buffer_load_dwordx4 v[248:251], %[mo], %[rs], s60 offen sc1\n\t" \
    "ds_read_b128 v[176:179], %[l0] offset:4096\n\t" "ds_read_b128 v[180:183], %[l0] offset:5120\n\t" "ds_read_b128 v[184:187], %[l0] offset:6144\n\t" \
    "ds_read_b128 v[188:191], %[l0] offset:7168\n\t" "s_nop 3\n\t" "buffer_load_dwordx4 v[252:255], v216, %[rs], s60 offen sc1\n\t" "v_mov_b32 v208, 0xd028965f\n\t" \
    "v_mov_b32 v209, 0xff800000\n\t" "s_mov_b32 s62, 0\n\t" "s_mov_b32 s63, -1\n\t" "v_mov_b32 v210, 0\n\t" "v_cmp_gt_i32 vcc, 0, %[ps]\n\t" \
    "v_cndmask_b32 v204, 0, v209, s[62:63]\n\t" "s_nop 0\n\t" "v_cndmask_b32 v200, 0, v208, vcc\n\t" "v_cmp_gt_i32 vcc, 1, %[ps]\n\t" \
    "v_cndmask_b32 v205, 0, v209, s[62:63]\n\t" "s_nop 0\n\t" "v_cndmask_b32 v201, 0, v208, vcc\n\t" "v_cmp_gt_i32 vcc, 2, %[ps]\n\t" \
    "v_cndmask_b32 v206, 0, v209, s[62:63]\n\t" "s_nop 0\n\t" "v_cndmask_b32 v202, 0, v208, vcc\n\t" "v_cmp_gt_i32 vcc, 3, %[ps]\n\t" \
    "v_cndmask_b32 v207, 0, v209, s[62:63]\n\t" "s_nop 0\n\t" "v_cndmask_b32 v203, 0, v208, vcc\n\t" "s_waitcnt vmcnt(3)\n\t" XT(84, 85) \
    "v_smfmac_f32_16x16x64_bf16 v[224:227], v[240:243], a[0:7], %[ix]\n\t" "ds_write_b128 %[l1], v[240:243] offset:8192\n\t" \
    "v_smfmac_f32_16x16x64_bf16 v[228:231], v[240:243], a[8:15], %[ix]\n\t" "v_smfmac_f32_16x16x64_bf16 v[232:235], v[240:243], v[32:39], %[ix]\n\t" \
    "s_waitcnt lgkmcnt(7)\n\t" "v_smfmac_f32_16x16x64_bf16 v[236:239], v[240:243], v[160:167], %[ix]\n\t" "ds_read_b128 v[160:163], %[l0] offset:8192\n\t" \
    "ds_read_b128 v[164:167], %[l0] offset:9216\n\t" "s_waitcnt vmcnt(2)\n\t" "v_smfmac_f32_16x16x64_bf16 v[224:227], v[244:247], a[16:23], %[ix]\n\t" \
    "ds_write_b128 %[l1], v[244:247] offset:9216\n\t" "v_smfmac_f32_16x16x64_bf16 v[228:231], v[244:247], a[24:31], %[ix]\n\t" \
    "v_smfmac_f32_16x16x64_bf16 v[232:235], v[244:247], v[40:47], %[ix]\n\t" "s_waitcnt lgkmcnt(8)\n\t" \
    "v_smfmac_f32_16x16x64_bf16 v[236:239], v[244:247], v[168:175], %[ix]\n\t" "ds_read_b128 v[168:171], %[l0] offset:10240\n\t" \
    "ds_read_b128 v[172:175], %[l0] offset:11264\n\t" "s_waitcnt vmcnt(1)\n\t" "v_smfmac_f32_16x16x64_bf16 v[224:227], v[248:251], a[32:39], %[ix]\n\t" \
    "ds_write_b128 %[l1], v[248:251] offset:10240\n\t" "v_smfmac_f32_16x16x64_bf16 v[228:231], v[248:251], a[40:47], %[ix]\n\t" "s_waitcnt lgkmcnt(3)\n\t" "s_barrier\n\t" \
    XT(86, 87) "v_smfmac_f32_16x16x64_bf16 v[232:235], v[248:251], v[48:55], %[ix]\n\t" "ds_read_b128 v[192:195], %[l1] offset:12288\n\t" \
    "ds_read_b128 v[196:199], %[l1] offset:0\n\t" "v_smfmac_f32_16x16x64_bf16 v[236:239], v[248:251], v[176:183], %[ix]\n\t" "ds_read_b128 v[176:179], %[l0] offset:12288\n\t" \
    "ds_read_b128 v[180:183], %[l0] offset:13312\n\t" "s_waitcnt vmcnt(0)\n\t" "v_smfmac_f32_16x16x64_bf16 v[224:227], v[252:255], a[48:55], %[ix]\n\t" \
    "ds_write_b128 %[l1], v[252:255] offset:11264\n\t" "v_smfmac_f32_16x16x64_bf16 v[228:231], v[252:255], a[56:63], %[ix]\n\t" \
    "ds_read_b128 v[240:243], %[l1] offset:4096\n\t" "ds_read_b128 v[244:247], %[l1] offset:13312\n\t" \
    "v_smfmac_f32_16x16x64_bf16 v[232:235], v[252:255], v[56:63], %[ix]\n\t" "v_smfmac_f32_16x16x64_bf16 v[236:239], v[252:255], v[184:191], %[ix]\n\t" \
    "ds_read_b128 v[184:187], %[l0] offset:14336\n\t" "ds_read_b128 v[188:191], %[l0] offset:15360\n\t" "s_waitcnt lgkmcnt(8)\n\t" \
    "v_smfmac_f32_16x16x64_bf16 v[224:227], v[192:195], a[64:71], %[ix]\n\t" "v_smfmac_f32_16x16x64_bf16 v[228:231], v[192:195], a[72:79], %[ix]\n\t" \
    "v_smfmac_f32_16x16x64_bf16 v[232:235], v[192:195], v[64:71], %[ix]\n\t" "s_waitcnt lgkmcnt(4)\n\t" "s_barrier\n\t" XT(88, 89) \
    "v_smfmac_f32_16x16x64_bf16 v[236:239], v[192:195], v[160:167], %[ix]\n\t" "ds_read_b128 v[160:163], %[l0] offset:16384\n\t" \
    "ds_read_b128 v[164:167], %[l0] offset:17408\n\t" "v_smfmac_f32_16x16x64_bf16 v[224:227], v[196:199], a[80:87], %[ix]\n\t" \
    "ds_read_b128 v[248:251], %[l1] offset:1024\n\t" "ds_read_b128 v[252:255], %[l1] offset:5120\n\t" "v_smfmac_f32_16x16x64_bf16 v[228:231], v[196:199], a[88:95], %[ix]\n\t" \
    "ds_read_b128 v[192:195], %[l1] offset:14336\n\t" "v_smfmac_f32_16x16x64_bf16 v[232:235], v[196:199], v[72:79], %[ix]\n\t" \
    "buffer_load_dwordx4 v[16:19], %[hq], %[rs], %[qb] offen offset:0 sc1\n\t" "v_smfmac_f32_16x16x64_bf16 v[236:239], v[196:199], v[168:175], %[ix]\n\t" \
    "ds_read_b128 v[168:171], %[l0] offset:18432\n\t" "ds_read_b128 v[172:175], %[l0] offset:19456\n\t" "s_waitcnt lgkmcnt(10)\n\t" \
    "v_smfmac_f32_16x16x64_bf16 v[224:227], v[240:243], a[96:103], %[ix]\n\t" "ds_read_b128 v[196:199], %[l1] offset:2048\n\t" \
    "v_smfmac_f32_16x16x64_bf16 v[228:231], v[240:243], a[104:111], %[ix]\n\t" "v_smfmac_f32_16x16x64_bf16 v[232:235], v[240:243], v[80:87], %[ix]\n\t" \
    "buffer_load_dwordx4 v[20:23], %[hq], %[rs], %[qb] offen offset:2048 sc1\n\t" "v_smfmac_f32_16x16x64_bf16 v[236:239], v[240:243], v[176:183], %[ix]\n\t" \
    "ds_read_b128 v[176:179], %[l0] offset:20480\n\t" "ds_read_b128 v[180:183], %[l0] offset:21504\n\t" "s_waitcnt lgkmcnt(12)\n\t" \
    "v_smfmac_f32_16x16x64_bf16 v[224:227], v[244:247], a[112:119], %[ix]\n\t" "ds_read_b128 v[240:243], %[l1] offset:6144\n\t" \
    "v_smfmac_f32_16x16x64_bf16 v[228:231], v[244:247], a[120:127], %[ix]\n\t" "v_smfmac_f32_16x16x64_bf16 v[232:235], v[244:247], v[88:95], %[ix]\n\t" \
    "buffer_load_dwordx4 v[24:27], %[hq], %[rs], s61 offen offset:0 sc1\n\t" "s_waitcnt lgkmcnt(11)\n\t" \
    "v_smfmac_f32_16x16x64_bf16 v[236:239], v[244:247], v[184:191], %[ix]\n\t" "ds_read_b128 v[184:187], %[l0] offset:22528\n\t" \
    "ds_read_b128 v[188:191], %[l0] offset:23552\n\t" "s_waitcnt lgkmcnt(10)\n\t" "v_smfmac_f32_16x16x64_bf16 v[224:227], v[248:251], a[128:135], %[ix]\n\t" \
    "ds_read_b128 v[244:247], %[l1] offset:15360\n\t" "v_smfmac_f32_16x16x64_bf16 v[228:231], v[248:251], a[136:143], %[ix]\n\t" \
    "v_smfmac_f32_16x16x64_bf16 v[232:235], v[248:251], v[96:103], %[ix]\n\t" "buffer_load_dwordx4 v[28:31], %[hq], %[rs], s61 offen offset:2048 sc1\n\t" \
    "v_smfmac_f32_16x16x64_bf16 v[236:239], v[248:251], v[160:167], %[ix]\n\t" "ds_read_b128 v[160:163], %[l0] offset:24576\n\t" \
    "ds_read_b128 v[164:167], %[l0] offset:25600\n\t" "s_waitcnt lgkmcnt(12)\n\t" "v_smfmac_f32_16x16x64_bf16 v[224:227], v[252:255], a[144:151], %[ix]\n\t" \
    "ds_read_b128 v[248:251], %[l1] offset:3072\n\t" "v_smfmac_f32_16x16x64_bf16 v[228:231], v[252:255], a[152:159], %[ix]\n\t" \
    "v_smfmac_f32_16x16x64_bf16 v[232:235], v[252:255], v[104:111], %[ix]\n\t" "s_waitcnt lgkmcnt(10)\n\t" \
    "v_smfmac_f32_16x16x64_bf16 v[236:239], v[252:255], v[168:175], %[ix]\n\t" "ds_read_b128 v[168:171], %[l0] offset:26624\n\t" \
    "ds_read_b128 v[172:175], %[l0] offset:27648\n\t" "v_smfmac_f32_16x16x64_bf16 v[224:227], v[192:195], a[160:167], %[ix]\n\t" \
    "ds_read_b128 v[252:255], %[l1] offset:7168\n\t" "v_smfmac_f32_16x16x64_bf16 v[228:231], v[192:195], a[168:175], %[ix]\n\t" "s_waitcnt lgkmcnt(12)\n\t" \
    "v_smfmac_f32_16x16x64_bf16 v[224:227], v[196:199], a[176:183], %[ix]\n\t" "v_smfmac_f32_16x16x64_bf16 v[228:231], v[196:199], a[184:191], %[ix]\n\t" \
    "s_waitcnt lgkmcnt(9)\n\t" "v_smfmac_f32_16x16x64_bf16 v[224:227], v[240:243], a[192:199], %[ix]\n\t" \
    "v_smfmac_f32_16x16x64_bf16 v[228:231], v[240:243], a[200:207], %[ix]\n\t" "s_waitcnt lgkmcnt(6)\n\t" \
    "v_smfmac_f32_16x16x64_bf16 v[224:227], v[244:247], a[208:215], %[ix]\n\t" "v_smfmac_f32_16x16x64_bf16 v[228:231], v[244:247], a[216:223], %[ix]\n\t" \
    "s_waitcnt lgkmcnt(3)\n\t" "v_smfmac_f32_16x16x64_bf16 v[224:227], v[248:251], a[224:231], %[ix]\n\t" \
    "v_smfmac_f32_16x16x64_bf16 v[228:231], v[248:251], a[232:239], %[ix]\n\t" "s_waitcnt lgkmcnt(0)\n\t" \
    "v_smfmac_f32_16x16x64_bf16 v[224:227], v[252:255], a[240:247], %[ix]\n\t" "s_nop 1\n\t" "v_smfmac_f32_16x16x64_bf16 v[228:231], v[252:255], a[248:255], %[ix]\n\t" \
    XT(90, 91) "s_nop 1\n\t" "v_smfmac_f32_16x16x64_bf16 v[232:235], v[192:195], v[112:119], %[ix]\n\t" "s_nop 1\n\t" \
    "v_smfmac_f32_16x16x64_bf16 v[236:239], v[192:195], v[176:183], %[ix]\n\t" "ds_read_b128 v[176:179], %[l0] offset:28672\n\t" "v_add_f32 v224, v224, v225\n\t" \
    "v_add_f32 v226, v226, v227\n\t" "v_smfmac_f32_16x16x64_bf16 v[232:235], v[196:199], v[120:127], %[ix]\n\t" "ds_read_b128 v[180:183], %[l0] offset:29696\n\t" \
    "v_add_f32 v228, v228, v229\n\t" "v_add_f32 v230, v230, v231\n\t" "v_cmp_u_f32 vcc, v224, v226\n\t" \
    "v_smfmac_f32_16x16x64_bf16 v[236:239], v[196:199], v[184:191], %[ix]\n\t" "ds_read_b128 v[184:187], %[l0] offset:30720\n\t" "s_cbranch_vccnz 31f\n\t" "32:\n\t" \
    "v_cvt_pk_bf16_f32 v218, v224, v228\n\t" "v_cvt_pk_bf16_f32 v219, v226, v230\n\t" "v_smfmac_f32_16x16x64_bf16 v[232:235], v[240:243], v[128:135], %[ix]\n\t" \
    "ds_write2_b32 %[qw], v218, v219 offset1:8\n\t" "ds_read_b128 v[188:191], %[l0] offset:31744\n\t" \
    "v_smfmac_f32_16x16x64_bf16 v[236:239], v[240:243], v[160:167], %[ix]\n\t" "v_smfmac_f32_16x16x64_bf16 v[232:235], v[244:247], v[136:143], %[ix]\n\t" \
    "v_smfmac_f32_16x16x64_bf16 v[236:239], v[244:247], v[168:175], %[ix]\n\t" "s_waitcnt lgkmcnt(1)\n\t" "s_barrier\n\t" \
    "v_smfmac_f32_16x16x64_bf16 v[232:235], v[248:251], v[144:151], %[ix]\n\t" "v_smfmac_f32_16x16x64_bf16 v[236:239], v[248:251], v[176:183], %[ix]\n\t" \
    "v_smfmac_f32_16x16x64_bf16 v[232:235], v[252:255], v[152:159], %[ix]\n\t" "s_waitcnt lgkmcnt(0)\n\t" \
    "v_smfmac_f32_16x16x64_bf16 v[236:239], v[252:255], v[184:191], %[ix]\n\t" XT(92, 93) "s_nop 6\n\t" "v_add_f32 v232, v232, v233\n\t" "v_add_f32 v234, v234, v235\n\t" \
    "v_add_f32 v236, v236, v237\n\t" "v_add_f32 v238, v238, v239\n\t" "v_cvt_pk_bf16_f32 v232, v232, v236\n\t" "v_cvt_pk_bf16_f32 v233, v234, v238\n\t" \
    "ds_write2_b32 %[kw], v232, v233 offset1:68\n\t" "s_waitcnt lgkmcnt(0)\n\t" "s_barrier\n\t" XT(94, 95) "s_cmp_ge_u32 %[sq], %[sn]\n\t" "s_cbranch_scc1 377f\n\t" \
    "375:\n\t" "global_load_dword v240, v210, %[fq] sc1\n\t" "s_waitcnt vmcnt(0)\n\t" "v_readfirstlane_b32 %[sq], v240\n\t" "s_nop 3\n\t" "s_cmp_ge_u32 %[sq], %[sn]\n\t" \
    "s_cbranch_scc1 376f\n\t" "s_cmp_eq_u32 %[bd], 0\n\t" "s_cbranch_scc1 376f\n\t" "s_sub_u32 %[bd], %[bd], 1\n\t" "s_sleep 2\n\t" "s_branch 375b\n\t" "376:\n\t" \
    "buffer_load_dwordx4 v[16:19], %[hq], %[rs], %[qb] offen offset:0 sc1\n\t" "buffer_load_dwordx4 v[20:23], %[hq], %[rs], %[qb] offen offset:2048 sc1\n\t" \
    "buffer_load_dwordx4 v[24:27], %[hq], %[rs], s61 offen offset:0 sc1\n\t" "buffer_load_dwordx4 v[28:31], %[hq], %[rs], s61 offen offset:2048 sc1\n\t" "377:\n\t" \
    "s_nop 3\n\t" "ds_read_b128 v[160:163], %[kx] offset:0\n\t" "ds_read_b128 v[164:167], %[kx] offset:2048\n\t" "ds_read_b128 v[168:171], %[kx] offset:4096\n\t" \
    "ds_read_b128 v[172:175], %[kx] offset:6144\n\t" "ds_read_b128 v[176:179], %[kx] offset:256\n\t" "ds_read_b128 v[180:183], %[kx] offset:2304\n\t" \
    "ds_read_b128 v[184:187], %[kx] offset:4352\n\t" "ds_read_b128 v[188:191], %[kx] offset:6400\n\t" "ds_read_b128 v[224:227], %[km] offset:0\n\t" \
    "ds_read_b128 v[228:231], %[km] offset:64\n\t" "ds_read_b128 v[232:235], %[km] offset:128\n\t" "ds_read_b128 v[236:239], %[km] offset:192\n\t" "s_waitcnt vmcnt(0)\n\t" \
    "global_load_dword v240, v210, %[fq] sc1\n\t" "s_waitcnt lgkmcnt(11)\n\t" "v_mfma_f32_16x16x32_bf16 v[248:251], v[160:163], v[16:19], 0\n\t" "s_waitcnt lgkmcnt(10)\n\t" \
    "v_mfma_f32_16x16x32_bf16 v[248:251], v[164:167], v[20:23], v[248:251]\n\t" "s_waitcnt lgkmcnt(9)\n\t" \
    "v_mfma_f32_16x16x32_bf16 v[248:251], v[168:171], v[24:27], v[248:251]\n\t" "s_waitcnt lgkmcnt(8)\n\t" \
    "v_mfma_f32_16x16x32_bf16 v[248:251], v[172:175], v[28:31], v[248:251]\n\t" "s_waitcnt lgkmcnt(7)\n\t" \
    "v_mfma_f32_16x16x32_bf16 v[252:255], v[176:179], v[16:19], v[200:203]\n\t" "s_waitcnt lgkmcnt(6)\n\t" \
    "v_mfma_f32_16x16x32_bf16 v[252:255], v[180:183], v[20:23], v[252:255]\n\t" "s_waitcnt lgkmcnt(5)\n\t" \
    "v_mfma_f32_16x16x32_bf16 v[252:255], v[184:187], v[24:27], v[252:255]\n\t" "s_waitcnt lgkmcnt(4)\n\t" \
    "v_mfma_f32_16x16x32_bf16 v[252:255], v[188:191], v[28:31], v[252:255]\n\t" "s_waitcnt lgkmcnt(3)\n\t" \
    "v_mfma_f32_16x16x32_bf16 v[244:247], v[224:227], v[16:19], v[204:207]\n\t" "s_waitcnt lgkmcnt(2)\n\t" \
    "v_mfma_f32_16x16x32_bf16 v[244:247], v[228:231], v[20:23], v[244:247]\n\t" "s_waitcnt lgkmcnt(1)\n\t" \
    "v_mfma_f32_16x16x32_bf16 v[244:247], v[232:235], v[24:27], v[244:247]\n\t" "s_waitcnt lgkmcnt(0)\n\t" \
    "v_mfma_f32_16x16x32_bf16 v[244:247], v[236:239], v[28:31], v[244:247]\n\t" "v_mov_b32 v192, -1\n\t" "v_mov_b32 v193, -1\n\t" "v_mov_b32 v194, -1\n\t" \
    "v_mov_b32 v195, -1\n\t" "v_max3_f32 v242, v248, v249, v250\n\t" "v_max3_f32 v242, v242, v251, v252\n\t" "v_max3_f32 v242, v242, v253, v254\n\t" \
    "v_max_f32 v242, v242, v255\n\t" "v_max3_f32 v242, v242, v244, v245\n\t" "v_max3_f32 v242, v242, v246, v247\n\t" "v_mov_b32 v243, v242\n\t" "s_nop 1\n\t" \
    "v_permlane32_swap_b32 v242, v243\n\t" "v_max_f32 v242, v242, v243\n\t" "v_mov_b32 v243, v242\n\t" "s_nop 1\n\t" "v_permlane16_swap_b32 v242, v243\n\t" \
    "v_max_f32 v242, v242, v243\n\t" "v_pk_add_f32 v[244:245], v[244:245], v[242:243] op_sel_hi:[1,0] neg_lo:[0,1] neg_hi:[0,1]\n\t" \
    "v_pk_add_f32 v[246:247], v[246:247], v[242:243] op_sel_hi:[1,0] neg_lo:[0,1] neg_hi:[0,1]\n\t" \
    "v_pk_add_f32 v[248:249], v[248:249], v[242:243] op_sel_hi:[1,0] neg_lo:[0,1] neg_hi:[0,1]\n\t" \
    "v_pk_add_f32 v[250:251], v[250:251], v[242:243] op_sel_hi:[1,0] neg_lo:[0,1] neg_hi:[0,1]\n\t" \
    "v_pk_add_f32 v[252:253], v[252:253], v[242:243] op_sel_hi:[1,0] neg_lo:[0,1] neg_hi:[0,1]\n\t" \
    "v_pk_add_f32 v[254:255], v[254:255], v[242:243] op_sel_hi:[1,0] neg_lo:[0,1] neg_hi:[0,1]\n\t" "v_pk_mul_f32 v[244:245], v[244:245], s[66:67]\n\t" \
    "v_pk_mul_f32 v[246:247], v[246:247], s[66:67]\n\t" "v_pk_mul_f32 v[248:249], v[248:249], s[66:67]\n\t" "v_pk_mul_f32 v[250:251], v[250:251], s[66:67]\n\t" \
    "v_pk_mul_f32 v[252:253], v[252:253], s[66:67]\n\t" "v_pk_mul_f32 v[254:255], v[254:255], s[66:67]\n\t" "v_exp_f32 v244, v244\n\t" "v_exp_f32 v245, v245\n\t" \
    "v_exp_f32 v246, v246\n\t" "v_exp_f32 v247, v247\n\t" "v_exp_f32 v248, v248\n\t" "v_exp_f32 v249, v249\n\t" "v_exp_f32 v250, v250\n\t" "v_exp_f32 v251, v251\n\t" \
    "v_exp_f32 v252, v252\n\t" "v_exp_f32 v253, v253\n\t" "v_exp_f32 v254, v254\n\t" "v_exp_f32 v255, v255\n\t" "v_pk_add_f32 v[242:243], v[244:245], v[246:247]\n\t" \
    "v_pk_add_f32 v[242:243], v[242:243], v[248:249]\n\t" "v_pk_add_f32 v[242:243], v[242:243], v[250:251]\n\t" "v_pk_add_f32 v[242:243], v[242:243], v[252:253]\n\t" \
    "v_pk_add_f32 v[242:243], v[242:243], v[254:255]\n\t" "v_add_f32 v242, v242, v243\n\t" "v_mov_b32 v243, v242\n\t" "s_nop 1\n\t" "v_permlane32_swap_b32 v242, v243\n\t" \
    "v_add_f32 v242, v242, v243\n\t" "v_mov_b32 v243, v242\n\t" "s_nop 1\n\t" "v_permlane16_swap_b32 v242, v243\n\t" "v_add_f32 v242, v242, v243\n\t" \
    "v_rcp_f32 v242, v242\n\t" "s_nop 0\n\t" "v_pk_mul_f32 v[244:245], v[244:245], v[242:243] op_sel_hi:[1,0]\n\t" \
    "v_pk_mul_f32 v[246:247], v[246:247], v[242:243] op_sel_hi:[1,0]\n\t" "v_pk_mul_f32 v[248:249], v[248:249], v[242:243] op_sel_hi:[1,0]\n\t" \
    "v_pk_mul_f32 v[250:251], v[250:251], v[242:243] op_sel_hi:[1,0]\n\t" "v_pk_mul_f32 v[252:253], v[252:253], v[242:243] op_sel_hi:[1,0]\n\t" \
    "v_pk_mul_f32 v[254:255], v[254:255], v[242:243] op_sel_hi:[1,0]\n\t" "v_cvt_pk_bf16_f32 v162, v244, v245\n\t" "v_cvt_pk_bf16_f32 v163, v246, v247\n\t" \
    "v_cvt_pk_bf16_f32 v164, v248, v249\n\t" "v_cvt_pk_bf16_f32 v165, v250, v251\n\t" "v_cvt_pk_bf16_f32 v166, v252, v253\n\t" "v_cvt_pk_bf16_f32 v167, v254, v255\n\t" \
    "buffer_store_dwordx4 v[164:167], %[px], %[rs], %[tb] offen sc1\n\t" "buffer_store_dwordx2 v[162:163], %[pm], %[rs], %[tb] offen sc1\n\t" \
    "buffer_store_dwordx4 v[192:195], %[px], %[rs], %[tr] offen sc1\n\t" "buffer_store_dwordx2 v[192:193], %[pm], %[rs], %[tr] offen sc1\n\t" "s_waitcnt vmcnt(4)\n\t" \
    "v_readfirstlane_b32 s62, v240\n\t" "s_nop 3\n\t" "s_max_u32 %[sq], %[sq], s62\n\t" XT(96, 97) "s_branch 99f\n\t" "31:\n\t" "s_mov_b64 s[62:63], 0\n\t" \
    "ds_read_b128 v[208:211], %[l1] offset:0\n\t" "ds_read_b128 v[212:215], %[l1] offset:1024\n\t" "ds_read_b128 v[216:219], %[l1] offset:2048\n\t" \
    "ds_read_b128 v[220:223], %[l1] offset:3072\n\t" "s_waitcnt lgkmcnt(3)\n\t" "v_max3_u32 v208, v208, v209, v210\n\t" "v_max_u32 v208, v208, v211\n\t" \
    "v_cmp_eq_u32 vcc, -1, v208\n\t" "ds_read_b128 v[208:211], %[l1] offset:4096\n\t" "s_or_b64 s[62:63], s[62:63], vcc\n\t" "s_waitcnt lgkmcnt(3)\n\t" \
    "v_max3_u32 v212, v212, v213, v214\n\t" "v_max_u32 v212, v212, v215\n\t" "v_cmp_eq_u32 vcc, -1, v212\n\t" "ds_read_b128 v[212:215], %[l1] offset:5120\n\t" \
    "s_or_b64 s[62:63], s[62:63], vcc\n\t" "s_waitcnt lgkmcnt(3)\n\t" "v_max3_u32 v216, v216, v217, v218\n\t" "v_max_u32 v216, v216, v219\n\t" \
    "v_cmp_eq_u32 vcc, -1, v216\n\t" "ds_read_b128 v[216:219], %[l1] offset:6144\n\t" "s_or_b64 s[62:63], s[62:63], vcc\n\t" "s_waitcnt lgkmcnt(3)\n\t" \
    "v_max3_u32 v220, v220, v221, v222\n\t" "v_max_u32 v220, v220, v223\n\t" "v_cmp_eq_u32 vcc, -1, v220\n\t" "ds_read_b128 v[220:223], %[l1] offset:7168\n\t" \
    "s_or_b64 s[62:63], s[62:63], vcc\n\t" "s_waitcnt lgkmcnt(3)\n\t" "v_max3_u32 v208, v208, v209, v210\n\t" "v_max_u32 v208, v208, v211\n\t" \
    "v_cmp_eq_u32 vcc, -1, v208\n\t" "ds_read_b128 v[208:211], %[l1] offset:8192\n\t" "s_or_b64 s[62:63], s[62:63], vcc\n\t" "s_waitcnt lgkmcnt(3)\n\t" \
    "v_max3_u32 v212, v212, v213, v214\n\t" "v_max_u32 v212, v212, v215\n\t" "v_cmp_eq_u32 vcc, -1, v212\n\t" "ds_read_b128 v[212:215], %[l1] offset:9216\n\t" \
    "s_or_b64 s[62:63], s[62:63], vcc\n\t" "s_waitcnt lgkmcnt(3)\n\t" "v_max3_u32 v216, v216, v217, v218\n\t" "v_max_u32 v216, v216, v219\n\t" \
    "v_cmp_eq_u32 vcc, -1, v216\n\t" "ds_read_b128 v[216:219], %[l1] offset:10240\n\t" "s_or_b64 s[62:63], s[62:63], vcc\n\t" "s_waitcnt lgkmcnt(3)\n\t" \
    "v_max3_u32 v220, v220, v221, v222\n\t" "v_max_u32 v220, v220, v223\n\t" "v_cmp_eq_u32 vcc, -1, v220\n\t" "ds_read_b128 v[220:223], %[l1] offset:11264\n\t" \
    "s_or_b64 s[62:63], s[62:63], vcc\n\t" "s_waitcnt lgkmcnt(3)\n\t" "v_max3_u32 v208, v208, v209, v210\n\t" "v_max_u32 v208, v208, v211\n\t" \
    "v_cmp_eq_u32 vcc, -1, v208\n\t" "ds_read_b128 v[208:211], %[l1] offset:12288\n\t" "s_or_b64 s[62:63], s[62:63], vcc\n\t" "s_waitcnt lgkmcnt(3)\n\t" \
    "v_max3_u32 v212, v212, v213, v214\n\t" "v_max_u32 v212, v212, v215\n\t" "v_cmp_eq_u32 vcc, -1, v212\n\t" "ds_read_b128 v[212:215], %[l1] offset:13312\n\t" \
    "s_or_b64 s[62:63], s[62:63], vcc\n\t" "s_waitcnt lgkmcnt(3)\n\t" "v_max3_u32 v216, v216, v217, v218\n\t" "v_max_u32 v216, v216, v219\n\t" \
    "v_cmp_eq_u32 vcc, -1, v216\n\t" "ds_read_b128 v[216:219], %[l1] offset:14336\n\t" "s_or_b64 s[62:63], s[62:63], vcc\n\t" "s_waitcnt lgkmcnt(3)\n\t" \
    "v_max3_u32 v220, v220, v221, v222\n\t" "v_max_u32 v220, v220, v223\n\t" "v_cmp_eq_u32 vcc, -1, v220\n\t" "ds_read_b128 v[220:223], %[l1] offset:15360\n\t" \
    "s_or_b64 s[62:63], s[62:63], vcc\n\t" "s_waitcnt lgkmcnt(3)\n\t" "v_max3_u32 v208, v208, v209, v210\n\t" "v_max_u32 v208, v208, v211\n\t" \
    "v_cmp_eq_u32 vcc, -1, v208\n\t" "s_or_b64 s[62:63], s[62:63], vcc\n\t" "s_waitcnt lgkmcnt(2)\n\t" "v_max3_u32 v212, v212, v213, v214\n\t" \
    "v_max_u32 v212, v212, v215\n\t" "v_cmp_eq_u32 vcc, -1, v212\n\t" "s_or_b64 s[62:63], s[62:63], vcc\n\t" "s_waitcnt lgkmcnt(1)\n\t" \
    "v_max3_u32 v216, v216, v217, v218\n\t" "v_max_u32 v216, v216, v219\n\t" "v_cmp_eq_u32 vcc, -1, v216\n\t" "s_or_b64 s[62:63], s[62:63], vcc\n\t" \
    "s_waitcnt lgkmcnt(0)\n\t" "v_max3_u32 v220, v220, v221, v222\n\t" "v_max_u32 v220, v220, v223\n\t" "v_cmp_eq_u32 vcc, -1, v220\n\t" \
    "s_or_b64 s[62:63], s[62:63], vcc\n\t" "v_mov_b64 v[208:209], 0\n\t" "v_mov_b64 v[210:211], 0\n\t" "v_mov_b64 v[212:213], 0\n\t" "v_mov_b64 v[214:215], 0\n\t" \
    "s_cmp_eq_u64 s[62:63], 0\n\t" "s_cbranch_scc1 32b\n\t" "s_cmp_eq_u32 %[rt], 0\n\t" "s_cbranch_scc1 32b\n\t" "s_sub_u32 %[rt], %[rt], 1\n\t" \
    "v_xor_b32 v216, 0x400, %[mo]\n\t" XCNT(100) "34:\n\t" "buffer_load_dwordx4 v[240:243], %[mo], %[rs], %[sb] offen sc1\n\t" \
    "buffer_load_dwordx4 v[244:247], v216, %[rs], %[sb] offen sc1\n\t" "buffer_load_dwordx4 v[248:251], %[mo], %[rs], s60 offen sc1\n\t" \
    "buffer_load_dwordx4 v[252:255], v216, %[rs], s60 offen sc1\n\t" "s_waitcnt vmcnt(0)\n\t" "v_max_u32 v220, v240, v241\n\t" "v_max3_u32 v220, v220, v242, v243\n\t" \
    "v_max3_u32 v220, v220, v244, v245\n\t" "v_max3_u32 v220, v220, v246, v247\n\t" "v_max3_u32 v220, v220, v248, v249\n\t" "v_max3_u32 v220, v220, v250, v251\n\t" \
    "v_max3_u32 v220, v220, v252, v253\n\t" "v_max3_u32 v220, v220, v254, v255\n\t" "v_cmp_eq_u32 vcc, -1, v220\n\t" "s_cbranch_vccz 35f\n\t" "s_cmp_eq_u32 %[bd], 0\n\t" \
    "s_cbranch_scc1 35f\n\t" "s_sub_u32 %[bd], %[bd], 1\n\t" XCNT(101) "s_sleep 1\n\t" "s_branch 34b\n\t" "35:\n\t" "s_barrier\n\t" "s_branch 30b\n\t" "99:\n\t"
#define QK_STEP3 \
    "v_xor_b32 v216, 0x400, %[mo]\n\t" "s_xor_b32 s60, %[sb], 0x800\n\t" "s_mov_b32 s67, %[pb]\n\t" XT(80, 81) "s_getpc_b64 s[62:63]\n\t" "48:\n\t" "s_memtime s[64:65]\n\t" \
    "s_waitcnt lgkmcnt(0)\n\t" "s_sub_u32 s61, %[tg], s64\n\t" "s_cmp_gt_i32 s61, 124\n\t" "s_cbranch_scc1 48b\n\t" "s_max_i32 s61, s61, 0\n\t" "s_and_b32 s61, s61, 124\n\t" \
    "s_sub_u32 s61, 49f-48b, s61\n\t" "s_add_u32 s62, s62, s61\n\t" "s_addc_u32 s63, s63, 0\n\t" "s_setpc_b64 s[62:63]\n\t" ".rept 31\n\t" "s_nop 0\n\t" ".endr\n\t" "49:\n\t" \
    XT(98, 99) "s_barrier\n\t" XT(82, 83) "40:\n\t" "buffer_load_dwordx4 v[240:243], %[mo], %[rs], %[sb] offen sc1\n\t" "v_mov_b64 v[224:225], 0\n\t" \
    "v_mov_b64 v[226:227], 0\n\t" "v_mov_b64 v[228:229], 0\n\t" "v_mov_b64 v[230:231], 0\n\t" "v_mov_b64 v[232:233], 0\n\t" "v_mov_b64 v[234:235], 0\n\t" \
    "v_mov_b64 v[236:237], 0\n\t" "v_mov_b64 v[238:239], 0\n\t" "buffer_load_dwordx4 v[244:247], v216, %[rs], %[sb] offen sc1\n\t" \
    "ds_read_b128 v[160:163], %[l0] offset:0\n\t" "ds_read_b128 v[164:167], %[l0] offset:1024\n\t" "ds_read_b128 v[168:171], %[l0] offset:2048\n\t" \
    "ds_read_b128 v[172:175], %[l0] offset:3072\n\t" "s_nop 3\n\t" "buffer_load_dwordx4 v[248:251], %[mo], %[rs], s60 offen sc1\n\t" \
    "ds_read_b128 v[176:179], %[l0] offset:4096\n\t" "ds_read_b128 v[180:183], %[l0] offset:5120\n\t" "ds_read_b128 v[184:187], %[l0] offset:6144\n\t" \
    "ds_read_b128 v[188:191], %[l0] offset:7168\n\t" "s_nop 3\n\t" "buffer_load_dwordx4 v[252:255], v216, %[rs], s60 offen sc1\n\t" "s_waitcnt vmcnt(3)\n\t" XT(84, 85) \
    "v_smfmac_f32_16x16x64_bf16 v[224:227], v[240:243], a[0:7], %[ix]\n\t" "ds_write_b128 %[l1], v[240:243] offset:12288\n\t" \
    "v_smfmac_f32_16x16x64_bf16 v[228:231], v[240:243], a[8:15], %[ix]\n\t" "v_smfmac_f32_16x16x64_bf16 v[232:235], v[240:243], v[32:39], %[ix]\n\t" \
    "s_waitcnt lgkmcnt(7)\n\t" "v_smfmac_f32_16x16x64_bf16 v[236:239], v[240:243], v[160:167], %[ix]\n\t" "ds_read_b128 v[160:163], %[l0] offset:8192\n\t" \
    "ds_read_b128 v[164:167], %[l0] offset:9216\n\t" "s_waitcnt vmcnt(2)\n\t" "v_smfmac_f32_16x16x64_bf16 v[224:227], v[244:247], a[16:23], %[ix]\n\t" \
    "ds_write_b128 %[l1], v[244:247] offset:13312\n\t" "v_smfmac_f32_16x16x64_bf16 v[228:231], v[244:247], a[24:31], %[ix]\n\t" \
    "v_smfmac_f32_16x16x64_bf16 v[232:235], v[244:247], v[40:47], %[ix]\n\t" "s_waitcnt lgkmcnt(8)\n\t" \
    "v_smfmac_f32_16x16x64_bf16 v[236:239], v[244:247], v[168:175], %[ix]\n\t" "ds_read_b128 v[168:171], %[l0] offset:10240\n\t" \
    "ds_read_b128 v[172:175], %[l0] offset:11264\n\t" "s_waitcnt vmcnt(1)\n\t" "v_smfmac_f32_16x16x64_bf16 v[224:227], v[248:251], a[32:39], %[ix]\n\t" \
    "ds_write_b128 %[l1], v[248:251] offset:14336\n\t" "v_smfmac_f32_16x16x64_bf16 v[228:231], v[248:251], a[40:47], %[ix]\n\t" "s_waitcnt lgkmcnt(3)\n\t" "s_barrier\n\t" \
    XT(86, 87) "v_smfmac_f32_16x16x64_bf16 v[232:235], v[248:251], v[48:55], %[ix]\n\t" "ds_read_b128 v[192:195], %[l1] offset:0\n\t" \
    "ds_read_b128 v[196:199], %[l1] offset:4096\n\t" "v_smfmac_f32_16x16x64_bf16 v[236:239], v[248:251], v[176:183], %[ix]\n\t" \
    "ds_read_b128 v[176:179], %[l0] offset:12288\n\t" "ds_read_b128 v[180:183], %[l0] offset:13312\n\t" "s_waitcnt vmcnt(0)\n\t" \
    "v_smfmac_f32_16x16x64_bf16 v[224:227], v[252:255], a[48:55], %[ix]\n\t" "ds_write_b128 %[l1], v[252:255] offset:15360\n\t" \
    "v_smfmac_f32_16x16x64_bf16 v[228:231], v[252:255], a[56:63], %[ix]\n\t" "ds_read_b128 v[240:243], %[l1] offset:8192\n\t" "ds_read_b128 v[244:247], %[l1] offset:1024\n\t" \
    "v_smfmac_f32_16x16x64_bf16 v[232:235], v[252:255], v[56:63], %[ix]\n\t" "v_smfmac_f32_16x16x64_bf16 v[236:239], v[252:255], v[184:191], %[ix]\n\t" \
    "ds_read_b128 v[184:187], %[l0] offset:14336\n\t" "ds_read_b128 v[188:191], %[l0] offset:15360\n\t" "s_waitcnt lgkmcnt(8)\n\t" \
    "v_smfmac_f32_16x16x64_bf16 v[224:227], v[192:195], a[64:71], %[ix]\n\t" "v_smfmac_f32_16x16x64_bf16 v[228:231], v[192:195], a[72:79], %[ix]\n\t" \
    "v_smfmac_f32_16x16x64_bf16 v[232:235], v[192:195], v[64:71], %[ix]\n\t" "s_waitcnt lgkmcnt(4)\n\t" "s_barrier\n\t" XT(88, 89) \
    "v_smfmac_f32_16x16x64_bf16 v[236:239], v[192:195], v[160:167], %[ix]\n\t" "ds_read_b128 v[160:163], %[l0] offset:16384\n\t" \
    "ds_read_b128 v[164:167], %[l0] offset:17408\n\t" "v_smfmac_f32_16x16x64_bf16 v[224:227], v[196:199], a[80:87], %[ix]\n\t" \
    "ds_read_b128 v[248:251], %[l1] offset:5120\n\t" "ds_read_b128 v[252:255], %[l1] offset:9216\n\t" "v_smfmac_f32_16x16x64_bf16 v[228:231], v[196:199], a[88:95], %[ix]\n\t" \
    "ds_read_b128 v[192:195], %[l1] offset:2048\n\t" "v_smfmac_f32_16x16x64_bf16 v[232:235], v[196:199], v[72:79], %[ix]\n\t" \
    "v_smfmac_f32_16x16x64_bf16 v[236:239], v[196:199], v[168:175], %[ix]\n\t" "ds_read_b128 v[168:171], %[l0] offset:18432\n\t" \
    "ds_read_b128 v[172:175], %[l0] offset:19456\n\t" "s_waitcnt lgkmcnt(10)\n\t" "v_smfmac_f32_16x16x64_bf16 v[224:227], v[240:243], a[96:103], %[ix]\n\t" \
    "ds_read_b128 v[196:199], %[l1] offset:6144\n\t" "v_smfmac_f32_16x16x64_bf16 v[228:231], v[240:243], a[104:111], %[ix]\n\t" \
    "v_smfmac_f32_16x16x64_bf16 v[232:235], v[240:243], v[80:87], %[ix]\n\t" "v_smfmac_f32_16x16x64_bf16 v[236:239], v[240:243], v[176:183], %[ix]\n\t" \
    "ds_read_b128 v[176:179], %[l0] offset:20480\n\t" "ds_read_b128 v[180:183], %[l0] offset:21504\n\t" "s_waitcnt lgkmcnt(12)\n\t" \
    "v_smfmac_f32_16x16x64_bf16 v[224:227], v[244:247], a[112:119], %[ix]\n\t" "ds_read_b128 v[240:243], %[l1] offset:10240\n\t" \
    "v_smfmac_f32_16x16x64_bf16 v[228:231], v[244:247], a[120:127], %[ix]\n\t" "v_smfmac_f32_16x16x64_bf16 v[232:235], v[244:247], v[88:95], %[ix]\n\t" \
    "s_waitcnt lgkmcnt(11)\n\t" "v_smfmac_f32_16x16x64_bf16 v[236:239], v[244:247], v[184:191], %[ix]\n\t" "ds_read_b128 v[184:187], %[l0] offset:22528\n\t" \
    "ds_read_b128 v[188:191], %[l0] offset:23552\n\t" "s_waitcnt lgkmcnt(10)\n\t" "v_smfmac_f32_16x16x64_bf16 v[224:227], v[248:251], a[128:135], %[ix]\n\t" \
    "ds_read_b128 v[244:247], %[l1] offset:3072\n\t" "v_smfmac_f32_16x16x64_bf16 v[228:231], v[248:251], a[136:143], %[ix]\n\t" \
    "v_smfmac_f32_16x16x64_bf16 v[232:235], v[248:251], v[96:103], %[ix]\n\t" "v_smfmac_f32_16x16x64_bf16 v[236:239], v[248:251], v[160:167], %[ix]\n\t" \
    "ds_read_b128 v[160:163], %[l0] offset:24576\n\t" "ds_read_b128 v[164:167], %[l0] offset:25600\n\t" "s_waitcnt lgkmcnt(12)\n\t" \
    "v_smfmac_f32_16x16x64_bf16 v[224:227], v[252:255], a[144:151], %[ix]\n\t" "ds_read_b128 v[248:251], %[l1] offset:7168\n\t" \
    "v_smfmac_f32_16x16x64_bf16 v[228:231], v[252:255], a[152:159], %[ix]\n\t" "v_smfmac_f32_16x16x64_bf16 v[232:235], v[252:255], v[104:111], %[ix]\n\t" \
    "s_waitcnt lgkmcnt(10)\n\t" "v_smfmac_f32_16x16x64_bf16 v[236:239], v[252:255], v[168:175], %[ix]\n\t" "ds_read_b128 v[168:171], %[l0] offset:26624\n\t" \
    "ds_read_b128 v[172:175], %[l0] offset:27648\n\t" "v_smfmac_f32_16x16x64_bf16 v[224:227], v[192:195], a[160:167], %[ix]\n\t" \
    "ds_read_b128 v[252:255], %[l1] offset:11264\n\t" "v_smfmac_f32_16x16x64_bf16 v[228:231], v[192:195], a[168:175], %[ix]\n\t" "s_waitcnt lgkmcnt(12)\n\t" \
    "v_smfmac_f32_16x16x64_bf16 v[224:227], v[196:199], a[176:183], %[ix]\n\t" "v_smfmac_f32_16x16x64_bf16 v[228:231], v[196:199], a[184:191], %[ix]\n\t" \
    "s_waitcnt lgkmcnt(9)\n\t" "v_smfmac_f32_16x16x64_bf16 v[224:227], v[240:243], a[192:199], %[ix]\n\t" \
    "v_smfmac_f32_16x16x64_bf16 v[228:231], v[240:243], a[200:207], %[ix]\n\t" "s_waitcnt lgkmcnt(6)\n\t" \
    "v_smfmac_f32_16x16x64_bf16 v[224:227], v[244:247], a[208:215], %[ix]\n\t" "v_smfmac_f32_16x16x64_bf16 v[228:231], v[244:247], a[216:223], %[ix]\n\t" \
    "s_waitcnt lgkmcnt(3)\n\t" "v_smfmac_f32_16x16x64_bf16 v[224:227], v[248:251], a[224:231], %[ix]\n\t" \
    "v_smfmac_f32_16x16x64_bf16 v[228:231], v[248:251], a[232:239], %[ix]\n\t" "s_waitcnt lgkmcnt(0)\n\t" \
    "v_smfmac_f32_16x16x64_bf16 v[224:227], v[252:255], a[240:247], %[ix]\n\t" "s_nop 1\n\t" "v_smfmac_f32_16x16x64_bf16 v[228:231], v[252:255], a[248:255], %[ix]\n\t" \
    XT(90, 91) "s_nop 1\n\t" "v_smfmac_f32_16x16x64_bf16 v[232:235], v[192:195], v[112:119], %[ix]\n\t" "s_nop 1\n\t" \
    "v_smfmac_f32_16x16x64_bf16 v[236:239], v[192:195], v[176:183], %[ix]\n\t" "ds_read_b128 v[176:179], %[l0] offset:28672\n\t" "v_add_f32 v224, v224, v225\n\t" \
    "v_add_f32 v226, v226, v227\n\t" "v_smfmac_f32_16x16x64_bf16 v[232:235], v[196:199], v[120:127], %[ix]\n\t" "ds_read_b128 v[180:183], %[l0] offset:29696\n\t" \
    "v_add_f32 v228, v228, v229\n\t" "v_add_f32 v230, v230, v231\n\t" "v_cmp_u_f32 vcc, v224, v226\n\t" \
    "v_smfmac_f32_16x16x64_bf16 v[236:239], v[196:199], v[184:191], %[ix]\n\t" "ds_read_b128 v[184:187], %[l0] offset:30720\n\t" "s_cbranch_vccnz 41f\n\t" "42:\n\t" \
    "v_cvt_pk_bf16_f32 v218, v224, v228\n\t" "v_cvt_pk_bf16_f32 v219, v226, v230\n\t" "v_smfmac_f32_16x16x64_bf16 v[232:235], v[240:243], v[128:135], %[ix]\n\t" \
    "s_memtime s[64:65]\n\t" "ds_write2_b32 %[qw], v218, v219 offset1:8\n\t" "v_smfmac_f32_16x16x64_bf16 v[236:239], v[240:243], v[160:167], %[ix]\n\t" \
    "ds_read_b128 v[188:191], %[l0] offset:31744\n\t" "v_smfmac_f32_16x16x64_bf16 v[232:235], v[244:247], v[136:143], %[ix]\n\t" \
    "v_smfmac_f32_16x16x64_bf16 v[236:239], v[244:247], v[168:175], %[ix]\n\t" "s_waitcnt lgkmcnt(1)\n\t" "s_barrier\n\t" \
    "v_smfmac_f32_16x16x64_bf16 v[232:235], v[248:251], v[144:151], %[ix]\n\t" "s_sub_u32 s62, s64, %[ca]\n\t" "s_mul_hi_u32 s63, s62, %[k1]\n\t" \
    "s_mul_i32 s62, s62, %[k1]\n\t" "v_smfmac_f32_16x16x64_bf16 v[236:239], v[248:251], v[176:183], %[ix]\n\t" "s_lshr_b64 s[62:63], s[62:63], 16\n\t" \
    "s_add_u32 s62, s62, %[ga]\n\t" "s_bitset0_b32 s62, 0\n\t" "v_smfmac_f32_16x16x64_bf16 v[232:235], v[252:255], v[152:159], %[ix]\n\t" "s_waitcnt lgkmcnt(0)\n\t" \
    "v_smfmac_f32_16x16x64_bf16 v[236:239], v[252:255], v[184:191], %[ix]\n\t" XT(92, 93) "v_mov_b32 v217, s62\n\t" "buffer_store_dword v217, %[hq], %[rs], s67 offen sc1\n\t" \
    "s_nop 4\n\t" "v_add_f32 v232, v232, v233\n\t" "v_add_f32 v234, v234, v235\n\t" "v_add_f32 v236, v236, v237\n\t" "v_add_f32 v238, v238, v239\n\t" \
    "v_cvt_pk_bf16_f32 v232, v232, v236\n\t" "v_cvt_pk_bf16_f32 v233, v234, v238\n\t" "ds_write2_b32 %[kw], v232, v233 offset1:68\n\t" "s_waitcnt lgkmcnt(0)\n\t" \
    "s_barrier\n\t" XT(94, 95) XT(96, 97) "s_branch 99f\n\t" "41:\n\t" "s_mov_b64 s[62:63], 0\n\t" "ds_read_b128 v[208:211], %[l1] offset:0\n\t" \
    "ds_read_b128 v[212:215], %[l1] offset:1024\n\t" "ds_read_b128 v[216:219], %[l1] offset:2048\n\t" "ds_read_b128 v[220:223], %[l1] offset:3072\n\t" \
    "s_waitcnt lgkmcnt(3)\n\t" "v_max3_u32 v208, v208, v209, v210\n\t" "v_max_u32 v208, v208, v211\n\t" "v_cmp_eq_u32 vcc, -1, v208\n\t" \
    "ds_read_b128 v[208:211], %[l1] offset:4096\n\t" "s_or_b64 s[62:63], s[62:63], vcc\n\t" "s_waitcnt lgkmcnt(3)\n\t" "v_max3_u32 v212, v212, v213, v214\n\t" \
    "v_max_u32 v212, v212, v215\n\t" "v_cmp_eq_u32 vcc, -1, v212\n\t" "ds_read_b128 v[212:215], %[l1] offset:5120\n\t" "s_or_b64 s[62:63], s[62:63], vcc\n\t" \
    "s_waitcnt lgkmcnt(3)\n\t" "v_max3_u32 v216, v216, v217, v218\n\t" "v_max_u32 v216, v216, v219\n\t" "v_cmp_eq_u32 vcc, -1, v216\n\t" \
    "ds_read_b128 v[216:219], %[l1] offset:6144\n\t" "s_or_b64 s[62:63], s[62:63], vcc\n\t" "s_waitcnt lgkmcnt(3)\n\t" "v_max3_u32 v220, v220, v221, v222\n\t" \
    "v_max_u32 v220, v220, v223\n\t" "v_cmp_eq_u32 vcc, -1, v220\n\t" "ds_read_b128 v[220:223], %[l1] offset:7168\n\t" "s_or_b64 s[62:63], s[62:63], vcc\n\t" \
    "s_waitcnt lgkmcnt(3)\n\t" "v_max3_u32 v208, v208, v209, v210\n\t" "v_max_u32 v208, v208, v211\n\t" "v_cmp_eq_u32 vcc, -1, v208\n\t" \
    "ds_read_b128 v[208:211], %[l1] offset:8192\n\t" "s_or_b64 s[62:63], s[62:63], vcc\n\t" "s_waitcnt lgkmcnt(3)\n\t" "v_max3_u32 v212, v212, v213, v214\n\t" \
    "v_max_u32 v212, v212, v215\n\t" "v_cmp_eq_u32 vcc, -1, v212\n\t" "ds_read_b128 v[212:215], %[l1] offset:9216\n\t" "s_or_b64 s[62:63], s[62:63], vcc\n\t" \
    "s_waitcnt lgkmcnt(3)\n\t" "v_max3_u32 v216, v216, v217, v218\n\t" "v_max_u32 v216, v216, v219\n\t" "v_cmp_eq_u32 vcc, -1, v216\n\t" \
    "ds_read_b128 v[216:219], %[l1] offset:10240\n\t" "s_or_b64 s[62:63], s[62:63], vcc\n\t" "s_waitcnt lgkmcnt(3)\n\t" "v_max3_u32 v220, v220, v221, v222\n\t" \
    "v_max_u32 v220, v220, v223\n\t" "v_cmp_eq_u32 vcc, -1, v220\n\t" "ds_read_b128 v[220:223], %[l1] offset:11264\n\t" "s_or_b64 s[62:63], s[62:63], vcc\n\t" \
    "s_waitcnt lgkmcnt(3)\n\t" "v_max3_u32 v208, v208, v209, v210\n\t" "v_max_u32 v208, v208, v211\n\t" "v_cmp_eq_u32 vcc, -1, v208\n\t" \
    "ds_read_b128 v[208:211], %[l1] offset:12288\n\t" "s_or_b64 s[62:63], s[62:63], vcc\n\t" "s_waitcnt lgkmcnt(3)\n\t" "v_max3_u32 v212, v212, v213, v214\n\t" \
    "v_max_u32 v212, v212, v215\n\t" "v_cmp_eq_u32 vcc, -1, v212\n\t" "ds_read_b128 v[212:215], %[l1] offset:13312\n\t" "s_or_b64 s[62:63], s[62:63], vcc\n\t" \
    "s_waitcnt lgkmcnt(3)\n\t" "v_max3_u32 v216, v216, v217, v218\n\t" "v_max_u32 v216, v216, v219\n\t" "v_cmp_eq_u32 vcc, -1, v216\n\t" \
    "ds_read_b128 v[216:219], %[l1] offset:14336\n\t" "s_or_b64 s[62:63], s[62:63], vcc\n\t" "s_waitcnt lgkmcnt(3)\n\t" "v_max3_u32 v220, v220, v221, v222\n\t" \
    "v_max_u32 v220, v220, v223\n\t" "v_cmp_eq_u32 vcc, -1, v220\n\t" "ds_read_b128 v[220:223], %[l1] offset:15360\n\t" "s_or_b64 s[62:63], s[62:63], vcc\n\t" \
    "s_waitcnt lgkmcnt(3)\n\t" "v_max3_u32 v208, v208, v209, v210\n\t" "v_max_u32 v208, v208, v211\n\t" "v_cmp_eq_u32 vcc, -1, v208\n\t" \
    "s_or_b64 s[62:63], s[62:63], vcc\n\t" "s_waitcnt lgkmcnt(2)\n\t" "v_max3_u32 v212, v212, v213, v214\n\t" "v_max_u32 v212, v212, v215\n\t" \
    "v_cmp_eq_u32 vcc, -1, v212\n\t" "s_or_b64 s[62:63], s[62:63], vcc\n\t" "s_waitcnt lgkmcnt(1)\n\t" "v_max3_u32 v216, v216, v217, v218\n\t" \
    "v_max_u32 v216, v216, v219\n\t" "v_cmp_eq_u32 vcc, -1, v216\n\t" "s_or_b64 s[62:63], s[62:63], vcc\n\t" "s_waitcnt lgkmcnt(0)\n\t" \
    "v_max3_u32 v220, v220, v221, v222\n\t" "v_max_u32 v220, v220, v223\n\t" "v_cmp_eq_u32 vcc, -1, v220\n\t" "s_or_b64 s[62:63], s[62:63], vcc\n\t" \
    "v_mov_b64 v[208:209], 0\n\t" "v_mov_b64 v[210:211], 0\n\t" "v_mov_b64 v[212:213], 0\n\t" "v_mov_b64 v[214:215], 0\n\t" "s_cmp_eq_u64 s[62:63], 0\n\t" \
    "s_cbranch_scc1 42b\n\t" "s_cmp_eq_u32 %[rt], 0\n\t" "s_cbranch_scc1 42b\n\t" "s_sub_u32 %[rt], %[rt], 1\n\t" "v_xor_b32 v216, 0x400, %[mo]\n\t" XCNT(100) "44:\n\t" \
    "buffer_load_dwordx4 v[240:243], %[mo], %[rs], %[sb] offen sc1\n\t" "buffer_load_dwordx4 v[244:247], v216, %[rs], %[sb] offen sc1\n\t" \
    "buffer_load_dwordx4 v[248:251], %[mo], %[rs], s60 offen sc1\n\t" "buffer_load_dwordx4 v[252:255], v216, %[rs], s60 offen sc1\n\t" "s_waitcnt vmcnt(0)\n\t" \
    "v_max_u32 v220, v240, v241\n\t" "v_max3_u32 v220, v220, v242, v243\n\t" "v_max3_u32 v220, v220, v244, v245\n\t" "v_max3_u32 v220, v220, v246, v247\n\t" \
    "v_max3_u32 v220, v220, v248, v249\n\t" "v_max3_u32 v220, v220, v250, v251\n\t" "v_max3_u32 v220, v220, v252, v253\n\t" "v_max3_u32 v220, v220, v254, v255\n\t" \
    "v_cmp_eq_u32 vcc, -1, v220\n\t" "s_cbranch_vccz 45f\n\t" "s_cmp_eq_u32 %[bd], 0\n\t" "s_cbranch_scc1 45f\n\t" "s_sub_u32 %[bd], %[bd], 1\n\t" XCNT(101) "s_sleep 1\n\t" \
    "s_branch 44b\n\t" "45:\n\t" "s_barrier\n\t" "s_memtime s[64:65]\n\t" "s_waitcnt lgkmcnt(0)\n\t" "s_add_u32 s64, s64, %[dr]\n\t" "s_sub_u32 s62, s64, %[ca]\n\t" \
    "s_mul_hi_u32 s63, s62, %[k1]\n\t" "s_mul_i32 s62, s62, %[k1]\n\t" "s_lshr_b64 s[62:63], s[62:63], 16\n\t" "s_add_u32 s62, s62, %[ga]\n\t" "s_bitset0_b32 s62, 0\n\t" \
    "v_mov_b32 v220, s62\n\t" "buffer_store_dword v220, %[hq], %[rs], s67 offen sc1\n\t" "s_mov_b32 s67, 0x7f000000\n\t" "s_branch 40b\n\t" "99:\n\t" "mk_tail:\n\t"
#define VO_STEP \
    "v_xor_b32 v224, 0x400, %[mo]\n\t" "s_xor_b32 s60, %[sb], 0x800\n\t" "s_mov_b32 s67, %[pb]\n\t" "s_cmp_eq_u32 %[w], 1\n\t" "s_cbranch_scc1 91f\n\t" \
    "s_cmp_eq_u32 %[w], 2\n\t" "s_cbranch_scc1 92f\n\t" "s_cmp_eq_u32 %[w], 3\n\t" "s_cbranch_scc1 93f\n\t" XT(80, 81) "s_getpc_b64 s[62:63]\n\t" "18:\n\t" \
    "s_memtime s[64:65]\n\t" "s_waitcnt lgkmcnt(0)\n\t" "s_sub_u32 s61, %[tg], s64\n\t" "s_cmp_gt_i32 s61, 124\n\t" "s_cbranch_scc1 18b\n\t" "s_max_i32 s61, s61, 0\n\t" \
    "s_and_b32 s61, s61, 124\n\t" "s_sub_u32 s61, 19f-18b, s61\n\t" "s_add_u32 s62, s62, s61\n\t" "s_addc_u32 s63, s63, 0\n\t" "s_setpc_b64 s[62:63]\n\t" ".rept 31\n\t" \
    "s_nop 0\n\t" ".endr\n\t" "19:\n\t" "s_barrier\n\t" XT(82, 83) "10:\n\t" "buffer_load_dwordx4 v[240:243], %[mo], %[rs], %[sb] offen sc1\n\t" "v_mov_b64 v[232:233], 0\n\t" \
    "v_mov_b64 v[234:235], 0\n\t" "v_mov_b64 v[236:237], 0\n\t" "v_mov_b64 v[238:239], 0\n\t" "v_mov_b64 v[192:193], 0\n\t" "v_mov_b64 v[194:195], 0\n\t" \
    "v_mov_b64 v[196:197], 0\n\t" "v_mov_b64 v[198:199], 0\n\t" "buffer_load_dwordx4 v[244:247], v224, %[rs], %[sb] offen sc1\n\t" \
    "ds_read_b128 v[160:163], %[l0] offset:0\n\t" "ds_read_b128 v[164:167], %[l0] offset:1024\n\t" "ds_read_b128 v[168:171], %[l0] offset:2048\n\t" \
    "ds_read_b128 v[172:175], %[l0] offset:3072\n\t" "s_nop 3\n\t" "buffer_load_dwordx4 v[248:251], %[mo], %[rs], s60 offen sc1\n\t" \
    "ds_read_b128 v[176:179], %[l0] offset:4096\n\t" "ds_read_b128 v[180:183], %[l0] offset:5120\n\t" "ds_read_b128 v[184:187], %[l0] offset:6144\n\t" \
    "ds_read_b128 v[188:191], %[l0] offset:7168\n\t" "s_nop 3\n\t" "buffer_load_dwordx4 v[252:255], v224, %[rs], s60 offen sc1\n\t" "v_mov_b64 v[200:201], 0\n\t" \
    "v_mov_b64 v[202:203], 0\n\t" "v_mov_b64 v[204:205], 0\n\t" "v_mov_b64 v[206:207], 0\n\t" "v_mov_b64 v[208:209], 0\n\t" "v_mov_b64 v[210:211], 0\n\t" \
    "v_mov_b64 v[212:213], 0\n\t" "v_mov_b64 v[214:215], 0\n\t" "v_mov_b64 v[216:217], 0\n\t" "v_mov_b64 v[218:219], 0\n\t" "v_mov_b64 v[220:221], 0\n\t" \
    "v_mov_b64 v[222:223], 0\n\t" "s_waitcnt vmcnt(3)\n\t" XT(84, 85) "v_smfmac_f32_16x16x64_bf16 v[232:235], v[240:243], a[0:7], %[ix]\n\t" \
    "ds_write_b128 %[l1], v[240:243] offset:0\n\t" "v_smfmac_f32_16x16x64_bf16 v[236:239], v[240:243], a[8:15], %[ix]\n\t" "s_waitcnt vmcnt(2)\n\t" \
    "v_smfmac_f32_16x16x64_bf16 v[232:235], v[244:247], a[16:23], %[ix]\n\t" "ds_write_b128 %[l1], v[244:247] offset:1024\n\t" "s_waitcnt lgkmcnt(8)\n\t" \
    "v_smfmac_f32_16x16x64_bf16 v[236:239], v[244:247], v[160:167], %[ix]\n\t" "s_waitcnt lgkmcnt(0)\n\t" "s_barrier\n\t" XT(86, 87) \
    "ds_read_b128 v[16:19], %[l1] offset:4096\n\t" "ds_read_b128 v[20:23], %[l1] offset:8192\n\t" "ds_read_b128 v[24:27], %[l1] offset:12288\n\t" "s_waitcnt vmcnt(1)\n\t" \
    "v_smfmac_f32_16x16x64_bf16 v[232:235], v[248:251], a[24:31], %[ix]\n\t" "ds_write_b128 %[l1], v[248:251] offset:2048\n\t" \
    "v_smfmac_f32_16x16x64_bf16 v[236:239], v[248:251], a[32:39], %[ix]\n\t" "ds_read_b128 v[28:31], %[l1] offset:5120\n\t" "ds_read_b128 v[240:243], %[l1] offset:9216\n\t" \
    "ds_read_b128 v[244:247], %[l1] offset:13312\n\t" "s_waitcnt vmcnt(0)\n\t" "v_smfmac_f32_16x16x64_bf16 v[232:235], v[252:255], a[40:47], %[ix]\n\t" \
    "ds_write_b128 %[l1], v[252:255] offset:3072\n\t" "v_smfmac_f32_16x16x64_bf16 v[236:239], v[252:255], v[168:175], %[ix]\n\t" \
    "ds_read_b128 v[160:163], %[l0] offset:8192\n\t" "ds_read_b128 v[164:167], %[l0] offset:9216\n\t" "s_waitcnt lgkmcnt(9)\n\t" \
    "v_smfmac_f32_16x16x64_bf16 v[232:235], v[16:19], a[48:55], %[ix]\n\t" "ds_read_b128 v[168:171], %[l0] offset:10240\n\t" "ds_read_b128 v[172:175], %[l0] offset:11264\n\t" \
    "v_smfmac_f32_16x16x64_bf16 v[236:239], v[16:19], a[56:63], %[ix]\n\t" "s_waitcnt lgkmcnt(10)\n\t" "v_smfmac_f32_16x16x64_bf16 v[232:235], v[20:23], a[64:71], %[ix]\n\t" \
    "v_smfmac_f32_16x16x64_bf16 v[236:239], v[20:23], v[176:183], %[ix]\n\t" "s_waitcnt lgkmcnt(4)\n\t" "s_barrier\n\t" XT(88, 89) \
    "v_smfmac_f32_16x16x64_bf16 v[232:235], v[24:27], a[72:79], %[ix]\n\t" "ds_read_b128 v[176:179], %[l0] offset:12288\n\t" "ds_read_b128 v[180:183], %[l0] offset:13312\n\t" \
    "v_smfmac_f32_16x16x64_bf16 v[236:239], v[24:27], a[80:87], %[ix]\n\t" "ds_read_b128 v[248:251], %[l1] offset:6144\n\t" "ds_read_b128 v[252:255], %[l1] offset:10240\n\t" \
    "v_smfmac_f32_16x16x64_bf16 v[232:235], v[28:31], a[88:95], %[ix]\n\t" "ds_read_b128 v[16:19], %[l1] offset:14336\n\t" "ds_read_b128 v[20:23], %[l1] offset:7168\n\t" \
    "v_smfmac_f32_16x16x64_bf16 v[236:239], v[28:31], v[184:191], %[ix]\n\t" "ds_read_b128 v[24:27], %[l1] offset:11264\n\t" "ds_read_b128 v[184:187], %[l0] offset:14336\n\t" \
    "v_smfmac_f32_16x16x64_bf16 v[232:235], v[240:243], a[96:103], %[ix]\n\t" "ds_read_b128 v[188:191], %[l0] offset:15360\n\t" \
    "ds_read_b128 v[28:31], %[l1] offset:15360\n\t" "v_smfmac_f32_16x16x64_bf16 v[236:239], v[240:243], a[104:111], %[ix]\n\t" \
    "v_smfmac_f32_16x16x64_bf16 v[232:235], v[244:247], a[112:119], %[ix]\n\t" "s_waitcnt lgkmcnt(12)\n\t" \
    "v_smfmac_f32_16x16x64_bf16 v[236:239], v[244:247], v[160:167], %[ix]\n\t" "ds_read_b128 v[160:163], %[l0] offset:16384\n\t" \
    "ds_read_b128 v[164:167], %[l0] offset:17408\n\t" "s_waitcnt lgkmcnt(9)\n\t" "v_smfmac_f32_16x16x64_bf16 v[232:235], v[248:251], a[120:127], %[ix]\n\t" \
    "v_smfmac_f32_16x16x64_bf16 v[236:239], v[248:251], a[128:135], %[ix]\n\t" "s_waitcnt lgkmcnt(8)\n\t" \
    "v_smfmac_f32_16x16x64_bf16 v[232:235], v[252:255], a[136:143], %[ix]\n\t" "v_smfmac_f32_16x16x64_bf16 v[236:239], v[252:255], v[168:175], %[ix]\n\t" \
    "ds_read_b128 v[168:171], %[l0] offset:18432\n\t" "ds_read_b128 v[172:175], %[l0] offset:19456\n\t" "s_waitcnt lgkmcnt(9)\n\t" \
    "v_smfmac_f32_16x16x64_bf16 v[232:235], v[16:19], a[144:151], %[ix]\n\t" "v_smfmac_f32_16x16x64_bf16 v[236:239], v[16:19], a[152:159], %[ix]\n\t" \
    "s_waitcnt lgkmcnt(8)\n\t" "v_smfmac_f32_16x16x64_bf16 v[232:235], v[20:23], a[160:167], %[ix]\n\t" \
    "v_smfmac_f32_16x16x64_bf16 v[236:239], v[20:23], v[176:183], %[ix]\n\t" "ds_read_b128 v[176:179], %[l0] offset:20480\n\t" \
    "ds_read_b128 v[180:183], %[l0] offset:21504\n\t" "s_waitcnt lgkmcnt(9)\n\t" "v_smfmac_f32_16x16x64_bf16 v[232:235], v[24:27], a[168:175], %[ix]\n\t" \
    "v_smfmac_f32_16x16x64_bf16 v[236:239], v[24:27], a[176:183], %[ix]\n\t" "s_waitcnt lgkmcnt(6)\n\t" \
    "v_smfmac_f32_16x16x64_bf16 v[232:235], v[28:31], a[184:191], %[ix]\n\t" "v_smfmac_f32_16x16x64_bf16 v[236:239], v[28:31], v[184:191], %[ix]\n\t" \
    "ds_read_b128 v[184:187], %[l0] offset:22528\n\t" "ds_read_b128 v[188:191], %[l0] offset:23552\n\t" XT(90, 91) "s_nop 4\n\t" "v_add_f32 v232, v232, v233\n\t" \
    "v_add_f32 v234, v234, v235\n\t" "v_add_f32 v236, v236, v237\n\t" "v_add_f32 v238, v238, v239\n\t" "v_cvt_pk_bf16_f32 v236, v232, v236\n\t" \
    "v_cvt_pk_bf16_f32 v237, v234, v238\n\t" "ds_write2_b32 %[vw], v236, v237 offset1:68\n\t" "v_cmp_u_f32 vcc, v232, v234\n\t" "s_cbranch_vccnz 11f\n\t" "12:\n\t" \
    "s_memtime s[64:65]\n\t" "s_waitcnt lgkmcnt(0)\n\t" "s_barrier\n\t" XT(92, 93) "ds_read_b128 v[240:243], %[vr] offset:0\n\t" \
    "ds_read_b128 v[244:247], %[vr] offset:64\n\t" "ds_read_b128 v[248:251], %[vr] offset:128\n\t" "ds_read_b128 v[252:255], %[vr] offset:192\n\t" \
    "s_sub_u32 s62, s64, %[ca]\n\t" "s_mul_hi_u32 s63, s62, %[k1]\n\t" "s_mul_i32 s62, s62, %[k1]\n\t" "s_lshr_b64 s[62:63], s[62:63], 16\n\t" "s_add_u32 s62, s62, %[ga]\n\t" \
    "s_bitset0_b32 s62, 0\n\t" "v_mov_b32 v226, s62\n\t" "buffer_store_dword v226, %[hq], %[rs], s67 offen sc1\n\t" "s_mov_b32 s61, %[ub]\n\t" "s_waitcnt lgkmcnt(3)\n\t" \
    "v_smfmac_f32_16x16x64_bf16 v[192:195], v[240:243], v[32:39], %[ix]\n\t" "v_smfmac_f32_16x16x64_bf16 v[196:199], v[240:243], v[48:55], %[ix]\n\t" \
    "s_waitcnt lgkmcnt(2)\n\t" "v_smfmac_f32_16x16x64_bf16 v[192:195], v[244:247], v[40:47], %[ix]\n\t" \
    "v_smfmac_f32_16x16x64_bf16 v[196:199], v[244:247], v[56:63], %[ix]\n\t" "s_waitcnt lgkmcnt(1)\n\t" \
    "v_smfmac_f32_16x16x64_bf16 v[192:195], v[248:251], a[192:199], %[ix]\n\t" "v_smfmac_f32_16x16x64_bf16 v[196:199], v[248:251], a[200:207], %[ix]\n\t" \
    "s_waitcnt lgkmcnt(0)\n\t" "v_smfmac_f32_16x16x64_bf16 v[192:195], v[252:255], v[160:167], %[ix]\n\t" "ds_read_b128 v[160:163], %[l0] offset:24576\n\t" \
    "ds_read_b128 v[164:167], %[l0] offset:25600\n\t" "v_smfmac_f32_16x16x64_bf16 v[196:199], v[252:255], v[168:175], %[ix]\n\t" \
    "ds_read_b128 v[168:171], %[l0] offset:26624\n\t" "ds_read_b128 v[172:175], %[l0] offset:27648\n\t" \
    "v_smfmac_f32_16x16x64_bf16 v[200:203], v[240:243], v[64:71], %[ix]\n\t" "s_nop 1\n\t" "v_smfmac_f32_16x16x64_bf16 v[204:207], v[240:243], v[80:87], %[ix]\n\t" \
    "v_cvt_pk_bf16_f32 v224, v192, v194\n\t" "v_cvt_pk_bf16_f32 v225, v193, v195\n\t" "v_smfmac_f32_16x16x64_bf16 v[200:203], v[244:247], v[72:79], %[ix]\n\t" \
    "v_cvt_pk_bf16_f32 v226, v196, v198\n\t" "v_cvt_pk_bf16_f32 v227, v197, v199\n\t" "v_smfmac_f32_16x16x64_bf16 v[204:207], v[244:247], v[88:95], %[ix]\n\t" "s_nop 0\n\t" \
    "v_permlane32_swap_b32 v224, v226\n\t" "v_smfmac_f32_16x16x64_bf16 v[200:203], v[248:251], a[208:215], %[ix]\n\t" "v_permlane32_swap_b32 v225, v227\n\t" "s_nop 0\n\t" \
    "v_smfmac_f32_16x16x64_bf16 v[204:207], v[248:251], a[216:223], %[ix]\n\t" "v_permlane16_swap_b32 v224, v225\n\t" "v_permlane16_swap_b32 v226, v227\n\t" \
    "v_smfmac_f32_16x16x64_bf16 v[200:203], v[252:255], v[176:183], %[ix]\n\t" "ds_read_b128 v[176:179], %[l0] offset:28672\n\t" \
    "buffer_store_dwordx4 v[224:227], %[uo], %[rs], s61 offen sc1\n\t" "s_add_u32 s61, s61, 0x10000\n\t" XT(94, 95) \
    "v_smfmac_f32_16x16x64_bf16 v[204:207], v[252:255], v[184:191], %[ix]\n\t" "ds_read_b128 v[180:183], %[l0] offset:29696\n\t" \
    "ds_read_b128 v[184:187], %[l0] offset:30720\n\t" "v_smfmac_f32_16x16x64_bf16 v[208:211], v[240:243], v[96:103], %[ix]\n\t" \
    "ds_read_b128 v[188:191], %[l0] offset:31744\n\t" "s_nop 0\n\t" "v_smfmac_f32_16x16x64_bf16 v[212:215], v[240:243], v[112:119], %[ix]\n\t" \
    "v_cvt_pk_bf16_f32 v228, v200, v202\n\t" "v_cvt_pk_bf16_f32 v229, v201, v203\n\t" "v_smfmac_f32_16x16x64_bf16 v[208:211], v[244:247], v[104:111], %[ix]\n\t" \
    "v_cvt_pk_bf16_f32 v230, v204, v206\n\t" "v_cvt_pk_bf16_f32 v231, v205, v207\n\t" "v_smfmac_f32_16x16x64_bf16 v[212:215], v[244:247], v[120:127], %[ix]\n\t" "s_nop 0\n\t" \
    "v_permlane32_swap_b32 v228, v230\n\t" "v_smfmac_f32_16x16x64_bf16 v[208:211], v[248:251], a[224:231], %[ix]\n\t" "v_permlane32_swap_b32 v229, v231\n\t" "s_nop 0\n\t" \
    "v_smfmac_f32_16x16x64_bf16 v[212:215], v[248:251], a[232:239], %[ix]\n\t" "v_permlane16_swap_b32 v228, v229\n\t" "v_permlane16_swap_b32 v230, v231\n\t" \
    "s_waitcnt lgkmcnt(6)\n\t" "v_smfmac_f32_16x16x64_bf16 v[208:211], v[252:255], v[160:167], %[ix]\n\t" "buffer_store_dwordx4 v[228:231], %[uo], %[rs], s61 offen sc1\n\t" \
    "s_add_u32 s61, s61, 0x10000\n\t" "s_waitcnt lgkmcnt(4)\n\t" "v_smfmac_f32_16x16x64_bf16 v[212:215], v[252:255], v[168:175], %[ix]\n\t" "s_nop 1\n\t" \
    "v_smfmac_f32_16x16x64_bf16 v[216:219], v[240:243], v[128:135], %[ix]\n\t" "s_nop 1\n\t" "v_smfmac_f32_16x16x64_bf16 v[220:223], v[240:243], v[144:151], %[ix]\n\t" \
    "v_cvt_pk_bf16_f32 v224, v208, v210\n\t" "v_cvt_pk_bf16_f32 v225, v209, v211\n\t" "v_smfmac_f32_16x16x64_bf16 v[216:219], v[244:247], v[136:143], %[ix]\n\t" \
    "v_cvt_pk_bf16_f32 v226, v212, v214\n\t" "v_cvt_pk_bf16_f32 v227, v213, v215\n\t" "v_smfmac_f32_16x16x64_bf16 v[220:223], v[244:247], v[152:159], %[ix]\n\t" "s_nop 0\n\t" \
    "v_permlane32_swap_b32 v224, v226\n\t" "v_smfmac_f32_16x16x64_bf16 v[216:219], v[248:251], a[240:247], %[ix]\n\t" "v_permlane32_swap_b32 v225, v227\n\t" "s_nop 0\n\t" \
    "v_smfmac_f32_16x16x64_bf16 v[220:223], v[248:251], a[248:255], %[ix]\n\t" "v_permlane16_swap_b32 v224, v225\n\t" "v_permlane16_swap_b32 v226, v227\n\t" \
    "s_waitcnt lgkmcnt(2)\n\t" "v_smfmac_f32_16x16x64_bf16 v[216:219], v[252:255], v[176:183], %[ix]\n\t" "buffer_store_dwordx4 v[224:227], %[uo], %[rs], s61 offen sc1\n\t" \
    "s_add_u32 s61, s61, 0x10000\n\t" "s_waitcnt lgkmcnt(0)\n\t" "v_smfmac_f32_16x16x64_bf16 v[220:223], v[252:255], v[184:191], %[ix]\n\t" "s_nop 5\n\t" \
    "v_cvt_pk_bf16_f32 v228, v216, v218\n\t" "v_cvt_pk_bf16_f32 v229, v217, v219\n\t" "v_cvt_pk_bf16_f32 v230, v220, v222\n\t" "v_cvt_pk_bf16_f32 v231, v221, v223\n\t" \
    "s_nop 0\n\t" "v_permlane32_swap_b32 v228, v230\n\t" "v_permlane32_swap_b32 v229, v231\n\t" "s_nop 1\n\t" "v_permlane16_swap_b32 v228, v229\n\t" \
    "v_permlane16_swap_b32 v230, v231\n\t" "buffer_store_dwordx4 v[228:231], %[uo], %[rs], s61 offen sc1\n\t" "s_nop 1\n\t" XT(96, 97) "s_branch 99f\n\t" "11:\n\t" \
    "s_mov_b64 s[62:63], 0\n\t" "ds_read_b128 v[16:19], %[l1] offset:0\n\t" "ds_read_b128 v[20:23], %[l1] offset:1024\n\t" "ds_read_b128 v[24:27], %[l1] offset:2048\n\t" \
    "ds_read_b128 v[28:31], %[l1] offset:3072\n\t" "s_waitcnt lgkmcnt(3)\n\t" "v_max3_u32 v16, v16, v17, v18\n\t" "v_max_u32 v16, v16, v19\n\t" \
    "v_cmp_eq_u32 vcc, -1, v16\n\t" "ds_read_b128 v[16:19], %[l1] offset:4096\n\t" "s_or_b64 s[62:63], s[62:63], vcc\n\t" "s_waitcnt lgkmcnt(3)\n\t" \
    "v_max3_u32 v20, v20, v21, v22\n\t" "v_max_u32 v20, v20, v23\n\t" "v_cmp_eq_u32 vcc, -1, v20\n\t" "ds_read_b128 v[20:23], %[l1] offset:5120\n\t" \
    "s_or_b64 s[62:63], s[62:63], vcc\n\t" "s_waitcnt lgkmcnt(3)\n\t" "v_max3_u32 v24, v24, v25, v26\n\t" "v_max_u32 v24, v24, v27\n\t" "v_cmp_eq_u32 vcc, -1, v24\n\t" \
    "ds_read_b128 v[24:27], %[l1] offset:6144\n\t" "s_or_b64 s[62:63], s[62:63], vcc\n\t" "s_waitcnt lgkmcnt(3)\n\t" "v_max3_u32 v28, v28, v29, v30\n\t" \
    "v_max_u32 v28, v28, v31\n\t" "v_cmp_eq_u32 vcc, -1, v28\n\t" "ds_read_b128 v[28:31], %[l1] offset:7168\n\t" "s_or_b64 s[62:63], s[62:63], vcc\n\t" \
    "s_waitcnt lgkmcnt(3)\n\t" "v_max3_u32 v16, v16, v17, v18\n\t" "v_max_u32 v16, v16, v19\n\t" "v_cmp_eq_u32 vcc, -1, v16\n\t" \
    "ds_read_b128 v[16:19], %[l1] offset:8192\n\t" "s_or_b64 s[62:63], s[62:63], vcc\n\t" "s_waitcnt lgkmcnt(3)\n\t" "v_max3_u32 v20, v20, v21, v22\n\t" \
    "v_max_u32 v20, v20, v23\n\t" "v_cmp_eq_u32 vcc, -1, v20\n\t" "ds_read_b128 v[20:23], %[l1] offset:9216\n\t" "s_or_b64 s[62:63], s[62:63], vcc\n\t" \
    "s_waitcnt lgkmcnt(3)\n\t" "v_max3_u32 v24, v24, v25, v26\n\t" "v_max_u32 v24, v24, v27\n\t" "v_cmp_eq_u32 vcc, -1, v24\n\t" \
    "ds_read_b128 v[24:27], %[l1] offset:10240\n\t" "s_or_b64 s[62:63], s[62:63], vcc\n\t" "s_waitcnt lgkmcnt(3)\n\t" "v_max3_u32 v28, v28, v29, v30\n\t" \
    "v_max_u32 v28, v28, v31\n\t" "v_cmp_eq_u32 vcc, -1, v28\n\t" "ds_read_b128 v[28:31], %[l1] offset:11264\n\t" "s_or_b64 s[62:63], s[62:63], vcc\n\t" \
    "s_waitcnt lgkmcnt(3)\n\t" "v_max3_u32 v16, v16, v17, v18\n\t" "v_max_u32 v16, v16, v19\n\t" "v_cmp_eq_u32 vcc, -1, v16\n\t" \
    "ds_read_b128 v[16:19], %[l1] offset:12288\n\t" "s_or_b64 s[62:63], s[62:63], vcc\n\t" "s_waitcnt lgkmcnt(3)\n\t" "v_max3_u32 v20, v20, v21, v22\n\t" \
    "v_max_u32 v20, v20, v23\n\t" "v_cmp_eq_u32 vcc, -1, v20\n\t" "ds_read_b128 v[20:23], %[l1] offset:13312\n\t" "s_or_b64 s[62:63], s[62:63], vcc\n\t" \
    "s_waitcnt lgkmcnt(3)\n\t" "v_max3_u32 v24, v24, v25, v26\n\t" "v_max_u32 v24, v24, v27\n\t" "v_cmp_eq_u32 vcc, -1, v24\n\t" \
    "ds_read_b128 v[24:27], %[l1] offset:14336\n\t" "s_or_b64 s[62:63], s[62:63], vcc\n\t" "s_waitcnt lgkmcnt(3)\n\t" "v_max3_u32 v28, v28, v29, v30\n\t" \
    "v_max_u32 v28, v28, v31\n\t" "v_cmp_eq_u32 vcc, -1, v28\n\t" "ds_read_b128 v[28:31], %[l1] offset:15360\n\t" "s_or_b64 s[62:63], s[62:63], vcc\n\t" \
    "s_waitcnt lgkmcnt(3)\n\t" "v_max3_u32 v16, v16, v17, v18\n\t" "v_max_u32 v16, v16, v19\n\t" "v_cmp_eq_u32 vcc, -1, v16\n\t" "s_or_b64 s[62:63], s[62:63], vcc\n\t" \
    "s_waitcnt lgkmcnt(2)\n\t" "v_max3_u32 v20, v20, v21, v22\n\t" "v_max_u32 v20, v20, v23\n\t" "v_cmp_eq_u32 vcc, -1, v20\n\t" "s_or_b64 s[62:63], s[62:63], vcc\n\t" \
    "s_waitcnt lgkmcnt(1)\n\t" "v_max3_u32 v24, v24, v25, v26\n\t" "v_max_u32 v24, v24, v27\n\t" "v_cmp_eq_u32 vcc, -1, v24\n\t" "s_or_b64 s[62:63], s[62:63], vcc\n\t" \
    "s_waitcnt lgkmcnt(0)\n\t" "v_max3_u32 v28, v28, v29, v30\n\t" "v_max_u32 v28, v28, v31\n\t" "v_cmp_eq_u32 vcc, -1, v28\n\t" "s_or_b64 s[62:63], s[62:63], vcc\n\t" \
    "s_cmp_eq_u64 s[62:63], 0\n\t" "s_cbranch_scc1 12b\n\t" "s_cmp_eq_u32 %[rt], 0\n\t" "s_cbranch_scc1 12b\n\t" "s_sub_u32 %[rt], %[rt], 1\n\t" XCNT(100) "14:\n\t" \
    "buffer_load_dwordx4 v[240:243], %[mo], %[rs], %[sb] offen sc1\n\t" "buffer_load_dwordx4 v[244:247], v224, %[rs], %[sb] offen sc1\n\t" \
    "buffer_load_dwordx4 v[248:251], %[mo], %[rs], s60 offen sc1\n\t" "buffer_load_dwordx4 v[252:255], v224, %[rs], s60 offen sc1\n\t" "s_waitcnt vmcnt(0)\n\t" \
    "v_max_u32 v28, v240, v241\n\t" "v_max3_u32 v28, v28, v242, v243\n\t" "v_max3_u32 v28, v28, v244, v245\n\t" "v_max3_u32 v28, v28, v246, v247\n\t" \
    "v_max3_u32 v28, v28, v248, v249\n\t" "v_max3_u32 v28, v28, v250, v251\n\t" "v_max3_u32 v28, v28, v252, v253\n\t" "v_max3_u32 v28, v28, v254, v255\n\t" \
    "v_cmp_eq_u32 vcc, -1, v28\n\t" "s_cbranch_vccz 15f\n\t" "s_cmp_eq_u32 %[bd], 0\n\t" "s_cbranch_scc1 15f\n\t" "s_sub_u32 %[bd], %[bd], 1\n\t" XCNT(101) "s_sleep 1\n\t" \
    "s_branch 14b\n\t" "15:\n\t" "s_barrier\n\t" "s_memtime s[64:65]\n\t" "s_waitcnt lgkmcnt(0)\n\t" "s_add_u32 s64, s64, %[dr]\n\t" "s_sub_u32 s62, s64, %[ca]\n\t" \
    "s_mul_hi_u32 s63, s62, %[k1]\n\t" "s_mul_i32 s62, s62, %[k1]\n\t" "s_lshr_b64 s[62:63], s[62:63], 16\n\t" "s_add_u32 s62, s62, %[ga]\n\t" "s_bitset0_b32 s62, 0\n\t" \
    "v_mov_b32 v28, s62\n\t" "buffer_store_dword v28, %[hq], %[rs], s67 offen sc1\n\t" "s_mov_b32 s67, 0x7f000000\n\t" "s_branch 10b\n\t" "91:\n\t" XT(80, 81) "s_barrier\n\t" \
    XT(82, 83) "20:\n\t" "buffer_load_dwordx4 v[240:243], %[mo], %[rs], %[sb] offen sc1\n\t" "v_mov_b64 v[232:233], 0\n\t" "v_mov_b64 v[234:235], 0\n\t" \
    "v_mov_b64 v[236:237], 0\n\t" "v_mov_b64 v[238:239], 0\n\t" "v_mov_b64 v[192:193], 0\n\t" "v_mov_b64 v[194:195], 0\n\t" "v_mov_b64 v[196:197], 0\n\t" \
    "v_mov_b64 v[198:199], 0\n\t" "buffer_load_dwordx4 v[244:247], v224, %[rs], %[sb] offen sc1\n\t" "ds_read_b128 v[160:163], %[l0] offset:0\n\t" \
    "ds_read_b128 v[164:167], %[l0] offset:1024\n\t" "ds_read_b128 v[168:171], %[l0] offset:2048\n\t" "ds_read_b128 v[172:175], %[l0] offset:3072\n\t" "s_nop 3\n\t" \
    "buffer_load_dwordx4 v[248:251], %[mo], %[rs], s60 offen sc1\n\t" "ds_read_b128 v[176:179], %[l0] offset:4096\n\t" "ds_read_b128 v[180:183], %[l0] offset:5120\n\t" \
    "ds_read_b128 v[184:187], %[l0] offset:6144\n\t" "ds_read_b128 v[188:191], %[l0] offset:7168\n\t" "s_nop 3\n\t" \
    "buffer_load_dwordx4 v[252:255], v224, %[rs], s60 offen sc1\n\t" "v_mov_b64 v[200:201], 0\n\t" "v_mov_b64 v[202:203], 0\n\t" "v_mov_b64 v[204:205], 0\n\t" \
    "v_mov_b64 v[206:207], 0\n\t" "v_mov_b64 v[208:209], 0\n\t" "v_mov_b64 v[210:211], 0\n\t" "v_mov_b64 v[212:213], 0\n\t" "v_mov_b64 v[214:215], 0\n\t" \
    "v_mov_b64 v[216:217], 0\n\t" "v_mov_b64 v[218:219], 0\n\t" "v_mov_b64 v[220:221], 0\n\t" "v_mov_b64 v[222:223], 0\n\t" "s_waitcnt vmcnt(3)\n\t" XT(84, 85) \
    "v_smfmac_f32_16x16x64_bf16 v[232:235], v[240:243], a[0:7], %[ix]\n\t" "ds_write_b128 %[l1], v[240:243] offset:4096\n\t" \
    "v_smfmac_f32_16x16x64_bf16 v[236:239], v[240:243], a[8:15], %[ix]\n\t" "s_waitcnt vmcnt(2)\n\t" "v_smfmac_f32_16x16x64_bf16 v[232:235], v[244:247], a[16:23], %[ix]\n\t" \
    "ds_write_b128 %[l1], v[244:247] offset:5120\n\t" "s_waitcnt lgkmcnt(8)\n\t" "v_smfmac_f32_16x16x64_bf16 v[236:239], v[244:247], v[160:167], %[ix]\n\t" \
    "s_waitcnt lgkmcnt(0)\n\t" "s_barrier\n\t" XT(86, 87) "ds_read_b128 v[16:19], %[l1] offset:8192\n\t" "ds_read_b128 v[20:23], %[l1] offset:12288\n\t" \
    "ds_read_b128 v[24:27], %[l1] offset:0\n\t" "s_waitcnt vmcnt(1)\n\t" "v_smfmac_f32_16x16x64_bf16 v[232:235], v[248:251], a[24:31], %[ix]\n\t" \
    "ds_write_b128 %[l1], v[248:251] offset:6144\n\t" "v_smfmac_f32_16x16x64_bf16 v[236:239], v[248:251], a[32:39], %[ix]\n\t" "ds_read_b128 v[28:31], %[l1] offset:9216\n\t" \
    "ds_read_b128 v[240:243], %[l1] offset:13312\n\t" "ds_read_b128 v[244:247], %[l1] offset:1024\n\t" "s_waitcnt vmcnt(0)\n\t" \
    "v_smfmac_f32_16x16x64_bf16 v[232:235], v[252:255], a[40:47], %[ix]\n\t" "ds_write_b128 %[l1], v[252:255] offset:7168\n\t" \
    "v_smfmac_f32_16x16x64_bf16 v[236:239], v[252:255], v[168:175], %[ix]\n\t" "ds_read_b128 v[160:163], %[l0] offset:8192\n\t" \
    "ds_read_b128 v[164:167], %[l0] offset:9216\n\t" "s_waitcnt lgkmcnt(9)\n\t" "v_smfmac_f32_16x16x64_bf16 v[232:235], v[16:19], a[48:55], %[ix]\n\t" \
    "ds_read_b128 v[168:171], %[l0] offset:10240\n\t" "ds_read_b128 v[172:175], %[l0] offset:11264\n\t" "v_smfmac_f32_16x16x64_bf16 v[236:239], v[16:19], a[56:63], %[ix]\n\t" \
    "s_waitcnt lgkmcnt(10)\n\t" "v_smfmac_f32_16x16x64_bf16 v[232:235], v[20:23], a[64:71], %[ix]\n\t" \
    "v_smfmac_f32_16x16x64_bf16 v[236:239], v[20:23], v[176:183], %[ix]\n\t" "s_waitcnt lgkmcnt(4)\n\t" "s_barrier\n\t" XT(88, 89) \
    "v_smfmac_f32_16x16x64_bf16 v[232:235], v[24:27], a[72:79], %[ix]\n\t" "ds_read_b128 v[176:179], %[l0] offset:12288\n\t" "ds_read_b128 v[180:183], %[l0] offset:13312\n\t" \
    "v_smfmac_f32_16x16x64_bf16 v[236:239], v[24:27], a[80:87], %[ix]\n\t" "ds_read_b128 v[248:251], %[l1] offset:10240\n\t" "ds_read_b128 v[252:255], %[l1] offset:14336\n\t" \
    "v_smfmac_f32_16x16x64_bf16 v[232:235], v[28:31], a[88:95], %[ix]\n\t" "ds_read_b128 v[16:19], %[l1] offset:2048\n\t" "ds_read_b128 v[20:23], %[l1] offset:11264\n\t" \
    "v_smfmac_f32_16x16x64_bf16 v[236:239], v[28:31], v[184:191], %[ix]\n\t" "ds_read_b128 v[24:27], %[l1] offset:15360\n\t" "ds_read_b128 v[184:187], %[l0] offset:14336\n\t" \
    "v_smfmac_f32_16x16x64_bf16 v[232:235], v[240:243], a[96:103], %[ix]\n\t" "ds_read_b128 v[188:191], %[l0] offset:15360\n\t" "ds_read_b128 v[28:31], %[l1] offset:3072\n\t" \
    "v_smfmac_f32_16x16x64_bf16 v[236:239], v[240:243], a[104:111], %[ix]\n\t" "v_smfmac_f32_16x16x64_bf16 v[232:235], v[244:247], a[112:119], %[ix]\n\t" \
    "s_waitcnt lgkmcnt(12)\n\t" "v_smfmac_f32_16x16x64_bf16 v[236:239], v[244:247], v[160:167], %[ix]\n\t" "ds_read_b128 v[160:163], %[l0] offset:16384\n\t" \
    "ds_read_b128 v[164:167], %[l0] offset:17408\n\t" "s_waitcnt lgkmcnt(9)\n\t" "v_smfmac_f32_16x16x64_bf16 v[232:235], v[248:251], a[120:127], %[ix]\n\t" \
    "v_smfmac_f32_16x16x64_bf16 v[236:239], v[248:251], a[128:135], %[ix]\n\t" "s_waitcnt lgkmcnt(8)\n\t" \
    "v_smfmac_f32_16x16x64_bf16 v[232:235], v[252:255], a[136:143], %[ix]\n\t" "v_smfmac_f32_16x16x64_bf16 v[236:239], v[252:255], v[168:175], %[ix]\n\t" \
    "ds_read_b128 v[168:171], %[l0] offset:18432\n\t" "ds_read_b128 v[172:175], %[l0] offset:19456\n\t" "s_waitcnt lgkmcnt(9)\n\t" \
    "v_smfmac_f32_16x16x64_bf16 v[232:235], v[16:19], a[144:151], %[ix]\n\t" "v_smfmac_f32_16x16x64_bf16 v[236:239], v[16:19], a[152:159], %[ix]\n\t" \
    "s_waitcnt lgkmcnt(8)\n\t" "v_smfmac_f32_16x16x64_bf16 v[232:235], v[20:23], a[160:167], %[ix]\n\t" \
    "v_smfmac_f32_16x16x64_bf16 v[236:239], v[20:23], v[176:183], %[ix]\n\t" "ds_read_b128 v[176:179], %[l0] offset:20480\n\t" \
    "ds_read_b128 v[180:183], %[l0] offset:21504\n\t" "s_waitcnt lgkmcnt(9)\n\t" "v_smfmac_f32_16x16x64_bf16 v[232:235], v[24:27], a[168:175], %[ix]\n\t" \
    "v_smfmac_f32_16x16x64_bf16 v[236:239], v[24:27], a[176:183], %[ix]\n\t" "s_waitcnt lgkmcnt(6)\n\t" \
    "v_smfmac_f32_16x16x64_bf16 v[232:235], v[28:31], a[184:191], %[ix]\n\t" "v_smfmac_f32_16x16x64_bf16 v[236:239], v[28:31], v[184:191], %[ix]\n\t" \
    "ds_read_b128 v[184:187], %[l0] offset:22528\n\t" "ds_read_b128 v[188:191], %[l0] offset:23552\n\t" XT(90, 91) "s_nop 4\n\t" "v_add_f32 v232, v232, v233\n\t" \
    "v_add_f32 v234, v234, v235\n\t" "v_add_f32 v236, v236, v237\n\t" "v_add_f32 v238, v238, v239\n\t" "v_cvt_pk_bf16_f32 v236, v232, v236\n\t" \
    "v_cvt_pk_bf16_f32 v237, v234, v238\n\t" "ds_write2_b32 %[vw], v236, v237 offset1:68\n\t" "v_cmp_u_f32 vcc, v232, v234\n\t" "s_cbranch_vccnz 21f\n\t" "22:\n\t" \
    "s_waitcnt lgkmcnt(0)\n\t" "s_barrier\n\t" XT(92, 93) "ds_read_b128 v[240:243], %[vr] offset:0\n\t" "ds_read_b128 v[244:247], %[vr] offset:64\n\t" \
    "ds_read_b128 v[248:251], %[vr] offset:128\n\t" "ds_read_b128 v[252:255], %[vr] offset:192\n\t" "s_mov_b32 s61, %[ub]\n\t" "s_waitcnt lgkmcnt(3)\n\t" \
    "v_smfmac_f32_16x16x64_bf16 v[192:195], v[240:243], v[32:39], %[ix]\n\t" "v_smfmac_f32_16x16x64_bf16 v[196:199], v[240:243], v[48:55], %[ix]\n\t" \
    "s_waitcnt lgkmcnt(2)\n\t" "v_smfmac_f32_16x16x64_bf16 v[192:195], v[244:247], v[40:47], %[ix]\n\t" \
    "v_smfmac_f32_16x16x64_bf16 v[196:199], v[244:247], v[56:63], %[ix]\n\t" "s_waitcnt lgkmcnt(1)\n\t" \
    "v_smfmac_f32_16x16x64_bf16 v[192:195], v[248:251], a[192:199], %[ix]\n\t" "v_smfmac_f32_16x16x64_bf16 v[196:199], v[248:251], a[200:207], %[ix]\n\t" \
    "s_waitcnt lgkmcnt(0)\n\t" "v_smfmac_f32_16x16x64_bf16 v[192:195], v[252:255], v[160:167], %[ix]\n\t" "ds_read_b128 v[160:163], %[l0] offset:24576\n\t" \
    "ds_read_b128 v[164:167], %[l0] offset:25600\n\t" "v_smfmac_f32_16x16x64_bf16 v[196:199], v[252:255], v[168:175], %[ix]\n\t" \
    "ds_read_b128 v[168:171], %[l0] offset:26624\n\t" "ds_read_b128 v[172:175], %[l0] offset:27648\n\t" \
    "v_smfmac_f32_16x16x64_bf16 v[200:203], v[240:243], v[64:71], %[ix]\n\t" "s_nop 1\n\t" "v_smfmac_f32_16x16x64_bf16 v[204:207], v[240:243], v[80:87], %[ix]\n\t" \
    "v_cvt_pk_bf16_f32 v224, v192, v194\n\t" "v_cvt_pk_bf16_f32 v225, v193, v195\n\t" "v_smfmac_f32_16x16x64_bf16 v[200:203], v[244:247], v[72:79], %[ix]\n\t" \
    "v_cvt_pk_bf16_f32 v226, v196, v198\n\t" "v_cvt_pk_bf16_f32 v227, v197, v199\n\t" "v_smfmac_f32_16x16x64_bf16 v[204:207], v[244:247], v[88:95], %[ix]\n\t" "s_nop 0\n\t" \
    "v_permlane32_swap_b32 v224, v226\n\t" "v_smfmac_f32_16x16x64_bf16 v[200:203], v[248:251], a[208:215], %[ix]\n\t" "v_permlane32_swap_b32 v225, v227\n\t" "s_nop 0\n\t" \
    "v_smfmac_f32_16x16x64_bf16 v[204:207], v[248:251], a[216:223], %[ix]\n\t" "v_permlane16_swap_b32 v224, v225\n\t" "v_permlane16_swap_b32 v226, v227\n\t" \
    "v_smfmac_f32_16x16x64_bf16 v[200:203], v[252:255], v[176:183], %[ix]\n\t" "ds_read_b128 v[176:179], %[l0] offset:28672\n\t" \
    "buffer_store_dwordx4 v[224:227], %[uo], %[rs], s61 offen sc1\n\t" "s_add_u32 s61, s61, 0x10000\n\t" XT(94, 95) \
    "v_smfmac_f32_16x16x64_bf16 v[204:207], v[252:255], v[184:191], %[ix]\n\t" "ds_read_b128 v[180:183], %[l0] offset:29696\n\t" \
    "ds_read_b128 v[184:187], %[l0] offset:30720\n\t" "v_smfmac_f32_16x16x64_bf16 v[208:211], v[240:243], v[96:103], %[ix]\n\t" \
    "ds_read_b128 v[188:191], %[l0] offset:31744\n\t" "s_nop 0\n\t" "v_smfmac_f32_16x16x64_bf16 v[212:215], v[240:243], v[112:119], %[ix]\n\t" \
    "v_cvt_pk_bf16_f32 v228, v200, v202\n\t" "v_cvt_pk_bf16_f32 v229, v201, v203\n\t" "v_smfmac_f32_16x16x64_bf16 v[208:211], v[244:247], v[104:111], %[ix]\n\t" \
    "v_cvt_pk_bf16_f32 v230, v204, v206\n\t" "v_cvt_pk_bf16_f32 v231, v205, v207\n\t" "v_smfmac_f32_16x16x64_bf16 v[212:215], v[244:247], v[120:127], %[ix]\n\t" "s_nop 0\n\t" \
    "v_permlane32_swap_b32 v228, v230\n\t" "v_smfmac_f32_16x16x64_bf16 v[208:211], v[248:251], a[224:231], %[ix]\n\t" "v_permlane32_swap_b32 v229, v231\n\t" "s_nop 0\n\t" \
    "v_smfmac_f32_16x16x64_bf16 v[212:215], v[248:251], a[232:239], %[ix]\n\t" "v_permlane16_swap_b32 v228, v229\n\t" "v_permlane16_swap_b32 v230, v231\n\t" \
    "s_waitcnt lgkmcnt(6)\n\t" "v_smfmac_f32_16x16x64_bf16 v[208:211], v[252:255], v[160:167], %[ix]\n\t" "buffer_store_dwordx4 v[228:231], %[uo], %[rs], s61 offen sc1\n\t" \
    "s_add_u32 s61, s61, 0x10000\n\t" "s_waitcnt lgkmcnt(4)\n\t" "v_smfmac_f32_16x16x64_bf16 v[212:215], v[252:255], v[168:175], %[ix]\n\t" "s_nop 1\n\t" \
    "v_smfmac_f32_16x16x64_bf16 v[216:219], v[240:243], v[128:135], %[ix]\n\t" "s_nop 1\n\t" "v_smfmac_f32_16x16x64_bf16 v[220:223], v[240:243], v[144:151], %[ix]\n\t" \
    "v_cvt_pk_bf16_f32 v224, v208, v210\n\t" "v_cvt_pk_bf16_f32 v225, v209, v211\n\t" "v_smfmac_f32_16x16x64_bf16 v[216:219], v[244:247], v[136:143], %[ix]\n\t" \
    "v_cvt_pk_bf16_f32 v226, v212, v214\n\t" "v_cvt_pk_bf16_f32 v227, v213, v215\n\t" "v_smfmac_f32_16x16x64_bf16 v[220:223], v[244:247], v[152:159], %[ix]\n\t" "s_nop 0\n\t" \
    "v_permlane32_swap_b32 v224, v226\n\t" "v_smfmac_f32_16x16x64_bf16 v[216:219], v[248:251], a[240:247], %[ix]\n\t" "v_permlane32_swap_b32 v225, v227\n\t" "s_nop 0\n\t" \
    "v_smfmac_f32_16x16x64_bf16 v[220:223], v[248:251], a[248:255], %[ix]\n\t" "v_permlane16_swap_b32 v224, v225\n\t" "v_permlane16_swap_b32 v226, v227\n\t" \
    "s_waitcnt lgkmcnt(2)\n\t" "v_smfmac_f32_16x16x64_bf16 v[216:219], v[252:255], v[176:183], %[ix]\n\t" "buffer_store_dwordx4 v[224:227], %[uo], %[rs], s61 offen sc1\n\t" \
    "s_add_u32 s61, s61, 0x10000\n\t" "s_waitcnt lgkmcnt(0)\n\t" "v_smfmac_f32_16x16x64_bf16 v[220:223], v[252:255], v[184:191], %[ix]\n\t" "s_nop 5\n\t" \
    "v_cvt_pk_bf16_f32 v228, v216, v218\n\t" "v_cvt_pk_bf16_f32 v229, v217, v219\n\t" "v_cvt_pk_bf16_f32 v230, v220, v222\n\t" "v_cvt_pk_bf16_f32 v231, v221, v223\n\t" \
    "s_nop 0\n\t" "v_permlane32_swap_b32 v228, v230\n\t" "v_permlane32_swap_b32 v229, v231\n\t" "s_nop 1\n\t" "v_permlane16_swap_b32 v228, v229\n\t" \
    "v_permlane16_swap_b32 v230, v231\n\t" "buffer_store_dwordx4 v[228:231], %[uo], %[rs], s61 offen sc1\n\t" "s_nop 1\n\t" XT(96, 97) "s_branch 99f\n\t" "21:\n\t" \
    "s_mov_b64 s[62:63], 0\n\t" "ds_read_b128 v[16:19], %[l1] offset:0\n\t" "ds_read_b128 v[20:23], %[l1] offset:1024\n\t" "ds_read_b128 v[24:27], %[l1] offset:2048\n\t" \
    "ds_read_b128 v[28:31], %[l1] offset:3072\n\t" "s_waitcnt lgkmcnt(3)\n\t" "v_max3_u32 v16, v16, v17, v18\n\t" "v_max_u32 v16, v16, v19\n\t" \
    "v_cmp_eq_u32 vcc, -1, v16\n\t" "ds_read_b128 v[16:19], %[l1] offset:4096\n\t" "s_or_b64 s[62:63], s[62:63], vcc\n\t" "s_waitcnt lgkmcnt(3)\n\t" \
    "v_max3_u32 v20, v20, v21, v22\n\t" "v_max_u32 v20, v20, v23\n\t" "v_cmp_eq_u32 vcc, -1, v20\n\t" "ds_read_b128 v[20:23], %[l1] offset:5120\n\t" \
    "s_or_b64 s[62:63], s[62:63], vcc\n\t" "s_waitcnt lgkmcnt(3)\n\t" "v_max3_u32 v24, v24, v25, v26\n\t" "v_max_u32 v24, v24, v27\n\t" "v_cmp_eq_u32 vcc, -1, v24\n\t" \
    "ds_read_b128 v[24:27], %[l1] offset:6144\n\t" "s_or_b64 s[62:63], s[62:63], vcc\n\t" "s_waitcnt lgkmcnt(3)\n\t" "v_max3_u32 v28, v28, v29, v30\n\t" \
    "v_max_u32 v28, v28, v31\n\t" "v_cmp_eq_u32 vcc, -1, v28\n\t" "ds_read_b128 v[28:31], %[l1] offset:7168\n\t" "s_or_b64 s[62:63], s[62:63], vcc\n\t" \
    "s_waitcnt lgkmcnt(3)\n\t" "v_max3_u32 v16, v16, v17, v18\n\t" "v_max_u32 v16, v16, v19\n\t" "v_cmp_eq_u32 vcc, -1, v16\n\t" \
    "ds_read_b128 v[16:19], %[l1] offset:8192\n\t" "s_or_b64 s[62:63], s[62:63], vcc\n\t" "s_waitcnt lgkmcnt(3)\n\t" "v_max3_u32 v20, v20, v21, v22\n\t" \
    "v_max_u32 v20, v20, v23\n\t" "v_cmp_eq_u32 vcc, -1, v20\n\t" "ds_read_b128 v[20:23], %[l1] offset:9216\n\t" "s_or_b64 s[62:63], s[62:63], vcc\n\t" \
    "s_waitcnt lgkmcnt(3)\n\t" "v_max3_u32 v24, v24, v25, v26\n\t" "v_max_u32 v24, v24, v27\n\t" "v_cmp_eq_u32 vcc, -1, v24\n\t" \
    "ds_read_b128 v[24:27], %[l1] offset:10240\n\t" "s_or_b64 s[62:63], s[62:63], vcc\n\t" "s_waitcnt lgkmcnt(3)\n\t" "v_max3_u32 v28, v28, v29, v30\n\t" \
    "v_max_u32 v28, v28, v31\n\t" "v_cmp_eq_u32 vcc, -1, v28\n\t" "ds_read_b128 v[28:31], %[l1] offset:11264\n\t" "s_or_b64 s[62:63], s[62:63], vcc\n\t" \
    "s_waitcnt lgkmcnt(3)\n\t" "v_max3_u32 v16, v16, v17, v18\n\t" "v_max_u32 v16, v16, v19\n\t" "v_cmp_eq_u32 vcc, -1, v16\n\t" \
    "ds_read_b128 v[16:19], %[l1] offset:12288\n\t" "s_or_b64 s[62:63], s[62:63], vcc\n\t" "s_waitcnt lgkmcnt(3)\n\t" "v_max3_u32 v20, v20, v21, v22\n\t" \
    "v_max_u32 v20, v20, v23\n\t" "v_cmp_eq_u32 vcc, -1, v20\n\t" "ds_read_b128 v[20:23], %[l1] offset:13312\n\t" "s_or_b64 s[62:63], s[62:63], vcc\n\t" \
    "s_waitcnt lgkmcnt(3)\n\t" "v_max3_u32 v24, v24, v25, v26\n\t" "v_max_u32 v24, v24, v27\n\t" "v_cmp_eq_u32 vcc, -1, v24\n\t" \
    "ds_read_b128 v[24:27], %[l1] offset:14336\n\t" "s_or_b64 s[62:63], s[62:63], vcc\n\t" "s_waitcnt lgkmcnt(3)\n\t" "v_max3_u32 v28, v28, v29, v30\n\t" \
    "v_max_u32 v28, v28, v31\n\t" "v_cmp_eq_u32 vcc, -1, v28\n\t" "ds_read_b128 v[28:31], %[l1] offset:15360\n\t" "s_or_b64 s[62:63], s[62:63], vcc\n\t" \
    "s_waitcnt lgkmcnt(3)\n\t" "v_max3_u32 v16, v16, v17, v18\n\t" "v_max_u32 v16, v16, v19\n\t" "v_cmp_eq_u32 vcc, -1, v16\n\t" "s_or_b64 s[62:63], s[62:63], vcc\n\t" \
    "s_waitcnt lgkmcnt(2)\n\t" "v_max3_u32 v20, v20, v21, v22\n\t" "v_max_u32 v20, v20, v23\n\t" "v_cmp_eq_u32 vcc, -1, v20\n\t" "s_or_b64 s[62:63], s[62:63], vcc\n\t" \
    "s_waitcnt lgkmcnt(1)\n\t" "v_max3_u32 v24, v24, v25, v26\n\t" "v_max_u32 v24, v24, v27\n\t" "v_cmp_eq_u32 vcc, -1, v24\n\t" "s_or_b64 s[62:63], s[62:63], vcc\n\t" \
    "s_waitcnt lgkmcnt(0)\n\t" "v_max3_u32 v28, v28, v29, v30\n\t" "v_max_u32 v28, v28, v31\n\t" "v_cmp_eq_u32 vcc, -1, v28\n\t" "s_or_b64 s[62:63], s[62:63], vcc\n\t" \
    "s_cmp_eq_u64 s[62:63], 0\n\t" "s_cbranch_scc1 22b\n\t" "s_cmp_eq_u32 %[rt], 0\n\t" "s_cbranch_scc1 22b\n\t" "s_sub_u32 %[rt], %[rt], 1\n\t" XCNT(100) "24:\n\t" \
    "buffer_load_dwordx4 v[240:243], %[mo], %[rs], %[sb] offen sc1\n\t" "buffer_load_dwordx4 v[244:247], v224, %[rs], %[sb] offen sc1\n\t" \
    "buffer_load_dwordx4 v[248:251], %[mo], %[rs], s60 offen sc1\n\t" "buffer_load_dwordx4 v[252:255], v224, %[rs], s60 offen sc1\n\t" "s_waitcnt vmcnt(0)\n\t" \
    "v_max_u32 v28, v240, v241\n\t" "v_max3_u32 v28, v28, v242, v243\n\t" "v_max3_u32 v28, v28, v244, v245\n\t" "v_max3_u32 v28, v28, v246, v247\n\t" \
    "v_max3_u32 v28, v28, v248, v249\n\t" "v_max3_u32 v28, v28, v250, v251\n\t" "v_max3_u32 v28, v28, v252, v253\n\t" "v_max3_u32 v28, v28, v254, v255\n\t" \
    "v_cmp_eq_u32 vcc, -1, v28\n\t" "s_cbranch_vccz 25f\n\t" "s_cmp_eq_u32 %[bd], 0\n\t" "s_cbranch_scc1 25f\n\t" "s_sub_u32 %[bd], %[bd], 1\n\t" XCNT(101) "s_sleep 1\n\t" \
    "s_branch 24b\n\t" "25:\n\t" "s_barrier\n\t" "s_branch 20b\n\t" "92:\n\t" XT(80, 81) "s_barrier\n\t" XT(82, 83) "30:\n\t" \
    "buffer_load_dwordx4 v[240:243], %[mo], %[rs], %[sb] offen sc1\n\t" "v_mov_b64 v[232:233], 0\n\t" "v_mov_b64 v[234:235], 0\n\t" "v_mov_b64 v[236:237], 0\n\t" \
    "v_mov_b64 v[238:239], 0\n\t" "v_mov_b64 v[192:193], 0\n\t" "v_mov_b64 v[194:195], 0\n\t" "v_mov_b64 v[196:197], 0\n\t" "v_mov_b64 v[198:199], 0\n\t" \
    "buffer_load_dwordx4 v[244:247], v224, %[rs], %[sb] offen sc1\n\t" "ds_read_b128 v[160:163], %[l0] offset:0\n\t" "ds_read_b128 v[164:167], %[l0] offset:1024\n\t" \
    "ds_read_b128 v[168:171], %[l0] offset:2048\n\t" "ds_read_b128 v[172:175], %[l0] offset:3072\n\t" "s_nop 3\n\t" \
    "buffer_load_dwordx4 v[248:251], %[mo], %[rs], s60 offen sc1\n\t" "ds_read_b128 v[176:179], %[l0] offset:4096\n\t" "ds_read_b128 v[180:183], %[l0] offset:5120\n\t" \
    "ds_read_b128 v[184:187], %[l0] offset:6144\n\t" "ds_read_b128 v[188:191], %[l0] offset:7168\n\t" "s_nop 3\n\t" \
    "buffer_load_dwordx4 v[252:255], v224, %[rs], s60 offen sc1\n\t" "v_mov_b64 v[200:201], 0\n\t" "v_mov_b64 v[202:203], 0\n\t" "v_mov_b64 v[204:205], 0\n\t" \
    "v_mov_b64 v[206:207], 0\n\t" "v_mov_b64 v[208:209], 0\n\t" "v_mov_b64 v[210:211], 0\n\t" "v_mov_b64 v[212:213], 0\n\t" "v_mov_b64 v[214:215], 0\n\t" \
    "v_mov_b64 v[216:217], 0\n\t" "v_mov_b64 v[218:219], 0\n\t" "v_mov_b64 v[220:221], 0\n\t" "v_mov_b64 v[222:223], 0\n\t" "s_waitcnt vmcnt(3)\n\t" XT(84, 85) \
    "v_smfmac_f32_16x16x64_bf16 v[232:235], v[240:243], a[0:7], %[ix]\n\t" "ds_write_b128 %[l1], v[240:243] offset:8192\n\t" \
    "v_smfmac_f32_16x16x64_bf16 v[236:239], v[240:243], a[8:15], %[ix]\n\t" "s_waitcnt vmcnt(2)\n\t" "v_smfmac_f32_16x16x64_bf16 v[232:235], v[244:247], a[16:23], %[ix]\n\t" \
    "ds_write_b128 %[l1], v[244:247] offset:9216\n\t" "s_waitcnt lgkmcnt(8)\n\t" "v_smfmac_f32_16x16x64_bf16 v[236:239], v[244:247], v[160:167], %[ix]\n\t" \
    "s_waitcnt lgkmcnt(0)\n\t" "s_barrier\n\t" XT(86, 87) "ds_read_b128 v[16:19], %[l1] offset:12288\n\t" "ds_read_b128 v[20:23], %[l1] offset:0\n\t" \
    "ds_read_b128 v[24:27], %[l1] offset:4096\n\t" "s_waitcnt vmcnt(1)\n\t" "v_smfmac_f32_16x16x64_bf16 v[232:235], v[248:251], a[24:31], %[ix]\n\t" \
    "ds_write_b128 %[l1], v[248:251] offset:10240\n\t" "v_smfmac_f32_16x16x64_bf16 v[236:239], v[248:251], a[32:39], %[ix]\n\t" \
    "ds_read_b128 v[28:31], %[l1] offset:13312\n\t" "ds_read_b128 v[240:243], %[l1] offset:1024\n\t" "ds_read_b128 v[244:247], %[l1] offset:5120\n\t" "s_waitcnt vmcnt(0)\n\t" \
    "v_smfmac_f32_16x16x64_bf16 v[232:235], v[252:255], a[40:47], %[ix]\n\t" "ds_write_b128 %[l1], v[252:255] offset:11264\n\t" \
    "v_smfmac_f32_16x16x64_bf16 v[236:239], v[252:255], v[168:175], %[ix]\n\t" "ds_read_b128 v[160:163], %[l0] offset:8192\n\t" \
    "ds_read_b128 v[164:167], %[l0] offset:9216\n\t" "s_waitcnt lgkmcnt(9)\n\t" "v_smfmac_f32_16x16x64_bf16 v[232:235], v[16:19], a[48:55], %[ix]\n\t" \
    "ds_read_b128 v[168:171], %[l0] offset:10240\n\t" "ds_read_b128 v[172:175], %[l0] offset:11264\n\t" "v_smfmac_f32_16x16x64_bf16 v[236:239], v[16:19], a[56:63], %[ix]\n\t" \
    "s_waitcnt lgkmcnt(10)\n\t" "v_smfmac_f32_16x16x64_bf16 v[232:235], v[20:23], a[64:71], %[ix]\n\t" \
    "v_smfmac_f32_16x16x64_bf16 v[236:239], v[20:23], v[176:183], %[ix]\n\t" "s_waitcnt lgkmcnt(4)\n\t" "s_barrier\n\t" XT(88, 89) \
    "v_smfmac_f32_16x16x64_bf16 v[232:235], v[24:27], a[72:79], %[ix]\n\t" "ds_read_b128 v[176:179], %[l0] offset:12288\n\t" "ds_read_b128 v[180:183], %[l0] offset:13312\n\t" \
    "v_smfmac_f32_16x16x64_bf16 v[236:239], v[24:27], a[80:87], %[ix]\n\t" "ds_read_b128 v[248:251], %[l1] offset:14336\n\t" "ds_read_b128 v[252:255], %[l1] offset:2048\n\t" \
    "v_smfmac_f32_16x16x64_bf16 v[232:235], v[28:31], a[88:95], %[ix]\n\t" "ds_read_b128 v[16:19], %[l1] offset:6144\n\t" "ds_read_b128 v[20:23], %[l1] offset:15360\n\t" \
    "v_smfmac_f32_16x16x64_bf16 v[236:239], v[28:31], v[184:191], %[ix]\n\t" "ds_read_b128 v[24:27], %[l1] offset:3072\n\t" "ds_read_b128 v[184:187], %[l0] offset:14336\n\t" \
    "v_smfmac_f32_16x16x64_bf16 v[232:235], v[240:243], a[96:103], %[ix]\n\t" "ds_read_b128 v[188:191], %[l0] offset:15360\n\t" "ds_read_b128 v[28:31], %[l1] offset:7168\n\t" \
    "v_smfmac_f32_16x16x64_bf16 v[236:239], v[240:243], a[104:111], %[ix]\n\t" "v_smfmac_f32_16x16x64_bf16 v[232:235], v[244:247], a[112:119], %[ix]\n\t" \
    "s_waitcnt lgkmcnt(12)\n\t" "v_smfmac_f32_16x16x64_bf16 v[236:239], v[244:247], v[160:167], %[ix]\n\t" "ds_read_b128 v[160:163], %[l0] offset:16384\n\t" \
    "ds_read_b128 v[164:167], %[l0] offset:17408\n\t" "s_waitcnt lgkmcnt(9)\n\t" "v_smfmac_f32_16x16x64_bf16 v[232:235], v[248:251], a[120:127], %[ix]\n\t" \
    "v_smfmac_f32_16x16x64_bf16 v[236:239], v[248:251], a[128:135], %[ix]\n\t" "s_waitcnt lgkmcnt(8)\n\t" \
    "v_smfmac_f32_16x16x64_bf16 v[232:235], v[252:255], a[136:143], %[ix]\n\t" "v_smfmac_f32_16x16x64_bf16 v[236:239], v[252:255], v[168:175], %[ix]\n\t" \
    "ds_read_b128 v[168:171], %[l0] offset:18432\n\t" "ds_read_b128 v[172:175], %[l0] offset:19456\n\t" "s_waitcnt lgkmcnt(9)\n\t" \
    "v_smfmac_f32_16x16x64_bf16 v[232:235], v[16:19], a[144:151], %[ix]\n\t" "v_smfmac_f32_16x16x64_bf16 v[236:239], v[16:19], a[152:159], %[ix]\n\t" \
    "s_waitcnt lgkmcnt(8)\n\t" "v_smfmac_f32_16x16x64_bf16 v[232:235], v[20:23], a[160:167], %[ix]\n\t" \
    "v_smfmac_f32_16x16x64_bf16 v[236:239], v[20:23], v[176:183], %[ix]\n\t" "ds_read_b128 v[176:179], %[l0] offset:20480\n\t" \
    "ds_read_b128 v[180:183], %[l0] offset:21504\n\t" "s_waitcnt lgkmcnt(9)\n\t" "v_smfmac_f32_16x16x64_bf16 v[232:235], v[24:27], a[168:175], %[ix]\n\t" \
    "v_smfmac_f32_16x16x64_bf16 v[236:239], v[24:27], a[176:183], %[ix]\n\t" "s_waitcnt lgkmcnt(6)\n\t" \
    "v_smfmac_f32_16x16x64_bf16 v[232:235], v[28:31], a[184:191], %[ix]\n\t" "v_smfmac_f32_16x16x64_bf16 v[236:239], v[28:31], v[184:191], %[ix]\n\t" \
    "ds_read_b128 v[184:187], %[l0] offset:22528\n\t" "ds_read_b128 v[188:191], %[l0] offset:23552\n\t" XT(90, 91) "s_nop 4\n\t" "v_add_f32 v232, v232, v233\n\t" \
    "v_add_f32 v234, v234, v235\n\t" "v_add_f32 v236, v236, v237\n\t" "v_add_f32 v238, v238, v239\n\t" "v_cvt_pk_bf16_f32 v236, v232, v236\n\t" \
    "v_cvt_pk_bf16_f32 v237, v234, v238\n\t" "ds_write2_b32 %[vw], v236, v237 offset1:68\n\t" "v_cmp_u_f32 vcc, v232, v234\n\t" "s_cbranch_vccnz 31f\n\t" "32:\n\t" \
    "s_waitcnt lgkmcnt(0)\n\t" "s_barrier\n\t" XT(92, 93) "ds_read_b128 v[240:243], %[vr] offset:0\n\t" "ds_read_b128 v[244:247], %[vr] offset:64\n\t" \
    "ds_read_b128 v[248:251], %[vr] offset:128\n\t" "ds_read_b128 v[252:255], %[vr] offset:192\n\t" "s_mov_b32 s61, %[ub]\n\t" "s_waitcnt lgkmcnt(3)\n\t" \
    "v_smfmac_f32_16x16x64_bf16 v[192:195], v[240:243], v[32:39], %[ix]\n\t" "v_smfmac_f32_16x16x64_bf16 v[196:199], v[240:243], v[48:55], %[ix]\n\t" \
    "s_waitcnt lgkmcnt(2)\n\t" "v_smfmac_f32_16x16x64_bf16 v[192:195], v[244:247], v[40:47], %[ix]\n\t" \
    "v_smfmac_f32_16x16x64_bf16 v[196:199], v[244:247], v[56:63], %[ix]\n\t" "s_waitcnt lgkmcnt(1)\n\t" \
    "v_smfmac_f32_16x16x64_bf16 v[192:195], v[248:251], a[192:199], %[ix]\n\t" "v_smfmac_f32_16x16x64_bf16 v[196:199], v[248:251], a[200:207], %[ix]\n\t" \
    "s_waitcnt lgkmcnt(0)\n\t" "v_smfmac_f32_16x16x64_bf16 v[192:195], v[252:255], v[160:167], %[ix]\n\t" "ds_read_b128 v[160:163], %[l0] offset:24576\n\t" \
    "ds_read_b128 v[164:167], %[l0] offset:25600\n\t" "v_smfmac_f32_16x16x64_bf16 v[196:199], v[252:255], v[168:175], %[ix]\n\t" \
    "ds_read_b128 v[168:171], %[l0] offset:26624\n\t" "ds_read_b128 v[172:175], %[l0] offset:27648\n\t" \
    "v_smfmac_f32_16x16x64_bf16 v[200:203], v[240:243], v[64:71], %[ix]\n\t" "s_nop 1\n\t" "v_smfmac_f32_16x16x64_bf16 v[204:207], v[240:243], v[80:87], %[ix]\n\t" \
    "v_cvt_pk_bf16_f32 v224, v192, v194\n\t" "v_cvt_pk_bf16_f32 v225, v193, v195\n\t" "v_smfmac_f32_16x16x64_bf16 v[200:203], v[244:247], v[72:79], %[ix]\n\t" \
    "v_cvt_pk_bf16_f32 v226, v196, v198\n\t" "v_cvt_pk_bf16_f32 v227, v197, v199\n\t" "v_smfmac_f32_16x16x64_bf16 v[204:207], v[244:247], v[88:95], %[ix]\n\t" "s_nop 0\n\t" \
    "v_permlane32_swap_b32 v224, v226\n\t" "v_smfmac_f32_16x16x64_bf16 v[200:203], v[248:251], a[208:215], %[ix]\n\t" "v_permlane32_swap_b32 v225, v227\n\t" "s_nop 0\n\t" \
    "v_smfmac_f32_16x16x64_bf16 v[204:207], v[248:251], a[216:223], %[ix]\n\t" "v_permlane16_swap_b32 v224, v225\n\t" "v_permlane16_swap_b32 v226, v227\n\t" \
    "v_smfmac_f32_16x16x64_bf16 v[200:203], v[252:255], v[176:183], %[ix]\n\t" "ds_read_b128 v[176:179], %[l0] offset:28672\n\t" \
    "buffer_store_dwordx4 v[224:227], %[uo], %[rs], s61 offen sc1\n\t" "s_add_u32 s61, s61, 0x10000\n\t" XT(94, 95) \
    "v_smfmac_f32_16x16x64_bf16 v[204:207], v[252:255], v[184:191], %[ix]\n\t" "ds_read_b128 v[180:183], %[l0] offset:29696\n\t" \
    "ds_read_b128 v[184:187], %[l0] offset:30720\n\t" "v_smfmac_f32_16x16x64_bf16 v[208:211], v[240:243], v[96:103], %[ix]\n\t" \
    "ds_read_b128 v[188:191], %[l0] offset:31744\n\t" "s_nop 0\n\t" "v_smfmac_f32_16x16x64_bf16 v[212:215], v[240:243], v[112:119], %[ix]\n\t" \
    "v_cvt_pk_bf16_f32 v228, v200, v202\n\t" "v_cvt_pk_bf16_f32 v229, v201, v203\n\t" "v_smfmac_f32_16x16x64_bf16 v[208:211], v[244:247], v[104:111], %[ix]\n\t" \
    "v_cvt_pk_bf16_f32 v230, v204, v206\n\t" "v_cvt_pk_bf16_f32 v231, v205, v207\n\t" "v_smfmac_f32_16x16x64_bf16 v[212:215], v[244:247], v[120:127], %[ix]\n\t" "s_nop 0\n\t" \
    "v_permlane32_swap_b32 v228, v230\n\t" "v_smfmac_f32_16x16x64_bf16 v[208:211], v[248:251], a[224:231], %[ix]\n\t" "v_permlane32_swap_b32 v229, v231\n\t" "s_nop 0\n\t" \
    "v_smfmac_f32_16x16x64_bf16 v[212:215], v[248:251], a[232:239], %[ix]\n\t" "v_permlane16_swap_b32 v228, v229\n\t" "v_permlane16_swap_b32 v230, v231\n\t" \
    "s_waitcnt lgkmcnt(6)\n\t" "v_smfmac_f32_16x16x64_bf16 v[208:211], v[252:255], v[160:167], %[ix]\n\t" "buffer_store_dwordx4 v[228:231], %[uo], %[rs], s61 offen sc1\n\t" \
    "s_add_u32 s61, s61, 0x10000\n\t" "s_waitcnt lgkmcnt(4)\n\t" "v_smfmac_f32_16x16x64_bf16 v[212:215], v[252:255], v[168:175], %[ix]\n\t" "s_nop 1\n\t" \
    "v_smfmac_f32_16x16x64_bf16 v[216:219], v[240:243], v[128:135], %[ix]\n\t" "s_nop 1\n\t" "v_smfmac_f32_16x16x64_bf16 v[220:223], v[240:243], v[144:151], %[ix]\n\t" \
    "v_cvt_pk_bf16_f32 v224, v208, v210\n\t" "v_cvt_pk_bf16_f32 v225, v209, v211\n\t" "v_smfmac_f32_16x16x64_bf16 v[216:219], v[244:247], v[136:143], %[ix]\n\t" \
    "v_cvt_pk_bf16_f32 v226, v212, v214\n\t" "v_cvt_pk_bf16_f32 v227, v213, v215\n\t" "v_smfmac_f32_16x16x64_bf16 v[220:223], v[244:247], v[152:159], %[ix]\n\t" "s_nop 0\n\t" \
    "v_permlane32_swap_b32 v224, v226\n\t" "v_smfmac_f32_16x16x64_bf16 v[216:219], v[248:251], a[240:247], %[ix]\n\t" "v_permlane32_swap_b32 v225, v227\n\t" "s_nop 0\n\t" \
    "v_smfmac_f32_16x16x64_bf16 v[220:223], v[248:251], a[248:255], %[ix]\n\t" "v_permlane16_swap_b32 v224, v225\n\t" "v_permlane16_swap_b32 v226, v227\n\t" \
    "s_waitcnt lgkmcnt(2)\n\t" "v_smfmac_f32_16x16x64_bf16 v[216:219], v[252:255], v[176:183], %[ix]\n\t" "buffer_store_dwordx4 v[224:227], %[uo], %[rs], s61 offen sc1\n\t" \
    "s_add_u32 s61, s61, 0x10000\n\t" "s_waitcnt lgkmcnt(0)\n\t" "v_smfmac_f32_16x16x64_bf16 v[220:223], v[252:255], v[184:191], %[ix]\n\t" "s_nop 5\n\t" \
    "v_cvt_pk_bf16_f32 v228, v216, v218\n\t" "v_cvt_pk_bf16_f32 v229, v217, v219\n\t" "v_cvt_pk_bf16_f32 v230, v220, v222\n\t" "v_cvt_pk_bf16_f32 v231, v221, v223\n\t" \
    "s_nop 0\n\t" "v_permlane32_swap_b32 v228, v230\n\t" "v_permlane32_swap_b32 v229, v231\n\t" "s_nop 1\n\t" "v_permlane16_swap_b32 v228, v229\n\t" \
    "v_permlane16_swap_b32 v230, v231\n\t" "buffer_store_dwordx4 v[228:231], %[uo], %[rs], s61 offen sc1\n\t" "s_nop 1\n\t" XT(96, 97) "s_branch 99f\n\t" "31:\n\t" \
    "s_mov_b64 s[62:63], 0\n\t" "ds_read_b128 v[16:19], %[l1] offset:0\n\t" "ds_read_b128 v[20:23], %[l1] offset:1024\n\t" "ds_read_b128 v[24:27], %[l1] offset:2048\n\t" \
    "ds_read_b128 v[28:31], %[l1] offset:3072\n\t" "s_waitcnt lgkmcnt(3)\n\t" "v_max3_u32 v16, v16, v17, v18\n\t" "v_max_u32 v16, v16, v19\n\t" \
    "v_cmp_eq_u32 vcc, -1, v16\n\t" "ds_read_b128 v[16:19], %[l1] offset:4096\n\t" "s_or_b64 s[62:63], s[62:63], vcc\n\t" "s_waitcnt lgkmcnt(3)\n\t" \
    "v_max3_u32 v20, v20, v21, v22\n\t" "v_max_u32 v20, v20, v23\n\t" "v_cmp_eq_u32 vcc, -1, v20\n\t" "ds_read_b128 v[20:23], %[l1] offset:5120\n\t" \
    "s_or_b64 s[62:63], s[62:63], vcc\n\t" "s_waitcnt lgkmcnt(3)\n\t" "v_max3_u32 v24, v24, v25, v26\n\t" "v_max_u32 v24, v24, v27\n\t" "v_cmp_eq_u32 vcc, -1, v24\n\t" \
    "ds_read_b128 v[24:27], %[l1] offset:6144\n\t" "s_or_b64 s[62:63], s[62:63], vcc\n\t" "s_waitcnt lgkmcnt(3)\n\t" "v_max3_u32 v28, v28, v29, v30\n\t" \
    "v_max_u32 v28, v28, v31\n\t" "v_cmp_eq_u32 vcc, -1, v28\n\t" "ds_read_b128 v[28:31], %[l1] offset:7168\n\t" "s_or_b64 s[62:63], s[62:63], vcc\n\t" \
    "s_waitcnt lgkmcnt(3)\n\t" "v_max3_u32 v16, v16, v17, v18\n\t" "v_max_u32 v16, v16, v19\n\t" "v_cmp_eq_u32 vcc, -1, v16\n\t" \
    "ds_read_b128 v[16:19], %[l1] offset:8192\n\t" "s_or_b64 s[62:63], s[62:63], vcc\n\t" "s_waitcnt lgkmcnt(3)\n\t" "v_max3_u32 v20, v20, v21, v22\n\t" \
    "v_max_u32 v20, v20, v23\n\t" "v_cmp_eq_u32 vcc, -1, v20\n\t" "ds_read_b128 v[20:23], %[l1] offset:9216\n\t" "s_or_b64 s[62:63], s[62:63], vcc\n\t" \
    "s_waitcnt lgkmcnt(3)\n\t" "v_max3_u32 v24, v24, v25, v26\n\t" "v_max_u32 v24, v24, v27\n\t" "v_cmp_eq_u32 vcc, -1, v24\n\t" \
    "ds_read_b128 v[24:27], %[l1] offset:10240\n\t" "s_or_b64 s[62:63], s[62:63], vcc\n\t" "s_waitcnt lgkmcnt(3)\n\t" "v_max3_u32 v28, v28, v29, v30\n\t" \
    "v_max_u32 v28, v28, v31\n\t" "v_cmp_eq_u32 vcc, -1, v28\n\t" "ds_read_b128 v[28:31], %[l1] offset:11264\n\t" "s_or_b64 s[62:63], s[62:63], vcc\n\t" \
    "s_waitcnt lgkmcnt(3)\n\t" "v_max3_u32 v16, v16, v17, v18\n\t" "v_max_u32 v16, v16, v19\n\t" "v_cmp_eq_u32 vcc, -1, v16\n\t" \
    "ds_read_b128 v[16:19], %[l1] offset:12288\n\t" "s_or_b64 s[62:63], s[62:63], vcc\n\t" "s_waitcnt lgkmcnt(3)\n\t" "v_max3_u32 v20, v20, v21, v22\n\t" \
    "v_max_u32 v20, v20, v23\n\t" "v_cmp_eq_u32 vcc, -1, v20\n\t" "ds_read_b128 v[20:23], %[l1] offset:13312\n\t" "s_or_b64 s[62:63], s[62:63], vcc\n\t" \
    "s_waitcnt lgkmcnt(3)\n\t" "v_max3_u32 v24, v24, v25, v26\n\t" "v_max_u32 v24, v24, v27\n\t" "v_cmp_eq_u32 vcc, -1, v24\n\t" \
    "ds_read_b128 v[24:27], %[l1] offset:14336\n\t" "s_or_b64 s[62:63], s[62:63], vcc\n\t" "s_waitcnt lgkmcnt(3)\n\t" "v_max3_u32 v28, v28, v29, v30\n\t" \
    "v_max_u32 v28, v28, v31\n\t" "v_cmp_eq_u32 vcc, -1, v28\n\t" "ds_read_b128 v[28:31], %[l1] offset:15360\n\t" "s_or_b64 s[62:63], s[62:63], vcc\n\t" \
    "s_waitcnt lgkmcnt(3)\n\t" "v_max3_u32 v16, v16, v17, v18\n\t" "v_max_u32 v16, v16, v19\n\t" "v_cmp_eq_u32 vcc, -1, v16\n\t" "s_or_b64 s[62:63], s[62:63], vcc\n\t" \
    "s_waitcnt lgkmcnt(2)\n\t" "v_max3_u32 v20, v20, v21, v22\n\t" "v_max_u32 v20, v20, v23\n\t" "v_cmp_eq_u32 vcc, -1, v20\n\t" "s_or_b64 s[62:63], s[62:63], vcc\n\t" \
    "s_waitcnt lgkmcnt(1)\n\t" "v_max3_u32 v24, v24, v25, v26\n\t" "v_max_u32 v24, v24, v27\n\t" "v_cmp_eq_u32 vcc, -1, v24\n\t" "s_or_b64 s[62:63], s[62:63], vcc\n\t" \
    "s_waitcnt lgkmcnt(0)\n\t" "v_max3_u32 v28, v28, v29, v30\n\t" "v_max_u32 v28, v28, v31\n\t" "v_cmp_eq_u32 vcc, -1, v28\n\t" "s_or_b64 s[62:63], s[62:63], vcc\n\t" \
    "s_cmp_eq_u64 s[62:63], 0\n\t" "s_cbranch_scc1 32b\n\t" "s_cmp_eq_u32 %[rt], 0\n\t" "s_cbranch_scc1 32b\n\t" "s_sub_u32 %[rt], %[rt], 1\n\t" XCNT(100) "34:\n\t" \
    "buffer_load_dwordx4 v[240:243], %[mo], %[rs], %[sb] offen sc1\n\t" "buffer_load_dwordx4 v[244:247], v224, %[rs], %[sb] offen sc1\n\t" \
    "buffer_load_dwordx4 v[248:251], %[mo], %[rs], s60 offen sc1\n\t" "buffer_load_dwordx4 v[252:255], v224, %[rs], s60 offen sc1\n\t" "s_waitcnt vmcnt(0)\n\t" \
    "v_max_u32 v28, v240, v241\n\t" "v_max3_u32 v28, v28, v242, v243\n\t" "v_max3_u32 v28, v28, v244, v245\n\t" "v_max3_u32 v28, v28, v246, v247\n\t" \
    "v_max3_u32 v28, v28, v248, v249\n\t" "v_max3_u32 v28, v28, v250, v251\n\t" "v_max3_u32 v28, v28, v252, v253\n\t" "v_max3_u32 v28, v28, v254, v255\n\t" \
    "v_cmp_eq_u32 vcc, -1, v28\n\t" "s_cbranch_vccz 35f\n\t" "s_cmp_eq_u32 %[bd], 0\n\t" "s_cbranch_scc1 35f\n\t" "s_sub_u32 %[bd], %[bd], 1\n\t" XCNT(101) "s_sleep 1\n\t" \
    "s_branch 34b\n\t" "35:\n\t" "s_barrier\n\t" "s_branch 30b\n\t" "93:\n\t" XT(80, 81) "s_barrier\n\t" XT(82, 83) "40:\n\t" \
    "buffer_load_dwordx4 v[240:243], %[mo], %[rs], %[sb] offen sc1\n\t" "v_mov_b64 v[232:233], 0\n\t" "v_mov_b64 v[234:235], 0\n\t" "v_mov_b64 v[236:237], 0\n\t" \
    "v_mov_b64 v[238:239], 0\n\t" "v_mov_b64 v[192:193], 0\n\t" "v_mov_b64 v[194:195], 0\n\t" "v_mov_b64 v[196:197], 0\n\t" "v_mov_b64 v[198:199], 0\n\t" \
    "buffer_load_dwordx4 v[244:247], v224, %[rs], %[sb] offen sc1\n\t" "ds_read_b128 v[160:163], %[l0] offset:0\n\t" "ds_read_b128 v[164:167], %[l0] offset:1024\n\t" \
    "ds_read_b128 v[168:171], %[l0] offset:2048\n\t" "ds_read_b128 v[172:175], %[l0] offset:3072\n\t" "s_nop 3\n\t" \
    "buffer_load_dwordx4 v[248:251], %[mo], %[rs], s60 offen sc1\n\t" "ds_read_b128 v[176:179], %[l0] offset:4096\n\t" "ds_read_b128 v[180:183], %[l0] offset:5120\n\t" \
    "ds_read_b128 v[184:187], %[l0] offset:6144\n\t" "ds_read_b128 v[188:191], %[l0] offset:7168\n\t" "s_nop 3\n\t" \
    "buffer_load_dwordx4 v[252:255], v224, %[rs], s60 offen sc1\n\t" "v_mov_b64 v[200:201], 0\n\t" "v_mov_b64 v[202:203], 0\n\t" "v_mov_b64 v[204:205], 0\n\t" \
    "v_mov_b64 v[206:207], 0\n\t" "v_mov_b64 v[208:209], 0\n\t" "v_mov_b64 v[210:211], 0\n\t" "v_mov_b64 v[212:213], 0\n\t" "v_mov_b64 v[214:215], 0\n\t" \
    "v_mov_b64 v[216:217], 0\n\t" "v_mov_b64 v[218:219], 0\n\t" "v_mov_b64 v[220:221], 0\n\t" "v_mov_b64 v[222:223], 0\n\t" "s_waitcnt vmcnt(3)\n\t" XT(84, 85) \
    "v_smfmac_f32_16x16x64_bf16 v[232:235], v[240:243], a[0:7], %[ix]\n\t" "ds_write_b128 %[l1], v[240:243] offset:12288\n\t" \
    "v_smfmac_f32_16x16x64_bf16 v[236:239], v[240:243], a[8:15], %[ix]\n\t" "s_waitcnt vmcnt(2)\n\t" "v_smfmac_f32_16x16x64_bf16 v[232:235], v[244:247], a[16:23], %[ix]\n\t" \
    "ds_write_b128 %[l1], v[244:247] offset:13312\n\t" "s_waitcnt lgkmcnt(8)\n\t" "v_smfmac_f32_16x16x64_bf16 v[236:239], v[244:247], v[160:167], %[ix]\n\t" \
    "s_waitcnt lgkmcnt(0)\n\t" "s_barrier\n\t" XT(86, 87) "ds_read_b128 v[16:19], %[l1] offset:0\n\t" "ds_read_b128 v[20:23], %[l1] offset:4096\n\t" \
    "ds_read_b128 v[24:27], %[l1] offset:8192\n\t" "s_waitcnt vmcnt(1)\n\t" "v_smfmac_f32_16x16x64_bf16 v[232:235], v[248:251], a[24:31], %[ix]\n\t" \
    "ds_write_b128 %[l1], v[248:251] offset:14336\n\t" "v_smfmac_f32_16x16x64_bf16 v[236:239], v[248:251], a[32:39], %[ix]\n\t" "ds_read_b128 v[28:31], %[l1] offset:1024\n\t" \
    "ds_read_b128 v[240:243], %[l1] offset:5120\n\t" "ds_read_b128 v[244:247], %[l1] offset:9216\n\t" "s_waitcnt vmcnt(0)\n\t" \
    "v_smfmac_f32_16x16x64_bf16 v[232:235], v[252:255], a[40:47], %[ix]\n\t" "ds_write_b128 %[l1], v[252:255] offset:15360\n\t" \
    "v_smfmac_f32_16x16x64_bf16 v[236:239], v[252:255], v[168:175], %[ix]\n\t" "ds_read_b128 v[160:163], %[l0] offset:8192\n\t" \
    "ds_read_b128 v[164:167], %[l0] offset:9216\n\t" "s_waitcnt lgkmcnt(9)\n\t" "v_smfmac_f32_16x16x64_bf16 v[232:235], v[16:19], a[48:55], %[ix]\n\t" \
    "ds_read_b128 v[168:171], %[l0] offset:10240\n\t" "ds_read_b128 v[172:175], %[l0] offset:11264\n\t" "v_smfmac_f32_16x16x64_bf16 v[236:239], v[16:19], a[56:63], %[ix]\n\t" \
    "s_waitcnt lgkmcnt(10)\n\t" "v_smfmac_f32_16x16x64_bf16 v[232:235], v[20:23], a[64:71], %[ix]\n\t" \
    "v_smfmac_f32_16x16x64_bf16 v[236:239], v[20:23], v[176:183], %[ix]\n\t" "s_waitcnt lgkmcnt(4)\n\t" "s_barrier\n\t" XT(88, 89) \
    "v_smfmac_f32_16x16x64_bf16 v[232:235], v[24:27], a[72:79], %[ix]\n\t" "ds_read_b128 v[176:179], %[l0] offset:12288\n\t" "ds_read_b128 v[180:183], %[l0] offset:13312\n\t" \
    "v_smfmac_f32_16x16x64_bf16 v[236:239], v[24:27], a[80:87], %[ix]\n\t" "ds_read_b128 v[248:251], %[l1] offset:2048\n\t" "ds_read_b128 v[252:255], %[l1] offset:6144\n\t" \
    "v_smfmac_f32_16x16x64_bf16 v[232:235], v[28:31], a[88:95], %[ix]\n\t" "ds_read_b128 v[16:19], %[l1] offset:10240\n\t" "ds_read_b128 v[20:23], %[l1] offset:3072\n\t" \
    "v_smfmac_f32_16x16x64_bf16 v[236:239], v[28:31], v[184:191], %[ix]\n\t" "ds_read_b128 v[24:27], %[l1] offset:7168\n\t" "ds_read_b128 v[184:187], %[l0] offset:14336\n\t" \
    "v_smfmac_f32_16x16x64_bf16 v[232:235], v[240:243], a[96:103], %[ix]\n\t" "ds_read_b128 v[188:191], %[l0] offset:15360\n\t" \
    "ds_read_b128 v[28:31], %[l1] offset:11264\n\t" "v_smfmac_f32_16x16x64_bf16 v[236:239], v[240:243], a[104:111], %[ix]\n\t" \
    "v_smfmac_f32_16x16x64_bf16 v[232:235], v[244:247], a[112:119], %[ix]\n\t" "s_waitcnt lgkmcnt(12)\n\t" \
    "v_smfmac_f32_16x16x64_bf16 v[236:239], v[244:247], v[160:167], %[ix]\n\t" "ds_read_b128 v[160:163], %[l0] offset:16384\n\t" \
    "ds_read_b128 v[164:167], %[l0] offset:17408\n\t" "s_waitcnt lgkmcnt(9)\n\t" "v_smfmac_f32_16x16x64_bf16 v[232:235], v[248:251], a[120:127], %[ix]\n\t" \
    "v_smfmac_f32_16x16x64_bf16 v[236:239], v[248:251], a[128:135], %[ix]\n\t" "s_waitcnt lgkmcnt(8)\n\t" \
    "v_smfmac_f32_16x16x64_bf16 v[232:235], v[252:255], a[136:143], %[ix]\n\t" "v_smfmac_f32_16x16x64_bf16 v[236:239], v[252:255], v[168:175], %[ix]\n\t" \
    "ds_read_b128 v[168:171], %[l0] offset:18432\n\t" "ds_read_b128 v[172:175], %[l0] offset:19456\n\t" "s_waitcnt lgkmcnt(9)\n\t" \
    "v_smfmac_f32_16x16x64_bf16 v[232:235], v[16:19], a[144:151], %[ix]\n\t" "v_smfmac_f32_16x16x64_bf16 v[236:239], v[16:19], a[152:159], %[ix]\n\t" \
    "s_waitcnt lgkmcnt(8)\n\t" "v_smfmac_f32_16x16x64_bf16 v[232:235], v[20:23], a[160:167], %[ix]\n\t" \
    "v_smfmac_f32_16x16x64_bf16 v[236:239], v[20:23], v[176:183], %[ix]\n\t" "ds_read_b128 v[176:179], %[l0] offset:20480\n\t" \
    "ds_read_b128 v[180:183], %[l0] offset:21504\n\t" "s_waitcnt lgkmcnt(9)\n\t" "v_smfmac_f32_16x16x64_bf16 v[232:235], v[24:27], a[168:175], %[ix]\n\t" \
    "v_smfmac_f32_16x16x64_bf16 v[236:239], v[24:27], a[176:183], %[ix]\n\t" "s_waitcnt lgkmcnt(6)\n\t" \
    "v_smfmac_f32_16x16x64_bf16 v[232:235], v[28:31], a[184:191], %[ix]\n\t" "v_smfmac_f32_16x16x64_bf16 v[236:239], v[28:31], v[184:191], %[ix]\n\t" \
    "ds_read_b128 v[184:187], %[l0] offset:22528\n\t" "ds_read_b128 v[188:191], %[l0] offset:23552\n\t" XT(90, 91) "s_nop 4\n\t" "v_add_f32 v232, v232, v233\n\t" \
    "v_add_f32 v234, v234, v235\n\t" "v_add_f32 v236, v236, v237\n\t" "v_add_f32 v238, v238, v239\n\t" "v_cvt_pk_bf16_f32 v236, v232, v236\n\t" \
    "v_cvt_pk_bf16_f32 v237, v234, v238\n\t" "ds_write2_b32 %[vw], v236, v237 offset1:68\n\t" "v_cmp_u_f32 vcc, v232, v234\n\t" "s_cbranch_vccnz 41f\n\t" "42:\n\t" \
    "s_waitcnt lgkmcnt(0)\n\t" "s_barrier\n\t" XT(92, 93) "ds_read_b128 v[240:243], %[vr] offset:0\n\t" "ds_read_b128 v[244:247], %[vr] offset:64\n\t" \
    "ds_read_b128 v[248:251], %[vr] offset:128\n\t" "ds_read_b128 v[252:255], %[vr] offset:192\n\t" "s_mov_b32 s61, %[ub]\n\t" "s_waitcnt lgkmcnt(3)\n\t" \
    "v_smfmac_f32_16x16x64_bf16 v[192:195], v[240:243], v[32:39], %[ix]\n\t" "v_smfmac_f32_16x16x64_bf16 v[196:199], v[240:243], v[48:55], %[ix]\n\t" \
    "s_waitcnt lgkmcnt(2)\n\t" "v_smfmac_f32_16x16x64_bf16 v[192:195], v[244:247], v[40:47], %[ix]\n\t" \
    "v_smfmac_f32_16x16x64_bf16 v[196:199], v[244:247], v[56:63], %[ix]\n\t" "s_waitcnt lgkmcnt(1)\n\t" \
    "v_smfmac_f32_16x16x64_bf16 v[192:195], v[248:251], a[192:199], %[ix]\n\t" "v_smfmac_f32_16x16x64_bf16 v[196:199], v[248:251], a[200:207], %[ix]\n\t" \
    "s_waitcnt lgkmcnt(0)\n\t" "v_smfmac_f32_16x16x64_bf16 v[192:195], v[252:255], v[160:167], %[ix]\n\t" "ds_read_b128 v[160:163], %[l0] offset:24576\n\t" \
    "ds_read_b128 v[164:167], %[l0] offset:25600\n\t" "v_smfmac_f32_16x16x64_bf16 v[196:199], v[252:255], v[168:175], %[ix]\n\t" \
    "ds_read_b128 v[168:171], %[l0] offset:26624\n\t" "ds_read_b128 v[172:175], %[l0] offset:27648\n\t" \
    "v_smfmac_f32_16x16x64_bf16 v[200:203], v[240:243], v[64:71], %[ix]\n\t" "s_nop 1\n\t" "v_smfmac_f32_16x16x64_bf16 v[204:207], v[240:243], v[80:87], %[ix]\n\t" \
    "v_cvt_pk_bf16_f32 v224, v192, v194\n\t" "v_cvt_pk_bf16_f32 v225, v193, v195\n\t" "v_smfmac_f32_16x16x64_bf16 v[200:203], v[244:247], v[72:79], %[ix]\n\t" \
    "v_cvt_pk_bf16_f32 v226, v196, v198\n\t" "v_cvt_pk_bf16_f32 v227, v197, v199\n\t" "v_smfmac_f32_16x16x64_bf16 v[204:207], v[244:247], v[88:95], %[ix]\n\t" "s_nop 0\n\t" \
    "v_permlane32_swap_b32 v224, v226\n\t" "v_smfmac_f32_16x16x64_bf16 v[200:203], v[248:251], a[208:215], %[ix]\n\t" "v_permlane32_swap_b32 v225, v227\n\t" "s_nop 0\n\t" \
    "v_smfmac_f32_16x16x64_bf16 v[204:207], v[248:251], a[216:223], %[ix]\n\t" "v_permlane16_swap_b32 v224, v225\n\t" "v_permlane16_swap_b32 v226, v227\n\t" \
    "v_smfmac_f32_16x16x64_bf16 v[200:203], v[252:255], v[176:183], %[ix]\n\t" "ds_read_b128 v[176:179], %[l0] offset:28672\n\t" \
    "buffer_store_dwordx4 v[224:227], %[uo], %[rs], s61 offen sc1\n\t" "s_add_u32 s61, s61, 0x10000\n\t" XT(94, 95) \
    "v_smfmac_f32_16x16x64_bf16 v[204:207], v[252:255], v[184:191], %[ix]\n\t" "ds_read_b128 v[180:183], %[l0] offset:29696\n\t" \
    "ds_read_b128 v[184:187], %[l0] offset:30720\n\t" "v_smfmac_f32_16x16x64_bf16 v[208:211], v[240:243], v[96:103], %[ix]\n\t" \
    "ds_read_b128 v[188:191], %[l0] offset:31744\n\t" "s_nop 0\n\t" "v_smfmac_f32_16x16x64_bf16 v[212:215], v[240:243], v[112:119], %[ix]\n\t" \
    "v_cvt_pk_bf16_f32 v228, v200, v202\n\t" "v_cvt_pk_bf16_f32 v229, v201, v203\n\t" "v_smfmac_f32_16x16x64_bf16 v[208:211], v[244:247], v[104:111], %[ix]\n\t" \
    "v_cvt_pk_bf16_f32 v230, v204, v206\n\t" "v_cvt_pk_bf16_f32 v231, v205, v207\n\t" "v_smfmac_f32_16x16x64_bf16 v[212:215], v[244:247], v[120:127], %[ix]\n\t" "s_nop 0\n\t" \
    "v_permlane32_swap_b32 v228, v230\n\t" "v_smfmac_f32_16x16x64_bf16 v[208:211], v[248:251], a[224:231], %[ix]\n\t" "v_permlane32_swap_b32 v229, v231\n\t" "s_nop 0\n\t" \
    "v_smfmac_f32_16x16x64_bf16 v[212:215], v[248:251], a[232:239], %[ix]\n\t" "v_permlane16_swap_b32 v228, v229\n\t" "v_permlane16_swap_b32 v230, v231\n\t" \
    "s_waitcnt lgkmcnt(6)\n\t" "v_smfmac_f32_16x16x64_bf16 v[208:211], v[252:255], v[160:167], %[ix]\n\t" "buffer_store_dwordx4 v[228:231], %[uo], %[rs], s61 offen sc1\n\t" \
    "s_add_u32 s61, s61, 0x10000\n\t" "s_waitcnt lgkmcnt(4)\n\t" "v_smfmac_f32_16x16x64_bf16 v[212:215], v[252:255], v[168:175], %[ix]\n\t" "s_nop 1\n\t" \
    "v_smfmac_f32_16x16x64_bf16 v[216:219], v[240:243], v[128:135], %[ix]\n\t" "s_nop 1\n\t" "v_smfmac_f32_16x16x64_bf16 v[220:223], v[240:243], v[144:151], %[ix]\n\t" \
    "v_cvt_pk_bf16_f32 v224, v208, v210\n\t" "v_cvt_pk_bf16_f32 v225, v209, v211\n\t" "v_smfmac_f32_16x16x64_bf16 v[216:219], v[244:247], v[136:143], %[ix]\n\t" \
    "v_cvt_pk_bf16_f32 v226, v212, v214\n\t" "v_cvt_pk_bf16_f32 v227, v213, v215\n\t" "v_smfmac_f32_16x16x64_bf16 v[220:223], v[244:247], v[152:159], %[ix]\n\t" "s_nop 0\n\t" \
    "v_permlane32_swap_b32 v224, v226\n\t" "v_smfmac_f32_16x16x64_bf16 v[216:219], v[248:251], a[240:247], %[ix]\n\t" "v_permlane32_swap_b32 v225, v227\n\t" "s_nop 0\n\t" \
    "v_smfmac_f32_16x16x64_bf16 v[220:223], v[248:251], a[248:255], %[ix]\n\t" "v_permlane16_swap_b32 v224, v225\n\t" "v_permlane16_swap_b32 v226, v227\n\t" \
    "s_waitcnt lgkmcnt(2)\n\t" "v_smfmac_f32_16x16x64_bf16 v[216:219], v[252:255], v[176:183], %[ix]\n\t" "buffer_store_dwordx4 v[224:227], %[uo], %[rs], s61 offen sc1\n\t" \
    "s_add_u32 s61, s61, 0x10000\n\t" "s_waitcnt lgkmcnt(0)\n\t" "v_smfmac_f32_16x16x64_bf16 v[220:223], v[252:255], v[184:191], %[ix]\n\t" "s_nop 5\n\t" \
    "v_cvt_pk_bf16_f32 v228, v216, v218\n\t" "v_cvt_pk_bf16_f32 v229, v217, v219\n\t" "v_cvt_pk_bf16_f32 v230, v220, v222\n\t" "v_cvt_pk_bf16_f32 v231, v221, v223\n\t" \
    "s_nop 0\n\t" "v_permlane32_swap_b32 v228, v230\n\t" "v_permlane32_swap_b32 v229, v231\n\t" "s_nop 1\n\t" "v_permlane16_swap_b32 v228, v229\n\t" \
    "v_permlane16_swap_b32 v230, v231\n\t" "buffer_store_dwordx4 v[228:231], %[uo], %[rs], s61 offen sc1\n\t" "s_nop 1\n\t" XT(96, 97) "s_branch 99f\n\t" "41:\n\t" \
    "s_mov_b64 s[62:63], 0\n\t" "ds_read_b128 v[16:19], %[l1] offset:0\n\t" "ds_read_b128 v[20:23], %[l1] offset:1024\n\t" "ds_read_b128 v[24:27], %[l1] offset:2048\n\t" \
    "ds_read_b128 v[28:31], %[l1] offset:3072\n\t" "s_waitcnt lgkmcnt(3)\n\t" "v_max3_u32 v16, v16, v17, v18\n\t" "v_max_u32 v16, v16, v19\n\t" \
    "v_cmp_eq_u32 vcc, -1, v16\n\t" "ds_read_b128 v[16:19], %[l1] offset:4096\n\t" "s_or_b64 s[62:63], s[62:63], vcc\n\t" "s_waitcnt lgkmcnt(3)\n\t" \
    "v_max3_u32 v20, v20, v21, v22\n\t" "v_max_u32 v20, v20, v23\n\t" "v_cmp_eq_u32 vcc, -1, v20\n\t" "ds_read_b128 v[20:23], %[l1] offset:5120\n\t" \
    "s_or_b64 s[62:63], s[62:63], vcc\n\t" "s_waitcnt lgkmcnt(3)\n\t" "v_max3_u32 v24, v24, v25, v26\n\t" "v_max_u32 v24, v24, v27\n\t" "v_cmp_eq_u32 vcc, -1, v24\n\t" \
    "ds_read_b128 v[24:27], %[l1] offset:6144\n\t" "s_or_b64 s[62:63], s[62:63], vcc\n\t" "s_waitcnt lgkmcnt(3)\n\t" "v_max3_u32 v28, v28, v29, v30\n\t" \
    "v_max_u32 v28, v28, v31\n\t" "v_cmp_eq_u32 vcc, -1, v28\n\t" "ds_read_b128 v[28:31], %[l1] offset:7168\n\t" "s_or_b64 s[62:63], s[62:63], vcc\n\t" \
    "s_waitcnt lgkmcnt(3)\n\t" "v_max3_u32 v16, v16, v17, v18\n\t" "v_max_u32 v16, v16, v19\n\t" "v_cmp_eq_u32 vcc, -1, v16\n\t" \
    "ds_read_b128 v[16:19], %[l1] offset:8192\n\t" "s_or_b64 s[62:63], s[62:63], vcc\n\t" "s_waitcnt lgkmcnt(3)\n\t" "v_max3_u32 v20, v20, v21, v22\n\t" \
    "v_max_u32 v20, v20, v23\n\t" "v_cmp_eq_u32 vcc, -1, v20\n\t" "ds_read_b128 v[20:23], %[l1] offset:9216\n\t" "s_or_b64 s[62:63], s[62:63], vcc\n\t" \
    "s_waitcnt lgkmcnt(3)\n\t" "v_max3_u32 v24, v24, v25, v26\n\t" "v_max_u32 v24, v24, v27\n\t" "v_cmp_eq_u32 vcc, -1, v24\n\t" \
    "ds_read_b128 v[24:27], %[l1] offset:10240\n\t" "s_or_b64 s[62:63], s[62:63], vcc\n\t" "s_waitcnt lgkmcnt(3)\n\t" "v_max3_u32 v28, v28, v29, v30\n\t" \
    "v_max_u32 v28, v28, v31\n\t" "v_cmp_eq_u32 vcc, -1, v28\n\t" "ds_read_b128 v[28:31], %[l1] offset:11264\n\t" "s_or_b64 s[62:63], s[62:63], vcc\n\t" \
    "s_waitcnt lgkmcnt(3)\n\t" "v_max3_u32 v16, v16, v17, v18\n\t" "v_max_u32 v16, v16, v19\n\t" "v_cmp_eq_u32 vcc, -1, v16\n\t" \
    "ds_read_b128 v[16:19], %[l1] offset:12288\n\t" "s_or_b64 s[62:63], s[62:63], vcc\n\t" "s_waitcnt lgkmcnt(3)\n\t" "v_max3_u32 v20, v20, v21, v22\n\t" \
    "v_max_u32 v20, v20, v23\n\t" "v_cmp_eq_u32 vcc, -1, v20\n\t" "ds_read_b128 v[20:23], %[l1] offset:13312\n\t" "s_or_b64 s[62:63], s[62:63], vcc\n\t" \
    "s_waitcnt lgkmcnt(3)\n\t" "v_max3_u32 v24, v24, v25, v26\n\t" "v_max_u32 v24, v24, v27\n\t" "v_cmp_eq_u32 vcc, -1, v24\n\t" \
    "ds_read_b128 v[24:27], %[l1] offset:14336\n\t" "s_or_b64 s[62:63], s[62:63], vcc\n\t" "s_waitcnt lgkmcnt(3)\n\t" "v_max3_u32 v28, v28, v29, v30\n\t" \
    "v_max_u32 v28, v28, v31\n\t" "v_cmp_eq_u32 vcc, -1, v28\n\t" "ds_read_b128 v[28:31], %[l1] offset:15360\n\t" "s_or_b64 s[62:63], s[62:63], vcc\n\t" \
    "s_waitcnt lgkmcnt(3)\n\t" "v_max3_u32 v16, v16, v17, v18\n\t" "v_max_u32 v16, v16, v19\n\t" "v_cmp_eq_u32 vcc, -1, v16\n\t" "s_or_b64 s[62:63], s[62:63], vcc\n\t" \
    "s_waitcnt lgkmcnt(2)\n\t" "v_max3_u32 v20, v20, v21, v22\n\t" "v_max_u32 v20, v20, v23\n\t" "v_cmp_eq_u32 vcc, -1, v20\n\t" "s_or_b64 s[62:63], s[62:63], vcc\n\t" \
    "s_waitcnt lgkmcnt(1)\n\t" "v_max3_u32 v24, v24, v25, v26\n\t" "v_max_u32 v24, v24, v27\n\t" "v_cmp_eq_u32 vcc, -1, v24\n\t" "s_or_b64 s[62:63], s[62:63], vcc\n\t" \
    "s_waitcnt lgkmcnt(0)\n\t" "v_max3_u32 v28, v28, v29, v30\n\t" "v_max_u32 v28, v28, v31\n\t" "v_cmp_eq_u32 vcc, -1, v28\n\t" "s_or_b64 s[62:63], s[62:63], vcc\n\t" \
    "s_cmp_eq_u64 s[62:63], 0\n\t" "s_cbranch_scc1 42b\n\t" "s_cmp_eq_u32 %[rt], 0\n\t" "s_cbranch_scc1 42b\n\t" "s_sub_u32 %[rt], %[rt], 1\n\t" XCNT(100) "44:\n\t" \
    "buffer_load_dwordx4 v[240:243], %[mo], %[rs], %[sb] offen sc1\n\t" "buffer_load_dwordx4 v[244:247], v224, %[rs], %[sb] offen sc1\n\t" \
    "buffer_load_dwordx4 v[248:251], %[mo], %[rs], s60 offen sc1\n\t" "buffer_load_dwordx4 v[252:255], v224, %[rs], s60 offen sc1\n\t" "s_waitcnt vmcnt(0)\n\t" \
    "v_max_u32 v28, v240, v241\n\t" "v_max3_u32 v28, v28, v242, v243\n\t" "v_max3_u32 v28, v28, v244, v245\n\t" "v_max3_u32 v28, v28, v246, v247\n\t" \
    "v_max3_u32 v28, v28, v248, v249\n\t" "v_max3_u32 v28, v28, v250, v251\n\t" "v_max3_u32 v28, v28, v252, v253\n\t" "v_max3_u32 v28, v28, v254, v255\n\t" \
    "v_cmp_eq_u32 vcc, -1, v28\n\t" "s_cbranch_vccz 45f\n\t" "s_cmp_eq_u32 %[bd], 0\n\t" "s_cbranch_scc1 45f\n\t" "s_sub_u32 %[bd], %[bd], 1\n\t" XCNT(101) "s_sleep 1\n\t" \
    "s_branch 44b\n\t" "45:\n\t" "s_barrier\n\t" "s_branch 40b\n\t" "99:\n\t"
#define Y_STEP \
    "v_xor_b32 v128, 0, %[u0]\n\t" "v_xor_b32 v132, 0, %[pm]\n\t" "v_xor_b32 v129, 2048, %[u0]\n\t" "v_xor_b32 v133, 1024, %[pm]\n\t" "v_xor_b32 v130, 4096, %[u0]\n\t" \
    "v_xor_b32 v134, 2048, %[pm]\n\t" "v_xor_b32 v131, 6144, %[u0]\n\t" "v_xor_b32 v135, 3072, %[pm]\n\t" "s_getpc_b64 s[62:63]\n\t" "11:\n\t" "s_memtime s[64:65]\n\t" \
    "s_waitcnt lgkmcnt(0)\n\t" "s_sub_u32 s61, %[tu], s64\n\t" "s_cmp_gt_i32 s61, 124\n\t" "s_cbranch_scc1 11b\n\t" "s_max_i32 s61, s61, 0\n\t" "s_and_b32 s61, s61, 124\n\t" \
    "s_sub_u32 s61, 12f-11b, s61\n\t" "s_add_u32 s62, s62, s61\n\t" "s_addc_u32 s63, s63, 0\n\t" "s_setpc_b64 s[62:63]\n\t" ".rept 31\n\t" "s_nop 0\n\t" ".endr\n\t" "12:\n\t" \
    XT(90, 91) "buffer_load_dwordx4 v[144:147], v128, %[rs], %[ub] offen offset:0 sc1\n\t" "buffer_load_dwordx4 v[148:151], v128, %[rs], %[ub] offen offset:1024 sc1\n\t" \
    "buffer_load_dwordx4 v[152:155], v129, %[rs], %[ub] offen offset:0 sc1\n\t" "buffer_load_dwordx4 v[156:159], v129, %[rs], %[ub] offen offset:1024 sc1\n\t" \
    "buffer_load_dwordx4 v[160:163], v130, %[rs], %[ub] offen offset:0 sc1\n\t" "buffer_load_dwordx4 v[164:167], v130, %[rs], %[ub] offen offset:1024 sc1\n\t" \
    "buffer_load_dwordx4 v[168:171], v131, %[rs], %[ub] offen offset:0 sc1\n\t" "buffer_load_dwordx4 v[172:175], v131, %[rs], %[ub] offen offset:1024 sc1\n\t" \
    "s_cmp_lg_u32 %[late], 0\n\t" "s_cbranch_scc1 14f\n\t" "s_getpc_b64 s[62:63]\n\t" "13:\n\t" "s_memtime s[64:65]\n\t" "s_waitcnt lgkmcnt(0)\n\t" \
    "s_sub_u32 s61, %[tg], s64\n\t" "s_cmp_gt_i32 s61, 124\n\t" "s_cbranch_scc1 13b\n\t" "s_max_i32 s61, s61, 0\n\t" "s_and_b32 s61, s61, 124\n\t" \
    "s_sub_u32 s61, 14f-13b, s61\n\t" "s_add_u32 s62, s62, s61\n\t" "s_addc_u32 s63, s63, 0\n\t" "s_setpc_b64 s[62:63]\n\t" ".rept 31\n\t" "s_nop 0\n\t" ".endr\n\t" "14:\n\t" \
    XT(92, 93) "20:\n\t" "buffer_load_dwordx2 v[176:177], v132, %[rs], %[pb] offen offset:0 sc1\n\t" \
    "buffer_load_dwordx2 v[178:179], v132, %[rs], %[pb] offen offset:128 sc1\n\t" "buffer_load_dwordx4 v[192:195], %[x0], %[rs], %[pb] offen offset:0 sc1\n\t" \
    "buffer_load_dwordx4 v[196:199], %[x0], %[rs], %[pb] offen offset:1024 sc1\n\t" "buffer_load_dwordx4 v[200:203], %[x0], %[rs], %[pb] offen offset:2048 sc1\n\t" \
    "buffer_load_dwordx4 v[204:207], %[x0], %[rs], %[pb] offen offset:3072 sc1\n\t" "v_mov_b64 v[120:121], 0\n\t" "v_mov_b64 v[122:123], 0\n\t" \
    "buffer_load_dwordx2 v[180:181], v133, %[rs], %[pb] offen offset:0 sc1\n\t" "buffer_load_dwordx2 v[182:183], v133, %[rs], %[pb] offen offset:128 sc1\n\t" \
    "buffer_load_dwordx4 v[208:211], %[x1], %[rs], %[pb] offen offset:0 sc1\n\t" "buffer_load_dwordx4 v[212:215], %[x1], %[rs], %[pb] offen offset:1024 sc1\n\t" \
    "buffer_load_dwordx4 v[216:219], %[x1], %[rs], %[pb] offen offset:2048 sc1\n\t" "buffer_load_dwordx4 v[220:223], %[x1], %[rs], %[pb] offen offset:3072 sc1\n\t" \
    "buffer_load_dwordx2 v[184:185], v134, %[rs], %[pb] offen offset:0 sc1\n\t" "buffer_load_dwordx2 v[186:187], v134, %[rs], %[pb] offen offset:128 sc1\n\t" \
    "buffer_load_dwordx4 v[224:227], %[x2], %[rs], %[pb] offen offset:0 sc1\n\t" "buffer_load_dwordx4 v[228:231], %[x2], %[rs], %[pb] offen offset:1024 sc1\n\t" \
    "buffer_load_dwordx4 v[232:235], %[x2], %[rs], %[pb] offen offset:2048 sc1\n\t" "buffer_load_dwordx4 v[236:239], %[x2], %[rs], %[pb] offen offset:3072 sc1\n\t" \
    "buffer_load_dwordx2 v[188:189], v135, %[rs], %[pb] offen offset:0 sc1\n\t" "buffer_load_dwordx2 v[190:191], v135, %[rs], %[pb] offen offset:128 sc1\n\t" \
    "buffer_load_dwordx4 v[240:243], %[x3], %[rs], %[pb] offen offset:0 sc1\n\t" "buffer_load_dwordx4 v[244:247], %[x3], %[rs], %[pb] offen offset:1024 sc1\n\t" \
    "buffer_load_dwordx4 v[248:251], %[x3], %[rs], %[pb] offen offset:2048 sc1\n\t" "buffer_load_dwordx4 v[252:255], %[x3], %[rs], %[pb] offen offset:3072 sc1\n\t" \
    "buffer_load_dword %[sv], %[so], %[rs], %[pb] offen sc1\n\t" "v_mov_b32 v127, -1\n\t" "s_waitcnt vmcnt(23)\n\t" XT(94, 95) \
    "v_smfmac_f32_16x16x64_bf16 v[120:123], v[176:179], v[144:151], %[ix]\n\t" "s_waitcnt vmcnt(22)\n\t" \
    "v_smfmac_f32_16x16x64_bf16 v[120:123], v[192:195], a[0:7], %[ix]\n\t" "s_waitcnt vmcnt(21)\n\t" "v_smfmac_f32_16x16x64_bf16 v[120:123], v[196:199], a[8:15], %[ix]\n\t" \
    "s_waitcnt vmcnt(20)\n\t" "v_smfmac_f32_16x16x64_bf16 v[120:123], v[200:203], a[16:23], %[ix]\n\t" "s_waitcnt vmcnt(19)\n\t" \
    "v_smfmac_f32_16x16x64_bf16 v[120:123], v[204:207], a[24:31], %[ix]\n\t" "s_waitcnt vmcnt(17)\n\t" \
    "v_smfmac_f32_16x16x64_bf16 v[120:123], v[180:183], v[152:159], %[ix]\n\t" "s_waitcnt vmcnt(16)\n\t" \
    "v_smfmac_f32_16x16x64_bf16 v[120:123], v[208:211], a[32:39], %[ix]\n\t" "s_waitcnt vmcnt(15)\n\t" \
    "v_smfmac_f32_16x16x64_bf16 v[120:123], v[212:215], a[40:47], %[ix]\n\t" "s_waitcnt vmcnt(14)\n\t" \
    "v_smfmac_f32_16x16x64_bf16 v[120:123], v[216:219], a[48:55], %[ix]\n\t" "s_waitcnt vmcnt(13)\n\t" \
    "v_smfmac_f32_16x16x64_bf16 v[120:123], v[220:223], a[56:63], %[ix]\n\t" "s_waitcnt vmcnt(11)\n\t" \
    "v_smfmac_f32_16x16x64_bf16 v[120:123], v[184:187], v[160:167], %[ix]\n\t" "s_waitcnt vmcnt(10)\n\t" \
    "v_smfmac_f32_16x16x64_bf16 v[120:123], v[224:227], a[64:71], %[ix]\n\t" "s_waitcnt vmcnt(9)\n\t" "v_smfmac_f32_16x16x64_bf16 v[120:123], v[228:231], a[72:79], %[ix]\n\t" \
    "s_waitcnt vmcnt(8)\n\t" "v_smfmac_f32_16x16x64_bf16 v[120:123], v[232:235], a[80:87], %[ix]\n\t" "s_waitcnt vmcnt(7)\n\t" \
    "v_smfmac_f32_16x16x64_bf16 v[120:123], v[236:239], a[88:95], %[ix]\n\t" "s_waitcnt vmcnt(5)\n\t" \
    "v_smfmac_f32_16x16x64_bf16 v[120:123], v[188:191], v[168:175], %[ix]\n\t" "s_waitcnt vmcnt(4)\n\t" \
    "v_smfmac_f32_16x16x64_bf16 v[120:123], v[240:243], a[96:103], %[ix]\n\t" "s_waitcnt vmcnt(3)\n\t" \
    "v_smfmac_f32_16x16x64_bf16 v[120:123], v[244:247], a[104:111], %[ix]\n\t" "s_waitcnt vmcnt(2)\n\t" \
    "v_smfmac_f32_16x16x64_bf16 v[120:123], v[248:251], a[112:119], %[ix]\n\t" "s_waitcnt vmcnt(1)\n\t" \
    "v_smfmac_f32_16x16x64_bf16 v[120:123], v[252:255], a[120:127], %[ix]\n\t" XT(82, 83) "s_nop 7\n\t" "v_add_f32 v120, v120, v121\n\t" "v_add_f32 v122, v122, v123\n\t" \
    "v_cvt_pk_bf16_f32 v124, v120, v122\n\t" "v_cmp_u_f32 vcc, v120, v122\n\t" "s_cbranch_vccnz 30f\n\t" "21:\n\t" \
    "v_mov_b32_dpp v125, v124 quad_perm:[1,0,3,2] row_mask:0xf bank_mask:0xf\n\t" "v_perm_b32 v124, v125, v124, %[ps]\n\t" "v_min_u32 v124, -2, v124\n\t" \
    "buffer_store_dword v124, %[mw], %[rs], %[mb] offen sc1\n\t" XT(84, 85) "buffer_store_dword v127, %[mw], %[rs], %[mr] offen sc1\n\t" "s_memtime s[64:65]\n\t" \
    "s_barrier\n\t" XT(98, 99) "s_waitcnt lgkmcnt(0)\n\t" "s_and_b32 %[bad], s64, -2\n\t" "s_branch 40f\n\t" "30:\n\t" "v_max_u32 v126, v144, v145\n\t" \
    "v_max3_u32 v126, v126, v146, v147\n\t" "v_max3_u32 v126, v126, v148, v149\n\t" "v_max3_u32 v126, v126, v150, v151\n\t" "v_max3_u32 v126, v126, v152, v153\n\t" \
    "v_max3_u32 v126, v126, v154, v155\n\t" "v_max3_u32 v126, v126, v156, v157\n\t" "v_max3_u32 v126, v126, v158, v159\n\t" "v_max3_u32 v126, v126, v160, v161\n\t" \
    "v_max3_u32 v126, v126, v162, v163\n\t" "v_max3_u32 v126, v126, v164, v165\n\t" "v_max3_u32 v126, v126, v166, v167\n\t" "v_max3_u32 v126, v126, v168, v169\n\t" \
    "v_max3_u32 v126, v126, v170, v171\n\t" "v_max3_u32 v126, v126, v172, v173\n\t" "v_max3_u32 v126, v126, v174, v175\n\t" UCNT "v_max3_u32 v126, v126, v176, v177\n\t" \
    "v_max3_u32 v126, v126, v178, v179\n\t" "v_max3_u32 v126, v126, v180, v181\n\t" "v_max3_u32 v126, v126, v182, v183\n\t" "v_max3_u32 v126, v126, v184, v185\n\t" \
    "v_max3_u32 v126, v126, v186, v187\n\t" "v_max3_u32 v126, v126, v188, v189\n\t" "v_max3_u32 v126, v126, v190, v191\n\t" "v_max3_u32 v126, v126, v192, v193\n\t" \
    "v_max3_u32 v126, v126, v194, v195\n\t" "v_max3_u32 v126, v126, v196, v197\n\t" "v_max3_u32 v126, v126, v198, v199\n\t" "v_max3_u32 v126, v126, v200, v201\n\t" \
    "v_max3_u32 v126, v126, v202, v203\n\t" "v_max3_u32 v126, v126, v204, v205\n\t" "v_max3_u32 v126, v126, v206, v207\n\t" "v_max3_u32 v126, v126, v208, v209\n\t" \
    "v_max3_u32 v126, v126, v210, v211\n\t" "v_max3_u32 v126, v126, v212, v213\n\t" "v_max3_u32 v126, v126, v214, v215\n\t" "v_max3_u32 v126, v126, v216, v217\n\t" \
    "v_max3_u32 v126, v126, v218, v219\n\t" "v_max3_u32 v126, v126, v220, v221\n\t" "v_max3_u32 v126, v126, v222, v223\n\t" "v_max3_u32 v126, v126, v224, v225\n\t" \
    "v_max3_u32 v126, v126, v226, v227\n\t" "v_max3_u32 v126, v126, v228, v229\n\t" "v_max3_u32 v126, v126, v230, v231\n\t" "v_max3_u32 v126, v126, v232, v233\n\t" \
    "v_max3_u32 v126, v126, v234, v235\n\t" "v_max3_u32 v126, v126, v236, v237\n\t" "v_max3_u32 v126, v126, v238, v239\n\t" "v_max3_u32 v126, v126, v240, v241\n\t" \
    "v_max3_u32 v126, v126, v242, v243\n\t" "v_max3_u32 v126, v126, v244, v245\n\t" "v_max3_u32 v126, v126, v246, v247\n\t" "v_max3_u32 v126, v126, v248, v249\n\t" \
    "v_max3_u32 v126, v126, v250, v251\n\t" "v_max3_u32 v126, v126, v252, v253\n\t" "v_max3_u32 v126, v126, v254, v255\n\t" "v_cmp_eq_u32 vcc, -1, v126\n\t" \
    "s_cbranch_vccz 21b\n\t" "s_cmp_eq_u32 %[bd], 0\n\t" "s_cbranch_scc1 21b\n\t" XCNT(100) "s_mov_b32 %[bad], 1\n\t" "s_waitcnt vmcnt(0)\n\t" "40:\n\t"
#define G_LOOP \
    "v_xor_b32 v72, 0, %[xo]\n\t" "v_add_u32 v76, 0x10000, v72\n\t" "v_xor_b32 v88, 0, %[xs]\n\t" "v_add_u32 v88, 0, v88\n\t" "v_xor_b32 v73, 64, %[xo]\n\t" \
    "v_add_u32 v77, 0x10000, v73\n\t" "v_xor_b32 v89, 16, %[xs]\n\t" "v_add_u32 v89, 2048, v89\n\t" "v_xor_b32 v74, 128, %[xo]\n\t" "v_add_u32 v78, 0x10000, v74\n\t" \
    "v_xor_b32 v90, 32, %[xs]\n\t" "v_add_u32 v90, 4096, v90\n\t" "v_xor_b32 v75, 192, %[xo]\n\t" "v_add_u32 v79, 0x10000, v75\n\t" "v_xor_b32 v91, 48, %[xs]\n\t" \
    "v_add_u32 v91, 6144, v91\n\t" "v_xor_b32 v92, 64, %[xs]\n\t" "v_add_u32 v92, 8192, v92\n\t" "v_xor_b32 v93, 80, %[xs]\n\t" "v_add_u32 v93, 10240, v93\n\t" \
    "v_xor_b32 v94, 96, %[xs]\n\t" "v_add_u32 v94, 12288, v94\n\t" "v_xor_b32 v95, 112, %[xs]\n\t" "v_add_u32 v95, 14336, v95\n\t" "v_mov_b32 v96, 2.0\n\t" \
    "s_mov_b32 s60, 0\n\t" "s_mov_b32 s62, 0\n\t" "s_waitcnt vmcnt(0)\n\t" "s_barrier\n\t" "ds_read_b128 v[100:103], v72 offset:0\n\t" \
    "ds_read_b128 v[104:107], v72 offset:32768\n\t" "ds_read_b128 v[108:111], v73 offset:0\n\t" "ds_read_b128 v[112:115], v73 offset:32768\n\t" \
    "ds_read_b128 v[116:119], v74 offset:0\n\t" "10:\n\t" XT(80, 81) "s_waitcnt lgkmcnt(4)\n\t" "v_mfma_f32_16x16x32_bf16 v[32:35], a[0:3], v[100:103], 0\n\t" \
    "ds_read_b128 v[120:123], v74 offset:32768\n\t" "s_add_u32 s61, s60, 1\n\t" "s_and_b32 s61, s61, 63\n\t" "v_mfma_f32_16x16x32_bf16 v[36:39], a[4:7], v[100:103], 0\n\t" \
    "s_lshl_b32 s61, s61, 16\n\t" "s_add_u32 s63, %[lw], 65536\n\t" "s_waitcnt lgkmcnt(4)\n\t" "v_mfma_f32_16x16x32_bf16 v[40:43], a[0:3], v[104:107], 0\n\t" \
    "ds_read_b128 v[124:127], v75 offset:0\n\t" "v_mfma_f32_16x16x32_bf16 v[44:47], a[4:7], v[104:107], 0\n\t" "s_waitcnt lgkmcnt(4)\n\t" \
    "v_mfma_f32_16x16x32_bf16 v[32:35], a[8:11], v[108:111], v[32:35]\n\t" "ds_read_b128 v[128:131], v75 offset:32768\n\t" "v_cvt_pk_bf16_f32 v64, v48, v49\n\t" \
    "v_mfma_f32_16x16x32_bf16 v[36:39], a[12:15], v[108:111], v[36:39]\n\t" "v_cvt_pk_bf16_f32 v65, v50, v51\n\t" "v_cvt_pk_bf16_f32 v66, v52, v53\n\t" \
    "s_waitcnt lgkmcnt(4)\n\t" "v_mfma_f32_16x16x32_bf16 v[40:43], a[8:11], v[112:115], v[40:43]\n\t" "ds_read_b128 v[100:103], v72 offset:256\n\t" \
    "v_cvt_pk_bf16_f32 v67, v54, v55\n\t" "v_mfma_f32_16x16x32_bf16 v[44:47], a[12:15], v[112:115], v[44:47]\n\t" "v_cvt_pk_bf16_f32 v68, v56, v57\n\t" \
    "v_cvt_pk_bf16_f32 v69, v58, v59\n\t" "s_waitcnt lgkmcnt(4)\n\t" "v_mfma_f32_16x16x32_bf16 v[32:35], a[16:19], v[116:119], v[32:35]\n\t" \
    "ds_read_b128 v[104:107], v72 offset:33024\n\t" "v_cvt_pk_bf16_f32 v70, v60, v61\n\t" "v_mfma_f32_16x16x32_bf16 v[36:39], a[20:23], v[116:119], v[36:39]\n\t" \
    "v_cvt_pk_bf16_f32 v71, v62, v63\n\t" "buffer_store_dwordx4 v[64:67], v96, %[rs], s62 offen offset:0 sc1\n\t" "s_waitcnt lgkmcnt(4)\n\t" \
    "v_mfma_f32_16x16x32_bf16 v[40:43], a[16:19], v[120:123], v[40:43]\n\t" "ds_read_b128 v[108:111], v73 offset:256\n\t" \
    "v_mfma_f32_16x16x32_bf16 v[44:47], a[20:23], v[120:123], v[44:47]\n\t" "s_waitcnt lgkmcnt(4)\n\t" "v_mfma_f32_16x16x32_bf16 v[32:35], a[24:27], v[124:127], v[32:35]\n\t" \
    "ds_read_b128 v[112:115], v73 offset:33024\n\t" "v_mfma_f32_16x16x32_bf16 v[36:39], a[28:31], v[124:127], v[36:39]\n\t" \
    "buffer_store_dwordx4 v[68:71], v96, %[rs], s62 offen offset:256 sc1\n\t" "s_waitcnt lgkmcnt(4)\n\t" \
    "v_mfma_f32_16x16x32_bf16 v[40:43], a[24:27], v[128:131], v[40:43]\n\t" "ds_read_b128 v[116:119], v74 offset:256\n\t" "s_mov_b32 m0, s63\n\t" \
    "v_mfma_f32_16x16x32_bf16 v[44:47], a[28:31], v[128:131], v[44:47]\n\t" "s_add_u32 s63, s63, 0x800\n\t" "buffer_load_dwordx4 v88, %[rx], s61 offen lds\n\t" \
    "s_waitcnt lgkmcnt(4)\n\t" "v_mfma_f32_16x16x32_bf16 v[32:35], a[32:35], v[100:103], v[32:35]\n\t" "ds_read_b128 v[120:123], v74 offset:33024\n\t" \
    "v_mfma_f32_16x16x32_bf16 v[36:39], a[36:39], v[100:103], v[36:39]\n\t" "buffer_load_dwordx4 v88, %[rx], s61 offen offset:1024 lds\n\t" "s_waitcnt lgkmcnt(4)\n\t" \
    "v_mfma_f32_16x16x32_bf16 v[40:43], a[32:35], v[104:107], v[40:43]\n\t" "ds_read_b128 v[124:127], v75 offset:256\n\t" "s_mov_b32 m0, s63\n\t" \
    "v_mfma_f32_16x16x32_bf16 v[44:47], a[36:39], v[104:107], v[44:47]\n\t" "s_add_u32 s63, s63, 0x800\n\t" "buffer_load_dwordx4 v89, %[rx], s61 offen lds\n\t" \
    "s_waitcnt lgkmcnt(4)\n\t" "v_mfma_f32_16x16x32_bf16 v[32:35], a[40:43], v[108:111], v[32:35]\n\t" "ds_read_b128 v[128:131], v75 offset:33024\n\t" \
    "v_mfma_f32_16x16x32_bf16 v[36:39], a[44:47], v[108:111], v[36:39]\n\t" "buffer_load_dwordx4 v89, %[rx], s61 offen offset:1024 lds\n\t" "s_waitcnt lgkmcnt(4)\n\t" \
    "v_mfma_f32_16x16x32_bf16 v[40:43], a[40:43], v[112:115], v[40:43]\n\t" "ds_read_b128 v[100:103], v72 offset:512\n\t" "s_mov_b32 m0, s63\n\t" \
    "v_mfma_f32_16x16x32_bf16 v[44:47], a[44:47], v[112:115], v[44:47]\n\t" "s_add_u32 s63, s63, 0x800\n\t" "buffer_load_dwordx4 v90, %[rx], s61 offen lds\n\t" \
    "s_waitcnt lgkmcnt(4)\n\t" "v_mfma_f32_16x16x32_bf16 v[32:35], a[48:51], v[116:119], v[32:35]\n\t" "ds_read_b128 v[104:107], v72 offset:33280\n\t" \
    "v_mfma_f32_16x16x32_bf16 v[36:39], a[52:55], v[116:119], v[36:39]\n\t" "buffer_load_dwordx4 v90, %[rx], s61 offen offset:1024 lds\n\t" "s_waitcnt lgkmcnt(4)\n\t" \
    "v_mfma_f32_16x16x32_bf16 v[40:43], a[48:51], v[120:123], v[40:43]\n\t" "ds_read_b128 v[108:111], v73 offset:512\n\t" "s_mov_b32 m0, s63\n\t" \
    "v_mfma_f32_16x16x32_bf16 v[44:47], a[52:55], v[120:123], v[44:47]\n\t" "s_add_u32 s63, s63, 0x800\n\t" "buffer_load_dwordx4 v91, %[rx], s61 offen lds\n\t" \
    "s_waitcnt lgkmcnt(4)\n\t" "v_mfma_f32_16x16x32_bf16 v[32:35], a[56:59], v[124:127], v[32:35]\n\t" "ds_read_b128 v[112:115], v73 offset:33280\n\t" \
    "v_mfma_f32_16x16x32_bf16 v[36:39], a[60:63], v[124:127], v[36:39]\n\t" "buffer_load_dwordx4 v91, %[rx], s61 offen offset:1024 lds\n\t" "s_waitcnt lgkmcnt(4)\n\t" \
    "v_mfma_f32_16x16x32_bf16 v[40:43], a[56:59], v[128:131], v[40:43]\n\t" "ds_read_b128 v[116:119], v74 offset:512\n\t" "s_mov_b32 m0, s63\n\t" \
    "v_mfma_f32_16x16x32_bf16 v[44:47], a[60:63], v[128:131], v[44:47]\n\t" "s_add_u32 s63, s63, 0x800\n\t" "buffer_load_dwordx4 v92, %[rx], s61 offen lds\n\t" \
    "s_waitcnt lgkmcnt(4)\n\t" "v_mfma_f32_16x16x32_bf16 v[32:35], a[64:67], v[100:103], v[32:35]\n\t" "ds_read_b128 v[120:123], v74 offset:33280\n\t" \
    "v_mfma_f32_16x16x32_bf16 v[36:39], a[68:71], v[100:103], v[36:39]\n\t" "buffer_load_dwordx4 v92, %[rx], s61 offen offset:1024 lds\n\t" "s_waitcnt lgkmcnt(4)\n\t" \
    "v_mfma_f32_16x16x32_bf16 v[40:43], a[64:67], v[104:107], v[40:43]\n\t" "ds_read_b128 v[124:127], v75 offset:512\n\t" "s_mov_b32 m0, s63\n\t" \
    "v_mfma_f32_16x16x32_bf16 v[44:47], a[68:71], v[104:107], v[44:47]\n\t" "s_add_u32 s63, s63, 0x800\n\t" "buffer_load_dwordx4 v93, %[rx], s61 offen lds\n\t" \
    "s_waitcnt lgkmcnt(4)\n\t" "v_mfma_f32_16x16x32_bf16 v[32:35], a[72:75], v[108:111], v[32:35]\n\t" "ds_read_b128 v[128:131], v75 offset:33280\n\t" \
    "v_mfma_f32_16x16x32_bf16 v[36:39], a[76:79], v[108:111], v[36:39]\n\t" "buffer_load_dwordx4 v93, %[rx], s61 offen offset:1024 lds\n\t" "s_waitcnt lgkmcnt(4)\n\t" \
    "v_mfma_f32_16x16x32_bf16 v[40:43], a[72:75], v[112:115], v[40:43]\n\t" "ds_read_b128 v[100:103], v72 offset:768\n\t" "s_mov_b32 m0, s63\n\t" \
    "v_mfma_f32_16x16x32_bf16 v[44:47], a[76:79], v[112:115], v[44:47]\n\t" "s_add_u32 s63, s63, 0x800\n\t" "buffer_load_dwordx4 v94, %[rx], s61 offen lds\n\t" \
    "s_waitcnt lgkmcnt(4)\n\t" "v_mfma_f32_16x16x32_bf16 v[32:35], a[80:83], v[116:119], v[32:35]\n\t" "ds_read_b128 v[104:107], v72 offset:33536\n\t" \
    "v_mfma_f32_16x16x32_bf16 v[36:39], a[84:87], v[116:119], v[36:39]\n\t" "buffer_load_dwordx4 v94, %[rx], s61 offen offset:1024 lds\n\t" "s_waitcnt lgkmcnt(4)\n\t" \
    "v_mfma_f32_16x16x32_bf16 v[40:43], a[80:83], v[120:123], v[40:43]\n\t" "ds_read_b128 v[108:111], v73 offset:768\n\t" "s_mov_b32 m0, s63\n\t" \
    "v_mfma_f32_16x16x32_bf16 v[44:47], a[84:87], v[120:123], v[44:47]\n\t" "s_add_u32 s63, s63, 0x800\n\t" "buffer_load_dwordx4 v95, %[rx], s61 offen lds\n\t" \
    "s_waitcnt lgkmcnt(4)\n\t" "v_mfma_f32_16x16x32_bf16 v[32:35], a[88:91], v[124:127], v[32:35]\n\t" "ds_read_b128 v[112:115], v73 offset:33536\n\t" \
    "v_mfma_f32_16x16x32_bf16 v[36:39], a[92:95], v[124:127], v[36:39]\n\t" "buffer_load_dwordx4 v95, %[rx], s61 offen offset:1024 lds\n\t" "s_waitcnt lgkmcnt(4)\n\t" \
    "v_mfma_f32_16x16x32_bf16 v[40:43], a[88:91], v[128:131], v[40:43]\n\t" "ds_read_b128 v[116:119], v74 offset:768\n\t" \
    "v_mfma_f32_16x16x32_bf16 v[44:47], a[92:95], v[128:131], v[44:47]\n\t" "s_waitcnt lgkmcnt(4)\n\t" "v_mfma_f32_16x16x32_bf16 v[32:35], a[96:99], v[100:103], v[32:35]\n\t" \
    "ds_read_b128 v[120:123], v74 offset:33536\n\t" "s_lshr_b32 s64, s60, 2\n\t" "s_xor_b32 s64, s64, %[xp]\n\t" \
    "v_mfma_f32_16x16x32_bf16 v[36:39], a[100:103], v[100:103], v[36:39]\n\t" "s_and_b32 s64, s64, 1\n\t" "s_lshl_b32 s64, s64, 13\n\t" "s_waitcnt lgkmcnt(4)\n\t" \
    "v_mfma_f32_16x16x32_bf16 v[40:43], a[96:99], v[104:107], v[40:43]\n\t" "ds_read_b128 v[124:127], v75 offset:768\n\t" "s_lshl_b32 s62, s60, 19\n\t" \
    "v_mfma_f32_16x16x32_bf16 v[44:47], a[100:103], v[104:107], v[44:47]\n\t" "s_add_u32 s62, s62, s64\n\t" "s_add_u32 s62, s62, %[ob]\n\t" "s_waitcnt lgkmcnt(4)\n\t" \
    "v_mfma_f32_16x16x32_bf16 v[32:35], a[104:107], v[108:111], v[32:35]\n\t" "ds_read_b128 v[128:131], v75 offset:33536\n\t" \
    "v_mfma_f32_16x16x32_bf16 v[36:39], a[108:111], v[108:111], v[36:39]\n\t" "s_waitcnt lgkmcnt(4)\n\t" \
    "v_mfma_f32_16x16x32_bf16 v[40:43], a[104:107], v[112:115], v[40:43]\n\t" "ds_read_b128 v[100:103], v72 offset:1024\n\t" \
    "v_mfma_f32_16x16x32_bf16 v[44:47], a[108:111], v[112:115], v[44:47]\n\t" "s_waitcnt lgkmcnt(4)\n\t" \
    "v_mfma_f32_16x16x32_bf16 v[32:35], a[112:115], v[116:119], v[32:35]\n\t" "ds_read_b128 v[104:107], v72 offset:33792\n\t" \
    "v_mfma_f32_16x16x32_bf16 v[36:39], a[116:119], v[116:119], v[36:39]\n\t" "s_waitcnt lgkmcnt(4)\n\t" \
    "v_mfma_f32_16x16x32_bf16 v[40:43], a[112:115], v[120:123], v[40:43]\n\t" "ds_read_b128 v[108:111], v73 offset:1024\n\t" \
    "v_mfma_f32_16x16x32_bf16 v[44:47], a[116:119], v[120:123], v[44:47]\n\t" "s_waitcnt lgkmcnt(4)\n\t" \
    "v_mfma_f32_16x16x32_bf16 v[32:35], a[120:123], v[124:127], v[32:35]\n\t" "ds_read_b128 v[112:115], v73 offset:33792\n\t" \
    "v_mfma_f32_16x16x32_bf16 v[36:39], a[124:127], v[124:127], v[36:39]\n\t" "s_waitcnt lgkmcnt(4)\n\t" \
    "v_mfma_f32_16x16x32_bf16 v[40:43], a[120:123], v[128:131], v[40:43]\n\t" "ds_read_b128 v[116:119], v74 offset:1024\n\t" \
    "v_mfma_f32_16x16x32_bf16 v[44:47], a[124:127], v[128:131], v[44:47]\n\t" "s_waitcnt lgkmcnt(4)\n\t" \
    "v_mfma_f32_16x16x32_bf16 v[32:35], a[128:131], v[100:103], v[32:35]\n\t" "ds_read_b128 v[120:123], v74 offset:33792\n\t" \
    "v_mfma_f32_16x16x32_bf16 v[36:39], a[132:135], v[100:103], v[36:39]\n\t" "s_waitcnt lgkmcnt(4)\n\t" \
    "v_mfma_f32_16x16x32_bf16 v[40:43], a[128:131], v[104:107], v[40:43]\n\t" "ds_read_b128 v[124:127], v75 offset:1024\n\t" \
    "v_mfma_f32_16x16x32_bf16 v[44:47], a[132:135], v[104:107], v[44:47]\n\t" "s_waitcnt lgkmcnt(4)\n\t" \
    "v_mfma_f32_16x16x32_bf16 v[32:35], a[136:139], v[108:111], v[32:35]\n\t" "ds_read_b128 v[128:131], v75 offset:33792\n\t" "s_waitcnt vmcnt(16)\n\t" "s_barrier\n\t" \
    "v_mfma_f32_16x16x32_bf16 v[36:39], a[140:143], v[108:111], v[36:39]\n\t" "v_mov_b32 v98, s60\n\t" "buffer_store_dword v98, %[fo], %[rc], %[fl] offen sc1\n\t" \
    "s_waitcnt lgkmcnt(4)\n\t" "v_mfma_f32_16x16x32_bf16 v[40:43], a[136:139], v[112:115], v[40:43]\n\t" "ds_read_b128 v[100:103], v72 offset:1280\n\t" \
    "v_mfma_f32_16x16x32_bf16 v[44:47], a[140:143], v[112:115], v[44:47]\n\t" "s_waitcnt lgkmcnt(4)\n\t" \
    "v_mfma_f32_16x16x32_bf16 v[32:35], a[144:147], v[116:119], v[32:35]\n\t" "ds_read_b128 v[104:107], v72 offset:34048\n\t" \
    "v_mfma_f32_16x16x32_bf16 v[36:39], a[148:151], v[116:119], v[36:39]\n\t" "s_waitcnt lgkmcnt(4)\n\t" \
    "v_mfma_f32_16x16x32_bf16 v[40:43], a[144:147], v[120:123], v[40:43]\n\t" "ds_read_b128 v[108:111], v73 offset:1280\n\t" \
    "v_mfma_f32_16x16x32_bf16 v[44:47], a[148:151], v[120:123], v[44:47]\n\t" "s_waitcnt lgkmcnt(4)\n\t" \
    "v_mfma_f32_16x16x32_bf16 v[32:35], a[152:155], v[124:127], v[32:35]\n\t" "ds_read_b128 v[112:115], v73 offset:34048\n\t" \
    "v_mfma_f32_16x16x32_bf16 v[36:39], a[156:159], v[124:127], v[36:39]\n\t" "s_waitcnt lgkmcnt(4)\n\t" \
    "v_mfma_f32_16x16x32_bf16 v[40:43], a[152:155], v[128:131], v[40:43]\n\t" "ds_read_b128 v[116:119], v74 offset:1280\n\t" \
    "v_mfma_f32_16x16x32_bf16 v[44:47], a[156:159], v[128:131], v[44:47]\n\t" "s_waitcnt lgkmcnt(4)\n\t" \
    "v_mfma_f32_16x16x32_bf16 v[32:35], a[160:163], v[100:103], v[32:35]\n\t" "ds_read_b128 v[120:123], v74 offset:34048\n\t" \
    "v_mfma_f32_16x16x32_bf16 v[36:39], a[164:167], v[100:103], v[36:39]\n\t" "s_waitcnt lgkmcnt(4)\n\t" \
    "v_mfma_f32_16x16x32_bf16 v[40:43], a[160:163], v[104:107], v[40:43]\n\t" "ds_read_b128 v[124:127], v75 offset:1280\n\t" \
    "v_mfma_f32_16x16x32_bf16 v[44:47], a[164:167], v[104:107], v[44:47]\n\t" "s_waitcnt lgkmcnt(4)\n\t" \
    "v_mfma_f32_16x16x32_bf16 v[32:35], a[168:171], v[108:111], v[32:35]\n\t" "ds_read_b128 v[128:131], v75 offset:34048\n\t" \
    "v_mfma_f32_16x16x32_bf16 v[36:39], a[172:175], v[108:111], v[36:39]\n\t" "s_waitcnt lgkmcnt(4)\n\t" \
    "v_mfma_f32_16x16x32_bf16 v[40:43], a[168:171], v[112:115], v[40:43]\n\t" "ds_read_b128 v[100:103], v72 offset:1536\n\t" \
    "v_mfma_f32_16x16x32_bf16 v[44:47], a[172:175], v[112:115], v[44:47]\n\t" "s_waitcnt lgkmcnt(4)\n\t" \
    "v_mfma_f32_16x16x32_bf16 v[32:35], a[176:179], v[116:119], v[32:35]\n\t" "ds_read_b128 v[104:107], v72 offset:34304\n\t" \
    "v_mfma_f32_16x16x32_bf16 v[36:39], a[180:183], v[116:119], v[36:39]\n\t" "s_waitcnt lgkmcnt(4)\n\t" \
    "v_mfma_f32_16x16x32_bf16 v[40:43], a[176:179], v[120:123], v[40:43]\n\t" "ds_read_b128 v[108:111], v73 offset:1536\n\t" \
    "v_mfma_f32_16x16x32_bf16 v[44:47], a[180:183], v[120:123], v[44:47]\n\t" "s_waitcnt lgkmcnt(4)\n\t" \
    "v_mfma_f32_16x16x32_bf16 v[32:35], a[184:187], v[124:127], v[32:35]\n\t" "ds_read_b128 v[112:115], v73 offset:34304\n\t" \
    "v_mfma_f32_16x16x32_bf16 v[36:39], a[188:191], v[124:127], v[36:39]\n\t" "s_waitcnt lgkmcnt(4)\n\t" \
    "v_mfma_f32_16x16x32_bf16 v[40:43], a[184:187], v[128:131], v[40:43]\n\t" "ds_read_b128 v[116:119], v74 offset:1536\n\t" \
    "v_mfma_f32_16x16x32_bf16 v[44:47], a[188:191], v[128:131], v[44:47]\n\t" "s_waitcnt lgkmcnt(4)\n\t" \
    "v_mfma_f32_16x16x32_bf16 v[32:35], a[192:195], v[100:103], v[32:35]\n\t" "ds_read_b128 v[120:123], v74 offset:34304\n\t" \
    "v_mfma_f32_16x16x32_bf16 v[36:39], a[196:199], v[100:103], v[36:39]\n\t" "s_waitcnt lgkmcnt(4)\n\t" \
    "v_mfma_f32_16x16x32_bf16 v[40:43], a[192:195], v[104:107], v[40:43]\n\t" "ds_read_b128 v[124:127], v75 offset:1536\n\t" \
    "v_mfma_f32_16x16x32_bf16 v[44:47], a[196:199], v[104:107], v[44:47]\n\t" "s_waitcnt lgkmcnt(4)\n\t" \
    "v_mfma_f32_16x16x32_bf16 v[32:35], a[200:203], v[108:111], v[32:35]\n\t" "ds_read_b128 v[128:131], v75 offset:34304\n\t" \
    "v_mfma_f32_16x16x32_bf16 v[36:39], a[204:207], v[108:111], v[36:39]\n\t" "s_waitcnt lgkmcnt(4)\n\t" \
    "v_mfma_f32_16x16x32_bf16 v[40:43], a[200:203], v[112:115], v[40:43]\n\t" "ds_read_b128 v[100:103], v72 offset:1792\n\t" \
    "v_mfma_f32_16x16x32_bf16 v[44:47], a[204:207], v[112:115], v[44:47]\n\t" "s_waitcnt lgkmcnt(4)\n\t" \
    "v_mfma_f32_16x16x32_bf16 v[32:35], a[208:211], v[116:119], v[32:35]\n\t" "ds_read_b128 v[104:107], v72 offset:34560\n\t" \
    "v_mfma_f32_16x16x32_bf16 v[36:39], a[212:215], v[116:119], v[36:39]\n\t" "s_waitcnt lgkmcnt(4)\n\t" \
    "v_mfma_f32_16x16x32_bf16 v[40:43], a[208:211], v[120:123], v[40:43]\n\t" "ds_read_b128 v[108:111], v73 offset:1792\n\t" \
    "v_mfma_f32_16x16x32_bf16 v[44:47], a[212:215], v[120:123], v[44:47]\n\t" "s_waitcnt lgkmcnt(4)\n\t" \
    "v_mfma_f32_16x16x32_bf16 v[32:35], a[216:219], v[124:127], v[32:35]\n\t" "ds_read_b128 v[112:115], v73 offset:34560\n\t" \
    "v_mfma_f32_16x16x32_bf16 v[36:39], a[220:223], v[124:127], v[36:39]\n\t" "s_waitcnt lgkmcnt(4)\n\t" \
    "v_mfma_f32_16x16x32_bf16 v[40:43], a[216:219], v[128:131], v[40:43]\n\t" "ds_read_b128 v[116:119], v74 offset:1792\n\t" \
    "v_mfma_f32_16x16x32_bf16 v[44:47], a[220:223], v[128:131], v[44:47]\n\t" "s_waitcnt lgkmcnt(4)\n\t" \
    "v_mfma_f32_16x16x32_bf16 v[32:35], a[224:227], v[100:103], v[32:35]\n\t" "ds_read_b128 v[120:123], v74 offset:34560\n\t" \
    "v_mfma_f32_16x16x32_bf16 v[36:39], a[228:231], v[100:103], v[36:39]\n\t" "s_waitcnt lgkmcnt(4)\n\t" \
    "v_mfma_f32_16x16x32_bf16 v[40:43], a[224:227], v[104:107], v[40:43]\n\t" "ds_read_b128 v[124:127], v75 offset:1792\n\t" \
    "v_mfma_f32_16x16x32_bf16 v[44:47], a[228:231], v[104:107], v[44:47]\n\t" "s_waitcnt lgkmcnt(4)\n\t" \
    "v_mfma_f32_16x16x32_bf16 v[32:35], a[232:235], v[108:111], v[32:35]\n\t" "ds_read_b128 v[128:131], v75 offset:34560\n\t" \
    "v_mfma_f32_16x16x32_bf16 v[36:39], a[236:239], v[108:111], v[36:39]\n\t" "s_waitcnt lgkmcnt(4)\n\t" \
    "v_mfma_f32_16x16x32_bf16 v[40:43], a[232:235], v[112:115], v[40:43]\n\t" "v_mfma_f32_16x16x32_bf16 v[44:47], a[236:239], v[112:115], v[44:47]\n\t" \
    "s_waitcnt lgkmcnt(3)\n\t" "v_mfma_f32_16x16x32_bf16 v[32:35], a[240:243], v[116:119], v[32:35]\n\t" \
    "v_mfma_f32_16x16x32_bf16 v[36:39], a[244:247], v[116:119], v[36:39]\n\t" "s_waitcnt vmcnt(1) lgkmcnt(0)\n\t" "s_barrier\n\t" XT(82, 83) \
    "v_mfma_f32_16x16x32_bf16 v[40:43], a[240:243], v[120:123], v[40:43]\n\t" "ds_read_b128 v[100:103], v76 offset:0\n\t" \
    "v_mfma_f32_16x16x32_bf16 v[44:47], a[244:247], v[120:123], v[44:47]\n\t" "v_mfma_f32_16x16x32_bf16 v[32:35], a[248:251], v[124:127], v[32:35]\n\t" \
    "ds_read_b128 v[104:107], v76 offset:32768\n\t" "v_mfma_f32_16x16x32_bf16 v[36:39], a[252:255], v[124:127], v[36:39]\n\t" "ds_read_b128 v[108:111], v77 offset:0\n\t" \
    "v_mfma_f32_16x16x32_bf16 v[40:43], a[248:251], v[128:131], v[40:43]\n\t" "ds_read_b128 v[112:115], v77 offset:32768\n\t" "ds_read_b128 v[116:119], v78 offset:0\n\t" \
    "v_mfma_f32_16x16x32_bf16 v[44:47], a[252:255], v[128:131], v[44:47]\n\t" "s_add_u32 s60, s60, 1\n\t" GSUM "s_cmp_eq_u32 s60, 1\n\t" "s_cbranch_scc1 20f\n\t" "11:\n\t" \
    XT(80, 81) "s_waitcnt lgkmcnt(4)\n\t" "v_mfma_f32_16x16x32_bf16 v[48:51], a[0:3], v[100:103], 0\n\t" "ds_read_b128 v[120:123], v78 offset:32768\n\t" \
    "s_add_u32 s61, s60, 1\n\t" "s_and_b32 s61, s61, 63\n\t" "v_mfma_f32_16x16x32_bf16 v[52:55], a[4:7], v[100:103], 0\n\t" "s_lshl_b32 s61, s61, 16\n\t" \
    "s_add_u32 s63, %[lw], 0\n\t" "s_waitcnt lgkmcnt(4)\n\t" "v_mfma_f32_16x16x32_bf16 v[56:59], a[0:3], v[104:107], 0\n\t" "ds_read_b128 v[124:127], v79 offset:0\n\t" \
    "v_mfma_f32_16x16x32_bf16 v[60:63], a[4:7], v[104:107], 0\n\t" "s_waitcnt lgkmcnt(4)\n\t" "v_mfma_f32_16x16x32_bf16 v[48:51], a[8:11], v[108:111], v[48:51]\n\t" \
    "ds_read_b128 v[128:131], v79 offset:32768\n\t" "v_cvt_pk_bf16_f32 v64, v32, v33\n\t" "v_mfma_f32_16x16x32_bf16 v[52:55], a[12:15], v[108:111], v[52:55]\n\t" \
    "v_cvt_pk_bf16_f32 v65, v34, v35\n\t" "v_cvt_pk_bf16_f32 v66, v36, v37\n\t" "s_waitcnt lgkmcnt(4)\n\t" \
    "v_mfma_f32_16x16x32_bf16 v[56:59], a[8:11], v[112:115], v[56:59]\n\t" "ds_read_b128 v[100:103], v76 offset:256\n\t" "v_cvt_pk_bf16_f32 v67, v38, v39\n\t" \
    "v_mfma_f32_16x16x32_bf16 v[60:63], a[12:15], v[112:115], v[60:63]\n\t" "v_cvt_pk_bf16_f32 v68, v40, v41\n\t" "v_cvt_pk_bf16_f32 v69, v42, v43\n\t" \
    "s_waitcnt lgkmcnt(4)\n\t" "v_mfma_f32_16x16x32_bf16 v[48:51], a[16:19], v[116:119], v[48:51]\n\t" "ds_read_b128 v[104:107], v76 offset:33024\n\t" \
    "v_cvt_pk_bf16_f32 v70, v44, v45\n\t" "v_mfma_f32_16x16x32_bf16 v[52:55], a[20:23], v[116:119], v[52:55]\n\t" "v_cvt_pk_bf16_f32 v71, v46, v47\n\t" \
    "buffer_store_dwordx4 v[64:67], v96, %[rs], s62 offen offset:0 sc1\n\t" "s_waitcnt lgkmcnt(4)\n\t" "v_mfma_f32_16x16x32_bf16 v[56:59], a[16:19], v[120:123], v[56:59]\n\t" \
    "ds_read_b128 v[108:111], v77 offset:256\n\t" "v_mfma_f32_16x16x32_bf16 v[60:63], a[20:23], v[120:123], v[60:63]\n\t" "s_waitcnt lgkmcnt(4)\n\t" \
    "v_mfma_f32_16x16x32_bf16 v[48:51], a[24:27], v[124:127], v[48:51]\n\t" "ds_read_b128 v[112:115], v77 offset:33024\n\t" \
    "v_mfma_f32_16x16x32_bf16 v[52:55], a[28:31], v[124:127], v[52:55]\n\t" "buffer_store_dwordx4 v[68:71], v96, %[rs], s62 offen offset:256 sc1\n\t" \
    "s_waitcnt lgkmcnt(4)\n\t" "v_mfma_f32_16x16x32_bf16 v[56:59], a[24:27], v[128:131], v[56:59]\n\t" "ds_read_b128 v[116:119], v78 offset:256\n\t" "s_mov_b32 m0, s63\n\t" \
    "v_mfma_f32_16x16x32_bf16 v[60:63], a[28:31], v[128:131], v[60:63]\n\t" "s_add_u32 s63, s63, 0x800\n\t" "buffer_load_dwordx4 v88, %[rx], s61 offen lds\n\t" \
    "s_waitcnt lgkmcnt(4)\n\t" "v_mfma_f32_16x16x32_bf16 v[48:51], a[32:35], v[100:103], v[48:51]\n\t" "ds_read_b128 v[120:123], v78 offset:33024\n\t" \
    "v_mfma_f32_16x16x32_bf16 v[52:55], a[36:39], v[100:103], v[52:55]\n\t" "buffer_load_dwordx4 v88, %[rx], s61 offen offset:1024 lds\n\t" "s_waitcnt lgkmcnt(4)\n\t" \
    "v_mfma_f32_16x16x32_bf16 v[56:59], a[32:35], v[104:107], v[56:59]\n\t" "ds_read_b128 v[124:127], v79 offset:256\n\t" "s_mov_b32 m0, s63\n\t" \
    "v_mfma_f32_16x16x32_bf16 v[60:63], a[36:39], v[104:107], v[60:63]\n\t" "s_add_u32 s63, s63, 0x800\n\t" "buffer_load_dwordx4 v89, %[rx], s61 offen lds\n\t" \
    "s_waitcnt lgkmcnt(4)\n\t" "v_mfma_f32_16x16x32_bf16 v[48:51], a[40:43], v[108:111], v[48:51]\n\t" "ds_read_b128 v[128:131], v79 offset:33024\n\t" \
    "v_mfma_f32_16x16x32_bf16 v[52:55], a[44:47], v[108:111], v[52:55]\n\t" "buffer_load_dwordx4 v89, %[rx], s61 offen offset:1024 lds\n\t" "s_waitcnt lgkmcnt(4)\n\t" \
    "v_mfma_f32_16x16x32_bf16 v[56:59], a[40:43], v[112:115], v[56:59]\n\t" "ds_read_b128 v[100:103], v76 offset:512\n\t" "s_mov_b32 m0, s63\n\t" \
    "v_mfma_f32_16x16x32_bf16 v[60:63], a[44:47], v[112:115], v[60:63]\n\t" "s_add_u32 s63, s63, 0x800\n\t" "buffer_load_dwordx4 v90, %[rx], s61 offen lds\n\t" \
    "s_waitcnt lgkmcnt(4)\n\t" "v_mfma_f32_16x16x32_bf16 v[48:51], a[48:51], v[116:119], v[48:51]\n\t" "ds_read_b128 v[104:107], v76 offset:33280\n\t" \
    "v_mfma_f32_16x16x32_bf16 v[52:55], a[52:55], v[116:119], v[52:55]\n\t" "buffer_load_dwordx4 v90, %[rx], s61 offen offset:1024 lds\n\t" "s_waitcnt lgkmcnt(4)\n\t" \
    "v_mfma_f32_16x16x32_bf16 v[56:59], a[48:51], v[120:123], v[56:59]\n\t" "ds_read_b128 v[108:111], v77 offset:512\n\t" "s_mov_b32 m0, s63\n\t" \
    "v_mfma_f32_16x16x32_bf16 v[60:63], a[52:55], v[120:123], v[60:63]\n\t" "s_add_u32 s63, s63, 0x800\n\t" "buffer_load_dwordx4 v91, %[rx], s61 offen lds\n\t" \
    "s_waitcnt lgkmcnt(4)\n\t" "v_mfma_f32_16x16x32_bf16 v[48:51], a[56:59], v[124:127], v[48:51]\n\t" "ds_read_b128 v[112:115], v77 offset:33280\n\t" \
    "v_mfma_f32_16x16x32_bf16 v[52:55], a[60:63], v[124:127], v[52:55]\n\t" "buffer_load_dwordx4 v91, %[rx], s61 offen offset:1024 lds\n\t" "s_waitcnt lgkmcnt(4)\n\t" \
    "v_mfma_f32_16x16x32_bf16 v[56:59], a[56:59], v[128:131], v[56:59]\n\t" "ds_read_b128 v[116:119], v78 offset:512\n\t" "s_mov_b32 m0, s63\n\t" \
    "v_mfma_f32_16x16x32_bf16 v[60:63], a[60:63], v[128:131], v[60:63]\n\t" "s_add_u32 s63, s63, 0x800\n\t" "buffer_load_dwordx4 v92, %[rx], s61 offen lds\n\t" \
    "s_waitcnt lgkmcnt(4)\n\t" "v_mfma_f32_16x16x32_bf16 v[48:51], a[64:67], v[100:103], v[48:51]\n\t" "ds_read_b128 v[120:123], v78 offset:33280\n\t" \
    "v_mfma_f32_16x16x32_bf16 v[52:55], a[68:71], v[100:103], v[52:55]\n\t" "buffer_load_dwordx4 v92, %[rx], s61 offen offset:1024 lds\n\t" "s_waitcnt lgkmcnt(4)\n\t" \
    "v_mfma_f32_16x16x32_bf16 v[56:59], a[64:67], v[104:107], v[56:59]\n\t" "ds_read_b128 v[124:127], v79 offset:512\n\t" "s_mov_b32 m0, s63\n\t" \
    "v_mfma_f32_16x16x32_bf16 v[60:63], a[68:71], v[104:107], v[60:63]\n\t" "s_add_u32 s63, s63, 0x800\n\t" "buffer_load_dwordx4 v93, %[rx], s61 offen lds\n\t" \
    "s_waitcnt lgkmcnt(4)\n\t" "v_mfma_f32_16x16x32_bf16 v[48:51], a[72:75], v[108:111], v[48:51]\n\t" "ds_read_b128 v[128:131], v79 offset:33280\n\t" \
    "v_mfma_f32_16x16x32_bf16 v[52:55], a[76:79], v[108:111], v[52:55]\n\t" "buffer_load_dwordx4 v93, %[rx], s61 offen offset:1024 lds\n\t" "s_waitcnt lgkmcnt(4)\n\t" \
    "v_mfma_f32_16x16x32_bf16 v[56:59], a[72:75], v[112:115], v[56:59]\n\t" "ds_read_b128 v[100:103], v76 offset:768\n\t" "s_mov_b32 m0, s63\n\t" \
    "v_mfma_f32_16x16x32_bf16 v[60:63], a[76:79], v[112:115], v[60:63]\n\t" "s_add_u32 s63, s63, 0x800\n\t" "buffer_load_dwordx4 v94, %[rx], s61 offen lds\n\t" \
    "s_waitcnt lgkmcnt(4)\n\t" "v_mfma_f32_16x16x32_bf16 v[48:51], a[80:83], v[116:119], v[48:51]\n\t" "ds_read_b128 v[104:107], v76 offset:33536\n\t" \
    "v_mfma_f32_16x16x32_bf16 v[52:55], a[84:87], v[116:119], v[52:55]\n\t" "buffer_load_dwordx4 v94, %[rx], s61 offen offset:1024 lds\n\t" "s_waitcnt lgkmcnt(4)\n\t" \
    "v_mfma_f32_16x16x32_bf16 v[56:59], a[80:83], v[120:123], v[56:59]\n\t" "ds_read_b128 v[108:111], v77 offset:768\n\t" "s_mov_b32 m0, s63\n\t" \
    "v_mfma_f32_16x16x32_bf16 v[60:63], a[84:87], v[120:123], v[60:63]\n\t" "s_add_u32 s63, s63, 0x800\n\t" "buffer_load_dwordx4 v95, %[rx], s61 offen lds\n\t" \
    "s_waitcnt lgkmcnt(4)\n\t" "v_mfma_f32_16x16x32_bf16 v[48:51], a[88:91], v[124:127], v[48:51]\n\t" "ds_read_b128 v[112:115], v77 offset:33536\n\t" \
    "v_mfma_f32_16x16x32_bf16 v[52:55], a[92:95], v[124:127], v[52:55]\n\t" "buffer_load_dwordx4 v95, %[rx], s61 offen offset:1024 lds\n\t" "s_waitcnt lgkmcnt(4)\n\t" \
    "v_mfma_f32_16x16x32_bf16 v[56:59], a[88:91], v[128:131], v[56:59]\n\t" "ds_read_b128 v[116:119], v78 offset:768\n\t" \
    "v_mfma_f32_16x16x32_bf16 v[60:63], a[92:95], v[128:131], v[60:63]\n\t" "s_waitcnt lgkmcnt(4)\n\t" "v_mfma_f32_16x16x32_bf16 v[48:51], a[96:99], v[100:103], v[48:51]\n\t" \
    "ds_read_b128 v[120:123], v78 offset:33536\n\t" "s_lshr_b32 s64, s60, 2\n\t" "s_xor_b32 s64, s64, %[xp]\n\t" \
    "v_mfma_f32_16x16x32_bf16 v[52:55], a[100:103], v[100:103], v[52:55]\n\t" "s_and_b32 s64, s64, 1\n\t" "s_lshl_b32 s64, s64, 13\n\t" "s_waitcnt lgkmcnt(4)\n\t" \
    "v_mfma_f32_16x16x32_bf16 v[56:59], a[96:99], v[104:107], v[56:59]\n\t" "ds_read_b128 v[124:127], v79 offset:768\n\t" "s_lshl_b32 s62, s60, 19\n\t" \
    "v_mfma_f32_16x16x32_bf16 v[60:63], a[100:103], v[104:107], v[60:63]\n\t" "s_add_u32 s62, s62, s64\n\t" "s_add_u32 s62, s62, %[ob]\n\t" "s_waitcnt lgkmcnt(4)\n\t" \
    "v_mfma_f32_16x16x32_bf16 v[48:51], a[104:107], v[108:111], v[48:51]\n\t" "ds_read_b128 v[128:131], v79 offset:33536\n\t" \
    "v_mfma_f32_16x16x32_bf16 v[52:55], a[108:111], v[108:111], v[52:55]\n\t" "s_waitcnt lgkmcnt(4)\n\t" \
    "v_mfma_f32_16x16x32_bf16 v[56:59], a[104:107], v[112:115], v[56:59]\n\t" "ds_read_b128 v[100:103], v76 offset:1024\n\t" \
    "v_mfma_f32_16x16x32_bf16 v[60:63], a[108:111], v[112:115], v[60:63]\n\t" "s_waitcnt lgkmcnt(4)\n\t" \
    "v_mfma_f32_16x16x32_bf16 v[48:51], a[112:115], v[116:119], v[48:51]\n\t" "ds_read_b128 v[104:107], v76 offset:33792\n\t" \
    "v_mfma_f32_16x16x32_bf16 v[52:55], a[116:119], v[116:119], v[52:55]\n\t" "s_waitcnt lgkmcnt(4)\n\t" \
    "v_mfma_f32_16x16x32_bf16 v[56:59], a[112:115], v[120:123], v[56:59]\n\t" "ds_read_b128 v[108:111], v77 offset:1024\n\t" \
    "v_mfma_f32_16x16x32_bf16 v[60:63], a[116:119], v[120:123], v[60:63]\n\t" "s_waitcnt lgkmcnt(4)\n\t" \
    "v_mfma_f32_16x16x32_bf16 v[48:51], a[120:123], v[124:127], v[48:51]\n\t" "ds_read_b128 v[112:115], v77 offset:33792\n\t" \
    "v_mfma_f32_16x16x32_bf16 v[52:55], a[124:127], v[124:127], v[52:55]\n\t" "s_waitcnt lgkmcnt(4)\n\t" \
    "v_mfma_f32_16x16x32_bf16 v[56:59], a[120:123], v[128:131], v[56:59]\n\t" "ds_read_b128 v[116:119], v78 offset:1024\n\t" \
    "v_mfma_f32_16x16x32_bf16 v[60:63], a[124:127], v[128:131], v[60:63]\n\t" "s_waitcnt lgkmcnt(4)\n\t" \
    "v_mfma_f32_16x16x32_bf16 v[48:51], a[128:131], v[100:103], v[48:51]\n\t" "ds_read_b128 v[120:123], v78 offset:33792\n\t" \
    "v_mfma_f32_16x16x32_bf16 v[52:55], a[132:135], v[100:103], v[52:55]\n\t" "s_waitcnt lgkmcnt(4)\n\t" \
    "v_mfma_f32_16x16x32_bf16 v[56:59], a[128:131], v[104:107], v[56:59]\n\t" "ds_read_b128 v[124:127], v79 offset:1024\n\t" \
    "v_mfma_f32_16x16x32_bf16 v[60:63], a[132:135], v[104:107], v[60:63]\n\t" "s_waitcnt lgkmcnt(4)\n\t" \
    "v_mfma_f32_16x16x32_bf16 v[48:51], a[136:139], v[108:111], v[48:51]\n\t" "ds_read_b128 v[128:131], v79 offset:33792\n\t" "s_waitcnt vmcnt(16)\n\t" "s_barrier\n\t" \
    "v_mfma_f32_16x16x32_bf16 v[52:55], a[140:143], v[108:111], v[52:55]\n\t" "v_mov_b32 v98, s60\n\t" "buffer_store_dword v98, %[fo], %[rc], %[fl] offen sc1\n\t" \
    "s_waitcnt lgkmcnt(4)\n\t" "v_mfma_f32_16x16x32_bf16 v[56:59], a[136:139], v[112:115], v[56:59]\n\t" "ds_read_b128 v[100:103], v76 offset:1280\n\t" \
    "v_mfma_f32_16x16x32_bf16 v[60:63], a[140:143], v[112:115], v[60:63]\n\t" "s_waitcnt lgkmcnt(4)\n\t" \
    "v_mfma_f32_16x16x32_bf16 v[48:51], a[144:147], v[116:119], v[48:51]\n\t" "ds_read_b128 v[104:107], v76 offset:34048\n\t" \
    "v_mfma_f32_16x16x32_bf16 v[52:55], a[148:151], v[116:119], v[52:55]\n\t" "s_waitcnt lgkmcnt(4)\n\t" \
    "v_mfma_f32_16x16x32_bf16 v[56:59], a[144:147], v[120:123], v[56:59]\n\t" "ds_read_b128 v[108:111], v77 offset:1280\n\t" \
    "v_mfma_f32_16x16x32_bf16 v[60:63], a[148:151], v[120:123], v[60:63]\n\t" "s_waitcnt lgkmcnt(4)\n\t" \
    "v_mfma_f32_16x16x32_bf16 v[48:51], a[152:155], v[124:127], v[48:51]\n\t" "ds_read_b128 v[112:115], v77 offset:34048\n\t" \
    "v_mfma_f32_16x16x32_bf16 v[52:55], a[156:159], v[124:127], v[52:55]\n\t" "s_waitcnt lgkmcnt(4)\n\t" \
    "v_mfma_f32_16x16x32_bf16 v[56:59], a[152:155], v[128:131], v[56:59]\n\t" "ds_read_b128 v[116:119], v78 offset:1280\n\t" \
    "v_mfma_f32_16x16x32_bf16 v[60:63], a[156:159], v[128:131], v[60:63]\n\t" "s_waitcnt lgkmcnt(4)\n\t" \
    "v_mfma_f32_16x16x32_bf16 v[48:51], a[160:163], v[100:103], v[48:51]\n\t" "ds_read_b128 v[120:123], v78 offset:34048\n\t" \
    "v_mfma_f32_16x16x32_bf16 v[52:55], a[164:167], v[100:103], v[52:55]\n\t" "s_waitcnt lgkmcnt(4)\n\t" \
    "v_mfma_f32_16x16x32_bf16 v[56:59], a[160:163], v[104:107], v[56:59]\n\t" "ds_read_b128 v[124:127], v79 offset:1280\n\t" \
    "v_mfma_f32_16x16x32_bf16 v[60:63], a[164:167], v[104:107], v[60:63]\n\t" "s_waitcnt lgkmcnt(4)\n\t" \
    "v_mfma_f32_16x16x32_bf16 v[48:51], a[168:171], v[108:111], v[48:51]\n\t" "ds_read_b128 v[128:131], v79 offset:34048\n\t" \
    "v_mfma_f32_16x16x32_bf16 v[52:55], a[172:175], v[108:111], v[52:55]\n\t" "s_waitcnt lgkmcnt(4)\n\t" \
    "v_mfma_f32_16x16x32_bf16 v[56:59], a[168:171], v[112:115], v[56:59]\n\t" "ds_read_b128 v[100:103], v76 offset:1536\n\t" \
    "v_mfma_f32_16x16x32_bf16 v[60:63], a[172:175], v[112:115], v[60:63]\n\t" "s_waitcnt lgkmcnt(4)\n\t" \
    "v_mfma_f32_16x16x32_bf16 v[48:51], a[176:179], v[116:119], v[48:51]\n\t" "ds_read_b128 v[104:107], v76 offset:34304\n\t" \
    "v_mfma_f32_16x16x32_bf16 v[52:55], a[180:183], v[116:119], v[52:55]\n\t" "s_waitcnt lgkmcnt(4)\n\t" \
    "v_mfma_f32_16x16x32_bf16 v[56:59], a[176:179], v[120:123], v[56:59]\n\t" "ds_read_b128 v[108:111], v77 offset:1536\n\t" \
    "v_mfma_f32_16x16x32_bf16 v[60:63], a[180:183], v[120:123], v[60:63]\n\t" "s_waitcnt lgkmcnt(4)\n\t" \
    "v_mfma_f32_16x16x32_bf16 v[48:51], a[184:187], v[124:127], v[48:51]\n\t" "ds_read_b128 v[112:115], v77 offset:34304\n\t" \
    "v_mfma_f32_16x16x32_bf16 v[52:55], a[188:191], v[124:127], v[52:55]\n\t" "s_waitcnt lgkmcnt(4)\n\t" \
    "v_mfma_f32_16x16x32_bf16 v[56:59], a[184:187], v[128:131], v[56:59]\n\t" "ds_read_b128 v[116:119], v78 offset:1536\n\t" \
    "v_mfma_f32_16x16x32_bf16 v[60:63], a[188:191], v[128:131], v[60:63]\n\t" "s_waitcnt lgkmcnt(4)\n\t" \
    "v_mfma_f32_16x16x32_bf16 v[48:51], a[192:195], v[100:103], v[48:51]\n\t" "ds_read_b128 v[120:123], v78 offset:34304\n\t" \
    "v_mfma_f32_16x16x32_bf16 v[52:55], a[196:199], v[100:103], v[52:55]\n\t" "s_waitcnt lgkmcnt(4)\n\t" \
    "v_mfma_f32_16x16x32_bf16 v[56:59], a[192:195], v[104:107], v[56:59]\n\t" "ds_read_b128 v[124:127], v79 offset:1536\n\t" \
    "v_mfma_f32_16x16x32_bf16 v[60:63], a[196:199], v[104:107], v[60:63]\n\t" "s_waitcnt lgkmcnt(4)\n\t" \
    "v_mfma_f32_16x16x32_bf16 v[48:51], a[200:203], v[108:111], v[48:51]\n\t" "ds_read_b128 v[128:131], v79 offset:34304\n\t" \
    "v_mfma_f32_16x16x32_bf16 v[52:55], a[204:207], v[108:111], v[52:55]\n\t" "s_waitcnt lgkmcnt(4)\n\t" \
    "v_mfma_f32_16x16x32_bf16 v[56:59], a[200:203], v[112:115], v[56:59]\n\t" "ds_read_b128 v[100:103], v76 offset:1792\n\t" \
    "v_mfma_f32_16x16x32_bf16 v[60:63], a[204:207], v[112:115], v[60:63]\n\t" "s_waitcnt lgkmcnt(4)\n\t" \
    "v_mfma_f32_16x16x32_bf16 v[48:51], a[208:211], v[116:119], v[48:51]\n\t" "ds_read_b128 v[104:107], v76 offset:34560\n\t" \
    "v_mfma_f32_16x16x32_bf16 v[52:55], a[212:215], v[116:119], v[52:55]\n\t" "s_waitcnt lgkmcnt(4)\n\t" \
    "v_mfma_f32_16x16x32_bf16 v[56:59], a[208:211], v[120:123], v[56:59]\n\t" "ds_read_b128 v[108:111], v77 offset:1792\n\t" \
    "v_mfma_f32_16x16x32_bf16 v[60:63], a[212:215], v[120:123], v[60:63]\n\t" "s_waitcnt lgkmcnt(4)\n\t" \
    "v_mfma_f32_16x16x32_bf16 v[48:51], a[216:219], v[124:127], v[48:51]\n\t" "ds_read_b128 v[112:115], v77 offset:34560\n\t" \
    "v_mfma_f32_16x16x32_bf16 v[52:55], a[220:223], v[124:127], v[52:55]\n\t" "s_waitcnt lgkmcnt(4)\n\t" \
    "v_mfma_f32_16x16x32_bf16 v[56:59], a[216:219], v[128:131], v[56:59]\n\t" "ds_read_b128 v[116:119], v78 offset:1792\n\t" \
    "v_mfma_f32_16x16x32_bf16 v[60:63], a[220:223], v[128:131], v[60:63]\n\t" "s_waitcnt lgkmcnt(4)\n\t" \
    "v_mfma_f32_16x16x32_bf16 v[48:51], a[224:227], v[100:103], v[48:51]\n\t" "ds_read_b128 v[120:123], v78 offset:34560\n\t" \
    "v_mfma_f32_16x16x32_bf16 v[52:55], a[228:231], v[100:103], v[52:55]\n\t" "s_waitcnt lgkmcnt(4)\n\t" \
    "v_mfma_f32_16x16x32_bf16 v[56:59], a[224:227], v[104:107], v[56:59]\n\t" "ds_read_b128 v[124:127], v79 offset:1792\n\t" \
    "v_mfma_f32_16x16x32_bf16 v[60:63], a[228:231], v[104:107], v[60:63]\n\t" "s_waitcnt lgkmcnt(4)\n\t" \
    "v_mfma_f32_16x16x32_bf16 v[48:51], a[232:235], v[108:111], v[48:51]\n\t" "ds_read_b128 v[128:131], v79 offset:34560\n\t" \
    "v_mfma_f32_16x16x32_bf16 v[52:55], a[236:239], v[108:111], v[52:55]\n\t" "s_waitcnt lgkmcnt(4)\n\t" \
    "v_mfma_f32_16x16x32_bf16 v[56:59], a[232:235], v[112:115], v[56:59]\n\t" "v_mfma_f32_16x16x32_bf16 v[60:63], a[236:239], v[112:115], v[60:63]\n\t" \
    "s_waitcnt lgkmcnt(3)\n\t" "v_mfma_f32_16x16x32_bf16 v[48:51], a[240:243], v[116:119], v[48:51]\n\t" \
    "v_mfma_f32_16x16x32_bf16 v[52:55], a[244:247], v[116:119], v[52:55]\n\t" "s_waitcnt vmcnt(1) lgkmcnt(0)\n\t" "s_barrier\n\t" XT(82, 83) \
    "v_mfma_f32_16x16x32_bf16 v[56:59], a[240:243], v[120:123], v[56:59]\n\t" "ds_read_b128 v[100:103], v72 offset:0\n\t" \
    "v_mfma_f32_16x16x32_bf16 v[60:63], a[244:247], v[120:123], v[60:63]\n\t" "v_mfma_f32_16x16x32_bf16 v[48:51], a[248:251], v[124:127], v[48:51]\n\t" \
    "ds_read_b128 v[104:107], v72 offset:32768\n\t" "v_mfma_f32_16x16x32_bf16 v[52:55], a[252:255], v[124:127], v[52:55]\n\t" "ds_read_b128 v[108:111], v73 offset:0\n\t" \
    "v_mfma_f32_16x16x32_bf16 v[56:59], a[248:251], v[128:131], v[56:59]\n\t" "ds_read_b128 v[112:115], v73 offset:32768\n\t" "ds_read_b128 v[116:119], v74 offset:0\n\t" \
    "v_mfma_f32_16x16x32_bf16 v[60:63], a[252:255], v[128:131], v[60:63]\n\t" "s_add_u32 s60, s60, 1\n\t" GSUM "s_cmp_lt_u32 s60, 64\n\t" "s_cbranch_scc1 10b\n\t" \
    "s_nop 7\n\t" "v_cvt_pk_bf16_f32 v64, v48, v49\n\t" "v_cvt_pk_bf16_f32 v65, v50, v51\n\t" "v_cvt_pk_bf16_f32 v66, v52, v53\n\t" "v_cvt_pk_bf16_f32 v67, v54, v55\n\t" \
    "v_cvt_pk_bf16_f32 v68, v56, v57\n\t" "v_cvt_pk_bf16_f32 v69, v58, v59\n\t" "v_cvt_pk_bf16_f32 v70, v60, v61\n\t" "v_cvt_pk_bf16_f32 v71, v62, v63\n\t" \
    "buffer_store_dwordx4 v[64:67], v96, %[rs], s62 offen offset:0 sc1\n\t" "buffer_store_dwordx4 v[68:71], v96, %[rs], s62 offen offset:256 sc1\n\t" "s_waitcnt vmcnt(0)\n\t" \
    "s_barrier\n\t" "v_mov_b32 v98, s60\n\t" "buffer_store_dword v98, %[fo], %[rc], %[fl] offen sc1\n\t" "s_waitcnt lgkmcnt(0)\n\t" "s_branch 30f\n\t" "20:\n\t" \
    "v_mov_b32 v96, %[so]\n\t" "s_nop 7\n\t" "v_cvt_pk_bf16_f32 v64, v32, v33\n\t" "v_cvt_pk_bf16_f32 v65, v34, v35\n\t" "v_cvt_pk_bf16_f32 v66, v36, v37\n\t" \
    "v_cvt_pk_bf16_f32 v67, v38, v39\n\t" "v_cvt_pk_bf16_f32 v68, v40, v41\n\t" "v_cvt_pk_bf16_f32 v69, v42, v43\n\t" "v_cvt_pk_bf16_f32 v70, v44, v45\n\t" \
    "v_cvt_pk_bf16_f32 v71, v46, v47\n\t" "buffer_store_dwordx4 v[64:67], v96, %[rs], s62 offen offset:0 sc1\n\t" \
    "buffer_store_dwordx4 v[68:71], v96, %[rs], s62 offen offset:256 sc1\n\t" "s_waitcnt vmcnt(0)\n\t" "s_barrier\n\t" "v_mov_b32 v98, s60\n\t" \
    "buffer_store_dword v98, %[fo], %[rc], %[fl] offen sc1\n\t" "s_branch 11b\n\t" "30:\n\t"
#define ZC_LOOP \
    "s_mov_b32 s60, 0\n\t" "s_mov_b32 s61, 0\n\t" "s_mov_b32 s62, 0\n\t" "s_mov_b32 s63, 0x7f000000\n\t" "s_cmp_ge_u32 s61, %[z0]\n\t" "s_cbranch_scc1 52f\n\t" "51:\n\t" \
    "buffer_load_dword v120, %[fo], %[rc], %[fv] offen sc1\n\t" "buffer_load_dword v121, %[fo], %[rc], %[vd] offen sc1\n\t" "s_waitcnt vmcnt(0)\n\t" \
    "v_readfirstlane_b32 s61, v120\n\t" "v_readfirstlane_b32 s66, v121\n\t" "s_nop 3\n\t" "s_cmp_ge_u32 s61, %[z0]\n\t" "s_cbranch_scc1 52f\n\t" "s_bitcmp1_b32 s66, 1\n\t" \
    "s_cbranch_scc1 90f\n\t" "s_cmp_eq_u32 %[bd], 0\n\t" "s_cbranch_scc1 52f\n\t" "s_sub_u32 %[bd], %[bd], 1\n\t" "s_sleep 2\n\t" "s_branch 51b\n\t" "52:\n\t" \
    "s_lshr_b32 s66, s60, 2\n\t" "s_xor_b32 s66, s66, %[xp]\n\t" "s_and_b32 s66, s66, 1\n\t" "s_lshl_b32 s66, s66, 13\n\t" "s_lshl_b32 s64, s60, 19\n\t" \
    "s_add_u32 s64, s64, s66\n\t" "s_add_u32 s64, s64, %[vb]\n\t" "buffer_load_dwordx4 v[16:19], %[ao], %[rs], s64 offen offset:0 sc1\n\t" \
    "buffer_load_dwordx4 v[20:23], %[ao], %[rs], s64 offen offset:256 sc1\n\t" "buffer_load_dwordx4 v[24:27], %[ao], %[rs], s64 offen offset:2048 sc1\n\t" \
    "buffer_load_dwordx4 v[28:31], %[ao], %[rs], s64 offen offset:2304 sc1\n\t" "buffer_load_dwordx4 v[32:35], %[a1], %[rs], s64 offen offset:0 sc1\n\t" \
    "buffer_load_dwordx4 v[36:39], %[a1], %[rs], s64 offen offset:256 sc1\n\t" "buffer_load_dwordx4 v[40:43], %[a1], %[rs], s64 offen offset:2048 sc1\n\t" \
    "buffer_load_dwordx4 v[44:47], %[a1], %[rs], s64 offen offset:2304 sc1\n\t" "s_waitcnt vmcnt(0)\n\t" "10:\n\t" XT(80, 81) "s_add_u32 s66, s62, %[rg]\n\t" \
    "s_cmp_lt_u32 s66, s60\n\t" "s_cbranch_scc1 60f\n\t" "63:\n\t" "v_mfma_f32_16x16x32_bf16 v[80:83], v[16:19], a[0:3], 0\n\t" \
    "v_mfma_f32_16x16x32_bf16 v[84:87], v[20:23], a[0:3], 0\n\t" "v_mfma_f32_16x16x32_bf16 v[80:83], v[24:27], a[4:7], v[80:83]\n\t" \
    "v_mfma_f32_16x16x32_bf16 v[84:87], v[28:31], a[4:7], v[84:87]\n\t" "v_mfma_f32_16x16x32_bf16 v[80:83], v[32:35], a[8:11], v[80:83]\n\t" \
    "v_cvt_pk_bf16_f32 v112, v96, v97\n\t" "v_mfma_f32_16x16x32_bf16 v[84:87], v[36:39], a[8:11], v[84:87]\n\t" "v_cvt_pk_bf16_f32 v113, v98, v99\n\t" \
    "v_mfma_f32_16x16x32_bf16 v[80:83], v[40:43], a[12:15], v[80:83]\n\t" "v_cvt_pk_bf16_f32 v114, v100, v101\n\t" \
    "v_mfma_f32_16x16x32_bf16 v[84:87], v[44:47], a[12:15], v[84:87]\n\t" "v_cvt_pk_bf16_f32 v115, v102, v103\n\t" \
    "v_mfma_f32_16x16x32_bf16 v[88:91], v[16:19], a[16:19], 0\n\t" "v_cvt_pk_bf16_f32 v116, v104, v105\n\t" "v_mfma_f32_16x16x32_bf16 v[92:95], v[20:23], a[16:19], 0\n\t" \
    "v_cvt_pk_bf16_f32 v117, v106, v107\n\t" "v_mfma_f32_16x16x32_bf16 v[88:91], v[24:27], a[20:23], v[88:91]\n\t" \
    "buffer_store_dwordx4 v[112:115], %[z1], %[rs], s63 offen sc1\n\t" "v_mfma_f32_16x16x32_bf16 v[92:95], v[28:31], a[20:23], v[92:95]\n\t" \
    "v_mfma_f32_16x16x32_bf16 v[88:91], v[32:35], a[24:27], v[88:91]\n\t" "v_cvt_pk_bf16_f32 v118, v108, v109\n\t" \
    "v_mfma_f32_16x16x32_bf16 v[92:95], v[36:39], a[24:27], v[92:95]\n\t" "v_cvt_pk_bf16_f32 v119, v110, v111\n\t" \
    "v_mfma_f32_16x16x32_bf16 v[88:91], v[40:43], a[28:31], v[88:91]\n\t" "buffer_store_dwordx4 v[116:119], %[z2], %[rs], s63 offen sc1\n\t" "s_and_b32 s63, s60, %[rm]\n\t" \
    "v_mfma_f32_16x16x32_bf16 v[92:95], v[44:47], a[28:31], v[92:95]\n\t" "s_lshl_b32 s63, s63, 22\n\t" "s_add_u32 s63, s63, %[zb]\n\t" \
    "v_mfma_f32_16x16x32_bf16 v[96:99], v[16:19], a[32:35], 0\n\t" "buffer_load_dword v120, %[fo], %[rc], %[fv] offen sc1\n\t" \
    "v_mfma_f32_16x16x32_bf16 v[100:103], v[20:23], a[32:35], 0\n\t" "v_mfma_f32_16x16x32_bf16 v[96:99], v[24:27], a[36:39], v[96:99]\n\t" \
    "buffer_load_dword v121, %[fo], %[rc], %[fs] offen sc1\n\t" "v_mfma_f32_16x16x32_bf16 v[100:103], v[28:31], a[36:39], v[100:103]\n\t" \
    "v_mfma_f32_16x16x32_bf16 v[96:99], v[32:35], a[40:43], v[96:99]\n\t" "v_cvt_pk_bf16_f32 v112, v80, v81\n\t" \
    "v_mfma_f32_16x16x32_bf16 v[100:103], v[36:39], a[40:43], v[100:103]\n\t" "v_cvt_pk_bf16_f32 v113, v82, v83\n\t" \
    "v_mfma_f32_16x16x32_bf16 v[96:99], v[40:43], a[44:47], v[96:99]\n\t" "v_cvt_pk_bf16_f32 v114, v84, v85\n\t" \
    "v_mfma_f32_16x16x32_bf16 v[100:103], v[44:47], a[44:47], v[100:103]\n\t" "v_cvt_pk_bf16_f32 v115, v86, v87\n\t" \
    "v_mfma_f32_16x16x32_bf16 v[104:107], v[16:19], a[48:51], 0\n\t" "v_cvt_pk_bf16_f32 v116, v88, v89\n\t" "v_mfma_f32_16x16x32_bf16 v[108:111], v[20:23], a[48:51], 0\n\t" \
    "v_cvt_pk_bf16_f32 v117, v90, v91\n\t" "v_mfma_f32_16x16x32_bf16 v[104:107], v[24:27], a[52:55], v[104:107]\n\t" \
    "buffer_store_dwordx4 v[112:115], %[z1], %[rs], s63 offen sc1\n\t" "v_mfma_f32_16x16x32_bf16 v[108:111], v[28:31], a[52:55], v[108:111]\n\t" \
    "v_mfma_f32_16x16x32_bf16 v[104:107], v[32:35], a[56:59], v[104:107]\n\t" "v_cvt_pk_bf16_f32 v118, v92, v93\n\t" \
    "v_mfma_f32_16x16x32_bf16 v[108:111], v[36:39], a[56:59], v[108:111]\n\t" "v_cvt_pk_bf16_f32 v119, v94, v95\n\t" \
    "v_mfma_f32_16x16x32_bf16 v[104:107], v[40:43], a[60:63], v[104:107]\n\t" "buffer_store_dwordx4 v[116:119], %[z2], %[rs], s63 offen sc1\n\t" \
    "v_mfma_f32_16x16x32_bf16 v[108:111], v[44:47], a[60:63], v[108:111]\n\t" "s_add_u32 s63, s63, 0x20000\n\t" "v_mfma_f32_16x16x32_bf16 v[80:83], v[16:19], a[64:67], 0\n\t" \
    "s_add_u32 s65, s60, 1\n\t" "s_lshr_b32 s66, s65, 2\n\t" "v_mfma_f32_16x16x32_bf16 v[84:87], v[20:23], a[64:67], 0\n\t" "s_xor_b32 s66, s66, %[xp]\n\t" \
    "s_and_b32 s66, s66, 1\n\t" "v_mfma_f32_16x16x32_bf16 v[80:83], v[24:27], a[68:71], v[80:83]\n\t" "s_lshl_b32 s66, s66, 13\n\t" "s_lshl_b32 s64, s65, 19\n\t" \
    "v_mfma_f32_16x16x32_bf16 v[84:87], v[28:31], a[68:71], v[84:87]\n\t" "s_add_u32 s64, s64, s66\n\t" "s_add_u32 s64, s64, %[vb]\n\t" \
    "v_mfma_f32_16x16x32_bf16 v[80:83], v[32:35], a[72:75], v[80:83]\n\t" "v_cvt_pk_bf16_f32 v112, v96, v97\n\t" \
    "v_mfma_f32_16x16x32_bf16 v[84:87], v[36:39], a[72:75], v[84:87]\n\t" "v_cvt_pk_bf16_f32 v113, v98, v99\n\t" \
    "v_mfma_f32_16x16x32_bf16 v[80:83], v[40:43], a[76:79], v[80:83]\n\t" "v_cvt_pk_bf16_f32 v114, v100, v101\n\t" \
    "v_mfma_f32_16x16x32_bf16 v[84:87], v[44:47], a[76:79], v[84:87]\n\t" "v_cvt_pk_bf16_f32 v115, v102, v103\n\t" \
    "v_mfma_f32_16x16x32_bf16 v[88:91], v[16:19], a[80:83], 0\n\t" "v_cvt_pk_bf16_f32 v116, v104, v105\n\t" "v_mfma_f32_16x16x32_bf16 v[92:95], v[20:23], a[80:83], 0\n\t" \
    "v_cvt_pk_bf16_f32 v117, v106, v107\n\t" "v_mfma_f32_16x16x32_bf16 v[88:91], v[24:27], a[84:87], v[88:91]\n\t" \
    "buffer_store_dwordx4 v[112:115], %[z1], %[rs], s63 offen sc1\n\t" "v_mfma_f32_16x16x32_bf16 v[92:95], v[28:31], a[84:87], v[92:95]\n\t" \
    "v_mfma_f32_16x16x32_bf16 v[88:91], v[32:35], a[88:91], v[88:91]\n\t" "v_cvt_pk_bf16_f32 v118, v108, v109\n\t" \
    "v_mfma_f32_16x16x32_bf16 v[92:95], v[36:39], a[88:91], v[92:95]\n\t" "v_cvt_pk_bf16_f32 v119, v110, v111\n\t" \
    "v_mfma_f32_16x16x32_bf16 v[88:91], v[40:43], a[92:95], v[88:91]\n\t" "buffer_store_dwordx4 v[116:119], %[z2], %[rs], s63 offen sc1\n\t" \
    "v_mfma_f32_16x16x32_bf16 v[92:95], v[44:47], a[92:95], v[92:95]\n\t" "s_add_u32 s63, s63, 0x20000\n\t" "v_mfma_f32_16x16x32_bf16 v[96:99], v[16:19], a[96:99], 0\n\t" \
    "v_mfma_f32_16x16x32_bf16 v[100:103], v[20:23], a[96:99], 0\n\t" "v_mfma_f32_16x16x32_bf16 v[96:99], v[24:27], a[100:103], v[96:99]\n\t" \
    "v_mfma_f32_16x16x32_bf16 v[100:103], v[28:31], a[100:103], v[100:103]\n\t" "v_mfma_f32_16x16x32_bf16 v[96:99], v[32:35], a[104:107], v[96:99]\n\t" \
    "v_cvt_pk_bf16_f32 v112, v80, v81\n\t" "v_mfma_f32_16x16x32_bf16 v[100:103], v[36:39], a[104:107], v[100:103]\n\t" "v_cvt_pk_bf16_f32 v113, v82, v83\n\t" \
    "v_mfma_f32_16x16x32_bf16 v[96:99], v[40:43], a[108:111], v[96:99]\n\t" "v_cvt_pk_bf16_f32 v114, v84, v85\n\t" \
    "v_mfma_f32_16x16x32_bf16 v[100:103], v[44:47], a[108:111], v[100:103]\n\t" "v_cvt_pk_bf16_f32 v115, v86, v87\n\t" \
    "v_mfma_f32_16x16x32_bf16 v[104:107], v[16:19], a[112:115], 0\n\t" "v_cvt_pk_bf16_f32 v116, v88, v89\n\t" \
    "v_mfma_f32_16x16x32_bf16 v[108:111], v[20:23], a[112:115], 0\n\t" "v_cvt_pk_bf16_f32 v117, v90, v91\n\t" \
    "v_mfma_f32_16x16x32_bf16 v[104:107], v[24:27], a[116:119], v[104:107]\n\t" "buffer_store_dwordx4 v[112:115], %[z1], %[rs], s63 offen sc1\n\t" \
    "v_mfma_f32_16x16x32_bf16 v[108:111], v[28:31], a[116:119], v[108:111]\n\t" "v_mfma_f32_16x16x32_bf16 v[104:107], v[32:35], a[120:123], v[104:107]\n\t" \
    "v_cvt_pk_bf16_f32 v118, v92, v93\n\t" "v_mfma_f32_16x16x32_bf16 v[108:111], v[36:39], a[120:123], v[108:111]\n\t" "v_cvt_pk_bf16_f32 v119, v94, v95\n\t" \
    "v_mfma_f32_16x16x32_bf16 v[104:107], v[40:43], a[124:127], v[104:107]\n\t" "buffer_store_dwordx4 v[116:119], %[z2], %[rs], s63 offen sc1\n\t" \
    "v_mfma_f32_16x16x32_bf16 v[108:111], v[44:47], a[124:127], v[108:111]\n\t" "s_add_u32 s63, s63, 0x20000\n\t" \
    "v_mfma_f32_16x16x32_bf16 v[80:83], v[16:19], a[128:131], 0\n\t" "s_waitcnt vmcnt(6)\n\t" "v_readfirstlane_b32 s66, v120\n\t" "v_readfirstlane_b32 s67, v121\n\t" \
    "v_mfma_f32_16x16x32_bf16 v[84:87], v[20:23], a[128:131], 0\n\t" "v_mov_b32 v122, s60\n\t" "buffer_store_dword v122, %[fo], %[rc], %[fz] offen sc1\n\t" \
    "v_mfma_f32_16x16x32_bf16 v[80:83], v[24:27], a[132:135], v[80:83]\n\t" "s_max_u32 s61, s61, s66\n\t" "s_max_u32 s62, s62, s67\n\t" "s_cmp_gt_u32 s61, s65\n\t" \
    "v_mfma_f32_16x16x32_bf16 v[84:87], v[28:31], a[132:135], v[84:87]\n\t" "s_cselect_b32 s65, 1, 0\n\t" "s_cselect_b32 s64, s64, 0x7f000000\n\t" \
    "v_mfma_f32_16x16x32_bf16 v[80:83], v[32:35], a[136:139], v[80:83]\n\t" "buffer_load_dwordx4 v[48:51], %[ao], %[rs], s64 offen offset:0 sc1\n\t" \
    "v_cvt_pk_bf16_f32 v112, v96, v97\n\t" "v_mfma_f32_16x16x32_bf16 v[84:87], v[36:39], a[136:139], v[84:87]\n\t" "v_cvt_pk_bf16_f32 v113, v98, v99\n\t" \
    "v_mfma_f32_16x16x32_bf16 v[80:83], v[40:43], a[140:143], v[80:83]\n\t" "buffer_load_dwordx4 v[52:55], %[ao], %[rs], s64 offen offset:256 sc1\n\t" \
    "v_cvt_pk_bf16_f32 v114, v100, v101\n\t" "v_mfma_f32_16x16x32_bf16 v[84:87], v[44:47], a[140:143], v[84:87]\n\t" "v_cvt_pk_bf16_f32 v115, v102, v103\n\t" \
    "v_mfma_f32_16x16x32_bf16 v[88:91], v[16:19], a[144:147], 0\n\t" "buffer_load_dwordx4 v[56:59], %[ao], %[rs], s64 offen offset:2048 sc1\n\t" \
    "v_cvt_pk_bf16_f32 v116, v104, v105\n\t" "v_mfma_f32_16x16x32_bf16 v[92:95], v[20:23], a[144:147], 0\n\t" "v_cvt_pk_bf16_f32 v117, v106, v107\n\t" \
    "v_mfma_f32_16x16x32_bf16 v[88:91], v[24:27], a[148:151], v[88:91]\n\t" "buffer_load_dwordx4 v[60:63], %[ao], %[rs], s64 offen offset:2304 sc1\n\t" \
    "v_mfma_f32_16x16x32_bf16 v[92:95], v[28:31], a[148:151], v[92:95]\n\t" "buffer_store_dwordx4 v[112:115], %[z1], %[rs], s63 offen sc1\n\t" \
    "v_mfma_f32_16x16x32_bf16 v[88:91], v[32:35], a[152:155], v[88:91]\n\t" "buffer_load_dwordx4 v[64:67], %[a1], %[rs], s64 offen offset:0 sc1\n\t" \
    "v_cvt_pk_bf16_f32 v118, v108, v109\n\t" "v_mfma_f32_16x16x32_bf16 v[92:95], v[36:39], a[152:155], v[92:95]\n\t" "v_cvt_pk_bf16_f32 v119, v110, v111\n\t" \
    "v_mfma_f32_16x16x32_bf16 v[88:91], v[40:43], a[156:159], v[88:91]\n\t" "buffer_load_dwordx4 v[68:71], %[a1], %[rs], s64 offen offset:256 sc1\n\t" \
    "buffer_store_dwordx4 v[116:119], %[z2], %[rs], s63 offen sc1\n\t" "v_mfma_f32_16x16x32_bf16 v[92:95], v[44:47], a[156:159], v[92:95]\n\t" \
    "s_add_u32 s63, s63, 0x20000\n\t" "v_mfma_f32_16x16x32_bf16 v[96:99], v[16:19], a[160:163], 0\n\t" \
    "buffer_load_dwordx4 v[72:75], %[a1], %[rs], s64 offen offset:2048 sc1\n\t" "v_mfma_f32_16x16x32_bf16 v[100:103], v[20:23], a[160:163], 0\n\t" \
    "v_mfma_f32_16x16x32_bf16 v[96:99], v[24:27], a[164:167], v[96:99]\n\t" "buffer_load_dwordx4 v[76:79], %[a1], %[rs], s64 offen offset:2304 sc1\n\t" \
    "v_mfma_f32_16x16x32_bf16 v[100:103], v[28:31], a[164:167], v[100:103]\n\t" "v_mfma_f32_16x16x32_bf16 v[96:99], v[32:35], a[168:171], v[96:99]\n\t" \
    "v_cvt_pk_bf16_f32 v112, v80, v81\n\t" "v_mfma_f32_16x16x32_bf16 v[100:103], v[36:39], a[168:171], v[100:103]\n\t" "v_cvt_pk_bf16_f32 v113, v82, v83\n\t" \
    "v_mfma_f32_16x16x32_bf16 v[96:99], v[40:43], a[172:175], v[96:99]\n\t" "v_cvt_pk_bf16_f32 v114, v84, v85\n\t" \
    "v_mfma_f32_16x16x32_bf16 v[100:103], v[44:47], a[172:175], v[100:103]\n\t" "v_cvt_pk_bf16_f32 v115, v86, v87\n\t" \
    "v_mfma_f32_16x16x32_bf16 v[104:107], v[16:19], a[176:179], 0\n\t" "v_cvt_pk_bf16_f32 v116, v88, v89\n\t" \
    "v_mfma_f32_16x16x32_bf16 v[108:111], v[20:23], a[176:179], 0\n\t" "v_cvt_pk_bf16_f32 v117, v90, v91\n\t" \
    "v_mfma_f32_16x16x32_bf16 v[104:107], v[24:27], a[180:183], v[104:107]\n\t" "buffer_store_dwordx4 v[112:115], %[z1], %[rs], s63 offen sc1\n\t" \
    "v_mfma_f32_16x16x32_bf16 v[108:111], v[28:31], a[180:183], v[108:111]\n\t" "v_mfma_f32_16x16x32_bf16 v[104:107], v[32:35], a[184:187], v[104:107]\n\t" \
    "v_cvt_pk_bf16_f32 v118, v92, v93\n\t" "v_mfma_f32_16x16x32_bf16 v[108:111], v[36:39], a[184:187], v[108:111]\n\t" "v_cvt_pk_bf16_f32 v119, v94, v95\n\t" \
    "v_mfma_f32_16x16x32_bf16 v[104:107], v[40:43], a[188:191], v[104:107]\n\t" "buffer_store_dwordx4 v[116:119], %[z2], %[rs], s63 offen sc1\n\t" \
    "v_mfma_f32_16x16x32_bf16 v[108:111], v[44:47], a[188:191], v[108:111]\n\t" "s_add_u32 s63, s63, 0x20000\n\t" \
    "v_mfma_f32_16x16x32_bf16 v[80:83], v[16:19], a[192:195], 0\n\t" "v_mfma_f32_16x16x32_bf16 v[84:87], v[20:23], a[192:195], 0\n\t" \
    "v_mfma_f32_16x16x32_bf16 v[80:83], v[24:27], a[196:199], v[80:83]\n\t" "v_mfma_f32_16x16x32_bf16 v[84:87], v[28:31], a[196:199], v[84:87]\n\t" \
    "v_mfma_f32_16x16x32_bf16 v[80:83], v[32:35], a[200:203], v[80:83]\n\t" "v_cvt_pk_bf16_f32 v112, v96, v97\n\t" \
    "v_mfma_f32_16x16x32_bf16 v[84:87], v[36:39], a[200:203], v[84:87]\n\t" "v_cvt_pk_bf16_f32 v113, v98, v99\n\t" \
    "v_mfma_f32_16x16x32_bf16 v[80:83], v[40:43], a[204:207], v[80:83]\n\t" "v_cvt_pk_bf16_f32 v114, v100, v101\n\t" \
    "v_mfma_f32_16x16x32_bf16 v[84:87], v[44:47], a[204:207], v[84:87]\n\t" "v_cvt_pk_bf16_f32 v115, v102, v103\n\t" \
    "v_mfma_f32_16x16x32_bf16 v[88:91], v[16:19], a[208:211], 0\n\t" "v_cvt_pk_bf16_f32 v116, v104, v105\n\t" "v_mfma_f32_16x16x32_bf16 v[92:95], v[20:23], a[208:211], 0\n\t" \
    "v_cvt_pk_bf16_f32 v117, v106, v107\n\t" "v_mfma_f32_16x16x32_bf16 v[88:91], v[24:27], a[212:215], v[88:91]\n\t" \
    "buffer_store_dwordx4 v[112:115], %[z1], %[rs], s63 offen sc1\n\t" "v_mfma_f32_16x16x32_bf16 v[92:95], v[28:31], a[212:215], v[92:95]\n\t" \
    "v_mfma_f32_16x16x32_bf16 v[88:91], v[32:35], a[216:219], v[88:91]\n\t" "v_cvt_pk_bf16_f32 v118, v108, v109\n\t" \
    "v_mfma_f32_16x16x32_bf16 v[92:95], v[36:39], a[216:219], v[92:95]\n\t" "v_cvt_pk_bf16_f32 v119, v110, v111\n\t" \
    "v_mfma_f32_16x16x32_bf16 v[88:91], v[40:43], a[220:223], v[88:91]\n\t" "buffer_store_dwordx4 v[116:119], %[z2], %[rs], s63 offen sc1\n\t" \
    "v_mfma_f32_16x16x32_bf16 v[92:95], v[44:47], a[220:223], v[92:95]\n\t" "s_add_u32 s63, s63, 0x20000\n\t" "v_mfma_f32_16x16x32_bf16 v[96:99], v[16:19], a[224:227], 0\n\t" \
    "v_mfma_f32_16x16x32_bf16 v[100:103], v[20:23], a[224:227], 0\n\t" "v_mfma_f32_16x16x32_bf16 v[96:99], v[24:27], a[228:231], v[96:99]\n\t" \
    "v_mfma_f32_16x16x32_bf16 v[100:103], v[28:31], a[228:231], v[100:103]\n\t" "v_mfma_f32_16x16x32_bf16 v[96:99], v[32:35], a[232:235], v[96:99]\n\t" \
    "v_cvt_pk_bf16_f32 v112, v80, v81\n\t" "v_mfma_f32_16x16x32_bf16 v[100:103], v[36:39], a[232:235], v[100:103]\n\t" "v_cvt_pk_bf16_f32 v113, v82, v83\n\t" \
    "v_mfma_f32_16x16x32_bf16 v[96:99], v[40:43], a[236:239], v[96:99]\n\t" "v_cvt_pk_bf16_f32 v114, v84, v85\n\t" \
    "v_mfma_f32_16x16x32_bf16 v[100:103], v[44:47], a[236:239], v[100:103]\n\t" "v_cvt_pk_bf16_f32 v115, v86, v87\n\t" \
    "v_mfma_f32_16x16x32_bf16 v[104:107], v[16:19], a[240:243], 0\n\t" "v_cvt_pk_bf16_f32 v116, v88, v89\n\t" \
    "v_mfma_f32_16x16x32_bf16 v[108:111], v[20:23], a[240:243], 0\n\t" "v_cvt_pk_bf16_f32 v117, v90, v91\n\t" \
    "v_mfma_f32_16x16x32_bf16 v[104:107], v[24:27], a[244:247], v[104:107]\n\t" "buffer_store_dwordx4 v[112:115], %[z1], %[rs], s63 offen sc1\n\t" \
    "v_mfma_f32_16x16x32_bf16 v[108:111], v[28:31], a[244:247], v[108:111]\n\t" "v_mfma_f32_16x16x32_bf16 v[104:107], v[32:35], a[248:251], v[104:107]\n\t" \
    "v_cvt_pk_bf16_f32 v118, v92, v93\n\t" "v_mfma_f32_16x16x32_bf16 v[108:111], v[36:39], a[248:251], v[108:111]\n\t" "v_cvt_pk_bf16_f32 v119, v94, v95\n\t" \
    "v_mfma_f32_16x16x32_bf16 v[104:107], v[40:43], a[252:255], v[104:107]\n\t" "buffer_store_dwordx4 v[116:119], %[z2], %[rs], s63 offen sc1\n\t" \
    "s_add_u32 s63, s63, 0x20000\n\t" "s_add_u32 s60, s60, 1\n\t" "v_mfma_f32_16x16x32_bf16 v[108:111], v[44:47], a[252:255], v[108:111]\n\t" "s_cmp_eq_u32 s65, 0\n\t" \
    "s_waitcnt vmcnt(6)\n\t" ZSUM "s_cbranch_scc1 20f\n\t" "23:\n\t" XT(80, 81) "s_add_u32 s66, s62, %[rg]\n\t" "s_cmp_lt_u32 s66, s60\n\t" "s_cbranch_scc1 70f\n\t" "73:\n\t" \
    "v_mfma_f32_16x16x32_bf16 v[80:83], v[48:51], a[0:3], 0\n\t" "v_mfma_f32_16x16x32_bf16 v[84:87], v[52:55], a[0:3], 0\n\t" \
    "v_mfma_f32_16x16x32_bf16 v[80:83], v[56:59], a[4:7], v[80:83]\n\t" "v_mfma_f32_16x16x32_bf16 v[84:87], v[60:63], a[4:7], v[84:87]\n\t" \
    "v_mfma_f32_16x16x32_bf16 v[80:83], v[64:67], a[8:11], v[80:83]\n\t" "v_cvt_pk_bf16_f32 v112, v96, v97\n\t" \
    "v_mfma_f32_16x16x32_bf16 v[84:87], v[68:71], a[8:11], v[84:87]\n\t" "v_cvt_pk_bf16_f32 v113, v98, v99\n\t" \
    "v_mfma_f32_16x16x32_bf16 v[80:83], v[72:75], a[12:15], v[80:83]\n\t" "v_cvt_pk_bf16_f32 v114, v100, v101\n\t" \
    "v_mfma_f32_16x16x32_bf16 v[84:87], v[76:79], a[12:15], v[84:87]\n\t" "v_cvt_pk_bf16_f32 v115, v102, v103\n\t" \
    "v_mfma_f32_16x16x32_bf16 v[88:91], v[48:51], a[16:19], 0\n\t" "v_cvt_pk_bf16_f32 v116, v104, v105\n\t" "v_mfma_f32_16x16x32_bf16 v[92:95], v[52:55], a[16:19], 0\n\t" \
    "v_cvt_pk_bf16_f32 v117, v106, v107\n\t" "v_mfma_f32_16x16x32_bf16 v[88:91], v[56:59], a[20:23], v[88:91]\n\t" \
    "buffer_store_dwordx4 v[112:115], %[z1], %[rs], s63 offen sc1\n\t" "v_mfma_f32_16x16x32_bf16 v[92:95], v[60:63], a[20:23], v[92:95]\n\t" \
    "v_mfma_f32_16x16x32_bf16 v[88:91], v[64:67], a[24:27], v[88:91]\n\t" "v_cvt_pk_bf16_f32 v118, v108, v109\n\t" \
    "v_mfma_f32_16x16x32_bf16 v[92:95], v[68:71], a[24:27], v[92:95]\n\t" "v_cvt_pk_bf16_f32 v119, v110, v111\n\t" \
    "v_mfma_f32_16x16x32_bf16 v[88:91], v[72:75], a[28:31], v[88:91]\n\t" "buffer_store_dwordx4 v[116:119], %[z2], %[rs], s63 offen sc1\n\t" "s_and_b32 s63, s60, %[rm]\n\t" \
    "v_mfma_f32_16x16x32_bf16 v[92:95], v[76:79], a[28:31], v[92:95]\n\t" "s_lshl_b32 s63, s63, 22\n\t" "s_add_u32 s63, s63, %[zb]\n\t" \
    "v_mfma_f32_16x16x32_bf16 v[96:99], v[48:51], a[32:35], 0\n\t" "buffer_load_dword v120, %[fo], %[rc], %[fv] offen sc1\n\t" \
    "v_mfma_f32_16x16x32_bf16 v[100:103], v[52:55], a[32:35], 0\n\t" "v_mfma_f32_16x16x32_bf16 v[96:99], v[56:59], a[36:39], v[96:99]\n\t" \
    "buffer_load_dword v121, %[fo], %[rc], %[fs] offen sc1\n\t" "v_mfma_f32_16x16x32_bf16 v[100:103], v[60:63], a[36:39], v[100:103]\n\t" \
    "v_mfma_f32_16x16x32_bf16 v[96:99], v[64:67], a[40:43], v[96:99]\n\t" "v_cvt_pk_bf16_f32 v112, v80, v81\n\t" \
    "v_mfma_f32_16x16x32_bf16 v[100:103], v[68:71], a[40:43], v[100:103]\n\t" "v_cvt_pk_bf16_f32 v113, v82, v83\n\t" \
    "v_mfma_f32_16x16x32_bf16 v[96:99], v[72:75], a[44:47], v[96:99]\n\t" "v_cvt_pk_bf16_f32 v114, v84, v85\n\t" \
    "v_mfma_f32_16x16x32_bf16 v[100:103], v[76:79], a[44:47], v[100:103]\n\t" "v_cvt_pk_bf16_f32 v115, v86, v87\n\t" \
    "v_mfma_f32_16x16x32_bf16 v[104:107], v[48:51], a[48:51], 0\n\t" "v_cvt_pk_bf16_f32 v116, v88, v89\n\t" "v_mfma_f32_16x16x32_bf16 v[108:111], v[52:55], a[48:51], 0\n\t" \
    "v_cvt_pk_bf16_f32 v117, v90, v91\n\t" "v_mfma_f32_16x16x32_bf16 v[104:107], v[56:59], a[52:55], v[104:107]\n\t" \
    "buffer_store_dwordx4 v[112:115], %[z1], %[rs], s63 offen sc1\n\t" "v_mfma_f32_16x16x32_bf16 v[108:111], v[60:63], a[52:55], v[108:111]\n\t" \
    "v_mfma_f32_16x16x32_bf16 v[104:107], v[64:67], a[56:59], v[104:107]\n\t" "v_cvt_pk_bf16_f32 v118, v92, v93\n\t" \
    "v_mfma_f32_16x16x32_bf16 v[108:111], v[68:71], a[56:59], v[108:111]\n\t" "v_cvt_pk_bf16_f32 v119, v94, v95\n\t" \
    "v_mfma_f32_16x16x32_bf16 v[104:107], v[72:75], a[60:63], v[104:107]\n\t" "buffer_store_dwordx4 v[116:119], %[z2], %[rs], s63 offen sc1\n\t" \
    "v_mfma_f32_16x16x32_bf16 v[108:111], v[76:79], a[60:63], v[108:111]\n\t" "s_add_u32 s63, s63, 0x20000\n\t" "v_mfma_f32_16x16x32_bf16 v[80:83], v[48:51], a[64:67], 0\n\t" \
    "s_add_u32 s65, s60, 1\n\t" "s_lshr_b32 s66, s65, 2\n\t" "v_mfma_f32_16x16x32_bf16 v[84:87], v[52:55], a[64:67], 0\n\t" "s_xor_b32 s66, s66, %[xp]\n\t" \
    "s_and_b32 s66, s66, 1\n\t" "v_mfma_f32_16x16x32_bf16 v[80:83], v[56:59], a[68:71], v[80:83]\n\t" "s_lshl_b32 s66, s66, 13\n\t" "s_lshl_b32 s64, s65, 19\n\t" \
    "v_mfma_f32_16x16x32_bf16 v[84:87], v[60:63], a[68:71], v[84:87]\n\t" "s_add_u32 s64, s64, s66\n\t" "s_add_u32 s64, s64, %[vb]\n\t" \
    "v_mfma_f32_16x16x32_bf16 v[80:83], v[64:67], a[72:75], v[80:83]\n\t" "v_cvt_pk_bf16_f32 v112, v96, v97\n\t" \
    "v_mfma_f32_16x16x32_bf16 v[84:87], v[68:71], a[72:75], v[84:87]\n\t" "v_cvt_pk_bf16_f32 v113, v98, v99\n\t" \
    "v_mfma_f32_16x16x32_bf16 v[80:83], v[72:75], a[76:79], v[80:83]\n\t" "v_cvt_pk_bf16_f32 v114, v100, v101\n\t" \
    "v_mfma_f32_16x16x32_bf16 v[84:87], v[76:79], a[76:79], v[84:87]\n\t" "v_cvt_pk_bf16_f32 v115, v102, v103\n\t" \
    "v_mfma_f32_16x16x32_bf16 v[88:91], v[48:51], a[80:83], 0\n\t" "v_cvt_pk_bf16_f32 v116, v104, v105\n\t" "v_mfma_f32_16x16x32_bf16 v[92:95], v[52:55], a[80:83], 0\n\t" \
    "v_cvt_pk_bf16_f32 v117, v106, v107\n\t" "v_mfma_f32_16x16x32_bf16 v[88:91], v[56:59], a[84:87], v[88:91]\n\t" \
    "buffer_store_dwordx4 v[112:115], %[z1], %[rs], s63 offen sc1\n\t" "v_mfma_f32_16x16x32_bf16 v[92:95], v[60:63], a[84:87], v[92:95]\n\t" \
    "v_mfma_f32_16x16x32_bf16 v[88:91], v[64:67], a[88:91], v[88:91]\n\t" "v_cvt_pk_bf16_f32 v118, v108, v109\n\t" \
    "v_mfma_f32_16x16x32_bf16 v[92:95], v[68:71], a[88:91], v[92:95]\n\t" "v_cvt_pk_bf16_f32 v119, v110, v111\n\t" \
    "v_mfma_f32_16x16x32_bf16 v[88:91], v[72:75], a[92:95], v[88:91]\n\t" "buffer_store_dwordx4 v[116:119], %[z2], %[rs], s63 offen sc1\n\t" \
    "v_mfma_f32_16x16x32_bf16 v[92:95], v[76:79], a[92:95], v[92:95]\n\t" "s_add_u32 s63, s63, 0x20000\n\t" "v_mfma_f32_16x16x32_bf16 v[96:99], v[48:51], a[96:99], 0\n\t" \
    "v_mfma_f32_16x16x32_bf16 v[100:103], v[52:55], a[96:99], 0\n\t" "v_mfma_f32_16x16x32_bf16 v[96:99], v[56:59], a[100:103], v[96:99]\n\t" \
    "v_mfma_f32_16x16x32_bf16 v[100:103], v[60:63], a[100:103], v[100:103]\n\t" "v_mfma_f32_16x16x32_bf16 v[96:99], v[64:67], a[104:107], v[96:99]\n\t" \
    "v_cvt_pk_bf16_f32 v112, v80, v81\n\t" "v_mfma_f32_16x16x32_bf16 v[100:103], v[68:71], a[104:107], v[100:103]\n\t" "v_cvt_pk_bf16_f32 v113, v82, v83\n\t" \
    "v_mfma_f32_16x16x32_bf16 v[96:99], v[72:75], a[108:111], v[96:99]\n\t" "v_cvt_pk_bf16_f32 v114, v84, v85\n\t" \
    "v_mfma_f32_16x16x32_bf16 v[100:103], v[76:79], a[108:111], v[100:103]\n\t" "v_cvt_pk_bf16_f32 v115, v86, v87\n\t" \
    "v_mfma_f32_16x16x32_bf16 v[104:107], v[48:51], a[112:115], 0\n\t" "v_cvt_pk_bf16_f32 v116, v88, v89\n\t" \
    "v_mfma_f32_16x16x32_bf16 v[108:111], v[52:55], a[112:115], 0\n\t" "v_cvt_pk_bf16_f32 v117, v90, v91\n\t" \
    "v_mfma_f32_16x16x32_bf16 v[104:107], v[56:59], a[116:119], v[104:107]\n\t" "buffer_store_dwordx4 v[112:115], %[z1], %[rs], s63 offen sc1\n\t" \
    "v_mfma_f32_16x16x32_bf16 v[108:111], v[60:63], a[116:119], v[108:111]\n\t" "v_mfma_f32_16x16x32_bf16 v[104:107], v[64:67], a[120:123], v[104:107]\n\t" \
    "v_cvt_pk_bf16_f32 v118, v92, v93\n\t" "v_mfma_f32_16x16x32_bf16 v[108:111], v[68:71], a[120:123], v[108:111]\n\t" "v_cvt_pk_bf16_f32 v119, v94, v95\n\t" \
    "v_mfma_f32_16x16x32_bf16 v[104:107], v[72:75], a[124:127], v[104:107]\n\t" "buffer_store_dwordx4 v[116:119], %[z2], %[rs], s63 offen sc1\n\t" \
    "v_mfma_f32_16x16x32_bf16 v[108:111], v[76:79], a[124:127], v[108:111]\n\t" "s_add_u32 s63, s63, 0x20000\n\t" \
    "v_mfma_f32_16x16x32_bf16 v[80:83], v[48:51], a[128:131], 0\n\t" "s_waitcnt vmcnt(6)\n\t" "v_readfirstlane_b32 s66, v120\n\t" "v_readfirstlane_b32 s67, v121\n\t" \
    "v_mfma_f32_16x16x32_bf16 v[84:87], v[52:55], a[128:131], 0\n\t" "v_mov_b32 v122, s60\n\t" "buffer_store_dword v122, %[fo], %[rc], %[fz] offen sc1\n\t" \
    "v_mfma_f32_16x16x32_bf16 v[80:83], v[56:59], a[132:135], v[80:83]\n\t" "s_max_u32 s61, s61, s66\n\t" "s_max_u32 s62, s62, s67\n\t" "s_cmp_gt_u32 s61, s65\n\t" \
    "v_mfma_f32_16x16x32_bf16 v[84:87], v[60:63], a[132:135], v[84:87]\n\t" "s_cselect_b32 s65, 1, 0\n\t" "s_cselect_b32 s64, s64, 0x7f000000\n\t" \
    "v_mfma_f32_16x16x32_bf16 v[80:83], v[64:67], a[136:139], v[80:83]\n\t" "buffer_load_dwordx4 v[16:19], %[ao], %[rs], s64 offen offset:0 sc1\n\t" \
    "v_cvt_pk_bf16_f32 v112, v96, v97\n\t" "v_mfma_f32_16x16x32_bf16 v[84:87], v[68:71], a[136:139], v[84:87]\n\t" "v_cvt_pk_bf16_f32 v113, v98, v99\n\t" \
    "v_mfma_f32_16x16x32_bf16 v[80:83], v[72:75], a[140:143], v[80:83]\n\t" "buffer_load_dwordx4 v[20:23], %[ao], %[rs], s64 offen offset:256 sc1\n\t" \
    "v_cvt_pk_bf16_f32 v114, v100, v101\n\t" "v_mfma_f32_16x16x32_bf16 v[84:87], v[76:79], a[140:143], v[84:87]\n\t" "v_cvt_pk_bf16_f32 v115, v102, v103\n\t" \
    "v_mfma_f32_16x16x32_bf16 v[88:91], v[48:51], a[144:147], 0\n\t" "buffer_load_dwordx4 v[24:27], %[ao], %[rs], s64 offen offset:2048 sc1\n\t" \
    "v_cvt_pk_bf16_f32 v116, v104, v105\n\t" "v_mfma_f32_16x16x32_bf16 v[92:95], v[52:55], a[144:147], 0\n\t" "v_cvt_pk_bf16_f32 v117, v106, v107\n\t" \
    "v_mfma_f32_16x16x32_bf16 v[88:91], v[56:59], a[148:151], v[88:91]\n\t" "buffer_load_dwordx4 v[28:31], %[ao], %[rs], s64 offen offset:2304 sc1\n\t" \
    "v_mfma_f32_16x16x32_bf16 v[92:95], v[60:63], a[148:151], v[92:95]\n\t" "buffer_store_dwordx4 v[112:115], %[z1], %[rs], s63 offen sc1\n\t" \
    "v_mfma_f32_16x16x32_bf16 v[88:91], v[64:67], a[152:155], v[88:91]\n\t" "buffer_load_dwordx4 v[32:35], %[a1], %[rs], s64 offen offset:0 sc1\n\t" \
    "v_cvt_pk_bf16_f32 v118, v108, v109\n\t" "v_mfma_f32_16x16x32_bf16 v[92:95], v[68:71], a[152:155], v[92:95]\n\t" "v_cvt_pk_bf16_f32 v119, v110, v111\n\t" \
    "v_mfma_f32_16x16x32_bf16 v[88:91], v[72:75], a[156:159], v[88:91]\n\t" "buffer_load_dwordx4 v[36:39], %[a1], %[rs], s64 offen offset:256 sc1\n\t" \
    "buffer_store_dwordx4 v[116:119], %[z2], %[rs], s63 offen sc1\n\t" "v_mfma_f32_16x16x32_bf16 v[92:95], v[76:79], a[156:159], v[92:95]\n\t" \
    "s_add_u32 s63, s63, 0x20000\n\t" "v_mfma_f32_16x16x32_bf16 v[96:99], v[48:51], a[160:163], 0\n\t" \
    "buffer_load_dwordx4 v[40:43], %[a1], %[rs], s64 offen offset:2048 sc1\n\t" "v_mfma_f32_16x16x32_bf16 v[100:103], v[52:55], a[160:163], 0\n\t" \
    "v_mfma_f32_16x16x32_bf16 v[96:99], v[56:59], a[164:167], v[96:99]\n\t" "buffer_load_dwordx4 v[44:47], %[a1], %[rs], s64 offen offset:2304 sc1\n\t" \
    "v_mfma_f32_16x16x32_bf16 v[100:103], v[60:63], a[164:167], v[100:103]\n\t" "v_mfma_f32_16x16x32_bf16 v[96:99], v[64:67], a[168:171], v[96:99]\n\t" \
    "v_cvt_pk_bf16_f32 v112, v80, v81\n\t" "v_mfma_f32_16x16x32_bf16 v[100:103], v[68:71], a[168:171], v[100:103]\n\t" "v_cvt_pk_bf16_f32 v113, v82, v83\n\t" \
    "v_mfma_f32_16x16x32_bf16 v[96:99], v[72:75], a[172:175], v[96:99]\n\t" "v_cvt_pk_bf16_f32 v114, v84, v85\n\t" \
    "v_mfma_f32_16x16x32_bf16 v[100:103], v[76:79], a[172:175], v[100:103]\n\t" "v_cvt_pk_bf16_f32 v115, v86, v87\n\t" \
    "v_mfma_f32_16x16x32_bf16 v[104:107], v[48:51], a[176:179], 0\n\t" "v_cvt_pk_bf16_f32 v116, v88, v89\n\t" \
    "v_mfma_f32_16x16x32_bf16 v[108:111], v[52:55], a[176:179], 0\n\t" "v_cvt_pk_bf16_f32 v117, v90, v91\n\t" \
    "v_mfma_f32_16x16x32_bf16 v[104:107], v[56:59], a[180:183], v[104:107]\n\t" "buffer_store_dwordx4 v[112:115], %[z1], %[rs], s63 offen sc1\n\t" \
    "v_mfma_f32_16x16x32_bf16 v[108:111], v[60:63], a[180:183], v[108:111]\n\t" "v_mfma_f32_16x16x32_bf16 v[104:107], v[64:67], a[184:187], v[104:107]\n\t" \
    "v_cvt_pk_bf16_f32 v118, v92, v93\n\t" "v_mfma_f32_16x16x32_bf16 v[108:111], v[68:71], a[184:187], v[108:111]\n\t" "v_cvt_pk_bf16_f32 v119, v94, v95\n\t" \
    "v_mfma_f32_16x16x32_bf16 v[104:107], v[72:75], a[188:191], v[104:107]\n\t" "buffer_store_dwordx4 v[116:119], %[z2], %[rs], s63 offen sc1\n\t" \
    "v_mfma_f32_16x16x32_bf16 v[108:111], v[76:79], a[188:191], v[108:111]\n\t" "s_add_u32 s63, s63, 0x20000\n\t" \
    "v_mfma_f32_16x16x32_bf16 v[80:83], v[48:51], a[192:195], 0\n\t" "v_mfma_f32_16x16x32_bf16 v[84:87], v[52:55], a[192:195], 0\n\t" \
    "v_mfma_f32_16x16x32_bf16 v[80:83], v[56:59], a[196:199], v[80:83]\n\t" "v_mfma_f32_16x16x32_bf16 v[84:87], v[60:63], a[196:199], v[84:87]\n\t" \
    "v_mfma_f32_16x16x32_bf16 v[80:83], v[64:67], a[200:203], v[80:83]\n\t" "v_cvt_pk_bf16_f32 v112, v96, v97\n\t" \
    "v_mfma_f32_16x16x32_bf16 v[84:87], v[68:71], a[200:203], v[84:87]\n\t" "v_cvt_pk_bf16_f32 v113, v98, v99\n\t" \
    "v_mfma_f32_16x16x32_bf16 v[80:83], v[72:75], a[204:207], v[80:83]\n\t" "v_cvt_pk_bf16_f32 v114, v100, v101\n\t" \
    "v_mfma_f32_16x16x32_bf16 v[84:87], v[76:79], a[204:207], v[84:87]\n\t" "v_cvt_pk_bf16_f32 v115, v102, v103\n\t" \
    "v_mfma_f32_16x16x32_bf16 v[88:91], v[48:51], a[208:211], 0\n\t" "v_cvt_pk_bf16_f32 v116, v104, v105\n\t" "v_mfma_f32_16x16x32_bf16 v[92:95], v[52:55], a[208:211], 0\n\t" \
    "v_cvt_pk_bf16_f32 v117, v106, v107\n\t" "v_mfma_f32_16x16x32_bf16 v[88:91], v[56:59], a[212:215], v[88:91]\n\t" \
    "buffer_store_dwordx4 v[112:115], %[z1], %[rs], s63 offen sc1\n\t" "v_mfma_f32_16x16x32_bf16 v[92:95], v[60:63], a[212:215], v[92:95]\n\t" \
    "v_mfma_f32_16x16x32_bf16 v[88:91], v[64:67], a[216:219], v[88:91]\n\t" "v_cvt_pk_bf16_f32 v118, v108, v109\n\t" \
    "v_mfma_f32_16x16x32_bf16 v[92:95], v[68:71], a[216:219], v[92:95]\n\t" "v_cvt_pk_bf16_f32 v119, v110, v111\n\t" \
    "v_mfma_f32_16x16x32_bf16 v[88:91], v[72:75], a[220:223], v[88:91]\n\t" "buffer_store_dwordx4 v[116:119], %[z2], %[rs], s63 offen sc1\n\t" \
    "v_mfma_f32_16x16x32_bf16 v[92:95], v[76:79], a[220:223], v[92:95]\n\t" "s_add_u32 s63, s63, 0x20000\n\t" "v_mfma_f32_16x16x32_bf16 v[96:99], v[48:51], a[224:227], 0\n\t" \
    "v_mfma_f32_16x16x32_bf16 v[100:103], v[52:55], a[224:227], 0\n\t" "v_mfma_f32_16x16x32_bf16 v[96:99], v[56:59], a[228:231], v[96:99]\n\t" \
    "v_mfma_f32_16x16x32_bf16 v[100:103], v[60:63], a[228:231], v[100:103]\n\t" "v_mfma_f32_16x16x32_bf16 v[96:99], v[64:67], a[232:235], v[96:99]\n\t" \
    "v_cvt_pk_bf16_f32 v112, v80, v81\n\t" "v_mfma_f32_16x16x32_bf16 v[100:103], v[68:71], a[232:235], v[100:103]\n\t" "v_cvt_pk_bf16_f32 v113, v82, v83\n\t" \
    "v_mfma_f32_16x16x32_bf16 v[96:99], v[72:75], a[236:239], v[96:99]\n\t" "v_cvt_pk_bf16_f32 v114, v84, v85\n\t" \
    "v_mfma_f32_16x16x32_bf16 v[100:103], v[76:79], a[236:239], v[100:103]\n\t" "v_cvt_pk_bf16_f32 v115, v86, v87\n\t" \
    "v_mfma_f32_16x16x32_bf16 v[104:107], v[48:51], a[240:243], 0\n\t" "v_cvt_pk_bf16_f32 v116, v88, v89\n\t" \
    "v_mfma_f32_16x16x32_bf16 v[108:111], v[52:55], a[240:243], 0\n\t" "v_cvt_pk_bf16_f32 v117, v90, v91\n\t" \
    "v_mfma_f32_16x16x32_bf16 v[104:107], v[56:59], a[244:247], v[104:107]\n\t" "buffer_store_dwordx4 v[112:115], %[z1], %[rs], s63 offen sc1\n\t" \
    "v_mfma_f32_16x16x32_bf16 v[108:111], v[60:63], a[244:247], v[108:111]\n\t" "v_mfma_f32_16x16x32_bf16 v[104:107], v[64:67], a[248:251], v[104:107]\n\t" \
    "v_cvt_pk_bf16_f32 v118, v92, v93\n\t" "v_mfma_f32_16x16x32_bf16 v[108:111], v[68:71], a[248:251], v[108:111]\n\t" "v_cvt_pk_bf16_f32 v119, v94, v95\n\t" \
    "v_mfma_f32_16x16x32_bf16 v[104:107], v[72:75], a[252:255], v[104:107]\n\t" "buffer_store_dwordx4 v[116:119], %[z2], %[rs], s63 offen sc1\n\t" \
    "s_add_u32 s63, s63, 0x20000\n\t" "s_add_u32 s60, s60, 1\n\t" "v_mfma_f32_16x16x32_bf16 v[108:111], v[76:79], a[252:255], v[108:111]\n\t" "s_cmp_eq_u32 s65, 0\n\t" \
    "s_waitcnt vmcnt(6)\n\t" ZSUM "s_cbranch_scc1 30f\n\t" "33:\n\t" "s_branch 10b\n\t" "20:\n\t" "s_nop 7\n\t" "v_cvt_pk_bf16_f32 v112, v96, v97\n\t" \
    "v_cvt_pk_bf16_f32 v113, v98, v99\n\t" "v_cvt_pk_bf16_f32 v114, v100, v101\n\t" "v_cvt_pk_bf16_f32 v115, v102, v103\n\t" "v_cvt_pk_bf16_f32 v116, v104, v105\n\t" \
    "v_cvt_pk_bf16_f32 v117, v106, v107\n\t" "v_cvt_pk_bf16_f32 v118, v108, v109\n\t" "v_cvt_pk_bf16_f32 v119, v110, v111\n\t" \
    "buffer_store_dwordx4 v[112:115], %[z1], %[rs], s63 offen sc1\n\t" "buffer_store_dwordx4 v[116:119], %[z2], %[rs], s63 offen sc1\n\t" \
    "buffer_load_dword v120, %[fo], %[rc], %[fv] offen sc1\n\t" "s_waitcnt vmcnt(0)\n\t" "v_readfirstlane_b32 s61, v120\n\t" "s_lshr_b32 s66, s60, 2\n\t" \
    "s_xor_b32 s66, s66, %[xp]\n\t" "s_and_b32 s66, s66, 1\n\t" "s_lshl_b32 s66, s66, 13\n\t" "s_lshl_b32 s64, s60, 19\n\t" "s_add_u32 s64, s64, s66\n\t" \
    "s_add_u32 s64, s64, %[vb]\n\t" "s_cmp_gt_u32 s61, s60\n\t" "s_cbranch_scc0 24f\n\t" "s_cmp_eq_u32 s60, 64\n\t" "s_cbranch_scc1 24f\n\t" \
    "buffer_load_dwordx4 v[48:51], %[ao], %[rs], s64 offen offset:0 sc1\n\t" "buffer_load_dwordx4 v[52:55], %[ao], %[rs], s64 offen offset:256 sc1\n\t" \
    "buffer_load_dwordx4 v[56:59], %[ao], %[rs], s64 offen offset:2048 sc1\n\t" "buffer_load_dwordx4 v[60:63], %[ao], %[rs], s64 offen offset:2304 sc1\n\t" \
    "buffer_load_dwordx4 v[64:67], %[a1], %[rs], s64 offen offset:0 sc1\n\t" "buffer_load_dwordx4 v[68:71], %[a1], %[rs], s64 offen offset:256 sc1\n\t" \
    "buffer_load_dwordx4 v[72:75], %[a1], %[rs], s64 offen offset:2048 sc1\n\t" "buffer_load_dwordx4 v[76:79], %[a1], %[rs], s64 offen offset:2304 sc1\n\t" \
    "v_mov_b32 v122, s60\n\t" "buffer_store_dword v122, %[fo], %[rc], %[fz] offen sc1\n\t" "s_waitcnt vmcnt(1)\n\t" "s_branch 23b\n\t" "24:\n\t" "v_mov_b32 v122, s60\n\t" \
    "buffer_store_dword v122, %[fo], %[rc], %[fz] offen sc1\n\t" "s_cmp_eq_u32 s60, 64\n\t" "s_cbranch_scc1 90f\n\t" "s_add_u32 s67, s60, 1\n\t" "s_cmp_ge_u32 s61, s67\n\t" \
    "s_cbranch_scc1 22f\n\t" "21:\n\t" "buffer_load_dword v120, %[fo], %[rc], %[fv] offen sc1\n\t" "buffer_load_dword v121, %[fo], %[rc], %[vd] offen sc1\n\t" \
    "s_waitcnt vmcnt(0)\n\t" "v_readfirstlane_b32 s61, v120\n\t" "v_readfirstlane_b32 s66, v121\n\t" "s_nop 3\n\t" "s_cmp_ge_u32 s61, s67\n\t" "s_cbranch_scc1 22f\n\t" \
    "s_bitcmp1_b32 s66, 1\n\t" "s_cbranch_scc1 90f\n\t" "s_cmp_eq_u32 %[bd], 0\n\t" "s_cbranch_scc1 22f\n\t" "s_sub_u32 %[bd], %[bd], 1\n\t" "s_sleep 2\n\t" \
    "s_branch 21b\n\t" "22:\n\t" "s_lshr_b32 s66, s60, 2\n\t" "s_xor_b32 s66, s66, %[xp]\n\t" "s_and_b32 s66, s66, 1\n\t" "s_lshl_b32 s66, s66, 13\n\t" \
    "s_lshl_b32 s64, s60, 19\n\t" "s_add_u32 s64, s64, s66\n\t" "s_add_u32 s64, s64, %[vb]\n\t" "buffer_load_dwordx4 v[48:51], %[ao], %[rs], s64 offen offset:0 sc1\n\t" \
    "buffer_load_dwordx4 v[52:55], %[ao], %[rs], s64 offen offset:256 sc1\n\t" "buffer_load_dwordx4 v[56:59], %[ao], %[rs], s64 offen offset:2048 sc1\n\t" \
    "buffer_load_dwordx4 v[60:63], %[ao], %[rs], s64 offen offset:2304 sc1\n\t" "buffer_load_dwordx4 v[64:67], %[a1], %[rs], s64 offen offset:0 sc1\n\t" \
    "buffer_load_dwordx4 v[68:71], %[a1], %[rs], s64 offen offset:256 sc1\n\t" "buffer_load_dwordx4 v[72:75], %[a1], %[rs], s64 offen offset:2048 sc1\n\t" \
    "buffer_load_dwordx4 v[76:79], %[a1], %[rs], s64 offen offset:2304 sc1\n\t" "s_waitcnt vmcnt(0)\n\t" "s_branch 23b\n\t" "60:\n\t" "s_sub_u32 s67, s60, %[rg]\n\t" \
    "s_cmp_ge_u32 s62, s67\n\t" "s_cbranch_scc1 62f\n\t" "61:\n\t" "buffer_load_dword v120, %[fo], %[rc], %[fs] offen sc1\n\t" \
    "buffer_load_dword v121, %[fo], %[rc], %[vd] offen sc1\n\t" "s_waitcnt vmcnt(0)\n\t" "v_readfirstlane_b32 s62, v120\n\t" "v_readfirstlane_b32 s66, v121\n\t" "s_nop 3\n\t" \
    "s_cmp_ge_u32 s62, s67\n\t" "s_cbranch_scc1 62f\n\t" "s_bitcmp1_b32 s66, 1\n\t" "s_cbranch_scc1 90f\n\t" "s_cmp_eq_u32 %[bd], 0\n\t" "s_cbranch_scc1 62f\n\t" \
    "s_sub_u32 %[bd], %[bd], 1\n\t" "s_sleep 2\n\t" "s_branch 61b\n\t" "62:\n\t" "s_branch 63b\n\t" "30:\n\t" "s_nop 7\n\t" "v_cvt_pk_bf16_f32 v112, v96, v97\n\t" \
    "v_cvt_pk_bf16_f32 v113, v98, v99\n\t" "v_cvt_pk_bf16_f32 v114, v100, v101\n\t" "v_cvt_pk_bf16_f32 v115, v102, v103\n\t" "v_cvt_pk_bf16_f32 v116, v104, v105\n\t" \
    "v_cvt_pk_bf16_f32 v117, v106, v107\n\t" "v_cvt_pk_bf16_f32 v118, v108, v109\n\t" "v_cvt_pk_bf16_f32 v119, v110, v111\n\t" \
    "buffer_store_dwordx4 v[112:115], %[z1], %[rs], s63 offen sc1\n\t" "buffer_store_dwordx4 v[116:119], %[z2], %[rs], s63 offen sc1\n\t" \
    "buffer_load_dword v120, %[fo], %[rc], %[fv] offen sc1\n\t" "s_waitcnt vmcnt(0)\n\t" "v_readfirstlane_b32 s61, v120\n\t" "s_lshr_b32 s66, s60, 2\n\t" \
    "s_xor_b32 s66, s66, %[xp]\n\t" "s_and_b32 s66, s66, 1\n\t" "s_lshl_b32 s66, s66, 13\n\t" "s_lshl_b32 s64, s60, 19\n\t" "s_add_u32 s64, s64, s66\n\t" \
    "s_add_u32 s64, s64, %[vb]\n\t" "s_cmp_gt_u32 s61, s60\n\t" "s_cbranch_scc0 34f\n\t" "s_cmp_eq_u32 s60, 64\n\t" "s_cbranch_scc1 34f\n\t" \
    "buffer_load_dwordx4 v[16:19], %[ao], %[rs], s64 offen offset:0 sc1\n\t" "buffer_load_dwordx4 v[20:23], %[ao], %[rs], s64 offen offset:256 sc1\n\t" \
    "buffer_load_dwordx4 v[24:27], %[ao], %[rs], s64 offen offset:2048 sc1\n\t" "buffer_load_dwordx4 v[28:31], %[ao], %[rs], s64 offen offset:2304 sc1\n\t" \
    "buffer_load_dwordx4 v[32:35], %[a1], %[rs], s64 offen offset:0 sc1\n\t" "buffer_load_dwordx4 v[36:39], %[a1], %[rs], s64 offen offset:256 sc1\n\t" \
    "buffer_load_dwordx4 v[40:43], %[a1], %[rs], s64 offen offset:2048 sc1\n\t" "buffer_load_dwordx4 v[44:47], %[a1], %[rs], s64 offen offset:2304 sc1\n\t" \
    "v_mov_b32 v122, s60\n\t" "buffer_store_dword v122, %[fo], %[rc], %[fz] offen sc1\n\t" "s_waitcnt vmcnt(1)\n\t" "s_branch 33b\n\t" "34:\n\t" "v_mov_b32 v122, s60\n\t" \
    "buffer_store_dword v122, %[fo], %[rc], %[fz] offen sc1\n\t" "s_cmp_eq_u32 s60, 64\n\t" "s_cbranch_scc1 90f\n\t" "s_add_u32 s67, s60, 1\n\t" "s_cmp_ge_u32 s61, s67\n\t" \
    "s_cbranch_scc1 32f\n\t" "31:\n\t" "buffer_load_dword v120, %[fo], %[rc], %[fv] offen sc1\n\t" "buffer_load_dword v121, %[fo], %[rc], %[vd] offen sc1\n\t" \
    "s_waitcnt vmcnt(0)\n\t" "v_readfirstlane_b32 s61, v120\n\t" "v_readfirstlane_b32 s66, v121\n\t" "s_nop 3\n\t" "s_cmp_ge_u32 s61, s67\n\t" "s_cbranch_scc1 32f\n\t" \
    "s_bitcmp1_b32 s66, 1\n\t" "s_cbranch_scc1 90f\n\t" "s_cmp_eq_u32 %[bd], 0\n\t" "s_cbranch_scc1 32f\n\t" "s_sub_u32 %[bd], %[bd], 1\n\t" "s_sleep 2\n\t" \
    "s_branch 31b\n\t" "32:\n\t" "s_lshr_b32 s66, s60, 2\n\t" "s_xor_b32 s66, s66, %[xp]\n\t" "s_and_b32 s66, s66, 1\n\t" "s_lshl_b32 s66, s66, 13\n\t" \
    "s_lshl_b32 s64, s60, 19\n\t" "s_add_u32 s64, s64, s66\n\t" "s_add_u32 s64, s64, %[vb]\n\t" "buffer_load_dwordx4 v[16:19], %[ao], %[rs], s64 offen offset:0 sc1\n\t" \
    "buffer_load_dwordx4 v[20:23], %[ao], %[rs], s64 offen offset:256 sc1\n\t" "buffer_load_dwordx4 v[24:27], %[ao], %[rs], s64 offen offset:2048 sc1\n\t" \
    "buffer_load_dwordx4 v[28:31], %[ao], %[rs], s64 offen offset:2304 sc1\n\t" "buffer_load_dwordx4 v[32:35], %[a1], %[rs], s64 offen offset:0 sc1\n\t" \
    "buffer_load_dwordx4 v[36:39], %[a1], %[rs], s64 offen offset:256 sc1\n\t" "buffer_load_dwordx4 v[40:43], %[a1], %[rs], s64 offen offset:2048 sc1\n\t" \
    "buffer_load_dwordx4 v[44:47], %[a1], %[rs], s64 offen offset:2304 sc1\n\t" "s_waitcnt vmcnt(0)\n\t" "s_branch 33b\n\t" "70:\n\t" "s_sub_u32 s67, s60, %[rg]\n\t" \
    "s_cmp_ge_u32 s62, s67\n\t" "s_cbranch_scc1 72f\n\t" "71:\n\t" "buffer_load_dword v120, %[fo], %[rc], %[fs] offen sc1\n\t" \
    "buffer_load_dword v121, %[fo], %[rc], %[vd] offen sc1\n\t" "s_waitcnt vmcnt(0)\n\t" "v_readfirstlane_b32 s62, v120\n\t" "v_readfirstlane_b32 s66, v121\n\t" "s_nop 3\n\t" \
    "s_cmp_ge_u32 s62, s67\n\t" "s_cbranch_scc1 72f\n\t" "s_bitcmp1_b32 s66, 1\n\t" "s_cbranch_scc1 90f\n\t" "s_cmp_eq_u32 %[bd], 0\n\t" "s_cbranch_scc1 72f\n\t" \
    "s_sub_u32 %[bd], %[bd], 1\n\t" "s_sleep 2\n\t" "s_branch 71b\n\t" "72:\n\t" "s_branch 73b\n\t" "90:\n\t"

#define T_DQ 760
#define T_DV 1060
#define T_RQ 1650
#define T_RV 1450
#define T_M1 110
#define T_DY 660
#define T_DX 1150
#define T_MIN 35
#define T_PEN 250
#define T_SAFE 120
#define T_RETRY 900
#define T_POLL 3
#define T_RST 12
#define T_U0 540
constexpr int T_OVH = 118;
constexpr unsigned NS(int ns) { return (unsigned)(ns * 64 / 10); }
struct Clk { unsigned cyc, g, k1, k2; };
DEV void anchor(Clk& k) {
  unsigned long long c, r[8];
  asm volatile("s_memtime %0\n\ts_memrealtime %1\n\ts_nop 2\n\ts_memrealtime %2\n\ts_nop 2\n\ts_memrealtime %3\n\ts_nop 2\n\ts_memrealtime %4\n\ts_nop 2\n\t"
               "s_memrealtime %5\n\ts_nop 2\n\ts_memrealtime %6\n\ts_nop 2\n\ts_memrealtime %7\n\ts_waitcnt lgkmcnt(0)"
               : "=s"(c), "=s"(r[0]), "=s"(r[1]), "=s"(r[2]), "=s"(r[3]), "=s"(r[4]), "=s"(r[5]), "=s"(r[6]) :: "memory");
  const unsigned a = (unsigned)r[0];
  unsigned n = 0;
#pragma unroll
  for (int i = 1; i < 7; ++i) asm("s_cmp_eq_u32 %1, %2\n\ts_addc_u32 %0, %0, 0" : "+s"(n) : "s"((unsigned)r[i]), "s"(a) : "scc");
  const unsigned nx = (a + 4u) << 6, cy = (unsigned)c + 12u + 16u * n;
  const float dc = (float)(cy - k.cyc), dg = (float)(nx - k.g);
  asm volatile("s_nop 0\n\tv_readfirstlane_b32 %0, %2\n\tv_readfirstlane_b32 %1, %3\n\ts_nop 3\n\ts_min_u32 %0, %0, 0x80000\n\ts_min_u32 %1, %1, 0x8000"
               : "=&s"(k.k1), "=&s"(k.k2) : "v"((unsigned)(dg * __builtin_amdgcn_rcpf(dc) * 65536.f)), "v"((unsigned)(dc * __builtin_amdgcn_rcpf(dg) * 65536.f)) : "scc");
  k.cyc = cy; k.g = nx;
}
DEV unsigned to_cyc(const Clk& k, unsigned g) {
  unsigned d;
  asm("s_max_i32 %0, %1, 0xffff8000\n\ts_min_i32 %0, %0, 0x8000\n\ts_mul_i32 %0, %0, %2\n\ts_ashr_i32 %0, %0, 16" : "=&s"(d) : "s"(g - k.g), "s"(k.k2) : "scc");
  return k.cyc + d - T_OVH;
}
DEV void wait_cyc(unsigned t) { asm volatile(TIMED_WAIT :: "s"(t) : CL_S); }
DEV bool slow_head(const u16* q, int h) { return ((((unsigned)(size_t)q >> 8) + h) & 3) == 1; }
DEV int turn(int h, int w) { return ((h >> 3) ^ (w & 1)) & 3; }
DEV void hold(const Ctx& c, int ticks) {
#pragma unroll 1
  while ((unsigned)__builtin_amdgcn_s_memrealtime() - c.t0 < (unsigned)ticks) __builtin_amdgcn_s_sleep(4);
}
DEV int u_slot(int n, int par, int para) { return n == 2 ? R_U2 + par * 8192 : R_U + n * MB + para * 8192; }
DEV unsigned ask_stamps(Ctx& c, int par, int slot) { return __builtin_amdgcn_raw_buffer_load_b32(c.rs, amap(24576, par) + fresh_lane() * 128, slot, AUX_SC1); }
DEV unsigned latest(Ctx& c, const Clk& k, int par, int slot, unsigned v) {
  if (__builtin_expect(!allok(v), 0)) {
#pragma unroll 1
    do {
      __builtin_amdgcn_s_sleep(T_POLL);
      asm volatile("" ::: "memory");
      const unsigned t = __builtin_amdgcn_raw_buffer_load_b32(c.rs, v == SENT ? amap(24576, par) + fresh_lane() * 128 : 1 << 30, slot, AUX_SC1);
      v = v == SENT ? t : v;
    } while (!allok(v) && !spend(c));
  }
  int d = (int)(v - k.g);
  d = max(d, __builtin_amdgcn_update_dpp(d, d, 0x128, 0xf, 0xf, true));
  d = max(d, __builtin_amdgcn_update_dpp(d, d, 0x124, 0xf, 0xf, true));
  d = max(d, __builtin_amdgcn_update_dpp(d, d, 0x122, 0xf, 0xf, true));
  d = max(d, __builtin_amdgcn_update_dpp(d, d, 0x121, 0xf, 0xf, true));
  unsigned m;
  asm("s_max_i32 %0, %1, %2\n\ts_max_i32 %0, %0, %3\n\ts_max_i32 %0, %0, %4" : "=&s"(m)
      : "s"(__builtin_amdgcn_readlane(d, 0)), "s"(__builtin_amdgcn_readlane(d, 16)), "s"(__builtin_amdgcn_readlane(d, 32)), "s"(__builtin_amdgcn_readlane(d, 48)) : "scc");
  return k.g + m;
}
struct Plan { unsigned gx, dx; };
DEV unsigned smax(unsigned a, unsigned b) { return (int)(a - b) > 0 ? a : b; }
DEV unsigned when_p(const Plan& t, unsigned last) {
  const unsigned gy = t.gx + t.dx + NS(T_M1);
  return (int)(last + NS(T_MIN) - gy) > 0 ? smax(last + NS(T_PEN), gy + NS(T_RETRY)) : gy;
}
DEV void advance(Plan& t, unsigned last) {
  const unsigned gy = t.gx + t.dx + NS(T_M1), dur = last - t.gx, gp = when_p(t, last);
  const unsigned nx = gp + NS(T_DY) + (gp != gy ? NS(T_SAFE) : 0u);
  t.dx = dur < t.dx + NS(60) ? (3u * t.dx + dur) >> 2 : t.dx + NS(8);
  t.gx = nx;
}
DEV unsigned next_go(Ctx& c, Clk& k, Plan& t, int par, int slot, unsigned v) {
  SCHED();
  anchor(k);
  SCHED();
  advance(t, latest(c, k, par, slot, v));
  return to_cyc(k, t.gx);
}

DEV constexpr int qk_r(int j) { return j < 4 ? 0 : 1 + (j - 4) % 3; }
DEV constexpr int qk_i(int j) { return j < 4 ? j : (j - 4) / 3; }
DEV constexpr int qk_upto(int u) { return u < 6 ? u + 7 : u < 10 ? 12 + (u - 4) / 2 : u < 12 ? 15 : 16; }
DEV void qk_role(const Params& p, Ctx& c, int h) {
  constexpr int WL = 0, MEMS = 131072, KXL = 147456, QKX = 155648, QROW = 272;
  c.budget = SPIN_CAP;
  const int w = c.w;
  Clk k = {0u, 0u, 0u, 0u};
  anchor(k);
  v8u wa[32], wh[16];
  claim(c, 0);
  hold(c, QK_AT - (slow_head(p.Wq, h) ? SLOW_LEAD : 0));
  {
    const int lane = fresh_lane(), g = lane >> 4, cc = lane & 15;
    const rsrc_t rq = mkrs(p.Wq, 1024 * 4096 * 2), rk = mkrs(p.Wk, 1024 * 4096 * 2);
    int vo[4];
#pragma unroll
    for (int i = 0; i < 4; ++i) vo[i] = opq((256 * w + 64 * (i ^ turn(h, w)) + krow_lane(g)) * 8192 + (h * 128 + 8 * cc) * 2);
    v4u row[16][8];
    auto request = [&](int u) {
#pragma unroll
      for (int r = 0; r < 8; ++r) row[u][r] = ld16w(u < 8 ? rq : rk, vo[(u >> 1) & 3], krow(4 * (u & 1) + (r >> 1), r & 1) * 8192);
    };
#pragma unroll
    for (int u = 0; u < qk_upto(-1); ++u) request(u);
    SCHED();
    const int sw = MEMS + w * 8192 + g * 256, lw = opq(WL + w * 32768 + lane * 16);
    int sr[2];
#pragma unroll
    for (int t = 0; t < 2; ++t) { const int i = (2 * cc + t) & 7; sr[t] = opq(i * 1024 + g * 256 + ((4 * w + (cc >> 2) + 2 * i) & 15) * 16); }
    sfor<16>([&](auto U) __attribute__((always_inline)) {
      constexpr int u = U.value;
#pragma unroll
      for (int i = 0; i < 8; ++i) {
        v4u f;
#pragma unroll
        for (int r = 0; r < 4; ++r) f[r] = prm(row[u][2 * r + 1][i >> 1], row[u][2 * r][i >> 1], i & 1);
        sts16(c.L, sw + i * 1024 + ((cc + 2 * i) & 15) * 16, f);
      }
      SCHED();
      if (qk_upto(u) > qk_upto(u - 1)) request(qk_upto(u - 1));
      BAR();
      v4u in[4][2];
#pragma unroll
      for (int t = 1; t >= 0; --t)
#pragma unroll
        for (int r = 0; r < 4; ++r) in[r][t] = lds16(c.L, sr[t] + MEMS + ((w + r) & 3) * 8192);
      SCHED();
#pragma unroll
      for (int j = 0; j < 16; ++j) {
        if (qk_i(j) != ((u >> 1) & 3)) continue;
#pragma unroll
        for (int t = 1; t >= 0; --t) {
          const v4u o = in[qk_r(j)][t];
          if (u >= 8 && t == 1) sts16(c.L, lw + j * 2048 + (u & 1) * 1024, o);
          else {
            v8u& d = u < 8 ? wa[2 * j + t] : wh[j];
#pragma unroll
            for (int r = 0; r < 4; ++r) d[4 * (u & 1) + r] = o[r];
#define M_(r) asm volatile("" : "+" r(d))
            if (u & 1) { if (u < 8) AT_ACC(M_, 2 * j + t); else AT_HOME(M_, j); }
#undef M_
          }
        }
      }
      BAR();
    });
  }
  STAMP(0);
  BAR();
  const unsigned vote = wait_verdict(c);
  const int par = vote_b(vote), para = vote_a(vote);
  const int lane = fresh_lane(), g = lane >> 4, cc = lane & 15;
  const int moff = opq(amap(w * 4096 + lane * 16, par) + (turn(h, w) & 1) * 1024), m2 = R_MEM + (turn(h, w) & 2) * 1024;
  unsigned tries = 1u << 12;
  const int idx = opq((lane & 1) ? 0xEEEE : 0x4444);
  TICK_DECL;
  const int l0 = opq(WL + w * 32768 + lane * 16), l1 = opq(MEMS + lane * 16);
  const int qw = opq(QKX + (2 * w + (cc >> 3)) * 256 + g * 64 + (cc & 1) * 16 + ((cc >> 1) & 3) * 4), kw = opq(QKX + (8 + 2 * g) * QROW + (32 * w + 2 * cc) * 2);
#define QK_PINS PIN_ACC0(wa); PIN_ACC1(wa); PIN_HOME(wh)
#define QK_IN [l0] "v"(l0), [l1] "v"(l1), [mo] "v"(moff), [ix] "v"(idx), [qw] "v"(qw), [kw] "v"(kw), [rs] "s"(c.rs), [sb] "s"(m2 + (s & 3) * MEM_SLOT)
#define QK_CL CL_S, CL_16_32, CL_160_200, CL_200_216, CL_216_256
  if (w == 3) {
    const int hq = opq(lane ? 1 << 30 : amap(24576 + h * 128, par));
    anchor(k);
    unsigned tg = k.cyc;
    Plan tt = {k.g - NS(100000), NS(T_DX)};
    for (int s = 0; s < NSEG; ++s) {
      const int pb = R_P + (s & 3) * P_SLOT;
      QK_PINS;
      asm volatile(QK_STEP3 : [bd] "+s"(c.budget), [rt] "+s"(tries) : QK_IN, [hq] "v"(hq), [pb] "s"(pb), [tg] "s"(tg), [ca] "s"(k.cyc - T_DQ), [k1] "s"(k.k1), [ga] "s"(k.g), [dr] "n"(T_RQ) : QK_CL);
      QK_PINS;
      __builtin_amdgcn_s_sleep(3);
      tg = next_go(c, k, tt, par, pb, ask_stamps(c, par, pb));
      st4s(c.rs, hq, R_P + ((s + 2) & 3) * P_SLOT, fresh_sent());
      st4s(mkrs((const void*)c.ctrl, CTRL_BYTES), hq & (1 << 30), C_FLAG + (F_S + h) * 128, (unsigned)s + 1u);
      TICK_SUM(10);
    }
  } else {
    const int kx0 = opq(KXL + g * 512 + cc * 16), kmrd = opq(QKX + g * 16 + (8 + (cc & 7)) * QROW);
    unsigned seen = NOFLAG;
    if (w == 0) {
      const int prow = (2 * g + (cc & 1)) * 2 + ((cc >> 1) & 1);
      const int px = opq(amap((h >> 1) * 1024 + ((2 * (h & 1) + (cc >> 3)) * 16 + prow) * 16 + ((cc >> 2) & 1) * 8, par));
      const int pm = opq(cc < 8 ? amap(16384 + h * 128 + prow * 8 + (cc >> 2) * 4, par) : 1 << 30);
      const int ps = opq((cc & 1) ? 0x03020706 : 0x05040100);
      for (int s = 0; s < NSEG; ++s) {
        if (seen < (unsigned)s + 1u) seen = wait_flag(c, F_K + h, s + 1);
        QK_PINS;
        asm volatile(QK_STEP0 : [bd] "+s"(c.budget), [rt] "+s"(tries)
                     : QK_IN, [kx] "v"(kx0), [km] "v"(kmrd), [px] "v"(px), [pm] "v"(pm), [ps] "v"(ps), [pb] "s"(R_P + (s & 3) * P_SLOT), [pr] "s"(R_P + ((s + 1) & 3) * P_SLOT),
                       [kb] "s"(R_KX - MEMS + xblk(s, h, para)) : QK_CL);
        QK_PINS;
        TICK_SUM(10);
      }
    } else {
      const int qt = w - 1;
      const int px = opq(amap(qt ? 16384 + h * 1024 + lane * 16 : h * 512 + (g >> 1) * 256 + cc * 16 + (g & 1) * 8, para));
      const int pm = opq(g < 2 ? amap(49152 + (qt * 32 + h) * 256 + cc * 16 + g * 8, para) : 1 << 30);
      const int ps = opq(cc - 4 * g), hq = opq(g * 512 + (16 * qt + cc) * 16);
#define QK_TOK(STEP) for (int s = 0; s < NSEG; ++s) { QK_PINS; \
        asm volatile(STEP : [bd] "+s"(c.budget), [rt] "+s"(tries), [sq] "+s"(seen) \
                     : QK_IN, [kx] "v"(kx0), [km] "v"(kmrd), [px] "v"(px), [pm] "v"(pm), [ps] "v"(ps), [hq] "v"(hq), [qb] "s"(R_QX + xblk(s, h, para)), [tb] "s"(R_PT + (s & 7) * PT_SLOT), \
                       [tr] "s"(R_PT + ((s + 3) & 7) * PT_SLOT), [sn] "s"(s + 1), [fq] "s"(c.ctrl + (C_FLAG + (F_Q + h) * 128) / 4) : QK_CL); \
        QK_PINS; TICK_SUM(10); }
      if (w == 1) { QK_TOK(QK_STEP1) } else { QK_TOK(QK_STEP2) }
    }
  }
  TICK_OUT();
  if (c.budget == 0u) __hip_atomic_store(c.ctrl + C_TMO / 4, 1u, RLX_AGENT);
}

DEV void shuffle_cols(Ctx& c, int scratch, const v4u (&rows)[8], v4u (&o)[8]) {
  const int g = c.lane >> 4, cc = c.lane & 15;
#pragma unroll
  for (int i = 0; i < 8; ++i) {
    v4u f;
#pragma unroll
    for (int r = 0; r < 4; ++r) f[r] = prm(rows[2 * r + 1][i >> 1], rows[2 * r][i >> 1], i & 1);
    sts16(c.L, scratch + (8 * cc + i) * 64 + ((g ^ (cc & 3)) << 4), f);
  }
  asm volatile("s_waitcnt lgkmcnt(0)" ::: "memory");
#pragma unroll
  for (int i = 0; i < 8; ++i) { const int col = 16 * i + cc; o[i] = lds16(c.L, scratch + col * 64 + ((g ^ ((col >> 3) & 3)) << 4)); }
  asm volatile("s_waitcnt lgkmcnt(0)" ::: "memory");
}

DEV void vo_role(const Params& p, Ctx& c, int h) {
  constexpr int WL = 0, MEMS = 131072, VM = 147456, VROW = 272;
  c.budget = SPIN_CAP;
  const int w = c.w, lane = c.lane = fresh_lane(), g = lane >> 4, cc = lane & 15;
  Clk k = {0u, 0u, 0u, 0u};
  anchor(k);
  v8u wa[32], wh[16];
  claim(c, 0);
  hold(c, VO_AT);
  const rsrc_t rwo = mkrs(p.Wo, 4096 * 1024 * 2);
  const int voo = (h * 128 + 4 * g) * 2048 + (256 * w + 8 * cc) * 2;
  v4u buf[3][8];
  auto request_o = [&](int u) {
#pragma unroll
    for (int r = 0; r < 8; ++r) buf[u % 3][r] = ld16w(rwo, voo, (32 * (u & 3) + 16 * (r >> 2) + (r & 3)) * 2048 + (u >> 2) * 256);
  };
  {
    const rsrc_t rw = mkrs(p.Wv, 1024 * 4096 * 2);
    const int vo = (256 * w + krow_lane(g)) * 8192 + (h * 128 + 8 * cc) * 2;
    v4u vrow[8][8];
    auto request = [&](int u) {
#pragma unroll
      for (int r = 0; r < 8; ++r) vrow[u][r] = ld16w(rw, vo, (64 * ((u >> 1) ^ turn(h, w)) + krow(4 * (u & 1) + (r >> 1), r & 1)) * 8192);
    };
#pragma unroll
    for (int u = 0; u < 5; ++u) request(u);
    SCHED();
    const int wr_ = MEMS + w * 8192 + g * 256;
    int sr[2];
#pragma unroll
    for (int t = 0; t < 2; ++t) { const int i = (2 * cc + t) & 7; sr[t] = opq(i * 1024 + g * 256 + ((4 * w + (cc >> 2) + 2 * i) & 15) * 16); }
#pragma unroll
    for (int u = 0; u < 8; ++u) {
#pragma unroll
      for (int i = 0; i < 8; ++i) {
        v4u f;
#pragma unroll
        for (int r = 0; r < 4; ++r) f[r] = prm(vrow[u][2 * r + 1][i >> 1], vrow[u][2 * r][i >> 1], i & 1);
        sts16(c.L, wr_ + i * 1024 + ((cc + 2 * i) & 15) * 16, f);
      }
      SCHED();
      if (u + 5 < 8) request(u + 5); else if (u < 6) request_o(u - 3);
      BAR();
      v4u in[4][2];
#pragma unroll
      for (int t = 1; t >= 0; --t)
#pragma unroll
        for (int r = 0; r < 4; ++r) in[r][t] = lds16(c.L, sr[t] + MEMS + ((w + r) & 3) * 8192);
      SCHED();
#pragma unroll
      for (int j = 0; j < 16; ++j) {
        if (qk_i(j) != (u >> 1)) continue;
#pragma unroll
        for (int t = 1; t >= 0; --t) {
          const v4u o = in[qk_r(j)][t];
          if (t == 1 && (j & 1)) sts16(c.L, WL + (w * 16 + (j >> 1)) * 2048 + (u & 1) * 1024 + lane * 16, o);
          else {
            const int n = 3 * (j >> 1) + ((j & 1) ? 2 : t);
#pragma unroll
            for (int r = 0; r < 4; ++r) wa[n][4 * (u & 1) + r] = o[r];
#define M_(r) asm volatile("" : "+" r(wa[n]))
            if (u & 1) AT_ACC(M_, n);
#undef M_
          }
        }
      }
      BAR();
    }
  }
#pragma unroll
  for (int u = 0; u < 8; ++u) {
    v4u o[8];
    shuffle_cols(c, MEMS + w * 8192, buf[u % 3], o);
    SCHED();
    if (u + 3 < 8) request_o(u + 3);
#pragma unroll
    for (int i = 0; i < 4; ++i) {
      const int pr = 4 * (u >> 2) + i, ch = u & 3;
      const v8u f = {o[2 * i][0], o[2 * i + 1][0], o[2 * i][1], o[2 * i + 1][1], o[2 * i][2], o[2 * i + 1][2], o[2 * i][3], o[2 * i + 1][3]};
      if (ch < 2) {
        wh[2 * pr + ch] = f;
#define M_(r) asm volatile("" : "+" r(wh[2 * pr + ch]))
        AT_HOME(M_, 2 * pr + ch);
#undef M_
      } else if (ch == 2) {
        wa[24 + pr] = f;
#define M_(r) asm volatile("" : "+" r(wa[24 + pr]))
        AT_ACC(M_, 24 + pr);
#undef M_
      } else {
        sts16(c.L, WL + (w * 16 + 8 + pr) * 2048 + lane * 16, lo4(f));
        sts16(c.L, WL + (w * 16 + 8 + pr) * 2048 + 1024 + lane * 16, hi4(f));
      }
    }
    SCHED();
  }
  BAR();
  STAMP(0);
  const unsigned vote = wait_verdict(c);
  const int par = vote_b(vote), para = vote_a(vote);
  const int moff = opq(amap(w * 4096 + lane * 16, par) + (turn(h, w) & 1) * 1024), m2 = R_MEM + (turn(h, w) & 2) * 1024;
  unsigned tries = 1u << 12;
  const int idx = opq((lane & 1) ? 0xEEEE : 0x4444);
  const int u_off = opq((16 * w + g) * 16384 + h * 256 + cc * 16);
  const int hq = opq((lane | w) ? 1 << 30 : amap(24576 + (32 + h) * 128, par));
  const int l0 = opq(WL + w * 32768 + lane * 16), l1 = opq(MEMS + lane * 16);
  const int vmw = opq(VM + 2 * g * VROW + (32 * w + 2 * cc) * 2);
  const int vmr = opq(VM + (cc >> 1) * VROW + g * 16);
  TICK_DECL;
  anchor(k);
  unsigned tg = k.cyc;
  Plan tt = {k.g - NS(100000), NS(T_DX)};
  int u3 = 0;
  for (int s = 0; s < NSEG; ++s) {
    const int pb = R_P + (s & 3) * P_SLOT;
    PIN_ACC0(wa); PIN_ACC1(wa); PIN_HOME(wh);
    asm volatile(VO_STEP
        : [bd] "+s"(c.budget), [rt] "+s"(tries)
        : [l0] "v"(l0), [l1] "v"(l1), [mo] "v"(moff), [ix] "v"(idx), [vw] "v"(vmw), [vr] "v"(vmr), [uo] "v"(u_off), [w] "s"(w), [rs] "s"(c.rs),
          [sb] "s"(m2 + (s & 3) * MEM_SLOT), [ub] "s"(u_slot(u3, par, para)), [tg] "s"(tg), [hq] "v"(hq), [pb] "s"(pb),
          [ca] "s"(k.cyc - T_DV), [k1] "s"(k.k1), [ga] "s"(k.g), [dr] "n"(T_RV)
        : CL_S, CL_16_32, CL_160_200, CL_200_216, CL_216_256);
    PIN_ACC0(wa); PIN_ACC1(wa); PIN_HOME(wh);
    if (w == 0) tg = next_go(c, k, tt, par, pb, ask_stamps(c, par, pb)); else __builtin_amdgcn_s_sleep(T_RST);
    {
      const int ur = u_slot(u3 ? u3 - 1 : 2, par, para);
      const v4u se = {fresh_sent(), fresh_sent(), fresh_sent(), fresh_sent()};
#pragma unroll
      for (int i = 0; i < 4; ++i) st16s(c.rs, u_off, ur + i * 65536, se);
      st4s(c.rs, hq, R_P + ((s + 2) & 3) * P_SLOT, se[0]);
      u3 = u3 == 2 ? 0 : u3 + 1;
    }
    TICK_SUM(10);
  }
  TICK_OUT();
  if (c.budget == 0u) __hip_atomic_store(c.ctrl + C_TMO / 4, 1u, RLX_AGENT);
}

DEV void y_role(const Params& p, Ctx& c, int y) {
  constexpr int ZR = 0, UL = 131072, CW = 155648;
  c.budget = SPIN_CAP;
  const int w = c.w, lane = c.lane = fresh_lane(), g = lane >> 4, cc = lane & 15;
  const v4u sent4 = {SENT, SENT, SENT, SENT};
  STAMP(2);
  Clk k = {0u, 0u, 0u, 0u};
  anchor(k);
  unsigned m0 = 0;
  if (w == 0) {
    unsigned best[2] = {~0u, ~0u};
    st4s(c.rs, y * 16384 + (lane >> 5) * 8192 + (lane & 31) * 4, R_U, SENT);
#pragma unroll 1
    for (int n = 0; n < 12; ++n) {
#pragma unroll
      for (int r = 0; r < 2; ++r) {
        VMWAIT(0);
        const unsigned t0 = (unsigned)__builtin_amdgcn_s_memtime();
        LGKM(0);
        st4s(c.rs, y * 16384 + (lane & 31) * 4, R_U + r * 8192, SENT);
        VMWAIT(0);
        const unsigned t1 = (unsigned)__builtin_amdgcn_s_memtime();
        LGKM(0);
        best[r] = min(best[r], t1 - t0);
      }
      if (n + 1 >= PROBES && min(best[0], best[1]) < 600u && max(best[0], best[1]) > min(best[0], best[1]) + 128u) break;
    }
    STAMP(5);
    claim(c, CL_VOTED | (best[1] < best[0] ? CL_A : 0u));
    m0 = *(const GAS unsigned*)(p.mem0 + (lane >> 3) * 1024 + 16 * y + 2 * (lane & 7));
    m0 = m0 < SENT - 1u ? m0 : SENT - 1u;
    if (lane < 8) *(LAS unsigned*)(c.L + CW + lane * 4) = 0u;
    const unsigned v = wait_vote(c);
    STAMP(7);
    const int oa = y * 16384 + vote_a(v) * 8192 + lane * 16, ob = amap(y * 3072 + lane * 16, vote_b(v));
#pragma unroll
    for (int i = 0; i < 16; ++i) st16s(c.rs, oa + (i & 3) * 1024, R_U + (i >> 3) * MB + ((i >> 2) & 1) * 4096, sent4);
#pragma unroll
    for (int i = 0; i < 3; ++i) st16s(c.rs, amap(y * 3072 + i * 1024 + lane * 16, vote_b(v)), R_MEM, sent4);
#pragma unroll
    for (int i = 0; i < 8; ++i) st16s(c.rs, oa + (i & 3) * 1024, R_PT + (i >> 2) * 4096, sent4);
    (void)ob;
    VMWAIT(0);
    if (lane == 0) __hip_atomic_fetch_add(c.ctrl + (C_CLAIM + c.att * 1024) / 4 + c.slot, CL_FILLED, RLX_AGENT);
    STAMP(6);
  }
  BAR();
  const unsigned vote = wait_verdict(c);
  const int par = vote_b(vote), para = vote_a(vote);
  const int mo = amap(y * 256 + lane * 4, par);
  if (w == 0) st4s(c.rs, mo, R_MEM, m0);
  STAMP(0);
  volatile LAS unsigned* const GO = (volatile LAS unsigned*)(c.L + CW);
  TICK_DECL;
  if (w == 0) {
    const int rot = y >> 4;
    int xo[4];
#pragma unroll
    for (int i = 0; i < 4; ++i) xo[i] = amap((i ^ rot) * 4096 + lane * 16, par);
    const int pmo = amap(16384 + rot * 1024 + 2 * g * 128 + cc * 8, par), psel = (cc & 1) ? 0x03020706 : 0x05040100;
    const int lead = T_U0 - 128 * ((y >> 2) & 3), late = __builtin_amdgcn_readfirstlane(opq(lead < 264));
    anchor(k);
    Plan tt = {k.g - NS(100000), NS(T_DX)};
    unsigned done = k.cyc;
    const int so = amap(24576, par) + lane * 128;
    const int idx = opq((lane & 1) ? 0xEEEE : 0x4444);
    const int uo = y * 16384 + lane * 16;
    int u3 = 0;
    const int mw = amap(y * 256 + (2 * g + (cc & 1)) * 32 + (cc >> 1) * 4, par);
    for (int s = 0; s < NSEG - 1; ++s) {
      if (s == 0) RAWBAR();
      const int za = ZR + (s & 3) * 32768 + rot * 8192 + lane * 16;
      v32u z0, z1, z2, z3;
      asm volatile(
          "ds_read_b128 a[0:3], %[z0] offset:0\n\t" "ds_read_b128 a[4:7], %[z0] offset:1024\n\t" "ds_read_b128 a[8:11], %[z0] offset:2048\n\t" "ds_read_b128 a[12:15], %[z0] offset:3072\n\t"
          "ds_read_b128 a[16:19], %[z0] offset:4096\n\t" "ds_read_b128 a[20:23], %[z0] offset:5120\n\t" "ds_read_b128 a[24:27], %[z0] offset:6144\n\t" "ds_read_b128 a[28:31], %[z0] offset:7168\n\t"
          "ds_read_b128 a[32:35], %[z1] offset:0\n\t" "ds_read_b128 a[36:39], %[z1] offset:1024\n\t" "ds_read_b128 a[40:43], %[z1] offset:2048\n\t" "ds_read_b128 a[44:47], %[z1] offset:3072\n\t"
          "ds_read_b128 a[48:51], %[z1] offset:4096\n\t" "ds_read_b128 a[52:55], %[z1] offset:5120\n\t" "ds_read_b128 a[56:59], %[z1] offset:6144\n\t" "ds_read_b128 a[60:63], %[z1] offset:7168\n\t"
          "ds_read_b128 a[64:67], %[z2] offset:0\n\t" "ds_read_b128 a[68:71], %[z2] offset:1024\n\t" "ds_read_b128 a[72:75], %[z2] offset:2048\n\t" "ds_read_b128 a[76:79], %[z2] offset:3072\n\t"
          "ds_read_b128 a[80:83], %[z2] offset:4096\n\t" "ds_read_b128 a[84:87], %[z2] offset:5120\n\t" "ds_read_b128 a[88:91], %[z2] offset:6144\n\t" "ds_read_b128 a[92:95], %[z2] offset:7168\n\t"
          "ds_read_b128 a[96:99], %[z3] offset:0\n\t" "ds_read_b128 a[100:103], %[z3] offset:1024\n\t" "ds_read_b128 a[104:107], %[z3] offset:2048\n\t" "ds_read_b128 a[108:111], %[z3] offset:3072\n\t"
          "ds_read_b128 a[112:115], %[z3] offset:4096\n\t" "ds_read_b128 a[116:119], %[z3] offset:5120\n\t" "ds_read_b128 a[120:123], %[z3] offset:6144\n\t" "ds_read_b128 a[124:127], %[z3] offset:7168\n\t"
          "s_waitcnt lgkmcnt(0)"
          : "={a[0:31]}"(z0), "={a[32:63]}"(z1), "={a[64:95]}"(z2), "={a[96:127]}"(z3) : [z0] "v"(za), [z1] "v"(za ^ 8192), [z2] "v"(za ^ 16384), [z3] "v"(za ^ 24576) : "memory");
      const int pb = R_P + (s & 3) * P_SLOT;
      v32u uu;
      const int ub = u_slot(u3, par, para);
      TICK(0);
      anchor(k);
      TICK(8);
      unsigned tg = to_cyc(k, tt.gx + tt.dx + NS(T_M1)), lt = late, sv = SENT;
      unsigned bad = (int)(done - to_cyc(k, tt.gx)) > T_OVH;
      TICK(4);
#pragma unroll 1
      for (;;) {
        if (bad) {
          const bool all = allok(sv);
          const unsigned last = latest(c, k, par, pb, sv);
          if (!all) anchor(k);
          tg = to_cyc(k, when_p(tt, last));
          lt = 1;
        }
        asm volatile(Y_STEP
          : "=&{v[144:175]}"(uu), [bad] "=&s"(bad), [sv] "=&v"(sv)
          : [x0] "v"(xo[0]), [x1] "v"(xo[1]), [x2] "v"(xo[2]), [x3] "v"(xo[3]), [pm] "v"(pmo), [u0] "v"(uo + rot * 2048), [mw] "v"(mw), [ps] "v"(psel), [ix] "v"(idx),
            [rs] "s"(c.rs), [pb] "s"(pb), [ub] "s"(ub), [mb] "s"(R_MEM + ((s + 1) & 3) * MEM_SLOT), [mr] "s"(R_MEM + ((s + 3) & 3) * MEM_SLOT), [tu] "s"(tg - (lt ? 128 : lead)), [tg] "s"(tg), [late] "s"(lt),
            [so] "v"(so), [bd] "s"(c.budget),
            "{a[0:31]}"(z0), "{a[32:63]}"(z1), "{a[64:95]}"(z2), "{a[96:127]}"(z3)
          : CL_S, CL_120_128, CL_176_200, CL_200_216, CL_216_256);
        bad = __builtin_amdgcn_readfirstlane(bad);
        if (bad != 1u) break;
        spend(c);
      }
      done = bad;
      SCHED();
      const int ul = opq(UL + rot * 2048 + lane * 16);
      u3 = u3 == 2 ? 0 : u3 + 1;
#pragma unroll
      for (int i = 0; i < 8; ++i) sts16(c.L, (ul ^ ((i >> 1) * 2048)) + (i & 1) * 1024, v4u{uu[4 * i], uu[4 * i + 1], uu[4 * i + 2], uu[4 * i + 3]});
      asm volatile("" ::: "memory");
      *GO = s + 1;
      asm volatile("s_waitcnt vmcnt(2)" : "+v"(sv));
      advance(tt, latest(c, k, par, pb, sv));
      TICK(3);
      TICK_LDS(CW + 256);
    }
    {
      anchor(k);
      wait_cyc(to_cyc(k, tt.gx + tt.dx + NS(T_M1)));
      const int ub = u_slot(u3, par, para);
      v4u u[8];
#pragma unroll 1
      for (;;) {
        asm volatile("" ::: "memory");
#pragma unroll
        for (int i = 0; i < 8; ++i) u[i] = ld16s(c.rs, uo + (i & 3) * 1024, ub + (i >> 2) * 4096);
        SCHED();
        unsigned t = 0;
#pragma unroll
        for (int i = 0; i < 8; ++i) t = umax(t, top(u[i]));
        if (allok(t) || spend(c)) break;
      }
      RAWBAR();
#pragma unroll
      for (int i = 0; i < 8; ++i) sts16(c.L, UL + i * 1024 + lane * 16, u[i]);
      asm volatile("" ::: "memory");
      *GO = NSEG;
    }
    TICK_DUMP(CW + 256);
  } else if (w < 3) {
    const rsrc_t ro = mkrs(p.out, 2048 * 2048 * 2);
    auto tokens = [&](auto QT) {
      constexpr int qt = QT.value, NX = qt ? 32 : 16;
      RAWBAR();
      for (int s = 0; s < NSEG; ++s) {
        const int tb = R_PT + (s & 7) * PT_SLOT + para * 8192;
        const int ua = opq(UL + lane * 16);
        const int za = opq(ZR + (s & 3) * 32768 + (qt ? lane * 16 : (g >> 1) * 1024 + (g & 1) * 512 + cc * 16));
        SCHED();
        RAWBAR();
        TICK(0);
        f32x4 a;
#pragma unroll 1
        for (;;) {
          asm volatile("" ::: "memory");
          v4u pt[NX + 8];
#pragma unroll
          for (int i = 0; i < NX; ++i) pt[i] = ld16s(c.rs, (i & 3) * 1024 + lane * 16, tb + amap(qt * 16384 + (i >> 2) * 4096, 0));
#pragma unroll
          for (int i = 0; i < 8; ++i) pt[NX + i] = ld16s(c.rs, (i & 3) * 1024 + lane * 16, tb + amap(49152 + qt * 8192 + (i >> 2) * 4096, 0));
          SCHED();
          TICK(1);
#pragma unroll 1
          while (__builtin_amdgcn_readfirstlane(*GO) < (unsigned)s + 1u) { if (spend(c)) break; }
          asm volatile("" ::: "memory");
          f32x4 d[2] = {f32x4{0.f, 0.f, 0.f, 0.f}, f32x4{0.f, 0.f, 0.f, 0.f}};
          v4u fa[8];
          auto frag = [&](int i) {
            if (i >= NX) fa[i & 7] = lds16(c.L, ua + (i - NX) * 1024);
            else if (qt) fa[i & 7] = lds16(c.L, za + i * 1024);
            else {
              const v2u lo = *(LAS v2u*)(c.L + za + i * 2048), hi = *(LAS v2u*)(c.L + za + i * 2048 + 256);
              fa[i & 7] = v4u{lo.x, lo.y, hi.x, hi.y};
            }
          };
#pragma unroll
          for (int i = 0; i < 7; ++i) frag(i);
          SCHED();
#pragma unroll
          for (int i = 0; i < NX + 8; ++i) {
            if (i + 7 < NX + 8) frag(i + 7);
            d[i & 1] = mfma16(fa[i & 7], pt[i], d[i & 1]);
            SCHED();
          }
          a = d[0] + d[1];
          if (!__any(a[0] != a[0])) break;
          unsigned t = 0;
#pragma unroll
          for (int i = 0; i < NX + 8; ++i) t = umax(t, top(pt[i]));
          if (allok(t) || spend(c)) break;
        }
        __builtin_amdgcn_raw_buffer_store_b64(v2u{pk(a[0], a[1]), pk(a[2], a[3])}, ro, (16 * qt + cc) * 4096 + (16 * y + 4 * g) * 2, s * 32 * 4096, AUX_NT);
        TICK(2);
        TICK_SUM(3);
      }
    };
    if (w == 1) tokens(IC<0>{}); else tokens(IC<1>{});
  } else {
    unsigned seenZ = NOFLAG;
    const rsrc_t rc = mkrs((const void*)c.ctrl, CTRL_BYTES);
    const int fo = lane < 32 ? lane * 128 : 1 << 30;
    __builtin_amdgcn_s_setprio(3);
    auto least = [&](unsigned v) {
      v |= lane < 32 ? 0u : ~0u;
#pragma unroll
      for (int o = 1; o < 32; o <<= 1) v = min(v, (unsigned)__shfl_xor((int)v, o));
      seenZ = umax(seenZ, __builtin_amdgcn_readfirstlane(v));
    };
    auto await = [&](unsigned need) {
#pragma unroll 1
      while (seenZ < need) {
        asm volatile("" ::: "memory");
        least(__builtin_amdgcn_raw_buffer_load_b32(rc, fo, C_FLAG + (F_Z + (y >> 4) * 32) * 128, AUX_SC1));
        if (seenZ >= need || spend(c)) break;
        __builtin_amdgcn_s_sleep(4);
      }
      __builtin_amdgcn_fence(__ATOMIC_ACQUIRE, "wavefront");
    };
    for (int n = 0; n < NSEG; ++n) {
      TICK(0);
      await(n + 1);
      TICK(1);
      v4u z[32];
#pragma unroll
      for (int i = 0; i < 32; ++i) {
        z[i] = ld16s(c.rs, (i & 3) * 1024 + lane * 16, R_Z + (n & (ZRING - 1)) * Z_SLOT + y * 65536 + ((para ^ (y >> 5)) & 1) * 8192 + amap((i >> 2) * 4096, 0));
        SCHED();
        asm volatile("s_nop " STR(Z_GAP));
        SCHED();
      }
      const unsigned look = __builtin_amdgcn_raw_buffer_load_b32(rc, seenZ < (unsigned)n + 2u ? fo : 1 << 30, C_FLAG + (F_Z + (y >> 4) * 32) * 128, AUX_SC1);
      SCHED();
      TICK(4);
      const int za = opq(ZR + (n & 3) * 32768 + lane * 16);
#pragma unroll
      for (int i = 0; i < 32; ++i) {
        sts16(c.L, za + i * 1024, z[i]);
        if (i == 30) { TICK(3); }
      }
      least(look);
      if (n == 0) await(PRIME);
      TICK(2);
      { const int s = n; (void)s; TICK_SUM(3); }
      BAR();
    }
    RAWBAR();
  }
  TICK_OUT();
  if (c.budget == 0u) __hip_atomic_store(c.ctrl + C_TMO / 4, 1u, RLX_AGENT);
}

#define MF1A(d, a, b) asm volatile(MF16 "%0, %1, %2, %0" : "+v"(d) : "a"(a), "v"(b))
#define MF32A(d, a, b) asm volatile("v_mfma_f32_32x32x16_bf16 %0, %1, %2, %0" : "+v"(d) : "a"(a), "v"(b))
#define MF32AZ(d, a, b) asm volatile("v_mfma_f32_32x32x16_bf16 %0, %1, %2, 0" : "=&v"(d) : "a"(a), "v"(b))
#define MF1B(d, a, b) asm volatile(MF16 "%0, %1, %2, %0" : "+v"(d) : "v"(a), "a"(b))
#define MF1BZ(d, a, b) asm volatile(MF16 "%0, %1, %2, 0" : "=&v"(d) : "v"(a), "a"(b))
#define MF1AZ(d, a, b) asm volatile(MF16 "%0, %1, %2, 0" : "=&v"(d) : "a"(a), "v"(b))
DEV void g_role(const Params& p, Ctx& c, int mat, int h, int gi) {
  constexpr int EX = 131072;
  c.budget = SPIN_CAP;
  const int w = c.w, lane = c.lane = fresh_lane(), g = lane >> 4, cc = lane & 15;
  if (mat < 2) {
    unsigned t = 0;
#pragma unroll
    for (int i = 0; i < 3; ++i) t += __builtin_amdgcn_raw_buffer_load_b32(mkrs(p.Wv, 8 * MB), (256 * w + (lane >> 1)) * 8192 + h * 256 + (lane & 1) * 128, (2 + 2 * i + mat) * 32 * 8192, 0);
    if (mat == 0) {
      hold(c, X_AT);
#pragma unroll
      for (int i = 0; i < 4; ++i) t += __builtin_amdgcn_raw_buffer_load_b32(mkrs(p.x, 4 * MB), ((4 * h + w) * 64 + lane) * 128, i * MB, 0);
    }
    asm volatile("" :: "v"(t));
    claim(c, 0);
    hold(c, (mat ? GK_AT : GQ_AT) - (slow_head(p.Wq, h) ? SLOW_LEAD : 0));
  }
  const rsrc_t rx = mkrs(p.x, 2048 * 2048);
  const int xs = w * 16384 + ((lane * 16) ^ ((w & 1) << 7));

  v32u wa[8];
  {
    const rsrc_t rw = mkrs(mat == 0 ? p.Wq : mat == 1 ? p.Wk : p.Wv, 1024 * 4096 * 2);
    const int vo = (256 * w + 8 * g) * 8192 + (h * 128 + 8 * cc) * 2;
    v4u row[8][8];
    auto request = [&](int u) {
#pragma unroll
      for (int r = 0; r < 8; ++r) row[u][r] = ld16w(rw, vo, (32 * u + r) * 8192);
    };
#pragma unroll
    for (int u = 0; u < 2; ++u) request(u);
#pragma unroll
    for (int u = 0; u < 16; ++u) dma16(rx, c.L + w * 16384 + u * 1024, (xs ^ ((u >> 1) << 4)) + u * 1024);
#pragma unroll
    for (int u = 2; u < 6; ++u) request(u);
    SCHED();
    const int sw_ = EX + w * 8192 + g * 256;
    const int sr0 = opq(EX + (cc & 3) * 1024 + g * 256 + ((4 * w + (cc >> 2) + 4 * cc) & 15) * 16), sr1 = sr0 + 4096;
#pragma unroll
    for (int u = 0; u < 8; ++u) {
#pragma unroll
      for (int i = 0; i < 8; ++i) {
        v4u f;
#pragma unroll
        for (int r = 0; r < 4; ++r) f[r] = prm(row[u][2 * r + 1][i >> 1], row[u][2 * r][i >> 1], i & 1);
        sts16(c.L, sw_ + i * 1024 + ((cc + 4 * i) & 15) * 16, f);
      }
      SCHED();
      if (u + 6 < 8) request(u + 6);
      if (u == 7 && mat == 2) claim(c, 0);
      BAR();
      v8u in[4];
#pragma unroll
      for (int q = 0; q < 4; ++q) in[q] = cat(lds16(c.L, sr0 + q * 8192), lds16(c.L, sr1 + q * 8192));
      SCHED();
#pragma unroll
      for (int q = 0; q < 4; ++q) {
        v8u f = in[q];
#define M_(r) asm volatile("" : "+" r(f))
        AT_ACC(M_, 8 * q + u);
#undef M_
#pragma unroll
        for (int r = 0; r < 8; ++r) wa[2 * q + (u >> 2)][8 * (u & 3) + r] = f[r];
      }
      BAR();
    }
  }
  STAMP(0);
  const int xpar = vote_a(wait_vote(c)) ^ (mat >> 1);
  TICK_DECL;
  asm volatile(G_LOOP
      :: [xo] "v"(cc * 2048 + ((g ^ cc) << 4)), [xs] "v"(xs), [so] "v"((4 * w + g) * 512 + cc * 16), [fo] "v"((w | lane) ? 1 << 30 : 0),
         [rs] "s"(c.rs), [rx] "s"(rx), [rc] "s"(mkrs((const void*)c.ctrl, CTRL_BYTES)), [fl] "s"(C_FLAG + (mat * 32 + h) * 128), [xp] "s"(xpar),
         [ob] "s"((mat == 1 ? R_KX : R_QX) + h * 16384), [lw] "s"(w * 16384), GTRACE ACC_IN(wa)
      : CL_S, CL_16_32, CL_32_100);
}

DEV void zc_role(const Params& p, Ctx& c, int h) {
  c.budget = SPIN_CAP;
  const int w = c.w, lane = c.lane = fresh_lane(), g = lane >> 4, cc = lane & 15, h2 = lane >> 5, r32 = lane & 31;
  v32u wa[8];
  hold(c, ZC_AT);
  {
    const rsrc_t rw = mkrs(p.Wo, 4096 * 1024 * 2);
    const int vo = (h * 128 + 8 * g) * 2048 + (256 * w + 8 * cc) * 2;
    v4u row[8][8];
    auto request = [&](int u) {
#pragma unroll
      for (int r = 0; r < 8; ++r) row[u][r] = ld16w(rw, vo, (32 * (u & 3) + r) * 2048 + (u >> 2) * 256);
    };
#pragma unroll
    for (int u = 0; u < 2; ++u) request(u);
    SCHED();
    claim(c, 0);
    SCHED();
#pragma unroll
    for (int u = 2; u < 6; ++u) request(u);
    const int sw_ = w * 8192 + g * 256, sr_ = w * 8192 + (cc & 7) * 1024 + g * 256;
    v8u two[8];
#pragma unroll
    for (int u = 0; u < 8; ++u) {
#pragma unroll
      for (int i = 0; i < 8; ++i) {
        v4u f;
#pragma unroll
        for (int r = 0; r < 4; ++r) f[r] = prm(row[u][2 * r + 1][i >> 1], row[u][2 * r][i >> 1], i & 1);
        sts16(c.L, sw_ + i * 1024 + ((cc + 2 * i) & 15) * 16, f);
      }
      SCHED();
      if (u + 6 < 8) request(u + 6);
      LGKM(0);
#pragma unroll
      for (int q = 0; q < 8; ++q) {
        const v4u o = lds16(c.L, sr_ + ((2 * q + (cc >> 3) + 2 * cc) & 15) * 16);
#pragma unroll
        for (int r = 0; r < 4; ++r) two[q][4 * (u & 1) + r] = o[r];
      }
      SCHED();
      if (u & 1) {
#pragma unroll
        for (int q = 0; q < 8; ++q) {
          const int n = 2 * (8 * (u >> 2) + q) + ((u >> 1) & 1);
#define M_(r) asm volatile("" : "+" r(two[q]))
          AT_ACC(M_, n);
#undef M_
#pragma unroll
          for (int r = 0; r < 8; ++r) wa[n >> 2][8 * (n & 3) + r] = two[q][r];
        }
      }
      LGKM(0);
      SCHED();
    }
  }
  STAMP(0);
  BAR();
  const int para = vote_a(wait_vote(c));
  const int ao = g * 512 + cc * 16, zo = 16 * w * 65536 + lane * 16 + ((para ^ (w >> 1)) & 1) * 8192 + amap(h * 1024, 0);
  TICK_DECL;
  asm volatile(ZC_LOOP
      : [bd] "+s"(c.budget)
      : [ao] "v"(ao), [a1] "v"(ao + 4096), [z1] "v"(zo), [z2] "v"(zo + 65536), [fo] "v"(lane ? 1 << 30 : 0),
        [rs] "s"(c.rs), [rc] "s"(mkrs((const void*)c.ctrl, CTRL_BYTES)), [fv] "s"(C_FLAG + (F_V + h) * 128), [fs] "s"(C_FLAG + (F_S + h) * 128),
        [fz] "s"(C_FLAG + (F_Z + w * 32 + h) * 128), [xp] "s"(para ^ 1), [vb] "s"(R_QX + h * 16384), [vd] "s"(C_VERDICT + c.att * 128), [z0] "n"(ZC_PRIME), [zb] "n"(R_Z), [rg] "n"(ZRING - 2), [rm] "n"(ZRING - 1), GTRACE ACC_IN(wa)
      : CL_S, CL_16_32, CL_32_100);
  if (c.budget == 0u) __hip_atomic_store(c.ctrl + C_TMO / 4, 1u, RLX_AGENT);
}

DEV void conv_role(const Params& p, Ctx& c, int cg, int far) {
  constexpr int FS = 4160, PL = 1280, UO = 4 * FS, WS = UO + 8 * PL;
  const int w = c.w, lane = c.lane = fresh_lane(), tid = w * 64 + lane, g = lane >> 4, cc = lane & 15;
  const int d0 = 8 * cg + 2 * w;
  const int pg = ((far ^ (cg >> 5)) & 1) * 8192;
  auto crow = [](int ch) { return (ch >> 1) * 16384 + (ch & 1) * 4096; };
  const int de = (cg >> 3) * 64 + (tid & 7) * 8, t0 = (cg & 7) * 256 + (tid >> 3) * 8;
  auto group_sync = [&](int phase) {
    gu32* cnt = c.ctrl + (C_CONV + (cg >> 3) * 16 + phase * 8) / 4;
    VMWAIT(0);
    BAR();
    if (w == 0) {
      if (lane == 0) __hip_atomic_fetch_add(cnt, 1u, RLX_AGENT);
#pragma unroll 1
      while (__builtin_amdgcn_readfirstlane(__hip_atomic_load(cnt, RLX_AGENT)) < 8u) { if (spend(c)) break; __builtin_amdgcn_s_sleep(2); }
    }
    BAR();
  };
  v4u fr[2][4], ur[2][4], qr[8];
  unsigned up[2][4];
  const int so = cc * 64 + g * 16;
  const unsigned dd = __builtin_amdgcn_raw_buffer_load_b32(mkrs(p.ssmD, 2048), d0 * 2, 0, CONV_AUX);
  BAR();
  {
    const rsrc_t rk = mkrs(p.bk, 2048 * 2048), rv = mkrs(p.bv, 2048 * 2048);
    v4u kr[11], vr[8], sh[4];
#pragma unroll
    for (int i = 0; i < 4; ++i) sh[i] = ld16c(mkrs(p.shift, 8192), de * 8 + i * 16, 0);
#pragma unroll
    for (int i = 0; i < 11; ++i) kr[i] = ld16c(rk, (t0 + i - 3) * 2048 + de * 2, 0);
#pragma unroll
    for (int i = 0; i < 8; ++i) vr[i] = ld16c(rv, (t0 + i) * 2048 + de * 2, 0);
#pragma unroll
    for (int i = 0; i < 8; ++i) fr[i >> 2][i & 3] = ld16c(mkrs(p.ssmk, 4 * MB), lane * 16 + (i & 3) * 1024, (d0 + (i >> 2)) * 4096);
    SCHED();
    if (lane < 16) sts16(c.L, w * WS + (lane >> 2) * FS + (lane & 3) * 16, v4u{0, 0, 0, 0});
#pragma unroll
    for (int i = 0; i < 2; ++i) sts16(c.L, w * WS + UO + (4 * i + g) * PL + cc * 16, v4u{0, 0, 0, 0});
#pragma unroll
    for (int ch = 0; ch < 8; ++ch) {
      const unsigned sa = sh[ch >> 1][(ch & 1) * 2], sb = sh[ch >> 1][(ch & 1) * 2 + 1];
      const float s0 = blo(sa), s1 = bhi(sa), s2 = blo(sb), s3 = bhi(sb);
      float kk[11], o[8];
#pragma unroll
      for (int i = 0; i < 11; ++i) kk[i] = (ch & 1) ? bhi(kr[i][ch >> 1]) : blo(kr[i][ch >> 1]);
#pragma unroll
      for (int i = 0; i < 8; ++i) o[i] = fmaf(kk[i + 3], s0, fmaf(kk[i + 2], s1, fmaf(kk[i + 1], s2, kk[i] * s3))) * ((ch & 1) ? bhi(vr[i][ch >> 1]) : blo(vr[i][ch >> 1]));
      st16s(c.rs, de * 8192 + t0 * 2, R_KVT + pg + crow(ch), v4u{pk(o[0], o[1]), pk(o[2], o[3]), pk(o[4], o[5]), pk(o[6], o[7])});
    }
    group_sync(0);
#pragma unroll
    for (int i = 0; i < 8; ++i) {
      const int b = R_KVT + pg + d0 * 8192 + (i >> 2) * 4096 + (i & 3) * 1024;
      ur[i >> 2][i & 3] = ld16s(c.rs, so, b);
      up[i >> 2][i & 3] = __builtin_amdgcn_raw_buffer_load_b32(c.rs, (i & 3) == 0 && lane == 0 ? 1 << 30 : so, b - 4, AUX_SC1);
    }
#pragma unroll
    for (int i = 0; i < 8; ++i) qr[i] = ld16c(mkrs(p.bq, 4 * MB), (t0 + i) * 2048 + de * 2, 0);
  }
  STAMP(5);
  const int fw = opq(w * WS + 48 + lane * 16), uw = opq(w * WS + UO + g * PL + cc * 16);
  const int fa = opq(w * WS + (cc & 3) * FS + (3 + (cc >> 2) - g) * 16);
  sfor<2>([&](auto CH) __attribute__((always_inline)) {
    constexpr int ch = CH.value;
    SCHED();
#pragma unroll
    for (int i = 0; i < 4; ++i) {
      const v4u f = fr[ch][i], a = ur[ch][i];
      sts16(c.L, fw + 3 * FS + i * 1024, f);
      sts8(c.L, fw + FS + i * 1024 + 8, v2u{f[0], f[1]}); sts8(c.L, fw + FS + i * 1024 + 16, v2u{f[2], f[3]});
#pragma unroll
      for (int r = 0; r < 2; ++r) {
        const int o = fw + 2 * r * FS + i * 1024 + 12 - 8 * r;
        *(LAS unsigned*)(c.L + o) = f[0]; sts8(c.L, o + 4, v2u{f[1], f[2]}); *(LAS unsigned*)(c.L + o + 12) = f[3];
      }
      v4u r0, r1;
#pragma unroll
      for (int r = 0; r < 4; ++r) {
        r0[r] = __builtin_amdgcn_alignbyte(a[3 - r], a[3 - r], 2);
        r1[r] = __builtin_amdgcn_perm(r < 3 ? a[2 - r] : up[ch][i], a[3 - r], 0x07060100u);
      }
      sts16(c.L, uw + (16 + 16 * i) * 16, r0);
      sts16(c.L, uw + 4 * PL + (16 + 16 * i) * 16, r1);
    }
    f32x4 acc[4][2];
#pragma unroll
    for (int i = 0; i < 8; ++i) acc[i >> 1][i & 1] = f32x4{0.f, 0.f, 0.f, 0.f};
    v4u A[2][4], B[2][8];
    auto frag = [&](int d, int n) {
      constexpr int order[12] = {0, 4, 5, 6, 7, 8, 9, 10, 11, 1, 2, 3};
      const int x = order[n];
      if (x < 4) A[d & 1][x] = lds16(c.L, fa + (d + 16 * x) * 64);
      else B[d & 1][x - 4] = lds16(c.L, uw + ((x - 4) & 1) * 4 * PL + (16 + 16 * ((x - 4) >> 1) - d) * 16);
    };
    asm volatile("" ::: "memory");
#pragma unroll
    for (int n = 0; n < 12; ++n) frag(0, n);
    SCHED();
    sfor<16>([&](auto D) __attribute__((always_inline)) {
      constexpr int d = D.value;
      int n = 0;
#pragma unroll
      for (int k = 0; k < 4; ++k)
#pragma unroll
        for (int m = 0; m + k < 4; ++m)
#pragma unroll
          for (int e = 0; e < 2; ++e) {
            acc[m + k][e] = mfma16(A[d & 1][k], B[d & 1][2 * m + e], acc[m + k][e]);
            if (d < 15 && n < 12) frag(d + 1, n);
            ++n;
            SCHED();
          }
    });
    const float dv = ch ? bhi(dd) : blo(dd);
#pragma unroll
    for (int q = 0; q < 4; ++q) {
      v4u z;
#pragma unroll
      for (int i = 0; i < 4; ++i) {
        const unsigned yy = pk(acc[q][1][i], acc[q][0][i]), ud = pk(blo(ur[ch][q][i]) * dv, bhi(ur[ch][q][i]) * dv);
        z[i] = pk(blo(yy) + blo(ud), bhi(yy) + bhi(ud));
      }
      st16s(c.rs, so + q * 1024, R_YT + pg + d0 * 8192 + ch * 4096, z);
    }
  });
  STAMP(6);
  {
    const rsrc_t ro = mkrs(p.out, 2048 * 2048 * 2);
    v4u yv[8];
    group_sync(1);
#pragma unroll
    for (int ch = 0; ch < 8; ++ch) yv[ch] = ld16s(c.rs, de * 8192 + t0 * 2, R_YT + pg + crow(ch));
#pragma unroll
    for (int i = 0; i < 8; ++i) {
      float o[8];
#pragma unroll
      for (int ch = 0; ch < 8; ++ch) o[ch] = ((ch & 1) ? bhi(qr[i][ch >> 1]) : blo(qr[i][ch >> 1])) * ((i & 1) ? bhi(yv[ch][i >> 1]) : blo(yv[ch][i >> 1]));
      __builtin_amdgcn_raw_buffer_store_b128(v4u{pk(o[0], o[1]), pk(o[2], o[3]), pk(o[4], o[5]), pk(o[6], o[7])}, ro, (t0 + i) * 4096 + (1024 + de) * 2, 0, AUX_NT);
    }
  }
  if (c.budget == 0u) __hip_atomic_store(c.ctrl + C_TMO / 4, 1u, RLX_AGENT);
}

extern "C" __global__ void __launch_bounds__(256, 1) mk_mega(Params p) {
  extern __shared__ __attribute__((aligned(16))) unsigned char lds_raw[];
  Ctx c;
  c.ctrl = (gu32*)p.ctrl;
  c.rs = mkrs(p.data, R_END);
  c.L = (lds_t)lds_raw;
  c.w = __builtin_amdgcn_readfirstlane(threadIdx.x >> 6);
  c.lane = threadIdx.x & 63;
  c.t0 = (unsigned)__builtin_amdgcn_s_memrealtime();
  c.b = (int)(blockIdx.x & 255u);
  c.att = (int)(blockIdx.x >> 8);
  c.ask = 0;
  if (c.w == 1) {
    unsigned long long base; unsigned len, t;
    asm volatile("s_getpc_b64 s[60:61]\n1:\n\ts_sub_u32 s60, s60, 1b-mk_mega\n\ts_subb_u32 s61, s61, 0\n\ts_mov_b64 %0, s[60:61]\n\ts_mov_b32 %1, mk_tail-mk_mega"
                 : "=s"(base), "=s"(len) :: "s60", "s61", "scc");
    t = __builtin_amdgcn_raw_buffer_load_b32(mkrs((const void*)base, len), (c.lane * 32 + (c.b >> 3)) * 128, 0, 0);
    asm volatile("" :: "v"(t));
  }
  if (c.w == 0 && c.att == 0) {
    c.ask = __builtin_amdgcn_readfirstlane(__hip_atomic_load(c.ctrl + C_ECHO / 4 + c.b, RLX_AGENT)) + 1u;
    if (c.lane == 0) __hip_atomic_store(c.ctrl + (C_ASK + c.b * 64) / 4, c.ask, RLX_AGENT);
  }
  c.slot = (c.att ? (c.b & 7) : (int)((unsigned)__builtin_amdgcn_s_getreg(20 | (3 << 11)) & 7u)) * 32 + (c.b >> 3);
  if (c.b == SERVER && c.w == 3) serve(c);
  const int x = c.slot >> 5, t = c.slot & 31;
  if (x < 4) {
    if (t < 8) qk_role(p, c, x * 8 + t);
    else if (t < 16) vo_role(p, c, x * 8 + t - 8);
    else y_role(p, c, x * 16 + t - 16);
  } else {
    if (t < 24) g_role(p, c, t >> 3, (x - 4) * 8 + (t & 7), t);
    else zc_role(p, c, (x - 4) * 8 + t - 24);
    STAMP(2);
    c.budget = SPIN_CAP;
    conv_role(p, c, (x - 4) * 32 + t, vote_a(wait_verdict(c)) ^ 1);
  }
  STAMP(3);
}

}

extern "C" void kernel_launch(void* const* d_in, const int* in_sizes, int n_in, void* d_out, int out_size, void* d_ws, size_t ws_size,
                              hipStream_t stream) {
  using namespace mk;
  (void)in_sizes; (void)n_in; (void)out_size; (void)ws_size;
  Params p;
  p.x = (const u16*)d_in[0]; p.mem0 = (const u16*)d_in[1];
  p.Wq = (const u16*)d_in[2]; p.Wk = (const u16*)d_in[3]; p.Wv = (const u16*)d_in[4]; p.Wo = (const u16*)d_in[5];
  p.bq = (const u16*)d_in[7]; p.bk = (const u16*)d_in[8]; p.bv = (const u16*)d_in[9];
  p.shift = (const u16*)d_in[10]; p.ssmk = (const u16*)d_in[11]; p.ssmD = (const u16*)d_in[12];
  p.out = (u16*)d_out;
  p.ctrl = (unsigned char*)d_ws;
  p.data = (unsigned char*)(((size_t)d_ws + CTRL_BYTES + (2u << 20) - 1) & ~(size_t)((2u << 20) - 1));
  (void)hipFuncSetAttribute((const void*)mk_mega, hipFuncAttributeMaxDynamicSharedMemorySize, LDS_BYTES);
  hipLaunchKernelGGL(mk_mega, dim3(256), dim3(256), LDS_BYTES, stream, p);
}
```

```cpp
#include <hip/hip_runtime.h>
#include <hip/hip_bf16.h>

namespace mk {

#define GAS __attribute__((address_space(1)))
#define LAS __attribute__((address_space(3)))
#define DEV __device__ __forceinline__
#define RLX_AGENT __ATOMIC_RELAXED, __HIP_MEMORY_SCOPE_AGENT
typedef unsigned short u16;
typedef unsigned v2u __attribute__((ext_vector_type(2)));
typedef unsigned v4u __attribute__((ext_vector_type(4)));
typedef unsigned v8u __attribute__((ext_vector_type(8)));
typedef unsigned v16u __attribute__((ext_vector_type(16)));
typedef unsigned v32u __attribute__((ext_vector_type(32)));
typedef float f32x2 __attribute__((ext_vector_type(2)));
typedef float f32x4 __attribute__((ext_vector_type(4)));
typedef float f32x16 __attribute__((ext_vector_type(16)));
typedef __bf16 bf16x2 __attribute__((ext_vector_type(2)));
typedef __bf16 bf16x8 __attribute__((ext_vector_type(8)));
typedef __bf16 bf16x16 __attribute__((ext_vector_type(16)));
typedef GAS unsigned gu32;
typedef __amdgpu_buffer_rsrc_t rsrc_t;
typedef LAS unsigned char* lds_t;

#define XCNT(r) ""
#define UCNT ""
#define CL_T
#define XT(a, b) ""
#define GSUM ""
#define ZSUM ""
#define GTRACE
#define MIXED 1
#define NOFLAG 0
#define TOK_KICK 1
#define ZRING 16
#define Z_AT 16
#define Z_GAP 4
#define STR_(x) #x
#define STR(x) STR_(x)
#define GQ_AT 600
#define GK_AT 300
#define PULL_AT 800
#define QK_AT 100
#define SERVER 194
#define SLOW_LEAD 100
#define PULL_DEPTH 1
#define PRIME 2
#define X_AT 150
#define VO_AT 250
#define ZC_AT 0
#define CONV_AUX 2
#define W_AUX 0
#define ZC_PRIME 1
#define CODE_PULL 1
#define PROBES 2
#define Z_WIN 20
#define TOK_WAIT 0
#define Z_AUX 0
constexpr int NSEG = 64;
constexpr unsigned SENT = 0xFFFFFFFFu;
constexpr int AUX_SC1 = 16, AUX_NT = 2;
constexpr int T_LAG = 61;
constexpr int G_AHEAD = 4;
constexpr unsigned SPIN_CAP = 1u << 21;
constexpr int LDS_BYTES = 163840;

constexpr int C_ECHO = 0, C_CLAIM = 1024, C_VERDICT = 3072, C_TMO = 3328, C_CONV = 3456, C_VOTE = 3840, C_FLAG = 4096, C_ASK = C_FLAG + 256 * 128;
constexpr int F_Q = 0, F_K = 32, F_V = 64, F_S = 96, F_Z = 128;
constexpr int CTRL_BYTES = C_ASK + 256 * 64;
constexpr int MB = 1 << 20;
constexpr int R_U = 136 * MB;
constexpr int R_MEM = 2 * MB, MEM_SLOT = 32768;
constexpr int R_P = R_MEM + 4 * MEM_SLOT, P_SLOT = 65536;
constexpr int R_PT = 4 * MB, PT_SLOT = 131072;
constexpr int R_QX = 8 * MB, R_KX = 40 * MB, R_Z = 72 * MB, Z_SLOT = 4 * MB, R_KVT = R_KX, R_YT = R_KX + 8 * MB, R_END = 152 * MB;

constexpr size_t R_DIAG = 0;
#define TOP 1
static inline unsigned char* data_base(void* ws, size_t size) {
  return (unsigned char*)((TOP ? (size_t)ws + size - R_END - R_DIAG : (size_t)ws + CTRL_BYTES + 2 * MB - 1) & ~(size_t)(2 * MB - 1));
}
struct Params {
  const u16 *x, *mem0, *Wq, *Wk, *Wv, *Wo, *bq, *bk, *bv, *shift, *ssmk, *ssmD;
  u16* out;
  unsigned char* ctrl;
  unsigned char* data;
};

DEV rsrc_t mkrs(const void* p, unsigned bytes) { return __builtin_amdgcn_make_buffer_rsrc((void*)p, 0, bytes, 0x00020000); }
DEV v4u ld16s(rsrc_t rs, int v, int s) { return __builtin_amdgcn_raw_buffer_load_b128(rs, v, s, AUX_SC1); }
DEV v2u ld8s(rsrc_t rs, int v, int s) { return __builtin_amdgcn_raw_buffer_load_b64(rs, v, s, AUX_SC1); }
DEV v4u ld16(rsrc_t rs, int v, int s) { return __builtin_amdgcn_raw_buffer_load_b128(rs, v, s, 0); }
DEV v4u ld16z(rsrc_t rs, int v, int s) { return __builtin_amdgcn_raw_buffer_load_b128(rs, v, s, AUX_SC1 | Z_AUX); }
DEV v4u ld16c(rsrc_t rs, int v, int s) { return __builtin_amdgcn_raw_buffer_load_b128(rs, v, s, CONV_AUX); }
DEV v4u ld16w(rsrc_t rs, int v, int s) { return __builtin_amdgcn_raw_buffer_load_b128(rs, v, s, W_AUX); }
DEV v2u ld8(rsrc_t rs, int v, int s) { return __builtin_amdgcn_raw_buffer_load_b64(rs, v, s, 0); }
DEV void dma16(rsrc_t rs, lds_t l, int v) {
#if __HIP_DEVICE_COMPILE__
  __builtin_amdgcn_raw_ptr_buffer_load_lds(rs, (LAS void*)l, 16, v, 0, 0, 0);
#endif
}
DEV void st16s(rsrc_t rs, int v, int s, v4u d) { __builtin_amdgcn_raw_buffer_store_b128(d, rs, v, s, AUX_SC1); }
DEV void st8s(rsrc_t rs, int v, int s, v2u d) { __builtin_amdgcn_raw_buffer_store_b64(d, rs, v, s, AUX_SC1); }
DEV void st4s(rsrc_t rs, int v, int s, unsigned d) { __builtin_amdgcn_raw_buffer_store_b32(d, rs, v, s, AUX_SC1); }
DEV int amap(int o, int par) { return ((o >> 13) << 14) | (par << 13) | (o & 8191); }
DEV int xblk(int s, int h, int par) { return (s * 32 + h) * 16384 + ((par ^ (s >> 2)) & 1) * 8192; }
DEV unsigned umax(unsigned a, unsigned b) { return a > b ? a : b; }
DEV unsigned top(v4u v) { return umax(umax(v.x, v.y), umax(v.z, v.w)); }
DEV unsigned pk(float a, float b) { bf16x2 v = {(__bf16)a, (__bf16)b}; return __builtin_bit_cast(unsigned, v); }
DEV unsigned pks(float a, float b) { unsigned u = pk(a, b); return u < SENT - 1u ? u : SENT - 1u; }
DEV float blo(unsigned u) { return __builtin_bit_cast(float, u << 16); }
DEV float bhi(unsigned u) { return __builtin_bit_cast(float, u & 0xFFFF0000u); }
DEV float rb(float a) { return blo(pk(a, a)); }
DEV unsigned fu(float f) { return __builtin_bit_cast(unsigned, f); }
DEV float uf(unsigned u) { return __builtin_bit_cast(float, u); }
DEV f32x4 mfma16(v4u a, v4u b, f32x4 c) {
  return __builtin_amdgcn_mfma_f32_16x16x32_bf16(__builtin_bit_cast(bf16x8, a), __builtin_bit_cast(bf16x8, b), c, 0, 0, 0);
}
DEV f32x16 mfma32(v4u a, v4u b, f32x16 c) {
  return __builtin_amdgcn_mfma_f32_32x32x16_bf16(__builtin_bit_cast(bf16x8, a), __builtin_bit_cast(bf16x8, b), c, 0, 0, 0);
}
DEV f32x4 smf(v4u a, v8u b, f32x4 c, int idx) {
  return __builtin_amdgcn_smfmac_f32_16x16x64_bf16(__builtin_bit_cast(bf16x8, a), __builtin_bit_cast(bf16x16, b), c, idx, 0, 0);
}
DEV v8u cat(v4u a, v4u b) { return __builtin_shufflevector(a, b, 0, 1, 2, 3, 4, 5, 6, 7); }
DEV v4u lo4(v8u a) { return __builtin_shufflevector(a, a, 0, 1, 2, 3); }
DEV v4u hi4(v8u a) { return __builtin_shufflevector(a, a, 4, 5, 6, 7); }
DEV v4u lds16(lds_t L, int o) { return *(LAS v4u*)(L + o); }
DEV void sts16(lds_t L, int o, v4u v) { *(LAS v4u*)(L + o) = v; }
DEV void sts8(lds_t L, int o, v2u v) { *(LAS v2u*)(L + o) = v; }
#define SCHED() __builtin_amdgcn_sched_barrier(0)
#define PIN_A(x) asm volatile("" : "+a"(x))
#define PIN_V(x) asm volatile("" : "+v"(x))
DEV int opq(int v) { asm volatile("" : "+v"(v)); return v; }
DEV int fresh_lane() {
  int l; asm volatile("v_mbcnt_lo_u32_b32 %0, -1, 0\n\tv_mbcnt_hi_u32_b32 %0, -1, %0" : "=v"(l)); return l;
}
DEV f32x4 fresh_zero() {
  f32x2 a, b;
  asm volatile("v_mov_b64 %0, 0" : "=v"(a));
  asm volatile("v_mov_b64 %0, 0" : "=v"(b));
  return __builtin_shufflevector(a, b, 0, 1, 2, 3);
}
DEV unsigned fresh_sent() { unsigned z; asm volatile("v_mov_b32 %0, -1" : "=v"(z)); return z; }
#define SMF "v_smfmac_f32_16x16x64_bf16 "
#define MF16 "v_mfma_f32_16x16x32_bf16 "
#define SMF1(cls, d, a, b) asm volatile(SMF "%0, %1, %2, %3" : "+v"(d) : "v"(a), cls(b), "v"(idx))
#define SMF1F(cls, lo, hi, d, a, b) asm volatile(SMF "v[" #lo ":" #hi "], %1, %2, %3" : "+{v[" #lo ":" #hi "]}"(d) : "v"(a), cls(b), "v"(idx))
#define AT_HOME(M, j) do { const int j_ = (j); if (j_ == 0) M("{v[32:39]}"); if (j_ == 1) M("{v[40:47]}"); if (j_ == 2) M("{v[48:55]}"); if (j_ == 3) M("{v[56:63]}"); if (j_ == 4) M("{v[64:71]}"); if (j_ == 5) M("{v[72:79]}"); if (j_ == 6) M("{v[80:87]}"); if (j_ == 7) M("{v[88:95]}"); if (j_ == 8) M("{v[96:103]}"); if (j_ == 9) M("{v[104:111]}"); if (j_ == 10) M("{v[112:119]}"); if (j_ == 11) M("{v[120:127]}"); if (j_ == 12) M("{v[128:135]}"); if (j_ == 13) M("{v[136:143]}"); if (j_ == 14) M("{v[144:151]}"); if (j_ == 15) M("{v[152:159]}"); } while (0)
#define MF1(d, a, b) asm volatile(MF16 "%0, %1, %2, %0" : "+v"(d) : "v"(a), "v"(b))
#define MF1Z(d, a, b) asm volatile(MF16 "%0, %1, %2, 0" : "=&v"(d) : "v"(a), "v"(b))
#define LGKM(n) asm volatile("s_waitcnt lgkmcnt(" #n ")" ::: "memory")
#define RAWBAR() do { __builtin_amdgcn_s_barrier(); asm volatile("" ::: "memory"); } while (0)
DEV void settle3(f32x4& s0, f32x4& s1, f32x4& s2) { asm volatile("s_nop 7" : "+v"(s0), "+v"(s1), "+v"(s2)); }
DEV void settle4(f32x4 (&d)[4]) { asm volatile("s_nop 7" : "+v"(d[0]), "+v"(d[1]), "+v"(d[2]), "+v"(d[3])); }
#define BAR() do { asm volatile("s_waitcnt lgkmcnt(0)" ::: "memory"); __builtin_amdgcn_s_barrier(); asm volatile("" ::: "memory"); } while (0)
#define VMWAIT(n) asm volatile("s_waitcnt vmcnt(" #n ")" ::: "memory")
DEV float add1(float a, float b) {
  float o; asm("v_add_f32 %0, %1, %2" : "=v"(o) : "v"(a), "v"(b)); return o;
}
DEV float xmax(float v) {
  auto r = __builtin_amdgcn_permlane32_swap(fu(v), fu(v), false, false); v = fmaxf(uf(r[0]), uf(r[1]));
  auto q = __builtin_amdgcn_permlane16_swap(fu(v), fu(v), false, false); return fmaxf(uf(q[0]), uf(q[1]));
}
DEV float xsum(float v) {
  auto r = __builtin_amdgcn_permlane32_swap(fu(v), fu(v), false, false); v = uf(r[0]) + uf(r[1]);
  auto q = __builtin_amdgcn_permlane16_swap(fu(v), fu(v), false, false); return uf(q[0]) + uf(q[1]);
}

DEV float ror8(float v) { return uf(__builtin_amdgcn_update_dpp(0u, fu(v), 0x128, 0xf, 0xf, true)); }
DEV float fmax2(float a, float b) { return __builtin_amdgcn_fmed3f(a, b, __builtin_inff()); }
DEV float fmax3(float a, float b, float c) { float r; asm("v_max3_f32 %0, %1, %2, %3" : "=v"(r) : "v"(a), "v"(b), "v"(c)); return r; }
DEV bool allok(unsigned t) { return __all(t != SENT); }

template <int N> struct IC { static constexpr int value = N; };
template <int N, class F> DEV void sfor(F&& f) { if constexpr (N > 0) { sfor<N - 1>(f); f(IC<N - 1>{}); } }
struct Ctx {
  gu32* ctrl;
  rsrc_t rs;
  lds_t L;
  int w, lane;
  int b, slot, att;
  unsigned ask;
  unsigned t0;
  unsigned budget;
};
DEV bool spend(Ctx& c) {
  const unsigned old = c.budget;
  asm volatile("s_cmp_lg_u32 %0, 0\n\ts_subb_u32 %0, %0, 0" : "+s"(c.budget) :: "scc");
  return old == 0u;
}
DEV unsigned wait_flag(Ctx& c, int flag, unsigned need) {
  gu32* f = c.ctrl + (C_FLAG + flag * 128) / 4;
  unsigned v;
#pragma unroll 1
  for (;;) {
    v = __builtin_amdgcn_readfirstlane(__hip_atomic_load(f, RLX_AGENT));
    if (v >= need || spend(c)) break;
    __builtin_amdgcn_s_sleep(2);
  }
  __builtin_amdgcn_fence(__ATOMIC_ACQUIRE, "wavefront");
  return v;
}
DEV void post_flag(Ctx& c, int flag, unsigned v, int off = 0) {
  st4s(mkrs((const void*)c.ctrl, CTRL_BYTES), (c.w | off | fresh_lane()) ? 1 << 30 : 0, C_FLAG + flag * 128, v);
}
constexpr unsigned CL_A = 1u << 16, CL_VOTED = 1u << 24, CL_FILLED = 1u << 28;
DEV void claim(Ctx& c, unsigned votes) {
  if (c.w != 0) return;
  if (c.att == 0) {
#pragma unroll 1
    while (__builtin_amdgcn_readfirstlane(__hip_atomic_load(c.ctrl + C_ECHO / 4 + c.b, RLX_AGENT)) != c.ask) {
      if (spend(c)) break;
      __builtin_amdgcn_s_sleep(2);
    }
  }
  if (fresh_lane() == 0) __hip_atomic_fetch_add(c.ctrl + (C_CLAIM + c.att * 1024) / 4 + c.slot, 1u + votes, RLX_AGENT);
}
DEV unsigned wait_verdict(Ctx& c) {
  unsigned v;
#pragma unroll 1
  for (;;) {
    v = __builtin_amdgcn_readfirstlane(__hip_atomic_load(c.ctrl + (C_VERDICT + c.att * 128) / 4, RLX_AGENT));
    if ((v & 1u) || spend(c)) break;
    __builtin_amdgcn_s_sleep(2);
  }
  __builtin_amdgcn_fence(__ATOMIC_ACQUIRE, "wavefront");
  if ((v & 3u) == 3u && c.att == 0) {
    asm volatile("s_waitcnt vmcnt(0) lgkmcnt(0)\n\ts_barrier\n\ts_mov_b64 s[0:1], %0\n\ts_mov_b32 s2, %1\n\tv_mov_b32 v0, %2\n\t"
                 "s_getpc_b64 s[4:5]\n1:\n\ts_sub_u32 s4, s4, 1b-mk_mega\n\ts_subb_u32 s5, s5, 0\n\ts_setpc_b64 s[4:5]"
                 :: "s"(__builtin_amdgcn_kernarg_segment_ptr()), "s"(c.b + 256), "v"(c.w * 64 + fresh_lane()) : "s0", "s1", "s2", "s4", "s5", "v0", "scc", "memory");
    __builtin_unreachable();
  }
  return v;
}
DEV unsigned wait_vote(Ctx& c) {
  unsigned v;
#pragma unroll 1
  for (;;) {
    v = __builtin_amdgcn_readfirstlane(__hip_atomic_load(c.ctrl + C_VOTE / 4, RLX_AGENT));
    if ((v & 1u) || spend(c)) break;
    __builtin_amdgcn_s_sleep(2);
  }
  return v;
}
DEV void serve(Ctx& c) {
  c.budget = SPIN_CAP;
  const int lane = fresh_lane();
  const rsrc_t rc = mkrs((const void*)c.ctrl, CTRL_BYTES);
  if (c.att == 0) {
#pragma unroll
    for (int i = C_CLAIM / 1024; i < C_ASK / 1024; ++i) st16s(rc, lane * 16, i * 1024, v4u{0, 0, 0, 0});
    VMWAIT(0);
  }
  unsigned vote = c.att ? wait_vote(c) : 0u;
  bool echo = c.att == 0, first = true;
  v4u last = {0u, 0u, 0u, 0u};
#pragma unroll 1
  for (;;) {
    asm volatile("" ::: "memory");
    const v4u cl = ld16s(rc, lane * 16, C_CLAIM + c.att * 1024);
    if (echo) {
      v4u e;
#pragma unroll
      for (int i = 0; i < 4; ++i) e[i] = __builtin_amdgcn_raw_buffer_load_b32(rc, (lane * 4 + i) * 64, C_ASK, AUX_SC1);
      if (first || __any(e[0] != last[0] || e[1] != last[1] || e[2] != last[2] || e[3] != last[3])) st16s(rc, lane * 16, C_ECHO, e);
      last = e; first = false;
    }
    unsigned n = 0, one = 1;
    int va = 0, nv = 0, nf = 0;
#pragma unroll
    for (int i = 0; i < 4; ++i) {
      n += cl[i] & 0xFFFFu; one &= (cl[i] & 0xFFFFu) == 1u;
      va += __popcll(__ballot(cl[i] & CL_A)); nv += __popcll(__ballot(cl[i] & CL_VOTED)); nf += __popcll(__ballot(cl[i] & CL_FILLED));
    }
#pragma unroll
    for (int o = 1; o < 64; o <<= 1) n += (unsigned)__shfl_xor((int)n, o);
    const bool all = __builtin_amdgcn_readfirstlane(n) >= 256u, good = __all(one);
    echo = echo && !all;
    if (!vote && (all || max(va, nv - va) > 32)) {
      vote = va > 32 ? 5u : 9u;
      if (lane == 0) __hip_atomic_store(c.ctrl + C_VOTE / 4, vote, RLX_AGENT);
    }
    if (all && (!good || nf >= 64)) {
      if (lane == 0) __hip_atomic_store(c.ctrl + (C_VERDICT + c.att * 128) / 4, vote | (good ? 0u : 2u), RLX_AGENT);
      break;
    }
    if (spend(c)) break;
  }
}
DEV int vote_a(unsigned v) { return (int)((v >> 2) & 1u); }
DEV int vote_b(unsigned v) { return (int)((v >> 3) & 1u); }

#define STAMP(slot) do {} while (0)
#define TICK_DECL
#define LOG(i, s, v) do {} while (0)
#define LITE(s) do {} while (0)
#define TICK(i) do {} while (0)
#define TICK_SUM(n) do {} while (0)
#define TICK_LDS(b) do {} while (0)
#define TICK_DUMP(b) do {} while (0)
#define TICK_OUT() do {} while (0)

DEV constexpr int krow(int rho8, int i) { return 32 * (rho8 >> 2) + 8 * (rho8 & 1) + 2 * ((rho8 >> 1) & 1) + i; }
DEV int krow_lane(int g) { return 16 * (g >> 1) + 4 * (g & 1); }
DEV unsigned prm(unsigned hi, unsigned lo, int odd) { return __builtin_amdgcn_perm(hi, lo, odd ? 0x07060302u : 0x05040100u); }

#define AT_ACC(M, n) do { const int n_ = (n); if (n_ == 0) M("{a[0:7]}"); if (n_ == 1) M("{a[8:15]}"); if (n_ == 2) M("{a[16:23]}"); if (n_ == 3) M("{a[24:31]}"); if (n_ == 4) M("{a[32:39]}"); if (n_ == 5) M("{a[40:47]}"); if (n_ == 6) M("{a[48:55]}"); if (n_ == 7) M("{a[56:63]}"); if (n_ == 8) M("{a[64:71]}"); if (n_ == 9) M("{a[72:79]}"); if (n_ == 10) M("{a[80:87]}"); if (n_ == 11) M("{a[88:95]}"); if (n_ == 12) M("{a[96:103]}"); if (n_ == 13) M("{a[104:111]}"); if (n_ == 14) M("{a[112:119]}"); if (n_ == 15) M("{a[120:127]}"); if (n_ == 16) M("{a[128:135]}"); if (n_ == 17) M("{a[136:143]}"); if (n_ == 18) M("{a[144:151]}"); if (n_ == 19) M("{a[152:159]}"); if (n_ == 20) M("{a[160:167]}"); if (n_ == 21) M("{a[168:175]}"); if (n_ == 22) M("{a[176:183]}"); if (n_ == 23) M("{a[184:191]}"); if (n_ == 24) M("{a[192:199]}"); if (n_ == 25) M("{a[200:207]}"); if (n_ == 26) M("{a[208:215]}"); if (n_ == 27) M("{a[216:223]}"); if (n_ == 28) M("{a[224:231]}"); if (n_ == 29) M("{a[232:239]}"); if (n_ == 30) M("{a[240:247]}"); if (n_ == 31) M("{a[248:255]}"); } while (0)
#define PIN_ACC0(x) asm volatile("" :: "{a[0:7]}"(x[0]), "{a[8:15]}"(x[1]), "{a[16:23]}"(x[2]), "{a[24:31]}"(x[3]), "{a[32:39]}"(x[4]), "{a[40:47]}"(x[5]), "{a[48:55]}"(x[6]), "{a[56:63]}"(x[7]), "{a[64:71]}"(x[8]), "{a[72:79]}"(x[9]), "{a[80:87]}"(x[10]), "{a[88:95]}"(x[11]), "{a[96:103]}"(x[12]), "{a[104:111]}"(x[13]), "{a[112:119]}"(x[14]), "{a[120:127]}"(x[15]))
#define PIN_ACC1(x) asm volatile("" :: "{a[128:135]}"(x[16]), "{a[136:143]}"(x[17]), "{a[144:151]}"(x[18]), "{a[152:159]}"(x[19]), "{a[160:167]}"(x[20]), "{a[168:175]}"(x[21]), "{a[176:183]}"(x[22]), "{a[184:191]}"(x[23]), "{a[192:199]}"(x[24]), "{a[200:207]}"(x[25]), "{a[208:215]}"(x[26]), "{a[216:223]}"(x[27]), "{a[224:231]}"(x[28]), "{a[232:239]}"(x[29]), "{a[240:247]}"(x[30]), "{a[248:255]}"(x[31]))
#define PIN_HOME(x) asm volatile("" :: "{v[32:39]}"(x[0]), "{v[40:47]}"(x[1]), "{v[48:55]}"(x[2]), "{v[56:63]}"(x[3]), "{v[64:71]}"(x[4]), "{v[72:79]}"(x[5]), "{v[80:87]}"(x[6]), "{v[88:95]}"(x[7]), "{v[96:103]}"(x[8]), "{v[104:111]}"(x[9]), "{v[112:119]}"(x[10]), "{v[120:127]}"(x[11]), "{v[128:135]}"(x[12]), "{v[136:143]}"(x[13]), "{v[144:151]}"(x[14]), "{v[152:159]}"(x[15]))
#define CL_32_100 "v32", "v33", "v34", "v35", "v36", "v37", "v38", "v39", "v40", "v41", "v42", "v43", "v44", "v45", "v46", "v47", "v48", "v49", "v50", "v51", "v52", "v53", "v54", "v55", "v56", "v57", "v58", "v59", "v60", "v61", "v62", "v63", "v64", "v65", "v66", "v67", "v68", "v69", "v70", "v71", "v72", "v73", "v74", "v75", "v76", "v77", "v78", "v79", "v80", "v81", "v82", "v83", "v84", "v85", "v86", "v87", "v88", "v89", "v90", "v91", "v92", "v93", "v94", "v95", "v96", "v97", "v98", "v99", "v100", "v101", "v102", "v103", "v104", "v105", "v106", "v107", "v108", "v109", "v110", "v111", "v112", "v113", "v114", "v115", "v116", "v117", "v118", "v119", "v120", "v121", "v122", "v123", "v124", "v125", "v126", "v127", "v128", "v129", "v130", "v131"
#define ACC_IN(x) "{a[0:31]}"(x[0]), "{a[32:63]}"(x[1]), "{a[64:95]}"(x[2]), "{a[96:127]}"(x[3]), "{a[128:159]}"(x[4]), "{a[160:191]}"(x[5]), "{a[192:223]}"(x[6]), "{a[224:255]}"(x[7])
#define CL_16_32 "v16", "v17", "v18", "v19", "v20", "v21", "v22", "v23", "v24", "v25", "v26", "v27", "v28", "v29", "v30", "v31"
#define CL_120_128 "v120", "v121", "v122", "v123", "v124", "v125", "v126", "v127", "v128", "v129", "v130", "v131", "v132", "v133", "v134", "v135"
#define CL_160_200 "v160", "v161", "v162", "v163", "v164", "v165", "v166", "v167", "v168", "v169", "v170", "v171", "v172", "v173", "v174", "v175", "v176", "v177", "v178", "v179", "v180", "v181", "v182", "v183", "v184", "v185", "v186", "v187", "v188", "v189", "v190", "v191", "v192", "v193", "v194", "v195", "v196", "v197", "v198", "v199"
#define CL_176_200 "v176", "v177", "v178", "v179", "v180", "v181", "v182", "v183", "v184", "v185", "v186", "v187", "v188", "v189", "v190", "v191", "v192", "v193", "v194", "v195", "v196", "v197", "v198", "v199"
#define CL_200_216 "v200", "v201", "v202", "v203", "v204", "v205", "v206", "v207", "v208", "v209", "v210", "v211", "v212", "v213", "v214", "v215"
#define CL_216_256 "v216", "v217", "v218", "v219", "v220", "v221", "v222", "v223", "v224", "v225", "v226", "v227", "v228", "v229", "v230", "v231", "v232", "v233", "v234", "v235", "v236", "v237", "v238", "v239", "v240", "v241", "v242", "v243", "v244", "v245", "v246", "v247", "v248", "v249", "v250", "v251", "v252", "v253", "v254", "v255"
#define CL_S "memory", "vcc", "scc", "s60", "s61", "s62", "s63", "s64", "s65", "s66", "s67", "s68" CL_T
#define TIMED_WAIT \
    "s_getpc_b64 s[62:63]\n\t" "1:\n\t" "s_memtime s[64:65]\n\t" "s_waitcnt lgkmcnt(0)\n\t" "s_sub_u32 s61, %0, s64\n\t" "s_cmp_gt_i32 s61, 124\n\t" "s_cbranch_scc1 1b\n\t" \
    "s_max_i32 s61, s61, 0\n\t" "s_and_b32 s61, s61, 124\n\t" "s_sub_u32 s61, 2f-1b, s61\n\t" "s_add_u32 s62, s62, s61\n\t" "s_addc_u32 s63, s63, 0\n\t" \
    "s_setpc_b64 s[62:63]\n\t" ".rept 31\n\t" "s_nop 0\n\t" ".endr\n\t" "2:\n\t"
#define QK_STEP0 \
    "v_xor_b32 v216, 0x400, %[mo]\n\t" "s_xor_b32 s60, %[sb], 0x800\n\t" "s_mov_b32 s66, 0x3e0293ee\n\t" "s_mov_b32 s67, 0x3e0293ee\n\t" XT(80, 81) "s_sleep %[ks]\n\t" \
    "s_add_u32 s61, %[kb], 0x1000\n\t" "buffer_load_dwordx4 v[16:19], %[l1], %[rs], %[kb] offen offset:0 sc1\n\t" \
    "buffer_load_dwordx4 v[20:23], %[l1], %[rs], %[kb] offen offset:1024 sc1\n\t" "buffer_load_dwordx4 v[24:27], %[l1], %[rs], %[kb] offen offset:2048 sc1\n\t" \
    "buffer_load_dwordx4 v[28:31], %[l1], %[rs], %[kb] offen offset:3072 sc1\n\t" "buffer_load_dwordx4 v[200:203], %[l1], %[rs], s61 offen offset:0 sc1\n\t" \
    "buffer_load_dwordx4 v[204:207], %[l1], %[rs], s61 offen offset:1024 sc1\n\t" "buffer_load_dwordx4 v[208:211], %[l1], %[rs], s61 offen offset:2048 sc1\n\t" \
    "buffer_load_dwordx4 v[212:215], %[l1], %[rs], s61 offen offset:3072 sc1\n\t" "s_waitcnt vmcnt(0)\n\t" XT(98, 99) "s_barrier\n\t" XT(82, 83) "10:\n\t" \
    "buffer_load_dwordx4 v[240:243], %[mo], %[rs], %[sb] offen sc1\n\t" "v_mov_b64 v[224:225], 0\n\t" "v_mov_b64 v[226:227], 0\n\t" "v_mov_b64 v[228:229], 0\n\t" \
    "v_mov_b64 v[230:231], 0\n\t" "v_mov_b64 v[232:233], 0\n\t" "v_mov_b64 v[234:235], 0\n\t" "v_mov_b64 v[236:237], 0\n\t" "v_mov_b64 v[238:239], 0\n\t" \
    "buffer_load_dwordx4 v[244:247], v216, %[rs], %[sb] offen sc1\n\t" "ds_read_b128 v[160:163], %[l0] offset:0\n\t" "ds_read_b128 v[164:167], %[l0] offset:1024\n\t" \
    "ds_read_b128 v[168:171], %[l0] offset:2048\n\t" "ds_read_b128 v[172:175], %[l0] offset:3072\n\t" "s_nop 3\n\t" \
    "buffer_load_dwordx4 v[248:251], %[mo], %[rs], s60 offen sc1\n\t" "ds_read_b128 v[176:179], %[l0] offset:4096\n\t" "ds_read_b128 v[180:183], %[l0] offset:5120\n\t" \
    "ds_read_b128 v[184:187], %[l0] offset:6144\n\t" "ds_read_b128 v[188:191], %[l0] offset:7168\n\t" "s_nop 3\n\t" \
    "buffer_load_dwordx4 v[252:255], v216, %[rs], s60 offen sc1\n\t" "v_mov_b64 v[220:221], -1\n\t" "v_mov_b64 v[222:223], -1\n\t" "v_mov_b32 v217, 0\n\t" \
    "buffer_store_dwordx2 v[220:221], %[px], %[rs], %[pr] offen sc1\n\t" "buffer_store_dword v220, %[pm], %[rs], %[pr] offen sc1\n\t" \
    "global_load_dword v217, v217, %[fq] sc1\n\t" "s_cmp_lg_u32 s61, 0\n\t" "s_cbranch_scc0 177f\n\t" "ds_write_b128 %[l1], v[16:19] offset:16384\n\t" \
    "ds_write_b128 %[l1], v[20:23] offset:17408\n\t" "ds_write_b128 %[l1], v[24:27] offset:18432\n\t" "ds_write_b128 %[l1], v[28:31] offset:19456\n\t" \
    "ds_write_b128 %[l1], v[200:203] offset:20480\n\t" "ds_write_b128 %[l1], v[204:207] offset:21504\n\t" "ds_write_b128 %[l1], v[208:211] offset:22528\n\t" \
    "ds_write_b128 %[l1], v[212:215] offset:23552\n\t" "s_mov_b32 s61, 0\n\t" "s_waitcnt lgkmcnt(0)\n\t" "177:\n\t" "v_mov_b64 v[208:209], 0\n\t" \
    "v_mov_b64 v[210:211], 0\n\t" "v_mov_b64 v[212:213], 0\n\t" "v_mov_b64 v[214:215], 0\n\t" "s_waitcnt vmcnt(6)\n\t" XT(84, 85) \
    "v_smfmac_f32_16x16x64_bf16 v[224:227], v[240:243], a[0:7], %[ix]\n\t" "ds_write_b128 %[l1], v[240:243] offset:0\n\t" \
    "v_smfmac_f32_16x16x64_bf16 v[228:231], v[240:243], a[8:15], %[ix]\n\t" "v_smfmac_f32_16x16x64_bf16 v[232:235], v[240:243], v[32:39], %[ix]\n\t" \
    "s_waitcnt lgkmcnt(7)\n\t" "v_smfmac_f32_16x16x64_bf16 v[236:239], v[240:243], v[160:167], %[ix]\n\t" "ds_read_b128 v[160:163], %[l0] offset:8192\n\t" \
    "ds_read_b128 v[164:167], %[l0] offset:9216\n\t" "s_waitcnt vmcnt(5)\n\t" "v_smfmac_f32_16x16x64_bf16 v[224:227], v[244:247], a[16:23], %[ix]\n\t" \
    "ds_write_b128 %[l1], v[244:247] offset:1024\n\t" "v_smfmac_f32_16x16x64_bf16 v[228:231], v[244:247], a[24:31], %[ix]\n\t" \
    "v_smfmac_f32_16x16x64_bf16 v[232:235], v[244:247], v[40:47], %[ix]\n\t" "s_waitcnt lgkmcnt(8)\n\t" \
    "v_smfmac_f32_16x16x64_bf16 v[236:239], v[244:247], v[168:175], %[ix]\n\t" "ds_read_b128 v[168:171], %[l0] offset:10240\n\t" \
    "ds_read_b128 v[172:175], %[l0] offset:11264\n\t" "s_waitcnt vmcnt(4)\n\t" "v_smfmac_f32_16x16x64_bf16 v[224:227], v[248:251], a[32:39], %[ix]\n\t" \
    "ds_write_b128 %[l1], v[248:251] offset:2048\n\t" "v_smfmac_f32_16x16x64_bf16 v[228:231], v[248:251], a[40:47], %[ix]\n\t" "s_waitcnt lgkmcnt(3)\n\t" "s_barrier\n\t" \
    XT(86, 87) "v_smfmac_f32_16x16x64_bf16 v[232:235], v[248:251], v[48:55], %[ix]\n\t" "ds_read_b128 v[192:195], %[l1] offset:4096\n\t" \
    "ds_read_b128 v[196:199], %[l1] offset:8192\n\t" "v_smfmac_f32_16x16x64_bf16 v[236:239], v[248:251], v[176:183], %[ix]\n\t" \
    "ds_read_b128 v[176:179], %[l0] offset:12288\n\t" "ds_read_b128 v[180:183], %[l0] offset:13312\n\t" "s_waitcnt vmcnt(3)\n\t" \
    "v_smfmac_f32_16x16x64_bf16 v[224:227], v[252:255], a[48:55], %[ix]\n\t" "ds_write_b128 %[l1], v[252:255] offset:3072\n\t" \
    "v_smfmac_f32_16x16x64_bf16 v[228:231], v[252:255], a[56:63], %[ix]\n\t" "ds_read_b128 v[240:243], %[l1] offset:12288\n\t" \
    "ds_read_b128 v[244:247], %[l1] offset:5120\n\t" "v_smfmac_f32_16x16x64_bf16 v[232:235], v[252:255], v[56:63], %[ix]\n\t" \
    "v_smfmac_f32_16x16x64_bf16 v[236:239], v[252:255], v[184:191], %[ix]\n\t" "ds_read_b128 v[184:187], %[l0] offset:14336\n\t" \
    "ds_read_b128 v[188:191], %[l0] offset:15360\n\t" "s_waitcnt lgkmcnt(8)\n\t" "v_smfmac_f32_16x16x64_bf16 v[224:227], v[192:195], a[64:71], %[ix]\n\t" \
    "v_smfmac_f32_16x16x64_bf16 v[228:231], v[192:195], a[72:79], %[ix]\n\t" "v_smfmac_f32_16x16x64_bf16 v[232:235], v[192:195], v[64:71], %[ix]\n\t" \
    "s_waitcnt lgkmcnt(4)\n\t" "s_barrier\n\t" XT(88, 89) "v_smfmac_f32_16x16x64_bf16 v[236:239], v[192:195], v[160:167], %[ix]\n\t" \
    "ds_read_b128 v[160:163], %[l0] offset:16384\n\t" "ds_read_b128 v[164:167], %[l0] offset:17408\n\t" \
    "v_smfmac_f32_16x16x64_bf16 v[224:227], v[196:199], a[80:87], %[ix]\n\t" "ds_read_b128 v[248:251], %[l1] offset:9216\n\t" \
    "ds_read_b128 v[252:255], %[l1] offset:13312\n\t" "v_smfmac_f32_16x16x64_bf16 v[228:231], v[196:199], a[88:95], %[ix]\n\t" \
    "ds_read_b128 v[192:195], %[l1] offset:6144\n\t" "v_smfmac_f32_16x16x64_bf16 v[232:235], v[196:199], v[72:79], %[ix]\n\t" "ds_read_b128 v[16:19], %[kx] offset:0\n\t" \
    "v_smfmac_f32_16x16x64_bf16 v[236:239], v[196:199], v[168:175], %[ix]\n\t" "ds_read_b128 v[168:171], %[l0] offset:18432\n\t" \
    "ds_read_b128 v[172:175], %[l0] offset:19456\n\t" "s_waitcnt lgkmcnt(11)\n\t" "v_smfmac_f32_16x16x64_bf16 v[224:227], v[240:243], a[96:103], %[ix]\n\t" \
    "ds_read_b128 v[196:199], %[l1] offset:10240\n\t" "v_smfmac_f32_16x16x64_bf16 v[228:231], v[240:243], a[104:111], %[ix]\n\t" \
    "ds_read_b128 v[20:23], %[kx] offset:2048\n\t" "v_smfmac_f32_16x16x64_bf16 v[232:235], v[240:243], v[80:87], %[ix]\n\t" \
    "v_smfmac_f32_16x16x64_bf16 v[236:239], v[240:243], v[176:183], %[ix]\n\t" "ds_read_b128 v[176:179], %[l0] offset:20480\n\t" \
    "ds_read_b128 v[180:183], %[l0] offset:21504\n\t" "s_waitcnt lgkmcnt(14)\n\t" "v_smfmac_f32_16x16x64_bf16 v[224:227], v[244:247], a[112:119], %[ix]\n\t" \
    "ds_read_b128 v[240:243], %[l1] offset:14336\n\t" "ds_read_b128 v[24:27], %[kx] offset:4096\n\t" \
    "v_smfmac_f32_16x16x64_bf16 v[228:231], v[244:247], a[120:127], %[ix]\n\t" "v_smfmac_f32_16x16x64_bf16 v[232:235], v[244:247], v[88:95], %[ix]\n\t" \
    "s_waitcnt lgkmcnt(14)\n\t" "v_smfmac_f32_16x16x64_bf16 v[236:239], v[244:247], v[184:191], %[ix]\n\t" "ds_read_b128 v[184:187], %[l0] offset:22528\n\t" \
    "ds_read_b128 v[188:191], %[l0] offset:23552\n\t" "s_waitcnt lgkmcnt(13)\n\t" "v_smfmac_f32_16x16x64_bf16 v[224:227], v[248:251], a[128:135], %[ix]\n\t" \
    "ds_read_b128 v[244:247], %[l1] offset:7168\n\t" "ds_read_b128 v[28:31], %[kx] offset:6144\n\t" "v_smfmac_f32_16x16x64_bf16 v[228:231], v[248:251], a[136:143], %[ix]\n\t" \
    "v_smfmac_f32_16x16x64_bf16 v[232:235], v[248:251], v[96:103], %[ix]\n\t" "ds_read_b128 v[200:203], %[kx] offset:256\n\t" \
    "v_smfmac_f32_16x16x64_bf16 v[236:239], v[248:251], v[160:167], %[ix]\n\t" "ds_read_b128 v[160:163], %[l0] offset:24576\n\t" \
    "ds_read_b128 v[164:167], %[l0] offset:25600\n\t" "s_waitcnt lgkmcnt(14)\n\t" "v_smfmac_f32_16x16x64_bf16 v[224:227], v[252:255], a[144:151], %[ix]\n\t" \
    "ds_read_b128 v[248:251], %[l1] offset:11264\n\t" "v_smfmac_f32_16x16x64_bf16 v[228:231], v[252:255], a[152:159], %[ix]\n\t" \
    "ds_read_b128 v[204:207], %[kx] offset:2304\n\t" "v_smfmac_f32_16x16x64_bf16 v[232:235], v[252:255], v[104:111], %[ix]\n\t" "s_waitcnt lgkmcnt(14)\n\t" \
    "v_smfmac_f32_16x16x64_bf16 v[236:239], v[252:255], v[168:175], %[ix]\n\t" "ds_read_b128 v[168:171], %[l0] offset:26624\n\t" \
    "ds_read_b128 v[172:175], %[l0] offset:27648\n\t" "v_smfmac_f32_16x16x64_bf16 v[224:227], v[192:195], a[160:167], %[ix]\n\t" \
    "ds_read_b128 v[252:255], %[l1] offset:15360\n\t" "v_smfmac_f32_16x16x64_bf16 v[228:231], v[192:195], a[168:175], %[ix]\n\t" \
    "v_smfmac_f32_16x16x64_bf16 v[224:227], v[196:199], a[176:183], %[ix]\n\t" "v_smfmac_f32_16x16x64_bf16 v[228:231], v[196:199], a[184:191], %[ix]\n\t" \
    "s_waitcnt lgkmcnt(13)\n\t" "v_smfmac_f32_16x16x64_bf16 v[224:227], v[240:243], a[192:199], %[ix]\n\t" \
    "v_smfmac_f32_16x16x64_bf16 v[228:231], v[240:243], a[200:207], %[ix]\n\t" "s_waitcnt vmcnt(0)\n\t" "v_readfirstlane_b32 s62, v217\n\t" "s_waitcnt lgkmcnt(9)\n\t" \
    "v_smfmac_f32_16x16x64_bf16 v[224:227], v[244:247], a[208:215], %[ix]\n\t" "v_smfmac_f32_16x16x64_bf16 v[228:231], v[244:247], a[216:223], %[ix]\n\t" \
    "s_max_u32 %[sq], %[sq], s62\n\t" "s_waitcnt lgkmcnt(4)\n\t" "v_smfmac_f32_16x16x64_bf16 v[224:227], v[248:251], a[224:231], %[ix]\n\t" \
    "v_smfmac_f32_16x16x64_bf16 v[228:231], v[248:251], a[232:239], %[ix]\n\t" "s_waitcnt lgkmcnt(0)\n\t" \
    "v_smfmac_f32_16x16x64_bf16 v[224:227], v[252:255], a[240:247], %[ix]\n\t" "s_nop 1\n\t" "v_smfmac_f32_16x16x64_bf16 v[228:231], v[252:255], a[248:255], %[ix]\n\t" \
    XT(90, 91) "s_nop 1\n\t" "v_smfmac_f32_16x16x64_bf16 v[232:235], v[192:195], v[112:119], %[ix]\n\t" "s_nop 1\n\t" \
    "v_smfmac_f32_16x16x64_bf16 v[236:239], v[192:195], v[176:183], %[ix]\n\t" "ds_read_b128 v[176:179], %[l0] offset:28672\n\t" "v_add_f32 v224, v224, v225\n\t" \
    "v_add_f32 v226, v226, v227\n\t" "v_smfmac_f32_16x16x64_bf16 v[232:235], v[196:199], v[120:127], %[ix]\n\t" "ds_read_b128 v[180:183], %[l0] offset:29696\n\t" \
    "v_add_f32 v228, v228, v229\n\t" "v_add_f32 v230, v230, v231\n\t" "v_cmp_u_f32 vcc, v224, v226\n\t" \
    "v_smfmac_f32_16x16x64_bf16 v[236:239], v[196:199], v[184:191], %[ix]\n\t" "ds_read_b128 v[184:187], %[l0] offset:30720\n\t" "s_cbranch_vccnz 11f\n\t" "12:\n\t" \
    "v_cvt_pk_bf16_f32 v218, v224, v228\n\t" "v_cvt_pk_bf16_f32 v219, v226, v230\n\t" "v_smfmac_f32_16x16x64_bf16 v[232:235], v[240:243], v[128:135], %[ix]\n\t" \
    "ds_write2_b32 %[qw], v218, v219 offset1:8\n\t" "ds_read_b128 v[188:191], %[l0] offset:31744\n\t" \
    "v_smfmac_f32_16x16x64_bf16 v[236:239], v[240:243], v[160:167], %[ix]\n\t" "ds_read_b128 v[192:195], %[kx] offset:4352\n\t" \
    "ds_read_b128 v[196:199], %[kx] offset:6400\n\t" "v_smfmac_f32_16x16x64_bf16 v[232:235], v[244:247], v[136:143], %[ix]\n\t" \
    "v_smfmac_f32_16x16x64_bf16 v[236:239], v[244:247], v[168:175], %[ix]\n\t" "s_waitcnt lgkmcnt(3)\n\t" "s_barrier\n\t" \
    "v_smfmac_f32_16x16x64_bf16 v[232:235], v[248:251], v[144:151], %[ix]\n\t" "ds_read_b128 v[216:219], %[l1] offset:24576\n\t" \
    "ds_read_b128 v[220:223], %[l1] offset:25600\n\t" "v_smfmac_f32_16x16x64_bf16 v[236:239], v[248:251], v[176:183], %[ix]\n\t" \
    "v_smfmac_f32_16x16x64_bf16 v[232:235], v[252:255], v[152:159], %[ix]\n\t" "s_waitcnt lgkmcnt(4)\n\t" \
    "v_smfmac_f32_16x16x64_bf16 v[236:239], v[252:255], v[184:191], %[ix]\n\t" XT(92, 93) "s_waitcnt lgkmcnt(1)\n\t" \
    "v_smfmac_f32_16x16x64_bf16 v[208:211], v[216:219], v[16:23], %[ix]\n\t" "s_nop 1\n\t" "v_smfmac_f32_16x16x64_bf16 v[212:215], v[216:219], v[200:207], %[ix]\n\t" \
    "s_waitcnt lgkmcnt(0)\n\t" "s_nop 0\n\t" "v_add_f32 v232, v232, v233\n\t" "v_add_f32 v234, v234, v235\n\t" "v_add_f32 v236, v236, v237\n\t" \
    "v_add_f32 v238, v238, v239\n\t" "v_cvt_pk_bf16_f32 v232, v232, v236\n\t" "v_cvt_pk_bf16_f32 v233, v234, v238\n\t" "ds_write2_b32 %[kw], v232, v233 offset1:68\n\t" \
    "v_smfmac_f32_16x16x64_bf16 v[208:211], v[220:223], v[24:31], %[ix]\n\t" "v_smfmac_f32_16x16x64_bf16 v[212:215], v[220:223], v[192:199], %[ix]\n\t" \
    "v_mov_b64 v[224:225], 0\n\t" "v_mov_b64 v[226:227], 0\n\t" "s_waitcnt lgkmcnt(0)\n\t" "s_barrier\n\t" XT(94, 95) "ds_read_b128 v[160:163], %[km] offset:0\n\t" \
    "ds_read_b128 v[164:167], %[km] offset:64\n\t" "ds_read_b128 v[168:171], %[km] offset:128\n\t" "ds_read_b128 v[172:175], %[km] offset:192\n\t" \
    "v_add_f32 v178, v208, v209\n\t" "v_add_f32 v179, v210, v211\n\t" "v_add_f32 v180, v212, v213\n\t" "v_add_f32 v181, v214, v215\n\t" "v_max_f32 v182, v178, v180\n\t" \
    "v_max_f32 v183, v179, v181\n\t" "s_nop 0\n\t" "v_max_f32_dpp v182, v182, v182 row_ror:8 row_mask:0xf bank_mask:0xf\n\t" \
    "v_max_f32_dpp v183, v183, v183 row_ror:8 row_mask:0xf bank_mask:0xf\n\t" "s_nop 0\n\t" "v_max_f32_dpp v182, v182, v182 row_ror:4 row_mask:0xf bank_mask:0xf\n\t" \
    "v_max_f32_dpp v183, v183, v183 row_ror:4 row_mask:0xf bank_mask:0xf\n\t" "s_nop 0\n\t" "v_max_f32_dpp v182, v182, v182 row_ror:2 row_mask:0xf bank_mask:0xf\n\t" \
    "v_max_f32_dpp v183, v183, v183 row_ror:2 row_mask:0xf bank_mask:0xf\n\t" "s_nop 0\n\t" "v_max_f32_dpp v182, v182, v182 row_ror:1 row_mask:0xf bank_mask:0xf\n\t" \
    "v_max_f32_dpp v183, v183, v183 row_ror:1 row_mask:0xf bank_mask:0xf\n\t" "s_waitcnt lgkmcnt(2)\n\t" \
    "v_smfmac_f32_16x16x64_bf16 v[224:227], v[216:219], v[160:167], %[ix]\n\t" "s_waitcnt lgkmcnt(0)\n\t" \
    "v_smfmac_f32_16x16x64_bf16 v[224:227], v[220:223], v[168:175], %[ix]\n\t" "s_nop 7\n\t" "v_add_f32 v176, v224, v225\n\t" "v_add_f32 v177, v226, v227\n\t" \
    "v_max_f32 v182, v176, v182\n\t" "v_max_f32 v183, v177, v183\n\t" "s_nop 0\n\t" "v_max_f32_dpp v182, v182, v182 row_ror:4 row_mask:0xf bank_mask:0xf\n\t" \
    "v_max_f32_dpp v183, v183, v183 row_ror:4 row_mask:0xf bank_mask:0xf\n\t" "s_nop 0\n\t" "v_max_f32_dpp v182, v182, v182 row_ror:2 row_mask:0xf bank_mask:0xf\n\t" \
    "v_max_f32_dpp v183, v183, v183 row_ror:2 row_mask:0xf bank_mask:0xf\n\t" "s_nop 0\n\t" "v_max_f32_dpp v182, v182, v182 row_ror:1 row_mask:0xf bank_mask:0xf\n\t" \
    "v_max_f32_dpp v183, v183, v183 row_ror:1 row_mask:0xf bank_mask:0xf\n\t" "v_pk_add_f32 v[178:179], v[178:179], v[182:183] neg_lo:[0,1] neg_hi:[0,1]\n\t" \
    "v_pk_add_f32 v[180:181], v[180:181], v[182:183] neg_lo:[0,1] neg_hi:[0,1]\n\t" "v_pk_add_f32 v[176:177], v[176:177], v[182:183] neg_lo:[0,1] neg_hi:[0,1]\n\t" \
    "v_pk_mul_f32 v[178:179], v[178:179], s[66:67]\n\t" "v_pk_mul_f32 v[180:181], v[180:181], s[66:67]\n\t" "v_pk_mul_f32 v[176:177], v[176:177], s[66:67]\n\t" \
    "v_exp_f32 v178, v178\n\t" "v_exp_f32 v179, v179\n\t" "v_exp_f32 v180, v180\n\t" "v_exp_f32 v181, v181\n\t" "v_exp_f32 v176, v176\n\t" \
    "v_pk_add_f32 v[182:183], v[178:179], v[180:181]\n\t" "v_exp_f32 v177, v177\n\t" "s_nop 0\n\t" "v_add_f32_dpp v182, v182, v182 row_ror:8 row_mask:0xf bank_mask:0xf\n\t" \
    "v_add_f32_dpp v183, v183, v183 row_ror:8 row_mask:0xf bank_mask:0xf\n\t" "v_add_f32 v182, v182, v176\n\t" "v_add_f32 v183, v183, v177\n\t" "s_nop 0\n\t" \
    "v_add_f32_dpp v182, v182, v182 row_ror:4 row_mask:0xf bank_mask:0xf\n\t" "v_add_f32_dpp v183, v183, v183 row_ror:4 row_mask:0xf bank_mask:0xf\n\t" "s_nop 0\n\t" \
    "v_add_f32_dpp v182, v182, v182 row_ror:2 row_mask:0xf bank_mask:0xf\n\t" "v_add_f32_dpp v183, v183, v183 row_ror:2 row_mask:0xf bank_mask:0xf\n\t" "s_nop 0\n\t" \
    "v_add_f32_dpp v182, v182, v182 row_ror:1 row_mask:0xf bank_mask:0xf\n\t" "v_add_f32_dpp v183, v183, v183 row_ror:1 row_mask:0xf bank_mask:0xf\n\t" \
    "v_rcp_f32 v182, v182\n\t" "v_rcp_f32 v183, v183\n\t" "s_nop 0\n\t" "v_pk_mul_f32 v[176:177], v[176:177], v[182:183]\n\t" \
    "v_pk_mul_f32 v[178:179], v[178:179], v[182:183]\n\t" "v_pk_mul_f32 v[180:181], v[180:181], v[182:183]\n\t" "v_cvt_pk_bf16_f32 v176, v176, v177\n\t" \
    "v_cvt_pk_bf16_f32 v178, v178, v179\n\t" "v_cvt_pk_bf16_f32 v180, v180, v181\n\t" "v_mov_b32_dpp v177, v176 quad_perm:[1,0,3,2] row_mask:0xf bank_mask:0xf\n\t" \
    "v_mov_b32_dpp v179, v178 quad_perm:[1,0,3,2] row_mask:0xf bank_mask:0xf\n\t" "v_mov_b32_dpp v181, v180 quad_perm:[1,0,3,2] row_mask:0xf bank_mask:0xf\n\t" \
    "v_perm_b32 v184, v179, v178, %[ps]\n\t" "v_perm_b32 v185, v181, v180, %[ps]\n\t" "buffer_store_dwordx2 v[184:185], %[px], %[rs], %[pb] offen sc1\n\t" \
    "v_perm_b32 v186, v177, v176, %[ps]\n\t" "buffer_store_dword v186, %[pm], %[rs], %[pb] offen sc1\n\t" XT(96, 97) "s_branch 99f\n\t" "11:\n\t" "s_mov_b64 s[62:63], 0\n\t" \
    "ds_read_b128 v[208:211], %[l1] offset:0\n\t" "ds_read_b128 v[212:215], %[l1] offset:1024\n\t" "ds_read_b128 v[216:219], %[l1] offset:2048\n\t" \
    "ds_read_b128 v[220:223], %[l1] offset:3072\n\t" "s_waitcnt lgkmcnt(3)\n\t" "v_max3_u32 v208, v208, v209, v210\n\t" "v_max_u32 v208, v208, v211\n\t" \
    "v_cmp_eq_u32 vcc, -1, v208\n\t" "ds_read_b128 v[208:211], %[l1] offset:4096\n\t" "s_or_b64 s[62:63], s[62:63], vcc\n\t" "s_waitcnt lgkmcnt(3)\n\t" \
    "v_max3_u32 v212, v212, v213, v214\n\t" "v_max_u32 v212, v212, v215\n\t" "v_cmp_eq_u32 vcc, -1, v212\n\t" "ds_read_b128 v[212:215], %[l1] offset:5120\n\t" \
    "s_or_b64 s[62:63], s[62:63], vcc\n\t" "s_waitcnt lgkmcnt(3)\n\t" "v_max3_u32 v216, v216, v217, v218\n\t" "v_max_u32 v216, v216, v219\n\t" \
    "v_cmp_eq_u32 vcc, -1, v216\n\t" "ds_read_b128 v[216:219], %[l1] offset:6144\n\t" "s_or_b64 s[62:63], s[62:63], vcc\n\t" "s_waitcnt lgkmcnt(3)\n\t" \
    "v_max3_u32 v220, v220, v221, v222\n\t" "v_max_u32 v220, v220, v223\n\t" "v_cmp_eq_u32 vcc, -1, v220\n\t" "ds_read_b128 v[220:223], %[l1] offset:7168\n\t" \
    "s_or_b64 s[62:63], s[62:63], vcc\n\t" "s_waitcnt lgkmcnt(3)\n\t" "v_max3_u32 v208, v208, v209, v210\n\t" "v_max_u32 v208, v208, v211\n\t" \
    "v_cmp_eq_u32 vcc, -1, v208\n\t" "ds_read_b128 v[208:211], %[l1] offset:8192\n\t" "s_or_b64 s[62:63], s[62:63], vcc\n\t" "s_waitcnt lgkmcnt(3)\n\t" \
    "v_max3_u32 v212, v212, v213, v214\n\t" "v_max_u32 v212, v212, v215\n\t" "v_cmp_eq_u32 vcc, -1, v212\n\t" "ds_read_b128 v[212:215], %[l1] offset:9216\n\t" \
    "s_or_b64 s[62:63], s[62:63], vcc\n\t" "s_waitcnt lgkmcnt(3)\n\t" "v_max3_u32 v216, v216, v217, v218\n\t" "v_max_u32 v216, v216, v219\n\t" \
    "v_cmp_eq_u32 vcc, -1, v216\n\t" "ds_read_b128 v[216:219], %[l1] offset:10240\n\t" "s_or_b64 s[62:63], s[62:63], vcc\n\t" "s_waitcnt lgkmcnt(3)\n\t" \
    "v_max3_u32 v220, v220, v221, v222\n\t" "v_max_u32 v220, v220, v223\n\t" "v_cmp_eq_u32 vcc, -1, v220\n\t" "ds_read_b128 v[220:223], %[l1] offset:11264\n\t" \
    "s_or_b64 s[62:63], s[62:63], vcc\n\t" "s_waitcnt lgkmcnt(3)\n\t" "v_max3_u32 v208, v208, v209, v210\n\t" "v_max_u32 v208, v208, v211\n\t" \
    "v_cmp_eq_u32 vcc, -1, v208\n\t" "ds_read_b128 v[208:211], %[l1] offset:12288\n\t" "s_or_b64 s[62:63], s[62:63], vcc\n\t" "s_waitcnt lgkmcnt(3)\n\t" \
    "v_max3_u32 v212, v212, v213, v214\n\t" "v_max_u32 v212, v212, v215\n\t" "v_cmp_eq_u32 vcc, -1, v212\n\t" "ds_read_b128 v[212:215], %[l1] offset:13312\n\t" \
    "s_or_b64 s[62:63], s[62:63], vcc\n\t" "s_waitcnt lgkmcnt(3)\n\t" "v_max3_u32 v216, v216, v217, v218\n\t" "v_max_u32 v216, v216, v219\n\t" \
    "v_cmp_eq_u32 vcc, -1, v216\n\t" "ds_read_b128 v[216:219], %[l1] offset:14336\n\t" "s_or_b64 s[62:63], s[62:63], vcc\n\t" "s_waitcnt lgkmcnt(3)\n\t" \
    "v_max3_u32 v220, v220, v221, v222\n\t" "v_max_u32 v220, v220, v223\n\t" "v_cmp_eq_u32 vcc, -1, v220\n\t" "ds_read_b128 v[220:223], %[l1] offset:15360\n\t" \
    "s_or_b64 s[62:63], s[62:63], vcc\n\t" "s_waitcnt lgkmcnt(3)\n\t" "v_max3_u32 v208, v208, v209, v210\n\t" "v_max_u32 v208, v208, v211\n\t" \
    "v_cmp_eq_u32 vcc, -1, v208\n\t" "s_or_b64 s[62:63], s[62:63], vcc\n\t" "s_waitcnt lgkmcnt(2)\n\t" "v_max3_u32 v212, v212, v213, v214\n\t" \
    "v_max_u32 v212, v212, v215\n\t" "v_cmp_eq_u32 vcc, -1, v212\n\t" "s_or_b64 s[62:63], s[62:63], vcc\n\t" "s_waitcnt lgkmcnt(1)\n\t" \
    "v_max3_u32 v216, v216, v217, v218\n\t" "v_max_u32 v216, v216, v219\n\t" "v_cmp_eq_u32 vcc, -1, v216\n\t" "s_or_b64 s[62:63], s[62:63], vcc\n\t" \
    "s_waitcnt lgkmcnt(0)\n\t" "v_max3_u32 v220, v220, v221, v222\n\t" "v_max_u32 v220, v220, v223\n\t" "v_cmp_eq_u32 vcc, -1, v220\n\t" \
    "s_or_b64 s[62:63], s[62:63], vcc\n\t" "v_mov_b64 v[208:209], 0\n\t" "v_mov_b64 v[210:211], 0\n\t" "v_mov_b64 v[212:213], 0\n\t" "v_mov_b64 v[214:215], 0\n\t" \
    "s_cmp_eq_u64 s[62:63], 0\n\t" "s_cbranch_scc1 12b\n\t" "s_cmp_eq_u32 %[rt], 0\n\t" "s_cbranch_scc1 12b\n\t" "s_sub_u32 %[rt], %[rt], 1\n\t" \
    "v_xor_b32 v216, 0x400, %[mo]\n\t" XCNT(100) "14:\n\t" "buffer_load_dwordx4 v[240:243], %[mo], %[rs], %[sb] offen sc1\n\t" \
    "buffer_load_dwordx4 v[244:247], v216, %[rs], %[sb] offen sc1\n\t" "buffer_load_dwordx4 v[248:251], %[mo], %[rs], s60 offen sc1\n\t" \
    "buffer_load_dwordx4 v[252:255], v216, %[rs], s60 offen sc1\n\t" "s_waitcnt vmcnt(0)\n\t" "v_max_u32 v220, v240, v241\n\t" "v_max3_u32 v220, v220, v242, v243\n\t" \
    "v_max3_u32 v220, v220, v244, v245\n\t" "v_max3_u32 v220, v220, v246, v247\n\t" "v_max3_u32 v220, v220, v248, v249\n\t" "v_max3_u32 v220, v220, v250, v251\n\t" \
    "v_max3_u32 v220, v220, v252, v253\n\t" "v_max3_u32 v220, v220, v254, v255\n\t" "v_cmp_eq_u32 vcc, -1, v220\n\t" "s_cbranch_vccz 15f\n\t" "s_cmp_eq_u32 %[bd], 0\n\t" \
    "s_cbranch_scc1 15f\n\t" "s_sub_u32 %[bd], %[bd], 1\n\t" XCNT(101) "s_sleep 1\n\t" "s_branch 14b\n\t" "15:\n\t" "s_barrier\n\t" "s_branch 10b\n\t" "99:\n\t"
#define QK_STEP1 \
    "v_xor_b32 v216, 0x400, %[mo]\n\t" "s_xor_b32 s60, %[sb], 0x800\n\t" "s_add_u32 s61, %[qb], 0x1000\n\t" "s_mov_b32 s66, 0x3e0293ee\n\t" "s_mov_b32 s67, 0x3e0293ee\n\t" \
    XT(80, 81) XT(98, 99) "s_barrier\n\t" XT(82, 83) "20:\n\t" "buffer_load_dwordx4 v[240:243], %[mo], %[rs], %[sb] offen sc1\n\t" "v_mov_b64 v[224:225], 0\n\t" \
    "v_mov_b64 v[226:227], 0\n\t" "v_mov_b64 v[228:229], 0\n\t" "v_mov_b64 v[230:231], 0\n\t" "v_mov_b64 v[232:233], 0\n\t" "v_mov_b64 v[234:235], 0\n\t" \
    "v_mov_b64 v[236:237], 0\n\t" "v_mov_b64 v[238:239], 0\n\t" "buffer_load_dwordx4 v[244:247], v216, %[rs], %[sb] offen sc1\n\t" \
    "ds_read_b128 v[160:163], %[l0] offset:0\n\t" "ds_read_b128 v[164:167], %[l0] offset:1024\n\t" "ds_read_b128 v[168:171], %[l0] offset:2048\n\t" \
    "ds_read_b128 v[172:175], %[l0] offset:3072\n\t" "s_nop 3\n\t" "buffer_load_dwordx4 v[248:251], %[mo], %[rs], s60 offen sc1\n\t" \
    "ds_read_b128 v[176:179], %[l0] offset:4096\n\t" "ds_read_b128 v[180:183], %[l0] offset:5120\n\t" "ds_read_b128 v[184:187], %[l0] offset:6144\n\t" \
    "ds_read_b128 v[188:191], %[l0] offset:7168\n\t" "s_nop 3\n\t" "buffer_load_dwordx4 v[252:255], v216, %[rs], s60 offen sc1\n\t" "v_mov_b64 v[220:221], -1\n\t" \
    "v_mov_b64 v[222:223], -1\n\t" "buffer_store_dwordx2 v[220:221], %[px], %[rs], %[tr] offen sc1\n\t" "buffer_store_dwordx2 v[220:221], %[pm], %[rs], %[tr] offen sc1\n\t" \
    "v_mov_b32 v208, 0xd028965f\n\t" "v_mov_b32 v209, 0xff800000\n\t" "s_mov_b32 s62, 0\n\t" "s_mov_b32 s63, -1\n\t" "v_mov_b32 v210, 0\n\t" "v_cmp_gt_i32 vcc, 0, %[ps]\n\t" \
    "v_cndmask_b32 v204, 0, v209, s[62:63]\n\t" "s_nop 0\n\t" "v_cndmask_b32 v200, 0, v208, vcc\n\t" "v_cmp_gt_i32 vcc, 1, %[ps]\n\t" \
    "v_cndmask_b32 v205, 0, v209, s[62:63]\n\t" "s_nop 0\n\t" "v_cndmask_b32 v201, 0, v208, vcc\n\t" "v_cmp_gt_i32 vcc, 2, %[ps]\n\t" \
    "v_cndmask_b32 v206, 0, v209, s[62:63]\n\t" "s_nop 0\n\t" "v_cndmask_b32 v202, 0, v208, vcc\n\t" "v_cmp_gt_i32 vcc, 3, %[ps]\n\t" \
    "v_cndmask_b32 v207, 0, v209, s[62:63]\n\t" "s_nop 0\n\t" "v_cndmask_b32 v203, 0, v208, vcc\n\t" "s_waitcnt vmcnt(5)\n\t" XT(84, 85) \
    "v_smfmac_f32_16x16x64_bf16 v[224:227], v[240:243], a[0:7], %[ix]\n\t" "ds_write_b128 %[l1], v[240:243] offset:4096\n\t" \
    "v_smfmac_f32_16x16x64_bf16 v[228:231], v[240:243], a[8:15], %[ix]\n\t" "v_smfmac_f32_16x16x64_bf16 v[232:235], v[240:243], v[32:39], %[ix]\n\t" \
    "s_waitcnt lgkmcnt(7)\n\t" "v_smfmac_f32_16x16x64_bf16 v[236:239], v[240:243], v[160:167], %[ix]\n\t" "ds_read_b128 v[160:163], %[l0] offset:8192\n\t" \
    "ds_read_b128 v[164:167], %[l0] offset:9216\n\t" "s_waitcnt vmcnt(4)\n\t" "v_smfmac_f32_16x16x64_bf16 v[224:227], v[244:247], a[16:23], %[ix]\n\t" \
    "ds_write_b128 %[l1], v[244:247] offset:5120\n\t" "v_smfmac_f32_16x16x64_bf16 v[228:231], v[244:247], a[24:31], %[ix]\n\t" \
    "v_smfmac_f32_16x16x64_bf16 v[232:235], v[244:247], v[40:47], %[ix]\n\t" "s_waitcnt lgkmcnt(8)\n\t" \
    "v_smfmac_f32_16x16x64_bf16 v[236:239], v[244:247], v[168:175], %[ix]\n\t" "ds_read_b128 v[168:171], %[l0] offset:10240\n\t" \
    "ds_read_b128 v[172:175], %[l0] offset:11264\n\t" "s_waitcnt vmcnt(3)\n\t" "v_smfmac_f32_16x16x64_bf16 v[224:227], v[248:251], a[32:39], %[ix]\n\t" \
    "ds_write_b128 %[l1], v[248:251] offset:6144\n\t" "v_smfmac_f32_16x16x64_bf16 v[228:231], v[248:251], a[40:47], %[ix]\n\t" "s_waitcnt lgkmcnt(3)\n\t" "s_barrier\n\t" \
    XT(86, 87) "v_smfmac_f32_16x16x64_bf16 v[232:235], v[248:251], v[48:55], %[ix]\n\t" "ds_read_b128 v[192:195], %[l1] offset:8192\n\t" \
    "ds_read_b128 v[196:199], %[l1] offset:12288\n\t" "v_smfmac_f32_16x16x64_bf16 v[236:239], v[248:251], v[176:183], %[ix]\n\t" \
    "ds_read_b128 v[176:179], %[l0] offset:12288\n\t" "ds_read_b128 v[180:183], %[l0] offset:13312\n\t" "s_waitcnt vmcnt(2)\n\t" \
    "v_smfmac_f32_16x16x64_bf16 v[224:227], v[252:255], a[48:55], %[ix]\n\t" "ds_write_b128 %[l1], v[252:255] offset:7168\n\t" \
    "v_smfmac_f32_16x16x64_bf16 v[228:231], v[252:255], a[56:63], %[ix]\n\t" "ds_read_b128 v[240:243], %[l1] offset:0\n\t" "ds_read_b128 v[244:247], %[l1] offset:9216\n\t" \
    "v_smfmac_f32_16x16x64_bf16 v[232:235], v[252:255], v[56:63], %[ix]\n\t" "v_smfmac_f32_16x16x64_bf16 v[236:239], v[252:255], v[184:191], %[ix]\n\t" \
    "ds_read_b128 v[184:187], %[l0] offset:14336\n\t" "ds_read_b128 v[188:191], %[l0] offset:15360\n\t" "s_waitcnt lgkmcnt(8)\n\t" \
    "v_smfmac_f32_16x16x64_bf16 v[224:227], v[192:195], a[64:71], %[ix]\n\t" "v_smfmac_f32_16x16x64_bf16 v[228:231], v[192:195], a[72:79], %[ix]\n\t" \
    "v_smfmac_f32_16x16x64_bf16 v[232:235], v[192:195], v[64:71], %[ix]\n\t" "s_waitcnt lgkmcnt(4)\n\t" "s_barrier\n\t" XT(88, 89) \
    "v_smfmac_f32_16x16x64_bf16 v[236:239], v[192:195], v[160:167], %[ix]\n\t" "ds_read_b128 v[160:163], %[l0] offset:16384\n\t" \
    "ds_read_b128 v[164:167], %[l0] offset:17408\n\t" "v_smfmac_f32_16x16x64_bf16 v[224:227], v[196:199], a[80:87], %[ix]\n\t" \
    "ds_read_b128 v[248:251], %[l1] offset:13312\n\t" "ds_read_b128 v[252:255], %[l1] offset:1024\n\t" \
    "v_smfmac_f32_16x16x64_bf16 v[228:231], v[196:199], a[88:95], %[ix]\n\t" "ds_read_b128 v[192:195], %[l1] offset:10240\n\t" \
    "v_smfmac_f32_16x16x64_bf16 v[232:235], v[196:199], v[72:79], %[ix]\n\t" "buffer_load_dwordx4 v[16:19], %[hq], %[rs], %[qb] offen offset:0 sc1\n\t" \
    "v_smfmac_f32_16x16x64_bf16 v[236:239], v[196:199], v[168:175], %[ix]\n\t" "ds_read_b128 v[168:171], %[l0] offset:18432\n\t" \
    "ds_read_b128 v[172:175], %[l0] offset:19456\n\t" "s_waitcnt lgkmcnt(10)\n\t" "v_smfmac_f32_16x16x64_bf16 v[224:227], v[240:243], a[96:103], %[ix]\n\t" \
    "ds_read_b128 v[196:199], %[l1] offset:14336\n\t" "v_smfmac_f32_16x16x64_bf16 v[228:231], v[240:243], a[104:111], %[ix]\n\t" \
    "v_smfmac_f32_16x16x64_bf16 v[232:235], v[240:243], v[80:87], %[ix]\n\t" "buffer_load_dwordx4 v[20:23], %[hq], %[rs], %[qb] offen offset:2048 sc1\n\t" \
    "v_smfmac_f32_16x16x64_bf16 v[236:239], v[240:243], v[176:183], %[ix]\n\t" "ds_read_b128 v[176:179], %[l0] offset:20480\n\t" \
    "ds_read_b128 v[180:183], %[l0] offset:21504\n\t" "s_waitcnt lgkmcnt(12)\n\t" "v_smfmac_f32_16x16x64_bf16 v[224:227], v[244:247], a[112:119], %[ix]\n\t" \
    "ds_read_b128 v[240:243], %[l1] offset:2048\n\t" "v_smfmac_f32_16x16x64_bf16 v[228:231], v[244:247], a[120:127], %[ix]\n\t" \
    "v_smfmac_f32_16x16x64_bf16 v[232:235], v[244:247], v[88:95], %[ix]\n\t" "buffer_load_dwordx4 v[24:27], %[hq], %[rs], s61 offen offset:0 sc1\n\t" \
    "s_waitcnt lgkmcnt(11)\n\t" "v_smfmac_f32_16x16x64_bf16 v[236:239], v[244:247], v[184:191], %[ix]\n\t" "ds_read_b128 v[184:187], %[l0] offset:22528\n\t" \
    "ds_read_b128 v[188:191], %[l0] offset:23552\n\t" "s_waitcnt lgkmcnt(10)\n\t" "v_smfmac_f32_16x16x64_bf16 v[224:227], v[248:251], a[128:135], %[ix]\n\t" \
    "ds_read_b128 v[244:247], %[l1] offset:11264\n\t" "v_smfmac_f32_16x16x64_bf16 v[228:231], v[248:251], a[136:143], %[ix]\n\t" \
    "v_smfmac_f32_16x16x64_bf16 v[232:235], v[248:251], v[96:103], %[ix]\n\t" "buffer_load_dwordx4 v[28:31], %[hq], %[rs], s61 offen offset:2048 sc1\n\t" \
    "v_smfmac_f32_16x16x64_bf16 v[236:239], v[248:251], v[160:167], %[ix]\n\t" "ds_read_b128 v[160:163], %[l0] offset:24576\n\t" \
    "ds_read_b128 v[164:167], %[l0] offset:25600\n\t" "s_waitcnt lgkmcnt(12)\n\t" "v_smfmac_f32_16x16x64_bf16 v[224:227], v[252:255], a[144:151], %[ix]\n\t" \
    "ds_read_b128 v[248:251], %[l1] offset:15360\n\t" "v_smfmac_f32_16x16x64_bf16 v[228:231], v[252:255], a[152:159], %[ix]\n\t" \
    "v_smfmac_f32_16x16x64_bf16 v[232:235], v[252:255], v[104:111], %[ix]\n\t" "s_waitcnt lgkmcnt(10)\n\t" \
    "v_smfmac_f32_16x16x64_bf16 v[236:239], v[252:255], v[168:175], %[ix]\n\t" "ds_read_b128 v[168:171], %[l0] offset:26624\n\t" \
    "ds_read_b128 v[172:175], %[l0] offset:27648\n\t" "v_smfmac_f32_16x16x64_bf16 v[224:227], v[192:195], a[160:167], %[ix]\n\t" \
    "ds_read_b128 v[252:255], %[l1] offset:3072\n\t" "v_smfmac_f32_16x16x64_bf16 v[228:231], v[192:195], a[168:175], %[ix]\n\t" "s_waitcnt lgkmcnt(12)\n\t" \
    "v_smfmac_f32_16x16x64_bf16 v[224:227], v[196:199], a[176:183], %[ix]\n\t" "v_smfmac_f32_16x16x64_bf16 v[228:231], v[196:199], a[184:191], %[ix]\n\t" \
    "s_waitcnt lgkmcnt(9)\n\t" "v_smfmac_f32_16x16x64_bf16 v[224:227], v[240:243], a[192:199], %[ix]\n\t" \
    "v_smfmac_f32_16x16x64_bf16 v[228:231], v[240:243], a[200:207], %[ix]\n\t" "s_waitcnt lgkmcnt(6)\n\t" \
    "v_smfmac_f32_16x16x64_bf16 v[224:227], v[244:247], a[208:215], %[ix]\n\t" "v_smfmac_f32_16x16x64_bf16 v[228:231], v[244:247], a[216:223], %[ix]\n\t" \
    "s_waitcnt vmcnt(4)\n\t" "s_waitcnt lgkmcnt(3)\n\t" "v_smfmac_f32_16x16x64_bf16 v[224:227], v[248:251], a[224:231], %[ix]\n\t" \
    "v_smfmac_f32_16x16x64_bf16 v[228:231], v[248:251], a[232:239], %[ix]\n\t" "s_waitcnt lgkmcnt(0)\n\t" \
    "v_smfmac_f32_16x16x64_bf16 v[224:227], v[252:255], a[240:247], %[ix]\n\t" "s_nop 1\n\t" "v_smfmac_f32_16x16x64_bf16 v[228:231], v[252:255], a[248:255], %[ix]\n\t" \
    XT(90, 91) "s_nop 1\n\t" "v_smfmac_f32_16x16x64_bf16 v[232:235], v[192:195], v[112:119], %[ix]\n\t" "s_nop 1\n\t" \
    "v_smfmac_f32_16x16x64_bf16 v[236:239], v[192:195], v[176:183], %[ix]\n\t" "ds_read_b128 v[176:179], %[l0] offset:28672\n\t" "v_add_f32 v224, v224, v225\n\t" \
    "v_add_f32 v226, v226, v227\n\t" "v_smfmac_f32_16x16x64_bf16 v[232:235], v[196:199], v[120:127], %[ix]\n\t" "ds_read_b128 v[180:183], %[l0] offset:29696\n\t" \
    "v_add_f32 v228, v228, v229\n\t" "v_add_f32 v230, v230, v231\n\t" "v_cmp_u_f32 vcc, v224, v226\n\t" \
    "v_smfmac_f32_16x16x64_bf16 v[236:239], v[196:199], v[184:191], %[ix]\n\t" "ds_read_b128 v[184:187], %[l0] offset:30720\n\t" "s_cbranch_vccnz 21f\n\t" "22:\n\t" \
    "v_cvt_pk_bf16_f32 v218, v224, v228\n\t" "v_cvt_pk_bf16_f32 v219, v226, v230\n\t" "v_smfmac_f32_16x16x64_bf16 v[232:235], v[240:243], v[128:135], %[ix]\n\t" \
    "ds_write2_b32 %[qw], v218, v219 offset1:8\n\t" "ds_read_b128 v[188:191], %[l0] offset:31744\n\t" \
    "v_smfmac_f32_16x16x64_bf16 v[236:239], v[240:243], v[160:167], %[ix]\n\t" "v_smfmac_f32_16x16x64_bf16 v[232:235], v[244:247], v[136:143], %[ix]\n\t" \
    "v_smfmac_f32_16x16x64_bf16 v[236:239], v[244:247], v[168:175], %[ix]\n\t" "s_waitcnt lgkmcnt(1)\n\t" "s_barrier\n\t" \
    "v_smfmac_f32_16x16x64_bf16 v[232:235], v[248:251], v[144:151], %[ix]\n\t" "v_smfmac_f32_16x16x64_bf16 v[236:239], v[248:251], v[176:183], %[ix]\n\t" \
    "v_smfmac_f32_16x16x64_bf16 v[232:235], v[252:255], v[152:159], %[ix]\n\t" "s_waitcnt lgkmcnt(0)\n\t" \
    "v_smfmac_f32_16x16x64_bf16 v[236:239], v[252:255], v[184:191], %[ix]\n\t" XT(92, 93) "s_nop 6\n\t" "v_add_f32 v232, v232, v233\n\t" "v_add_f32 v234, v234, v235\n\t" \
    "v_add_f32 v236, v236, v237\n\t" "v_add_f32 v238, v238, v239\n\t" "v_cvt_pk_bf16_f32 v232, v232, v236\n\t" "v_cvt_pk_bf16_f32 v233, v234, v238\n\t" \
    "ds_write2_b32 %[kw], v232, v233 offset1:68\n\t" "s_waitcnt lgkmcnt(0)\n\t" "s_barrier\n\t" XT(94, 95) "s_cmp_ge_u32 %[sq], %[sn]\n\t" "s_cbranch_scc1 277f\n\t" \
    "275:\n\t" "global_load_dword v240, v210, %[fq] sc1\n\t" "s_waitcnt vmcnt(0)\n\t" "v_readfirstlane_b32 %[sq], v240\n\t" "s_nop 3\n\t" "s_cmp_ge_u32 %[sq], %[sn]\n\t" \
    "s_cbranch_scc1 276f\n\t" "s_cmp_eq_u32 %[bd], 0\n\t" "s_cbranch_scc1 276f\n\t" "s_sub_u32 %[bd], %[bd], 1\n\t" "s_sleep 2\n\t" "s_branch 275b\n\t" "276:\n\t" \
    "buffer_load_dwordx4 v[16:19], %[hq], %[rs], %[qb] offen offset:0 sc1\n\t" "buffer_load_dwordx4 v[20:23], %[hq], %[rs], %[qb] offen offset:2048 sc1\n\t" \
    "buffer_load_dwordx4 v[24:27], %[hq], %[rs], s61 offen offset:0 sc1\n\t" "buffer_load_dwordx4 v[28:31], %[hq], %[rs], s61 offen offset:2048 sc1\n\t" "277:\n\t" \
    "s_nop 3\n\t" "ds_read_b128 v[160:163], %[kx] offset:0\n\t" "ds_read_b128 v[164:167], %[kx] offset:2048\n\t" "ds_read_b128 v[168:171], %[kx] offset:4096\n\t" \
    "ds_read_b128 v[172:175], %[kx] offset:6144\n\t" "ds_read_b128 v[224:227], %[km] offset:0\n\t" "ds_read_b128 v[228:231], %[km] offset:64\n\t" \
    "ds_read_b128 v[232:235], %[km] offset:128\n\t" "ds_read_b128 v[236:239], %[km] offset:192\n\t" "s_waitcnt vmcnt(0)\n\t" "global_load_dword v240, v210, %[fq] sc1\n\t" \
    "s_waitcnt lgkmcnt(7)\n\t" "v_mfma_f32_16x16x32_bf16 v[248:251], v[160:163], v[16:19], v[200:203]\n\t" "s_waitcnt lgkmcnt(6)\n\t" \
    "v_mfma_f32_16x16x32_bf16 v[248:251], v[164:167], v[20:23], v[248:251]\n\t" "s_waitcnt lgkmcnt(5)\n\t" \
    "v_mfma_f32_16x16x32_bf16 v[248:251], v[168:171], v[24:27], v[248:251]\n\t" "s_waitcnt lgkmcnt(4)\n\t" \
    "v_mfma_f32_16x16x32_bf16 v[248:251], v[172:175], v[28:31], v[248:251]\n\t" "s_waitcnt lgkmcnt(3)\n\t" \
    "v_mfma_f32_16x16x32_bf16 v[244:247], v[224:227], v[16:19], v[204:207]\n\t" "s_waitcnt lgkmcnt(2)\n\t" \
    "v_mfma_f32_16x16x32_bf16 v[244:247], v[228:231], v[20:23], v[244:247]\n\t" "s_waitcnt lgkmcnt(1)\n\t" \
    "v_mfma_f32_16x16x32_bf16 v[244:247], v[232:235], v[24:27], v[244:247]\n\t" "s_waitcnt lgkmcnt(0)\n\t" \
    "v_mfma_f32_16x16x32_bf16 v[244:247], v[236:239], v[28:31], v[244:247]\n\t" "s_nop 3\n\t" "v_max3_f32 v242, v248, v249, v250\n\t" "v_max_f32 v242, v242, v251\n\t" \
    "s_nop 1\n\t" "v_max3_f32 v242, v242, v244, v245\n\t" "v_max3_f32 v242, v242, v246, v247\n\t" "v_mov_b32 v243, v242\n\t" "s_nop 1\n\t" \
    "v_permlane32_swap_b32 v242, v243\n\t" "v_max_f32 v242, v242, v243\n\t" "v_mov_b32 v243, v242\n\t" "s_nop 1\n\t" "v_permlane16_swap_b32 v242, v243\n\t" \
    "v_max_f32 v242, v242, v243\n\t" "v_pk_add_f32 v[244:245], v[244:245], v[242:243] op_sel_hi:[1,0] neg_lo:[0,1] neg_hi:[0,1]\n\t" \
    "v_pk_add_f32 v[246:247], v[246:247], v[242:243] op_sel_hi:[1,0] neg_lo:[0,1] neg_hi:[0,1]\n\t" \
    "v_pk_add_f32 v[248:249], v[248:249], v[242:243] op_sel_hi:[1,0] neg_lo:[0,1] neg_hi:[0,1]\n\t" \
    "v_pk_add_f32 v[250:251], v[250:251], v[242:243] op_sel_hi:[1,0] neg_lo:[0,1] neg_hi:[0,1]\n\t" "v_pk_mul_f32 v[244:245], v[244:245], s[66:67]\n\t" \
    "v_pk_mul_f32 v[246:247], v[246:247], s[66:67]\n\t" "v_pk_mul_f32 v[248:249], v[248:249], s[66:67]\n\t" "v_pk_mul_f32 v[250:251], v[250:251], s[66:67]\n\t" \
    "v_exp_f32 v244, v244\n\t" "v_exp_f32 v245, v245\n\t" "v_exp_f32 v246, v246\n\t" "v_exp_f32 v247, v247\n\t" "v_exp_f32 v248, v248\n\t" "v_exp_f32 v249, v249\n\t" \
    "v_exp_f32 v250, v250\n\t" "v_exp_f32 v251, v251\n\t" "v_pk_add_f32 v[242:243], v[244:245], v[246:247]\n\t" "v_pk_add_f32 v[242:243], v[242:243], v[248:249]\n\t" \
    "v_pk_add_f32 v[242:243], v[242:243], v[250:251]\n\t" "v_add_f32 v242, v242, v243\n\t" "v_mov_b32 v243, v242\n\t" "s_nop 1\n\t" "v_permlane32_swap_b32 v242, v243\n\t" \
    "v_add_f32 v242, v242, v243\n\t" "v_mov_b32 v243, v242\n\t" "s_nop 1\n\t" "v_permlane16_swap_b32 v242, v243\n\t" "v_add_f32 v242, v242, v243\n\t" \
    "v_rcp_f32 v242, v242\n\t" "s_nop 0\n\t" "v_pk_mul_f32 v[244:245], v[244:245], v[242:243] op_sel_hi:[1,0]\n\t" \
    "v_pk_mul_f32 v[246:247], v[246:247], v[242:243] op_sel_hi:[1,0]\n\t" "v_pk_mul_f32 v[248:249], v[248:249], v[242:243] op_sel_hi:[1,0]\n\t" \
    "v_pk_mul_f32 v[250:251], v[250:251], v[242:243] op_sel_hi:[1,0]\n\t" "v_cvt_pk_bf16_f32 v162, v244, v245\n\t" "v_cvt_pk_bf16_f32 v163, v246, v247\n\t" \
    "v_cvt_pk_bf16_f32 v164, v248, v249\n\t" "v_cvt_pk_bf16_f32 v165, v250, v251\n\t" "s_sleep %[ts]+2\n\t" \
    "buffer_store_dwordx2 v[164:165], %[px], %[rs], %[tb] offen sc1\n\t" "buffer_store_dwordx2 v[162:163], %[pm], %[rs], %[tb] offen sc1\n\t" "s_waitcnt vmcnt(2)\n\t" \
    "v_readfirstlane_b32 s62, v240\n\t" "s_nop 3\n\t" "s_max_u32 %[sq], %[sq], s62\n\t" XT(96, 97) "s_branch 99f\n\t" "21:\n\t" "s_mov_b64 s[62:63], 0\n\t" \
    "ds_read_b128 v[208:211], %[l1] offset:0\n\t" "ds_read_b128 v[212:215], %[l1] offset:1024\n\t" "ds_read_b128 v[216:219], %[l1] offset:2048\n\t" \
    "ds_read_b128 v[220:223], %[l1] offset:3072\n\t" "s_waitcnt lgkmcnt(3)\n\t" "v_max3_u32 v208, v208, v209, v210\n\t" "v_max_u32 v208, v208, v211\n\t" \
    "v_cmp_eq_u32 vcc, -1, v208\n\t" "ds_read_b128 v[208:211], %[l1] offset:4096\n\t" "s_or_b64 s[62:63], s[62:63], vcc\n\t" "s_waitcnt lgkmcnt(3)\n\t" \
    "v_max3_u32 v212, v212, v213, v214\n\t" "v_max_u32 v212, v212, v215\n\t" "v_cmp_eq_u32 vcc, -1, v212\n\t" "ds_read_b128 v[212:215], %[l1] offset:5120\n\t" \
    "s_or_b64 s[62:63], s[62:63], vcc\n\t" "s_waitcnt lgkmcnt(3)\n\t" "v_max3_u32 v216, v216, v217, v218\n\t" "v_max_u32 v216, v216, v219\n\t" \
    "v_cmp_eq_u32 vcc, -1, v216\n\t" "ds_read_b128 v[216:219], %[l1] offset:6144\n\t" "s_or_b64 s[62:63], s[62:63], vcc\n\t" "s_waitcnt lgkmcnt(3)\n\t" \
    "v_max3_u32 v220, v220, v221, v222\n\t" "v_max_u32 v220, v220, v223\n\t" "v_cmp_eq_u32 vcc, -1, v220\n\t" "ds_read_b128 v[220:223], %[l1] offset:7168\n\t" \
    "s_or_b64 s[62:63], s[62:63], vcc\n\t" "s_waitcnt lgkmcnt(3)\n\t" "v_max3_u32 v208, v208, v209, v210\n\t" "v_max_u32 v208, v208, v211\n\t" \
    "v_cmp_eq_u32 vcc, -1, v208\n\t" "ds_read_b128 v[208:211], %[l1] offset:8192\n\t" "s_or_b64 s[62:63], s[62:63], vcc\n\t" "s_waitcnt lgkmcnt(3)\n\t" \
    "v_max3_u32 v212, v212, v213, v214\n\t" "v_max_u32 v212, v212, v215\n\t" "v_cmp_eq_u32 vcc, -1, v212\n\t" "ds_read_b128 v[212:215], %[l1] offset:9216\n\t" \
    "s_or_b64 s[62:63], s[62:63], vcc\n\t" "s_waitcnt lgkmcnt(3)\n\t" "v_max3_u32 v216, v216, v217, v218\n\t" "v_max_u32 v216, v216, v219\n\t" \
    "v_cmp_eq_u32 vcc, -1, v216\n\t" "ds_read_b128 v[216:219], %[l1] offset:10240\n\t" "s_or_b64 s[62:63], s[62:63], vcc\n\t" "s_waitcnt lgkmcnt(3)\n\t" \
    "v_max3_u32 v220, v220, v221, v222\n\t" "v_max_u32 v220, v220, v223\n\t" "v_cmp_eq_u32 vcc, -1, v220\n\t" "ds_read_b128 v[220:223], %[l1] offset:11264\n\t" \
    "s_or_b64 s[62:63], s[62:63], vcc\n\t" "s_waitcnt lgkmcnt(3)\n\t" "v_max3_u32 v208, v208, v209, v210\n\t" "v_max_u32 v208, v208, v211\n\t" \
    "v_cmp_eq_u32 vcc, -1, v208\n\t" "ds_read_b128 v[208:211], %[l1] offset:12288\n\t" "s_or_b64 s[62:63], s[62:63], vcc\n\t" "s_waitcnt lgkmcnt(3)\n\t" \
    "v_max3_u32 v212, v212, v213, v214\n\t" "v_max_u32 v212, v212, v215\n\t" "v_cmp_eq_u32 vcc, -1, v212\n\t" "ds_read_b128 v[212:215], %[l1] offset:13312\n\t" \
    "s_or_b64 s[62:63], s[62:63], vcc\n\t" "s_waitcnt lgkmcnt(3)\n\t" "v_max3_u32 v216, v216, v217, v218\n\t" "v_max_u32 v216, v216, v219\n\t" \
    "v_cmp_eq_u32 vcc, -1, v216\n\t" "ds_read_b128 v[216:219], %[l1] offset:14336\n\t" "s_or_b64 s[62:63], s[62:63], vcc\n\t" "s_waitcnt lgkmcnt(3)\n\t" \
    "v_max3_u32 v220, v220, v221, v222\n\t" "v_max_u32 v220, v220, v223\n\t" "v_cmp_eq_u32 vcc, -1, v220\n\t" "ds_read_b128 v[220:223], %[l1] offset:15360\n\t" \
    "s_or_b64 s[62:63], s[62:63], vcc\n\t" "s_waitcnt lgkmcnt(3)\n\t" "v_max3_u32 v208, v208, v209, v210\n\t" "v_max_u32 v208, v208, v211\n\t" \
    "v_cmp_eq_u32 vcc, -1, v208\n\t" "s_or_b64 s[62:63], s[62:63], vcc\n\t" "s_waitcnt lgkmcnt(2)\n\t" "v_max3_u32 v212, v212, v213, v214\n\t" \
    "v_max_u32 v212, v212, v215\n\t" "v_cmp_eq_u32 vcc, -1, v212\n\t" "s_or_b64 s[62:63], s[62:63], vcc\n\t" "s_waitcnt lgkmcnt(1)\n\t" \
    "v_max3_u32 v216, v216, v217, v218\n\t" "v_max_u32 v216, v216, v219\n\t" "v_cmp_eq_u32 vcc, -1, v216\n\t" "s_or_b64 s[62:63], s[62:63], vcc\n\t" \
    "s_waitcnt lgkmcnt(0)\n\t" "v_max3_u32 v220, v220, v221, v222\n\t" "v_max_u32 v220, v220, v223\n\t" "v_cmp_eq_u32 vcc, -1, v220\n\t" \
    "s_or_b64 s[62:63], s[62:63], vcc\n\t" "v_mov_b64 v[208:209], 0\n\t" "v_mov_b64 v[210:211], 0\n\t" "v_mov_b64 v[212:213], 0\n\t" "v_mov_b64 v[214:215], 0\n\t" \
    "s_cmp_eq_u64 s[62:63], 0\n\t" "s_cbranch_scc1 22b\n\t" "s_cmp_eq_u32 %[rt], 0\n\t" "s_cbranch_scc1 22b\n\t" "s_sub_u32 %[rt], %[rt], 1\n\t" \
    "v_xor_b32 v216, 0x400, %[mo]\n\t" XCNT(100) "24:\n\t" "buffer_load_dwordx4 v[240:243], %[mo], %[rs], %[sb] offen sc1\n\t" \
    "buffer_load_dwordx4 v[244:247], v216, %[rs], %[sb] offen sc1\n\t" "buffer_load_dwordx4 v[248:251], %[mo], %[rs], s60 offen sc1\n\t" \
    "buffer_load_dwordx4 v[252:255], v216, %[rs], s60 offen sc1\n\t" "s_waitcnt vmcnt(0)\n\t" "v_max_u32 v220, v240, v241\n\t" "v_max3_u32 v220, v220, v242, v243\n\t" \
    "v_max3_u32 v220, v220, v244, v245\n\t" "v_max3_u32 v220, v220, v246, v247\n\t" "v_max3_u32 v220, v220, v248, v249\n\t" "v_max3_u32 v220, v220, v250, v251\n\t" \
    "v_max3_u32 v220, v220, v252, v253\n\t" "v_max3_u32 v220, v220, v254, v255\n\t" "v_cmp_eq_u32 vcc, -1, v220\n\t" "s_cbranch_vccz 25f\n\t" "s_cmp_eq_u32 %[bd], 0\n\t" \
    "s_cbranch_scc1 25f\n\t" "s_sub_u32 %[bd], %[bd], 1\n\t" XCNT(101) "s_sleep 1\n\t" "s_branch 24b\n\t" "25:\n\t" "s_barrier\n\t" "s_branch 20b\n\t" "99:\n\t"
#define QK_STEP2 \
    "v_xor_b32 v216, 0x400, %[mo]\n\t" "s_xor_b32 s60, %[sb], 0x800\n\t" "s_add_u32 s61, %[qb], 0x1000\n\t" "s_mov_b32 s66, 0x3e0293ee\n\t" "s_mov_b32 s67, 0x3e0293ee\n\t" \
    XT(80, 81) XT(98, 99) "s_barrier\n\t" XT(82, 83) "30:\n\t" "buffer_load_dwordx4 v[240:243], %[mo], %[rs], %[sb] offen sc1\n\t" "v_mov_b64 v[224:225], 0\n\t" \
    "v_mov_b64 v[226:227], 0\n\t" "v_mov_b64 v[228:229], 0\n\t" "v_mov_b64 v[230:231], 0\n\t" "v_mov_b64 v[232:233], 0\n\t" "v_mov_b64 v[234:235], 0\n\t" \
    "v_mov_b64 v[236:237], 0\n\t" "v_mov_b64 v[238:239], 0\n\t" "buffer_load_dwordx4 v[244:247], v216, %[rs], %[sb] offen sc1\n\t" \
    "ds_read_b128 v[160:163], %[l0] offset:0\n\t" "ds_read_b128 v[164:167], %[l0] offset:1024\n\t" "ds_read_b128 v[168:171], %[l0] offset:2048\n\t" \
    "ds_read_b128 v[172:175], %[l0] offset:3072\n\t" "s_nop 3\n\t" "buffer_load_dwordx4 v[248:251], %[mo], %[rs], s60 offen sc1\n\t" \
    "ds_read_b128 v[176:179], %[l0] offset:4096\n\t" "ds_read_b128 v[180:183], %[l0] offset:5120\n\t" "ds_read_b128 v[184:187], %[l0] offset:6144\n\t" \
    "ds_read_b128 v[188:191], %[l0] offset:7168\n\t" "s_nop 3\n\t" "buffer_load_dwordx4 v[252:255], v216, %[rs], s60 offen sc1\n\t" "v_mov_b64 v[220:221], -1\n\t" \
    "v_mov_b64 v[222:223], -1\n\t" "buffer_store_dwordx4 v[220:223], %[px], %[rs], %[tr] offen sc1\n\t" "buffer_store_dwordx2 v[220:221], %[pm], %[rs], %[tr] offen sc1\n\t" \
    "v_mov_b32 v208, 0xd028965f\n\t" "v_mov_b32 v209, 0xff800000\n\t" "s_mov_b32 s62, 0\n\t" "s_mov_b32 s63, -1\n\t" "v_mov_b32 v210, 0\n\t" "v_cmp_gt_i32 vcc, 0, %[ps]\n\t" \
    "v_cndmask_b32 v204, 0, v209, s[62:63]\n\t" "s_nop 0\n\t" "v_cndmask_b32 v200, 0, v208, vcc\n\t" "v_cmp_gt_i32 vcc, 1, %[ps]\n\t" \
    "v_cndmask_b32 v205, 0, v209, s[62:63]\n\t" "s_nop 0\n\t" "v_cndmask_b32 v201, 0, v208, vcc\n\t" "v_cmp_gt_i32 vcc, 2, %[ps]\n\t" \
    "v_cndmask_b32 v206, 0, v209, s[62:63]\n\t" "s_nop 0\n\t" "v_cndmask_b32 v202, 0, v208, vcc\n\t" "v_cmp_gt_i32 vcc, 3, %[ps]\n\t" \
    "v_cndmask_b32 v207, 0, v209, s[62:63]\n\t" "s_nop 0\n\t" "v_cndmask_b32 v203, 0, v208, vcc\n\t" "s_waitcnt vmcnt(5)\n\t" XT(84, 85) \
    "v_smfmac_f32_16x16x64_bf16 v[224:227], v[240:243], a[0:7], %[ix]\n\t" "ds_write_b128 %[l1], v[240:243] offset:8192\n\t" \
    "v_smfmac_f32_16x16x64_bf16 v[228:231], v[240:243], a[8:15], %[ix]\n\t" "v_smfmac_f32_16x16x64_bf16 v[232:235], v[240:243], v[32:39], %[ix]\n\t" \
    "s_waitcnt lgkmcnt(7)\n\t" "v_smfmac_f32_16x16x64_bf16 v[236:239], v[240:243], v[160:167], %[ix]\n\t" "ds_read_b128 v[160:163], %[l0] offset:8192\n\t" \
    "ds_read_b128 v[164:167], %[l0] offset:9216\n\t" "s_waitcnt vmcnt(4)\n\t" "v_smfmac_f32_16x16x64_bf16 v[224:227], v[244:247], a[16:23], %[ix]\n\t" \
    "ds_write_b128 %[l1], v[244:247] offset:9216\n\t" "v_smfmac_f32_16x16x64_bf16 v[228:231], v[244:247], a[24:31], %[ix]\n\t" \
    "v_smfmac_f32_16x16x64_bf16 v[232:235], v[244:247], v[40:47], %[ix]\n\t" "s_waitcnt lgkmcnt(8)\n\t" \
    "v_smfmac_f32_16x16x64_bf16 v[236:239], v[244:247], v[168:175], %[ix]\n\t" "ds_read_b128 v[168:171], %[l0] offset:10240\n\t" \
    "ds_read_b128 v[172:175], %[l0] offset:11264\n\t" "s_waitcnt vmcnt(3)\n\t" "v_smfmac_f32_16x16x64_bf16 v[224:227], v[248:251], a[32:39], %[ix]\n\t" \
    "ds_write_b128 %[l1], v[248:251] offset:10240\n\t" "v_smfmac_f32_16x16x64_bf16 v[228:231], v[248:251], a[40:47], %[ix]\n\t" "s_waitcnt lgkmcnt(3)\n\t" "s_barrier\n\t" \
    XT(86, 87) "v_smfmac_f32_16x16x64_bf16 v[232:235], v[248:251], v[48:55], %[ix]\n\t" "ds_read_b128 v[192:195], %[l1] offset:12288\n\t" \
    "ds_read_b128 v[196:199], %[l1] offset:0\n\t" "v_smfmac_f32_16x16x64_bf16 v[236:239], v[248:251], v[176:183], %[ix]\n\t" "ds_read_b128 v[176:179], %[l0] offset:12288\n\t" \
    "ds_read_b128 v[180:183], %[l0] offset:13312\n\t" "s_waitcnt vmcnt(2)\n\t" "v_smfmac_f32_16x16x64_bf16 v[224:227], v[252:255], a[48:55], %[ix]\n\t" \
    "ds_write_b128 %[l1], v[252:255] offset:11264\n\t" "v_smfmac_f32_16x16x64_bf16 v[228:231], v[252:255], a[56:63], %[ix]\n\t" \
    "ds_read_b128 v[240:243], %[l1] offset:4096\n\t" "ds_read_b128 v[244:247], %[l1] offset:13312\n\t" \
    "v_smfmac_f32_16x16x64_bf16 v[232:235], v[252:255], v[56:63], %[ix]\n\t" "v_smfmac_f32_16x16x64_bf16 v[236:239], v[252:255], v[184:191], %[ix]\n\t" \
    "ds_read_b128 v[184:187], %[l0] offset:14336\n\t" "ds_read_b128 v[188:191], %[l0] offset:15360\n\t" "s_waitcnt lgkmcnt(8)\n\t" \
    "v_smfmac_f32_16x16x64_bf16 v[224:227], v[192:195], a[64:71], %[ix]\n\t" "v_smfmac_f32_16x16x64_bf16 v[228:231], v[192:195], a[72:79], %[ix]\n\t" \
    "v_smfmac_f32_16x16x64_bf16 v[232:235], v[192:195], v[64:71], %[ix]\n\t" "s_waitcnt lgkmcnt(4)\n\t" "s_barrier\n\t" XT(88, 89) \
    "v_smfmac_f32_16x16x64_bf16 v[236:239], v[192:195], v[160:167], %[ix]\n\t" "ds_read_b128 v[160:163], %[l0] offset:16384\n\t" \
    "ds_read_b128 v[164:167], %[l0] offset:17408\n\t" "v_smfmac_f32_16x16x64_bf16 v[224:227], v[196:199], a[80:87], %[ix]\n\t" \
    "ds_read_b128 v[248:251], %[l1] offset:1024\n\t" "ds_read_b128 v[252:255], %[l1] offset:5120\n\t" "v_smfmac_f32_16x16x64_bf16 v[228:231], v[196:199], a[88:95], %[ix]\n\t" \
    "ds_read_b128 v[192:195], %[l1] offset:14336\n\t" "v_smfmac_f32_16x16x64_bf16 v[232:235], v[196:199], v[72:79], %[ix]\n\t" \
    "buffer_load_dwordx4 v[16:19], %[hq], %[rs], %[qb] offen offset:0 sc1\n\t" "v_smfmac_f32_16x16x64_bf16 v[236:239], v[196:199], v[168:175], %[ix]\n\t" \
    "ds_read_b128 v[168:171], %[l0] offset:18432\n\t" "ds_read_b128 v[172:175], %[l0] offset:19456\n\t" "s_waitcnt lgkmcnt(10)\n\t" \
    "v_smfmac_f32_16x16x64_bf16 v[224:227], v[240:243], a[96:103], %[ix]\n\t" "ds_read_b128 v[196:199], %[l1] offset:2048\n\t" \
    "v_smfmac_f32_16x16x64_bf16 v[228:231], v[240:243], a[104:111], %[ix]\n\t" "v_smfmac_f32_16x16x64_bf16 v[232:235], v[240:243], v[80:87], %[ix]\n\t" \
    "buffer_load_dwordx4 v[20:23], %[hq], %[rs], %[qb] offen offset:2048 sc1\n\t" "v_smfmac_f32_16x16x64_bf16 v[236:239], v[240:243], v[176:183], %[ix]\n\t" \
    "ds_read_b128 v[176:179], %[l0] offset:20480\n\t" "ds_read_b128 v[180:183], %[l0] offset:21504\n\t" "s_waitcnt lgkmcnt(12)\n\t" \
    "v_smfmac_f32_16x16x64_bf16 v[224:227], v[244:247], a[112:119], %[ix]\n\t" "ds_read_b128 v[240:243], %[l1] offset:6144\n\t" \
    "v_smfmac_f32_16x16x64_bf16 v[228:231], v[244:247], a[120:127], %[ix]\n\t" "v_smfmac_f32_16x16x64_bf16 v[232:235], v[244:247], v[88:95], %[ix]\n\t" \
    "buffer_load_dwordx4 v[24:27], %[hq], %[rs], s61 offen offset:0 sc1\n\t" "s_waitcnt lgkmcnt(11)\n\t" \
    "v_smfmac_f32_16x16x64_bf16 v[236:239], v[244:247], v[184:191], %[ix]\n\t" "ds_read_b128 v[184:187], %[l0] offset:22528\n\t" \
    "ds_read_b128 v[188:191], %[l0] offset:23552\n\t" "s_waitcnt lgkmcnt(10)\n\t" "v_smfmac_f32_16x16x64_bf16 v[224:227], v[248:251], a[128:135], %[ix]\n\t" \
    "ds_read_b128 v[244:247], %[l1] offset:15360\n\t" "v_smfmac_f32_16x16x64_bf16 v[228:231], v[248:251], a[136:143], %[ix]\n\t" \
    "v_smfmac_f32_16x16x64_bf16 v[232:235], v[248:251], v[96:103], %[ix]\n\t" "buffer_load_dwordx4 v[28:31], %[hq], %[rs], s61 offen offset:2048 sc1\n\t" \
    "v_smfmac_f32_16x16x64_bf16 v[236:239], v[248:251], v[160:167], %[ix]\n\t" "ds_read_b128 v[160:163], %[l0] offset:24576\n\t" \
    "ds_read_b128 v[164:167], %[l0] offset:25600\n\t" "s_waitcnt lgkmcnt(12)\n\t" "v_smfmac_f32_16x16x64_bf16 v[224:227], v[252:255], a[144:151], %[ix]\n\t" \
    "ds_read_b128 v[248:251], %[l1] offset:3072\n\t" "v_smfmac_f32_16x16x64_bf16 v[228:231], v[252:255], a[152:159], %[ix]\n\t" \
    "v_smfmac_f32_16x16x64_bf16 v[232:235], v[252:255], v[104:111], %[ix]\n\t" "s_waitcnt lgkmcnt(10)\n\t" \
    "v_smfmac_f32_16x16x64_bf16 v[236:239], v[252:255], v[168:175], %[ix]\n\t" "ds_read_b128 v[168:171], %[l0] offset:26624\n\t" \
    "ds_read_b128 v[172:175], %[l0] offset:27648\n\t" "v_smfmac_f32_16x16x64_bf16 v[224:227], v[192:195], a[160:167], %[ix]\n\t" \
    "ds_read_b128 v[252:255], %[l1] offset:7168\n\t" "v_smfmac_f32_16x16x64_bf16 v[228:231], v[192:195], a[168:175], %[ix]\n\t" "s_waitcnt lgkmcnt(12)\n\t" \
    "v_smfmac_f32_16x16x64_bf16 v[224:227], v[196:199], a[176:183], %[ix]\n\t" "v_smfmac_f32_16x16x64_bf16 v[228:231], v[196:199], a[184:191], %[ix]\n\t" \
    "s_waitcnt lgkmcnt(9)\n\t" "v_smfmac_f32_16x16x64_bf16 v[224:227], v[240:243], a[192:199], %[ix]\n\t" \
    "v_smfmac_f32_16x16x64_bf16 v[228:231], v[240:243], a[200:207], %[ix]\n\t" "s_waitcnt lgkmcnt(6)\n\t" \
    "v_smfmac_f32_16x16x64_bf16 v[224:227], v[244:247], a[208:215], %[ix]\n\t" "v_smfmac_f32_16x16x64_bf16 v[228:231], v[244:247], a[216:223], %[ix]\n\t" \
    "s_waitcnt vmcnt(4)\n\t" "s_waitcnt lgkmcnt(3)\n\t" "v_smfmac_f32_16x16x64_bf16 v[224:227], v[248:251], a[224:231], %[ix]\n\t" \
    "v_smfmac_f32_16x16x64_bf16 v[228:231], v[248:251], a[232:239], %[ix]\n\t" "s_waitcnt lgkmcnt(0)\n\t" \
    "v_smfmac_f32_16x16x64_bf16 v[224:227], v[252:255], a[240:247], %[ix]\n\t" "s_nop 1\n\t" "v_smfmac_f32_16x16x64_bf16 v[228:231], v[252:255], a[248:255], %[ix]\n\t" \
    XT(90, 91) "s_nop 1\n\t" "v_smfmac_f32_16x16x64_bf16 v[232:235], v[192:195], v[112:119], %[ix]\n\t" "s_nop 1\n\t" \
    "v_smfmac_f32_16x16x64_bf16 v[236:239], v[192:195], v[176:183], %[ix]\n\t" "ds_read_b128 v[176:179], %[l0] offset:28672\n\t" "v_add_f32 v224, v224, v225\n\t" \
    "v_add_f32 v226, v226, v227\n\t" "v_smfmac_f32_16x16x64_bf16 v[232:235], v[196:199], v[120:127], %[ix]\n\t" "ds_read_b128 v[180:183], %[l0] offset:29696\n\t" \
    "v_add_f32 v228, v228, v229\n\t" "v_add_f32 v230, v230, v231\n\t" "v_cmp_u_f32 vcc, v224, v226\n\t" \
    "v_smfmac_f32_16x16x64_bf16 v[236:239], v[196:199], v[184:191], %[ix]\n\t" "ds_read_b128 v[184:187], %[l0] offset:30720\n\t" "s_cbranch_vccnz 31f\n\t" "32:\n\t" \
    "v_cvt_pk_bf16_f32 v218, v224, v228\n\t" "v_cvt_pk_bf16_f32 v219, v226, v230\n\t" "v_smfmac_f32_16x16x64_bf16 v[232:235], v[240:243], v[128:135], %[ix]\n\t" \
    "ds_write2_b32 %[qw], v218, v219 offset1:8\n\t" "ds_read_b128 v[188:191], %[l0] offset:31744\n\t" \
    "v_smfmac_f32_16x16x64_bf16 v[236:239], v[240:243], v[160:167], %[ix]\n\t" "v_smfmac_f32_16x16x64_bf16 v[232:235], v[244:247], v[136:143], %[ix]\n\t" \
    "v_smfmac_f32_16x16x64_bf16 v[236:239], v[244:247], v[168:175], %[ix]\n\t" "s_waitcnt lgkmcnt(1)\n\t" "s_barrier\n\t" \
    "v_smfmac_f32_16x16x64_bf16 v[232:235], v[248:251], v[144:151], %[ix]\n\t" "v_smfmac_f32_16x16x64_bf16 v[236:239], v[248:251], v[176:183], %[ix]\n\t" \
    "v_smfmac_f32_16x16x64_bf16 v[232:235], v[252:255], v[152:159], %[ix]\n\t" "s_waitcnt lgkmcnt(0)\n\t" \
    "v_smfmac_f32_16x16x64_bf16 v[236:239], v[252:255], v[184:191], %[ix]\n\t" XT(92, 93) "s_nop 6\n\t" "v_add_f32 v232, v232, v233\n\t" "v_add_f32 v234, v234, v235\n\t" \
    "v_add_f32 v236, v236, v237\n\t" "v_add_f32 v238, v238, v239\n\t" "v_cvt_pk_bf16_f32 v232, v232, v236\n\t" "v_cvt_pk_bf16_f32 v233, v234, v238\n\t" \
    "ds_write2_b32 %[kw], v232, v233 offset1:68\n\t" "s_waitcnt lgkmcnt(0)\n\t" "s_barrier\n\t" XT(94, 95) "s_cmp_ge_u32 %[sq], %[sn]\n\t" "s_cbranch_scc1 377f\n\t" \
    "375:\n\t" "global_load_dword v240, v210, %[fq] sc1\n\t" "s_waitcnt vmcnt(0)\n\t" "v_readfirstlane_b32 %[sq], v240\n\t" "s_nop 3\n\t" "s_cmp_ge_u32 %[sq], %[sn]\n\t" \
    "s_cbranch_scc1 376f\n\t" "s_cmp_eq_u32 %[bd], 0\n\t" "s_cbranch_scc1 376f\n\t" "s_sub_u32 %[bd], %[bd], 1\n\t" "s_sleep 2\n\t" "s_branch 375b\n\t" "376:\n\t" \
    "buffer_load_dwordx4 v[16:19], %[hq], %[rs], %[qb] offen offset:0 sc1\n\t" "buffer_load_dwordx4 v[20:23], %[hq], %[rs], %[qb] offen offset:2048 sc1\n\t" \
    "buffer_load_dwordx4 v[24:27], %[hq], %[rs], s61 offen offset:0 sc1\n\t" "buffer_load_dwordx4 v[28:31], %[hq], %[rs], s61 offen offset:2048 sc1\n\t" "377:\n\t" \
    "s_nop 3\n\t" "ds_read_b128 v[160:163], %[kx] offset:0\n\t" "ds_read_b128 v[164:167], %[kx] offset:2048\n\t" "ds_read_b128 v[168:171], %[kx] offset:4096\n\t" \
    "ds_read_b128 v[172:175], %[kx] offset:6144\n\t" "ds_read_b128 v[176:179], %[kx] offset:256\n\t" "ds_read_b128 v[180:183], %[kx] offset:2304\n\t" \
    "ds_read_b128 v[184:187], %[kx] offset:4352\n\t" "ds_read_b128 v[188:191], %[kx] offset:6400\n\t" "ds_read_b128 v[224:227], %[km] offset:0\n\t" \
    "ds_read_b128 v[228:231], %[km] offset:64\n\t" "ds_read_b128 v[232:235], %[km] offset:128\n\t" "ds_read_b128 v[236:239], %[km] offset:192\n\t" "s_waitcnt vmcnt(0)\n\t" \
    "global_load_dword v240, v210, %[fq] sc1\n\t" "s_waitcnt lgkmcnt(11)\n\t" "v_mfma_f32_16x16x32_bf16 v[248:251], v[160:163], v[16:19], 0\n\t" "s_waitcnt lgkmcnt(10)\n\t" \
    "v_mfma_f32_16x16x32_bf16 v[248:251], v[164:167], v[20:23], v[248:251]\n\t" "s_waitcnt lgkmcnt(9)\n\t" \
    "v_mfma_f32_16x16x32_bf16 v[248:251], v[168:171], v[24:27], v[248:251]\n\t" "s_waitcnt lgkmcnt(8)\n\t" \
    "v_mfma_f32_16x16x32_bf16 v[248:251], v[172:175], v[28:31], v[248:251]\n\t" "s_waitcnt lgkmcnt(7)\n\t" \
    "v_mfma_f32_16x16x32_bf16 v[252:255], v[176:179], v[16:19], v[200:203]\n\t" "s_waitcnt lgkmcnt(6)\n\t" \
    "v_mfma_f32_16x16x32_bf16 v[252:255], v[180:183], v[20:23], v[252:255]\n\t" "s_waitcnt lgkmcnt(5)\n\t" \
    "v_mfma_f32_16x16x32_bf16 v[252:255], v[184:187], v[24:27], v[252:255]\n\t" "s_waitcnt lgkmcnt(4)\n\t" \
    "v_mfma_f32_16x16x32_bf16 v[252:255], v[188:191], v[28:31], v[252:255]\n\t" "s_waitcnt lgkmcnt(3)\n\t" \
    "v_mfma_f32_16x16x32_bf16 v[244:247], v[224:227], v[16:19], v[204:207]\n\t" "s_waitcnt lgkmcnt(2)\n\t" \
    "v_mfma_f32_16x16x32_bf16 v[244:247], v[228:231], v[20:23], v[244:247]\n\t" "s_waitcnt lgkmcnt(1)\n\t" \
    "v_mfma_f32_16x16x32_bf16 v[244:247], v[232:235], v[24:27], v[244:247]\n\t" "s_waitcnt lgkmcnt(0)\n\t" \
    "v_mfma_f32_16x16x32_bf16 v[244:247], v[236:239], v[28:31], v[244:247]\n\t" "s_nop 3\n\t" "v_max3_f32 v242, v248, v249, v250\n\t" "v_max3_f32 v242, v242, v251, v252\n\t" \
    "v_max3_f32 v242, v242, v253, v254\n\t" "v_max_f32 v242, v242, v255\n\t" "v_max3_f32 v242, v242, v244, v245\n\t" "v_max3_f32 v242, v242, v246, v247\n\t" \
    "v_mov_b32 v243, v242\n\t" "s_nop 1\n\t" "v_permlane32_swap_b32 v242, v243\n\t" "v_max_f32 v242, v242, v243\n\t" "v_mov_b32 v243, v242\n\t" "s_nop 1\n\t" \
    "v_permlane16_swap_b32 v242, v243\n\t" "v_max_f32 v242, v242, v243\n\t" "v_pk_add_f32 v[244:245], v[244:245], v[242:243] op_sel_hi:[1,0] neg_lo:[0,1] neg_hi:[0,1]\n\t" \
    "v_pk_add_f32 v[246:247], v[246:247], v[242:243] op_sel_hi:[1,0] neg_lo:[0,1] neg_hi:[0,1]\n\t" \
    "v_pk_add_f32 v[248:249], v[248:249], v[242:243] op_sel_hi:[1,0] neg_lo:[0,1] neg_hi:[0,1]\n\t" \
    "v_pk_add_f32 v[250:251], v[250:251], v[242:243] op_sel_hi:[1,0] neg_lo:[0,1] neg_hi:[0,1]\n\t" \
    "v_pk_add_f32 v[252:253], v[252:253], v[242:243] op_sel_hi:[1,0] neg_lo:[0,1] neg_hi:[0,1]\n\t" \
    "v_pk_add_f32 v[254:255], v[254:255], v[242:243] op_sel_hi:[1,0] neg_lo:[0,1] neg_hi:[0,1]\n\t" "v_pk_mul_f32 v[244:245], v[244:245], s[66:67]\n\t" \
    "v_pk_mul_f32 v[246:247], v[246:247], s[66:67]\n\t" "v_pk_mul_f32 v[248:249], v[248:249], s[66:67]\n\t" "v_pk_mul_f32 v[250:251], v[250:251], s[66:67]\n\t" \
    "v_pk_mul_f32 v[252:253], v[252:253], s[66:67]\n\t" "v_pk_mul_f32 v[254:255], v[254:255], s[66:67]\n\t" "v_exp_f32 v244, v244\n\t" "v_exp_f32 v245, v245\n\t" \
    "v_exp_f32 v246, v246\n\t" "v_exp_f32 v247, v247\n\t" "v_exp_f32 v248, v248\n\t" "v_exp_f32 v249, v249\n\t" "v_exp_f32 v250, v250\n\t" "v_exp_f32 v251, v251\n\t" \
    "v_exp_f32 v252, v252\n\t" "v_exp_f32 v253, v253\n\t" "v_exp_f32 v254, v254\n\t" "v_exp_f32 v255, v255\n\t" "v_pk_add_f32 v[242:243], v[244:245], v[246:247]\n\t" \
    "v_pk_add_f32 v[242:243], v[242:243], v[248:249]\n\t" "v_pk_add_f32 v[242:243], v[242:243], v[250:251]\n\t" "v_pk_add_f32 v[242:243], v[242:243], v[252:253]\n\t" \
    "v_pk_add_f32 v[242:243], v[242:243], v[254:255]\n\t" "v_add_f32 v242, v242, v243\n\t" "v_mov_b32 v243, v242\n\t" "s_nop 1\n\t" "v_permlane32_swap_b32 v242, v243\n\t" \
    "v_add_f32 v242, v242, v243\n\t" "v_mov_b32 v243, v242\n\t" "s_nop 1\n\t" "v_permlane16_swap_b32 v242, v243\n\t" "v_add_f32 v242, v242, v243\n\t" \
    "v_rcp_f32 v242, v242\n\t" "s_nop 0\n\t" "v_pk_mul_f32 v[244:245], v[244:245], v[242:243] op_sel_hi:[1,0]\n\t" \
    "v_pk_mul_f32 v[246:247], v[246:247], v[242:243] op_sel_hi:[1,0]\n\t" "v_pk_mul_f32 v[248:249], v[248:249], v[242:243] op_sel_hi:[1,0]\n\t" \
    "v_pk_mul_f32 v[250:251], v[250:251], v[242:243] op_sel_hi:[1,0]\n\t" "v_pk_mul_f32 v[252:253], v[252:253], v[242:243] op_sel_hi:[1,0]\n\t" \
    "v_pk_mul_f32 v[254:255], v[254:255], v[242:243] op_sel_hi:[1,0]\n\t" "v_cvt_pk_bf16_f32 v162, v244, v245\n\t" "v_cvt_pk_bf16_f32 v163, v246, v247\n\t" \
    "v_cvt_pk_bf16_f32 v164, v248, v249\n\t" "v_cvt_pk_bf16_f32 v165, v250, v251\n\t" "v_cvt_pk_bf16_f32 v166, v252, v253\n\t" "v_cvt_pk_bf16_f32 v167, v254, v255\n\t" \
    "s_sleep %[ts]+0\n\t" "buffer_store_dwordx4 v[164:167], %[px], %[rs], %[tb] offen sc1\n\t" "buffer_store_dwordx2 v[162:163], %[pm], %[rs], %[tb] offen sc1\n\t" \
    "s_waitcnt vmcnt(2)\n\t" "v_readfirstlane_b32 s62, v240\n\t" "s_nop 3\n\t" "s_max_u32 %[sq], %[sq], s62\n\t" XT(96, 97) "s_branch 99f\n\t" "31:\n\t" \
    "s_mov_b64 s[62:63], 0\n\t" "ds_read_b128 v[208:211], %[l1] offset:0\n\t" "ds_read_b128 v[212:215], %[l1] offset:1024\n\t" \
    "ds_read_b128 v[216:219], %[l1] offset:2048\n\t" "ds_read_b128 v[220:223], %[l1] offset:3072\n\t" "s_waitcnt lgkmcnt(3)\n\t" "v_max3_u32 v208, v208, v209, v210\n\t" \
    "v_max_u32 v208, v208, v211\n\t" "v_cmp_eq_u32 vcc, -1, v208\n\t" "ds_read_b128 v[208:211], %[l1] offset:4096\n\t" "s_or_b64 s[62:63], s[62:63], vcc\n\t" \
    "s_waitcnt lgkmcnt(3)\n\t" "v_max3_u32 v212, v212, v213, v214\n\t" "v_max_u32 v212, v212, v215\n\t" "v_cmp_eq_u32 vcc, -1, v212\n\t" \
    "ds_read_b128 v[212:215], %[l1] offset:5120\n\t" "s_or_b64 s[62:63], s[62:63], vcc\n\t" "s_waitcnt lgkmcnt(3)\n\t" "v_max3_u32 v216, v216, v217, v218\n\t" \
    "v_max_u32 v216, v216, v219\n\t" "v_cmp_eq_u32 vcc, -1, v216\n\t" "ds_read_b128 v[216:219], %[l1] offset:6144\n\t" "s_or_b64 s[62:63], s[62:63], vcc\n\t" \
    "s_waitcnt lgkmcnt(3)\n\t" "v_max3_u32 v220, v220, v221, v222\n\t" "v_max_u32 v220, v220, v223\n\t" "v_cmp_eq_u32 vcc, -1, v220\n\t" \
    "ds_read_b128 v[220:223], %[l1] offset:7168\n\t" "s_or_b64 s[62:63], s[62:63], vcc\n\t" "s_waitcnt lgkmcnt(3)\n\t" "v_max3_u32 v208, v208, v209, v210\n\t" \
    "v_max_u32 v208, v208, v211\n\t" "v_cmp_eq_u32 vcc, -1, v208\n\t" "ds_read_b128 v[208:211], %[l1] offset:8192\n\t" "s_or_b64 s[62:63], s[62:63], vcc\n\t" \
    "s_waitcnt lgkmcnt(3)\n\t" "v_max3_u32 v212, v212, v213, v214\n\t" "v_max_u32 v212, v212, v215\n\t" "v_cmp_eq_u32 vcc, -1, v212\n\t" \
    "ds_read_b128 v[212:215], %[l1] offset:9216\n\t" "s_or_b64 s[62:63], s[62:63], vcc\n\t" "s_waitcnt lgkmcnt(3)\n\t" "v_max3_u32 v216, v216, v217, v218\n\t" \
    "v_max_u32 v216, v216, v219\n\t" "v_cmp_eq_u32 vcc, -1, v216\n\t" "ds_read_b128 v[216:219], %[l1] offset:10240\n\t" "s_or_b64 s[62:63], s[62:63], vcc\n\t" \
    "s_waitcnt lgkmcnt(3)\n\t" "v_max3_u32 v220, v220, v221, v222\n\t" "v_max_u32 v220, v220, v223\n\t" "v_cmp_eq_u32 vcc, -1, v220\n\t" \
    "ds_read_b128 v[220:223], %[l1] offset:11264\n\t" "s_or_b64 s[62:63], s[62:63], vcc\n\t" "s_waitcnt lgkmcnt(3)\n\t" "v_max3_u32 v208, v208, v209, v210\n\t" \
    "v_max_u32 v208, v208, v211\n\t" "v_cmp_eq_u32 vcc, -1, v208\n\t" "ds_read_b128 v[208:211], %[l1] offset:12288\n\t" "s_or_b64 s[62:63], s[62:63], vcc\n\t" \
    "s_waitcnt lgkmcnt(3)\n\t" "v_max3_u32 v212, v212, v213, v214\n\t" "v_max_u32 v212, v212, v215\n\t" "v_cmp_eq_u32 vcc, -1, v212\n\t" \
    "ds_read_b128 v[212:215], %[l1] offset:13312\n\t" "s_or_b64 s[62:63], s[62:63], vcc\n\t" "s_waitcnt lgkmcnt(3)\n\t" "v_max3_u32 v216, v216, v217, v218\n\t" \
    "v_max_u32 v216, v216, v219\n\t" "v_cmp_eq_u32 vcc, -1, v216\n\t" "ds_read_b128 v[216:219], %[l1] offset:14336\n\t" "s_or_b64 s[62:63], s[62:63], vcc\n\t" \
    "s_waitcnt lgkmcnt(3)\n\t" "v_max3_u32 v220, v220, v221, v222\n\t" "v_max_u32 v220, v220, v223\n\t" "v_cmp_eq_u32 vcc, -1, v220\n\t" \
    "ds_read_b128 v[220:223], %[l1] offset:15360\n\t" "s_or_b64 s[62:63], s[62:63], vcc\n\t" "s_waitcnt lgkmcnt(3)\n\t" "v_max3_u32 v208, v208, v209, v210\n\t" \
    "v_max_u32 v208, v208, v211\n\t" "v_cmp_eq_u32 vcc, -1, v208\n\t" "s_or_b64 s[62:63], s[62:63], vcc\n\t" "s_waitcnt lgkmcnt(2)\n\t" \
    "v_max3_u32 v212, v212, v213, v214\n\t" "v_max_u32 v212, v212, v215\n\t" "v_cmp_eq_u32 vcc, -1, v212\n\t" "s_or_b64 s[62:63], s[62:63], vcc\n\t" \
    "s_waitcnt lgkmcnt(1)\n\t" "v_max3_u32 v216, v216, v217, v218\n\t" "v_max_u32 v216, v216, v219\n\t" "v_cmp_eq_u32 vcc, -1, v216\n\t" \
    "s_or_b64 s[62:63], s[62:63], vcc\n\t" "s_waitcnt lgkmcnt(0)\n\t" "v_max3_u32 v220, v220, v221, v222\n\t" "v_max_u32 v220, v220, v223\n\t" \
    "v_cmp_eq_u32 vcc, -1, v220\n\t" "s_or_b64 s[62:63], s[62:63], vcc\n\t" "v_mov_b64 v[208:209], 0\n\t" "v_mov_b64 v[210:211], 0\n\t" "v_mov_b64 v[212:213], 0\n\t" \
    "v_mov_b64 v[214:215], 0\n\t" "s_cmp_eq_u64 s[62:63], 0\n\t" "s_cbranch_scc1 32b\n\t" "s_cmp_eq_u32 %[rt], 0\n\t" "s_cbranch_scc1 32b\n\t" "s_sub_u32 %[rt], %[rt], 1\n\t" \
    "v_xor_b32 v216, 0x400, %[mo]\n\t" XCNT(100) "34:\n\t" "buffer_load_dwordx4 v[240:243], %[mo], %[rs], %[sb] offen sc1\n\t" \
    "buffer_load_dwordx4 v[244:247], v216, %[rs], %[sb] offen sc1\n\t" "buffer_load_dwordx4 v[248:251], %[mo], %[rs], s60 offen sc1\n\t" \
    "buffer_load_dwordx4 v[252:255], v216, %[rs], s60 offen sc1\n\t" "s_waitcnt vmcnt(0)\n\t" "v_max_u32 v220, v240, v241\n\t" "v_max3_u32 v220, v220, v242, v243\n\t" \
    "v_max3_u32 v220, v220, v244, v245\n\t" "v_max3_u32 v220, v220, v246, v247\n\t" "v_max3_u32 v220, v220, v248, v249\n\t" "v_max3_u32 v220, v220, v250, v251\n\t" \
    "v_max3_u32 v220, v220, v252, v253\n\t" "v_max3_u32 v220, v220, v254, v255\n\t" "v_cmp_eq_u32 vcc, -1, v220\n\t" "s_cbranch_vccz 35f\n\t" "s_cmp_eq_u32 %[bd], 0\n\t" \
    "s_cbranch_scc1 35f\n\t" "s_sub_u32 %[bd], %[bd], 1\n\t" XCNT(101) "s_sleep 1\n\t" "s_branch 34b\n\t" "35:\n\t" "s_barrier\n\t" "s_branch 30b\n\t" "99:\n\t"
#define QK_STEP3 \
    "v_xor_b32 v216, 0x400, %[mo]\n\t" "s_xor_b32 s60, %[sb], 0x800\n\t" "s_mov_b32 s67, %[pb]\n\t" XT(80, 81) "s_getpc_b64 s[62:63]\n\t" "48:\n\t" "s_memtime s[64:65]\n\t" \
    "s_waitcnt lgkmcnt(0)\n\t" "s_sub_u32 s61, %[tg], s64\n\t" "s_cmp_gt_i32 s61, 124\n\t" "s_cbranch_scc1 48b\n\t" "s_max_i32 s61, s61, 0\n\t" "s_and_b32 s61, s61, 124\n\t" \
    "s_sub_u32 s61, 49f-48b, s61\n\t" "s_add_u32 s62, s62, s61\n\t" "s_addc_u32 s63, s63, 0\n\t" "s_setpc_b64 s[62:63]\n\t" ".rept 31\n\t" "s_nop 0\n\t" ".endr\n\t" "49:\n\t" \
    XT(98, 99) "s_barrier\n\t" XT(82, 83) "40:\n\t" "buffer_load_dwordx4 v[240:243], %[mo], %[rs], %[sb] offen sc1\n\t" "v_mov_b64 v[224:225], 0\n\t" \
    "v_mov_b64 v[226:227], 0\n\t" "v_mov_b64 v[228:229], 0\n\t" "v_mov_b64 v[230:231], 0\n\t" "v_mov_b64 v[232:233], 0\n\t" "v_mov_b64 v[234:235], 0\n\t" \
    "v_mov_b64 v[236:237], 0\n\t" "v_mov_b64 v[238:239], 0\n\t" "buffer_load_dwordx4 v[244:247], v216, %[rs], %[sb] offen sc1\n\t" \
    "ds_read_b128 v[160:163], %[l0] offset:0\n\t" "ds_read_b128 v[164:167], %[l0] offset:1024\n\t" "ds_read_b128 v[168:171], %[l0] offset:2048\n\t" \
    "ds_read_b128 v[172:175], %[l0] offset:3072\n\t" "s_nop 3\n\t" "buffer_load_dwordx4 v[248:251], %[mo], %[rs], s60 offen sc1\n\t" \
    "ds_read_b128 v[176:179], %[l0] offset:4096\n\t" "ds_read_b128 v[180:183], %[l0] offset:5120\n\t" "ds_read_b128 v[184:187], %[l0] offset:6144\n\t" \
    "ds_read_b128 v[188:191], %[l0] offset:7168\n\t" "s_nop 3\n\t" "buffer_load_dwordx4 v[252:255], v216, %[rs], s60 offen sc1\n\t" "s_waitcnt vmcnt(3)\n\t" XT(84, 85) \
    "v_smfmac_f32_16x16x64_bf16 v[224:227], v[240:243], a[0:7], %[ix]\n\t" "ds_write_b128 %[l1], v[240:243] offset:12288\n\t" \
    "v_smfmac_f32_16x16x64_bf16 v[228:231], v[240:243], a[8:15], %[ix]\n\t" "v_smfmac_f32_16x16x64_bf16 v[232:235], v[240:243], v[32:39], %[ix]\n\t" \
    "s_waitcnt lgkmcnt(7)\n\t" "v_smfmac_f32_16x16x64_bf16 v[236:239], v[240:243], v[160:167], %[ix]\n\t" "ds_read_b128 v[160:163], %[l0] offset:8192\n\t" \
    "ds_read_b128 v[164:167], %[l0] offset:9216\n\t" "s_waitcnt vmcnt(2)\n\t" "v_smfmac_f32_16x16x64_bf16 v[224:227], v[244:247], a[16:23], %[ix]\n\t" \
    "ds_write_b128 %[l1], v[244:247] offset:13312\n\t" "v_smfmac_f32_16x16x64_bf16 v[228:231], v[244:247], a[24:31], %[ix]\n\t" \
    "v_smfmac_f32_16x16x64_bf16 v[232:235], v[244:247], v[40:47], %[ix]\n\t" "s_waitcnt lgkmcnt(8)\n\t" \
    "v_smfmac_f32_16x16x64_bf16 v[236:239], v[244:247], v[168:175], %[ix]\n\t" "ds_read_b128 v[168:171], %[l0] offset:10240\n\t" \
    "ds_read_b128 v[172:175], %[l0] offset:11264\n\t" "s_waitcnt vmcnt(1)\n\t" "v_smfmac_f32_16x16x64_bf16 v[224:227], v[248:251], a[32:39], %[ix]\n\t" \
    "ds_write_b128 %[l1], v[248:251] offset:14336\n\t" "v_smfmac_f32_16x16x64_bf16 v[228:231], v[248:251], a[40:47], %[ix]\n\t" "s_waitcnt lgkmcnt(3)\n\t" "s_barrier\n\t" \
    XT(86, 87) "v_smfmac_f32_16x16x64_bf16 v[232:235], v[248:251], v[48:55], %[ix]\n\t" "ds_read_b128 v[192:195], %[l1] offset:0\n\t" \
    "ds_read_b128 v[196:199], %[l1] offset:4096\n\t" "v_smfmac_f32_16x16x64_bf16 v[236:239], v[248:251], v[176:183], %[ix]\n\t" \
    "ds_read_b128 v[176:179], %[l0] offset:12288\n\t" "ds_read_b128 v[180:183], %[l0] offset:13312\n\t" "s_waitcnt vmcnt(0)\n\t" \
    "v_smfmac_f32_16x16x64_bf16 v[224:227], v[252:255], a[48:55], %[ix]\n\t" "ds_write_b128 %[l1], v[252:255] offset:15360\n\t" \
    "v_smfmac_f32_16x16x64_bf16 v[228:231], v[252:255], a[56:63], %[ix]\n\t" "ds_read_b128 v[240:243], %[l1] offset:8192\n\t" "ds_read_b128 v[244:247], %[l1] offset:1024\n\t" \
    "v_smfmac_f32_16x16x64_bf16 v[232:235], v[252:255], v[56:63], %[ix]\n\t" "v_smfmac_f32_16x16x64_bf16 v[236:239], v[252:255], v[184:191], %[ix]\n\t" \
    "ds_read_b128 v[184:187], %[l0] offset:14336\n\t" "ds_read_b128 v[188:191], %[l0] offset:15360\n\t" "s_waitcnt lgkmcnt(8)\n\t" \
    "v_smfmac_f32_16x16x64_bf16 v[224:227], v[192:195], a[64:71], %[ix]\n\t" "v_smfmac_f32_16x16x64_bf16 v[228:231], v[192:195], a[72:79], %[ix]\n\t" \
    "v_smfmac_f32_16x16x64_bf16 v[232:235], v[192:195], v[64:71], %[ix]\n\t" "s_waitcnt lgkmcnt(4)\n\t" "s_barrier\n\t" XT(88, 89) \
    "v_smfmac_f32_16x16x64_bf16 v[236:239], v[192:195], v[160:167], %[ix]\n\t" "ds_read_b128 v[160:163], %[l0] offset:16384\n\t" \
    "ds_read_b128 v[164:167], %[l0] offset:17408\n\t" "v_smfmac_f32_16x16x64_bf16 v[224:227], v[196:199], a[80:87], %[ix]\n\t" \
    "ds_read_b128 v[248:251], %[l1] offset:5120\n\t" "ds_read_b128 v[252:255], %[l1] offset:9216\n\t" "v_smfmac_f32_16x16x64_bf16 v[228:231], v[196:199], a[88:95], %[ix]\n\t" \
    "v_lshlrev_b32 v16, 3, %[l1]\n\t" "ds_read_b128 v[192:195], %[l1] offset:2048\n\t" "v_smfmac_f32_16x16x64_bf16 v[232:235], v[196:199], v[72:79], %[ix]\n\t" \
    "buffer_load_dword v17, v16, %[rs], %[kn] offen sc1\n\t" "v_smfmac_f32_16x16x64_bf16 v[236:239], v[196:199], v[168:175], %[ix]\n\t" \
    "ds_read_b128 v[168:171], %[l0] offset:18432\n\t" "ds_read_b128 v[172:175], %[l0] offset:19456\n\t" "s_waitcnt lgkmcnt(10)\n\t" \
    "v_smfmac_f32_16x16x64_bf16 v[224:227], v[240:243], a[96:103], %[ix]\n\t" "ds_read_b128 v[196:199], %[l1] offset:6144\n\t" \
    "v_smfmac_f32_16x16x64_bf16 v[228:231], v[240:243], a[104:111], %[ix]\n\t" "v_smfmac_f32_16x16x64_bf16 v[232:235], v[240:243], v[80:87], %[ix]\n\t" \
    "v_smfmac_f32_16x16x64_bf16 v[236:239], v[240:243], v[176:183], %[ix]\n\t" "ds_read_b128 v[176:179], %[l0] offset:20480\n\t" \
    "ds_read_b128 v[180:183], %[l0] offset:21504\n\t" "s_waitcnt lgkmcnt(12)\n\t" "v_smfmac_f32_16x16x64_bf16 v[224:227], v[244:247], a[112:119], %[ix]\n\t" \
    "ds_read_b128 v[240:243], %[l1] offset:10240\n\t" "v_smfmac_f32_16x16x64_bf16 v[228:231], v[244:247], a[120:127], %[ix]\n\t" \
    "v_smfmac_f32_16x16x64_bf16 v[232:235], v[244:247], v[88:95], %[ix]\n\t" "s_waitcnt lgkmcnt(11)\n\t" \
    "v_smfmac_f32_16x16x64_bf16 v[236:239], v[244:247], v[184:191], %[ix]\n\t" "ds_read_b128 v[184:187], %[l0] offset:22528\n\t" \
    "ds_read_b128 v[188:191], %[l0] offset:23552\n\t" "s_waitcnt lgkmcnt(10)\n\t" "v_smfmac_f32_16x16x64_bf16 v[224:227], v[248:251], a[128:135], %[ix]\n\t" \
    "ds_read_b128 v[244:247], %[l1] offset:3072\n\t" "v_smfmac_f32_16x16x64_bf16 v[228:231], v[248:251], a[136:143], %[ix]\n\t" \
    "v_smfmac_f32_16x16x64_bf16 v[232:235], v[248:251], v[96:103], %[ix]\n\t" "v_smfmac_f32_16x16x64_bf16 v[236:239], v[248:251], v[160:167], %[ix]\n\t" \
    "ds_read_b128 v[160:163], %[l0] offset:24576\n\t" "ds_read_b128 v[164:167], %[l0] offset:25600\n\t" "s_waitcnt lgkmcnt(12)\n\t" \
    "v_smfmac_f32_16x16x64_bf16 v[224:227], v[252:255], a[144:151], %[ix]\n\t" "ds_read_b128 v[248:251], %[l1] offset:7168\n\t" \
    "v_smfmac_f32_16x16x64_bf16 v[228:231], v[252:255], a[152:159], %[ix]\n\t" "v_smfmac_f32_16x16x64_bf16 v[232:235], v[252:255], v[104:111], %[ix]\n\t" \
    "s_waitcnt lgkmcnt(10)\n\t" "v_smfmac_f32_16x16x64_bf16 v[236:239], v[252:255], v[168:175], %[ix]\n\t" "ds_read_b128 v[168:171], %[l0] offset:26624\n\t" \
    "ds_read_b128 v[172:175], %[l0] offset:27648\n\t" "v_smfmac_f32_16x16x64_bf16 v[224:227], v[192:195], a[160:167], %[ix]\n\t" \
    "ds_read_b128 v[252:255], %[l1] offset:11264\n\t" "v_smfmac_f32_16x16x64_bf16 v[228:231], v[192:195], a[168:175], %[ix]\n\t" "s_waitcnt lgkmcnt(12)\n\t" \
    "v_smfmac_f32_16x16x64_bf16 v[224:227], v[196:199], a[176:183], %[ix]\n\t" "v_smfmac_f32_16x16x64_bf16 v[228:231], v[196:199], a[184:191], %[ix]\n\t" \
    "s_waitcnt lgkmcnt(9)\n\t" "v_smfmac_f32_16x16x64_bf16 v[224:227], v[240:243], a[192:199], %[ix]\n\t" \
    "v_smfmac_f32_16x16x64_bf16 v[228:231], v[240:243], a[200:207], %[ix]\n\t" "s_waitcnt lgkmcnt(6)\n\t" \
    "v_smfmac_f32_16x16x64_bf16 v[224:227], v[244:247], a[208:215], %[ix]\n\t" "v_smfmac_f32_16x16x64_bf16 v[228:231], v[244:247], a[216:223], %[ix]\n\t" \
    "s_waitcnt lgkmcnt(3)\n\t" "v_smfmac_f32_16x16x64_bf16 v[224:227], v[248:251], a[224:231], %[ix]\n\t" \
    "v_smfmac_f32_16x16x64_bf16 v[228:231], v[248:251], a[232:239], %[ix]\n\t" "s_waitcnt lgkmcnt(0)\n\t" \
    "v_smfmac_f32_16x16x64_bf16 v[224:227], v[252:255], a[240:247], %[ix]\n\t" "s_nop 1\n\t" "v_smfmac_f32_16x16x64_bf16 v[228:231], v[252:255], a[248:255], %[ix]\n\t" \
    XT(90, 91) "s_nop 1\n\t" "v_smfmac_f32_16x16x64_bf16 v[232:235], v[192:195], v[112:119], %[ix]\n\t" "s_nop 1\n\t" \
    "v_smfmac_f32_16x16x64_bf16 v[236:239], v[192:195], v[176:183], %[ix]\n\t" "ds_read_b128 v[176:179], %[l0] offset:28672\n\t" "v_add_f32 v224, v224, v225\n\t" \
    "v_add_f32 v226, v226, v227\n\t" "v_smfmac_f32_16x16x64_bf16 v[232:235], v[196:199], v[120:127], %[ix]\n\t" "ds_read_b128 v[180:183], %[l0] offset:29696\n\t" \
    "v_add_f32 v228, v228, v229\n\t" "v_add_f32 v230, v230, v231\n\t" "v_cmp_u_f32 vcc, v224, v226\n\t" \
    "v_smfmac_f32_16x16x64_bf16 v[236:239], v[196:199], v[184:191], %[ix]\n\t" "ds_read_b128 v[184:187], %[l0] offset:30720\n\t" "s_cbranch_vccnz 41f\n\t" "42:\n\t" \
    "v_cvt_pk_bf16_f32 v218, v224, v228\n\t" "v_cvt_pk_bf16_f32 v219, v226, v230\n\t" "v_smfmac_f32_16x16x64_bf16 v[232:235], v[240:243], v[128:135], %[ix]\n\t" \
    "s_memtime s[64:65]\n\t" "ds_write2_b32 %[qw], v218, v219 offset1:8\n\t" "v_smfmac_f32_16x16x64_bf16 v[236:239], v[240:243], v[160:167], %[ix]\n\t" \
    "ds_read_b128 v[188:191], %[l0] offset:31744\n\t" "v_smfmac_f32_16x16x64_bf16 v[232:235], v[244:247], v[136:143], %[ix]\n\t" \
    "v_smfmac_f32_16x16x64_bf16 v[236:239], v[244:247], v[168:175], %[ix]\n\t" "s_waitcnt lgkmcnt(1)\n\t" "s_barrier\n\t" \
    "v_smfmac_f32_16x16x64_bf16 v[232:235], v[248:251], v[144:151], %[ix]\n\t" "s_sub_u32 s62, s64, %[ca]\n\t" "s_mul_hi_u32 s63, s62, %[k1]\n\t" \
    "s_mul_i32 s62, s62, %[k1]\n\t" "v_smfmac_f32_16x16x64_bf16 v[236:239], v[248:251], v[176:183], %[ix]\n\t" "s_lshr_b64 s[62:63], s[62:63], 16\n\t" \
    "s_add_u32 s62, s62, %[ga]\n\t" "s_bitset0_b32 s62, 0\n\t" "v_smfmac_f32_16x16x64_bf16 v[232:235], v[252:255], v[152:159], %[ix]\n\t" "s_waitcnt lgkmcnt(0)\n\t" \
    "v_smfmac_f32_16x16x64_bf16 v[236:239], v[252:255], v[184:191], %[ix]\n\t" XT(92, 93) "v_mov_b32 v217, s62\n\t" "buffer_store_dword v217, %[hq], %[rs], s67 offen sc1\n\t" \
    "s_nop 4\n\t" "v_add_f32 v232, v232, v233\n\t" "v_add_f32 v234, v234, v235\n\t" "v_add_f32 v236, v236, v237\n\t" "v_add_f32 v238, v238, v239\n\t" \
    "v_cvt_pk_bf16_f32 v232, v232, v236\n\t" "v_cvt_pk_bf16_f32 v233, v234, v238\n\t" "ds_write2_b32 %[kw], v232, v233 offset1:68\n\t" "s_waitcnt lgkmcnt(0)\n\t" \
    "s_barrier\n\t" XT(94, 95) XT(96, 97) "s_branch 99f\n\t" "41:\n\t" "s_mov_b64 s[62:63], 0\n\t" "ds_read_b128 v[208:211], %[l1] offset:0\n\t" \
    "ds_read_b128 v[212:215], %[l1] offset:1024\n\t" "ds_read_b128 v[216:219], %[l1] offset:2048\n\t" "ds_read_b128 v[220:223], %[l1] offset:3072\n\t" \
    "s_waitcnt lgkmcnt(3)\n\t" "v_max3_u32 v208, v208, v209, v210\n\t" "v_max_u32 v208, v208, v211\n\t" "v_cmp_eq_u32 vcc, -1, v208\n\t" \
    "ds_read_b128 v[208:211], %[l1] offset:4096\n\t" "s_or_b64 s[62:63], s[62:63], vcc\n\t" "s_waitcnt lgkmcnt(3)\n\t" "v_max3_u32 v212, v212, v213, v214\n\t" \
    "v_max_u32 v212, v212, v215\n\t" "v_cmp_eq_u32 vcc, -1, v212\n\t" "ds_read_b128 v[212:215], %[l1] offset:5120\n\t" "s_or_b64 s[62:63], s[62:63], vcc\n\t" \
    "s_waitcnt lgkmcnt(3)\n\t" "v_max3_u32 v216, v216, v217, v218\n\t" "v_max_u32 v216, v216, v219\n\t" "v_cmp_eq_u32 vcc, -1, v216\n\t" \
    "ds_read_b128 v[216:219], %[l1] offset:6144\n\t" "s_or_b64 s[62:63], s[62:63], vcc\n\t" "s_waitcnt lgkmcnt(3)\n\t" "v_max3_u32 v220, v220, v221, v222\n\t" \
    "v_max_u32 v220, v220, v223\n\t" "v_cmp_eq_u32 vcc, -1, v220\n\t" "ds_read_b128 v[220:223], %[l1] offset:7168\n\t" "s_or_b64 s[62:63], s[62:63], vcc\n\t" \
    "s_waitcnt lgkmcnt(3)\n\t" "v_max3_u32 v208, v208, v209, v210\n\t" "v_max_u32 v208, v208, v211\n\t" "v_cmp_eq_u32 vcc, -1, v208\n\t" \
    "ds_read_b128 v[208:211], %[l1] offset:8192\n\t" "s_or_b64 s[62:63], s[62:63], vcc\n\t" "s_waitcnt lgkmcnt(3)\n\t" "v_max3_u32 v212, v212, v213, v214\n\t" \
    "v_max_u32 v212, v212, v215\n\t" "v_cmp_eq_u32 vcc, -1, v212\n\t" "ds_read_b128 v[212:215], %[l1] offset:9216\n\t" "s_or_b64 s[62:63], s[62:63], vcc\n\t" \
    "s_waitcnt lgkmcnt(3)\n\t" "v_max3_u32 v216, v216, v217, v218\n\t" "v_max_u32 v216, v216, v219\n\t" "v_cmp_eq_u32 vcc, -1, v216\n\t" \
    "ds_read_b128 v[216:219], %[l1] offset:10240\n\t" "s_or_b64 s[62:63], s[62:63], vcc\n\t" "s_waitcnt lgkmcnt(3)\n\t" "v_max3_u32 v220, v220, v221, v222\n\t" \
    "v_max_u32 v220, v220, v223\n\t" "v_cmp_eq_u32 vcc, -1, v220\n\t" "ds_read_b128 v[220:223], %[l1] offset:11264\n\t" "s_or_b64 s[62:63], s[62:63], vcc\n\t" \
    "s_waitcnt lgkmcnt(3)\n\t" "v_max3_u32 v208, v208, v209, v210\n\t" "v_max_u32 v208, v208, v211\n\t" "v_cmp_eq_u32 vcc, -1, v208\n\t" \
    "ds_read_b128 v[208:211], %[l1] offset:12288\n\t" "s_or_b64 s[62:63], s[62:63], vcc\n\t" "s_waitcnt lgkmcnt(3)\n\t" "v_max3_u32 v212, v212, v213, v214\n\t" \
    "v_max_u32 v212, v212, v215\n\t" "v_cmp_eq_u32 vcc, -1, v212\n\t" "ds_read_b128 v[212:215], %[l1] offset:13312\n\t" "s_or_b64 s[62:63], s[62:63], vcc\n\t" \
    "s_waitcnt lgkmcnt(3)\n\t" "v_max3_u32 v216, v216, v217, v218\n\t" "v_max_u32 v216, v216, v219\n\t" "v_cmp_eq_u32 vcc, -1, v216\n\t" \
    "ds_read_b128 v[216:219], %[l1] offset:14336\n\t" "s_or_b64 s[62:63], s[62:63], vcc\n\t" "s_waitcnt lgkmcnt(3)\n\t" "v_max3_u32 v220, v220, v221, v222\n\t" \
    "v_max_u32 v220, v220, v223\n\t" "v_cmp_eq_u32 vcc, -1, v220\n\t" "ds_read_b128 v[220:223], %[l1] offset:15360\n\t" "s_or_b64 s[62:63], s[62:63], vcc\n\t" \
    "s_waitcnt lgkmcnt(3)\n\t" "v_max3_u32 v208, v208, v209, v210\n\t" "v_max_u32 v208, v208, v211\n\t" "v_cmp_eq_u32 vcc, -1, v208\n\t" \
    "s_or_b64 s[62:63], s[62:63], vcc\n\t" "s_waitcnt lgkmcnt(2)\n\t" "v_max3_u32 v212, v212, v213, v214\n\t" "v_max_u32 v212, v212, v215\n\t" \
    "v_cmp_eq_u32 vcc, -1, v212\n\t" "s_or_b64 s[62:63], s[62:63], vcc\n\t" "s_waitcnt lgkmcnt(1)\n\t" "v_max3_u32 v216, v216, v217, v218\n\t" \
    "v_max_u32 v216, v216, v219\n\t" "v_cmp_eq_u32 vcc, -1, v216\n\t" "s_or_b64 s[62:63], s[62:63], vcc\n\t" "s_waitcnt lgkmcnt(0)\n\t" \
    "v_max3_u32 v220, v220, v221, v222\n\t" "v_max_u32 v220, v220, v223\n\t" "v_cmp_eq_u32 vcc, -1, v220\n\t" "s_or_b64 s[62:63], s[62:63], vcc\n\t" \
    "v_mov_b64 v[208:209], 0\n\t" "v_mov_b64 v[210:211], 0\n\t" "v_mov_b64 v[212:213], 0\n\t" "v_mov_b64 v[214:215], 0\n\t" "s_cmp_eq_u64 s[62:63], 0\n\t" \
    "s_cbranch_scc1 42b\n\t" "s_cmp_eq_u32 %[rt], 0\n\t" "s_cbranch_scc1 42b\n\t" "s_sub_u32 %[rt], %[rt], 1\n\t" "v_xor_b32 v216, 0x400, %[mo]\n\t" XCNT(100) "44:\n\t" \
    "buffer_load_dwordx4 v[240:243], %[mo], %[rs], %[sb] offen sc1\n\t" "buffer_load_dwordx4 v[244:247], v216, %[rs], %[sb] offen sc1\n\t" \
    "buffer_load_dwordx4 v[248:251], %[mo], %[rs], s60 offen sc1\n\t" "buffer_load_dwordx4 v[252:255], v216, %[rs], s60 offen sc1\n\t" "s_waitcnt vmcnt(0)\n\t" \
    "v_max_u32 v220, v240, v241\n\t" "v_max3_u32 v220, v220, v242, v243\n\t" "v_max3_u32 v220, v220, v244, v245\n\t" "v_max3_u32 v220, v220, v246, v247\n\t" \
    "v_max3_u32 v220, v220, v248, v249\n\t" "v_max3_u32 v220, v220, v250, v251\n\t" "v_max3_u32 v220, v220, v252, v253\n\t" "v_max3_u32 v220, v220, v254, v255\n\t" \
    "v_cmp_eq_u32 vcc, -1, v220\n\t" "s_cbranch_vccz 45f\n\t" "s_cmp_eq_u32 %[bd], 0\n\t" "s_cbranch_scc1 45f\n\t" "s_sub_u32 %[bd], %[bd], 1\n\t" XCNT(101) "s_sleep 1\n\t" \
    "s_branch 44b\n\t" "45:\n\t" "s_barrier\n\t" "s_memtime s[64:65]\n\t" "s_waitcnt lgkmcnt(0)\n\t" "s_add_u32 s64, s64, %[dr]\n\t" "s_sub_u32 s62, s64, %[ca]\n\t" \
    "s_mul_hi_u32 s63, s62, %[k1]\n\t" "s_mul_i32 s62, s62, %[k1]\n\t" "s_lshr_b64 s[62:63], s[62:63], 16\n\t" "s_add_u32 s62, s62, %[ga]\n\t" "s_bitset0_b32 s62, 0\n\t" \
    "v_mov_b32 v220, s62\n\t" "buffer_store_dword v220, %[hq], %[rs], s67 offen sc1\n\t" "s_mov_b32 s67, 0x7f000000\n\t" "s_branch 40b\n\t" "99:\n\t" "mk_tail:\n\t"
#define VO_STEP \
    "v_xor_b32 v224, 0x400, %[mo]\n\t" "s_xor_b32 s60, %[sb], 0x800\n\t" "s_mov_b32 s67, %[pb]\n\t" "s_cmp_eq_u32 %[w], 1\n\t" "s_cbranch_scc1 91f\n\t" \
    "s_cmp_eq_u32 %[w], 2\n\t" "s_cbranch_scc1 92f\n\t" "s_cmp_eq_u32 %[w], 3\n\t" "s_cbranch_scc1 93f\n\t" XT(80, 81) "s_getpc_b64 s[62:63]\n\t" "18:\n\t" \
    "s_memtime s[64:65]\n\t" "s_waitcnt lgkmcnt(0)\n\t" "s_sub_u32 s61, %[tg], s64\n\t" "s_cmp_gt_i32 s61, 124\n\t" "s_cbranch_scc1 18b\n\t" "s_max_i32 s61, s61, 0\n\t" \
    "s_and_b32 s61, s61, 124\n\t" "s_sub_u32 s61, 19f-18b, s61\n\t" "s_add_u32 s62, s62, s61\n\t" "s_addc_u32 s63, s63, 0\n\t" "s_setpc_b64 s[62:63]\n\t" ".rept 31\n\t" \
    "s_nop 0\n\t" ".endr\n\t" "19:\n\t" "s_barrier\n\t" XT(82, 83) "10:\n\t" "buffer_load_dwordx4 v[240:243], %[mo], %[rs], %[sb] offen sc1\n\t" "v_mov_b64 v[232:233], 0\n\t" \
    "v_mov_b64 v[234:235], 0\n\t" "v_mov_b64 v[236:237], 0\n\t" "v_mov_b64 v[238:239], 0\n\t" "v_mov_b64 v[192:193], 0\n\t" "v_mov_b64 v[194:195], 0\n\t" \
    "v_mov_b64 v[196:197], 0\n\t" "v_mov_b64 v[198:199], 0\n\t" "buffer_load_dwordx4 v[244:247], v224, %[rs], %[sb] offen sc1\n\t" \
    "ds_read_b128 v[160:163], %[l0] offset:0\n\t" "ds_read_b128 v[164:167], %[l0] offset:1024\n\t" "ds_read_b128 v[168:171], %[l0] offset:2048\n\t" \
    "ds_read_b128 v[172:175], %[l0] offset:3072\n\t" "s_nop 3\n\t" "buffer_load_dwordx4 v[248:251], %[mo], %[rs], s60 offen sc1\n\t" \
    "ds_read_b128 v[176:179], %[l0] offset:4096\n\t" "ds_read_b128 v[180:183], %[l0] offset:5120\n\t" "ds_read_b128 v[184:187], %[l0] offset:6144\n\t" \
    "ds_read_b128 v[188:191], %[l0] offset:7168\n\t" "s_nop 3\n\t" "buffer_load_dwordx4 v[252:255], v224, %[rs], s60 offen sc1\n\t" "v_mov_b64 v[200:201], 0\n\t" \
    "v_mov_b64 v[202:203], 0\n\t" "v_mov_b64 v[204:205], 0\n\t" "v_mov_b64 v[206:207], 0\n\t" "v_mov_b64 v[208:209], 0\n\t" "v_mov_b64 v[210:211], 0\n\t" \
    "v_mov_b64 v[212:213], 0\n\t" "v_mov_b64 v[214:215], 0\n\t" "v_mov_b64 v[216:217], 0\n\t" "v_mov_b64 v[218:219], 0\n\t" "v_mov_b64 v[220:221], 0\n\t" \
    "v_mov_b64 v[222:223], 0\n\t" "s_waitcnt vmcnt(3)\n\t" XT(84, 85) "v_smfmac_f32_16x16x64_bf16 v[232:235], v[240:243], a[0:7], %[ix]\n\t" \
    "ds_write_b128 %[l1], v[240:243] offset:0\n\t" "v_smfmac_f32_16x16x64_bf16 v[236:239], v[240:243], a[8:15], %[ix]\n\t" "s_waitcnt vmcnt(2)\n\t" \
    "v_smfmac_f32_16x16x64_bf16 v[232:235], v[244:247], a[16:23], %[ix]\n\t" "ds_write_b128 %[l1], v[244:247] offset:1024\n\t" "s_waitcnt lgkmcnt(8)\n\t" \
    "v_smfmac_f32_16x16x64_bf16 v[236:239], v[244:247], v[160:167], %[ix]\n\t" "s_waitcnt lgkmcnt(0)\n\t" "s_barrier\n\t" XT(86, 87) \
    "ds_read_b128 v[16:19], %[l1] offset:4096\n\t" "ds_read_b128 v[20:23], %[l1] offset:8192\n\t" "ds_read_b128 v[24:27], %[l1] offset:12288\n\t" "s_waitcnt vmcnt(1)\n\t" \
    "v_smfmac_f32_16x16x64_bf16 v[232:235], v[248:251], a[24:31], %[ix]\n\t" "ds_write_b128 %[l1], v[248:251] offset:2048\n\t" \
    "v_smfmac_f32_16x16x64_bf16 v[236:239], v[248:251], a[32:39], %[ix]\n\t" "ds_read_b128 v[28:31], %[l1] offset:5120\n\t" "ds_read_b128 v[240:243], %[l1] offset:9216\n\t" \
    "ds_read_b128 v[244:247], %[l1] offset:13312\n\t" "s_waitcnt vmcnt(0)\n\t" "v_smfmac_f32_16x16x64_bf16 v[232:235], v[252:255], a[40:47], %[ix]\n\t" \
    "ds_write_b128 %[l1], v[252:255] offset:3072\n\t" "v_smfmac_f32_16x16x64_bf16 v[236:239], v[252:255], v[168:175], %[ix]\n\t" \
    "ds_read_b128 v[160:163], %[l0] offset:8192\n\t" "ds_read_b128 v[164:167], %[l0] offset:9216\n\t" "s_waitcnt lgkmcnt(9)\n\t" \
    "v_smfmac_f32_16x16x64_bf16 v[232:235], v[16:19], a[48:55], %[ix]\n\t" "ds_read_b128 v[168:171], %[l0] offset:10240\n\t" "ds_read_b128 v[172:175], %[l0] offset:11264\n\t" \
    "v_smfmac_f32_16x16x64_bf16 v[236:239], v[16:19], a[56:63], %[ix]\n\t" "s_waitcnt lgkmcnt(10)\n\t" "v_smfmac_f32_16x16x64_bf16 v[232:235], v[20:23], a[64:71], %[ix]\n\t" \
    "v_smfmac_f32_16x16x64_bf16 v[236:239], v[20:23], v[176:183], %[ix]\n\t" "s_waitcnt lgkmcnt(4)\n\t" "s_barrier\n\t" XT(88, 89) \
    "v_smfmac_f32_16x16x64_bf16 v[232:235], v[24:27], a[72:79], %[ix]\n\t" "ds_read_b128 v[176:179], %[l0] offset:12288\n\t" "ds_read_b128 v[180:183], %[l0] offset:13312\n\t" \
    "v_smfmac_f32_16x16x64_bf16 v[236:239], v[24:27], a[80:87], %[ix]\n\t" "ds_read_b128 v[248:251], %[l1] offset:6144\n\t" "ds_read_b128 v[252:255], %[l1] offset:10240\n\t" \
    "v_smfmac_f32_16x16x64_bf16 v[232:235], v[28:31], a[88:95], %[ix]\n\t" "ds_read_b128 v[16:19], %[l1] offset:14336\n\t" "ds_read_b128 v[20:23], %[l1] offset:7168\n\t" \
    "v_smfmac_f32_16x16x64_bf16 v[236:239], v[28:31], v[184:191], %[ix]\n\t" "ds_read_b128 v[24:27], %[l1] offset:11264\n\t" "ds_read_b128 v[184:187], %[l0] offset:14336\n\t" \
    "v_smfmac_f32_16x16x64_bf16 v[232:235], v[240:243], a[96:103], %[ix]\n\t" "ds_read_b128 v[188:191], %[l0] offset:15360\n\t" \
    "ds_read_b128 v[28:31], %[l1] offset:15360\n\t" "v_smfmac_f32_16x16x64_bf16 v[236:239], v[240:243], a[104:111], %[ix]\n\t" \
    "v_smfmac_f32_16x16x64_bf16 v[232:235], v[244:247], a[112:119], %[ix]\n\t" "s_waitcnt lgkmcnt(12)\n\t" \
    "v_smfmac_f32_16x16x64_bf16 v[236:239], v[244:247], v[160:167], %[ix]\n\t" "ds_read_b128 v[160:163], %[l0] offset:16384\n\t" \
    "ds_read_b128 v[164:167], %[l0] offset:17408\n\t" "s_waitcnt lgkmcnt(9)\n\t" "v_smfmac_f32_16x16x64_bf16 v[232:235], v[248:251], a[120:127], %[ix]\n\t" \
    "v_smfmac_f32_16x16x64_bf16 v[236:239], v[248:251], a[128:135], %[ix]\n\t" "s_waitcnt lgkmcnt(8)\n\t" \
    "v_smfmac_f32_16x16x64_bf16 v[232:235], v[252:255], a[136:143], %[ix]\n\t" "v_smfmac_f32_16x16x64_bf16 v[236:239], v[252:255], v[168:175], %[ix]\n\t" \
    "ds_read_b128 v[168:171], %[l0] offset:18432\n\t" "ds_read_b128 v[172:175], %[l0] offset:19456\n\t" "s_waitcnt lgkmcnt(9)\n\t" \
    "v_smfmac_f32_16x16x64_bf16 v[232:235], v[16:19], a[144:151], %[ix]\n\t" "v_smfmac_f32_16x16x64_bf16 v[236:239], v[16:19], a[152:159], %[ix]\n\t" \
    "s_waitcnt lgkmcnt(8)\n\t" "v_smfmac_f32_16x16x64_bf16 v[232:235], v[20:23], a[160:167], %[ix]\n\t" \
    "v_smfmac_f32_16x16x64_bf16 v[236:239], v[20:23], v[176:183], %[ix]\n\t" "ds_read_b128 v[176:179], %[l0] offset:20480\n\t" \
    "ds_read_b128 v[180:183], %[l0] offset:21504\n\t" "s_waitcnt lgkmcnt(9)\n\t" "v_smfmac_f32_16x16x64_bf16 v[232:235], v[24:27], a[168:175], %[ix]\n\t" \
    "v_smfmac_f32_16x16x64_bf16 v[236:239], v[24:27], a[176:183], %[ix]\n\t" "s_waitcnt lgkmcnt(6)\n\t" \
    "v_smfmac_f32_16x16x64_bf16 v[232:235], v[28:31], a[184:191], %[ix]\n\t" "v_smfmac_f32_16x16x64_bf16 v[236:239], v[28:31], v[184:191], %[ix]\n\t" \
    "ds_read_b128 v[184:187], %[l0] offset:22528\n\t" "ds_read_b128 v[188:191], %[l0] offset:23552\n\t" XT(90, 91) "s_nop 4\n\t" "v_add_f32 v232, v232, v233\n\t" \
    "v_add_f32 v234, v234, v235\n\t" "v_add_f32 v236, v236, v237\n\t" "v_add_f32 v238, v238, v239\n\t" "v_cvt_pk_bf16_f32 v236, v232, v236\n\t" \
    "v_cvt_pk_bf16_f32 v237, v234, v238\n\t" "ds_write2_b32 %[vw], v236, v237 offset1:68\n\t" "v_cmp_u_f32 vcc, v232, v234\n\t" "s_cbranch_vccnz 11f\n\t" "12:\n\t" \
    "s_memtime s[64:65]\n\t" "s_waitcnt lgkmcnt(0)\n\t" "s_barrier\n\t" XT(92, 93) "ds_read_b128 v[240:243], %[vr] offset:0\n\t" \
    "ds_read_b128 v[244:247], %[vr] offset:64\n\t" "ds_read_b128 v[248:251], %[vr] offset:128\n\t" "ds_read_b128 v[252:255], %[vr] offset:192\n\t" \
    "s_sub_u32 s62, s64, %[ca]\n\t" "s_mul_hi_u32 s63, s62, %[k1]\n\t" "s_mul_i32 s62, s62, %[k1]\n\t" "s_lshr_b64 s[62:63], s[62:63], 16\n\t" "s_add_u32 s62, s62, %[ga]\n\t" \
    "s_bitset0_b32 s62, 0\n\t" "v_mov_b32 v226, s62\n\t" "buffer_store_dword v226, %[hq], %[rs], s67 offen sc1\n\t" "s_mov_b32 s61, %[ub]\n\t" "s_waitcnt lgkmcnt(3)\n\t" \
    "v_smfmac_f32_16x16x64_bf16 v[192:195], v[240:243], v[32:39], %[ix]\n\t" "v_smfmac_f32_16x16x64_bf16 v[196:199], v[240:243], v[48:55], %[ix]\n\t" \
    "s_waitcnt lgkmcnt(2)\n\t" "v_smfmac_f32_16x16x64_bf16 v[192:195], v[244:247], v[40:47], %[ix]\n\t" \
    "v_smfmac_f32_16x16x64_bf16 v[196:199], v[244:247], v[56:63], %[ix]\n\t" "s_waitcnt lgkmcnt(1)\n\t" \
    "v_smfmac_f32_16x16x64_bf16 v[192:195], v[248:251], a[192:199], %[ix]\n\t" "v_smfmac_f32_16x16x64_bf16 v[196:199], v[248:251], a[200:207], %[ix]\n\t" \
    "s_waitcnt lgkmcnt(0)\n\t" "v_smfmac_f32_16x16x64_bf16 v[192:195], v[252:255], v[160:167], %[ix]\n\t" "ds_read_b128 v[160:163], %[l0] offset:24576\n\t" \
    "ds_read_b128 v[164:167], %[l0] offset:25600\n\t" "v_smfmac_f32_16x16x64_bf16 v[196:199], v[252:255], v[168:175], %[ix]\n\t" \
    "ds_read_b128 v[168:171], %[l0] offset:26624\n\t" "ds_read_b128 v[172:175], %[l0] offset:27648\n\t" \
    "v_smfmac_f32_16x16x64_bf16 v[200:203], v[240:243], v[64:71], %[ix]\n\t" "s_nop 1\n\t" "v_smfmac_f32_16x16x64_bf16 v[204:207], v[240:243], v[80:87], %[ix]\n\t" \
    "v_cvt_pk_bf16_f32 v224, v192, v194\n\t" "v_cvt_pk_bf16_f32 v225, v193, v195\n\t" "v_smfmac_f32_16x16x64_bf16 v[200:203], v[244:247], v[72:79], %[ix]\n\t" \
    "v_cvt_pk_bf16_f32 v226, v196, v198\n\t" "v_cvt_pk_bf16_f32 v227, v197, v199\n\t" "v_smfmac_f32_16x16x64_bf16 v[204:207], v[244:247], v[88:95], %[ix]\n\t" "s_nop 0\n\t" \
    "v_permlane32_swap_b32 v224, v226\n\t" "v_smfmac_f32_16x16x64_bf16 v[200:203], v[248:251], a[208:215], %[ix]\n\t" "v_permlane32_swap_b32 v225, v227\n\t" "s_nop 0\n\t" \
    "v_smfmac_f32_16x16x64_bf16 v[204:207], v[248:251], a[216:223], %[ix]\n\t" "v_permlane16_swap_b32 v224, v225\n\t" "v_permlane16_swap_b32 v226, v227\n\t" \
    "v_smfmac_f32_16x16x64_bf16 v[200:203], v[252:255], v[176:183], %[ix]\n\t" "ds_read_b128 v[176:179], %[l0] offset:28672\n\t" \
    "buffer_store_dwordx4 v[224:227], %[uo], %[rs], s61 offen sc1\n\t" "s_xor_b32 s61, s61, 0x40000\n\t" XT(94, 95) \
    "v_smfmac_f32_16x16x64_bf16 v[204:207], v[252:255], v[184:191], %[ix]\n\t" "ds_read_b128 v[180:183], %[l0] offset:29696\n\t" \
    "ds_read_b128 v[184:187], %[l0] offset:30720\n\t" "v_smfmac_f32_16x16x64_bf16 v[208:211], v[240:243], v[96:103], %[ix]\n\t" \
    "ds_read_b128 v[188:191], %[l0] offset:31744\n\t" "s_nop 0\n\t" "v_smfmac_f32_16x16x64_bf16 v[212:215], v[240:243], v[112:119], %[ix]\n\t" \
    "v_cvt_pk_bf16_f32 v228, v200, v202\n\t" "v_cvt_pk_bf16_f32 v229, v201, v203\n\t" "v_smfmac_f32_16x16x64_bf16 v[208:211], v[244:247], v[104:111], %[ix]\n\t" \
    "v_cvt_pk_bf16_f32 v230, v204, v206\n\t" "v_cvt_pk_bf16_f32 v231, v205, v207\n\t" "v_smfmac_f32_16x16x64_bf16 v[212:215], v[244:247], v[120:127], %[ix]\n\t" "s_nop 0\n\t" \
    "v_permlane32_swap_b32 v228, v230\n\t" "v_smfmac_f32_16x16x64_bf16 v[208:211], v[248:251], a[224:231], %[ix]\n\t" "v_permlane32_swap_b32 v229, v231\n\t" "s_nop 0\n\t" \
    "v_smfmac_f32_16x16x64_bf16 v[212:215], v[248:251], a[232:239], %[ix]\n\t" "v_permlane16_swap_b32 v228, v229\n\t" "v_permlane16_swap_b32 v230, v231\n\t" \
    "s_waitcnt lgkmcnt(6)\n\t" "v_smfmac_f32_16x16x64_bf16 v[208:211], v[252:255], v[160:167], %[ix]\n\t" "buffer_store_dwordx4 v[228:231], %[uo], %[rs], s61 offen sc1\n\t" \
    "s_xor_b32 s61, s61, 0xc0000\n\t" "s_waitcnt lgkmcnt(4)\n\t" "v_smfmac_f32_16x16x64_bf16 v[212:215], v[252:255], v[168:175], %[ix]\n\t" "s_nop 1\n\t" \
    "v_smfmac_f32_16x16x64_bf16 v[216:219], v[240:243], v[128:135], %[ix]\n\t" "s_nop 1\n\t" "v_smfmac_f32_16x16x64_bf16 v[220:223], v[240:243], v[144:151], %[ix]\n\t" \
    "v_cvt_pk_bf16_f32 v224, v208, v210\n\t" "v_cvt_pk_bf16_f32 v225, v209, v211\n\t" "v_smfmac_f32_16x16x64_bf16 v[216:219], v[244:247], v[136:143], %[ix]\n\t" \
    "v_cvt_pk_bf16_f32 v226, v212, v214\n\t" "v_cvt_pk_bf16_f32 v227, v213, v215\n\t" "v_smfmac_f32_16x16x64_bf16 v[220:223], v[244:247], v[152:159], %[ix]\n\t" "s_nop 0\n\t" \
    "v_permlane32_swap_b32 v224, v226\n\t" "v_smfmac_f32_16x16x64_bf16 v[216:219], v[248:251], a[240:247], %[ix]\n\t" "v_permlane32_swap_b32 v225, v227\n\t" "s_nop 0\n\t" \
    "v_smfmac_f32_16x16x64_bf16 v[220:223], v[248:251], a[248:255], %[ix]\n\t" "v_permlane16_swap_b32 v224, v225\n\t" "v_permlane16_swap_b32 v226, v227\n\t" \
    "s_waitcnt lgkmcnt(2)\n\t" "v_smfmac_f32_16x16x64_bf16 v[216:219], v[252:255], v[176:183], %[ix]\n\t" "buffer_store_dwordx4 v[224:227], %[uo], %[rs], s61 offen sc1\n\t" \
    "s_xor_b32 s61, s61, 0x40000\n\t" "s_waitcnt lgkmcnt(0)\n\t" "v_smfmac_f32_16x16x64_bf16 v[220:223], v[252:255], v[184:191], %[ix]\n\t" "s_nop 5\n\t" \
    "v_cvt_pk_bf16_f32 v228, v216, v218\n\t" "v_cvt_pk_bf16_f32 v229, v217, v219\n\t" "v_cvt_pk_bf16_f32 v230, v220, v222\n\t" "v_cvt_pk_bf16_f32 v231, v221, v223\n\t" \
    "s_nop 0\n\t" "v_permlane32_swap_b32 v228, v230\n\t" "v_permlane32_swap_b32 v229, v231\n\t" "s_nop 1\n\t" "v_permlane16_swap_b32 v228, v229\n\t" \
    "v_permlane16_swap_b32 v230, v231\n\t" "buffer_store_dwordx4 v[228:231], %[uo], %[rs], s61 offen sc1\n\t" "s_nop 1\n\t" XT(96, 97) "s_branch 99f\n\t" "11:\n\t" \
    "s_mov_b64 s[62:63], 0\n\t" "ds_read_b128 v[16:19], %[l1] offset:0\n\t" "ds_read_b128 v[20:23], %[l1] offset:1024\n\t" "ds_read_b128 v[24:27], %[l1] offset:2048\n\t" \
    "ds_read_b128 v[28:31], %[l1] offset:3072\n\t" "s_waitcnt lgkmcnt(3)\n\t" "v_max3_u32 v16, v16, v17, v18\n\t" "v_max_u32 v16, v16, v19\n\t" \
    "v_cmp_eq_u32 vcc, -1, v16\n\t" "ds_read_b128 v[16:19], %[l1] offset:4096\n\t" "s_or_b64 s[62:63], s[62:63], vcc\n\t" "s_waitcnt lgkmcnt(3)\n\t" \
    "v_max3_u32 v20, v20, v21, v22\n\t" "v_max_u32 v20, v20, v23\n\t" "v_cmp_eq_u32 vcc, -1, v20\n\t" "ds_read_b128 v[20:23], %[l1] offset:5120\n\t" \
    "s_or_b64 s[62:63], s[62:63], vcc\n\t" "s_waitcnt lgkmcnt(3)\n\t" "v_max3_u32 v24, v24, v25, v26\n\t" "v_max_u32 v24, v24, v27\n\t" "v_cmp_eq_u32 vcc, -1, v24\n\t" \
    "ds_read_b128 v[24:27], %[l1] offset:6144\n\t" "s_or_b64 s[62:63], s[62:63], vcc\n\t" "s_waitcnt lgkmcnt(3)\n\t" "v_max3_u32 v28, v28, v29, v30\n\t" \
    "v_max_u32 v28, v28, v31\n\t" "v_cmp_eq_u32 vcc, -1, v28\n\t" "ds_read_b128 v[28:31], %[l1] offset:7168\n\t" "s_or_b64 s[62:63], s[62:63], vcc\n\t" \
    "s_waitcnt lgkmcnt(3)\n\t" "v_max3_u32 v16, v16, v17, v18\n\t" "v_max_u32 v16, v16, v19\n\t" "v_cmp_eq_u32 vcc, -1, v16\n\t" \
    "ds_read_b128 v[16:19], %[l1] offset:8192\n\t" "s_or_b64 s[62:63], s[62:63], vcc\n\t" "s_waitcnt lgkmcnt(3)\n\t" "v_max3_u32 v20, v20, v21, v22\n\t" \
    "v_max_u32 v20, v20, v23\n\t" "v_cmp_eq_u32 vcc, -1, v20\n\t" "ds_read_b128 v[20:23], %[l1] offset:9216\n\t" "s_or_b64 s[62:63], s[62:63], vcc\n\t" \
    "s_waitcnt lgkmcnt(3)\n\t" "v_max3_u32 v24, v24, v25, v26\n\t" "v_max_u32 v24, v24, v27\n\t" "v_cmp_eq_u32 vcc, -1, v24\n\t" \
    "ds_read_b128 v[24:27], %[l1] offset:10240\n\t" "s_or_b64 s[62:63], s[62:63], vcc\n\t" "s_waitcnt lgkmcnt(3)\n\t" "v_max3_u32 v28, v28, v29, v30\n\t" \
    "v_max_u32 v28, v28, v31\n\t" "v_cmp_eq_u32 vcc, -1, v28\n\t" "ds_read_b128 v[28:31], %[l1] offset:11264\n\t" "s_or_b64 s[62:63], s[62:63], vcc\n\t" \
    "s_waitcnt lgkmcnt(3)\n\t" "v_max3_u32 v16, v16, v17, v18\n\t" "v_max_u32 v16, v16, v19\n\t" "v_cmp_eq_u32 vcc, -1, v16\n\t" \
    "ds_read_b128 v[16:19], %[l1] offset:12288\n\t" "s_or_b64 s[62:63], s[62:63], vcc\n\t" "s_waitcnt lgkmcnt(3)\n\t" "v_max3_u32 v20, v20, v21, v22\n\t" \
    "v_max_u32 v20, v20, v23\n\t" "v_cmp_eq_u32 vcc, -1, v20\n\t" "ds_read_b128 v[20:23], %[l1] offset:13312\n\t" "s_or_b64 s[62:63], s[62:63], vcc\n\t" \
    "s_waitcnt lgkmcnt(3)\n\t" "v_max3_u32 v24, v24, v25, v26\n\t" "v_max_u32 v24, v24, v27\n\t" "v_cmp_eq_u32 vcc, -1, v24\n\t" \
    "ds_read_b128 v[24:27], %[l1] offset:14336\n\t" "s_or_b64 s[62:63], s[62:63], vcc\n\t" "s_waitcnt lgkmcnt(3)\n\t" "v_max3_u32 v28, v28, v29, v30\n\t" \
    "v_max_u32 v28, v28, v31\n\t" "v_cmp_eq_u32 vcc, -1, v28\n\t" "ds_read_b128 v[28:31], %[l1] offset:15360\n\t" "s_or_b64 s[62:63], s[62:63], vcc\n\t" \
    "s_waitcnt lgkmcnt(3)\n\t" "v_max3_u32 v16, v16, v17, v18\n\t" "v_max_u32 v16, v16, v19\n\t" "v_cmp_eq_u32 vcc, -1, v16\n\t" "s_or_b64 s[62:63], s[62:63], vcc\n\t" \
    "s_waitcnt lgkmcnt(2)\n\t" "v_max3_u32 v20, v20, v21, v22\n\t" "v_max_u32 v20, v20, v23\n\t" "v_cmp_eq_u32 vcc, -1, v20\n\t" "s_or_b64 s[62:63], s[62:63], vcc\n\t" \
    "s_waitcnt lgkmcnt(1)\n\t" "v_max3_u32 v24, v24, v25, v26\n\t" "v_max_u32 v24, v24, v27\n\t" "v_cmp_eq_u32 vcc, -1, v24\n\t" "s_or_b64 s[62:63], s[62:63], vcc\n\t" \
    "s_waitcnt lgkmcnt(0)\n\t" "v_max3_u32 v28, v28, v29, v30\n\t" "v_max_u32 v28, v28, v31\n\t" "v_cmp_eq_u32 vcc, -1, v28\n\t" "s_or_b64 s[62:63], s[62:63], vcc\n\t" \
    "s_cmp_eq_u64 s[62:63], 0\n\t" "s_cbranch_scc1 12b\n\t" "s_cmp_eq_u32 %[rt], 0\n\t" "s_cbranch_scc1 12b\n\t" "s_sub_u32 %[rt], %[rt], 1\n\t" XCNT(100) "14:\n\t" \
    "buffer_load_dwordx4 v[240:243], %[mo], %[rs], %[sb] offen sc1\n\t" "buffer_load_dwordx4 v[244:247], v224, %[rs], %[sb] offen sc1\n\t" \
    "buffer_load_dwordx4 v[248:251], %[mo], %[rs], s60 offen sc1\n\t" "buffer_load_dwordx4 v[252:255], v224, %[rs], s60 offen sc1\n\t" "s_waitcnt vmcnt(0)\n\t" \
    "v_max_u32 v28, v240, v241\n\t" "v_max3_u32 v28, v28, v242, v243\n\t" "v_max3_u32 v28, v28, v244, v245\n\t" "v_max3_u32 v28, v28, v246, v247\n\t" \
    "v_max3_u32 v28, v28, v248, v249\n\t" "v_max3_u32 v28, v28, v250, v251\n\t" "v_max3_u32 v28, v28, v252, v253\n\t" "v_max3_u32 v28, v28, v254, v255\n\t" \
    "v_cmp_eq_u32 vcc, -1, v28\n\t" "s_cbranch_vccz 15f\n\t" "s_cmp_eq_u32 %[bd], 0\n\t" "s_cbranch_scc1 15f\n\t" "s_sub_u32 %[bd], %[bd], 1\n\t" XCNT(101) "s_sleep 1\n\t" \
    "s_branch 14b\n\t" "15:\n\t" "s_barrier\n\t" "s_memtime s[64:65]\n\t" "s_waitcnt lgkmcnt(0)\n\t" "s_add_u32 s64, s64, %[dr]\n\t" "s_sub_u32 s62, s64, %[ca]\n\t" \
    "s_mul_hi_u32 s63, s62, %[k1]\n\t" "s_mul_i32 s62, s62, %[k1]\n\t" "s_lshr_b64 s[62:63], s[62:63], 16\n\t" "s_add_u32 s62, s62, %[ga]\n\t" "s_bitset0_b32 s62, 0\n\t" \
    "v_mov_b32 v28, s62\n\t" "buffer_store_dword v28, %[hq], %[rs], s67 offen sc1\n\t" "s_mov_b32 s67, 0x7f000000\n\t" "s_branch 10b\n\t" "91:\n\t" XT(80, 81) "s_barrier\n\t" \
    XT(82, 83) "20:\n\t" "buffer_load_dwordx4 v[240:243], %[mo], %[rs], %[sb] offen sc1\n\t" "v_mov_b64 v[232:233], 0\n\t" "v_mov_b64 v[234:235], 0\n\t" \
    "v_mov_b64 v[236:237], 0\n\t" "v_mov_b64 v[238:239], 0\n\t" "v_mov_b64 v[192:193], 0\n\t" "v_mov_b64 v[194:195], 0\n\t" "v_mov_b64 v[196:197], 0\n\t" \
    "v_mov_b64 v[198:199], 0\n\t" "buffer_load_dwordx4 v[244:247], v224, %[rs], %[sb] offen sc1\n\t" "ds_read_b128 v[160:163], %[l0] offset:0\n\t" \
    "ds_read_b128 v[164:167], %[l0] offset:1024\n\t" "ds_read_b128 v[168:171], %[l0] offset:2048\n\t" "ds_read_b128 v[172:175], %[l0] offset:3072\n\t" "s_nop 3\n\t" \
    "buffer_load_dwordx4 v[248:251], %[mo], %[rs], s60 offen sc1\n\t" "ds_read_b128 v[176:179], %[l0] offset:4096\n\t" "ds_read_b128 v[180:183], %[l0] offset:5120\n\t" \
    "ds_read_b128 v[184:187], %[l0] offset:6144\n\t" "ds_read_b128 v[188:191], %[l0] offset:7168\n\t" "s_nop 3\n\t" \
    "buffer_load_dwordx4 v[252:255], v224, %[rs], s60 offen sc1\n\t" "v_mov_b64 v[200:201], 0\n\t" "v_mov_b64 v[202:203], 0\n\t" "v_mov_b64 v[204:205], 0\n\t" \
    "v_mov_b64 v[206:207], 0\n\t" "v_mov_b64 v[208:209], 0\n\t" "v_mov_b64 v[210:211], 0\n\t" "v_mov_b64 v[212:213], 0\n\t" "v_mov_b64 v[214:215], 0\n\t" \
    "v_mov_b64 v[216:217], 0\n\t" "v_mov_b64 v[218:219], 0\n\t" "v_mov_b64 v[220:221], 0\n\t" "v_mov_b64 v[222:223], 0\n\t" "s_waitcnt vmcnt(3)\n\t" XT(84, 85) \
    "v_smfmac_f32_16x16x64_bf16 v[232:235], v[240:243], a[0:7], %[ix]\n\t" "ds_write_b128 %[l1], v[240:243] offset:4096\n\t" \
    "v_smfmac_f32_16x16x64_bf16 v[236:239], v[240:243], a[8:15], %[ix]\n\t" "s_waitcnt vmcnt(2)\n\t" "v_smfmac_f32_16x16x64_bf16 v[232:235], v[244:247], a[16:23], %[ix]\n\t" \
    "ds_write_b128 %[l1], v[244:247] offset:5120\n\t" "s_waitcnt lgkmcnt(8)\n\t" "v_smfmac_f32_16x16x64_bf16 v[236:239], v[244:247], v[160:167], %[ix]\n\t" \
    "s_waitcnt lgkmcnt(0)\n\t" "s_barrier\n\t" XT(86, 87) "ds_read_b128 v[16:19], %[l1] offset:8192\n\t" "ds_read_b128 v[20:23], %[l1] offset:12288\n\t" \
    "ds_read_b128 v[24:27], %[l1] offset:0\n\t" "s_waitcnt vmcnt(1)\n\t" "v_smfmac_f32_16x16x64_bf16 v[232:235], v[248:251], a[24:31], %[ix]\n\t" \
    "ds_write_b128 %[l1], v[248:251] offset:6144\n\t" "v_smfmac_f32_16x16x64_bf16 v[236:239], v[248:251], a[32:39], %[ix]\n\t" "ds_read_b128 v[28:31], %[l1] offset:9216\n\t" \
    "ds_read_b128 v[240:243], %[l1] offset:13312\n\t" "ds_read_b128 v[244:247], %[l1] offset:1024\n\t" "s_waitcnt vmcnt(0)\n\t" \
    "v_smfmac_f32_16x16x64_bf16 v[232:235], v[252:255], a[40:47], %[ix]\n\t" "ds_write_b128 %[l1], v[252:255] offset:7168\n\t" \
    "v_smfmac_f32_16x16x64_bf16 v[236:239], v[252:255], v[168:175], %[ix]\n\t" "ds_read_b128 v[160:163], %[l0] offset:8192\n\t" \
    "ds_read_b128 v[164:167], %[l0] offset:9216\n\t" "s_waitcnt lgkmcnt(9)\n\t" "v_smfmac_f32_16x16x64_bf16 v[232:235], v[16:19], a[48:55], %[ix]\n\t" \
    "ds_read_b128 v[168:171], %[l0] offset:10240\n\t" "ds_read_b128 v[172:175], %[l0] offset:11264\n\t" "v_smfmac_f32_16x16x64_bf16 v[236:239], v[16:19], a[56:63], %[ix]\n\t" \
    "s_waitcnt lgkmcnt(10)\n\t" "v_smfmac_f32_16x16x64_bf16 v[232:235], v[20:23], a[64:71], %[ix]\n\t" \
    "v_smfmac_f32_16x16x64_bf16 v[236:239], v[20:23], v[176:183], %[ix]\n\t" "s_waitcnt lgkmcnt(4)\n\t" "s_barrier\n\t" XT(88, 89) \
    "v_smfmac_f32_16x16x64_bf16 v[232:235], v[24:27], a[72:79], %[ix]\n\t" "ds_read_b128 v[176:179], %[l0] offset:12288\n\t" "ds_read_b128 v[180:183], %[l0] offset:13312\n\t" \
    "v_smfmac_f32_16x16x64_bf16 v[236:239], v[24:27], a[80:87], %[ix]\n\t" "ds_read_b128 v[248:251], %[l1] offset:10240\n\t" "ds_read_b128 v[252:255], %[l1] offset:14336\n\t" \
    "v_smfmac_f32_16x16x64_bf16 v[232:235], v[28:31], a[88:95], %[ix]\n\t" "ds_read_b128 v[16:19], %[l1] offset:2048\n\t" "ds_read_b128 v[20:23], %[l1] offset:11264\n\t" \
    "v_smfmac_f32_16x16x64_bf16 v[236:239], v[28:31], v[184:191], %[ix]\n\t" "ds_read_b128 v[24:27], %[l1] offset:15360\n\t" "ds_read_b128 v[184:187], %[l0] offset:14336\n\t" \
    "v_smfmac_f32_16x16x64_bf16 v[232:235], v[240:243], a[96:103], %[ix]\n\t" "ds_read_b128 v[188:191], %[l0] offset:15360\n\t" "ds_read_b128 v[28:31], %[l1] offset:3072\n\t" \
    "v_smfmac_f32_16x16x64_bf16 v[236:239], v[240:243], a[104:111], %[ix]\n\t" "v_smfmac_f32_16x16x64_bf16 v[232:235], v[244:247], a[112:119], %[ix]\n\t" \
    "s_waitcnt lgkmcnt(12)\n\t" "v_smfmac_f32_16x16x64_bf16 v[236:239], v[244:247], v[160:167], %[ix]\n\t" "ds_read_b128 v[160:163], %[l0] offset:16384\n\t" \
    "ds_read_b128 v[164:167], %[l0] offset:17408\n\t" "s_waitcnt lgkmcnt(9)\n\t" "v_smfmac_f32_16x16x64_bf16 v[232:235], v[248:251], a[120:127], %[ix]\n\t" \
    "v_smfmac_f32_16x16x64_bf16 v[236:239], v[248:251], a[128:135], %[ix]\n\t" "s_waitcnt lgkmcnt(8)\n\t" \
    "v_smfmac_f32_16x16x64_bf16 v[232:235], v[252:255], a[136:143], %[ix]\n\t" "v_smfmac_f32_16x16x64_bf16 v[236:239], v[252:255], v[168:175], %[ix]\n\t" \
    "ds_read_b128 v[168:171], %[l0] offset:18432\n\t" "ds_read_b128 v[172:175], %[l0] offset:19456\n\t" "s_waitcnt lgkmcnt(9)\n\t" \
    "v_smfmac_f32_16x16x64_bf16 v[232:235], v[16:19], a[144:151], %[ix]\n\t" "v_smfmac_f32_16x16x64_bf16 v[236:239], v[16:19], a[152:159], %[ix]\n\t" \
    "s_waitcnt lgkmcnt(8)\n\t" "v_smfmac_f32_16x16x64_bf16 v[232:235], v[20:23], a[160:167], %[ix]\n\t" \
    "v_smfmac_f32_16x16x64_bf16 v[236:239], v[20:23], v[176:183], %[ix]\n\t" "ds_read_b128 v[176:179], %[l0] offset:20480\n\t" \
    "ds_read_b128 v[180:183], %[l0] offset:21504\n\t" "s_waitcnt lgkmcnt(9)\n\t" "v_smfmac_f32_16x16x64_bf16 v[232:235], v[24:27], a[168:175], %[ix]\n\t" \
    "v_smfmac_f32_16x16x64_bf16 v[236:239], v[24:27], a[176:183], %[ix]\n\t" "s_waitcnt lgkmcnt(6)\n\t" \
    "v_smfmac_f32_16x16x64_bf16 v[232:235], v[28:31], a[184:191], %[ix]\n\t" "v_smfmac_f32_16x16x64_bf16 v[236:239], v[28:31], v[184:191], %[ix]\n\t" \
    "ds_read_b128 v[184:187], %[l0] offset:22528\n\t" "ds_read_b128 v[188:191], %[l0] offset:23552\n\t" XT(90, 91) "s_nop 4\n\t" "v_add_f32 v232, v232, v233\n\t" \
    "v_add_f32 v234, v234, v235\n\t" "v_add_f32 v236, v236, v237\n\t" "v_add_f32 v238, v238, v239\n\t" "v_cvt_pk_bf16_f32 v236, v232, v236\n\t" \
    "v_cvt_pk_bf16_f32 v237, v234, v238\n\t" "ds_write2_b32 %[vw], v236, v237 offset1:68\n\t" "v_cmp_u_f32 vcc, v232, v234\n\t" "s_cbranch_vccnz 21f\n\t" "22:\n\t" \
    "s_waitcnt lgkmcnt(0)\n\t" "s_barrier\n\t" XT(92, 93) "ds_read_b128 v[240:243], %[vr] offset:0\n\t" "ds_read_b128 v[244:247], %[vr] offset:64\n\t" \
    "ds_read_b128 v[248:251], %[vr] offset:128\n\t" "ds_read_b128 v[252:255], %[vr] offset:192\n\t" "s_mov_b32 s61, %[ub]\n\t" "s_waitcnt lgkmcnt(3)\n\t" \
    "v_smfmac_f32_16x16x64_bf16 v[192:195], v[240:243], v[32:39], %[ix]\n\t" "v_smfmac_f32_16x16x64_bf16 v[196:199], v[240:243], v[48:55], %[ix]\n\t" \
    "s_waitcnt lgkmcnt(2)\n\t" "v_smfmac_f32_16x16x64_bf16 v[192:195], v[244:247], v[40:47], %[ix]\n\t" \
    "v_smfmac_f32_16x16x64_bf16 v[196:199], v[244:247], v[56:63], %[ix]\n\t" "s_waitcnt lgkmcnt(1)\n\t" \
    "v_smfmac_f32_16x16x64_bf16 v[192:195], v[248:251], a[192:199], %[ix]\n\t" "v_smfmac_f32_16x16x64_bf16 v[196:199], v[248:251], a[200:207], %[ix]\n\t" \
    "s_waitcnt lgkmcnt(0)\n\t" "v_smfmac_f32_16x16x64_bf16 v[192:195], v[252:255], v[160:167], %[ix]\n\t" "ds_read_b128 v[160:163], %[l0] offset:24576\n\t" \
    "ds_read_b128 v[164:167], %[l0] offset:25600\n\t" "v_smfmac_f32_16x16x64_bf16 v[196:199], v[252:255], v[168:175], %[ix]\n\t" \
    "ds_read_b128 v[168:171], %[l0] offset:26624\n\t" "ds_read_b128 v[172:175], %[l0] offset:27648\n\t" \
    "v_smfmac_f32_16x16x64_bf16 v[200:203], v[240:243], v[64:71], %[ix]\n\t" "s_nop 1\n\t" "v_smfmac_f32_16x16x64_bf16 v[204:207], v[240:243], v[80:87], %[ix]\n\t" \
    "v_cvt_pk_bf16_f32 v224, v192, v194\n\t" "v_cvt_pk_bf16_f32 v225, v193, v195\n\t" "v_smfmac_f32_16x16x64_bf16 v[200:203], v[244:247], v[72:79], %[ix]\n\t" \
    "v_cvt_pk_bf16_f32 v226, v196, v198\n\t" "v_cvt_pk_bf16_f32 v227, v197, v199\n\t" "v_smfmac_f32_16x16x64_bf16 v[204:207], v[244:247], v[88:95], %[ix]\n\t" "s_nop 0\n\t" \
    "v_permlane32_swap_b32 v224, v226\n\t" "v_smfmac_f32_16x16x64_bf16 v[200:203], v[248:251], a[208:215], %[ix]\n\t" "v_permlane32_swap_b32 v225, v227\n\t" "s_nop 0\n\t" \
    "v_smfmac_f32_16x16x64_bf16 v[204:207], v[248:251], a[216:223], %[ix]\n\t" "v_permlane16_swap_b32 v224, v225\n\t" "v_permlane16_swap_b32 v226, v227\n\t" \
    "v_smfmac_f32_16x16x64_bf16 v[200:203], v[252:255], v[176:183], %[ix]\n\t" "ds_read_b128 v[176:179], %[l0] offset:28672\n\t" \
    "buffer_store_dwordx4 v[224:227], %[uo], %[rs], s61 offen sc1\n\t" "s_xor_b32 s61, s61, 0x40000\n\t" XT(94, 95) \
    "v_smfmac_f32_16x16x64_bf16 v[204:207], v[252:255], v[184:191], %[ix]\n\t" "ds_read_b128 v[180:183], %[l0] offset:29696\n\t" \
    "ds_read_b128 v[184:187], %[l0] offset:30720\n\t" "v_smfmac_f32_16x16x64_bf16 v[208:211], v[240:243], v[96:103], %[ix]\n\t" \
    "ds_read_b128 v[188:191], %[l0] offset:31744\n\t" "s_nop 0\n\t" "v_smfmac_f32_16x16x64_bf16 v[212:215], v[240:243], v[112:119], %[ix]\n\t" \
    "v_cvt_pk_bf16_f32 v228, v200, v202\n\t" "v_cvt_pk_bf16_f32 v229, v201, v203\n\t" "v_smfmac_f32_16x16x64_bf16 v[208:211], v[244:247], v[104:111], %[ix]\n\t" \
    "v_cvt_pk_bf16_f32 v230, v204, v206\n\t" "v_cvt_pk_bf16_f32 v231, v205, v207\n\t" "v_smfmac_f32_16x16x64_bf16 v[212:215], v[244:247], v[120:127], %[ix]\n\t" "s_nop 0\n\t" \
    "v_permlane32_swap_b32 v228, v230\n\t" "v_smfmac_f32_16x16x64_bf16 v[208:211], v[248:251], a[224:231], %[ix]\n\t" "v_permlane32_swap_b32 v229, v231\n\t" "s_nop 0\n\t" \
    "v_smfmac_f32_16x16x64_bf16 v[212:215], v[248:251], a[232:239], %[ix]\n\t" "v_permlane16_swap_b32 v228, v229\n\t" "v_permlane16_swap_b32 v230, v231\n\t" \
    "s_waitcnt lgkmcnt(6)\n\t" "v_smfmac_f32_16x16x64_bf16 v[208:211], v[252:255], v[160:167], %[ix]\n\t" "buffer_store_dwordx4 v[228:231], %[uo], %[rs], s61 offen sc1\n\t" \
    "s_xor_b32 s61, s61, 0xc0000\n\t" "s_waitcnt lgkmcnt(4)\n\t" "v_smfmac_f32_16x16x64_bf16 v[212:215], v[252:255], v[168:175], %[ix]\n\t" "s_nop 1\n\t" \
    "v_smfmac_f32_16x16x64_bf16 v[216:219], v[240:243], v[128:135], %[ix]\n\t" "s_nop 1\n\t" "v_smfmac_f32_16x16x64_bf16 v[220:223], v[240:243], v[144:151], %[ix]\n\t" \
    "v_cvt_pk_bf16_f32 v224, v208, v210\n\t" "v_cvt_pk_bf16_f32 v225, v209, v211\n\t" "v_smfmac_f32_16x16x64_bf16 v[216:219], v[244:247], v[136:143], %[ix]\n\t" \
    "v_cvt_pk_bf16_f32 v226, v212, v214\n\t" "v_cvt_pk_bf16_f32 v227, v213, v215\n\t" "v_smfmac_f32_16x16x64_bf16 v[220:223], v[244:247], v[152:159], %[ix]\n\t" "s_nop 0\n\t" \
    "v_permlane32_swap_b32 v224, v226\n\t" "v_smfmac_f32_16x16x64_bf16 v[216:219], v[248:251], a[240:247], %[ix]\n\t" "v_permlane32_swap_b32 v225, v227\n\t" "s_nop 0\n\t" \
    "v_smfmac_f32_16x16x64_bf16 v[220:223], v[248:251], a[248:255], %[ix]\n\t" "v_permlane16_swap_b32 v224, v225\n\t" "v_permlane16_swap_b32 v226, v227\n\t" \
    "s_waitcnt lgkmcnt(2)\n\t" "v_smfmac_f32_16x16x64_bf16 v[216:219], v[252:255], v[176:183], %[ix]\n\t" "buffer_store_dwordx4 v[224:227], %[uo], %[rs], s61 offen sc1\n\t" \
    "s_xor_b32 s61, s61, 0x40000\n\t" "s_waitcnt lgkmcnt(0)\n\t" "v_smfmac_f32_16x16x64_bf16 v[220:223], v[252:255], v[184:191], %[ix]\n\t" "s_nop 5\n\t" \
    "v_cvt_pk_bf16_f32 v228, v216, v218\n\t" "v_cvt_pk_bf16_f32 v229, v217, v219\n\t" "v_cvt_pk_bf16_f32 v230, v220, v222\n\t" "v_cvt_pk_bf16_f32 v231, v221, v223\n\t" \
    "s_nop 0\n\t" "v_permlane32_swap_b32 v228, v230\n\t" "v_permlane32_swap_b32 v229, v231\n\t" "s_nop 1\n\t" "v_permlane16_swap_b32 v228, v229\n\t" \
    "v_permlane16_swap_b32 v230, v231\n\t" "buffer_store_dwordx4 v[228:231], %[uo], %[rs], s61 offen sc1\n\t" "s_nop 1\n\t" XT(96, 97) "s_branch 99f\n\t" "21:\n\t" \
    "s_mov_b64 s[62:63], 0\n\t" "ds_read_b128 v[16:19], %[l1] offset:0\n\t" "ds_read_b128 v[20:23], %[l1] offset:1024\n\t" "ds_read_b128 v[24:27], %[l1] offset:2048\n\t" \
    "ds_read_b128 v[28:31], %[l1] offset:3072\n\t" "s_waitcnt lgkmcnt(3)\n\t" "v_max3_u32 v16, v16, v17, v18\n\t" "v_max_u32 v16, v16, v19\n\t" \
    "v_cmp_eq_u32 vcc, -1, v16\n\t" "ds_read_b128 v[16:19], %[l1] offset:4096\n\t" "s_or_b64 s[62:63], s[62:63], vcc\n\t" "s_waitcnt lgkmcnt(3)\n\t" \
    "v_max3_u32 v20, v20, v21, v22\n\t" "v_max_u32 v20, v20, v23\n\t" "v_cmp_eq_u32 vcc, -1, v20\n\t" "ds_read_b128 v[20:23], %[l1] offset:5120\n\t" \
    "s_or_b64 s[62:63], s[62:63], vcc\n\t" "s_waitcnt lgkmcnt(3)\n\t" "v_max3_u32 v24, v24, v25, v26\n\t" "v_max_u32 v24, v24, v27\n\t" "v_cmp_eq_u32 vcc, -1, v24\n\t" \
    "ds_read_b128 v[24:27], %[l1] offset:6144\n\t" "s_or_b64 s[62:63], s[62:63], vcc\n\t" "s_waitcnt lgkmcnt(3)\n\t" "v_max3_u32 v28, v28, v29, v30\n\t" \
    "v_max_u32 v28, v28, v31\n\t" "v_cmp_eq_u32 vcc, -1, v28\n\t" "ds_read_b128 v[28:31], %[l1] offset:7168\n\t" "s_or_b64 s[62:63], s[62:63], vcc\n\t" \
    "s_waitcnt lgkmcnt(3)\n\t" "v_max3_u32 v16, v16, v17, v18\n\t" "v_max_u32 v16, v16, v19\n\t" "v_cmp_eq_u32 vcc, -1, v16\n\t" \
    "ds_read_b128 v[16:19], %[l1] offset:8192\n\t" "s_or_b64 s[62:63], s[62:63], vcc\n\t" "s_waitcnt lgkmcnt(3)\n\t" "v_max3_u32 v20, v20, v21, v22\n\t" \
    "v_max_u32 v20, v20, v23\n\t" "v_cmp_eq_u32 vcc, -1, v20\n\t" "ds_read_b128 v[20:23], %[l1] offset:9216\n\t" "s_or_b64 s[62:63], s[62:63], vcc\n\t" \
    "s_waitcnt lgkmcnt(3)\n\t" "v_max3_u32 v24, v24, v25, v26\n\t" "v_max_u32 v24, v24, v27\n\t" "v_cmp_eq_u32 vcc, -1, v24\n\t" \
    "ds_read_b128 v[24:27], %[l1] offset:10240\n\t" "s_or_b64 s[62:63], s[62:63], vcc\n\t" "s_waitcnt lgkmcnt(3)\n\t" "v_max3_u32 v28, v28, v29, v30\n\t" \
    "v_max_u32 v28, v28, v31\n\t" "v_cmp_eq_u32 vcc, -1, v28\n\t" "ds_read_b128 v[28:31], %[l1] offset:11264\n\t" "s_or_b64 s[62:63], s[62:63], vcc\n\t" \
    "s_waitcnt lgkmcnt(3)\n\t" "v_max3_u32 v16, v16, v17, v18\n\t" "v_max_u32 v16, v16, v19\n\t" "v_cmp_eq_u32 vcc, -1, v16\n\t" \
    "ds_read_b128 v[16:19], %[l1] offset:12288\n\t" "s_or_b64 s[62:63], s[62:63], vcc\n\t" "s_waitcnt lgkmcnt(3)\n\t" "v_max3_u32 v20, v20, v21, v22\n\t" \
    "v_max_u32 v20, v20, v23\n\t" "v_cmp_eq_u32 vcc, -1, v20\n\t" "ds_read_b128 v[20:23], %[l1] offset:13312\n\t" "s_or_b64 s[62:63], s[62:63], vcc\n\t" \
    "s_waitcnt lgkmcnt(3)\n\t" "v_max3_u32 v24, v24, v25, v26\n\t" "v_max_u32 v24, v24, v27\n\t" "v_cmp_eq_u32 vcc, -1, v24\n\t" \
    "ds_read_b128 v[24:27], %[l1] offset:14336\n\t" "s_or_b64 s[62:63], s[62:63], vcc\n\t" "s_waitcnt lgkmcnt(3)\n\t" "v_max3_u32 v28, v28, v29, v30\n\t" \
    "v_max_u32 v28, v28, v31\n\t" "v_cmp_eq_u32 vcc, -1, v28\n\t" "ds_read_b128 v[28:31], %[l1] offset:15360\n\t" "s_or_b64 s[62:63], s[62:63], vcc\n\t" \
    "s_waitcnt lgkmcnt(3)\n\t" "v_max3_u32 v16, v16, v17, v18\n\t" "v_max_u32 v16, v16, v19\n\t" "v_cmp_eq_u32 vcc, -1, v16\n\t" "s_or_b64 s[62:63], s[62:63], vcc\n\t" \
    "s_waitcnt lgkmcnt(2)\n\t" "v_max3_u32 v20, v20, v21, v22\n\t" "v_max_u32 v20, v20, v23\n\t" "v_cmp_eq_u32 vcc, -1, v20\n\t" "s_or_b64 s[62:63], s[62:63], vcc\n\t" \
    "s_waitcnt lgkmcnt(1)\n\t" "v_max3_u32 v24, v24, v25, v26\n\t" "v_max_u32 v24, v24, v27\n\t" "v_cmp_eq_u32 vcc, -1, v24\n\t" "s_or_b64 s[62:63], s[62:63], vcc\n\t" \
    "s_waitcnt lgkmcnt(0)\n\t" "v_max3_u32 v28, v28, v29, v30\n\t" "v_max_u32 v28, v28, v31\n\t" "v_cmp_eq_u32 vcc, -1, v28\n\t" "s_or_b64 s[62:63], s[62:63], vcc\n\t" \
    "s_cmp_eq_u64 s[62:63], 0\n\t" "s_cbranch_scc1 22b\n\t" "s_cmp_eq_u32 %[rt], 0\n\t" "s_cbranch_scc1 22b\n\t" "s_sub_u32 %[rt], %[rt], 1\n\t" XCNT(100) "24:\n\t" \
    "buffer_load_dwordx4 v[240:243], %[mo], %[rs], %[sb] offen sc1\n\t" "buffer_load_dwordx4 v[244:247], v224, %[rs], %[sb] offen sc1\n\t" \
    "buffer_load_dwordx4 v[248:251], %[mo], %[rs], s60 offen sc1\n\t" "buffer_load_dwordx4 v[252:255], v224, %[rs], s60 offen sc1\n\t" "s_waitcnt vmcnt(0)\n\t" \
    "v_max_u32 v28, v240, v241\n\t" "v_max3_u32 v28, v28, v242, v243\n\t" "v_max3_u32 v28, v28, v244, v245\n\t" "v_max3_u32 v28, v28, v246, v247\n\t" \
    "v_max3_u32 v28, v28, v248, v249\n\t" "v_max3_u32 v28, v28, v250, v251\n\t" "v_max3_u32 v28, v28, v252, v253\n\t" "v_max3_u32 v28, v28, v254, v255\n\t" \
    "v_cmp_eq_u32 vcc, -1, v28\n\t" "s_cbranch_vccz 25f\n\t" "s_cmp_eq_u32 %[bd], 0\n\t" "s_cbranch_scc1 25f\n\t" "s_sub_u32 %[bd], %[bd], 1\n\t" XCNT(101) "s_sleep 1\n\t" \
    "s_branch 24b\n\t" "25:\n\t" "s_barrier\n\t" "s_branch 20b\n\t" "92:\n\t" XT(80, 81) "s_barrier\n\t" XT(82, 83) "30:\n\t" \
    "buffer_load_dwordx4 v[240:243], %[mo], %[rs], %[sb] offen sc1\n\t" "v_mov_b64 v[232:233], 0\n\t" "v_mov_b64 v[234:235], 0\n\t" "v_mov_b64 v[236:237], 0\n\t" \
    "v_mov_b64 v[238:239], 0\n\t" "v_mov_b64 v[192:193], 0\n\t" "v_mov_b64 v[194:195], 0\n\t" "v_mov_b64 v[196:197], 0\n\t" "v_mov_b64 v[198:199], 0\n\t" \
    "buffer_load_dwordx4 v[244:247], v224, %[rs], %[sb] offen sc1\n\t" "ds_read_b128 v[160:163], %[l0] offset:0\n\t" "ds_read_b128 v[164:167], %[l0] offset:1024\n\t" \
    "ds_read_b128 v[168:171], %[l0] offset:2048\n\t" "ds_read_b128 v[172:175], %[l0] offset:3072\n\t" "s_nop 3\n\t" \
    "buffer_load_dwordx4 v[248:251], %[mo], %[rs], s60 offen sc1\n\t" "ds_read_b128 v[176:179], %[l0] offset:4096\n\t" "ds_read_b128 v[180:183], %[l0] offset:5120\n\t" \
    "ds_read_b128 v[184:187], %[l0] offset:6144\n\t" "ds_read_b128 v[188:191], %[l0] offset:7168\n\t" "s_nop 3\n\t" \
    "buffer_load_dwordx4 v[252:255], v224, %[rs], s60 offen sc1\n\t" "v_mov_b64 v[200:201], 0\n\t" "v_mov_b64 v[202:203], 0\n\t" "v_mov_b64 v[204:205], 0\n\t" \
    "v_mov_b64 v[206:207], 0\n\t" "v_mov_b64 v[208:209], 0\n\t" "v_mov_b64 v[210:211], 0\n\t" "v_mov_b64 v[212:213], 0\n\t" "v_mov_b64 v[214:215], 0\n\t" \
    "v_mov_b64 v[216:217], 0\n\t" "v_mov_b64 v[218:219], 0\n\t" "v_mov_b64 v[220:221], 0\n\t" "v_mov_b64 v[222:223], 0\n\t" "s_waitcnt vmcnt(3)\n\t" XT(84, 85) \
    "v_smfmac_f32_16x16x64_bf16 v[232:235], v[240:243], a[0:7], %[ix]\n\t" "ds_write_b128 %[l1], v[240:243] offset:8192\n\t" \
    "v_smfmac_f32_16x16x64_bf16 v[236:239], v[240:243], a[8:15], %[ix]\n\t" "s_waitcnt vmcnt(2)\n\t" "v_smfmac_f32_16x16x64_bf16 v[232:235], v[244:247], a[16:23], %[ix]\n\t" \
    "ds_write_b128 %[l1], v[244:247] offset:9216\n\t" "s_waitcnt lgkmcnt(8)\n\t" "v_smfmac_f32_16x16x64_bf16 v[236:239], v[244:247], v[160:167], %[ix]\n\t" \
    "s_waitcnt lgkmcnt(0)\n\t" "s_barrier\n\t" XT(86, 87) "ds_read_b128 v[16:19], %[l1] offset:12288\n\t" "ds_read_b128 v[20:23], %[l1] offset:0\n\t" \
    "ds_read_b128 v[24:27], %[l1] offset:4096\n\t" "s_waitcnt vmcnt(1)\n\t" "v_smfmac_f32_16x16x64_bf16 v[232:235], v[248:251], a[24:31], %[ix]\n\t" \
    "ds_write_b128 %[l1], v[248:251] offset:10240\n\t" "v_smfmac_f32_16x16x64_bf16 v[236:239], v[248:251], a[32:39], %[ix]\n\t" \
    "ds_read_b128 v[28:31], %[l1] offset:13312\n\t" "ds_read_b128 v[240:243], %[l1] offset:1024\n\t" "ds_read_b128 v[244:247], %[l1] offset:5120\n\t" "s_waitcnt vmcnt(0)\n\t" \
    "v_smfmac_f32_16x16x64_bf16 v[232:235], v[252:255], a[40:47], %[ix]\n\t" "ds_write_b128 %[l1], v[252:255] offset:11264\n\t" \
    "v_smfmac_f32_16x16x64_bf16 v[236:239], v[252:255], v[168:175], %[ix]\n\t" "ds_read_b128 v[160:163], %[l0] offset:8192\n\t" \
    "ds_read_b128 v[164:167], %[l0] offset:9216\n\t" "s_waitcnt lgkmcnt(9)\n\t" "v_smfmac_f32_16x16x64_bf16 v[232:235], v[16:19], a[48:55], %[ix]\n\t" \
    "ds_read_b128 v[168:171], %[l0] offset:10240\n\t" "ds_read_b128 v[172:175], %[l0] offset:11264\n\t" "v_smfmac_f32_16x16x64_bf16 v[236:239], v[16:19], a[56:63], %[ix]\n\t" \
    "s_waitcnt lgkmcnt(10)\n\t" "v_smfmac_f32_16x16x64_bf16 v[232:235], v[20:23], a[64:71], %[ix]\n\t" \
    "v_smfmac_f32_16x16x64_bf16 v[236:239], v[20:23], v[176:183], %[ix]\n\t" "s_waitcnt lgkmcnt(4)\n\t" "s_barrier\n\t" XT(88, 89) \
    "v_smfmac_f32_16x16x64_bf16 v[232:235], v[24:27], a[72:79], %[ix]\n\t" "ds_read_b128 v[176:179], %[l0] offset:12288\n\t" "ds_read_b128 v[180:183], %[l0] offset:13312\n\t" \
    "v_smfmac_f32_16x16x64_bf16 v[236:239], v[24:27], a[80:87], %[ix]\n\t" "ds_read_b128 v[248:251], %[l1] offset:14336\n\t" "ds_read_b128 v[252:255], %[l1] offset:2048\n\t" \
    "v_smfmac_f32_16x16x64_bf16 v[232:235], v[28:31], a[88:95], %[ix]\n\t" "ds_read_b128 v[16:19], %[l1] offset:6144\n\t" "ds_read_b128 v[20:23], %[l1] offset:15360\n\t" \
    "v_smfmac_f32_16x16x64_bf16 v[236:239], v[28:31], v[184:191], %[ix]\n\t" "ds_read_b128 v[24:27], %[l1] offset:3072\n\t" "ds_read_b128 v[184:187], %[l0] offset:14336\n\t" \
    "v_smfmac_f32_16x16x64_bf16 v[232:235], v[240:243], a[96:103], %[ix]\n\t" "ds_read_b128 v[188:191], %[l0] offset:15360\n\t" "ds_read_b128 v[28:31], %[l1] offset:7168\n\t" \
    "v_smfmac_f32_16x16x64_bf16 v[236:239], v[240:243], a[104:111], %[ix]\n\t" "v_smfmac_f32_16x16x64_bf16 v[232:235], v[244:247], a[112:119], %[ix]\n\t" \
    "s_waitcnt lgkmcnt(12)\n\t" "v_smfmac_f32_16x16x64_bf16 v[236:239], v[244:247], v[160:167], %[ix]\n\t" "ds_read_b128 v[160:163], %[l0] offset:16384\n\t" \
    "ds_read_b128 v[164:167], %[l0] offset:17408\n\t" "s_waitcnt lgkmcnt(9)\n\t" "v_smfmac_f32_16x16x64_bf16 v[232:235], v[248:251], a[120:127], %[ix]\n\t" \
    "v_smfmac_f32_16x16x64_bf16 v[236:239], v[248:251], a[128:135], %[ix]\n\t" "s_waitcnt lgkmcnt(8)\n\t" \
    "v_smfmac_f32_16x16x64_bf16 v[232:235], v[252:255], a[136:143], %[ix]\n\t" "v_smfmac_f32_16x16x64_bf16 v[236:239], v[252:255], v[168:175], %[ix]\n\t" \
    "ds_read_b128 v[168:171], %[l0] offset:18432\n\t" "ds_read_b128 v[172:175], %[l0] offset:19456\n\t" "s_waitcnt lgkmcnt(9)\n\t" \
    "v_smfmac_f32_16x16x64_bf16 v[232:235], v[16:19], a[144:151], %[ix]\n\t" "v_smfmac_f32_16x16x64_bf16 v[236:239], v[16:19], a[152:159], %[ix]\n\t" \
    "s_waitcnt lgkmcnt(8)\n\t" "v_smfmac_f32_16x16x64_bf16 v[232:235], v[20:23], a[160:167], %[ix]\n\t" \
    "v_smfmac_f32_16x16x64_bf16 v[236:239], v[20:23], v[176:183], %[ix]\n\t" "ds_read_b128 v[176:179], %[l0] offset:20480\n\t" \
    "ds_read_b128 v[180:183], %[l0] offset:21504\n\t" "s_waitcnt lgkmcnt(9)\n\t" "v_smfmac_f32_16x16x64_bf16 v[232:235], v[24:27], a[168:175], %[ix]\n\t" \
    "v_smfmac_f32_16x16x64_bf16 v[236:239], v[24:27], a[176:183], %[ix]\n\t" "s_waitcnt lgkmcnt(6)\n\t" \
    "v_smfmac_f32_16x16x64_bf16 v[232:235], v[28:31], a[184:191], %[ix]\n\t" "v_smfmac_f32_16x16x64_bf16 v[236:239], v[28:31], v[184:191], %[ix]\n\t" \
    "ds_read_b128 v[184:187], %[l0] offset:22528\n\t" "ds_read_b128 v[188:191], %[l0] offset:23552\n\t" XT(90, 91) "s_nop 4\n\t" "v_add_f32 v232, v232, v233\n\t" \
    "v_add_f32 v234, v234, v235\n\t" "v_add_f32 v236, v236, v237\n\t" "v_add_f32 v238, v238, v239\n\t" "v_cvt_pk_bf16_f32 v236, v232, v236\n\t" \
    "v_cvt_pk_bf16_f32 v237, v234, v238\n\t" "ds_write2_b32 %[vw], v236, v237 offset1:68\n\t" "v_cmp_u_f32 vcc, v232, v234\n\t" "s_cbranch_vccnz 31f\n\t" "32:\n\t" \
    "s_waitcnt lgkmcnt(0)\n\t" "s_barrier\n\t" XT(92, 93) "ds_read_b128 v[240:243], %[vr] offset:0\n\t" "ds_read_b128 v[244:247], %[vr] offset:64\n\t" \
    "ds_read_b128 v[248:251], %[vr] offset:128\n\t" "ds_read_b128 v[252:255], %[vr] offset:192\n\t" "s_mov_b32 s61, %[ub]\n\t" "s_waitcnt lgkmcnt(3)\n\t" \
    "v_smfmac_f32_16x16x64_bf16 v[192:195], v[240:243], v[32:39], %[ix]\n\t" "v_smfmac_f32_16x16x64_bf16 v[196:199], v[240:243], v[48:55], %[ix]\n\t" \
    "s_waitcnt lgkmcnt(2)\n\t" "v_smfmac_f32_16x16x64_bf16 v[192:195], v[244:247], v[40:47], %[ix]\n\t" \
    "v_smfmac_f32_16x16x64_bf16 v[196:199], v[244:247], v[56:63], %[ix]\n\t" "s_waitcnt lgkmcnt(1)\n\t" \
    "v_smfmac_f32_16x16x64_bf16 v[192:195], v[248:251], a[192:199], %[ix]\n\t" "v_smfmac_f32_16x16x64_bf16 v[196:199], v[248:251], a[200:207], %[ix]\n\t" \
    "s_waitcnt lgkmcnt(0)\n\t" "v_smfmac_f32_16x16x64_bf16 v[192:195], v[252:255], v[160:167], %[ix]\n\t" "ds_read_b128 v[160:163], %[l0] offset:24576\n\t" \
    "ds_read_b128 v[164:167], %[l0] offset:25600\n\t" "v_smfmac_f32_16x16x64_bf16 v[196:199], v[252:255], v[168:175], %[ix]\n\t" \
    "ds_read_b128 v[168:171], %[l0] offset:26624\n\t" "ds_read_b128 v[172:175], %[l0] offset:27648\n\t" \
    "v_smfmac_f32_16x16x64_bf16 v[200:203], v[240:243], v[64:71], %[ix]\n\t" "s_nop 1\n\t" "v_smfmac_f32_16x16x64_bf16 v[204:207], v[240:243], v[80:87], %[ix]\n\t" \
    "v_cvt_pk_bf16_f32 v224, v192, v194\n\t" "v_cvt_pk_bf16_f32 v225, v193, v195\n\t" "v_smfmac_f32_16x16x64_bf16 v[200:203], v[244:247], v[72:79], %[ix]\n\t" \
    "v_cvt_pk_bf16_f32 v226, v196, v198\n\t" "v_cvt_pk_bf16_f32 v227, v197, v199\n\t" "v_smfmac_f32_16x16x64_bf16 v[204:207], v[244:247], v[88:95], %[ix]\n\t" "s_nop 0\n\t" \
    "v_permlane32_swap_b32 v224, v226\n\t" "v_smfmac_f32_16x16x64_bf16 v[200:203], v[248:251], a[208:215], %[ix]\n\t" "v_permlane32_swap_b32 v225, v227\n\t" "s_nop 0\n\t" \
    "v_smfmac_f32_16x16x64_bf16 v[204:207], v[248:251], a[216:223], %[ix]\n\t" "v_permlane16_swap_b32 v224, v225\n\t" "v_permlane16_swap_b32 v226, v227\n\t" \
    "v_smfmac_f32_16x16x64_bf16 v[200:203], v[252:255], v[176:183], %[ix]\n\t" "ds_read_b128 v[176:179], %[l0] offset:28672\n\t" \
    "buffer_store_dwordx4 v[224:227], %[uo], %[rs], s61 offen sc1\n\t" "s_xor_b32 s61, s61, 0x40000\n\t" XT(94, 95) \
    "v_smfmac_f32_16x16x64_bf16 v[204:207], v[252:255], v[184:191], %[ix]\n\t" "ds_read_b128 v[180:183], %[l0] offset:29696\n\t" \
    "ds_read_b128 v[184:187], %[l0] offset:30720\n\t" "v_smfmac_f32_16x16x64_bf16 v[208:211], v[240:243], v[96:103], %[ix]\n\t" \
    "ds_read_b128 v[188:191], %[l0] offset:31744\n\t" "s_nop 0\n\t" "v_smfmac_f32_16x16x64_bf16 v[212:215], v[240:243], v[112:119], %[ix]\n\t" \
    "v_cvt_pk_bf16_f32 v228, v200, v202\n\t" "v_cvt_pk_bf16_f32 v229, v201, v203\n\t" "v_smfmac_f32_16x16x64_bf16 v[208:211], v[244:247], v[104:111], %[ix]\n\t" \
    "v_cvt_pk_bf16_f32 v230, v204, v206\n\t" "v_cvt_pk_bf16_f32 v231, v205, v207\n\t" "v_smfmac_f32_16x16x64_bf16 v[212:215], v[244:247], v[120:127], %[ix]\n\t" "s_nop 0\n\t" \
    "v_permlane32_swap_b32 v228, v230\n\t" "v_smfmac_f32_16x16x64_bf16 v[208:211], v[248:251], a[224:231], %[ix]\n\t" "v_permlane32_swap_b32 v229, v231\n\t" "s_nop 0\n\t" \
    "v_smfmac_f32_16x16x64_bf16 v[212:215], v[248:251], a[232:239], %[ix]\n\t" "v_permlane16_swap_b32 v228, v229\n\t" "v_permlane16_swap_b32 v230, v231\n\t" \
    "s_waitcnt lgkmcnt(6)\n\t" "v_smfmac_f32_16x16x64_bf16 v[208:211], v[252:255], v[160:167], %[ix]\n\t" "buffer_store_dwordx4 v[228:231], %[uo], %[rs], s61 offen sc1\n\t" \
    "s_xor_b32 s61, s61, 0xc0000\n\t" "s_waitcnt lgkmcnt(4)\n\t" "v_smfmac_f32_16x16x64_bf16 v[212:215], v[252:255], v[168:175], %[ix]\n\t" "s_nop 1\n\t" \
    "v_smfmac_f32_16x16x64_bf16 v[216:219], v[240:243], v[128:135], %[ix]\n\t" "s_nop 1\n\t" "v_smfmac_f32_16x16x64_bf16 v[220:223], v[240:243], v[144:151], %[ix]\n\t" \
    "v_cvt_pk_bf16_f32 v224, v208, v210\n\t" "v_cvt_pk_bf16_f32 v225, v209, v211\n\t" "v_smfmac_f32_16x16x64_bf16 v[216:219], v[244:247], v[136:143], %[ix]\n\t" \
    "v_cvt_pk_bf16_f32 v226, v212, v214\n\t" "v_cvt_pk_bf16_f32 v227, v213, v215\n\t" "v_smfmac_f32_16x16x64_bf16 v[220:223], v[244:247], v[152:159], %[ix]\n\t" "s_nop 0\n\t" \
    "v_permlane32_swap_b32 v224, v226\n\t" "v_smfmac_f32_16x16x64_bf16 v[216:219], v[248:251], a[240:247], %[ix]\n\t" "v_permlane32_swap_b32 v225, v227\n\t" "s_nop 0\n\t" \
    "v_smfmac_f32_16x16x64_bf16 v[220:223], v[248:251], a[248:255], %[ix]\n\t" "v_permlane16_swap_b32 v224, v225\n\t" "v_permlane16_swap_b32 v226, v227\n\t" \
    "s_waitcnt lgkmcnt(2)\n\t" "v_smfmac_f32_16x16x64_bf16 v[216:219], v[252:255], v[176:183], %[ix]\n\t" "buffer_store_dwordx4 v[224:227], %[uo], %[rs], s61 offen sc1\n\t" \
    "s_xor_b32 s61, s61, 0x40000\n\t" "s_waitcnt lgkmcnt(0)\n\t" "v_smfmac_f32_16x16x64_bf16 v[220:223], v[252:255], v[184:191], %[ix]\n\t" "s_nop 5\n\t" \
    "v_cvt_pk_bf16_f32 v228, v216, v218\n\t" "v_cvt_pk_bf16_f32 v229, v217, v219\n\t" "v_cvt_pk_bf16_f32 v230, v220, v222\n\t" "v_cvt_pk_bf16_f32 v231, v221, v223\n\t" \
    "s_nop 0\n\t" "v_permlane32_swap_b32 v228, v230\n\t" "v_permlane32_swap_b32 v229, v231\n\t" "s_nop 1\n\t" "v_permlane16_swap_b32 v228, v229\n\t" \
    "v_permlane16_swap_b32 v230, v231\n\t" "buffer_store_dwordx4 v[228:231], %[uo], %[rs], s61 offen sc1\n\t" "s_nop 1\n\t" XT(96, 97) "s_branch 99f\n\t" "31:\n\t" \
    "s_mov_b64 s[62:63], 0\n\t" "ds_read_b128 v[16:19], %[l1] offset:0\n\t" "ds_read_b128 v[20:23], %[l1] offset:1024\n\t" "ds_read_b128 v[24:27], %[l1] offset:2048\n\t" \
    "ds_read_b128 v[28:31], %[l1] offset:3072\n\t" "s_waitcnt lgkmcnt(3)\n\t" "v_max3_u32 v16, v16, v17, v18\n\t" "v_max_u32 v16, v16, v19\n\t" \
    "v_cmp_eq_u32 vcc, -1, v16\n\t" "ds_read_b128 v[16:19], %[l1] offset:4096\n\t" "s_or_b64 s[62:63], s[62:63], vcc\n\t" "s_waitcnt lgkmcnt(3)\n\t" \
    "v_max3_u32 v20, v20, v21, v22\n\t" "v_max_u32 v20, v20, v23\n\t" "v_cmp_eq_u32 vcc, -1, v20\n\t" "ds_read_b128 v[20:23], %[l1] offset:5120\n\t" \
    "s_or_b64 s[62:63], s[62:63], vcc\n\t" "s_waitcnt lgkmcnt(3)\n\t" "v_max3_u32 v24, v24, v25, v26\n\t" "v_max_u32 v24, v24, v27\n\t" "v_cmp_eq_u32 vcc, -1, v24\n\t" \
    "ds_read_b128 v[24:27], %[l1] offset:6144\n\t" "s_or_b64 s[62:63], s[62:63], vcc\n\t" "s_waitcnt lgkmcnt(3)\n\t" "v_max3_u32 v28, v28, v29, v30\n\t" \
    "v_max_u32 v28, v28, v31\n\t" "v_cmp_eq_u32 vcc, -1, v28\n\t" "ds_read_b128 v[28:31], %[l1] offset:7168\n\t" "s_or_b64 s[62:63], s[62:63], vcc\n\t" \
    "s_waitcnt lgkmcnt(3)\n\t" "v_max3_u32 v16, v16, v17, v18\n\t" "v_max_u32 v16, v16, v19\n\t" "v_cmp_eq_u32 vcc, -1, v16\n\t" \
    "ds_read_b128 v[16:19], %[l1] offset:8192\n\t" "s_or_b64 s[62:63], s[62:63], vcc\n\t" "s_waitcnt lgkmcnt(3)\n\t" "v_max3_u32 v20, v20, v21, v22\n\t" \
    "v_max_u32 v20, v20, v23\n\t" "v_cmp_eq_u32 vcc, -1, v20\n\t" "ds_read_b128 v[20:23], %[l1] offset:9216\n\t" "s_or_b64 s[62:63], s[62:63], vcc\n\t" \
    "s_waitcnt lgkmcnt(3)\n\t" "v_max3_u32 v24, v24, v25, v26\n\t" "v_max_u32 v24, v24, v27\n\t" "v_cmp_eq_u32 vcc, -1, v24\n\t" \
    "ds_read_b128 v[24:27], %[l1] offset:10240\n\t" "s_or_b64 s[62:63], s[62:63], vcc\n\t" "s_waitcnt lgkmcnt(3)\n\t" "v_max3_u32 v28, v28, v29, v30\n\t" \
    "v_max_u32 v28, v28, v31\n\t" "v_cmp_eq_u32 vcc, -1, v28\n\t" "ds_read_b128 v[28:31], %[l1] offset:11264\n\t" "s_or_b64 s[62:63], s[62:63], vcc\n\t" \
    "s_waitcnt lgkmcnt(3)\n\t" "v_max3_u32 v16, v16, v17, v18\n\t" "v_max_u32 v16, v16, v19\n\t" "v_cmp_eq_u32 vcc, -1, v16\n\t" \
    "ds_read_b128 v[16:19], %[l1] offset:12288\n\t" "s_or_b64 s[62:63], s[62:63], vcc\n\t" "s_waitcnt lgkmcnt(3)\n\t" "v_max3_u32 v20, v20, v21, v22\n\t" \
    "v_max_u32 v20, v20, v23\n\t" "v_cmp_eq_u32 vcc, -1, v20\n\t" "ds_read_b128 v[20:23], %[l1] offset:13312\n\t" "s_or_b64 s[62:63], s[62:63], vcc\n\t" \
    "s_waitcnt lgkmcnt(3)\n\t" "v_max3_u32 v24, v24, v25, v26\n\t" "v_max_u32 v24, v24, v27\n\t" "v_cmp_eq_u32 vcc, -1, v24\n\t" \
    "ds_read_b128 v[24:27], %[l1] offset:14336\n\t" "s_or_b64 s[62:63], s[62:63], vcc\n\t" "s_waitcnt lgkmcnt(3)\n\t" "v_max3_u32 v28, v28, v29, v30\n\t" \
    "v_max_u32 v28, v28, v31\n\t" "v_cmp_eq_u32 vcc, -1, v28\n\t" "ds_read_b128 v[28:31], %[l1] offset:15360\n\t" "s_or_b64 s[62:63], s[62:63], vcc\n\t" \
    "s_waitcnt lgkmcnt(3)\n\t" "v_max3_u32 v16, v16, v17, v18\n\t" "v_max_u32 v16, v16, v19\n\t" "v_cmp_eq_u32 vcc, -1, v16\n\t" "s_or_b64 s[62:63], s[62:63], vcc\n\t" \
    "s_waitcnt lgkmcnt(2)\n\t" "v_max3_u32 v20, v20, v21, v22\n\t" "v_max_u32 v20, v20, v23\n\t" "v_cmp_eq_u32 vcc, -1, v20\n\t" "s_or_b64 s[62:63], s[62:63], vcc\n\t" \
    "s_waitcnt lgkmcnt(1)\n\t" "v_max3_u32 v24, v24, v25, v26\n\t" "v_max_u32 v24, v24, v27\n\t" "v_cmp_eq_u32 vcc, -1, v24\n\t" "s_or_b64 s[62:63], s[62:63], vcc\n\t" \
    "s_waitcnt lgkmcnt(0)\n\t" "v_max3_u32 v28, v28, v29, v30\n\t" "v_max_u32 v28, v28, v31\n\t" "v_cmp_eq_u32 vcc, -1, v28\n\t" "s_or_b64 s[62:63], s[62:63], vcc\n\t" \
    "s_cmp_eq_u64 s[62:63], 0\n\t" "s_cbranch_scc1 32b\n\t" "s_cmp_eq_u32 %[rt], 0\n\t" "s_cbranch_scc1 32b\n\t" "s_sub_u32 %[rt], %[rt], 1\n\t" XCNT(100) "34:\n\t" \
    "buffer_load_dwordx4 v[240:243], %[mo], %[rs], %[sb] offen sc1\n\t" "buffer_load_dwordx4 v[244:247], v224, %[rs], %[sb] offen sc1\n\t" \
    "buffer_load_dwordx4 v[248:251], %[mo], %[rs], s60 offen sc1\n\t" "buffer_load_dwordx4 v[252:255], v224, %[rs], s60 offen sc1\n\t" "s_waitcnt vmcnt(0)\n\t" \
    "v_max_u32 v28, v240, v241\n\t" "v_max3_u32 v28, v28, v242, v243\n\t" "v_max3_u32 v28, v28, v244, v245\n\t" "v_max3_u32 v28, v28, v246, v247\n\t" \
    "v_max3_u32 v28, v28, v248, v249\n\t" "v_max3_u32 v28, v28, v250, v251\n\t" "v_max3_u32 v28, v28, v252, v253\n\t" "v_max3_u32 v28, v28, v254, v255\n\t" \
    "v_cmp_eq_u32 vcc, -1, v28\n\t" "s_cbranch_vccz 35f\n\t" "s_cmp_eq_u32 %[bd], 0\n\t" "s_cbranch_scc1 35f\n\t" "s_sub_u32 %[bd], %[bd], 1\n\t" XCNT(101) "s_sleep 1\n\t" \
    "s_branch 34b\n\t" "35:\n\t" "s_barrier\n\t" "s_branch 30b\n\t" "93:\n\t" XT(80, 81) "s_barrier\n\t" XT(82, 83) "40:\n\t" \
    "buffer_load_dwordx4 v[240:243], %[mo], %[rs], %[sb] offen sc1\n\t" "v_mov_b64 v[232:233], 0\n\t" "v_mov_b64 v[234:235], 0\n\t" "v_mov_b64 v[236:237], 0\n\t" \
    "v_mov_b64 v[238:239], 0\n\t" "v_mov_b64 v[192:193], 0\n\t" "v_mov_b64 v[194:195], 0\n\t" "v_mov_b64 v[196:197], 0\n\t" "v_mov_b64 v[198:199], 0\n\t" \
    "buffer_load_dwordx4 v[244:247], v224, %[rs], %[sb] offen sc1\n\t" "ds_read_b128 v[160:163], %[l0] offset:0\n\t" "ds_read_b128 v[164:167], %[l0] offset:1024\n\t" \
    "ds_read_b128 v[168:171], %[l0] offset:2048\n\t" "ds_read_b128 v[172:175], %[l0] offset:3072\n\t" "s_nop 3\n\t" \
    "buffer_load_dwordx4 v[248:251], %[mo], %[rs], s60 offen sc1\n\t" "ds_read_b128 v[176:179], %[l0] offset:4096\n\t" "ds_read_b128 v[180:183], %[l0] offset:5120\n\t" \
    "ds_read_b128 v[184:187], %[l0] offset:6144\n\t" "ds_read_b128 v[188:191], %[l0] offset:7168\n\t" "s_nop 3\n\t" \
    "buffer_load_dwordx4 v[252:255], v224, %[rs], s60 offen sc1\n\t" "v_mov_b64 v[200:201], 0\n\t" "v_mov_b64 v[202:203], 0\n\t" "v_mov_b64 v[204:205], 0\n\t" \
    "v_mov_b64 v[206:207], 0\n\t" "v_mov_b64 v[208:209], 0\n\t" "v_mov_b64 v[210:211], 0\n\t" "v_mov_b64 v[212:213], 0\n\t" "v_mov_b64 v[214:215], 0\n\t" \
    "v_mov_b64 v[216:217], 0\n\t" "v_mov_b64 v[218:219], 0\n\t" "v_mov_b64 v[220:221], 0\n\t" "v_mov_b64 v[222:223], 0\n\t" "s_waitcnt vmcnt(3)\n\t" XT(84, 85) \
    "v_smfmac_f32_16x16x64_bf16 v[232:235], v[240:243], a[0:7], %[ix]\n\t" "ds_write_b128 %[l1], v[240:243] offset:12288\n\t" \
    "v_smfmac_f32_16x16x64_bf16 v[236:239], v[240:243], a[8:15], %[ix]\n\t" "s_waitcnt vmcnt(2)\n\t" "v_smfmac_f32_16x16x64_bf16 v[232:235], v[244:247], a[16:23], %[ix]\n\t" \
    "ds_write_b128 %[l1], v[244:247] offset:13312\n\t" "s_waitcnt lgkmcnt(8)\n\t" "v_smfmac_f32_16x16x64_bf16 v[236:239], v[244:247], v[160:167], %[ix]\n\t" \
    "s_waitcnt lgkmcnt(0)\n\t" "s_barrier\n\t" XT(86, 87) "ds_read_b128 v[16:19], %[l1] offset:0\n\t" "ds_read_b128 v[20:23], %[l1] offset:4096\n\t" \
    "ds_read_b128 v[24:27], %[l1] offset:8192\n\t" "s_waitcnt vmcnt(1)\n\t" "v_smfmac_f32_16x16x64_bf16 v[232:235], v[248:251], a[24:31], %[ix]\n\t" \
    "ds_write_b128 %[l1], v[248:251] offset:14336\n\t" "v_smfmac_f32_16x16x64_bf16 v[236:239], v[248:251], a[32:39], %[ix]\n\t" "ds_read_b128 v[28:31], %[l1] offset:1024\n\t" \
    "ds_read_b128 v[240:243], %[l1] offset:5120\n\t" "ds_read_b128 v[244:247], %[l1] offset:9216\n\t" "s_waitcnt vmcnt(0)\n\t" \
    "v_smfmac_f32_16x16x64_bf16 v[232:235], v[252:255], a[40:47], %[ix]\n\t" "ds_write_b128 %[l1], v[252:255] offset:15360\n\t" \
    "v_smfmac_f32_16x16x64_bf16 v[236:239], v[252:255], v[168:175], %[ix]\n\t" "ds_read_b128 v[160:163], %[l0] offset:8192\n\t" \
    "ds_read_b128 v[164:167], %[l0] offset:9216\n\t" "s_waitcnt lgkmcnt(9)\n\t" "v_smfmac_f32_16x16x64_bf16 v[232:235], v[16:19], a[48:55], %[ix]\n\t" \
    "ds_read_b128 v[168:171], %[l0] offset:10240\n\t" "ds_read_b128 v[172:175], %[l0] offset:11264\n\t" "v_smfmac_f32_16x16x64_bf16 v[236:239], v[16:19], a[56:63], %[ix]\n\t" \
    "s_waitcnt lgkmcnt(10)\n\t" "v_smfmac_f32_16x16x64_bf16 v[232:235], v[20:23], a[64:71], %[ix]\n\t" \
    "v_smfmac_f32_16x16x64_bf16 v[236:239], v[20:23], v[176:183], %[ix]\n\t" "s_waitcnt lgkmcnt(4)\n\t" "s_barrier\n\t" XT(88, 89) \
    "v_smfmac_f32_16x16x64_bf16 v[232:235], v[24:27], a[72:79], %[ix]\n\t" "ds_read_b128 v[176:179], %[l0] offset:12288\n\t" "ds_read_b128 v[180:183], %[l0] offset:13312\n\t" \
    "v_smfmac_f32_16x16x64_bf16 v[236:239], v[24:27], a[80:87], %[ix]\n\t" "ds_read_b128 v[248:251], %[l1] offset:2048\n\t" "ds_read_b128 v[252:255], %[l1] offset:6144\n\t" \
    "v_smfmac_f32_16x16x64_bf16 v[232:235], v[28:31], a[88:95], %[ix]\n\t" "ds_read_b128 v[16:19], %[l1] offset:10240\n\t" "ds_read_b128 v[20:23], %[l1] offset:3072\n\t" \
    "v_smfmac_f32_16x16x64_bf16 v[236:239], v[28:31], v[184:191], %[ix]\n\t" "ds_read_b128 v[24:27], %[l1] offset:7168\n\t" "ds_read_b128 v[184:187], %[l0] offset:14336\n\t" \
    "v_smfmac_f32_16x16x64_bf16 v[232:235], v[240:243], a[96:103], %[ix]\n\t" "ds_read_b128 v[188:191], %[l0] offset:15360\n\t" \
    "ds_read_b128 v[28:31], %[l1] offset:11264\n\t" "v_smfmac_f32_16x16x64_bf16 v[236:239], v[240:243], a[104:111], %[ix]\n\t" \
    "v_smfmac_f32_16x16x64_bf16 v[232:235], v[244:247], a[112:119], %[ix]\n\t" "s_waitcnt lgkmcnt(12)\n\t" \
    "v_smfmac_f32_16x16x64_bf16 v[236:239], v[244:247], v[160:167], %[ix]\n\t" "ds_read_b128 v[160:163], %[l0] offset:16384\n\t" \
    "ds_read_b128 v[164:167], %[l0] offset:17408\n\t" "s_waitcnt lgkmcnt(9)\n\t" "v_smfmac_f32_16x16x64_bf16 v[232:235], v[248:251], a[120:127], %[ix]\n\t" \
    "v_smfmac_f32_16x16x64_bf16 v[236:239], v[248:251], a[128:135], %[ix]\n\t" "s_waitcnt lgkmcnt(8)\n\t" \
    "v_smfmac_f32_16x16x64_bf16 v[232:235], v[252:255], a[136:143], %[ix]\n\t" "v_smfmac_f32_16x16x64_bf16 v[236:239], v[252:255], v[168:175], %[ix]\n\t" \
    "ds_read_b128 v[168:171], %[l0] offset:18432\n\t" "ds_read_b128 v[172:175], %[l0] offset:19456\n\t" "s_waitcnt lgkmcnt(9)\n\t" \
    "v_smfmac_f32_16x16x64_bf16 v[232:235], v[16:19], a[144:151], %[ix]\n\t" "v_smfmac_f32_16x16x64_bf16 v[236:239], v[16:19], a[152:159], %[ix]\n\t" \
    "s_waitcnt lgkmcnt(8)\n\t" "v_smfmac_f32_16x16x64_bf16 v[232:235], v[20:23], a[160:167], %[ix]\n\t" \
    "v_smfmac_f32_16x16x64_bf16 v[236:239], v[20:23], v[176:183], %[ix]\n\t" "ds_read_b128 v[176:179], %[l0] offset:20480\n\t" \
    "ds_read_b128 v[180:183], %[l0] offset:21504\n\t" "s_waitcnt lgkmcnt(9)\n\t" "v_smfmac_f32_16x16x64_bf16 v[232:235], v[24:27], a[168:175], %[ix]\n\t" \
    "v_smfmac_f32_16x16x64_bf16 v[236:239], v[24:27], a[176:183], %[ix]\n\t" "s_waitcnt lgkmcnt(6)\n\t" \
    "v_smfmac_f32_16x16x64_bf16 v[232:235], v[28:31], a[184:191], %[ix]\n\t" "v_smfmac_f32_16x16x64_bf16 v[236:239], v[28:31], v[184:191], %[ix]\n\t" \
    "ds_read_b128 v[184:187], %[l0] offset:22528\n\t" "ds_read_b128 v[188:191], %[l0] offset:23552\n\t" XT(90, 91) "s_nop 4\n\t" "v_add_f32 v232, v232, v233\n\t" \
    "v_add_f32 v234, v234, v235\n\t" "v_add_f32 v236, v236, v237\n\t" "v_add_f32 v238, v238, v239\n\t" "v_cvt_pk_bf16_f32 v236, v232, v236\n\t" \
    "v_cvt_pk_bf16_f32 v237, v234, v238\n\t" "ds_write2_b32 %[vw], v236, v237 offset1:68\n\t" "v_cmp_u_f32 vcc, v232, v234\n\t" "s_cbranch_vccnz 41f\n\t" "42:\n\t" \
    "s_waitcnt lgkmcnt(0)\n\t" "s_barrier\n\t" XT(92, 93) "ds_read_b128 v[240:243], %[vr] offset:0\n\t" "ds_read_b128 v[244:247], %[vr] offset:64\n\t" \
    "ds_read_b128 v[248:251], %[vr] offset:128\n\t" "ds_read_b128 v[252:255], %[vr] offset:192\n\t" "s_mov_b32 s61, %[ub]\n\t" "s_waitcnt lgkmcnt(3)\n\t" \
    "v_smfmac_f32_16x16x64_bf16 v[192:195], v[240:243], v[32:39], %[ix]\n\t" "v_smfmac_f32_16x16x64_bf16 v[196:199], v[240:243], v[48:55], %[ix]\n\t" \
    "s_waitcnt lgkmcnt(2)\n\t" "v_smfmac_f32_16x16x64_bf16 v[192:195], v[244:247], v[40:47], %[ix]\n\t" \
    "v_smfmac_f32_16x16x64_bf16 v[196:199], v[244:247], v[56:63], %[ix]\n\t" "s_waitcnt lgkmcnt(1)\n\t" \
    "v_smfmac_f32_16x16x64_bf16 v[192:195], v[248:251], a[192:199], %[ix]\n\t" "v_smfmac_f32_16x16x64_bf16 v[196:199], v[248:251], a[200:207], %[ix]\n\t" \
    "s_waitcnt lgkmcnt(0)\n\t" "v_smfmac_f32_16x16x64_bf16 v[192:195], v[252:255], v[160:167], %[ix]\n\t" "ds_read_b128 v[160:163], %[l0] offset:24576\n\t" \
    "ds_read_b128 v[164:167], %[l0] offset:25600\n\t" "v_smfmac_f32_16x16x64_bf16 v[196:199], v[252:255], v[168:175], %[ix]\n\t" \
    "ds_read_b128 v[168:171], %[l0] offset:26624\n\t" "ds_read_b128 v[172:175], %[l0] offset:27648\n\t" \
    "v_smfmac_f32_16x16x64_bf16 v[200:203], v[240:243], v[64:71], %[ix]\n\t" "s_nop 1\n\t" "v_smfmac_f32_16x16x64_bf16 v[204:207], v[240:243], v[80:87], %[ix]\n\t" \
    "v_cvt_pk_bf16_f32 v224, v192, v194\n\t" "v_cvt_pk_bf16_f32 v225, v193, v195\n\t" "v_smfmac_f32_16x16x64_bf16 v[200:203], v[244:247], v[72:79], %[ix]\n\t" \
    "v_cvt_pk_bf16_f32 v226, v196, v198\n\t" "v_cvt_pk_bf16_f32 v227, v197, v199\n\t" "v_smfmac_f32_16x16x64_bf16 v[204:207], v[244:247], v[88:95], %[ix]\n\t" "s_nop 0\n\t" \
    "v_permlane32_swap_b32 v224, v226\n\t" "v_smfmac_f32_16x16x64_bf16 v[200:203], v[248:251], a[208:215], %[ix]\n\t" "v_permlane32_swap_b32 v225, v227\n\t" "s_nop 0\n\t" \
    "v_smfmac_f32_16x16x64_bf16 v[204:207], v[248:251], a[216:223], %[ix]\n\t" "v_permlane16_swap_b32 v224, v225\n\t" "v_permlane16_swap_b32 v226, v227\n\t" \
    "v_smfmac_f32_16x16x64_bf16 v[200:203], v[252:255], v[176:183], %[ix]\n\t" "ds_read_b128 v[176:179], %[l0] offset:28672\n\t" \
    "buffer_store_dwordx4 v[224:227], %[uo], %[rs], s61 offen sc1\n\t" "s_xor_b32 s61, s61, 0x40000\n\t" XT(94, 95) \
    "v_smfmac_f32_16x16x64_bf16 v[204:207], v[252:255], v[184:191], %[ix]\n\t" "ds_read_b128 v[180:183], %[l0] offset:29696\n\t" \
    "ds_read_b128 v[184:187], %[l0] offset:30720\n\t" "v_smfmac_f32_16x16x64_bf16 v[208:211], v[240:243], v[96:103], %[ix]\n\t" \
    "ds_read_b128 v[188:191], %[l0] offset:31744\n\t" "s_nop 0\n\t" "v_smfmac_f32_16x16x64_bf16 v[212:215], v[240:243], v[112:119], %[ix]\n\t" \
    "v_cvt_pk_bf16_f32 v228, v200, v202\n\t" "v_cvt_pk_bf16_f32 v229, v201, v203\n\t" "v_smfmac_f32_16x16x64_bf16 v[208:211], v[244:247], v[104:111], %[ix]\n\t" \
    "v_cvt_pk_bf16_f32 v230, v204, v206\n\t" "v_cvt_pk_bf16_f32 v231, v205, v207\n\t" "v_smfmac_f32_16x16x64_bf16 v[212:215], v[244:247], v[120:127], %[ix]\n\t" "s_nop 0\n\t" \
    "v_permlane32_swap_b32 v228, v230\n\t" "v_smfmac_f32_16x16x64_bf16 v[208:211], v[248:251], a[224:231], %[ix]\n\t" "v_permlane32_swap_b32 v229, v231\n\t" "s_nop 0\n\t" \
    "v_smfmac_f32_16x16x64_bf16 v[212:215], v[248:251], a[232:239], %[ix]\n\t" "v_permlane16_swap_b32 v228, v229\n\t" "v_permlane16_swap_b32 v230, v231\n\t" \
    "s_waitcnt lgkmcnt(6)\n\t" "v_smfmac_f32_16x16x64_bf16 v[208:211], v[252:255], v[160:167], %[ix]\n\t" "buffer_store_dwordx4 v[228:231], %[uo], %[rs], s61 offen sc1\n\t" \
    "s_xor_b32 s61, s61, 0xc0000\n\t" "s_waitcnt lgkmcnt(4)\n\t" "v_smfmac_f32_16x16x64_bf16 v[212:215], v[252:255], v[168:175], %[ix]\n\t" "s_nop 1\n\t" \
    "v_smfmac_f32_16x16x64_bf16 v[216:219], v[240:243], v[128:135], %[ix]\n\t" "s_nop 1\n\t" "v_smfmac_f32_16x16x64_bf16 v[220:223], v[240:243], v[144:151], %[ix]\n\t" \
    "v_cvt_pk_bf16_f32 v224, v208, v210\n\t" "v_cvt_pk_bf16_f32 v225, v209, v211\n\t" "v_smfmac_f32_16x16x64_bf16 v[216:219], v[244:247], v[136:143], %[ix]\n\t" \
    "v_cvt_pk_bf16_f32 v226, v212, v214\n\t" "v_cvt_pk_bf16_f32 v227, v213, v215\n\t" "v_smfmac_f32_16x16x64_bf16 v[220:223], v[244:247], v[152:159], %[ix]\n\t" "s_nop 0\n\t" \
    "v_permlane32_swap_b32 v224, v226\n\t" "v_smfmac_f32_16x16x64_bf16 v[216:219], v[248:251], a[240:247], %[ix]\n\t" "v_permlane32_swap_b32 v225, v227\n\t" "s_nop 0\n\t" \
    "v_smfmac_f32_16x16x64_bf16 v[220:223], v[248:251], a[248:255], %[ix]\n\t" "v_permlane16_swap_b32 v224, v225\n\t" "v_permlane16_swap_b32 v226, v227\n\t" \
    "s_waitcnt lgkmcnt(2)\n\t" "v_smfmac_f32_16x16x64_bf16 v[216:219], v[252:255], v[176:183], %[ix]\n\t" "buffer_store_dwordx4 v[224:227], %[uo], %[rs], s61 offen sc1\n\t" \
    "s_xor_b32 s61, s61, 0x40000\n\t" "s_waitcnt lgkmcnt(0)\n\t" "v_smfmac_f32_16x16x64_bf16 v[220:223], v[252:255], v[184:191], %[ix]\n\t" "s_nop 5\n\t" \
    "v_cvt_pk_bf16_f32 v228, v216, v218\n\t" "v_cvt_pk_bf16_f32 v229, v217, v219\n\t" "v_cvt_pk_bf16_f32 v230, v220, v222\n\t" "v_cvt_pk_bf16_f32 v231, v221, v223\n\t" \
    "s_nop 0\n\t" "v_permlane32_swap_b32 v228, v230\n\t" "v_permlane32_swap_b32 v229, v231\n\t" "s_nop 1\n\t" "v_permlane16_swap_b32 v228, v229\n\t" \
    "v_permlane16_swap_b32 v230, v231\n\t" "buffer_store_dwordx4 v[228:231], %[uo], %[rs], s61 offen sc1\n\t" "s_nop 1\n\t" XT(96, 97) "s_branch 99f\n\t" "41:\n\t" \
    "s_mov_b64 s[62:63], 0\n\t" "ds_read_b128 v[16:19], %[l1] offset:0\n\t" "ds_read_b128 v[20:23], %[l1] offset:1024\n\t" "ds_read_b128 v[24:27], %[l1] offset:2048\n\t" \
    "ds_read_b128 v[28:31], %[l1] offset:3072\n\t" "s_waitcnt lgkmcnt(3)\n\t" "v_max3_u32 v16, v16, v17, v18\n\t" "v_max_u32 v16, v16, v19\n\t" \
    "v_cmp_eq_u32 vcc, -1, v16\n\t" "ds_read_b128 v[16:19], %[l1] offset:4096\n\t" "s_or_b64 s[62:63], s[62:63], vcc\n\t" "s_waitcnt lgkmcnt(3)\n\t" \
    "v_max3_u32 v20, v20, v21, v22\n\t" "v_max_u32 v20, v20, v23\n\t" "v_cmp_eq_u32 vcc, -1, v20\n\t" "ds_read_b128 v[20:23], %[l1] offset:5120\n\t" \
    "s_or_b64 s[62:63], s[62:63], vcc\n\t" "s_waitcnt lgkmcnt(3)\n\t" "v_max3_u32 v24, v24, v25, v26\n\t" "v_max_u32 v24, v24, v27\n\t" "v_cmp_eq_u32 vcc, -1, v24\n\t" \
    "ds_read_b128 v[24:27], %[l1] offset:6144\n\t" "s_or_b64 s[62:63], s[62:63], vcc\n\t" "s_waitcnt lgkmcnt(3)\n\t" "v_max3_u32 v28, v28, v29, v30\n\t" \
    "v_max_u32 v28, v28, v31\n\t" "v_cmp_eq_u32 vcc, -1, v28\n\t" "ds_read_b128 v[28:31], %[l1] offset:7168\n\t" "s_or_b64 s[62:63], s[62:63], vcc\n\t" \
    "s_waitcnt lgkmcnt(3)\n\t" "v_max3_u32 v16, v16, v17, v18\n\t" "v_max_u32 v16, v16, v19\n\t" "v_cmp_eq_u32 vcc, -1, v16\n\t" \
    "ds_read_b128 v[16:19], %[l1] offset:8192\n\t" "s_or_b64 s[62:63], s[62:63], vcc\n\t" "s_waitcnt lgkmcnt(3)\n\t" "v_max3_u32 v20, v20, v21, v22\n\t" \
    "v_max_u32 v20, v20, v23\n\t" "v_cmp_eq_u32 vcc, -1, v20\n\t" "ds_read_b128 v[20:23], %[l1] offset:9216\n\t" "s_or_b64 s[62:63], s[62:63], vcc\n\t" \
    "s_waitcnt lgkmcnt(3)\n\t" "v_max3_u32 v24, v24, v25, v26\n\t" "v_max_u32 v24, v24, v27\n\t" "v_cmp_eq_u32 vcc, -1, v24\n\t" \
    "ds_read_b128 v[24:27], %[l1] offset:10240\n\t" "s_or_b64 s[62:63], s[62:63], vcc\n\t" "s_waitcnt lgkmcnt(3)\n\t" "v_max3_u32 v28, v28, v29, v30\n\t" \
    "v_max_u32 v28, v28, v31\n\t" "v_cmp_eq_u32 vcc, -1, v28\n\t" "ds_read_b128 v[28:31], %[l1] offset:11264\n\t" "s_or_b64 s[62:63], s[62:63], vcc\n\t" \
    "s_waitcnt lgkmcnt(3)\n\t" "v_max3_u32 v16, v16, v17, v18\n\t" "v_max_u32 v16, v16, v19\n\t" "v_cmp_eq_u32 vcc, -1, v16\n\t" \
    "ds_read_b128 v[16:19], %[l1] offset:12288\n\t" "s_or_b64 s[62:63], s[62:63], vcc\n\t" "s_waitcnt lgkmcnt(3)\n\t" "v_max3_u32 v20, v20, v21, v22\n\t" \
    "v_max_u32 v20, v20, v23\n\t" "v_cmp_eq_u32 vcc, -1, v20\n\t" "ds_read_b128 v[20:23], %[l1] offset:13312\n\t" "s_or_b64 s[62:63], s[62:63], vcc\n\t" \
    "s_waitcnt lgkmcnt(3)\n\t" "v_max3_u32 v24, v24, v25, v26\n\t" "v_max_u32 v24, v24, v27\n\t" "v_cmp_eq_u32 vcc, -1, v24\n\t" \
    "ds_read_b128 v[24:27], %[l1] offset:14336\n\t" "s_or_b64 s[62:63], s[62:63], vcc\n\t" "s_waitcnt lgkmcnt(3)\n\t" "v_max3_u32 v28, v28, v29, v30\n\t" \
    "v_max_u32 v28, v28, v31\n\t" "v_cmp_eq_u32 vcc, -1, v28\n\t" "ds_read_b128 v[28:31], %[l1] offset:15360\n\t" "s_or_b64 s[62:63], s[62:63], vcc\n\t" \
    "s_waitcnt lgkmcnt(3)\n\t" "v_max3_u32 v16, v16, v17, v18\n\t" "v_max_u32 v16, v16, v19\n\t" "v_cmp_eq_u32 vcc, -1, v16\n\t" "s_or_b64 s[62:63], s[62:63], vcc\n\t" \
    "s_waitcnt lgkmcnt(2)\n\t" "v_max3_u32 v20, v20, v21, v22\n\t" "v_max_u32 v20, v20, v23\n\t" "v_cmp_eq_u32 vcc, -1, v20\n\t" "s_or_b64 s[62:63], s[62:63], vcc\n\t" \
    "s_waitcnt lgkmcnt(1)\n\t" "v_max3_u32 v24, v24, v25, v26\n\t" "v_max_u32 v24, v24, v27\n\t" "v_cmp_eq_u32 vcc, -1, v24\n\t" "s_or_b64 s[62:63], s[62:63], vcc\n\t" \
    "s_waitcnt lgkmcnt(0)\n\t" "v_max3_u32 v28, v28, v29, v30\n\t" "v_max_u32 v28, v28, v31\n\t" "v_cmp_eq_u32 vcc, -1, v28\n\t" "s_or_b64 s[62:63], s[62:63], vcc\n\t" \
    "s_cmp_eq_u64 s[62:63], 0\n\t" "s_cbranch_scc1 42b\n\t" "s_cmp_eq_u32 %[rt], 0\n\t" "s_cbranch_scc1 42b\n\t" "s_sub_u32 %[rt], %[rt], 1\n\t" XCNT(100) "44:\n\t" \
    "buffer_load_dwordx4 v[240:243], %[mo], %[rs], %[sb] offen sc1\n\t" "buffer_load_dwordx4 v[244:247], v224, %[rs], %[sb] offen sc1\n\t" \
    "buffer_load_dwordx4 v[248:251], %[mo], %[rs], s60 offen sc1\n\t" "buffer_load_dwordx4 v[252:255], v224, %[rs], s60 offen sc1\n\t" "s_waitcnt vmcnt(0)\n\t" \
    "v_max_u32 v28, v240, v241\n\t" "v_max3_u32 v28, v28, v242, v243\n\t" "v_max3_u32 v28, v28, v244, v245\n\t" "v_max3_u32 v28, v28, v246, v247\n\t" \
    "v_max3_u32 v28, v28, v248, v249\n\t" "v_max3_u32 v28, v28, v250, v251\n\t" "v_max3_u32 v28, v28, v252, v253\n\t" "v_max3_u32 v28, v28, v254, v255\n\t" \
    "v_cmp_eq_u32 vcc, -1, v28\n\t" "s_cbranch_vccz 45f\n\t" "s_cmp_eq_u32 %[bd], 0\n\t" "s_cbranch_scc1 45f\n\t" "s_sub_u32 %[bd], %[bd], 1\n\t" XCNT(101) "s_sleep 1\n\t" \
    "s_branch 44b\n\t" "45:\n\t" "s_barrier\n\t" "s_branch 40b\n\t" "99:\n\t"
#define Y_STEP \
    "v_xor_b32 v128, 0, %[u0]\n\t" "v_xor_b32 v132, 0, %[pm]\n\t" "v_xor_b32 v129, 2048, %[u0]\n\t" "v_xor_b32 v133, 1024, %[pm]\n\t" "v_xor_b32 v130, 4096, %[u0]\n\t" \
    "v_xor_b32 v134, 2048, %[pm]\n\t" "v_xor_b32 v131, 6144, %[u0]\n\t" "v_xor_b32 v135, 3072, %[pm]\n\t" "s_getpc_b64 s[62:63]\n\t" "11:\n\t" "s_memtime s[64:65]\n\t" \
    "s_waitcnt lgkmcnt(0)\n\t" "s_sub_u32 s61, %[tu], s64\n\t" "s_cmp_gt_i32 s61, 124\n\t" "s_cbranch_scc1 11b\n\t" "s_max_i32 s61, s61, 0\n\t" "s_and_b32 s61, s61, 124\n\t" \
    "s_sub_u32 s61, 12f-11b, s61\n\t" "s_add_u32 s62, s62, s61\n\t" "s_addc_u32 s63, s63, 0\n\t" "s_setpc_b64 s[62:63]\n\t" ".rept 31\n\t" "s_nop 0\n\t" ".endr\n\t" "12:\n\t" \
    XT(90, 91) "buffer_load_dwordx4 v[144:147], v128, %[rs], %[ub] offen offset:0 sc1\n\t" "buffer_load_dwordx4 v[148:151], v128, %[rs], %[ub] offen offset:1024 sc1\n\t" \
    "buffer_load_dwordx4 v[152:155], v129, %[rs], %[ub] offen offset:0 sc1\n\t" "buffer_load_dwordx4 v[156:159], v129, %[rs], %[ub] offen offset:1024 sc1\n\t" \
    "s_cmp_lg_u32 %[late], 0\n\t" "s_cbranch_scc1 14f\n\t" "s_getpc_b64 s[62:63]\n\t" "13:\n\t" "s_memtime s[64:65]\n\t" "s_waitcnt lgkmcnt(0)\n\t" \
    "s_sub_u32 s61, %[tg], s64\n\t" "s_cmp_gt_i32 s61, 124\n\t" "s_cbranch_scc1 13b\n\t" "s_max_i32 s61, s61, 0\n\t" "s_and_b32 s61, s61, 124\n\t" \
    "s_sub_u32 s61, 14f-13b, s61\n\t" "s_add_u32 s62, s62, s61\n\t" "s_addc_u32 s63, s63, 0\n\t" "s_setpc_b64 s[62:63]\n\t" ".rept 31\n\t" "s_nop 0\n\t" ".endr\n\t" "14:\n\t" \
    XT(92, 93) "20:\n\t" "buffer_load_dwordx4 v[160:163], v130, %[rs], %[ub] offen offset:0 sc1\n\t" \
    "buffer_load_dwordx4 v[164:167], v130, %[rs], %[ub] offen offset:1024 sc1\n\t" "buffer_load_dwordx4 v[192:195], %[x0], %[rs], %[pb] offen offset:0 sc1\n\t" \
    "buffer_load_dwordx4 v[196:199], %[x0], %[rs], %[pb] offen offset:1024 sc1\n\t" "buffer_load_dwordx4 v[200:203], %[x0], %[rs], %[pb] offen offset:2048 sc1\n\t" \
    "buffer_load_dwordx4 v[204:207], %[x0], %[rs], %[pb] offen offset:3072 sc1\n\t" "buffer_load_dwordx2 v[176:177], v132, %[rs], %[pb] offen offset:0 sc1\n\t" \
    "buffer_load_dwordx2 v[178:179], v132, %[rs], %[pb] offen offset:128 sc1\n\t" "v_mov_b64 v[120:121], 0\n\t" "v_mov_b64 v[122:123], 0\n\t" \
    "buffer_load_dwordx4 v[208:211], %[x1], %[rs], %[pb] offen offset:0 sc1\n\t" "buffer_load_dwordx4 v[212:215], %[x1], %[rs], %[pb] offen offset:1024 sc1\n\t" \
    "buffer_load_dwordx4 v[216:219], %[x1], %[rs], %[pb] offen offset:2048 sc1\n\t" "buffer_load_dwordx4 v[220:223], %[x1], %[rs], %[pb] offen offset:3072 sc1\n\t" \
    "buffer_load_dwordx2 v[180:181], v133, %[rs], %[pb] offen offset:0 sc1\n\t" "buffer_load_dwordx2 v[182:183], v133, %[rs], %[pb] offen offset:128 sc1\n\t" \
    "buffer_load_dwordx4 v[224:227], %[x2], %[rs], %[pb] offen offset:0 sc1\n\t" "buffer_load_dwordx4 v[228:231], %[x2], %[rs], %[pb] offen offset:1024 sc1\n\t" \
    "buffer_load_dwordx4 v[232:235], %[x2], %[rs], %[pb] offen offset:2048 sc1\n\t" "buffer_load_dwordx4 v[236:239], %[x2], %[rs], %[pb] offen offset:3072 sc1\n\t" \
    "buffer_load_dwordx2 v[184:185], v134, %[rs], %[pb] offen offset:0 sc1\n\t" "buffer_load_dwordx2 v[186:187], v134, %[rs], %[pb] offen offset:128 sc1\n\t" \
    "buffer_load_dwordx4 v[240:243], %[x3], %[rs], %[pb] offen offset:0 sc1\n\t" "buffer_load_dwordx4 v[244:247], %[x3], %[rs], %[pb] offen offset:1024 sc1\n\t" \
    "buffer_load_dwordx4 v[248:251], %[x3], %[rs], %[pb] offen offset:2048 sc1\n\t" "buffer_load_dwordx4 v[252:255], %[x3], %[rs], %[pb] offen offset:3072 sc1\n\t" \
    "buffer_load_dwordx4 v[168:171], v131, %[rs], %[ub] offen offset:0 sc1\n\t" "buffer_load_dwordx4 v[172:175], v131, %[rs], %[ub] offen offset:1024 sc1\n\t" \
    "buffer_load_dwordx2 v[188:189], v135, %[rs], %[pb] offen offset:0 sc1\n\t" "buffer_load_dwordx2 v[190:191], v135, %[rs], %[pb] offen offset:128 sc1\n\t" \
    "buffer_load_dword %[sv], %[so], %[rs], %[pb] offen sc1\n\t" "v_mov_b32 v127, -1\n\t" "s_waitcnt vmcnt(26)\n\t" XT(94, 95) \
    "v_smfmac_f32_16x16x64_bf16 v[120:123], v[192:195], a[0:7], %[ix]\n\t" "s_waitcnt vmcnt(25)\n\t" "v_smfmac_f32_16x16x64_bf16 v[120:123], v[196:199], a[8:15], %[ix]\n\t" \
    "s_waitcnt vmcnt(24)\n\t" "v_smfmac_f32_16x16x64_bf16 v[120:123], v[200:203], a[16:23], %[ix]\n\t" "s_waitcnt vmcnt(23)\n\t" \
    "v_smfmac_f32_16x16x64_bf16 v[120:123], v[204:207], a[24:31], %[ix]\n\t" "s_waitcnt vmcnt(21)\n\t" \
    "v_smfmac_f32_16x16x64_bf16 v[120:123], v[176:179], v[144:151], %[ix]\n\t" "s_waitcnt vmcnt(20)\n\t" \
    "v_smfmac_f32_16x16x64_bf16 v[120:123], v[208:211], a[32:39], %[ix]\n\t" "s_waitcnt vmcnt(19)\n\t" \
    "v_smfmac_f32_16x16x64_bf16 v[120:123], v[212:215], a[40:47], %[ix]\n\t" "s_waitcnt vmcnt(18)\n\t" \
    "v_smfmac_f32_16x16x64_bf16 v[120:123], v[216:219], a[48:55], %[ix]\n\t" "s_waitcnt vmcnt(17)\n\t" \
    "v_smfmac_f32_16x16x64_bf16 v[120:123], v[220:223], a[56:63], %[ix]\n\t" "s_waitcnt vmcnt(15)\n\t" \
    "v_smfmac_f32_16x16x64_bf16 v[120:123], v[180:183], v[152:159], %[ix]\n\t" "s_waitcnt vmcnt(14)\n\t" \
    "v_smfmac_f32_16x16x64_bf16 v[120:123], v[224:227], a[64:71], %[ix]\n\t" "s_waitcnt vmcnt(13)\n\t" \
    "v_smfmac_f32_16x16x64_bf16 v[120:123], v[228:231], a[72:79], %[ix]\n\t" "s_waitcnt vmcnt(12)\n\t" \
    "v_smfmac_f32_16x16x64_bf16 v[120:123], v[232:235], a[80:87], %[ix]\n\t" "s_waitcnt vmcnt(11)\n\t" \
    "v_smfmac_f32_16x16x64_bf16 v[120:123], v[236:239], a[88:95], %[ix]\n\t" "s_waitcnt vmcnt(9)\n\t" \
    "v_smfmac_f32_16x16x64_bf16 v[120:123], v[184:187], v[160:167], %[ix]\n\t" "s_waitcnt vmcnt(8)\n\t" \
    "v_smfmac_f32_16x16x64_bf16 v[120:123], v[240:243], a[96:103], %[ix]\n\t" "s_waitcnt vmcnt(7)\n\t" \
    "v_smfmac_f32_16x16x64_bf16 v[120:123], v[244:247], a[104:111], %[ix]\n\t" "s_waitcnt vmcnt(6)\n\t" \
    "v_smfmac_f32_16x16x64_bf16 v[120:123], v[248:251], a[112:119], %[ix]\n\t" "s_waitcnt vmcnt(5)\n\t" \
    "v_smfmac_f32_16x16x64_bf16 v[120:123], v[252:255], a[120:127], %[ix]\n\t" "s_waitcnt vmcnt(1)\n\t" \
    "v_smfmac_f32_16x16x64_bf16 v[120:123], v[188:191], v[168:175], %[ix]\n\t" XT(82, 83) "s_nop 7\n\t" "v_add_f32 v120, v120, v121\n\t" "v_add_f32 v122, v122, v123\n\t" \
    "v_cvt_pk_bf16_f32 v124, v120, v122\n\t" "v_cmp_u_f32 vcc, v120, v122\n\t" "s_cbranch_vccnz 30f\n\t" "21:\n\t" \
    "v_mov_b32_dpp v125, v124 quad_perm:[1,0,3,2] row_mask:0xf bank_mask:0xf\n\t" "v_perm_b32 v124, v125, v124, %[ps]\n\t" "v_min_u32 v124, -2, v124\n\t" \
    "buffer_store_dword v124, %[mw], %[rs], %[mb] offen sc1\n\t" XT(84, 85) "buffer_store_dword v127, %[mw], %[rs], %[mr] offen sc1\n\t" "s_memtime s[64:65]\n\t" \
    "s_barrier\n\t" XT(98, 99) "s_waitcnt lgkmcnt(0)\n\t" "s_and_b32 %[bad], s64, -2\n\t" "s_branch 40f\n\t" "30:\n\t" "v_max_u32 v126, v144, v145\n\t" \
    "v_max3_u32 v126, v126, v146, v147\n\t" "v_max3_u32 v126, v126, v148, v149\n\t" "v_max3_u32 v126, v126, v150, v151\n\t" "v_max3_u32 v126, v126, v152, v153\n\t" \
    "v_max3_u32 v126, v126, v154, v155\n\t" "v_max3_u32 v126, v126, v156, v157\n\t" "v_max3_u32 v126, v126, v158, v159\n\t" "v_max3_u32 v126, v126, v160, v161\n\t" \
    "v_max3_u32 v126, v126, v162, v163\n\t" "v_max3_u32 v126, v126, v164, v165\n\t" "v_max3_u32 v126, v126, v166, v167\n\t" "v_max3_u32 v126, v126, v168, v169\n\t" \
    "v_max3_u32 v126, v126, v170, v171\n\t" "v_max3_u32 v126, v126, v172, v173\n\t" "v_max3_u32 v126, v126, v174, v175\n\t" UCNT "v_max3_u32 v126, v126, v176, v177\n\t" \
    "v_max3_u32 v126, v126, v178, v179\n\t" "v_max3_u32 v126, v126, v180, v181\n\t" "v_max3_u32 v126, v126, v182, v183\n\t" "v_max3_u32 v126, v126, v184, v185\n\t" \
    "v_max3_u32 v126, v126, v186, v187\n\t" "v_max3_u32 v126, v126, v188, v189\n\t" "v_max3_u32 v126, v126, v190, v191\n\t" "v_max3_u32 v126, v126, v192, v193\n\t" \
    "v_max3_u32 v126, v126, v194, v195\n\t" "v_max3_u32 v126, v126, v196, v197\n\t" "v_max3_u32 v126, v126, v198, v199\n\t" "v_max3_u32 v126, v126, v200, v201\n\t" \
    "v_max3_u32 v126, v126, v202, v203\n\t" "v_max3_u32 v126, v126, v204, v205\n\t" "v_max3_u32 v126, v126, v206, v207\n\t" "v_max3_u32 v126, v126, v208, v209\n\t" \
    "v_max3_u32 v126, v126, v210, v211\n\t" "v_max3_u32 v126, v126, v212, v213\n\t" "v_max3_u32 v126, v126, v214, v215\n\t" "v_max3_u32 v126, v126, v216, v217\n\t" \
    "v_max3_u32 v126, v126, v218, v219\n\t" "v_max3_u32 v126, v126, v220, v221\n\t" "v_max3_u32 v126, v126, v222, v223\n\t" "v_max3_u32 v126, v126, v224, v225\n\t" \
    "v_max3_u32 v126, v126, v226, v227\n\t" "v_max3_u32 v126, v126, v228, v229\n\t" "v_max3_u32 v126, v126, v230, v231\n\t" "v_max3_u32 v126, v126, v232, v233\n\t" \
    "v_max3_u32 v126, v126, v234, v235\n\t" "v_max3_u32 v126, v126, v236, v237\n\t" "v_max3_u32 v126, v126, v238, v239\n\t" "v_max3_u32 v126, v126, v240, v241\n\t" \
    "v_max3_u32 v126, v126, v242, v243\n\t" "v_max3_u32 v126, v126, v244, v245\n\t" "v_max3_u32 v126, v126, v246, v247\n\t" "v_max3_u32 v126, v126, v248, v249\n\t" \
    "v_max3_u32 v126, v126, v250, v251\n\t" "v_max3_u32 v126, v126, v252, v253\n\t" "v_max3_u32 v126, v126, v254, v255\n\t" "v_cmp_eq_u32 vcc, -1, v126\n\t" \
    "s_cbranch_vccz 21b\n\t" "s_cmp_eq_u32 %[bd], 0\n\t" "s_cbranch_scc1 21b\n\t" XCNT(100) "s_mov_b32 %[bad], 1\n\t" "s_waitcnt vmcnt(0)\n\t" "40:\n\t"
#define G_LOOP \
    "v_xor_b32 v72, 0, %[xo]\n\t" "v_add_u32 v76, 0x10000, v72\n\t" "v_xor_b32 v88, 0, %[xs]\n\t" "v_add_u32 v88, 0, v88\n\t" "v_xor_b32 v73, 64, %[xo]\n\t" \
    "v_add_u32 v77, 0x10000, v73\n\t" "v_xor_b32 v89, 16, %[xs]\n\t" "v_add_u32 v89, 2048, v89\n\t" "v_xor_b32 v74, 128, %[xo]\n\t" "v_add_u32 v78, 0x10000, v74\n\t" \
    "v_xor_b32 v90, 32, %[xs]\n\t" "v_add_u32 v90, 4096, v90\n\t" "v_xor_b32 v75, 192, %[xo]\n\t" "v_add_u32 v79, 0x10000, v75\n\t" "v_xor_b32 v91, 48, %[xs]\n\t" \
    "v_add_u32 v91, 6144, v91\n\t" "v_xor_b32 v92, 64, %[xs]\n\t" "v_add_u32 v92, 8192, v92\n\t" "v_xor_b32 v93, 80, %[xs]\n\t" "v_add_u32 v93, 10240, v93\n\t" \
    "v_xor_b32 v94, 96, %[xs]\n\t" "v_add_u32 v94, 12288, v94\n\t" "v_xor_b32 v95, 112, %[xs]\n\t" "v_add_u32 v95, 14336, v95\n\t" "v_mov_b32 v96, 2.0\n\t" \
    "s_mov_b32 s60, 0\n\t" "s_mov_b32 s62, 0\n\t" "s_waitcnt vmcnt(0)\n\t" "s_barrier\n\t" "ds_read_b128 v[100:103], v72 offset:0\n\t" \
    "ds_read_b128 v[104:107], v72 offset:32768\n\t" "ds_read_b128 v[108:111], v73 offset:0\n\t" "ds_read_b128 v[112:115], v73 offset:32768\n\t" \
    "ds_read_b128 v[116:119], v74 offset:0\n\t" "10:\n\t" XT(80, 81) "s_waitcnt lgkmcnt(4)\n\t" "v_mfma_f32_16x16x32_bf16 v[32:35], a[0:3], v[100:103], 0\n\t" \
    "ds_read_b128 v[120:123], v74 offset:32768\n\t" "s_add_u32 s61, s60, 1\n\t" "s_and_b32 s61, s61, 63\n\t" "v_mfma_f32_16x16x32_bf16 v[36:39], a[4:7], v[100:103], 0\n\t" \
    "s_lshl_b32 s61, s61, 16\n\t" "s_add_u32 s63, %[lw], 65536\n\t" "s_waitcnt lgkmcnt(4)\n\t" "v_mfma_f32_16x16x32_bf16 v[40:43], a[0:3], v[104:107], 0\n\t" \
    "ds_read_b128 v[124:127], v75 offset:0\n\t" "v_mfma_f32_16x16x32_bf16 v[44:47], a[4:7], v[104:107], 0\n\t" "s_waitcnt lgkmcnt(4)\n\t" \
    "v_mfma_f32_16x16x32_bf16 v[32:35], a[8:11], v[108:111], v[32:35]\n\t" "ds_read_b128 v[128:131], v75 offset:32768\n\t" "v_cvt_pk_bf16_f32 v64, v48, v49\n\t" \
    "v_mfma_f32_16x16x32_bf16 v[36:39], a[12:15], v[108:111], v[36:39]\n\t" "v_cvt_pk_bf16_f32 v65, v50, v51\n\t" "v_cvt_pk_bf16_f32 v66, v52, v53\n\t" \
    "s_waitcnt lgkmcnt(4)\n\t" "v_mfma_f32_16x16x32_bf16 v[40:43], a[8:11], v[112:115], v[40:43]\n\t" "ds_read_b128 v[100:103], v72 offset:256\n\t" \
    "v_cvt_pk_bf16_f32 v67, v54, v55\n\t" "v_mfma_f32_16x16x32_bf16 v[44:47], a[12:15], v[112:115], v[44:47]\n\t" "v_cvt_pk_bf16_f32 v68, v56, v57\n\t" \
    "v_cvt_pk_bf16_f32 v69, v58, v59\n\t" "s_waitcnt lgkmcnt(4)\n\t" "v_mfma_f32_16x16x32_bf16 v[32:35], a[16:19], v[116:119], v[32:35]\n\t" \
    "ds_read_b128 v[104:107], v72 offset:33024\n\t" "v_cvt_pk_bf16_f32 v70, v60, v61\n\t" "v_mfma_f32_16x16x32_bf16 v[36:39], a[20:23], v[116:119], v[36:39]\n\t" \
    "v_cvt_pk_bf16_f32 v71, v62, v63\n\t" "buffer_store_dwordx4 v[64:67], v96, %[rs], s62 offen offset:0 sc1\n\t" "s_waitcnt lgkmcnt(4)\n\t" \
    "v_mfma_f32_16x16x32_bf16 v[40:43], a[16:19], v[120:123], v[40:43]\n\t" "ds_read_b128 v[108:111], v73 offset:256\n\t" \
    "v_mfma_f32_16x16x32_bf16 v[44:47], a[20:23], v[120:123], v[44:47]\n\t" "s_waitcnt lgkmcnt(4)\n\t" "v_mfma_f32_16x16x32_bf16 v[32:35], a[24:27], v[124:127], v[32:35]\n\t" \
    "ds_read_b128 v[112:115], v73 offset:33024\n\t" "v_mfma_f32_16x16x32_bf16 v[36:39], a[28:31], v[124:127], v[36:39]\n\t" \
    "buffer_store_dwordx4 v[68:71], v96, %[rs], s62 offen offset:256 sc1\n\t" "s_waitcnt lgkmcnt(4)\n\t" \
    "v_mfma_f32_16x16x32_bf16 v[40:43], a[24:27], v[128:131], v[40:43]\n\t" "ds_read_b128 v[116:119], v74 offset:256\n\t" "s_mov_b32 m0, s63\n\t" \
    "v_mfma_f32_16x16x32_bf16 v[44:47], a[28:31], v[128:131], v[44:47]\n\t" "s_add_u32 s63, s63, 0x800\n\t" "buffer_load_dwordx4 v88, %[rx], s61 offen lds\n\t" \
    "s_waitcnt lgkmcnt(4)\n\t" "v_mfma_f32_16x16x32_bf16 v[32:35], a[32:35], v[100:103], v[32:35]\n\t" "ds_read_b128 v[120:123], v74 offset:33024\n\t" \
    "v_mfma_f32_16x16x32_bf16 v[36:39], a[36:39], v[100:103], v[36:39]\n\t" "buffer_load_dwordx4 v88, %[rx], s61 offen offset:1024 lds\n\t" "s_waitcnt lgkmcnt(4)\n\t" \
    "v_mfma_f32_16x16x32_bf16 v[40:43], a[32:35], v[104:107], v[40:43]\n\t" "ds_read_b128 v[124:127], v75 offset:256\n\t" "s_mov_b32 m0, s63\n\t" \
    "v_mfma_f32_16x16x32_bf16 v[44:47], a[36:39], v[104:107], v[44:47]\n\t" "s_add_u32 s63, s63, 0x800\n\t" "buffer_load_dwordx4 v89, %[rx], s61 offen lds\n\t" \
    "s_waitcnt lgkmcnt(4)\n\t" "v_mfma_f32_16x16x32_bf16 v[32:35], a[40:43], v[108:111], v[32:35]\n\t" "ds_read_b128 v[128:131], v75 offset:33024\n\t" \
    "v_mfma_f32_16x16x32_bf16 v[36:39], a[44:47], v[108:111], v[36:39]\n\t" "buffer_load_dwordx4 v89, %[rx], s61 offen offset:1024 lds\n\t" "s_waitcnt lgkmcnt(4)\n\t" \
    "v_mfma_f32_16x16x32_bf16 v[40:43], a[40:43], v[112:115], v[40:43]\n\t" "ds_read_b128 v[100:103], v72 offset:512\n\t" "s_mov_b32 m0, s63\n\t" \
    "v_mfma_f32_16x16x32_bf16 v[44:47], a[44:47], v[112:115], v[44:47]\n\t" "s_add_u32 s63, s63, 0x800\n\t" "buffer_load_dwordx4 v90, %[rx], s61 offen lds\n\t" \
    "s_waitcnt lgkmcnt(4)\n\t" "v_mfma_f32_16x16x32_bf16 v[32:35], a[48:51], v[116:119], v[32:35]\n\t" "ds_read_b128 v[104:107], v72 offset:33280\n\t" \
    "v_mfma_f32_16x16x32_bf16 v[36:39], a[52:55], v[116:119], v[36:39]\n\t" "buffer_load_dwordx4 v90, %[rx], s61 offen offset:1024 lds\n\t" "s_waitcnt lgkmcnt(4)\n\t" \
    "v_mfma_f32_16x16x32_bf16 v[40:43], a[48:51], v[120:123], v[40:43]\n\t" "ds_read_b128 v[108:111], v73 offset:512\n\t" "s_mov_b32 m0, s63\n\t" \
    "v_mfma_f32_16x16x32_bf16 v[44:47], a[52:55], v[120:123], v[44:47]\n\t" "s_add_u32 s63, s63, 0x800\n\t" "buffer_load_dwordx4 v91, %[rx], s61 offen lds\n\t" \
    "s_waitcnt lgkmcnt(4)\n\t" "v_mfma_f32_16x16x32_bf16 v[32:35], a[56:59], v[124:127], v[32:35]\n\t" "ds_read_b128 v[112:115], v73 offset:33280\n\t" \
    "v_mfma_f32_16x16x32_bf16 v[36:39], a[60:63], v[124:127], v[36:39]\n\t" "buffer_load_dwordx4 v91, %[rx], s61 offen offset:1024 lds\n\t" "s_waitcnt lgkmcnt(4)\n\t" \
    "v_mfma_f32_16x16x32_bf16 v[40:43], a[56:59], v[128:131], v[40:43]\n\t" "ds_read_b128 v[116:119], v74 offset:512\n\t" "s_mov_b32 m0, s63\n\t" \
    "v_mfma_f32_16x16x32_bf16 v[44:47], a[60:63], v[128:131], v[44:47]\n\t" "s_add_u32 s63, s63, 0x800\n\t" "buffer_load_dwordx4 v92, %[rx], s61 offen lds\n\t" \
    "s_waitcnt lgkmcnt(4)\n\t" "v_mfma_f32_16x16x32_bf16 v[32:35], a[64:67], v[100:103], v[32:35]\n\t" "ds_read_b128 v[120:123], v74 offset:33280\n\t" \
    "v_mfma_f32_16x16x32_bf16 v[36:39], a[68:71], v[100:103], v[36:39]\n\t" "buffer_load_dwordx4 v92, %[rx], s61 offen offset:1024 lds\n\t" "s_waitcnt lgkmcnt(4)\n\t" \
    "v_mfma_f32_16x16x32_bf16 v[40:43], a[64:67], v[104:107], v[40:43]\n\t" "ds_read_b128 v[124:127], v75 offset:512\n\t" "s_mov_b32 m0, s63\n\t" \
    "v_mfma_f32_16x16x32_bf16 v[44:47], a[68:71], v[104:107], v[44:47]\n\t" "s_add_u32 s63, s63, 0x800\n\t" "buffer_load_dwordx4 v93, %[rx], s61 offen lds\n\t" \
    "s_waitcnt lgkmcnt(4)\n\t" "v_mfma_f32_16x16x32_bf16 v[32:35], a[72:75], v[108:111], v[32:35]\n\t" "ds_read_b128 v[128:131], v75 offset:33280\n\t" \
    "v_mfma_f32_16x16x32_bf16 v[36:39], a[76:79], v[108:111], v[36:39]\n\t" "buffer_load_dwordx4 v93, %[rx], s61 offen offset:1024 lds\n\t" "s_waitcnt lgkmcnt(4)\n\t" \
    "v_mfma_f32_16x16x32_bf16 v[40:43], a[72:75], v[112:115], v[40:43]\n\t" "ds_read_b128 v[100:103], v72 offset:768\n\t" "s_mov_b32 m0, s63\n\t" \
    "v_mfma_f32_16x16x32_bf16 v[44:47], a[76:79], v[112:115], v[44:47]\n\t" "s_add_u32 s63, s63, 0x800\n\t" "buffer_load_dwordx4 v94, %[rx], s61 offen lds\n\t" \
    "s_waitcnt lgkmcnt(4)\n\t" "v_mfma_f32_16x16x32_bf16 v[32:35], a[80:83], v[116:119], v[32:35]\n\t" "ds_read_b128 v[104:107], v72 offset:33536\n\t" \
    "v_mfma_f32_16x16x32_bf16 v[36:39], a[84:87], v[116:119], v[36:39]\n\t" "buffer_load_dwordx4 v94, %[rx], s61 offen offset:1024 lds\n\t" "s_waitcnt lgkmcnt(4)\n\t" \
    "v_mfma_f32_16x16x32_bf16 v[40:43], a[80:83], v[120:123], v[40:43]\n\t" "ds_read_b128 v[108:111], v73 offset:768\n\t" "s_mov_b32 m0, s63\n\t" \
    "v_mfma_f32_16x16x32_bf16 v[44:47], a[84:87], v[120:123], v[44:47]\n\t" "s_add_u32 s63, s63, 0x800\n\t" "buffer_load_dwordx4 v95, %[rx], s61 offen lds\n\t" \
    "s_waitcnt lgkmcnt(4)\n\t" "v_mfma_f32_16x16x32_bf16 v[32:35], a[88:91], v[124:127], v[32:35]\n\t" "ds_read_b128 v[112:115], v73 offset:33536\n\t" \
    "v_mfma_f32_16x16x32_bf16 v[36:39], a[92:95], v[124:127], v[36:39]\n\t" "buffer_load_dwordx4 v95, %[rx], s61 offen offset:1024 lds\n\t" "s_waitcnt lgkmcnt(4)\n\t" \
    "v_mfma_f32_16x16x32_bf16 v[40:43], a[88:91], v[128:131], v[40:43]\n\t" "ds_read_b128 v[116:119], v74 offset:768\n\t" \
    "v_mfma_f32_16x16x32_bf16 v[44:47], a[92:95], v[128:131], v[44:47]\n\t" "s_waitcnt lgkmcnt(4)\n\t" "v_mfma_f32_16x16x32_bf16 v[32:35], a[96:99], v[100:103], v[32:35]\n\t" \
    "ds_read_b128 v[120:123], v74 offset:33536\n\t" "s_lshr_b32 s64, s60, 2\n\t" "s_xor_b32 s64, s64, %[xp]\n\t" \
    "v_mfma_f32_16x16x32_bf16 v[36:39], a[100:103], v[100:103], v[36:39]\n\t" "s_and_b32 s64, s64, 1\n\t" "s_lshl_b32 s64, s64, 13\n\t" "s_waitcnt lgkmcnt(4)\n\t" \
    "v_mfma_f32_16x16x32_bf16 v[40:43], a[96:99], v[104:107], v[40:43]\n\t" "ds_read_b128 v[124:127], v75 offset:768\n\t" "s_lshl_b32 s62, s60, 19\n\t" \
    "v_mfma_f32_16x16x32_bf16 v[44:47], a[100:103], v[104:107], v[44:47]\n\t" "s_add_u32 s62, s62, s64\n\t" "s_add_u32 s62, s62, %[ob]\n\t" "s_waitcnt lgkmcnt(4)\n\t" \
    "v_mfma_f32_16x16x32_bf16 v[32:35], a[104:107], v[108:111], v[32:35]\n\t" "ds_read_b128 v[128:131], v75 offset:33536\n\t" \
    "v_mfma_f32_16x16x32_bf16 v[36:39], a[108:111], v[108:111], v[36:39]\n\t" "s_waitcnt lgkmcnt(4)\n\t" \
    "v_mfma_f32_16x16x32_bf16 v[40:43], a[104:107], v[112:115], v[40:43]\n\t" "ds_read_b128 v[100:103], v72 offset:1024\n\t" \
    "v_mfma_f32_16x16x32_bf16 v[44:47], a[108:111], v[112:115], v[44:47]\n\t" "s_waitcnt lgkmcnt(4)\n\t" \
    "v_mfma_f32_16x16x32_bf16 v[32:35], a[112:115], v[116:119], v[32:35]\n\t" "ds_read_b128 v[104:107], v72 offset:33792\n\t" \
    "v_mfma_f32_16x16x32_bf16 v[36:39], a[116:119], v[116:119], v[36:39]\n\t" "s_waitcnt lgkmcnt(4)\n\t" \
    "v_mfma_f32_16x16x32_bf16 v[40:43], a[112:115], v[120:123], v[40:43]\n\t" "ds_read_b128 v[108:111], v73 offset:1024\n\t" \
    "v_mfma_f32_16x16x32_bf16 v[44:47], a[116:119], v[120:123], v[44:47]\n\t" "s_waitcnt lgkmcnt(4)\n\t" \
    "v_mfma_f32_16x16x32_bf16 v[32:35], a[120:123], v[124:127], v[32:35]\n\t" "ds_read_b128 v[112:115], v73 offset:33792\n\t" \
    "v_mfma_f32_16x16x32_bf16 v[36:39], a[124:127], v[124:127], v[36:39]\n\t" "s_waitcnt lgkmcnt(4)\n\t" \
    "v_mfma_f32_16x16x32_bf16 v[40:43], a[120:123], v[128:131], v[40:43]\n\t" "ds_read_b128 v[116:119], v74 offset:1024\n\t" \
    "v_mfma_f32_16x16x32_bf16 v[44:47], a[124:127], v[128:131], v[44:47]\n\t" "s_waitcnt lgkmcnt(4)\n\t" \
    "v_mfma_f32_16x16x32_bf16 v[32:35], a[128:131], v[100:103], v[32:35]\n\t" "ds_read_b128 v[120:123], v74 offset:33792\n\t" \
    "v_mfma_f32_16x16x32_bf16 v[36:39], a[132:135], v[100:103], v[36:39]\n\t" "s_waitcnt lgkmcnt(4)\n\t" \
    "v_mfma_f32_16x16x32_bf16 v[40:43], a[128:131], v[104:107], v[40:43]\n\t" "ds_read_b128 v[124:127], v75 offset:1024\n\t" \
    "v_mfma_f32_16x16x32_bf16 v[44:47], a[132:135], v[104:107], v[44:47]\n\t" "s_waitcnt lgkmcnt(4)\n\t" \
    "v_mfma_f32_16x16x32_bf16 v[32:35], a[136:139], v[108:111], v[32:35]\n\t" "ds_read_b128 v[128:131], v75 offset:33792\n\t" "s_waitcnt vmcnt(16)\n\t" "s_barrier\n\t" \
    "v_mfma_f32_16x16x32_bf16 v[36:39], a[140:143], v[108:111], v[36:39]\n\t" "v_mov_b32 v98, s60\n\t" "buffer_store_dword v98, %[fo], %[rc], %[fl] offen sc1\n\t" \
    "s_waitcnt lgkmcnt(4)\n\t" "v_mfma_f32_16x16x32_bf16 v[40:43], a[136:139], v[112:115], v[40:43]\n\t" "ds_read_b128 v[100:103], v72 offset:1280\n\t" \
    "v_mfma_f32_16x16x32_bf16 v[44:47], a[140:143], v[112:115], v[44:47]\n\t" "s_waitcnt lgkmcnt(4)\n\t" \
    "v_mfma_f32_16x16x32_bf16 v[32:35], a[144:147], v[116:119], v[32:35]\n\t" "ds_read_b128 v[104:107], v72 offset:34048\n\t" \
    "v_mfma_f32_16x16x32_bf16 v[36:39], a[148:151], v[116:119], v[36:39]\n\t" "s_waitcnt lgkmcnt(4)\n\t" \
    "v_mfma_f32_16x16x32_bf16 v[40:43], a[144:147], v[120:123], v[40:43]\n\t" "ds_read_b128 v[108:111], v73 offset:1280\n\t" \
    "v_mfma_f32_16x16x32_bf16 v[44:47], a[148:151], v[120:123], v[44:47]\n\t" "s_waitcnt lgkmcnt(4)\n\t" \
    "v_mfma_f32_16x16x32_bf16 v[32:35], a[152:155], v[124:127], v[32:35]\n\t" "ds_read_b128 v[112:115], v73 offset:34048\n\t" \
    "v_mfma_f32_16x16x32_bf16 v[36:39], a[156:159], v[124:127], v[36:39]\n\t" "s_waitcnt lgkmcnt(4)\n\t" \
    "v_mfma_f32_16x16x32_bf16 v[40:43], a[152:155], v[128:131], v[40:43]\n\t" "ds_read_b128 v[116:119], v74 offset:1280\n\t" \
    "v_mfma_f32_16x16x32_bf16 v[44:47], a[156:159], v[128:131], v[44:47]\n\t" "s_waitcnt lgkmcnt(4)\n\t" \
    "v_mfma_f32_16x16x32_bf16 v[32:35], a[160:163], v[100:103], v[32:35]\n\t" "ds_read_b128 v[120:123], v74 offset:34048\n\t" \
    "v_mfma_f32_16x16x32_bf16 v[36:39], a[164:167], v[100:103], v[36:39]\n\t" "s_waitcnt lgkmcnt(4)\n\t" \
    "v_mfma_f32_16x16x32_bf16 v[40:43], a[160:163], v[104:107], v[40:43]\n\t" "ds_read_b128 v[124:127], v75 offset:1280\n\t" \
    "v_mfma_f32_16x16x32_bf16 v[44:47], a[164:167], v[104:107], v[44:47]\n\t" "s_waitcnt lgkmcnt(4)\n\t" \
    "v_mfma_f32_16x16x32_bf16 v[32:35], a[168:171], v[108:111], v[32:35]\n\t" "ds_read_b128 v[128:131], v75 offset:34048\n\t" \
    "v_mfma_f32_16x16x32_bf16 v[36:39], a[172:175], v[108:111], v[36:39]\n\t" "s_waitcnt lgkmcnt(4)\n\t" \
    "v_mfma_f32_16x16x32_bf16 v[40:43], a[168:171], v[112:115], v[40:43]\n\t" "ds_read_b128 v[100:103], v72 offset:1536\n\t" \
    "v_mfma_f32_16x16x32_bf16 v[44:47], a[172:175], v[112:115], v[44:47]\n\t" "s_waitcnt lgkmcnt(4)\n\t" \
    "v_mfma_f32_16x16x32_bf16 v[32:35], a[176:179], v[116:119], v[32:35]\n\t" "ds_read_b128 v[104:107], v72 offset:34304\n\t" \
    "v_mfma_f32_16x16x32_bf16 v[36:39], a[180:183], v[116:119], v[36:39]\n\t" "s_waitcnt lgkmcnt(4)\n\t" \
    "v_mfma_f32_16x16x32_bf16 v[40:43], a[176:179], v[120:123], v[40:43]\n\t" "ds_read_b128 v[108:111], v73 offset:1536\n\t" \
    "v_mfma_f32_16x16x32_bf16 v[44:47], a[180:183], v[120:123], v[44:47]\n\t" "s_waitcnt lgkmcnt(4)\n\t" \
    "v_mfma_f32_16x16x32_bf16 v[32:35], a[184:187], v[124:127], v[32:35]\n\t" "ds_read_b128 v[112:115], v73 offset:34304\n\t" \
    "v_mfma_f32_16x16x32_bf16 v[36:39], a[188:191], v[124:127], v[36:39]\n\t" "s_waitcnt lgkmcnt(4)\n\t" \
    "v_mfma_f32_16x16x32_bf16 v[40:43], a[184:187], v[128:131], v[40:43]\n\t" "ds_read_b128 v[116:119], v74 offset:1536\n\t" \
    "v_mfma_f32_16x16x32_bf16 v[44:47], a[188:191], v[128:131], v[44:47]\n\t" "s_waitcnt lgkmcnt(4)\n\t" \
    "v_mfma_f32_16x16x32_bf16 v[32:35], a[192:195], v[100:103], v[32:35]\n\t" "ds_read_b128 v[120:123], v74 offset:34304\n\t" \
    "v_mfma_f32_16x16x32_bf16 v[36:39], a[196:199], v[100:103], v[36:39]\n\t" "s_waitcnt lgkmcnt(4)\n\t" \
    "v_mfma_f32_16x16x32_bf16 v[40:43], a[192:195], v[104:107], v[40:43]\n\t" "ds_read_b128 v[124:127], v75 offset:1536\n\t" \
    "v_mfma_f32_16x16x32_bf16 v[44:47], a[196:199], v[104:107], v[44:47]\n\t" "s_waitcnt lgkmcnt(4)\n\t" \
    "v_mfma_f32_16x16x32_bf16 v[32:35], a[200:203], v[108:111], v[32:35]\n\t" "ds_read_b128 v[128:131], v75 offset:34304\n\t" \
    "v_mfma_f32_16x16x32_bf16 v[36:39], a[204:207], v[108:111], v[36:39]\n\t" "s_waitcnt lgkmcnt(4)\n\t" \
    "v_mfma_f32_16x16x32_bf16 v[40:43], a[200:203], v[112:115], v[40:43]\n\t" "ds_read_b128 v[100:103], v72 offset:1792\n\t" \
    "v_mfma_f32_16x16x32_bf16 v[44:47], a[204:207], v[112:115], v[44:47]\n\t" "s_waitcnt lgkmcnt(4)\n\t" \
    "v_mfma_f32_16x16x32_bf16 v[32:35], a[208:211], v[116:119], v[32:35]\n\t" "ds_read_b128 v[104:107], v72 offset:34560\n\t" \
    "v_mfma_f32_16x16x32_bf16 v[36:39], a[212:215], v[116:119], v[36:39]\n\t" "s_waitcnt lgkmcnt(4)\n\t" \
    "v_mfma_f32_16x16x32_bf16 v[40:43], a[208:211], v[120:123], v[40:43]\n\t" "ds_read_b128 v[108:111], v73 offset:1792\n\t" \
    "v_mfma_f32_16x16x32_bf16 v[44:47], a[212:215], v[120:123], v[44:47]\n\t" "s_waitcnt lgkmcnt(4)\n\t" \
    "v_mfma_f32_16x16x32_bf16 v[32:35], a[216:219], v[124:127], v[32:35]\n\t" "ds_read_b128 v[112:115], v73 offset:34560\n\t" \
    "v_mfma_f32_16x16x32_bf16 v[36:39], a[220:223], v[124:127], v[36:39]\n\t" "s_waitcnt lgkmcnt(4)\n\t" \
    "v_mfma_f32_16x16x32_bf16 v[40:43], a[216:219], v[128:131], v[40:43]\n\t" "ds_read_b128 v[116:119], v74 offset:1792\n\t" \
    "v_mfma_f32_16x16x32_bf16 v[44:47], a[220:223], v[128:131], v[44:47]\n\t" "s_waitcnt lgkmcnt(4)\n\t" \
    "v_mfma_f32_16x16x32_bf16 v[32:35], a[224:227], v[100:103], v[32:35]\n\t" "ds_read_b128 v[120:123], v74 offset:34560\n\t" \
    "v_mfma_f32_16x16x32_bf16 v[36:39], a[228:231], v[100:103], v[36:39]\n\t" "s_waitcnt lgkmcnt(4)\n\t" \
    "v_mfma_f32_16x16x32_bf16 v[40:43], a[224:227], v[104:107], v[40:43]\n\t" "ds_read_b128 v[124:127], v75 offset:1792\n\t" \
    "v_mfma_f32_16x16x32_bf16 v[44:47], a[228:231], v[104:107], v[44:47]\n\t" "s_waitcnt lgkmcnt(4)\n\t" \
    "v_mfma_f32_16x16x32_bf16 v[32:35], a[232:235], v[108:111], v[32:35]\n\t" "ds_read_b128 v[128:131], v75 offset:34560\n\t" \
    "v_mfma_f32_16x16x32_bf16 v[36:39], a[236:239], v[108:111], v[36:39]\n\t" "s_waitcnt lgkmcnt(4)\n\t" \
    "v_mfma_f32_16x16x32_bf16 v[40:43], a[232:235], v[112:115], v[40:43]\n\t" "v_mfma_f32_16x16x32_bf16 v[44:47], a[236:239], v[112:115], v[44:47]\n\t" \
    "s_waitcnt lgkmcnt(3)\n\t" "v_mfma_f32_16x16x32_bf16 v[32:35], a[240:243], v[116:119], v[32:35]\n\t" \
    "v_mfma_f32_16x16x32_bf16 v[36:39], a[244:247], v[116:119], v[36:39]\n\t" "s_waitcnt vmcnt(1) lgkmcnt(0)\n\t" "s_barrier\n\t" XT(82, 83) \
    "v_mfma_f32_16x16x32_bf16 v[40:43], a[240:243], v[120:123], v[40:43]\n\t" "ds_read_b128 v[100:103], v76 offset:0\n\t" \
    "v_mfma_f32_16x16x32_bf16 v[44:47], a[244:247], v[120:123], v[44:47]\n\t" "v_mfma_f32_16x16x32_bf16 v[32:35], a[248:251], v[124:127], v[32:35]\n\t" \
    "ds_read_b128 v[104:107], v76 offset:32768\n\t" "v_mfma_f32_16x16x32_bf16 v[36:39], a[252:255], v[124:127], v[36:39]\n\t" "ds_read_b128 v[108:111], v77 offset:0\n\t" \
    "v_mfma_f32_16x16x32_bf16 v[40:43], a[248:251], v[128:131], v[40:43]\n\t" "ds_read_b128 v[112:115], v77 offset:32768\n\t" "ds_read_b128 v[116:119], v78 offset:0\n\t" \
    "v_mfma_f32_16x16x32_bf16 v[44:47], a[252:255], v[128:131], v[44:47]\n\t" "s_add_u32 s60, s60, 1\n\t" GSUM "s_cmp_eq_u32 s60, 1\n\t" "s_cbranch_scc1 20f\n\t" "11:\n\t" \
    XT(80, 81) "s_waitcnt lgkmcnt(4)\n\t" "v_mfma_f32_16x16x32_bf16 v[48:51], a[0:3], v[100:103], 0\n\t" "ds_read_b128 v[120:123], v78 offset:32768\n\t" \
    "s_add_u32 s61, s60, 1\n\t" "s_and_b32 s61, s61, 63\n\t" "v_mfma_f32_16x16x32_bf16 v[52:55], a[4:7], v[100:103], 0\n\t" "s_lshl_b32 s61, s61, 16\n\t" \
    "s_add_u32 s63, %[lw], 0\n\t" "s_waitcnt lgkmcnt(4)\n\t" "v_mfma_f32_16x16x32_bf16 v[56:59], a[0:3], v[104:107], 0\n\t" "ds_read_b128 v[124:127], v79 offset:0\n\t" \
    "v_mfma_f32_16x16x32_bf16 v[60:63], a[4:7], v[104:107], 0\n\t" "s_waitcnt lgkmcnt(4)\n\t" "v_mfma_f32_16x16x32_bf16 v[48:51], a[8:11], v[108:111], v[48:51]\n\t" \
    "ds_read_b128 v[128:131], v79 offset:32768\n\t" "v_cvt_pk_bf16_f32 v64, v32, v33\n\t" "v_mfma_f32_16x16x32_bf16 v[52:55], a[12:15], v[108:111], v[52:55]\n\t" \
    "v_cvt_pk_bf16_f32 v65, v34, v35\n\t" "v_cvt_pk_bf16_f32 v66, v36, v37\n\t" "s_waitcnt lgkmcnt(4)\n\t" \
    "v_mfma_f32_16x16x32_bf16 v[56:59], a[8:11], v[112:115], v[56:59]\n\t" "ds_read_b128 v[100:103], v76 offset:256\n\t" "v_cvt_pk_bf16_f32 v67, v38, v39\n\t" \
    "v_mfma_f32_16x16x32_bf16 v[60:63], a[12:15], v[112:115], v[60:63]\n\t" "v_cvt_pk_bf16_f32 v68, v40, v41\n\t" "v_cvt_pk_bf16_f32 v69, v42, v43\n\t" \
    "s_waitcnt lgkmcnt(4)\n\t" "v_mfma_f32_16x16x32_bf16 v[48:51], a[16:19], v[116:119], v[48:51]\n\t" "ds_read_b128 v[104:107], v76 offset:33024\n\t" \
    "v_cvt_pk_bf16_f32 v70, v44, v45\n\t" "v_mfma_f32_16x16x32_bf16 v[52:55], a[20:23], v[116:119], v[52:55]\n\t" "v_cvt_pk_bf16_f32 v71, v46, v47\n\t" \
    "buffer_store_dwordx4 v[64:67], v96, %[rs], s62 offen offset:0 sc1\n\t" "s_waitcnt lgkmcnt(4)\n\t" "v_mfma_f32_16x16x32_bf16 v[56:59], a[16:19], v[120:123], v[56:59]\n\t" \
    "ds_read_b128 v[108:111], v77 offset:256\n\t" "v_mfma_f32_16x16x32_bf16 v[60:63], a[20:23], v[120:123], v[60:63]\n\t" "s_waitcnt lgkmcnt(4)\n\t" \
    "v_mfma_f32_16x16x32_bf16 v[48:51], a[24:27], v[124:127], v[48:51]\n\t" "ds_read_b128 v[112:115], v77 offset:33024\n\t" \
    "v_mfma_f32_16x16x32_bf16 v[52:55], a[28:31], v[124:127], v[52:55]\n\t" "buffer_store_dwordx4 v[68:71], v96, %[rs], s62 offen offset:256 sc1\n\t" \
    "s_waitcnt lgkmcnt(4)\n\t" "v_mfma_f32_16x16x32_bf16 v[56:59], a[24:27], v[128:131], v[56:59]\n\t" "ds_read_b128 v[116:119], v78 offset:256\n\t" "s_mov_b32 m0, s63\n\t" \
    "v_mfma_f32_16x16x32_bf16 v[60:63], a[28:31], v[128:131], v[60:63]\n\t" "s_add_u32 s63, s63, 0x800\n\t" "buffer_load_dwordx4 v88, %[rx], s61 offen lds\n\t" \
    "s_waitcnt lgkmcnt(4)\n\t" "v_mfma_f32_16x16x32_bf16 v[48:51], a[32:35], v[100:103], v[48:51]\n\t" "ds_read_b128 v[120:123], v78 offset:33024\n\t" \
    "v_mfma_f32_16x16x32_bf16 v[52:55], a[36:39], v[100:103], v[52:55]\n\t" "buffer_load_dwordx4 v88, %[rx], s61 offen offset:1024 lds\n\t" "s_waitcnt lgkmcnt(4)\n\t" \
    "v_mfma_f32_16x16x32_bf16 v[56:59], a[32:35], v[104:107], v[56:59]\n\t" "ds_read_b128 v[124:127], v79 offset:256\n\t" "s_mov_b32 m0, s63\n\t" \
    "v_mfma_f32_16x16x32_bf16 v[60:63], a[36:39], v[104:107], v[60:63]\n\t" "s_add_u32 s63, s63, 0x800\n\t" "buffer_load_dwordx4 v89, %[rx], s61 offen lds\n\t" \
    "s_waitcnt lgkmcnt(4)\n\t" "v_mfma_f32_16x16x32_bf16 v[48:51], a[40:43], v[108:111], v[48:51]\n\t" "ds_read_b128 v[128:131], v79 offset:33024\n\t" \
    "v_mfma_f32_16x16x32_bf16 v[52:55], a[44:47], v[108:111], v[52:55]\n\t" "buffer_load_dwordx4 v89, %[rx], s61 offen offset:1024 lds\n\t" "s_waitcnt lgkmcnt(4)\n\t" \
    "v_mfma_f32_16x16x32_bf16 v[56:59], a[40:43], v[112:115], v[56:59]\n\t" "ds_read_b128 v[100:103], v76 offset:512\n\t" "s_mov_b32 m0, s63\n\t" \
    "v_mfma_f32_16x16x32_bf16 v[60:63], a[44:47], v[112:115], v[60:63]\n\t" "s_add_u32 s63, s63, 0x800\n\t" "buffer_load_dwordx4 v90, %[rx], s61 offen lds\n\t" \
    "s_waitcnt lgkmcnt(4)\n\t" "v_mfma_f32_16x16x32_bf16 v[48:51], a[48:51], v[116:119], v[48:51]\n\t" "ds_read_b128 v[104:107], v76 offset:33280\n\t" \
    "v_mfma_f32_16x16x32_bf16 v[52:55], a[52:55], v[116:119], v[52:55]\n\t" "buffer_load_dwordx4 v90, %[rx], s61 offen offset:1024 lds\n\t" "s_waitcnt lgkmcnt(4)\n\t" \
    "v_mfma_f32_16x16x32_bf16 v[56:59], a[48:51], v[120:123], v[56:59]\n\t" "ds_read_b128 v[108:111], v77 offset:512\n\t" "s_mov_b32 m0, s63\n\t" \
    "v_mfma_f32_16x16x32_bf16 v[60:63], a[52:55], v[120:123], v[60:63]\n\t" "s_add_u32 s63, s63, 0x800\n\t" "buffer_load_dwordx4 v91, %[rx], s61 offen lds\n\t" \
    "s_waitcnt lgkmcnt(4)\n\t" "v_mfma_f32_16x16x32_bf16 v[48:51], a[56:59], v[124:127], v[48:51]\n\t" "ds_read_b128 v[112:115], v77 offset:33280\n\t" \
    "v_mfma_f32_16x16x32_bf16 v[52:55], a[60:63], v[124:127], v[52:55]\n\t" "buffer_load_dwordx4 v91, %[rx], s61 offen offset:1024 lds\n\t" "s_waitcnt lgkmcnt(4)\n\t" \
    "v_mfma_f32_16x16x32_bf16 v[56:59], a[56:59], v[128:131], v[56:59]\n\t" "ds_read_b128 v[116:119], v78 offset:512\n\t" "s_mov_b32 m0, s63\n\t" \
    "v_mfma_f32_16x16x32_bf16 v[60:63], a[60:63], v[128:131], v[60:63]\n\t" "s_add_u32 s63, s63, 0x800\n\t" "buffer_load_dwordx4 v92, %[rx], s61 offen lds\n\t" \
    "s_waitcnt lgkmcnt(4)\n\t" "v_mfma_f32_16x16x32_bf16 v[48:51], a[64:67], v[100:103], v[48:51]\n\t" "ds_read_b128 v[120:123], v78 offset:33280\n\t" \
    "v_mfma_f32_16x16x32_bf16 v[52:55], a[68:71], v[100:103], v[52:55]\n\t" "buffer_load_dwordx4 v92, %[rx], s61 offen offset:1024 lds\n\t" "s_waitcnt lgkmcnt(4)\n\t" \
    "v_mfma_f32_16x16x32_bf16 v[56:59], a[64:67], v[104:107], v[56:59]\n\t" "ds_read_b128 v[124:127], v79 offset:512\n\t" "s_mov_b32 m0, s63\n\t" \
    "v_mfma_f32_16x16x32_bf16 v[60:63], a[68:71], v[104:107], v[60:63]\n\t" "s_add_u32 s63, s63, 0x800\n\t" "buffer_load_dwordx4 v93, %[rx], s61 offen lds\n\t" \
    "s_waitcnt lgkmcnt(4)\n\t" "v_mfma_f32_16x16x32_bf16 v[48:51], a[72:75], v[108:111], v[48:51]\n\t" "ds_read_b128 v[128:131], v79 offset:33280\n\t" \
    "v_mfma_f32_16x16x32_bf16 v[52:55], a[76:79], v[108:111], v[52:55]\n\t" "buffer_load_dwordx4 v93, %[rx], s61 offen offset:1024 lds\n\t" "s_waitcnt lgkmcnt(4)\n\t" \
    "v_mfma_f32_16x16x32_bf16 v[56:59], a[72:75], v[112:115], v[56:59]\n\t" "ds_read_b128 v[100:103], v76 offset:768\n\t" "s_mov_b32 m0, s63\n\t" \
    "v_mfma_f32_16x16x32_bf16 v[60:63], a[76:79], v[112:115], v[60:63]\n\t" "s_add_u32 s63, s63, 0x800\n\t" "buffer_load_dwordx4 v94, %[rx], s61 offen lds\n\t" \
    "s_waitcnt lgkmcnt(4)\n\t" "v_mfma_f32_16x16x32_bf16 v[48:51], a[80:83], v[116:119], v[48:51]\n\t" "ds_read_b128 v[104:107], v76 offset:33536\n\t" \
    "v_mfma_f32_16x16x32_bf16 v[52:55], a[84:87], v[116:119], v[52:55]\n\t" "buffer_load_dwordx4 v94, %[rx], s61 offen offset:1024 lds\n\t" "s_waitcnt lgkmcnt(4)\n\t" \
    "v_mfma_f32_16x16x32_bf16 v[56:59], a[80:83], v[120:123], v[56:59]\n\t" "ds_read_b128 v[108:111], v77 offset:768\n\t" "s_mov_b32 m0, s63\n\t" \
    "v_mfma_f32_16x16x32_bf16 v[60:63], a[84:87], v[120:123], v[60:63]\n\t" "s_add_u32 s63, s63, 0x800\n\t" "buffer_load_dwordx4 v95, %[rx], s61 offen lds\n\t" \
    "s_waitcnt lgkmcnt(4)\n\t" "v_mfma_f32_16x16x32_bf16 v[48:51], a[88:91], v[124:127], v[48:51]\n\t" "ds_read_b128 v[112:115], v77 offset:33536\n\t" \
    "v_mfma_f32_16x16x32_bf16 v[52:55], a[92:95], v[124:127], v[52:55]\n\t" "buffer_load_dwordx4 v95, %[rx], s61 offen offset:1024 lds\n\t" "s_waitcnt lgkmcnt(4)\n\t" \
    "v_mfma_f32_16x16x32_bf16 v[56:59], a[88:91], v[128:131], v[56:59]\n\t" "ds_read_b128 v[116:119], v78 offset:768\n\t" \
    "v_mfma_f32_16x16x32_bf16 v[60:63], a[92:95], v[128:131], v[60:63]\n\t" "s_waitcnt lgkmcnt(4)\n\t" "v_mfma_f32_16x16x32_bf16 v[48:51], a[96:99], v[100:103], v[48:51]\n\t" \
    "ds_read_b128 v[120:123], v78 offset:33536\n\t" "s_lshr_b32 s64, s60, 2\n\t" "s_xor_b32 s64, s64, %[xp]\n\t" \
    "v_mfma_f32_16x16x32_bf16 v[52:55], a[100:103], v[100:103], v[52:55]\n\t" "s_and_b32 s64, s64, 1\n\t" "s_lshl_b32 s64, s64, 13\n\t" "s_waitcnt lgkmcnt(4)\n\t" \
    "v_mfma_f32_16x16x32_bf16 v[56:59], a[96:99], v[104:107], v[56:59]\n\t" "ds_read_b128 v[124:127], v79 offset:768\n\t" "s_lshl_b32 s62, s60, 19\n\t" \
    "v_mfma_f32_16x16x32_bf16 v[60:63], a[100:103], v[104:107], v[60:63]\n\t" "s_add_u32 s62, s62, s64\n\t" "s_add_u32 s62, s62, %[ob]\n\t" "s_waitcnt lgkmcnt(4)\n\t" \
    "v_mfma_f32_16x16x32_bf16 v[48:51], a[104:107], v[108:111], v[48:51]\n\t" "ds_read_b128 v[128:131], v79 offset:33536\n\t" \
    "v_mfma_f32_16x16x32_bf16 v[52:55], a[108:111], v[108:111], v[52:55]\n\t" "s_waitcnt lgkmcnt(4)\n\t" \
    "v_mfma_f32_16x16x32_bf16 v[56:59], a[104:107], v[112:115], v[56:59]\n\t" "ds_read_b128 v[100:103], v76 offset:1024\n\t" \
    "v_mfma_f32_16x16x32_bf16 v[60:63], a[108:111], v[112:115], v[60:63]\n\t" "s_waitcnt lgkmcnt(4)\n\t" \
    "v_mfma_f32_16x16x32_bf16 v[48:51], a[112:115], v[116:119], v[48:51]\n\t" "ds_read_b128 v[104:107], v76 offset:33792\n\t" \
    "v_mfma_f32_16x16x32_bf16 v[52:55], a[116:119], v[116:119], v[52:55]\n\t" "s_waitcnt lgkmcnt(4)\n\t" \
    "v_mfma_f32_16x16x32_bf16 v[56:59], a[112:115], v[120:123], v[56:59]\n\t" "ds_read_b128 v[108:111], v77 offset:1024\n\t" \
    "v_mfma_f32_16x16x32_bf16 v[60:63], a[116:119], v[120:123], v[60:63]\n\t" "s_waitcnt lgkmcnt(4)\n\t" \
    "v_mfma_f32_16x16x32_bf16 v[48:51], a[120:123], v[124:127], v[48:51]\n\t" "ds_read_b128 v[112:115], v77 offset:33792\n\t" \
    "v_mfma_f32_16x16x32_bf16 v[52:55], a[124:127], v[124:127], v[52:55]\n\t" "s_waitcnt lgkmcnt(4)\n\t" \
    "v_mfma_f32_16x16x32_bf16 v[56:59], a[120:123], v[128:131], v[56:59]\n\t" "ds_read_b128 v[116:119], v78 offset:1024\n\t" \
    "v_mfma_f32_16x16x32_bf16 v[60:63], a[124:127], v[128:131], v[60:63]\n\t" "s_waitcnt lgkmcnt(4)\n\t" \
    "v_mfma_f32_16x16x32_bf16 v[48:51], a[128:131], v[100:103], v[48:51]\n\t" "ds_read_b128 v[120:123], v78 offset:33792\n\t" \
    "v_mfma_f32_16x16x32_bf16 v[52:55], a[132:135], v[100:103], v[52:55]\n\t" "s_waitcnt lgkmcnt(4)\n\t" \
    "v_mfma_f32_16x16x32_bf16 v[56:59], a[128:131], v[104:107], v[56:59]\n\t" "ds_read_b128 v[124:127], v79 offset:1024\n\t" \
    "v_mfma_f32_16x16x32_bf16 v[60:63], a[132:135], v[104:107], v[60:63]\n\t" "s_waitcnt lgkmcnt(4)\n\t" \
    "v_mfma_f32_16x16x32_bf16 v[48:51], a[136:139], v[108:111], v[48:51]\n\t" "ds_read_b128 v[128:131], v79 offset:33792\n\t" "s_waitcnt vmcnt(16)\n\t" "s_barrier\n\t" \
    "v_mfma_f32_16x16x32_bf16 v[52:55], a[140:143], v[108:111], v[52:55]\n\t" "v_mov_b32 v98, s60\n\t" "buffer_store_dword v98, %[fo], %[rc], %[fl] offen sc1\n\t" \
    "s_waitcnt lgkmcnt(4)\n\t" "v_mfma_f32_16x16x32_bf16 v[56:59], a[136:139], v[112:115], v[56:59]\n\t" "ds_read_b128 v[100:103], v76 offset:1280\n\t" \
    "v_mfma_f32_16x16x32_bf16 v[60:63], a[140:143], v[112:115], v[60:63]\n\t" "s_waitcnt lgkmcnt(4)\n\t" \
    "v_mfma_f32_16x16x32_bf16 v[48:51], a[144:147], v[116:119], v[48:51]\n\t" "ds_read_b128 v[104:107], v76 offset:34048\n\t" \
    "v_mfma_f32_16x16x32_bf16 v[52:55], a[148:151], v[116:119], v[52:55]\n\t" "s_waitcnt lgkmcnt(4)\n\t" \
    "v_mfma_f32_16x16x32_bf16 v[56:59], a[144:147], v[120:123], v[56:59]\n\t" "ds_read_b128 v[108:111], v77 offset:1280\n\t" \
    "v_mfma_f32_16x16x32_bf16 v[60:63], a[148:151], v[120:123], v[60:63]\n\t" "s_waitcnt lgkmcnt(4)\n\t" \
    "v_mfma_f32_16x16x32_bf16 v[48:51], a[152:155], v[124:127], v[48:51]\n\t" "ds_read_b128 v[112:115], v77 offset:34048\n\t" \
    "v_mfma_f32_16x16x32_bf16 v[52:55], a[156:159], v[124:127], v[52:55]\n\t" "s_waitcnt lgkmcnt(4)\n\t" \
    "v_mfma_f32_16x16x32_bf16 v[56:59], a[152:155], v[128:131], v[56:59]\n\t" "ds_read_b128 v[116:119], v78 offset:1280\n\t" \
    "v_mfma_f32_16x16x32_bf16 v[60:63], a[156:159], v[128:131], v[60:63]\n\t" "s_waitcnt lgkmcnt(4)\n\t" \
    "v_mfma_f32_16x16x32_bf16 v[48:51], a[160:163], v[100:103], v[48:51]\n\t" "ds_read_b128 v[120:123], v78 offset:34048\n\t" \
    "v_mfma_f32_16x16x32_bf16 v[52:55], a[164:167], v[100:103], v[52:55]\n\t" "s_waitcnt lgkmcnt(4)\n\t" \
    "v_mfma_f32_16x16x32_bf16 v[56:59], a[160:163], v[104:107], v[56:59]\n\t" "ds_read_b128 v[124:127], v79 offset:1280\n\t" \
    "v_mfma_f32_16x16x32_bf16 v[60:63], a[164:167], v[104:107], v[60:63]\n\t" "s_waitcnt lgkmcnt(4)\n\t" \
    "v_mfma_f32_16x16x32_bf16 v[48:51], a[168:171], v[108:111], v[48:51]\n\t" "ds_read_b128 v[128:131], v79 offset:34048\n\t" \
    "v_mfma_f32_16x16x32_bf16 v[52:55], a[172:175], v[108:111], v[52:55]\n\t" "s_waitcnt lgkmcnt(4)\n\t" \
    "v_mfma_f32_16x16x32_bf16 v[56:59], a[168:171], v[112:115], v[56:59]\n\t" "ds_read_b128 v[100:103], v76 offset:1536\n\t" \
    "v_mfma_f32_16x16x32_bf16 v[60:63], a[172:175], v[112:115], v[60:63]\n\t" "s_waitcnt lgkmcnt(4)\n\t" \
    "v_mfma_f32_16x16x32_bf16 v[48:51], a[176:179], v[116:119], v[48:51]\n\t" "ds_read_b128 v[104:107], v76 offset:34304\n\t" \
    "v_mfma_f32_16x16x32_bf16 v[52:55], a[180:183], v[116:119], v[52:55]\n\t" "s_waitcnt lgkmcnt(4)\n\t" \
    "v_mfma_f32_16x16x32_bf16 v[56:59], a[176:179], v[120:123], v[56:59]\n\t" "ds_read_b128 v[108:111], v77 offset:1536\n\t" \
    "v_mfma_f32_16x16x32_bf16 v[60:63], a[180:183], v[120:123], v[60:63]\n\t" "s_waitcnt lgkmcnt(4)\n\t" \
    "v_mfma_f32_16x16x32_bf16 v[48:51], a[184:187], v[124:127], v[48:51]\n\t" "ds_read_b128 v[112:115], v77 offset:34304\n\t" \
    "v_mfma_f32_16x16x32_bf16 v[52:55], a[188:191], v[124:127], v[52:55]\n\t" "s_waitcnt lgkmcnt(4)\n\t" \
    "v_mfma_f32_16x16x32_bf16 v[56:59], a[184:187], v[128:131], v[56:59]\n\t" "ds_read_b128 v[116:119], v78 offset:1536\n\t" \
    "v_mfma_f32_16x16x32_bf16 v[60:63], a[188:191], v[128:131], v[60:63]\n\t" "s_waitcnt lgkmcnt(4)\n\t" \
    "v_mfma_f32_16x16x32_bf16 v[48:51], a[192:195], v[100:103], v[48:51]\n\t" "ds_read_b128 v[120:123], v78 offset:34304\n\t" \
    "v_mfma_f32_16x16x32_bf16 v[52:55], a[196:199], v[100:103], v[52:55]\n\t" "s_waitcnt lgkmcnt(4)\n\t" \
    "v_mfma_f32_16x16x32_bf16 v[56:59], a[192:195], v[104:107], v[56:59]\n\t" "ds_read_b128 v[124:127], v79 offset:1536\n\t" \
    "v_mfma_f32_16x16x32_bf16 v[60:63], a[196:199], v[104:107], v[60:63]\n\t" "s_waitcnt lgkmcnt(4)\n\t" \
    "v_mfma_f32_16x16x32_bf16 v[48:51], a[200:203], v[108:111], v[48:51]\n\t" "ds_read_b128 v[128:131], v79 offset:34304\n\t" \
    "v_mfma_f32_16x16x32_bf16 v[52:55], a[204:207], v[108:111], v[52:55]\n\t" "s_waitcnt lgkmcnt(4)\n\t" \
    "v_mfma_f32_16x16x32_bf16 v[56:59], a[200:203], v[112:115], v[56:59]\n\t" "ds_read_b128 v[100:103], v76 offset:1792\n\t" \
    "v_mfma_f32_16x16x32_bf16 v[60:63], a[204:207], v[112:115], v[60:63]\n\t" "s_waitcnt lgkmcnt(4)\n\t" \
    "v_mfma_f32_16x16x32_bf16 v[48:51], a[208:211], v[116:119], v[48:51]\n\t" "ds_read_b128 v[104:107], v76 offset:34560\n\t" \
    "v_mfma_f32_16x16x32_bf16 v[52:55], a[212:215], v[116:119], v[52:55]\n\t" "s_waitcnt lgkmcnt(4)\n\t" \
    "v_mfma_f32_16x16x32_bf16 v[56:59], a[208:211], v[120:123], v[56:59]\n\t" "ds_read_b128 v[108:111], v77 offset:1792\n\t" \
    "v_mfma_f32_16x16x32_bf16 v[60:63], a[212:215], v[120:123], v[60:63]\n\t" "s_waitcnt lgkmcnt(4)\n\t" \
    "v_mfma_f32_16x16x32_bf16 v[48:51], a[216:219], v[124:127], v[48:51]\n\t" "ds_read_b128 v[112:115], v77 offset:34560\n\t" \
    "v_mfma_f32_16x16x32_bf16 v[52:55], a[220:223], v[124:127], v[52:55]\n\t" "s_waitcnt lgkmcnt(4)\n\t" \
    "v_mfma_f32_16x16x32_bf16 v[56:59], a[216:219], v[128:131], v[56:59]\n\t" "ds_read_b128 v[116:119], v78 offset:1792\n\t" \
    "v_mfma_f32_16x16x32_bf16 v[60:63], a[220:223], v[128:131], v[60:63]\n\t" "s_waitcnt lgkmcnt(4)\n\t" \
    "v_mfma_f32_16x16x32_bf16 v[48:51], a[224:227], v[100:103], v[48:51]\n\t" "ds_read_b128 v[120:123], v78 offset:34560\n\t" \
    "v_mfma_f32_16x16x32_bf16 v[52:55], a[228:231], v[100:103], v[52:55]\n\t" "s_waitcnt lgkmcnt(4)\n\t" \
    "v_mfma_f32_16x16x32_bf16 v[56:59], a[224:227], v[104:107], v[56:59]\n\t" "ds_read_b128 v[124:127], v79 offset:1792\n\t" \
    "v_mfma_f32_16x16x32_bf16 v[60:63], a[228:231], v[104:107], v[60:63]\n\t" "s_waitcnt lgkmcnt(4)\n\t" \
    "v_mfma_f32_16x16x32_bf16 v[48:51], a[232:235], v[108:111], v[48:51]\n\t" "ds_read_b128 v[128:131], v79 offset:34560\n\t" \
    "v_mfma_f32_16x16x32_bf16 v[52:55], a[236:239], v[108:111], v[52:55]\n\t" "s_waitcnt lgkmcnt(4)\n\t" \
    "v_mfma_f32_16x16x32_bf16 v[56:59], a[232:235], v[112:115], v[56:59]\n\t" "v_mfma_f32_16x16x32_bf16 v[60:63], a[236:239], v[112:115], v[60:63]\n\t" \
    "s_waitcnt lgkmcnt(3)\n\t" "v_mfma_f32_16x16x32_bf16 v[48:51], a[240:243], v[116:119], v[48:51]\n\t" \
    "v_mfma_f32_16x16x32_bf16 v[52:55], a[244:247], v[116:119], v[52:55]\n\t" "s_waitcnt vmcnt(1) lgkmcnt(0)\n\t" "s_barrier\n\t" XT(82, 83) \
    "v_mfma_f32_16x16x32_bf16 v[56:59], a[240:243], v[120:123], v[56:59]\n\t" "ds_read_b128 v[100:103], v72 offset:0\n\t" \
    "v_mfma_f32_16x16x32_bf16 v[60:63], a[244:247], v[120:123], v[60:63]\n\t" "v_mfma_f32_16x16x32_bf16 v[48:51], a[248:251], v[124:127], v[48:51]\n\t" \
    "ds_read_b128 v[104:107], v72 offset:32768\n\t" "v_mfma_f32_16x16x32_bf16 v[52:55], a[252:255], v[124:127], v[52:55]\n\t" "ds_read_b128 v[108:111], v73 offset:0\n\t" \
    "v_mfma_f32_16x16x32_bf16 v[56:59], a[248:251], v[128:131], v[56:59]\n\t" "ds_read_b128 v[112:115], v73 offset:32768\n\t" "ds_read_b128 v[116:119], v74 offset:0\n\t" \
    "v_mfma_f32_16x16x32_bf16 v[60:63], a[252:255], v[128:131], v[60:63]\n\t" "s_add_u32 s60, s60, 1\n\t" GSUM "s_cmp_lt_u32 s60, 64\n\t" "s_cbranch_scc1 10b\n\t" \
    "s_nop 7\n\t" "v_cvt_pk_bf16_f32 v64, v48, v49\n\t" "v_cvt_pk_bf16_f32 v65, v50, v51\n\t" "v_cvt_pk_bf16_f32 v66, v52, v53\n\t" "v_cvt_pk_bf16_f32 v67, v54, v55\n\t" \
    "v_cvt_pk_bf16_f32 v68, v56, v57\n\t" "v_cvt_pk_bf16_f32 v69, v58, v59\n\t" "v_cvt_pk_bf16_f32 v70, v60, v61\n\t" "v_cvt_pk_bf16_f32 v71, v62, v63\n\t" \
    "buffer_store_dwordx4 v[64:67], v96, %[rs], s62 offen offset:0 sc1\n\t" "buffer_store_dwordx4 v[68:71], v96, %[rs], s62 offen offset:256 sc1\n\t" "s_waitcnt vmcnt(0)\n\t" \
    "s_barrier\n\t" "v_mov_b32 v98, s60\n\t" "buffer_store_dword v98, %[fo], %[rc], %[fl] offen sc1\n\t" "s_waitcnt lgkmcnt(0)\n\t" "s_branch 30f\n\t" "20:\n\t" \
    "v_mov_b32 v96, %[so]\n\t" "s_nop 7\n\t" "v_cvt_pk_bf16_f32 v64, v32, v33\n\t" "v_cvt_pk_bf16_f32 v65, v34, v35\n\t" "v_cvt_pk_bf16_f32 v66, v36, v37\n\t" \
    "v_cvt_pk_bf16_f32 v67, v38, v39\n\t" "v_cvt_pk_bf16_f32 v68, v40, v41\n\t" "v_cvt_pk_bf16_f32 v69, v42, v43\n\t" "v_cvt_pk_bf16_f32 v70, v44, v45\n\t" \
    "v_cvt_pk_bf16_f32 v71, v46, v47\n\t" "buffer_store_dwordx4 v[64:67], v96, %[rs], s62 offen offset:0 sc1\n\t" \
    "buffer_store_dwordx4 v[68:71], v96, %[rs], s62 offen offset:256 sc1\n\t" "s_waitcnt vmcnt(0)\n\t" "s_barrier\n\t" "v_mov_b32 v98, s60\n\t" \
    "buffer_store_dword v98, %[fo], %[rc], %[fl] offen sc1\n\t" "s_branch 11b\n\t" "30:\n\t"
#define ZC_LOOP \
    "v_mov_b64 v[124:125], -1\n\t" "v_mov_b64 v[126:127], -1\n\t" "s_mov_b32 s60, 0\n\t" "s_mov_b32 s61, 0\n\t" "s_mov_b32 s62, 0\n\t" "s_mov_b32 s63, 0x7f000000\n\t" \
    "s_cmp_ge_u32 s61, %[z0]\n\t" "s_cbranch_scc1 52f\n\t" "51:\n\t" "buffer_load_dword v120, %[fo], %[rc], %[fv] offen sc1\n\t" \
    "buffer_load_dword v121, %[fo], %[rc], %[vd] offen sc1\n\t" "s_waitcnt vmcnt(0)\n\t" "v_readfirstlane_b32 s61, v120\n\t" "v_readfirstlane_b32 s66, v121\n\t" "s_nop 3\n\t" \
    "s_cmp_ge_u32 s61, %[z0]\n\t" "s_cbranch_scc1 52f\n\t" "s_bitcmp1_b32 s66, 1\n\t" "s_cbranch_scc1 90f\n\t" "s_cmp_eq_u32 %[bd], 0\n\t" "s_cbranch_scc1 52f\n\t" \
    "s_sub_u32 %[bd], %[bd], 1\n\t" "s_sleep 2\n\t" "s_branch 51b\n\t" "52:\n\t" "s_lshr_b32 s66, s60, 2\n\t" "s_xor_b32 s66, s66, %[xp]\n\t" "s_and_b32 s66, s66, 1\n\t" \
    "s_lshl_b32 s66, s66, 13\n\t" "s_lshl_b32 s64, s60, 19\n\t" "s_add_u32 s64, s64, s66\n\t" "s_add_u32 s64, s64, %[vb]\n\t" \
    "buffer_load_dwordx4 v[16:19], %[ao], %[rs], s64 offen offset:0 sc1\n\t" "buffer_load_dwordx4 v[20:23], %[ao], %[rs], s64 offen offset:256 sc1\n\t" \
    "buffer_load_dwordx4 v[24:27], %[ao], %[rs], s64 offen offset:2048 sc1\n\t" "buffer_load_dwordx4 v[28:31], %[ao], %[rs], s64 offen offset:2304 sc1\n\t" \
    "buffer_load_dwordx4 v[32:35], %[a1], %[rs], s64 offen offset:0 sc1\n\t" "buffer_load_dwordx4 v[36:39], %[a1], %[rs], s64 offen offset:256 sc1\n\t" \
    "buffer_load_dwordx4 v[40:43], %[a1], %[rs], s64 offen offset:2048 sc1\n\t" "buffer_load_dwordx4 v[44:47], %[a1], %[rs], s64 offen offset:2304 sc1\n\t" \
    "s_waitcnt vmcnt(0)\n\t" "10:\n\t" XT(80, 81) "s_add_u32 s66, s62, %[rg]\n\t" "s_cmp_lt_u32 s66, s60\n\t" "s_cbranch_scc1 60f\n\t" "63:\n\t" \
    "v_mfma_f32_16x16x32_bf16 v[80:83], v[16:19], a[0:3], 0\n\t" "s_add_u32 s66, s60, 1\n\t" "s_and_b32 s66, s66, %[rm]\n\t" \
    "v_mfma_f32_16x16x32_bf16 v[84:87], v[20:23], a[0:3], 0\n\t" "s_lshl_b32 s68, s66, 20\n\t" "s_lshr_b32 s66, s66, 1\n\t" \
    "v_mfma_f32_16x16x32_bf16 v[80:83], v[24:27], a[4:7], v[80:83]\n\t" "s_xor_b32 s66, s66, %[xp]\n\t" "s_andn2_b32 s66, 1, s66\n\t" \
    "v_mfma_f32_16x16x32_bf16 v[84:87], v[28:31], a[4:7], v[84:87]\n\t" "s_lshl_b32 s66, s66, 13\n\t" "s_add_u32 s68, s68, s66\n\t" \
    "v_mfma_f32_16x16x32_bf16 v[80:83], v[32:35], a[8:11], v[80:83]\n\t" "v_cvt_pk_bf16_f32 v112, v96, v97\n\t" \
    "v_mfma_f32_16x16x32_bf16 v[84:87], v[36:39], a[8:11], v[84:87]\n\t" "v_cvt_pk_bf16_f32 v113, v98, v99\n\t" \
    "v_mfma_f32_16x16x32_bf16 v[80:83], v[40:43], a[12:15], v[80:83]\n\t" "v_cvt_pk_bf16_f32 v114, v100, v101\n\t" \
    "v_mfma_f32_16x16x32_bf16 v[84:87], v[44:47], a[12:15], v[84:87]\n\t" "v_cvt_pk_bf16_f32 v115, v102, v103\n\t" \
    "v_mfma_f32_16x16x32_bf16 v[88:91], v[16:19], a[16:19], 0\n\t" "v_cvt_pk_bf16_f32 v116, v104, v105\n\t" "v_mfma_f32_16x16x32_bf16 v[92:95], v[20:23], a[16:19], 0\n\t" \
    "v_cvt_pk_bf16_f32 v117, v106, v107\n\t" "v_mfma_f32_16x16x32_bf16 v[88:91], v[24:27], a[20:23], v[88:91]\n\t" \
    "buffer_store_dwordx4 v[112:115], %[z1], %[rs], s63 offen sc1\n\t" "v_mfma_f32_16x16x32_bf16 v[92:95], v[28:31], a[20:23], v[92:95]\n\t" \
    "v_mfma_f32_16x16x32_bf16 v[88:91], v[32:35], a[24:27], v[88:91]\n\t" "v_cvt_pk_bf16_f32 v118, v108, v109\n\t" \
    "v_mfma_f32_16x16x32_bf16 v[92:95], v[36:39], a[24:27], v[92:95]\n\t" "v_cvt_pk_bf16_f32 v119, v110, v111\n\t" \
    "v_mfma_f32_16x16x32_bf16 v[88:91], v[40:43], a[28:31], v[88:91]\n\t" "buffer_store_dwordx4 v[116:119], %[z2], %[rs], s63 offen sc1\n\t" "s_and_b32 s63, s60, %[rm]\n\t" \
    "v_mfma_f32_16x16x32_bf16 v[92:95], v[44:47], a[28:31], v[92:95]\n\t" "s_lshl_b32 s63, s63, 22\n\t" "s_add_u32 s63, s63, %[zb]\n\t" \
    "v_mfma_f32_16x16x32_bf16 v[96:99], v[16:19], a[32:35], 0\n\t" "buffer_load_dword v120, %[fo], %[rc], %[fv] offen sc1\n\t" \
    "v_mfma_f32_16x16x32_bf16 v[100:103], v[20:23], a[32:35], 0\n\t" "v_mfma_f32_16x16x32_bf16 v[96:99], v[24:27], a[36:39], v[96:99]\n\t" \
    "buffer_load_dword v121, %[fo], %[rc], %[fs] offen sc1\n\t" "v_mfma_f32_16x16x32_bf16 v[100:103], v[28:31], a[36:39], v[100:103]\n\t" \
    "v_mfma_f32_16x16x32_bf16 v[96:99], v[32:35], a[40:43], v[96:99]\n\t" "v_cvt_pk_bf16_f32 v112, v80, v81\n\t" \
    "v_mfma_f32_16x16x32_bf16 v[100:103], v[36:39], a[40:43], v[100:103]\n\t" "v_cvt_pk_bf16_f32 v113, v82, v83\n\t" \
    "v_mfma_f32_16x16x32_bf16 v[96:99], v[40:43], a[44:47], v[96:99]\n\t" "v_cvt_pk_bf16_f32 v114, v84, v85\n\t" \
    "v_mfma_f32_16x16x32_bf16 v[100:103], v[44:47], a[44:47], v[100:103]\n\t" "v_cvt_pk_bf16_f32 v115, v86, v87\n\t" \
    "v_mfma_f32_16x16x32_bf16 v[104:107], v[16:19], a[48:51], 0\n\t" "v_cvt_pk_bf16_f32 v116, v88, v89\n\t" "v_mfma_f32_16x16x32_bf16 v[108:111], v[20:23], a[48:51], 0\n\t" \
    "v_cvt_pk_bf16_f32 v117, v90, v91\n\t" "v_mfma_f32_16x16x32_bf16 v[104:107], v[24:27], a[52:55], v[104:107]\n\t" \
    "buffer_store_dwordx4 v[112:115], %[z1], %[rs], s63 offen sc1\n\t" "v_mfma_f32_16x16x32_bf16 v[108:111], v[28:31], a[52:55], v[108:111]\n\t" \
    "v_mfma_f32_16x16x32_bf16 v[104:107], v[32:35], a[56:59], v[104:107]\n\t" "v_cvt_pk_bf16_f32 v118, v92, v93\n\t" \
    "v_mfma_f32_16x16x32_bf16 v[108:111], v[36:39], a[56:59], v[108:111]\n\t" "v_cvt_pk_bf16_f32 v119, v94, v95\n\t" \
    "v_mfma_f32_16x16x32_bf16 v[104:107], v[40:43], a[60:63], v[104:107]\n\t" "buffer_store_dwordx4 v[116:119], %[z2], %[rs], s63 offen sc1\n\t" \
    "v_mfma_f32_16x16x32_bf16 v[108:111], v[44:47], a[60:63], v[108:111]\n\t" "s_add_u32 s63, s63, 0x20000\n\t" "v_mfma_f32_16x16x32_bf16 v[80:83], v[16:19], a[64:67], 0\n\t" \
    "s_add_u32 s65, s60, 1\n\t" "s_lshr_b32 s66, s65, 2\n\t" "v_mfma_f32_16x16x32_bf16 v[84:87], v[20:23], a[64:67], 0\n\t" "s_xor_b32 s66, s66, %[xp]\n\t" \
    "s_and_b32 s66, s66, 1\n\t" "v_mfma_f32_16x16x32_bf16 v[80:83], v[24:27], a[68:71], v[80:83]\n\t" \
    "buffer_store_dwordx4 v[124:127], %[uo], %[rs], s68 offen offset:0 sc1\n\t" "v_mfma_f32_16x16x32_bf16 v[84:87], v[28:31], a[68:71], v[84:87]\n\t" \
    "v_mfma_f32_16x16x32_bf16 v[80:83], v[32:35], a[72:75], v[80:83]\n\t" "v_cvt_pk_bf16_f32 v112, v96, v97\n\t" \
    "v_mfma_f32_16x16x32_bf16 v[84:87], v[36:39], a[72:75], v[84:87]\n\t" "v_cvt_pk_bf16_f32 v113, v98, v99\n\t" \
    "v_mfma_f32_16x16x32_bf16 v[80:83], v[40:43], a[76:79], v[80:83]\n\t" "v_cvt_pk_bf16_f32 v114, v100, v101\n\t" \
    "v_mfma_f32_16x16x32_bf16 v[84:87], v[44:47], a[76:79], v[84:87]\n\t" "v_cvt_pk_bf16_f32 v115, v102, v103\n\t" \
    "v_mfma_f32_16x16x32_bf16 v[88:91], v[16:19], a[80:83], 0\n\t" "v_cvt_pk_bf16_f32 v116, v104, v105\n\t" "v_mfma_f32_16x16x32_bf16 v[92:95], v[20:23], a[80:83], 0\n\t" \
    "v_cvt_pk_bf16_f32 v117, v106, v107\n\t" "v_mfma_f32_16x16x32_bf16 v[88:91], v[24:27], a[84:87], v[88:91]\n\t" \
    "buffer_store_dwordx4 v[112:115], %[z1], %[rs], s63 offen sc1\n\t" "v_mfma_f32_16x16x32_bf16 v[92:95], v[28:31], a[84:87], v[92:95]\n\t" \
    "v_mfma_f32_16x16x32_bf16 v[88:91], v[32:35], a[88:91], v[88:91]\n\t" "v_cvt_pk_bf16_f32 v118, v108, v109\n\t" \
    "v_mfma_f32_16x16x32_bf16 v[92:95], v[36:39], a[88:91], v[92:95]\n\t" "v_cvt_pk_bf16_f32 v119, v110, v111\n\t" \
    "v_mfma_f32_16x16x32_bf16 v[88:91], v[40:43], a[92:95], v[88:91]\n\t" "buffer_store_dwordx4 v[116:119], %[z2], %[rs], s63 offen sc1\n\t" \
    "v_mfma_f32_16x16x32_bf16 v[92:95], v[44:47], a[92:95], v[92:95]\n\t" "s_add_u32 s63, s63, 0x20000\n\t" "v_mfma_f32_16x16x32_bf16 v[96:99], v[16:19], a[96:99], 0\n\t" \
    "s_lshl_b32 s66, s66, 13\n\t" "s_lshl_b32 s64, s65, 19\n\t" "v_mfma_f32_16x16x32_bf16 v[100:103], v[20:23], a[96:99], 0\n\t" "s_add_u32 s64, s64, s66\n\t" \
    "s_add_u32 s64, s64, %[vb]\n\t" "v_mfma_f32_16x16x32_bf16 v[96:99], v[24:27], a[100:103], v[96:99]\n\t" \
    "buffer_store_dwordx4 v[124:127], %[uo], %[rs], s68 offen offset:1024 sc1\n\t" "v_mfma_f32_16x16x32_bf16 v[100:103], v[28:31], a[100:103], v[100:103]\n\t" \
    "v_mfma_f32_16x16x32_bf16 v[96:99], v[32:35], a[104:107], v[96:99]\n\t" "v_cvt_pk_bf16_f32 v112, v80, v81\n\t" \
    "v_mfma_f32_16x16x32_bf16 v[100:103], v[36:39], a[104:107], v[100:103]\n\t" "v_cvt_pk_bf16_f32 v113, v82, v83\n\t" \
    "v_mfma_f32_16x16x32_bf16 v[96:99], v[40:43], a[108:111], v[96:99]\n\t" "v_cvt_pk_bf16_f32 v114, v84, v85\n\t" \
    "v_mfma_f32_16x16x32_bf16 v[100:103], v[44:47], a[108:111], v[100:103]\n\t" "v_cvt_pk_bf16_f32 v115, v86, v87\n\t" \
    "v_mfma_f32_16x16x32_bf16 v[104:107], v[16:19], a[112:115], 0\n\t" "v_cvt_pk_bf16_f32 v116, v88, v89\n\t" \
    "v_mfma_f32_16x16x32_bf16 v[108:111], v[20:23], a[112:115], 0\n\t" "v_cvt_pk_bf16_f32 v117, v90, v91\n\t" \
    "v_mfma_f32_16x16x32_bf16 v[104:107], v[24:27], a[116:119], v[104:107]\n\t" "buffer_store_dwordx4 v[112:115], %[z1], %[rs], s63 offen sc1\n\t" \
    "v_mfma_f32_16x16x32_bf16 v[108:111], v[28:31], a[116:119], v[108:111]\n\t" "v_mfma_f32_16x16x32_bf16 v[104:107], v[32:35], a[120:123], v[104:107]\n\t" \
    "v_cvt_pk_bf16_f32 v118, v92, v93\n\t" "v_mfma_f32_16x16x32_bf16 v[108:111], v[36:39], a[120:123], v[108:111]\n\t" "v_cvt_pk_bf16_f32 v119, v94, v95\n\t" \
    "v_mfma_f32_16x16x32_bf16 v[104:107], v[40:43], a[124:127], v[104:107]\n\t" "buffer_store_dwordx4 v[116:119], %[z2], %[rs], s63 offen sc1\n\t" \
    "v_mfma_f32_16x16x32_bf16 v[108:111], v[44:47], a[124:127], v[108:111]\n\t" "s_add_u32 s63, s63, 0x20000\n\t" \
    "v_mfma_f32_16x16x32_bf16 v[80:83], v[16:19], a[128:131], 0\n\t" "s_waitcnt vmcnt(8)\n\t" "v_readfirstlane_b32 s66, v120\n\t" "v_readfirstlane_b32 s67, v121\n\t" \
    "v_mfma_f32_16x16x32_bf16 v[84:87], v[20:23], a[128:131], 0\n\t" "v_add_u32 v122, s60, %[at]\n\t" "buffer_store_dword v122, %[fo], %[rc], %[fz] offen sc1\n\t" \
    "v_mfma_f32_16x16x32_bf16 v[80:83], v[24:27], a[132:135], v[80:83]\n\t" "s_max_u32 s61, s61, s66\n\t" "s_max_u32 s62, s62, s67\n\t" "s_cmp_gt_u32 s61, s65\n\t" \
    "v_mfma_f32_16x16x32_bf16 v[84:87], v[28:31], a[132:135], v[84:87]\n\t" "s_cselect_b32 s65, 1, 0\n\t" "s_cselect_b32 s64, s64, 0x7f000000\n\t" \
    "v_mfma_f32_16x16x32_bf16 v[80:83], v[32:35], a[136:139], v[80:83]\n\t" "buffer_load_dwordx4 v[48:51], %[ao], %[rs], s64 offen offset:0 sc1\n\t" \
    "v_cvt_pk_bf16_f32 v112, v96, v97\n\t" "v_mfma_f32_16x16x32_bf16 v[84:87], v[36:39], a[136:139], v[84:87]\n\t" "v_cvt_pk_bf16_f32 v113, v98, v99\n\t" \
    "v_mfma_f32_16x16x32_bf16 v[80:83], v[40:43], a[140:143], v[80:83]\n\t" "buffer_load_dwordx4 v[52:55], %[ao], %[rs], s64 offen offset:256 sc1\n\t" \
    "v_cvt_pk_bf16_f32 v114, v100, v101\n\t" "v_mfma_f32_16x16x32_bf16 v[84:87], v[44:47], a[140:143], v[84:87]\n\t" "v_cvt_pk_bf16_f32 v115, v102, v103\n\t" \
    "v_mfma_f32_16x16x32_bf16 v[88:91], v[16:19], a[144:147], 0\n\t" "buffer_load_dwordx4 v[56:59], %[ao], %[rs], s64 offen offset:2048 sc1\n\t" \
    "v_cvt_pk_bf16_f32 v116, v104, v105\n\t" "v_mfma_f32_16x16x32_bf16 v[92:95], v[20:23], a[144:147], 0\n\t" "v_cvt_pk_bf16_f32 v117, v106, v107\n\t" \
    "v_mfma_f32_16x16x32_bf16 v[88:91], v[24:27], a[148:151], v[88:91]\n\t" "buffer_load_dwordx4 v[60:63], %[ao], %[rs], s64 offen offset:2304 sc1\n\t" \
    "v_mfma_f32_16x16x32_bf16 v[92:95], v[28:31], a[148:151], v[92:95]\n\t" "buffer_store_dwordx4 v[112:115], %[z1], %[rs], s63 offen sc1\n\t" \
    "v_mfma_f32_16x16x32_bf16 v[88:91], v[32:35], a[152:155], v[88:91]\n\t" "buffer_load_dwordx4 v[64:67], %[a1], %[rs], s64 offen offset:0 sc1\n\t" \
    "v_cvt_pk_bf16_f32 v118, v108, v109\n\t" "v_mfma_f32_16x16x32_bf16 v[92:95], v[36:39], a[152:155], v[92:95]\n\t" "v_cvt_pk_bf16_f32 v119, v110, v111\n\t" \
    "v_mfma_f32_16x16x32_bf16 v[88:91], v[40:43], a[156:159], v[88:91]\n\t" "buffer_load_dwordx4 v[68:71], %[a1], %[rs], s64 offen offset:256 sc1\n\t" \
    "buffer_store_dwordx4 v[116:119], %[z2], %[rs], s63 offen sc1\n\t" "v_mfma_f32_16x16x32_bf16 v[92:95], v[44:47], a[156:159], v[92:95]\n\t" \
    "s_add_u32 s63, s63, 0x20000\n\t" "v_mfma_f32_16x16x32_bf16 v[96:99], v[16:19], a[160:163], 0\n\t" \
    "buffer_load_dwordx4 v[72:75], %[a1], %[rs], s64 offen offset:2048 sc1\n\t" "v_mfma_f32_16x16x32_bf16 v[100:103], v[20:23], a[160:163], 0\n\t" \
    "v_mfma_f32_16x16x32_bf16 v[96:99], v[24:27], a[164:167], v[96:99]\n\t" "buffer_load_dwordx4 v[76:79], %[a1], %[rs], s64 offen offset:2304 sc1\n\t" \
    "v_mfma_f32_16x16x32_bf16 v[100:103], v[28:31], a[164:167], v[100:103]\n\t" "v_mfma_f32_16x16x32_bf16 v[96:99], v[32:35], a[168:171], v[96:99]\n\t" \
    "v_cvt_pk_bf16_f32 v112, v80, v81\n\t" "v_mfma_f32_16x16x32_bf16 v[100:103], v[36:39], a[168:171], v[100:103]\n\t" "v_cvt_pk_bf16_f32 v113, v82, v83\n\t" \
    "v_mfma_f32_16x16x32_bf16 v[96:99], v[40:43], a[172:175], v[96:99]\n\t" "v_cvt_pk_bf16_f32 v114, v84, v85\n\t" \
    "v_mfma_f32_16x16x32_bf16 v[100:103], v[44:47], a[172:175], v[100:103]\n\t" "v_cvt_pk_bf16_f32 v115, v86, v87\n\t" \
    "v_mfma_f32_16x16x32_bf16 v[104:107], v[16:19], a[176:179], 0\n\t" "v_cvt_pk_bf16_f32 v116, v88, v89\n\t" \
    "v_mfma_f32_16x16x32_bf16 v[108:111], v[20:23], a[176:179], 0\n\t" "v_cvt_pk_bf16_f32 v117, v90, v91\n\t" \
    "v_mfma_f32_16x16x32_bf16 v[104:107], v[24:27], a[180:183], v[104:107]\n\t" "buffer_store_dwordx4 v[112:115], %[z1], %[rs], s63 offen sc1\n\t" \
    "v_mfma_f32_16x16x32_bf16 v[108:111], v[28:31], a[180:183], v[108:111]\n\t" "v_mfma_f32_16x16x32_bf16 v[104:107], v[32:35], a[184:187], v[104:107]\n\t" \
    "v_cvt_pk_bf16_f32 v118, v92, v93\n\t" "v_mfma_f32_16x16x32_bf16 v[108:111], v[36:39], a[184:187], v[108:111]\n\t" "v_cvt_pk_bf16_f32 v119, v94, v95\n\t" \
    "v_mfma_f32_16x16x32_bf16 v[104:107], v[40:43], a[188:191], v[104:107]\n\t" "buffer_store_dwordx4 v[116:119], %[z2], %[rs], s63 offen sc1\n\t" \
    "v_mfma_f32_16x16x32_bf16 v[108:111], v[44:47], a[188:191], v[108:111]\n\t" "s_add_u32 s63, s63, 0x20000\n\t" \
    "v_mfma_f32_16x16x32_bf16 v[80:83], v[16:19], a[192:195], 0\n\t" "v_mfma_f32_16x16x32_bf16 v[84:87], v[20:23], a[192:195], 0\n\t" \
    "v_mfma_f32_16x16x32_bf16 v[80:83], v[24:27], a[196:199], v[80:83]\n\t" "buffer_store_dwordx4 v[124:127], %[uo], %[rs], s68 offen offset:2048 sc1\n\t" \
    "v_mfma_f32_16x16x32_bf16 v[84:87], v[28:31], a[196:199], v[84:87]\n\t" "v_mfma_f32_16x16x32_bf16 v[80:83], v[32:35], a[200:203], v[80:83]\n\t" \
    "v_cvt_pk_bf16_f32 v112, v96, v97\n\t" "v_mfma_f32_16x16x32_bf16 v[84:87], v[36:39], a[200:203], v[84:87]\n\t" "v_cvt_pk_bf16_f32 v113, v98, v99\n\t" \
    "v_mfma_f32_16x16x32_bf16 v[80:83], v[40:43], a[204:207], v[80:83]\n\t" "v_cvt_pk_bf16_f32 v114, v100, v101\n\t" \
    "v_mfma_f32_16x16x32_bf16 v[84:87], v[44:47], a[204:207], v[84:87]\n\t" "v_cvt_pk_bf16_f32 v115, v102, v103\n\t" \
    "v_mfma_f32_16x16x32_bf16 v[88:91], v[16:19], a[208:211], 0\n\t" "v_cvt_pk_bf16_f32 v116, v104, v105\n\t" "v_mfma_f32_16x16x32_bf16 v[92:95], v[20:23], a[208:211], 0\n\t" \
    "v_cvt_pk_bf16_f32 v117, v106, v107\n\t" "v_mfma_f32_16x16x32_bf16 v[88:91], v[24:27], a[212:215], v[88:91]\n\t" \
    "buffer_store_dwordx4 v[112:115], %[z1], %[rs], s63 offen sc1\n\t" "v_mfma_f32_16x16x32_bf16 v[92:95], v[28:31], a[212:215], v[92:95]\n\t" \
    "v_mfma_f32_16x16x32_bf16 v[88:91], v[32:35], a[216:219], v[88:91]\n\t" "v_cvt_pk_bf16_f32 v118, v108, v109\n\t" \
    "v_mfma_f32_16x16x32_bf16 v[92:95], v[36:39], a[216:219], v[92:95]\n\t" "v_cvt_pk_bf16_f32 v119, v110, v111\n\t" \
    "v_mfma_f32_16x16x32_bf16 v[88:91], v[40:43], a[220:223], v[88:91]\n\t" "buffer_store_dwordx4 v[116:119], %[z2], %[rs], s63 offen sc1\n\t" \
    "v_mfma_f32_16x16x32_bf16 v[92:95], v[44:47], a[220:223], v[92:95]\n\t" "s_add_u32 s63, s63, 0x20000\n\t" "v_mfma_f32_16x16x32_bf16 v[96:99], v[16:19], a[224:227], 0\n\t" \
    "v_mfma_f32_16x16x32_bf16 v[100:103], v[20:23], a[224:227], 0\n\t" "v_mfma_f32_16x16x32_bf16 v[96:99], v[24:27], a[228:231], v[96:99]\n\t" \
    "buffer_store_dwordx4 v[124:127], %[uo], %[rs], s68 offen offset:3072 sc1\n\t" "v_mfma_f32_16x16x32_bf16 v[100:103], v[28:31], a[228:231], v[100:103]\n\t" \
    "v_mfma_f32_16x16x32_bf16 v[96:99], v[32:35], a[232:235], v[96:99]\n\t" "v_cvt_pk_bf16_f32 v112, v80, v81\n\t" \
    "v_mfma_f32_16x16x32_bf16 v[100:103], v[36:39], a[232:235], v[100:103]\n\t" "v_cvt_pk_bf16_f32 v113, v82, v83\n\t" \
    "v_mfma_f32_16x16x32_bf16 v[96:99], v[40:43], a[236:239], v[96:99]\n\t" "v_cvt_pk_bf16_f32 v114, v84, v85\n\t" \
    "v_mfma_f32_16x16x32_bf16 v[100:103], v[44:47], a[236:239], v[100:103]\n\t" "v_cvt_pk_bf16_f32 v115, v86, v87\n\t" \
    "v_mfma_f32_16x16x32_bf16 v[104:107], v[16:19], a[240:243], 0\n\t" "v_cvt_pk_bf16_f32 v116, v88, v89\n\t" \
    "v_mfma_f32_16x16x32_bf16 v[108:111], v[20:23], a[240:243], 0\n\t" "v_cvt_pk_bf16_f32 v117, v90, v91\n\t" \
    "v_mfma_f32_16x16x32_bf16 v[104:107], v[24:27], a[244:247], v[104:107]\n\t" "buffer_store_dwordx4 v[112:115], %[z1], %[rs], s63 offen sc1\n\t" \
    "v_mfma_f32_16x16x32_bf16 v[108:111], v[28:31], a[244:247], v[108:111]\n\t" "v_mfma_f32_16x16x32_bf16 v[104:107], v[32:35], a[248:251], v[104:107]\n\t" \
    "v_cvt_pk_bf16_f32 v118, v92, v93\n\t" "v_mfma_f32_16x16x32_bf16 v[108:111], v[36:39], a[248:251], v[108:111]\n\t" "v_cvt_pk_bf16_f32 v119, v94, v95\n\t" \
    "v_mfma_f32_16x16x32_bf16 v[104:107], v[40:43], a[252:255], v[104:107]\n\t" "buffer_store_dwordx4 v[116:119], %[z2], %[rs], s63 offen sc1\n\t" \
    "s_add_u32 s63, s63, 0x20000\n\t" "s_add_u32 s60, s60, 1\n\t" "v_mfma_f32_16x16x32_bf16 v[108:111], v[44:47], a[252:255], v[108:111]\n\t" "s_cmp_eq_u32 s65, 0\n\t" \
    "s_waitcnt vmcnt(8)\n\t" ZSUM "s_cbranch_scc1 20f\n\t" "23:\n\t" XT(80, 81) "s_add_u32 s66, s62, %[rg]\n\t" "s_cmp_lt_u32 s66, s60\n\t" "s_cbranch_scc1 70f\n\t" "73:\n\t" \
    "v_mfma_f32_16x16x32_bf16 v[80:83], v[48:51], a[0:3], 0\n\t" "s_add_u32 s66, s60, 1\n\t" "s_and_b32 s66, s66, %[rm]\n\t" \
    "v_mfma_f32_16x16x32_bf16 v[84:87], v[52:55], a[0:3], 0\n\t" "s_lshl_b32 s68, s66, 20\n\t" "s_lshr_b32 s66, s66, 1\n\t" \
    "v_mfma_f32_16x16x32_bf16 v[80:83], v[56:59], a[4:7], v[80:83]\n\t" "s_xor_b32 s66, s66, %[xp]\n\t" "s_andn2_b32 s66, 1, s66\n\t" \
    "v_mfma_f32_16x16x32_bf16 v[84:87], v[60:63], a[4:7], v[84:87]\n\t" "s_lshl_b32 s66, s66, 13\n\t" "s_add_u32 s68, s68, s66\n\t" \
    "v_mfma_f32_16x16x32_bf16 v[80:83], v[64:67], a[8:11], v[80:83]\n\t" "v_cvt_pk_bf16_f32 v112, v96, v97\n\t" \
    "v_mfma_f32_16x16x32_bf16 v[84:87], v[68:71], a[8:11], v[84:87]\n\t" "v_cvt_pk_bf16_f32 v113, v98, v99\n\t" \
    "v_mfma_f32_16x16x32_bf16 v[80:83], v[72:75], a[12:15], v[80:83]\n\t" "v_cvt_pk_bf16_f32 v114, v100, v101\n\t" \
    "v_mfma_f32_16x16x32_bf16 v[84:87], v[76:79], a[12:15], v[84:87]\n\t" "v_cvt_pk_bf16_f32 v115, v102, v103\n\t" \
    "v_mfma_f32_16x16x32_bf16 v[88:91], v[48:51], a[16:19], 0\n\t" "v_cvt_pk_bf16_f32 v116, v104, v105\n\t" "v_mfma_f32_16x16x32_bf16 v[92:95], v[52:55], a[16:19], 0\n\t" \
    "v_cvt_pk_bf16_f32 v117, v106, v107\n\t" "v_mfma_f32_16x16x32_bf16 v[88:91], v[56:59], a[20:23], v[88:91]\n\t" \
    "buffer_store_dwordx4 v[112:115], %[z1], %[rs], s63 offen sc1\n\t" "v_mfma_f32_16x16x32_bf16 v[92:95], v[60:63], a[20:23], v[92:95]\n\t" \
    "v_mfma_f32_16x16x32_bf16 v[88:91], v[64:67], a[24:27], v[88:91]\n\t" "v_cvt_pk_bf16_f32 v118, v108, v109\n\t" \
    "v_mfma_f32_16x16x32_bf16 v[92:95], v[68:71], a[24:27], v[92:95]\n\t" "v_cvt_pk_bf16_f32 v119, v110, v111\n\t" \
    "v_mfma_f32_16x16x32_bf16 v[88:91], v[72:75], a[28:31], v[88:91]\n\t" "buffer_store_dwordx4 v[116:119], %[z2], %[rs], s63 offen sc1\n\t" "s_and_b32 s63, s60, %[rm]\n\t" \
    "v_mfma_f32_16x16x32_bf16 v[92:95], v[76:79], a[28:31], v[92:95]\n\t" "s_lshl_b32 s63, s63, 22\n\t" "s_add_u32 s63, s63, %[zb]\n\t" \
    "v_mfma_f32_16x16x32_bf16 v[96:99], v[48:51], a[32:35], 0\n\t" "buffer_load_dword v120, %[fo], %[rc], %[fv] offen sc1\n\t" \
    "v_mfma_f32_16x16x32_bf16 v[100:103], v[52:55], a[32:35], 0\n\t" "v_mfma_f32_16x16x32_bf16 v[96:99], v[56:59], a[36:39], v[96:99]\n\t" \
    "buffer_load_dword v121, %[fo], %[rc], %[fs] offen sc1\n\t" "v_mfma_f32_16x16x32_bf16 v[100:103], v[60:63], a[36:39], v[100:103]\n\t" \
    "v_mfma_f32_16x16x32_bf16 v[96:99], v[64:67], a[40:43], v[96:99]\n\t" "v_cvt_pk_bf16_f32 v112, v80, v81\n\t" \
    "v_mfma_f32_16x16x32_bf16 v[100:103], v[68:71], a[40:43], v[100:103]\n\t" "v_cvt_pk_bf16_f32 v113, v82, v83\n\t" \
    "v_mfma_f32_16x16x32_bf16 v[96:99], v[72:75], a[44:47], v[96:99]\n\t" "v_cvt_pk_bf16_f32 v114, v84, v85\n\t" \
    "v_mfma_f32_16x16x32_bf16 v[100:103], v[76:79], a[44:47], v[100:103]\n\t" "v_cvt_pk_bf16_f32 v115, v86, v87\n\t" \
    "v_mfma_f32_16x16x32_bf16 v[104:107], v[48:51], a[48:51], 0\n\t" "v_cvt_pk_bf16_f32 v116, v88, v89\n\t" "v_mfma_f32_16x16x32_bf16 v[108:111], v[52:55], a[48:51], 0\n\t" \
    "v_cvt_pk_bf16_f32 v117, v90, v91\n\t" "v_mfma_f32_16x16x32_bf16 v[104:107], v[56:59], a[52:55], v[104:107]\n\t" \
    "buffer_store_dwordx4 v[112:115], %[z1], %[rs], s63 offen sc1\n\t" "v_mfma_f32_16x16x32_bf16 v[108:111], v[60:63], a[52:55], v[108:111]\n\t" \
    "v_mfma_f32_16x16x32_bf16 v[104:107], v[64:67], a[56:59], v[104:107]\n\t" "v_cvt_pk_bf16_f32 v118, v92, v93\n\t" \
    "v_mfma_f32_16x16x32_bf16 v[108:111], v[68:71], a[56:59], v[108:111]\n\t" "v_cvt_pk_bf16_f32 v119, v94, v95\n\t" \
    "v_mfma_f32_16x16x32_bf16 v[104:107], v[72:75], a[60:63], v[104:107]\n\t" "buffer_store_dwordx4 v[116:119], %[z2], %[rs], s63 offen sc1\n\t" \
    "v_mfma_f32_16x16x32_bf16 v[108:111], v[76:79], a[60:63], v[108:111]\n\t" "s_add_u32 s63, s63, 0x20000\n\t" "v_mfma_f32_16x16x32_bf16 v[80:83], v[48:51], a[64:67], 0\n\t" \
    "s_add_u32 s65, s60, 1\n\t" "s_lshr_b32 s66, s65, 2\n\t" "v_mfma_f32_16x16x32_bf16 v[84:87], v[52:55], a[64:67], 0\n\t" "s_xor_b32 s66, s66, %[xp]\n\t" \
    "s_and_b32 s66, s66, 1\n\t" "v_mfma_f32_16x16x32_bf16 v[80:83], v[56:59], a[68:71], v[80:83]\n\t" \
    "buffer_store_dwordx4 v[124:127], %[uo], %[rs], s68 offen offset:0 sc1\n\t" "v_mfma_f32_16x16x32_bf16 v[84:87], v[60:63], a[68:71], v[84:87]\n\t" \
    "v_mfma_f32_16x16x32_bf16 v[80:83], v[64:67], a[72:75], v[80:83]\n\t" "v_cvt_pk_bf16_f32 v112, v96, v97\n\t" \
    "v_mfma_f32_16x16x32_bf16 v[84:87], v[68:71], a[72:75], v[84:87]\n\t" "v_cvt_pk_bf16_f32 v113, v98, v99\n\t" \
    "v_mfma_f32_16x16x32_bf16 v[80:83], v[72:75], a[76:79], v[80:83]\n\t" "v_cvt_pk_bf16_f32 v114, v100, v101\n\t" \
    "v_mfma_f32_16x16x32_bf16 v[84:87], v[76:79], a[76:79], v[84:87]\n\t" "v_cvt_pk_bf16_f32 v115, v102, v103\n\t" \
    "v_mfma_f32_16x16x32_bf16 v[88:91], v[48:51], a[80:83], 0\n\t" "v_cvt_pk_bf16_f32 v116, v104, v105\n\t" "v_mfma_f32_16x16x32_bf16 v[92:95], v[52:55], a[80:83], 0\n\t" \
    "v_cvt_pk_bf16_f32 v117, v106, v107\n\t" "v_mfma_f32_16x16x32_bf16 v[88:91], v[56:59], a[84:87], v[88:91]\n\t" \
    "buffer_store_dwordx4 v[112:115], %[z1], %[rs], s63 offen sc1\n\t" "v_mfma_f32_16x16x32_bf16 v[92:95], v[60:63], a[84:87], v[92:95]\n\t" \
    "v_mfma_f32_16x16x32_bf16 v[88:91], v[64:67], a[88:91], v[88:91]\n\t" "v_cvt_pk_bf16_f32 v118, v108, v109\n\t" \
    "v_mfma_f32_16x16x32_bf16 v[92:95], v[68:71], a[88:91], v[92:95]\n\t" "v_cvt_pk_bf16_f32 v119, v110, v111\n\t" \
    "v_mfma_f32_16x16x32_bf16 v[88:91], v[72:75], a[92:95], v[88:91]\n\t" "buffer_store_dwordx4 v[116:119], %[z2], %[rs], s63 offen sc1\n\t" \
    "v_mfma_f32_16x16x32_bf16 v[92:95], v[76:79], a[92:95], v[92:95]\n\t" "s_add_u32 s63, s63, 0x20000\n\t" "v_mfma_f32_16x16x32_bf16 v[96:99], v[48:51], a[96:99], 0\n\t" \
    "s_lshl_b32 s66, s66, 13\n\t" "s_lshl_b32 s64, s65, 19\n\t" "v_mfma_f32_16x16x32_bf16 v[100:103], v[52:55], a[96:99], 0\n\t" "s_add_u32 s64, s64, s66\n\t" \
    "s_add_u32 s64, s64, %[vb]\n\t" "v_mfma_f32_16x16x32_bf16 v[96:99], v[56:59], a[100:103], v[96:99]\n\t" \
    "buffer_store_dwordx4 v[124:127], %[uo], %[rs], s68 offen offset:1024 sc1\n\t" "v_mfma_f32_16x16x32_bf16 v[100:103], v[60:63], a[100:103], v[100:103]\n\t" \
    "v_mfma_f32_16x16x32_bf16 v[96:99], v[64:67], a[104:107], v[96:99]\n\t" "v_cvt_pk_bf16_f32 v112, v80, v81\n\t" \
    "v_mfma_f32_16x16x32_bf16 v[100:103], v[68:71], a[104:107], v[100:103]\n\t" "v_cvt_pk_bf16_f32 v113, v82, v83\n\t" \
    "v_mfma_f32_16x16x32_bf16 v[96:99], v[72:75], a[108:111], v[96:99]\n\t" "v_cvt_pk_bf16_f32 v114, v84, v85\n\t" \
    "v_mfma_f32_16x16x32_bf16 v[100:103], v[76:79], a[108:111], v[100:103]\n\t" "v_cvt_pk_bf16_f32 v115, v86, v87\n\t" \
    "v_mfma_f32_16x16x32_bf16 v[104:107], v[48:51], a[112:115], 0\n\t" "v_cvt_pk_bf16_f32 v116, v88, v89\n\t" \
    "v_mfma_f32_16x16x32_bf16 v[108:111], v[52:55], a[112:115], 0\n\t" "v_cvt_pk_bf16_f32 v117, v90, v91\n\t" \
    "v_mfma_f32_16x16x32_bf16 v[104:107], v[56:59], a[116:119], v[104:107]\n\t" "buffer_store_dwordx4 v[112:115], %[z1], %[rs], s63 offen sc1\n\t" \
    "v_mfma_f32_16x16x32_bf16 v[108:111], v[60:63], a[116:119], v[108:111]\n\t" "v_mfma_f32_16x16x32_bf16 v[104:107], v[64:67], a[120:123], v[104:107]\n\t" \
    "v_cvt_pk_bf16_f32 v118, v92, v93\n\t" "v_mfma_f32_16x16x32_bf16 v[108:111], v[68:71], a[120:123], v[108:111]\n\t" "v_cvt_pk_bf16_f32 v119, v94, v95\n\t" \
    "v_mfma_f32_16x16x32_bf16 v[104:107], v[72:75], a[124:127], v[104:107]\n\t" "buffer_store_dwordx4 v[116:119], %[z2], %[rs], s63 offen sc1\n\t" \
    "v_mfma_f32_16x16x32_bf16 v[108:111], v[76:79], a[124:127], v[108:111]\n\t" "s_add_u32 s63, s63, 0x20000\n\t" \
    "v_mfma_f32_16x16x32_bf16 v[80:83], v[48:51], a[128:131], 0\n\t" "s_waitcnt vmcnt(8)\n\t" "v_readfirstlane_b32 s66, v120\n\t" "v_readfirstlane_b32 s67, v121\n\t" \
    "v_mfma_f32_16x16x32_bf16 v[84:87], v[52:55], a[128:131], 0\n\t" "v_add_u32 v122, s60, %[at]\n\t" "buffer_store_dword v122, %[fo], %[rc], %[fz] offen sc1\n\t" \
    "v_mfma_f32_16x16x32_bf16 v[80:83], v[56:59], a[132:135], v[80:83]\n\t" "s_max_u32 s61, s61, s66\n\t" "s_max_u32 s62, s62, s67\n\t" "s_cmp_gt_u32 s61, s65\n\t" \
    "v_mfma_f32_16x16x32_bf16 v[84:87], v[60:63], a[132:135], v[84:87]\n\t" "s_cselect_b32 s65, 1, 0\n\t" "s_cselect_b32 s64, s64, 0x7f000000\n\t" \
    "v_mfma_f32_16x16x32_bf16 v[80:83], v[64:67], a[136:139], v[80:83]\n\t" "buffer_load_dwordx4 v[16:19], %[ao], %[rs], s64 offen offset:0 sc1\n\t" \
    "v_cvt_pk_bf16_f32 v112, v96, v97\n\t" "v_mfma_f32_16x16x32_bf16 v[84:87], v[68:71], a[136:139], v[84:87]\n\t" "v_cvt_pk_bf16_f32 v113, v98, v99\n\t" \
    "v_mfma_f32_16x16x32_bf16 v[80:83], v[72:75], a[140:143], v[80:83]\n\t" "buffer_load_dwordx4 v[20:23], %[ao], %[rs], s64 offen offset:256 sc1\n\t" \
    "v_cvt_pk_bf16_f32 v114, v100, v101\n\t" "v_mfma_f32_16x16x32_bf16 v[84:87], v[76:79], a[140:143], v[84:87]\n\t" "v_cvt_pk_bf16_f32 v115, v102, v103\n\t" \
    "v_mfma_f32_16x16x32_bf16 v[88:91], v[48:51], a[144:147], 0\n\t" "buffer_load_dwordx4 v[24:27], %[ao], %[rs], s64 offen offset:2048 sc1\n\t" \
    "v_cvt_pk_bf16_f32 v116, v104, v105\n\t" "v_mfma_f32_16x16x32_bf16 v[92:95], v[52:55], a[144:147], 0\n\t" "v_cvt_pk_bf16_f32 v117, v106, v107\n\t" \
    "v_mfma_f32_16x16x32_bf16 v[88:91], v[56:59], a[148:151], v[88:91]\n\t" "buffer_load_dwordx4 v[28:31], %[ao], %[rs], s64 offen offset:2304 sc1\n\t" \
    "v_mfma_f32_16x16x32_bf16 v[92:95], v[60:63], a[148:151], v[92:95]\n\t" "buffer_store_dwordx4 v[112:115], %[z1], %[rs], s63 offen sc1\n\t" \
    "v_mfma_f32_16x16x32_bf16 v[88:91], v[64:67], a[152:155], v[88:91]\n\t" "buffer_load_dwordx4 v[32:35], %[a1], %[rs], s64 offen offset:0 sc1\n\t" \
    "v_cvt_pk_bf16_f32 v118, v108, v109\n\t" "v_mfma_f32_16x16x32_bf16 v[92:95], v[68:71], a[152:155], v[92:95]\n\t" "v_cvt_pk_bf16_f32 v119, v110, v111\n\t" \
    "v_mfma_f32_16x16x32_bf16 v[88:91], v[72:75], a[156:159], v[88:91]\n\t" "buffer_load_dwordx4 v[36:39], %[a1], %[rs], s64 offen offset:256 sc1\n\t" \
    "buffer_store_dwordx4 v[116:119], %[z2], %[rs], s63 offen sc1\n\t" "v_mfma_f32_16x16x32_bf16 v[92:95], v[76:79], a[156:159], v[92:95]\n\t" \
    "s_add_u32 s63, s63, 0x20000\n\t" "v_mfma_f32_16x16x32_bf16 v[96:99], v[48:51], a[160:163], 0\n\t" \
    "buffer_load_dwordx4 v[40:43], %[a1], %[rs], s64 offen offset:2048 sc1\n\t" "v_mfma_f32_16x16x32_bf16 v[100:103], v[52:55], a[160:163], 0\n\t" \
    "v_mfma_f32_16x16x32_bf16 v[96:99], v[56:59], a[164:167], v[96:99]\n\t" "buffer_load_dwordx4 v[44:47], %[a1], %[rs], s64 offen offset:2304 sc1\n\t" \
    "v_mfma_f32_16x16x32_bf16 v[100:103], v[60:63], a[164:167], v[100:103]\n\t" "v_mfma_f32_16x16x32_bf16 v[96:99], v[64:67], a[168:171], v[96:99]\n\t" \
    "v_cvt_pk_bf16_f32 v112, v80, v81\n\t" "v_mfma_f32_16x16x32_bf16 v[100:103], v[68:71], a[168:171], v[100:103]\n\t" "v_cvt_pk_bf16_f32 v113, v82, v83\n\t" \
    "v_mfma_f32_16x16x32_bf16 v[96:99], v[72:75], a[172:175], v[96:99]\n\t" "v_cvt_pk_bf16_f32 v114, v84, v85\n\t" \
    "v_mfma_f32_16x16x32_bf16 v[100:103], v[76:79], a[172:175], v[100:103]\n\t" "v_cvt_pk_bf16_f32 v115, v86, v87\n\t" \
    "v_mfma_f32_16x16x32_bf16 v[104:107], v[48:51], a[176:179], 0\n\t" "v_cvt_pk_bf16_f32 v116, v88, v89\n\t" \
    "v_mfma_f32_16x16x32_bf16 v[108:111], v[52:55], a[176:179], 0\n\t" "v_cvt_pk_bf16_f32 v117, v90, v91\n\t" \
    "v_mfma_f32_16x16x32_bf16 v[104:107], v[56:59], a[180:183], v[104:107]\n\t" "buffer_store_dwordx4 v[112:115], %[z1], %[rs], s63 offen sc1\n\t" \
    "v_mfma_f32_16x16x32_bf16 v[108:111], v[60:63], a[180:183], v[108:111]\n\t" "v_mfma_f32_16x16x32_bf16 v[104:107], v[64:67], a[184:187], v[104:107]\n\t" \
    "v_cvt_pk_bf16_f32 v118, v92, v93\n\t" "v_mfma_f32_16x16x32_bf16 v[108:111], v[68:71], a[184:187], v[108:111]\n\t" "v_cvt_pk_bf16_f32 v119, v94, v95\n\t" \
    "v_mfma_f32_16x16x32_bf16 v[104:107], v[72:75], a[188:191], v[104:107]\n\t" "buffer_store_dwordx4 v[116:119], %[z2], %[rs], s63 offen sc1\n\t" \
    "v_mfma_f32_16x16x32_bf16 v[108:111], v[76:79], a[188:191], v[108:111]\n\t" "s_add_u32 s63, s63, 0x20000\n\t" \
    "v_mfma_f32_16x16x32_bf16 v[80:83], v[48:51], a[192:195], 0\n\t" "v_mfma_f32_16x16x32_bf16 v[84:87], v[52:55], a[192:195], 0\n\t" \
    "v_mfma_f32_16x16x32_bf16 v[80:83], v[56:59], a[196:199], v[80:83]\n\t" "buffer_store_dwordx4 v[124:127], %[uo], %[rs], s68 offen offset:2048 sc1\n\t" \
    "v_mfma_f32_16x16x32_bf16 v[84:87], v[60:63], a[196:199], v[84:87]\n\t" "v_mfma_f32_16x16x32_bf16 v[80:83], v[64:67], a[200:203], v[80:83]\n\t" \
    "v_cvt_pk_bf16_f32 v112, v96, v97\n\t" "v_mfma_f32_16x16x32_bf16 v[84:87], v[68:71], a[200:203], v[84:87]\n\t" "v_cvt_pk_bf16_f32 v113, v98, v99\n\t" \
    "v_mfma_f32_16x16x32_bf16 v[80:83], v[72:75], a[204:207], v[80:83]\n\t" "v_cvt_pk_bf16_f32 v114, v100, v101\n\t" \
    "v_mfma_f32_16x16x32_bf16 v[84:87], v[76:79], a[204:207], v[84:87]\n\t" "v_cvt_pk_bf16_f32 v115, v102, v103\n\t" \
    "v_mfma_f32_16x16x32_bf16 v[88:91], v[48:51], a[208:211], 0\n\t" "v_cvt_pk_bf16_f32 v116, v104, v105\n\t" "v_mfma_f32_16x16x32_bf16 v[92:95], v[52:55], a[208:211], 0\n\t" \
    "v_cvt_pk_bf16_f32 v117, v106, v107\n\t" "v_mfma_f32_16x16x32_bf16 v[88:91], v[56:59], a[212:215], v[88:91]\n\t" \
    "buffer_store_dwordx4 v[112:115], %[z1], %[rs], s63 offen sc1\n\t" "v_mfma_f32_16x16x32_bf16 v[92:95], v[60:63], a[212:215], v[92:95]\n\t" \
    "v_mfma_f32_16x16x32_bf16 v[88:91], v[64:67], a[216:219], v[88:91]\n\t" "v_cvt_pk_bf16_f32 v118, v108, v109\n\t" \
    "v_mfma_f32_16x16x32_bf16 v[92:95], v[68:71], a[216:219], v[92:95]\n\t" "v_cvt_pk_bf16_f32 v119, v110, v111\n\t" \
    "v_mfma_f32_16x16x32_bf16 v[88:91], v[72:75], a[220:223], v[88:91]\n\t" "buffer_store_dwordx4 v[116:119], %[z2], %[rs], s63 offen sc1\n\t" \
    "v_mfma_f32_16x16x32_bf16 v[92:95], v[76:79], a[220:223], v[92:95]\n\t" "s_add_u32 s63, s63, 0x20000\n\t" "v_mfma_f32_16x16x32_bf16 v[96:99], v[48:51], a[224:227], 0\n\t" \
    "v_mfma_f32_16x16x32_bf16 v[100:103], v[52:55], a[224:227], 0\n\t" "v_mfma_f32_16x16x32_bf16 v[96:99], v[56:59], a[228:231], v[96:99]\n\t" \
    "buffer_store_dwordx4 v[124:127], %[uo], %[rs], s68 offen offset:3072 sc1\n\t" "v_mfma_f32_16x16x32_bf16 v[100:103], v[60:63], a[228:231], v[100:103]\n\t" \
    "v_mfma_f32_16x16x32_bf16 v[96:99], v[64:67], a[232:235], v[96:99]\n\t" "v_cvt_pk_bf16_f32 v112, v80, v81\n\t" \
    "v_mfma_f32_16x16x32_bf16 v[100:103], v[68:71], a[232:235], v[100:103]\n\t" "v_cvt_pk_bf16_f32 v113, v82, v83\n\t" \
    "v_mfma_f32_16x16x32_bf16 v[96:99], v[72:75], a[236:239], v[96:99]\n\t" "v_cvt_pk_bf16_f32 v114, v84, v85\n\t" \
    "v_mfma_f32_16x16x32_bf16 v[100:103], v[76:79], a[236:239], v[100:103]\n\t" "v_cvt_pk_bf16_f32 v115, v86, v87\n\t" \
    "v_mfma_f32_16x16x32_bf16 v[104:107], v[48:51], a[240:243], 0\n\t" "v_cvt_pk_bf16_f32 v116, v88, v89\n\t" \
    "v_mfma_f32_16x16x32_bf16 v[108:111], v[52:55], a[240:243], 0\n\t" "v_cvt_pk_bf16_f32 v117, v90, v91\n\t" \
    "v_mfma_f32_16x16x32_bf16 v[104:107], v[56:59], a[244:247], v[104:107]\n\t" "buffer_store_dwordx4 v[112:115], %[z1], %[rs], s63 offen sc1\n\t" \
    "v_mfma_f32_16x16x32_bf16 v[108:111], v[60:63], a[244:247], v[108:111]\n\t" "v_mfma_f32_16x16x32_bf16 v[104:107], v[64:67], a[248:251], v[104:107]\n\t" \
    "v_cvt_pk_bf16_f32 v118, v92, v93\n\t" "v_mfma_f32_16x16x32_bf16 v[108:111], v[68:71], a[248:251], v[108:111]\n\t" "v_cvt_pk_bf16_f32 v119, v94, v95\n\t" \
    "v_mfma_f32_16x16x32_bf16 v[104:107], v[72:75], a[252:255], v[104:107]\n\t" "buffer_store_dwordx4 v[116:119], %[z2], %[rs], s63 offen sc1\n\t" \
    "s_add_u32 s63, s63, 0x20000\n\t" "s_add_u32 s60, s60, 1\n\t" "v_mfma_f32_16x16x32_bf16 v[108:111], v[76:79], a[252:255], v[108:111]\n\t" "s_cmp_eq_u32 s65, 0\n\t" \
    "s_waitcnt vmcnt(8)\n\t" ZSUM "s_cbranch_scc1 30f\n\t" "33:\n\t" "s_branch 10b\n\t" "20:\n\t" "s_nop 7\n\t" "v_cvt_pk_bf16_f32 v112, v96, v97\n\t" \
    "v_cvt_pk_bf16_f32 v113, v98, v99\n\t" "v_cvt_pk_bf16_f32 v114, v100, v101\n\t" "v_cvt_pk_bf16_f32 v115, v102, v103\n\t" "v_cvt_pk_bf16_f32 v116, v104, v105\n\t" \
    "v_cvt_pk_bf16_f32 v117, v106, v107\n\t" "v_cvt_pk_bf16_f32 v118, v108, v109\n\t" "v_cvt_pk_bf16_f32 v119, v110, v111\n\t" \
    "buffer_store_dwordx4 v[112:115], %[z1], %[rs], s63 offen sc1\n\t" "buffer_store_dwordx4 v[116:119], %[z2], %[rs], s63 offen sc1\n\t" \
    "buffer_load_dword v120, %[fo], %[rc], %[fv] offen sc1\n\t" "s_waitcnt vmcnt(0)\n\t" "v_readfirstlane_b32 s61, v120\n\t" "s_lshr_b32 s66, s60, 2\n\t" \
    "s_xor_b32 s66, s66, %[xp]\n\t" "s_and_b32 s66, s66, 1\n\t" "s_lshl_b32 s66, s66, 13\n\t" "s_lshl_b32 s64, s60, 19\n\t" "s_add_u32 s64, s64, s66\n\t" \
    "s_add_u32 s64, s64, %[vb]\n\t" "s_cmp_gt_u32 s61, s60\n\t" "s_cbranch_scc0 24f\n\t" "s_cmp_eq_u32 s60, 64\n\t" "s_cbranch_scc1 24f\n\t" \
    "buffer_load_dwordx4 v[48:51], %[ao], %[rs], s64 offen offset:0 sc1\n\t" "buffer_load_dwordx4 v[52:55], %[ao], %[rs], s64 offen offset:256 sc1\n\t" \
    "buffer_load_dwordx4 v[56:59], %[ao], %[rs], s64 offen offset:2048 sc1\n\t" "buffer_load_dwordx4 v[60:63], %[ao], %[rs], s64 offen offset:2304 sc1\n\t" \
    "buffer_load_dwordx4 v[64:67], %[a1], %[rs], s64 offen offset:0 sc1\n\t" "buffer_load_dwordx4 v[68:71], %[a1], %[rs], s64 offen offset:256 sc1\n\t" \
    "buffer_load_dwordx4 v[72:75], %[a1], %[rs], s64 offen offset:2048 sc1\n\t" "buffer_load_dwordx4 v[76:79], %[a1], %[rs], s64 offen offset:2304 sc1\n\t" \
    "v_add_u32 v122, s60, %[at]\n\t" "buffer_store_dword v122, %[fo], %[rc], %[fz] offen sc1\n\t" "s_waitcnt vmcnt(1)\n\t" "s_branch 23b\n\t" "24:\n\t" \
    "v_add_u32 v122, s60, %[at]\n\t" "buffer_store_dword v122, %[fo], %[rc], %[fz] offen sc1\n\t" "s_cmp_eq_u32 s60, 64\n\t" "s_cbranch_scc1 90f\n\t" \
    "s_add_u32 s67, s60, 1\n\t" "s_cmp_ge_u32 s61, s67\n\t" "s_cbranch_scc1 22f\n\t" "21:\n\t" "buffer_load_dword v120, %[fo], %[rc], %[fv] offen sc1\n\t" \
    "buffer_load_dword v121, %[fo], %[rc], %[vd] offen sc1\n\t" "s_waitcnt vmcnt(0)\n\t" "v_readfirstlane_b32 s61, v120\n\t" "v_readfirstlane_b32 s66, v121\n\t" "s_nop 3\n\t" \
    "s_cmp_ge_u32 s61, s67\n\t" "s_cbranch_scc1 22f\n\t" "s_bitcmp1_b32 s66, 1\n\t" "s_cbranch_scc1 90f\n\t" "s_cmp_eq_u32 %[bd], 0\n\t" "s_cbranch_scc1 22f\n\t" \
    "s_sub_u32 %[bd], %[bd], 1\n\t" "s_sleep 2\n\t" "s_branch 21b\n\t" "22:\n\t" "s_lshr_b32 s66, s60, 2\n\t" "s_xor_b32 s66, s66, %[xp]\n\t" "s_and_b32 s66, s66, 1\n\t" \
    "s_lshl_b32 s66, s66, 13\n\t" "s_lshl_b32 s64, s60, 19\n\t" "s_add_u32 s64, s64, s66\n\t" "s_add_u32 s64, s64, %[vb]\n\t" \
    "buffer_load_dwordx4 v[48:51], %[ao], %[rs], s64 offen offset:0 sc1\n\t" "buffer_load_dwordx4 v[52:55], %[ao], %[rs], s64 offen offset:256 sc1\n\t" \
    "buffer_load_dwordx4 v[56:59], %[ao], %[rs], s64 offen offset:2048 sc1\n\t" "buffer_load_dwordx4 v[60:63], %[ao], %[rs], s64 offen offset:2304 sc1\n\t" \
    "buffer_load_dwordx4 v[64:67], %[a1], %[rs], s64 offen offset:0 sc1\n\t" "buffer_load_dwordx4 v[68:71], %[a1], %[rs], s64 offen offset:256 sc1\n\t" \
    "buffer_load_dwordx4 v[72:75], %[a1], %[rs], s64 offen offset:2048 sc1\n\t" "buffer_load_dwordx4 v[76:79], %[a1], %[rs], s64 offen offset:2304 sc1\n\t" \
    "s_waitcnt vmcnt(0)\n\t" "s_branch 23b\n\t" "60:\n\t" "s_sub_u32 s67, s60, %[rg]\n\t" "s_cmp_ge_u32 s62, s67\n\t" "s_cbranch_scc1 62f\n\t" "61:\n\t" \
    "buffer_load_dword v120, %[fo], %[rc], %[fs] offen sc1\n\t" "buffer_load_dword v121, %[fo], %[rc], %[vd] offen sc1\n\t" "s_waitcnt vmcnt(0)\n\t" \
    "v_readfirstlane_b32 s62, v120\n\t" "v_readfirstlane_b32 s66, v121\n\t" "s_nop 3\n\t" "s_cmp_ge_u32 s62, s67\n\t" "s_cbranch_scc1 62f\n\t" "s_bitcmp1_b32 s66, 1\n\t" \
    "s_cbranch_scc1 90f\n\t" "s_cmp_eq_u32 %[bd], 0\n\t" "s_cbranch_scc1 62f\n\t" "s_sub_u32 %[bd], %[bd], 1\n\t" "s_sleep 2\n\t" "s_branch 61b\n\t" "62:\n\t" \
    "s_branch 63b\n\t" "30:\n\t" "s_nop 7\n\t" "v_cvt_pk_bf16_f32 v112, v96, v97\n\t" "v_cvt_pk_bf16_f32 v113, v98, v99\n\t" "v_cvt_pk_bf16_f32 v114, v100, v101\n\t" \
    "v_cvt_pk_bf16_f32 v115, v102, v103\n\t" "v_cvt_pk_bf16_f32 v116, v104, v105\n\t" "v_cvt_pk_bf16_f32 v117, v106, v107\n\t" "v_cvt_pk_bf16_f32 v118, v108, v109\n\t" \
    "v_cvt_pk_bf16_f32 v119, v110, v111\n\t" "buffer_store_dwordx4 v[112:115], %[z1], %[rs], s63 offen sc1\n\t" \
    "buffer_store_dwordx4 v[116:119], %[z2], %[rs], s63 offen sc1\n\t" "buffer_load_dword v120, %[fo], %[rc], %[fv] offen sc1\n\t" "s_waitcnt vmcnt(0)\n\t" \
    "v_readfirstlane_b32 s61, v120\n\t" "s_lshr_b32 s66, s60, 2\n\t" "s_xor_b32 s66, s66, %[xp]\n\t" "s_and_b32 s66, s66, 1\n\t" "s_lshl_b32 s66, s66, 13\n\t" \
    "s_lshl_b32 s64, s60, 19\n\t" "s_add_u32 s64, s64, s66\n\t" "s_add_u32 s64, s64, %[vb]\n\t" "s_cmp_gt_u32 s61, s60\n\t" "s_cbranch_scc0 34f\n\t" \
    "s_cmp_eq_u32 s60, 64\n\t" "s_cbranch_scc1 34f\n\t" "buffer_load_dwordx4 v[16:19], %[ao], %[rs], s64 offen offset:0 sc1\n\t" \
    "buffer_load_dwordx4 v[20:23], %[ao], %[rs], s64 offen offset:256 sc1\n\t" "buffer_load_dwordx4 v[24:27], %[ao], %[rs], s64 offen offset:2048 sc1\n\t" \
    "buffer_load_dwordx4 v[28:31], %[ao], %[rs], s64 offen offset:2304 sc1\n\t" "buffer_load_dwordx4 v[32:35], %[a1], %[rs], s64 offen offset:0 sc1\n\t" \
    "buffer_load_dwordx4 v[36:39], %[a1], %[rs], s64 offen offset:256 sc1\n\t" "buffer_load_dwordx4 v[40:43], %[a1], %[rs], s64 offen offset:2048 sc1\n\t" \
    "buffer_load_dwordx4 v[44:47], %[a1], %[rs], s64 offen offset:2304 sc1\n\t" "v_add_u32 v122, s60, %[at]\n\t" "buffer_store_dword v122, %[fo], %[rc], %[fz] offen sc1\n\t" \
    "s_waitcnt vmcnt(1)\n\t" "s_branch 33b\n\t" "34:\n\t" "v_add_u32 v122, s60, %[at]\n\t" "buffer_store_dword v122, %[fo], %[rc], %[fz] offen sc1\n\t" \
    "s_cmp_eq_u32 s60, 64\n\t" "s_cbranch_scc1 90f\n\t" "s_add_u32 s67, s60, 1\n\t" "s_cmp_ge_u32 s61, s67\n\t" "s_cbranch_scc1 32f\n\t" "31:\n\t" \
    "buffer_load_dword v120, %[fo], %[rc], %[fv] offen sc1\n\t" "buffer_load_dword v121, %[fo], %[rc], %[vd] offen sc1\n\t" "s_waitcnt vmcnt(0)\n\t" \
    "v_readfirstlane_b32 s61, v120\n\t" "v_readfirstlane_b32 s66, v121\n\t" "s_nop 3\n\t" "s_cmp_ge_u32 s61, s67\n\t" "s_cbranch_scc1 32f\n\t" "s_bitcmp1_b32 s66, 1\n\t" \
    "s_cbranch_scc1 90f\n\t" "s_cmp_eq_u32 %[bd], 0\n\t" "s_cbranch_scc1 32f\n\t" "s_sub_u32 %[bd], %[bd], 1\n\t" "s_sleep 2\n\t" "s_branch 31b\n\t" "32:\n\t" \
    "s_lshr_b32 s66, s60, 2\n\t" "s_xor_b32 s66, s66, %[xp]\n\t" "s_and_b32 s66, s66, 1\n\t" "s_lshl_b32 s66, s66, 13\n\t" "s_lshl_b32 s64, s60, 19\n\t" \
    "s_add_u32 s64, s64, s66\n\t" "s_add_u32 s64, s64, %[vb]\n\t" "buffer_load_dwordx4 v[16:19], %[ao], %[rs], s64 offen offset:0 sc1\n\t" \
    "buffer_load_dwordx4 v[20:23], %[ao], %[rs], s64 offen offset:256 sc1\n\t" "buffer_load_dwordx4 v[24:27], %[ao], %[rs], s64 offen offset:2048 sc1\n\t" \
    "buffer_load_dwordx4 v[28:31], %[ao], %[rs], s64 offen offset:2304 sc1\n\t" "buffer_load_dwordx4 v[32:35], %[a1], %[rs], s64 offen offset:0 sc1\n\t" \
    "buffer_load_dwordx4 v[36:39], %[a1], %[rs], s64 offen offset:256 sc1\n\t" "buffer_load_dwordx4 v[40:43], %[a1], %[rs], s64 offen offset:2048 sc1\n\t" \
    "buffer_load_dwordx4 v[44:47], %[a1], %[rs], s64 offen offset:2304 sc1\n\t" "s_waitcnt vmcnt(0)\n\t" "s_branch 33b\n\t" "70:\n\t" "s_sub_u32 s67, s60, %[rg]\n\t" \
    "s_cmp_ge_u32 s62, s67\n\t" "s_cbranch_scc1 72f\n\t" "71:\n\t" "buffer_load_dword v120, %[fo], %[rc], %[fs] offen sc1\n\t" \
    "buffer_load_dword v121, %[fo], %[rc], %[vd] offen sc1\n\t" "s_waitcnt vmcnt(0)\n\t" "v_readfirstlane_b32 s62, v120\n\t" "v_readfirstlane_b32 s66, v121\n\t" "s_nop 3\n\t" \
    "s_cmp_ge_u32 s62, s67\n\t" "s_cbranch_scc1 72f\n\t" "s_bitcmp1_b32 s66, 1\n\t" "s_cbranch_scc1 90f\n\t" "s_cmp_eq_u32 %[bd], 0\n\t" "s_cbranch_scc1 72f\n\t" \
    "s_sub_u32 %[bd], %[bd], 1\n\t" "s_sleep 2\n\t" "s_branch 71b\n\t" "72:\n\t" "s_branch 73b\n\t" "90:\n\t"

#define T_DQ 760
#define T_DV 900
#define T_RQ 2100
#define T_RV 2000
#define T_M1 120
#define T_DY 600
#define T_DX 1240
#define T_MIN 35
#define T_PEN 250
#define T_SAFE 120
#define T_RETRY 900
#define T_POLL 3
#define T_KS 0
#define T_TS 0
#define T_VLAG 0
#define T_U0 400
constexpr int T_OVH = 118, T_NEAR = 460;
constexpr unsigned NS(int ns) { return (unsigned)(ns * 64 / 10); }
struct Clk { unsigned cyc, g, pc, pg, k1, k2; };
DEV void mark(Clk& k) {
  unsigned long long c, r[8];
  asm volatile("s_memtime %0\n\ts_memrealtime %1\n\ts_nop 2\n\ts_memrealtime %2\n\ts_nop 2\n\ts_memrealtime %3\n\ts_nop 2\n\ts_memrealtime %4\n\ts_nop 2\n\t"
               "s_memrealtime %5\n\ts_nop 2\n\ts_memrealtime %6\n\ts_nop 2\n\ts_memrealtime %7\n\ts_waitcnt lgkmcnt(0)"
               : "=s"(c), "=s"(r[0]), "=s"(r[1]), "=s"(r[2]), "=s"(r[3]), "=s"(r[4]), "=s"(r[5]), "=s"(r[6]) :: "memory");
  const unsigned a = (unsigned)r[0];
  unsigned n = 0;
#pragma unroll
  for (int i = 1; i < 7; ++i) asm("s_cmp_eq_u32 %1, %2\n\ts_addc_u32 %0, %0, 0" : "+s"(n) : "s"((unsigned)r[i]), "s"(a) : "scc");
  k.g = (a + 4u) << 6; k.cyc = (unsigned)c + 12u + 16u * n;
}
DEV void anchor(Clk& k, bool any = false) {
  mark(k);
  const unsigned dg_ = k.g - k.pg;
  const float dc = (float)(k.cyc - k.pc), dg = (float)dg_;
  unsigned k1, k2;
  asm volatile("s_nop 0\n\tv_readfirstlane_b32 %0, %2\n\tv_readfirstlane_b32 %1, %3\n\ts_nop 3\n\ts_min_u32 %0, %0, 0x80000\n\ts_min_u32 %1, %1, 0x8000"
               : "=&s"(k1), "=&s"(k2) : "v"((unsigned)(dg * __builtin_amdgcn_rcpf(dc) * 65536.f)), "v"((unsigned)(dc * __builtin_amdgcn_rcpf(dg) * 65536.f)) : "scc");
  if (any) { k.k1 = k1; k.k2 = k2; }
  else asm("s_cmp_lt_u32 %4, %5\n\ts_cselect_b32 %2, %2, %0\n\ts_cselect_b32 %3, %3, %1\n\ts_add_u32 %0, %0, %2\n\ts_lshr_b32 %0, %0, 1\n\ts_add_u32 %1, %1, %3\n\ts_lshr_b32 %1, %1, 1"
           : "+s"(k.k1), "+s"(k.k2), "+s"(k1), "+s"(k2) : "s"(dg_ - NS(1400)), "n"(NS(800)) : "scc");
  k.pc = k.cyc; k.pg = k.g;
}
DEV unsigned to_cyc(const Clk& k, unsigned g) {
  unsigned d;
  asm("s_max_i32 %0, %1, 0xffff8000\n\ts_min_i32 %0, %0, 0x8000\n\ts_mul_i32 %0, %0, %2\n\ts_ashr_i32 %0, %0, 16" : "=&s"(d) : "s"(g - k.g), "s"(k.k2) : "scc");
  return k.cyc + d - T_OVH;
}
DEV void wait_cyc(unsigned t) { asm volatile(TIMED_WAIT :: "s"(t) : CL_S); }
DEV void close_in(Clk& k, unsigned t) {
  if ((int)(t - (unsigned)__builtin_amdgcn_s_memtime()) > T_NEAR + 60) { wait_cyc(t - T_NEAR); mark(k); }
}
DEV bool slow_head(const u16* q, int h) { return ((((unsigned)(size_t)q >> 8) + h) & 3) == 1; }
DEV int turn(int h, int w) { return ((h >> 3) ^ (w & 1)) & 3; }
DEV void hold(const Ctx& c, int ticks) {
#pragma unroll 1
  while ((unsigned)__builtin_amdgcn_s_memrealtime() - c.t0 < (unsigned)ticks) __builtin_amdgcn_s_sleep(4);
}
DEV int z_mix(int y) { return (y >> 4) | ((y & 1) << 2); }
DEV int u_slot(int s, int para) { return R_U + (s & (ZRING - 1)) * MB + ((para ^ (s >> 1)) & 1) * 8192; }
DEV unsigned ask_stamps(Ctx& c, int par, int slot) { return __builtin_amdgcn_raw_buffer_load_b32(c.rs, amap(24576, par) + fresh_lane() * 128, slot, AUX_SC1); }
DEV unsigned latest(Ctx& c, const Clk& k, int par, int slot, unsigned v) {
  if (__builtin_expect(!allok(v), 0)) {
#pragma unroll 1
    do {
      __builtin_amdgcn_s_sleep(T_POLL);
      asm volatile("" ::: "memory");
      const unsigned t = __builtin_amdgcn_raw_buffer_load_b32(c.rs, v == SENT ? amap(24576, par) + fresh_lane() * 128 : 1 << 30, slot, AUX_SC1);
      v = v == SENT ? t : v;
    } while (!allok(v) && !spend(c));
  }
  int d = (int)(v - k.g);
  d = max(d, __builtin_amdgcn_update_dpp(d, d, 0x128, 0xf, 0xf, true));
  d = max(d, __builtin_amdgcn_update_dpp(d, d, 0x124, 0xf, 0xf, true));
  d = max(d, __builtin_amdgcn_update_dpp(d, d, 0x122, 0xf, 0xf, true));
  d = max(d, __builtin_amdgcn_update_dpp(d, d, 0x121, 0xf, 0xf, true));
  unsigned m;
  asm("s_max_i32 %0, %1, %2\n\ts_max_i32 %0, %0, %3\n\ts_max_i32 %0, %0, %4" : "=&s"(m)
      : "s"(__builtin_amdgcn_readlane(d, 0)), "s"(__builtin_amdgcn_readlane(d, 16)), "s"(__builtin_amdgcn_readlane(d, 32)), "s"(__builtin_amdgcn_readlane(d, 48)) : "scc");
  return k.g + m;
}
struct Plan { unsigned gx, dx; };
DEV unsigned smax(unsigned a, unsigned b) { return (int)(a - b) > 0 ? a : b; }
DEV unsigned when_p(const Plan& t, unsigned last) {
  const unsigned gy = t.gx + t.dx + NS(T_M1);
  return (int)(last + NS(T_MIN) - gy) > 0 ? smax(last + NS(T_PEN), gy + NS(T_RETRY)) : gy;
}
DEV void advance(Plan& t, unsigned last) {
  const unsigned gy = t.gx + t.dx + NS(T_M1), dur = last - t.gx, gp = when_p(t, last);
  const unsigned nx = gp + NS(T_DY) + (gp != gy ? NS(T_SAFE) : 0u);
  t.dx = dur < t.dx ? (7u * t.dx + dur) >> 3 : dur < t.dx + NS(80) ? (t.dx + dur) >> 1 : t.dx + NS(4);
  t.gx = nx;
}
DEV unsigned next_go(Ctx& c, Clk& k, Plan& t, int par, int slot, unsigned v) {
  SCHED();
  anchor(k);
  SCHED();
  advance(t, latest(c, k, par, slot, v));
  return to_cyc(k, t.gx);
}

DEV constexpr int qk_r(int j) { return j < 4 ? 0 : 1 + (j - 4) % 3; }
DEV constexpr int qk_i(int j) { return j < 4 ? j : (j - 4) / 3; }
DEV constexpr int qk_upto(int u) { return u < 6 ? u + 7 : u < 10 ? 12 + (u - 4) / 2 : u < 12 ? 15 : 16; }
DEV void qk_role(const Params& p, Ctx& c, int h) {
  constexpr int WL = 0, MEMS = 131072, KXL = 147456, QKX = 155648, QROW = 272;
  c.budget = SPIN_CAP;
  const int w = c.w;
  Clk k = {0u, 0u, 0u, 0u, 0u, 0u};
  anchor(k, true);
  v8u wa[32], wh[16];
  claim(c, 0);
  hold(c, QK_AT - (slow_head(p.Wq, h) ? SLOW_LEAD : 0));
  {
    const int lane = fresh_lane(), g = lane >> 4, cc = lane & 15;
    const rsrc_t rq = mkrs(p.Wq, 1024 * 4096 * 2), rk = mkrs(p.Wk, 1024 * 4096 * 2);
    int vo[4];
#pragma unroll
    for (int i = 0; i < 4; ++i) vo[i] = opq((256 * w + 64 * (i ^ turn(h, w)) + krow_lane(g)) * 8192 + (h * 128 + 8 * cc) * 2);
    v4u row[16][8];
    auto request = [&](int u) {
#pragma unroll
      for (int r = 0; r < 8; ++r) row[u][r] = ld16w(u < 8 ? rq : rk, vo[(u >> 1) & 3], krow(4 * (u & 1) + (r >> 1), r & 1) * 8192);
    };
#pragma unroll
    for (int u = 0; u < qk_upto(-1); ++u) request(u);
    SCHED();
    const int sw = MEMS + w * 8192 + g * 256, lw = opq(WL + w * 32768 + lane * 16);
    int sr[2];
#pragma unroll
    for (int t = 0; t < 2; ++t) { const int i = (2 * cc + t) & 7; sr[t] = opq(i * 1024 + g * 256 + ((4 * w + (cc >> 2) + 2 * i) & 15) * 16); }
    sfor<16>([&](auto U) __attribute__((always_inline)) {
      constexpr int u = U.value;
#pragma unroll
      for (int i = 0; i < 8; ++i) {
        v4u f;
#pragma unroll
        for (int r = 0; r < 4; ++r) f[r] = prm(row[u][2 * r + 1][i >> 1], row[u][2 * r][i >> 1], i & 1);
        sts16(c.L, sw + i * 1024 + ((cc + 2 * i) & 15) * 16, f);
      }
      SCHED();
      if (qk_upto(u) > qk_upto(u - 1)) request(qk_upto(u - 1));
      BAR();
      v4u in[4][2];
#pragma unroll
      for (int t = 1; t >= 0; --t)
#pragma unroll
        for (int r = 0; r < 4; ++r) in[r][t] = lds16(c.L, sr[t] + MEMS + ((w + r) & 3) * 8192);
      SCHED();
#pragma unroll
      for (int j = 0; j < 16; ++j) {
        if (qk_i(j) != ((u >> 1) & 3)) continue;
#pragma unroll
        for (int t = 1; t >= 0; --t) {
          const v4u o = in[qk_r(j)][t];
          if (u >= 8 && t == 1) sts16(c.L, lw + j * 2048 + (u & 1) * 1024, o);
          else {
            v8u& d = u < 8 ? wa[2 * j + t] : wh[j];
#pragma unroll
            for (int r = 0; r < 4; ++r) d[4 * (u & 1) + r] = o[r];
#define M_(r) asm volatile("" : "+" r(d))
            if (u & 1) { if (u < 8) AT_ACC(M_, 2 * j + t); else AT_HOME(M_, j); }
#undef M_
          }
        }
      }
      BAR();
    });
  }
  STAMP(0);
  BAR();
  const unsigned vote = wait_verdict(c);
  const int par = vote_b(vote), para = vote_a(vote);
  const int lane = fresh_lane(), g = lane >> 4, cc = lane & 15;
  const int moff = opq(amap(w * 4096 + lane * 16, par) + (turn(h, w) & 1) * 1024), m2 = R_MEM + (turn(h, w) & 2) * 1024;
  unsigned tries = 1u << 12;
  const int idx = opq((lane & 1) ? 0xEEEE : 0x4444);
  TICK_DECL;
  const int l0 = opq(WL + w * 32768 + lane * 16), l1 = opq(MEMS + lane * 16);
  const int qw = opq(QKX + (2 * w + (cc >> 3)) * 256 + g * 64 + (cc & 1) * 16 + ((cc >> 1) & 3) * 4), kw = opq(QKX + (8 + 2 * g) * QROW + (32 * w + 2 * cc) * 2);
#define QK_PINS PIN_ACC0(wa); PIN_ACC1(wa); PIN_HOME(wh)
#define QK_IN [l0] "v"(l0), [l1] "v"(l1), [mo] "v"(moff), [ix] "v"(idx), [qw] "v"(qw), [kw] "v"(kw), [rs] "s"(c.rs), [sb] "s"(m2 + (s & 3) * MEM_SLOT)
#define QK_CL CL_S, CL_16_32, CL_160_200, CL_200_216, CL_216_256
  if (w == 3) {
    const int hq = opq(lane ? 1 << 30 : amap(24576 + h * 128, par));
    anchor(k, true);
    unsigned tg = k.cyc;
    Plan tt = {k.g - NS(100000), NS(T_DX)};
    for (int s = 0; s < NSEG; ++s) {
      const int pb = R_P + (s & 3) * P_SLOT;
      QK_PINS;
      asm volatile(QK_STEP3 : [bd] "+s"(c.budget), [rt] "+s"(tries) : QK_IN, [hq] "v"(hq), [pb] "s"(pb), [tg] "s"(tg), [ca] "s"(k.cyc - T_DQ), [k1] "s"(k.k1), [ga] "s"(k.g), [dr] "n"(T_RQ),
                     [kn] "s"(R_KX - 8 * MEMS + xblk(min(s + 1, NSEG - 1), h, para)) : QK_CL);
      QK_PINS;
      LOG(1, s, tg);
      __builtin_amdgcn_s_sleep(3);
      tg = next_go(c, k, tt, par, pb, ask_stamps(c, par, pb));
      st4s(c.rs, hq, R_P + ((s + 2) & 3) * P_SLOT, fresh_sent());
      LOG(2, s, k.cyc); LOG(3, s, k.g); LOG(4, s, tt.dx);
      st4s(mkrs((const void*)c.ctrl, CTRL_BYTES), hq & (1 << 30), C_FLAG + (F_S + h) * 128, (unsigned)s + 1u);
      TICK_SUM(10);
      close_in(k, tg);
      tg = to_cyc(k, tt.gx);
    }
  } else {
    const int kx0 = opq(KXL + g * 512 + cc * 16), kmrd = opq(QKX + g * 16 + (8 + (cc & 7)) * QROW);
    unsigned seen = NOFLAG;
    if (w == 0) {
      const int prow = (2 * g + (cc & 1)) * 2 + ((cc >> 1) & 1);
      const int px = opq(amap((h >> 1) * 1024 + ((2 * (h & 1) + (cc >> 3)) * 16 + prow) * 16 + ((cc >> 2) & 1) * 8, par));
      const int pm = opq(cc < 8 ? amap(16384 + h * 128 + prow * 8 + (cc >> 2) * 4, par) : 1 << 30);
      const int ps = opq((cc & 1) ? 0x03020706 : 0x05040100);
      for (int s = 0; s < NSEG; ++s) {
        if (seen < (unsigned)s + 1u) seen = wait_flag(c, F_K + h, s + 1);
        QK_PINS;
        asm volatile(QK_STEP0 : [bd] "+s"(c.budget), [rt] "+s"(tries), [sq] "+s"(seen)
                     : QK_IN, [kx] "v"(kx0), [km] "v"(kmrd), [px] "v"(px), [pm] "v"(pm), [ps] "v"(ps), [pb] "s"(R_P + (s & 3) * P_SLOT), [pr] "s"(R_P + ((s + 1) & 3) * P_SLOT),
                       [kb] "s"(R_KX - MEMS + xblk(s, h, para)), [fq] "s"(c.ctrl + (C_FLAG + (F_K + h) * 128) / 4), [ks] "n"(T_KS) : QK_CL);
        QK_PINS;
        LITE(s);
        TICK_SUM(10);
      }
    } else {
      const int qt = w - 1;
      const int px = opq(amap(qt ? 16384 + h * 1024 + lane * 16 : h * 512 + (g >> 1) * 256 + cc * 16 + (g & 1) * 8, para));
      const int pm = opq(g < 2 ? amap(49152 + (qt * 32 + h) * 256 + cc * 16 + g * 8, para) : 1 << 30);
      const int ps = opq(cc - 4 * g), hq = opq(g * 512 + (16 * qt + cc) * 16);
#define QK_TOK(STEP) for (int s = 0; s < NSEG; ++s) { QK_PINS; \
        asm volatile(STEP : [bd] "+s"(c.budget), [rt] "+s"(tries), [sq] "+s"(seen) \
                     : QK_IN, [kx] "v"(kx0), [km] "v"(kmrd), [px] "v"(px), [pm] "v"(pm), [ps] "v"(ps), [hq] "v"(hq), [qb] "s"(R_QX + xblk(s, h, para)), [tb] "s"(R_PT + (s & 7) * PT_SLOT), \
                       [tr] "s"(R_PT + ((s + 2) & 7) * PT_SLOT), [ts] "n"(T_TS), [sn] "s"(s + 1), [fq] "s"(c.ctrl + (C_FLAG + (F_Q + h) * 128) / 4) : QK_CL); \
        QK_PINS; TICK_SUM(10); }
      if (w == 1) { QK_TOK(QK_STEP1) } else { QK_TOK(QK_STEP2) }
    }
  }
  TICK_OUT();
  if (c.budget == 0u) __hip_atomic_store(c.ctrl + C_TMO / 4, 1u, RLX_AGENT);
}

DEV void shuffle_cols(Ctx& c, int scratch, const v4u (&rows)[8], v4u (&o)[8]) {
  const int g = c.lane >> 4, cc = c.lane & 15;
#pragma unroll
  for (int i = 0; i < 8; ++i) {
    v4u f;
#pragma unroll
    for (int r = 0; r < 4; ++r) f[r] = prm(rows[2 * r + 1][i >> 1], rows[2 * r][i >> 1], i & 1);
    sts16(c.L, scratch + (8 * cc + i) * 64 + ((g ^ (cc & 3)) << 4), f);
  }
  asm volatile("s_waitcnt lgkmcnt(0)" ::: "memory");
#pragma unroll
  for (int i = 0; i < 8; ++i) { const int col = 16 * i + cc; o[i] = lds16(c.L, scratch + col * 64 + ((g ^ ((col >> 3) & 3)) << 4)); }
  asm volatile("s_waitcnt lgkmcnt(0)" ::: "memory");
}

DEV void vo_role(const Params& p, Ctx& c, int h) {
  constexpr int WL = 0, MEMS = 131072, VM = 147456, VROW = 272;
  c.budget = SPIN_CAP;
  const int w = c.w, lane = c.lane = fresh_lane(), g = lane >> 4, cc = lane & 15;
  Clk k = {0u, 0u, 0u, 0u, 0u, 0u};
  anchor(k, true);
  v8u wa[32], wh[16];
  claim(c, 0);
  hold(c, VO_AT);
  const rsrc_t rwo = mkrs(p.Wo, 4096 * 1024 * 2);
  const int voo = (h * 128 + 4 * g) * 2048 + (256 * ((cc >> 3) ^ (h >> 3)) + 64 * w + 8 * (cc & 7)) * 2;
  v4u buf[3][8];
  auto request_o = [&](int u) {
#pragma unroll
    for (int r = 0; r < 8; ++r) buf[u % 3][r] = ld16w(rwo, voo ^ ((u >> 2) * 1024), (32 * (u & 3) + 16 * (r >> 2) + (r & 3)) * 2048);
  };
  {
    const rsrc_t rw = mkrs(p.Wv, 1024 * 4096 * 2);
    const int vo = (256 * w + krow_lane(g)) * 8192 + (h * 128 + 8 * cc) * 2;
    v4u vrow[8][8];
    auto request = [&](int u) {
#pragma unroll
      for (int r = 0; r < 8; ++r) vrow[u][r] = ld16w(rw, vo, (64 * ((u >> 1) ^ turn(h, w)) + krow(4 * (u & 1) + (r >> 1), r & 1)) * 8192);
    };
#pragma unroll
    for (int u = 0; u < 5; ++u) request(u);
    SCHED();
    const int wr_ = MEMS + w * 8192 + g * 256;
    int sr[2];
#pragma unroll
    for (int t = 0; t < 2; ++t) { const int i = (2 * cc + t) & 7; sr[t] = opq(i * 1024 + g * 256 + ((4 * w + (cc >> 2) + 2 * i) & 15) * 16); }
#pragma unroll
    for (int u = 0; u < 8; ++u) {
#pragma unroll
      for (int i = 0; i < 8; ++i) {
        v4u f;
#pragma unroll
        for (int r = 0; r < 4; ++r) f[r] = prm(vrow[u][2 * r + 1][i >> 1], vrow[u][2 * r][i >> 1], i & 1);
        sts16(c.L, wr_ + i * 1024 + ((cc + 2 * i) & 15) * 16, f);
      }
      SCHED();
      if (u + 5 < 8) request(u + 5); else if (u < 6) request_o(u - 3);
      BAR();
      v4u in[4][2];
#pragma unroll
      for (int t = 1; t >= 0; --t)
#pragma unroll
        for (int r = 0; r < 4; ++r) in[r][t] = lds16(c.L, sr[t] + MEMS + ((w + r) & 3) * 8192);
      SCHED();
#pragma unroll
      for (int j = 0; j < 16; ++j) {
        if (qk_i(j) != (u >> 1)) continue;
#pragma unroll
        for (int t = 1; t >= 0; --t) {
          const v4u o = in[qk_r(j)][t];
          if (t == 1 && (j & 1)) sts16(c.L, WL + (w * 16 + (j >> 1)) * 2048 + (u & 1) * 1024 + lane * 16, o);
          else {
            const int n = 3 * (j >> 1) + ((j & 1) ? 2 : t);
#pragma unroll
            for (int r = 0; r < 4; ++r) wa[n][4 * (u & 1) + r] = o[r];
#define M_(r) asm volatile("" : "+" r(wa[n]))
            if (u & 1) AT_ACC(M_, n);
#undef M_
          }
        }
      }
      BAR();
    }
  }
#pragma unroll
  for (int u = 0; u < 8; ++u) {
    v4u o[8];
    shuffle_cols(c, MEMS + w * 8192, buf[u % 3], o);
    SCHED();
    if (u + 3 < 8) request_o(u + 3);
#pragma unroll
    for (int i = 0; i < 4; ++i) {
      const int pr = 4 * (u >> 2) + i, ch = u & 3;
      const v8u f = {o[2 * i][0], o[2 * i + 1][0], o[2 * i][1], o[2 * i + 1][1], o[2 * i][2], o[2 * i + 1][2], o[2 * i][3], o[2 * i + 1][3]};
      if (ch < 2) {
        wh[2 * pr + ch] = f;
#define M_(r) asm volatile("" : "+" r(wh[2 * pr + ch]))
        AT_HOME(M_, 2 * pr + ch);
#undef M_
      } else if (ch == 2) {
        wa[24 + pr] = f;
#define M_(r) asm volatile("" : "+" r(wa[24 + pr]))
        AT_ACC(M_, 24 + pr);
#undef M_
      } else {
        sts16(c.L, WL + (w * 16 + 8 + pr) * 2048 + lane * 16, lo4(f));
        sts16(c.L, WL + (w * 16 + 8 + pr) * 2048 + 1024 + lane * 16, hi4(f));
      }
    }
    SCHED();
  }
  BAR();
  STAMP(0);
  const unsigned vote = wait_verdict(c);
  const int par = vote_b(vote), para = vote_a(vote);
  const int moff = opq(amap(w * 4096 + lane * 16, par) + (turn(h, w) & 1) * 1024), m2 = R_MEM + (turn(h, w) & 2) * 1024;
  unsigned tries = 1u << 12;
  const int idx = opq((lane & 1) ? 0xEEEE : 0x4444);
  const int u_off = opq((4 * w + g) * 16384 + (h ^ (8 * g)) * 256 + cc * 16);
  const int hq = opq((lane | w) ? 1 << 30 : amap(24576 + (32 + h) * 128, par));
  const int l0 = opq(WL + w * 32768 + lane * 16), l1 = opq(MEMS + lane * 16);
  const int vmw = opq(VM + 2 * g * VROW + (32 * w + 2 * cc) * 2);
  const int vmr = opq(VM + (cc >> 1) * VROW + g * 16);
  TICK_DECL;
  anchor(k, true);
  unsigned tg = k.cyc;
  Plan tt = {k.g - NS(100000), NS(T_DX)};
  for (int s = 0; s < NSEG; ++s) {
    const int pb = R_P + (s & 3) * P_SLOT;
    PIN_ACC0(wa); PIN_ACC1(wa); PIN_HOME(wh);
    asm volatile(VO_STEP
        : [bd] "+s"(c.budget), [rt] "+s"(tries)
        : [l0] "v"(l0), [l1] "v"(l1), [mo] "v"(moff), [ix] "v"(idx), [vw] "v"(vmw), [vr] "v"(vmr), [uo] "v"(u_off), [w] "s"(w), [rs] "s"(c.rs),
          [sb] "s"(m2 + (s & 3) * MEM_SLOT), [ub] "s"(u_slot(s, para) + (h >> 3) * 16 * 16384), [tg] "s"(tg + T_VLAG), [hq] "v"(hq), [pb] "s"(pb),
          [ca] "s"(k.cyc - T_DV), [k1] "s"(k.k1), [ga] "s"(k.g), [dr] "n"(T_RV)
        : CL_S, CL_16_32, CL_160_200, CL_200_216, CL_216_256);
    PIN_ACC0(wa); PIN_ACC1(wa); PIN_HOME(wh);
    if (w == 0) {
      LITE(s);
      LOG(1, s, tg);
      tg = next_go(c, k, tt, par, pb, ask_stamps(c, par, pb));
      st4s(c.rs, hq, R_P + ((s + 2) & 3) * P_SLOT, fresh_sent());
      LOG(2, s, k.cyc); LOG(3, s, k.g);
    }
    TICK_SUM(10);
    if (w == 0) { close_in(k, tg); tg = to_cyc(k, tt.gx); }
  }
  TICK_OUT();
  if (c.budget == 0u) __hip_atomic_store(c.ctrl + C_TMO / 4, 1u, RLX_AGENT);
}

DEV void y_role(const Params& p, Ctx& c, int y) {
  constexpr int ZR = 0, UL = 131072, CW = 155648;
  c.budget = SPIN_CAP;
  const int w = c.w, lane = c.lane = fresh_lane(), g = lane >> 4, cc = lane & 15;
  const v4u sent4 = {SENT, SENT, SENT, SENT};
  STAMP(2);
  Clk k = {0u, 0u, 0u, 0u, 0u, 0u};
  anchor(k, true);
  unsigned m0 = 0;
  if (w == 0) {
    unsigned best[2] = {~0u, ~0u};
    st4s(c.rs, y * 16384 + (lane >> 5) * 8192 + (lane & 31) * 4, R_U, SENT);
#pragma unroll 1
    for (int n = 0; n < 12; ++n) {
#pragma unroll
      for (int r = 0; r < 2; ++r) {
        VMWAIT(0);
        const unsigned t0 = (unsigned)__builtin_amdgcn_s_memtime();
        LGKM(0);
        st4s(c.rs, y * 16384 + (lane & 31) * 4, R_U + r * 8192, SENT);
        VMWAIT(0);
        const unsigned t1 = (unsigned)__builtin_amdgcn_s_memtime();
        LGKM(0);
        best[r] = min(best[r], t1 - t0);
      }
      if (n + 1 >= PROBES && min(best[0], best[1]) < 600u && max(best[0], best[1]) > min(best[0], best[1]) + 128u) break;
    }
    STAMP(5);
    claim(c, CL_VOTED | (best[1] < best[0] ? CL_A : 0u));
    m0 = *(const GAS unsigned*)(p.mem0 + (lane >> 3) * 1024 + 16 * y + 2 * (lane & 7));
    m0 = m0 < SENT - 1u ? m0 : SENT - 1u;
    if (lane < 8) *(LAS unsigned*)(c.L + CW + lane * 4) = 0u;
    const unsigned v = wait_vote(c);
    STAMP(7);
    const int oa = y * 16384 + vote_a(v) * 8192 + lane * 16, ob = amap(y * 3072 + lane * 16, vote_b(v));
#pragma unroll
    for (int i = 0; i < 8; ++i) st16s(c.rs, oa + (i & 3) * 1024, R_U + (i >> 2) * 4096, sent4);
#pragma unroll
    for (int i = 0; i < 3; ++i) st16s(c.rs, amap(y * 3072 + i * 1024 + lane * 16, vote_b(v)), R_MEM, sent4);
#pragma unroll
    for (int i = 0; i < 8; ++i) st16s(c.rs, oa + (i & 3) * 1024, R_PT + (i >> 2) * 4096, sent4);
    (void)ob;
    VMWAIT(0);
    if (lane == 0) __hip_atomic_fetch_add(c.ctrl + (C_CLAIM + c.att * 1024) / 4 + c.slot, CL_FILLED, RLX_AGENT);
    STAMP(6);
  }
  BAR();
  const unsigned vote = wait_verdict(c);
  const int par = vote_b(vote), para = vote_a(vote);
  const int mo = amap(y * 256 + lane * 4, par);
  if (w == 0) st4s(c.rs, mo, R_MEM, m0);
  STAMP(0);
  const int rot = y >> 4;
  volatile LAS unsigned* const GO = (volatile LAS unsigned*)(c.L + CW);
  TICK_DECL;
  if (w == 0) {
    int xo[4];
#pragma unroll
    for (int i = 0; i < 4; ++i) xo[i] = amap((i ^ rot) * 4096 + lane * 16, par);
    const int pmo = amap(16384 + rot * 1024 + 2 * g * 128 + cc * 8, par), psel = (cc & 1) ? 0x03020706 : 0x05040100;
    constexpr int lead = T_U0, late = 0;
    anchor(k, true);
    Plan tt = {k.g - NS(100000), NS(T_DX)};
    unsigned done = k.cyc;
    const int so = amap(24576, par) + lane * 128;
    const int idx = opq((lane & 1) ? 0xEEEE : 0x4444);
    const int uo = y * 16384 + lane * 16;
    const int mw = amap(y * 256 + (2 * g + (cc & 1)) * 32 + (cc >> 1) * 4, par);
    for (int s = 0; s < NSEG - 1; ++s) {
      if (s == 0) RAWBAR();
      const int za = ZR + (s & 3) * 32768 + rot * 8192 + lane * 16;
      v32u z0, z1, z2, z3;
      asm volatile(
          "ds_read_b128 a[0:3], %[z0] offset:0\n\t" "ds_read_b128 a[4:7], %[z0] offset:1024\n\t" "ds_read_b128 a[8:11], %[z0] offset:2048\n\t" "ds_read_b128 a[12:15], %[z0] offset:3072\n\t"
          "ds_read_b128 a[16:19], %[z0] offset:4096\n\t" "ds_read_b128 a[20:23], %[z0] offset:5120\n\t" "ds_read_b128 a[24:27], %[z0] offset:6144\n\t" "ds_read_b128 a[28:31], %[z0] offset:7168\n\t"
          "ds_read_b128 a[32:35], %[z1] offset:0\n\t" "ds_read_b128 a[36:39], %[z1] offset:1024\n\t" "ds_read_b128 a[40:43], %[z1] offset:2048\n\t" "ds_read_b128 a[44:47], %[z1] offset:3072\n\t"
          "ds_read_b128 a[48:51], %[z1] offset:4096\n\t" "ds_read_b128 a[52:55], %[z1] offset:5120\n\t" "ds_read_b128 a[56:59], %[z1] offset:6144\n\t" "ds_read_b128 a[60:63], %[z1] offset:7168\n\t"
          "ds_read_b128 a[64:67], %[z2] offset:0\n\t" "ds_read_b128 a[68:71], %[z2] offset:1024\n\t" "ds_read_b128 a[72:75], %[z2] offset:2048\n\t" "ds_read_b128 a[76:79], %[z2] offset:3072\n\t"
          "ds_read_b128 a[80:83], %[z2] offset:4096\n\t" "ds_read_b128 a[84:87], %[z2] offset:5120\n\t" "ds_read_b128 a[88:91], %[z2] offset:6144\n\t" "ds_read_b128 a[92:95], %[z2] offset:7168\n\t"
          "ds_read_b128 a[96:99], %[z3] offset:0\n\t" "ds_read_b128 a[100:103], %[z3] offset:1024\n\t" "ds_read_b128 a[104:107], %[z3] offset:2048\n\t" "ds_read_b128 a[108:111], %[z3] offset:3072\n\t"
          "ds_read_b128 a[112:115], %[z3] offset:4096\n\t" "ds_read_b128 a[116:119], %[z3] offset:5120\n\t" "ds_read_b128 a[120:123], %[z3] offset:6144\n\t" "ds_read_b128 a[124:127], %[z3] offset:7168\n\t"
          "s_waitcnt lgkmcnt(0)"
          : "={a[0:31]}"(z0), "={a[32:63]}"(z1), "={a[64:95]}"(z2), "={a[96:127]}"(z3) : [z0] "v"(za), [z1] "v"(za ^ 8192), [z2] "v"(za ^ 16384), [z3] "v"(za ^ 24576) : "memory");
      const int pb = R_P + (s & 3) * P_SLOT;
      v32u uu;
      const int ub = u_slot(s, para);
      TICK(0);
      anchor(k);
      TICK(8);
      unsigned gp = tt.gx + tt.dx + NS(T_M1), lt = late, sv = SENT;
      unsigned bad = (int)(done - to_cyc(k, tt.gx)) > T_OVH;
      TICK(4);
      LOG(2, s, k.cyc); LOG(3, s, k.g);
#pragma unroll 1
      for (;;) {
        if (bad) {
          const bool all = allok(sv);
          const unsigned last = latest(c, k, par, pb, sv);
          if (!all) mark(k);
          gp = when_p(tt, last);
          lt = 1;
        }
        close_in(k, to_cyc(k, gp) - (lt ? 96 : lead));
        const unsigned tg = to_cyc(k, gp);
        if (!bad) LOG(1, s, tg);
        asm volatile(Y_STEP
          : "=&{v[144:175]}"(uu), [bad] "=&s"(bad), [sv] "=&v"(sv)
          : [x0] "v"(xo[0]), [x1] "v"(xo[1]), [x2] "v"(xo[2]), [x3] "v"(xo[3]), [pm] "v"(pmo), [u0] "v"(uo + (rot ^ (y & 3)) * 2048), [mw] "v"(mw), [ps] "v"(psel), [ix] "v"(idx),
            [rs] "s"(c.rs), [pb] "s"(pb), [ub] "s"(ub), [mb] "s"(R_MEM + ((s + 1) & 3) * MEM_SLOT), [mr] "s"(R_MEM + ((s + 3) & 3) * MEM_SLOT), [tu] "s"(tg - (lt ? 96 : lead)), [tg] "s"(tg - 32), [late] "s"(lt),
            [so] "v"(so), [bd] "s"(c.budget),
            "{a[0:31]}"(z0), "{a[32:63]}"(z1), "{a[64:95]}"(z2), "{a[96:127]}"(z3)
          : CL_S, CL_120_128, CL_176_200, CL_200_216, CL_216_256);
        bad = __builtin_amdgcn_readfirstlane(bad);
        if (bad != 1u) break;
        spend(c);
      }
      done = bad;
      SCHED();
      const int ul = opq(UL + rot * 2048 + lane * 16);
#pragma unroll
      for (int i = 0; i < 8; ++i) sts16(c.L, (ul ^ ((i >> 1) * 2048)) + (i & 1) * 1024, v4u{uu[4 * i], uu[4 * i + 1], uu[4 * i + 2], uu[4 * i + 3]});
      asm volatile("" ::: "memory");
      *GO = s + 1;
      asm volatile("s_waitcnt vmcnt(2)" : "+v"(sv));
      advance(tt, latest(c, k, par, pb, sv));
      TICK(3);
      LITE(s); LOG(0, s, done);
      TICK_LDS(CW + 256);
    }
    {
      anchor(k);
      wait_cyc(to_cyc(k, tt.gx + tt.dx + NS(T_M1)));
      const int ub = u_slot(NSEG - 1, para);
      v4u u[8];
#pragma unroll 1
      for (;;) {
        asm volatile("" ::: "memory");
#pragma unroll
        for (int i = 0; i < 8; ++i) u[i] = ld16s(c.rs, uo + (i & 3) * 1024, ub + (i >> 2) * 4096);
        SCHED();
        unsigned t = 0;
#pragma unroll
        for (int i = 0; i < 8; ++i) t = umax(t, top(u[i]));
        if (allok(t) || spend(c)) break;
      }
      RAWBAR();
#pragma unroll
      for (int i = 0; i < 8; ++i) sts16(c.L, UL + (i ^ (2 * (y & 3))) * 1024 + lane * 16, u[i]);
      asm volatile("" ::: "memory");
      *GO = NSEG;
    }
    TICK_DUMP(CW + 256);
  } else if (w < 3) {
    const rsrc_t ro = mkrs(p.out, 2048 * 2048 * 2);
    auto tokens = [&](auto QT) {
      constexpr int qt = QT.value, NX = qt ? 32 : 16;
      RAWBAR();
      for (int s = 0; s < NSEG; ++s) {
        const int tb = R_PT + (s & 7) * PT_SLOT + para * 8192;
        int ua[4], za[4], xb[4], mb[4];
#pragma unroll
        for (int i = 0; i < 4; ++i) {
          ua[i] = opq(UL + (i ^ rot) * 2048 + lane * 16);
          za[i] = opq(ZR + (s & 3) * 32768 + (i ^ rot) * 8192 + (qt ? lane * 16 : (g >> 1) * 1024 + (g & 1) * 512 + cc * 16));
          xb[i] = tb + amap(qt * 16384 + (i ^ rot) * (qt ? 8192 : 4096), 0);
          mb[i] = tb + amap(49152 + qt * 8192, 0) + (i ^ rot) * 2048;
        }
        SCHED();
        RAWBAR();
        if (TOK_WAIT) __builtin_amdgcn_s_sleep(TOK_WAIT);
        TICK(0);
        f32x4 a;
#pragma unroll 1
        for (;;) {
          asm volatile("" ::: "memory");
          v4u pt[NX + 8];
#pragma unroll
          for (int i = 0; i < NX; ++i) pt[i] = ld16s(c.rs, (i & 3) * 1024 + lane * 16, qt ? xb[i >> 3] + ((i >> 2) & 1) * 4096 : xb[i >> 2]);
#pragma unroll
          for (int i = 0; i < 8; ++i) pt[NX + i] = ld16s(c.rs, (i & 1) * 1024 + lane * 16, mb[i >> 1]);
          SCHED();
          TICK(1);
#pragma unroll 1
          while (__builtin_amdgcn_readfirstlane(*GO) < (unsigned)s + 1u) { if (spend(c)) break; }
          asm volatile("" ::: "memory");
          f32x4 d[2] = {f32x4{0.f, 0.f, 0.f, 0.f}, f32x4{0.f, 0.f, 0.f, 0.f}};
          v4u fa[8];
          auto frag = [&](int i) {
            if (i >= NX) fa[i & 7] = lds16(c.L, ua[(i - NX) >> 1] + (i & 1) * 1024);
            else if (qt) fa[i & 7] = lds16(c.L, za[i >> 3] + (i & 7) * 1024);
            else {
              const v2u lo = *(LAS v2u*)(c.L + za[i >> 2] + (i & 3) * 2048), hi = *(LAS v2u*)(c.L + za[i >> 2] + (i & 3) * 2048 + 256);
              fa[i & 7] = v4u{lo.x, lo.y, hi.x, hi.y};
            }
          };
#pragma unroll
          for (int i = 0; i < 7; ++i) frag(i);
          SCHED();
#pragma unroll
          for (int i = 0; i < NX + 8; ++i) {
            if (i + 7 < NX + 8) frag(i + 7);
            d[i & 1] = mfma16(fa[i & 7], pt[i], d[i & 1]);
            SCHED();
          }
          a = d[0] + d[1];
          if (!__any(a[0] != a[0])) break;
          unsigned t = 0;
#pragma unroll
          for (int i = 0; i < NX + 8; ++i) t = umax(t, top(pt[i]));
          if (allok(t) || spend(c)) break;
        }
        __builtin_amdgcn_raw_buffer_store_b64(v2u{pk(a[0], a[1]), pk(a[2], a[3])}, ro, (16 * qt + cc) * 4096 + (16 * y + 4 * g) * 2, s * 32 * 4096, AUX_NT);
        TICK(2);
        TICK_SUM(3);
      }
    };
    if (w == 1) tokens(IC<0>{}); else tokens(IC<1>{});
  } else {
    unsigned seenZ = NOFLAG;
    const rsrc_t rc = mkrs((const void*)c.ctrl, CTRL_BYTES);
    const int fo = lane < 32 ? lane * 128 : 1 << 30;
    __builtin_amdgcn_s_setprio(3);
    auto least = [&](unsigned v) {
      v |= lane < 32 ? 0u : ~0u;
#pragma unroll
      for (int o = 1; o < 32; o <<= 1) v = min(v, (unsigned)__shfl_xor((int)v, o));
      seenZ = (unsigned)max((int)seenZ, (int)__builtin_amdgcn_readfirstlane(v) - 256 * c.att);
    };
    auto await = [&](unsigned need) {
#pragma unroll 1
      while (seenZ < need) {
        asm volatile("" ::: "memory");
        least(__builtin_amdgcn_raw_buffer_load_b32(rc, fo, C_FLAG + (F_Z + (y >> 4) * 32) * 128, AUX_SC1));
        if (seenZ >= need || spend(c)) break;
        __builtin_amdgcn_s_sleep(4);
      }
      __builtin_amdgcn_fence(__ATOMIC_ACQUIRE, "wavefront");
    };
    for (int n = 0; n < NSEG; ++n) {
      TICK(0);
      await(n + 1);
      TICK(1);
      v4u z[32];
      int za[8];
#pragma unroll
      for (int i = 0; i < 8; ++i) za[i] = opq(ZR + (n & 3) * 32768 + (i ^ z_mix(y)) * 1024 + lane * 16);
      unsigned look;
#pragma unroll
      for (int i = 0; i < 32 + Z_WIN; ++i) {
        if (i < 32) z[i] = ld16z(c.rs, (i & 3) * 1024 + lane * 16, R_Z + (n & (ZRING - 1)) * Z_SLOT + y * 65536 + ((para ^ (y >> 5)) & 1) * 8192 + amap((i >> 2) * 4096, 0));
        if (i == 32) { look = __builtin_amdgcn_raw_buffer_load_b32(rc, seenZ < (unsigned)n + 2u ? fo : 1 << 30, C_FLAG + (F_Z + (y >> 4) * 32) * 128, AUX_SC1); TICK(4); }
        SCHED();
        if (i >= Z_WIN) sts16(c.L, za[(i - Z_WIN) & 7] + ((i - Z_WIN) >> 3) * 8192, z[i - Z_WIN]);
        else asm volatile("s_nop " STR(Z_GAP));
        if (i == 30 + Z_WIN) { TICK(3); }
        SCHED();
      }
      least(look);
      if (n == 0) await(PRIME);
      TICK(2);
      { const int s = n; (void)s; TICK_SUM(3); }
      BAR();
    }
    RAWBAR();
  }
  TICK_OUT();
  if (c.budget == 0u) __hip_atomic_store(c.ctrl + C_TMO / 4, 1u, RLX_AGENT);
}

#define MF1A(d, a, b) asm volatile(MF16 "%0, %1, %2, %0" : "+v"(d) : "a"(a), "v"(b))
#define MF32A(d, a, b) asm volatile("v_mfma_f32_32x32x16_bf16 %0, %1, %2, %0" : "+v"(d) : "a"(a), "v"(b))
#define MF32AZ(d, a, b) asm volatile("v_mfma_f32_32x32x16_bf16 %0, %1, %2, 0" : "=&v"(d) : "a"(a), "v"(b))
#define MF1B(d, a, b) asm volatile(MF16 "%0, %1, %2, %0" : "+v"(d) : "v"(a), "a"(b))
#define MF1BZ(d, a, b) asm volatile(MF16 "%0, %1, %2, 0" : "=&v"(d) : "v"(a), "a"(b))
#define MF1AZ(d, a, b) asm volatile(MF16 "%0, %1, %2, 0" : "=&v"(d) : "a"(a), "v"(b))
DEV void g_role(const Params& p, Ctx& c, int mat, int h, int gi) {
  constexpr int EX = 131072;
  c.budget = SPIN_CAP;
  const int w = c.w, lane = c.lane = fresh_lane(), g = lane >> 4, cc = lane & 15;
  if (mat < 2) {
    unsigned t = 0;
#pragma unroll
    for (int i = 0; i < 3; ++i) t += __builtin_amdgcn_raw_buffer_load_b32(mkrs(p.Wv, 8 * MB), (256 * w + (lane >> 1)) * 8192 + h * 256 + (lane & 1) * 128, (2 + 2 * i + mat) * 32 * 8192, 0);
    if (mat == 0) {
      hold(c, X_AT);
#pragma unroll
      for (int i = 0; i < 4; ++i) t += __builtin_amdgcn_raw_buffer_load_b32(mkrs(p.x, 4 * MB), ((4 * h + w) * 64 + lane) * 128, i * MB, 0);
    }
    asm volatile("" :: "v"(t));
    claim(c, 0);
    hold(c, (mat ? GK_AT : GQ_AT) - (slow_head(p.Wq, h) ? SLOW_LEAD : 0));
  }
  const rsrc_t rx = mkrs(p.x, 2048 * 2048);
  const int xs = w * 16384 + ((lane * 16) ^ ((w & 1) << 7));

  v32u wa[8];
  {
    const rsrc_t rw = mkrs(mat == 0 ? p.Wq : mat == 1 ? p.Wk : p.Wv, 1024 * 4096 * 2);
    const int vo = (256 * w + 8 * g) * 8192 + (h * 128 + 8 * cc) * 2;
    v4u row[8][8];
    auto request = [&](int u) {
#pragma unroll
      for (int r = 0; r < 8; ++r) row[u][r] = ld16w(rw, vo, (32 * u + r) * 8192);
    };
#pragma unroll
    for (int u = 0; u < 2; ++u) request(u);
#pragma unroll
    for (int u = 0; u < 16; ++u) dma16(rx, c.L + w * 16384 + u * 1024, (xs ^ ((u >> 1) << 4)) + u * 1024);
#pragma unroll
    for (int u = 2; u < 6; ++u) request(u);
    SCHED();
    const int sw_ = EX + w * 8192 + g * 256;
    const int sr0 = opq(EX + (cc & 3) * 1024 + g * 256 + ((4 * w + (cc >> 2) + 4 * cc) & 15) * 16), sr1 = sr0 + 4096;
#pragma unroll
    for (int u = 0; u < 8; ++u) {
#pragma unroll
      for (int i = 0; i < 8; ++i) {
        v4u f;
#pragma unroll
        for (int r = 0; r < 4; ++r) f[r] = prm(row[u][2 * r + 1][i >> 1], row[u][2 * r][i >> 1], i & 1);
        sts16(c.L, sw_ + i * 1024 + ((cc + 4 * i) & 15) * 16, f);
      }
      SCHED();
      if (u + 6 < 8) request(u + 6);
      if (u == 7 && mat == 2) claim(c, 0);
      BAR();
      v8u in[4];
#pragma unroll
      for (int q = 0; q < 4; ++q) in[q] = cat(lds16(c.L, sr0 + q * 8192), lds16(c.L, sr1 + q * 8192));
      SCHED();
#pragma unroll
      for (int q = 0; q < 4; ++q) {
        v8u f = in[q];
#define M_(r) asm volatile("" : "+" r(f))
        AT_ACC(M_, 8 * q + u);
#undef M_
#pragma unroll
        for (int r = 0; r < 8; ++r) wa[2 * q + (u >> 2)][8 * (u & 3) + r] = f[r];
      }
      BAR();
    }
  }
  STAMP(0);
  const int xpar = vote_a(wait_vote(c)) ^ (mat >> 1);
  TICK_DECL;
  asm volatile(G_LOOP
      :: [xo] "v"(cc * 2048 + ((g ^ cc) << 4)), [xs] "v"(xs), [so] "v"((4 * w + g) * 512 + cc * 16), [fo] "v"((w | lane) ? 1 << 30 : 0),
         [rs] "s"(c.rs), [rx] "s"(rx), [rc] "s"(mkrs((const void*)c.ctrl, CTRL_BYTES)), [fl] "s"(C_FLAG + (mat * 32 + h) * 128), [xp] "s"(xpar),
         [ob] "s"((mat == 1 ? R_KX : R_QX) + h * 16384), [lw] "s"(w * 16384), GTRACE ACC_IN(wa)
      : CL_S, CL_16_32, CL_32_100);
}

DEV void zc_role(const Params& p, Ctx& c, int h) {
  c.budget = SPIN_CAP;
  const int w = c.w, lane = c.lane = fresh_lane(), g = lane >> 4, cc = lane & 15, h2 = lane >> 5, r32 = lane & 31;
  v32u wa[8];
  hold(c, ZC_AT);
  {
    const rsrc_t rw = mkrs(p.Wo, 4096 * 1024 * 2);
    const int vo = (h * 128 + 8 * g) * 2048 + (256 * w + 8 * cc) * 2;
    v4u row[8][8];
    auto request = [&](int u) {
#pragma unroll
      for (int r = 0; r < 8; ++r) row[u][r] = ld16w(rw, vo, (32 * (u & 3) + r) * 2048 + (u >> 2) * 256);
    };
#pragma unroll
    for (int u = 0; u < 2; ++u) request(u);
    SCHED();
    claim(c, 0);
    SCHED();
#pragma unroll
    for (int u = 2; u < 6; ++u) request(u);
    const int sw_ = w * 8192 + g * 256, sr_ = w * 8192 + (cc & 7) * 1024 + g * 256;
    v8u two[8];
#pragma unroll
    for (int u = 0; u < 8; ++u) {
#pragma unroll
      for (int i = 0; i < 8; ++i) {
        v4u f;
#pragma unroll
        for (int r = 0; r < 4; ++r) f[r] = prm(row[u][2 * r + 1][i >> 1], row[u][2 * r][i >> 1], i & 1);
        sts16(c.L, sw_ + i * 1024 + ((cc + 2 * i) & 15) * 16, f);
      }
      SCHED();
      if (u + 6 < 8) request(u + 6);
      LGKM(0);
#pragma unroll
      for (int q = 0; q < 8; ++q) {
        const v4u o = lds16(c.L, sr_ + ((2 * q + (cc >> 3) + 2 * cc) & 15) * 16);
#pragma unroll
        for (int r = 0; r < 4; ++r) two[q][4 * (u & 1) + r] = o[r];
      }
      SCHED();
      if (u & 1) {
#pragma unroll
        for (int q = 0; q < 8; ++q) {
          const int n = 2 * (8 * (u >> 2) + q) + ((u >> 1) & 1);
#define M_(r) asm volatile("" : "+" r(two[q]))
          AT_ACC(M_, n);
#undef M_
#pragma unroll
          for (int r = 0; r < 8; ++r) wa[n >> 2][8 * (n & 3) + r] = two[q][r];
        }
      }
      LGKM(0);
      SCHED();
    }
  }
  STAMP(0);
  BAR();
  const int para = vote_a(wait_vote(c));
  const int ao = g * 512 + cc * 16, zo = 16 * w * 65536 + lane * 16 + ((para ^ (w >> 1)) & 1) * 8192;
  TICK_DECL;
  asm volatile(ZC_LOOP
      : [bd] "+s"(c.budget)
      : [ao] "v"(ao), [a1] "v"(ao + 4096), [at] "v"(c.att * 256), [z1] "v"(zo + amap((h ^ z_mix(16 * w)) * 1024, 0)), [z2] "v"(zo + 65536 + amap((h ^ z_mix(16 * w + 1)) * 1024, 0)), [uo] "v"(R_U + (16 * w + (h >> 1)) * 16384 + (h & 1) * 4096 + lane * 16), [fo] "v"(lane ? 1 << 30 : 0),
        [rs] "s"(c.rs), [rc] "s"(mkrs((const void*)c.ctrl, CTRL_BYTES)), [fv] "s"(C_FLAG + (F_V + h) * 128), [fs] "s"(C_FLAG + (F_S + h) * 128),
        [fz] "s"(C_FLAG + (F_Z + w * 32 + h) * 128), [xp] "s"(para ^ 1), [vb] "s"(R_QX + h * 16384), [vd] "s"(C_VERDICT + c.att * 128), [z0] "n"(ZC_PRIME), [zb] "n"(R_Z), [rg] "n"(ZRING - 3), [rm] "n"(ZRING - 1), GTRACE ACC_IN(wa)
      : CL_S, CL_16_32, CL_32_100);
  if (c.budget == 0u) __hip_atomic_store(c.ctrl + C_TMO / 4, 1u, RLX_AGENT);
}

DEV void conv_role(const Params& p, Ctx& c, int cg, int far) {
  constexpr int FS = 4160, PL = 1280, UO = 4 * FS, WS = UO + 8 * PL;
  const int w = c.w, lane = c.lane = fresh_lane(), tid = w * 64 + lane, g = lane >> 4, cc = lane & 15;
  const int d0 = 8 * cg + 2 * w;
  const int pg = ((far ^ (cg >> 5)) & 1) * 8192;
  auto crow = [](int ch) { return (ch >> 1) * 16384 + (ch & 1) * 4096; };
  const int de = (cg >> 3) * 64 + (tid & 7) * 8, t0 = (cg & 7) * 256 + (tid >> 3) * 8;
  auto group_sync = [&](int phase) {
    gu32* cnt = c.ctrl + (C_CONV + (cg >> 3) * 16 + phase * 8) / 4;
    VMWAIT(0);
    BAR();
    if (w == 0) {
      if (lane == 0) __hip_atomic_fetch_add(cnt, 1u, RLX_AGENT);
#pragma unroll 1
      while (__builtin_amdgcn_readfirstlane(__hip_atomic_load(cnt, RLX_AGENT)) < 8u) { if (spend(c)) break; __builtin_amdgcn_s_sleep(2); }
    }
    BAR();
  };
  v4u fr[2][4], ur[2][4], qr[8];
  unsigned up[2][4];
  const int so = cc * 64 + g * 16;
  const unsigned dd = __builtin_amdgcn_raw_buffer_load_b32(mkrs(p.ssmD, 2048), d0 * 2, 0, CONV_AUX);
  BAR();
  {
    const rsrc_t rk = mkrs(p.bk, 2048 * 2048), rv = mkrs(p.bv, 2048 * 2048);
    v4u kr[11], vr[8], sh[4];
#pragma unroll
    for (int i = 0; i < 4; ++i) sh[i] = ld16c(mkrs(p.shift, 8192), de * 8 + i * 16, 0);
#pragma unroll
    for (int i = 0; i < 11; ++i) kr[i] = ld16c(rk, (t0 + i - 3) * 2048 + de * 2, 0);
#pragma unroll
    for (int i = 0; i < 8; ++i) vr[i] = ld16c(rv, (t0 + i) * 2048 + de * 2, 0);
#pragma unroll
    for (int i = 0; i < 8; ++i) fr[i >> 2][i & 3] = ld16c(mkrs(p.ssmk, 4 * MB), lane * 16 + (i & 3) * 1024, (d0 + (i >> 2)) * 4096);
    SCHED();
    if (lane < 16) sts16(c.L, w * WS + (lane >> 2) * FS + (lane & 3) * 16, v4u{0, 0, 0, 0});
#pragma unroll
    for (int i = 0; i < 2; ++i) sts16(c.L, w * WS + UO + (4 * i + g) * PL + cc * 16, v4u{0, 0, 0, 0});
#pragma unroll
    for (int ch = 0; ch < 8; ++ch) {
      const unsigned sa = sh[ch >> 1][(ch & 1) * 2], sb = sh[ch >> 1][(ch & 1) * 2 + 1];
      const float s0 = blo(sa), s1 = bhi(sa), s2 = blo(sb), s3 = bhi(sb);
      float kk[11], o[8];
#pragma unroll
      for (int i = 0; i < 11; ++i) kk[i] = (ch & 1) ? bhi(kr[i][ch >> 1]) : blo(kr[i][ch >> 1]);
#pragma unroll
      for (int i = 0; i < 8; ++i) o[i] = fmaf(kk[i + 3], s0, fmaf(kk[i + 2], s1, fmaf(kk[i + 1], s2, kk[i] * s3))) * ((ch & 1) ? bhi(vr[i][ch >> 1]) : blo(vr[i][ch >> 1]));
      st16s(c.rs, de * 8192 + t0 * 2, R_KVT + pg + crow(ch), v4u{pk(o[0], o[1]), pk(o[2], o[3]), pk(o[4], o[5]), pk(o[6], o[7])});
    }
    group_sync(0);
#pragma unroll
    for (int i = 0; i < 8; ++i) {
      const int b = R_KVT + pg + d0 * 8192 + (i >> 2) * 4096 + (i & 3) * 1024;
      ur[i >> 2][i & 3] = ld16s(c.rs, so, b);
      up[i >> 2][i & 3] = __builtin_amdgcn_raw_buffer_load_b32(c.rs, (i & 3) == 0 && lane == 0 ? 1 << 30 : so, b - 4, AUX_SC1);
    }
#pragma unroll
    for (int i = 0; i < 8; ++i) qr[i] = ld16c(mkrs(p.bq, 4 * MB), (t0 + i) * 2048 + de * 2, 0);
  }
  STAMP(5);
  const int fw = opq(w * WS + 48 + lane * 16), uw = opq(w * WS + UO + g * PL + cc * 16);
  const int fa = opq(w * WS + (cc & 3) * FS + (3 + (cc >> 2) - g) * 16);
  sfor<2>([&](auto CH) __attribute__((always_inline)) {
    constexpr int ch = CH.value;
    SCHED();
#pragma unroll
    for (int i = 0; i < 4; ++i) {
      const v4u f = fr[ch][i], a = ur[ch][i];
      sts16(c.L, fw + 3 * FS + i * 1024, f);
      sts8(c.L, fw + FS + i * 1024 + 8, v2u{f[0], f[1]}); sts8(c.L, fw + FS + i * 1024 + 16, v2u{f[2], f[3]});
#pragma unroll
      for (int r = 0; r < 2; ++r) {
        const int o = fw + 2 * r * FS + i * 1024 + 12 - 8 * r;
        *(LAS unsigned*)(c.L + o) = f[0]; sts8(c.L, o + 4, v2u{f[1], f[2]}); *(LAS unsigned*)(c.L + o + 12) = f[3];
      }
      v4u r0, r1;
#pragma unroll
      for (int r = 0; r < 4; ++r) {
        r0[r] = __builtin_amdgcn_alignbyte(a[3 - r], a[3 - r], 2);
        r1[r] = __builtin_amdgcn_perm(r < 3 ? a[2 - r] : up[ch][i], a[3 - r], 0x07060100u);
      }
      sts16(c.L, uw + (16 + 16 * i) * 16, r0);
      sts16(c.L, uw + 4 * PL + (16 + 16 * i) * 16, r1);
    }
    f32x4 acc[4][2];
#pragma unroll
    for (int i = 0; i < 8; ++i) acc[i >> 1][i & 1] = f32x4{0.f, 0.f, 0.f, 0.f};
    v4u A[2][4], B[2][8];
    auto frag = [&](int d, int n) {
      constexpr int order[12] = {0, 4, 5, 6, 7, 8, 9, 10, 11, 1, 2, 3};
      const int x = order[n];
      if (x < 4) A[d & 1][x] = lds16(c.L, fa + (d + 16 * x) * 64);
      else B[d & 1][x - 4] = lds16(c.L, uw + ((x - 4) & 1) * 4 * PL + (16 + 16 * ((x - 4) >> 1) - d) * 16);
    };
    asm volatile("" ::: "memory");
#pragma unroll
    for (int n = 0; n < 12; ++n) frag(0, n);
    SCHED();
    sfor<16>([&](auto D) __attribute__((always_inline)) {
      constexpr int d = D.value;
      int n = 0;
#pragma unroll
      for (int k = 0; k < 4; ++k)
#pragma unroll
        for (int m = 0; m + k < 4; ++m)
#pragma unroll
          for (int e = 0; e < 2; ++e) {
            acc[m + k][e] = mfma16(A[d & 1][k], B[d & 1][2 * m + e], acc[m + k][e]);
            if (d < 15 && n < 12) frag(d + 1, n);
            ++n;
            SCHED();
          }
    });
    const float dv = ch ? bhi(dd) : blo(dd);
#pragma unroll
    for (int q = 0; q < 4; ++q) {
      v4u z;
#pragma unroll
      for (int i = 0; i < 4; ++i) {
        const unsigned yy = pk(acc[q][1][i], acc[q][0][i]), ud = pk(blo(ur[ch][q][i]) * dv, bhi(ur[ch][q][i]) * dv);
        z[i] = pk(blo(yy) + blo(ud), bhi(yy) + bhi(ud));
      }
      st16s(c.rs, so + q * 1024, R_YT + pg + d0 * 8192 + ch * 4096, z);
    }
  });
  STAMP(6);
  {
    const rsrc_t ro = mkrs(p.out, 2048 * 2048 * 2);
    v4u yv[8];
    group_sync(1);
#pragma unroll
    for (int ch = 0; ch < 8; ++ch) yv[ch] = ld16s(c.rs, de * 8192 + t0 * 2, R_YT + pg + crow(ch));
#pragma unroll
    for (int i = 0; i < 8; ++i) {
      float o[8];
#pragma unroll
      for (int ch = 0; ch < 8; ++ch) o[ch] = ((ch & 1) ? bhi(qr[i][ch >> 1]) : blo(qr[i][ch >> 1])) * ((i & 1) ? bhi(yv[ch][i >> 1]) : blo(yv[ch][i >> 1]));
      __builtin_amdgcn_raw_buffer_store_b128(v4u{pk(o[0], o[1]), pk(o[2], o[3]), pk(o[4], o[5]), pk(o[6], o[7])}, ro, (t0 + i) * 4096 + (1024 + de) * 2, 0, AUX_NT);
    }
  }
  if (c.budget == 0u) __hip_atomic_store(c.ctrl + C_TMO / 4, 1u, RLX_AGENT);
}

extern "C" __global__ void __launch_bounds__(256, 1) mk_mega(Params p) {
  extern __shared__ __attribute__((aligned(16))) unsigned char lds_raw[];
  Ctx c;
  c.ctrl = (gu32*)p.ctrl;
  c.rs = mkrs(p.data, R_END);
  c.L = (lds_t)lds_raw;
  c.w = __builtin_amdgcn_readfirstlane(threadIdx.x >> 6);
  c.lane = threadIdx.x & 63;
  c.t0 = (unsigned)__builtin_amdgcn_s_memrealtime();
  c.b = (int)(blockIdx.x & 255u);
  c.att = (int)(blockIdx.x >> 8);
  c.ask = 0;
  if (c.w == 1) {
    unsigned long long base; unsigned len, t;
    asm volatile("s_getpc_b64 s[60:61]\n1:\n\ts_sub_u32 s60, s60, 1b-mk_mega\n\ts_subb_u32 s61, s61, 0\n\ts_mov_b64 %0, s[60:61]\n\ts_mov_b32 %1, mk_tail-mk_mega"
                 : "=s"(base), "=s"(len) :: "s60", "s61", "scc");
    t = __builtin_amdgcn_raw_buffer_load_b32(mkrs((const void*)base, len), (c.lane * 32 + (c.b >> 3)) * 128, 0, 0);
    asm volatile("" :: "v"(t));
  }
  if (c.w == 0 && c.att == 0) {
    c.ask = __builtin_amdgcn_readfirstlane(__hip_atomic_load(c.ctrl + C_ECHO / 4 + c.b, RLX_AGENT)) + 1u;
    if (c.lane == 0) __hip_atomic_store(c.ctrl + (C_ASK + c.b * 64) / 4, c.ask, RLX_AGENT);
  }
  c.slot = (c.att ? (c.b & 7) : (int)((unsigned)__builtin_amdgcn_s_getreg(20 | (3 << 11)) & 7u)) * 32 + (c.b >> 3);
  if (c.b == SERVER && c.w == 3) serve(c);
  const int x = c.slot >> 5, t = c.slot & 31;
  if (x < 4) {
    if (t < 8) qk_role(p, c, x * 8 + t);
    else if (t < 16) vo_role(p, c, x * 8 + t - 8);
    else y_role(p, c, x * 16 + t - 16);
  } else {
    if (t < 24) g_role(p, c, t >> 3, (x - 4) * 8 + (t & 7), t);
    else zc_role(p, c, (x - 4) * 8 + t - 24);
    STAMP(2);
    c.budget = SPIN_CAP;
    conv_role(p, c, (x - 4) * 32 + t, vote_a(wait_verdict(c)) ^ 1);
  }
  STAMP(3);
}

}

extern "C" void kernel_launch(void* const* d_in, const int* in_sizes, int n_in, void* d_out, int out_size, void* d_ws, size_t ws_size,
                              hipStream_t stream) {
  using namespace mk;
  (void)in_sizes; (void)n_in; (void)out_size;
  Params p;
  p.x = (const u16*)d_in[0]; p.mem0 = (const u16*)d_in[1];
  p.Wq = (const u16*)d_in[2]; p.Wk = (const u16*)d_in[3]; p.Wv = (const u16*)d_in[4]; p.Wo = (const u16*)d_in[5];
  p.bq = (const u16*)d_in[7]; p.bk = (const u16*)d_in[8]; p.bv = (const u16*)d_in[9];
  p.shift = (const u16*)d_in[10]; p.ssmk = (const u16*)d_in[11]; p.ssmD = (const u16*)d_in[12];
  p.out = (u16*)d_out;
  p.ctrl = (unsigned char*)d_ws;
  p.data = data_base(d_ws, ws_size);
  (void)hipFuncSetAttribute((const void*)mk_mega, hipFuncAttributeMaxDynamicSharedMemorySize, LDS_BYTES);
  hipLaunchKernelGGL(mk_mega, dim3(256), dim3(256), LDS_BYTES, stream, p);
}
```

```cpp
#include <hip/hip_runtime.h>
#include <hip/hip_bf16.h>

namespace mk {

#define GAS __attribute__((address_space(1)))
#define LAS __attribute__((address_space(3)))
#define DEV __device__ __forceinline__
#define RLX_AGENT __ATOMIC_RELAXED, __HIP_MEMORY_SCOPE_AGENT
typedef unsigned short u16;
typedef unsigned v2u __attribute__((ext_vector_type(2)));
typedef unsigned v4u __attribute__((ext_vector_type(4)));
typedef unsigned v8u __attribute__((ext_vector_type(8)));
typedef unsigned v16u __attribute__((ext_vector_type(16)));
typedef unsigned v32u __attribute__((ext_vector_type(32)));
typedef float f32x2 __attribute__((ext_vector_type(2)));
typedef float f32x4 __attribute__((ext_vector_type(4)));
typedef float f32x16 __attribute__((ext_vector_type(16)));
typedef __bf16 bf16x2 __attribute__((ext_vector_type(2)));
typedef __bf16 bf16x8 __attribute__((ext_vector_type(8)));
typedef __bf16 bf16x16 __attribute__((ext_vector_type(16)));
typedef GAS unsigned gu32;
typedef __amdgpu_buffer_rsrc_t rsrc_t;
typedef LAS unsigned char* lds_t;

#define XCNT(r) ""
#define UCNT ""
#define CL_T
#define XT(a, b) ""
#define LMARK ""
#define GSUM ""
#define ZSUM ""
#define GTRACE
#define MIXED 1
#define NOFLAG 0
#define TOK_KICK 1
#define ZRING 16
#define Z_AT 16
#define Z_GAP 4
#define STR_(x) #x
#define STR(x) STR_(x)
#define GQ_AT 600
#define GK_AT 300
#define PULL_AT 800
#define QK_AT 100
#define SERVER 194
#define SLOW_LEAD 100
#define PULL_DEPTH 1
#define PRIME 2
#define X_AT 150
#define VO_AT 250
#define ZC_AT 0
#define CONV_AUX 2
#define W_AUX 0
#define ZC_PRIME 1
#define CODE_PULL 1
#define PROBES 2
#define Z_WIN 20
#define TOK_WAIT 0
#define Z_AUX 0
constexpr int NSEG = 64;
constexpr unsigned SENT = 0xFFFFFFFFu;
constexpr int AUX_SC1 = 16, AUX_NT = 2;
constexpr int T_LAG = 61;
constexpr int G_AHEAD = 4;
constexpr unsigned SPIN_CAP = 1u << 21;
constexpr int LDS_BYTES = 163840;

constexpr int C_ECHO = 0, C_CLAIM = 1024, C_VERDICT = 3072, C_TMO = 3328, C_CONV = 3456, C_VOTE = 3840, C_FLAG = 4096, C_ASK = C_FLAG + 256 * 128;
constexpr int F_Q = 0, F_K = 32, F_V = 64, F_S = 96, F_Z = 128;
constexpr int CTRL_BYTES = C_ASK + 256 * 64;
constexpr int MB = 1 << 20;
constexpr int R_U = 136 * MB;
constexpr int R_MEM = 2 * MB, MEM_SLOT = 32768;
constexpr int R_P = R_MEM + 4 * MEM_SLOT, P_SLOT = 65536;
constexpr int R_PT = 4 * MB, PT_SLOT = 131072;
constexpr int R_QX = 8 * MB, R_KX = 40 * MB, R_Z = 72 * MB, Z_SLOT = 4 * MB, R_KVT = R_KX, R_YT = R_KX + 8 * MB, R_END = 152 * MB;

constexpr size_t R_DIAG = 0;
#define TOP 1
static inline unsigned char* data_base(void* ws, size_t size) {
  return (unsigned char*)((TOP ? (size_t)ws + size - R_END - R_DIAG : (size_t)ws + CTRL_BYTES + 2 * MB - 1) & ~(size_t)(2 * MB - 1));
}
struct Params {
  const u16 *x, *mem0, *Wq, *Wk, *Wv, *Wo, *bq, *bk, *bv, *shift, *ssmk, *ssmD;
  u16* out;
  unsigned char* ctrl;
  unsigned char* data;
};

DEV rsrc_t mkrs(const void* p, unsigned bytes) { return __builtin_amdgcn_make_buffer_rsrc((void*)p, 0, bytes, 0x00020000); }
DEV v4u ld16s(rsrc_t rs, int v, int s) { return __builtin_amdgcn_raw_buffer_load_b128(rs, v, s, AUX_SC1); }
DEV v2u ld8s(rsrc_t rs, int v, int s) { return __builtin_amdgcn_raw_buffer_load_b64(rs, v, s, AUX_SC1); }
DEV v4u ld16(rsrc_t rs, int v, int s) { return __builtin_amdgcn_raw_buffer_load_b128(rs, v, s, 0); }
DEV v4u ld16z(rsrc_t rs, int v, int s) { return __builtin_amdgcn_raw_buffer_load_b128(rs, v, s, AUX_SC1 | Z_AUX); }
DEV v4u ld16c(rsrc_t rs, int v, int s) { return __builtin_amdgcn_raw_buffer_load_b128(rs, v, s, CONV_AUX); }
DEV v4u ld16w(rsrc_t rs, int v, int s) { return __builtin_amdgcn_raw_buffer_load_b128(rs, v, s, W_AUX); }
DEV v2u ld8(rsrc_t rs, int v, int s) { return __builtin_amdgcn_raw_buffer_load_b64(rs, v, s, 0); }
DEV void dma16(rsrc_t rs, lds_t l, int v) {
#if __HIP_DEVICE_COMPILE__
  __builtin_amdgcn_raw_ptr_buffer_load_lds(rs, (LAS void*)l, 16, v, 0, 0, 0);
#endif
}
DEV void st16s(rsrc_t rs, int v, int s, v4u d) { __builtin_amdgcn_raw_buffer_store_b128(d, rs, v, s, AUX_SC1); }
DEV void st8s(rsrc_t rs, int v, int s, v2u d) { __builtin_amdgcn_raw_buffer_store_b64(d, rs, v, s, AUX_SC1); }
DEV void st4s(rsrc_t rs, int v, int s, unsigned d) { __builtin_amdgcn_raw_buffer_store_b32(d, rs, v, s, AUX_SC1); }
DEV int amap(int o, int par) { return ((o >> 13) << 14) | (par << 13) | (o & 8191); }
DEV int xblk(int s, int h, int par) { return (s * 32 + h) * 16384 + ((par ^ (s >> 2)) & 1) * 8192; }
DEV unsigned umax(unsigned a, unsigned b) { return a > b ? a : b; }
DEV unsigned top(v4u v) { return umax(umax(v.x, v.y), umax(v.z, v.w)); }
DEV unsigned pk(float a, float b) { bf16x2 v = {(__bf16)a, (__bf16)b}; return __builtin_bit_cast(unsigned, v); }
DEV unsigned pks(float a, float b) { unsigned u = pk(a, b); return u < SENT - 1u ? u : SENT - 1u; }
DEV float blo(unsigned u) { return __builtin_bit_cast(float, u << 16); }
DEV float bhi(unsigned u) { return __builtin_bit_cast(float, u & 0xFFFF0000u); }
DEV float rb(float a) { return blo(pk(a, a)); }
DEV unsigned fu(float f) { return __builtin_bit_cast(unsigned, f); }
DEV float uf(unsigned u) { return __builtin_bit_cast(float, u); }
DEV f32x4 mfma16(v4u a, v4u b, f32x4 c) {
  return __builtin_amdgcn_mfma_f32_16x16x32_bf16(__builtin_bit_cast(bf16x8, a), __builtin_bit_cast(bf16x8, b), c, 0, 0, 0);
}
DEV f32x16 mfma32(v4u a, v4u b, f32x16 c) {
  return __builtin_amdgcn_mfma_f32_32x32x16_bf16(__builtin_bit_cast(bf16x8, a), __builtin_bit_cast(bf16x8, b), c, 0, 0, 0);
}
DEV f32x4 smf(v4u a, v8u b, f32x4 c, int idx) {
  return __builtin_amdgcn_smfmac_f32_16x16x64_bf16(__builtin_bit_cast(bf16x8, a), __builtin_bit_cast(bf16x16, b), c, idx, 0, 0);
}
DEV v8u cat(v4u a, v4u b) { return __builtin_shufflevector(a, b, 0, 1, 2, 3, 4, 5, 6, 7); }
DEV v4u lo4(v8u a) { return __builtin_shufflevector(a, a, 0, 1, 2, 3); }
DEV v4u hi4(v8u a) { return __builtin_shufflevector(a, a, 4, 5, 6, 7); }
DEV v4u lds16(lds_t L, int o) { return *(LAS v4u*)(L + o); }
DEV void sts16(lds_t L, int o, v4u v) { *(LAS v4u*)(L + o) = v; }
DEV void sts8(lds_t L, int o, v2u v) { *(LAS v2u*)(L + o) = v; }
#define SCHED() __builtin_amdgcn_sched_barrier(0)
#define PIN_A(x) asm volatile("" : "+a"(x))
#define PIN_V(x) asm volatile("" : "+v"(x))
DEV int opq(int v) { asm volatile("" : "+v"(v)); return v; }
DEV int fresh_lane() {
  int l; asm volatile("v_mbcnt_lo_u32_b32 %0, -1, 0\n\tv_mbcnt_hi_u32_b32 %0, -1, %0" : "=v"(l)); return l;
}
DEV f32x4 fresh_zero() {
  f32x2 a, b;
  asm volatile("v_mov_b64 %0, 0" : "=v"(a));
  asm volatile("v_mov_b64 %0, 0" : "=v"(b));
  return __builtin_shufflevector(a, b, 0, 1, 2, 3);
}
DEV unsigned fresh_sent() { unsigned z; asm volatile("v_mov_b32 %0, -1" : "=v"(z)); return z; }
#define SMF "v_smfmac_f32_16x16x64_bf16 "
#define MF16 "v_mfma_f32_16x16x32_bf16 "
#define SMF1(cls, d, a, b) asm volatile(SMF "%0, %1, %2, %3" : "+v"(d) : "v"(a), cls(b), "v"(idx))
#define SMF1F(cls, lo, hi, d, a, b) asm volatile(SMF "v[" #lo ":" #hi "], %1, %2, %3" : "+{v[" #lo ":" #hi "]}"(d) : "v"(a), cls(b), "v"(idx))
#define AT_HOME(M, j) do { const int j_ = (j); if (j_ == 0) M("{v[32:39]}"); if (j_ == 1) M("{v[40:47]}"); if (j_ == 2) M("{v[48:55]}"); if (j_ == 3) M("{v[56:63]}"); if (j_ == 4) M("{v[64:71]}"); if (j_ == 5) M("{v[72:79]}"); if (j_ == 6) M("{v[80:87]}"); if (j_ == 7) M("{v[88:95]}"); if (j_ == 8) M("{v[96:103]}"); if (j_ == 9) M("{v[104:111]}"); if (j_ == 10) M("{v[112:119]}"); if (j_ == 11) M("{v[120:127]}"); if (j_ == 12) M("{v[128:135]}"); if (j_ == 13) M("{v[136:143]}"); if (j_ == 14) M("{v[144:151]}"); if (j_ == 15) M("{v[152:159]}"); } while (0)
#define MF1(d, a, b) asm volatile(MF16 "%0, %1, %2, %0" : "+v"(d) : "v"(a), "v"(b))
#define MF1Z(d, a, b) asm volatile(MF16 "%0, %1, %2, 0" : "=&v"(d) : "v"(a), "v"(b))
#define LGKM(n) asm volatile("s_waitcnt lgkmcnt(" #n ")" ::: "memory")
#define RAWBAR() do { __builtin_amdgcn_s_barrier(); asm volatile("" ::: "memory"); } while (0)
DEV void settle3(f32x4& s0, f32x4& s1, f32x4& s2) { asm volatile("s_nop 7" : "+v"(s0), "+v"(s1), "+v"(s2)); }
DEV void settle4(f32x4 (&d)[4]) { asm volatile("s_nop 7" : "+v"(d[0]), "+v"(d[1]), "+v"(d[2]), "+v"(d[3])); }
#define BAR() do { asm volatile("s_waitcnt lgkmcnt(0)" ::: "memory"); __builtin_amdgcn_s_barrier(); asm volatile("" ::: "memory"); } while (0)
#define VMWAIT(n) asm volatile("s_waitcnt vmcnt(" #n ")" ::: "memory")
DEV float add1(float a, float b) {
  float o; asm("v_add_f32 %0, %1, %2" : "=v"(o) : "v"(a), "v"(b)); return o;
}
DEV float xmax(float v) {
  auto r = __builtin_amdgcn_permlane32_swap(fu(v), fu(v), false, false); v = fmaxf(uf(r[0]), uf(r[1]));
  auto q = __builtin_amdgcn_permlane16_swap(fu(v), fu(v), false, false); return fmaxf(uf(q[0]), uf(q[1]));
}
DEV float xsum(float v) {
  auto r = __builtin_amdgcn_permlane32_swap(fu(v), fu(v), false, false); v = uf(r[0]) + uf(r[1]);
  auto q = __builtin_amdgcn_permlane16_swap(fu(v), fu(v), false, false); return uf(q[0]) + uf(q[1]);
}

DEV float ror8(float v) { return uf(__builtin_amdgcn_update_dpp(0u, fu(v), 0x128, 0xf, 0xf, true)); }
DEV float fmax2(float a, float b) { return __builtin_amdgcn_fmed3f(a, b, __builtin_inff()); }
DEV float fmax3(float a, float b, float c) { float r; asm("v_max3_f32 %0, %1, %2, %3" : "=v"(r) : "v"(a), "v"(b), "v"(c)); return r; }
DEV bool allok(unsigned t) { return __all(t != SENT); }

template <int N> struct IC { static constexpr int value = N; };
template <int N, class F> DEV void sfor(F&& f) { if constexpr (N > 0) { sfor<N - 1>(f); f(IC<N - 1>{}); } }
struct Ctx {
  gu32* ctrl;
  rsrc_t rs;
  lds_t L;
  int w, lane;
  int b, slot, att;
  unsigned ask;
  unsigned t0;
  unsigned budget;
};
DEV bool spend(Ctx& c) {
  const unsigned old = c.budget;
  asm volatile("s_cmp_lg_u32 %0, 0\n\ts_subb_u32 %0, %0, 0" : "+s"(c.budget) :: "scc");
  return old == 0u;
}
DEV unsigned wait_flag(Ctx& c, int flag, unsigned need) {
  gu32* f = c.ctrl + (C_FLAG + flag * 128) / 4;
  unsigned v;
#pragma unroll 1
  for (;;) {
    v = __builtin_amdgcn_readfirstlane(__hip_atomic_load(f, RLX_AGENT));
    if (v >= need || spend(c)) break;
    __builtin_amdgcn_s_sleep(2);
  }
  __builtin_amdgcn_fence(__ATOMIC_ACQUIRE, "wavefront");
  return v;
}
DEV void post_flag(Ctx& c, int flag, unsigned v, int off = 0) {
  st4s(mkrs((const void*)c.ctrl, CTRL_BYTES), (c.w | off | fresh_lane()) ? 1 << 30 : 0, C_FLAG + flag * 128, v);
}
constexpr unsigned CL_A = 1u << 16, CL_VOTED = 1u << 24, CL_FILLED = 1u << 28;
DEV void claim(Ctx& c, unsigned votes) {
  if (c.w != 0) return;
  if (c.att == 0) {
#pragma unroll 1
    while (__builtin_amdgcn_readfirstlane(__hip_atomic_load(c.ctrl + C_ECHO / 4 + c.b, RLX_AGENT)) != c.ask) {
      if (spend(c)) break;
      __builtin_amdgcn_s_sleep(2);
    }
  }
  if (fresh_lane() == 0) __hip_atomic_fetch_add(c.ctrl + (C_CLAIM + c.att * 1024) / 4 + c.slot, 1u + votes, RLX_AGENT);
}
DEV unsigned wait_verdict(Ctx& c) {
  unsigned v;
#pragma unroll 1
  for (;;) {
    v = __builtin_amdgcn_readfirstlane(__hip_atomic_load(c.ctrl + (C_VERDICT + c.att * 128) / 4, RLX_AGENT));
    if ((v & 1u) || spend(c)) break;
    __builtin_amdgcn_s_sleep(2);
  }
  __builtin_amdgcn_fence(__ATOMIC_ACQUIRE, "wavefront");
  if ((v & 3u) == 3u && c.att == 0) {
    asm volatile("s_waitcnt vmcnt(0) lgkmcnt(0)\n\ts_barrier\n\ts_mov_b64 s[0:1], %0\n\ts_mov_b32 s2, %1\n\tv_mov_b32 v0, %2\n\t"
                 "s_getpc_b64 s[4:5]\n1:\n\ts_sub_u32 s4, s4, 1b-mk_mega\n\ts_subb_u32 s5, s5, 0\n\ts_setpc_b64 s[4:5]"
                 :: "s"(__builtin_amdgcn_kernarg_segment_ptr()), "s"(c.b + 256), "v"(c.w * 64 + fresh_lane()) : "s0", "s1", "s2", "s4", "s5", "v0", "scc", "memory");
    __builtin_unreachable();
  }
  return v;
}
DEV unsigned wait_vote(Ctx& c) {
  unsigned v;
#pragma unroll 1
  for (;;) {
    v = __builtin_amdgcn_readfirstlane(__hip_atomic_load(c.ctrl + C_VOTE / 4, RLX_AGENT));
    if ((v & 1u) || spend(c)) break;
    __builtin_amdgcn_s_sleep(2);
  }
  return v;
}
DEV void serve(Ctx& c) {
  c.budget = SPIN_CAP;
  const int lane = fresh_lane();
  const rsrc_t rc = mkrs((const void*)c.ctrl, CTRL_BYTES);
  if (c.att == 0) {
#pragma unroll
    for (int i = C_CLAIM / 1024; i < C_ASK / 1024; ++i) st16s(rc, lane * 16, i * 1024, v4u{0, 0, 0, 0});
    VMWAIT(0);
  }
  unsigned vote = c.att ? wait_vote(c) : 0u;
  bool echo = c.att == 0, first = true;
  v4u last = {0u, 0u, 0u, 0u};
#pragma unroll 1
  for (;;) {
    asm volatile("" ::: "memory");
    const v4u cl = ld16s(rc, lane * 16, C_CLAIM + c.att * 1024);
    if (echo) {
      v4u e;
#pragma unroll
      for (int i = 0; i < 4; ++i) e[i] = __builtin_amdgcn_raw_buffer_load_b32(rc, (lane * 4 + i) * 64, C_ASK, AUX_SC1);
      if (first || __any(e[0] != last[0] || e[1] != last[1] || e[2] != last[2] || e[3] != last[3])) st16s(rc, lane * 16, C_ECHO, e);
      last = e; first = false;
    }
    unsigned n = 0, one = 1;
    int va = 0, nv = 0, nf = 0;
#pragma unroll
    for (int i = 0; i < 4; ++i) {
      n += cl[i] & 0xFFFFu; one &= (cl[i] & 0xFFFFu) == 1u;
      va += __popcll(__ballot(cl[i] & CL_A)); nv += __popcll(__ballot(cl[i] & CL_VOTED)); nf += __popcll(__ballot(cl[i] & CL_FILLED));
    }
#pragma unroll
    for (int o = 1; o < 64; o <<= 1) n += (unsigned)__shfl_xor((int)n, o);
    const bool all = __builtin_amdgcn_readfirstlane(n) >= 256u, good = __all(one);
    echo = echo && !all;
    if (!vote && (all || max(va, nv - va) > 32)) {
      vote = va > 32 ? 5u : 9u;
      if (lane == 0) __hip_atomic_store(c.ctrl + C_VOTE / 4, vote, RLX_AGENT);
    }
    if (all && (!good || nf >= 64)) {
      if (lane == 0) __hip_atomic_store(c.ctrl + (C_VERDICT + c.att * 128) / 4, vote | (good ? 0u : 2u), RLX_AGENT);
      break;
    }
    if (spend(c)) break;
  }
}
DEV int vote_a(unsigned v) { return (int)((v >> 2) & 1u); }
DEV int vote_b(unsigned v) { return (int)((v >> 3) & 1u); }

#define STAMP(slot) do {} while (0)
#define TICK_DECL
#define LOG(i, s, v) do {} while (0)
#define LOGS(i, s) do {} while (0)
#define LITE(s) do {} while (0)
#define TICK(i) do {} while (0)
#define TICK_SUM(n) do {} while (0)
#define TICK_LDS(b) do {} while (0)
#define TICK_DUMP(b) do {} while (0)
#define TICK_OUT() do {} while (0)

DEV constexpr int krow(int rho8, int i) { return 32 * (rho8 >> 2) + 8 * (rho8 & 1) + 2 * ((rho8 >> 1) & 1) + i; }
DEV int krow_lane(int g) { return 16 * (g >> 1) + 4 * (g & 1); }
DEV unsigned prm(unsigned hi, unsigned lo, int odd) { return __builtin_amdgcn_perm(hi, lo, odd ? 0x07060302u : 0x05040100u); }

#define AT_ACC(M, n) do { const int n_ = (n); if (n_ == 0) M("{a[0:7]}"); if (n_ == 1) M("{a[8:15]}"); if (n_ == 2) M("{a[16:23]}"); if (n_ == 3) M("{a[24:31]}"); if (n_ == 4) M("{a[32:39]}"); if (n_ == 5) M("{a[40:47]}"); if (n_ == 6) M("{a[48:55]}"); if (n_ == 7) M("{a[56:63]}"); if (n_ == 8) M("{a[64:71]}"); if (n_ == 9) M("{a[72:79]}"); if (n_ == 10) M("{a[80:87]}"); if (n_ == 11) M("{a[88:95]}"); if (n_ == 12) M("{a[96:103]}"); if (n_ == 13) M("{a[104:111]}"); if (n_ == 14) M("{a[112:119]}"); if (n_ == 15) M("{a[120:127]}"); if (n_ == 16) M("{a[128:135]}"); if (n_ == 17) M("{a[136:143]}"); if (n_ == 18) M("{a[144:151]}"); if (n_ == 19) M("{a[152:159]}"); if (n_ == 20) M("{a[160:167]}"); if (n_ == 21) M("{a[168:175]}"); if (n_ == 22) M("{a[176:183]}"); if (n_ == 23) M("{a[184:191]}"); if (n_ == 24) M("{a[192:199]}"); if (n_ == 25) M("{a[200:207]}"); if (n_ == 26) M("{a[208:215]}"); if (n_ == 27) M("{a[216:223]}"); if (n_ == 28) M("{a[224:231]}"); if (n_ == 29) M("{a[232:239]}"); if (n_ == 30) M("{a[240:247]}"); if (n_ == 31) M("{a[248:255]}"); } while (0)
#define PIN_ACC0(x) asm volatile("" :: "{a[0:7]}"(x[0]), "{a[8:15]}"(x[1]), "{a[16:23]}"(x[2]), "{a[24:31]}"(x[3]), "{a[32:39]}"(x[4]), "{a[40:47]}"(x[5]), "{a[48:55]}"(x[6]), "{a[56:63]}"(x[7]), "{a[64:71]}"(x[8]), "{a[72:79]}"(x[9]), "{a[80:87]}"(x[10]), "{a[88:95]}"(x[11]), "{a[96:103]}"(x[12]), "{a[104:111]}"(x[13]), "{a[112:119]}"(x[14]), "{a[120:127]}"(x[15]))
#define PIN_ACC1(x) asm volatile("" :: "{a[128:135]}"(x[16]), "{a[136:143]}"(x[17]), "{a[144:151]}"(x[18]), "{a[152:159]}"(x[19]), "{a[160:167]}"(x[20]), "{a[168:175]}"(x[21]), "{a[176:183]}"(x[22]), "{a[184:191]}"(x[23]), "{a[192:199]}"(x[24]), "{a[200:207]}"(x[25]), "{a[208:215]}"(x[26]), "{a[216:223]}"(x[27]), "{a[224:231]}"(x[28]), "{a[232:239]}"(x[29]), "{a[240:247]}"(x[30]), "{a[248:255]}"(x[31]))
#define PIN_HOME(x) asm volatile("" :: "{v[32:39]}"(x[0]), "{v[40:47]}"(x[1]), "{v[48:55]}"(x[2]), "{v[56:63]}"(x[3]), "{v[64:71]}"(x[4]), "{v[72:79]}"(x[5]), "{v[80:87]}"(x[6]), "{v[88:95]}"(x[7]), "{v[96:103]}"(x[8]), "{v[104:111]}"(x[9]), "{v[112:119]}"(x[10]), "{v[120:127]}"(x[11]), "{v[128:135]}"(x[12]), "{v[136:143]}"(x[13]), "{v[144:151]}"(x[14]), "{v[152:159]}"(x[15]))
#define CL_32_100 "v32", "v33", "v34", "v35", "v36", "v37", "v38", "v39", "v40", "v41", "v42", "v43", "v44", "v45", "v46", "v47", "v48", "v49", "v50", "v51", "v52", "v53", "v54", "v55", "v56", "v57", "v58", "v59", "v60", "v61", "v62", "v63", "v64", "v65", "v66", "v67", "v68", "v69", "v70", "v71", "v72", "v73", "v74", "v75", "v76", "v77", "v78", "v79", "v80", "v81", "v82", "v83", "v84", "v85", "v86", "v87", "v88", "v89", "v90", "v91", "v92", "v93", "v94", "v95", "v96", "v97", "v98", "v99", "v100", "v101", "v102", "v103", "v104", "v105", "v106", "v107", "v108", "v109", "v110", "v111", "v112", "v113", "v114", "v115", "v116", "v117", "v118", "v119", "v120", "v121", "v122", "v123", "v124", "v125", "v126", "v127", "v128", "v129", "v130", "v131"
#define ACC_IN(x) "{a[0:31]}"(x[0]), "{a[32:63]}"(x[1]), "{a[64:95]}"(x[2]), "{a[96:127]}"(x[3]), "{a[128:159]}"(x[4]), "{a[160:191]}"(x[5]), "{a[192:223]}"(x[6]), "{a[224:255]}"(x[7])
#define CL_16_32 "v16", "v17", "v18", "v19", "v20", "v21", "v22", "v23", "v24", "v25", "v26", "v27", "v28", "v29", "v30", "v31"
#define CL_120_128 "v120", "v121", "v122", "v123", "v124", "v125", "v126", "v127", "v128", "v129", "v130", "v131", "v132", "v133", "v134", "v135"
#define CL_160_200 "v160", "v161", "v162", "v163", "v164", "v165", "v166", "v167", "v168", "v169", "v170", "v171", "v172", "v173", "v174", "v175", "v176", "v177", "v178", "v179", "v180", "v181", "v182", "v183", "v184", "v185", "v186", "v187", "v188", "v189", "v190", "v191", "v192", "v193", "v194", "v195", "v196", "v197", "v198", "v199"
#define CL_176_200 "v176", "v177", "v178", "v179", "v180", "v181", "v182", "v183", "v184", "v185", "v186", "v187", "v188", "v189", "v190", "v191", "v192", "v193", "v194", "v195", "v196", "v197", "v198", "v199"
#define CL_200_216 "v200", "v201", "v202", "v203", "v204", "v205", "v206", "v207", "v208", "v209", "v210", "v211", "v212", "v213", "v214", "v215"
#define CL_216_256 "v216", "v217", "v218", "v219", "v220", "v221", "v222", "v223", "v224", "v225", "v226", "v227", "v228", "v229", "v230", "v231", "v232", "v233", "v234", "v235", "v236", "v237", "v238", "v239", "v240", "v241", "v242", "v243", "v244", "v245", "v246", "v247", "v248", "v249", "v250", "v251", "v252", "v253", "v254", "v255"
#define CL_S "memory", "vcc", "scc", "s60", "s61", "s62", "s63", "s64", "s65", "s66", "s67", "s68" CL_T
#define TIMED_WAIT \
    "s_getpc_b64 s[62:63]\n\t" "1:\n\t" "s_memtime s[64:65]\n\t" "s_waitcnt lgkmcnt(0)\n\t" "s_sub_u32 s61, %0, s64\n\t" "s_cmp_gt_i32 s61, 124\n\t" "s_cbranch_scc1 1b\n\t" \
    "s_max_i32 s61, s61, 0\n\t" "s_and_b32 s61, s61, 124\n\t" "s_sub_u32 s61, 2f-1b, s61\n\t" "s_add_u32 s62, s62, s61\n\t" "s_addc_u32 s63, s63, 0\n\t" \
    "s_setpc_b64 s[62:63]\n\t" ".rept 31\n\t" "s_nop 0\n\t" ".endr\n\t" "2:\n\t"
#define QK_STEP0 \
    "v_xor_b32 v216, 0x400, %[mo]\n\t" "s_xor_b32 s60, %[sb], 0x800\n\t" "s_mov_b32 s66, 0x3e0293ee\n\t" "s_mov_b32 s67, 0x3e0293ee\n\t" XT(80, 81) \
    "s_add_u32 s61, %[kb], 0x1000\n\t" "buffer_load_dwordx4 v[16:19], %[kx], %[rs], %[kb] offen offset:0 sc1\n\t" \
    "buffer_load_dwordx4 v[20:23], %[kx], %[rs], %[kb] offen offset:2048 sc1\n\t" "buffer_load_dwordx4 v[24:27], %[kx], %[rs], s61 offen offset:0 sc1\n\t" \
    "buffer_load_dwordx4 v[28:31], %[kx], %[rs], s61 offen offset:2048 sc1\n\t" "buffer_load_dwordx4 v[200:203], %[kx], %[rs], %[kb] offen offset:256 sc1\n\t" \
    "buffer_load_dwordx4 v[204:207], %[kx], %[rs], %[kb] offen offset:2304 sc1\n\t" "buffer_load_dwordx4 v[208:211], %[kx], %[rs], s61 offen offset:256 sc1\n\t" \
    "buffer_load_dwordx4 v[212:215], %[kx], %[rs], s61 offen offset:2304 sc1\n\t" "s_waitcnt vmcnt(7)\n\t" "ds_write_b128 %[kx], v[16:19] offset:0\n\t" \
    "s_waitcnt vmcnt(6)\n\t" "ds_write_b128 %[kx], v[20:23] offset:2048\n\t" "s_waitcnt vmcnt(5)\n\t" "ds_write_b128 %[kx], v[24:27] offset:4096\n\t" "s_waitcnt vmcnt(4)\n\t" \
    "ds_write_b128 %[kx], v[28:31] offset:6144\n\t" "s_waitcnt vmcnt(3)\n\t" "ds_write_b128 %[kx], v[200:203] offset:256\n\t" "s_waitcnt vmcnt(2)\n\t" \
    "ds_write_b128 %[kx], v[204:207] offset:2304\n\t" "s_waitcnt vmcnt(1)\n\t" "ds_write_b128 %[kx], v[208:211] offset:4352\n\t" "s_waitcnt vmcnt(0)\n\t" \
    "ds_write_b128 %[kx], v[212:215] offset:6400\n\t" "s_waitcnt lgkmcnt(0)\n\t" "v_mov_b64 v[208:209], 0\n\t" "v_mov_b64 v[210:211], 0\n\t" "v_mov_b64 v[212:213], 0\n\t" \
    "v_mov_b64 v[214:215], 0\n\t" XT(98, 99) "s_barrier\n\t" XT(82, 83) "buffer_load_dwordx4 v[240:243], %[mo], %[rs], %[sb] offen sc1\n\t" "v_mov_b64 v[224:225], 0\n\t" \
    "v_mov_b64 v[226:227], 0\n\t" "v_mov_b64 v[228:229], 0\n\t" "v_mov_b64 v[230:231], 0\n\t" "v_mov_b64 v[232:233], 0\n\t" "v_mov_b64 v[234:235], 0\n\t" \
    "v_mov_b64 v[236:237], 0\n\t" "v_mov_b64 v[238:239], 0\n\t" "buffer_load_dwordx4 v[244:247], v216, %[rs], %[sb] offen sc1\n\t" \
    "ds_read_b128 v[160:163], %[l0] offset:0\n\t" "ds_read_b128 v[164:167], %[l0] offset:1024\n\t" "ds_read_b128 v[168:171], %[l0] offset:2048\n\t" \
    "ds_read_b128 v[172:175], %[l0] offset:3072\n\t" "s_nop 3\n\t" "buffer_load_dwordx4 v[248:251], %[mo], %[rs], s60 offen sc1\n\t" \
    "ds_read_b128 v[176:179], %[l0] offset:4096\n\t" "ds_read_b128 v[180:183], %[l0] offset:5120\n\t" "ds_read_b128 v[184:187], %[l0] offset:6144\n\t" \
    "ds_read_b128 v[188:191], %[l0] offset:7168\n\t" "s_nop 3\n\t" "buffer_load_dwordx4 v[252:255], v216, %[rs], s60 offen sc1\n\t" "v_mov_b32 v219, 160512\n\t" \
    "v_mov_b64 v[220:221], -1\n\t" "v_mov_b64 v[222:223], -1\n\t" "v_mov_b32 v217, 0\n\t" "buffer_store_dwordx2 v[220:221], %[px], %[rs], %[pr] offen sc1\n\t" \
    "buffer_store_dword v220, %[pm], %[rs], %[pr] offen sc1\n\t" "global_load_dword v217, v217, %[fq] sc1\n\t" "10:\n\t" "s_waitcnt vmcnt(6)\n\t" XT(84, 85) \
    "v_smfmac_f32_16x16x64_bf16 v[224:227], v[240:243], a[0:7], %[ix]\n\t" "ds_write_b128 %[l1], v[240:243] offset:0\n\t" \
    "v_smfmac_f32_16x16x64_bf16 v[228:231], v[240:243], a[8:15], %[ix]\n\t" "v_smfmac_f32_16x16x64_bf16 v[232:235], v[240:243], v[32:39], %[ix]\n\t" \
    "s_waitcnt lgkmcnt(7)\n\t" "v_smfmac_f32_16x16x64_bf16 v[236:239], v[240:243], v[160:167], %[ix]\n\t" "ds_read_b128 v[160:163], %[l0] offset:8192\n\t" \
    "ds_read_b128 v[164:167], %[l0] offset:9216\n\t" "s_waitcnt vmcnt(5)\n\t" "v_smfmac_f32_16x16x64_bf16 v[224:227], v[244:247], a[16:23], %[ix]\n\t" \
    "ds_write_b128 %[l1], v[244:247] offset:1024\n\t" "v_smfmac_f32_16x16x64_bf16 v[228:231], v[244:247], a[24:31], %[ix]\n\t" \
    "v_smfmac_f32_16x16x64_bf16 v[232:235], v[244:247], v[40:47], %[ix]\n\t" "s_waitcnt lgkmcnt(8)\n\t" \
    "v_smfmac_f32_16x16x64_bf16 v[236:239], v[244:247], v[168:175], %[ix]\n\t" "ds_read_b128 v[168:171], %[l0] offset:10240\n\t" \
    "ds_read_b128 v[172:175], %[l0] offset:11264\n\t" "s_waitcnt vmcnt(4)\n\t" "v_smfmac_f32_16x16x64_bf16 v[224:227], v[248:251], a[32:39], %[ix]\n\t" \
    "ds_write_b128 %[l1], v[248:251] offset:2048\n\t" "v_smfmac_f32_16x16x64_bf16 v[228:231], v[248:251], a[40:47], %[ix]\n\t" "s_waitcnt lgkmcnt(3)\n\t" "s_barrier\n\t" \
    XT(86, 87) "v_smfmac_f32_16x16x64_bf16 v[232:235], v[248:251], v[48:55], %[ix]\n\t" "ds_read_b128 v[192:195], %[l1] offset:4096\n\t" \
    "ds_read_b128 v[196:199], %[l1] offset:8192\n\t" "v_smfmac_f32_16x16x64_bf16 v[236:239], v[248:251], v[176:183], %[ix]\n\t" \
    "ds_read_b128 v[176:179], %[l0] offset:12288\n\t" "ds_read_b128 v[180:183], %[l0] offset:13312\n\t" "s_waitcnt vmcnt(3)\n\t" \
    "v_smfmac_f32_16x16x64_bf16 v[224:227], v[252:255], a[48:55], %[ix]\n\t" "ds_write_b128 %[l1], v[252:255] offset:3072\n\t" \
    "v_smfmac_f32_16x16x64_bf16 v[228:231], v[252:255], a[56:63], %[ix]\n\t" "ds_read_b128 v[240:243], %[l1] offset:12288\n\t" \
    "ds_read_b128 v[244:247], %[l1] offset:5120\n\t" "v_smfmac_f32_16x16x64_bf16 v[232:235], v[252:255], v[56:63], %[ix]\n\t" "s_nop 1\n\t" \
    "v_smfmac_f32_16x16x64_bf16 v[236:239], v[252:255], v[184:191], %[ix]\n\t" "ds_read_b128 v[184:187], %[l0] offset:14336\n\t" "v_cmp_u_f32 vcc, v224, v225\n\t" \
    "v_cmp_u_f32 s[62:63], v226, v227\n\t" "s_waitcnt lgkmcnt(7)\n\t" "v_smfmac_f32_16x16x64_bf16 v[224:227], v[192:195], a[64:71], %[ix]\n\t" \
    "ds_read_b128 v[188:191], %[l0] offset:15360\n\t" "v_smfmac_f32_16x16x64_bf16 v[228:231], v[192:195], a[72:79], %[ix]\n\t" "s_or_b64 vcc, vcc, s[62:63]\n\t" \
    "s_cbranch_scc1 11f\n\t" "12:\n\t" "v_smfmac_f32_16x16x64_bf16 v[232:235], v[192:195], v[64:71], %[ix]\n\t" "s_waitcnt lgkmcnt(4)\n\t" "s_barrier\n\t" XT(88, 89) \
    "ds_read_b32 v218, v219\n\t" "v_smfmac_f32_16x16x64_bf16 v[236:239], v[192:195], v[160:167], %[ix]\n\t" "ds_read_b128 v[160:163], %[l0] offset:16384\n\t" \
    "ds_read_b128 v[164:167], %[l0] offset:17408\n\t" "v_smfmac_f32_16x16x64_bf16 v[224:227], v[196:199], a[80:87], %[ix]\n\t" \
    "ds_read_b128 v[248:251], %[l1] offset:9216\n\t" "ds_read_b128 v[252:255], %[l1] offset:13312\n\t" \
    "v_smfmac_f32_16x16x64_bf16 v[228:231], v[196:199], a[88:95], %[ix]\n\t" "ds_read_b128 v[192:195], %[l1] offset:6144\n\t" \
    "v_smfmac_f32_16x16x64_bf16 v[232:235], v[196:199], v[72:79], %[ix]\n\t" "v_smfmac_f32_16x16x64_bf16 v[236:239], v[196:199], v[168:175], %[ix]\n\t" \
    "s_waitcnt lgkmcnt(5)\n\t" "v_cmp_eq_u32 vcc, %[rt], v218\n\t" "v_smfmac_f32_16x16x64_bf16 v[224:227], v[240:243], a[96:103], %[ix]\n\t" "s_cbranch_vccnz 13f\n\t" \
    "ds_read_b128 v[168:171], %[l0] offset:18432\n\t" "v_smfmac_f32_16x16x64_bf16 v[228:231], v[240:243], a[104:111], %[ix]\n\t" \
    "ds_read_b128 v[172:175], %[l0] offset:19456\n\t" "ds_read_b128 v[196:199], %[l1] offset:10240\n\t" \
    "v_smfmac_f32_16x16x64_bf16 v[232:235], v[240:243], v[80:87], %[ix]\n\t" "v_smfmac_f32_16x16x64_bf16 v[236:239], v[240:243], v[176:183], %[ix]\n\t" \
    "ds_read_b128 v[176:179], %[l0] offset:20480\n\t" "ds_read_b128 v[180:183], %[l0] offset:21504\n\t" \
    "v_smfmac_f32_16x16x64_bf16 v[224:227], v[244:247], a[112:119], %[ix]\n\t" "ds_read_b128 v[240:243], %[l1] offset:14336\n\t" \
    "v_smfmac_f32_16x16x64_bf16 v[228:231], v[244:247], a[120:127], %[ix]\n\t" "v_smfmac_f32_16x16x64_bf16 v[232:235], v[244:247], v[88:95], %[ix]\n\t" \
    "v_smfmac_f32_16x16x64_bf16 v[236:239], v[244:247], v[184:191], %[ix]\n\t" "ds_read_b128 v[184:187], %[l0] offset:22528\n\t" \
    "ds_read_b128 v[188:191], %[l0] offset:23552\n\t" "s_waitcnt lgkmcnt(10)\n\t" "v_smfmac_f32_16x16x64_bf16 v[224:227], v[248:251], a[128:135], %[ix]\n\t" \
    "ds_read_b128 v[244:247], %[l1] offset:7168\n\t" "v_smfmac_f32_16x16x64_bf16 v[228:231], v[248:251], a[136:143], %[ix]\n\t" \
    "v_smfmac_f32_16x16x64_bf16 v[232:235], v[248:251], v[96:103], %[ix]\n\t" "v_smfmac_f32_16x16x64_bf16 v[236:239], v[248:251], v[160:167], %[ix]\n\t" \
    "ds_read_b128 v[160:163], %[l0] offset:24576\n\t" "ds_read_b128 v[164:167], %[l0] offset:25600\n\t" "s_waitcnt lgkmcnt(12)\n\t" \
    "v_smfmac_f32_16x16x64_bf16 v[224:227], v[252:255], a[144:151], %[ix]\n\t" "ds_read_b128 v[248:251], %[l1] offset:11264\n\t" \
    "v_smfmac_f32_16x16x64_bf16 v[228:231], v[252:255], a[152:159], %[ix]\n\t" "v_smfmac_f32_16x16x64_bf16 v[232:235], v[252:255], v[104:111], %[ix]\n\t" \
    "s_waitcnt lgkmcnt(10)\n\t" "v_smfmac_f32_16x16x64_bf16 v[236:239], v[252:255], v[168:175], %[ix]\n\t" "ds_read_b128 v[168:171], %[l0] offset:26624\n\t" \
    "ds_read_b128 v[172:175], %[l0] offset:27648\n\t" "v_smfmac_f32_16x16x64_bf16 v[224:227], v[192:195], a[160:167], %[ix]\n\t" \
    "ds_read_b128 v[252:255], %[l1] offset:15360\n\t" "v_smfmac_f32_16x16x64_bf16 v[228:231], v[192:195], a[168:175], %[ix]\n\t" "s_waitcnt lgkmcnt(12)\n\t" \
    "v_smfmac_f32_16x16x64_bf16 v[224:227], v[196:199], a[176:183], %[ix]\n\t" "v_smfmac_f32_16x16x64_bf16 v[228:231], v[196:199], a[184:191], %[ix]\n\t" \
    "s_waitcnt lgkmcnt(9)\n\t" "v_smfmac_f32_16x16x64_bf16 v[224:227], v[240:243], a[192:199], %[ix]\n\t" \
    "v_smfmac_f32_16x16x64_bf16 v[228:231], v[240:243], a[200:207], %[ix]\n\t" "s_waitcnt vmcnt(0)\n\t" "v_readfirstlane_b32 s62, v217\n\t" "s_waitcnt lgkmcnt(6)\n\t" \
    "v_smfmac_f32_16x16x64_bf16 v[224:227], v[244:247], a[208:215], %[ix]\n\t" "v_smfmac_f32_16x16x64_bf16 v[228:231], v[244:247], a[216:223], %[ix]\n\t" \
    "s_max_u32 %[sq], %[sq], s62\n\t" "s_waitcnt lgkmcnt(3)\n\t" "v_smfmac_f32_16x16x64_bf16 v[224:227], v[248:251], a[224:231], %[ix]\n\t" \
    "v_smfmac_f32_16x16x64_bf16 v[228:231], v[248:251], a[232:239], %[ix]\n\t" "s_waitcnt lgkmcnt(0)\n\t" \
    "v_smfmac_f32_16x16x64_bf16 v[224:227], v[252:255], a[240:247], %[ix]\n\t" "s_nop 1\n\t" "v_smfmac_f32_16x16x64_bf16 v[228:231], v[252:255], a[248:255], %[ix]\n\t" \
    XT(90, 91) "s_nop 1\n\t" "v_smfmac_f32_16x16x64_bf16 v[232:235], v[192:195], v[112:119], %[ix]\n\t" "s_nop 1\n\t" \
    "v_smfmac_f32_16x16x64_bf16 v[236:239], v[192:195], v[176:183], %[ix]\n\t" "ds_read_b128 v[176:179], %[l0] offset:28672\n\t" "v_add_f32 v224, v224, v225\n\t" \
    "v_add_f32 v226, v226, v227\n\t" "v_smfmac_f32_16x16x64_bf16 v[232:235], v[196:199], v[120:127], %[ix]\n\t" "ds_read_b128 v[180:183], %[l0] offset:29696\n\t" \
    "v_add_f32 v228, v228, v229\n\t" "v_add_f32 v230, v230, v231\n\t" "v_smfmac_f32_16x16x64_bf16 v[236:239], v[196:199], v[184:191], %[ix]\n\t" \
    "ds_read_b128 v[184:187], %[l0] offset:30720\n\t" "v_cvt_pk_bf16_f32 v218, v224, v228\n\t" "v_cvt_pk_bf16_f32 v219, v226, v230\n\t" \
    "v_smfmac_f32_16x16x64_bf16 v[232:235], v[240:243], v[128:135], %[ix]\n\t" "ds_write2_b32 %[qw], v218, v219 offset1:8\n\t" \
    "ds_read_b128 v[188:191], %[l0] offset:31744\n\t" "v_smfmac_f32_16x16x64_bf16 v[236:239], v[240:243], v[160:167], %[ix]\n\t" \
    "ds_read_b128 v[192:195], %[kx] offset:4352\n\t" "ds_read_b128 v[196:199], %[kx] offset:6400\n\t" \
    "v_smfmac_f32_16x16x64_bf16 v[232:235], v[244:247], v[136:143], %[ix]\n\t" "v_smfmac_f32_16x16x64_bf16 v[236:239], v[244:247], v[168:175], %[ix]\n\t" \
    "s_waitcnt lgkmcnt(3)\n\t" "s_barrier\n\t" "v_smfmac_f32_16x16x64_bf16 v[232:235], v[248:251], v[144:151], %[ix]\n\t" "ds_read_b128 v[216:219], %[l1] offset:24576\n\t" \
    "ds_read_b128 v[220:223], %[l1] offset:25600\n\t" "v_smfmac_f32_16x16x64_bf16 v[236:239], v[248:251], v[176:183], %[ix]\n\t" \
    "v_smfmac_f32_16x16x64_bf16 v[232:235], v[252:255], v[152:159], %[ix]\n\t" "s_waitcnt lgkmcnt(4)\n\t" \
    "v_smfmac_f32_16x16x64_bf16 v[236:239], v[252:255], v[184:191], %[ix]\n\t" XT(92, 93) "s_waitcnt lgkmcnt(1)\n\t" \
    "v_smfmac_f32_16x16x64_bf16 v[208:211], v[216:219], v[16:23], %[ix]\n\t" "s_nop 1\n\t" "v_smfmac_f32_16x16x64_bf16 v[212:215], v[216:219], v[200:207], %[ix]\n\t" \
    "s_waitcnt lgkmcnt(0)\n\t" "s_nop 0\n\t" "v_add_f32 v232, v232, v233\n\t" "v_add_f32 v234, v234, v235\n\t" "v_add_f32 v236, v236, v237\n\t" \
    "v_add_f32 v238, v238, v239\n\t" "v_cvt_pk_bf16_f32 v232, v232, v236\n\t" "v_cvt_pk_bf16_f32 v233, v234, v238\n\t" "ds_write2_b32 %[kw], v232, v233 offset1:68\n\t" \
    "v_smfmac_f32_16x16x64_bf16 v[208:211], v[220:223], v[24:31], %[ix]\n\t" "v_smfmac_f32_16x16x64_bf16 v[212:215], v[220:223], v[192:199], %[ix]\n\t" \
    "v_mov_b64 v[224:225], 0\n\t" "v_mov_b64 v[226:227], 0\n\t" "s_waitcnt lgkmcnt(0)\n\t" "s_barrier\n\t" XT(94, 95) "ds_read_b128 v[160:163], %[km] offset:0\n\t" \
    "ds_read_b128 v[164:167], %[km] offset:64\n\t" "ds_read_b128 v[168:171], %[km] offset:128\n\t" "ds_read_b128 v[172:175], %[km] offset:192\n\t" \
    "v_add_f32 v178, v208, v209\n\t" "v_add_f32 v179, v210, v211\n\t" "v_add_f32 v180, v212, v213\n\t" "v_add_f32 v181, v214, v215\n\t" "v_max_f32 v182, v178, v180\n\t" \
    "v_max_f32 v183, v179, v181\n\t" "s_nop 0\n\t" "v_max_f32_dpp v182, v182, v182 row_ror:8 row_mask:0xf bank_mask:0xf\n\t" \
    "v_max_f32_dpp v183, v183, v183 row_ror:8 row_mask:0xf bank_mask:0xf\n\t" "s_nop 0\n\t" "v_max_f32_dpp v182, v182, v182 row_ror:4 row_mask:0xf bank_mask:0xf\n\t" \
    "v_max_f32_dpp v183, v183, v183 row_ror:4 row_mask:0xf bank_mask:0xf\n\t" "s_nop 0\n\t" "v_max_f32_dpp v182, v182, v182 row_ror:2 row_mask:0xf bank_mask:0xf\n\t" \
    "v_max_f32_dpp v183, v183, v183 row_ror:2 row_mask:0xf bank_mask:0xf\n\t" "s_nop 0\n\t" "v_max_f32_dpp v182, v182, v182 row_ror:1 row_mask:0xf bank_mask:0xf\n\t" \
    "v_max_f32_dpp v183, v183, v183 row_ror:1 row_mask:0xf bank_mask:0xf\n\t" "s_waitcnt lgkmcnt(2)\n\t" \
    "v_smfmac_f32_16x16x64_bf16 v[224:227], v[216:219], v[160:167], %[ix]\n\t" "s_waitcnt lgkmcnt(0)\n\t" \
    "v_smfmac_f32_16x16x64_bf16 v[224:227], v[220:223], v[168:175], %[ix]\n\t" "s_nop 7\n\t" "v_add_f32 v176, v224, v225\n\t" "v_add_f32 v177, v226, v227\n\t" \
    "v_max_f32 v182, v176, v182\n\t" "v_max_f32 v183, v177, v183\n\t" "s_nop 0\n\t" "v_max_f32_dpp v182, v182, v182 row_ror:4 row_mask:0xf bank_mask:0xf\n\t" \
    "v_max_f32_dpp v183, v183, v183 row_ror:4 row_mask:0xf bank_mask:0xf\n\t" "s_nop 0\n\t" "v_max_f32_dpp v182, v182, v182 row_ror:2 row_mask:0xf bank_mask:0xf\n\t" \
    "v_max_f32_dpp v183, v183, v183 row_ror:2 row_mask:0xf bank_mask:0xf\n\t" "s_nop 0\n\t" "v_max_f32_dpp v182, v182, v182 row_ror:1 row_mask:0xf bank_mask:0xf\n\t" \
    "v_max_f32_dpp v183, v183, v183 row_ror:1 row_mask:0xf bank_mask:0xf\n\t" "v_pk_add_f32 v[178:179], v[178:179], v[182:183] neg_lo:[0,1] neg_hi:[0,1]\n\t" \
    "v_pk_add_f32 v[180:181], v[180:181], v[182:183] neg_lo:[0,1] neg_hi:[0,1]\n\t" "v_pk_add_f32 v[176:177], v[176:177], v[182:183] neg_lo:[0,1] neg_hi:[0,1]\n\t" \
    "v_pk_mul_f32 v[178:179], v[178:179], s[66:67]\n\t" "v_pk_mul_f32 v[180:181], v[180:181], s[66:67]\n\t" "v_pk_mul_f32 v[176:177], v[176:177], s[66:67]\n\t" \
    "v_exp_f32 v178, v178\n\t" "v_exp_f32 v179, v179\n\t" "v_exp_f32 v180, v180\n\t" "v_exp_f32 v181, v181\n\t" "v_exp_f32 v176, v176\n\t" \
    "v_pk_add_f32 v[182:183], v[178:179], v[180:181]\n\t" "v_exp_f32 v177, v177\n\t" "s_nop 0\n\t" "v_add_f32_dpp v182, v182, v182 row_ror:8 row_mask:0xf bank_mask:0xf\n\t" \
    "v_add_f32_dpp v183, v183, v183 row_ror:8 row_mask:0xf bank_mask:0xf\n\t" "v_add_f32 v182, v182, v176\n\t" "v_add_f32 v183, v183, v177\n\t" "s_nop 0\n\t" \
    "v_add_f32_dpp v182, v182, v182 row_ror:4 row_mask:0xf bank_mask:0xf\n\t" "v_add_f32_dpp v183, v183, v183 row_ror:4 row_mask:0xf bank_mask:0xf\n\t" "s_nop 0\n\t" \
    "v_add_f32_dpp v182, v182, v182 row_ror:2 row_mask:0xf bank_mask:0xf\n\t" "v_add_f32_dpp v183, v183, v183 row_ror:2 row_mask:0xf bank_mask:0xf\n\t" "s_nop 0\n\t" \
    "v_add_f32_dpp v182, v182, v182 row_ror:1 row_mask:0xf bank_mask:0xf\n\t" "v_add_f32_dpp v183, v183, v183 row_ror:1 row_mask:0xf bank_mask:0xf\n\t" \
    "v_rcp_f32 v182, v182\n\t" "v_rcp_f32 v183, v183\n\t" "s_nop 0\n\t" "v_pk_mul_f32 v[176:177], v[176:177], v[182:183]\n\t" \
    "v_pk_mul_f32 v[178:179], v[178:179], v[182:183]\n\t" "v_pk_mul_f32 v[180:181], v[180:181], v[182:183]\n\t" "v_cvt_pk_bf16_f32 v176, v176, v177\n\t" \
    "v_cvt_pk_bf16_f32 v178, v178, v179\n\t" "v_cvt_pk_bf16_f32 v180, v180, v181\n\t" "v_mov_b32_dpp v177, v176 quad_perm:[1,0,3,2] row_mask:0xf bank_mask:0xf\n\t" \
    "v_mov_b32_dpp v179, v178 quad_perm:[1,0,3,2] row_mask:0xf bank_mask:0xf\n\t" "v_mov_b32_dpp v181, v180 quad_perm:[1,0,3,2] row_mask:0xf bank_mask:0xf\n\t" \
    "v_perm_b32 v184, v179, v178, %[ps]\n\t" "v_perm_b32 v185, v181, v180, %[ps]\n\t" "buffer_store_dwordx2 v[184:185], %[px], %[rs], %[pb] offen sc1\n\t" \
    "v_perm_b32 v186, v177, v176, %[ps]\n\t" "buffer_store_dword v186, %[pm], %[rs], %[pb] offen sc1\n\t" XT(96, 97) LMARK "s_barrier\n\t" "s_branch 99f\n\t" "11:\n\t" \
    "ds_read_b128 v[220:223], %[l1] offset:0\n\t" "s_waitcnt lgkmcnt(0)\n\t" "v_max3_u32 v218, v220, v221, v222\n\t" "v_max_u32 v218, v218, v223\n\t" \
    "ds_read_b128 v[220:223], %[l1] offset:1024\n\t" "s_waitcnt lgkmcnt(0)\n\t" "v_max3_u32 v218, v218, v220, v221\n\t" "v_max3_u32 v218, v218, v222, v223\n\t" \
    "ds_read_b128 v[220:223], %[l1] offset:2048\n\t" "s_waitcnt lgkmcnt(0)\n\t" "v_max3_u32 v218, v218, v220, v221\n\t" "v_max3_u32 v218, v218, v222, v223\n\t" \
    "ds_read_b128 v[220:223], %[l1] offset:3072\n\t" "s_waitcnt lgkmcnt(0)\n\t" "v_max3_u32 v218, v218, v220, v221\n\t" "v_max3_u32 v218, v218, v222, v223\n\t" \
    "v_cmp_eq_u32 vcc, -1, v218\n\t" "s_cbranch_vccz 12b\n\t" "s_cmp_eq_u32 %[rt], 1\n\t" "s_cbranch_scc1 12b\n\t" "v_mov_b32 v218, %[rt]\n\t" "ds_write_b32 v219, v218\n\t" \
    "s_waitcnt lgkmcnt(0)\n\t" "s_branch 12b\n\t" "13:\n\t" "s_sub_u32 %[rt], %[rt], 1\n\t" "s_waitcnt lgkmcnt(0)\n\t" XCNT(100) "14:\n\t" \
    "buffer_load_dwordx4 v[240:243], %[mo], %[rs], %[sb] offen sc1\n\t" "buffer_load_dwordx4 v[244:247], v216, %[rs], %[sb] offen sc1\n\t" \
    "buffer_load_dwordx4 v[248:251], %[mo], %[rs], s60 offen sc1\n\t" "buffer_load_dwordx4 v[252:255], v216, %[rs], s60 offen sc1\n\t" "s_waitcnt vmcnt(0)\n\t" \
    "v_max_u32 v218, v240, v241\n\t" "v_max3_u32 v218, v218, v242, v243\n\t" "v_max3_u32 v218, v218, v244, v245\n\t" "v_max3_u32 v218, v218, v246, v247\n\t" \
    "v_max3_u32 v218, v218, v248, v249\n\t" "v_max3_u32 v218, v218, v250, v251\n\t" "v_max3_u32 v218, v218, v252, v253\n\t" "v_max3_u32 v218, v218, v254, v255\n\t" \
    "v_cmp_eq_u32 vcc, -1, v218\n\t" "s_cbranch_vccz 15f\n\t" "s_cmp_eq_u32 %[bd], 0\n\t" "s_cbranch_scc1 15f\n\t" "s_sub_u32 %[bd], %[bd], 1\n\t" XCNT(101) \
    "s_branch 14b\n\t" "15:\n\t" "ds_read_b128 v[160:163], %[l0] offset:0\n\t" "ds_read_b128 v[164:167], %[l0] offset:1024\n\t" \
    "ds_read_b128 v[168:171], %[l0] offset:2048\n\t" "ds_read_b128 v[172:175], %[l0] offset:3072\n\t" "ds_read_b128 v[176:179], %[l0] offset:4096\n\t" \
    "ds_read_b128 v[180:183], %[l0] offset:5120\n\t" "ds_read_b128 v[184:187], %[l0] offset:6144\n\t" "ds_read_b128 v[188:191], %[l0] offset:7168\n\t" \
    "v_mov_b64 v[224:225], 0\n\t" "v_mov_b64 v[226:227], 0\n\t" "v_mov_b64 v[228:229], 0\n\t" "v_mov_b64 v[230:231], 0\n\t" "v_mov_b64 v[232:233], 0\n\t" \
    "v_mov_b64 v[234:235], 0\n\t" "v_mov_b64 v[236:237], 0\n\t" "v_mov_b64 v[238:239], 0\n\t" "s_barrier\n\t" "s_branch 10b\n\t" "99:\n\t"
#define QK_STEP1 \
    "v_xor_b32 v216, 0x400, %[mo]\n\t" "s_xor_b32 s60, %[sb], 0x800\n\t" "s_add_u32 s61, %[qb], 0x1000\n\t" "s_mov_b32 s66, 0x3e0293ee\n\t" "s_mov_b32 s67, 0x3e0293ee\n\t" \
    XT(80, 81) XT(98, 99) "s_barrier\n\t" XT(82, 83) "buffer_load_dwordx4 v[240:243], %[mo], %[rs], %[sb] offen sc1\n\t" "v_mov_b64 v[224:225], 0\n\t" \
    "v_mov_b64 v[226:227], 0\n\t" "v_mov_b64 v[228:229], 0\n\t" "v_mov_b64 v[230:231], 0\n\t" "v_mov_b64 v[232:233], 0\n\t" "v_mov_b64 v[234:235], 0\n\t" \
    "v_mov_b64 v[236:237], 0\n\t" "v_mov_b64 v[238:239], 0\n\t" "buffer_load_dwordx4 v[244:247], v216, %[rs], %[sb] offen sc1\n\t" \
    "ds_read_b128 v[160:163], %[l0] offset:0\n\t" "ds_read_b128 v[164:167], %[l0] offset:1024\n\t" "ds_read_b128 v[168:171], %[l0] offset:2048\n\t" \
    "ds_read_b128 v[172:175], %[l0] offset:3072\n\t" "s_nop 3\n\t" "buffer_load_dwordx4 v[248:251], %[mo], %[rs], s60 offen sc1\n\t" \
    "ds_read_b128 v[176:179], %[l0] offset:4096\n\t" "ds_read_b128 v[180:183], %[l0] offset:5120\n\t" "ds_read_b128 v[184:187], %[l0] offset:6144\n\t" \
    "ds_read_b128 v[188:191], %[l0] offset:7168\n\t" "s_nop 3\n\t" "buffer_load_dwordx4 v[252:255], v216, %[rs], s60 offen sc1\n\t" "v_mov_b32 v219, 160512\n\t" \
    "v_mov_b64 v[220:221], -1\n\t" "v_mov_b64 v[222:223], -1\n\t" "buffer_store_dwordx2 v[220:221], %[px], %[rs], %[tr] offen sc1\n\t" \
    "buffer_store_dwordx2 v[220:221], %[pm], %[rs], %[tr] offen sc1\n\t" "v_mov_b32 v208, 0xd028965f\n\t" "v_mov_b32 v209, 0xff800000\n\t" "s_mov_b32 s62, 0\n\t" \
    "s_mov_b32 s63, -1\n\t" "v_mov_b32 v210, 0\n\t" "v_cmp_gt_i32 vcc, 0, %[ps]\n\t" "v_cndmask_b32 v204, 0, v209, s[62:63]\n\t" "s_nop 0\n\t" \
    "v_cndmask_b32 v200, 0, v208, vcc\n\t" "v_cmp_gt_i32 vcc, 1, %[ps]\n\t" "v_cndmask_b32 v205, 0, v209, s[62:63]\n\t" "s_nop 0\n\t" "v_cndmask_b32 v201, 0, v208, vcc\n\t" \
    "v_cmp_gt_i32 vcc, 2, %[ps]\n\t" "v_cndmask_b32 v206, 0, v209, s[62:63]\n\t" "s_nop 0\n\t" "v_cndmask_b32 v202, 0, v208, vcc\n\t" "v_cmp_gt_i32 vcc, 3, %[ps]\n\t" \
    "v_cndmask_b32 v207, 0, v209, s[62:63]\n\t" "s_nop 0\n\t" "v_cndmask_b32 v203, 0, v208, vcc\n\t" "20:\n\t" "s_waitcnt vmcnt(5)\n\t" XT(84, 85) \
    "v_smfmac_f32_16x16x64_bf16 v[224:227], v[240:243], a[0:7], %[ix]\n\t" "ds_write_b128 %[l1], v[240:243] offset:4096\n\t" \
    "v_smfmac_f32_16x16x64_bf16 v[228:231], v[240:243], a[8:15], %[ix]\n\t" "v_smfmac_f32_16x16x64_bf16 v[232:235], v[240:243], v[32:39], %[ix]\n\t" \
    "s_waitcnt lgkmcnt(7)\n\t" "v_smfmac_f32_16x16x64_bf16 v[236:239], v[240:243], v[160:167], %[ix]\n\t" "ds_read_b128 v[160:163], %[l0] offset:8192\n\t" \
    "ds_read_b128 v[164:167], %[l0] offset:9216\n\t" "s_waitcnt vmcnt(4)\n\t" "v_smfmac_f32_16x16x64_bf16 v[224:227], v[244:247], a[16:23], %[ix]\n\t" \
    "ds_write_b128 %[l1], v[244:247] offset:5120\n\t" "v_smfmac_f32_16x16x64_bf16 v[228:231], v[244:247], a[24:31], %[ix]\n\t" \
    "v_smfmac_f32_16x16x64_bf16 v[232:235], v[244:247], v[40:47], %[ix]\n\t" "s_waitcnt lgkmcnt(8)\n\t" \
    "v_smfmac_f32_16x16x64_bf16 v[236:239], v[244:247], v[168:175], %[ix]\n\t" "ds_read_b128 v[168:171], %[l0] offset:10240\n\t" \
    "ds_read_b128 v[172:175], %[l0] offset:11264\n\t" "s_waitcnt vmcnt(3)\n\t" "v_smfmac_f32_16x16x64_bf16 v[224:227], v[248:251], a[32:39], %[ix]\n\t" \
    "ds_write_b128 %[l1], v[248:251] offset:6144\n\t" "v_smfmac_f32_16x16x64_bf16 v[228:231], v[248:251], a[40:47], %[ix]\n\t" "s_waitcnt lgkmcnt(3)\n\t" "s_barrier\n\t" \
    XT(86, 87) "v_smfmac_f32_16x16x64_bf16 v[232:235], v[248:251], v[48:55], %[ix]\n\t" "ds_read_b128 v[192:195], %[l1] offset:8192\n\t" \
    "ds_read_b128 v[196:199], %[l1] offset:12288\n\t" "v_smfmac_f32_16x16x64_bf16 v[236:239], v[248:251], v[176:183], %[ix]\n\t" \
    "ds_read_b128 v[176:179], %[l0] offset:12288\n\t" "ds_read_b128 v[180:183], %[l0] offset:13312\n\t" "s_waitcnt vmcnt(2)\n\t" \
    "v_smfmac_f32_16x16x64_bf16 v[224:227], v[252:255], a[48:55], %[ix]\n\t" "ds_write_b128 %[l1], v[252:255] offset:7168\n\t" \
    "v_smfmac_f32_16x16x64_bf16 v[228:231], v[252:255], a[56:63], %[ix]\n\t" "ds_read_b128 v[240:243], %[l1] offset:0\n\t" "ds_read_b128 v[244:247], %[l1] offset:9216\n\t" \
    "v_smfmac_f32_16x16x64_bf16 v[232:235], v[252:255], v[56:63], %[ix]\n\t" "s_nop 1\n\t" "v_smfmac_f32_16x16x64_bf16 v[236:239], v[252:255], v[184:191], %[ix]\n\t" \
    "ds_read_b128 v[184:187], %[l0] offset:14336\n\t" "v_cmp_u_f32 vcc, v224, v225\n\t" "v_cmp_u_f32 s[62:63], v226, v227\n\t" "s_waitcnt lgkmcnt(7)\n\t" \
    "v_smfmac_f32_16x16x64_bf16 v[224:227], v[192:195], a[64:71], %[ix]\n\t" "ds_read_b128 v[188:191], %[l0] offset:15360\n\t" \
    "v_smfmac_f32_16x16x64_bf16 v[228:231], v[192:195], a[72:79], %[ix]\n\t" "s_or_b64 vcc, vcc, s[62:63]\n\t" "s_cbranch_scc1 21f\n\t" "22:\n\t" \
    "v_smfmac_f32_16x16x64_bf16 v[232:235], v[192:195], v[64:71], %[ix]\n\t" "s_waitcnt lgkmcnt(4)\n\t" "s_barrier\n\t" XT(88, 89) "ds_read_b32 v218, v219\n\t" \
    "v_smfmac_f32_16x16x64_bf16 v[236:239], v[192:195], v[160:167], %[ix]\n\t" "ds_read_b128 v[160:163], %[l0] offset:16384\n\t" \
    "ds_read_b128 v[164:167], %[l0] offset:17408\n\t" "v_smfmac_f32_16x16x64_bf16 v[224:227], v[196:199], a[80:87], %[ix]\n\t" \
    "ds_read_b128 v[248:251], %[l1] offset:13312\n\t" "ds_read_b128 v[252:255], %[l1] offset:1024\n\t" \
    "v_smfmac_f32_16x16x64_bf16 v[228:231], v[196:199], a[88:95], %[ix]\n\t" "ds_read_b128 v[192:195], %[l1] offset:10240\n\t" \
    "v_smfmac_f32_16x16x64_bf16 v[232:235], v[196:199], v[72:79], %[ix]\n\t" "v_smfmac_f32_16x16x64_bf16 v[236:239], v[196:199], v[168:175], %[ix]\n\t" \
    "s_waitcnt lgkmcnt(5)\n\t" "v_cmp_eq_u32 vcc, %[rt], v218\n\t" "v_smfmac_f32_16x16x64_bf16 v[224:227], v[240:243], a[96:103], %[ix]\n\t" "s_cbranch_vccnz 23f\n\t" \
    "ds_read_b128 v[168:171], %[l0] offset:18432\n\t" "v_smfmac_f32_16x16x64_bf16 v[228:231], v[240:243], a[104:111], %[ix]\n\t" \
    "ds_read_b128 v[172:175], %[l0] offset:19456\n\t" "ds_read_b128 v[196:199], %[l1] offset:14336\n\t" \
    "v_smfmac_f32_16x16x64_bf16 v[232:235], v[240:243], v[80:87], %[ix]\n\t" "buffer_load_dwordx4 v[16:19], %[hq], %[rs], %[qb] offen offset:0 sc1\n\t" \
    "v_smfmac_f32_16x16x64_bf16 v[236:239], v[240:243], v[176:183], %[ix]\n\t" "ds_read_b128 v[176:179], %[l0] offset:20480\n\t" \
    "ds_read_b128 v[180:183], %[l0] offset:21504\n\t" "v_smfmac_f32_16x16x64_bf16 v[224:227], v[244:247], a[112:119], %[ix]\n\t" \
    "ds_read_b128 v[240:243], %[l1] offset:2048\n\t" "v_smfmac_f32_16x16x64_bf16 v[228:231], v[244:247], a[120:127], %[ix]\n\t" \
    "v_smfmac_f32_16x16x64_bf16 v[232:235], v[244:247], v[88:95], %[ix]\n\t" "buffer_load_dwordx4 v[20:23], %[hq], %[rs], %[qb] offen offset:2048 sc1\n\t" \
    "v_smfmac_f32_16x16x64_bf16 v[236:239], v[244:247], v[184:191], %[ix]\n\t" "ds_read_b128 v[184:187], %[l0] offset:22528\n\t" \
    "ds_read_b128 v[188:191], %[l0] offset:23552\n\t" "s_waitcnt lgkmcnt(10)\n\t" "v_smfmac_f32_16x16x64_bf16 v[224:227], v[248:251], a[128:135], %[ix]\n\t" \
    "ds_read_b128 v[244:247], %[l1] offset:11264\n\t" "v_smfmac_f32_16x16x64_bf16 v[228:231], v[248:251], a[136:143], %[ix]\n\t" \
    "v_smfmac_f32_16x16x64_bf16 v[232:235], v[248:251], v[96:103], %[ix]\n\t" "buffer_load_dwordx4 v[24:27], %[hq], %[rs], s61 offen offset:0 sc1\n\t" \
    "v_smfmac_f32_16x16x64_bf16 v[236:239], v[248:251], v[160:167], %[ix]\n\t" "ds_read_b128 v[160:163], %[l0] offset:24576\n\t" \
    "ds_read_b128 v[164:167], %[l0] offset:25600\n\t" "s_waitcnt lgkmcnt(12)\n\t" "v_smfmac_f32_16x16x64_bf16 v[224:227], v[252:255], a[144:151], %[ix]\n\t" \
    "ds_read_b128 v[248:251], %[l1] offset:15360\n\t" "v_smfmac_f32_16x16x64_bf16 v[228:231], v[252:255], a[152:159], %[ix]\n\t" \
    "v_smfmac_f32_16x16x64_bf16 v[232:235], v[252:255], v[104:111], %[ix]\n\t" "buffer_load_dwordx4 v[28:31], %[hq], %[rs], s61 offen offset:2048 sc1\n\t" \
    "s_waitcnt lgkmcnt(10)\n\t" "v_smfmac_f32_16x16x64_bf16 v[236:239], v[252:255], v[168:175], %[ix]\n\t" "ds_read_b128 v[168:171], %[l0] offset:26624\n\t" \
    "ds_read_b128 v[172:175], %[l0] offset:27648\n\t" "v_smfmac_f32_16x16x64_bf16 v[224:227], v[192:195], a[160:167], %[ix]\n\t" \
    "ds_read_b128 v[252:255], %[l1] offset:3072\n\t" "v_smfmac_f32_16x16x64_bf16 v[228:231], v[192:195], a[168:175], %[ix]\n\t" "s_waitcnt lgkmcnt(12)\n\t" \
    "v_smfmac_f32_16x16x64_bf16 v[224:227], v[196:199], a[176:183], %[ix]\n\t" "v_smfmac_f32_16x16x64_bf16 v[228:231], v[196:199], a[184:191], %[ix]\n\t" \
    "s_waitcnt lgkmcnt(9)\n\t" "v_smfmac_f32_16x16x64_bf16 v[224:227], v[240:243], a[192:199], %[ix]\n\t" \
    "v_smfmac_f32_16x16x64_bf16 v[228:231], v[240:243], a[200:207], %[ix]\n\t" "s_waitcnt lgkmcnt(6)\n\t" \
    "v_smfmac_f32_16x16x64_bf16 v[224:227], v[244:247], a[208:215], %[ix]\n\t" "v_smfmac_f32_16x16x64_bf16 v[228:231], v[244:247], a[216:223], %[ix]\n\t" \
    "s_waitcnt vmcnt(4)\n\t" "s_waitcnt lgkmcnt(3)\n\t" "v_smfmac_f32_16x16x64_bf16 v[224:227], v[248:251], a[224:231], %[ix]\n\t" \
    "v_smfmac_f32_16x16x64_bf16 v[228:231], v[248:251], a[232:239], %[ix]\n\t" "s_waitcnt lgkmcnt(0)\n\t" \
    "v_smfmac_f32_16x16x64_bf16 v[224:227], v[252:255], a[240:247], %[ix]\n\t" "s_nop 1\n\t" "v_smfmac_f32_16x16x64_bf16 v[228:231], v[252:255], a[248:255], %[ix]\n\t" \
    XT(90, 91) "s_nop 1\n\t" "v_smfmac_f32_16x16x64_bf16 v[232:235], v[192:195], v[112:119], %[ix]\n\t" "s_nop 1\n\t" \
    "v_smfmac_f32_16x16x64_bf16 v[236:239], v[192:195], v[176:183], %[ix]\n\t" "ds_read_b128 v[176:179], %[l0] offset:28672\n\t" "v_add_f32 v224, v224, v225\n\t" \
    "v_add_f32 v226, v226, v227\n\t" "v_smfmac_f32_16x16x64_bf16 v[232:235], v[196:199], v[120:127], %[ix]\n\t" "ds_read_b128 v[180:183], %[l0] offset:29696\n\t" \
    "v_add_f32 v228, v228, v229\n\t" "v_add_f32 v230, v230, v231\n\t" "v_smfmac_f32_16x16x64_bf16 v[236:239], v[196:199], v[184:191], %[ix]\n\t" \
    "ds_read_b128 v[184:187], %[l0] offset:30720\n\t" "v_cvt_pk_bf16_f32 v218, v224, v228\n\t" "v_cvt_pk_bf16_f32 v219, v226, v230\n\t" \
    "v_smfmac_f32_16x16x64_bf16 v[232:235], v[240:243], v[128:135], %[ix]\n\t" "ds_write2_b32 %[qw], v218, v219 offset1:8\n\t" \
    "ds_read_b128 v[188:191], %[l0] offset:31744\n\t" "v_smfmac_f32_16x16x64_bf16 v[236:239], v[240:243], v[160:167], %[ix]\n\t" \
    "v_smfmac_f32_16x16x64_bf16 v[232:235], v[244:247], v[136:143], %[ix]\n\t" "v_smfmac_f32_16x16x64_bf16 v[236:239], v[244:247], v[168:175], %[ix]\n\t" \
    "s_waitcnt lgkmcnt(1)\n\t" "s_barrier\n\t" "v_smfmac_f32_16x16x64_bf16 v[232:235], v[248:251], v[144:151], %[ix]\n\t" \
    "v_smfmac_f32_16x16x64_bf16 v[236:239], v[248:251], v[176:183], %[ix]\n\t" "v_smfmac_f32_16x16x64_bf16 v[232:235], v[252:255], v[152:159], %[ix]\n\t" \
    "s_waitcnt lgkmcnt(0)\n\t" "v_smfmac_f32_16x16x64_bf16 v[236:239], v[252:255], v[184:191], %[ix]\n\t" XT(92, 93) "s_nop 6\n\t" "v_add_f32 v232, v232, v233\n\t" \
    "v_add_f32 v234, v234, v235\n\t" "v_add_f32 v236, v236, v237\n\t" "v_add_f32 v238, v238, v239\n\t" "v_cvt_pk_bf16_f32 v232, v232, v236\n\t" \
    "v_cvt_pk_bf16_f32 v233, v234, v238\n\t" "ds_write2_b32 %[kw], v232, v233 offset1:68\n\t" "s_waitcnt lgkmcnt(0)\n\t" "s_barrier\n\t" XT(94, 95) \
    "s_cmp_ge_u32 %[sq], %[sn]\n\t" "s_cbranch_scc1 277f\n\t" "275:\n\t" "global_load_dword v240, v210, %[fq] sc1\n\t" "s_waitcnt vmcnt(0)\n\t" \
    "v_readfirstlane_b32 %[sq], v240\n\t" "s_nop 3\n\t" "s_cmp_ge_u32 %[sq], %[sn]\n\t" "s_cbranch_scc1 276f\n\t" "s_cmp_eq_u32 %[bd], 0\n\t" "s_cbranch_scc1 276f\n\t" \
    "s_sub_u32 %[bd], %[bd], 1\n\t" "s_sleep 2\n\t" "s_branch 275b\n\t" "276:\n\t" "buffer_load_dwordx4 v[16:19], %[hq], %[rs], %[qb] offen offset:0 sc1\n\t" \
    "buffer_load_dwordx4 v[20:23], %[hq], %[rs], %[qb] offen offset:2048 sc1\n\t" "buffer_load_dwordx4 v[24:27], %[hq], %[rs], s61 offen offset:0 sc1\n\t" \
    "buffer_load_dwordx4 v[28:31], %[hq], %[rs], s61 offen offset:2048 sc1\n\t" "277:\n\t" "s_nop 3\n\t" "ds_read_b128 v[160:163], %[kx] offset:0\n\t" \
    "ds_read_b128 v[164:167], %[kx] offset:2048\n\t" "ds_read_b128 v[168:171], %[kx] offset:4096\n\t" "ds_read_b128 v[172:175], %[kx] offset:6144\n\t" \
    "ds_read_b128 v[224:227], %[km] offset:0\n\t" "ds_read_b128 v[228:231], %[km] offset:64\n\t" "ds_read_b128 v[232:235], %[km] offset:128\n\t" \
    "ds_read_b128 v[236:239], %[km] offset:192\n\t" "s_waitcnt vmcnt(0)\n\t" "global_load_dword v240, v210, %[fq] sc1\n\t" "s_waitcnt lgkmcnt(7)\n\t" \
    "v_mfma_f32_16x16x32_bf16 v[248:251], v[160:163], v[16:19], v[200:203]\n\t" "s_waitcnt lgkmcnt(6)\n\t" \
    "v_mfma_f32_16x16x32_bf16 v[248:251], v[164:167], v[20:23], v[248:251]\n\t" "s_waitcnt lgkmcnt(5)\n\t" \
    "v_mfma_f32_16x16x32_bf16 v[248:251], v[168:171], v[24:27], v[248:251]\n\t" "s_waitcnt lgkmcnt(4)\n\t" \
    "v_mfma_f32_16x16x32_bf16 v[248:251], v[172:175], v[28:31], v[248:251]\n\t" "s_waitcnt lgkmcnt(3)\n\t" \
    "v_mfma_f32_16x16x32_bf16 v[244:247], v[224:227], v[16:19], v[204:207]\n\t" "s_waitcnt lgkmcnt(2)\n\t" \
    "v_mfma_f32_16x16x32_bf16 v[244:247], v[228:231], v[20:23], v[244:247]\n\t" "s_waitcnt lgkmcnt(1)\n\t" \
    "v_mfma_f32_16x16x32_bf16 v[244:247], v[232:235], v[24:27], v[244:247]\n\t" "s_waitcnt lgkmcnt(0)\n\t" \
    "v_mfma_f32_16x16x32_bf16 v[244:247], v[236:239], v[28:31], v[244:247]\n\t" "s_nop 3\n\t" "v_max3_f32 v242, v248, v249, v250\n\t" "v_max_f32 v242, v242, v251\n\t" \
    "s_nop 1\n\t" "v_max3_f32 v242, v242, v244, v245\n\t" "v_max3_f32 v242, v242, v246, v247\n\t" "v_mov_b32 v243, v242\n\t" "s_nop 1\n\t" \
    "v_permlane32_swap_b32 v242, v243\n\t" "v_max_f32 v242, v242, v243\n\t" "v_mov_b32 v243, v242\n\t" "s_nop 1\n\t" "v_permlane16_swap_b32 v242, v243\n\t" \
    "v_max_f32 v242, v242, v243\n\t" "v_pk_add_f32 v[244:245], v[244:245], v[242:243] op_sel_hi:[1,0] neg_lo:[0,1] neg_hi:[0,1]\n\t" \
    "v_pk_add_f32 v[246:247], v[246:247], v[242:243] op_sel_hi:[1,0] neg_lo:[0,1] neg_hi:[0,1]\n\t" \
    "v_pk_add_f32 v[248:249], v[248:249], v[242:243] op_sel_hi:[1,0] neg_lo:[0,1] neg_hi:[0,1]\n\t" \
    "v_pk_add_f32 v[250:251], v[250:251], v[242:243] op_sel_hi:[1,0] neg_lo:[0,1] neg_hi:[0,1]\n\t" "v_pk_mul_f32 v[244:245], v[244:245], s[66:67]\n\t" \
    "v_pk_mul_f32 v[246:247], v[246:247], s[66:67]\n\t" "v_pk_mul_f32 v[248:249], v[248:249], s[66:67]\n\t" "v_pk_mul_f32 v[250:251], v[250:251], s[66:67]\n\t" \
    "v_exp_f32 v244, v244\n\t" "v_exp_f32 v245, v245\n\t" "v_exp_f32 v246, v246\n\t" "v_exp_f32 v247, v247\n\t" "v_exp_f32 v248, v248\n\t" "v_exp_f32 v249, v249\n\t" \
    "v_exp_f32 v250, v250\n\t" "v_exp_f32 v251, v251\n\t" "v_pk_add_f32 v[242:243], v[244:245], v[246:247]\n\t" "v_pk_add_f32 v[242:243], v[242:243], v[248:249]\n\t" \
    "v_pk_add_f32 v[242:243], v[242:243], v[250:251]\n\t" "v_add_f32 v242, v242, v243\n\t" "v_mov_b32 v243, v242\n\t" "s_nop 1\n\t" "v_permlane32_swap_b32 v242, v243\n\t" \
    "v_add_f32 v242, v242, v243\n\t" "v_mov_b32 v243, v242\n\t" "s_nop 1\n\t" "v_permlane16_swap_b32 v242, v243\n\t" "v_add_f32 v242, v242, v243\n\t" \
    "v_rcp_f32 v242, v242\n\t" "s_nop 0\n\t" "v_pk_mul_f32 v[244:245], v[244:245], v[242:243] op_sel_hi:[1,0]\n\t" \
    "v_pk_mul_f32 v[246:247], v[246:247], v[242:243] op_sel_hi:[1,0]\n\t" "v_pk_mul_f32 v[248:249], v[248:249], v[242:243] op_sel_hi:[1,0]\n\t" \
    "v_pk_mul_f32 v[250:251], v[250:251], v[242:243] op_sel_hi:[1,0]\n\t" "v_cvt_pk_bf16_f32 v162, v244, v245\n\t" "v_cvt_pk_bf16_f32 v163, v246, v247\n\t" \
    "v_cvt_pk_bf16_f32 v164, v248, v249\n\t" "v_cvt_pk_bf16_f32 v165, v250, v251\n\t" "s_barrier\n\t" "buffer_store_dwordx2 v[164:165], %[px], %[rs], %[tb] offen sc1\n\t" \
    "buffer_store_dwordx2 v[162:163], %[pm], %[rs], %[tb] offen sc1\n\t" "s_waitcnt vmcnt(2)\n\t" "v_readfirstlane_b32 s62, v240\n\t" "s_nop 3\n\t" \
    "s_max_u32 %[sq], %[sq], s62\n\t" XT(96, 97) "s_branch 99f\n\t" "21:\n\t" "ds_read_b128 v[220:223], %[l1] offset:4096\n\t" "s_waitcnt lgkmcnt(0)\n\t" \
    "v_max3_u32 v218, v220, v221, v222\n\t" "v_max_u32 v218, v218, v223\n\t" "ds_read_b128 v[220:223], %[l1] offset:5120\n\t" "s_waitcnt lgkmcnt(0)\n\t" \
    "v_max3_u32 v218, v218, v220, v221\n\t" "v_max3_u32 v218, v218, v222, v223\n\t" "ds_read_b128 v[220:223], %[l1] offset:6144\n\t" "s_waitcnt lgkmcnt(0)\n\t" \
    "v_max3_u32 v218, v218, v220, v221\n\t" "v_max3_u32 v218, v218, v222, v223\n\t" "ds_read_b128 v[220:223], %[l1] offset:7168\n\t" "s_waitcnt lgkmcnt(0)\n\t" \
    "v_max3_u32 v218, v218, v220, v221\n\t" "v_max3_u32 v218, v218, v222, v223\n\t" "v_cmp_eq_u32 vcc, -1, v218\n\t" "s_cbranch_vccz 22b\n\t" "s_cmp_eq_u32 %[rt], 1\n\t" \
    "s_cbranch_scc1 22b\n\t" "v_mov_b32 v218, %[rt]\n\t" "ds_write_b32 v219, v218\n\t" "s_waitcnt lgkmcnt(0)\n\t" "s_branch 22b\n\t" "23:\n\t" "s_sub_u32 %[rt], %[rt], 1\n\t" \
    "s_waitcnt lgkmcnt(0)\n\t" XCNT(100) "24:\n\t" "buffer_load_dwordx4 v[240:243], %[mo], %[rs], %[sb] offen sc1\n\t" \
    "buffer_load_dwordx4 v[244:247], v216, %[rs], %[sb] offen sc1\n\t" "buffer_load_dwordx4 v[248:251], %[mo], %[rs], s60 offen sc1\n\t" \
    "buffer_load_dwordx4 v[252:255], v216, %[rs], s60 offen sc1\n\t" "s_waitcnt vmcnt(0)\n\t" "v_max_u32 v218, v240, v241\n\t" "v_max3_u32 v218, v218, v242, v243\n\t" \
    "v_max3_u32 v218, v218, v244, v245\n\t" "v_max3_u32 v218, v218, v246, v247\n\t" "v_max3_u32 v218, v218, v248, v249\n\t" "v_max3_u32 v218, v218, v250, v251\n\t" \
    "v_max3_u32 v218, v218, v252, v253\n\t" "v_max3_u32 v218, v218, v254, v255\n\t" "v_cmp_eq_u32 vcc, -1, v218\n\t" "s_cbranch_vccz 25f\n\t" "s_cmp_eq_u32 %[bd], 0\n\t" \
    "s_cbranch_scc1 25f\n\t" "s_sub_u32 %[bd], %[bd], 1\n\t" XCNT(101) "s_branch 24b\n\t" "25:\n\t" "ds_read_b128 v[160:163], %[l0] offset:0\n\t" \
    "ds_read_b128 v[164:167], %[l0] offset:1024\n\t" "ds_read_b128 v[168:171], %[l0] offset:2048\n\t" "ds_read_b128 v[172:175], %[l0] offset:3072\n\t" \
    "ds_read_b128 v[176:179], %[l0] offset:4096\n\t" "ds_read_b128 v[180:183], %[l0] offset:5120\n\t" "ds_read_b128 v[184:187], %[l0] offset:6144\n\t" \
    "ds_read_b128 v[188:191], %[l0] offset:7168\n\t" "v_mov_b64 v[224:225], 0\n\t" "v_mov_b64 v[226:227], 0\n\t" "v_mov_b64 v[228:229], 0\n\t" "v_mov_b64 v[230:231], 0\n\t" \
    "v_mov_b64 v[232:233], 0\n\t" "v_mov_b64 v[234:235], 0\n\t" "v_mov_b64 v[236:237], 0\n\t" "v_mov_b64 v[238:239], 0\n\t" "s_barrier\n\t" "s_branch 20b\n\t" "99:\n\t"
#define QK_STEP2 \
    "v_xor_b32 v216, 0x400, %[mo]\n\t" "s_xor_b32 s60, %[sb], 0x800\n\t" "s_add_u32 s61, %[qb], 0x1000\n\t" "s_mov_b32 s66, 0x3e0293ee\n\t" "s_mov_b32 s67, 0x3e0293ee\n\t" \
    XT(80, 81) XT(98, 99) "s_barrier\n\t" XT(82, 83) "buffer_load_dwordx4 v[240:243], %[mo], %[rs], %[sb] offen sc1\n\t" "v_mov_b64 v[224:225], 0\n\t" \
    "v_mov_b64 v[226:227], 0\n\t" "v_mov_b64 v[228:229], 0\n\t" "v_mov_b64 v[230:231], 0\n\t" "v_mov_b64 v[232:233], 0\n\t" "v_mov_b64 v[234:235], 0\n\t" \
    "v_mov_b64 v[236:237], 0\n\t" "v_mov_b64 v[238:239], 0\n\t" "buffer_load_dwordx4 v[244:247], v216, %[rs], %[sb] offen sc1\n\t" \
    "ds_read_b128 v[160:163], %[l0] offset:0\n\t" "ds_read_b128 v[164:167], %[l0] offset:1024\n\t" "ds_read_b128 v[168:171], %[l0] offset:2048\n\t" \
    "ds_read_b128 v[172:175], %[l0] offset:3072\n\t" "s_nop 3\n\t" "buffer_load_dwordx4 v[248:251], %[mo], %[rs], s60 offen sc1\n\t" \
    "ds_read_b128 v[176:179], %[l0] offset:4096\n\t" "ds_read_b128 v[180:183], %[l0] offset:5120\n\t" "ds_read_b128 v[184:187], %[l0] offset:6144\n\t" \
    "ds_read_b128 v[188:191], %[l0] offset:7168\n\t" "s_nop 3\n\t" "buffer_load_dwordx4 v[252:255], v216, %[rs], s60 offen sc1\n\t" "v_mov_b32 v219, 160512\n\t" \
    "v_mov_b64 v[220:221], -1\n\t" "v_mov_b64 v[222:223], -1\n\t" "buffer_store_dwordx4 v[220:223], %[px], %[rs], %[tr] offen sc1\n\t" \
    "buffer_store_dwordx2 v[220:221], %[pm], %[rs], %[tr] offen sc1\n\t" "v_mov_b32 v208, 0xd028965f\n\t" "v_mov_b32 v209, 0xff800000\n\t" "s_mov_b32 s62, 0\n\t" \
    "s_mov_b32 s63, -1\n\t" "v_mov_b32 v210, 0\n\t" "v_cmp_gt_i32 vcc, 0, %[ps]\n\t" "v_cndmask_b32 v204, 0, v209, s[62:63]\n\t" "s_nop 0\n\t" \
    "v_cndmask_b32 v200, 0, v208, vcc\n\t" "v_cmp_gt_i32 vcc, 1, %[ps]\n\t" "v_cndmask_b32 v205, 0, v209, s[62:63]\n\t" "s_nop 0\n\t" "v_cndmask_b32 v201, 0, v208, vcc\n\t" \
    "v_cmp_gt_i32 vcc, 2, %[ps]\n\t" "v_cndmask_b32 v206, 0, v209, s[62:63]\n\t" "s_nop 0\n\t" "v_cndmask_b32 v202, 0, v208, vcc\n\t" "v_cmp_gt_i32 vcc, 3, %[ps]\n\t" \
    "v_cndmask_b32 v207, 0, v209, s[62:63]\n\t" "s_nop 0\n\t" "v_cndmask_b32 v203, 0, v208, vcc\n\t" "30:\n\t" "s_waitcnt vmcnt(5)\n\t" XT(84, 85) \
    "v_smfmac_f32_16x16x64_bf16 v[224:227], v[240:243], a[0:7], %[ix]\n\t" "ds_write_b128 %[l1], v[240:243] offset:8192\n\t" \
    "v_smfmac_f32_16x16x64_bf16 v[228:231], v[240:243], a[8:15], %[ix]\n\t" "v_smfmac_f32_16x16x64_bf16 v[232:235], v[240:243], v[32:39], %[ix]\n\t" \
    "s_waitcnt lgkmcnt(7)\n\t" "v_smfmac_f32_16x16x64_bf16 v[236:239], v[240:243], v[160:167], %[ix]\n\t" "ds_read_b128 v[160:163], %[l0] offset:8192\n\t" \
    "ds_read_b128 v[164:167], %[l0] offset:9216\n\t" "s_waitcnt vmcnt(4)\n\t" "v_smfmac_f32_16x16x64_bf16 v[224:227], v[244:247], a[16:23], %[ix]\n\t" \
    "ds_write_b128 %[l1], v[244:247] offset:9216\n\t" "v_smfmac_f32_16x16x64_bf16 v[228:231], v[244:247], a[24:31], %[ix]\n\t" \
    "v_smfmac_f32_16x16x64_bf16 v[232:235], v[244:247], v[40:47], %[ix]\n\t" "s_waitcnt lgkmcnt(8)\n\t" \
    "v_smfmac_f32_16x16x64_bf16 v[236:239], v[244:247], v[168:175], %[ix]\n\t" "ds_read_b128 v[168:171], %[l0] offset:10240\n\t" \
    "ds_read_b128 v[172:175], %[l0] offset:11264\n\t" "s_waitcnt vmcnt(3)\n\t" "v_smfmac_f32_16x16x64_bf16 v[224:227], v[248:251], a[32:39], %[ix]\n\t" \
    "ds_write_b128 %[l1], v[248:251] offset:10240\n\t" "v_smfmac_f32_16x16x64_bf16 v[228:231], v[248:251], a[40:47], %[ix]\n\t" "s_waitcnt lgkmcnt(3)\n\t" "s_barrier\n\t" \
    XT(86, 87) "v_smfmac_f32_16x16x64_bf16 v[232:235], v[248:251], v[48:55], %[ix]\n\t" "ds_read_b128 v[192:195], %[l1] offset:12288\n\t" \
    "ds_read_b128 v[196:199], %[l1] offset:0\n\t" "v_smfmac_f32_16x16x64_bf16 v[236:239], v[248:251], v[176:183], %[ix]\n\t" "ds_read_b128 v[176:179], %[l0] offset:12288\n\t" \
    "ds_read_b128 v[180:183], %[l0] offset:13312\n\t" "s_waitcnt vmcnt(2)\n\t" "v_smfmac_f32_16x16x64_bf16 v[224:227], v[252:255], a[48:55], %[ix]\n\t" \
    "ds_write_b128 %[l1], v[252:255] offset:11264\n\t" "v_smfmac_f32_16x16x64_bf16 v[228:231], v[252:255], a[56:63], %[ix]\n\t" \
    "ds_read_b128 v[240:243], %[l1] offset:4096\n\t" "ds_read_b128 v[244:247], %[l1] offset:13312\n\t" \
    "v_smfmac_f32_16x16x64_bf16 v[232:235], v[252:255], v[56:63], %[ix]\n\t" "s_nop 1\n\t" "v_smfmac_f32_16x16x64_bf16 v[236:239], v[252:255], v[184:191], %[ix]\n\t" \
    "ds_read_b128 v[184:187], %[l0] offset:14336\n\t" "v_cmp_u_f32 vcc, v224, v225\n\t" "v_cmp_u_f32 s[62:63], v226, v227\n\t" "s_waitcnt lgkmcnt(7)\n\t" \
    "v_smfmac_f32_16x16x64_bf16 v[224:227], v[192:195], a[64:71], %[ix]\n\t" "ds_read_b128 v[188:191], %[l0] offset:15360\n\t" \
    "v_smfmac_f32_16x16x64_bf16 v[228:231], v[192:195], a[72:79], %[ix]\n\t" "s_or_b64 vcc, vcc, s[62:63]\n\t" "s_cbranch_scc1 31f\n\t" "32:\n\t" \
    "v_smfmac_f32_16x16x64_bf16 v[232:235], v[192:195], v[64:71], %[ix]\n\t" "s_waitcnt lgkmcnt(4)\n\t" "s_barrier\n\t" XT(88, 89) "ds_read_b32 v218, v219\n\t" \
    "v_smfmac_f32_16x16x64_bf16 v[236:239], v[192:195], v[160:167], %[ix]\n\t" "ds_read_b128 v[160:163], %[l0] offset:16384\n\t" \
    "ds_read_b128 v[164:167], %[l0] offset:17408\n\t" "v_smfmac_f32_16x16x64_bf16 v[224:227], v[196:199], a[80:87], %[ix]\n\t" \
    "ds_read_b128 v[248:251], %[l1] offset:1024\n\t" "ds_read_b128 v[252:255], %[l1] offset:5120\n\t" "v_smfmac_f32_16x16x64_bf16 v[228:231], v[196:199], a[88:95], %[ix]\n\t" \
    "ds_read_b128 v[192:195], %[l1] offset:14336\n\t" "v_smfmac_f32_16x16x64_bf16 v[232:235], v[196:199], v[72:79], %[ix]\n\t" \
    "v_smfmac_f32_16x16x64_bf16 v[236:239], v[196:199], v[168:175], %[ix]\n\t" "s_waitcnt lgkmcnt(5)\n\t" "v_cmp_eq_u32 vcc, %[rt], v218\n\t" \
    "v_smfmac_f32_16x16x64_bf16 v[224:227], v[240:243], a[96:103], %[ix]\n\t" "s_cbranch_vccnz 33f\n\t" "ds_read_b128 v[168:171], %[l0] offset:18432\n\t" \
    "v_smfmac_f32_16x16x64_bf16 v[228:231], v[240:243], a[104:111], %[ix]\n\t" "ds_read_b128 v[172:175], %[l0] offset:19456\n\t" \
    "ds_read_b128 v[196:199], %[l1] offset:2048\n\t" "v_smfmac_f32_16x16x64_bf16 v[232:235], v[240:243], v[80:87], %[ix]\n\t" \
    "buffer_load_dwordx4 v[16:19], %[hq], %[rs], %[qb] offen offset:0 sc1\n\t" "v_smfmac_f32_16x16x64_bf16 v[236:239], v[240:243], v[176:183], %[ix]\n\t" \
    "ds_read_b128 v[176:179], %[l0] offset:20480\n\t" "ds_read_b128 v[180:183], %[l0] offset:21504\n\t" \
    "v_smfmac_f32_16x16x64_bf16 v[224:227], v[244:247], a[112:119], %[ix]\n\t" "ds_read_b128 v[240:243], %[l1] offset:6144\n\t" \
    "v_smfmac_f32_16x16x64_bf16 v[228:231], v[244:247], a[120:127], %[ix]\n\t" "v_smfmac_f32_16x16x64_bf16 v[232:235], v[244:247], v[88:95], %[ix]\n\t" \
    "buffer_load_dwordx4 v[20:23], %[hq], %[rs], %[qb] offen offset:2048 sc1\n\t" "v_smfmac_f32_16x16x64_bf16 v[236:239], v[244:247], v[184:191], %[ix]\n\t" \
    "ds_read_b128 v[184:187], %[l0] offset:22528\n\t" "ds_read_b128 v[188:191], %[l0] offset:23552\n\t" "s_waitcnt lgkmcnt(10)\n\t" \
    "v_smfmac_f32_16x16x64_bf16 v[224:227], v[248:251], a[128:135], %[ix]\n\t" "ds_read_b128 v[244:247], %[l1] offset:15360\n\t" \
    "v_smfmac_f32_16x16x64_bf16 v[228:231], v[248:251], a[136:143], %[ix]\n\t" "v_smfmac_f32_16x16x64_bf16 v[232:235], v[248:251], v[96:103], %[ix]\n\t" \
    "buffer_load_dwordx4 v[24:27], %[hq], %[rs], s61 offen offset:0 sc1\n\t" "v_smfmac_f32_16x16x64_bf16 v[236:239], v[248:251], v[160:167], %[ix]\n\t" \
    "ds_read_b128 v[160:163], %[l0] offset:24576\n\t" "ds_read_b128 v[164:167], %[l0] offset:25600\n\t" "s_waitcnt lgkmcnt(12)\n\t" \
    "v_smfmac_f32_16x16x64_bf16 v[224:227], v[252:255], a[144:151], %[ix]\n\t" "ds_read_b128 v[248:251], %[l1] offset:3072\n\t" \
    "v_smfmac_f32_16x16x64_bf16 v[228:231], v[252:255], a[152:159], %[ix]\n\t" "v_smfmac_f32_16x16x64_bf16 v[232:235], v[252:255], v[104:111], %[ix]\n\t" \
    "buffer_load_dwordx4 v[28:31], %[hq], %[rs], s61 offen offset:2048 sc1\n\t" "s_waitcnt lgkmcnt(10)\n\t" \
    "v_smfmac_f32_16x16x64_bf16 v[236:239], v[252:255], v[168:175], %[ix]\n\t" "ds_read_b128 v[168:171], %[l0] offset:26624\n\t" \
    "ds_read_b128 v[172:175], %[l0] offset:27648\n\t" "v_smfmac_f32_16x16x64_bf16 v[224:227], v[192:195], a[160:167], %[ix]\n\t" \
    "ds_read_b128 v[252:255], %[l1] offset:7168\n\t" "v_smfmac_f32_16x16x64_bf16 v[228:231], v[192:195], a[168:175], %[ix]\n\t" "s_waitcnt lgkmcnt(12)\n\t" \
    "v_smfmac_f32_16x16x64_bf16 v[224:227], v[196:199], a[176:183], %[ix]\n\t" "v_smfmac_f32_16x16x64_bf16 v[228:231], v[196:199], a[184:191], %[ix]\n\t" \
    "s_waitcnt lgkmcnt(9)\n\t" "v_smfmac_f32_16x16x64_bf16 v[224:227], v[240:243], a[192:199], %[ix]\n\t" \
    "v_smfmac_f32_16x16x64_bf16 v[228:231], v[240:243], a[200:207], %[ix]\n\t" "s_waitcnt lgkmcnt(6)\n\t" \
    "v_smfmac_f32_16x16x64_bf16 v[224:227], v[244:247], a[208:215], %[ix]\n\t" "v_smfmac_f32_16x16x64_bf16 v[228:231], v[244:247], a[216:223], %[ix]\n\t" \
    "s_waitcnt vmcnt(4)\n\t" "s_waitcnt lgkmcnt(3)\n\t" "v_smfmac_f32_16x16x64_bf16 v[224:227], v[248:251], a[224:231], %[ix]\n\t" \
    "v_smfmac_f32_16x16x64_bf16 v[228:231], v[248:251], a[232:239], %[ix]\n\t" "s_waitcnt lgkmcnt(0)\n\t" \
    "v_smfmac_f32_16x16x64_bf16 v[224:227], v[252:255], a[240:247], %[ix]\n\t" "s_nop 1\n\t" "v_smfmac_f32_16x16x64_bf16 v[228:231], v[252:255], a[248:255], %[ix]\n\t" \
    XT(90, 91) "s_nop 1\n\t" "v_smfmac_f32_16x16x64_bf16 v[232:235], v[192:195], v[112:119], %[ix]\n\t" "s_nop 1\n\t" \
    "v_smfmac_f32_16x16x64_bf16 v[236:239], v[192:195], v[176:183], %[ix]\n\t" "ds_read_b128 v[176:179], %[l0] offset:28672\n\t" "v_add_f32 v224, v224, v225\n\t" \
    "v_add_f32 v226, v226, v227\n\t" "v_smfmac_f32_16x16x64_bf16 v[232:235], v[196:199], v[120:127], %[ix]\n\t" "ds_read_b128 v[180:183], %[l0] offset:29696\n\t" \
    "v_add_f32 v228, v228, v229\n\t" "v_add_f32 v230, v230, v231\n\t" "v_smfmac_f32_16x16x64_bf16 v[236:239], v[196:199], v[184:191], %[ix]\n\t" \
    "ds_read_b128 v[184:187], %[l0] offset:30720\n\t" "v_cvt_pk_bf16_f32 v218, v224, v228\n\t" "v_cvt_pk_bf16_f32 v219, v226, v230\n\t" \
    "v_smfmac_f32_16x16x64_bf16 v[232:235], v[240:243], v[128:135], %[ix]\n\t" "ds_write2_b32 %[qw], v218, v219 offset1:8\n\t" \
    "ds_read_b128 v[188:191], %[l0] offset:31744\n\t" "v_smfmac_f32_16x16x64_bf16 v[236:239], v[240:243], v[160:167], %[ix]\n\t" \
    "v_smfmac_f32_16x16x64_bf16 v[232:235], v[244:247], v[136:143], %[ix]\n\t" "v_smfmac_f32_16x16x64_bf16 v[236:239], v[244:247], v[168:175], %[ix]\n\t" \
    "s_waitcnt lgkmcnt(1)\n\t" "s_barrier\n\t" "v_smfmac_f32_16x16x64_bf16 v[232:235], v[248:251], v[144:151], %[ix]\n\t" \
    "v_smfmac_f32_16x16x64_bf16 v[236:239], v[248:251], v[176:183], %[ix]\n\t" "v_smfmac_f32_16x16x64_bf16 v[232:235], v[252:255], v[152:159], %[ix]\n\t" \
    "s_waitcnt lgkmcnt(0)\n\t" "v_smfmac_f32_16x16x64_bf16 v[236:239], v[252:255], v[184:191], %[ix]\n\t" XT(92, 93) "s_nop 6\n\t" "v_add_f32 v232, v232, v233\n\t" \
    "v_add_f32 v234, v234, v235\n\t" "v_add_f32 v236, v236, v237\n\t" "v_add_f32 v238, v238, v239\n\t" "v_cvt_pk_bf16_f32 v232, v232, v236\n\t" \
    "v_cvt_pk_bf16_f32 v233, v234, v238\n\t" "ds_write2_b32 %[kw], v232, v233 offset1:68\n\t" "s_waitcnt lgkmcnt(0)\n\t" "s_barrier\n\t" XT(94, 95) \
    "s_cmp_ge_u32 %[sq], %[sn]\n\t" "s_cbranch_scc1 377f\n\t" "375:\n\t" "global_load_dword v240, v210, %[fq] sc1\n\t" "s_waitcnt vmcnt(0)\n\t" \
    "v_readfirstlane_b32 %[sq], v240\n\t" "s_nop 3\n\t" "s_cmp_ge_u32 %[sq], %[sn]\n\t" "s_cbranch_scc1 376f\n\t" "s_cmp_eq_u32 %[bd], 0\n\t" "s_cbranch_scc1 376f\n\t" \
    "s_sub_u32 %[bd], %[bd], 1\n\t" "s_sleep 2\n\t" "s_branch 375b\n\t" "376:\n\t" "buffer_load_dwordx4 v[16:19], %[hq], %[rs], %[qb] offen offset:0 sc1\n\t" \
    "buffer_load_dwordx4 v[20:23], %[hq], %[rs], %[qb] offen offset:2048 sc1\n\t" "buffer_load_dwordx4 v[24:27], %[hq], %[rs], s61 offen offset:0 sc1\n\t" \
    "buffer_load_dwordx4 v[28:31], %[hq], %[rs], s61 offen offset:2048 sc1\n\t" "377:\n\t" "s_nop 3\n\t" "ds_read_b128 v[160:163], %[kx] offset:0\n\t" \
    "ds_read_b128 v[164:167], %[kx] offset:2048\n\t" "ds_read_b128 v[168:171], %[kx] offset:4096\n\t" "ds_read_b128 v[172:175], %[kx] offset:6144\n\t" \
    "ds_read_b128 v[176:179], %[kx] offset:256\n\t" "ds_read_b128 v[180:183], %[kx] offset:2304\n\t" "ds_read_b128 v[184:187], %[kx] offset:4352\n\t" \
    "ds_read_b128 v[188:191], %[kx] offset:6400\n\t" "ds_read_b128 v[224:227], %[km] offset:0\n\t" "ds_read_b128 v[228:231], %[km] offset:64\n\t" \
    "ds_read_b128 v[232:235], %[km] offset:128\n\t" "ds_read_b128 v[236:239], %[km] offset:192\n\t" "s_waitcnt vmcnt(0)\n\t" "global_load_dword v240, v210, %[fq] sc1\n\t" \
    "s_waitcnt lgkmcnt(11)\n\t" "v_mfma_f32_16x16x32_bf16 v[248:251], v[160:163], v[16:19], 0\n\t" "s_waitcnt lgkmcnt(10)\n\t" \
    "v_mfma_f32_16x16x32_bf16 v[248:251], v[164:167], v[20:23], v[248:251]\n\t" "s_waitcnt lgkmcnt(9)\n\t" \
    "v_mfma_f32_16x16x32_bf16 v[248:251], v[168:171], v[24:27], v[248:251]\n\t" "s_waitcnt lgkmcnt(8)\n\t" \
    "v_mfma_f32_16x16x32_bf16 v[248:251], v[172:175], v[28:31], v[248:251]\n\t" "s_waitcnt lgkmcnt(7)\n\t" \
    "v_mfma_f32_16x16x32_bf16 v[252:255], v[176:179], v[16:19], v[200:203]\n\t" "s_waitcnt lgkmcnt(6)\n\t" \
    "v_mfma_f32_16x16x32_bf16 v[252:255], v[180:183], v[20:23], v[252:255]\n\t" "s_waitcnt lgkmcnt(5)\n\t" \
    "v_mfma_f32_16x16x32_bf16 v[252:255], v[184:187], v[24:27], v[252:255]\n\t" "s_waitcnt lgkmcnt(4)\n\t" \
    "v_mfma_f32_16x16x32_bf16 v[252:255], v[188:191], v[28:31], v[252:255]\n\t" "s_waitcnt lgkmcnt(3)\n\t" \
    "v_mfma_f32_16x16x32_bf16 v[244:247], v[224:227], v[16:19], v[204:207]\n\t" "s_waitcnt lgkmcnt(2)\n\t" \
    "v_mfma_f32_16x16x32_bf16 v[244:247], v[228:231], v[20:23], v[244:247]\n\t" "s_waitcnt lgkmcnt(1)\n\t" \
    "v_mfma_f32_16x16x32_bf16 v[244:247], v[232:235], v[24:27], v[244:247]\n\t" "s_waitcnt lgkmcnt(0)\n\t" \
    "v_mfma_f32_16x16x32_bf16 v[244:247], v[236:239], v[28:31], v[244:247]\n\t" "s_nop 3\n\t" "v_max3_f32 v242, v248, v249, v250\n\t" "v_max3_f32 v242, v242, v251, v252\n\t" \
    "v_max3_f32 v242, v242, v253, v254\n\t" "v_max_f32 v242, v242, v255\n\t" "v_max3_f32 v242, v242, v244, v245\n\t" "v_max3_f32 v242, v242, v246, v247\n\t" \
    "v_mov_b32 v243, v242\n\t" "s_nop 1\n\t" "v_permlane32_swap_b32 v242, v243\n\t" "v_max_f32 v242, v242, v243\n\t" "v_mov_b32 v243, v242\n\t" "s_nop 1\n\t" \
    "v_permlane16_swap_b32 v242, v243\n\t" "v_max_f32 v242, v242, v243\n\t" "v_pk_add_f32 v[244:245], v[244:245], v[242:243] op_sel_hi:[1,0] neg_lo:[0,1] neg_hi:[0,1]\n\t" \
    "v_pk_add_f32 v[246:247], v[246:247], v[242:243] op_sel_hi:[1,0] neg_lo:[0,1] neg_hi:[0,1]\n\t" \
    "v_pk_add_f32 v[248:249], v[248:249], v[242:243] op_sel_hi:[1,0] neg_lo:[0,1] neg_hi:[0,1]\n\t" \
    "v_pk_add_f32 v[250:251], v[250:251], v[242:243] op_sel_hi:[1,0] neg_lo:[0,1] neg_hi:[0,1]\n\t" \
    "v_pk_add_f32 v[252:253], v[252:253], v[242:243] op_sel_hi:[1,0] neg_lo:[0,1] neg_hi:[0,1]\n\t" \
    "v_pk_add_f32 v[254:255], v[254:255], v[242:243] op_sel_hi:[1,0] neg_lo:[0,1] neg_hi:[0,1]\n\t" "v_pk_mul_f32 v[244:245], v[244:245], s[66:67]\n\t" \
    "v_pk_mul_f32 v[246:247], v[246:247], s[66:67]\n\t" "v_pk_mul_f32 v[248:249], v[248:249], s[66:67]\n\t" "v_pk_mul_f32 v[250:251], v[250:251], s[66:67]\n\t" \
    "v_pk_mul_f32 v[252:253], v[252:253], s[66:67]\n\t" "v_pk_mul_f32 v[254:255], v[254:255], s[66:67]\n\t" "v_exp_f32 v244, v244\n\t" "v_exp_f32 v245, v245\n\t" \
    "v_exp_f32 v246, v246\n\t" "v_exp_f32 v247, v247\n\t" "v_exp_f32 v248, v248\n\t" "v_exp_f32 v249, v249\n\t" "v_exp_f32 v250, v250\n\t" "v_exp_f32 v251, v251\n\t" \
    "v_exp_f32 v252, v252\n\t" "v_exp_f32 v253, v253\n\t" "v_exp_f32 v254, v254\n\t" "v_exp_f32 v255, v255\n\t" "v_pk_add_f32 v[242:243], v[244:245], v[246:247]\n\t" \
    "v_pk_add_f32 v[242:243], v[242:243], v[248:249]\n\t" "v_pk_add_f32 v[242:243], v[242:243], v[250:251]\n\t" "v_pk_add_f32 v[242:243], v[242:243], v[252:253]\n\t" \
    "v_pk_add_f32 v[242:243], v[242:243], v[254:255]\n\t" "v_add_f32 v242, v242, v243\n\t" "v_mov_b32 v243, v242\n\t" "s_nop 1\n\t" "v_permlane32_swap_b32 v242, v243\n\t" \
    "v_add_f32 v242, v242, v243\n\t" "v_mov_b32 v243, v242\n\t" "s_nop 1\n\t" "v_permlane16_swap_b32 v242, v243\n\t" "v_add_f32 v242, v242, v243\n\t" \
    "v_rcp_f32 v242, v242\n\t" "s_nop 0\n\t" "v_pk_mul_f32 v[244:245], v[244:245], v[242:243] op_sel_hi:[1,0]\n\t" \
    "v_pk_mul_f32 v[246:247], v[246:247], v[242:243] op_sel_hi:[1,0]\n\t" "v_pk_mul_f32 v[248:249], v[248:249], v[242:243] op_sel_hi:[1,0]\n\t" \
    "v_pk_mul_f32 v[250:251], v[250:251], v[242:243] op_sel_hi:[1,0]\n\t" "v_pk_mul_f32 v[252:253], v[252:253], v[242:243] op_sel_hi:[1,0]\n\t" \
    "v_pk_mul_f32 v[254:255], v[254:255], v[242:243] op_sel_hi:[1,0]\n\t" "v_cvt_pk_bf16_f32 v162, v244, v245\n\t" "v_cvt_pk_bf16_f32 v163, v246, v247\n\t" \
    "v_cvt_pk_bf16_f32 v164, v248, v249\n\t" "v_cvt_pk_bf16_f32 v165, v250, v251\n\t" "v_cvt_pk_bf16_f32 v166, v252, v253\n\t" "v_cvt_pk_bf16_f32 v167, v254, v255\n\t" \
    "s_barrier\n\t" "buffer_store_dwordx4 v[164:167], %[px], %[rs], %[tb] offen sc1\n\t" "buffer_store_dwordx2 v[162:163], %[pm], %[rs], %[tb] offen sc1\n\t" \
    "s_waitcnt vmcnt(2)\n\t" "v_readfirstlane_b32 s62, v240\n\t" "s_nop 3\n\t" "s_max_u32 %[sq], %[sq], s62\n\t" XT(96, 97) "s_branch 99f\n\t" "31:\n\t" \
    "ds_read_b128 v[220:223], %[l1] offset:8192\n\t" "s_waitcnt lgkmcnt(0)\n\t" "v_max3_u32 v218, v220, v221, v222\n\t" "v_max_u32 v218, v218, v223\n\t" \
    "ds_read_b128 v[220:223], %[l1] offset:9216\n\t" "s_waitcnt lgkmcnt(0)\n\t" "v_max3_u32 v218, v218, v220, v221\n\t" "v_max3_u32 v218, v218, v222, v223\n\t" \
    "ds_read_b128 v[220:223], %[l1] offset:10240\n\t" "s_waitcnt lgkmcnt(0)\n\t" "v_max3_u32 v218, v218, v220, v221\n\t" "v_max3_u32 v218, v218, v222, v223\n\t" \
    "ds_read_b128 v[220:223], %[l1] offset:11264\n\t" "s_waitcnt lgkmcnt(0)\n\t" "v_max3_u32 v218, v218, v220, v221\n\t" "v_max3_u32 v218, v218, v222, v223\n\t" \
    "v_cmp_eq_u32 vcc, -1, v218\n\t" "s_cbranch_vccz 32b\n\t" "s_cmp_eq_u32 %[rt], 1\n\t" "s_cbranch_scc1 32b\n\t" "v_mov_b32 v218, %[rt]\n\t" "ds_write_b32 v219, v218\n\t" \
    "s_waitcnt lgkmcnt(0)\n\t" "s_branch 32b\n\t" "33:\n\t" "s_sub_u32 %[rt], %[rt], 1\n\t" "s_waitcnt lgkmcnt(0)\n\t" XCNT(100) "34:\n\t" \
    "buffer_load_dwordx4 v[240:243], %[mo], %[rs], %[sb] offen sc1\n\t" "buffer_load_dwordx4 v[244:247], v216, %[rs], %[sb] offen sc1\n\t" \
    "buffer_load_dwordx4 v[248:251], %[mo], %[rs], s60 offen sc1\n\t" "buffer_load_dwordx4 v[252:255], v216, %[rs], s60 offen sc1\n\t" "s_waitcnt vmcnt(0)\n\t" \
    "v_max_u32 v218, v240, v241\n\t" "v_max3_u32 v218, v218, v242, v243\n\t" "v_max3_u32 v218, v218, v244, v245\n\t" "v_max3_u32 v218, v218, v246, v247\n\t" \
    "v_max3_u32 v218, v218, v248, v249\n\t" "v_max3_u32 v218, v218, v250, v251\n\t" "v_max3_u32 v218, v218, v252, v253\n\t" "v_max3_u32 v218, v218, v254, v255\n\t" \
    "v_cmp_eq_u32 vcc, -1, v218\n\t" "s_cbranch_vccz 35f\n\t" "s_cmp_eq_u32 %[bd], 0\n\t" "s_cbranch_scc1 35f\n\t" "s_sub_u32 %[bd], %[bd], 1\n\t" XCNT(101) \
    "s_branch 34b\n\t" "35:\n\t" "ds_read_b128 v[160:163], %[l0] offset:0\n\t" "ds_read_b128 v[164:167], %[l0] offset:1024\n\t" \
    "ds_read_b128 v[168:171], %[l0] offset:2048\n\t" "ds_read_b128 v[172:175], %[l0] offset:3072\n\t" "ds_read_b128 v[176:179], %[l0] offset:4096\n\t" \
    "ds_read_b128 v[180:183], %[l0] offset:5120\n\t" "ds_read_b128 v[184:187], %[l0] offset:6144\n\t" "ds_read_b128 v[188:191], %[l0] offset:7168\n\t" \
    "v_mov_b64 v[224:225], 0\n\t" "v_mov_b64 v[226:227], 0\n\t" "v_mov_b64 v[228:229], 0\n\t" "v_mov_b64 v[230:231], 0\n\t" "v_mov_b64 v[232:233], 0\n\t" \
    "v_mov_b64 v[234:235], 0\n\t" "v_mov_b64 v[236:237], 0\n\t" "v_mov_b64 v[238:239], 0\n\t" "s_barrier\n\t" "s_branch 30b\n\t" "99:\n\t"
#define QK_STEP3 \
    "v_xor_b32 v216, 0x400, %[mo]\n\t" "s_xor_b32 s60, %[sb], 0x800\n\t" "s_mov_b32 s67, %[pb]\n\t" XT(80, 81) "s_getpc_b64 s[62:63]\n\t" "48:\n\t" "s_memtime s[64:65]\n\t" \
    "s_waitcnt lgkmcnt(0)\n\t" "s_sub_u32 s61, %[tg], s64\n\t" "s_cmp_gt_i32 s61, 124\n\t" "s_cbranch_scc1 48b\n\t" "s_max_i32 s61, s61, 0\n\t" "s_and_b32 s61, s61, 124\n\t" \
    "s_sub_u32 s61, 49f-48b, s61\n\t" "s_add_u32 s62, s62, s61\n\t" "s_addc_u32 s63, s63, 0\n\t" "s_setpc_b64 s[62:63]\n\t" ".rept 31\n\t" "s_nop 0\n\t" ".endr\n\t" "49:\n\t" \
    XT(98, 99) "s_barrier\n\t" XT(82, 83) "buffer_load_dwordx4 v[240:243], %[mo], %[rs], %[sb] offen sc1\n\t" "v_mov_b64 v[224:225], 0\n\t" "v_mov_b64 v[226:227], 0\n\t" \
    "v_mov_b64 v[228:229], 0\n\t" "v_mov_b64 v[230:231], 0\n\t" "v_mov_b64 v[232:233], 0\n\t" "v_mov_b64 v[234:235], 0\n\t" "v_mov_b64 v[236:237], 0\n\t" \
    "v_mov_b64 v[238:239], 0\n\t" "buffer_load_dwordx4 v[244:247], v216, %[rs], %[sb] offen sc1\n\t" "ds_read_b128 v[160:163], %[l0] offset:0\n\t" \
    "ds_read_b128 v[164:167], %[l0] offset:1024\n\t" "ds_read_b128 v[168:171], %[l0] offset:2048\n\t" "ds_read_b128 v[172:175], %[l0] offset:3072\n\t" "s_nop 3\n\t" \
    "buffer_load_dwordx4 v[248:251], %[mo], %[rs], s60 offen sc1\n\t" "ds_read_b128 v[176:179], %[l0] offset:4096\n\t" "ds_read_b128 v[180:183], %[l0] offset:5120\n\t" \
    "ds_read_b128 v[184:187], %[l0] offset:6144\n\t" "ds_read_b128 v[188:191], %[l0] offset:7168\n\t" "s_nop 3\n\t" \
    "buffer_load_dwordx4 v[252:255], v216, %[rs], s60 offen sc1\n\t" "v_mov_b32 v219, 160512\n\t" "v_mov_b32 v220, -1\n\t" \
    "buffer_store_dword v220, %[hq], %[rs], %[pr] offen sc1\n\t" "40:\n\t" "s_waitcnt vmcnt(4)\n\t" XT(84, 85) \
    "v_smfmac_f32_16x16x64_bf16 v[224:227], v[240:243], a[0:7], %[ix]\n\t" "ds_write_b128 %[l1], v[240:243] offset:12288\n\t" \
    "v_smfmac_f32_16x16x64_bf16 v[228:231], v[240:243], a[8:15], %[ix]\n\t" "v_smfmac_f32_16x16x64_bf16 v[232:235], v[240:243], v[32:39], %[ix]\n\t" \
    "s_waitcnt lgkmcnt(7)\n\t" "v_smfmac_f32_16x16x64_bf16 v[236:239], v[240:243], v[160:167], %[ix]\n\t" "ds_read_b128 v[160:163], %[l0] offset:8192\n\t" \
    "ds_read_b128 v[164:167], %[l0] offset:9216\n\t" "s_waitcnt vmcnt(3)\n\t" "v_smfmac_f32_16x16x64_bf16 v[224:227], v[244:247], a[16:23], %[ix]\n\t" \
    "ds_write_b128 %[l1], v[244:247] offset:13312\n\t" "v_smfmac_f32_16x16x64_bf16 v[228:231], v[244:247], a[24:31], %[ix]\n\t" \
    "v_smfmac_f32_16x16x64_bf16 v[232:235], v[244:247], v[40:47], %[ix]\n\t" "s_waitcnt lgkmcnt(8)\n\t" \
    "v_smfmac_f32_16x16x64_bf16 v[236:239], v[244:247], v[168:175], %[ix]\n\t" "ds_read_b128 v[168:171], %[l0] offset:10240\n\t" \
    "ds_read_b128 v[172:175], %[l0] offset:11264\n\t" "s_waitcnt vmcnt(2)\n\t" "v_smfmac_f32_16x16x64_bf16 v[224:227], v[248:251], a[32:39], %[ix]\n\t" \
    "ds_write_b128 %[l1], v[248:251] offset:14336\n\t" "v_smfmac_f32_16x16x64_bf16 v[228:231], v[248:251], a[40:47], %[ix]\n\t" "s_waitcnt lgkmcnt(3)\n\t" "s_barrier\n\t" \
    XT(86, 87) "v_smfmac_f32_16x16x64_bf16 v[232:235], v[248:251], v[48:55], %[ix]\n\t" "ds_read_b128 v[192:195], %[l1] offset:0\n\t" \
    "ds_read_b128 v[196:199], %[l1] offset:4096\n\t" "v_smfmac_f32_16x16x64_bf16 v[236:239], v[248:251], v[176:183], %[ix]\n\t" \
    "ds_read_b128 v[176:179], %[l0] offset:12288\n\t" "ds_read_b128 v[180:183], %[l0] offset:13312\n\t" "s_waitcnt vmcnt(1)\n\t" \
    "v_smfmac_f32_16x16x64_bf16 v[224:227], v[252:255], a[48:55], %[ix]\n\t" "ds_write_b128 %[l1], v[252:255] offset:15360\n\t" \
    "v_smfmac_f32_16x16x64_bf16 v[228:231], v[252:255], a[56:63], %[ix]\n\t" "ds_read_b128 v[240:243], %[l1] offset:8192\n\t" "ds_read_b128 v[244:247], %[l1] offset:1024\n\t" \
    "v_smfmac_f32_16x16x64_bf16 v[232:235], v[252:255], v[56:63], %[ix]\n\t" "s_nop 1\n\t" "v_smfmac_f32_16x16x64_bf16 v[236:239], v[252:255], v[184:191], %[ix]\n\t" \
    "ds_read_b128 v[184:187], %[l0] offset:14336\n\t" "v_cmp_u_f32 vcc, v224, v225\n\t" "v_cmp_u_f32 s[62:63], v226, v227\n\t" "s_waitcnt lgkmcnt(7)\n\t" \
    "v_smfmac_f32_16x16x64_bf16 v[224:227], v[192:195], a[64:71], %[ix]\n\t" "ds_read_b128 v[188:191], %[l0] offset:15360\n\t" \
    "v_smfmac_f32_16x16x64_bf16 v[228:231], v[192:195], a[72:79], %[ix]\n\t" "s_or_b64 vcc, vcc, s[62:63]\n\t" "s_cbranch_scc1 41f\n\t" "42:\n\t" \
    "v_smfmac_f32_16x16x64_bf16 v[232:235], v[192:195], v[64:71], %[ix]\n\t" "s_waitcnt lgkmcnt(4)\n\t" "s_barrier\n\t" XT(88, 89) "ds_read_b32 v218, v219\n\t" \
    "v_smfmac_f32_16x16x64_bf16 v[236:239], v[192:195], v[160:167], %[ix]\n\t" "ds_read_b128 v[160:163], %[l0] offset:16384\n\t" \
    "ds_read_b128 v[164:167], %[l0] offset:17408\n\t" "v_smfmac_f32_16x16x64_bf16 v[224:227], v[196:199], a[80:87], %[ix]\n\t" \
    "ds_read_b128 v[248:251], %[l1] offset:5120\n\t" "ds_read_b128 v[252:255], %[l1] offset:9216\n\t" "v_smfmac_f32_16x16x64_bf16 v[228:231], v[196:199], a[88:95], %[ix]\n\t" \
    "s_memtime s[64:65]\n\t" "ds_read_b128 v[192:195], %[l1] offset:2048\n\t" "v_smfmac_f32_16x16x64_bf16 v[232:235], v[196:199], v[72:79], %[ix]\n\t" \
    "v_smfmac_f32_16x16x64_bf16 v[236:239], v[196:199], v[168:175], %[ix]\n\t" "s_waitcnt lgkmcnt(5)\n\t" "v_cmp_eq_u32 vcc, %[rt], v218\n\t" \
    "v_smfmac_f32_16x16x64_bf16 v[224:227], v[240:243], a[96:103], %[ix]\n\t" "s_cbranch_vccnz 43f\n\t" "ds_read_b128 v[168:171], %[l0] offset:18432\n\t" \
    "v_smfmac_f32_16x16x64_bf16 v[228:231], v[240:243], a[104:111], %[ix]\n\t" "ds_read_b128 v[172:175], %[l0] offset:19456\n\t" \
    "ds_read_b128 v[196:199], %[l1] offset:6144\n\t" "v_smfmac_f32_16x16x64_bf16 v[232:235], v[240:243], v[80:87], %[ix]\n\t" "s_sub_u32 s62, s64, %[ca]\n\t" \
    "s_mul_hi_u32 s63, s62, %[k1]\n\t" "v_smfmac_f32_16x16x64_bf16 v[236:239], v[240:243], v[176:183], %[ix]\n\t" "s_mul_i32 s62, s62, %[k1]\n\t" \
    "s_lshr_b64 s[62:63], s[62:63], 16\n\t" "v_smfmac_f32_16x16x64_bf16 v[224:227], v[244:247], a[112:119], %[ix]\n\t" "s_add_u32 s62, s62, %[ga]\n\t" \
    "s_bitset0_b32 s62, 0\n\t" "v_smfmac_f32_16x16x64_bf16 v[228:231], v[244:247], a[120:127], %[ix]\n\t" "v_mov_b32 v217, s62\n\t" \
    "buffer_store_dword v217, %[hq], %[rs], s67 offen sc1\n\t" "v_smfmac_f32_16x16x64_bf16 v[232:235], v[244:247], v[88:95], %[ix]\n\t" \
    "ds_read_b128 v[176:179], %[l0] offset:20480\n\t" "ds_read_b128 v[180:183], %[l0] offset:21504\n\t" \
    "v_smfmac_f32_16x16x64_bf16 v[236:239], v[244:247], v[184:191], %[ix]\n\t" "ds_read_b128 v[240:243], %[l1] offset:10240\n\t" \
    "ds_read_b128 v[184:187], %[l0] offset:22528\n\t" "s_waitcnt lgkmcnt(9)\n\t" "v_smfmac_f32_16x16x64_bf16 v[224:227], v[248:251], a[128:135], %[ix]\n\t" \
    "ds_read_b128 v[188:191], %[l0] offset:23552\n\t" "ds_read_b128 v[244:247], %[l1] offset:3072\n\t" \
    "v_smfmac_f32_16x16x64_bf16 v[228:231], v[248:251], a[136:143], %[ix]\n\t" "v_lshlrev_b32 v16, 3, %[l1]\n\t" "buffer_load_dword v17, v16, %[rs], %[kn] offen sc1\n\t" \
    "v_smfmac_f32_16x16x64_bf16 v[232:235], v[248:251], v[96:103], %[ix]\n\t" "v_smfmac_f32_16x16x64_bf16 v[236:239], v[248:251], v[160:167], %[ix]\n\t" \
    "ds_read_b128 v[160:163], %[l0] offset:24576\n\t" "ds_read_b128 v[164:167], %[l0] offset:25600\n\t" "s_waitcnt lgkmcnt(12)\n\t" \
    "v_smfmac_f32_16x16x64_bf16 v[224:227], v[252:255], a[144:151], %[ix]\n\t" "ds_read_b128 v[248:251], %[l1] offset:7168\n\t" \
    "v_smfmac_f32_16x16x64_bf16 v[228:231], v[252:255], a[152:159], %[ix]\n\t" "v_smfmac_f32_16x16x64_bf16 v[232:235], v[252:255], v[104:111], %[ix]\n\t" \
    "s_waitcnt lgkmcnt(10)\n\t" "v_smfmac_f32_16x16x64_bf16 v[236:239], v[252:255], v[168:175], %[ix]\n\t" "ds_read_b128 v[168:171], %[l0] offset:26624\n\t" \
    "ds_read_b128 v[172:175], %[l0] offset:27648\n\t" "v_smfmac_f32_16x16x64_bf16 v[224:227], v[192:195], a[160:167], %[ix]\n\t" \
    "ds_read_b128 v[252:255], %[l1] offset:11264\n\t" "v_smfmac_f32_16x16x64_bf16 v[228:231], v[192:195], a[168:175], %[ix]\n\t" "s_waitcnt lgkmcnt(12)\n\t" \
    "v_smfmac_f32_16x16x64_bf16 v[224:227], v[196:199], a[176:183], %[ix]\n\t" "v_smfmac_f32_16x16x64_bf16 v[228:231], v[196:199], a[184:191], %[ix]\n\t" \
    "s_waitcnt lgkmcnt(9)\n\t" "v_smfmac_f32_16x16x64_bf16 v[224:227], v[240:243], a[192:199], %[ix]\n\t" \
    "v_smfmac_f32_16x16x64_bf16 v[228:231], v[240:243], a[200:207], %[ix]\n\t" "s_waitcnt lgkmcnt(6)\n\t" \
    "v_smfmac_f32_16x16x64_bf16 v[224:227], v[244:247], a[208:215], %[ix]\n\t" "v_smfmac_f32_16x16x64_bf16 v[228:231], v[244:247], a[216:223], %[ix]\n\t" \
    "s_waitcnt lgkmcnt(3)\n\t" "v_smfmac_f32_16x16x64_bf16 v[224:227], v[248:251], a[224:231], %[ix]\n\t" \
    "v_smfmac_f32_16x16x64_bf16 v[228:231], v[248:251], a[232:239], %[ix]\n\t" "s_waitcnt lgkmcnt(0)\n\t" \
    "v_smfmac_f32_16x16x64_bf16 v[224:227], v[252:255], a[240:247], %[ix]\n\t" "s_nop 1\n\t" "v_smfmac_f32_16x16x64_bf16 v[228:231], v[252:255], a[248:255], %[ix]\n\t" \
    XT(90, 91) "s_nop 1\n\t" "v_smfmac_f32_16x16x64_bf16 v[232:235], v[192:195], v[112:119], %[ix]\n\t" "s_nop 1\n\t" \
    "v_smfmac_f32_16x16x64_bf16 v[236:239], v[192:195], v[176:183], %[ix]\n\t" "ds_read_b128 v[176:179], %[l0] offset:28672\n\t" "v_add_f32 v224, v224, v225\n\t" \
    "v_add_f32 v226, v226, v227\n\t" "v_smfmac_f32_16x16x64_bf16 v[232:235], v[196:199], v[120:127], %[ix]\n\t" "ds_read_b128 v[180:183], %[l0] offset:29696\n\t" \
    "v_add_f32 v228, v228, v229\n\t" "v_add_f32 v230, v230, v231\n\t" "v_smfmac_f32_16x16x64_bf16 v[236:239], v[196:199], v[184:191], %[ix]\n\t" \
    "ds_read_b128 v[184:187], %[l0] offset:30720\n\t" "v_cvt_pk_bf16_f32 v218, v224, v228\n\t" "v_cvt_pk_bf16_f32 v219, v226, v230\n\t" \
    "v_smfmac_f32_16x16x64_bf16 v[232:235], v[240:243], v[128:135], %[ix]\n\t" "ds_write2_b32 %[qw], v218, v219 offset1:8\n\t" \
    "ds_read_b128 v[188:191], %[l0] offset:31744\n\t" "v_smfmac_f32_16x16x64_bf16 v[236:239], v[240:243], v[160:167], %[ix]\n\t" \
    "v_smfmac_f32_16x16x64_bf16 v[232:235], v[244:247], v[136:143], %[ix]\n\t" "v_smfmac_f32_16x16x64_bf16 v[236:239], v[244:247], v[168:175], %[ix]\n\t" \
    "s_waitcnt lgkmcnt(1)\n\t" "s_barrier\n\t" "v_smfmac_f32_16x16x64_bf16 v[232:235], v[248:251], v[144:151], %[ix]\n\t" \
    "v_smfmac_f32_16x16x64_bf16 v[236:239], v[248:251], v[176:183], %[ix]\n\t" "v_smfmac_f32_16x16x64_bf16 v[232:235], v[252:255], v[152:159], %[ix]\n\t" \
    "s_waitcnt lgkmcnt(0)\n\t" "v_smfmac_f32_16x16x64_bf16 v[236:239], v[252:255], v[184:191], %[ix]\n\t" XT(92, 93) "s_nop 6\n\t" "v_add_f32 v232, v232, v233\n\t" \
    "v_add_f32 v234, v234, v235\n\t" "v_add_f32 v236, v236, v237\n\t" "v_add_f32 v238, v238, v239\n\t" "v_cvt_pk_bf16_f32 v232, v232, v236\n\t" \
    "v_cvt_pk_bf16_f32 v233, v234, v238\n\t" "ds_write2_b32 %[kw], v232, v233 offset1:68\n\t" "s_waitcnt lgkmcnt(0)\n\t" "s_barrier\n\t" XT(94, 95) XT(96, 97) \
    "s_branch 99f\n\t" "41:\n\t" "ds_read_b128 v[220:223], %[l1] offset:12288\n\t" "s_waitcnt lgkmcnt(0)\n\t" "v_max3_u32 v218, v220, v221, v222\n\t" \
    "v_max_u32 v218, v218, v223\n\t" "ds_read_b128 v[220:223], %[l1] offset:13312\n\t" "s_waitcnt lgkmcnt(0)\n\t" "v_max3_u32 v218, v218, v220, v221\n\t" \
    "v_max3_u32 v218, v218, v222, v223\n\t" "ds_read_b128 v[220:223], %[l1] offset:14336\n\t" "s_waitcnt lgkmcnt(0)\n\t" "v_max3_u32 v218, v218, v220, v221\n\t" \
    "v_max3_u32 v218, v218, v222, v223\n\t" "ds_read_b128 v[220:223], %[l1] offset:15360\n\t" "s_waitcnt lgkmcnt(0)\n\t" "v_max3_u32 v218, v218, v220, v221\n\t" \
    "v_max3_u32 v218, v218, v222, v223\n\t" "v_cmp_eq_u32 vcc, -1, v218\n\t" "s_cbranch_vccz 42b\n\t" "s_cmp_eq_u32 %[rt], 1\n\t" "s_cbranch_scc1 42b\n\t" \
    "v_mov_b32 v218, %[rt]\n\t" "ds_write_b32 v219, v218\n\t" "s_waitcnt lgkmcnt(0)\n\t" "s_branch 42b\n\t" "43:\n\t" "s_sub_u32 %[rt], %[rt], 1\n\t" \
    "s_waitcnt lgkmcnt(0)\n\t" XCNT(100) "44:\n\t" "buffer_load_dwordx4 v[240:243], %[mo], %[rs], %[sb] offen sc1\n\t" \
    "buffer_load_dwordx4 v[244:247], v216, %[rs], %[sb] offen sc1\n\t" "buffer_load_dwordx4 v[248:251], %[mo], %[rs], s60 offen sc1\n\t" \
    "buffer_load_dwordx4 v[252:255], v216, %[rs], s60 offen sc1\n\t" "s_waitcnt vmcnt(0)\n\t" "v_max_u32 v218, v240, v241\n\t" "v_max3_u32 v218, v218, v242, v243\n\t" \
    "v_max3_u32 v218, v218, v244, v245\n\t" "v_max3_u32 v218, v218, v246, v247\n\t" "v_max3_u32 v218, v218, v248, v249\n\t" "v_max3_u32 v218, v218, v250, v251\n\t" \
    "v_max3_u32 v218, v218, v252, v253\n\t" "v_max3_u32 v218, v218, v254, v255\n\t" "v_cmp_eq_u32 vcc, -1, v218\n\t" "s_cbranch_vccz 45f\n\t" "s_cmp_eq_u32 %[bd], 0\n\t" \
    "s_cbranch_scc1 45f\n\t" "s_sub_u32 %[bd], %[bd], 1\n\t" XCNT(101) "s_branch 44b\n\t" "45:\n\t" "ds_read_b128 v[160:163], %[l0] offset:0\n\t" \
    "ds_read_b128 v[164:167], %[l0] offset:1024\n\t" "ds_read_b128 v[168:171], %[l0] offset:2048\n\t" "ds_read_b128 v[172:175], %[l0] offset:3072\n\t" \
    "ds_read_b128 v[176:179], %[l0] offset:4096\n\t" "ds_read_b128 v[180:183], %[l0] offset:5120\n\t" "ds_read_b128 v[184:187], %[l0] offset:6144\n\t" \
    "ds_read_b128 v[188:191], %[l0] offset:7168\n\t" "v_mov_b64 v[224:225], 0\n\t" "v_mov_b64 v[226:227], 0\n\t" "v_mov_b64 v[228:229], 0\n\t" "v_mov_b64 v[230:231], 0\n\t" \
    "v_mov_b64 v[232:233], 0\n\t" "v_mov_b64 v[234:235], 0\n\t" "v_mov_b64 v[236:237], 0\n\t" "v_mov_b64 v[238:239], 0\n\t" "s_barrier\n\t" "s_memtime s[64:65]\n\t" \
    "s_waitcnt lgkmcnt(0)\n\t" "s_add_u32 s64, s64, %[dr]\n\t" "s_sub_u32 s62, s64, %[ca]\n\t" "s_mul_hi_u32 s63, s62, %[k1]\n\t" "s_mul_i32 s62, s62, %[k1]\n\t" \
    "s_lshr_b64 s[62:63], s[62:63], 16\n\t" "s_add_u32 s62, s62, %[ga]\n\t" "s_bitset0_b32 s62, 0\n\t" "v_mov_b32 v218, s62\n\t" \
    "buffer_store_dword v218, %[hq], %[rs], s67 offen sc1\n\t" "s_mov_b32 s67, 0x7f000000\n\t" "s_branch 40b\n\t" "99:\n\t" "mk_tail:\n\t"
#define VO_STEP \
    "v_xor_b32 v224, 0x400, %[mo]\n\t" "s_xor_b32 s60, %[sb], 0x800\n\t" "s_mov_b32 s67, %[pb]\n\t" "s_cmp_eq_u32 %[w], 1\n\t" "s_cbranch_scc1 91f\n\t" \
    "s_cmp_eq_u32 %[w], 2\n\t" "s_cbranch_scc1 92f\n\t" "s_cmp_eq_u32 %[w], 3\n\t" "s_cbranch_scc1 93f\n\t" XT(80, 81) "s_getpc_b64 s[62:63]\n\t" "18:\n\t" \
    "s_memtime s[64:65]\n\t" "s_waitcnt lgkmcnt(0)\n\t" "s_sub_u32 s61, %[tg], s64\n\t" "s_cmp_gt_i32 s61, 124\n\t" "s_cbranch_scc1 18b\n\t" "s_max_i32 s61, s61, 0\n\t" \
    "s_and_b32 s61, s61, 124\n\t" "s_sub_u32 s61, 19f-18b, s61\n\t" "s_add_u32 s62, s62, s61\n\t" "s_addc_u32 s63, s63, 0\n\t" "s_setpc_b64 s[62:63]\n\t" ".rept 31\n\t" \
    "s_nop 0\n\t" ".endr\n\t" "19:\n\t" "s_barrier\n\t" XT(82, 83) "buffer_load_dwordx4 v[240:243], %[mo], %[rs], %[sb] offen sc1\n\t" "v_mov_b64 v[232:233], 0\n\t" \
    "v_mov_b64 v[234:235], 0\n\t" "v_mov_b64 v[236:237], 0\n\t" "v_mov_b64 v[238:239], 0\n\t" "v_mov_b64 v[192:193], 0\n\t" "v_mov_b64 v[194:195], 0\n\t" \
    "v_mov_b64 v[196:197], 0\n\t" "v_mov_b64 v[198:199], 0\n\t" "buffer_load_dwordx4 v[244:247], v224, %[rs], %[sb] offen sc1\n\t" \
    "ds_read_b128 v[160:163], %[l0] offset:0\n\t" "ds_read_b128 v[164:167], %[l0] offset:1024\n\t" "ds_read_b128 v[168:171], %[l0] offset:2048\n\t" \
    "ds_read_b128 v[172:175], %[l0] offset:3072\n\t" "s_nop 3\n\t" "buffer_load_dwordx4 v[248:251], %[mo], %[rs], s60 offen sc1\n\t" \
    "ds_read_b128 v[176:179], %[l0] offset:4096\n\t" "ds_read_b128 v[180:183], %[l0] offset:5120\n\t" "ds_read_b128 v[184:187], %[l0] offset:6144\n\t" \
    "ds_read_b128 v[188:191], %[l0] offset:7168\n\t" "s_nop 3\n\t" "buffer_load_dwordx4 v[252:255], v224, %[rs], s60 offen sc1\n\t" "v_mov_b64 v[200:201], 0\n\t" \
    "v_mov_b64 v[202:203], 0\n\t" "v_mov_b64 v[204:205], 0\n\t" "v_mov_b64 v[206:207], 0\n\t" "v_mov_b64 v[208:209], 0\n\t" "v_mov_b64 v[210:211], 0\n\t" \
    "v_mov_b64 v[212:213], 0\n\t" "v_mov_b64 v[214:215], 0\n\t" "v_mov_b64 v[216:217], 0\n\t" "v_mov_b64 v[218:219], 0\n\t" "v_mov_b64 v[220:221], 0\n\t" \
    "v_mov_b64 v[222:223], 0\n\t" "v_mov_b32 v226, -1\n\t" "buffer_store_dword v226, %[hq], %[rs], %[pr] offen sc1\n\t" "v_mov_b32 v227, 160512\n\t" "10:\n\t" \
    "s_waitcnt vmcnt(4)\n\t" XT(84, 85) "v_smfmac_f32_16x16x64_bf16 v[232:235], v[240:243], a[0:7], %[ix]\n\t" "ds_write_b128 %[l1], v[240:243] offset:0\n\t" \
    "v_smfmac_f32_16x16x64_bf16 v[236:239], v[240:243], a[8:15], %[ix]\n\t" "s_waitcnt vmcnt(3)\n\t" "v_smfmac_f32_16x16x64_bf16 v[232:235], v[244:247], a[16:23], %[ix]\n\t" \
    "ds_write_b128 %[l1], v[244:247] offset:1024\n\t" "s_waitcnt lgkmcnt(8)\n\t" "v_smfmac_f32_16x16x64_bf16 v[236:239], v[244:247], v[160:167], %[ix]\n\t" \
    "s_waitcnt lgkmcnt(0)\n\t" "s_barrier\n\t" XT(86, 87) "ds_read_b128 v[16:19], %[l1] offset:4096\n\t" "ds_read_b128 v[20:23], %[l1] offset:8192\n\t" \
    "ds_read_b128 v[24:27], %[l1] offset:12288\n\t" "s_waitcnt vmcnt(2)\n\t" "v_smfmac_f32_16x16x64_bf16 v[232:235], v[248:251], a[24:31], %[ix]\n\t" \
    "ds_write_b128 %[l1], v[248:251] offset:2048\n\t" "v_smfmac_f32_16x16x64_bf16 v[236:239], v[248:251], a[32:39], %[ix]\n\t" "ds_read_b128 v[28:31], %[l1] offset:5120\n\t" \
    "ds_read_b128 v[240:243], %[l1] offset:9216\n\t" "ds_read_b128 v[244:247], %[l1] offset:13312\n\t" "s_waitcnt vmcnt(1)\n\t" \
    "v_smfmac_f32_16x16x64_bf16 v[232:235], v[252:255], a[40:47], %[ix]\n\t" "ds_write_b128 %[l1], v[252:255] offset:3072\n\t" \
    "v_smfmac_f32_16x16x64_bf16 v[236:239], v[252:255], v[168:175], %[ix]\n\t" "ds_read_b128 v[160:163], %[l0] offset:8192\n\t" \
    "ds_read_b128 v[164:167], %[l0] offset:9216\n\t" "ds_read_b128 v[168:171], %[l0] offset:10240\n\t" "ds_read_b128 v[172:175], %[l0] offset:11264\n\t" "s_nop 1\n\t" \
    "v_cmp_u_f32 vcc, v232, v233\n\t" "v_cmp_u_f32 s[62:63], v234, v235\n\t" "s_waitcnt lgkmcnt(11)\n\t" \
    "v_smfmac_f32_16x16x64_bf16 v[232:235], v[16:19], a[48:55], %[ix]\n\t" "v_smfmac_f32_16x16x64_bf16 v[236:239], v[16:19], a[56:63], %[ix]\n\t" \
    "s_or_b64 vcc, vcc, s[62:63]\n\t" "s_cbranch_scc1 11f\n\t" "12:\n\t" "s_waitcnt lgkmcnt(10)\n\t" "v_smfmac_f32_16x16x64_bf16 v[232:235], v[20:23], a[64:71], %[ix]\n\t" \
    "v_smfmac_f32_16x16x64_bf16 v[236:239], v[20:23], v[176:183], %[ix]\n\t" "s_waitcnt lgkmcnt(4)\n\t" "s_barrier\n\t" XT(88, 89) "ds_read_b32 v225, v227\n\t" \
    "v_smfmac_f32_16x16x64_bf16 v[232:235], v[24:27], a[72:79], %[ix]\n\t" "ds_read_b128 v[176:179], %[l0] offset:12288\n\t" "ds_read_b128 v[180:183], %[l0] offset:13312\n\t" \
    "v_smfmac_f32_16x16x64_bf16 v[236:239], v[24:27], a[80:87], %[ix]\n\t" "ds_read_b128 v[248:251], %[l1] offset:6144\n\t" "ds_read_b128 v[252:255], %[l1] offset:10240\n\t" \
    "v_smfmac_f32_16x16x64_bf16 v[232:235], v[28:31], a[88:95], %[ix]\n\t" "ds_read_b128 v[16:19], %[l1] offset:14336\n\t" "ds_read_b128 v[20:23], %[l1] offset:7168\n\t" \
    "v_smfmac_f32_16x16x64_bf16 v[236:239], v[28:31], v[184:191], %[ix]\n\t" "s_memtime s[64:65]\n\t" "ds_read_b128 v[24:27], %[l1] offset:11264\n\t" \
    "v_smfmac_f32_16x16x64_bf16 v[232:235], v[240:243], a[96:103], %[ix]\n\t" "ds_read_b128 v[184:187], %[l0] offset:14336\n\t" \
    "ds_read_b128 v[188:191], %[l0] offset:15360\n\t" "v_smfmac_f32_16x16x64_bf16 v[236:239], v[240:243], a[104:111], %[ix]\n\t" "s_waitcnt lgkmcnt(9)\n\t" \
    "v_cmp_eq_u32 vcc, %[rt], v225\n\t" "v_smfmac_f32_16x16x64_bf16 v[232:235], v[244:247], a[112:119], %[ix]\n\t" "s_cbranch_vccnz 13f\n\t" \
    "ds_read_b128 v[28:31], %[l1] offset:15360\n\t" "v_smfmac_f32_16x16x64_bf16 v[236:239], v[244:247], v[160:167], %[ix]\n\t" \
    "ds_read_b128 v[160:163], %[l0] offset:16384\n\t" "ds_read_b128 v[164:167], %[l0] offset:17408\n\t" "s_waitcnt lgkmcnt(9)\n\t" \
    "v_smfmac_f32_16x16x64_bf16 v[232:235], v[248:251], a[120:127], %[ix]\n\t" "s_sub_u32 s62, s64, %[ca]\n\t" "s_mul_hi_u32 s63, s62, %[k1]\n\t" \
    "v_smfmac_f32_16x16x64_bf16 v[236:239], v[248:251], a[128:135], %[ix]\n\t" "s_mul_i32 s62, s62, %[k1]\n\t" "s_lshr_b64 s[62:63], s[62:63], 16\n\t" \
    "s_waitcnt lgkmcnt(8)\n\t" "v_smfmac_f32_16x16x64_bf16 v[232:235], v[252:255], a[136:143], %[ix]\n\t" "s_add_u32 s62, s62, %[ga]\n\t" "s_bitset0_b32 s62, 0\n\t" \
    "v_smfmac_f32_16x16x64_bf16 v[236:239], v[252:255], v[168:175], %[ix]\n\t" "v_mov_b32 v226, s62\n\t" "buffer_store_dword v226, %[hq], %[rs], s67 offen sc1\n\t" \
    "s_waitcnt lgkmcnt(7)\n\t" "v_smfmac_f32_16x16x64_bf16 v[232:235], v[16:19], a[144:151], %[ix]\n\t" "ds_read_b128 v[168:171], %[l0] offset:18432\n\t" \
    "ds_read_b128 v[172:175], %[l0] offset:19456\n\t" "v_smfmac_f32_16x16x64_bf16 v[236:239], v[16:19], a[152:159], %[ix]\n\t" "s_waitcnt lgkmcnt(8)\n\t" \
    "v_smfmac_f32_16x16x64_bf16 v[232:235], v[20:23], a[160:167], %[ix]\n\t" "v_smfmac_f32_16x16x64_bf16 v[236:239], v[20:23], v[176:183], %[ix]\n\t" \
    "ds_read_b128 v[176:179], %[l0] offset:20480\n\t" "ds_read_b128 v[180:183], %[l0] offset:21504\n\t" "s_waitcnt lgkmcnt(9)\n\t" \
    "v_smfmac_f32_16x16x64_bf16 v[232:235], v[24:27], a[168:175], %[ix]\n\t" "v_smfmac_f32_16x16x64_bf16 v[236:239], v[24:27], a[176:183], %[ix]\n\t" \
    "s_waitcnt lgkmcnt(6)\n\t" "v_smfmac_f32_16x16x64_bf16 v[232:235], v[28:31], a[184:191], %[ix]\n\t" \
    "v_smfmac_f32_16x16x64_bf16 v[236:239], v[28:31], v[184:191], %[ix]\n\t" "ds_read_b128 v[184:187], %[l0] offset:22528\n\t" \
    "ds_read_b128 v[188:191], %[l0] offset:23552\n\t" XT(90, 91) "s_nop 4\n\t" "v_add_f32 v232, v232, v233\n\t" "v_add_f32 v234, v234, v235\n\t" \
    "v_add_f32 v236, v236, v237\n\t" "v_add_f32 v238, v238, v239\n\t" "v_cvt_pk_bf16_f32 v236, v232, v236\n\t" "v_cvt_pk_bf16_f32 v237, v234, v238\n\t" \
    "ds_write2_b32 %[vw], v236, v237 offset1:68\n\t" "s_waitcnt lgkmcnt(0)\n\t" "s_barrier\n\t" XT(92, 93) "ds_read_b128 v[240:243], %[vr] offset:0\n\t" \
    "ds_read_b128 v[244:247], %[vr] offset:64\n\t" "ds_read_b128 v[248:251], %[vr] offset:128\n\t" "ds_read_b128 v[252:255], %[vr] offset:192\n\t" "s_mov_b32 s61, %[ub]\n\t" \
    "s_waitcnt lgkmcnt(3)\n\t" "v_smfmac_f32_16x16x64_bf16 v[192:195], v[240:243], v[32:39], %[ix]\n\t" \
    "v_smfmac_f32_16x16x64_bf16 v[196:199], v[240:243], v[48:55], %[ix]\n\t" "s_waitcnt lgkmcnt(2)\n\t" \
    "v_smfmac_f32_16x16x64_bf16 v[192:195], v[244:247], v[40:47], %[ix]\n\t" "v_smfmac_f32_16x16x64_bf16 v[196:199], v[244:247], v[56:63], %[ix]\n\t" \
    "s_waitcnt lgkmcnt(1)\n\t" "v_smfmac_f32_16x16x64_bf16 v[192:195], v[248:251], a[192:199], %[ix]\n\t" \
    "v_smfmac_f32_16x16x64_bf16 v[196:199], v[248:251], a[200:207], %[ix]\n\t" "s_waitcnt lgkmcnt(0)\n\t" \
    "v_smfmac_f32_16x16x64_bf16 v[192:195], v[252:255], v[160:167], %[ix]\n\t" "ds_read_b128 v[160:163], %[l0] offset:24576\n\t" \
    "ds_read_b128 v[164:167], %[l0] offset:25600\n\t" "v_smfmac_f32_16x16x64_bf16 v[196:199], v[252:255], v[168:175], %[ix]\n\t" \
    "ds_read_b128 v[168:171], %[l0] offset:26624\n\t" "ds_read_b128 v[172:175], %[l0] offset:27648\n\t" \
    "v_smfmac_f32_16x16x64_bf16 v[200:203], v[240:243], v[64:71], %[ix]\n\t" "s_nop 1\n\t" "v_smfmac_f32_16x16x64_bf16 v[204:207], v[240:243], v[80:87], %[ix]\n\t" \
    "v_cvt_pk_bf16_f32 v224, v192, v194\n\t" "v_cvt_pk_bf16_f32 v225, v193, v195\n\t" "v_smfmac_f32_16x16x64_bf16 v[200:203], v[244:247], v[72:79], %[ix]\n\t" \
    "v_cvt_pk_bf16_f32 v226, v196, v198\n\t" "v_cvt_pk_bf16_f32 v227, v197, v199\n\t" "v_smfmac_f32_16x16x64_bf16 v[204:207], v[244:247], v[88:95], %[ix]\n\t" "s_nop 0\n\t" \
    "v_permlane32_swap_b32 v224, v226\n\t" "v_smfmac_f32_16x16x64_bf16 v[200:203], v[248:251], a[208:215], %[ix]\n\t" "v_permlane32_swap_b32 v225, v227\n\t" "s_nop 0\n\t" \
    "v_smfmac_f32_16x16x64_bf16 v[204:207], v[248:251], a[216:223], %[ix]\n\t" "v_permlane16_swap_b32 v224, v225\n\t" "v_permlane16_swap_b32 v226, v227\n\t" \
    "v_smfmac_f32_16x16x64_bf16 v[200:203], v[252:255], v[176:183], %[ix]\n\t" "ds_read_b128 v[176:179], %[l0] offset:28672\n\t" \
    "buffer_store_dwordx4 v[224:227], %[uo], %[rs], s61 offen sc1\n\t" "s_xor_b32 s61, s61, 0x40000\n\t" XT(94, 95) \
    "v_smfmac_f32_16x16x64_bf16 v[204:207], v[252:255], v[184:191], %[ix]\n\t" "ds_read_b128 v[180:183], %[l0] offset:29696\n\t" \
    "ds_read_b128 v[184:187], %[l0] offset:30720\n\t" "v_smfmac_f32_16x16x64_bf16 v[208:211], v[240:243], v[96:103], %[ix]\n\t" \
    "ds_read_b128 v[188:191], %[l0] offset:31744\n\t" "s_nop 0\n\t" "v_smfmac_f32_16x16x64_bf16 v[212:215], v[240:243], v[112:119], %[ix]\n\t" \
    "v_cvt_pk_bf16_f32 v228, v200, v202\n\t" "v_cvt_pk_bf16_f32 v229, v201, v203\n\t" "v_smfmac_f32_16x16x64_bf16 v[208:211], v[244:247], v[104:111], %[ix]\n\t" \
    "v_cvt_pk_bf16_f32 v230, v204, v206\n\t" "v_cvt_pk_bf16_f32 v231, v205, v207\n\t" "v_smfmac_f32_16x16x64_bf16 v[212:215], v[244:247], v[120:127], %[ix]\n\t" "s_nop 0\n\t" \
    "v_permlane32_swap_b32 v228, v230\n\t" "v_smfmac_f32_16x16x64_bf16 v[208:211], v[248:251], a[224:231], %[ix]\n\t" "v_permlane32_swap_b32 v229, v231\n\t" "s_nop 0\n\t" \
    "v_smfmac_f32_16x16x64_bf16 v[212:215], v[248:251], a[232:239], %[ix]\n\t" "v_permlane16_swap_b32 v228, v229\n\t" "v_permlane16_swap_b32 v230, v231\n\t" \
    "s_waitcnt lgkmcnt(6)\n\t" "v_smfmac_f32_16x16x64_bf16 v[208:211], v[252:255], v[160:167], %[ix]\n\t" "buffer_store_dwordx4 v[228:231], %[uo], %[rs], s61 offen sc1\n\t" \
    "s_xor_b32 s61, s61, 0xc0000\n\t" "s_waitcnt lgkmcnt(4)\n\t" "v_smfmac_f32_16x16x64_bf16 v[212:215], v[252:255], v[168:175], %[ix]\n\t" "s_nop 1\n\t" \
    "v_smfmac_f32_16x16x64_bf16 v[216:219], v[240:243], v[128:135], %[ix]\n\t" "s_nop 1\n\t" "v_smfmac_f32_16x16x64_bf16 v[220:223], v[240:243], v[144:151], %[ix]\n\t" \
    "v_cvt_pk_bf16_f32 v224, v208, v210\n\t" "v_cvt_pk_bf16_f32 v225, v209, v211\n\t" "v_smfmac_f32_16x16x64_bf16 v[216:219], v[244:247], v[136:143], %[ix]\n\t" \
    "v_cvt_pk_bf16_f32 v226, v212, v214\n\t" "v_cvt_pk_bf16_f32 v227, v213, v215\n\t" "v_smfmac_f32_16x16x64_bf16 v[220:223], v[244:247], v[152:159], %[ix]\n\t" "s_nop 0\n\t" \
    "v_permlane32_swap_b32 v224, v226\n\t" "v_smfmac_f32_16x16x64_bf16 v[216:219], v[248:251], a[240:247], %[ix]\n\t" "v_permlane32_swap_b32 v225, v227\n\t" "s_nop 0\n\t" \
    "v_smfmac_f32_16x16x64_bf16 v[220:223], v[248:251], a[248:255], %[ix]\n\t" "v_permlane16_swap_b32 v224, v225\n\t" "v_permlane16_swap_b32 v226, v227\n\t" \
    "s_waitcnt lgkmcnt(2)\n\t" "v_smfmac_f32_16x16x64_bf16 v[216:219], v[252:255], v[176:183], %[ix]\n\t" "buffer_store_dwordx4 v[224:227], %[uo], %[rs], s61 offen sc1\n\t" \
    "s_xor_b32 s61, s61, 0x40000\n\t" "s_waitcnt lgkmcnt(0)\n\t" "v_smfmac_f32_16x16x64_bf16 v[220:223], v[252:255], v[184:191], %[ix]\n\t" "s_nop 5\n\t" \
    "v_cvt_pk_bf16_f32 v228, v216, v218\n\t" "v_cvt_pk_bf16_f32 v229, v217, v219\n\t" "v_cvt_pk_bf16_f32 v230, v220, v222\n\t" "v_cvt_pk_bf16_f32 v231, v221, v223\n\t" \
    "s_nop 0\n\t" "v_permlane32_swap_b32 v228, v230\n\t" "v_permlane32_swap_b32 v229, v231\n\t" "s_nop 1\n\t" "v_permlane16_swap_b32 v228, v229\n\t" \
    "v_permlane16_swap_b32 v230, v231\n\t" "buffer_store_dwordx4 v[228:231], %[uo], %[rs], s61 offen sc1\n\t" "s_nop 1\n\t" XT(96, 97) "s_branch 99f\n\t" "11:\n\t" \
    "ds_read_b128 v[228:231], %[l1] offset:0\n\t" "s_waitcnt lgkmcnt(0)\n\t" "v_max3_u32 v225, v228, v229, v230\n\t" "v_max_u32 v225, v225, v231\n\t" \
    "ds_read_b128 v[228:231], %[l1] offset:1024\n\t" "s_waitcnt lgkmcnt(0)\n\t" "v_max3_u32 v225, v225, v228, v229\n\t" "v_max3_u32 v225, v225, v230, v231\n\t" \
    "ds_read_b128 v[228:231], %[l1] offset:2048\n\t" "s_waitcnt lgkmcnt(0)\n\t" "v_max3_u32 v225, v225, v228, v229\n\t" "v_max3_u32 v225, v225, v230, v231\n\t" \
    "ds_read_b128 v[228:231], %[l1] offset:3072\n\t" "s_waitcnt lgkmcnt(0)\n\t" "v_max3_u32 v225, v225, v228, v229\n\t" "v_max3_u32 v225, v225, v230, v231\n\t" \
    "v_cmp_eq_u32 vcc, -1, v225\n\t" "s_cbranch_vccz 12b\n\t" "s_cmp_eq_u32 %[rt], 1\n\t" "s_cbranch_scc1 12b\n\t" "v_mov_b32 v225, %[rt]\n\t" "ds_write_b32 v227, v225\n\t" \
    "s_waitcnt lgkmcnt(0)\n\t" "s_branch 12b\n\t" "13:\n\t" "s_sub_u32 %[rt], %[rt], 1\n\t" "s_waitcnt lgkmcnt(0)\n\t" XCNT(100) "14:\n\t" \
    "buffer_load_dwordx4 v[240:243], %[mo], %[rs], %[sb] offen sc1\n\t" "buffer_load_dwordx4 v[244:247], v224, %[rs], %[sb] offen sc1\n\t" \
    "buffer_load_dwordx4 v[248:251], %[mo], %[rs], s60 offen sc1\n\t" "buffer_load_dwordx4 v[252:255], v224, %[rs], s60 offen sc1\n\t" "s_waitcnt vmcnt(0)\n\t" \
    "v_max_u32 v225, v240, v241\n\t" "v_max3_u32 v225, v225, v242, v243\n\t" "v_max3_u32 v225, v225, v244, v245\n\t" "v_max3_u32 v225, v225, v246, v247\n\t" \
    "v_max3_u32 v225, v225, v248, v249\n\t" "v_max3_u32 v225, v225, v250, v251\n\t" "v_max3_u32 v225, v225, v252, v253\n\t" "v_max3_u32 v225, v225, v254, v255\n\t" \
    "v_cmp_eq_u32 vcc, -1, v225\n\t" "s_cbranch_vccz 15f\n\t" "s_cmp_eq_u32 %[bd], 0\n\t" "s_cbranch_scc1 15f\n\t" "s_sub_u32 %[bd], %[bd], 1\n\t" XCNT(101) \
    "s_branch 14b\n\t" "15:\n\t" "ds_read_b128 v[160:163], %[l0] offset:0\n\t" "ds_read_b128 v[164:167], %[l0] offset:1024\n\t" \
    "ds_read_b128 v[168:171], %[l0] offset:2048\n\t" "ds_read_b128 v[172:175], %[l0] offset:3072\n\t" "ds_read_b128 v[176:179], %[l0] offset:4096\n\t" \
    "ds_read_b128 v[180:183], %[l0] offset:5120\n\t" "ds_read_b128 v[184:187], %[l0] offset:6144\n\t" "ds_read_b128 v[188:191], %[l0] offset:7168\n\t" \
    "v_mov_b64 v[232:233], 0\n\t" "v_mov_b64 v[234:235], 0\n\t" "v_mov_b64 v[236:237], 0\n\t" "v_mov_b64 v[238:239], 0\n\t" "s_barrier\n\t" "s_memtime s[64:65]\n\t" \
    "s_waitcnt lgkmcnt(0)\n\t" "s_add_u32 s64, s64, %[dr]\n\t" "s_sub_u32 s62, s64, %[ca]\n\t" "s_mul_hi_u32 s63, s62, %[k1]\n\t" "s_mul_i32 s62, s62, %[k1]\n\t" \
    "s_lshr_b64 s[62:63], s[62:63], 16\n\t" "s_add_u32 s62, s62, %[ga]\n\t" "s_bitset0_b32 s62, 0\n\t" "v_mov_b32 v225, s62\n\t" \
    "buffer_store_dword v225, %[hq], %[rs], s67 offen sc1\n\t" "s_mov_b32 s67, 0x7f000000\n\t" "s_branch 10b\n\t" "91:\n\t" XT(80, 81) "s_barrier\n\t" XT(82, 83) \
    "buffer_load_dwordx4 v[240:243], %[mo], %[rs], %[sb] offen sc1\n\t" "v_mov_b64 v[232:233], 0\n\t" "v_mov_b64 v[234:235], 0\n\t" "v_mov_b64 v[236:237], 0\n\t" \
    "v_mov_b64 v[238:239], 0\n\t" "v_mov_b64 v[192:193], 0\n\t" "v_mov_b64 v[194:195], 0\n\t" "v_mov_b64 v[196:197], 0\n\t" "v_mov_b64 v[198:199], 0\n\t" \
    "buffer_load_dwordx4 v[244:247], v224, %[rs], %[sb] offen sc1\n\t" "ds_read_b128 v[160:163], %[l0] offset:0\n\t" "ds_read_b128 v[164:167], %[l0] offset:1024\n\t" \
    "ds_read_b128 v[168:171], %[l0] offset:2048\n\t" "ds_read_b128 v[172:175], %[l0] offset:3072\n\t" "s_nop 3\n\t" \
    "buffer_load_dwordx4 v[248:251], %[mo], %[rs], s60 offen sc1\n\t" "ds_read_b128 v[176:179], %[l0] offset:4096\n\t" "ds_read_b128 v[180:183], %[l0] offset:5120\n\t" \
    "ds_read_b128 v[184:187], %[l0] offset:6144\n\t" "ds_read_b128 v[188:191], %[l0] offset:7168\n\t" "s_nop 3\n\t" \
    "buffer_load_dwordx4 v[252:255], v224, %[rs], s60 offen sc1\n\t" "v_mov_b64 v[200:201], 0\n\t" "v_mov_b64 v[202:203], 0\n\t" "v_mov_b64 v[204:205], 0\n\t" \
    "v_mov_b64 v[206:207], 0\n\t" "v_mov_b64 v[208:209], 0\n\t" "v_mov_b64 v[210:211], 0\n\t" "v_mov_b64 v[212:213], 0\n\t" "v_mov_b64 v[214:215], 0\n\t" \
    "v_mov_b64 v[216:217], 0\n\t" "v_mov_b64 v[218:219], 0\n\t" "v_mov_b64 v[220:221], 0\n\t" "v_mov_b64 v[222:223], 0\n\t" "v_mov_b32 v227, 160512\n\t" "20:\n\t" \
    "s_waitcnt vmcnt(3)\n\t" XT(84, 85) "v_smfmac_f32_16x16x64_bf16 v[232:235], v[240:243], a[0:7], %[ix]\n\t" "ds_write_b128 %[l1], v[240:243] offset:4096\n\t" \
    "v_smfmac_f32_16x16x64_bf16 v[236:239], v[240:243], a[8:15], %[ix]\n\t" "s_waitcnt vmcnt(2)\n\t" "v_smfmac_f32_16x16x64_bf16 v[232:235], v[244:247], a[16:23], %[ix]\n\t" \
    "ds_write_b128 %[l1], v[244:247] offset:5120\n\t" "s_waitcnt lgkmcnt(8)\n\t" "v_smfmac_f32_16x16x64_bf16 v[236:239], v[244:247], v[160:167], %[ix]\n\t" \
    "s_waitcnt lgkmcnt(0)\n\t" "s_barrier\n\t" XT(86, 87) "ds_read_b128 v[16:19], %[l1] offset:8192\n\t" "ds_read_b128 v[20:23], %[l1] offset:12288\n\t" \
    "ds_read_b128 v[24:27], %[l1] offset:0\n\t" "s_waitcnt vmcnt(1)\n\t" "v_smfmac_f32_16x16x64_bf16 v[232:235], v[248:251], a[24:31], %[ix]\n\t" \
    "ds_write_b128 %[l1], v[248:251] offset:6144\n\t" "v_smfmac_f32_16x16x64_bf16 v[236:239], v[248:251], a[32:39], %[ix]\n\t" "ds_read_b128 v[28:31], %[l1] offset:9216\n\t" \
    "ds_read_b128 v[240:243], %[l1] offset:13312\n\t" "ds_read_b128 v[244:247], %[l1] offset:1024\n\t" "s_waitcnt vmcnt(0)\n\t" \
    "v_smfmac_f32_16x16x64_bf16 v[232:235], v[252:255], a[40:47], %[ix]\n\t" "ds_write_b128 %[l1], v[252:255] offset:7168\n\t" \
    "v_smfmac_f32_16x16x64_bf16 v[236:239], v[252:255], v[168:175], %[ix]\n\t" "ds_read_b128 v[160:163], %[l0] offset:8192\n\t" \
    "ds_read_b128 v[164:167], %[l0] offset:9216\n\t" "ds_read_b128 v[168:171], %[l0] offset:10240\n\t" "ds_read_b128 v[172:175], %[l0] offset:11264\n\t" "s_nop 1\n\t" \
    "v_cmp_u_f32 vcc, v232, v233\n\t" "v_cmp_u_f32 s[62:63], v234, v235\n\t" "s_waitcnt lgkmcnt(11)\n\t" \
    "v_smfmac_f32_16x16x64_bf16 v[232:235], v[16:19], a[48:55], %[ix]\n\t" "v_smfmac_f32_16x16x64_bf16 v[236:239], v[16:19], a[56:63], %[ix]\n\t" \
    "s_or_b64 vcc, vcc, s[62:63]\n\t" "s_cbranch_scc1 21f\n\t" "22:\n\t" "s_waitcnt lgkmcnt(10)\n\t" "v_smfmac_f32_16x16x64_bf16 v[232:235], v[20:23], a[64:71], %[ix]\n\t" \
    "v_smfmac_f32_16x16x64_bf16 v[236:239], v[20:23], v[176:183], %[ix]\n\t" "s_waitcnt lgkmcnt(4)\n\t" "s_barrier\n\t" XT(88, 89) "ds_read_b32 v225, v227\n\t" \
    "v_smfmac_f32_16x16x64_bf16 v[232:235], v[24:27], a[72:79], %[ix]\n\t" "ds_read_b128 v[176:179], %[l0] offset:12288\n\t" "ds_read_b128 v[180:183], %[l0] offset:13312\n\t" \
    "v_smfmac_f32_16x16x64_bf16 v[236:239], v[24:27], a[80:87], %[ix]\n\t" "ds_read_b128 v[248:251], %[l1] offset:10240\n\t" "ds_read_b128 v[252:255], %[l1] offset:14336\n\t" \
    "v_smfmac_f32_16x16x64_bf16 v[232:235], v[28:31], a[88:95], %[ix]\n\t" "ds_read_b128 v[16:19], %[l1] offset:2048\n\t" "ds_read_b128 v[20:23], %[l1] offset:11264\n\t" \
    "v_smfmac_f32_16x16x64_bf16 v[236:239], v[28:31], v[184:191], %[ix]\n\t" "ds_read_b128 v[24:27], %[l1] offset:15360\n\t" "ds_read_b128 v[184:187], %[l0] offset:14336\n\t" \
    "v_smfmac_f32_16x16x64_bf16 v[232:235], v[240:243], a[96:103], %[ix]\n\t" "ds_read_b128 v[188:191], %[l0] offset:15360\n\t" "ds_read_b128 v[28:31], %[l1] offset:3072\n\t" \
    "v_smfmac_f32_16x16x64_bf16 v[236:239], v[240:243], a[104:111], %[ix]\n\t" "s_waitcnt lgkmcnt(10)\n\t" "v_cmp_eq_u32 vcc, %[rt], v225\n\t" \
    "v_smfmac_f32_16x16x64_bf16 v[232:235], v[244:247], a[112:119], %[ix]\n\t" "s_cbranch_vccnz 23f\n\t" \
    "v_smfmac_f32_16x16x64_bf16 v[236:239], v[244:247], v[160:167], %[ix]\n\t" "ds_read_b128 v[160:163], %[l0] offset:16384\n\t" \
    "ds_read_b128 v[164:167], %[l0] offset:17408\n\t" "s_waitcnt lgkmcnt(9)\n\t" "v_smfmac_f32_16x16x64_bf16 v[232:235], v[248:251], a[120:127], %[ix]\n\t" \
    "v_smfmac_f32_16x16x64_bf16 v[236:239], v[248:251], a[128:135], %[ix]\n\t" "s_waitcnt lgkmcnt(8)\n\t" \
    "v_smfmac_f32_16x16x64_bf16 v[232:235], v[252:255], a[136:143], %[ix]\n\t" "v_smfmac_f32_16x16x64_bf16 v[236:239], v[252:255], v[168:175], %[ix]\n\t" \
    "ds_read_b128 v[168:171], %[l0] offset:18432\n\t" "ds_read_b128 v[172:175], %[l0] offset:19456\n\t" "s_waitcnt lgkmcnt(9)\n\t" \
    "v_smfmac_f32_16x16x64_bf16 v[232:235], v[16:19], a[144:151], %[ix]\n\t" "v_smfmac_f32_16x16x64_bf16 v[236:239], v[16:19], a[152:159], %[ix]\n\t" \
    "s_waitcnt lgkmcnt(8)\n\t" "v_smfmac_f32_16x16x64_bf16 v[232:235], v[20:23], a[160:167], %[ix]\n\t" \
    "v_smfmac_f32_16x16x64_bf16 v[236:239], v[20:23], v[176:183], %[ix]\n\t" "ds_read_b128 v[176:179], %[l0] offset:20480\n\t" \
    "ds_read_b128 v[180:183], %[l0] offset:21504\n\t" "s_waitcnt lgkmcnt(9)\n\t" "v_smfmac_f32_16x16x64_bf16 v[232:235], v[24:27], a[168:175], %[ix]\n\t" \
    "v_smfmac_f32_16x16x64_bf16 v[236:239], v[24:27], a[176:183], %[ix]\n\t" "s_waitcnt lgkmcnt(6)\n\t" \
    "v_smfmac_f32_16x16x64_bf16 v[232:235], v[28:31], a[184:191], %[ix]\n\t" "v_smfmac_f32_16x16x64_bf16 v[236:239], v[28:31], v[184:191], %[ix]\n\t" \
    "ds_read_b128 v[184:187], %[l0] offset:22528\n\t" "ds_read_b128 v[188:191], %[l0] offset:23552\n\t" XT(90, 91) "s_nop 4\n\t" "v_add_f32 v232, v232, v233\n\t" \
    "v_add_f32 v234, v234, v235\n\t" "v_add_f32 v236, v236, v237\n\t" "v_add_f32 v238, v238, v239\n\t" "v_cvt_pk_bf16_f32 v236, v232, v236\n\t" \
    "v_cvt_pk_bf16_f32 v237, v234, v238\n\t" "ds_write2_b32 %[vw], v236, v237 offset1:68\n\t" "s_waitcnt lgkmcnt(0)\n\t" "s_barrier\n\t" XT(92, 93) \
    "ds_read_b128 v[240:243], %[vr] offset:0\n\t" "ds_read_b128 v[244:247], %[vr] offset:64\n\t" "ds_read_b128 v[248:251], %[vr] offset:128\n\t" \
    "ds_read_b128 v[252:255], %[vr] offset:192\n\t" "s_mov_b32 s61, %[ub]\n\t" "s_waitcnt lgkmcnt(3)\n\t" \
    "v_smfmac_f32_16x16x64_bf16 v[192:195], v[240:243], v[32:39], %[ix]\n\t" "v_smfmac_f32_16x16x64_bf16 v[196:199], v[240:243], v[48:55], %[ix]\n\t" \
    "s_waitcnt lgkmcnt(2)\n\t" "v_smfmac_f32_16x16x64_bf16 v[192:195], v[244:247], v[40:47], %[ix]\n\t" \
    "v_smfmac_f32_16x16x64_bf16 v[196:199], v[244:247], v[56:63], %[ix]\n\t" "s_waitcnt lgkmcnt(1)\n\t" \
    "v_smfmac_f32_16x16x64_bf16 v[192:195], v[248:251], a[192:199], %[ix]\n\t" "v_smfmac_f32_16x16x64_bf16 v[196:199], v[248:251], a[200:207], %[ix]\n\t" \
    "s_waitcnt lgkmcnt(0)\n\t" "v_smfmac_f32_16x16x64_bf16 v[192:195], v[252:255], v[160:167], %[ix]\n\t" "ds_read_b128 v[160:163], %[l0] offset:24576\n\t" \
    "ds_read_b128 v[164:167], %[l0] offset:25600\n\t" "v_smfmac_f32_16x16x64_bf16 v[196:199], v[252:255], v[168:175], %[ix]\n\t" \
    "ds_read_b128 v[168:171], %[l0] offset:26624\n\t" "ds_read_b128 v[172:175], %[l0] offset:27648\n\t" \
    "v_smfmac_f32_16x16x64_bf16 v[200:203], v[240:243], v[64:71], %[ix]\n\t" "s_nop 1\n\t" "v_smfmac_f32_16x16x64_bf16 v[204:207], v[240:243], v[80:87], %[ix]\n\t" \
    "v_cvt_pk_bf16_f32 v224, v192, v194\n\t" "v_cvt_pk_bf16_f32 v225, v193, v195\n\t" "v_smfmac_f32_16x16x64_bf16 v[200:203], v[244:247], v[72:79], %[ix]\n\t" \
    "v_cvt_pk_bf16_f32 v226, v196, v198\n\t" "v_cvt_pk_bf16_f32 v227, v197, v199\n\t" "v_smfmac_f32_16x16x64_bf16 v[204:207], v[244:247], v[88:95], %[ix]\n\t" "s_nop 0\n\t" \
    "v_permlane32_swap_b32 v224, v226\n\t" "v_smfmac_f32_16x16x64_bf16 v[200:203], v[248:251], a[208:215], %[ix]\n\t" "v_permlane32_swap_b32 v225, v227\n\t" "s_nop 0\n\t" \
    "v_smfmac_f32_16x16x64_bf16 v[204:207], v[248:251], a[216:223], %[ix]\n\t" "v_permlane16_swap_b32 v224, v225\n\t" "v_permlane16_swap_b32 v226, v227\n\t" \
    "v_smfmac_f32_16x16x64_bf16 v[200:203], v[252:255], v[176:183], %[ix]\n\t" "ds_read_b128 v[176:179], %[l0] offset:28672\n\t" \
    "buffer_store_dwordx4 v[224:227], %[uo], %[rs], s61 offen sc1\n\t" "s_xor_b32 s61, s61, 0x40000\n\t" XT(94, 95) \
    "v_smfmac_f32_16x16x64_bf16 v[204:207], v[252:255], v[184:191], %[ix]\n\t" "ds_read_b128 v[180:183], %[l0] offset:29696\n\t" \
    "ds_read_b128 v[184:187], %[l0] offset:30720\n\t" "v_smfmac_f32_16x16x64_bf16 v[208:211], v[240:243], v[96:103], %[ix]\n\t" \
    "ds_read_b128 v[188:191], %[l0] offset:31744\n\t" "s_nop 0\n\t" "v_smfmac_f32_16x16x64_bf16 v[212:215], v[240:243], v[112:119], %[ix]\n\t" \
    "v_cvt_pk_bf16_f32 v228, v200, v202\n\t" "v_cvt_pk_bf16_f32 v229, v201, v203\n\t" "v_smfmac_f32_16x16x64_bf16 v[208:211], v[244:247], v[104:111], %[ix]\n\t" \
    "v_cvt_pk_bf16_f32 v230, v204, v206\n\t" "v_cvt_pk_bf16_f32 v231, v205, v207\n\t" "v_smfmac_f32_16x16x64_bf16 v[212:215], v[244:247], v[120:127], %[ix]\n\t" "s_nop 0\n\t" \
    "v_permlane32_swap_b32 v228, v230\n\t" "v_smfmac_f32_16x16x64_bf16 v[208:211], v[248:251], a[224:231], %[ix]\n\t" "v_permlane32_swap_b32 v229, v231\n\t" "s_nop 0\n\t" \
    "v_smfmac_f32_16x16x64_bf16 v[212:215], v[248:251], a[232:239], %[ix]\n\t" "v_permlane16_swap_b32 v228, v229\n\t" "v_permlane16_swap_b32 v230, v231\n\t" \
    "s_waitcnt lgkmcnt(6)\n\t" "v_smfmac_f32_16x16x64_bf16 v[208:211], v[252:255], v[160:167], %[ix]\n\t" "buffer_store_dwordx4 v[228:231], %[uo], %[rs], s61 offen sc1\n\t" \
    "s_xor_b32 s61, s61, 0xc0000\n\t" "s_waitcnt lgkmcnt(4)\n\t" "v_smfmac_f32_16x16x64_bf16 v[212:215], v[252:255], v[168:175], %[ix]\n\t" "s_nop 1\n\t" \
    "v_smfmac_f32_16x16x64_bf16 v[216:219], v[240:243], v[128:135], %[ix]\n\t" "s_nop 1\n\t" "v_smfmac_f32_16x16x64_bf16 v[220:223], v[240:243], v[144:151], %[ix]\n\t" \
    "v_cvt_pk_bf16_f32 v224, v208, v210\n\t" "v_cvt_pk_bf16_f32 v225, v209, v211\n\t" "v_smfmac_f32_16x16x64_bf16 v[216:219], v[244:247], v[136:143], %[ix]\n\t" \
    "v_cvt_pk_bf16_f32 v226, v212, v214\n\t" "v_cvt_pk_bf16_f32 v227, v213, v215\n\t" "v_smfmac_f32_16x16x64_bf16 v[220:223], v[244:247], v[152:159], %[ix]\n\t" "s_nop 0\n\t" \
    "v_permlane32_swap_b32 v224, v226\n\t" "v_smfmac_f32_16x16x64_bf16 v[216:219], v[248:251], a[240:247], %[ix]\n\t" "v_permlane32_swap_b32 v225, v227\n\t" "s_nop 0\n\t" \
    "v_smfmac_f32_16x16x64_bf16 v[220:223], v[248:251], a[248:255], %[ix]\n\t" "v_permlane16_swap_b32 v224, v225\n\t" "v_permlane16_swap_b32 v226, v227\n\t" \
    "s_waitcnt lgkmcnt(2)\n\t" "v_smfmac_f32_16x16x64_bf16 v[216:219], v[252:255], v[176:183], %[ix]\n\t" "buffer_store_dwordx4 v[224:227], %[uo], %[rs], s61 offen sc1\n\t" \
    "s_xor_b32 s61, s61, 0x40000\n\t" "s_waitcnt lgkmcnt(0)\n\t" "v_smfmac_f32_16x16x64_bf16 v[220:223], v[252:255], v[184:191], %[ix]\n\t" "s_nop 5\n\t" \
    "v_cvt_pk_bf16_f32 v228, v216, v218\n\t" "v_cvt_pk_bf16_f32 v229, v217, v219\n\t" "v_cvt_pk_bf16_f32 v230, v220, v222\n\t" "v_cvt_pk_bf16_f32 v231, v221, v223\n\t" \
    "s_nop 0\n\t" "v_permlane32_swap_b32 v228, v230\n\t" "v_permlane32_swap_b32 v229, v231\n\t" "s_nop 1\n\t" "v_permlane16_swap_b32 v228, v229\n\t" \
    "v_permlane16_swap_b32 v230, v231\n\t" "buffer_store_dwordx4 v[228:231], %[uo], %[rs], s61 offen sc1\n\t" "s_nop 1\n\t" XT(96, 97) "s_branch 99f\n\t" "21:\n\t" \
    "ds_read_b128 v[228:231], %[l1] offset:4096\n\t" "s_waitcnt lgkmcnt(0)\n\t" "v_max3_u32 v225, v228, v229, v230\n\t" "v_max_u32 v225, v225, v231\n\t" \
    "ds_read_b128 v[228:231], %[l1] offset:5120\n\t" "s_waitcnt lgkmcnt(0)\n\t" "v_max3_u32 v225, v225, v228, v229\n\t" "v_max3_u32 v225, v225, v230, v231\n\t" \
    "ds_read_b128 v[228:231], %[l1] offset:6144\n\t" "s_waitcnt lgkmcnt(0)\n\t" "v_max3_u32 v225, v225, v228, v229\n\t" "v_max3_u32 v225, v225, v230, v231\n\t" \
    "ds_read_b128 v[228:231], %[l1] offset:7168\n\t" "s_waitcnt lgkmcnt(0)\n\t" "v_max3_u32 v225, v225, v228, v229\n\t" "v_max3_u32 v225, v225, v230, v231\n\t" \
    "v_cmp_eq_u32 vcc, -1, v225\n\t" "s_cbranch_vccz 22b\n\t" "s_cmp_eq_u32 %[rt], 1\n\t" "s_cbranch_scc1 22b\n\t" "v_mov_b32 v225, %[rt]\n\t" "ds_write_b32 v227, v225\n\t" \
    "s_waitcnt lgkmcnt(0)\n\t" "s_branch 22b\n\t" "23:\n\t" "s_sub_u32 %[rt], %[rt], 1\n\t" "s_waitcnt lgkmcnt(0)\n\t" XCNT(100) "24:\n\t" \
    "buffer_load_dwordx4 v[240:243], %[mo], %[rs], %[sb] offen sc1\n\t" "buffer_load_dwordx4 v[244:247], v224, %[rs], %[sb] offen sc1\n\t" \
    "buffer_load_dwordx4 v[248:251], %[mo], %[rs], s60 offen sc1\n\t" "buffer_load_dwordx4 v[252:255], v224, %[rs], s60 offen sc1\n\t" "s_waitcnt vmcnt(0)\n\t" \
    "v_max_u32 v225, v240, v241\n\t" "v_max3_u32 v225, v225, v242, v243\n\t" "v_max3_u32 v225, v225, v244, v245\n\t" "v_max3_u32 v225, v225, v246, v247\n\t" \
    "v_max3_u32 v225, v225, v248, v249\n\t" "v_max3_u32 v225, v225, v250, v251\n\t" "v_max3_u32 v225, v225, v252, v253\n\t" "v_max3_u32 v225, v225, v254, v255\n\t" \
    "v_cmp_eq_u32 vcc, -1, v225\n\t" "s_cbranch_vccz 25f\n\t" "s_cmp_eq_u32 %[bd], 0\n\t" "s_cbranch_scc1 25f\n\t" "s_sub_u32 %[bd], %[bd], 1\n\t" XCNT(101) \
    "s_branch 24b\n\t" "25:\n\t" "ds_read_b128 v[160:163], %[l0] offset:0\n\t" "ds_read_b128 v[164:167], %[l0] offset:1024\n\t" \
    "ds_read_b128 v[168:171], %[l0] offset:2048\n\t" "ds_read_b128 v[172:175], %[l0] offset:3072\n\t" "ds_read_b128 v[176:179], %[l0] offset:4096\n\t" \
    "ds_read_b128 v[180:183], %[l0] offset:5120\n\t" "ds_read_b128 v[184:187], %[l0] offset:6144\n\t" "ds_read_b128 v[188:191], %[l0] offset:7168\n\t" \
    "v_mov_b64 v[232:233], 0\n\t" "v_mov_b64 v[234:235], 0\n\t" "v_mov_b64 v[236:237], 0\n\t" "v_mov_b64 v[238:239], 0\n\t" "s_barrier\n\t" "s_branch 20b\n\t" "92:\n\t" \
    XT(80, 81) "s_barrier\n\t" XT(82, 83) "buffer_load_dwordx4 v[240:243], %[mo], %[rs], %[sb] offen sc1\n\t" "v_mov_b64 v[232:233], 0\n\t" "v_mov_b64 v[234:235], 0\n\t" \
    "v_mov_b64 v[236:237], 0\n\t" "v_mov_b64 v[238:239], 0\n\t" "v_mov_b64 v[192:193], 0\n\t" "v_mov_b64 v[194:195], 0\n\t" "v_mov_b64 v[196:197], 0\n\t" \
    "v_mov_b64 v[198:199], 0\n\t" "buffer_load_dwordx4 v[244:247], v224, %[rs], %[sb] offen sc1\n\t" "ds_read_b128 v[160:163], %[l0] offset:0\n\t" \
    "ds_read_b128 v[164:167], %[l0] offset:1024\n\t" "ds_read_b128 v[168:171], %[l0] offset:2048\n\t" "ds_read_b128 v[172:175], %[l0] offset:3072\n\t" "s_nop 3\n\t" \
    "buffer_load_dwordx4 v[248:251], %[mo], %[rs], s60 offen sc1\n\t" "ds_read_b128 v[176:179], %[l0] offset:4096\n\t" "ds_read_b128 v[180:183], %[l0] offset:5120\n\t" \
    "ds_read_b128 v[184:187], %[l0] offset:6144\n\t" "ds_read_b128 v[188:191], %[l0] offset:7168\n\t" "s_nop 3\n\t" \
    "buffer_load_dwordx4 v[252:255], v224, %[rs], s60 offen sc1\n\t" "v_mov_b64 v[200:201], 0\n\t" "v_mov_b64 v[202:203], 0\n\t" "v_mov_b64 v[204:205], 0\n\t" \
    "v_mov_b64 v[206:207], 0\n\t" "v_mov_b64 v[208:209], 0\n\t" "v_mov_b64 v[210:211], 0\n\t" "v_mov_b64 v[212:213], 0\n\t" "v_mov_b64 v[214:215], 0\n\t" \
    "v_mov_b64 v[216:217], 0\n\t" "v_mov_b64 v[218:219], 0\n\t" "v_mov_b64 v[220:221], 0\n\t" "v_mov_b64 v[222:223], 0\n\t" "v_mov_b32 v227, 160512\n\t" "30:\n\t" \
    "s_waitcnt vmcnt(3)\n\t" XT(84, 85) "v_smfmac_f32_16x16x64_bf16 v[232:235], v[240:243], a[0:7], %[ix]\n\t" "ds_write_b128 %[l1], v[240:243] offset:8192\n\t" \
    "v_smfmac_f32_16x16x64_bf16 v[236:239], v[240:243], a[8:15], %[ix]\n\t" "s_waitcnt vmcnt(2)\n\t" "v_smfmac_f32_16x16x64_bf16 v[232:235], v[244:247], a[16:23], %[ix]\n\t" \
    "ds_write_b128 %[l1], v[244:247] offset:9216\n\t" "s_waitcnt lgkmcnt(8)\n\t" "v_smfmac_f32_16x16x64_bf16 v[236:239], v[244:247], v[160:167], %[ix]\n\t" \
    "s_waitcnt lgkmcnt(0)\n\t" "s_barrier\n\t" XT(86, 87) "ds_read_b128 v[16:19], %[l1] offset:12288\n\t" "ds_read_b128 v[20:23], %[l1] offset:0\n\t" \
    "ds_read_b128 v[24:27], %[l1] offset:4096\n\t" "s_waitcnt vmcnt(1)\n\t" "v_smfmac_f32_16x16x64_bf16 v[232:235], v[248:251], a[24:31], %[ix]\n\t" \
    "ds_write_b128 %[l1], v[248:251] offset:10240\n\t" "v_smfmac_f32_16x16x64_bf16 v[236:239], v[248:251], a[32:39], %[ix]\n\t" \
    "ds_read_b128 v[28:31], %[l1] offset:13312\n\t" "ds_read_b128 v[240:243], %[l1] offset:1024\n\t" "ds_read_b128 v[244:247], %[l1] offset:5120\n\t" "s_waitcnt vmcnt(0)\n\t" \
    "v_smfmac_f32_16x16x64_bf16 v[232:235], v[252:255], a[40:47], %[ix]\n\t" "ds_write_b128 %[l1], v[252:255] offset:11264\n\t" \
    "v_smfmac_f32_16x16x64_bf16 v[236:239], v[252:255], v[168:175], %[ix]\n\t" "ds_read_b128 v[160:163], %[l0] offset:8192\n\t" \
    "ds_read_b128 v[164:167], %[l0] offset:9216\n\t" "ds_read_b128 v[168:171], %[l0] offset:10240\n\t" "ds_read_b128 v[172:175], %[l0] offset:11264\n\t" "s_nop 1\n\t" \
    "v_cmp_u_f32 vcc, v232, v233\n\t" "v_cmp_u_f32 s[62:63], v234, v235\n\t" "s_waitcnt lgkmcnt(11)\n\t" \
    "v_smfmac_f32_16x16x64_bf16 v[232:235], v[16:19], a[48:55], %[ix]\n\t" "v_smfmac_f32_16x16x64_bf16 v[236:239], v[16:19], a[56:63], %[ix]\n\t" \
    "s_or_b64 vcc, vcc, s[62:63]\n\t" "s_cbranch_scc1 31f\n\t" "32:\n\t" "s_waitcnt lgkmcnt(10)\n\t" "v_smfmac_f32_16x16x64_bf16 v[232:235], v[20:23], a[64:71], %[ix]\n\t" \
    "v_smfmac_f32_16x16x64_bf16 v[236:239], v[20:23], v[176:183], %[ix]\n\t" "s_waitcnt lgkmcnt(4)\n\t" "s_barrier\n\t" XT(88, 89) "ds_read_b32 v225, v227\n\t" \
    "v_smfmac_f32_16x16x64_bf16 v[232:235], v[24:27], a[72:79], %[ix]\n\t" "ds_read_b128 v[176:179], %[l0] offset:12288\n\t" "ds_read_b128 v[180:183], %[l0] offset:13312\n\t" \
    "v_smfmac_f32_16x16x64_bf16 v[236:239], v[24:27], a[80:87], %[ix]\n\t" "ds_read_b128 v[248:251], %[l1] offset:14336\n\t" "ds_read_b128 v[252:255], %[l1] offset:2048\n\t" \
    "v_smfmac_f32_16x16x64_bf16 v[232:235], v[28:31], a[88:95], %[ix]\n\t" "ds_read_b128 v[16:19], %[l1] offset:6144\n\t" "ds_read_b128 v[20:23], %[l1] offset:15360\n\t" \
    "v_smfmac_f32_16x16x64_bf16 v[236:239], v[28:31], v[184:191], %[ix]\n\t" "ds_read_b128 v[24:27], %[l1] offset:3072\n\t" "ds_read_b128 v[184:187], %[l0] offset:14336\n\t" \
    "v_smfmac_f32_16x16x64_bf16 v[232:235], v[240:243], a[96:103], %[ix]\n\t" "ds_read_b128 v[188:191], %[l0] offset:15360\n\t" "ds_read_b128 v[28:31], %[l1] offset:7168\n\t" \
    "v_smfmac_f32_16x16x64_bf16 v[236:239], v[240:243], a[104:111], %[ix]\n\t" "s_waitcnt lgkmcnt(10)\n\t" "v_cmp_eq_u32 vcc, %[rt], v225\n\t" \
    "v_smfmac_f32_16x16x64_bf16 v[232:235], v[244:247], a[112:119], %[ix]\n\t" "s_cbranch_vccnz 33f\n\t" \
    "v_smfmac_f32_16x16x64_bf16 v[236:239], v[244:247], v[160:167], %[ix]\n\t" "ds_read_b128 v[160:163], %[l0] offset:16384\n\t" \
    "ds_read_b128 v[164:167], %[l0] offset:17408\n\t" "s_waitcnt lgkmcnt(9)\n\t" "v_smfmac_f32_16x16x64_bf16 v[232:235], v[248:251], a[120:127], %[ix]\n\t" \
    "v_smfmac_f32_16x16x64_bf16 v[236:239], v[248:251], a[128:135], %[ix]\n\t" "s_waitcnt lgkmcnt(8)\n\t" \
    "v_smfmac_f32_16x16x64_bf16 v[232:235], v[252:255], a[136:143], %[ix]\n\t" "v_smfmac_f32_16x16x64_bf16 v[236:239], v[252:255], v[168:175], %[ix]\n\t" \
    "ds_read_b128 v[168:171], %[l0] offset:18432\n\t" "ds_read_b128 v[172:175], %[l0] offset:19456\n\t" "s_waitcnt lgkmcnt(9)\n\t" \
    "v_smfmac_f32_16x16x64_bf16 v[232:235], v[16:19], a[144:151], %[ix]\n\t" "v_smfmac_f32_16x16x64_bf16 v[236:239], v[16:19], a[152:159], %[ix]\n\t" \
    "s_waitcnt lgkmcnt(8)\n\t" "v_smfmac_f32_16x16x64_bf16 v[232:235], v[20:23], a[160:167], %[ix]\n\t" \
    "v_smfmac_f32_16x16x64_bf16 v[236:239], v[20:23], v[176:183], %[ix]\n\t" "ds_read_b128 v[176:179], %[l0] offset:20480\n\t" \
    "ds_read_b128 v[180:183], %[l0] offset:21504\n\t" "s_waitcnt lgkmcnt(9)\n\t" "v_smfmac_f32_16x16x64_bf16 v[232:235], v[24:27], a[168:175], %[ix]\n\t" \
    "v_smfmac_f32_16x16x64_bf16 v[236:239], v[24:27], a[176:183], %[ix]\n\t" "s_waitcnt lgkmcnt(6)\n\t" \
    "v_smfmac_f32_16x16x64_bf16 v[232:235], v[28:31], a[184:191], %[ix]\n\t" "v_smfmac_f32_16x16x64_bf16 v[236:239], v[28:31], v[184:191], %[ix]\n\t" \
    "ds_read_b128 v[184:187], %[l0] offset:22528\n\t" "ds_read_b128 v[188:191], %[l0] offset:23552\n\t" XT(90, 91) "s_nop 4\n\t" "v_add_f32 v232, v232, v233\n\t" \
    "v_add_f32 v234, v234, v235\n\t" "v_add_f32 v236, v236, v237\n\t" "v_add_f32 v238, v238, v239\n\t" "v_cvt_pk_bf16_f32 v236, v232, v236\n\t" \
    "v_cvt_pk_bf16_f32 v237, v234, v238\n\t" "ds_write2_b32 %[vw], v236, v237 offset1:68\n\t" "s_waitcnt lgkmcnt(0)\n\t" "s_barrier\n\t" XT(92, 93) \
    "ds_read_b128 v[240:243], %[vr] offset:0\n\t" "ds_read_b128 v[244:247], %[vr] offset:64\n\t" "ds_read_b128 v[248:251], %[vr] offset:128\n\t" \
    "ds_read_b128 v[252:255], %[vr] offset:192\n\t" "s_mov_b32 s61, %[ub]\n\t" "s_waitcnt lgkmcnt(3)\n\t" \
    "v_smfmac_f32_16x16x64_bf16 v[192:195], v[240:243], v[32:39], %[ix]\n\t" "v_smfmac_f32_16x16x64_bf16 v[196:199], v[240:243], v[48:55], %[ix]\n\t" \
    "s_waitcnt lgkmcnt(2)\n\t" "v_smfmac_f32_16x16x64_bf16 v[192:195], v[244:247], v[40:47], %[ix]\n\t" \
    "v_smfmac_f32_16x16x64_bf16 v[196:199], v[244:247], v[56:63], %[ix]\n\t" "s_waitcnt lgkmcnt(1)\n\t" \
    "v_smfmac_f32_16x16x64_bf16 v[192:195], v[248:251], a[192:199], %[ix]\n\t" "v_smfmac_f32_16x16x64_bf16 v[196:199], v[248:251], a[200:207], %[ix]\n\t" \
    "s_waitcnt lgkmcnt(0)\n\t" "v_smfmac_f32_16x16x64_bf16 v[192:195], v[252:255], v[160:167], %[ix]\n\t" "ds_read_b128 v[160:163], %[l0] offset:24576\n\t" \
    "ds_read_b128 v[164:167], %[l0] offset:25600\n\t" "v_smfmac_f32_16x16x64_bf16 v[196:199], v[252:255], v[168:175], %[ix]\n\t" \
    "ds_read_b128 v[168:171], %[l0] offset:26624\n\t" "ds_read_b128 v[172:175], %[l0] offset:27648\n\t" \
    "v_smfmac_f32_16x16x64_bf16 v[200:203], v[240:243], v[64:71], %[ix]\n\t" "s_nop 1\n\t" "v_smfmac_f32_16x16x64_bf16 v[204:207], v[240:243], v[80:87], %[ix]\n\t" \
    "v_cvt_pk_bf16_f32 v224, v192, v194\n\t" "v_cvt_pk_bf16_f32 v225, v193, v195\n\t" "v_smfmac_f32_16x16x64_bf16 v[200:203], v[244:247], v[72:79], %[ix]\n\t" \
    "v_cvt_pk_bf16_f32 v226, v196, v198\n\t" "v_cvt_pk_bf16_f32 v227, v197, v199\n\t" "v_smfmac_f32_16x16x64_bf16 v[204:207], v[244:247], v[88:95], %[ix]\n\t" "s_nop 0\n\t" \
    "v_permlane32_swap_b32 v224, v226\n\t" "v_smfmac_f32_16x16x64_bf16 v[200:203], v[248:251], a[208:215], %[ix]\n\t" "v_permlane32_swap_b32 v225, v227\n\t" "s_nop 0\n\t" \
    "v_smfmac_f32_16x16x64_bf16 v[204:207], v[248:251], a[216:223], %[ix]\n\t" "v_permlane16_swap_b32 v224, v225\n\t" "v_permlane16_swap_b32 v226, v227\n\t" \
    "v_smfmac_f32_16x16x64_bf16 v[200:203], v[252:255], v[176:183], %[ix]\n\t" "ds_read_b128 v[176:179], %[l0] offset:28672\n\t" \
    "buffer_store_dwordx4 v[224:227], %[uo], %[rs], s61 offen sc1\n\t" "s_xor_b32 s61, s61, 0x40000\n\t" XT(94, 95) \
    "v_smfmac_f32_16x16x64_bf16 v[204:207], v[252:255], v[184:191], %[ix]\n\t" "ds_read_b128 v[180:183], %[l0] offset:29696\n\t" \
    "ds_read_b128 v[184:187], %[l0] offset:30720\n\t" "v_smfmac_f32_16x16x64_bf16 v[208:211], v[240:243], v[96:103], %[ix]\n\t" \
    "ds_read_b128 v[188:191], %[l0] offset:31744\n\t" "s_nop 0\n\t" "v_smfmac_f32_16x16x64_bf16 v[212:215], v[240:243], v[112:119], %[ix]\n\t" \
    "v_cvt_pk_bf16_f32 v228, v200, v202\n\t" "v_cvt_pk_bf16_f32 v229, v201, v203\n\t" "v_smfmac_f32_16x16x64_bf16 v[208:211], v[244:247], v[104:111], %[ix]\n\t" \
    "v_cvt_pk_bf16_f32 v230, v204, v206\n\t" "v_cvt_pk_bf16_f32 v231, v205, v207\n\t" "v_smfmac_f32_16x16x64_bf16 v[212:215], v[244:247], v[120:127], %[ix]\n\t" "s_nop 0\n\t" \
    "v_permlane32_swap_b32 v228, v230\n\t" "v_smfmac_f32_16x16x64_bf16 v[208:211], v[248:251], a[224:231], %[ix]\n\t" "v_permlane32_swap_b32 v229, v231\n\t" "s_nop 0\n\t" \
    "v_smfmac_f32_16x16x64_bf16 v[212:215], v[248:251], a[232:239], %[ix]\n\t" "v_permlane16_swap_b32 v228, v229\n\t" "v_permlane16_swap_b32 v230, v231\n\t" \
    "s_waitcnt lgkmcnt(6)\n\t" "v_smfmac_f32_16x16x64_bf16 v[208:211], v[252:255], v[160:167], %[ix]\n\t" "buffer_store_dwordx4 v[228:231], %[uo], %[rs], s61 offen sc1\n\t" \
    "s_xor_b32 s61, s61, 0xc0000\n\t" "s_waitcnt lgkmcnt(4)\n\t" "v_smfmac_f32_16x16x64_bf16 v[212:215], v[252:255], v[168:175], %[ix]\n\t" "s_nop 1\n\t" \
    "v_smfmac_f32_16x16x64_bf16 v[216:219], v[240:243], v[128:135], %[ix]\n\t" "s_nop 1\n\t" "v_smfmac_f32_16x16x64_bf16 v[220:223], v[240:243], v[144:151], %[ix]\n\t" \
    "v_cvt_pk_bf16_f32 v224, v208, v210\n\t" "v_cvt_pk_bf16_f32 v225, v209, v211\n\t" "v_smfmac_f32_16x16x64_bf16 v[216:219], v[244:247], v[136:143], %[ix]\n\t" \
    "v_cvt_pk_bf16_f32 v226, v212, v214\n\t" "v_cvt_pk_bf16_f32 v227, v213, v215\n\t" "v_smfmac_f32_16x16x64_bf16 v[220:223], v[244:247], v[152:159], %[ix]\n\t" "s_nop 0\n\t" \
    "v_permlane32_swap_b32 v224, v226\n\t" "v_smfmac_f32_16x16x64_bf16 v[216:219], v[248:251], a[240:247], %[ix]\n\t" "v_permlane32_swap_b32 v225, v227\n\t" "s_nop 0\n\t" \
    "v_smfmac_f32_16x16x64_bf16 v[220:223], v[248:251], a[248:255], %[ix]\n\t" "v_permlane16_swap_b32 v224, v225\n\t" "v_permlane16_swap_b32 v226, v227\n\t" \
    "s_waitcnt lgkmcnt(2)\n\t" "v_smfmac_f32_16x16x64_bf16 v[216:219], v[252:255], v[176:183], %[ix]\n\t" "buffer_store_dwordx4 v[224:227], %[uo], %[rs], s61 offen sc1\n\t" \
    "s_xor_b32 s61, s61, 0x40000\n\t" "s_waitcnt lgkmcnt(0)\n\t" "v_smfmac_f32_16x16x64_bf16 v[220:223], v[252:255], v[184:191], %[ix]\n\t" "s_nop 5\n\t" \
    "v_cvt_pk_bf16_f32 v228, v216, v218\n\t" "v_cvt_pk_bf16_f32 v229, v217, v219\n\t" "v_cvt_pk_bf16_f32 v230, v220, v222\n\t" "v_cvt_pk_bf16_f32 v231, v221, v223\n\t" \
    "s_nop 0\n\t" "v_permlane32_swap_b32 v228, v230\n\t" "v_permlane32_swap_b32 v229, v231\n\t" "s_nop 1\n\t" "v_permlane16_swap_b32 v228, v229\n\t" \
    "v_permlane16_swap_b32 v230, v231\n\t" "buffer_store_dwordx4 v[228:231], %[uo], %[rs], s61 offen sc1\n\t" "s_nop 1\n\t" XT(96, 97) "s_branch 99f\n\t" "31:\n\t" \
    "ds_read_b128 v[228:231], %[l1] offset:8192\n\t" "s_waitcnt lgkmcnt(0)\n\t" "v_max3_u32 v225, v228, v229, v230\n\t" "v_max_u32 v225, v225, v231\n\t" \
    "ds_read_b128 v[228:231], %[l1] offset:9216\n\t" "s_waitcnt lgkmcnt(0)\n\t" "v_max3_u32 v225, v225, v228, v229\n\t" "v_max3_u32 v225, v225, v230, v231\n\t" \
    "ds_read_b128 v[228:231], %[l1] offset:10240\n\t" "s_waitcnt lgkmcnt(0)\n\t" "v_max3_u32 v225, v225, v228, v229\n\t" "v_max3_u32 v225, v225, v230, v231\n\t" \
    "ds_read_b128 v[228:231], %[l1] offset:11264\n\t" "s_waitcnt lgkmcnt(0)\n\t" "v_max3_u32 v225, v225, v228, v229\n\t" "v_max3_u32 v225, v225, v230, v231\n\t" \
    "v_cmp_eq_u32 vcc, -1, v225\n\t" "s_cbranch_vccz 32b\n\t" "s_cmp_eq_u32 %[rt], 1\n\t" "s_cbranch_scc1 32b\n\t" "v_mov_b32 v225, %[rt]\n\t" "ds_write_b32 v227, v225\n\t" \
    "s_waitcnt lgkmcnt(0)\n\t" "s_branch 32b\n\t" "33:\n\t" "s_sub_u32 %[rt], %[rt], 1\n\t" "s_waitcnt lgkmcnt(0)\n\t" XCNT(100) "34:\n\t" \
    "buffer_load_dwordx4 v[240:243], %[mo], %[rs], %[sb] offen sc1\n\t" "buffer_load_dwordx4 v[244:247], v224, %[rs], %[sb] offen sc1\n\t" \
    "buffer_load_dwordx4 v[248:251], %[mo], %[rs], s60 offen sc1\n\t" "buffer_load_dwordx4 v[252:255], v224, %[rs], s60 offen sc1\n\t" "s_waitcnt vmcnt(0)\n\t" \
    "v_max_u32 v225, v240, v241\n\t" "v_max3_u32 v225, v225, v242, v243\n\t" "v_max3_u32 v225, v225, v244, v245\n\t" "v_max3_u32 v225, v225, v246, v247\n\t" \
    "v_max3_u32 v225, v225, v248, v249\n\t" "v_max3_u32 v225, v225, v250, v251\n\t" "v_max3_u32 v225, v225, v252, v253\n\t" "v_max3_u32 v225, v225, v254, v255\n\t" \
    "v_cmp_eq_u32 vcc, -1, v225\n\t" "s_cbranch_vccz 35f\n\t" "s_cmp_eq_u32 %[bd], 0\n\t" "s_cbranch_scc1 35f\n\t" "s_sub_u32 %[bd], %[bd], 1\n\t" XCNT(101) \
    "s_branch 34b\n\t" "35:\n\t" "ds_read_b128 v[160:163], %[l0] offset:0\n\t" "ds_read_b128 v[164:167], %[l0] offset:1024\n\t" \
    "ds_read_b128 v[168:171], %[l0] offset:2048\n\t" "ds_read_b128 v[172:175], %[l0] offset:3072\n\t" "ds_read_b128 v[176:179], %[l0] offset:4096\n\t" \
    "ds_read_b128 v[180:183], %[l0] offset:5120\n\t" "ds_read_b128 v[184:187], %[l0] offset:6144\n\t" "ds_read_b128 v[188:191], %[l0] offset:7168\n\t" \
    "v_mov_b64 v[232:233], 0\n\t" "v_mov_b64 v[234:235], 0\n\t" "v_mov_b64 v[236:237], 0\n\t" "v_mov_b64 v[238:239], 0\n\t" "s_barrier\n\t" "s_branch 30b\n\t" "93:\n\t" \
    XT(80, 81) "s_barrier\n\t" XT(82, 83) "buffer_load_dwordx4 v[240:243], %[mo], %[rs], %[sb] offen sc1\n\t" "v_mov_b64 v[232:233], 0\n\t" "v_mov_b64 v[234:235], 0\n\t" \
    "v_mov_b64 v[236:237], 0\n\t" "v_mov_b64 v[238:239], 0\n\t" "v_mov_b64 v[192:193], 0\n\t" "v_mov_b64 v[194:195], 0\n\t" "v_mov_b64 v[196:197], 0\n\t" \
    "v_mov_b64 v[198:199], 0\n\t" "buffer_load_dwordx4 v[244:247], v224, %[rs], %[sb] offen sc1\n\t" "ds_read_b128 v[160:163], %[l0] offset:0\n\t" \
    "ds_read_b128 v[164:167], %[l0] offset:1024\n\t" "ds_read_b128 v[168:171], %[l0] offset:2048\n\t" "ds_read_b128 v[172:175], %[l0] offset:3072\n\t" "s_nop 3\n\t" \
    "buffer_load_dwordx4 v[248:251], %[mo], %[rs], s60 offen sc1\n\t" "ds_read_b128 v[176:179], %[l0] offset:4096\n\t" "ds_read_b128 v[180:183], %[l0] offset:5120\n\t" \
    "ds_read_b128 v[184:187], %[l0] offset:6144\n\t" "ds_read_b128 v[188:191], %[l0] offset:7168\n\t" "s_nop 3\n\t" \
    "buffer_load_dwordx4 v[252:255], v224, %[rs], s60 offen sc1\n\t" "v_mov_b64 v[200:201], 0\n\t" "v_mov_b64 v[202:203], 0\n\t" "v_mov_b64 v[204:205], 0\n\t" \
    "v_mov_b64 v[206:207], 0\n\t" "v_mov_b64 v[208:209], 0\n\t" "v_mov_b64 v[210:211], 0\n\t" "v_mov_b64 v[212:213], 0\n\t" "v_mov_b64 v[214:215], 0\n\t" \
    "v_mov_b64 v[216:217], 0\n\t" "v_mov_b64 v[218:219], 0\n\t" "v_mov_b64 v[220:221], 0\n\t" "v_mov_b64 v[222:223], 0\n\t" "v_mov_b32 v227, 160512\n\t" "40:\n\t" \
    "s_waitcnt vmcnt(3)\n\t" XT(84, 85) "v_smfmac_f32_16x16x64_bf16 v[232:235], v[240:243], a[0:7], %[ix]\n\t" "ds_write_b128 %[l1], v[240:243] offset:12288\n\t" \
    "v_smfmac_f32_16x16x64_bf16 v[236:239], v[240:243], a[8:15], %[ix]\n\t" "s_waitcnt vmcnt(2)\n\t" "v_smfmac_f32_16x16x64_bf16 v[232:235], v[244:247], a[16:23], %[ix]\n\t" \
    "ds_write_b128 %[l1], v[244:247] offset:13312\n\t" "s_waitcnt lgkmcnt(8)\n\t" "v_smfmac_f32_16x16x64_bf16 v[236:239], v[244:247], v[160:167], %[ix]\n\t" \
    "s_waitcnt lgkmcnt(0)\n\t" "s_barrier\n\t" XT(86, 87) "ds_read_b128 v[16:19], %[l1] offset:0\n\t" "ds_read_b128 v[20:23], %[l1] offset:4096\n\t" \
    "ds_read_b128 v[24:27], %[l1] offset:8192\n\t" "s_waitcnt vmcnt(1)\n\t" "v_smfmac_f32_16x16x64_bf16 v[232:235], v[248:251], a[24:31], %[ix]\n\t" \
    "ds_write_b128 %[l1], v[248:251] offset:14336\n\t" "v_smfmac_f32_16x16x64_bf16 v[236:239], v[248:251], a[32:39], %[ix]\n\t" "ds_read_b128 v[28:31], %[l1] offset:1024\n\t" \
    "ds_read_b128 v[240:243], %[l1] offset:5120\n\t" "ds_read_b128 v[244:247], %[l1] offset:9216\n\t" "s_waitcnt vmcnt(0)\n\t" \
    "v_smfmac_f32_16x16x64_bf16 v[232:235], v[252:255], a[40:47], %[ix]\n\t" "ds_write_b128 %[l1], v[252:255] offset:15360\n\t" \
    "v_smfmac_f32_16x16x64_bf16 v[236:239], v[252:255], v[168:175], %[ix]\n\t" "ds_read_b128 v[160:163], %[l0] offset:8192\n\t" \
    "ds_read_b128 v[164:167], %[l0] offset:9216\n\t" "ds_read_b128 v[168:171], %[l0] offset:10240\n\t" "ds_read_b128 v[172:175], %[l0] offset:11264\n\t" "s_nop 1\n\t" \
    "v_cmp_u_f32 vcc, v232, v233\n\t" "v_cmp_u_f32 s[62:63], v234, v235\n\t" "s_waitcnt lgkmcnt(11)\n\t" \
    "v_smfmac_f32_16x16x64_bf16 v[232:235], v[16:19], a[48:55], %[ix]\n\t" "v_smfmac_f32_16x16x64_bf16 v[236:239], v[16:19], a[56:63], %[ix]\n\t" \
    "s_or_b64 vcc, vcc, s[62:63]\n\t" "s_cbranch_scc1 41f\n\t" "42:\n\t" "s_waitcnt lgkmcnt(10)\n\t" "v_smfmac_f32_16x16x64_bf16 v[232:235], v[20:23], a[64:71], %[ix]\n\t" \
    "v_smfmac_f32_16x16x64_bf16 v[236:239], v[20:23], v[176:183], %[ix]\n\t" "s_waitcnt lgkmcnt(4)\n\t" "s_barrier\n\t" XT(88, 89) "ds_read_b32 v225, v227\n\t" \
    "v_smfmac_f32_16x16x64_bf16 v[232:235], v[24:27], a[72:79], %[ix]\n\t" "ds_read_b128 v[176:179], %[l0] offset:12288\n\t" "ds_read_b128 v[180:183], %[l0] offset:13312\n\t" \
    "v_smfmac_f32_16x16x64_bf16 v[236:239], v[24:27], a[80:87], %[ix]\n\t" "ds_read_b128 v[248:251], %[l1] offset:2048\n\t" "ds_read_b128 v[252:255], %[l1] offset:6144\n\t" \
    "v_smfmac_f32_16x16x64_bf16 v[232:235], v[28:31], a[88:95], %[ix]\n\t" "ds_read_b128 v[16:19], %[l1] offset:10240\n\t" "ds_read_b128 v[20:23], %[l1] offset:3072\n\t" \
    "v_smfmac_f32_16x16x64_bf16 v[236:239], v[28:31], v[184:191], %[ix]\n\t" "ds_read_b128 v[24:27], %[l1] offset:7168\n\t" "ds_read_b128 v[184:187], %[l0] offset:14336\n\t" \
    "v_smfmac_f32_16x16x64_bf16 v[232:235], v[240:243], a[96:103], %[ix]\n\t" "ds_read_b128 v[188:191], %[l0] offset:15360\n\t" \
    "ds_read_b128 v[28:31], %[l1] offset:11264\n\t" "v_smfmac_f32_16x16x64_bf16 v[236:239], v[240:243], a[104:111], %[ix]\n\t" "s_waitcnt lgkmcnt(10)\n\t" \
    "v_cmp_eq_u32 vcc, %[rt], v225\n\t" "v_smfmac_f32_16x16x64_bf16 v[232:235], v[244:247], a[112:119], %[ix]\n\t" "s_cbranch_vccnz 43f\n\t" \
    "v_smfmac_f32_16x16x64_bf16 v[236:239], v[244:247], v[160:167], %[ix]\n\t" "ds_read_b128 v[160:163], %[l0] offset:16384\n\t" \
    "ds_read_b128 v[164:167], %[l0] offset:17408\n\t" "s_waitcnt lgkmcnt(9)\n\t" "v_smfmac_f32_16x16x64_bf16 v[232:235], v[248:251], a[120:127], %[ix]\n\t" \
    "v_smfmac_f32_16x16x64_bf16 v[236:239], v[248:251], a[128:135], %[ix]\n\t" "s_waitcnt lgkmcnt(8)\n\t" \
    "v_smfmac_f32_16x16x64_bf16 v[232:235], v[252:255], a[136:143], %[ix]\n\t" "v_smfmac_f32_16x16x64_bf16 v[236:239], v[252:255], v[168:175], %[ix]\n\t" \
    "ds_read_b128 v[168:171], %[l0] offset:18432\n\t" "ds_read_b128 v[172:175], %[l0] offset:19456\n\t" "s_waitcnt lgkmcnt(9)\n\t" \
    "v_smfmac_f32_16x16x64_bf16 v[232:235], v[16:19], a[144:151], %[ix]\n\t" "v_smfmac_f32_16x16x64_bf16 v[236:239], v[16:19], a[152:159], %[ix]\n\t" \
    "s_waitcnt lgkmcnt(8)\n\t" "v_smfmac_f32_16x16x64_bf16 v[232:235], v[20:23], a[160:167], %[ix]\n\t" \
    "v_smfmac_f32_16x16x64_bf16 v[236:239], v[20:23], v[176:183], %[ix]\n\t" "ds_read_b128 v[176:179], %[l0] offset:20480\n\t" \
    "ds_read_b128 v[180:183], %[l0] offset:21504\n\t" "s_waitcnt lgkmcnt(9)\n\t" "v_smfmac_f32_16x16x64_bf16 v[232:235], v[24:27], a[168:175], %[ix]\n\t" \
    "v_smfmac_f32_16x16x64_bf16 v[236:239], v[24:27], a[176:183], %[ix]\n\t" "s_waitcnt lgkmcnt(6)\n\t" \
    "v_smfmac_f32_16x16x64_bf16 v[232:235], v[28:31], a[184:191], %[ix]\n\t" "v_smfmac_f32_16x16x64_bf16 v[236:239], v[28:31], v[184:191], %[ix]\n\t" \
    "ds_read_b128 v[184:187], %[l0] offset:22528\n\t" "ds_read_b128 v[188:191], %[l0] offset:23552\n\t" XT(90, 91) "s_nop 4\n\t" "v_add_f32 v232, v232, v233\n\t" \
    "v_add_f32 v234, v234, v235\n\t" "v_add_f32 v236, v236, v237\n\t" "v_add_f32 v238, v238, v239\n\t" "v_cvt_pk_bf16_f32 v236, v232, v236\n\t" \
    "v_cvt_pk_bf16_f32 v237, v234, v238\n\t" "ds_write2_b32 %[vw], v236, v237 offset1:68\n\t" "s_waitcnt lgkmcnt(0)\n\t" "s_barrier\n\t" XT(92, 93) \
    "ds_read_b128 v[240:243], %[vr] offset:0\n\t" "ds_read_b128 v[244:247], %[vr] offset:64\n\t" "ds_read_b128 v[248:251], %[vr] offset:128\n\t" \
    "ds_read_b128 v[252:255], %[vr] offset:192\n\t" "s_mov_b32 s61, %[ub]\n\t" "s_waitcnt lgkmcnt(3)\n\t" \
    "v_smfmac_f32_16x16x64_bf16 v[192:195], v[240:243], v[32:39], %[ix]\n\t" "v_smfmac_f32_16x16x64_bf16 v[196:199], v[240:243], v[48:55], %[ix]\n\t" \
    "s_waitcnt lgkmcnt(2)\n\t" "v_smfmac_f32_16x16x64_bf16 v[192:195], v[244:247], v[40:47], %[ix]\n\t" \
    "v_smfmac_f32_16x16x64_bf16 v[196:199], v[244:247], v[56:63], %[ix]\n\t" "s_waitcnt lgkmcnt(1)\n\t" \
    "v_smfmac_f32_16x16x64_bf16 v[192:195], v[248:251], a[192:199], %[ix]\n\t" "v_smfmac_f32_16x16x64_bf16 v[196:199], v[248:251], a[200:207], %[ix]\n\t" \
    "s_waitcnt lgkmcnt(0)\n\t" "v_smfmac_f32_16x16x64_bf16 v[192:195], v[252:255], v[160:167], %[ix]\n\t" "ds_read_b128 v[160:163], %[l0] offset:24576\n\t" \
    "ds_read_b128 v[164:167], %[l0] offset:25600\n\t" "v_smfmac_f32_16x16x64_bf16 v[196:199], v[252:255], v[168:175], %[ix]\n\t" \
    "ds_read_b128 v[168:171], %[l0] offset:26624\n\t" "ds_read_b128 v[172:175], %[l0] offset:27648\n\t" \
    "v_smfmac_f32_16x16x64_bf16 v[200:203], v[240:243], v[64:71], %[ix]\n\t" "s_nop 1\n\t" "v_smfmac_f32_16x16x64_bf16 v[204:207], v[240:243], v[80:87], %[ix]\n\t" \
    "v_cvt_pk_bf16_f32 v224, v192, v194\n\t" "v_cvt_pk_bf16_f32 v225, v193, v195\n\t" "v_smfmac_f32_16x16x64_bf16 v[200:203], v[244:247], v[72:79], %[ix]\n\t" \
    "v_cvt_pk_bf16_f32 v226, v196, v198\n\t" "v_cvt_pk_bf16_f32 v227, v197, v199\n\t" "v_smfmac_f32_16x16x64_bf16 v[204:207], v[244:247], v[88:95], %[ix]\n\t" "s_nop 0\n\t" \
    "v_permlane32_swap_b32 v224, v226\n\t" "v_smfmac_f32_16x16x64_bf16 v[200:203], v[248:251], a[208:215], %[ix]\n\t" "v_permlane32_swap_b32 v225, v227\n\t" "s_nop 0\n\t" \
    "v_smfmac_f32_16x16x64_bf16 v[204:207], v[248:251], a[216:223], %[ix]\n\t" "v_permlane16_swap_b32 v224, v225\n\t" "v_permlane16_swap_b32 v226, v227\n\t" \
    "v_smfmac_f32_16x16x64_bf16 v[200:203], v[252:255], v[176:183], %[ix]\n\t" "ds_read_b128 v[176:179], %[l0] offset:28672\n\t" \
    "buffer_store_dwordx4 v[224:227], %[uo], %[rs], s61 offen sc1\n\t" "s_xor_b32 s61, s61, 0x40000\n\t" XT(94, 95) \
    "v_smfmac_f32_16x16x64_bf16 v[204:207], v[252:255], v[184:191], %[ix]\n\t" "ds_read_b128 v[180:183], %[l0] offset:29696\n\t" \
    "ds_read_b128 v[184:187], %[l0] offset:30720\n\t" "v_smfmac_f32_16x16x64_bf16 v[208:211], v[240:243], v[96:103], %[ix]\n\t" \
    "ds_read_b128 v[188:191], %[l0] offset:31744\n\t" "s_nop 0\n\t" "v_smfmac_f32_16x16x64_bf16 v[212:215], v[240:243], v[112:119], %[ix]\n\t" \
    "v_cvt_pk_bf16_f32 v228, v200, v202\n\t" "v_cvt_pk_bf16_f32 v229, v201, v203\n\t" "v_smfmac_f32_16x16x64_bf16 v[208:211], v[244:247], v[104:111], %[ix]\n\t" \
    "v_cvt_pk_bf16_f32 v230, v204, v206\n\t" "v_cvt_pk_bf16_f32 v231, v205, v207\n\t" "v_smfmac_f32_16x16x64_bf16 v[212:215], v[244:247], v[120:127], %[ix]\n\t" "s_nop 0\n\t" \
    "v_permlane32_swap_b32 v228, v230\n\t" "v_smfmac_f32_16x16x64_bf16 v[208:211], v[248:251], a[224:231], %[ix]\n\t" "v_permlane32_swap_b32 v229, v231\n\t" "s_nop 0\n\t" \
    "v_smfmac_f32_16x16x64_bf16 v[212:215], v[248:251], a[232:239], %[ix]\n\t" "v_permlane16_swap_b32 v228, v229\n\t" "v_permlane16_swap_b32 v230, v231\n\t" \
    "s_waitcnt lgkmcnt(6)\n\t" "v_smfmac_f32_16x16x64_bf16 v[208:211], v[252:255], v[160:167], %[ix]\n\t" "buffer_store_dwordx4 v[228:231], %[uo], %[rs], s61 offen sc1\n\t" \
    "s_xor_b32 s61, s61, 0xc0000\n\t" "s_waitcnt lgkmcnt(4)\n\t" "v_smfmac_f32_16x16x64_bf16 v[212:215], v[252:255], v[168:175], %[ix]\n\t" "s_nop 1\n\t" \
    "v_smfmac_f32_16x16x64_bf16 v[216:219], v[240:243], v[128:135], %[ix]\n\t" "s_nop 1\n\t" "v_smfmac_f32_16x16x64_bf16 v[220:223], v[240:243], v[144:151], %[ix]\n\t" \
    "v_cvt_pk_bf16_f32 v224, v208, v210\n\t" "v_cvt_pk_bf16_f32 v225, v209, v211\n\t" "v_smfmac_f32_16x16x64_bf16 v[216:219], v[244:247], v[136:143], %[ix]\n\t" \
    "v_cvt_pk_bf16_f32 v226, v212, v214\n\t" "v_cvt_pk_bf16_f32 v227, v213, v215\n\t" "v_smfmac_f32_16x16x64_bf16 v[220:223], v[244:247], v[152:159], %[ix]\n\t" "s_nop 0\n\t" \
    "v_permlane32_swap_b32 v224, v226\n\t" "v_smfmac_f32_16x16x64_bf16 v[216:219], v[248:251], a[240:247], %[ix]\n\t" "v_permlane32_swap_b32 v225, v227\n\t" "s_nop 0\n\t" \
    "v_smfmac_f32_16x16x64_bf16 v[220:223], v[248:251], a[248:255], %[ix]\n\t" "v_permlane16_swap_b32 v224, v225\n\t" "v_permlane16_swap_b32 v226, v227\n\t" \
    "s_waitcnt lgkmcnt(2)\n\t" "v_smfmac_f32_16x16x64_bf16 v[216:219], v[252:255], v[176:183], %[ix]\n\t" "buffer_store_dwordx4 v[224:227], %[uo], %[rs], s61 offen sc1\n\t" \
    "s_xor_b32 s61, s61, 0x40000\n\t" "s_waitcnt lgkmcnt(0)\n\t" "v_smfmac_f32_16x16x64_bf16 v[220:223], v[252:255], v[184:191], %[ix]\n\t" "s_nop 5\n\t" \
    "v_cvt_pk_bf16_f32 v228, v216, v218\n\t" "v_cvt_pk_bf16_f32 v229, v217, v219\n\t" "v_cvt_pk_bf16_f32 v230, v220, v222\n\t" "v_cvt_pk_bf16_f32 v231, v221, v223\n\t" \
    "s_nop 0\n\t" "v_permlane32_swap_b32 v228, v230\n\t" "v_permlane32_swap_b32 v229, v231\n\t" "s_nop 1\n\t" "v_permlane16_swap_b32 v228, v229\n\t" \
    "v_permlane16_swap_b32 v230, v231\n\t" "buffer_store_dwordx4 v[228:231], %[uo], %[rs], s61 offen sc1\n\t" "s_nop 1\n\t" XT(96, 97) "s_branch 99f\n\t" "41:\n\t" \
    "ds_read_b128 v[228:231], %[l1] offset:12288\n\t" "s_waitcnt lgkmcnt(0)\n\t" "v_max3_u32 v225, v228, v229, v230\n\t" "v_max_u32 v225, v225, v231\n\t" \
    "ds_read_b128 v[228:231], %[l1] offset:13312\n\t" "s_waitcnt lgkmcnt(0)\n\t" "v_max3_u32 v225, v225, v228, v229\n\t" "v_max3_u32 v225, v225, v230, v231\n\t" \
    "ds_read_b128 v[228:231], %[l1] offset:14336\n\t" "s_waitcnt lgkmcnt(0)\n\t" "v_max3_u32 v225, v225, v228, v229\n\t" "v_max3_u32 v225, v225, v230, v231\n\t" \
    "ds_read_b128 v[228:231], %[l1] offset:15360\n\t" "s_waitcnt lgkmcnt(0)\n\t" "v_max3_u32 v225, v225, v228, v229\n\t" "v_max3_u32 v225, v225, v230, v231\n\t" \
    "v_cmp_eq_u32 vcc, -1, v225\n\t" "s_cbranch_vccz 42b\n\t" "s_cmp_eq_u32 %[rt], 1\n\t" "s_cbranch_scc1 42b\n\t" "v_mov_b32 v225, %[rt]\n\t" "ds_write_b32 v227, v225\n\t" \
    "s_waitcnt lgkmcnt(0)\n\t" "s_branch 42b\n\t" "43:\n\t" "s_sub_u32 %[rt], %[rt], 1\n\t" "s_waitcnt lgkmcnt(0)\n\t" XCNT(100) "44:\n\t" \
    "buffer_load_dwordx4 v[240:243], %[mo], %[rs], %[sb] offen sc1\n\t" "buffer_load_dwordx4 v[244:247], v224, %[rs], %[sb] offen sc1\n\t" \
    "buffer_load_dwordx4 v[248:251], %[mo], %[rs], s60 offen sc1\n\t" "buffer_load_dwordx4 v[252:255], v224, %[rs], s60 offen sc1\n\t" "s_waitcnt vmcnt(0)\n\t" \
    "v_max_u32 v225, v240, v241\n\t" "v_max3_u32 v225, v225, v242, v243\n\t" "v_max3_u32 v225, v225, v244, v245\n\t" "v_max3_u32 v225, v225, v246, v247\n\t" \
    "v_max3_u32 v225, v225, v248, v249\n\t" "v_max3_u32 v225, v225, v250, v251\n\t" "v_max3_u32 v225, v225, v252, v253\n\t" "v_max3_u32 v225, v225, v254, v255\n\t" \
    "v_cmp_eq_u32 vcc, -1, v225\n\t" "s_cbranch_vccz 45f\n\t" "s_cmp_eq_u32 %[bd], 0\n\t" "s_cbranch_scc1 45f\n\t" "s_sub_u32 %[bd], %[bd], 1\n\t" XCNT(101) \
    "s_branch 44b\n\t" "45:\n\t" "ds_read_b128 v[160:163], %[l0] offset:0\n\t" "ds_read_b128 v[164:167], %[l0] offset:1024\n\t" \
    "ds_read_b128 v[168:171], %[l0] offset:2048\n\t" "ds_read_b128 v[172:175], %[l0] offset:3072\n\t" "ds_read_b128 v[176:179], %[l0] offset:4096\n\t" \
    "ds_read_b128 v[180:183], %[l0] offset:5120\n\t" "ds_read_b128 v[184:187], %[l0] offset:6144\n\t" "ds_read_b128 v[188:191], %[l0] offset:7168\n\t" \
    "v_mov_b64 v[232:233], 0\n\t" "v_mov_b64 v[234:235], 0\n\t" "v_mov_b64 v[236:237], 0\n\t" "v_mov_b64 v[238:239], 0\n\t" "s_barrier\n\t" "s_branch 40b\n\t" "99:\n\t"
#define Y_STEP \
    "v_xor_b32 v128, 0, %[u0]\n\t" "v_xor_b32 v132, 0, %[pm]\n\t" "v_xor_b32 v129, 2048, %[u0]\n\t" "v_xor_b32 v133, 1024, %[pm]\n\t" "v_xor_b32 v130, 4096, %[u0]\n\t" \
    "v_xor_b32 v134, 2048, %[pm]\n\t" "v_xor_b32 v131, 6144, %[u0]\n\t" "v_xor_b32 v135, 3072, %[pm]\n\t" "s_getpc_b64 s[62:63]\n\t" "11:\n\t" "s_memtime s[64:65]\n\t" \
    "s_waitcnt lgkmcnt(0)\n\t" "s_sub_u32 s61, %[tu], s64\n\t" "s_cmp_gt_i32 s61, 124\n\t" "s_cbranch_scc1 11b\n\t" "s_max_i32 s61, s61, 0\n\t" "s_and_b32 s61, s61, 124\n\t" \
    "s_sub_u32 s61, 12f-11b, s61\n\t" "s_add_u32 s62, s62, s61\n\t" "s_addc_u32 s63, s63, 0\n\t" "s_setpc_b64 s[62:63]\n\t" ".rept 31\n\t" "s_nop 0\n\t" ".endr\n\t" "12:\n\t" \
    XT(90, 91) "buffer_load_dwordx4 v[144:147], v128, %[rs], %[ub] offen offset:0 sc1\n\t" "buffer_load_dwordx4 v[148:151], v128, %[rs], %[ub] offen offset:1024 sc1\n\t" \
    "buffer_load_dwordx4 v[152:155], v129, %[rs], %[ub] offen offset:0 sc1\n\t" "buffer_load_dwordx4 v[156:159], v129, %[rs], %[ub] offen offset:1024 sc1\n\t" \
    "s_cmp_lg_u32 %[late], 0\n\t" "s_cbranch_scc1 14f\n\t" "s_getpc_b64 s[62:63]\n\t" "13:\n\t" "s_memtime s[64:65]\n\t" "s_waitcnt lgkmcnt(0)\n\t" \
    "s_sub_u32 s61, %[tg], s64\n\t" "s_cmp_gt_i32 s61, 124\n\t" "s_cbranch_scc1 13b\n\t" "s_max_i32 s61, s61, 0\n\t" "s_and_b32 s61, s61, 124\n\t" \
    "s_sub_u32 s61, 14f-13b, s61\n\t" "s_add_u32 s62, s62, s61\n\t" "s_addc_u32 s63, s63, 0\n\t" "s_setpc_b64 s[62:63]\n\t" ".rept 31\n\t" "s_nop 0\n\t" ".endr\n\t" "14:\n\t" \
    XT(92, 93) "20:\n\t" "buffer_load_dwordx4 v[160:163], v130, %[rs], %[ub] offen offset:0 sc1\n\t" \
    "buffer_load_dwordx4 v[164:167], v130, %[rs], %[ub] offen offset:1024 sc1\n\t" "buffer_load_dwordx4 v[192:195], %[x0], %[rs], %[pb] offen offset:0 sc1\n\t" \
    "buffer_load_dwordx4 v[196:199], %[x0], %[rs], %[pb] offen offset:1024 sc1\n\t" "buffer_load_dwordx4 v[200:203], %[x0], %[rs], %[pb] offen offset:2048 sc1\n\t" \
    "buffer_load_dwordx4 v[204:207], %[x0], %[rs], %[pb] offen offset:3072 sc1\n\t" "buffer_load_dwordx2 v[176:177], v132, %[rs], %[pb] offen offset:0 sc1\n\t" \
    "buffer_load_dwordx2 v[178:179], v132, %[rs], %[pb] offen offset:128 sc1\n\t" "v_mov_b64 v[120:121], 0\n\t" "v_mov_b64 v[122:123], 0\n\t" \
    "buffer_load_dwordx4 v[208:211], %[x1], %[rs], %[pb] offen offset:0 sc1\n\t" "buffer_load_dwordx4 v[212:215], %[x1], %[rs], %[pb] offen offset:1024 sc1\n\t" \
    "buffer_load_dwordx4 v[216:219], %[x1], %[rs], %[pb] offen offset:2048 sc1\n\t" "buffer_load_dwordx4 v[220:223], %[x1], %[rs], %[pb] offen offset:3072 sc1\n\t" \
    "buffer_load_dwordx2 v[180:181], v133, %[rs], %[pb] offen offset:0 sc1\n\t" "buffer_load_dwordx2 v[182:183], v133, %[rs], %[pb] offen offset:128 sc1\n\t" \
    "buffer_load_dwordx4 v[224:227], %[x2], %[rs], %[pb] offen offset:0 sc1\n\t" "buffer_load_dwordx4 v[228:231], %[x2], %[rs], %[pb] offen offset:1024 sc1\n\t" \
    "buffer_load_dwordx4 v[232:235], %[x2], %[rs], %[pb] offen offset:2048 sc1\n\t" "buffer_load_dwordx4 v[236:239], %[x2], %[rs], %[pb] offen offset:3072 sc1\n\t" \
    "buffer_load_dwordx2 v[184:185], v134, %[rs], %[pb] offen offset:0 sc1\n\t" "buffer_load_dwordx2 v[186:187], v134, %[rs], %[pb] offen offset:128 sc1\n\t" \
    "buffer_load_dwordx4 v[240:243], %[x3], %[rs], %[pb] offen offset:0 sc1\n\t" "buffer_load_dwordx4 v[244:247], %[x3], %[rs], %[pb] offen offset:1024 sc1\n\t" \
    "buffer_load_dwordx4 v[248:251], %[x3], %[rs], %[pb] offen offset:2048 sc1\n\t" "buffer_load_dwordx4 v[252:255], %[x3], %[rs], %[pb] offen offset:3072 sc1\n\t" \
    "buffer_load_dwordx4 v[168:171], v131, %[rs], %[ub] offen offset:0 sc1\n\t" "buffer_load_dwordx4 v[172:175], v131, %[rs], %[ub] offen offset:1024 sc1\n\t" \
    "buffer_load_dwordx2 v[188:189], v135, %[rs], %[pb] offen offset:0 sc1\n\t" "buffer_load_dwordx2 v[190:191], v135, %[rs], %[pb] offen offset:128 sc1\n\t" \
    "buffer_load_dword %[sv], %[so], %[rs], %[pb] offen sc1\n\t" "v_mov_b32 v127, -1\n\t" "s_waitcnt vmcnt(26)\n\t" XT(94, 95) \
    "v_smfmac_f32_16x16x64_bf16 v[120:123], v[192:195], a[0:7], %[ix]\n\t" "s_waitcnt vmcnt(25)\n\t" "v_smfmac_f32_16x16x64_bf16 v[120:123], v[196:199], a[8:15], %[ix]\n\t" \
    "s_waitcnt vmcnt(24)\n\t" "v_smfmac_f32_16x16x64_bf16 v[120:123], v[200:203], a[16:23], %[ix]\n\t" "s_waitcnt vmcnt(23)\n\t" \
    "v_smfmac_f32_16x16x64_bf16 v[120:123], v[204:207], a[24:31], %[ix]\n\t" "s_waitcnt vmcnt(21)\n\t" \
    "v_smfmac_f32_16x16x64_bf16 v[120:123], v[176:179], v[144:151], %[ix]\n\t" "s_waitcnt vmcnt(20)\n\t" \
    "v_smfmac_f32_16x16x64_bf16 v[120:123], v[208:211], a[32:39], %[ix]\n\t" "s_waitcnt vmcnt(19)\n\t" \
    "v_smfmac_f32_16x16x64_bf16 v[120:123], v[212:215], a[40:47], %[ix]\n\t" "s_waitcnt vmcnt(18)\n\t" \
    "v_smfmac_f32_16x16x64_bf16 v[120:123], v[216:219], a[48:55], %[ix]\n\t" "s_waitcnt vmcnt(17)\n\t" \
    "v_smfmac_f32_16x16x64_bf16 v[120:123], v[220:223], a[56:63], %[ix]\n\t" "s_waitcnt vmcnt(15)\n\t" \
    "v_smfmac_f32_16x16x64_bf16 v[120:123], v[180:183], v[152:159], %[ix]\n\t" "s_waitcnt vmcnt(14)\n\t" \
    "v_smfmac_f32_16x16x64_bf16 v[120:123], v[224:227], a[64:71], %[ix]\n\t" "s_waitcnt vmcnt(13)\n\t" \
    "v_smfmac_f32_16x16x64_bf16 v[120:123], v[228:231], a[72:79], %[ix]\n\t" "s_waitcnt vmcnt(12)\n\t" \
    "v_smfmac_f32_16x16x64_bf16 v[120:123], v[232:235], a[80:87], %[ix]\n\t" "s_waitcnt vmcnt(11)\n\t" \
    "v_smfmac_f32_16x16x64_bf16 v[120:123], v[236:239], a[88:95], %[ix]\n\t" "s_waitcnt vmcnt(9)\n\t" \
    "v_smfmac_f32_16x16x64_bf16 v[120:123], v[184:187], v[160:167], %[ix]\n\t" "s_waitcnt vmcnt(8)\n\t" \
    "v_smfmac_f32_16x16x64_bf16 v[120:123], v[240:243], a[96:103], %[ix]\n\t" "s_waitcnt vmcnt(7)\n\t" \
    "v_smfmac_f32_16x16x64_bf16 v[120:123], v[244:247], a[104:111], %[ix]\n\t" "s_waitcnt vmcnt(6)\n\t" \
    "v_smfmac_f32_16x16x64_bf16 v[120:123], v[248:251], a[112:119], %[ix]\n\t" "s_waitcnt vmcnt(5)\n\t" \
    "v_smfmac_f32_16x16x64_bf16 v[120:123], v[252:255], a[120:127], %[ix]\n\t" "s_waitcnt vmcnt(1)\n\t" \
    "v_smfmac_f32_16x16x64_bf16 v[120:123], v[188:191], v[168:175], %[ix]\n\t" XT(82, 83) "s_nop 7\n\t" "v_add_f32 v120, v120, v121\n\t" "v_add_f32 v122, v122, v123\n\t" \
    "v_cvt_pk_bf16_f32 v124, v120, v122\n\t" "v_cmp_u_f32 vcc, v120, v122\n\t" "s_cbranch_vccnz 30f\n\t" "21:\n\t" \
    "v_mov_b32_dpp v125, v124 quad_perm:[1,0,3,2] row_mask:0xf bank_mask:0xf\n\t" "v_perm_b32 v124, v125, v124, %[ps]\n\t" "v_min_u32 v124, -2, v124\n\t" \
    "buffer_store_dword v124, %[mw], %[rs], %[mb] offen sc1\n\t" XT(84, 85) "buffer_store_dword v127, %[mw], %[rs], %[mr] offen sc1\n\t" "s_memtime s[64:65]\n\t" \
    "s_barrier\n\t" XT(98, 99) "s_waitcnt lgkmcnt(0)\n\t" "s_and_b32 %[bad], s64, -2\n\t" "s_branch 40f\n\t" "30:\n\t" "s_cmp_lg_u32 %[late], 0\n\t" "s_cbranch_scc1 32f\n\t" \
    XCNT(100) "s_mov_b32 %[bad], 1\n\t" "s_waitcnt vmcnt(0)\n\t" "s_branch 40f\n\t" "32:\n\t" "v_max_u32 v126, v144, v145\n\t" "v_max3_u32 v126, v126, v146, v147\n\t" \
    "v_max3_u32 v126, v126, v148, v149\n\t" "v_max3_u32 v126, v126, v150, v151\n\t" "v_max3_u32 v126, v126, v152, v153\n\t" "v_max3_u32 v126, v126, v154, v155\n\t" \
    "v_max3_u32 v126, v126, v156, v157\n\t" "v_max3_u32 v126, v126, v158, v159\n\t" "v_max3_u32 v126, v126, v160, v161\n\t" "v_max3_u32 v126, v126, v162, v163\n\t" \
    "v_max3_u32 v126, v126, v164, v165\n\t" "v_max3_u32 v126, v126, v166, v167\n\t" "v_max3_u32 v126, v126, v168, v169\n\t" "v_max3_u32 v126, v126, v170, v171\n\t" \
    "v_max3_u32 v126, v126, v172, v173\n\t" "v_max3_u32 v126, v126, v174, v175\n\t" UCNT "v_max3_u32 v126, v126, v176, v177\n\t" "v_max3_u32 v126, v126, v178, v179\n\t" \
    "v_max3_u32 v126, v126, v180, v181\n\t" "v_max3_u32 v126, v126, v182, v183\n\t" "v_max3_u32 v126, v126, v184, v185\n\t" "v_max3_u32 v126, v126, v186, v187\n\t" \
    "v_max3_u32 v126, v126, v188, v189\n\t" "v_max3_u32 v126, v126, v190, v191\n\t" "v_max3_u32 v126, v126, v192, v193\n\t" "v_max3_u32 v126, v126, v194, v195\n\t" \
    "v_max3_u32 v126, v126, v196, v197\n\t" "v_max3_u32 v126, v126, v198, v199\n\t" "v_max3_u32 v126, v126, v200, v201\n\t" "v_max3_u32 v126, v126, v202, v203\n\t" \
    "v_max3_u32 v126, v126, v204, v205\n\t" "v_max3_u32 v126, v126, v206, v207\n\t" "v_max3_u32 v126, v126, v208, v209\n\t" "v_max3_u32 v126, v126, v210, v211\n\t" \
    "v_max3_u32 v126, v126, v212, v213\n\t" "v_max3_u32 v126, v126, v214, v215\n\t" "v_max3_u32 v126, v126, v216, v217\n\t" "v_max3_u32 v126, v126, v218, v219\n\t" \
    "v_max3_u32 v126, v126, v220, v221\n\t" "v_max3_u32 v126, v126, v222, v223\n\t" "v_max3_u32 v126, v126, v224, v225\n\t" "v_max3_u32 v126, v126, v226, v227\n\t" \
    "v_max3_u32 v126, v126, v228, v229\n\t" "v_max3_u32 v126, v126, v230, v231\n\t" "v_max3_u32 v126, v126, v232, v233\n\t" "v_max3_u32 v126, v126, v234, v235\n\t" \
    "v_max3_u32 v126, v126, v236, v237\n\t" "v_max3_u32 v126, v126, v238, v239\n\t" "v_max3_u32 v126, v126, v240, v241\n\t" "v_max3_u32 v126, v126, v242, v243\n\t" \
    "v_max3_u32 v126, v126, v244, v245\n\t" "v_max3_u32 v126, v126, v246, v247\n\t" "v_max3_u32 v126, v126, v248, v249\n\t" "v_max3_u32 v126, v126, v250, v251\n\t" \
    "v_max3_u32 v126, v126, v252, v253\n\t" "v_max3_u32 v126, v126, v254, v255\n\t" "v_cmp_eq_u32 vcc, -1, v126\n\t" "s_cbranch_vccz 21b\n\t" "s_cmp_eq_u32 %[bd], 0\n\t" \
    "s_cbranch_scc1 21b\n\t" XCNT(100) "s_mov_b32 %[bad], 1\n\t" "s_waitcnt vmcnt(0)\n\t" "40:\n\t"
#define G_LOOP \
    "v_xor_b32 v72, 0, %[xo]\n\t" "v_add_u32 v76, 0x10000, v72\n\t" "v_xor_b32 v88, 0, %[xs]\n\t" "v_add_u32 v88, 0, v88\n\t" "v_xor_b32 v73, 64, %[xo]\n\t" \
    "v_add_u32 v77, 0x10000, v73\n\t" "v_xor_b32 v89, 16, %[xs]\n\t" "v_add_u32 v89, 2048, v89\n\t" "v_xor_b32 v74, 128, %[xo]\n\t" "v_add_u32 v78, 0x10000, v74\n\t" \
    "v_xor_b32 v90, 32, %[xs]\n\t" "v_add_u32 v90, 4096, v90\n\t" "v_xor_b32 v75, 192, %[xo]\n\t" "v_add_u32 v79, 0x10000, v75\n\t" "v_xor_b32 v91, 48, %[xs]\n\t" \
    "v_add_u32 v91, 6144, v91\n\t" "v_xor_b32 v92, 64, %[xs]\n\t" "v_add_u32 v92, 8192, v92\n\t" "v_xor_b32 v93, 80, %[xs]\n\t" "v_add_u32 v93, 10240, v93\n\t" \
    "v_xor_b32 v94, 96, %[xs]\n\t" "v_add_u32 v94, 12288, v94\n\t" "v_xor_b32 v95, 112, %[xs]\n\t" "v_add_u32 v95, 14336, v95\n\t" "v_mov_b32 v96, 2.0\n\t" \
    "s_mov_b32 s60, 0\n\t" "s_mov_b32 s62, 0\n\t" "s_waitcnt vmcnt(0)\n\t" "s_barrier\n\t" "ds_read_b128 v[100:103], v72 offset:0\n\t" \
    "ds_read_b128 v[104:107], v72 offset:32768\n\t" "ds_read_b128 v[108:111], v73 offset:0\n\t" "ds_read_b128 v[112:115], v73 offset:32768\n\t" \
    "ds_read_b128 v[116:119], v74 offset:0\n\t" "10:\n\t" XT(80, 81) "s_waitcnt lgkmcnt(4)\n\t" "v_mfma_f32_16x16x32_bf16 v[32:35], a[0:3], v[100:103], 0\n\t" \
    "ds_read_b128 v[120:123], v74 offset:32768\n\t" "s_add_u32 s61, s60, 1\n\t" "s_and_b32 s61, s61, 63\n\t" "v_mfma_f32_16x16x32_bf16 v[36:39], a[4:7], v[100:103], 0\n\t" \
    "s_lshl_b32 s61, s61, 16\n\t" "s_add_u32 s63, %[lw], 65536\n\t" "s_waitcnt lgkmcnt(4)\n\t" "v_mfma_f32_16x16x32_bf16 v[40:43], a[0:3], v[104:107], 0\n\t" \
    "ds_read_b128 v[124:127], v75 offset:0\n\t" "v_mfma_f32_16x16x32_bf16 v[44:47], a[4:7], v[104:107], 0\n\t" "s_waitcnt lgkmcnt(4)\n\t" \
    "v_mfma_f32_16x16x32_bf16 v[32:35], a[8:11], v[108:111], v[32:35]\n\t" "ds_read_b128 v[128:131], v75 offset:32768\n\t" "v_cvt_pk_bf16_f32 v64, v48, v49\n\t" \
    "v_mfma_f32_16x16x32_bf16 v[36:39], a[12:15], v[108:111], v[36:39]\n\t" "v_cvt_pk_bf16_f32 v65, v50, v51\n\t" "v_cvt_pk_bf16_f32 v66, v52, v53\n\t" \
    "s_waitcnt lgkmcnt(4)\n\t" "v_mfma_f32_16x16x32_bf16 v[40:43], a[8:11], v[112:115], v[40:43]\n\t" "ds_read_b128 v[100:103], v72 offset:256\n\t" \
    "v_cvt_pk_bf16_f32 v67, v54, v55\n\t" "v_mfma_f32_16x16x32_bf16 v[44:47], a[12:15], v[112:115], v[44:47]\n\t" "v_cvt_pk_bf16_f32 v68, v56, v57\n\t" \
    "v_cvt_pk_bf16_f32 v69, v58, v59\n\t" "s_waitcnt lgkmcnt(4)\n\t" "v_mfma_f32_16x16x32_bf16 v[32:35], a[16:19], v[116:119], v[32:35]\n\t" \
    "ds_read_b128 v[104:107], v72 offset:33024\n\t" "v_cvt_pk_bf16_f32 v70, v60, v61\n\t" "v_mfma_f32_16x16x32_bf16 v[36:39], a[20:23], v[116:119], v[36:39]\n\t" \
    "v_cvt_pk_bf16_f32 v71, v62, v63\n\t" "buffer_store_dwordx4 v[64:67], v96, %[rs], s62 offen offset:0 sc1\n\t" "s_waitcnt lgkmcnt(4)\n\t" \
    "v_mfma_f32_16x16x32_bf16 v[40:43], a[16:19], v[120:123], v[40:43]\n\t" "ds_read_b128 v[108:111], v73 offset:256\n\t" \
    "v_mfma_f32_16x16x32_bf16 v[44:47], a[20:23], v[120:123], v[44:47]\n\t" "s_waitcnt lgkmcnt(4)\n\t" "v_mfma_f32_16x16x32_bf16 v[32:35], a[24:27], v[124:127], v[32:35]\n\t" \
    "ds_read_b128 v[112:115], v73 offset:33024\n\t" "v_mfma_f32_16x16x32_bf16 v[36:39], a[28:31], v[124:127], v[36:39]\n\t" \
    "buffer_store_dwordx4 v[68:71], v96, %[rs], s62 offen offset:256 sc1\n\t" "s_waitcnt lgkmcnt(4)\n\t" \
    "v_mfma_f32_16x16x32_bf16 v[40:43], a[24:27], v[128:131], v[40:43]\n\t" "ds_read_b128 v[116:119], v74 offset:256\n\t" "s_mov_b32 m0, s63\n\t" \
    "v_mfma_f32_16x16x32_bf16 v[44:47], a[28:31], v[128:131], v[44:47]\n\t" "s_add_u32 s63, s63, 0x800\n\t" "buffer_load_dwordx4 v88, %[rx], s61 offen lds\n\t" \
    "s_waitcnt lgkmcnt(4)\n\t" "v_mfma_f32_16x16x32_bf16 v[32:35], a[32:35], v[100:103], v[32:35]\n\t" "ds_read_b128 v[120:123], v74 offset:33024\n\t" \
    "v_mfma_f32_16x16x32_bf16 v[36:39], a[36:39], v[100:103], v[36:39]\n\t" "buffer_load_dwordx4 v88, %[rx], s61 offen offset:1024 lds\n\t" "s_waitcnt lgkmcnt(4)\n\t" \
    "v_mfma_f32_16x16x32_bf16 v[40:43], a[32:35], v[104:107], v[40:43]\n\t" "ds_read_b128 v[124:127], v75 offset:256\n\t" "s_mov_b32 m0, s63\n\t" \
    "v_mfma_f32_16x16x32_bf16 v[44:47], a[36:39], v[104:107], v[44:47]\n\t" "s_add_u32 s63, s63, 0x800\n\t" "buffer_load_dwordx4 v89, %[rx], s61 offen lds\n\t" \
    "s_waitcnt lgkmcnt(4)\n\t" "v_mfma_f32_16x16x32_bf16 v[32:35], a[40:43], v[108:111], v[32:35]\n\t" "ds_read_b128 v[128:131], v75 offset:33024\n\t" \
    "v_mfma_f32_16x16x32_bf16 v[36:39], a[44:47], v[108:111], v[36:39]\n\t" "buffer_load_dwordx4 v89, %[rx], s61 offen offset:1024 lds\n\t" "s_waitcnt lgkmcnt(4)\n\t" \
    "v_mfma_f32_16x16x32_bf16 v[40:43], a[40:43], v[112:115], v[40:43]\n\t" "ds_read_b128 v[100:103], v72 offset:512\n\t" "s_mov_b32 m0, s63\n\t" \
    "v_mfma_f32_16x16x32_bf16 v[44:47], a[44:47], v[112:115], v[44:47]\n\t" "s_add_u32 s63, s63, 0x800\n\t" "buffer_load_dwordx4 v90, %[rx], s61 offen lds\n\t" \
    "s_waitcnt lgkmcnt(4)\n\t" "v_mfma_f32_16x16x32_bf16 v[32:35], a[48:51], v[116:119], v[32:35]\n\t" "ds_read_b128 v[104:107], v72 offset:33280\n\t" \
    "v_mfma_f32_16x16x32_bf16 v[36:39], a[52:55], v[116:119], v[36:39]\n\t" "buffer_load_dwordx4 v90, %[rx], s61 offen offset:1024 lds\n\t" "s_waitcnt lgkmcnt(4)\n\t" \
    "v_mfma_f32_16x16x32_bf16 v[40:43], a[48:51], v[120:123], v[40:43]\n\t" "ds_read_b128 v[108:111], v73 offset:512\n\t" "s_mov_b32 m0, s63\n\t" \
    "v_mfma_f32_16x16x32_bf16 v[44:47], a[52:55], v[120:123], v[44:47]\n\t" "s_add_u32 s63, s63, 0x800\n\t" "buffer_load_dwordx4 v91, %[rx], s61 offen lds\n\t" \
    "s_waitcnt lgkmcnt(4)\n\t" "v_mfma_f32_16x16x32_bf16 v[32:35], a[56:59], v[124:127], v[32:35]\n\t" "ds_read_b128 v[112:115], v73 offset:33280\n\t" \
    "v_mfma_f32_16x16x32_bf16 v[36:39], a[60:63], v[124:127], v[36:39]\n\t" "buffer_load_dwordx4 v91, %[rx], s61 offen offset:1024 lds\n\t" "s_waitcnt lgkmcnt(4)\n\t" \
    "v_mfma_f32_16x16x32_bf16 v[40:43], a[56:59], v[128:131], v[40:43]\n\t" "ds_read_b128 v[116:119], v74 offset:512\n\t" "s_mov_b32 m0, s63\n\t" \
    "v_mfma_f32_16x16x32_bf16 v[44:47], a[60:63], v[128:131], v[44:47]\n\t" "s_add_u32 s63, s63, 0x800\n\t" "buffer_load_dwordx4 v92, %[rx], s61 offen lds\n\t" \
    "s_waitcnt lgkmcnt(4)\n\t" "v_mfma_f32_16x16x32_bf16 v[32:35], a[64:67], v[100:103], v[32:35]\n\t" "ds_read_b128 v[120:123], v74 offset:33280\n\t" \
    "v_mfma_f32_16x16x32_bf16 v[36:39], a[68:71], v[100:103], v[36:39]\n\t" "buffer_load_dwordx4 v92, %[rx], s61 offen offset:1024 lds\n\t" "s_waitcnt lgkmcnt(4)\n\t" \
    "v_mfma_f32_16x16x32_bf16 v[40:43], a[64:67], v[104:107], v[40:43]\n\t" "ds_read_b128 v[124:127], v75 offset:512\n\t" "s_mov_b32 m0, s63\n\t" \
    "v_mfma_f32_16x16x32_bf16 v[44:47], a[68:71], v[104:107], v[44:47]\n\t" "s_add_u32 s63, s63, 0x800\n\t" "buffer_load_dwordx4 v93, %[rx], s61 offen lds\n\t" \
    "s_waitcnt lgkmcnt(4)\n\t" "v_mfma_f32_16x16x32_bf16 v[32:35], a[72:75], v[108:111], v[32:35]\n\t" "ds_read_b128 v[128:131], v75 offset:33280\n\t" \
    "v_mfma_f32_16x16x32_bf16 v[36:39], a[76:79], v[108:111], v[36:39]\n\t" "buffer_load_dwordx4 v93, %[rx], s61 offen offset:1024 lds\n\t" "s_waitcnt lgkmcnt(4)\n\t" \
    "v_mfma_f32_16x16x32_bf16 v[40:43], a[72:75], v[112:115], v[40:43]\n\t" "ds_read_b128 v[100:103], v72 offset:768\n\t" "s_mov_b32 m0, s63\n\t" \
    "v_mfma_f32_16x16x32_bf16 v[44:47], a[76:79], v[112:115], v[44:47]\n\t" "s_add_u32 s63, s63, 0x800\n\t" "buffer_load_dwordx4 v94, %[rx], s61 offen lds\n\t" \
    "s_waitcnt lgkmcnt(4)\n\t" "v_mfma_f32_16x16x32_bf16 v[32:35], a[80:83], v[116:119], v[32:35]\n\t" "ds_read_b128 v[104:107], v72 offset:33536\n\t" \
    "v_mfma_f32_16x16x32_bf16 v[36:39], a[84:87], v[116:119], v[36:39]\n\t" "buffer_load_dwordx4 v94, %[rx], s61 offen offset:1024 lds\n\t" "s_waitcnt lgkmcnt(4)\n\t" \
    "v_mfma_f32_16x16x32_bf16 v[40:43], a[80:83], v[120:123], v[40:43]\n\t" "ds_read_b128 v[108:111], v73 offset:768\n\t" "s_mov_b32 m0, s63\n\t" \
    "v_mfma_f32_16x16x32_bf16 v[44:47], a[84:87], v[120:123], v[44:47]\n\t" "s_add_u32 s63, s63, 0x800\n\t" "buffer_load_dwordx4 v95, %[rx], s61 offen lds\n\t" \
    "s_waitcnt lgkmcnt(4)\n\t" "v_mfma_f32_16x16x32_bf16 v[32:35], a[88:91], v[124:127], v[32:35]\n\t" "ds_read_b128 v[112:115], v73 offset:33536\n\t" \
    "v_mfma_f32_16x16x32_bf16 v[36:39], a[92:95], v[124:127], v[36:39]\n\t" "buffer_load_dwordx4 v95, %[rx], s61 offen offset:1024 lds\n\t" "s_waitcnt lgkmcnt(4)\n\t" \
    "v_mfma_f32_16x16x32_bf16 v[40:43], a[88:91], v[128:131], v[40:43]\n\t" "ds_read_b128 v[116:119], v74 offset:768\n\t" \
    "v_mfma_f32_16x16x32_bf16 v[44:47], a[92:95], v[128:131], v[44:47]\n\t" "s_waitcnt lgkmcnt(4)\n\t" "v_mfma_f32_16x16x32_bf16 v[32:35], a[96:99], v[100:103], v[32:35]\n\t" \
    "ds_read_b128 v[120:123], v74 offset:33536\n\t" "s_lshr_b32 s64, s60, 2\n\t" "s_xor_b32 s64, s64, %[xp]\n\t" \
    "v_mfma_f32_16x16x32_bf16 v[36:39], a[100:103], v[100:103], v[36:39]\n\t" "s_and_b32 s64, s64, 1\n\t" "s_lshl_b32 s64, s64, 13\n\t" "s_waitcnt lgkmcnt(4)\n\t" \
    "v_mfma_f32_16x16x32_bf16 v[40:43], a[96:99], v[104:107], v[40:43]\n\t" "ds_read_b128 v[124:127], v75 offset:768\n\t" "s_lshl_b32 s62, s60, 19\n\t" \
    "v_mfma_f32_16x16x32_bf16 v[44:47], a[100:103], v[104:107], v[44:47]\n\t" "s_add_u32 s62, s62, s64\n\t" "s_add_u32 s62, s62, %[ob]\n\t" "s_waitcnt lgkmcnt(4)\n\t" \
    "v_mfma_f32_16x16x32_bf16 v[32:35], a[104:107], v[108:111], v[32:35]\n\t" "ds_read_b128 v[128:131], v75 offset:33536\n\t" \
    "v_mfma_f32_16x16x32_bf16 v[36:39], a[108:111], v[108:111], v[36:39]\n\t" "s_waitcnt lgkmcnt(4)\n\t" \
    "v_mfma_f32_16x16x32_bf16 v[40:43], a[104:107], v[112:115], v[40:43]\n\t" "ds_read_b128 v[100:103], v72 offset:1024\n\t" \
    "v_mfma_f32_16x16x32_bf16 v[44:47], a[108:111], v[112:115], v[44:47]\n\t" "s_waitcnt lgkmcnt(4)\n\t" \
    "v_mfma_f32_16x16x32_bf16 v[32:35], a[112:115], v[116:119], v[32:35]\n\t" "ds_read_b128 v[104:107], v72 offset:33792\n\t" \
    "v_mfma_f32_16x16x32_bf16 v[36:39], a[116:119], v[116:119], v[36:39]\n\t" "s_waitcnt lgkmcnt(4)\n\t" \
    "v_mfma_f32_16x16x32_bf16 v[40:43], a[112:115], v[120:123], v[40:43]\n\t" "ds_read_b128 v[108:111], v73 offset:1024\n\t" \
    "v_mfma_f32_16x16x32_bf16 v[44:47], a[116:119], v[120:123], v[44:47]\n\t" "s_waitcnt lgkmcnt(4)\n\t" \
    "v_mfma_f32_16x16x32_bf16 v[32:35], a[120:123], v[124:127], v[32:35]\n\t" "ds_read_b128 v[112:115], v73 offset:33792\n\t" \
    "v_mfma_f32_16x16x32_bf16 v[36:39], a[124:127], v[124:127], v[36:39]\n\t" "s_waitcnt lgkmcnt(4)\n\t" \
    "v_mfma_f32_16x16x32_bf16 v[40:43], a[120:123], v[128:131], v[40:43]\n\t" "ds_read_b128 v[116:119], v74 offset:1024\n\t" \
    "v_mfma_f32_16x16x32_bf16 v[44:47], a[124:127], v[128:131], v[44:47]\n\t" "s_waitcnt lgkmcnt(4)\n\t" \
    "v_mfma_f32_16x16x32_bf16 v[32:35], a[128:131], v[100:103], v[32:35]\n\t" "ds_read_b128 v[120:123], v74 offset:33792\n\t" \
    "v_mfma_f32_16x16x32_bf16 v[36:39], a[132:135], v[100:103], v[36:39]\n\t" "s_waitcnt lgkmcnt(4)\n\t" \
    "v_mfma_f32_16x16x32_bf16 v[40:43], a[128:131], v[104:107], v[40:43]\n\t" "ds_read_b128 v[124:127], v75 offset:1024\n\t" \
    "v_mfma_f32_16x16x32_bf16 v[44:47], a[132:135], v[104:107], v[44:47]\n\t" "s_waitcnt lgkmcnt(4)\n\t" \
    "v_mfma_f32_16x16x32_bf16 v[32:35], a[136:139], v[108:111], v[32:35]\n\t" "ds_read_b128 v[128:131], v75 offset:33792\n\t" "s_waitcnt vmcnt(16)\n\t" "s_barrier\n\t" \
    "v_mfma_f32_16x16x32_bf16 v[36:39], a[140:143], v[108:111], v[36:39]\n\t" "v_mov_b32 v98, s60\n\t" "buffer_store_dword v98, %[fo], %[rc], %[fl] offen sc1\n\t" \
    "s_waitcnt lgkmcnt(4)\n\t" "v_mfma_f32_16x16x32_bf16 v[40:43], a[136:139], v[112:115], v[40:43]\n\t" "ds_read_b128 v[100:103], v72 offset:1280\n\t" \
    "v_mfma_f32_16x16x32_bf16 v[44:47], a[140:143], v[112:115], v[44:47]\n\t" "s_waitcnt lgkmcnt(4)\n\t" \
    "v_mfma_f32_16x16x32_bf16 v[32:35], a[144:147], v[116:119], v[32:35]\n\t" "ds_read_b128 v[104:107], v72 offset:34048\n\t" \
    "v_mfma_f32_16x16x32_bf16 v[36:39], a[148:151], v[116:119], v[36:39]\n\t" "s_waitcnt lgkmcnt(4)\n\t" \
    "v_mfma_f32_16x16x32_bf16 v[40:43], a[144:147], v[120:123], v[40:43]\n\t" "ds_read_b128 v[108:111], v73 offset:1280\n\t" \
    "v_mfma_f32_16x16x32_bf16 v[44:47], a[148:151], v[120:123], v[44:47]\n\t" "s_waitcnt lgkmcnt(4)\n\t" \
    "v_mfma_f32_16x16x32_bf16 v[32:35], a[152:155], v[124:127], v[32:35]\n\t" "ds_read_b128 v[112:115], v73 offset:34048\n\t" \
    "v_mfma_f32_16x16x32_bf16 v[36:39], a[156:159], v[124:127], v[36:39]\n\t" "s_waitcnt lgkmcnt(4)\n\t" \
    "v_mfma_f32_16x16x32_bf16 v[40:43], a[152:155], v[128:131], v[40:43]\n\t" "ds_read_b128 v[116:119], v74 offset:1280\n\t" \
    "v_mfma_f32_16x16x32_bf16 v[44:47], a[156:159], v[128:131], v[44:47]\n\t" "s_waitcnt lgkmcnt(4)\n\t" \
    "v_mfma_f32_16x16x32_bf16 v[32:35], a[160:163], v[100:103], v[32:35]\n\t" "ds_read_b128 v[120:123], v74 offset:34048\n\t" \
    "v_mfma_f32_16x16x32_bf16 v[36:39], a[164:167], v[100:103], v[36:39]\n\t" "s_waitcnt lgkmcnt(4)\n\t" \
    "v_mfma_f32_16x16x32_bf16 v[40:43], a[160:163], v[104:107], v[40:43]\n\t" "ds_read_b128 v[124:127], v75 offset:1280\n\t" \
    "v_mfma_f32_16x16x32_bf16 v[44:47], a[164:167], v[104:107], v[44:47]\n\t" "s_waitcnt lgkmcnt(4)\n\t" \
    "v_mfma_f32_16x16x32_bf16 v[32:35], a[168:171], v[108:111], v[32:35]\n\t" "ds_read_b128 v[128:131], v75 offset:34048\n\t" \
    "v_mfma_f32_16x16x32_bf16 v[36:39], a[172:175], v[108:111], v[36:39]\n\t" "s_waitcnt lgkmcnt(4)\n\t" \
    "v_mfma_f32_16x16x32_bf16 v[40:43], a[168:171], v[112:115], v[40:43]\n\t" "ds_read_b128 v[100:103], v72 offset:1536\n\t" \
    "v_mfma_f32_16x16x32_bf16 v[44:47], a[172:175], v[112:115], v[44:47]\n\t" "s_waitcnt lgkmcnt(4)\n\t" \
    "v_mfma_f32_16x16x32_bf16 v[32:35], a[176:179], v[116:119], v[32:35]\n\t" "ds_read_b128 v[104:107], v72 offset:34304\n\t" \
    "v_mfma_f32_16x16x32_bf16 v[36:39], a[180:183], v[116:119], v[36:39]\n\t" "s_waitcnt lgkmcnt(4)\n\t" \
    "v_mfma_f32_16x16x32_bf16 v[40:43], a[176:179], v[120:123], v[40:43]\n\t" "ds_read_b128 v[108:111], v73 offset:1536\n\t" \
    "v_mfma_f32_16x16x32_bf16 v[44:47], a[180:183], v[120:123], v[44:47]\n\t" "s_waitcnt lgkmcnt(4)\n\t" \
    "v_mfma_f32_16x16x32_bf16 v[32:35], a[184:187], v[124:127], v[32:35]\n\t" "ds_read_b128 v[112:115], v73 offset:34304\n\t" \
    "v_mfma_f32_16x16x32_bf16 v[36:39], a[188:191], v[124:127], v[36:39]\n\t" "s_waitcnt lgkmcnt(4)\n\t" \
    "v_mfma_f32_16x16x32_bf16 v[40:43], a[184:187], v[128:131], v[40:43]\n\t" "ds_read_b128 v[116:119], v74 offset:1536\n\t" \
    "v_mfma_f32_16x16x32_bf16 v[44:47], a[188:191], v[128:131], v[44:47]\n\t" "s_waitcnt lgkmcnt(4)\n\t" \
    "v_mfma_f32_16x16x32_bf16 v[32:35], a[192:195], v[100:103], v[32:35]\n\t" "ds_read_b128 v[120:123], v74 offset:34304\n\t" \
    "v_mfma_f32_16x16x32_bf16 v[36:39], a[196:199], v[100:103], v[36:39]\n\t" "s_waitcnt lgkmcnt(4)\n\t" \
    "v_mfma_f32_16x16x32_bf16 v[40:43], a[192:195], v[104:107], v[40:43]\n\t" "ds_read_b128 v[124:127], v75 offset:1536\n\t" \
    "v_mfma_f32_16x16x32_bf16 v[44:47], a[196:199], v[104:107], v[44:47]\n\t" "s_waitcnt lgkmcnt(4)\n\t" \
    "v_mfma_f32_16x16x32_bf16 v[32:35], a[200:203], v[108:111], v[32:35]\n\t" "ds_read_b128 v[128:131], v75 offset:34304\n\t" \
    "v_mfma_f32_16x16x32_bf16 v[36:39], a[204:207], v[108:111], v[36:39]\n\t" "s_waitcnt lgkmcnt(4)\n\t" \
    "v_mfma_f32_16x16x32_bf16 v[40:43], a[200:203], v[112:115], v[40:43]\n\t" "ds_read_b128 v[100:103], v72 offset:1792\n\t" \
    "v_mfma_f32_16x16x32_bf16 v[44:47], a[204:207], v[112:115], v[44:47]\n\t" "s_waitcnt lgkmcnt(4)\n\t" \
    "v_mfma_f32_16x16x32_bf16 v[32:35], a[208:211], v[116:119], v[32:35]\n\t" "ds_read_b128 v[104:107], v72 offset:34560\n\t" \
    "v_mfma_f32_16x16x32_bf16 v[36:39], a[212:215], v[116:119], v[36:39]\n\t" "s_waitcnt lgkmcnt(4)\n\t" \
    "v_mfma_f32_16x16x32_bf16 v[40:43], a[208:211], v[120:123], v[40:43]\n\t" "ds_read_b128 v[108:111], v73 offset:1792\n\t" \
    "v_mfma_f32_16x16x32_bf16 v[44:47], a[212:215], v[120:123], v[44:47]\n\t" "s_waitcnt lgkmcnt(4)\n\t" \
    "v_mfma_f32_16x16x32_bf16 v[32:35], a[216:219], v[124:127], v[32:35]\n\t" "ds_read_b128 v[112:115], v73 offset:34560\n\t" \
    "v_mfma_f32_16x16x32_bf16 v[36:39], a[220:223], v[124:127], v[36:39]\n\t" "s_waitcnt lgkmcnt(4)\n\t" \
    "v_mfma_f32_16x16x32_bf16 v[40:43], a[216:219], v[128:131], v[40:43]\n\t" "ds_read_b128 v[116:119], v74 offset:1792\n\t" \
    "v_mfma_f32_16x16x32_bf16 v[44:47], a[220:223], v[128:131], v[44:47]\n\t" "s_waitcnt lgkmcnt(4)\n\t" \
    "v_mfma_f32_16x16x32_bf16 v[32:35], a[224:227], v[100:103], v[32:35]\n\t" "ds_read_b128 v[120:123], v74 offset:34560\n\t" \
    "v_mfma_f32_16x16x32_bf16 v[36:39], a[228:231], v[100:103], v[36:39]\n\t" "s_waitcnt lgkmcnt(4)\n\t" \
    "v_mfma_f32_16x16x32_bf16 v[40:43], a[224:227], v[104:107], v[40:43]\n\t" "ds_read_b128 v[124:127], v75 offset:1792\n\t" \
    "v_mfma_f32_16x16x32_bf16 v[44:47], a[228:231], v[104:107], v[44:47]\n\t" "s_waitcnt lgkmcnt(4)\n\t" \
    "v_mfma_f32_16x16x32_bf16 v[32:35], a[232:235], v[108:111], v[32:35]\n\t" "ds_read_b128 v[128:131], v75 offset:34560\n\t" \
    "v_mfma_f32_16x16x32_bf16 v[36:39], a[236:239], v[108:111], v[36:39]\n\t" "s_waitcnt lgkmcnt(4)\n\t" \
    "v_mfma_f32_16x16x32_bf16 v[40:43], a[232:235], v[112:115], v[40:43]\n\t" "v_mfma_f32_16x16x32_bf16 v[44:47], a[236:239], v[112:115], v[44:47]\n\t" \
    "s_waitcnt lgkmcnt(3)\n\t" "v_mfma_f32_16x16x32_bf16 v[32:35], a[240:243], v[116:119], v[32:35]\n\t" \
    "v_mfma_f32_16x16x32_bf16 v[36:39], a[244:247], v[116:119], v[36:39]\n\t" "s_waitcnt vmcnt(1) lgkmcnt(0)\n\t" "s_barrier\n\t" XT(82, 83) \
    "v_mfma_f32_16x16x32_bf16 v[40:43], a[240:243], v[120:123], v[40:43]\n\t" "ds_read_b128 v[100:103], v76 offset:0\n\t" \
    "v_mfma_f32_16x16x32_bf16 v[44:47], a[244:247], v[120:123], v[44:47]\n\t" "v_mfma_f32_16x16x32_bf16 v[32:35], a[248:251], v[124:127], v[32:35]\n\t" \
    "ds_read_b128 v[104:107], v76 offset:32768\n\t" "v_mfma_f32_16x16x32_bf16 v[36:39], a[252:255], v[124:127], v[36:39]\n\t" "ds_read_b128 v[108:111], v77 offset:0\n\t" \
    "v_mfma_f32_16x16x32_bf16 v[40:43], a[248:251], v[128:131], v[40:43]\n\t" "ds_read_b128 v[112:115], v77 offset:32768\n\t" "ds_read_b128 v[116:119], v78 offset:0\n\t" \
    "v_mfma_f32_16x16x32_bf16 v[44:47], a[252:255], v[128:131], v[44:47]\n\t" "s_add_u32 s60, s60, 1\n\t" GSUM "s_cmp_eq_u32 s60, 1\n\t" "s_cbranch_scc1 20f\n\t" "11:\n\t" \
    XT(80, 81) "s_waitcnt lgkmcnt(4)\n\t" "v_mfma_f32_16x16x32_bf16 v[48:51], a[0:3], v[100:103], 0\n\t" "ds_read_b128 v[120:123], v78 offset:32768\n\t" \
    "s_add_u32 s61, s60, 1\n\t" "s_and_b32 s61, s61, 63\n\t" "v_mfma_f32_16x16x32_bf16 v[52:55], a[4:7], v[100:103], 0\n\t" "s_lshl_b32 s61, s61, 16\n\t" \
    "s_add_u32 s63, %[lw], 0\n\t" "s_waitcnt lgkmcnt(4)\n\t" "v_mfma_f32_16x16x32_bf16 v[56:59], a[0:3], v[104:107], 0\n\t" "ds_read_b128 v[124:127], v79 offset:0\n\t" \
    "v_mfma_f32_16x16x32_bf16 v[60:63], a[4:7], v[104:107], 0\n\t" "s_waitcnt lgkmcnt(4)\n\t" "v_mfma_f32_16x16x32_bf16 v[48:51], a[8:11], v[108:111], v[48:51]\n\t" \
    "ds_read_b128 v[128:131], v79 offset:32768\n\t" "v_cvt_pk_bf16_f32 v64, v32, v33\n\t" "v_mfma_f32_16x16x32_bf16 v[52:55], a[12:15], v[108:111], v[52:55]\n\t" \
    "v_cvt_pk_bf16_f32 v65, v34, v35\n\t" "v_cvt_pk_bf16_f32 v66, v36, v37\n\t" "s_waitcnt lgkmcnt(4)\n\t" \
    "v_mfma_f32_16x16x32_bf16 v[56:59], a[8:11], v[112:115], v[56:59]\n\t" "ds_read_b128 v[100:103], v76 offset:256\n\t" "v_cvt_pk_bf16_f32 v67, v38, v39\n\t" \
    "v_mfma_f32_16x16x32_bf16 v[60:63], a[12:15], v[112:115], v[60:63]\n\t" "v_cvt_pk_bf16_f32 v68, v40, v41\n\t" "v_cvt_pk_bf16_f32 v69, v42, v43\n\t" \
    "s_waitcnt lgkmcnt(4)\n\t" "v_mfma_f32_16x16x32_bf16 v[48:51], a[16:19], v[116:119], v[48:51]\n\t" "ds_read_b128 v[104:107], v76 offset:33024\n\t" \
    "v_cvt_pk_bf16_f32 v70, v44, v45\n\t" "v_mfma_f32_16x16x32_bf16 v[52:55], a[20:23], v[116:119], v[52:55]\n\t" "v_cvt_pk_bf16_f32 v71, v46, v47\n\t" \
    "buffer_store_dwordx4 v[64:67], v96, %[rs], s62 offen offset:0 sc1\n\t" "s_waitcnt lgkmcnt(4)\n\t" "v_mfma_f32_16x16x32_bf16 v[56:59], a[16:19], v[120:123], v[56:59]\n\t" \
    "ds_read_b128 v[108:111], v77 offset:256\n\t" "v_mfma_f32_16x16x32_bf16 v[60:63], a[20:23], v[120:123], v[60:63]\n\t" "s_waitcnt lgkmcnt(4)\n\t" \
    "v_mfma_f32_16x16x32_bf16 v[48:51], a[24:27], v[124:127], v[48:51]\n\t" "ds_read_b128 v[112:115], v77 offset:33024\n\t" \
    "v_mfma_f32_16x16x32_bf16 v[52:55], a[28:31], v[124:127], v[52:55]\n\t" "buffer_store_dwordx4 v[68:71], v96, %[rs], s62 offen offset:256 sc1\n\t" \
    "s_waitcnt lgkmcnt(4)\n\t" "v_mfma_f32_16x16x32_bf16 v[56:59], a[24:27], v[128:131], v[56:59]\n\t" "ds_read_b128 v[116:119], v78 offset:256\n\t" "s_mov_b32 m0, s63\n\t" \
    "v_mfma_f32_16x16x32_bf16 v[60:63], a[28:31], v[128:131], v[60:63]\n\t" "s_add_u32 s63, s63, 0x800\n\t" "buffer_load_dwordx4 v88, %[rx], s61 offen lds\n\t" \
    "s_waitcnt lgkmcnt(4)\n\t" "v_mfma_f32_16x16x32_bf16 v[48:51], a[32:35], v[100:103], v[48:51]\n\t" "ds_read_b128 v[120:123], v78 offset:33024\n\t" \
    "v_mfma_f32_16x16x32_bf16 v[52:55], a[36:39], v[100:103], v[52:55]\n\t" "buffer_load_dwordx4 v88, %[rx], s61 offen offset:1024 lds\n\t" "s_waitcnt lgkmcnt(4)\n\t" \
    "v_mfma_f32_16x16x32_bf16 v[56:59], a[32:35], v[104:107], v[56:59]\n\t" "ds_read_b128 v[124:127], v79 offset:256\n\t" "s_mov_b32 m0, s63\n\t" \
    "v_mfma_f32_16x16x32_bf16 v[60:63], a[36:39], v[104:107], v[60:63]\n\t" "s_add_u32 s63, s63, 0x800\n\t" "buffer_load_dwordx4 v89, %[rx], s61 offen lds\n\t" \
    "s_waitcnt lgkmcnt(4)\n\t" "v_mfma_f32_16x16x32_bf16 v[48:51], a[40:43], v[108:111], v[48:51]\n\t" "ds_read_b128 v[128:131], v79 offset:33024\n\t" \
    "v_mfma_f32_16x16x32_bf16 v[52:55], a[44:47], v[108:111], v[52:55]\n\t" "buffer_load_dwordx4 v89, %[rx], s61 offen offset:1024 lds\n\t" "s_waitcnt lgkmcnt(4)\n\t" \
    "v_mfma_f32_16x16x32_bf16 v[56:59], a[40:43], v[112:115], v[56:59]\n\t" "ds_read_b128 v[100:103], v76 offset:512\n\t" "s_mov_b32 m0, s63\n\t" \
    "v_mfma_f32_16x16x32_bf16 v[60:63], a[44:47], v[112:115], v[60:63]\n\t" "s_add_u32 s63, s63, 0x800\n\t" "buffer_load_dwordx4 v90, %[rx], s61 offen lds\n\t" \
    "s_waitcnt lgkmcnt(4)\n\t" "v_mfma_f32_16x16x32_bf16 v[48:51], a[48:51], v[116:119], v[48:51]\n\t" "ds_read_b128 v[104:107], v76 offset:33280\n\t" \
    "v_mfma_f32_16x16x32_bf16 v[52:55], a[52:55], v[116:119], v[52:55]\n\t" "buffer_load_dwordx4 v90, %[rx], s61 offen offset:1024 lds\n\t" "s_waitcnt lgkmcnt(4)\n\t" \
    "v_mfma_f32_16x16x32_bf16 v[56:59], a[48:51], v[120:123], v[56:59]\n\t" "ds_read_b128 v[108:111], v77 offset:512\n\t" "s_mov_b32 m0, s63\n\t" \
    "v_mfma_f32_16x16x32_bf16 v[60:63], a[52:55], v[120:123], v[60:63]\n\t" "s_add_u32 s63, s63, 0x800\n\t" "buffer_load_dwordx4 v91, %[rx], s61 offen lds\n\t" \
    "s_waitcnt lgkmcnt(4)\n\t" "v_mfma_f32_16x16x32_bf16 v[48:51], a[56:59], v[124:127], v[48:51]\n\t" "ds_read_b128 v[112:115], v77 offset:33280\n\t" \
    "v_mfma_f32_16x16x32_bf16 v[52:55], a[60:63], v[124:127], v[52:55]\n\t" "buffer_load_dwordx4 v91, %[rx], s61 offen offset:1024 lds\n\t" "s_waitcnt lgkmcnt(4)\n\t" \
    "v_mfma_f32_16x16x32_bf16 v[56:59], a[56:59], v[128:131], v[56:59]\n\t" "ds_read_b128 v[116:119], v78 offset:512\n\t" "s_mov_b32 m0, s63\n\t" \
    "v_mfma_f32_16x16x32_bf16 v[60:63], a[60:63], v[128:131], v[60:63]\n\t" "s_add_u32 s63, s63, 0x800\n\t" "buffer_load_dwordx4 v92, %[rx], s61 offen lds\n\t" \
    "s_waitcnt lgkmcnt(4)\n\t" "v_mfma_f32_16x16x32_bf16 v[48:51], a[64:67], v[100:103], v[48:51]\n\t" "ds_read_b128 v[120:123], v78 offset:33280\n\t" \
    "v_mfma_f32_16x16x32_bf16 v[52:55], a[68:71], v[100:103], v[52:55]\n\t" "buffer_load_dwordx4 v92, %[rx], s61 offen offset:1024 lds\n\t" "s_waitcnt lgkmcnt(4)\n\t" \
    "v_mfma_f32_16x16x32_bf16 v[56:59], a[64:67], v[104:107], v[56:59]\n\t" "ds_read_b128 v[124:127], v79 offset:512\n\t" "s_mov_b32 m0, s63\n\t" \
    "v_mfma_f32_16x16x32_bf16 v[60:63], a[68:71], v[104:107], v[60:63]\n\t" "s_add_u32 s63, s63, 0x800\n\t" "buffer_load_dwordx4 v93, %[rx], s61 offen lds\n\t" \
    "s_waitcnt lgkmcnt(4)\n\t" "v_mfma_f32_16x16x32_bf16 v[48:51], a[72:75], v[108:111], v[48:51]\n\t" "ds_read_b128 v[128:131], v79 offset:33280\n\t" \
    "v_mfma_f32_16x16x32_bf16 v[52:55], a[76:79], v[108:111], v[52:55]\n\t" "buffer_load_dwordx4 v93, %[rx], s61 offen offset:1024 lds\n\t" "s_waitcnt lgkmcnt(4)\n\t" \
    "v_mfma_f32_16x16x32_bf16 v[56:59], a[72:75], v[112:115], v[56:59]\n\t" "ds_read_b128 v[100:103], v76 offset:768\n\t" "s_mov_b32 m0, s63\n\t" \
    "v_mfma_f32_16x16x32_bf16 v[60:63], a[76:79], v[112:115], v[60:63]\n\t" "s_add_u32 s63, s63, 0x800\n\t" "buffer_load_dwordx4 v94, %[rx], s61 offen lds\n\t" \
    "s_waitcnt lgkmcnt(4)\n\t" "v_mfma_f32_16x16x32_bf16 v[48:51], a[80:83], v[116:119], v[48:51]\n\t" "ds_read_b128 v[104:107], v76 offset:33536\n\t" \
    "v_mfma_f32_16x16x32_bf16 v[52:55], a[84:87], v[116:119], v[52:55]\n\t" "buffer_load_dwordx4 v94, %[rx], s61 offen offset:1024 lds\n\t" "s_waitcnt lgkmcnt(4)\n\t" \
    "v_mfma_f32_16x16x32_bf16 v[56:59], a[80:83], v[120:123], v[56:59]\n\t" "ds_read_b128 v[108:111], v77 offset:768\n\t" "s_mov_b32 m0, s63\n\t" \
    "v_mfma_f32_16x16x32_bf16 v[60:63], a[84:87], v[120:123], v[60:63]\n\t" "s_add_u32 s63, s63, 0x800\n\t" "buffer_load_dwordx4 v95, %[rx], s61 offen lds\n\t" \
    "s_waitcnt lgkmcnt(4)\n\t" "v_mfma_f32_16x16x32_bf16 v[48:51], a[88:91], v[124:127], v[48:51]\n\t" "ds_read_b128 v[112:115], v77 offset:33536\n\t" \
    "v_mfma_f32_16x16x32_bf16 v[52:55], a[92:95], v[124:127], v[52:55]\n\t" "buffer_load_dwordx4 v95, %[rx], s61 offen offset:1024 lds\n\t" "s_waitcnt lgkmcnt(4)\n\t" \
    "v_mfma_f32_16x16x32_bf16 v[56:59], a[88:91], v[128:131], v[56:59]\n\t" "ds_read_b128 v[116:119], v78 offset:768\n\t" \
    "v_mfma_f32_16x16x32_bf16 v[60:63], a[92:95], v[128:131], v[60:63]\n\t" "s_waitcnt lgkmcnt(4)\n\t" "v_mfma_f32_16x16x32_bf16 v[48:51], a[96:99], v[100:103], v[48:51]\n\t" \
    "ds_read_b128 v[120:123], v78 offset:33536\n\t" "s_lshr_b32 s64, s60, 2\n\t" "s_xor_b32 s64, s64, %[xp]\n\t" \
    "v_mfma_f32_16x16x32_bf16 v[52:55], a[100:103], v[100:103], v[52:55]\n\t" "s_and_b32 s64, s64, 1\n\t" "s_lshl_b32 s64, s64, 13\n\t" "s_waitcnt lgkmcnt(4)\n\t" \
    "v_mfma_f32_16x16x32_bf16 v[56:59], a[96:99], v[104:107], v[56:59]\n\t" "ds_read_b128 v[124:127], v79 offset:768\n\t" "s_lshl_b32 s62, s60, 19\n\t" \
    "v_mfma_f32_16x16x32_bf16 v[60:63], a[100:103], v[104:107], v[60:63]\n\t" "s_add_u32 s62, s62, s64\n\t" "s_add_u32 s62, s62, %[ob]\n\t" "s_waitcnt lgkmcnt(4)\n\t" \
    "v_mfma_f32_16x16x32_bf16 v[48:51], a[104:107], v[108:111], v[48:51]\n\t" "ds_read_b128 v[128:131], v79 offset:33536\n\t" \
    "v_mfma_f32_16x16x32_bf16 v[52:55], a[108:111], v[108:111], v[52:55]\n\t" "s_waitcnt lgkmcnt(4)\n\t" \
    "v_mfma_f32_16x16x32_bf16 v[56:59], a[104:107], v[112:115], v[56:59]\n\t" "ds_read_b128 v[100:103], v76 offset:1024\n\t" \
    "v_mfma_f32_16x16x32_bf16 v[60:63], a[108:111], v[112:115], v[60:63]\n\t" "s_waitcnt lgkmcnt(4)\n\t" \
    "v_mfma_f32_16x16x32_bf16 v[48:51], a[112:115], v[116:119], v[48:51]\n\t" "ds_read_b128 v[104:107], v76 offset:33792\n\t" \
    "v_mfma_f32_16x16x32_bf16 v[52:55], a[116:119], v[116:119], v[52:55]\n\t" "s_waitcnt lgkmcnt(4)\n\t" \
    "v_mfma_f32_16x16x32_bf16 v[56:59], a[112:115], v[120:123], v[56:59]\n\t" "ds_read_b128 v[108:111], v77 offset:1024\n\t" \
    "v_mfma_f32_16x16x32_bf16 v[60:63], a[116:119], v[120:123], v[60:63]\n\t" "s_waitcnt lgkmcnt(4)\n\t" \
    "v_mfma_f32_16x16x32_bf16 v[48:51], a[120:123], v[124:127], v[48:51]\n\t" "ds_read_b128 v[112:115], v77 offset:33792\n\t" \
    "v_mfma_f32_16x16x32_bf16 v[52:55], a[124:127], v[124:127], v[52:55]\n\t" "s_waitcnt lgkmcnt(4)\n\t" \
    "v_mfma_f32_16x16x32_bf16 v[56:59], a[120:123], v[128:131], v[56:59]\n\t" "ds_read_b128 v[116:119], v78 offset:1024\n\t" \
    "v_mfma_f32_16x16x32_bf16 v[60:63], a[124:127], v[128:131], v[60:63]\n\t" "s_waitcnt lgkmcnt(4)\n\t" \
    "v_mfma_f32_16x16x32_bf16 v[48:51], a[128:131], v[100:103], v[48:51]\n\t" "ds_read_b128 v[120:123], v78 offset:33792\n\t" \
    "v_mfma_f32_16x16x32_bf16 v[52:55], a[132:135], v[100:103], v[52:55]\n\t" "s_waitcnt lgkmcnt(4)\n\t" \
    "v_mfma_f32_16x16x32_bf16 v[56:59], a[128:131], v[104:107], v[56:59]\n\t" "ds_read_b128 v[124:127], v79 offset:1024\n\t" \
    "v_mfma_f32_16x16x32_bf16 v[60:63], a[132:135], v[104:107], v[60:63]\n\t" "s_waitcnt lgkmcnt(4)\n\t" \
    "v_mfma_f32_16x16x32_bf16 v[48:51], a[136:139], v[108:111], v[48:51]\n\t" "ds_read_b128 v[128:131], v79 offset:33792\n\t" "s_waitcnt vmcnt(16)\n\t" "s_barrier\n\t" \
    "v_mfma_f32_16x16x32_bf16 v[52:55], a[140:143], v[108:111], v[52:55]\n\t" "v_mov_b32 v98, s60\n\t" "buffer_store_dword v98, %[fo], %[rc], %[fl] offen sc1\n\t" \
    "s_waitcnt lgkmcnt(4)\n\t" "v_mfma_f32_16x16x32_bf16 v[56:59], a[136:139], v[112:115], v[56:59]\n\t" "ds_read_b128 v[100:103], v76 offset:1280\n\t" \
    "v_mfma_f32_16x16x32_bf16 v[60:63], a[140:143], v[112:115], v[60:63]\n\t" "s_waitcnt lgkmcnt(4)\n\t" \
    "v_mfma_f32_16x16x32_bf16 v[48:51], a[144:147], v[116:119], v[48:51]\n\t" "ds_read_b128 v[104:107], v76 offset:34048\n\t" \
    "v_mfma_f32_16x16x32_bf16 v[52:55], a[148:151], v[116:119], v[52:55]\n\t" "s_waitcnt lgkmcnt(4)\n\t" \
    "v_mfma_f32_16x16x32_bf16 v[56:59], a[144:147], v[120:123], v[56:59]\n\t" "ds_read_b128 v[108:111], v77 offset:1280\n\t" \
    "v_mfma_f32_16x16x32_bf16 v[60:63], a[148:151], v[120:123], v[60:63]\n\t" "s_waitcnt lgkmcnt(4)\n\t" \
    "v_mfma_f32_16x16x32_bf16 v[48:51], a[152:155], v[124:127], v[48:51]\n\t" "ds_read_b128 v[112:115], v77 offset:34048\n\t" \
    "v_mfma_f32_16x16x32_bf16 v[52:55], a[156:159], v[124:127], v[52:55]\n\t" "s_waitcnt lgkmcnt(4)\n\t" \
    "v_mfma_f32_16x16x32_bf16 v[56:59], a[152:155], v[128:131], v[56:59]\n\t" "ds_read_b128 v[116:119], v78 offset:1280\n\t" \
    "v_mfma_f32_16x16x32_bf16 v[60:63], a[156:159], v[128:131], v[60:63]\n\t" "s_waitcnt lgkmcnt(4)\n\t" \
    "v_mfma_f32_16x16x32_bf16 v[48:51], a[160:163], v[100:103], v[48:51]\n\t" "ds_read_b128 v[120:123], v78 offset:34048\n\t" \
    "v_mfma_f32_16x16x32_bf16 v[52:55], a[164:167], v[100:103], v[52:55]\n\t" "s_waitcnt lgkmcnt(4)\n\t" \
    "v_mfma_f32_16x16x32_bf16 v[56:59], a[160:163], v[104:107], v[56:59]\n\t" "ds_read_b128 v[124:127], v79 offset:1280\n\t" \
    "v_mfma_f32_16x16x32_bf16 v[60:63], a[164:167], v[104:107], v[60:63]\n\t" "s_waitcnt lgkmcnt(4)\n\t" \
    "v_mfma_f32_16x16x32_bf16 v[48:51], a[168:171], v[108:111], v[48:51]\n\t" "ds_read_b128 v[128:131], v79 offset:34048\n\t" \
    "v_mfma_f32_16x16x32_bf16 v[52:55], a[172:175], v[108:111], v[52:55]\n\t" "s_waitcnt lgkmcnt(4)\n\t" \
    "v_mfma_f32_16x16x32_bf16 v[56:59], a[168:171], v[112:115], v[56:59]\n\t" "ds_read_b128 v[100:103], v76 offset:1536\n\t" \
    "v_mfma_f32_16x16x32_bf16 v[60:63], a[172:175], v[112:115], v[60:63]\n\t" "s_waitcnt lgkmcnt(4)\n\t" \
    "v_mfma_f32_16x16x32_bf16 v[48:51], a[176:179], v[116:119], v[48:51]\n\t" "ds_read_b128 v[104:107], v76 offset:34304\n\t" \
    "v_mfma_f32_16x16x32_bf16 v[52:55], a[180:183], v[116:119], v[52:55]\n\t" "s_waitcnt lgkmcnt(4)\n\t" \
    "v_mfma_f32_16x16x32_bf16 v[56:59], a[176:179], v[120:123], v[56:59]\n\t" "ds_read_b128 v[108:111], v77 offset:1536\n\t" \
    "v_mfma_f32_16x16x32_bf16 v[60:63], a[180:183], v[120:123], v[60:63]\n\t" "s_waitcnt lgkmcnt(4)\n\t" \
    "v_mfma_f32_16x16x32_bf16 v[48:51], a[184:187], v[124:127], v[48:51]\n\t" "ds_read_b128 v[112:115], v77 offset:34304\n\t" \
    "v_mfma_f32_16x16x32_bf16 v[52:55], a[188:191], v[124:127], v[52:55]\n\t" "s_waitcnt lgkmcnt(4)\n\t" \
    "v_mfma_f32_16x16x32_bf16 v[56:59], a[184:187], v[128:131], v[56:59]\n\t" "ds_read_b128 v[116:119], v78 offset:1536\n\t" \
    "v_mfma_f32_16x16x32_bf16 v[60:63], a[188:191], v[128:131], v[60:63]\n\t" "s_waitcnt lgkmcnt(4)\n\t" \
    "v_mfma_f32_16x16x32_bf16 v[48:51], a[192:195], v[100:103], v[48:51]\n\t" "ds_read_b128 v[120:123], v78 offset:34304\n\t" \
    "v_mfma_f32_16x16x32_bf16 v[52:55], a[196:199], v[100:103], v[52:55]\n\t" "s_waitcnt lgkmcnt(4)\n\t" \
    "v_mfma_f32_16x16x32_bf16 v[56:59], a[192:195], v[104:107], v[56:59]\n\t" "ds_read_b128 v[124:127], v79 offset:1536\n\t" \
    "v_mfma_f32_16x16x32_bf16 v[60:63], a[196:199], v[104:107], v[60:63]\n\t" "s_waitcnt lgkmcnt(4)\n\t" \
    "v_mfma_f32_16x16x32_bf16 v[48:51], a[200:203], v[108:111], v[48:51]\n\t" "ds_read_b128 v[128:131], v79 offset:34304\n\t" \
    "v_mfma_f32_16x16x32_bf16 v[52:55], a[204:207], v[108:111], v[52:55]\n\t" "s_waitcnt lgkmcnt(4)\n\t" \
    "v_mfma_f32_16x16x32_bf16 v[56:59], a[200:203], v[112:115], v[56:59]\n\t" "ds_read_b128 v[100:103], v76 offset:1792\n\t" \
    "v_mfma_f32_16x16x32_bf16 v[60:63], a[204:207], v[112:115], v[60:63]\n\t" "s_waitcnt lgkmcnt(4)\n\t" \
    "v_mfma_f32_16x16x32_bf16 v[48:51], a[208:211], v[116:119], v[48:51]\n\t" "ds_read_b128 v[104:107], v76 offset:34560\n\t" \
    "v_mfma_f32_16x16x32_bf16 v[52:55], a[212:215], v[116:119], v[52:55]\n\t" "s_waitcnt lgkmcnt(4)\n\t" \
    "v_mfma_f32_16x16x32_bf16 v[56:59], a[208:211], v[120:123], v[56:59]\n\t" "ds_read_b128 v[108:111], v77 offset:1792\n\t" \
    "v_mfma_f32_16x16x32_bf16 v[60:63], a[212:215], v[120:123], v[60:63]\n\t" "s_waitcnt lgkmcnt(4)\n\t" \
    "v_mfma_f32_16x16x32_bf16 v[48:51], a[216:219], v[124:127], v[48:51]\n\t" "ds_read_b128 v[112:115], v77 offset:34560\n\t" \
    "v_mfma_f32_16x16x32_bf16 v[52:55], a[220:223], v[124:127], v[52:55]\n\t" "s_waitcnt lgkmcnt(4)\n\t" \
    "v_mfma_f32_16x16x32_bf16 v[56:59], a[216:219], v[128:131], v[56:59]\n\t" "ds_read_b128 v[116:119], v78 offset:1792\n\t" \
    "v_mfma_f32_16x16x32_bf16 v[60:63], a[220:223], v[128:131], v[60:63]\n\t" "s_waitcnt lgkmcnt(4)\n\t" \
    "v_mfma_f32_16x16x32_bf16 v[48:51], a[224:227], v[100:103], v[48:51]\n\t" "ds_read_b128 v[120:123], v78 offset:34560\n\t" \
    "v_mfma_f32_16x16x32_bf16 v[52:55], a[228:231], v[100:103], v[52:55]\n\t" "s_waitcnt lgkmcnt(4)\n\t" \
    "v_mfma_f32_16x16x32_bf16 v[56:59], a[224:227], v[104:107], v[56:59]\n\t" "ds_read_b128 v[124:127], v79 offset:1792\n\t" \
    "v_mfma_f32_16x16x32_bf16 v[60:63], a[228:231], v[104:107], v[60:63]\n\t" "s_waitcnt lgkmcnt(4)\n\t" \
    "v_mfma_f32_16x16x32_bf16 v[48:51], a[232:235], v[108:111], v[48:51]\n\t" "ds_read_b128 v[128:131], v79 offset:34560\n\t" \
    "v_mfma_f32_16x16x32_bf16 v[52:55], a[236:239], v[108:111], v[52:55]\n\t" "s_waitcnt lgkmcnt(4)\n\t" \
    "v_mfma_f32_16x16x32_bf16 v[56:59], a[232:235], v[112:115], v[56:59]\n\t" "v_mfma_f32_16x16x32_bf16 v[60:63], a[236:239], v[112:115], v[60:63]\n\t" \
    "s_waitcnt lgkmcnt(3)\n\t" "v_mfma_f32_16x16x32_bf16 v[48:51], a[240:243], v[116:119], v[48:51]\n\t" \
    "v_mfma_f32_16x16x32_bf16 v[52:55], a[244:247], v[116:119], v[52:55]\n\t" "s_waitcnt vmcnt(1) lgkmcnt(0)\n\t" "s_barrier\n\t" XT(82, 83) \
    "v_mfma_f32_16x16x32_bf16 v[56:59], a[240:243], v[120:123], v[56:59]\n\t" "ds_read_b128 v[100:103], v72 offset:0\n\t" \
    "v_mfma_f32_16x16x32_bf16 v[60:63], a[244:247], v[120:123], v[60:63]\n\t" "v_mfma_f32_16x16x32_bf16 v[48:51], a[248:251], v[124:127], v[48:51]\n\t" \
    "ds_read_b128 v[104:107], v72 offset:32768\n\t" "v_mfma_f32_16x16x32_bf16 v[52:55], a[252:255], v[124:127], v[52:55]\n\t" "ds_read_b128 v[108:111], v73 offset:0\n\t" \
    "v_mfma_f32_16x16x32_bf16 v[56:59], a[248:251], v[128:131], v[56:59]\n\t" "ds_read_b128 v[112:115], v73 offset:32768\n\t" "ds_read_b128 v[116:119], v74 offset:0\n\t" \
    "v_mfma_f32_16x16x32_bf16 v[60:63], a[252:255], v[128:131], v[60:63]\n\t" "s_add_u32 s60, s60, 1\n\t" GSUM "s_cmp_lt_u32 s60, 64\n\t" "s_cbranch_scc1 10b\n\t" \
    "s_nop 7\n\t" "v_cvt_pk_bf16_f32 v64, v48, v49\n\t" "v_cvt_pk_bf16_f32 v65, v50, v51\n\t" "v_cvt_pk_bf16_f32 v66, v52, v53\n\t" "v_cvt_pk_bf16_f32 v67, v54, v55\n\t" \
    "v_cvt_pk_bf16_f32 v68, v56, v57\n\t" "v_cvt_pk_bf16_f32 v69, v58, v59\n\t" "v_cvt_pk_bf16_f32 v70, v60, v61\n\t" "v_cvt_pk_bf16_f32 v71, v62, v63\n\t" \
    "buffer_store_dwordx4 v[64:67], v96, %[rs], s62 offen offset:0 sc1\n\t" "buffer_store_dwordx4 v[68:71], v96, %[rs], s62 offen offset:256 sc1\n\t" "s_waitcnt vmcnt(0)\n\t" \
    "s_barrier\n\t" "v_mov_b32 v98, s60\n\t" "buffer_store_dword v98, %[fo], %[rc], %[fl] offen sc1\n\t" "s_waitcnt lgkmcnt(0)\n\t" "s_branch 30f\n\t" "20:\n\t" \
    "v_mov_b32 v96, %[so]\n\t" "s_nop 7\n\t" "v_cvt_pk_bf16_f32 v64, v32, v33\n\t" "v_cvt_pk_bf16_f32 v65, v34, v35\n\t" "v_cvt_pk_bf16_f32 v66, v36, v37\n\t" \
    "v_cvt_pk_bf16_f32 v67, v38, v39\n\t" "v_cvt_pk_bf16_f32 v68, v40, v41\n\t" "v_cvt_pk_bf16_f32 v69, v42, v43\n\t" "v_cvt_pk_bf16_f32 v70, v44, v45\n\t" \
    "v_cvt_pk_bf16_f32 v71, v46, v47\n\t" "buffer_store_dwordx4 v[64:67], v96, %[rs], s62 offen offset:0 sc1\n\t" \
    "buffer_store_dwordx4 v[68:71], v96, %[rs], s62 offen offset:256 sc1\n\t" "s_waitcnt vmcnt(0)\n\t" "s_barrier\n\t" "v_mov_b32 v98, s60\n\t" \
    "buffer_store_dword v98, %[fo], %[rc], %[fl] offen sc1\n\t" "s_branch 11b\n\t" "30:\n\t"
#define ZC_LOOP \
    "v_mov_b64 v[124:125], -1\n\t" "v_mov_b64 v[126:127], -1\n\t" "s_mov_b32 s60, 0\n\t" "s_mov_b32 s61, 0\n\t" "s_mov_b32 s62, 0\n\t" "s_mov_b32 s63, 0x7f000000\n\t" \
    "s_cmp_ge_u32 s61, %[z0]\n\t" "s_cbranch_scc1 52f\n\t" "51:\n\t" "buffer_load_dword v120, %[fo], %[rc], %[fv] offen sc1\n\t" \
    "buffer_load_dword v121, %[fo], %[rc], %[vd] offen sc1\n\t" "s_waitcnt vmcnt(0)\n\t" "v_readfirstlane_b32 s61, v120\n\t" "v_readfirstlane_b32 s66, v121\n\t" "s_nop 3\n\t" \
    "s_cmp_ge_u32 s61, %[z0]\n\t" "s_cbranch_scc1 52f\n\t" "s_bitcmp1_b32 s66, 1\n\t" "s_cbranch_scc1 90f\n\t" "s_cmp_eq_u32 %[bd], 0\n\t" "s_cbranch_scc1 52f\n\t" \
    "s_sub_u32 %[bd], %[bd], 1\n\t" "s_sleep 2\n\t" "s_branch 51b\n\t" "52:\n\t" "s_lshr_b32 s66, s60, 2\n\t" "s_xor_b32 s66, s66, %[xp]\n\t" "s_and_b32 s66, s66, 1\n\t" \
    "s_lshl_b32 s66, s66, 13\n\t" "s_lshl_b32 s64, s60, 19\n\t" "s_add_u32 s64, s64, s66\n\t" "s_add_u32 s64, s64, %[vb]\n\t" \
    "buffer_load_dwordx4 v[16:19], %[ao], %[rs], s64 offen offset:0 sc1\n\t" "buffer_load_dwordx4 v[20:23], %[ao], %[rs], s64 offen offset:256 sc1\n\t" \
    "buffer_load_dwordx4 v[24:27], %[ao], %[rs], s64 offen offset:2048 sc1\n\t" "buffer_load_dwordx4 v[28:31], %[ao], %[rs], s64 offen offset:2304 sc1\n\t" \
    "buffer_load_dwordx4 v[32:35], %[a1], %[rs], s64 offen offset:0 sc1\n\t" "buffer_load_dwordx4 v[36:39], %[a1], %[rs], s64 offen offset:256 sc1\n\t" \
    "buffer_load_dwordx4 v[40:43], %[a1], %[rs], s64 offen offset:2048 sc1\n\t" "buffer_load_dwordx4 v[44:47], %[a1], %[rs], s64 offen offset:2304 sc1\n\t" \
    "s_waitcnt vmcnt(0)\n\t" "10:\n\t" XT(80, 81) "s_add_u32 s66, s62, %[rg]\n\t" "s_cmp_lt_u32 s66, s60\n\t" "s_cbranch_scc1 60f\n\t" "63:\n\t" \
    "v_mfma_f32_16x16x32_bf16 v[80:83], v[16:19], a[0:3], 0\n\t" "s_add_u32 s66, s60, 1\n\t" "s_and_b32 s66, s66, %[rm]\n\t" \
    "v_mfma_f32_16x16x32_bf16 v[84:87], v[20:23], a[0:3], 0\n\t" "s_lshl_b32 s68, s66, 20\n\t" "s_lshr_b32 s66, s66, 1\n\t" \
    "v_mfma_f32_16x16x32_bf16 v[80:83], v[24:27], a[4:7], v[80:83]\n\t" "s_xor_b32 s66, s66, %[xp]\n\t" "s_andn2_b32 s66, 1, s66\n\t" \
    "v_mfma_f32_16x16x32_bf16 v[84:87], v[28:31], a[4:7], v[84:87]\n\t" "s_lshl_b32 s66, s66, 13\n\t" "s_add_u32 s68, s68, s66\n\t" \
    "v_mfma_f32_16x16x32_bf16 v[80:83], v[32:35], a[8:11], v[80:83]\n\t" "v_cvt_pk_bf16_f32 v112, v96, v97\n\t" \
    "v_mfma_f32_16x16x32_bf16 v[84:87], v[36:39], a[8:11], v[84:87]\n\t" "v_cvt_pk_bf16_f32 v113, v98, v99\n\t" \
    "v_mfma_f32_16x16x32_bf16 v[80:83], v[40:43], a[12:15], v[80:83]\n\t" "v_cvt_pk_bf16_f32 v114, v100, v101\n\t" \
    "v_mfma_f32_16x16x32_bf16 v[84:87], v[44:47], a[12:15], v[84:87]\n\t" "v_cvt_pk_bf16_f32 v115, v102, v103\n\t" \
    "v_mfma_f32_16x16x32_bf16 v[88:91], v[16:19], a[16:19], 0\n\t" "v_cvt_pk_bf16_f32 v116, v104, v105\n\t" "v_mfma_f32_16x16x32_bf16 v[92:95], v[20:23], a[16:19], 0\n\t" \
    "v_cvt_pk_bf16_f32 v117, v106, v107\n\t" "v_mfma_f32_16x16x32_bf16 v[88:91], v[24:27], a[20:23], v[88:91]\n\t" \
    "buffer_store_dwordx4 v[112:115], %[z1], %[rs], s63 offen sc1\n\t" "v_mfma_f32_16x16x32_bf16 v[92:95], v[28:31], a[20:23], v[92:95]\n\t" \
    "v_mfma_f32_16x16x32_bf16 v[88:91], v[32:35], a[24:27], v[88:91]\n\t" "v_cvt_pk_bf16_f32 v118, v108, v109\n\t" \
    "v_mfma_f32_16x16x32_bf16 v[92:95], v[36:39], a[24:27], v[92:95]\n\t" "v_cvt_pk_bf16_f32 v119, v110, v111\n\t" \
    "v_mfma_f32_16x16x32_bf16 v[88:91], v[40:43], a[28:31], v[88:91]\n\t" "buffer_store_dwordx4 v[116:119], %[z2], %[rs], s63 offen sc1\n\t" "s_and_b32 s63, s60, %[rm]\n\t" \
    "v_mfma_f32_16x16x32_bf16 v[92:95], v[44:47], a[28:31], v[92:95]\n\t" "s_lshl_b32 s63, s63, 22\n\t" "s_add_u32 s63, s63, %[zb]\n\t" \
    "v_mfma_f32_16x16x32_bf16 v[96:99], v[16:19], a[32:35], 0\n\t" "buffer_load_dword v120, %[fo], %[rc], %[fv] offen sc1\n\t" \
    "v_mfma_f32_16x16x32_bf16 v[100:103], v[20:23], a[32:35], 0\n\t" "v_mfma_f32_16x16x32_bf16 v[96:99], v[24:27], a[36:39], v[96:99]\n\t" \
    "buffer_load_dword v121, %[fo], %[rc], %[fs] offen sc1\n\t" "v_mfma_f32_16x16x32_bf16 v[100:103], v[28:31], a[36:39], v[100:103]\n\t" \
    "v_mfma_f32_16x16x32_bf16 v[96:99], v[32:35], a[40:43], v[96:99]\n\t" "v_cvt_pk_bf16_f32 v112, v80, v81\n\t" \
    "v_mfma_f32_16x16x32_bf16 v[100:103], v[36:39], a[40:43], v[100:103]\n\t" "v_cvt_pk_bf16_f32 v113, v82, v83\n\t" \
    "v_mfma_f32_16x16x32_bf16 v[96:99], v[40:43], a[44:47], v[96:99]\n\t" "v_cvt_pk_bf16_f32 v114, v84, v85\n\t" \
    "v_mfma_f32_16x16x32_bf16 v[100:103], v[44:47], a[44:47], v[100:103]\n\t" "v_cvt_pk_bf16_f32 v115, v86, v87\n\t" \
    "v_mfma_f32_16x16x32_bf16 v[104:107], v[16:19], a[48:51], 0\n\t" "v_cvt_pk_bf16_f32 v116, v88, v89\n\t" "v_mfma_f32_16x16x32_bf16 v[108:111], v[20:23], a[48:51], 0\n\t" \
    "v_cvt_pk_bf16_f32 v117, v90, v91\n\t" "v_mfma_f32_16x16x32_bf16 v[104:107], v[24:27], a[52:55], v[104:107]\n\t" \
    "buffer_store_dwordx4 v[112:115], %[z1], %[rs], s63 offen sc1\n\t" "v_mfma_f32_16x16x32_bf16 v[108:111], v[28:31], a[52:55], v[108:111]\n\t" \
    "v_mfma_f32_16x16x32_bf16 v[104:107], v[32:35], a[56:59], v[104:107]\n\t" "v_cvt_pk_bf16_f32 v118, v92, v93\n\t" \
    "v_mfma_f32_16x16x32_bf16 v[108:111], v[36:39], a[56:59], v[108:111]\n\t" "v_cvt_pk_bf16_f32 v119, v94, v95\n\t" \
    "v_mfma_f32_16x16x32_bf16 v[104:107], v[40:43], a[60:63], v[104:107]\n\t" "buffer_store_dwordx4 v[116:119], %[z2], %[rs], s63 offen sc1\n\t" \
    "v_mfma_f32_16x16x32_bf16 v[108:111], v[44:47], a[60:63], v[108:111]\n\t" "s_add_u32 s63, s63, 0x20000\n\t" "v_mfma_f32_16x16x32_bf16 v[80:83], v[16:19], a[64:67], 0\n\t" \
    "s_add_u32 s65, s60, 1\n\t" "s_lshr_b32 s66, s65, 2\n\t" "v_mfma_f32_16x16x32_bf16 v[84:87], v[20:23], a[64:67], 0\n\t" "s_xor_b32 s66, s66, %[xp]\n\t" \
    "s_and_b32 s66, s66, 1\n\t" "v_mfma_f32_16x16x32_bf16 v[80:83], v[24:27], a[68:71], v[80:83]\n\t" \
    "buffer_store_dwordx4 v[124:127], %[uo], %[rs], s68 offen offset:0 sc1\n\t" "v_mfma_f32_16x16x32_bf16 v[84:87], v[28:31], a[68:71], v[84:87]\n\t" \
    "v_mfma_f32_16x16x32_bf16 v[80:83], v[32:35], a[72:75], v[80:83]\n\t" "v_cvt_pk_bf16_f32 v112, v96, v97\n\t" \
    "v_mfma_f32_16x16x32_bf16 v[84:87], v[36:39], a[72:75], v[84:87]\n\t" "v_cvt_pk_bf16_f32 v113, v98, v99\n\t" \
    "v_mfma_f32_16x16x32_bf16 v[80:83], v[40:43], a[76:79], v[80:83]\n\t" "v_cvt_pk_bf16_f32 v114, v100, v101\n\t" \
    "v_mfma_f32_16x16x32_bf16 v[84:87], v[44:47], a[76:79], v[84:87]\n\t" "v_cvt_pk_bf16_f32 v115, v102, v103\n\t" \
    "v_mfma_f32_16x16x32_bf16 v[88:91], v[16:19], a[80:83], 0\n\t" "v_cvt_pk_bf16_f32 v116, v104, v105\n\t" "v_mfma_f32_16x16x32_bf16 v[92:95], v[20:23], a[80:83], 0\n\t" \
    "v_cvt_pk_bf16_f32 v117, v106, v107\n\t" "v_mfma_f32_16x16x32_bf16 v[88:91], v[24:27], a[84:87], v[88:91]\n\t" \
    "buffer_store_dwordx4 v[112:115], %[z1], %[rs], s63 offen sc1\n\t" "v_mfma_f32_16x16x32_bf16 v[92:95], v[28:31], a[84:87], v[92:95]\n\t" \
    "v_mfma_f32_16x16x32_bf16 v[88:91], v[32:35], a[88:91], v[88:91]\n\t" "v_cvt_pk_bf16_f32 v118, v108, v109\n\t" \
    "v_mfma_f32_16x16x32_bf16 v[92:95], v[36:39], a[88:91], v[92:95]\n\t" "v_cvt_pk_bf16_f32 v119, v110, v111\n\t" \
    "v_mfma_f32_16x16x32_bf16 v[88:91], v[40:43], a[92:95], v[88:91]\n\t" "buffer_store_dwordx4 v[116:119], %[z2], %[rs], s63 offen sc1\n\t" \
    "v_mfma_f32_16x16x32_bf16 v[92:95], v[44:47], a[92:95], v[92:95]\n\t" "s_add_u32 s63, s63, 0x20000\n\t" "v_mfma_f32_16x16x32_bf16 v[96:99], v[16:19], a[96:99], 0\n\t" \
    "s_lshl_b32 s66, s66, 13\n\t" "s_lshl_b32 s64, s65, 19\n\t" "v_mfma_f32_16x16x32_bf16 v[100:103], v[20:23], a[96:99], 0\n\t" "s_add_u32 s64, s64, s66\n\t" \
    "s_add_u32 s64, s64, %[vb]\n\t" "v_mfma_f32_16x16x32_bf16 v[96:99], v[24:27], a[100:103], v[96:99]\n\t" \
    "buffer_store_dwordx4 v[124:127], %[uo], %[rs], s68 offen offset:1024 sc1\n\t" "v_mfma_f32_16x16x32_bf16 v[100:103], v[28:31], a[100:103], v[100:103]\n\t" \
    "v_mfma_f32_16x16x32_bf16 v[96:99], v[32:35], a[104:107], v[96:99]\n\t" "v_cvt_pk_bf16_f32 v112, v80, v81\n\t" \
    "v_mfma_f32_16x16x32_bf16 v[100:103], v[36:39], a[104:107], v[100:103]\n\t" "v_cvt_pk_bf16_f32 v113, v82, v83\n\t" \
    "v_mfma_f32_16x16x32_bf16 v[96:99], v[40:43], a[108:111], v[96:99]\n\t" "v_cvt_pk_bf16_f32 v114, v84, v85\n\t" \
    "v_mfma_f32_16x16x32_bf16 v[100:103], v[44:47], a[108:111], v[100:103]\n\t" "v_cvt_pk_bf16_f32 v115, v86, v87\n\t" \
    "v_mfma_f32_16x16x32_bf16 v[104:107], v[16:19], a[112:115], 0\n\t" "v_cvt_pk_bf16_f32 v116, v88, v89\n\t" \
    "v_mfma_f32_16x16x32_bf16 v[108:111], v[20:23], a[112:115], 0\n\t" "v_cvt_pk_bf16_f32 v117, v90, v91\n\t" \
    "v_mfma_f32_16x16x32_bf16 v[104:107], v[24:27], a[116:119], v[104:107]\n\t" "buffer_store_dwordx4 v[112:115], %[z1], %[rs], s63 offen sc1\n\t" \
    "v_mfma_f32_16x16x32_bf16 v[108:111], v[28:31], a[116:119], v[108:111]\n\t" "v_mfma_f32_16x16x32_bf16 v[104:107], v[32:35], a[120:123], v[104:107]\n\t" \
    "v_cvt_pk_bf16_f32 v118, v92, v93\n\t" "v_mfma_f32_16x16x32_bf16 v[108:111], v[36:39], a[120:123], v[108:111]\n\t" "v_cvt_pk_bf16_f32 v119, v94, v95\n\t" \
    "v_mfma_f32_16x16x32_bf16 v[104:107], v[40:43], a[124:127], v[104:107]\n\t" "buffer_store_dwordx4 v[116:119], %[z2], %[rs], s63 offen sc1\n\t" \
    "v_mfma_f32_16x16x32_bf16 v[108:111], v[44:47], a[124:127], v[108:111]\n\t" "s_add_u32 s63, s63, 0x20000\n\t" \
    "v_mfma_f32_16x16x32_bf16 v[80:83], v[16:19], a[128:131], 0\n\t" "s_waitcnt vmcnt(8)\n\t" "v_readfirstlane_b32 s66, v120\n\t" "v_readfirstlane_b32 s67, v121\n\t" \
    "v_mfma_f32_16x16x32_bf16 v[84:87], v[20:23], a[128:131], 0\n\t" "v_add_u32 v122, s60, %[at]\n\t" "buffer_store_dword v122, %[fo], %[rc], %[fz] offen sc1\n\t" \
    "v_mfma_f32_16x16x32_bf16 v[80:83], v[24:27], a[132:135], v[80:83]\n\t" "s_max_u32 s61, s61, s66\n\t" "s_max_u32 s62, s62, s67\n\t" "s_cmp_gt_u32 s61, s65\n\t" \
    "v_mfma_f32_16x16x32_bf16 v[84:87], v[28:31], a[132:135], v[84:87]\n\t" "s_cselect_b32 s65, 1, 0\n\t" "s_cselect_b32 s64, s64, 0x7f000000\n\t" \
    "v_mfma_f32_16x16x32_bf16 v[80:83], v[32:35], a[136:139], v[80:83]\n\t" "buffer_load_dwordx4 v[48:51], %[ao], %[rs], s64 offen offset:0 sc1\n\t" \
    "v_cvt_pk_bf16_f32 v112, v96, v97\n\t" "v_mfma_f32_16x16x32_bf16 v[84:87], v[36:39], a[136:139], v[84:87]\n\t" "v_cvt_pk_bf16_f32 v113, v98, v99\n\t" \
    "v_mfma_f32_16x16x32_bf16 v[80:83], v[40:43], a[140:143], v[80:83]\n\t" "buffer_load_dwordx4 v[52:55], %[ao], %[rs], s64 offen offset:256 sc1\n\t" \
    "v_cvt_pk_bf16_f32 v114, v100, v101\n\t" "v_mfma_f32_16x16x32_bf16 v[84:87], v[44:47], a[140:143], v[84:87]\n\t" "v_cvt_pk_bf16_f32 v115, v102, v103\n\t" \
    "v_mfma_f32_16x16x32_bf16 v[88:91], v[16:19], a[144:147], 0\n\t" "buffer_load_dwordx4 v[56:59], %[ao], %[rs], s64 offen offset:2048 sc1\n\t" \
    "v_cvt_pk_bf16_f32 v116, v104, v105\n\t" "v_mfma_f32_16x16x32_bf16 v[92:95], v[20:23], a[144:147], 0\n\t" "v_cvt_pk_bf16_f32 v117, v106, v107\n\t" \
    "v_mfma_f32_16x16x32_bf16 v[88:91], v[24:27], a[148:151], v[88:91]\n\t" "buffer_load_dwordx4 v[60:63], %[ao], %[rs], s64 offen offset:2304 sc1\n\t" \
    "v_mfma_f32_16x16x32_bf16 v[92:95], v[28:31], a[148:151], v[92:95]\n\t" "buffer_store_dwordx4 v[112:115], %[z1], %[rs], s63 offen sc1\n\t" \
    "v_mfma_f32_16x16x32_bf16 v[88:91], v[32:35], a[152:155], v[88:91]\n\t" "buffer_load_dwordx4 v[64:67], %[a1], %[rs], s64 offen offset:0 sc1\n\t" \
    "v_cvt_pk_bf16_f32 v118, v108, v109\n\t" "v_mfma_f32_16x16x32_bf16 v[92:95], v[36:39], a[152:155], v[92:95]\n\t" "v_cvt_pk_bf16_f32 v119, v110, v111\n\t" \
    "v_mfma_f32_16x16x32_bf16 v[88:91], v[40:43], a[156:159], v[88:91]\n\t" "buffer_load_dwordx4 v[68:71], %[a1], %[rs], s64 offen offset:256 sc1\n\t" \
    "buffer_store_dwordx4 v[116:119], %[z2], %[rs], s63 offen sc1\n\t" "v_mfma_f32_16x16x32_bf16 v[92:95], v[44:47], a[156:159], v[92:95]\n\t" \
    "s_add_u32 s63, s63, 0x20000\n\t" "v_mfma_f32_16x16x32_bf16 v[96:99], v[16:19], a[160:163], 0\n\t" \
    "buffer_load_dwordx4 v[72:75], %[a1], %[rs], s64 offen offset:2048 sc1\n\t" "v_mfma_f32_16x16x32_bf16 v[100:103], v[20:23], a[160:163], 0\n\t" \
    "v_mfma_f32_16x16x32_bf16 v[96:99], v[24:27], a[164:167], v[96:99]\n\t" "buffer_load_dwordx4 v[76:79], %[a1], %[rs], s64 offen offset:2304 sc1\n\t" \
    "v_mfma_f32_16x16x32_bf16 v[100:103], v[28:31], a[164:167], v[100:103]\n\t" "v_mfma_f32_16x16x32_bf16 v[96:99], v[32:35], a[168:171], v[96:99]\n\t" \
    "v_cvt_pk_bf16_f32 v112, v80, v81\n\t" "v_mfma_f32_16x16x32_bf16 v[100:103], v[36:39], a[168:171], v[100:103]\n\t" "v_cvt_pk_bf16_f32 v113, v82, v83\n\t" \
    "v_mfma_f32_16x16x32_bf16 v[96:99], v[40:43], a[172:175], v[96:99]\n\t" "v_cvt_pk_bf16_f32 v114, v84, v85\n\t" \
    "v_mfma_f32_16x16x32_bf16 v[100:103], v[44:47], a[172:175], v[100:103]\n\t" "v_cvt_pk_bf16_f32 v115, v86, v87\n\t" \
    "v_mfma_f32_16x16x32_bf16 v[104:107], v[16:19], a[176:179], 0\n\t" "v_cvt_pk_bf16_f32 v116, v88, v89\n\t" \
    "v_mfma_f32_16x16x32_bf16 v[108:111], v[20:23], a[176:179], 0\n\t" "v_cvt_pk_bf16_f32 v117, v90, v91\n\t" \
    "v_mfma_f32_16x16x32_bf16 v[104:107], v[24:27], a[180:183], v[104:107]\n\t" "buffer_store_dwordx4 v[112:115], %[z1], %[rs], s63 offen sc1\n\t" \
    "v_mfma_f32_16x16x32_bf16 v[108:111], v[28:31], a[180:183], v[108:111]\n\t" "v_mfma_f32_16x16x32_bf16 v[104:107], v[32:35], a[184:187], v[104:107]\n\t" \
    "v_cvt_pk_bf16_f32 v118, v92, v93\n\t" "v_mfma_f32_16x16x32_bf16 v[108:111], v[36:39], a[184:187], v[108:111]\n\t" "v_cvt_pk_bf16_f32 v119, v94, v95\n\t" \
    "v_mfma_f32_16x16x32_bf16 v[104:107], v[40:43], a[188:191], v[104:107]\n\t" "buffer_store_dwordx4 v[116:119], %[z2], %[rs], s63 offen sc1\n\t" \
    "v_mfma_f32_16x16x32_bf16 v[108:111], v[44:47], a[188:191], v[108:111]\n\t" "s_add_u32 s63, s63, 0x20000\n\t" \
    "v_mfma_f32_16x16x32_bf16 v[80:83], v[16:19], a[192:195], 0\n\t" "v_mfma_f32_16x16x32_bf16 v[84:87], v[20:23], a[192:195], 0\n\t" \
    "v_mfma_f32_16x16x32_bf16 v[80:83], v[24:27], a[196:199], v[80:83]\n\t" "buffer_store_dwordx4 v[124:127], %[uo], %[rs], s68 offen offset:2048 sc1\n\t" \
    "v_mfma_f32_16x16x32_bf16 v[84:87], v[28:31], a[196:199], v[84:87]\n\t" "v_mfma_f32_16x16x32_bf16 v[80:83], v[32:35], a[200:203], v[80:83]\n\t" \
    "v_cvt_pk_bf16_f32 v112, v96, v97\n\t" "v_mfma_f32_16x16x32_bf16 v[84:87], v[36:39], a[200:203], v[84:87]\n\t" "v_cvt_pk_bf16_f32 v113, v98, v99\n\t" \
    "v_mfma_f32_16x16x32_bf16 v[80:83], v[40:43], a[204:207], v[80:83]\n\t" "v_cvt_pk_bf16_f32 v114, v100, v101\n\t" \
    "v_mfma_f32_16x16x32_bf16 v[84:87], v[44:47], a[204:207], v[84:87]\n\t" "v_cvt_pk_bf16_f32 v115, v102, v103\n\t" \
    "v_mfma_f32_16x16x32_bf16 v[88:91], v[16:19], a[208:211], 0\n\t" "v_cvt_pk_bf16_f32 v116, v104, v105\n\t" "v_mfma_f32_16x16x32_bf16 v[92:95], v[20:23], a[208:211], 0\n\t" \
    "v_cvt_pk_bf16_f32 v117, v106, v107\n\t" "v_mfma_f32_16x16x32_bf16 v[88:91], v[24:27], a[212:215], v[88:91]\n\t" \
    "buffer_store_dwordx4 v[112:115], %[z1], %[rs], s63 offen sc1\n\t" "v_mfma_f32_16x16x32_bf16 v[92:95], v[28:31], a[212:215], v[92:95]\n\t" \
    "v_mfma_f32_16x16x32_bf16 v[88:91], v[32:35], a[216:219], v[88:91]\n\t" "v_cvt_pk_bf16_f32 v118, v108, v109\n\t" \
    "v_mfma_f32_16x16x32_bf16 v[92:95], v[36:39], a[216:219], v[92:95]\n\t" "v_cvt_pk_bf16_f32 v119, v110, v111\n\t" \
    "v_mfma_f32_16x16x32_bf16 v[88:91], v[40:43], a[220:223], v[88:91]\n\t" "buffer_store_dwordx4 v[116:119], %[z2], %[rs], s63 offen sc1\n\t" \
    "v_mfma_f32_16x16x32_bf16 v[92:95], v[44:47], a[220:223], v[92:95]\n\t" "s_add_u32 s63, s63, 0x20000\n\t" "v_mfma_f32_16x16x32_bf16 v[96:99], v[16:19], a[224:227], 0\n\t" \
    "v_mfma_f32_16x16x32_bf16 v[100:103], v[20:23], a[224:227], 0\n\t" "v_mfma_f32_16x16x32_bf16 v[96:99], v[24:27], a[228:231], v[96:99]\n\t" \
    "buffer_store_dwordx4 v[124:127], %[uo], %[rs], s68 offen offset:3072 sc1\n\t" "v_mfma_f32_16x16x32_bf16 v[100:103], v[28:31], a[228:231], v[100:103]\n\t" \
    "v_mfma_f32_16x16x32_bf16 v[96:99], v[32:35], a[232:235], v[96:99]\n\t" "v_cvt_pk_bf16_f32 v112, v80, v81\n\t" \
    "v_mfma_f32_16x16x32_bf16 v[100:103], v[36:39], a[232:235], v[100:103]\n\t" "v_cvt_pk_bf16_f32 v113, v82, v83\n\t" \
    "v_mfma_f32_16x16x32_bf16 v[96:99], v[40:43], a[236:239], v[96:99]\n\t" "v_cvt_pk_bf16_f32 v114, v84, v85\n\t" \
    "v_mfma_f32_16x16x32_bf16 v[100:103], v[44:47], a[236:239], v[100:103]\n\t" "v_cvt_pk_bf16_f32 v115, v86, v87\n\t" \
    "v_mfma_f32_16x16x32_bf16 v[104:107], v[16:19], a[240:243], 0\n\t" "v_cvt_pk_bf16_f32 v116, v88, v89\n\t" \
    "v_mfma_f32_16x16x32_bf16 v[108:111], v[20:23], a[240:243], 0\n\t" "v_cvt_pk_bf16_f32 v117, v90, v91\n\t" \
    "v_mfma_f32_16x16x32_bf16 v[104:107], v[24:27], a[244:247], v[104:107]\n\t" "buffer_store_dwordx4 v[112:115], %[z1], %[rs], s63 offen sc1\n\t" \
    "v_mfma_f32_16x16x32_bf16 v[108:111], v[28:31], a[244:247], v[108:111]\n\t" "v_mfma_f32_16x16x32_bf16 v[104:107], v[32:35], a[248:251], v[104:107]\n\t" \
    "v_cvt_pk_bf16_f32 v118, v92, v93\n\t" "v_mfma_f32_16x16x32_bf16 v[108:111], v[36:39], a[248:251], v[108:111]\n\t" "v_cvt_pk_bf16_f32 v119, v94, v95\n\t" \
    "v_mfma_f32_16x16x32_bf16 v[104:107], v[40:43], a[252:255], v[104:107]\n\t" "buffer_store_dwordx4 v[116:119], %[z2], %[rs], s63 offen sc1\n\t" \
    "s_add_u32 s63, s63, 0x20000\n\t" "s_add_u32 s60, s60, 1\n\t" "v_mfma_f32_16x16x32_bf16 v[108:111], v[44:47], a[252:255], v[108:111]\n\t" "s_cmp_eq_u32 s65, 0\n\t" \
    "s_waitcnt vmcnt(8)\n\t" ZSUM "s_cbranch_scc1 20f\n\t" "23:\n\t" XT(80, 81) "s_add_u32 s66, s62, %[rg]\n\t" "s_cmp_lt_u32 s66, s60\n\t" "s_cbranch_scc1 70f\n\t" "73:\n\t" \
    "v_mfma_f32_16x16x32_bf16 v[80:83], v[48:51], a[0:3], 0\n\t" "s_add_u32 s66, s60, 1\n\t" "s_and_b32 s66, s66, %[rm]\n\t" \
    "v_mfma_f32_16x16x32_bf16 v[84:87], v[52:55], a[0:3], 0\n\t" "s_lshl_b32 s68, s66, 20\n\t" "s_lshr_b32 s66, s66, 1\n\t" \
    "v_mfma_f32_16x16x32_bf16 v[80:83], v[56:59], a[4:7], v[80:83]\n\t" "s_xor_b32 s66, s66, %[xp]\n\t" "s_andn2_b32 s66, 1, s66\n\t" \
    "v_mfma_f32_16x16x32_bf16 v[84:87], v[60:63], a[4:7], v[84:87]\n\t" "s_lshl_b32 s66, s66, 13\n\t" "s_add_u32 s68, s68, s66\n\t" \
    "v_mfma_f32_16x16x32_bf16 v[80:83], v[64:67], a[8:11], v[80:83]\n\t" "v_cvt_pk_bf16_f32 v112, v96, v97\n\t" \
    "v_mfma_f32_16x16x32_bf16 v[84:87], v[68:71], a[8:11], v[84:87]\n\t" "v_cvt_pk_bf16_f32 v113, v98, v99\n\t" \
    "v_mfma_f32_16x16x32_bf16 v[80:83], v[72:75], a[12:15], v[80:83]\n\t" "v_cvt_pk_bf16_f32 v114, v100, v101\n\t" \
    "v_mfma_f32_16x16x32_bf16 v[84:87], v[76:79], a[12:15], v[84:87]\n\t" "v_cvt_pk_bf16_f32 v115, v102, v103\n\t" \
    "v_mfma_f32_16x16x32_bf16 v[88:91], v[48:51], a[16:19], 0\n\t" "v_cvt_pk_bf16_f32 v116, v104, v105\n\t" "v_mfma_f32_16x16x32_bf16 v[92:95], v[52:55], a[16:19], 0\n\t" \
    "v_cvt_pk_bf16_f32 v117, v106, v107\n\t" "v_mfma_f32_16x16x32_bf16 v[88:91], v[56:59], a[20:23], v[88:91]\n\t" \
    "buffer_store_dwordx4 v[112:115], %[z1], %[rs], s63 offen sc1\n\t" "v_mfma_f32_16x16x32_bf16 v[92:95], v[60:63], a[20:23], v[92:95]\n\t" \
    "v_mfma_f32_16x16x32_bf16 v[88:91], v[64:67], a[24:27], v[88:91]\n\t" "v_cvt_pk_bf16_f32 v118, v108, v109\n\t" \
    "v_mfma_f32_16x16x32_bf16 v[92:95], v[68:71], a[24:27], v[92:95]\n\t" "v_cvt_pk_bf16_f32 v119, v110, v111\n\t" \
    "v_mfma_f32_16x16x32_bf16 v[88:91], v[72:75], a[28:31], v[88:91]\n\t" "buffer_store_dwordx4 v[116:119], %[z2], %[rs], s63 offen sc1\n\t" "s_and_b32 s63, s60, %[rm]\n\t" \
    "v_mfma_f32_16x16x32_bf16 v[92:95], v[76:79], a[28:31], v[92:95]\n\t" "s_lshl_b32 s63, s63, 22\n\t" "s_add_u32 s63, s63, %[zb]\n\t" \
    "v_mfma_f32_16x16x32_bf16 v[96:99], v[48:51], a[32:35], 0\n\t" "buffer_load_dword v120, %[fo], %[rc], %[fv] offen sc1\n\t" \
    "v_mfma_f32_16x16x32_bf16 v[100:103], v[52:55], a[32:35], 0\n\t" "v_mfma_f32_16x16x32_bf16 v[96:99], v[56:59], a[36:39], v[96:99]\n\t" \
    "buffer_load_dword v121, %[fo], %[rc], %[fs] offen sc1\n\t" "v_mfma_f32_16x16x32_bf16 v[100:103], v[60:63], a[36:39], v[100:103]\n\t" \
    "v_mfma_f32_16x16x32_bf16 v[96:99], v[64:67], a[40:43], v[96:99]\n\t" "v_cvt_pk_bf16_f32 v112, v80, v81\n\t" \
    "v_mfma_f32_16x16x32_bf16 v[100:103], v[68:71], a[40:43], v[100:103]\n\t" "v_cvt_pk_bf16_f32 v113, v82, v83\n\t" \
    "v_mfma_f32_16x16x32_bf16 v[96:99], v[72:75], a[44:47], v[96:99]\n\t" "v_cvt_pk_bf16_f32 v114, v84, v85\n\t" \
    "v_mfma_f32_16x16x32_bf16 v[100:103], v[76:79], a[44:47], v[100:103]\n\t" "v_cvt_pk_bf16_f32 v115, v86, v87\n\t" \
    "v_mfma_f32_16x16x32_bf16 v[104:107], v[48:51], a[48:51], 0\n\t" "v_cvt_pk_bf16_f32 v116, v88, v89\n\t" "v_mfma_f32_16x16x32_bf16 v[108:111], v[52:55], a[48:51], 0\n\t" \
    "v_cvt_pk_bf16_f32 v117, v90, v91\n\t" "v_mfma_f32_16x16x32_bf16 v[104:107], v[56:59], a[52:55], v[104:107]\n\t" \
    "buffer_store_dwordx4 v[112:115], %[z1], %[rs], s63 offen sc1\n\t" "v_mfma_f32_16x16x32_bf16 v[108:111], v[60:63], a[52:55], v[108:111]\n\t" \
    "v_mfma_f32_16x16x32_bf16 v[104:107], v[64:67], a[56:59], v[104:107]\n\t" "v_cvt_pk_bf16_f32 v118, v92, v93\n\t" \
    "v_mfma_f32_16x16x32_bf16 v[108:111], v[68:71], a[56:59], v[108:111]\n\t" "v_cvt_pk_bf16_f32 v119, v94, v95\n\t" \
    "v_mfma_f32_16x16x32_bf16 v[104:107], v[72:75], a[60:63], v[104:107]\n\t" "buffer_store_dwordx4 v[116:119], %[z2], %[rs], s63 offen sc1\n\t" \
    "v_mfma_f32_16x16x32_bf16 v[108:111], v[76:79], a[60:63], v[108:111]\n\t" "s_add_u32 s63, s63, 0x20000\n\t" "v_mfma_f32_16x16x32_bf16 v[80:83], v[48:51], a[64:67], 0\n\t" \
    "s_add_u32 s65, s60, 1\n\t" "s_lshr_b32 s66, s65, 2\n\t" "v_mfma_f32_16x16x32_bf16 v[84:87], v[52:55], a[64:67], 0\n\t" "s_xor_b32 s66, s66, %[xp]\n\t" \
    "s_and_b32 s66, s66, 1\n\t" "v_mfma_f32_16x16x32_bf16 v[80:83], v[56:59], a[68:71], v[80:83]\n\t" \
    "buffer_store_dwordx4 v[124:127], %[uo], %[rs], s68 offen offset:0 sc1\n\t" "v_mfma_f32_16x16x32_bf16 v[84:87], v[60:63], a[68:71], v[84:87]\n\t" \
    "v_mfma_f32_16x16x32_bf16 v[80:83], v[64:67], a[72:75], v[80:83]\n\t" "v_cvt_pk_bf16_f32 v112, v96, v97\n\t" \
    "v_mfma_f32_16x16x32_bf16 v[84:87], v[68:71], a[72:75], v[84:87]\n\t" "v_cvt_pk_bf16_f32 v113, v98, v99\n\t" \
    "v_mfma_f32_16x16x32_bf16 v[80:83], v[72:75], a[76:79], v[80:83]\n\t" "v_cvt_pk_bf16_f32 v114, v100, v101\n\t" \
    "v_mfma_f32_16x16x32_bf16 v[84:87], v[76:79], a[76:79], v[84:87]\n\t" "v_cvt_pk_bf16_f32 v115, v102, v103\n\t" \
    "v_mfma_f32_16x16x32_bf16 v[88:91], v[48:51], a[80:83], 0\n\t" "v_cvt_pk_bf16_f32 v116, v104, v105\n\t" "v_mfma_f32_16x16x32_bf16 v[92:95], v[52:55], a[80:83], 0\n\t" \
    "v_cvt_pk_bf16_f32 v117, v106, v107\n\t" "v_mfma_f32_16x16x32_bf16 v[88:91], v[56:59], a[84:87], v[88:91]\n\t" \
    "buffer_store_dwordx4 v[112:115], %[z1], %[rs], s63 offen sc1\n\t" "v_mfma_f32_16x16x32_bf16 v[92:95], v[60:63], a[84:87], v[92:95]\n\t" \
    "v_mfma_f32_16x16x32_bf16 v[88:91], v[64:67], a[88:91], v[88:91]\n\t" "v_cvt_pk_bf16_f32 v118, v108, v109\n\t" \
    "v_mfma_f32_16x16x32_bf16 v[92:95], v[68:71], a[88:91], v[92:95]\n\t" "v_cvt_pk_bf16_f32 v119, v110, v111\n\t" \
    "v_mfma_f32_16x16x32_bf16 v[88:91], v[72:75], a[92:95], v[88:91]\n\t" "buffer_store_dwordx4 v[116:119], %[z2], %[rs], s63 offen sc1\n\t" \
    "v_mfma_f32_16x16x32_bf16 v[92:95], v[76:79], a[92:95], v[92:95]\n\t" "s_add_u32 s63, s63, 0x20000\n\t" "v_mfma_f32_16x16x32_bf16 v[96:99], v[48:51], a[96:99], 0\n\t" \
    "s_lshl_b32 s66, s66, 13\n\t" "s_lshl_b32 s64, s65, 19\n\t" "v_mfma_f32_16x16x32_bf16 v[100:103], v[52:55], a[96:99], 0\n\t" "s_add_u32 s64, s64, s66\n\t" \
    "s_add_u32 s64, s64, %[vb]\n\t" "v_mfma_f32_16x16x32_bf16 v[96:99], v[56:59], a[100:103], v[96:99]\n\t" \
    "buffer_store_dwordx4 v[124:127], %[uo], %[rs], s68 offen offset:1024 sc1\n\t" "v_mfma_f32_16x16x32_bf16 v[100:103], v[60:63], a[100:103], v[100:103]\n\t" \
    "v_mfma_f32_16x16x32_bf16 v[96:99], v[64:67], a[104:107], v[96:99]\n\t" "v_cvt_pk_bf16_f32 v112, v80, v81\n\t" \
    "v_mfma_f32_16x16x32_bf16 v[100:103], v[68:71], a[104:107], v[100:103]\n\t" "v_cvt_pk_bf16_f32 v113, v82, v83\n\t" \
    "v_mfma_f32_16x16x32_bf16 v[96:99], v[72:75], a[108:111], v[96:99]\n\t" "v_cvt_pk_bf16_f32 v114, v84, v85\n\t" \
    "v_mfma_f32_16x16x32_bf16 v[100:103], v[76:79], a[108:111], v[100:103]\n\t" "v_cvt_pk_bf16_f32 v115, v86, v87\n\t" \
    "v_mfma_f32_16x16x32_bf16 v[104:107], v[48:51], a[112:115], 0\n\t" "v_cvt_pk_bf16_f32 v116, v88, v89\n\t" \
    "v_mfma_f32_16x16x32_bf16 v[108:111], v[52:55], a[112:115], 0\n\t" "v_cvt_pk_bf16_f32 v117, v90, v91\n\t" \
    "v_mfma_f32_16x16x32_bf16 v[104:107], v[56:59], a[116:119], v[104:107]\n\t" "buffer_store_dwordx4 v[112:115], %[z1], %[rs], s63 offen sc1\n\t" \
    "v_mfma_f32_16x16x32_bf16 v[108:111], v[60:63], a[116:119], v[108:111]\n\t" "v_mfma_f32_16x16x32_bf16 v[104:107], v[64:67], a[120:123], v[104:107]\n\t" \
    "v_cvt_pk_bf16_f32 v118, v92, v93\n\t" "v_mfma_f32_16x16x32_bf16 v[108:111], v[68:71], a[120:123], v[108:111]\n\t" "v_cvt_pk_bf16_f32 v119, v94, v95\n\t" \
    "v_mfma_f32_16x16x32_bf16 v[104:107], v[72:75], a[124:127], v[104:107]\n\t" "buffer_store_dwordx4 v[116:119], %[z2], %[rs], s63 offen sc1\n\t" \
    "v_mfma_f32_16x16x32_bf16 v[108:111], v[76:79], a[124:127], v[108:111]\n\t" "s_add_u32 s63, s63, 0x20000\n\t" \
    "v_mfma_f32_16x16x32_bf16 v[80:83], v[48:51], a[128:131], 0\n\t" "s_waitcnt vmcnt(8)\n\t" "v_readfirstlane_b32 s66, v120\n\t" "v_readfirstlane_b32 s67, v121\n\t" \
    "v_mfma_f32_16x16x32_bf16 v[84:87], v[52:55], a[128:131], 0\n\t" "v_add_u32 v122, s60, %[at]\n\t" "buffer_store_dword v122, %[fo], %[rc], %[fz] offen sc1\n\t" \
    "v_mfma_f32_16x16x32_bf16 v[80:83], v[56:59], a[132:135], v[80:83]\n\t" "s_max_u32 s61, s61, s66\n\t" "s_max_u32 s62, s62, s67\n\t" "s_cmp_gt_u32 s61, s65\n\t" \
    "v_mfma_f32_16x16x32_bf16 v[84:87], v[60:63], a[132:135], v[84:87]\n\t" "s_cselect_b32 s65, 1, 0\n\t" "s_cselect_b32 s64, s64, 0x7f000000\n\t" \
    "v_mfma_f32_16x16x32_bf16 v[80:83], v[64:67], a[136:139], v[80:83]\n\t" "buffer_load_dwordx4 v[16:19], %[ao], %[rs], s64 offen offset:0 sc1\n\t" \
    "v_cvt_pk_bf16_f32 v112, v96, v97\n\t" "v_mfma_f32_16x16x32_bf16 v[84:87], v[68:71], a[136:139], v[84:87]\n\t" "v_cvt_pk_bf16_f32 v113, v98, v99\n\t" \
    "v_mfma_f32_16x16x32_bf16 v[80:83], v[72:75], a[140:143], v[80:83]\n\t" "buffer_load_dwordx4 v[20:23], %[ao], %[rs], s64 offen offset:256 sc1\n\t" \
    "v_cvt_pk_bf16_f32 v114, v100, v101\n\t" "v_mfma_f32_16x16x32_bf16 v[84:87], v[76:79], a[140:143], v[84:87]\n\t" "v_cvt_pk_bf16_f32 v115, v102, v103\n\t" \
    "v_mfma_f32_16x16x32_bf16 v[88:91], v[48:51], a[144:147], 0\n\t" "buffer_load_dwordx4 v[24:27], %[ao], %[rs], s64 offen offset:2048 sc1\n\t" \
    "v_cvt_pk_bf16_f32 v116, v104, v105\n\t" "v_mfma_f32_16x16x32_bf16 v[92:95], v[52:55], a[144:147], 0\n\t" "v_cvt_pk_bf16_f32 v117, v106, v107\n\t" \
    "v_mfma_f32_16x16x32_bf16 v[88:91], v[56:59], a[148:151], v[88:91]\n\t" "buffer_load_dwordx4 v[28:31], %[ao], %[rs], s64 offen offset:2304 sc1\n\t" \
    "v_mfma_f32_16x16x32_bf16 v[92:95], v[60:63], a[148:151], v[92:95]\n\t" "buffer_store_dwordx4 v[112:115], %[z1], %[rs], s63 offen sc1\n\t" \
    "v_mfma_f32_16x16x32_bf16 v[88:91], v[64:67], a[152:155], v[88:91]\n\t" "buffer_load_dwordx4 v[32:35], %[a1], %[rs], s64 offen offset:0 sc1\n\t" \
    "v_cvt_pk_bf16_f32 v118, v108, v109\n\t" "v_mfma_f32_16x16x32_bf16 v[92:95], v[68:71], a[152:155], v[92:95]\n\t" "v_cvt_pk_bf16_f32 v119, v110, v111\n\t" \
    "v_mfma_f32_16x16x32_bf16 v[88:91], v[72:75], a[156:159], v[88:91]\n\t" "buffer_load_dwordx4 v[36:39], %[a1], %[rs], s64 offen offset:256 sc1\n\t" \
    "buffer_store_dwordx4 v[116:119], %[z2], %[rs], s63 offen sc1\n\t" "v_mfma_f32_16x16x32_bf16 v[92:95], v[76:79], a[156:159], v[92:95]\n\t" \
    "s_add_u32 s63, s63, 0x20000\n\t" "v_mfma_f32_16x16x32_bf16 v[96:99], v[48:51], a[160:163], 0\n\t" \
    "buffer_load_dwordx4 v[40:43], %[a1], %[rs], s64 offen offset:2048 sc1\n\t" "v_mfma_f32_16x16x32_bf16 v[100:103], v[52:55], a[160:163], 0\n\t" \
    "v_mfma_f32_16x16x32_bf16 v[96:99], v[56:59], a[164:167], v[96:99]\n\t" "buffer_load_dwordx4 v[44:47], %[a1], %[rs], s64 offen offset:2304 sc1\n\t" \
    "v_mfma_f32_16x16x32_bf16 v[100:103], v[60:63], a[164:167], v[100:103]\n\t" "v_mfma_f32_16x16x32_bf16 v[96:99], v[64:67], a[168:171], v[96:99]\n\t" \
    "v_cvt_pk_bf16_f32 v112, v80, v81\n\t" "v_mfma_f32_16x16x32_bf16 v[100:103], v[68:71], a[168:171], v[100:103]\n\t" "v_cvt_pk_bf16_f32 v113, v82, v83\n\t" \
    "v_mfma_f32_16x16x32_bf16 v[96:99], v[72:75], a[172:175], v[96:99]\n\t" "v_cvt_pk_bf16_f32 v114, v84, v85\n\t" \
    "v_mfma_f32_16x16x32_bf16 v[100:103], v[76:79], a[172:175], v[100:103]\n\t" "v_cvt_pk_bf16_f32 v115, v86, v87\n\t" \
    "v_mfma_f32_16x16x32_bf16 v[104:107], v[48:51], a[176:179], 0\n\t" "v_cvt_pk_bf16_f32 v116, v88, v89\n\t" \
    "v_mfma_f32_16x16x32_bf16 v[108:111], v[52:55], a[176:179], 0\n\t" "v_cvt_pk_bf16_f32 v117, v90, v91\n\t" \
    "v_mfma_f32_16x16x32_bf16 v[104:107], v[56:59], a[180:183], v[104:107]\n\t" "buffer_store_dwordx4 v[112:115], %[z1], %[rs], s63 offen sc1\n\t" \
    "v_mfma_f32_16x16x32_bf16 v[108:111], v[60:63], a[180:183], v[108:111]\n\t" "v_mfma_f32_16x16x32_bf16 v[104:107], v[64:67], a[184:187], v[104:107]\n\t" \
    "v_cvt_pk_bf16_f32 v118, v92, v93\n\t" "v_mfma_f32_16x16x32_bf16 v[108:111], v[68:71], a[184:187], v[108:111]\n\t" "v_cvt_pk_bf16_f32 v119, v94, v95\n\t" \
    "v_mfma_f32_16x16x32_bf16 v[104:107], v[72:75], a[188:191], v[104:107]\n\t" "buffer_store_dwordx4 v[116:119], %[z2], %[rs], s63 offen sc1\n\t" \
    "v_mfma_f32_16x16x32_bf16 v[108:111], v[76:79], a[188:191], v[108:111]\n\t" "s_add_u32 s63, s63, 0x20000\n\t" \
    "v_mfma_f32_16x16x32_bf16 v[80:83], v[48:51], a[192:195], 0\n\t" "v_mfma_f32_16x16x32_bf16 v[84:87], v[52:55], a[192:195], 0\n\t" \
    "v_mfma_f32_16x16x32_bf16 v[80:83], v[56:59], a[196:199], v[80:83]\n\t" "buffer_store_dwordx4 v[124:127], %[uo], %[rs], s68 offen offset:2048 sc1\n\t" \
    "v_mfma_f32_16x16x32_bf16 v[84:87], v[60:63], a[196:199], v[84:87]\n\t" "v_mfma_f32_16x16x32_bf16 v[80:83], v[64:67], a[200:203], v[80:83]\n\t" \
    "v_cvt_pk_bf16_f32 v112, v96, v97\n\t" "v_mfma_f32_16x16x32_bf16 v[84:87], v[68:71], a[200:203], v[84:87]\n\t" "v_cvt_pk_bf16_f32 v113, v98, v99\n\t" \
    "v_mfma_f32_16x16x32_bf16 v[80:83], v[72:75], a[204:207], v[80:83]\n\t" "v_cvt_pk_bf16_f32 v114, v100, v101\n\t" \
    "v_mfma_f32_16x16x32_bf16 v[84:87], v[76:79], a[204:207], v[84:87]\n\t" "v_cvt_pk_bf16_f32 v115, v102, v103\n\t" \
    "v_mfma_f32_16x16x32_bf16 v[88:91], v[48:51], a[208:211], 0\n\t" "v_cvt_pk_bf16_f32 v116, v104, v105\n\t" "v_mfma_f32_16x16x32_bf16 v[92:95], v[52:55], a[208:211], 0\n\t" \
    "v_cvt_pk_bf16_f32 v117, v106, v107\n\t" "v_mfma_f32_16x16x32_bf16 v[88:91], v[56:59], a[212:215], v[88:91]\n\t" \
    "buffer_store_dwordx4 v[112:115], %[z1], %[rs], s63 offen sc1\n\t" "v_mfma_f32_16x16x32_bf16 v[92:95], v[60:63], a[212:215], v[92:95]\n\t" \
    "v_mfma_f32_16x16x32_bf16 v[88:91], v[64:67], a[216:219], v[88:91]\n\t" "v_cvt_pk_bf16_f32 v118, v108, v109\n\t" \
    "v_mfma_f32_16x16x32_bf16 v[92:95], v[68:71], a[216:219], v[92:95]\n\t" "v_cvt_pk_bf16_f32 v119, v110, v111\n\t" \
    "v_mfma_f32_16x16x32_bf16 v[88:91], v[72:75], a[220:223], v[88:91]\n\t" "buffer_store_dwordx4 v[116:119], %[z2], %[rs], s63 offen sc1\n\t" \
    "v_mfma_f32_16x16x32_bf16 v[92:95], v[76:79], a[220:223], v[92:95]\n\t" "s_add_u32 s63, s63, 0x20000\n\t" "v_mfma_f32_16x16x32_bf16 v[96:99], v[48:51], a[224:227], 0\n\t" \
    "v_mfma_f32_16x16x32_bf16 v[100:103], v[52:55], a[224:227], 0\n\t" "v_mfma_f32_16x16x32_bf16 v[96:99], v[56:59], a[228:231], v[96:99]\n\t" \
    "buffer_store_dwordx4 v[124:127], %[uo], %[rs], s68 offen offset:3072 sc1\n\t" "v_mfma_f32_16x16x32_bf16 v[100:103], v[60:63], a[228:231], v[100:103]\n\t" \
    "v_mfma_f32_16x16x32_bf16 v[96:99], v[64:67], a[232:235], v[96:99]\n\t" "v_cvt_pk_bf16_f32 v112, v80, v81\n\t" \
    "v_mfma_f32_16x16x32_bf16 v[100:103], v[68:71], a[232:235], v[100:103]\n\t" "v_cvt_pk_bf16_f32 v113, v82, v83\n\t" \
    "v_mfma_f32_16x16x32_bf16 v[96:99], v[72:75], a[236:239], v[96:99]\n\t" "v_cvt_pk_bf16_f32 v114, v84, v85\n\t" \
    "v_mfma_f32_16x16x32_bf16 v[100:103], v[76:79], a[236:239], v[100:103]\n\t" "v_cvt_pk_bf16_f32 v115, v86, v87\n\t" \
    "v_mfma_f32_16x16x32_bf16 v[104:107], v[48:51], a[240:243], 0\n\t" "v_cvt_pk_bf16_f32 v116, v88, v89\n\t" \
    "v_mfma_f32_16x16x32_bf16 v[108:111], v[52:55], a[240:243], 0\n\t" "v_cvt_pk_bf16_f32 v117, v90, v91\n\t" \
    "v_mfma_f32_16x16x32_bf16 v[104:107], v[56:59], a[244:247], v[104:107]\n\t" "buffer_store_dwordx4 v[112:115], %[z1], %[rs], s63 offen sc1\n\t" \
    "v_mfma_f32_16x16x32_bf16 v[108:111], v[60:63], a[244:247], v[108:111]\n\t" "v_mfma_f32_16x16x32_bf16 v[104:107], v[64:67], a[248:251], v[104:107]\n\t" \
    "v_cvt_pk_bf16_f32 v118, v92, v93\n\t" "v_mfma_f32_16x16x32_bf16 v[108:111], v[68:71], a[248:251], v[108:111]\n\t" "v_cvt_pk_bf16_f32 v119, v94, v95\n\t" \
    "v_mfma_f32_16x16x32_bf16 v[104:107], v[72:75], a[252:255], v[104:107]\n\t" "buffer_store_dwordx4 v[116:119], %[z2], %[rs], s63 offen sc1\n\t" \
    "s_add_u32 s63, s63, 0x20000\n\t" "s_add_u32 s60, s60, 1\n\t" "v_mfma_f32_16x16x32_bf16 v[108:111], v[76:79], a[252:255], v[108:111]\n\t" "s_cmp_eq_u32 s65, 0\n\t" \
    "s_waitcnt vmcnt(8)\n\t" ZSUM "s_cbranch_scc1 30f\n\t" "33:\n\t" "s_branch 10b\n\t" "20:\n\t" "s_nop 7\n\t" "v_cvt_pk_bf16_f32 v112, v96, v97\n\t" \
    "v_cvt_pk_bf16_f32 v113, v98, v99\n\t" "v_cvt_pk_bf16_f32 v114, v100, v101\n\t" "v_cvt_pk_bf16_f32 v115, v102, v103\n\t" "v_cvt_pk_bf16_f32 v116, v104, v105\n\t" \
    "v_cvt_pk_bf16_f32 v117, v106, v107\n\t" "v_cvt_pk_bf16_f32 v118, v108, v109\n\t" "v_cvt_pk_bf16_f32 v119, v110, v111\n\t" \
    "buffer_store_dwordx4 v[112:115], %[z1], %[rs], s63 offen sc1\n\t" "buffer_store_dwordx4 v[116:119], %[z2], %[rs], s63 offen sc1\n\t" \
    "buffer_load_dword v120, %[fo], %[rc], %[fv] offen sc1\n\t" "s_waitcnt vmcnt(0)\n\t" "v_readfirstlane_b32 s61, v120\n\t" "s_lshr_b32 s66, s60, 2\n\t" \
    "s_xor_b32 s66, s66, %[xp]\n\t" "s_and_b32 s66, s66, 1\n\t" "s_lshl_b32 s66, s66, 13\n\t" "s_lshl_b32 s64, s60, 19\n\t" "s_add_u32 s64, s64, s66\n\t" \
    "s_add_u32 s64, s64, %[vb]\n\t" "s_cmp_gt_u32 s61, s60\n\t" "s_cbranch_scc0 24f\n\t" "s_cmp_eq_u32 s60, 64\n\t" "s_cbranch_scc1 24f\n\t" \
    "buffer_load_dwordx4 v[48:51], %[ao], %[rs], s64 offen offset:0 sc1\n\t" "buffer_load_dwordx4 v[52:55], %[ao], %[rs], s64 offen offset:256 sc1\n\t" \
    "buffer_load_dwordx4 v[56:59], %[ao], %[rs], s64 offen offset:2048 sc1\n\t" "buffer_load_dwordx4 v[60:63], %[ao], %[rs], s64 offen offset:2304 sc1\n\t" \
    "buffer_load_dwordx4 v[64:67], %[a1], %[rs], s64 offen offset:0 sc1\n\t" "buffer_load_dwordx4 v[68:71], %[a1], %[rs], s64 offen offset:256 sc1\n\t" \
    "buffer_load_dwordx4 v[72:75], %[a1], %[rs], s64 offen offset:2048 sc1\n\t" "buffer_load_dwordx4 v[76:79], %[a1], %[rs], s64 offen offset:2304 sc1\n\t" \
    "v_add_u32 v122, s60, %[at]\n\t" "buffer_store_dword v122, %[fo], %[rc], %[fz] offen sc1\n\t" "s_waitcnt vmcnt(1)\n\t" "s_branch 23b\n\t" "24:\n\t" \
    "v_add_u32 v122, s60, %[at]\n\t" "buffer_store_dword v122, %[fo], %[rc], %[fz] offen sc1\n\t" "s_cmp_eq_u32 s60, 64\n\t" "s_cbranch_scc1 90f\n\t" \
    "s_add_u32 s67, s60, 1\n\t" "s_cmp_ge_u32 s61, s67\n\t" "s_cbranch_scc1 22f\n\t" "21:\n\t" "buffer_load_dword v120, %[fo], %[rc], %[fv] offen sc1\n\t" \
    "buffer_load_dword v121, %[fo], %[rc], %[vd] offen sc1\n\t" "s_waitcnt vmcnt(0)\n\t" "v_readfirstlane_b32 s61, v120\n\t" "v_readfirstlane_b32 s66, v121\n\t" "s_nop 3\n\t" \
    "s_cmp_ge_u32 s61, s67\n\t" "s_cbranch_scc1 22f\n\t" "s_bitcmp1_b32 s66, 1\n\t" "s_cbranch_scc1 90f\n\t" "s_cmp_eq_u32 %[bd], 0\n\t" "s_cbranch_scc1 22f\n\t" \
    "s_sub_u32 %[bd], %[bd], 1\n\t" "s_sleep 2\n\t" "s_branch 21b\n\t" "22:\n\t" "s_lshr_b32 s66, s60, 2\n\t" "s_xor_b32 s66, s66, %[xp]\n\t" "s_and_b32 s66, s66, 1\n\t" \
    "s_lshl_b32 s66, s66, 13\n\t" "s_lshl_b32 s64, s60, 19\n\t" "s_add_u32 s64, s64, s66\n\t" "s_add_u32 s64, s64, %[vb]\n\t" \
    "buffer_load_dwordx4 v[48:51], %[ao], %[rs], s64 offen offset:0 sc1\n\t" "buffer_load_dwordx4 v[52:55], %[ao], %[rs], s64 offen offset:256 sc1\n\t" \
    "buffer_load_dwordx4 v[56:59], %[ao], %[rs], s64 offen offset:2048 sc1\n\t" "buffer_load_dwordx4 v[60:63], %[ao], %[rs], s64 offen offset:2304 sc1\n\t" \
    "buffer_load_dwordx4 v[64:67], %[a1], %[rs], s64 offen offset:0 sc1\n\t" "buffer_load_dwordx4 v[68:71], %[a1], %[rs], s64 offen offset:256 sc1\n\t" \
    "buffer_load_dwordx4 v[72:75], %[a1], %[rs], s64 offen offset:2048 sc1\n\t" "buffer_load_dwordx4 v[76:79], %[a1], %[rs], s64 offen offset:2304 sc1\n\t" \
    "s_waitcnt vmcnt(0)\n\t" "s_branch 23b\n\t" "60:\n\t" "s_sub_u32 s67, s60, %[rg]\n\t" "s_cmp_ge_u32 s62, s67\n\t" "s_cbranch_scc1 62f\n\t" "61:\n\t" \
    "buffer_load_dword v120, %[fo], %[rc], %[fs] offen sc1\n\t" "buffer_load_dword v121, %[fo], %[rc], %[vd] offen sc1\n\t" "s_waitcnt vmcnt(0)\n\t" \
    "v_readfirstlane_b32 s62, v120\n\t" "v_readfirstlane_b32 s66, v121\n\t" "s_nop 3\n\t" "s_cmp_ge_u32 s62, s67\n\t" "s_cbranch_scc1 62f\n\t" "s_bitcmp1_b32 s66, 1\n\t" \
    "s_cbranch_scc1 90f\n\t" "s_cmp_eq_u32 %[bd], 0\n\t" "s_cbranch_scc1 62f\n\t" "s_sub_u32 %[bd], %[bd], 1\n\t" "s_sleep 2\n\t" "s_branch 61b\n\t" "62:\n\t" \
    "s_branch 63b\n\t" "30:\n\t" "s_nop 7\n\t" "v_cvt_pk_bf16_f32 v112, v96, v97\n\t" "v_cvt_pk_bf16_f32 v113, v98, v99\n\t" "v_cvt_pk_bf16_f32 v114, v100, v101\n\t" \
    "v_cvt_pk_bf16_f32 v115, v102, v103\n\t" "v_cvt_pk_bf16_f32 v116, v104, v105\n\t" "v_cvt_pk_bf16_f32 v117, v106, v107\n\t" "v_cvt_pk_bf16_f32 v118, v108, v109\n\t" \
    "v_cvt_pk_bf16_f32 v119, v110, v111\n\t" "buffer_store_dwordx4 v[112:115], %[z1], %[rs], s63 offen sc1\n\t" \
    "buffer_store_dwordx4 v[116:119], %[z2], %[rs], s63 offen sc1\n\t" "buffer_load_dword v120, %[fo], %[rc], %[fv] offen sc1\n\t" "s_waitcnt vmcnt(0)\n\t" \
    "v_readfirstlane_b32 s61, v120\n\t" "s_lshr_b32 s66, s60, 2\n\t" "s_xor_b32 s66, s66, %[xp]\n\t" "s_and_b32 s66, s66, 1\n\t" "s_lshl_b32 s66, s66, 13\n\t" \
    "s_lshl_b32 s64, s60, 19\n\t" "s_add_u32 s64, s64, s66\n\t" "s_add_u32 s64, s64, %[vb]\n\t" "s_cmp_gt_u32 s61, s60\n\t" "s_cbranch_scc0 34f\n\t" \
    "s_cmp_eq_u32 s60, 64\n\t" "s_cbranch_scc1 34f\n\t" "buffer_load_dwordx4 v[16:19], %[ao], %[rs], s64 offen offset:0 sc1\n\t" \
    "buffer_load_dwordx4 v[20:23], %[ao], %[rs], s64 offen offset:256 sc1\n\t" "buffer_load_dwordx4 v[24:27], %[ao], %[rs], s64 offen offset:2048 sc1\n\t" \
    "buffer_load_dwordx4 v[28:31], %[ao], %[rs], s64 offen offset:2304 sc1\n\t" "buffer_load_dwordx4 v[32:35], %[a1], %[rs], s64 offen offset:0 sc1\n\t" \
    "buffer_load_dwordx4 v[36:39], %[a1], %[rs], s64 offen offset:256 sc1\n\t" "buffer_load_dwordx4 v[40:43], %[a1], %[rs], s64 offen offset:2048 sc1\n\t" \
    "buffer_load_dwordx4 v[44:47], %[a1], %[rs], s64 offen offset:2304 sc1\n\t" "v_add_u32 v122, s60, %[at]\n\t" "buffer_store_dword v122, %[fo], %[rc], %[fz] offen sc1\n\t" \
    "s_waitcnt vmcnt(1)\n\t" "s_branch 33b\n\t" "34:\n\t" "v_add_u32 v122, s60, %[at]\n\t" "buffer_store_dword v122, %[fo], %[rc], %[fz] offen sc1\n\t" \
    "s_cmp_eq_u32 s60, 64\n\t" "s_cbranch_scc1 90f\n\t" "s_add_u32 s67, s60, 1\n\t" "s_cmp_ge_u32 s61, s67\n\t" "s_cbranch_scc1 32f\n\t" "31:\n\t" \
    "buffer_load_dword v120, %[fo], %[rc], %[fv] offen sc1\n\t" "buffer_load_dword v121, %[fo], %[rc], %[vd] offen sc1\n\t" "s_waitcnt vmcnt(0)\n\t" \
    "v_readfirstlane_b32 s61, v120\n\t" "v_readfirstlane_b32 s66, v121\n\t" "s_nop 3\n\t" "s_cmp_ge_u32 s61, s67\n\t" "s_cbranch_scc1 32f\n\t" "s_bitcmp1_b32 s66, 1\n\t" \
    "s_cbranch_scc1 90f\n\t" "s_cmp_eq_u32 %[bd], 0\n\t" "s_cbranch_scc1 32f\n\t" "s_sub_u32 %[bd], %[bd], 1\n\t" "s_sleep 2\n\t" "s_branch 31b\n\t" "32:\n\t" \
    "s_lshr_b32 s66, s60, 2\n\t" "s_xor_b32 s66, s66, %[xp]\n\t" "s_and_b32 s66, s66, 1\n\t" "s_lshl_b32 s66, s66, 13\n\t" "s_lshl_b32 s64, s60, 19\n\t" \
    "s_add_u32 s64, s64, s66\n\t" "s_add_u32 s64, s64, %[vb]\n\t" "buffer_load_dwordx4 v[16:19], %[ao], %[rs], s64 offen offset:0 sc1\n\t" \
    "buffer_load_dwordx4 v[20:23], %[ao], %[rs], s64 offen offset:256 sc1\n\t" "buffer_load_dwordx4 v[24:27], %[ao], %[rs], s64 offen offset:2048 sc1\n\t" \
    "buffer_load_dwordx4 v[28:31], %[ao], %[rs], s64 offen offset:2304 sc1\n\t" "buffer_load_dwordx4 v[32:35], %[a1], %[rs], s64 offen offset:0 sc1\n\t" \
    "buffer_load_dwordx4 v[36:39], %[a1], %[rs], s64 offen offset:256 sc1\n\t" "buffer_load_dwordx4 v[40:43], %[a1], %[rs], s64 offen offset:2048 sc1\n\t" \
    "buffer_load_dwordx4 v[44:47], %[a1], %[rs], s64 offen offset:2304 sc1\n\t" "s_waitcnt vmcnt(0)\n\t" "s_branch 33b\n\t" "70:\n\t" "s_sub_u32 s67, s60, %[rg]\n\t" \
    "s_cmp_ge_u32 s62, s67\n\t" "s_cbranch_scc1 72f\n\t" "71:\n\t" "buffer_load_dword v120, %[fo], %[rc], %[fs] offen sc1\n\t" \
    "buffer_load_dword v121, %[fo], %[rc], %[vd] offen sc1\n\t" "s_waitcnt vmcnt(0)\n\t" "v_readfirstlane_b32 s62, v120\n\t" "v_readfirstlane_b32 s66, v121\n\t" "s_nop 3\n\t" \
    "s_cmp_ge_u32 s62, s67\n\t" "s_cbranch_scc1 72f\n\t" "s_bitcmp1_b32 s66, 1\n\t" "s_cbranch_scc1 90f\n\t" "s_cmp_eq_u32 %[bd], 0\n\t" "s_cbranch_scc1 72f\n\t" \
    "s_sub_u32 %[bd], %[bd], 1\n\t" "s_sleep 2\n\t" "s_branch 71b\n\t" "72:\n\t" "s_branch 73b\n\t" "90:\n\t"

#define T_DQ 1390
#define T_DV 1290
#define T_RQ 700
#define T_RV 640
#define T_M1 120
#define T_DY 600
#define T_DX 1240
#define T_MIN 35
#define T_PEN 350
#define T_SAFE 80
#define T_RETRY 700
#define T_POLL 1
#define T_KS 0
#define T_TS 0
#define T_VLAG 0
#define T_U0 400
constexpr int T_OVH = 118, T_NEAR = 460;
constexpr unsigned NS(int ns) { return (unsigned)(ns * 64 / 10); }
struct Clk { unsigned cyc, g, pc, pg, k1, k2; };
DEV void mark(Clk& k) {
  unsigned long long c, r[8];
  asm volatile("s_memtime %0\n\ts_memrealtime %1\n\ts_nop 2\n\ts_memrealtime %2\n\ts_nop 2\n\ts_memrealtime %3\n\ts_nop 2\n\ts_memrealtime %4\n\ts_nop 2\n\t"
               "s_memrealtime %5\n\ts_nop 2\n\ts_memrealtime %6\n\ts_nop 2\n\ts_memrealtime %7\n\ts_waitcnt lgkmcnt(0)"
               : "=s"(c), "=s"(r[0]), "=s"(r[1]), "=s"(r[2]), "=s"(r[3]), "=s"(r[4]), "=s"(r[5]), "=s"(r[6]) :: "memory");
  const unsigned a = (unsigned)r[0];
  unsigned n = 0;
#pragma unroll
  for (int i = 1; i < 7; ++i) asm("s_cmp_eq_u32 %1, %2\n\ts_addc_u32 %0, %0, 0" : "+s"(n) : "s"((unsigned)r[i]), "s"(a) : "scc");
  k.g = (a + 4u) << 6; k.cyc = (unsigned)c + 12u + 16u * n;
}
DEV void anchor(Clk& k, bool any = false) {
  mark(k);
  const unsigned dg_ = k.g - k.pg;
  const float dc = (float)(k.cyc - k.pc), dg = (float)dg_;
  unsigned k1, k2;
  asm volatile("s_nop 0\n\tv_readfirstlane_b32 %0, %2\n\tv_readfirstlane_b32 %1, %3\n\ts_nop 3\n\ts_min_u32 %0, %0, 0x80000\n\ts_min_u32 %1, %1, 0x8000"
               : "=&s"(k1), "=&s"(k2) : "v"((unsigned)(dg * __builtin_amdgcn_rcpf(dc) * 65536.f)), "v"((unsigned)(dc * __builtin_amdgcn_rcpf(dg) * 65536.f)) : "scc");
  if (any) { k.k1 = k1; k.k2 = k2; }
  else asm("s_cmp_lt_u32 %4, %5\n\ts_cselect_b32 %2, %2, %0\n\ts_cselect_b32 %3, %3, %1\n\ts_add_u32 %0, %0, %2\n\ts_lshr_b32 %0, %0, 1\n\ts_add_u32 %1, %1, %3\n\ts_lshr_b32 %1, %1, 1"
           : "+s"(k.k1), "+s"(k.k2), "+s"(k1), "+s"(k2) : "s"(dg_ - NS(1400)), "n"(NS(800)) : "scc");
  k.pc = k.cyc; k.pg = k.g;
}
DEV unsigned to_cyc(const Clk& k, unsigned g) {
  unsigned d;
  asm("s_max_i32 %0, %1, 0xffff8000\n\ts_min_i32 %0, %0, 0x8000\n\ts_mul_i32 %0, %0, %2\n\ts_ashr_i32 %0, %0, 16" : "=&s"(d) : "s"(g - k.g), "s"(k.k2) : "scc");
  return k.cyc + d - T_OVH;
}
DEV void wait_cyc(unsigned t) { asm volatile(TIMED_WAIT :: "s"(t) : CL_S); }
DEV void close_in(Clk& k, unsigned t) {
  if ((int)(t - (unsigned)__builtin_amdgcn_s_memtime()) > T_NEAR + 60) { wait_cyc(t - T_NEAR); mark(k); }
}
DEV bool slow_head(const u16* q, int h) { return ((((unsigned)(size_t)q >> 8) + h) & 3) == 1; }
DEV int turn(int h, int w) { return ((h >> 3) ^ (w & 1)) & 3; }
DEV void hold(const Ctx& c, int ticks) {
#pragma unroll 1
  while ((unsigned)__builtin_amdgcn_s_memrealtime() - c.t0 < (unsigned)ticks) __builtin_amdgcn_s_sleep(4);
}
DEV int z_mix(int y) { return (y >> 4) | ((y & 1) << 2); }
DEV int u_slot(int s, int para) { return R_U + (s & (ZRING - 1)) * MB + ((para ^ (s >> 1)) & 1) * 8192; }
DEV unsigned ask_stamps(Ctx& c, int par, int slot) { return __builtin_amdgcn_raw_buffer_load_b32(c.rs, amap(24576, par) + fresh_lane() * 128, slot, AUX_SC1); }
DEV unsigned latest(Ctx& c, const Clk& k, int par, int slot, unsigned v) {
  if (__builtin_expect(!allok(v), 0)) {
#pragma unroll 1
    do {
      __builtin_amdgcn_s_sleep(T_POLL);
      asm volatile("" ::: "memory");
      const unsigned t = __builtin_amdgcn_raw_buffer_load_b32(c.rs, v == SENT ? amap(24576, par) + fresh_lane() * 128 : 1 << 30, slot, AUX_SC1);
      v = v == SENT ? t : v;
    } while (!allok(v) && !spend(c));
  }
  int d = (int)(v - k.g);
  d = max(d, __builtin_amdgcn_update_dpp(d, d, 0x128, 0xf, 0xf, true));
  d = max(d, __builtin_amdgcn_update_dpp(d, d, 0x124, 0xf, 0xf, true));
  d = max(d, __builtin_amdgcn_update_dpp(d, d, 0x122, 0xf, 0xf, true));
  d = max(d, __builtin_amdgcn_update_dpp(d, d, 0x121, 0xf, 0xf, true));
  unsigned m;
  asm("s_max_i32 %0, %1, %2\n\ts_max_i32 %0, %0, %3\n\ts_max_i32 %0, %0, %4" : "=&s"(m)
      : "s"(__builtin_amdgcn_readlane(d, 0)), "s"(__builtin_amdgcn_readlane(d, 16)), "s"(__builtin_amdgcn_readlane(d, 32)), "s"(__builtin_amdgcn_readlane(d, 48)) : "scc");
  return k.g + m;
}
struct Plan { unsigned gx, dx; };
DEV unsigned smax(unsigned a, unsigned b) { return (int)(a - b) > 0 ? a : b; }
DEV unsigned when_p(const Plan& t, unsigned last) {
  const unsigned gy = t.gx + t.dx + NS(T_M1);
  return (int)(last + NS(T_MIN) - gy) > 0 ? smax(last + NS(T_PEN), gy + NS(T_RETRY)) : gy;
}
DEV void advance(Plan& t, unsigned last) {
  const unsigned gy = t.gx + t.dx + NS(T_M1), dur = last - t.gx, gp = when_p(t, last);
  const unsigned nx = gp + NS(T_DY) + (gp != gy ? NS(T_SAFE) : 0u);
  t.dx = dur < t.dx ? (7u * t.dx + dur) >> 3 : dur < t.dx + NS(80) ? (t.dx + dur) >> 1 : t.dx + NS(4);
  t.gx = nx;
}
DEV unsigned next_go(Ctx& c, Clk& k, Plan& t, int par, int slot, unsigned v) {
  SCHED();
  anchor(k);
  SCHED();
  advance(t, latest(c, k, par, slot, v));
  return to_cyc(k, t.gx);
}

DEV constexpr int qk_r(int j) { return j < 4 ? 0 : 1 + (j - 4) % 3; }
DEV constexpr int qk_i(int j) { return j < 4 ? j : (j - 4) / 3; }
DEV constexpr int qk_upto(int u) { return u < 6 ? u + 7 : u < 10 ? 12 + (u - 4) / 2 : u < 12 ? 15 : 16; }
DEV void qk_role(const Params& p, Ctx& c, int h) {
  constexpr int WL = 0, MEMS = 131072, KXL = 147456, QKX = 155648, QROW = 272;
  c.budget = SPIN_CAP;
  const int w = c.w;
  Clk k = {0u, 0u, 0u, 0u, 0u, 0u};
  anchor(k, true);
  v8u wa[32], wh[16];
  claim(c, 0);
  hold(c, QK_AT - (slow_head(p.Wq, h) ? SLOW_LEAD : 0));
  {
    const int lane = fresh_lane(), g = lane >> 4, cc = lane & 15;
    const rsrc_t rq = mkrs(p.Wq, 1024 * 4096 * 2), rk = mkrs(p.Wk, 1024 * 4096 * 2);
    int vo[4];
#pragma unroll
    for (int i = 0; i < 4; ++i) vo[i] = opq((256 * w + 64 * (i ^ turn(h, w)) + krow_lane(g)) * 8192 + (h * 128 + 8 * cc) * 2);
    v4u row[16][8];
    auto request = [&](int u) {
#pragma unroll
      for (int r = 0; r < 8; ++r) row[u][r] = ld16w(u < 8 ? rq : rk, vo[(u >> 1) & 3], krow(4 * (u & 1) + (r >> 1), r & 1) * 8192);
    };
#pragma unroll
    for (int u = 0; u < qk_upto(-1); ++u) request(u);
    SCHED();
    const int sw = MEMS + w * 8192 + g * 256, lw = opq(WL + w * 32768 + lane * 16);
    int sr[2];
#pragma unroll
    for (int t = 0; t < 2; ++t) { const int i = (2 * cc + t) & 7; sr[t] = opq(i * 1024 + g * 256 + ((4 * w + (cc >> 2) + 2 * i) & 15) * 16); }
    sfor<16>([&](auto U) __attribute__((always_inline)) {
      constexpr int u = U.value;
#pragma unroll
      for (int i = 0; i < 8; ++i) {
        v4u f;
#pragma unroll
        for (int r = 0; r < 4; ++r) f[r] = prm(row[u][2 * r + 1][i >> 1], row[u][2 * r][i >> 1], i & 1);
        sts16(c.L, sw + i * 1024 + ((cc + 2 * i) & 15) * 16, f);
      }
      SCHED();
      if (qk_upto(u) > qk_upto(u - 1)) request(qk_upto(u - 1));
      BAR();
      v4u in[4][2];
#pragma unroll
      for (int t = 1; t >= 0; --t)
#pragma unroll
        for (int r = 0; r < 4; ++r) in[r][t] = lds16(c.L, sr[t] + MEMS + ((w + r) & 3) * 8192);
      SCHED();
#pragma unroll
      for (int j = 0; j < 16; ++j) {
        if (qk_i(j) != ((u >> 1) & 3)) continue;
#pragma unroll
        for (int t = 1; t >= 0; --t) {
          const v4u o = in[qk_r(j)][t];
          if (u >= 8 && t == 1) sts16(c.L, lw + j * 2048 + (u & 1) * 1024, o);
          else {
            v8u& d = u < 8 ? wa[2 * j + t] : wh[j];
#pragma unroll
            for (int r = 0; r < 4; ++r) d[4 * (u & 1) + r] = o[r];
#define M_(r) asm volatile("" : "+" r(d))
            if (u & 1) { if (u < 8) AT_ACC(M_, 2 * j + t); else AT_HOME(M_, j); }
#undef M_
          }
        }
      }
      BAR();
    });
  }
  STAMP(0);
  BAR();
  const unsigned vote = wait_verdict(c);
  const int par = vote_b(vote), para = vote_a(vote);
  const int lane = fresh_lane(), g = lane >> 4, cc = lane & 15;
  const int moff = opq(amap(w * 4096 + lane * 16, par) + (turn(h, w) & 1) * 1024), m2 = R_MEM + (turn(h, w) & 2) * 1024;
  unsigned tries = 1u << 12;
  *(volatile LAS unsigned*)(c.L + 160512) = 0u;
  LGKM(0);
  const int idx = opq((lane & 1) ? 0xEEEE : 0x4444);
  TICK_DECL;
  const int l0 = opq(WL + w * 32768 + lane * 16), l1 = opq(MEMS + lane * 16);
  const int qw = opq(QKX + (2 * w + (cc >> 3)) * 256 + g * 64 + (cc & 1) * 16 + ((cc >> 1) & 3) * 4), kw = opq(QKX + (8 + 2 * g) * QROW + (32 * w + 2 * cc) * 2);
#define QK_PINS PIN_ACC0(wa); PIN_ACC1(wa); PIN_HOME(wh)
#define QK_IN [l0] "v"(l0), [l1] "v"(l1), [mo] "v"(moff), [ix] "v"(idx), [qw] "v"(qw), [kw] "v"(kw), [rs] "s"(c.rs), [sb] "s"(m2 + (s & 3) * MEM_SLOT)
#define QK_CL CL_S, CL_16_32, CL_160_200, CL_200_216, CL_216_256
  if (w == 3) {
    const int hq = opq(lane ? 1 << 30 : amap(24576 + h * 128, par));
    anchor(k, true);
    unsigned tg = k.cyc;
    Plan tt = {k.g - NS(100000), NS(T_DX)};
    for (int s = 0; s < NSEG; ++s) {
      const int pb = R_P + (s & 3) * P_SLOT;
      QK_PINS;
      asm volatile(QK_STEP3 : [bd] "+s"(c.budget), [rt] "+s"(tries) : QK_IN, [hq] "v"(hq), [pb] "s"(pb), [tg] "s"(tg), [ca] "s"(k.cyc - T_DQ), [k1] "s"(k.k1), [ga] "s"(k.g), [dr] "n"(T_RQ), [pr] "s"(R_P + ((s + 1) & 3) * P_SLOT),
                     [kn] "s"(R_KX - 8 * MEMS + xblk(min(s + 1, NSEG - 1), h, para)) : QK_CL);
      QK_PINS;
      LOG(1, s, tg);
      const unsigned sv = ask_stamps(c, par, pb);
      SCHED();
      anchor(k);
      RAWBAR();
      SCHED();
      advance(tt, latest(c, k, par, pb, sv));
      tg = to_cyc(k, tt.gx);
      LOG(2, s, k.cyc); LOG(3, s, k.g); LOG(4, s, tt.dx);
      st4s(mkrs((const void*)c.ctrl, CTRL_BYTES), hq & (1 << 30), C_FLAG + (F_S + h) * 128, (unsigned)s + 1u);
      TICK_SUM(10);
      close_in(k, tg);
      tg = to_cyc(k, tt.gx);
    }
  } else {
    const int kx0 = opq(KXL + g * 512 + cc * 16), kmrd = opq(QKX + g * 16 + (8 + (cc & 7)) * QROW);
    unsigned seen = NOFLAG;
    if (w == 0) {
      const int prow = (2 * g + (cc & 1)) * 2 + ((cc >> 1) & 1);
      const int px = opq(amap((h >> 1) * 1024 + ((2 * (h & 1) + (cc >> 3)) * 16 + prow) * 16 + ((cc >> 2) & 1) * 8, par));
      const int pm = opq(cc < 8 ? amap(16384 + h * 128 + prow * 8 + (cc >> 2) * 4, par) : 1 << 30);
      const int ps = opq((cc & 1) ? 0x03020706 : 0x05040100);
      for (int s = 0; s < NSEG; ++s) {
        if (seen < (unsigned)s + 1u) seen = wait_flag(c, F_K + h, s + 1);
        QK_PINS;
        asm volatile(QK_STEP0 : [bd] "+s"(c.budget), [rt] "+s"(tries), [sq] "+s"(seen)
                     : QK_IN, [kx] "v"(kx0), [km] "v"(kmrd), [px] "v"(px), [pm] "v"(pm), [ps] "v"(ps), [pb] "s"(R_P + (s & 3) * P_SLOT), [pr] "s"(R_P + ((s + 1) & 3) * P_SLOT),
                       [kb] "s"(R_KX - KXL + xblk(s, h, para)), [fq] "s"(c.ctrl + (C_FLAG + (F_K + h) * 128) / 4) : QK_CL);
        QK_PINS;
        LITE(s); LOGS(0, s);
        TICK_SUM(10);
      }
    } else {
      const int qt = w - 1;
      const int px = opq(amap(qt ? 16384 + h * 1024 + lane * 16 : h * 512 + (g >> 1) * 256 + cc * 16 + (g & 1) * 8, para));
      const int pm = opq(g < 2 ? amap(49152 + (qt * 32 + h) * 256 + cc * 16 + g * 8, para) : 1 << 30);
      const int ps = opq(cc - 4 * g), hq = opq(g * 512 + (16 * qt + cc) * 16);
#define QK_TOK(STEP) for (int s = 0; s < NSEG; ++s) { QK_PINS; \
        asm volatile(STEP : [bd] "+s"(c.budget), [rt] "+s"(tries), [sq] "+s"(seen) \
                     : QK_IN, [kx] "v"(kx0), [km] "v"(kmrd), [px] "v"(px), [pm] "v"(pm), [ps] "v"(ps), [hq] "v"(hq), [qb] "s"(R_QX + xblk(s, h, para)), [tb] "s"(R_PT + (s & 7) * PT_SLOT), \
                       [tr] "s"(R_PT + ((s + 2) & 7) * PT_SLOT), [sn] "s"(s + 1), [fq] "s"(c.ctrl + (C_FLAG + (F_Q + h) * 128) / 4) : QK_CL); \
        QK_PINS; TICK_SUM(10); }
      if (w == 1) { QK_TOK(QK_STEP1) } else { QK_TOK(QK_STEP2) }
    }
  }
  TICK_OUT();
  if (c.budget == 0u) __hip_atomic_store(c.ctrl + C_TMO / 4, 1u, RLX_AGENT);
}

DEV void shuffle_cols(Ctx& c, int scratch, const v4u (&rows)[8], v4u (&o)[8]) {
  const int g = c.lane >> 4, cc = c.lane & 15;
#pragma unroll
  for (int i = 0; i < 8; ++i) {
    v4u f;
#pragma unroll
    for (int r = 0; r < 4; ++r) f[r] = prm(rows[2 * r + 1][i >> 1], rows[2 * r][i >> 1], i & 1);
    sts16(c.L, scratch + (8 * cc + i) * 64 + ((g ^ (cc & 3)) << 4), f);
  }
  asm volatile("s_waitcnt lgkmcnt(0)" ::: "memory");
#pragma unroll
  for (int i = 0; i < 8; ++i) { const int col = 16 * i + cc; o[i] = lds16(c.L, scratch + col * 64 + ((g ^ ((col >> 3) & 3)) << 4)); }
  asm volatile("s_waitcnt lgkmcnt(0)" ::: "memory");
}

DEV void vo_role(const Params& p, Ctx& c, int h) {
  constexpr int WL = 0, MEMS = 131072, VM = 147456, VROW = 272;
  c.budget = SPIN_CAP;
  const int w = c.w, lane = c.lane = fresh_lane(), g = lane >> 4, cc = lane & 15;
  Clk k = {0u, 0u, 0u, 0u, 0u, 0u};
  anchor(k, true);
  v8u wa[32], wh[16];
  claim(c, 0);
  hold(c, VO_AT);
  const rsrc_t rwo = mkrs(p.Wo, 4096 * 1024 * 2);
  const int voo = (h * 128 + 4 * g) * 2048 + (256 * ((cc >> 3) ^ (h >> 3)) + 64 * w + 8 * (cc & 7)) * 2;
  v4u buf[3][8];
  auto request_o = [&](int u) {
#pragma unroll
    for (int r = 0; r < 8; ++r) buf[u % 3][r] = ld16w(rwo, voo ^ ((u >> 2) * 1024), (32 * (u & 3) + 16 * (r >> 2) + (r & 3)) * 2048);
  };
  {
    const rsrc_t rw = mkrs(p.Wv, 1024 * 4096 * 2);
    const int vo = (256 * w + krow_lane(g)) * 8192 + (h * 128 + 8 * cc) * 2;
    v4u vrow[8][8];
    auto request = [&](int u) {
#pragma unroll
      for (int r = 0; r < 8; ++r) vrow[u][r] = ld16w(rw, vo, (64 * ((u >> 1) ^ turn(h, w)) + krow(4 * (u & 1) + (r >> 1), r & 1)) * 8192);
    };
#pragma unroll
    for (int u = 0; u < 5; ++u) request(u);
    SCHED();
    const int wr_ = MEMS + w * 8192 + g * 256;
    int sr[2];
#pragma unroll
    for (int t = 0; t < 2; ++t) { const int i = (2 * cc + t) & 7; sr[t] = opq(i * 1024 + g * 256 + ((4 * w + (cc >> 2) + 2 * i) & 15) * 16); }
#pragma unroll
    for (int u = 0; u < 8; ++u) {
#pragma unroll
      for (int i = 0; i < 8; ++i) {
        v4u f;
#pragma unroll
        for (int r = 0; r < 4; ++r) f[r] = prm(vrow[u][2 * r + 1][i >> 1], vrow[u][2 * r][i >> 1], i & 1);
        sts16(c.L, wr_ + i * 1024 + ((cc + 2 * i) & 15) * 16, f);
      }
      SCHED();
      if (u + 5 < 8) request(u + 5); else if (u < 6) request_o(u - 3);
      BAR();
      v4u in[4][2];
#pragma unroll
      for (int t = 1; t >= 0; --t)
#pragma unroll
        for (int r = 0; r < 4; ++r) in[r][t] = lds16(c.L, sr[t] + MEMS + ((w + r) & 3) * 8192);
      SCHED();
#pragma unroll
      for (int j = 0; j < 16; ++j) {
        if (qk_i(j) != (u >> 1)) continue;
#pragma unroll
        for (int t = 1; t >= 0; --t) {
          const v4u o = in[qk_r(j)][t];
          if (t == 1 && (j & 1)) sts16(c.L, WL + (w * 16 + (j >> 1)) * 2048 + (u & 1) * 1024 + lane * 16, o);
          else {
            const int n = 3 * (j >> 1) + ((j & 1) ? 2 : t);
#pragma unroll
            for (int r = 0; r < 4; ++r) wa[n][4 * (u & 1) + r] = o[r];
#define M_(r) asm volatile("" : "+" r(wa[n]))
            if (u & 1) AT_ACC(M_, n);
#undef M_
          }
        }
      }
      BAR();
    }
  }
#pragma unroll
  for (int u = 0; u < 8; ++u) {
    v4u o[8];
    shuffle_cols(c, MEMS + w * 8192, buf[u % 3], o);
    SCHED();
    if (u + 3 < 8) request_o(u + 3);
#pragma unroll
    for (int i = 0; i < 4; ++i) {
      const int pr = 4 * (u >> 2) + i, ch = u & 3;
      const v8u f = {o[2 * i][0], o[2 * i + 1][0], o[2 * i][1], o[2 * i + 1][1], o[2 * i][2], o[2 * i + 1][2], o[2 * i][3], o[2 * i + 1][3]};
      if (ch < 2) {
        wh[2 * pr + ch] = f;
#define M_(r) asm volatile("" : "+" r(wh[2 * pr + ch]))
        AT_HOME(M_, 2 * pr + ch);
#undef M_
      } else if (ch == 2) {
        wa[24 + pr] = f;
#define M_(r) asm volatile("" : "+" r(wa[24 + pr]))
        AT_ACC(M_, 24 + pr);
#undef M_
      } else {
        sts16(c.L, WL + (w * 16 + 8 + pr) * 2048 + lane * 16, lo4(f));
        sts16(c.L, WL + (w * 16 + 8 + pr) * 2048 + 1024 + lane * 16, hi4(f));
      }
    }
    SCHED();
  }
  BAR();
  STAMP(0);
  const unsigned vote = wait_verdict(c);
  const int par = vote_b(vote), para = vote_a(vote);
  const int moff = opq(amap(w * 4096 + lane * 16, par) + (turn(h, w) & 1) * 1024), m2 = R_MEM + (turn(h, w) & 2) * 1024;
  unsigned tries = 1u << 12;
  *(volatile LAS unsigned*)(c.L + 160512) = 0u;
  LGKM(0);
  const int idx = opq((lane & 1) ? 0xEEEE : 0x4444);
  const int u_off = opq((4 * w + g) * 16384 + (h ^ (8 * g)) * 256 + cc * 16);
  const int hq = opq((lane | w) ? 1 << 30 : amap(24576 + (32 + h) * 128, par));
  const int l0 = opq(WL + w * 32768 + lane * 16), l1 = opq(MEMS + lane * 16);
  const int vmw = opq(VM + 2 * g * VROW + (32 * w + 2 * cc) * 2);
  const int vmr = opq(VM + (cc >> 1) * VROW + g * 16);
  TICK_DECL;
  anchor(k, true);
  unsigned tg = k.cyc;
  Plan tt = {k.g - NS(100000), NS(T_DX)};
  for (int s = 0; s < NSEG; ++s) {
    const int pb = R_P + (s & 3) * P_SLOT;
    PIN_ACC0(wa); PIN_ACC1(wa); PIN_HOME(wh);
    asm volatile(VO_STEP
        : [bd] "+s"(c.budget), [rt] "+s"(tries)
        : [l0] "v"(l0), [l1] "v"(l1), [mo] "v"(moff), [ix] "v"(idx), [vw] "v"(vmw), [vr] "v"(vmr), [uo] "v"(u_off), [w] "s"(w), [rs] "s"(c.rs),
          [sb] "s"(m2 + (s & 3) * MEM_SLOT), [ub] "s"(u_slot(s, para) + (h >> 3) * 16 * 16384), [tg] "s"(tg + T_VLAG), [hq] "v"(hq), [pb] "s"(pb),
          [ca] "s"(k.cyc - T_DV), [k1] "s"(k.k1), [ga] "s"(k.g), [dr] "n"(T_RV), [pr] "s"(R_P + ((s + 1) & 3) * P_SLOT)
        : CL_S, CL_16_32, CL_160_200, CL_200_216, CL_216_256);
    PIN_ACC0(wa); PIN_ACC1(wa); PIN_HOME(wh);
    if (w == 0) {
      LITE(s);
      LOG(1, s, tg);
      tg = next_go(c, k, tt, par, pb, ask_stamps(c, par, pb));
      LOG(2, s, k.cyc); LOG(3, s, k.g);
    }
    TICK_SUM(10);
    if (w == 0) { close_in(k, tg); tg = to_cyc(k, tt.gx); }
  }
  TICK_OUT();
  if (c.budget == 0u) __hip_atomic_store(c.ctrl + C_TMO / 4, 1u, RLX_AGENT);
}

DEV void y_role(const Params& p, Ctx& c, int y) {
  constexpr int ZR = 0, UL = 131072, CW = 155648;
  c.budget = SPIN_CAP;
  const int w = c.w, lane = c.lane = fresh_lane(), g = lane >> 4, cc = lane & 15;
  const v4u sent4 = {SENT, SENT, SENT, SENT};
  STAMP(2);
  Clk k = {0u, 0u, 0u, 0u, 0u, 0u};
  anchor(k, true);
  unsigned m0 = 0;
  if (w == 0) {
    unsigned best[2] = {~0u, ~0u};
    st4s(c.rs, y * 16384 + (lane >> 5) * 8192 + (lane & 31) * 4, R_U, SENT);
#pragma unroll 1
    for (int n = 0; n < 12; ++n) {
#pragma unroll
      for (int r = 0; r < 2; ++r) {
        VMWAIT(0);
        const unsigned t0 = (unsigned)__builtin_amdgcn_s_memtime();
        LGKM(0);
        st4s(c.rs, y * 16384 + (lane & 31) * 4, R_U + r * 8192, SENT);
        VMWAIT(0);
        const unsigned t1 = (unsigned)__builtin_amdgcn_s_memtime();
        LGKM(0);
        best[r] = min(best[r], t1 - t0);
      }
      if (n + 1 >= PROBES && min(best[0], best[1]) < 600u && max(best[0], best[1]) > min(best[0], best[1]) + 128u) break;
    }
    STAMP(5);
    claim(c, CL_VOTED | (best[1] < best[0] ? CL_A : 0u));
    m0 = *(const GAS unsigned*)(p.mem0 + (lane >> 3) * 1024 + 16 * y + 2 * (lane & 7));
    m0 = m0 < SENT - 1u ? m0 : SENT - 1u;
    if (lane < 8) *(LAS unsigned*)(c.L + CW + lane * 4) = 0u;
    const unsigned v = wait_vote(c);
    STAMP(7);
    const int oa = y * 16384 + vote_a(v) * 8192 + lane * 16, ob = amap(y * 3072 + lane * 16, vote_b(v));
#pragma unroll
    for (int i = 0; i < 8; ++i) st16s(c.rs, oa + (i & 3) * 1024, R_U + (i >> 2) * 4096, sent4);
#pragma unroll
    for (int i = 0; i < 3; ++i) st16s(c.rs, amap(y * 3072 + i * 1024 + lane * 16, vote_b(v)), R_MEM, sent4);
#pragma unroll
    for (int i = 0; i < 8; ++i) st16s(c.rs, oa + (i & 3) * 1024, R_PT + (i >> 2) * 4096, sent4);
    (void)ob;
    VMWAIT(0);
    if (lane == 0) __hip_atomic_fetch_add(c.ctrl + (C_CLAIM + c.att * 1024) / 4 + c.slot, CL_FILLED, RLX_AGENT);
    STAMP(6);
  }
  BAR();
  const unsigned vote = wait_verdict(c);
  const int par = vote_b(vote), para = vote_a(vote);
  const int mo = amap(y * 256 + lane * 4, par);
  if (w == 0) st4s(c.rs, mo, R_MEM, m0);
  STAMP(0);
  const int rot = y >> 4;
  volatile LAS unsigned* const GO = (volatile LAS unsigned*)(c.L + CW);
  TICK_DECL;
  if (w == 0) {
    int xo[4];
#pragma unroll
    for (int i = 0; i < 4; ++i) xo[i] = amap((i ^ rot) * 4096 + lane * 16, par);
    const int pmo = amap(16384 + rot * 1024 + 2 * g * 128 + cc * 8, par), psel = (cc & 1) ? 0x03020706 : 0x05040100;
    constexpr int lead = T_U0, late = 0;
    anchor(k, true);
    Plan tt = {k.g - NS(100000), NS(T_DX)};
    unsigned done = k.cyc;
    const int so = amap(24576, par) + lane * 128;
    const int idx = opq((lane & 1) ? 0xEEEE : 0x4444);
    const int uo = y * 16384 + lane * 16;
    const int mw = amap(y * 256 + (2 * g + (cc & 1)) * 32 + (cc >> 1) * 4, par);
    for (int s = 0; s < NSEG - 1; ++s) {
      if (s == 0) RAWBAR();
      const int za = ZR + (s & 3) * 32768 + rot * 8192 + lane * 16;
      v32u z0, z1, z2, z3;
      asm volatile(
          "ds_read_b128 a[0:3], %[z0] offset:0\n\t" "ds_read_b128 a[4:7], %[z0] offset:1024\n\t" "ds_read_b128 a[8:11], %[z0] offset:2048\n\t" "ds_read_b128 a[12:15], %[z0] offset:3072\n\t"
          "ds_read_b128 a[16:19], %[z0] offset:4096\n\t" "ds_read_b128 a[20:23], %[z0] offset:5120\n\t" "ds_read_b128 a[24:27], %[z0] offset:6144\n\t" "ds_read_b128 a[28:31], %[z0] offset:7168\n\t"
          "ds_read_b128 a[32:35], %[z1] offset:0\n\t" "ds_read_b128 a[36:39], %[z1] offset:1024\n\t" "ds_read_b128 a[40:43], %[z1] offset:2048\n\t" "ds_read_b128 a[44:47], %[z1] offset:3072\n\t"
          "ds_read_b128 a[48:51], %[z1] offset:4096\n\t" "ds_read_b128 a[52:55], %[z1] offset:5120\n\t" "ds_read_b128 a[56:59], %[z1] offset:6144\n\t" "ds_read_b128 a[60:63], %[z1] offset:7168\n\t"
          "ds_read_b128 a[64:67], %[z2] offset:0\n\t" "ds_read_b128 a[68:71], %[z2] offset:1024\n\t" "ds_read_b128 a[72:75], %[z2] offset:2048\n\t" "ds_read_b128 a[76:79], %[z2] offset:3072\n\t"
          "ds_read_b128 a[80:83], %[z2] offset:4096\n\t" "ds_read_b128 a[84:87], %[z2] offset:5120\n\t" "ds_read_b128 a[88:91], %[z2] offset:6144\n\t" "ds_read_b128 a[92:95], %[z2] offset:7168\n\t"
          "ds_read_b128 a[96:99], %[z3] offset:0\n\t" "ds_read_b128 a[100:103], %[z3] offset:1024\n\t" "ds_read_b128 a[104:107], %[z3] offset:2048\n\t" "ds_read_b128 a[108:111], %[z3] offset:3072\n\t"
          "ds_read_b128 a[112:115], %[z3] offset:4096\n\t" "ds_read_b128 a[116:119], %[z3] offset:5120\n\t" "ds_read_b128 a[120:123], %[z3] offset:6144\n\t" "ds_read_b128 a[124:127], %[z3] offset:7168\n\t"
          "s_waitcnt lgkmcnt(0)"
          : "={a[0:31]}"(z0), "={a[32:63]}"(z1), "={a[64:95]}"(z2), "={a[96:127]}"(z3) : [z0] "v"(za), [z1] "v"(za ^ 8192), [z2] "v"(za ^ 16384), [z3] "v"(za ^ 24576) : "memory");
      const int pb = R_P + (s & 3) * P_SLOT;
      v32u uu;
      const int ub = u_slot(s, para);
      TICK(0);
      anchor(k);
      TICK(8);
      unsigned gp = tt.gx + tt.dx + NS(T_M1), lt = late, sv = SENT;
      unsigned bad = (int)(done - to_cyc(k, tt.gx)) > T_OVH;
      TICK(4);
      LOG(2, s, k.cyc); LOG(3, s, k.g);
#pragma unroll 1
      for (;;) {
        if (bad) {
          const bool all = allok(sv);
          const unsigned last = latest(c, k, par, pb, sv);
          if (!all) mark(k);
          gp = when_p(tt, last);
          lt = 1;
        }
        close_in(k, to_cyc(k, gp) - (lt ? 96 : lead));
        const unsigned tg = to_cyc(k, gp);
        if (!bad) LOG(1, s, tg);
        asm volatile(Y_STEP
          : "=&{v[144:175]}"(uu), [bad] "=&s"(bad), [sv] "=&v"(sv)
          : [x0] "v"(xo[0]), [x1] "v"(xo[1]), [x2] "v"(xo[2]), [x3] "v"(xo[3]), [pm] "v"(pmo), [u0] "v"(uo + (rot ^ (y & 3)) * 2048), [mw] "v"(mw), [ps] "v"(psel), [ix] "v"(idx),
            [rs] "s"(c.rs), [pb] "s"(pb), [ub] "s"(ub), [mb] "s"(R_MEM + ((s + 1) & 3) * MEM_SLOT), [mr] "s"(R_MEM + ((s + 3) & 3) * MEM_SLOT), [tu] "s"(tg - (lt ? 96 : lead)), [tg] "s"(tg - 32), [late] "s"(lt),
            [so] "v"(so), [bd] "s"(c.budget),
            "{a[0:31]}"(z0), "{a[32:63]}"(z1), "{a[64:95]}"(z2), "{a[96:127]}"(z3)
          : CL_S, CL_120_128, CL_176_200, CL_200_216, CL_216_256);
        bad = __builtin_amdgcn_readfirstlane(bad);
        if (bad != 1u) break;
        spend(c);
      }
      done = bad;
      SCHED();
      const int ul = opq(UL + rot * 2048 + lane * 16);
#pragma unroll
      for (int i = 0; i < 8; ++i) sts16(c.L, (ul ^ ((i >> 1) * 2048)) + (i & 1) * 1024, v4u{uu[4 * i], uu[4 * i + 1], uu[4 * i + 2], uu[4 * i + 3]});
      asm volatile("" ::: "memory");
      *GO = s + 1;
      asm volatile("s_waitcnt vmcnt(2)" : "+v"(sv));
      advance(tt, latest(c, k, par, pb, sv));
      TICK(3);
      LITE(s); LOG(0, s, done);
      TICK_LDS(CW + 256);
    }
    {
      anchor(k);
      wait_cyc(to_cyc(k, tt.gx + tt.dx + NS(T_M1)));
      const int ub = u_slot(NSEG - 1, para);
      v4u u[8];
#pragma unroll 1
      for (;;) {
        asm volatile("" ::: "memory");
#pragma unroll
        for (int i = 0; i < 8; ++i) u[i] = ld16s(c.rs, uo + (i & 3) * 1024, ub + (i >> 2) * 4096);
        SCHED();
        unsigned t = 0;
#pragma unroll
        for (int i = 0; i < 8; ++i) t = umax(t, top(u[i]));
        if (allok(t) || spend(c)) break;
      }
      RAWBAR();
#pragma unroll
      for (int i = 0; i < 8; ++i) sts16(c.L, UL + (i ^ (2 * (y & 3))) * 1024 + lane * 16, u[i]);
      asm volatile("" ::: "memory");
      *GO = NSEG;
    }
    TICK_DUMP(CW + 256);
  } else if (w < 3) {
    const rsrc_t ro = mkrs(p.out, 2048 * 2048 * 2);
    auto tokens = [&](auto QT) {
      constexpr int qt = QT.value, NX = qt ? 32 : 16;
      RAWBAR();
      for (int s = 0; s < NSEG; ++s) {
        const int tb = R_PT + (s & 7) * PT_SLOT + para * 8192;
        int ua[4], za[4], xb[4], mb[4];
#pragma unroll
        for (int i = 0; i < 4; ++i) {
          ua[i] = opq(UL + (i ^ rot) * 2048 + lane * 16);
          za[i] = opq(ZR + (s & 3) * 32768 + (i ^ rot) * 8192 + (qt ? lane * 16 : (g >> 1) * 1024 + (g & 1) * 512 + cc * 16));
          xb[i] = tb + amap(qt * 16384 + (i ^ rot) * (qt ? 8192 : 4096), 0);
          mb[i] = tb + amap(49152 + qt * 8192, 0) + (i ^ rot) * 2048;
        }
        SCHED();
        RAWBAR();
        if (TOK_WAIT) __builtin_amdgcn_s_sleep(TOK_WAIT);
        TICK(0);
        f32x4 a;
#pragma unroll 1
        for (;;) {
          asm volatile("" ::: "memory");
          v4u pt[NX + 8];
#pragma unroll
          for (int i = 0; i < NX; ++i) pt[i] = ld16s(c.rs, (i & 3) * 1024 + lane * 16, qt ? xb[i >> 3] + ((i >> 2) & 1) * 4096 : xb[i >> 2]);
#pragma unroll
          for (int i = 0; i < 8; ++i) pt[NX + i] = ld16s(c.rs, (i & 1) * 1024 + lane * 16, mb[i >> 1]);
          SCHED();
          TICK(1);
#pragma unroll 1
          while (__builtin_amdgcn_readfirstlane(*GO) < (unsigned)s + 1u) { if (spend(c)) break; }
          asm volatile("" ::: "memory");
          f32x4 d[2] = {f32x4{0.f, 0.f, 0.f, 0.f}, f32x4{0.f, 0.f, 0.f, 0.f}};
          v4u fa[8];
          auto frag = [&](int i) {
            if (i >= NX) fa[i & 7] = lds16(c.L, ua[(i - NX) >> 1] + (i & 1) * 1024);
            else if (qt) fa[i & 7] = lds16(c.L, za[i >> 3] + (i & 7) * 1024);
            else {
              const v2u lo = *(LAS v2u*)(c.L + za[i >> 2] + (i & 3) * 2048), hi = *(LAS v2u*)(c.L + za[i >> 2] + (i & 3) * 2048 + 256);
              fa[i & 7] = v4u{lo.x, lo.y, hi.x, hi.y};
            }
          };
#pragma unroll
          for (int i = 0; i < 7; ++i) frag(i);
          SCHED();
#pragma unroll
          for (int i = 0; i < NX + 8; ++i) {
            if (i + 7 < NX + 8) frag(i + 7);
            d[i & 1] = mfma16(fa[i & 7], pt[i], d[i & 1]);
            SCHED();
          }
          a = d[0] + d[1];
          if (!__any(a[0] != a[0])) break;
          unsigned t = 0;
#pragma unroll
          for (int i = 0; i < NX + 8; ++i) t = umax(t, top(pt[i]));
          if (allok(t) || spend(c)) break;
        }
        __builtin_amdgcn_raw_buffer_store_b64(v2u{pk(a[0], a[1]), pk(a[2], a[3])}, ro, (16 * qt + cc) * 4096 + (16 * y + 4 * g) * 2, s * 32 * 4096, AUX_NT);
        TICK(2);
        TICK_SUM(3);
      }
    };
    if (w == 1) tokens(IC<0>{}); else tokens(IC<1>{});
  } else {
    unsigned seenZ = NOFLAG;
    const rsrc_t rc = mkrs((const void*)c.ctrl, CTRL_BYTES);
    const int fo = lane < 32 ? lane * 128 : 1 << 30;
    __builtin_amdgcn_s_setprio(3);
    auto least = [&](unsigned v) {
      v |= lane < 32 ? 0u : ~0u;
#pragma unroll
      for (int o = 1; o < 32; o <<= 1) v = min(v, (unsigned)__shfl_xor((int)v, o));
      seenZ = (unsigned)max((int)seenZ, (int)__builtin_amdgcn_readfirstlane(v) - 256 * c.att);
    };
    auto await = [&](unsigned need) {
#pragma unroll 1
      while (seenZ < need) {
        asm volatile("" ::: "memory");
        least(__builtin_amdgcn_raw_buffer_load_b32(rc, fo, C_FLAG + (F_Z + (y >> 4) * 32) * 128, AUX_SC1));
        if (seenZ >= need || spend(c)) break;
        __builtin_amdgcn_s_sleep(4);
      }
      __builtin_amdgcn_fence(__ATOMIC_ACQUIRE, "wavefront");
    };
    for (int n = 0; n < NSEG; ++n) {
      TICK(0);
      await(n + 1);
      TICK(1);
      v4u z[32];
      int za[8];
#pragma unroll
      for (int i = 0; i < 8; ++i) za[i] = opq(ZR + (n & 3) * 32768 + (i ^ z_mix(y)) * 1024 + lane * 16);
      unsigned look;
#pragma unroll
      for (int i = 0; i < 32 + Z_WIN; ++i) {
        if (i < 32) z[i] = ld16z(c.rs, (i & 3) * 1024 + lane * 16, R_Z + (n & (ZRING - 1)) * Z_SLOT + y * 65536 + ((para ^ (y >> 5)) & 1) * 8192 + amap((i >> 2) * 4096, 0));
        if (i == 32) { look = __builtin_amdgcn_raw_buffer_load_b32(rc, seenZ < (unsigned)n + 2u ? fo : 1 << 30, C_FLAG + (F_Z + (y >> 4) * 32) * 128, AUX_SC1); TICK(4); }
        SCHED();
        if (i >= Z_WIN) sts16(c.L, za[(i - Z_WIN) & 7] + ((i - Z_WIN) >> 3) * 8192, z[i - Z_WIN]);
        else asm volatile("s_nop " STR(Z_GAP));
        if (i == 30 + Z_WIN) { TICK(3); }
        SCHED();
      }
      least(look);
      if (n == 0) await(PRIME);
      TICK(2);
      { const int s = n; (void)s; TICK_SUM(3); }
      BAR();
    }
    RAWBAR();
  }
  TICK_OUT();
  if (c.budget == 0u) __hip_atomic_store(c.ctrl + C_TMO / 4, 1u, RLX_AGENT);
}

#define MF1A(d, a, b) asm volatile(MF16 "%0, %1, %2, %0" : "+v"(d) : "a"(a), "v"(b))
#define MF32A(d, a, b) asm volatile("v_mfma_f32_32x32x16_bf16 %0, %1, %2, %0" : "+v"(d) : "a"(a), "v"(b))
#define MF32AZ(d, a, b) asm volatile("v_mfma_f32_32x32x16_bf16 %0, %1, %2, 0" : "=&v"(d) : "a"(a), "v"(b))
#define MF1B(d, a, b) asm volatile(MF16 "%0, %1, %2, %0" : "+v"(d) : "v"(a), "a"(b))
#define MF1BZ(d, a, b) asm volatile(MF16 "%0, %1, %2, 0" : "=&v"(d) : "v"(a), "a"(b))
#define MF1AZ(d, a, b) asm volatile(MF16 "%0, %1, %2, 0" : "=&v"(d) : "a"(a), "v"(b))
DEV void g_role(const Params& p, Ctx& c, int mat, int h, int gi) {
  constexpr int EX = 131072;
  c.budget = SPIN_CAP;
  const int w = c.w, lane = c.lane = fresh_lane(), g = lane >> 4, cc = lane & 15;
  if (mat < 2) {
    unsigned t = 0;
#pragma unroll
    for (int i = 0; i < 3; ++i) t += __builtin_amdgcn_raw_buffer_load_b32(mkrs(p.Wv, 8 * MB), (256 * w + (lane >> 1)) * 8192 + h * 256 + (lane & 1) * 128, (2 + 2 * i + mat) * 32 * 8192, 0);
    if (mat == 0) {
      hold(c, X_AT);
#pragma unroll
      for (int i = 0; i < 4; ++i) t += __builtin_amdgcn_raw_buffer_load_b32(mkrs(p.x, 4 * MB), ((4 * h + w) * 64 + lane) * 128, i * MB, 0);
    }
    asm volatile("" :: "v"(t));
    claim(c, 0);
    hold(c, (mat ? GK_AT : GQ_AT) - (slow_head(p.Wq, h) ? SLOW_LEAD : 0));
  }
  const rsrc_t rx = mkrs(p.x, 2048 * 2048);
  const int xs = w * 16384 + ((lane * 16) ^ ((w & 1) << 7));

  v32u wa[8];
  {
    const rsrc_t rw = mkrs(mat == 0 ? p.Wq : mat == 1 ? p.Wk : p.Wv, 1024 * 4096 * 2);
    const int vo = (256 * w + 8 * g) * 8192 + (h * 128 + 8 * cc) * 2;
    v4u row[8][8];
    auto request = [&](int u) {
#pragma unroll
      for (int r = 0; r < 8; ++r) row[u][r] = ld16w(rw, vo, (32 * u + r) * 8192);
    };
#pragma unroll
    for (int u = 0; u < 2; ++u) request(u);
#pragma unroll
    for (int u = 0; u < 16; ++u) dma16(rx, c.L + w * 16384 + u * 1024, (xs ^ ((u >> 1) << 4)) + u * 1024);
#pragma unroll
    for (int u = 2; u < 6; ++u) request(u);
    SCHED();
    const int sw_ = EX + w * 8192 + g * 256;
    const int sr0 = opq(EX + (cc & 3) * 1024 + g * 256 + ((4 * w + (cc >> 2) + 4 * cc) & 15) * 16), sr1 = sr0 + 4096;
#pragma unroll
    for (int u = 0; u < 8; ++u) {
#pragma unroll
      for (int i = 0; i < 8; ++i) {
        v4u f;
#pragma unroll
        for (int r = 0; r < 4; ++r) f[r] = prm(row[u][2 * r + 1][i >> 1], row[u][2 * r][i >> 1], i & 1);
        sts16(c.L, sw_ + i * 1024 + ((cc + 4 * i) & 15) * 16, f);
      }
      SCHED();
      if (u + 6 < 8) request(u + 6);
      if (u == 7 && mat == 2) claim(c, 0);
      BAR();
      v8u in[4];
#pragma unroll
      for (int q = 0; q < 4; ++q) in[q] = cat(lds16(c.L, sr0 + q * 8192), lds16(c.L, sr1 + q * 8192));
      SCHED();
#pragma unroll
      for (int q = 0; q < 4; ++q) {
        v8u f = in[q];
#define M_(r) asm volatile("" : "+" r(f))
        AT_ACC(M_, 8 * q + u);
#undef M_
#pragma unroll
        for (int r = 0; r < 8; ++r) wa[2 * q + (u >> 2)][8 * (u & 3) + r] = f[r];
      }
      BAR();
    }
  }
  STAMP(0);
  const int xpar = vote_a(wait_vote(c)) ^ (mat >> 1);
  TICK_DECL;
  asm volatile(G_LOOP
      :: [xo] "v"(cc * 2048 + ((g ^ cc) << 4)), [xs] "v"(xs), [so] "v"((4 * w + g) * 512 + cc * 16), [fo] "v"((w | lane) ? 1 << 30 : 0),
         [rs] "s"(c.rs), [rx] "s"(rx), [rc] "s"(mkrs((const void*)c.ctrl, CTRL_BYTES)), [fl] "s"(C_FLAG + (mat * 32 + h) * 128), [xp] "s"(xpar),
         [ob] "s"((mat == 1 ? R_KX : R_QX) + h * 16384), [lw] "s"(w * 16384), GTRACE ACC_IN(wa)
      : CL_S, CL_16_32, CL_32_100);
}

DEV void zc_role(const Params& p, Ctx& c, int h) {
  c.budget = SPIN_CAP;
  const int w = c.w, lane = c.lane = fresh_lane(), g = lane >> 4, cc = lane & 15, h2 = lane >> 5, r32 = lane & 31;
  v32u wa[8];
  hold(c, ZC_AT);
  {
    const rsrc_t rw = mkrs(p.Wo, 4096 * 1024 * 2);
    const int vo = (h * 128 + 8 * g) * 2048 + (256 * w + 8 * cc) * 2;
    v4u row[8][8];
    auto request = [&](int u) {
#pragma unroll
      for (int r = 0; r < 8; ++r) row[u][r] = ld16w(rw, vo, (32 * (u & 3) + r) * 2048 + (u >> 2) * 256);
    };
#pragma unroll
    for (int u = 0; u < 2; ++u) request(u);
    SCHED();
    claim(c, 0);
    SCHED();
#pragma unroll
    for (int u = 2; u < 6; ++u) request(u);
    const int sw_ = w * 8192 + g * 256, sr_ = w * 8192 + (cc & 7) * 1024 + g * 256;
    v8u two[8];
#pragma unroll
    for (int u = 0; u < 8; ++u) {
#pragma unroll
      for (int i = 0; i < 8; ++i) {
        v4u f;
#pragma unroll
        for (int r = 0; r < 4; ++r) f[r] = prm(row[u][2 * r + 1][i >> 1], row[u][2 * r][i >> 1], i & 1);
        sts16(c.L, sw_ + i * 1024 + ((cc + 2 * i) & 15) * 16, f);
      }
      SCHED();
      if (u + 6 < 8) request(u + 6);
      LGKM(0);
#pragma unroll
      for (int q = 0; q < 8; ++q) {
        const v4u o = lds16(c.L, sr_ + ((2 * q + (cc >> 3) + 2 * cc) & 15) * 16);
#pragma unroll
        for (int r = 0; r < 4; ++r) two[q][4 * (u & 1) + r] = o[r];
      }
      SCHED();
      if (u & 1) {
#pragma unroll
        for (int q = 0; q < 8; ++q) {
          const int n = 2 * (8 * (u >> 2) + q) + ((u >> 1) & 1);
#define M_(r) asm volatile("" : "+" r(two[q]))
          AT_ACC(M_, n);
#undef M_
#pragma unroll
          for (int r = 0; r < 8; ++r) wa[n >> 2][8 * (n & 3) + r] = two[q][r];
        }
      }
      LGKM(0);
      SCHED();
    }
  }
  STAMP(0);
  BAR();
  const int para = vote_a(wait_vote(c));
  const int ao = g * 512 + cc * 16, zo = 16 * w * 65536 + lane * 16 + ((para ^ (w >> 1)) & 1) * 8192;
  TICK_DECL;
  asm volatile(ZC_LOOP
      : [bd] "+s"(c.budget)
      : [ao] "v"(ao), [a1] "v"(ao + 4096), [at] "v"(c.att * 256), [z1] "v"(zo + amap((h ^ z_mix(16 * w)) * 1024, 0)), [z2] "v"(zo + 65536 + amap((h ^ z_mix(16 * w + 1)) * 1024, 0)), [uo] "v"(R_U + (16 * w + (h >> 1)) * 16384 + (h & 1) * 4096 + lane * 16), [fo] "v"(lane ? 1 << 30 : 0),
        [rs] "s"(c.rs), [rc] "s"(mkrs((const void*)c.ctrl, CTRL_BYTES)), [fv] "s"(C_FLAG + (F_V + h) * 128), [fs] "s"(C_FLAG + (F_S + h) * 128),
        [fz] "s"(C_FLAG + (F_Z + w * 32 + h) * 128), [xp] "s"(para ^ 1), [vb] "s"(R_QX + h * 16384), [vd] "s"(C_VERDICT + c.att * 128), [z0] "n"(ZC_PRIME), [zb] "n"(R_Z), [rg] "n"(ZRING - 3), [rm] "n"(ZRING - 1), GTRACE ACC_IN(wa)
      : CL_S, CL_16_32, CL_32_100);
  if (c.budget == 0u) __hip_atomic_store(c.ctrl + C_TMO / 4, 1u, RLX_AGENT);
}

DEV void conv_role(const Params& p, Ctx& c, int cg, int far) {
  constexpr int FS = 4160, PL = 1280, UO = 4 * FS, WS = UO + 8 * PL;
  const int w = c.w, lane = c.lane = fresh_lane(), tid = w * 64 + lane, g = lane >> 4, cc = lane & 15;
  const int d0 = 8 * cg + 2 * w;
  const int pg = ((far ^ (cg >> 5)) & 1) * 8192;
  auto crow = [](int ch) { return (ch >> 1) * 16384 + (ch & 1) * 4096; };
  const int de = (cg >> 3) * 64 + (tid & 7) * 8, t0 = (cg & 7) * 256 + (tid >> 3) * 8;
  auto group_sync = [&](int phase) {
    gu32* cnt = c.ctrl + (C_CONV + (cg >> 3) * 16 + phase * 8) / 4;
    VMWAIT(0);
    BAR();
    if (w == 0) {
      if (lane == 0) __hip_atomic_fetch_add(cnt, 1u, RLX_AGENT);
#pragma unroll 1
      while (__builtin_amdgcn_readfirstlane(__hip_atomic_load(cnt, RLX_AGENT)) < 8u) { if (spend(c)) break; __builtin_amdgcn_s_sleep(2); }
    }
    BAR();
  };
  v4u fr[2][4], ur[2][4], qr[8];
  unsigned up[2][4];
  const int so = cc * 64 + g * 16;
  const unsigned dd = __builtin_amdgcn_raw_buffer_load_b32(mkrs(p.ssmD, 2048), d0 * 2, 0, CONV_AUX);
  BAR();
  {
    const rsrc_t rk = mkrs(p.bk, 2048 * 2048), rv = mkrs(p.bv, 2048 * 2048);
    v4u kr[11], vr[8], sh[4];
#pragma unroll
    for (int i = 0; i < 4; ++i) sh[i] = ld16c(mkrs(p.shift, 8192), de * 8 + i * 16, 0);
#pragma unroll
    for (int i = 0; i < 11; ++i) kr[i] = ld16c(rk, (t0 + i - 3) * 2048 + de * 2, 0);
#pragma unroll
    for (int i = 0; i < 8; ++i) vr[i] = ld16c(rv, (t0 + i) * 2048 + de * 2, 0);
#pragma unroll
    for (int i = 0; i < 8; ++i) fr[i >> 2][i & 3] = ld16c(mkrs(p.ssmk, 4 * MB), lane * 16 + (i & 3) * 1024, (d0 + (i >> 2)) * 4096);
    SCHED();
    if (lane < 16) sts16(c.L, w * WS + (lane >> 2) * FS + (lane & 3) * 16, v4u{0, 0, 0, 0});
#pragma unroll
    for (int i = 0; i < 2; ++i) sts16(c.L, w * WS + UO + (4 * i + g) * PL + cc * 16, v4u{0, 0, 0, 0});
#pragma unroll
    for (int ch = 0; ch < 8; ++ch) {
      const unsigned sa = sh[ch >> 1][(ch & 1) * 2], sb = sh[ch >> 1][(ch & 1) * 2 + 1];
      const float s0 = blo(sa), s1 = bhi(sa), s2 = blo(sb), s3 = bhi(sb);
      float kk[11], o[8];
#pragma unroll
      for (int i = 0; i < 11; ++i) kk[i] = (ch & 1) ? bhi(kr[i][ch >> 1]) : blo(kr[i][ch >> 1]);
#pragma unroll
      for (int i = 0; i < 8; ++i) o[i] = fmaf(kk[i + 3], s0, fmaf(kk[i + 2], s1, fmaf(kk[i + 1], s2, kk[i] * s3))) * ((ch & 1) ? bhi(vr[i][ch >> 1]) : blo(vr[i][ch >> 1]));
      st16s(c.rs, de * 8192 + t0 * 2, R_KVT + pg + crow(ch), v4u{pk(o[0], o[1]), pk(o[2], o[3]), pk(o[4], o[5]), pk(o[6], o[7])});
    }
    group_sync(0);
#pragma unroll
    for (int i = 0; i < 8; ++i) {
      const int b = R_KVT + pg + d0 * 8192 + (i >> 2) * 4096 + (i & 3) * 1024;
      ur[i >> 2][i & 3] = ld16s(c.rs, so, b);
      up[i >> 2][i & 3] = __builtin_amdgcn_raw_buffer_load_b32(c.rs, (i & 3) == 0 && lane == 0 ? 1 << 30 : so, b - 4, AUX_SC1);
    }
#pragma unroll
    for (int i = 0; i < 8; ++i) qr[i] = ld16c(mkrs(p.bq, 4 * MB), (t0 + i) * 2048 + de * 2, 0);
  }
  STAMP(5);
  const int fw = opq(w * WS + 48 + lane * 16), uw = opq(w * WS + UO + g * PL + cc * 16);
  const int fa = opq(w * WS + (cc & 3) * FS + (3 + (cc >> 2) - g) * 16);
  sfor<2>([&](auto CH) __attribute__((always_inline)) {
    constexpr int ch = CH.value;
    SCHED();
#pragma unroll
    for (int i = 0; i < 4; ++i) {
      const v4u f = fr[ch][i], a = ur[ch][i];
      sts16(c.L, fw + 3 * FS + i * 1024, f);
      sts8(c.L, fw + FS + i * 1024 + 8, v2u{f[0], f[1]}); sts8(c.L, fw + FS + i * 1024 + 16, v2u{f[2], f[3]});
#pragma unroll
      for (int r = 0; r < 2; ++r) {
        const int o = fw + 2 * r * FS + i * 1024 + 12 - 8 * r;
        *(LAS unsigned*)(c.L + o) = f[0]; sts8(c.L, o + 4, v2u{f[1], f[2]}); *(LAS unsigned*)(c.L + o + 12) = f[3];
      }
      v4u r0, r1;
#pragma unroll
      for (int r = 0; r < 4; ++r) {
        r0[r] = __builtin_amdgcn_alignbyte(a[3 - r], a[3 - r], 2);
        r1[r] = __builtin_amdgcn_perm(r < 3 ? a[2 - r] : up[ch][i], a[3 - r], 0x07060100u);
      }
      sts16(c.L, uw + (16 + 16 * i) * 16, r0);
      sts16(c.L, uw + 4 * PL + (16 + 16 * i) * 16, r1);
    }
    f32x4 acc[4][2];
#pragma unroll
    for (int i = 0; i < 8; ++i) acc[i >> 1][i & 1] = f32x4{0.f, 0.f, 0.f, 0.f};
    v4u A[2][4], B[2][8];
    auto frag = [&](int d, int n) {
      constexpr int order[12] = {0, 4, 5, 6, 7, 8, 9, 10, 11, 1, 2, 3};
      const int x = order[n];
      if (x < 4) A[d & 1][x] = lds16(c.L, fa + (d + 16 * x) * 64);
      else B[d & 1][x - 4] = lds16(c.L, uw + ((x - 4) & 1) * 4 * PL + (16 + 16 * ((x - 4) >> 1) - d) * 16);
    };
    asm volatile("" ::: "memory");
#pragma unroll
    for (int n = 0; n < 12; ++n) frag(0, n);
    SCHED();
    sfor<16>([&](auto D) __attribute__((always_inline)) {
      constexpr int d = D.value;
      int n = 0;
#pragma unroll
      for (int k = 0; k < 4; ++k)
#pragma unroll
        for (int m = 0; m + k < 4; ++m)
#pragma unroll
          for (int e = 0; e < 2; ++e) {
            acc[m + k][e] = mfma16(A[d & 1][k], B[d & 1][2 * m + e], acc[m + k][e]);
            if (d < 15 && n < 12) frag(d + 1, n);
            ++n;
            SCHED();
          }
    });
    const float dv = ch ? bhi(dd) : blo(dd);
#pragma unroll
    for (int q = 0; q < 4; ++q) {
      v4u z;
#pragma unroll
      for (int i = 0; i < 4; ++i) {
        const unsigned yy = pk(acc[q][1][i], acc[q][0][i]), ud = pk(blo(ur[ch][q][i]) * dv, bhi(ur[ch][q][i]) * dv);
        z[i] = pk(blo(yy) + blo(ud), bhi(yy) + bhi(ud));
      }
      st16s(c.rs, so + q * 1024, R_YT + pg + d0 * 8192 + ch * 4096, z);
    }
  });
  STAMP(6);
  {
    const rsrc_t ro = mkrs(p.out, 2048 * 2048 * 2);
    v4u yv[8];
    group_sync(1);
#pragma unroll
    for (int ch = 0; ch < 8; ++ch) yv[ch] = ld16s(c.rs, de * 8192 + t0 * 2, R_YT + pg + crow(ch));
#pragma unroll
    for (int i = 0; i < 8; ++i) {
      float o[8];
#pragma unroll
      for (int ch = 0; ch < 8; ++ch) o[ch] = ((ch & 1) ? bhi(qr[i][ch >> 1]) : blo(qr[i][ch >> 1])) * ((i & 1) ? bhi(yv[ch][i >> 1]) : blo(yv[ch][i >> 1]));
      __builtin_amdgcn_raw_buffer_store_b128(v4u{pk(o[0], o[1]), pk(o[2], o[3]), pk(o[4], o[5]), pk(o[6], o[7])}, ro, (t0 + i) * 4096 + (1024 + de) * 2, 0, AUX_NT);
    }
  }
  if (c.budget == 0u) __hip_atomic_store(c.ctrl + C_TMO / 4, 1u, RLX_AGENT);
}

extern "C" __global__ void __launch_bounds__(256, 1) mk_mega(Params p) {
  extern __shared__ __attribute__((aligned(16))) unsigned char lds_raw[];
  Ctx c;
  c.ctrl = (gu32*)p.ctrl;
  c.rs = mkrs(p.data, R_END);
  c.L = (lds_t)lds_raw;
  c.w = __builtin_amdgcn_readfirstlane(threadIdx.x >> 6);
  c.lane = threadIdx.x & 63;
  c.t0 = (unsigned)__builtin_amdgcn_s_memrealtime();
  c.b = (int)(blockIdx.x & 255u);
  c.att = (int)(blockIdx.x >> 8);
  c.ask = 0;
  if (c.w == 1) {
    unsigned long long base; unsigned len, t;
    asm volatile("s_getpc_b64 s[60:61]\n1:\n\ts_sub_u32 s60, s60, 1b-mk_mega\n\ts_subb_u32 s61, s61, 0\n\ts_mov_b64 %0, s[60:61]\n\ts_mov_b32 %1, mk_tail-mk_mega"
                 : "=s"(base), "=s"(len) :: "s60", "s61", "scc");
    t = __builtin_amdgcn_raw_buffer_load_b32(mkrs((const void*)base, len), (c.lane * 32 + (c.b >> 3)) * 128, 0, 0);
    asm volatile("" :: "v"(t));
  }
  if (c.w == 0 && c.att == 0) {
    c.ask = __builtin_amdgcn_readfirstlane(__hip_atomic_load(c.ctrl + C_ECHO / 4 + c.b, RLX_AGENT)) + 1u;
    if (c.lane == 0) __hip_atomic_store(c.ctrl + (C_ASK + c.b * 64) / 4, c.ask, RLX_AGENT);
  }
  c.slot = (c.att ? (c.b & 7) : (int)((unsigned)__builtin_amdgcn_s_getreg(20 | (3 << 11)) & 7u)) * 32 + (c.b >> 3);
  if (c.b == SERVER && c.w == 3) serve(c);
  const int x = c.slot >> 5, t = c.slot & 31;
  if (x < 4) {
    if (t < 8) qk_role(p, c, x * 8 + t);
    else if (t < 16) vo_role(p, c, x * 8 + t - 8);
    else y_role(p, c, x * 16 + t - 16);
  } else {
    if (t < 24) g_role(p, c, t >> 3, (x - 4) * 8 + (t & 7), t);
    else zc_role(p, c, (x - 4) * 8 + t - 24);
    STAMP(2);
    c.budget = SPIN_CAP;
    conv_role(p, c, (x - 4) * 32 + t, vote_a(wait_verdict(c)) ^ 1);
  }
  STAMP(3);
}

}

extern "C" void kernel_launch(void* const* d_in, const int* in_sizes, int n_in, void* d_out, int out_size, void* d_ws, size_t ws_size,
                              hipStream_t stream) {
  using namespace mk;
  (void)in_sizes; (void)n_in; (void)out_size;
  Params p;
  p.x = (const u16*)d_in[0]; p.mem0 = (const u16*)d_in[1];
  p.Wq = (const u16*)d_in[2]; p.Wk = (const u16*)d_in[3]; p.Wv = (const u16*)d_in[4]; p.Wo = (const u16*)d_in[5];
  p.bq = (const u16*)d_in[7]; p.bk = (const u16*)d_in[8]; p.bv = (const u16*)d_in[9];
  p.shift = (const u16*)d_in[10]; p.ssmk = (const u16*)d_in[11]; p.ssmD = (const u16*)d_in[12];
  p.out = (u16*)d_out;
  p.ctrl = (unsigned char*)d_ws;
  p.data = data_base(d_ws, ws_size);
  (void)hipFuncSetAttribute((const void*)mk_mega, hipFuncAttributeMaxDynamicSharedMemorySize, LDS_BYTES);
  hipLaunchKernelGGL(mk_mega, dim3(256), dim3(256), LDS_BYTES, stream, p);
}
```

```cpp
#include <hip/hip_runtime.h>
#include <hip/hip_bf16.h>

namespace mk {

#define GAS __attribute__((address_space(1)))
#define LAS __attribute__((address_space(3)))
#define DEV __device__ __forceinline__
#define RLX_AGENT __ATOMIC_RELAXED, __HIP_MEMORY_SCOPE_AGENT
typedef unsigned short u16;
typedef unsigned v2u __attribute__((ext_vector_type(2)));
typedef unsigned v4u __attribute__((ext_vector_type(4)));
typedef unsigned v8u __attribute__((ext_vector_type(8)));
typedef unsigned v16u __attribute__((ext_vector_type(16)));
typedef unsigned v32u __attribute__((ext_vector_type(32)));
typedef float f32x2 __attribute__((ext_vector_type(2)));
typedef float f32x4 __attribute__((ext_vector_type(4)));
typedef float f32x16 __attribute__((ext_vector_type(16)));
typedef __bf16 bf16x2 __attribute__((ext_vector_type(2)));
typedef __bf16 bf16x8 __attribute__((ext_vector_type(8)));
typedef __bf16 bf16x16 __attribute__((ext_vector_type(16)));
typedef GAS unsigned gu32;
typedef __amdgpu_buffer_rsrc_t rsrc_t;
typedef LAS unsigned char* lds_t;

#define XCNT(r) ""
#define UCNT ""
#define CL_T
#define XT(a, b) ""
#define LMARK ""
#define GSUM ""
#define ZSUM ""
#define GTRACE
#define MIXED 1
#define NOFLAG 0
#define TOK_KICK 1
#define ZRING 16
#define Z_AT 16
#define Z_GAP 4
#define STR_(x) #x
#define STR(x) STR_(x)
#define GQ_AT 600
#define GK_AT 300
#define PULL_AT 800
#define QK_AT 100
#define SERVER 194
#define SLOW_LEAD 100
#define PULL_DEPTH 1
#define PRIME 2
#define X_AT 150
#define VO_AT 250
#define ZC_AT 0
#define CONV_AUX 2
#define W_AUX 0
#define ZC_PRIME 1
#define CODE_PULL 1
#define PROBES 2
#define Z_WIN 20
#define TOK_WAIT 0
#define Z_AUX 0
constexpr int NSEG = 64;
constexpr unsigned SENT = 0xFFFFFFFFu;
constexpr int AUX_SC1 = 16, AUX_NT = 2;
constexpr int T_LAG = 61;
constexpr int G_AHEAD = 4;
constexpr unsigned SPIN_CAP = 1u << 21;
constexpr int LDS_BYTES = 163840;

constexpr int C_ECHO = 0, C_CLAIM = 1024, C_VERDICT = 3072, C_TMO = 3328, C_CONV = 3456, C_VOTE = 3840, C_FLAG = 4096, C_ASK = C_FLAG + 256 * 128;
constexpr int F_Q = 0, F_K = 32, F_V = 64, F_S = 96, F_Z = 128;
constexpr int CTRL_BYTES = C_ASK + 256 * 64;
constexpr int MB = 1 << 20;
constexpr int R_U = 136 * MB;
constexpr int R_MEM = 2 * MB, MEM_SLOT = 32768;
constexpr int R_P = R_MEM + 4 * MEM_SLOT, P_SLOT = 65536;
constexpr int R_PT = 4 * MB, PT_SLOT = 131072;
constexpr int R_QX = 8 * MB, R_KX = 40 * MB, R_Z = 72 * MB, Z_SLOT = 4 * MB, R_KVT = R_KX, R_YT = R_KX + 8 * MB, R_END = 200 * MB;

constexpr size_t R_DIAG = 0;
#define TOP 1
static inline unsigned char* data_base(void* ws, size_t size) {
  return (unsigned char*)((TOP ? (size_t)ws + size - R_END - R_DIAG : (size_t)ws + CTRL_BYTES + 2 * MB - 1) & ~(size_t)(2 * MB - 1));
}
struct Params {
  const u16 *x, *mem0, *Wq, *Wk, *Wv, *Wo, *bq, *bk, *bv, *shift, *ssmk, *ssmD;
  u16* out;
  unsigned char* ctrl;
  unsigned char* data;
};

DEV rsrc_t mkrs(const void* p, unsigned bytes) { return __builtin_amdgcn_make_buffer_rsrc((void*)p, 0, bytes, 0x00020000); }
DEV v4u ld16s(rsrc_t rs, int v, int s) { return __builtin_amdgcn_raw_buffer_load_b128(rs, v, s, AUX_SC1); }
DEV v2u ld8s(rsrc_t rs, int v, int s) { return __builtin_amdgcn_raw_buffer_load_b64(rs, v, s, AUX_SC1); }
DEV v4u ld16(rsrc_t rs, int v, int s) { return __builtin_amdgcn_raw_buffer_load_b128(rs, v, s, 0); }
DEV v4u ld16z(rsrc_t rs, int v, int s) { return __builtin_amdgcn_raw_buffer_load_b128(rs, v, s, AUX_SC1 | Z_AUX); }
DEV v4u ld16c(rsrc_t rs, int v, int s) { return __builtin_amdgcn_raw_buffer_load_b128(rs, v, s, CONV_AUX); }
DEV v4u ld16w(rsrc_t rs, int v, int s) { return __builtin_amdgcn_raw_buffer_load_b128(rs, v, s, W_AUX); }
DEV v2u ld8(rsrc_t rs, int v, int s) { return __builtin_amdgcn_raw_buffer_load_b64(rs, v, s, 0); }
DEV void dma16(rsrc_t rs, lds_t l, int v) {
#if __HIP_DEVICE_COMPILE__
  __builtin_amdgcn_raw_ptr_buffer_load_lds(rs, (LAS void*)l, 16, v, 0, 0, 0);
#endif
}
DEV void st16s(rsrc_t rs, int v, int s, v4u d) { __builtin_amdgcn_raw_buffer_store_b128(d, rs, v, s, AUX_SC1); }
DEV void st8s(rsrc_t rs, int v, int s, v2u d) { __builtin_amdgcn_raw_buffer_store_b64(d, rs, v, s, AUX_SC1); }
DEV void st4s(rsrc_t rs, int v, int s, unsigned d) { __builtin_amdgcn_raw_buffer_store_b32(d, rs, v, s, AUX_SC1); }
DEV int amap(int o, int par) { return ((o >> 13) << 14) | (par << 13) | (o & 8191); }
DEV int xblk(int s, int h, int par) { return (s * 32 + h) * 16384 + ((par ^ (s >> 2)) & 1) * 8192; }
DEV unsigned umax(unsigned a, unsigned b) { return a > b ? a : b; }
DEV unsigned top(v4u v) { return umax(umax(v.x, v.y), umax(v.z, v.w)); }
DEV unsigned pk(float a, float b) { bf16x2 v = {(__bf16)a, (__bf16)b}; return __builtin_bit_cast(unsigned, v); }
DEV unsigned pks(float a, float b) { unsigned u = pk(a, b); return u < SENT - 1u ? u : SENT - 1u; }
DEV float blo(unsigned u) { return __builtin_bit_cast(float, u << 16); }
DEV float bhi(unsigned u) { return __builtin_bit_cast(float, u & 0xFFFF0000u); }
DEV float rb(float a) { return blo(pk(a, a)); }
DEV unsigned fu(float f) { return __builtin_bit_cast(unsigned, f); }
DEV float uf(unsigned u) { return __builtin_bit_cast(float, u); }
DEV f32x4 mfma16(v4u a, v4u b, f32x4 c) {
  return __builtin_amdgcn_mfma_f32_16x16x32_bf16(__builtin_bit_cast(bf16x8, a), __builtin_bit_cast(bf16x8, b), c, 0, 0, 0);
}
DEV f32x16 mfma32(v4u a, v4u b, f32x16 c) {
  return __builtin_amdgcn_mfma_f32_32x32x16_bf16(__builtin_bit_cast(bf16x8, a), __builtin_bit_cast(bf16x8, b), c, 0, 0, 0);
}
DEV f32x4 smf(v4u a, v8u b, f32x4 c, int idx) {
  return __builtin_amdgcn_smfmac_f32_16x16x64_bf16(__builtin_bit_cast(bf16x8, a), __builtin_bit_cast(bf16x16, b), c, idx, 0, 0);
}
DEV v8u cat(v4u a, v4u b) { return __builtin_shufflevector(a, b, 0, 1, 2, 3, 4, 5, 6, 7); }
DEV v4u lo4(v8u a) { return __builtin_shufflevector(a, a, 0, 1, 2, 3); }
DEV v4u hi4(v8u a) { return __builtin_shufflevector(a, a, 4, 5, 6, 7); }
DEV v4u lds16(lds_t L, int o) { return *(LAS v4u*)(L + o); }
DEV void sts16(lds_t L, int o, v4u v) { *(LAS v4u*)(L + o) = v; }
DEV void sts8(lds_t L, int o, v2u v) { *(LAS v2u*)(L + o) = v; }
#define SCHED() __builtin_amdgcn_sched_barrier(0)
#define PIN_A(x) asm volatile("" : "+a"(x))
#define PIN_V(x) asm volatile("" : "+v"(x))
DEV int opq(int v) { asm volatile("" : "+v"(v)); return v; }
DEV int fresh_lane() {
  int l; asm volatile("v_mbcnt_lo_u32_b32 %0, -1, 0\n\tv_mbcnt_hi_u32_b32 %0, -1, %0" : "=v"(l)); return l;
}
DEV f32x4 fresh_zero() {
  f32x2 a, b;
  asm volatile("v_mov_b64 %0, 0" : "=v"(a));
  asm volatile("v_mov_b64 %0, 0" : "=v"(b));
  return __builtin_shufflevector(a, b, 0, 1, 2, 3);
}
DEV unsigned fresh_sent() { unsigned z; asm volatile("v_mov_b32 %0, -1" : "=v"(z)); return z; }
#define SMF "v_smfmac_f32_16x16x64_bf16 "
#define MF16 "v_mfma_f32_16x16x32_bf16 "
#define SMF1(cls, d, a, b) asm volatile(SMF "%0, %1, %2, %3" : "+v"(d) : "v"(a), cls(b), "v"(idx))
#define SMF1F(cls, lo, hi, d, a, b) asm volatile(SMF "v[" #lo ":" #hi "], %1, %2, %3" : "+{v[" #lo ":" #hi "]}"(d) : "v"(a), cls(b), "v"(idx))
#define AT_HOME(M, j) do { const int j_ = (j); if (j_ == 0) M("{v[32:39]}"); if (j_ == 1) M("{v[40:47]}"); if (j_ == 2) M("{v[48:55]}"); if (j_ == 3) M("{v[56:63]}"); if (j_ == 4) M("{v[64:71]}"); if (j_ == 5) M("{v[72:79]}"); if (j_ == 6) M("{v[80:87]}"); if (j_ == 7) M("{v[88:95]}"); if (j_ == 8) M("{v[96:103]}"); if (j_ == 9) M("{v[104:111]}"); if (j_ == 10) M("{v[112:119]}"); if (j_ == 11) M("{v[120:127]}"); if (j_ == 12) M("{v[128:135]}"); if (j_ == 13) M("{v[136:143]}"); if (j_ == 14) M("{v[144:151]}"); if (j_ == 15) M("{v[152:159]}"); } while (0)
#define MF1(d, a, b) asm volatile(MF16 "%0, %1, %2, %0" : "+v"(d) : "v"(a), "v"(b))
#define MF1Z(d, a, b) asm volatile(MF16 "%0, %1, %2, 0" : "=&v"(d) : "v"(a), "v"(b))
#define LGKM(n) asm volatile("s_waitcnt lgkmcnt(" #n ")" ::: "memory")
#define RAWBAR() do { __builtin_amdgcn_s_barrier(); asm volatile("" ::: "memory"); } while (0)
DEV void settle3(f32x4& s0, f32x4& s1, f32x4& s2) { asm volatile("s_nop 7" : "+v"(s0), "+v"(s1), "+v"(s2)); }
DEV void settle4(f32x4 (&d)[4]) { asm volatile("s_nop 7" : "+v"(d[0]), "+v"(d[1]), "+v"(d[2]), "+v"(d[3])); }
#define BAR() do { asm volatile("s_waitcnt lgkmcnt(0)" ::: "memory"); __builtin_amdgcn_s_barrier(); asm volatile("" ::: "memory"); } while (0)
#define VMWAIT(n) asm volatile("s_waitcnt vmcnt(" #n ")" ::: "memory")
DEV float add1(float a, float b) {
  float o; asm("v_add_f32 %0, %1, %2" : "=v"(o) : "v"(a), "v"(b)); return o;
}
DEV float xmax(float v) {
  auto r = __builtin_amdgcn_permlane32_swap(fu(v), fu(v), false, false); v = fmaxf(uf(r[0]), uf(r[1]));
  auto q = __builtin_amdgcn_permlane16_swap(fu(v), fu(v), false, false); return fmaxf(uf(q[0]), uf(q[1]));
}
DEV float xsum(float v) {
  auto r = __builtin_amdgcn_permlane32_swap(fu(v), fu(v), false, false); v = uf(r[0]) + uf(r[1]);
  auto q = __builtin_amdgcn_permlane16_swap(fu(v), fu(v), false, false); return uf(q[0]) + uf(q[1]);
}

DEV float ror8(float v) { return uf(__builtin_amdgcn_update_dpp(0u, fu(v), 0x128, 0xf, 0xf, true)); }
DEV float fmax2(float a, float b) { return __builtin_amdgcn_fmed3f(a, b, __builtin_inff()); }
DEV float fmax3(float a, float b, float c) { float r; asm("v_max3_f32 %0, %1, %2, %3" : "=v"(r) : "v"(a), "v"(b), "v"(c)); return r; }
DEV bool allok(unsigned t) { return __all(t != SENT); }

template <int N> struct IC { static constexpr int value = N; };
template <int N, class F> DEV void sfor(F&& f) { if constexpr (N > 0) { sfor<N - 1>(f); f(IC<N - 1>{}); } }
struct Ctx {
  gu32* ctrl;
  rsrc_t rs;
  lds_t L;
  int w, lane;
  int b, slot, att;
  unsigned ask;
  unsigned t0;
  unsigned budget;
};
DEV bool spend(Ctx& c) {
  const unsigned old = c.budget;
  asm volatile("s_cmp_lg_u32 %0, 0\n\ts_subb_u32 %0, %0, 0" : "+s"(c.budget) :: "scc");
  return old == 0u;
}
DEV unsigned wait_flag(Ctx& c, int flag, unsigned need) {
  gu32* f = c.ctrl + (C_FLAG + flag * 128) / 4;
  unsigned v;
#pragma unroll 1
  for (;;) {
    v = __builtin_amdgcn_readfirstlane(__hip_atomic_load(f, RLX_AGENT));
    if (v >= need || spend(c)) break;
    __builtin_amdgcn_s_sleep(2);
  }
  __builtin_amdgcn_fence(__ATOMIC_ACQUIRE, "wavefront");
  return v;
}
DEV void post_flag(Ctx& c, int flag, unsigned v, int off = 0) {
  st4s(mkrs((const void*)c.ctrl, CTRL_BYTES), (c.w | off | fresh_lane()) ? 1 << 30 : 0, C_FLAG + flag * 128, v);
}
constexpr unsigned CL_A = 1u << 16, CL_VOTED = 1u << 24, CL_FILLED = 1u << 28;
DEV void claim(Ctx& c, unsigned votes) {
  if (c.w != 0) return;
  if (c.att == 0) {
#pragma unroll 1
    while (__builtin_amdgcn_readfirstlane(__hip_atomic_load(c.ctrl + C_ECHO / 4 + c.b, RLX_AGENT)) != c.ask) {
      if (spend(c)) break;
      __builtin_amdgcn_s_sleep(2);
    }
  }
  if (fresh_lane() == 0) __hip_atomic_fetch_add(c.ctrl + (C_CLAIM + c.att * 1024) / 4 + c.slot, 1u + votes, RLX_AGENT);
}
DEV unsigned wait_verdict(Ctx& c) {
  unsigned v;
#pragma unroll 1
  for (;;) {
    v = __builtin_amdgcn_readfirstlane(__hip_atomic_load(c.ctrl + (C_VERDICT + c.att * 128) / 4, RLX_AGENT));
    if ((v & 1u) || spend(c)) break;
    __builtin_amdgcn_s_sleep(2);
  }
  __builtin_amdgcn_fence(__ATOMIC_ACQUIRE, "wavefront");
  if ((v & 3u) == 3u && c.att == 0) {
    asm volatile("s_waitcnt vmcnt(0) lgkmcnt(0)\n\ts_barrier\n\ts_mov_b64 s[0:1], %0\n\ts_mov_b32 s2, %1\n\tv_mov_b32 v0, %2\n\t"
                 "s_getpc_b64 s[4:5]\n1:\n\ts_sub_u32 s4, s4, 1b-mk_mega\n\ts_subb_u32 s5, s5, 0\n\ts_setpc_b64 s[4:5]"
                 :: "s"(__builtin_amdgcn_kernarg_segment_ptr()), "s"(c.b + 256), "v"(c.w * 64 + fresh_lane()) : "s0", "s1", "s2", "s4", "s5", "v0", "scc", "memory");
    __builtin_unreachable();
  }
  return v;
}
DEV unsigned wait_vote(Ctx& c) {
  unsigned v;
#pragma unroll 1
  for (;;) {
    v = __builtin_amdgcn_readfirstlane(__hip_atomic_load(c.ctrl + C_VOTE / 4, RLX_AGENT));
    if ((v & 1u) || spend(c)) break;
    __builtin_amdgcn_s_sleep(2);
  }
  return v;
}
DEV void serve(Ctx& c) {
  c.budget = SPIN_CAP;
  const int lane = fresh_lane();
  const rsrc_t rc = mkrs((const void*)c.ctrl, CTRL_BYTES);
  if (c.att == 0) {
#pragma unroll
    for (int i = C_CLAIM / 1024; i < C_ASK / 1024; ++i) st16s(rc, lane * 16, i * 1024, v4u{0, 0, 0, 0});
    VMWAIT(0);
  }
  unsigned vote = c.att ? wait_vote(c) : 0u;
  bool echo = c.att == 0, first = true;
  v4u last = {0u, 0u, 0u, 0u};
#pragma unroll 1
  for (;;) {
    asm volatile("" ::: "memory");
    const v4u cl = ld16s(rc, lane * 16, C_CLAIM + c.att * 1024);
    if (echo) {
      v4u e;
#pragma unroll
      for (int i = 0; i < 4; ++i) e[i] = __builtin_amdgcn_raw_buffer_load_b32(rc, (lane * 4 + i) * 64, C_ASK, AUX_SC1);
      if (first || __any(e[0] != last[0] || e[1] != last[1] || e[2] != last[2] || e[3] != last[3])) st16s(rc, lane * 16, C_ECHO, e);
      last = e; first = false;
    }
    unsigned n = 0, one = 1;
    int va = 0, nv = 0, nf = 0;
#pragma unroll
    for (int i = 0; i < 4; ++i) {
      n += cl[i] & 0xFFFFu; one &= (cl[i] & 0xFFFFu) == 1u;
      va += __popcll(__ballot(cl[i] & CL_A)); nv += __popcll(__ballot(cl[i] & CL_VOTED)); nf += __popcll(__ballot(cl[i] & CL_FILLED));
    }
#pragma unroll
    for (int o = 1; o < 64; o <<= 1) n += (unsigned)__shfl_xor((int)n, o);
    const bool all = __builtin_amdgcn_readfirstlane(n) >= 256u, good = __all(one);
    echo = echo && !all;
    if (!vote && (all || max(va, nv - va) > 32)) {
      vote = va > 32 ? 5u : 9u;
      if (lane == 0) __hip_atomic_store(c.ctrl + C_VOTE / 4, vote, RLX_AGENT);
    }
    if (all && (!good || nf >= 64)) {
      if (lane == 0) __hip_atomic_store(c.ctrl + (C_VERDICT + c.att * 128) / 4, vote | (good ? 0u : 2u), RLX_AGENT);
      break;
    }
    if (spend(c)) break;
  }
}
DEV int vote_a(unsigned v) { return (int)((v >> 2) & 1u); }
DEV int vote_b(unsigned v) { return (int)((v >> 3) & 1u); }

#define STAMP(slot) do {} while (0)
#define TICK_DECL
#define LOG(i, s, v) do {} while (0)
#define LOGS(i, s) do {} while (0)
#define LITE(s) do {} while (0)
#define TICK(i) do {} while (0)
#define TICK_SUM(n) do {} while (0)
#define TICK_LDS(b) do {} while (0)
#define TICK_DUMP(b) do {} while (0)
#define TICK_OUT() do {} while (0)

DEV constexpr int krow(int rho8, int i) { return 32 * (rho8 >> 2) + 8 * (rho8 & 1) + 2 * ((rho8 >> 1) & 1) + i; }
DEV int krow_lane(int g) { return 16 * (g >> 1) + 4 * (g & 1); }
DEV unsigned prm(unsigned hi, unsigned lo, int odd) { return __builtin_amdgcn_perm(hi, lo, odd ? 0x07060302u : 0x05040100u); }

#define AT_ACC(M, n) do { const int n_ = (n); if (n_ == 0) M("{a[0:7]}"); if (n_ == 1) M("{a[8:15]}"); if (n_ == 2) M("{a[16:23]}"); if (n_ == 3) M("{a[24:31]}"); if (n_ == 4) M("{a[32:39]}"); if (n_ == 5) M("{a[40:47]}"); if (n_ == 6) M("{a[48:55]}"); if (n_ == 7) M("{a[56:63]}"); if (n_ == 8) M("{a[64:71]}"); if (n_ == 9) M("{a[72:79]}"); if (n_ == 10) M("{a[80:87]}"); if (n_ == 11) M("{a[88:95]}"); if (n_ == 12) M("{a[96:103]}"); if (n_ == 13) M("{a[104:111]}"); if (n_ == 14) M("{a[112:119]}"); if (n_ == 15) M("{a[120:127]}"); if (n_ == 16) M("{a[128:135]}"); if (n_ == 17) M("{a[136:143]}"); if (n_ == 18) M("{a[144:151]}"); if (n_ == 19) M("{a[152:159]}"); if (n_ == 20) M("{a[160:167]}"); if (n_ == 21) M("{a[168:175]}"); if (n_ == 22) M("{a[176:183]}"); if (n_ == 23) M("{a[184:191]}"); if (n_ == 24) M("{a[192:199]}"); if (n_ == 25) M("{a[200:207]}"); if (n_ == 26) M("{a[208:215]}"); if (n_ == 27) M("{a[216:223]}"); if (n_ == 28) M("{a[224:231]}"); if (n_ == 29) M("{a[232:239]}"); if (n_ == 30) M("{a[240:247]}"); if (n_ == 31) M("{a[248:255]}"); } while (0)
#define PIN_ACC0(x) asm volatile("" :: "{a[0:7]}"(x[0]), "{a[8:15]}"(x[1]), "{a[16:23]}"(x[2]), "{a[24:31]}"(x[3]), "{a[32:39]}"(x[4]), "{a[40:47]}"(x[5]), "{a[48:55]}"(x[6]), "{a[56:63]}"(x[7]), "{a[64:71]}"(x[8]), "{a[72:79]}"(x[9]), "{a[80:87]}"(x[10]), "{a[88:95]}"(x[11]), "{a[96:103]}"(x[12]), "{a[104:111]}"(x[13]), "{a[112:119]}"(x[14]), "{a[120:127]}"(x[15]))
#define PIN_ACC1(x) asm volatile("" :: "{a[128:135]}"(x[16]), "{a[136:143]}"(x[17]), "{a[144:151]}"(x[18]), "{a[152:159]}"(x[19]), "{a[160:167]}"(x[20]), "{a[168:175]}"(x[21]), "{a[176:183]}"(x[22]), "{a[184:191]}"(x[23]), "{a[192:199]}"(x[24]), "{a[200:207]}"(x[25]), "{a[208:215]}"(x[26]), "{a[216:223]}"(x[27]), "{a[224:231]}"(x[28]), "{a[232:239]}"(x[29]), "{a[240:247]}"(x[30]), "{a[248:255]}"(x[31]))
#define PIN_HOME(x) asm volatile("" :: "{v[32:39]}"(x[0]), "{v[40:47]}"(x[1]), "{v[48:55]}"(x[2]), "{v[56:63]}"(x[3]), "{v[64:71]}"(x[4]), "{v[72:79]}"(x[5]), "{v[80:87]}"(x[6]), "{v[88:95]}"(x[7]), "{v[96:103]}"(x[8]), "{v[104:111]}"(x[9]), "{v[112:119]}"(x[10]), "{v[120:127]}"(x[11]), "{v[128:135]}"(x[12]), "{v[136:143]}"(x[13]), "{v[144:151]}"(x[14]), "{v[152:159]}"(x[15]))
#define CL_32_100 "v32", "v33", "v34", "v35", "v36", "v37", "v38", "v39", "v40", "v41", "v42", "v43", "v44", "v45", "v46", "v47", "v48", "v49", "v50", "v51", "v52", "v53", "v54", "v55", "v56", "v57", "v58", "v59", "v60", "v61", "v62", "v63", "v64", "v65", "v66", "v67", "v68", "v69", "v70", "v71", "v72", "v73", "v74", "v75", "v76", "v77", "v78", "v79", "v80", "v81", "v82", "v83", "v84", "v85", "v86", "v87", "v88", "v89", "v90", "v91", "v92", "v93", "v94", "v95", "v96", "v97", "v98", "v99", "v100", "v101", "v102", "v103", "v104", "v105", "v106", "v107", "v108", "v109", "v110", "v111", "v112", "v113", "v114", "v115", "v116", "v117", "v118", "v119", "v120", "v121", "v122", "v123", "v124", "v125", "v126", "v127", "v128", "v129", "v130", "v131"
#define ACC_IN(x) "{a[0:31]}"(x[0]), "{a[32:63]}"(x[1]), "{a[64:95]}"(x[2]), "{a[96:127]}"(x[3]), "{a[128:159]}"(x[4]), "{a[160:191]}"(x[5]), "{a[192:223]}"(x[6]), "{a[224:255]}"(x[7])
#define CL_16_32 "v16", "v17", "v18", "v19", "v20", "v21", "v22", "v23", "v24", "v25", "v26", "v27", "v28", "v29", "v30", "v31"
#define CL_120_128 "v120", "v121", "v122", "v123", "v124", "v125", "v126", "v127", "v128", "v129", "v130", "v131", "v132", "v133", "v134", "v135"
#define CL_160_200 "v160", "v161", "v162", "v163", "v164", "v165", "v166", "v167", "v168", "v169", "v170", "v171", "v172", "v173", "v174", "v175", "v176", "v177", "v178", "v179", "v180", "v181", "v182", "v183", "v184", "v185", "v186", "v187", "v188", "v189", "v190", "v191", "v192", "v193", "v194", "v195", "v196", "v197", "v198", "v199"
#define CL_176_200 "v176", "v177", "v178", "v179", "v180", "v181", "v182", "v183", "v184", "v185", "v186", "v187", "v188", "v189", "v190", "v191", "v192", "v193", "v194", "v195", "v196", "v197", "v198", "v199"
#define CL_200_216 "v200", "v201", "v202", "v203", "v204", "v205", "v206", "v207", "v208", "v209", "v210", "v211", "v212", "v213", "v214", "v215"
#define CL_216_256 "v216", "v217", "v218", "v219", "v220", "v221", "v222", "v223", "v224", "v225", "v226", "v227", "v228", "v229", "v230", "v231", "v232", "v233", "v234", "v235", "v236", "v237", "v238", "v239", "v240", "v241", "v242", "v243", "v244", "v245", "v246", "v247", "v248", "v249", "v250", "v251", "v252", "v253", "v254", "v255"
#define CL_S "memory", "vcc", "scc", "s60", "s61", "s62", "s63", "s64", "s65", "s66", "s67", "s68" CL_T
#define TIMED_WAIT \
    "s_getpc_b64 s[62:63]\n\t" "1:\n\t" "s_memtime s[64:65]\n\t" "s_waitcnt lgkmcnt(0)\n\t" "s_sub_u32 s61, %0, s64\n\t" "s_cmp_gt_i32 s61, 124\n\t" "s_cbranch_scc1 1b\n\t" \
    "s_max_i32 s61, s61, 0\n\t" "s_and_b32 s61, s61, 124\n\t" "s_sub_u32 s61, 2f-1b, s61\n\t" "s_add_u32 s62, s62, s61\n\t" "s_addc_u32 s63, s63, 0\n\t" \
    "s_setpc_b64 s[62:63]\n\t" ".rept 31\n\t" "s_nop 0\n\t" ".endr\n\t" "2:\n\t"
#define QK_STEP0 \
    "v_xor_b32 v216, 0x400, %[mo]\n\t" "s_xor_b32 s60, %[sb], 0x800\n\t" "s_mov_b32 s66, 0x3e0293ee\n\t" "s_mov_b32 s67, 0x3e0293ee\n\t" XT(80, 81) \
    "s_add_u32 s61, %[kb], 0x1000\n\t" "buffer_load_dwordx4 v[16:19], %[kx], %[rs], %[kb] offen offset:0 sc1\n\t" \
    "buffer_load_dwordx4 v[20:23], %[kx], %[rs], %[kb] offen offset:2048 sc1\n\t" "buffer_load_dwordx4 v[24:27], %[kx], %[rs], s61 offen offset:0 sc1\n\t" \
    "buffer_load_dwordx4 v[28:31], %[kx], %[rs], s61 offen offset:2048 sc1\n\t" "buffer_load_dwordx4 v[200:203], %[kx], %[rs], %[kb] offen offset:256 sc1\n\t" \
    "buffer_load_dwordx4 v[204:207], %[kx], %[rs], %[kb] offen offset:2304 sc1\n\t" "buffer_load_dwordx4 v[208:211], %[kx], %[rs], s61 offen offset:256 sc1\n\t" \
    "buffer_load_dwordx4 v[212:215], %[kx], %[rs], s61 offen offset:2304 sc1\n\t" "s_barrier\n\t" "s_waitcnt vmcnt(7)\n\t" "ds_write_b128 %[kx], v[16:19] offset:0\n\t" \
    "s_waitcnt vmcnt(6)\n\t" "ds_write_b128 %[kx], v[20:23] offset:2048\n\t" "s_waitcnt vmcnt(5)\n\t" "ds_write_b128 %[kx], v[24:27] offset:4096\n\t" "s_waitcnt vmcnt(4)\n\t" \
    "ds_write_b128 %[kx], v[28:31] offset:6144\n\t" "s_waitcnt vmcnt(3)\n\t" "ds_write_b128 %[kx], v[200:203] offset:256\n\t" "s_waitcnt vmcnt(2)\n\t" \
    "ds_write_b128 %[kx], v[204:207] offset:2304\n\t" "s_waitcnt vmcnt(1)\n\t" "ds_write_b128 %[kx], v[208:211] offset:4352\n\t" "s_waitcnt vmcnt(0)\n\t" \
    "ds_write_b128 %[kx], v[212:215] offset:6400\n\t" "s_waitcnt lgkmcnt(0)\n\t" "v_mov_b64 v[208:209], 0\n\t" "v_mov_b64 v[210:211], 0\n\t" "v_mov_b64 v[212:213], 0\n\t" \
    "v_mov_b64 v[214:215], 0\n\t" XT(98, 99) "s_barrier\n\t" XT(82, 83) "buffer_load_dwordx4 v[240:243], %[mo], %[rs], %[sb] offen sc1\n\t" "v_mov_b64 v[224:225], 0\n\t" \
    "v_mov_b64 v[226:227], 0\n\t" "v_mov_b64 v[228:229], 0\n\t" "v_mov_b64 v[230:231], 0\n\t" "v_mov_b64 v[232:233], 0\n\t" "v_mov_b64 v[234:235], 0\n\t" \
    "v_mov_b64 v[236:237], 0\n\t" "v_mov_b64 v[238:239], 0\n\t" "buffer_load_dwordx4 v[244:247], v216, %[rs], %[sb] offen sc1\n\t" \
    "ds_read_b128 v[160:163], %[l0] offset:0\n\t" "ds_read_b128 v[164:167], %[l0] offset:1024\n\t" "ds_read_b128 v[168:171], %[l0] offset:2048\n\t" \
    "ds_read_b128 v[172:175], %[l0] offset:3072\n\t" "s_nop 3\n\t" "buffer_load_dwordx4 v[248:251], %[mo], %[rs], s60 offen sc1\n\t" \
    "ds_read_b128 v[176:179], %[l0] offset:4096\n\t" "ds_read_b128 v[180:183], %[l0] offset:5120\n\t" "ds_read_b128 v[184:187], %[l0] offset:6144\n\t" \
    "ds_read_b128 v[188:191], %[l0] offset:7168\n\t" "s_nop 3\n\t" "buffer_load_dwordx4 v[252:255], v216, %[rs], s60 offen sc1\n\t" "v_mov_b32 v219, 160512\n\t" \
    "v_mov_b64 v[220:221], -1\n\t" "v_mov_b64 v[222:223], -1\n\t" "v_mov_b32 v217, 0\n\t" "buffer_store_dwordx2 v[220:221], %[px], %[rs], %[pr] offen sc1\n\t" \
    "buffer_store_dword v220, %[pm], %[rs], %[pr] offen sc1\n\t" "global_load_dword v217, v217, %[fq] sc1\n\t" "10:\n\t" "s_waitcnt vmcnt(6)\n\t" XT(84, 85) \
    "v_smfmac_f32_16x16x64_bf16 v[224:227], v[240:243], a[0:7], %[ix]\n\t" "ds_write_b128 %[l1], v[240:243] offset:0\n\t" \
    "v_smfmac_f32_16x16x64_bf16 v[228:231], v[240:243], a[8:15], %[ix]\n\t" "v_smfmac_f32_16x16x64_bf16 v[232:235], v[240:243], v[32:39], %[ix]\n\t" \
    "s_waitcnt lgkmcnt(7)\n\t" "v_smfmac_f32_16x16x64_bf16 v[236:239], v[240:243], v[160:167], %[ix]\n\t" "ds_read_b128 v[160:163], %[l0] offset:8192\n\t" \
    "ds_read_b128 v[164:167], %[l0] offset:9216\n\t" "s_waitcnt vmcnt(5)\n\t" "v_smfmac_f32_16x16x64_bf16 v[224:227], v[244:247], a[16:23], %[ix]\n\t" \
    "ds_write_b128 %[l1], v[244:247] offset:1024\n\t" "v_smfmac_f32_16x16x64_bf16 v[228:231], v[244:247], a[24:31], %[ix]\n\t" \
    "v_smfmac_f32_16x16x64_bf16 v[232:235], v[244:247], v[40:47], %[ix]\n\t" "s_waitcnt lgkmcnt(8)\n\t" \
    "v_smfmac_f32_16x16x64_bf16 v[236:239], v[244:247], v[168:175], %[ix]\n\t" "ds_read_b128 v[168:171], %[l0] offset:10240\n\t" \
    "ds_read_b128 v[172:175], %[l0] offset:11264\n\t" "s_waitcnt vmcnt(4)\n\t" "v_smfmac_f32_16x16x64_bf16 v[224:227], v[248:251], a[32:39], %[ix]\n\t" \
    "ds_write_b128 %[l1], v[248:251] offset:2048\n\t" "v_smfmac_f32_16x16x64_bf16 v[228:231], v[248:251], a[40:47], %[ix]\n\t" "s_waitcnt lgkmcnt(3)\n\t" "s_barrier\n\t" \
    XT(86, 87) "v_smfmac_f32_16x16x64_bf16 v[232:235], v[248:251], v[48:55], %[ix]\n\t" "ds_read_b128 v[192:195], %[l1] offset:4096\n\t" \
    "ds_read_b128 v[196:199], %[l1] offset:8192\n\t" "v_smfmac_f32_16x16x64_bf16 v[236:239], v[248:251], v[176:183], %[ix]\n\t" \
    "ds_read_b128 v[176:179], %[l0] offset:12288\n\t" "ds_read_b128 v[180:183], %[l0] offset:13312\n\t" "s_waitcnt vmcnt(3)\n\t" \
    "v_smfmac_f32_16x16x64_bf16 v[224:227], v[252:255], a[48:55], %[ix]\n\t" "ds_write_b128 %[l1], v[252:255] offset:3072\n\t" \
    "v_smfmac_f32_16x16x64_bf16 v[228:231], v[252:255], a[56:63], %[ix]\n\t" "ds_read_b128 v[240:243], %[l1] offset:12288\n\t" \
    "ds_read_b128 v[244:247], %[l1] offset:5120\n\t" "v_smfmac_f32_16x16x64_bf16 v[232:235], v[252:255], v[56:63], %[ix]\n\t" "s_nop 1\n\t" \
    "v_smfmac_f32_16x16x64_bf16 v[236:239], v[252:255], v[184:191], %[ix]\n\t" "ds_read_b128 v[184:187], %[l0] offset:14336\n\t" "v_cmp_u_f32 vcc, v224, v225\n\t" \
    "v_cmp_u_f32 s[62:63], v226, v227\n\t" "s_waitcnt lgkmcnt(7)\n\t" "v_smfmac_f32_16x16x64_bf16 v[224:227], v[192:195], a[64:71], %[ix]\n\t" \
    "ds_read_b128 v[188:191], %[l0] offset:15360\n\t" "v_smfmac_f32_16x16x64_bf16 v[228:231], v[192:195], a[72:79], %[ix]\n\t" "s_or_b64 vcc, vcc, s[62:63]\n\t" \
    "s_cbranch_scc1 11f\n\t" "12:\n\t" "v_smfmac_f32_16x16x64_bf16 v[232:235], v[192:195], v[64:71], %[ix]\n\t" "s_waitcnt lgkmcnt(4)\n\t" "s_barrier\n\t" XT(88, 89) \
    "ds_read_b32 v218, v219\n\t" "v_smfmac_f32_16x16x64_bf16 v[236:239], v[192:195], v[160:167], %[ix]\n\t" "ds_read_b128 v[160:163], %[l0] offset:16384\n\t" \
    "ds_read_b128 v[164:167], %[l0] offset:17408\n\t" "v_smfmac_f32_16x16x64_bf16 v[224:227], v[196:199], a[80:87], %[ix]\n\t" \
    "ds_read_b128 v[248:251], %[l1] offset:9216\n\t" "ds_read_b128 v[252:255], %[l1] offset:13312\n\t" \
    "v_smfmac_f32_16x16x64_bf16 v[228:231], v[196:199], a[88:95], %[ix]\n\t" "ds_read_b128 v[192:195], %[l1] offset:6144\n\t" \
    "v_smfmac_f32_16x16x64_bf16 v[232:235], v[196:199], v[72:79], %[ix]\n\t" "v_smfmac_f32_16x16x64_bf16 v[236:239], v[196:199], v[168:175], %[ix]\n\t" \
    "s_waitcnt lgkmcnt(5)\n\t" "v_cmp_eq_u32 vcc, %[rt], v218\n\t" "v_smfmac_f32_16x16x64_bf16 v[224:227], v[240:243], a[96:103], %[ix]\n\t" "s_cbranch_vccnz 13f\n\t" \
    "ds_read_b128 v[168:171], %[l0] offset:18432\n\t" "v_smfmac_f32_16x16x64_bf16 v[228:231], v[240:243], a[104:111], %[ix]\n\t" \
    "ds_read_b128 v[172:175], %[l0] offset:19456\n\t" "ds_read_b128 v[196:199], %[l1] offset:10240\n\t" \
    "v_smfmac_f32_16x16x64_bf16 v[232:235], v[240:243], v[80:87], %[ix]\n\t" "v_smfmac_f32_16x16x64_bf16 v[236:239], v[240:243], v[176:183], %[ix]\n\t" \
    "ds_read_b128 v[176:179], %[l0] offset:20480\n\t" "ds_read_b128 v[180:183], %[l0] offset:21504\n\t" \
    "v_smfmac_f32_16x16x64_bf16 v[224:227], v[244:247], a[112:119], %[ix]\n\t" "ds_read_b128 v[240:243], %[l1] offset:14336\n\t" \
    "v_smfmac_f32_16x16x64_bf16 v[228:231], v[244:247], a[120:127], %[ix]\n\t" "v_smfmac_f32_16x16x64_bf16 v[232:235], v[244:247], v[88:95], %[ix]\n\t" \
    "v_smfmac_f32_16x16x64_bf16 v[236:239], v[244:247], v[184:191], %[ix]\n\t" "ds_read_b128 v[184:187], %[l0] offset:22528\n\t" \
    "ds_read_b128 v[188:191], %[l0] offset:23552\n\t" "s_waitcnt lgkmcnt(10)\n\t" "v_smfmac_f32_16x16x64_bf16 v[224:227], v[248:251], a[128:135], %[ix]\n\t" \
    "ds_read_b128 v[244:247], %[l1] offset:7168\n\t" "v_smfmac_f32_16x16x64_bf16 v[228:231], v[248:251], a[136:143], %[ix]\n\t" \
    "v_smfmac_f32_16x16x64_bf16 v[232:235], v[248:251], v[96:103], %[ix]\n\t" "v_smfmac_f32_16x16x64_bf16 v[236:239], v[248:251], v[160:167], %[ix]\n\t" \
    "ds_read_b128 v[160:163], %[l0] offset:24576\n\t" "ds_read_b128 v[164:167], %[l0] offset:25600\n\t" "s_waitcnt lgkmcnt(12)\n\t" \
    "v_smfmac_f32_16x16x64_bf16 v[224:227], v[252:255], a[144:151], %[ix]\n\t" "ds_read_b128 v[248:251], %[l1] offset:11264\n\t" \
    "v_smfmac_f32_16x16x64_bf16 v[228:231], v[252:255], a[152:159], %[ix]\n\t" "v_smfmac_f32_16x16x64_bf16 v[232:235], v[252:255], v[104:111], %[ix]\n\t" \
    "s_waitcnt lgkmcnt(10)\n\t" "v_smfmac_f32_16x16x64_bf16 v[236:239], v[252:255], v[168:175], %[ix]\n\t" "ds_read_b128 v[168:171], %[l0] offset:26624\n\t" \
    "ds_read_b128 v[172:175], %[l0] offset:27648\n\t" "v_smfmac_f32_16x16x64_bf16 v[224:227], v[192:195], a[160:167], %[ix]\n\t" \
    "ds_read_b128 v[252:255], %[l1] offset:15360\n\t" "v_smfmac_f32_16x16x64_bf16 v[228:231], v[192:195], a[168:175], %[ix]\n\t" "s_waitcnt lgkmcnt(12)\n\t" \
    "v_smfmac_f32_16x16x64_bf16 v[224:227], v[196:199], a[176:183], %[ix]\n\t" "v_smfmac_f32_16x16x64_bf16 v[228:231], v[196:199], a[184:191], %[ix]\n\t" \
    "s_waitcnt lgkmcnt(9)\n\t" "v_smfmac_f32_16x16x64_bf16 v[224:227], v[240:243], a[192:199], %[ix]\n\t" \
    "v_smfmac_f32_16x16x64_bf16 v[228:231], v[240:243], a[200:207], %[ix]\n\t" "s_waitcnt vmcnt(0)\n\t" "v_readfirstlane_b32 s62, v217\n\t" "s_waitcnt lgkmcnt(6)\n\t" \
    "v_smfmac_f32_16x16x64_bf16 v[224:227], v[244:247], a[208:215], %[ix]\n\t" "v_smfmac_f32_16x16x64_bf16 v[228:231], v[244:247], a[216:223], %[ix]\n\t" \
    "s_max_u32 %[sq], %[sq], s62\n\t" "s_waitcnt lgkmcnt(3)\n\t" "v_smfmac_f32_16x16x64_bf16 v[224:227], v[248:251], a[224:231], %[ix]\n\t" \
    "v_smfmac_f32_16x16x64_bf16 v[228:231], v[248:251], a[232:239], %[ix]\n\t" "s_waitcnt lgkmcnt(0)\n\t" \
    "v_smfmac_f32_16x16x64_bf16 v[224:227], v[252:255], a[240:247], %[ix]\n\t" "s_nop 1\n\t" "v_smfmac_f32_16x16x64_bf16 v[228:231], v[252:255], a[248:255], %[ix]\n\t" \
    XT(90, 91) "s_nop 1\n\t" "v_smfmac_f32_16x16x64_bf16 v[232:235], v[192:195], v[112:119], %[ix]\n\t" "s_nop 1\n\t" \
    "v_smfmac_f32_16x16x64_bf16 v[236:239], v[192:195], v[176:183], %[ix]\n\t" "ds_read_b128 v[176:179], %[l0] offset:28672\n\t" "v_add_f32 v224, v224, v225\n\t" \
    "v_add_f32 v226, v226, v227\n\t" "v_smfmac_f32_16x16x64_bf16 v[232:235], v[196:199], v[120:127], %[ix]\n\t" "ds_read_b128 v[180:183], %[l0] offset:29696\n\t" \
    "v_add_f32 v228, v228, v229\n\t" "v_add_f32 v230, v230, v231\n\t" "v_smfmac_f32_16x16x64_bf16 v[236:239], v[196:199], v[184:191], %[ix]\n\t" \
    "ds_read_b128 v[184:187], %[l0] offset:30720\n\t" "v_cvt_pk_bf16_f32 v218, v224, v228\n\t" "v_cvt_pk_bf16_f32 v219, v226, v230\n\t" \
    "v_smfmac_f32_16x16x64_bf16 v[232:235], v[240:243], v[128:135], %[ix]\n\t" "ds_write2_b32 %[qw], v218, v219 offset1:8\n\t" \
    "ds_read_b128 v[188:191], %[l0] offset:31744\n\t" "v_smfmac_f32_16x16x64_bf16 v[236:239], v[240:243], v[160:167], %[ix]\n\t" \
    "ds_read_b128 v[192:195], %[kx] offset:4352\n\t" "ds_read_b128 v[196:199], %[kx] offset:6400\n\t" \
    "v_smfmac_f32_16x16x64_bf16 v[232:235], v[244:247], v[136:143], %[ix]\n\t" "v_smfmac_f32_16x16x64_bf16 v[236:239], v[244:247], v[168:175], %[ix]\n\t" \
    "s_waitcnt lgkmcnt(3)\n\t" "s_barrier\n\t" "v_smfmac_f32_16x16x64_bf16 v[232:235], v[248:251], v[144:151], %[ix]\n\t" "ds_read_b128 v[216:219], %[l1] offset:24576\n\t" \
    "ds_read_b128 v[220:223], %[l1] offset:25600\n\t" "v_smfmac_f32_16x16x64_bf16 v[236:239], v[248:251], v[176:183], %[ix]\n\t" \
    "v_smfmac_f32_16x16x64_bf16 v[232:235], v[252:255], v[152:159], %[ix]\n\t" "s_waitcnt lgkmcnt(4)\n\t" \
    "v_smfmac_f32_16x16x64_bf16 v[236:239], v[252:255], v[184:191], %[ix]\n\t" XT(92, 93) "s_waitcnt lgkmcnt(1)\n\t" \
    "v_smfmac_f32_16x16x64_bf16 v[208:211], v[216:219], v[16:23], %[ix]\n\t" "s_nop 1\n\t" "v_smfmac_f32_16x16x64_bf16 v[212:215], v[216:219], v[200:207], %[ix]\n\t" \
    "s_waitcnt lgkmcnt(0)\n\t" "s_nop 0\n\t" "v_add_f32 v232, v232, v233\n\t" "v_add_f32 v234, v234, v235\n\t" "v_add_f32 v236, v236, v237\n\t" \
    "v_add_f32 v238, v238, v239\n\t" "v_cvt_pk_bf16_f32 v232, v232, v236\n\t" "v_cvt_pk_bf16_f32 v233, v234, v238\n\t" "ds_write2_b32 %[kw], v232, v233 offset1:68\n\t" \
    "v_smfmac_f32_16x16x64_bf16 v[208:211], v[220:223], v[24:31], %[ix]\n\t" "v_smfmac_f32_16x16x64_bf16 v[212:215], v[220:223], v[192:199], %[ix]\n\t" \
    "v_mov_b64 v[224:225], 0\n\t" "v_mov_b64 v[226:227], 0\n\t" "s_waitcnt lgkmcnt(0)\n\t" "s_barrier\n\t" XT(94, 95) "ds_read_b128 v[160:163], %[km] offset:0\n\t" \
    "ds_read_b128 v[164:167], %[km] offset:64\n\t" "ds_read_b128 v[168:171], %[km] offset:128\n\t" "ds_read_b128 v[172:175], %[km] offset:192\n\t" \
    "v_add_f32 v178, v208, v209\n\t" "v_add_f32 v179, v210, v211\n\t" "v_add_f32 v180, v212, v213\n\t" "v_add_f32 v181, v214, v215\n\t" "v_max_f32 v182, v178, v180\n\t" \
    "v_max_f32 v183, v179, v181\n\t" "s_nop 0\n\t" "v_max_f32_dpp v182, v182, v182 row_ror:8 row_mask:0xf bank_mask:0xf\n\t" \
    "v_max_f32_dpp v183, v183, v183 row_ror:8 row_mask:0xf bank_mask:0xf\n\t" "s_nop 0\n\t" "v_max_f32_dpp v182, v182, v182 row_ror:4 row_mask:0xf bank_mask:0xf\n\t" \
    "v_max_f32_dpp v183, v183, v183 row_ror:4 row_mask:0xf bank_mask:0xf\n\t" "s_nop 0\n\t" "v_max_f32_dpp v182, v182, v182 row_ror:2 row_mask:0xf bank_mask:0xf\n\t" \
    "v_max_f32_dpp v183, v183, v183 row_ror:2 row_mask:0xf bank_mask:0xf\n\t" "s_nop 0\n\t" "v_max_f32_dpp v182, v182, v182 row_ror:1 row_mask:0xf bank_mask:0xf\n\t" \
    "v_max_f32_dpp v183, v183, v183 row_ror:1 row_mask:0xf bank_mask:0xf\n\t" "s_waitcnt lgkmcnt(2)\n\t" \
    "v_smfmac_f32_16x16x64_bf16 v[224:227], v[216:219], v[160:167], %[ix]\n\t" "s_waitcnt lgkmcnt(0)\n\t" \
    "v_smfmac_f32_16x16x64_bf16 v[224:227], v[220:223], v[168:175], %[ix]\n\t" "s_nop 7\n\t" "v_add_f32 v176, v224, v225\n\t" "v_add_f32 v177, v226, v227\n\t" \
    "v_max_f32 v182, v176, v182\n\t" "v_max_f32 v183, v177, v183\n\t" "s_nop 0\n\t" "v_max_f32_dpp v182, v182, v182 row_ror:4 row_mask:0xf bank_mask:0xf\n\t" \
    "v_max_f32_dpp v183, v183, v183 row_ror:4 row_mask:0xf bank_mask:0xf\n\t" "s_nop 0\n\t" "v_max_f32_dpp v182, v182, v182 row_ror:2 row_mask:0xf bank_mask:0xf\n\t" \
    "v_max_f32_dpp v183, v183, v183 row_ror:2 row_mask:0xf bank_mask:0xf\n\t" "s_nop 0\n\t" "v_max_f32_dpp v182, v182, v182 row_ror:1 row_mask:0xf bank_mask:0xf\n\t" \
    "v_max_f32_dpp v183, v183, v183 row_ror:1 row_mask:0xf bank_mask:0xf\n\t" "v_pk_add_f32 v[178:179], v[178:179], v[182:183] neg_lo:[0,1] neg_hi:[0,1]\n\t" \
    "v_pk_add_f32 v[180:181], v[180:181], v[182:183] neg_lo:[0,1] neg_hi:[0,1]\n\t" "v_pk_add_f32 v[176:177], v[176:177], v[182:183] neg_lo:[0,1] neg_hi:[0,1]\n\t" \
    "v_pk_mul_f32 v[178:179], v[178:179], s[66:67]\n\t" "v_pk_mul_f32 v[180:181], v[180:181], s[66:67]\n\t" "v_pk_mul_f32 v[176:177], v[176:177], s[66:67]\n\t" \
    "v_exp_f32 v178, v178\n\t" "v_exp_f32 v179, v179\n\t" "v_exp_f32 v180, v180\n\t" "v_exp_f32 v181, v181\n\t" "v_exp_f32 v176, v176\n\t" \
    "v_pk_add_f32 v[182:183], v[178:179], v[180:181]\n\t" "v_exp_f32 v177, v177\n\t" "s_nop 0\n\t" "v_add_f32_dpp v182, v182, v182 row_ror:8 row_mask:0xf bank_mask:0xf\n\t" \
    "v_add_f32_dpp v183, v183, v183 row_ror:8 row_mask:0xf bank_mask:0xf\n\t" "v_add_f32 v182, v182, v176\n\t" "v_add_f32 v183, v183, v177\n\t" "s_nop 0\n\t" \
    "v_add_f32_dpp v182, v182, v182 row_ror:4 row_mask:0xf bank_mask:0xf\n\t" "v_add_f32_dpp v183, v183, v183 row_ror:4 row_mask:0xf bank_mask:0xf\n\t" "s_nop 0\n\t" \
    "v_add_f32_dpp v182, v182, v182 row_ror:2 row_mask:0xf bank_mask:0xf\n\t" "v_add_f32_dpp v183, v183, v183 row_ror:2 row_mask:0xf bank_mask:0xf\n\t" "s_nop 0\n\t" \
    "v_add_f32_dpp v182, v182, v182 row_ror:1 row_mask:0xf bank_mask:0xf\n\t" "v_add_f32_dpp v183, v183, v183 row_ror:1 row_mask:0xf bank_mask:0xf\n\t" \
    "v_rcp_f32 v182, v182\n\t" "v_rcp_f32 v183, v183\n\t" "s_nop 0\n\t" "v_pk_mul_f32 v[176:177], v[176:177], v[182:183]\n\t" \
    "v_pk_mul_f32 v[178:179], v[178:179], v[182:183]\n\t" "v_pk_mul_f32 v[180:181], v[180:181], v[182:183]\n\t" "v_cvt_pk_bf16_f32 v176, v176, v177\n\t" \
    "v_cvt_pk_bf16_f32 v178, v178, v179\n\t" "v_cvt_pk_bf16_f32 v180, v180, v181\n\t" "v_mov_b32_dpp v177, v176 quad_perm:[1,0,3,2] row_mask:0xf bank_mask:0xf\n\t" \
    "v_mov_b32_dpp v179, v178 quad_perm:[1,0,3,2] row_mask:0xf bank_mask:0xf\n\t" "v_mov_b32_dpp v181, v180 quad_perm:[1,0,3,2] row_mask:0xf bank_mask:0xf\n\t" \
    "v_perm_b32 v184, v179, v178, %[ps]\n\t" "v_perm_b32 v185, v181, v180, %[ps]\n\t" "buffer_store_dwordx2 v[184:185], %[px], %[rs], %[pb] offen sc1\n\t" \
    "v_perm_b32 v186, v177, v176, %[ps]\n\t" "buffer_store_dword v186, %[pm], %[rs], %[pb] offen sc1\n\t" XT(96, 97) LMARK "s_branch 99f\n\t" "11:\n\t" \
    "ds_read_b128 v[220:223], %[l1] offset:0\n\t" "s_waitcnt lgkmcnt(0)\n\t" "v_max3_u32 v218, v220, v221, v222\n\t" "v_max_u32 v218, v218, v223\n\t" \
    "ds_read_b128 v[220:223], %[l1] offset:1024\n\t" "s_waitcnt lgkmcnt(0)\n\t" "v_max3_u32 v218, v218, v220, v221\n\t" "v_max3_u32 v218, v218, v222, v223\n\t" \
    "ds_read_b128 v[220:223], %[l1] offset:2048\n\t" "s_waitcnt lgkmcnt(0)\n\t" "v_max3_u32 v218, v218, v220, v221\n\t" "v_max3_u32 v218, v218, v222, v223\n\t" \
    "ds_read_b128 v[220:223], %[l1] offset:3072\n\t" "s_waitcnt lgkmcnt(0)\n\t" "v_max3_u32 v218, v218, v220, v221\n\t" "v_max3_u32 v218, v218, v222, v223\n\t" \
    "v_cmp_eq_u32 vcc, -1, v218\n\t" "s_cbranch_vccz 12b\n\t" "s_cmp_eq_u32 %[rt], 1\n\t" "s_cbranch_scc1 12b\n\t" "v_mov_b32 v218, %[rt]\n\t" "ds_write_b32 v219, v218\n\t" \
    "s_waitcnt lgkmcnt(0)\n\t" "s_branch 12b\n\t" "13:\n\t" "s_sub_u32 %[rt], %[rt], 1\n\t" "s_waitcnt lgkmcnt(0)\n\t" XCNT(100) "14:\n\t" \
    "buffer_load_dwordx4 v[240:243], %[mo], %[rs], %[sb] offen sc1\n\t" "buffer_load_dwordx4 v[244:247], v216, %[rs], %[sb] offen sc1\n\t" \
    "buffer_load_dwordx4 v[248:251], %[mo], %[rs], s60 offen sc1\n\t" "buffer_load_dwordx4 v[252:255], v216, %[rs], s60 offen sc1\n\t" "s_waitcnt vmcnt(0)\n\t" \
    "v_max_u32 v218, v240, v241\n\t" "v_max3_u32 v218, v218, v242, v243\n\t" "v_max3_u32 v218, v218, v244, v245\n\t" "v_max3_u32 v218, v218, v246, v247\n\t" \
    "v_max3_u32 v218, v218, v248, v249\n\t" "v_max3_u32 v218, v218, v250, v251\n\t" "v_max3_u32 v218, v218, v252, v253\n\t" "v_max3_u32 v218, v218, v254, v255\n\t" \
    "v_cmp_eq_u32 vcc, -1, v218\n\t" "s_cbranch_vccz 15f\n\t" "s_cmp_eq_u32 %[bd], 0\n\t" "s_cbranch_scc1 15f\n\t" "s_sub_u32 %[bd], %[bd], 1\n\t" XCNT(101) \
    "s_branch 14b\n\t" "15:\n\t" "ds_read_b128 v[160:163], %[l0] offset:0\n\t" "ds_read_b128 v[164:167], %[l0] offset:1024\n\t" \
    "ds_read_b128 v[168:171], %[l0] offset:2048\n\t" "ds_read_b128 v[172:175], %[l0] offset:3072\n\t" "ds_read_b128 v[176:179], %[l0] offset:4096\n\t" \
    "ds_read_b128 v[180:183], %[l0] offset:5120\n\t" "ds_read_b128 v[184:187], %[l0] offset:6144\n\t" "ds_read_b128 v[188:191], %[l0] offset:7168\n\t" \
    "v_mov_b64 v[224:225], 0\n\t" "v_mov_b64 v[226:227], 0\n\t" "v_mov_b64 v[228:229], 0\n\t" "v_mov_b64 v[230:231], 0\n\t" "v_mov_b64 v[232:233], 0\n\t" \
    "v_mov_b64 v[234:235], 0\n\t" "v_mov_b64 v[236:237], 0\n\t" "v_mov_b64 v[238:239], 0\n\t" "s_barrier\n\t" "s_branch 10b\n\t" "99:\n\t"
#define QK_STEP1 \
    "v_xor_b32 v216, 0x400, %[mo]\n\t" "s_xor_b32 s60, %[sb], 0x800\n\t" "s_add_u32 s61, %[qb], 0x1000\n\t" "s_mov_b32 s66, 0x3e0293ee\n\t" "s_mov_b32 s67, 0x3e0293ee\n\t" \
    XT(80, 81) XT(98, 99) "s_barrier\n\t" XT(82, 83) "buffer_load_dwordx4 v[240:243], %[mo], %[rs], %[sb] offen sc1\n\t" "v_mov_b64 v[224:225], 0\n\t" \
    "v_mov_b64 v[226:227], 0\n\t" "v_mov_b64 v[228:229], 0\n\t" "v_mov_b64 v[230:231], 0\n\t" "v_mov_b64 v[232:233], 0\n\t" "v_mov_b64 v[234:235], 0\n\t" \
    "v_mov_b64 v[236:237], 0\n\t" "v_mov_b64 v[238:239], 0\n\t" "buffer_load_dwordx4 v[244:247], v216, %[rs], %[sb] offen sc1\n\t" \
    "ds_read_b128 v[160:163], %[l0] offset:0\n\t" "ds_read_b128 v[164:167], %[l0] offset:1024\n\t" "ds_read_b128 v[168:171], %[l0] offset:2048\n\t" \
    "ds_read_b128 v[172:175], %[l0] offset:3072\n\t" "s_nop 3\n\t" "buffer_load_dwordx4 v[248:251], %[mo], %[rs], s60 offen sc1\n\t" \
    "ds_read_b128 v[176:179], %[l0] offset:4096\n\t" "ds_read_b128 v[180:183], %[l0] offset:5120\n\t" "ds_read_b128 v[184:187], %[l0] offset:6144\n\t" \
    "ds_read_b128 v[188:191], %[l0] offset:7168\n\t" "s_nop 3\n\t" "buffer_load_dwordx4 v[252:255], v216, %[rs], s60 offen sc1\n\t" "v_mov_b32 v219, 160512\n\t" \
    "v_mov_b64 v[220:221], -1\n\t" "v_mov_b64 v[222:223], -1\n\t" "buffer_store_dwordx2 v[220:221], %[px], %[rs], %[tr] offen sc1\n\t" \
    "buffer_store_dwordx2 v[220:221], %[pm], %[rs], %[tr] offen sc1\n\t" "v_mov_b32 v208, 0xd028965f\n\t" "v_mov_b32 v209, 0xff800000\n\t" "s_mov_b32 s62, 0\n\t" \
    "s_mov_b32 s63, -1\n\t" "v_mov_b32 v210, 0\n\t" "v_cmp_gt_i32 vcc, 0, %[ps]\n\t" "v_cndmask_b32 v204, 0, v209, s[62:63]\n\t" "s_nop 0\n\t" \
    "v_cndmask_b32 v200, 0, v208, vcc\n\t" "v_cmp_gt_i32 vcc, 1, %[ps]\n\t" "v_cndmask_b32 v205, 0, v209, s[62:63]\n\t" "s_nop 0\n\t" "v_cndmask_b32 v201, 0, v208, vcc\n\t" \
    "v_cmp_gt_i32 vcc, 2, %[ps]\n\t" "v_cndmask_b32 v206, 0, v209, s[62:63]\n\t" "s_nop 0\n\t" "v_cndmask_b32 v202, 0, v208, vcc\n\t" "v_cmp_gt_i32 vcc, 3, %[ps]\n\t" \
    "v_cndmask_b32 v207, 0, v209, s[62:63]\n\t" "s_nop 0\n\t" "v_cndmask_b32 v203, 0, v208, vcc\n\t" "20:\n\t" "s_waitcnt vmcnt(5)\n\t" XT(84, 85) \
    "v_smfmac_f32_16x16x64_bf16 v[224:227], v[240:243], a[0:7], %[ix]\n\t" "ds_write_b128 %[l1], v[240:243] offset:4096\n\t" \
    "v_smfmac_f32_16x16x64_bf16 v[228:231], v[240:243], a[8:15], %[ix]\n\t" "v_smfmac_f32_16x16x64_bf16 v[232:235], v[240:243], v[32:39], %[ix]\n\t" \
    "s_waitcnt lgkmcnt(7)\n\t" "v_smfmac_f32_16x16x64_bf16 v[236:239], v[240:243], v[160:167], %[ix]\n\t" "ds_read_b128 v[160:163], %[l0] offset:8192\n\t" \
    "ds_read_b128 v[164:167], %[l0] offset:9216\n\t" "s_waitcnt vmcnt(4)\n\t" "v_smfmac_f32_16x16x64_bf16 v[224:227], v[244:247], a[16:23], %[ix]\n\t" \
    "ds_write_b128 %[l1], v[244:247] offset:5120\n\t" "v_smfmac_f32_16x16x64_bf16 v[228:231], v[244:247], a[24:31], %[ix]\n\t" \
    "v_smfmac_f32_16x16x64_bf16 v[232:235], v[244:247], v[40:47], %[ix]\n\t" "s_waitcnt lgkmcnt(8)\n\t" \
    "v_smfmac_f32_16x16x64_bf16 v[236:239], v[244:247], v[168:175], %[ix]\n\t" "ds_read_b128 v[168:171], %[l0] offset:10240\n\t" \
    "ds_read_b128 v[172:175], %[l0] offset:11264\n\t" "s_waitcnt vmcnt(3)\n\t" "v_smfmac_f32_16x16x64_bf16 v[224:227], v[248:251], a[32:39], %[ix]\n\t" \
    "ds_write_b128 %[l1], v[248:251] offset:6144\n\t" "v_smfmac_f32_16x16x64_bf16 v[228:231], v[248:251], a[40:47], %[ix]\n\t" "s_waitcnt lgkmcnt(3)\n\t" "s_barrier\n\t" \
    XT(86, 87) "v_smfmac_f32_16x16x64_bf16 v[232:235], v[248:251], v[48:55], %[ix]\n\t" "ds_read_b128 v[192:195], %[l1] offset:8192\n\t" \
    "ds_read_b128 v[196:199], %[l1] offset:12288\n\t" "v_smfmac_f32_16x16x64_bf16 v[236:239], v[248:251], v[176:183], %[ix]\n\t" \
    "ds_read_b128 v[176:179], %[l0] offset:12288\n\t" "ds_read_b128 v[180:183], %[l0] offset:13312\n\t" "s_waitcnt vmcnt(2)\n\t" \
    "v_smfmac_f32_16x16x64_bf16 v[224:227], v[252:255], a[48:55], %[ix]\n\t" "ds_write_b128 %[l1], v[252:255] offset:7168\n\t" \
    "v_smfmac_f32_16x16x64_bf16 v[228:231], v[252:255], a[56:63], %[ix]\n\t" "ds_read_b128 v[240:243], %[l1] offset:0\n\t" "ds_read_b128 v[244:247], %[l1] offset:9216\n\t" \
    "v_smfmac_f32_16x16x64_bf16 v[232:235], v[252:255], v[56:63], %[ix]\n\t" "s_nop 1\n\t" "v_smfmac_f32_16x16x64_bf16 v[236:239], v[252:255], v[184:191], %[ix]\n\t" \
    "ds_read_b128 v[184:187], %[l0] offset:14336\n\t" "v_cmp_u_f32 vcc, v224, v225\n\t" "v_cmp_u_f32 s[62:63], v226, v227\n\t" "s_waitcnt lgkmcnt(7)\n\t" \
    "v_smfmac_f32_16x16x64_bf16 v[224:227], v[192:195], a[64:71], %[ix]\n\t" "ds_read_b128 v[188:191], %[l0] offset:15360\n\t" \
    "v_smfmac_f32_16x16x64_bf16 v[228:231], v[192:195], a[72:79], %[ix]\n\t" "s_or_b64 vcc, vcc, s[62:63]\n\t" "s_cbranch_scc1 21f\n\t" "22:\n\t" \
    "v_smfmac_f32_16x16x64_bf16 v[232:235], v[192:195], v[64:71], %[ix]\n\t" "s_waitcnt lgkmcnt(4)\n\t" "s_barrier\n\t" XT(88, 89) "ds_read_b32 v218, v219\n\t" \
    "v_smfmac_f32_16x16x64_bf16 v[236:239], v[192:195], v[160:167], %[ix]\n\t" "ds_read_b128 v[160:163], %[l0] offset:16384\n\t" \
    "ds_read_b128 v[164:167], %[l0] offset:17408\n\t" "v_smfmac_f32_16x16x64_bf16 v[224:227], v[196:199], a[80:87], %[ix]\n\t" \
    "ds_read_b128 v[248:251], %[l1] offset:13312\n\t" "ds_read_b128 v[252:255], %[l1] offset:1024\n\t" \
    "v_smfmac_f32_16x16x64_bf16 v[228:231], v[196:199], a[88:95], %[ix]\n\t" "ds_read_b128 v[192:195], %[l1] offset:10240\n\t" \
    "v_smfmac_f32_16x16x64_bf16 v[232:235], v[196:199], v[72:79], %[ix]\n\t" "v_smfmac_f32_16x16x64_bf16 v[236:239], v[196:199], v[168:175], %[ix]\n\t" \
    "s_waitcnt lgkmcnt(5)\n\t" "v_cmp_eq_u32 vcc, %[rt], v218\n\t" "v_smfmac_f32_16x16x64_bf16 v[224:227], v[240:243], a[96:103], %[ix]\n\t" "s_cbranch_vccnz 23f\n\t" \
    "ds_read_b128 v[168:171], %[l0] offset:18432\n\t" "v_smfmac_f32_16x16x64_bf16 v[228:231], v[240:243], a[104:111], %[ix]\n\t" \
    "ds_read_b128 v[172:175], %[l0] offset:19456\n\t" "ds_read_b128 v[196:199], %[l1] offset:14336\n\t" \
    "v_smfmac_f32_16x16x64_bf16 v[232:235], v[240:243], v[80:87], %[ix]\n\t" "buffer_load_dwordx4 v[16:19], %[hq], %[rs], %[qb] offen offset:0 sc1\n\t" \
    "v_smfmac_f32_16x16x64_bf16 v[236:239], v[240:243], v[176:183], %[ix]\n\t" "ds_read_b128 v[176:179], %[l0] offset:20480\n\t" \
    "ds_read_b128 v[180:183], %[l0] offset:21504\n\t" "v_smfmac_f32_16x16x64_bf16 v[224:227], v[244:247], a[112:119], %[ix]\n\t" \
    "ds_read_b128 v[240:243], %[l1] offset:2048\n\t" "v_smfmac_f32_16x16x64_bf16 v[228:231], v[244:247], a[120:127], %[ix]\n\t" \
    "v_smfmac_f32_16x16x64_bf16 v[232:235], v[244:247], v[88:95], %[ix]\n\t" "buffer_load_dwordx4 v[20:23], %[hq], %[rs], %[qb] offen offset:2048 sc1\n\t" \
    "v_smfmac_f32_16x16x64_bf16 v[236:239], v[244:247], v[184:191], %[ix]\n\t" "ds_read_b128 v[184:187], %[l0] offset:22528\n\t" \
    "ds_read_b128 v[188:191], %[l0] offset:23552\n\t" "s_waitcnt lgkmcnt(10)\n\t" "v_smfmac_f32_16x16x64_bf16 v[224:227], v[248:251], a[128:135], %[ix]\n\t" \
    "ds_read_b128 v[244:247], %[l1] offset:11264\n\t" "v_smfmac_f32_16x16x64_bf16 v[228:231], v[248:251], a[136:143], %[ix]\n\t" \
    "v_smfmac_f32_16x16x64_bf16 v[232:235], v[248:251], v[96:103], %[ix]\n\t" "buffer_load_dwordx4 v[24:27], %[hq], %[rs], s61 offen offset:0 sc1\n\t" \
    "v_smfmac_f32_16x16x64_bf16 v[236:239], v[248:251], v[160:167], %[ix]\n\t" "ds_read_b128 v[160:163], %[l0] offset:24576\n\t" \
    "ds_read_b128 v[164:167], %[l0] offset:25600\n\t" "s_waitcnt lgkmcnt(12)\n\t" "v_smfmac_f32_16x16x64_bf16 v[224:227], v[252:255], a[144:151], %[ix]\n\t" \
    "ds_read_b128 v[248:251], %[l1] offset:15360\n\t" "v_smfmac_f32_16x16x64_bf16 v[228:231], v[252:255], a[152:159], %[ix]\n\t" \
    "v_smfmac_f32_16x16x64_bf16 v[232:235], v[252:255], v[104:111], %[ix]\n\t" "buffer_load_dwordx4 v[28:31], %[hq], %[rs], s61 offen offset:2048 sc1\n\t" \
    "s_waitcnt lgkmcnt(10)\n\t" "v_smfmac_f32_16x16x64_bf16 v[236:239], v[252:255], v[168:175], %[ix]\n\t" "ds_read_b128 v[168:171], %[l0] offset:26624\n\t" \
    "ds_read_b128 v[172:175], %[l0] offset:27648\n\t" "v_smfmac_f32_16x16x64_bf16 v[224:227], v[192:195], a[160:167], %[ix]\n\t" \
    "ds_read_b128 v[252:255], %[l1] offset:3072\n\t" "v_smfmac_f32_16x16x64_bf16 v[228:231], v[192:195], a[168:175], %[ix]\n\t" "s_waitcnt lgkmcnt(12)\n\t" \
    "v_smfmac_f32_16x16x64_bf16 v[224:227], v[196:199], a[176:183], %[ix]\n\t" "v_smfmac_f32_16x16x64_bf16 v[228:231], v[196:199], a[184:191], %[ix]\n\t" \
    "s_waitcnt lgkmcnt(9)\n\t" "v_smfmac_f32_16x16x64_bf16 v[224:227], v[240:243], a[192:199], %[ix]\n\t" \
    "v_smfmac_f32_16x16x64_bf16 v[228:231], v[240:243], a[200:207], %[ix]\n\t" "s_waitcnt lgkmcnt(6)\n\t" \
    "v_smfmac_f32_16x16x64_bf16 v[224:227], v[244:247], a[208:215], %[ix]\n\t" "v_smfmac_f32_16x16x64_bf16 v[228:231], v[244:247], a[216:223], %[ix]\n\t" \
    "s_waitcnt vmcnt(4)\n\t" "s_waitcnt lgkmcnt(3)\n\t" "v_smfmac_f32_16x16x64_bf16 v[224:227], v[248:251], a[224:231], %[ix]\n\t" \
    "v_smfmac_f32_16x16x64_bf16 v[228:231], v[248:251], a[232:239], %[ix]\n\t" "s_waitcnt lgkmcnt(0)\n\t" \
    "v_smfmac_f32_16x16x64_bf16 v[224:227], v[252:255], a[240:247], %[ix]\n\t" "s_nop 1\n\t" "v_smfmac_f32_16x16x64_bf16 v[228:231], v[252:255], a[248:255], %[ix]\n\t" \
    XT(90, 91) "s_nop 1\n\t" "v_smfmac_f32_16x16x64_bf16 v[232:235], v[192:195], v[112:119], %[ix]\n\t" "s_nop 1\n\t" \
    "v_smfmac_f32_16x16x64_bf16 v[236:239], v[192:195], v[176:183], %[ix]\n\t" "ds_read_b128 v[176:179], %[l0] offset:28672\n\t" "v_add_f32 v224, v224, v225\n\t" \
    "v_add_f32 v226, v226, v227\n\t" "v_smfmac_f32_16x16x64_bf16 v[232:235], v[196:199], v[120:127], %[ix]\n\t" "ds_read_b128 v[180:183], %[l0] offset:29696\n\t" \
    "v_add_f32 v228, v228, v229\n\t" "v_add_f32 v230, v230, v231\n\t" "v_smfmac_f32_16x16x64_bf16 v[236:239], v[196:199], v[184:191], %[ix]\n\t" \
    "ds_read_b128 v[184:187], %[l0] offset:30720\n\t" "v_cvt_pk_bf16_f32 v218, v224, v228\n\t" "v_cvt_pk_bf16_f32 v219, v226, v230\n\t" \
    "v_smfmac_f32_16x16x64_bf16 v[232:235], v[240:243], v[128:135], %[ix]\n\t" "ds_write2_b32 %[qw], v218, v219 offset1:8\n\t" \
    "ds_read_b128 v[188:191], %[l0] offset:31744\n\t" "v_smfmac_f32_16x16x64_bf16 v[236:239], v[240:243], v[160:167], %[ix]\n\t" \
    "v_smfmac_f32_16x16x64_bf16 v[232:235], v[244:247], v[136:143], %[ix]\n\t" "v_smfmac_f32_16x16x64_bf16 v[236:239], v[244:247], v[168:175], %[ix]\n\t" \
    "s_waitcnt lgkmcnt(1)\n\t" "s_barrier\n\t" "v_smfmac_f32_16x16x64_bf16 v[232:235], v[248:251], v[144:151], %[ix]\n\t" \
    "v_smfmac_f32_16x16x64_bf16 v[236:239], v[248:251], v[176:183], %[ix]\n\t" "v_smfmac_f32_16x16x64_bf16 v[232:235], v[252:255], v[152:159], %[ix]\n\t" \
    "s_waitcnt lgkmcnt(0)\n\t" "v_smfmac_f32_16x16x64_bf16 v[236:239], v[252:255], v[184:191], %[ix]\n\t" XT(92, 93) "s_nop 6\n\t" "v_add_f32 v232, v232, v233\n\t" \
    "v_add_f32 v234, v234, v235\n\t" "v_add_f32 v236, v236, v237\n\t" "v_add_f32 v238, v238, v239\n\t" "v_cvt_pk_bf16_f32 v232, v232, v236\n\t" \
    "v_cvt_pk_bf16_f32 v233, v234, v238\n\t" "ds_write2_b32 %[kw], v232, v233 offset1:68\n\t" "s_waitcnt lgkmcnt(0)\n\t" "s_barrier\n\t" XT(94, 95) \
    "s_cmp_ge_u32 %[sq], %[sn]\n\t" "s_cbranch_scc1 277f\n\t" "275:\n\t" "global_load_dword v240, v210, %[fq] sc1\n\t" "s_waitcnt vmcnt(0)\n\t" \
    "v_readfirstlane_b32 %[sq], v240\n\t" "s_nop 3\n\t" "s_cmp_ge_u32 %[sq], %[sn]\n\t" "s_cbranch_scc1 276f\n\t" "s_cmp_eq_u32 %[bd], 0\n\t" "s_cbranch_scc1 276f\n\t" \
    "s_sub_u32 %[bd], %[bd], 1\n\t" "s_sleep 2\n\t" "s_branch 275b\n\t" "276:\n\t" "buffer_load_dwordx4 v[16:19], %[hq], %[rs], %[qb] offen offset:0 sc1\n\t" \
    "buffer_load_dwordx4 v[20:23], %[hq], %[rs], %[qb] offen offset:2048 sc1\n\t" "buffer_load_dwordx4 v[24:27], %[hq], %[rs], s61 offen offset:0 sc1\n\t" \
    "buffer_load_dwordx4 v[28:31], %[hq], %[rs], s61 offen offset:2048 sc1\n\t" "277:\n\t" "s_nop 3\n\t" "ds_read_b128 v[160:163], %[kx] offset:0\n\t" \
    "ds_read_b128 v[164:167], %[kx] offset:2048\n\t" "ds_read_b128 v[168:171], %[kx] offset:4096\n\t" "ds_read_b128 v[172:175], %[kx] offset:6144\n\t" \
    "ds_read_b128 v[224:227], %[km] offset:0\n\t" "ds_read_b128 v[228:231], %[km] offset:64\n\t" "ds_read_b128 v[232:235], %[km] offset:128\n\t" \
    "ds_read_b128 v[236:239], %[km] offset:192\n\t" "s_waitcnt vmcnt(0)\n\t" "global_load_dword v240, v210, %[fq] sc1\n\t" "s_waitcnt lgkmcnt(7)\n\t" \
    "v_mfma_f32_16x16x32_bf16 v[248:251], v[160:163], v[16:19], v[200:203]\n\t" "s_waitcnt lgkmcnt(6)\n\t" \
    "v_mfma_f32_16x16x32_bf16 v[248:251], v[164:167], v[20:23], v[248:251]\n\t" "s_waitcnt lgkmcnt(5)\n\t" \
    "v_mfma_f32_16x16x32_bf16 v[248:251], v[168:171], v[24:27], v[248:251]\n\t" "s_waitcnt lgkmcnt(4)\n\t" \
    "v_mfma_f32_16x16x32_bf16 v[248:251], v[172:175], v[28:31], v[248:251]\n\t" "s_waitcnt lgkmcnt(3)\n\t" \
    "v_mfma_f32_16x16x32_bf16 v[244:247], v[224:227], v[16:19], v[204:207]\n\t" "s_waitcnt lgkmcnt(2)\n\t" \
    "v_mfma_f32_16x16x32_bf16 v[244:247], v[228:231], v[20:23], v[244:247]\n\t" "s_waitcnt lgkmcnt(1)\n\t" \
    "v_mfma_f32_16x16x32_bf16 v[244:247], v[232:235], v[24:27], v[244:247]\n\t" "s_waitcnt lgkmcnt(0)\n\t" \
    "v_mfma_f32_16x16x32_bf16 v[244:247], v[236:239], v[28:31], v[244:247]\n\t" "s_nop 3\n\t" "v_max3_f32 v242, v248, v249, v250\n\t" "v_max_f32 v242, v242, v251\n\t" \
    "s_nop 1\n\t" "v_max3_f32 v242, v242, v244, v245\n\t" "v_max3_f32 v242, v242, v246, v247\n\t" "v_mov_b32 v243, v242\n\t" "s_nop 1\n\t" \
    "v_permlane32_swap_b32 v242, v243\n\t" "v_max_f32 v242, v242, v243\n\t" "v_mov_b32 v243, v242\n\t" "s_nop 1\n\t" "v_permlane16_swap_b32 v242, v243\n\t" \
    "v_max_f32 v242, v242, v243\n\t" "v_pk_add_f32 v[244:245], v[244:245], v[242:243] op_sel_hi:[1,0] neg_lo:[0,1] neg_hi:[0,1]\n\t" \
    "v_pk_add_f32 v[246:247], v[246:247], v[242:243] op_sel_hi:[1,0] neg_lo:[0,1] neg_hi:[0,1]\n\t" \
    "v_pk_add_f32 v[248:249], v[248:249], v[242:243] op_sel_hi:[1,0] neg_lo:[0,1] neg_hi:[0,1]\n\t" \
    "v_pk_add_f32 v[250:251], v[250:251], v[242:243] op_sel_hi:[1,0] neg_lo:[0,1] neg_hi:[0,1]\n\t" "v_pk_mul_f32 v[244:245], v[244:245], s[66:67]\n\t" \
    "v_pk_mul_f32 v[246:247], v[246:247], s[66:67]\n\t" "v_pk_mul_f32 v[248:249], v[248:249], s[66:67]\n\t" "v_pk_mul_f32 v[250:251], v[250:251], s[66:67]\n\t" \
    "v_exp_f32 v244, v244\n\t" "v_exp_f32 v245, v245\n\t" "v_exp_f32 v246, v246\n\t" "v_exp_f32 v247, v247\n\t" "v_exp_f32 v248, v248\n\t" "v_exp_f32 v249, v249\n\t" \
    "v_exp_f32 v250, v250\n\t" "v_exp_f32 v251, v251\n\t" "v_pk_add_f32 v[242:243], v[244:245], v[246:247]\n\t" "v_pk_add_f32 v[242:243], v[242:243], v[248:249]\n\t" \
    "v_pk_add_f32 v[242:243], v[242:243], v[250:251]\n\t" "v_add_f32 v242, v242, v243\n\t" "v_mov_b32 v243, v242\n\t" "s_nop 1\n\t" "v_permlane32_swap_b32 v242, v243\n\t" \
    "v_add_f32 v242, v242, v243\n\t" "v_mov_b32 v243, v242\n\t" "s_nop 1\n\t" "v_permlane16_swap_b32 v242, v243\n\t" "v_add_f32 v242, v242, v243\n\t" \
    "v_rcp_f32 v242, v242\n\t" "s_nop 0\n\t" "v_pk_mul_f32 v[244:245], v[244:245], v[242:243] op_sel_hi:[1,0]\n\t" \
    "v_pk_mul_f32 v[246:247], v[246:247], v[242:243] op_sel_hi:[1,0]\n\t" "v_pk_mul_f32 v[248:249], v[248:249], v[242:243] op_sel_hi:[1,0]\n\t" \
    "v_pk_mul_f32 v[250:251], v[250:251], v[242:243] op_sel_hi:[1,0]\n\t" "v_cvt_pk_bf16_f32 v162, v244, v245\n\t" "v_cvt_pk_bf16_f32 v163, v246, v247\n\t" \
    "v_cvt_pk_bf16_f32 v164, v248, v249\n\t" "v_cvt_pk_bf16_f32 v165, v250, v251\n\t" "s_barrier\n\t" "buffer_store_dwordx2 v[164:165], %[px], %[rs], %[tb] offen sc1\n\t" \
    "buffer_store_dwordx2 v[162:163], %[pm], %[rs], %[tb] offen sc1\n\t" "s_waitcnt vmcnt(2)\n\t" "v_readfirstlane_b32 s62, v240\n\t" "s_nop 3\n\t" \
    "s_max_u32 %[sq], %[sq], s62\n\t" XT(96, 97) "s_branch 99f\n\t" "21:\n\t" "ds_read_b128 v[220:223], %[l1] offset:4096\n\t" "s_waitcnt lgkmcnt(0)\n\t" \
    "v_max3_u32 v218, v220, v221, v222\n\t" "v_max_u32 v218, v218, v223\n\t" "ds_read_b128 v[220:223], %[l1] offset:5120\n\t" "s_waitcnt lgkmcnt(0)\n\t" \
    "v_max3_u32 v218, v218, v220, v221\n\t" "v_max3_u32 v218, v218, v222, v223\n\t" "ds_read_b128 v[220:223], %[l1] offset:6144\n\t" "s_waitcnt lgkmcnt(0)\n\t" \
    "v_max3_u32 v218, v218, v220, v221\n\t" "v_max3_u32 v218, v218, v222, v223\n\t" "ds_read_b128 v[220:223], %[l1] offset:7168\n\t" "s_waitcnt lgkmcnt(0)\n\t" \
    "v_max3_u32 v218, v218, v220, v221\n\t" "v_max3_u32 v218, v218, v222, v223\n\t" "v_cmp_eq_u32 vcc, -1, v218\n\t" "s_cbranch_vccz 22b\n\t" "s_cmp_eq_u32 %[rt], 1\n\t" \
    "s_cbranch_scc1 22b\n\t" "v_mov_b32 v218, %[rt]\n\t" "ds_write_b32 v219, v218\n\t" "s_waitcnt lgkmcnt(0)\n\t" "s_branch 22b\n\t" "23:\n\t" "s_sub_u32 %[rt], %[rt], 1\n\t" \
    "s_waitcnt lgkmcnt(0)\n\t" XCNT(100) "24:\n\t" "buffer_load_dwordx4 v[240:243], %[mo], %[rs], %[sb] offen sc1\n\t" \
    "buffer_load_dwordx4 v[244:247], v216, %[rs], %[sb] offen sc1\n\t" "buffer_load_dwordx4 v[248:251], %[mo], %[rs], s60 offen sc1\n\t" \
    "buffer_load_dwordx4 v[252:255], v216, %[rs], s60 offen sc1\n\t" "s_waitcnt vmcnt(0)\n\t" "v_max_u32 v218, v240, v241\n\t" "v_max3_u32 v218, v218, v242, v243\n\t" \
    "v_max3_u32 v218, v218, v244, v245\n\t" "v_max3_u32 v218, v218, v246, v247\n\t" "v_max3_u32 v218, v218, v248, v249\n\t" "v_max3_u32 v218, v218, v250, v251\n\t" \
    "v_max3_u32 v218, v218, v252, v253\n\t" "v_max3_u32 v218, v218, v254, v255\n\t" "v_cmp_eq_u32 vcc, -1, v218\n\t" "s_cbranch_vccz 25f\n\t" "s_cmp_eq_u32 %[bd], 0\n\t" \
    "s_cbranch_scc1 25f\n\t" "s_sub_u32 %[bd], %[bd], 1\n\t" XCNT(101) "s_branch 24b\n\t" "25:\n\t" "ds_read_b128 v[160:163], %[l0] offset:0\n\t" \
    "ds_read_b128 v[164:167], %[l0] offset:1024\n\t" "ds_read_b128 v[168:171], %[l0] offset:2048\n\t" "ds_read_b128 v[172:175], %[l0] offset:3072\n\t" \
    "ds_read_b128 v[176:179], %[l0] offset:4096\n\t" "ds_read_b128 v[180:183], %[l0] offset:5120\n\t" "ds_read_b128 v[184:187], %[l0] offset:6144\n\t" \
    "ds_read_b128 v[188:191], %[l0] offset:7168\n\t" "v_mov_b64 v[224:225], 0\n\t" "v_mov_b64 v[226:227], 0\n\t" "v_mov_b64 v[228:229], 0\n\t" "v_mov_b64 v[230:231], 0\n\t" \
    "v_mov_b64 v[232:233], 0\n\t" "v_mov_b64 v[234:235], 0\n\t" "v_mov_b64 v[236:237], 0\n\t" "v_mov_b64 v[238:239], 0\n\t" "s_barrier\n\t" "s_branch 20b\n\t" "99:\n\t"
#define QK_STEP2 \
    "v_xor_b32 v216, 0x400, %[mo]\n\t" "s_xor_b32 s60, %[sb], 0x800\n\t" "s_add_u32 s61, %[qb], 0x1000\n\t" "s_mov_b32 s66, 0x3e0293ee\n\t" "s_mov_b32 s67, 0x3e0293ee\n\t" \
    XT(80, 81) XT(98, 99) "s_barrier\n\t" XT(82, 83) "buffer_load_dwordx4 v[240:243], %[mo], %[rs], %[sb] offen sc1\n\t" "v_mov_b64 v[224:225], 0\n\t" \
    "v_mov_b64 v[226:227], 0\n\t" "v_mov_b64 v[228:229], 0\n\t" "v_mov_b64 v[230:231], 0\n\t" "v_mov_b64 v[232:233], 0\n\t" "v_mov_b64 v[234:235], 0\n\t" \
    "v_mov_b64 v[236:237], 0\n\t" "v_mov_b64 v[238:239], 0\n\t" "buffer_load_dwordx4 v[244:247], v216, %[rs], %[sb] offen sc1\n\t" \
    "ds_read_b128 v[160:163], %[l0] offset:0\n\t" "ds_read_b128 v[164:167], %[l0] offset:1024\n\t" "ds_read_b128 v[168:171], %[l0] offset:2048\n\t" \
    "ds_read_b128 v[172:175], %[l0] offset:3072\n\t" "s_nop 3\n\t" "buffer_load_dwordx4 v[248:251], %[mo], %[rs], s60 offen sc1\n\t" \
    "ds_read_b128 v[176:179], %[l0] offset:4096\n\t" "ds_read_b128 v[180:183], %[l0] offset:5120\n\t" "ds_read_b128 v[184:187], %[l0] offset:6144\n\t" \
    "ds_read_b128 v[188:191], %[l0] offset:7168\n\t" "s_nop 3\n\t" "buffer_load_dwordx4 v[252:255], v216, %[rs], s60 offen sc1\n\t" "v_mov_b32 v219, 160512\n\t" \
    "v_mov_b64 v[220:221], -1\n\t" "v_mov_b64 v[222:223], -1\n\t" "buffer_store_dwordx4 v[220:223], %[px], %[rs], %[tr] offen sc1\n\t" \
    "buffer_store_dwordx2 v[220:221], %[pm], %[rs], %[tr] offen sc1\n\t" "v_mov_b32 v208, 0xd028965f\n\t" "v_mov_b32 v209, 0xff800000\n\t" "s_mov_b32 s62, 0\n\t" \
    "s_mov_b32 s63, -1\n\t" "v_mov_b32 v210, 0\n\t" "v_cmp_gt_i32 vcc, 0, %[ps]\n\t" "v_cndmask_b32 v204, 0, v209, s[62:63]\n\t" "s_nop 0\n\t" \
    "v_cndmask_b32 v200, 0, v208, vcc\n\t" "v_cmp_gt_i32 vcc, 1, %[ps]\n\t" "v_cndmask_b32 v205, 0, v209, s[62:63]\n\t" "s_nop 0\n\t" "v_cndmask_b32 v201, 0, v208, vcc\n\t" \
    "v_cmp_gt_i32 vcc, 2, %[ps]\n\t" "v_cndmask_b32 v206, 0, v209, s[62:63]\n\t" "s_nop 0\n\t" "v_cndmask_b32 v202, 0, v208, vcc\n\t" "v_cmp_gt_i32 vcc, 3, %[ps]\n\t" \
    "v_cndmask_b32 v207, 0, v209, s[62:63]\n\t" "s_nop 0\n\t" "v_cndmask_b32 v203, 0, v208, vcc\n\t" "30:\n\t" "s_waitcnt vmcnt(5)\n\t" XT(84, 85) \
    "v_smfmac_f32_16x16x64_bf16 v[224:227], v[240:243], a[0:7], %[ix]\n\t" "ds_write_b128 %[l1], v[240:243] offset:8192\n\t" \
    "v_smfmac_f32_16x16x64_bf16 v[228:231], v[240:243], a[8:15], %[ix]\n\t" "v_smfmac_f32_16x16x64_bf16 v[232:235], v[240:243], v[32:39], %[ix]\n\t" \
    "s_waitcnt lgkmcnt(7)\n\t" "v_smfmac_f32_16x16x64_bf16 v[236:239], v[240:243], v[160:167], %[ix]\n\t" "ds_read_b128 v[160:163], %[l0] offset:8192\n\t" \
    "ds_read_b128 v[164:167], %[l0] offset:9216\n\t" "s_waitcnt vmcnt(4)\n\t" "v_smfmac_f32_16x16x64_bf16 v[224:227], v[244:247], a[16:23], %[ix]\n\t" \
    "ds_write_b128 %[l1], v[244:247] offset:9216\n\t" "v_smfmac_f32_16x16x64_bf16 v[228:231], v[244:247], a[24:31], %[ix]\n\t" \
    "v_smfmac_f32_16x16x64_bf16 v[232:235], v[244:247], v[40:47], %[ix]\n\t" "s_waitcnt lgkmcnt(8)\n\t" \
    "v_smfmac_f32_16x16x64_bf16 v[236:239], v[244:247], v[168:175], %[ix]\n\t" "ds_read_b128 v[168:171], %[l0] offset:10240\n\t" \
    "ds_read_b128 v[172:175], %[l0] offset:11264\n\t" "s_waitcnt vmcnt(3)\n\t" "v_smfmac_f32_16x16x64_bf16 v[224:227], v[248:251], a[32:39], %[ix]\n\t" \
    "ds_write_b128 %[l1], v[248:251] offset:10240\n\t" "v_smfmac_f32_16x16x64_bf16 v[228:231], v[248:251], a[40:47], %[ix]\n\t" "s_waitcnt lgkmcnt(3)\n\t" "s_barrier\n\t" \
    XT(86, 87) "v_smfmac_f32_16x16x64_bf16 v[232:235], v[248:251], v[48:55], %[ix]\n\t" "ds_read_b128 v[192:195], %[l1] offset:12288\n\t" \
    "ds_read_b128 v[196:199], %[l1] offset:0\n\t" "v_smfmac_f32_16x16x64_bf16 v[236:239], v[248:251], v[176:183], %[ix]\n\t" "ds_read_b128 v[176:179], %[l0] offset:12288\n\t" \
    "ds_read_b128 v[180:183], %[l0] offset:13312\n\t" "s_waitcnt vmcnt(2)\n\t" "v_smfmac_f32_16x16x64_bf16 v[224:227], v[252:255], a[48:55], %[ix]\n\t" \
    "ds_write_b128 %[l1], v[252:255] offset:11264\n\t" "v_smfmac_f32_16x16x64_bf16 v[228:231], v[252:255], a[56:63], %[ix]\n\t" \
    "ds_read_b128 v[240:243], %[l1] offset:4096\n\t" "ds_read_b128 v[244:247], %[l1] offset:13312\n\t" \
    "v_smfmac_f32_16x16x64_bf16 v[232:235], v[252:255], v[56:63], %[ix]\n\t" "s_nop 1\n\t" "v_smfmac_f32_16x16x64_bf16 v[236:239], v[252:255], v[184:191], %[ix]\n\t" \
    "ds_read_b128 v[184:187], %[l0] offset:14336\n\t" "v_cmp_u_f32 vcc, v224, v225\n\t" "v_cmp_u_f32 s[62:63], v226, v227\n\t" "s_waitcnt lgkmcnt(7)\n\t" \
    "v_smfmac_f32_16x16x64_bf16 v[224:227], v[192:195], a[64:71], %[ix]\n\t" "ds_read_b128 v[188:191], %[l0] offset:15360\n\t" \
    "v_smfmac_f32_16x16x64_bf16 v[228:231], v[192:195], a[72:79], %[ix]\n\t" "s_or_b64 vcc, vcc, s[62:63]\n\t" "s_cbranch_scc1 31f\n\t" "32:\n\t" \
    "v_smfmac_f32_16x16x64_bf16 v[232:235], v[192:195], v[64:71], %[ix]\n\t" "s_waitcnt lgkmcnt(4)\n\t" "s_barrier\n\t" XT(88, 89) "ds_read_b32 v218, v219\n\t" \
    "v_smfmac_f32_16x16x64_bf16 v[236:239], v[192:195], v[160:167], %[ix]\n\t" "ds_read_b128 v[160:163], %[l0] offset:16384\n\t" \
    "ds_read_b128 v[164:167], %[l0] offset:17408\n\t" "v_smfmac_f32_16x16x64_bf16 v[224:227], v[196:199], a[80:87], %[ix]\n\t" \
    "ds_read_b128 v[248:251], %[l1] offset:1024\n\t" "ds_read_b128 v[252:255], %[l1] offset:5120\n\t" "v_smfmac_f32_16x16x64_bf16 v[228:231], v[196:199], a[88:95], %[ix]\n\t" \
    "ds_read_b128 v[192:195], %[l1] offset:14336\n\t" "v_smfmac_f32_16x16x64_bf16 v[232:235], v[196:199], v[72:79], %[ix]\n\t" \
    "v_smfmac_f32_16x16x64_bf16 v[236:239], v[196:199], v[168:175], %[ix]\n\t" "s_waitcnt lgkmcnt(5)\n\t" "v_cmp_eq_u32 vcc, %[rt], v218\n\t" \
    "v_smfmac_f32_16x16x64_bf16 v[224:227], v[240:243], a[96:103], %[ix]\n\t" "s_cbranch_vccnz 33f\n\t" "ds_read_b128 v[168:171], %[l0] offset:18432\n\t" \
    "v_smfmac_f32_16x16x64_bf16 v[228:231], v[240:243], a[104:111], %[ix]\n\t" "ds_read_b128 v[172:175], %[l0] offset:19456\n\t" \
    "ds_read_b128 v[196:199], %[l1] offset:2048\n\t" "v_smfmac_f32_16x16x64_bf16 v[232:235], v[240:243], v[80:87], %[ix]\n\t" \
    "buffer_load_dwordx4 v[16:19], %[hq], %[rs], %[qb] offen offset:0 sc1\n\t" "v_smfmac_f32_16x16x64_bf16 v[236:239], v[240:243], v[176:183], %[ix]\n\t" \
    "ds_read_b128 v[176:179], %[l0] offset:20480\n\t" "ds_read_b128 v[180:183], %[l0] offset:21504\n\t" \
    "v_smfmac_f32_16x16x64_bf16 v[224:227], v[244:247], a[112:119], %[ix]\n\t" "ds_read_b128 v[240:243], %[l1] offset:6144\n\t" \
    "v_smfmac_f32_16x16x64_bf16 v[228:231], v[244:247], a[120:127], %[ix]\n\t" "v_smfmac_f32_16x16x64_bf16 v[232:235], v[244:247], v[88:95], %[ix]\n\t" \
    "buffer_load_dwordx4 v[20:23], %[hq], %[rs], %[qb] offen offset:2048 sc1\n\t" "v_smfmac_f32_16x16x64_bf16 v[236:239], v[244:247], v[184:191], %[ix]\n\t" \
    "ds_read_b128 v[184:187], %[l0] offset:22528\n\t" "ds_read_b128 v[188:191], %[l0] offset:23552\n\t" "s_waitcnt lgkmcnt(10)\n\t" \
    "v_smfmac_f32_16x16x64_bf16 v[224:227], v[248:251], a[128:135], %[ix]\n\t" "ds_read_b128 v[244:247], %[l1] offset:15360\n\t" \
    "v_smfmac_f32_16x16x64_bf16 v[228:231], v[248:251], a[136:143], %[ix]\n\t" "v_smfmac_f32_16x16x64_bf16 v[232:235], v[248:251], v[96:103], %[ix]\n\t" \
    "buffer_load_dwordx4 v[24:27], %[hq], %[rs], s61 offen offset:0 sc1\n\t" "v_smfmac_f32_16x16x64_bf16 v[236:239], v[248:251], v[160:167], %[ix]\n\t" \
    "ds_read_b128 v[160:163], %[l0] offset:24576\n\t" "ds_read_b128 v[164:167], %[l0] offset:25600\n\t" "s_waitcnt lgkmcnt(12)\n\t" \
    "v_smfmac_f32_16x16x64_bf16 v[224:227], v[252:255], a[144:151], %[ix]\n\t" "ds_read_b128 v[248:251], %[l1] offset:3072\n\t" \
    "v_smfmac_f32_16x16x64_bf16 v[228:231], v[252:255], a[152:159], %[ix]\n\t" "v_smfmac_f32_16x16x64_bf16 v[232:235], v[252:255], v[104:111], %[ix]\n\t" \
    "buffer_load_dwordx4 v[28:31], %[hq], %[rs], s61 offen offset:2048 sc1\n\t" "s_waitcnt lgkmcnt(10)\n\t" \
    "v_smfmac_f32_16x16x64_bf16 v[236:239], v[252:255], v[168:175], %[ix]\n\t" "ds_read_b128 v[168:171], %[l0] offset:26624\n\t" \
    "ds_read_b128 v[172:175], %[l0] offset:27648\n\t" "v_smfmac_f32_16x16x64_bf16 v[224:227], v[192:195], a[160:167], %[ix]\n\t" \
    "ds_read_b128 v[252:255], %[l1] offset:7168\n\t" "v_smfmac_f32_16x16x64_bf16 v[228:231], v[192:195], a[168:175], %[ix]\n\t" "s_waitcnt lgkmcnt(12)\n\t" \
    "v_smfmac_f32_16x16x64_bf16 v[224:227], v[196:199], a[176:183], %[ix]\n\t" "v_smfmac_f32_16x16x64_bf16 v[228:231], v[196:199], a[184:191], %[ix]\n\t" \
    "s_waitcnt lgkmcnt(9)\n\t" "v_smfmac_f32_16x16x64_bf16 v[224:227], v[240:243], a[192:199], %[ix]\n\t" \
    "v_smfmac_f32_16x16x64_bf16 v[228:231], v[240:243], a[200:207], %[ix]\n\t" "s_waitcnt lgkmcnt(6)\n\t" \
    "v_smfmac_f32_16x16x64_bf16 v[224:227], v[244:247], a[208:215], %[ix]\n\t" "v_smfmac_f32_16x16x64_bf16 v[228:231], v[244:247], a[216:223], %[ix]\n\t" \
    "s_waitcnt vmcnt(4)\n\t" "s_waitcnt lgkmcnt(3)\n\t" "v_smfmac_f32_16x16x64_bf16 v[224:227], v[248:251], a[224:231], %[ix]\n\t" \
    "v_smfmac_f32_16x16x64_bf16 v[228:231], v[248:251], a[232:239], %[ix]\n\t" "s_waitcnt lgkmcnt(0)\n\t" \
    "v_smfmac_f32_16x16x64_bf16 v[224:227], v[252:255], a[240:247], %[ix]\n\t" "s_nop 1\n\t" "v_smfmac_f32_16x16x64_bf16 v[228:231], v[252:255], a[248:255], %[ix]\n\t" \
    XT(90, 91) "s_nop 1\n\t" "v_smfmac_f32_16x16x64_bf16 v[232:235], v[192:195], v[112:119], %[ix]\n\t" "s_nop 1\n\t" \
    "v_smfmac_f32_16x16x64_bf16 v[236:239], v[192:195], v[176:183], %[ix]\n\t" "ds_read_b128 v[176:179], %[l0] offset:28672\n\t" "v_add_f32 v224, v224, v225\n\t" \
    "v_add_f32 v226, v226, v227\n\t" "v_smfmac_f32_16x16x64_bf16 v[232:235], v[196:199], v[120:127], %[ix]\n\t" "ds_read_b128 v[180:183], %[l0] offset:29696\n\t" \
    "v_add_f32 v228, v228, v229\n\t" "v_add_f32 v230, v230, v231\n\t" "v_smfmac_f32_16x16x64_bf16 v[236:239], v[196:199], v[184:191], %[ix]\n\t" \
    "ds_read_b128 v[184:187], %[l0] offset:30720\n\t" "v_cvt_pk_bf16_f32 v218, v224, v228\n\t" "v_cvt_pk_bf16_f32 v219, v226, v230\n\t" \
    "v_smfmac_f32_16x16x64_bf16 v[232:235], v[240:243], v[128:135], %[ix]\n\t" "ds_write2_b32 %[qw], v218, v219 offset1:8\n\t" \
    "ds_read_b128 v[188:191], %[l0] offset:31744\n\t" "v_smfmac_f32_16x16x64_bf16 v[236:239], v[240:243], v[160:167], %[ix]\n\t" \
    "v_smfmac_f32_16x16x64_bf16 v[232:235], v[244:247], v[136:143], %[ix]\n\t" "v_smfmac_f32_16x16x64_bf16 v[236:239], v[244:247], v[168:175], %[ix]\n\t" \
    "s_waitcnt lgkmcnt(1)\n\t" "s_barrier\n\t" "v_smfmac_f32_16x16x64_bf16 v[232:235], v[248:251], v[144:151], %[ix]\n\t" \
    "v_smfmac_f32_16x16x64_bf16 v[236:239], v[248:251], v[176:183], %[ix]\n\t" "v_smfmac_f32_16x16x64_bf16 v[232:235], v[252:255], v[152:159], %[ix]\n\t" \
    "s_waitcnt lgkmcnt(0)\n\t" "v_smfmac_f32_16x16x64_bf16 v[236:239], v[252:255], v[184:191], %[ix]\n\t" XT(92, 93) "s_nop 6\n\t" "v_add_f32 v232, v232, v233\n\t" \
    "v_add_f32 v234, v234, v235\n\t" "v_add_f32 v236, v236, v237\n\t" "v_add_f32 v238, v238, v239\n\t" "v_cvt_pk_bf16_f32 v232, v232, v236\n\t" \
    "v_cvt_pk_bf16_f32 v233, v234, v238\n\t" "ds_write2_b32 %[kw], v232, v233 offset1:68\n\t" "s_waitcnt lgkmcnt(0)\n\t" "s_barrier\n\t" XT(94, 95) \
    "s_cmp_ge_u32 %[sq], %[sn]\n\t" "s_cbranch_scc1 377f\n\t" "375:\n\t" "global_load_dword v240, v210, %[fq] sc1\n\t" "s_waitcnt vmcnt(0)\n\t" \
    "v_readfirstlane_b32 %[sq], v240\n\t" "s_nop 3\n\t" "s_cmp_ge_u32 %[sq], %[sn]\n\t" "s_cbranch_scc1 376f\n\t" "s_cmp_eq_u32 %[bd], 0\n\t" "s_cbranch_scc1 376f\n\t" \
    "s_sub_u32 %[bd], %[bd], 1\n\t" "s_sleep 2\n\t" "s_branch 375b\n\t" "376:\n\t" "buffer_load_dwordx4 v[16:19], %[hq], %[rs], %[qb] offen offset:0 sc1\n\t" \
    "buffer_load_dwordx4 v[20:23], %[hq], %[rs], %[qb] offen offset:2048 sc1\n\t" "buffer_load_dwordx4 v[24:27], %[hq], %[rs], s61 offen offset:0 sc1\n\t" \
    "buffer_load_dwordx4 v[28:31], %[hq], %[rs], s61 offen offset:2048 sc1\n\t" "377:\n\t" "s_nop 3\n\t" "ds_read_b128 v[160:163], %[kx] offset:0\n\t" \
    "ds_read_b128 v[164:167], %[kx] offset:2048\n\t" "ds_read_b128 v[168:171], %[kx] offset:4096\n\t" "ds_read_b128 v[172:175], %[kx] offset:6144\n\t" \
    "ds_read_b128 v[176:179], %[kx] offset:256\n\t" "ds_read_b128 v[180:183], %[kx] offset:2304\n\t" "ds_read_b128 v[184:187], %[kx] offset:4352\n\t" \
    "ds_read_b128 v[188:191], %[kx] offset:6400\n\t" "ds_read_b128 v[224:227], %[km] offset:0\n\t" "ds_read_b128 v[228:231], %[km] offset:64\n\t" \
    "ds_read_b128 v[232:235], %[km] offset:128\n\t" "ds_read_b128 v[236:239], %[km] offset:192\n\t" "s_waitcnt vmcnt(0)\n\t" "global_load_dword v240, v210, %[fq] sc1\n\t" \
    "s_waitcnt lgkmcnt(11)\n\t" "v_mfma_f32_16x16x32_bf16 v[248:251], v[160:163], v[16:19], 0\n\t" "s_waitcnt lgkmcnt(10)\n\t" \
    "v_mfma_f32_16x16x32_bf16 v[248:251], v[164:167], v[20:23], v[248:251]\n\t" "s_waitcnt lgkmcnt(9)\n\t" \
    "v_mfma_f32_16x16x32_bf16 v[248:251], v[168:171], v[24:27], v[248:251]\n\t" "s_waitcnt lgkmcnt(8)\n\t" \
    "v_mfma_f32_16x16x32_bf16 v[248:251], v[172:175], v[28:31], v[248:251]\n\t" "s_waitcnt lgkmcnt(7)\n\t" \
    "v_mfma_f32_16x16x32_bf16 v[252:255], v[176:179], v[16:19], v[200:203]\n\t" "s_waitcnt lgkmcnt(6)\n\t" \
    "v_mfma_f32_16x16x32_bf16 v[252:255], v[180:183], v[20:23], v[252:255]\n\t" "s_waitcnt lgkmcnt(5)\n\t" \
    "v_mfma_f32_16x16x32_bf16 v[252:255], v[184:187], v[24:27], v[252:255]\n\t" "s_waitcnt lgkmcnt(4)\n\t" \
    "v_mfma_f32_16x16x32_bf16 v[252:255], v[188:191], v[28:31], v[252:255]\n\t" "s_waitcnt lgkmcnt(3)\n\t" \
    "v_mfma_f32_16x16x32_bf16 v[244:247], v[224:227], v[16:19], v[204:207]\n\t" "s_waitcnt lgkmcnt(2)\n\t" \
    "v_mfma_f32_16x16x32_bf16 v[244:247], v[228:231], v[20:23], v[244:247]\n\t" "s_waitcnt lgkmcnt(1)\n\t" \
    "v_mfma_f32_16x16x32_bf16 v[244:247], v[232:235], v[24:27], v[244:247]\n\t" "s_waitcnt lgkmcnt(0)\n\t" \
    "v_mfma_f32_16x16x32_bf16 v[244:247], v[236:239], v[28:31], v[244:247]\n\t" "s_nop 3\n\t" "v_max3_f32 v242, v248, v249, v250\n\t" "v_max3_f32 v242, v242, v251, v252\n\t" \
    "v_max3_f32 v242, v242, v253, v254\n\t" "v_max_f32 v242, v242, v255\n\t" "v_max3_f32 v242, v242, v244, v245\n\t" "v_max3_f32 v242, v242, v246, v247\n\t" \
    "v_mov_b32 v243, v242\n\t" "s_nop 1\n\t" "v_permlane32_swap_b32 v242, v243\n\t" "v_max_f32 v242, v242, v243\n\t" "v_mov_b32 v243, v242\n\t" "s_nop 1\n\t" \
    "v_permlane16_swap_b32 v242, v243\n\t" "v_max_f32 v242, v242, v243\n\t" "v_pk_add_f32 v[244:245], v[244:245], v[242:243] op_sel_hi:[1,0] neg_lo:[0,1] neg_hi:[0,1]\n\t" \
    "v_pk_add_f32 v[246:247], v[246:247], v[242:243] op_sel_hi:[1,0] neg_lo:[0,1] neg_hi:[0,1]\n\t" \
    "v_pk_add_f32 v[248:249], v[248:249], v[242:243] op_sel_hi:[1,0] neg_lo:[0,1] neg_hi:[0,1]\n\t" \
    "v_pk_add_f32 v[250:251], v[250:251], v[242:243] op_sel_hi:[1,0] neg_lo:[0,1] neg_hi:[0,1]\n\t" \
    "v_pk_add_f32 v[252:253], v[252:253], v[242:243] op_sel_hi:[1,0] neg_lo:[0,1] neg_hi:[0,1]\n\t" \
    "v_pk_add_f32 v[254:255], v[254:255], v[242:243] op_sel_hi:[1,0] neg_lo:[0,1] neg_hi:[0,1]\n\t" "v_pk_mul_f32 v[244:245], v[244:245], s[66:67]\n\t" \
    "v_pk_mul_f32 v[246:247], v[246:247], s[66:67]\n\t" "v_pk_mul_f32 v[248:249], v[248:249], s[66:67]\n\t" "v_pk_mul_f32 v[250:251], v[250:251], s[66:67]\n\t" \
    "v_pk_mul_f32 v[252:253], v[252:253], s[66:67]\n\t" "v_pk_mul_f32 v[254:255], v[254:255], s[66:67]\n\t" "v_exp_f32 v244, v244\n\t" "v_exp_f32 v245, v245\n\t" \
    "v_exp_f32 v246, v246\n\t" "v_exp_f32 v247, v247\n\t" "v_exp_f32 v248, v248\n\t" "v_exp_f32 v249, v249\n\t" "v_exp_f32 v250, v250\n\t" "v_exp_f32 v251, v251\n\t" \
    "v_exp_f32 v252, v252\n\t" "v_exp_f32 v253, v253\n\t" "v_exp_f32 v254, v254\n\t" "v_exp_f32 v255, v255\n\t" "v_pk_add_f32 v[242:243], v[244:245], v[246:247]\n\t" \
    "v_pk_add_f32 v[242:243], v[242:243], v[248:249]\n\t" "v_pk_add_f32 v[242:243], v[242:243], v[250:251]\n\t" "v_pk_add_f32 v[242:243], v[242:243], v[252:253]\n\t" \
    "v_pk_add_f32 v[242:243], v[242:243], v[254:255]\n\t" "v_add_f32 v242, v242, v243\n\t" "v_mov_b32 v243, v242\n\t" "s_nop 1\n\t" "v_permlane32_swap_b32 v242, v243\n\t" \
    "v_add_f32 v242, v242, v243\n\t" "v_mov_b32 v243, v242\n\t" "s_nop 1\n\t" "v_permlane16_swap_b32 v242, v243\n\t" "v_add_f32 v242, v242, v243\n\t" \
    "v_rcp_f32 v242, v242\n\t" "s_nop 0\n\t" "v_pk_mul_f32 v[244:245], v[244:245], v[242:243] op_sel_hi:[1,0]\n\t" \
    "v_pk_mul_f32 v[246:247], v[246:247], v[242:243] op_sel_hi:[1,0]\n\t" "v_pk_mul_f32 v[248:249], v[248:249], v[242:243] op_sel_hi:[1,0]\n\t" \
    "v_pk_mul_f32 v[250:251], v[250:251], v[242:243] op_sel_hi:[1,0]\n\t" "v_pk_mul_f32 v[252:253], v[252:253], v[242:243] op_sel_hi:[1,0]\n\t" \
    "v_pk_mul_f32 v[254:255], v[254:255], v[242:243] op_sel_hi:[1,0]\n\t" "v_cvt_pk_bf16_f32 v162, v244, v245\n\t" "v_cvt_pk_bf16_f32 v163, v246, v247\n\t" \
    "v_cvt_pk_bf16_f32 v164, v248, v249\n\t" "v_cvt_pk_bf16_f32 v165, v250, v251\n\t" "v_cvt_pk_bf16_f32 v166, v252, v253\n\t" "v_cvt_pk_bf16_f32 v167, v254, v255\n\t" \
    "s_barrier\n\t" "buffer_store_dwordx4 v[164:167], %[px], %[rs], %[tb] offen sc1\n\t" "buffer_store_dwordx2 v[162:163], %[pm], %[rs], %[tb] offen sc1\n\t" \
    "s_waitcnt vmcnt(2)\n\t" "v_readfirstlane_b32 s62, v240\n\t" "s_nop 3\n\t" "s_max_u32 %[sq], %[sq], s62\n\t" XT(96, 97) "s_branch 99f\n\t" "31:\n\t" \
    "ds_read_b128 v[220:223], %[l1] offset:8192\n\t" "s_waitcnt lgkmcnt(0)\n\t" "v_max3_u32 v218, v220, v221, v222\n\t" "v_max_u32 v218, v218, v223\n\t" \
    "ds_read_b128 v[220:223], %[l1] offset:9216\n\t" "s_waitcnt lgkmcnt(0)\n\t" "v_max3_u32 v218, v218, v220, v221\n\t" "v_max3_u32 v218, v218, v222, v223\n\t" \
    "ds_read_b128 v[220:223], %[l1] offset:10240\n\t" "s_waitcnt lgkmcnt(0)\n\t" "v_max3_u32 v218, v218, v220, v221\n\t" "v_max3_u32 v218, v218, v222, v223\n\t" \
    "ds_read_b128 v[220:223], %[l1] offset:11264\n\t" "s_waitcnt lgkmcnt(0)\n\t" "v_max3_u32 v218, v218, v220, v221\n\t" "v_max3_u32 v218, v218, v222, v223\n\t" \
    "v_cmp_eq_u32 vcc, -1, v218\n\t" "s_cbranch_vccz 32b\n\t" "s_cmp_eq_u32 %[rt], 1\n\t" "s_cbranch_scc1 32b\n\t" "v_mov_b32 v218, %[rt]\n\t" "ds_write_b32 v219, v218\n\t" \
    "s_waitcnt lgkmcnt(0)\n\t" "s_branch 32b\n\t" "33:\n\t" "s_sub_u32 %[rt], %[rt], 1\n\t" "s_waitcnt lgkmcnt(0)\n\t" XCNT(100) "34:\n\t" \
    "buffer_load_dwordx4 v[240:243], %[mo], %[rs], %[sb] offen sc1\n\t" "buffer_load_dwordx4 v[244:247], v216, %[rs], %[sb] offen sc1\n\t" \
    "buffer_load_dwordx4 v[248:251], %[mo], %[rs], s60 offen sc1\n\t" "buffer_load_dwordx4 v[252:255], v216, %[rs], s60 offen sc1\n\t" "s_waitcnt vmcnt(0)\n\t" \
    "v_max_u32 v218, v240, v241\n\t" "v_max3_u32 v218, v218, v242, v243\n\t" "v_max3_u32 v218, v218, v244, v245\n\t" "v_max3_u32 v218, v218, v246, v247\n\t" \
    "v_max3_u32 v218, v218, v248, v249\n\t" "v_max3_u32 v218, v218, v250, v251\n\t" "v_max3_u32 v218, v218, v252, v253\n\t" "v_max3_u32 v218, v218, v254, v255\n\t" \
    "v_cmp_eq_u32 vcc, -1, v218\n\t" "s_cbranch_vccz 35f\n\t" "s_cmp_eq_u32 %[bd], 0\n\t" "s_cbranch_scc1 35f\n\t" "s_sub_u32 %[bd], %[bd], 1\n\t" XCNT(101) \
    "s_branch 34b\n\t" "35:\n\t" "ds_read_b128 v[160:163], %[l0] offset:0\n\t" "ds_read_b128 v[164:167], %[l0] offset:1024\n\t" \
    "ds_read_b128 v[168:171], %[l0] offset:2048\n\t" "ds_read_b128 v[172:175], %[l0] offset:3072\n\t" "ds_read_b128 v[176:179], %[l0] offset:4096\n\t" \
    "ds_read_b128 v[180:183], %[l0] offset:5120\n\t" "ds_read_b128 v[184:187], %[l0] offset:6144\n\t" "ds_read_b128 v[188:191], %[l0] offset:7168\n\t" \
    "v_mov_b64 v[224:225], 0\n\t" "v_mov_b64 v[226:227], 0\n\t" "v_mov_b64 v[228:229], 0\n\t" "v_mov_b64 v[230:231], 0\n\t" "v_mov_b64 v[232:233], 0\n\t" \
    "v_mov_b64 v[234:235], 0\n\t" "v_mov_b64 v[236:237], 0\n\t" "v_mov_b64 v[238:239], 0\n\t" "s_barrier\n\t" "s_branch 30b\n\t" "99:\n\t"
#define QK_STEP3 \
    "v_xor_b32 v216, 0x400, %[mo]\n\t" "s_xor_b32 s60, %[sb], 0x800\n\t" "s_mov_b32 s67, %[pb]\n\t" XT(80, 81) "s_getpc_b64 s[62:63]\n\t" "48:\n\t" "s_memtime s[64:65]\n\t" \
    "s_waitcnt lgkmcnt(0)\n\t" "s_sub_u32 s61, %[tg], s64\n\t" "s_cmp_gt_i32 s61, 124\n\t" "s_cbranch_scc1 48b\n\t" "s_max_i32 s61, s61, 0\n\t" "s_and_b32 s61, s61, 124\n\t" \
    "s_sub_u32 s61, 49f-48b, s61\n\t" "s_add_u32 s62, s62, s61\n\t" "s_addc_u32 s63, s63, 0\n\t" "s_setpc_b64 s[62:63]\n\t" ".rept 31\n\t" "s_nop 0\n\t" ".endr\n\t" "49:\n\t" \
    XT(98, 99) "s_barrier\n\t" XT(82, 83) "buffer_load_dwordx4 v[240:243], %[mo], %[rs], %[sb] offen sc1\n\t" "v_mov_b64 v[224:225], 0\n\t" "v_mov_b64 v[226:227], 0\n\t" \
    "v_mov_b64 v[228:229], 0\n\t" "v_mov_b64 v[230:231], 0\n\t" "v_mov_b64 v[232:233], 0\n\t" "v_mov_b64 v[234:235], 0\n\t" "v_mov_b64 v[236:237], 0\n\t" \
    "v_mov_b64 v[238:239], 0\n\t" "buffer_load_dwordx4 v[244:247], v216, %[rs], %[sb] offen sc1\n\t" "ds_read_b128 v[160:163], %[l0] offset:0\n\t" \
    "ds_read_b128 v[164:167], %[l0] offset:1024\n\t" "ds_read_b128 v[168:171], %[l0] offset:2048\n\t" "ds_read_b128 v[172:175], %[l0] offset:3072\n\t" "s_nop 3\n\t" \
    "buffer_load_dwordx4 v[248:251], %[mo], %[rs], s60 offen sc1\n\t" "ds_read_b128 v[176:179], %[l0] offset:4096\n\t" "ds_read_b128 v[180:183], %[l0] offset:5120\n\t" \
    "ds_read_b128 v[184:187], %[l0] offset:6144\n\t" "ds_read_b128 v[188:191], %[l0] offset:7168\n\t" "s_nop 3\n\t" \
    "buffer_load_dwordx4 v[252:255], v216, %[rs], s60 offen sc1\n\t" "v_mov_b32 v219, 160512\n\t" "v_mov_b32 v220, -1\n\t" \
    "buffer_store_dword v220, %[hq], %[rs], %[pr] offen sc1\n\t" "40:\n\t" "s_waitcnt vmcnt(4)\n\t" XT(84, 85) \
    "v_smfmac_f32_16x16x64_bf16 v[224:227], v[240:243], a[0:7], %[ix]\n\t" "ds_write_b128 %[l1], v[240:243] offset:12288\n\t" \
    "v_smfmac_f32_16x16x64_bf16 v[228:231], v[240:243], a[8:15], %[ix]\n\t" "v_smfmac_f32_16x16x64_bf16 v[232:235], v[240:243], v[32:39], %[ix]\n\t" \
    "s_waitcnt lgkmcnt(7)\n\t" "v_smfmac_f32_16x16x64_bf16 v[236:239], v[240:243], v[160:167], %[ix]\n\t" "ds_read_b128 v[160:163], %[l0] offset:8192\n\t" \
    "ds_read_b128 v[164:167], %[l0] offset:9216\n\t" "s_waitcnt vmcnt(3)\n\t" "v_smfmac_f32_16x16x64_bf16 v[224:227], v[244:247], a[16:23], %[ix]\n\t" \
    "ds_write_b128 %[l1], v[244:247] offset:13312\n\t" "v_smfmac_f32_16x16x64_bf16 v[228:231], v[244:247], a[24:31], %[ix]\n\t" \
    "v_smfmac_f32_16x16x64_bf16 v[232:235], v[244:247], v[40:47], %[ix]\n\t" "s_waitcnt lgkmcnt(8)\n\t" \
    "v_smfmac_f32_16x16x64_bf16 v[236:239], v[244:247], v[168:175], %[ix]\n\t" "ds_read_b128 v[168:171], %[l0] offset:10240\n\t" \
    "ds_read_b128 v[172:175], %[l0] offset:11264\n\t" "s_waitcnt vmcnt(2)\n\t" "v_smfmac_f32_16x16x64_bf16 v[224:227], v[248:251], a[32:39], %[ix]\n\t" \
    "ds_write_b128 %[l1], v[248:251] offset:14336\n\t" "v_smfmac_f32_16x16x64_bf16 v[228:231], v[248:251], a[40:47], %[ix]\n\t" "s_waitcnt lgkmcnt(3)\n\t" "s_barrier\n\t" \
    XT(86, 87) "v_smfmac_f32_16x16x64_bf16 v[232:235], v[248:251], v[48:55], %[ix]\n\t" "ds_read_b128 v[192:195], %[l1] offset:0\n\t" \
    "ds_read_b128 v[196:199], %[l1] offset:4096\n\t" "v_smfmac_f32_16x16x64_bf16 v[236:239], v[248:251], v[176:183], %[ix]\n\t" \
    "ds_read_b128 v[176:179], %[l0] offset:12288\n\t" "ds_read_b128 v[180:183], %[l0] offset:13312\n\t" "s_waitcnt vmcnt(1)\n\t" \
    "v_smfmac_f32_16x16x64_bf16 v[224:227], v[252:255], a[48:55], %[ix]\n\t" "ds_write_b128 %[l1], v[252:255] offset:15360\n\t" \
    "v_smfmac_f32_16x16x64_bf16 v[228:231], v[252:255], a[56:63], %[ix]\n\t" "ds_read_b128 v[240:243], %[l1] offset:8192\n\t" "ds_read_b128 v[244:247], %[l1] offset:1024\n\t" \
    "v_smfmac_f32_16x16x64_bf16 v[232:235], v[252:255], v[56:63], %[ix]\n\t" "s_nop 1\n\t" "v_smfmac_f32_16x16x64_bf16 v[236:239], v[252:255], v[184:191], %[ix]\n\t" \
    "ds_read_b128 v[184:187], %[l0] offset:14336\n\t" "v_cmp_u_f32 vcc, v224, v225\n\t" "v_cmp_u_f32 s[62:63], v226, v227\n\t" "s_waitcnt lgkmcnt(7)\n\t" \
    "v_smfmac_f32_16x16x64_bf16 v[224:227], v[192:195], a[64:71], %[ix]\n\t" "ds_read_b128 v[188:191], %[l0] offset:15360\n\t" \
    "v_smfmac_f32_16x16x64_bf16 v[228:231], v[192:195], a[72:79], %[ix]\n\t" "s_or_b64 vcc, vcc, s[62:63]\n\t" "s_cbranch_scc1 41f\n\t" "42:\n\t" \
    "v_smfmac_f32_16x16x64_bf16 v[232:235], v[192:195], v[64:71], %[ix]\n\t" "s_waitcnt lgkmcnt(4)\n\t" "s_barrier\n\t" XT(88, 89) "ds_read_b32 v218, v219\n\t" \
    "v_smfmac_f32_16x16x64_bf16 v[236:239], v[192:195], v[160:167], %[ix]\n\t" "ds_read_b128 v[160:163], %[l0] offset:16384\n\t" \
    "ds_read_b128 v[164:167], %[l0] offset:17408\n\t" "v_smfmac_f32_16x16x64_bf16 v[224:227], v[196:199], a[80:87], %[ix]\n\t" \
    "ds_read_b128 v[248:251], %[l1] offset:5120\n\t" "ds_read_b128 v[252:255], %[l1] offset:9216\n\t" "v_smfmac_f32_16x16x64_bf16 v[228:231], v[196:199], a[88:95], %[ix]\n\t" \
    "s_memtime s[64:65]\n\t" "ds_read_b128 v[192:195], %[l1] offset:2048\n\t" "v_smfmac_f32_16x16x64_bf16 v[232:235], v[196:199], v[72:79], %[ix]\n\t" \
    "v_smfmac_f32_16x16x64_bf16 v[236:239], v[196:199], v[168:175], %[ix]\n\t" "s_waitcnt lgkmcnt(5)\n\t" "v_cmp_eq_u32 vcc, %[rt], v218\n\t" \
    "v_smfmac_f32_16x16x64_bf16 v[224:227], v[240:243], a[96:103], %[ix]\n\t" "s_cbranch_vccnz 43f\n\t" "ds_read_b128 v[168:171], %[l0] offset:18432\n\t" \
    "v_smfmac_f32_16x16x64_bf16 v[228:231], v[240:243], a[104:111], %[ix]\n\t" "ds_read_b128 v[172:175], %[l0] offset:19456\n\t" \
    "ds_read_b128 v[196:199], %[l1] offset:6144\n\t" "v_smfmac_f32_16x16x64_bf16 v[232:235], v[240:243], v[80:87], %[ix]\n\t" "s_sub_u32 s62, s64, %[ca]\n\t" \
    "s_mul_hi_u32 s63, s62, %[k1]\n\t" "v_smfmac_f32_16x16x64_bf16 v[236:239], v[240:243], v[176:183], %[ix]\n\t" "s_mul_i32 s62, s62, %[k1]\n\t" \
    "s_lshr_b64 s[62:63], s[62:63], 16\n\t" "v_smfmac_f32_16x16x64_bf16 v[224:227], v[244:247], a[112:119], %[ix]\n\t" "s_add_u32 s62, s62, %[ga]\n\t" \
    "s_bitset0_b32 s62, 0\n\t" "v_smfmac_f32_16x16x64_bf16 v[228:231], v[244:247], a[120:127], %[ix]\n\t" "v_mov_b32 v217, s62\n\t" \
    "buffer_store_dword v217, %[hq], %[rs], s67 offen sc1\n\t" "v_smfmac_f32_16x16x64_bf16 v[232:235], v[244:247], v[88:95], %[ix]\n\t" \
    "ds_read_b128 v[176:179], %[l0] offset:20480\n\t" "ds_read_b128 v[180:183], %[l0] offset:21504\n\t" \
    "v_smfmac_f32_16x16x64_bf16 v[236:239], v[244:247], v[184:191], %[ix]\n\t" "ds_read_b128 v[240:243], %[l1] offset:10240\n\t" \
    "ds_read_b128 v[184:187], %[l0] offset:22528\n\t" "s_waitcnt lgkmcnt(9)\n\t" "v_smfmac_f32_16x16x64_bf16 v[224:227], v[248:251], a[128:135], %[ix]\n\t" \
    "ds_read_b128 v[188:191], %[l0] offset:23552\n\t" "ds_read_b128 v[244:247], %[l1] offset:3072\n\t" \
    "v_smfmac_f32_16x16x64_bf16 v[228:231], v[248:251], a[136:143], %[ix]\n\t" "v_lshlrev_b32 v16, 3, %[l1]\n\t" "buffer_load_dword v17, v16, %[rs], %[kn] offen sc1\n\t" \
    "v_smfmac_f32_16x16x64_bf16 v[232:235], v[248:251], v[96:103], %[ix]\n\t" "v_smfmac_f32_16x16x64_bf16 v[236:239], v[248:251], v[160:167], %[ix]\n\t" \
    "ds_read_b128 v[160:163], %[l0] offset:24576\n\t" "ds_read_b128 v[164:167], %[l0] offset:25600\n\t" "s_waitcnt lgkmcnt(12)\n\t" \
    "v_smfmac_f32_16x16x64_bf16 v[224:227], v[252:255], a[144:151], %[ix]\n\t" "ds_read_b128 v[248:251], %[l1] offset:7168\n\t" \
    "v_smfmac_f32_16x16x64_bf16 v[228:231], v[252:255], a[152:159], %[ix]\n\t" "v_smfmac_f32_16x16x64_bf16 v[232:235], v[252:255], v[104:111], %[ix]\n\t" \
    "s_waitcnt lgkmcnt(10)\n\t" "v_smfmac_f32_16x16x64_bf16 v[236:239], v[252:255], v[168:175], %[ix]\n\t" "ds_read_b128 v[168:171], %[l0] offset:26624\n\t" \
    "ds_read_b128 v[172:175], %[l0] offset:27648\n\t" "v_smfmac_f32_16x16x64_bf16 v[224:227], v[192:195], a[160:167], %[ix]\n\t" \
    "ds_read_b128 v[252:255], %[l1] offset:11264\n\t" "v_smfmac_f32_16x16x64_bf16 v[228:231], v[192:195], a[168:175], %[ix]\n\t" "s_waitcnt lgkmcnt(12)\n\t" \
    "v_smfmac_f32_16x16x64_bf16 v[224:227], v[196:199], a[176:183], %[ix]\n\t" "v_smfmac_f32_16x16x64_bf16 v[228:231], v[196:199], a[184:191], %[ix]\n\t" \
    "s_waitcnt lgkmcnt(9)\n\t" "v_smfmac_f32_16x16x64_bf16 v[224:227], v[240:243], a[192:199], %[ix]\n\t" \
    "v_smfmac_f32_16x16x64_bf16 v[228:231], v[240:243], a[200:207], %[ix]\n\t" "s_waitcnt lgkmcnt(6)\n\t" \
    "v_smfmac_f32_16x16x64_bf16 v[224:227], v[244:247], a[208:215], %[ix]\n\t" "v_smfmac_f32_16x16x64_bf16 v[228:231], v[244:247], a[216:223], %[ix]\n\t" \
    "s_waitcnt lgkmcnt(3)\n\t" "v_smfmac_f32_16x16x64_bf16 v[224:227], v[248:251], a[224:231], %[ix]\n\t" \
    "v_smfmac_f32_16x16x64_bf16 v[228:231], v[248:251], a[232:239], %[ix]\n\t" "s_waitcnt lgkmcnt(0)\n\t" \
    "v_smfmac_f32_16x16x64_bf16 v[224:227], v[252:255], a[240:247], %[ix]\n\t" "s_nop 1\n\t" "v_smfmac_f32_16x16x64_bf16 v[228:231], v[252:255], a[248:255], %[ix]\n\t" \
    XT(90, 91) "s_nop 1\n\t" "v_smfmac_f32_16x16x64_bf16 v[232:235], v[192:195], v[112:119], %[ix]\n\t" "s_nop 1\n\t" \
    "v_smfmac_f32_16x16x64_bf16 v[236:239], v[192:195], v[176:183], %[ix]\n\t" "ds_read_b128 v[176:179], %[l0] offset:28672\n\t" "v_add_f32 v224, v224, v225\n\t" \
    "v_add_f32 v226, v226, v227\n\t" "v_smfmac_f32_16x16x64_bf16 v[232:235], v[196:199], v[120:127], %[ix]\n\t" "ds_read_b128 v[180:183], %[l0] offset:29696\n\t" \
    "v_add_f32 v228, v228, v229\n\t" "v_add_f32 v230, v230, v231\n\t" "v_smfmac_f32_16x16x64_bf16 v[236:239], v[196:199], v[184:191], %[ix]\n\t" \
    "ds_read_b128 v[184:187], %[l0] offset:30720\n\t" "v_cvt_pk_bf16_f32 v218, v224, v228\n\t" "v_cvt_pk_bf16_f32 v219, v226, v230\n\t" \
    "v_smfmac_f32_16x16x64_bf16 v[232:235], v[240:243], v[128:135], %[ix]\n\t" "ds_write2_b32 %[qw], v218, v219 offset1:8\n\t" \
    "ds_read_b128 v[188:191], %[l0] offset:31744\n\t" "v_smfmac_f32_16x16x64_bf16 v[236:239], v[240:243], v[160:167], %[ix]\n\t" \
    "v_smfmac_f32_16x16x64_bf16 v[232:235], v[244:247], v[136:143], %[ix]\n\t" "v_smfmac_f32_16x16x64_bf16 v[236:239], v[244:247], v[168:175], %[ix]\n\t" \
    "s_waitcnt lgkmcnt(1)\n\t" "s_barrier\n\t" "v_smfmac_f32_16x16x64_bf16 v[232:235], v[248:251], v[144:151], %[ix]\n\t" \
    "v_smfmac_f32_16x16x64_bf16 v[236:239], v[248:251], v[176:183], %[ix]\n\t" "v_smfmac_f32_16x16x64_bf16 v[232:235], v[252:255], v[152:159], %[ix]\n\t" \
    "s_waitcnt lgkmcnt(0)\n\t" "v_smfmac_f32_16x16x64_bf16 v[236:239], v[252:255], v[184:191], %[ix]\n\t" XT(92, 93) "s_nop 6\n\t" "v_add_f32 v232, v232, v233\n\t" \
    "v_add_f32 v234, v234, v235\n\t" "v_add_f32 v236, v236, v237\n\t" "v_add_f32 v238, v238, v239\n\t" "v_cvt_pk_bf16_f32 v232, v232, v236\n\t" \
    "v_cvt_pk_bf16_f32 v233, v234, v238\n\t" "ds_write2_b32 %[kw], v232, v233 offset1:68\n\t" "s_waitcnt lgkmcnt(0)\n\t" "s_barrier\n\t" XT(94, 95) XT(96, 97) \
    "s_branch 99f\n\t" "41:\n\t" "ds_read_b128 v[220:223], %[l1] offset:12288\n\t" "s_waitcnt lgkmcnt(0)\n\t" "v_max3_u32 v218, v220, v221, v222\n\t" \
    "v_max_u32 v218, v218, v223\n\t" "ds_read_b128 v[220:223], %[l1] offset:13312\n\t" "s_waitcnt lgkmcnt(0)\n\t" "v_max3_u32 v218, v218, v220, v221\n\t" \
    "v_max3_u32 v218, v218, v222, v223\n\t" "ds_read_b128 v[220:223], %[l1] offset:14336\n\t" "s_waitcnt lgkmcnt(0)\n\t" "v_max3_u32 v218, v218, v220, v221\n\t" \
    "v_max3_u32 v218, v218, v222, v223\n\t" "ds_read_b128 v[220:223], %[l1] offset:15360\n\t" "s_waitcnt lgkmcnt(0)\n\t" "v_max3_u32 v218, v218, v220, v221\n\t" \
    "v_max3_u32 v218, v218, v222, v223\n\t" "v_cmp_eq_u32 vcc, -1, v218\n\t" "s_cbranch_vccz 42b\n\t" "s_cmp_eq_u32 %[rt], 1\n\t" "s_cbranch_scc1 42b\n\t" \
    "v_mov_b32 v218, %[rt]\n\t" "ds_write_b32 v219, v218\n\t" "s_waitcnt lgkmcnt(0)\n\t" "s_branch 42b\n\t" "43:\n\t" "s_sub_u32 %[rt], %[rt], 1\n\t" \
    "s_waitcnt lgkmcnt(0)\n\t" XCNT(100) "44:\n\t" "buffer_load_dwordx4 v[240:243], %[mo], %[rs], %[sb] offen sc1\n\t" \
    "buffer_load_dwordx4 v[244:247], v216, %[rs], %[sb] offen sc1\n\t" "buffer_load_dwordx4 v[248:251], %[mo], %[rs], s60 offen sc1\n\t" \
    "buffer_load_dwordx4 v[252:255], v216, %[rs], s60 offen sc1\n\t" "s_waitcnt vmcnt(0)\n\t" "v_max_u32 v218, v240, v241\n\t" "v_max3_u32 v218, v218, v242, v243\n\t" \
    "v_max3_u32 v218, v218, v244, v245\n\t" "v_max3_u32 v218, v218, v246, v247\n\t" "v_max3_u32 v218, v218, v248, v249\n\t" "v_max3_u32 v218, v218, v250, v251\n\t" \
    "v_max3_u32 v218, v218, v252, v253\n\t" "v_max3_u32 v218, v218, v254, v255\n\t" "v_cmp_eq_u32 vcc, -1, v218\n\t" "s_cbranch_vccz 45f\n\t" "s_cmp_eq_u32 %[bd], 0\n\t" \
    "s_cbranch_scc1 45f\n\t" "s_sub_u32 %[bd], %[bd], 1\n\t" XCNT(101) "s_branch 44b\n\t" "45:\n\t" "ds_read_b128 v[160:163], %[l0] offset:0\n\t" \
    "ds_read_b128 v[164:167], %[l0] offset:1024\n\t" "ds_read_b128 v[168:171], %[l0] offset:2048\n\t" "ds_read_b128 v[172:175], %[l0] offset:3072\n\t" \
    "ds_read_b128 v[176:179], %[l0] offset:4096\n\t" "ds_read_b128 v[180:183], %[l0] offset:5120\n\t" "ds_read_b128 v[184:187], %[l0] offset:6144\n\t" \
    "ds_read_b128 v[188:191], %[l0] offset:7168\n\t" "v_mov_b64 v[224:225], 0\n\t" "v_mov_b64 v[226:227], 0\n\t" "v_mov_b64 v[228:229], 0\n\t" "v_mov_b64 v[230:231], 0\n\t" \
    "v_mov_b64 v[232:233], 0\n\t" "v_mov_b64 v[234:235], 0\n\t" "v_mov_b64 v[236:237], 0\n\t" "v_mov_b64 v[238:239], 0\n\t" "s_barrier\n\t" "s_memtime s[64:65]\n\t" \
    "s_waitcnt lgkmcnt(0)\n\t" "s_add_u32 s64, s64, %[dr]\n\t" "s_sub_u32 s62, s64, %[ca]\n\t" "s_mul_hi_u32 s63, s62, %[k1]\n\t" "s_mul_i32 s62, s62, %[k1]\n\t" \
    "s_lshr_b64 s[62:63], s[62:63], 16\n\t" "s_add_u32 s62, s62, %[ga]\n\t" "s_bitset0_b32 s62, 0\n\t" "v_mov_b32 v218, s62\n\t" \
    "buffer_store_dword v218, %[hq], %[rs], s67 offen sc1\n\t" "s_mov_b32 s67, 0x7f000000\n\t" "s_branch 40b\n\t" "99:\n\t" "mk_tail:\n\t"
#define VO_STEP \
    "v_xor_b32 v224, 0x400, %[mo]\n\t" "s_xor_b32 s60, %[sb], 0x800\n\t" "s_mov_b32 s67, %[pb]\n\t" "s_cmp_eq_u32 %[w], 1\n\t" "s_cbranch_scc1 91f\n\t" \
    "s_cmp_eq_u32 %[w], 2\n\t" "s_cbranch_scc1 92f\n\t" "s_cmp_eq_u32 %[w], 3\n\t" "s_cbranch_scc1 93f\n\t" XT(80, 81) "s_getpc_b64 s[62:63]\n\t" "18:\n\t" \
    "s_memtime s[64:65]\n\t" "s_waitcnt lgkmcnt(0)\n\t" "s_sub_u32 s61, %[tg], s64\n\t" "s_cmp_gt_i32 s61, 124\n\t" "s_cbranch_scc1 18b\n\t" "s_max_i32 s61, s61, 0\n\t" \
    "s_and_b32 s61, s61, 124\n\t" "s_sub_u32 s61, 19f-18b, s61\n\t" "s_add_u32 s62, s62, s61\n\t" "s_addc_u32 s63, s63, 0\n\t" "s_setpc_b64 s[62:63]\n\t" ".rept 31\n\t" \
    "s_nop 0\n\t" ".endr\n\t" "19:\n\t" "s_barrier\n\t" XT(82, 83) "buffer_load_dwordx4 v[240:243], %[mo], %[rs], %[sb] offen sc1\n\t" "v_mov_b64 v[232:233], 0\n\t" \
    "v_mov_b64 v[234:235], 0\n\t" "v_mov_b64 v[236:237], 0\n\t" "v_mov_b64 v[238:239], 0\n\t" "v_mov_b64 v[192:193], 0\n\t" "v_mov_b64 v[194:195], 0\n\t" \
    "v_mov_b64 v[196:197], 0\n\t" "v_mov_b64 v[198:199], 0\n\t" "buffer_load_dwordx4 v[244:247], v224, %[rs], %[sb] offen sc1\n\t" \
    "ds_read_b128 v[160:163], %[l0] offset:0\n\t" "ds_read_b128 v[164:167], %[l0] offset:1024\n\t" "ds_read_b128 v[168:171], %[l0] offset:2048\n\t" \
    "ds_read_b128 v[172:175], %[l0] offset:3072\n\t" "s_nop 3\n\t" "buffer_load_dwordx4 v[248:251], %[mo], %[rs], s60 offen sc1\n\t" \
    "ds_read_b128 v[176:179], %[l0] offset:4096\n\t" "ds_read_b128 v[180:183], %[l0] offset:5120\n\t" "ds_read_b128 v[184:187], %[l0] offset:6144\n\t" \
    "ds_read_b128 v[188:191], %[l0] offset:7168\n\t" "s_nop 3\n\t" "buffer_load_dwordx4 v[252:255], v224, %[rs], s60 offen sc1\n\t" "v_mov_b64 v[200:201], 0\n\t" \
    "v_mov_b64 v[202:203], 0\n\t" "v_mov_b64 v[204:205], 0\n\t" "v_mov_b64 v[206:207], 0\n\t" "v_mov_b64 v[208:209], 0\n\t" "v_mov_b64 v[210:211], 0\n\t" \
    "v_mov_b64 v[212:213], 0\n\t" "v_mov_b64 v[214:215], 0\n\t" "v_mov_b64 v[216:217], 0\n\t" "v_mov_b64 v[218:219], 0\n\t" "v_mov_b64 v[220:221], 0\n\t" \
    "v_mov_b64 v[222:223], 0\n\t" "v_mov_b32 v226, -1\n\t" "buffer_store_dword v226, %[hq], %[rs], %[pr] offen sc1\n\t" "v_mov_b32 v227, 160512\n\t" "10:\n\t" \
    "s_waitcnt vmcnt(4)\n\t" XT(84, 85) "v_smfmac_f32_16x16x64_bf16 v[232:235], v[240:243], a[0:7], %[ix]\n\t" "ds_write_b128 %[l1], v[240:243] offset:0\n\t" \
    "v_smfmac_f32_16x16x64_bf16 v[236:239], v[240:243], a[8:15], %[ix]\n\t" "s_waitcnt vmcnt(3)\n\t" "v_smfmac_f32_16x16x64_bf16 v[232:235], v[244:247], a[16:23], %[ix]\n\t" \
    "ds_write_b128 %[l1], v[244:247] offset:1024\n\t" "s_waitcnt lgkmcnt(8)\n\t" "v_smfmac_f32_16x16x64_bf16 v[236:239], v[244:247], v[160:167], %[ix]\n\t" \
    "s_waitcnt lgkmcnt(0)\n\t" "s_barrier\n\t" XT(86, 87) "ds_read_b128 v[16:19], %[l1] offset:4096\n\t" "ds_read_b128 v[20:23], %[l1] offset:8192\n\t" \
    "ds_read_b128 v[24:27], %[l1] offset:12288\n\t" "s_waitcnt vmcnt(2)\n\t" "v_smfmac_f32_16x16x64_bf16 v[232:235], v[248:251], a[24:31], %[ix]\n\t" \
    "ds_write_b128 %[l1], v[248:251] offset:2048\n\t" "v_smfmac_f32_16x16x64_bf16 v[236:239], v[248:251], a[32:39], %[ix]\n\t" "ds_read_b128 v[28:31], %[l1] offset:5120\n\t" \
    "ds_read_b128 v[240:243], %[l1] offset:9216\n\t" "ds_read_b128 v[244:247], %[l1] offset:13312\n\t" "s_waitcnt vmcnt(1)\n\t" \
    "v_smfmac_f32_16x16x64_bf16 v[232:235], v[252:255], a[40:47], %[ix]\n\t" "ds_write_b128 %[l1], v[252:255] offset:3072\n\t" \
    "v_smfmac_f32_16x16x64_bf16 v[236:239], v[252:255], v[168:175], %[ix]\n\t" "ds_read_b128 v[160:163], %[l0] offset:8192\n\t" \
    "ds_read_b128 v[164:167], %[l0] offset:9216\n\t" "ds_read_b128 v[168:171], %[l0] offset:10240\n\t" "ds_read_b128 v[172:175], %[l0] offset:11264\n\t" "s_nop 1\n\t" \
    "v_cmp_u_f32 vcc, v232, v233\n\t" "v_cmp_u_f32 s[62:63], v234, v235\n\t" "s_waitcnt lgkmcnt(11)\n\t" \
    "v_smfmac_f32_16x16x64_bf16 v[232:235], v[16:19], a[48:55], %[ix]\n\t" "v_smfmac_f32_16x16x64_bf16 v[236:239], v[16:19], a[56:63], %[ix]\n\t" \
    "s_or_b64 vcc, vcc, s[62:63]\n\t" "s_cbranch_scc1 11f\n\t" "12:\n\t" "s_waitcnt lgkmcnt(10)\n\t" "v_smfmac_f32_16x16x64_bf16 v[232:235], v[20:23], a[64:71], %[ix]\n\t" \
    "v_smfmac_f32_16x16x64_bf16 v[236:239], v[20:23], v[176:183], %[ix]\n\t" "s_waitcnt lgkmcnt(4)\n\t" "s_barrier\n\t" XT(88, 89) "ds_read_b32 v225, v227\n\t" \
    "v_smfmac_f32_16x16x64_bf16 v[232:235], v[24:27], a[72:79], %[ix]\n\t" "ds_read_b128 v[176:179], %[l0] offset:12288\n\t" "ds_read_b128 v[180:183], %[l0] offset:13312\n\t" \
    "v_smfmac_f32_16x16x64_bf16 v[236:239], v[24:27], a[80:87], %[ix]\n\t" "ds_read_b128 v[248:251], %[l1] offset:6144\n\t" "ds_read_b128 v[252:255], %[l1] offset:10240\n\t" \
    "v_smfmac_f32_16x16x64_bf16 v[232:235], v[28:31], a[88:95], %[ix]\n\t" "ds_read_b128 v[16:19], %[l1] offset:14336\n\t" "ds_read_b128 v[20:23], %[l1] offset:7168\n\t" \
    "v_smfmac_f32_16x16x64_bf16 v[236:239], v[28:31], v[184:191], %[ix]\n\t" "s_memtime s[64:65]\n\t" "ds_read_b128 v[24:27], %[l1] offset:11264\n\t" \
    "v_smfmac_f32_16x16x64_bf16 v[232:235], v[240:243], a[96:103], %[ix]\n\t" "ds_read_b128 v[184:187], %[l0] offset:14336\n\t" \
    "ds_read_b128 v[188:191], %[l0] offset:15360\n\t" "v_smfmac_f32_16x16x64_bf16 v[236:239], v[240:243], a[104:111], %[ix]\n\t" "s_waitcnt lgkmcnt(9)\n\t" \
    "v_cmp_eq_u32 vcc, %[rt], v225\n\t" "v_smfmac_f32_16x16x64_bf16 v[232:235], v[244:247], a[112:119], %[ix]\n\t" "s_cbranch_vccnz 13f\n\t" \
    "ds_read_b128 v[28:31], %[l1] offset:15360\n\t" "v_smfmac_f32_16x16x64_bf16 v[236:239], v[244:247], v[160:167], %[ix]\n\t" \
    "ds_read_b128 v[160:163], %[l0] offset:16384\n\t" "ds_read_b128 v[164:167], %[l0] offset:17408\n\t" "s_waitcnt lgkmcnt(9)\n\t" \
    "v_smfmac_f32_16x16x64_bf16 v[232:235], v[248:251], a[120:127], %[ix]\n\t" "s_sub_u32 s62, s64, %[ca]\n\t" "s_mul_hi_u32 s63, s62, %[k1]\n\t" \
    "v_smfmac_f32_16x16x64_bf16 v[236:239], v[248:251], a[128:135], %[ix]\n\t" "s_mul_i32 s62, s62, %[k1]\n\t" "s_lshr_b64 s[62:63], s[62:63], 16\n\t" \
    "s_waitcnt lgkmcnt(8)\n\t" "v_smfmac_f32_16x16x64_bf16 v[232:235], v[252:255], a[136:143], %[ix]\n\t" "s_add_u32 s62, s62, %[ga]\n\t" "s_bitset0_b32 s62, 0\n\t" \
    "v_smfmac_f32_16x16x64_bf16 v[236:239], v[252:255], v[168:175], %[ix]\n\t" "v_mov_b32 v226, s62\n\t" "buffer_store_dword v226, %[hq], %[rs], s67 offen sc1\n\t" \
    "s_waitcnt lgkmcnt(7)\n\t" "v_smfmac_f32_16x16x64_bf16 v[232:235], v[16:19], a[144:151], %[ix]\n\t" "ds_read_b128 v[168:171], %[l0] offset:18432\n\t" \
    "ds_read_b128 v[172:175], %[l0] offset:19456\n\t" "v_smfmac_f32_16x16x64_bf16 v[236:239], v[16:19], a[152:159], %[ix]\n\t" "s_waitcnt lgkmcnt(8)\n\t" \
    "v_smfmac_f32_16x16x64_bf16 v[232:235], v[20:23], a[160:167], %[ix]\n\t" "v_smfmac_f32_16x16x64_bf16 v[236:239], v[20:23], v[176:183], %[ix]\n\t" \
    "ds_read_b128 v[176:179], %[l0] offset:20480\n\t" "ds_read_b128 v[180:183], %[l0] offset:21504\n\t" "s_waitcnt lgkmcnt(9)\n\t" \
    "v_smfmac_f32_16x16x64_bf16 v[232:235], v[24:27], a[168:175], %[ix]\n\t" "v_smfmac_f32_16x16x64_bf16 v[236:239], v[24:27], a[176:183], %[ix]\n\t" \
    "s_waitcnt lgkmcnt(6)\n\t" "v_smfmac_f32_16x16x64_bf16 v[232:235], v[28:31], a[184:191], %[ix]\n\t" \
    "v_smfmac_f32_16x16x64_bf16 v[236:239], v[28:31], v[184:191], %[ix]\n\t" "ds_read_b128 v[184:187], %[l0] offset:22528\n\t" \
    "ds_read_b128 v[188:191], %[l0] offset:23552\n\t" XT(90, 91) "s_nop 4\n\t" "v_add_f32 v232, v232, v233\n\t" "v_add_f32 v234, v234, v235\n\t" \
    "v_add_f32 v236, v236, v237\n\t" "v_add_f32 v238, v238, v239\n\t" "v_cvt_pk_bf16_f32 v236, v232, v236\n\t" "v_cvt_pk_bf16_f32 v237, v234, v238\n\t" \
    "ds_write2_b32 %[vw], v236, v237 offset1:68\n\t" "s_waitcnt lgkmcnt(0)\n\t" "s_barrier\n\t" XT(92, 93) "ds_read_b128 v[240:243], %[vr] offset:0\n\t" \
    "ds_read_b128 v[244:247], %[vr] offset:64\n\t" "ds_read_b128 v[248:251], %[vr] offset:128\n\t" "ds_read_b128 v[252:255], %[vr] offset:192\n\t" "s_mov_b32 s61, %[ub]\n\t" \
    "s_waitcnt lgkmcnt(3)\n\t" "v_smfmac_f32_16x16x64_bf16 v[192:195], v[240:243], v[32:39], %[ix]\n\t" \
    "v_smfmac_f32_16x16x64_bf16 v[196:199], v[240:243], v[48:55], %[ix]\n\t" "s_waitcnt lgkmcnt(2)\n\t" \
    "v_smfmac_f32_16x16x64_bf16 v[192:195], v[244:247], v[40:47], %[ix]\n\t" "v_smfmac_f32_16x16x64_bf16 v[196:199], v[244:247], v[56:63], %[ix]\n\t" \
    "s_waitcnt lgkmcnt(1)\n\t" "v_smfmac_f32_16x16x64_bf16 v[192:195], v[248:251], a[192:199], %[ix]\n\t" \
    "v_smfmac_f32_16x16x64_bf16 v[196:199], v[248:251], a[200:207], %[ix]\n\t" "s_waitcnt lgkmcnt(0)\n\t" \
    "v_smfmac_f32_16x16x64_bf16 v[192:195], v[252:255], v[160:167], %[ix]\n\t" "ds_read_b128 v[160:163], %[l0] offset:24576\n\t" \
    "ds_read_b128 v[164:167], %[l0] offset:25600\n\t" "v_smfmac_f32_16x16x64_bf16 v[196:199], v[252:255], v[168:175], %[ix]\n\t" \
    "ds_read_b128 v[168:171], %[l0] offset:26624\n\t" "ds_read_b128 v[172:175], %[l0] offset:27648\n\t" \
    "v_smfmac_f32_16x16x64_bf16 v[200:203], v[240:243], v[64:71], %[ix]\n\t" "s_nop 1\n\t" "v_smfmac_f32_16x16x64_bf16 v[204:207], v[240:243], v[80:87], %[ix]\n\t" \
    "v_cvt_pk_bf16_f32 v224, v192, v194\n\t" "v_cvt_pk_bf16_f32 v225, v193, v195\n\t" "v_smfmac_f32_16x16x64_bf16 v[200:203], v[244:247], v[72:79], %[ix]\n\t" \
    "v_cvt_pk_bf16_f32 v226, v196, v198\n\t" "v_cvt_pk_bf16_f32 v227, v197, v199\n\t" "v_smfmac_f32_16x16x64_bf16 v[204:207], v[244:247], v[88:95], %[ix]\n\t" "s_nop 0\n\t" \
    "v_permlane32_swap_b32 v224, v226\n\t" "v_smfmac_f32_16x16x64_bf16 v[200:203], v[248:251], a[208:215], %[ix]\n\t" "v_permlane32_swap_b32 v225, v227\n\t" "s_nop 0\n\t" \
    "v_smfmac_f32_16x16x64_bf16 v[204:207], v[248:251], a[216:223], %[ix]\n\t" "v_permlane16_swap_b32 v224, v225\n\t" "v_permlane16_swap_b32 v226, v227\n\t" \
    "v_smfmac_f32_16x16x64_bf16 v[200:203], v[252:255], v[176:183], %[ix]\n\t" "ds_read_b128 v[176:179], %[l0] offset:28672\n\t" \
    "buffer_store_dwordx4 v[224:227], %[uo], %[rs], s61 offen sc1\n\t" "s_xor_b32 s61, s61, 0x40000\n\t" XT(94, 95) \
    "v_smfmac_f32_16x16x64_bf16 v[204:207], v[252:255], v[184:191], %[ix]\n\t" "ds_read_b128 v[180:183], %[l0] offset:29696\n\t" \
    "ds_read_b128 v[184:187], %[l0] offset:30720\n\t" "v_smfmac_f32_16x16x64_bf16 v[208:211], v[240:243], v[96:103], %[ix]\n\t" \
    "ds_read_b128 v[188:191], %[l0] offset:31744\n\t" "s_nop 0\n\t" "v_smfmac_f32_16x16x64_bf16 v[212:215], v[240:243], v[112:119], %[ix]\n\t" \
    "v_cvt_pk_bf16_f32 v228, v200, v202\n\t" "v_cvt_pk_bf16_f32 v229, v201, v203\n\t" "v_smfmac_f32_16x16x64_bf16 v[208:211], v[244:247], v[104:111], %[ix]\n\t" \
    "v_cvt_pk_bf16_f32 v230, v204, v206\n\t" "v_cvt_pk_bf16_f32 v231, v205, v207\n\t" "v_smfmac_f32_16x16x64_bf16 v[212:215], v[244:247], v[120:127], %[ix]\n\t" "s_nop 0\n\t" \
    "v_permlane32_swap_b32 v228, v230\n\t" "v_smfmac_f32_16x16x64_bf16 v[208:211], v[248:251], a[224:231], %[ix]\n\t" "v_permlane32_swap_b32 v229, v231\n\t" "s_nop 0\n\t" \
    "v_smfmac_f32_16x16x64_bf16 v[212:215], v[248:251], a[232:239], %[ix]\n\t" "v_permlane16_swap_b32 v228, v229\n\t" "v_permlane16_swap_b32 v230, v231\n\t" \
    "s_waitcnt lgkmcnt(6)\n\t" "v_smfmac_f32_16x16x64_bf16 v[208:211], v[252:255], v[160:167], %[ix]\n\t" "buffer_store_dwordx4 v[228:231], %[uo], %[rs], s61 offen sc1\n\t" \
    "s_xor_b32 s61, s61, 0xc0000\n\t" "s_waitcnt lgkmcnt(4)\n\t" "v_smfmac_f32_16x16x64_bf16 v[212:215], v[252:255], v[168:175], %[ix]\n\t" "s_nop 1\n\t" \
    "v_smfmac_f32_16x16x64_bf16 v[216:219], v[240:243], v[128:135], %[ix]\n\t" "s_nop 1\n\t" "v_smfmac_f32_16x16x64_bf16 v[220:223], v[240:243], v[144:151], %[ix]\n\t" \
    "v_cvt_pk_bf16_f32 v224, v208, v210\n\t" "v_cvt_pk_bf16_f32 v225, v209, v211\n\t" "v_smfmac_f32_16x16x64_bf16 v[216:219], v[244:247], v[136:143], %[ix]\n\t" \
    "v_cvt_pk_bf16_f32 v226, v212, v214\n\t" "v_cvt_pk_bf16_f32 v227, v213, v215\n\t" "v_smfmac_f32_16x16x64_bf16 v[220:223], v[244:247], v[152:159], %[ix]\n\t" "s_nop 0\n\t" \
    "v_permlane32_swap_b32 v224, v226\n\t" "v_smfmac_f32_16x16x64_bf16 v[216:219], v[248:251], a[240:247], %[ix]\n\t" "v_permlane32_swap_b32 v225, v227\n\t" "s_nop 0\n\t" \
    "v_smfmac_f32_16x16x64_bf16 v[220:223], v[248:251], a[248:255], %[ix]\n\t" "v_permlane16_swap_b32 v224, v225\n\t" "v_permlane16_swap_b32 v226, v227\n\t" \
    "s_waitcnt lgkmcnt(2)\n\t" "v_smfmac_f32_16x16x64_bf16 v[216:219], v[252:255], v[176:183], %[ix]\n\t" "buffer_store_dwordx4 v[224:227], %[uo], %[rs], s61 offen sc1\n\t" \
    "s_xor_b32 s61, s61, 0x40000\n\t" "s_waitcnt lgkmcnt(0)\n\t" "v_smfmac_f32_16x16x64_bf16 v[220:223], v[252:255], v[184:191], %[ix]\n\t" "s_nop 5\n\t" \
    "v_cvt_pk_bf16_f32 v228, v216, v218\n\t" "v_cvt_pk_bf16_f32 v229, v217, v219\n\t" "v_cvt_pk_bf16_f32 v230, v220, v222\n\t" "v_cvt_pk_bf16_f32 v231, v221, v223\n\t" \
    "s_nop 0\n\t" "v_permlane32_swap_b32 v228, v230\n\t" "v_permlane32_swap_b32 v229, v231\n\t" "s_nop 1\n\t" "v_permlane16_swap_b32 v228, v229\n\t" \
    "v_permlane16_swap_b32 v230, v231\n\t" "buffer_store_dwordx4 v[228:231], %[uo], %[rs], s61 offen sc1\n\t" "s_nop 1\n\t" XT(96, 97) "s_branch 99f\n\t" "11:\n\t" \
    "ds_read_b128 v[228:231], %[l1] offset:0\n\t" "s_waitcnt lgkmcnt(0)\n\t" "v_max3_u32 v225, v228, v229, v230\n\t" "v_max_u32 v225, v225, v231\n\t" \
    "ds_read_b128 v[228:231], %[l1] offset:1024\n\t" "s_waitcnt lgkmcnt(0)\n\t" "v_max3_u32 v225, v225, v228, v229\n\t" "v_max3_u32 v225, v225, v230, v231\n\t" \
    "ds_read_b128 v[228:231], %[l1] offset:2048\n\t" "s_waitcnt lgkmcnt(0)\n\t" "v_max3_u32 v225, v225, v228, v229\n\t" "v_max3_u32 v225, v225, v230, v231\n\t" \
    "ds_read_b128 v[228:231], %[l1] offset:3072\n\t" "s_waitcnt lgkmcnt(0)\n\t" "v_max3_u32 v225, v225, v228, v229\n\t" "v_max3_u32 v225, v225, v230, v231\n\t" \
    "v_cmp_eq_u32 vcc, -1, v225\n\t" "s_cbranch_vccz 12b\n\t" "s_cmp_eq_u32 %[rt], 1\n\t" "s_cbranch_scc1 12b\n\t" "v_mov_b32 v225, %[rt]\n\t" "ds_write_b32 v227, v225\n\t" \
    "s_waitcnt lgkmcnt(0)\n\t" "s_branch 12b\n\t" "13:\n\t" "s_sub_u32 %[rt], %[rt], 1\n\t" "s_waitcnt lgkmcnt(0)\n\t" XCNT(100) "14:\n\t" \
    "buffer_load_dwordx4 v[240:243], %[mo], %[rs], %[sb] offen sc1\n\t" "buffer_load_dwordx4 v[244:247], v224, %[rs], %[sb] offen sc1\n\t" \
    "buffer_load_dwordx4 v[248:251], %[mo], %[rs], s60 offen sc1\n\t" "buffer_load_dwordx4 v[252:255], v224, %[rs], s60 offen sc1\n\t" "s_waitcnt vmcnt(0)\n\t" \
    "v_max_u32 v225, v240, v241\n\t" "v_max3_u32 v225, v225, v242, v243\n\t" "v_max3_u32 v225, v225, v244, v245\n\t" "v_max3_u32 v225, v225, v246, v247\n\t" \
    "v_max3_u32 v225, v225, v248, v249\n\t" "v_max3_u32 v225, v225, v250, v251\n\t" "v_max3_u32 v225, v225, v252, v253\n\t" "v_max3_u32 v225, v225, v254, v255\n\t" \
    "v_cmp_eq_u32 vcc, -1, v225\n\t" "s_cbranch_vccz 15f\n\t" "s_cmp_eq_u32 %[bd], 0\n\t" "s_cbranch_scc1 15f\n\t" "s_sub_u32 %[bd], %[bd], 1\n\t" XCNT(101) \
    "s_branch 14b\n\t" "15:\n\t" "ds_read_b128 v[160:163], %[l0] offset:0\n\t" "ds_read_b128 v[164:167], %[l0] offset:1024\n\t" \
    "ds_read_b128 v[168:171], %[l0] offset:2048\n\t" "ds_read_b128 v[172:175], %[l0] offset:3072\n\t" "ds_read_b128 v[176:179], %[l0] offset:4096\n\t" \
    "ds_read_b128 v[180:183], %[l0] offset:5120\n\t" "ds_read_b128 v[184:187], %[l0] offset:6144\n\t" "ds_read_b128 v[188:191], %[l0] offset:7168\n\t" \
    "v_mov_b64 v[232:233], 0\n\t" "v_mov_b64 v[234:235], 0\n\t" "v_mov_b64 v[236:237], 0\n\t" "v_mov_b64 v[238:239], 0\n\t" "s_barrier\n\t" "s_memtime s[64:65]\n\t" \
    "s_waitcnt lgkmcnt(0)\n\t" "s_add_u32 s64, s64, %[dr]\n\t" "s_sub_u32 s62, s64, %[ca]\n\t" "s_mul_hi_u32 s63, s62, %[k1]\n\t" "s_mul_i32 s62, s62, %[k1]\n\t" \
    "s_lshr_b64 s[62:63], s[62:63], 16\n\t" "s_add_u32 s62, s62, %[ga]\n\t" "s_bitset0_b32 s62, 0\n\t" "v_mov_b32 v225, s62\n\t" \
    "buffer_store_dword v225, %[hq], %[rs], s67 offen sc1\n\t" "s_mov_b32 s67, 0x7f000000\n\t" "s_branch 10b\n\t" "91:\n\t" XT(80, 81) "s_barrier\n\t" XT(82, 83) \
    "buffer_load_dwordx4 v[240:243], %[mo], %[rs], %[sb] offen sc1\n\t" "v_mov_b64 v[232:233], 0\n\t" "v_mov_b64 v[234:235], 0\n\t" "v_mov_b64 v[236:237], 0\n\t" \
    "v_mov_b64 v[238:239], 0\n\t" "v_mov_b64 v[192:193], 0\n\t" "v_mov_b64 v[194:195], 0\n\t" "v_mov_b64 v[196:197], 0\n\t" "v_mov_b64 v[198:199], 0\n\t" \
    "buffer_load_dwordx4 v[244:247], v224, %[rs], %[sb] offen sc1\n\t" "ds_read_b128 v[160:163], %[l0] offset:0\n\t" "ds_read_b128 v[164:167], %[l0] offset:1024\n\t" \
    "ds_read_b128 v[168:171], %[l0] offset:2048\n\t" "ds_read_b128 v[172:175], %[l0] offset:3072\n\t" "s_nop 3\n\t" \
    "buffer_load_dwordx4 v[248:251], %[mo], %[rs], s60 offen sc1\n\t" "ds_read_b128 v[176:179], %[l0] offset:4096\n\t" "ds_read_b128 v[180:183], %[l0] offset:5120\n\t" \
    "ds_read_b128 v[184:187], %[l0] offset:6144\n\t" "ds_read_b128 v[188:191], %[l0] offset:7168\n\t" "s_nop 3\n\t" \
    "buffer_load_dwordx4 v[252:255], v224, %[rs], s60 offen sc1\n\t" "v_mov_b64 v[200:201], 0\n\t" "v_mov_b64 v[202:203], 0\n\t" "v_mov_b64 v[204:205], 0\n\t" \
    "v_mov_b64 v[206:207], 0\n\t" "v_mov_b64 v[208:209], 0\n\t" "v_mov_b64 v[210:211], 0\n\t" "v_mov_b64 v[212:213], 0\n\t" "v_mov_b64 v[214:215], 0\n\t" \
    "v_mov_b64 v[216:217], 0\n\t" "v_mov_b64 v[218:219], 0\n\t" "v_mov_b64 v[220:221], 0\n\t" "v_mov_b64 v[222:223], 0\n\t" "v_mov_b32 v227, 160512\n\t" "20:\n\t" \
    "s_waitcnt vmcnt(3)\n\t" XT(84, 85) "v_smfmac_f32_16x16x64_bf16 v[232:235], v[240:243], a[0:7], %[ix]\n\t" "ds_write_b128 %[l1], v[240:243] offset:4096\n\t" \
    "v_smfmac_f32_16x16x64_bf16 v[236:239], v[240:243], a[8:15], %[ix]\n\t" "s_waitcnt vmcnt(2)\n\t" "v_smfmac_f32_16x16x64_bf16 v[232:235], v[244:247], a[16:23], %[ix]\n\t" \
    "ds_write_b128 %[l1], v[244:247] offset:5120\n\t" "s_waitcnt lgkmcnt(8)\n\t" "v_smfmac_f32_16x16x64_bf16 v[236:239], v[244:247], v[160:167], %[ix]\n\t" \
    "s_waitcnt lgkmcnt(0)\n\t" "s_barrier\n\t" XT(86, 87) "ds_read_b128 v[16:19], %[l1] offset:8192\n\t" "ds_read_b128 v[20:23], %[l1] offset:12288\n\t" \
    "ds_read_b128 v[24:27], %[l1] offset:0\n\t" "s_waitcnt vmcnt(1)\n\t" "v_smfmac_f32_16x16x64_bf16 v[232:235], v[248:251], a[24:31], %[ix]\n\t" \
    "ds_write_b128 %[l1], v[248:251] offset:6144\n\t" "v_smfmac_f32_16x16x64_bf16 v[236:239], v[248:251], a[32:39], %[ix]\n\t" "ds_read_b128 v[28:31], %[l1] offset:9216\n\t" \
    "ds_read_b128 v[240:243], %[l1] offset:13312\n\t" "ds_read_b128 v[244:247], %[l1] offset:1024\n\t" "s_waitcnt vmcnt(0)\n\t" \
    "v_smfmac_f32_16x16x64_bf16 v[232:235], v[252:255], a[40:47], %[ix]\n\t" "ds_write_b128 %[l1], v[252:255] offset:7168\n\t" \
    "v_smfmac_f32_16x16x64_bf16 v[236:239], v[252:255], v[168:175], %[ix]\n\t" "ds_read_b128 v[160:163], %[l0] offset:8192\n\t" \
    "ds_read_b128 v[164:167], %[l0] offset:9216\n\t" "ds_read_b128 v[168:171], %[l0] offset:10240\n\t" "ds_read_b128 v[172:175], %[l0] offset:11264\n\t" "s_nop 1\n\t" \
    "v_cmp_u_f32 vcc, v232, v233\n\t" "v_cmp_u_f32 s[62:63], v234, v235\n\t" "s_waitcnt lgkmcnt(11)\n\t" \
    "v_smfmac_f32_16x16x64_bf16 v[232:235], v[16:19], a[48:55], %[ix]\n\t" "v_smfmac_f32_16x16x64_bf16 v[236:239], v[16:19], a[56:63], %[ix]\n\t" \
    "s_or_b64 vcc, vcc, s[62:63]\n\t" "s_cbranch_scc1 21f\n\t" "22:\n\t" "s_waitcnt lgkmcnt(10)\n\t" "v_smfmac_f32_16x16x64_bf16 v[232:235], v[20:23], a[64:71], %[ix]\n\t" \
    "v_smfmac_f32_16x16x64_bf16 v[236:239], v[20:23], v[176:183], %[ix]\n\t" "s_waitcnt lgkmcnt(4)\n\t" "s_barrier\n\t" XT(88, 89) "ds_read_b32 v225, v227\n\t" \
    "v_smfmac_f32_16x16x64_bf16 v[232:235], v[24:27], a[72:79], %[ix]\n\t" "ds_read_b128 v[176:179], %[l0] offset:12288\n\t" "ds_read_b128 v[180:183], %[l0] offset:13312\n\t" \
    "v_smfmac_f32_16x16x64_bf16 v[236:239], v[24:27], a[80:87], %[ix]\n\t" "ds_read_b128 v[248:251], %[l1] offset:10240\n\t" "ds_read_b128 v[252:255], %[l1] offset:14336\n\t" \
    "v_smfmac_f32_16x16x64_bf16 v[232:235], v[28:31], a[88:95], %[ix]\n\t" "ds_read_b128 v[16:19], %[l1] offset:2048\n\t" "ds_read_b128 v[20:23], %[l1] offset:11264\n\t" \
    "v_smfmac_f32_16x16x64_bf16 v[236:239], v[28:31], v[184:191], %[ix]\n\t" "ds_read_b128 v[24:27], %[l1] offset:15360\n\t" "ds_read_b128 v[184:187], %[l0] offset:14336\n\t" \
    "v_smfmac_f32_16x16x64_bf16 v[232:235], v[240:243], a[96:103], %[ix]\n\t" "ds_read_b128 v[188:191], %[l0] offset:15360\n\t" "ds_read_b128 v[28:31], %[l1] offset:3072\n\t" \
    "v_smfmac_f32_16x16x64_bf16 v[236:239], v[240:243], a[104:111], %[ix]\n\t" "s_waitcnt lgkmcnt(10)\n\t" "v_cmp_eq_u32 vcc, %[rt], v225\n\t" \
    "v_smfmac_f32_16x16x64_bf16 v[232:235], v[244:247], a[112:119], %[ix]\n\t" "s_cbranch_vccnz 23f\n\t" \
    "v_smfmac_f32_16x16x64_bf16 v[236:239], v[244:247], v[160:167], %[ix]\n\t" "ds_read_b128 v[160:163], %[l0] offset:16384\n\t" \
    "ds_read_b128 v[164:167], %[l0] offset:17408\n\t" "s_waitcnt lgkmcnt(9)\n\t" "v_smfmac_f32_16x16x64_bf16 v[232:235], v[248:251], a[120:127], %[ix]\n\t" \
    "v_smfmac_f32_16x16x64_bf16 v[236:239], v[248:251], a[128:135], %[ix]\n\t" "s_waitcnt lgkmcnt(8)\n\t" \
    "v_smfmac_f32_16x16x64_bf16 v[232:235], v[252:255], a[136:143], %[ix]\n\t" "v_smfmac_f32_16x16x64_bf16 v[236:239], v[252:255], v[168:175], %[ix]\n\t" \
    "ds_read_b128 v[168:171], %[l0] offset:18432\n\t" "ds_read_b128 v[172:175], %[l0] offset:19456\n\t" "s_waitcnt lgkmcnt(9)\n\t" \
    "v_smfmac_f32_16x16x64_bf16 v[232:235], v[16:19], a[144:151], %[ix]\n\t" "v_smfmac_f32_16x16x64_bf16 v[236:239], v[16:19], a[152:159], %[ix]\n\t" \
    "s_waitcnt lgkmcnt(8)\n\t" "v_smfmac_f32_16x16x64_bf16 v[232:235], v[20:23], a[160:167], %[ix]\n\t" \
    "v_smfmac_f32_16x16x64_bf16 v[236:239], v[20:23], v[176:183], %[ix]\n\t" "ds_read_b128 v[176:179], %[l0] offset:20480\n\t" \
    "ds_read_b128 v[180:183], %[l0] offset:21504\n\t" "s_waitcnt lgkmcnt(9)\n\t" "v_smfmac_f32_16x16x64_bf16 v[232:235], v[24:27], a[168:175], %[ix]\n\t" \
    "v_smfmac_f32_16x16x64_bf16 v[236:239], v[24:27], a[176:183], %[ix]\n\t" "s_waitcnt lgkmcnt(6)\n\t" \
    "v_smfmac_f32_16x16x64_bf16 v[232:235], v[28:31], a[184:191], %[ix]\n\t" "v_smfmac_f32_16x16x64_bf16 v[236:239], v[28:31], v[184:191], %[ix]\n\t" \
    "ds_read_b128 v[184:187], %[l0] offset:22528\n\t" "ds_read_b128 v[188:191], %[l0] offset:23552\n\t" XT(90, 91) "s_nop 4\n\t" "v_add_f32 v232, v232, v233\n\t" \
    "v_add_f32 v234, v234, v235\n\t" "v_add_f32 v236, v236, v237\n\t" "v_add_f32 v238, v238, v239\n\t" "v_cvt_pk_bf16_f32 v236, v232, v236\n\t" \
    "v_cvt_pk_bf16_f32 v237, v234, v238\n\t" "ds_write2_b32 %[vw], v236, v237 offset1:68\n\t" "s_waitcnt lgkmcnt(0)\n\t" "s_barrier\n\t" XT(92, 93) \
    "ds_read_b128 v[240:243], %[vr] offset:0\n\t" "ds_read_b128 v[244:247], %[vr] offset:64\n\t" "ds_read_b128 v[248:251], %[vr] offset:128\n\t" \
    "ds_read_b128 v[252:255], %[vr] offset:192\n\t" "s_mov_b32 s61, %[ub]\n\t" "s_waitcnt lgkmcnt(3)\n\t" \
    "v_smfmac_f32_16x16x64_bf16 v[192:195], v[240:243], v[32:39], %[ix]\n\t" "v_smfmac_f32_16x16x64_bf16 v[196:199], v[240:243], v[48:55], %[ix]\n\t" \
    "s_waitcnt lgkmcnt(2)\n\t" "v_smfmac_f32_16x16x64_bf16 v[192:195], v[244:247], v[40:47], %[ix]\n\t" \
    "v_smfmac_f32_16x16x64_bf16 v[196:199], v[244:247], v[56:63], %[ix]\n\t" "s_waitcnt lgkmcnt(1)\n\t" \
    "v_smfmac_f32_16x16x64_bf16 v[192:195], v[248:251], a[192:199], %[ix]\n\t" "v_smfmac_f32_16x16x64_bf16 v[196:199], v[248:251], a[200:207], %[ix]\n\t" \
    "s_waitcnt lgkmcnt(0)\n\t" "v_smfmac_f32_16x16x64_bf16 v[192:195], v[252:255], v[160:167], %[ix]\n\t" "ds_read_b128 v[160:163], %[l0] offset:24576\n\t" \
    "ds_read_b128 v[164:167], %[l0] offset:25600\n\t" "v_smfmac_f32_16x16x64_bf16 v[196:199], v[252:255], v[168:175], %[ix]\n\t" \
    "ds_read_b128 v[168:171], %[l0] offset:26624\n\t" "ds_read_b128 v[172:175], %[l0] offset:27648\n\t" \
    "v_smfmac_f32_16x16x64_bf16 v[200:203], v[240:243], v[64:71], %[ix]\n\t" "s_nop 1\n\t" "v_smfmac_f32_16x16x64_bf16 v[204:207], v[240:243], v[80:87], %[ix]\n\t" \
    "v_cvt_pk_bf16_f32 v224, v192, v194\n\t" "v_cvt_pk_bf16_f32 v225, v193, v195\n\t" "v_smfmac_f32_16x16x64_bf16 v[200:203], v[244:247], v[72:79], %[ix]\n\t" \
    "v_cvt_pk_bf16_f32 v226, v196, v198\n\t" "v_cvt_pk_bf16_f32 v227, v197, v199\n\t" "v_smfmac_f32_16x16x64_bf16 v[204:207], v[244:247], v[88:95], %[ix]\n\t" "s_nop 0\n\t" \
    "v_permlane32_swap_b32 v224, v226\n\t" "v_smfmac_f32_16x16x64_bf16 v[200:203], v[248:251], a[208:215], %[ix]\n\t" "v_permlane32_swap_b32 v225, v227\n\t" "s_nop 0\n\t" \
    "v_smfmac_f32_16x16x64_bf16 v[204:207], v[248:251], a[216:223], %[ix]\n\t" "v_permlane16_swap_b32 v224, v225\n\t" "v_permlane16_swap_b32 v226, v227\n\t" \
    "v_smfmac_f32_16x16x64_bf16 v[200:203], v[252:255], v[176:183], %[ix]\n\t" "ds_read_b128 v[176:179], %[l0] offset:28672\n\t" \
    "buffer_store_dwordx4 v[224:227], %[uo], %[rs], s61 offen sc1\n\t" "s_xor_b32 s61, s61, 0x40000\n\t" XT(94, 95) \
    "v_smfmac_f32_16x16x64_bf16 v[204:207], v[252:255], v[184:191], %[ix]\n\t" "ds_read_b128 v[180:183], %[l0] offset:29696\n\t" \
    "ds_read_b128 v[184:187], %[l0] offset:30720\n\t" "v_smfmac_f32_16x16x64_bf16 v[208:211], v[240:243], v[96:103], %[ix]\n\t" \
    "ds_read_b128 v[188:191], %[l0] offset:31744\n\t" "s_nop 0\n\t" "v_smfmac_f32_16x16x64_bf16 v[212:215], v[240:243], v[112:119], %[ix]\n\t" \
    "v_cvt_pk_bf16_f32 v228, v200, v202\n\t" "v_cvt_pk_bf16_f32 v229, v201, v203\n\t" "v_smfmac_f32_16x16x64_bf16 v[208:211], v[244:247], v[104:111], %[ix]\n\t" \
    "v_cvt_pk_bf16_f32 v230, v204, v206\n\t" "v_cvt_pk_bf16_f32 v231, v205, v207\n\t" "v_smfmac_f32_16x16x64_bf16 v[212:215], v[244:247], v[120:127], %[ix]\n\t" "s_nop 0\n\t" \
    "v_permlane32_swap_b32 v228, v230\n\t" "v_smfmac_f32_16x16x64_bf16 v[208:211], v[248:251], a[224:231], %[ix]\n\t" "v_permlane32_swap_b32 v229, v231\n\t" "s_nop 0\n\t" \
    "v_smfmac_f32_16x16x64_bf16 v[212:215], v[248:251], a[232:239], %[ix]\n\t" "v_permlane16_swap_b32 v228, v229\n\t" "v_permlane16_swap_b32 v230, v231\n\t" \
    "s_waitcnt lgkmcnt(6)\n\t" "v_smfmac_f32_16x16x64_bf16 v[208:211], v[252:255], v[160:167], %[ix]\n\t" "buffer_store_dwordx4 v[228:231], %[uo], %[rs], s61 offen sc1\n\t" \
    "s_xor_b32 s61, s61, 0xc0000\n\t" "s_waitcnt lgkmcnt(4)\n\t" "v_smfmac_f32_16x16x64_bf16 v[212:215], v[252:255], v[168:175], %[ix]\n\t" "s_nop 1\n\t" \
    "v_smfmac_f32_16x16x64_bf16 v[216:219], v[240:243], v[128:135], %[ix]\n\t" "s_nop 1\n\t" "v_smfmac_f32_16x16x64_bf16 v[220:223], v[240:243], v[144:151], %[ix]\n\t" \
    "v_cvt_pk_bf16_f32 v224, v208, v210\n\t" "v_cvt_pk_bf16_f32 v225, v209, v211\n\t" "v_smfmac_f32_16x16x64_bf16 v[216:219], v[244:247], v[136:143], %[ix]\n\t" \
    "v_cvt_pk_bf16_f32 v226, v212, v214\n\t" "v_cvt_pk_bf16_f32 v227, v213, v215\n\t" "v_smfmac_f32_16x16x64_bf16 v[220:223], v[244:247], v[152:159], %[ix]\n\t" "s_nop 0\n\t" \
    "v_permlane32_swap_b32 v224, v226\n\t" "v_smfmac_f32_16x16x64_bf16 v[216:219], v[248:251], a[240:247], %[ix]\n\t" "v_permlane32_swap_b32 v225, v227\n\t" "s_nop 0\n\t" \
    "v_smfmac_f32_16x16x64_bf16 v[220:223], v[248:251], a[248:255], %[ix]\n\t" "v_permlane16_swap_b32 v224, v225\n\t" "v_permlane16_swap_b32 v226, v227\n\t" \
    "s_waitcnt lgkmcnt(2)\n\t" "v_smfmac_f32_16x16x64_bf16 v[216:219], v[252:255], v[176:183], %[ix]\n\t" "buffer_store_dwordx4 v[224:227], %[uo], %[rs], s61 offen sc1\n\t" \
    "s_xor_b32 s61, s61, 0x40000\n\t" "s_waitcnt lgkmcnt(0)\n\t" "v_smfmac_f32_16x16x64_bf16 v[220:223], v[252:255], v[184:191], %[ix]\n\t" "s_nop 5\n\t" \
    "v_cvt_pk_bf16_f32 v228, v216, v218\n\t" "v_cvt_pk_bf16_f32 v229, v217, v219\n\t" "v_cvt_pk_bf16_f32 v230, v220, v222\n\t" "v_cvt_pk_bf16_f32 v231, v221, v223\n\t" \
    "s_nop 0\n\t" "v_permlane32_swap_b32 v228, v230\n\t" "v_permlane32_swap_b32 v229, v231\n\t" "s_nop 1\n\t" "v_permlane16_swap_b32 v228, v229\n\t" \
    "v_permlane16_swap_b32 v230, v231\n\t" "buffer_store_dwordx4 v[228:231], %[uo], %[rs], s61 offen sc1\n\t" "s_nop 1\n\t" XT(96, 97) "s_branch 99f\n\t" "21:\n\t" \
    "ds_read_b128 v[228:231], %[l1] offset:4096\n\t" "s_waitcnt lgkmcnt(0)\n\t" "v_max3_u32 v225, v228, v229, v230\n\t" "v_max_u32 v225, v225, v231\n\t" \
    "ds_read_b128 v[228:231], %[l1] offset:5120\n\t" "s_waitcnt lgkmcnt(0)\n\t" "v_max3_u32 v225, v225, v228, v229\n\t" "v_max3_u32 v225, v225, v230, v231\n\t" \
    "ds_read_b128 v[228:231], %[l1] offset:6144\n\t" "s_waitcnt lgkmcnt(0)\n\t" "v_max3_u32 v225, v225, v228, v229\n\t" "v_max3_u32 v225, v225, v230, v231\n\t" \
    "ds_read_b128 v[228:231], %[l1] offset:7168\n\t" "s_waitcnt lgkmcnt(0)\n\t" "v_max3_u32 v225, v225, v228, v229\n\t" "v_max3_u32 v225, v225, v230, v231\n\t" \
    "v_cmp_eq_u32 vcc, -1, v225\n\t" "s_cbranch_vccz 22b\n\t" "s_cmp_eq_u32 %[rt], 1\n\t" "s_cbranch_scc1 22b\n\t" "v_mov_b32 v225, %[rt]\n\t" "ds_write_b32 v227, v225\n\t" \
    "s_waitcnt lgkmcnt(0)\n\t" "s_branch 22b\n\t" "23:\n\t" "s_sub_u32 %[rt], %[rt], 1\n\t" "s_waitcnt lgkmcnt(0)\n\t" XCNT(100) "24:\n\t" \
    "buffer_load_dwordx4 v[240:243], %[mo], %[rs], %[sb] offen sc1\n\t" "buffer_load_dwordx4 v[244:247], v224, %[rs], %[sb] offen sc1\n\t" \
    "buffer_load_dwordx4 v[248:251], %[mo], %[rs], s60 offen sc1\n\t" "buffer_load_dwordx4 v[252:255], v224, %[rs], s60 offen sc1\n\t" "s_waitcnt vmcnt(0)\n\t" \
    "v_max_u32 v225, v240, v241\n\t" "v_max3_u32 v225, v225, v242, v243\n\t" "v_max3_u32 v225, v225, v244, v245\n\t" "v_max3_u32 v225, v225, v246, v247\n\t" \
    "v_max3_u32 v225, v225, v248, v249\n\t" "v_max3_u32 v225, v225, v250, v251\n\t" "v_max3_u32 v225, v225, v252, v253\n\t" "v_max3_u32 v225, v225, v254, v255\n\t" \
    "v_cmp_eq_u32 vcc, -1, v225\n\t" "s_cbranch_vccz 25f\n\t" "s_cmp_eq_u32 %[bd], 0\n\t" "s_cbranch_scc1 25f\n\t" "s_sub_u32 %[bd], %[bd], 1\n\t" XCNT(101) \
    "s_branch 24b\n\t" "25:\n\t" "ds_read_b128 v[160:163], %[l0] offset:0\n\t" "ds_read_b128 v[164:167], %[l0] offset:1024\n\t" \
    "ds_read_b128 v[168:171], %[l0] offset:2048\n\t" "ds_read_b128 v[172:175], %[l0] offset:3072\n\t" "ds_read_b128 v[176:179], %[l0] offset:4096\n\t" \
    "ds_read_b128 v[180:183], %[l0] offset:5120\n\t" "ds_read_b128 v[184:187], %[l0] offset:6144\n\t" "ds_read_b128 v[188:191], %[l0] offset:7168\n\t" \
    "v_mov_b64 v[232:233], 0\n\t" "v_mov_b64 v[234:235], 0\n\t" "v_mov_b64 v[236:237], 0\n\t" "v_mov_b64 v[238:239], 0\n\t" "s_barrier\n\t" "s_branch 20b\n\t" "92:\n\t" \
    XT(80, 81) "s_barrier\n\t" XT(82, 83) "buffer_load_dwordx4 v[240:243], %[mo], %[rs], %[sb] offen sc1\n\t" "v_mov_b64 v[232:233], 0\n\t" "v_mov_b64 v[234:235], 0\n\t" \
    "v_mov_b64 v[236:237], 0\n\t" "v_mov_b64 v[238:239], 0\n\t" "v_mov_b64 v[192:193], 0\n\t" "v_mov_b64 v[194:195], 0\n\t" "v_mov_b64 v[196:197], 0\n\t" \
    "v_mov_b64 v[198:199], 0\n\t" "buffer_load_dwordx4 v[244:247], v224, %[rs], %[sb] offen sc1\n\t" "ds_read_b128 v[160:163], %[l0] offset:0\n\t" \
    "ds_read_b128 v[164:167], %[l0] offset:1024\n\t" "ds_read_b128 v[168:171], %[l0] offset:2048\n\t" "ds_read_b128 v[172:175], %[l0] offset:3072\n\t" "s_nop 3\n\t" \
    "buffer_load_dwordx4 v[248:251], %[mo], %[rs], s60 offen sc1\n\t" "ds_read_b128 v[176:179], %[l0] offset:4096\n\t" "ds_read_b128 v[180:183], %[l0] offset:5120\n\t" \
    "ds_read_b128 v[184:187], %[l0] offset:6144\n\t" "ds_read_b128 v[188:191], %[l0] offset:7168\n\t" "s_nop 3\n\t" \
    "buffer_load_dwordx4 v[252:255], v224, %[rs], s60 offen sc1\n\t" "v_mov_b64 v[200:201], 0\n\t" "v_mov_b64 v[202:203], 0\n\t" "v_mov_b64 v[204:205], 0\n\t" \
    "v_mov_b64 v[206:207], 0\n\t" "v_mov_b64 v[208:209], 0\n\t" "v_mov_b64 v[210:211], 0\n\t" "v_mov_b64 v[212:213], 0\n\t" "v_mov_b64 v[214:215], 0\n\t" \
    "v_mov_b64 v[216:217], 0\n\t" "v_mov_b64 v[218:219], 0\n\t" "v_mov_b64 v[220:221], 0\n\t" "v_mov_b64 v[222:223], 0\n\t" "v_mov_b32 v227, 160512\n\t" "30:\n\t" \
    "s_waitcnt vmcnt(3)\n\t" XT(84, 85) "v_smfmac_f32_16x16x64_bf16 v[232:235], v[240:243], a[0:7], %[ix]\n\t" "ds_write_b128 %[l1], v[240:243] offset:8192\n\t" \
    "v_smfmac_f32_16x16x64_bf16 v[236:239], v[240:243], a[8:15], %[ix]\n\t" "s_waitcnt vmcnt(2)\n\t" "v_smfmac_f32_16x16x64_bf16 v[232:235], v[244:247], a[16:23], %[ix]\n\t" \
    "ds_write_b128 %[l1], v[244:247] offset:9216\n\t" "s_waitcnt lgkmcnt(8)\n\t" "v_smfmac_f32_16x16x64_bf16 v[236:239], v[244:247], v[160:167], %[ix]\n\t" \
    "s_waitcnt lgkmcnt(0)\n\t" "s_barrier\n\t" XT(86, 87) "ds_read_b128 v[16:19], %[l1] offset:12288\n\t" "ds_read_b128 v[20:23], %[l1] offset:0\n\t" \
    "ds_read_b128 v[24:27], %[l1] offset:4096\n\t" "s_waitcnt vmcnt(1)\n\t" "v_smfmac_f32_16x16x64_bf16 v[232:235], v[248:251], a[24:31], %[ix]\n\t" \
    "ds_write_b128 %[l1], v[248:251] offset:10240\n\t" "v_smfmac_f32_16x16x64_bf16 v[236:239], v[248:251], a[32:39], %[ix]\n\t" \
    "ds_read_b128 v[28:31], %[l1] offset:13312\n\t" "ds_read_b128 v[240:243], %[l1] offset:1024\n\t" "ds_read_b128 v[244:247], %[l1] offset:5120\n\t" "s_waitcnt vmcnt(0)\n\t" \
    "v_smfmac_f32_16x16x64_bf16 v[232:235], v[252:255], a[40:47], %[ix]\n\t" "ds_write_b128 %[l1], v[252:255] offset:11264\n\t" \
    "v_smfmac_f32_16x16x64_bf16 v[236:239], v[252:255], v[168:175], %[ix]\n\t" "ds_read_b128 v[160:163], %[l0] offset:8192\n\t" \
    "ds_read_b128 v[164:167], %[l0] offset:9216\n\t" "ds_read_b128 v[168:171], %[l0] offset:10240\n\t" "ds_read_b128 v[172:175], %[l0] offset:11264\n\t" "s_nop 1\n\t" \
    "v_cmp_u_f32 vcc, v232, v233\n\t" "v_cmp_u_f32 s[62:63], v234, v235\n\t" "s_waitcnt lgkmcnt(11)\n\t" \
    "v_smfmac_f32_16x16x64_bf16 v[232:235], v[16:19], a[48:55], %[ix]\n\t" "v_smfmac_f32_16x16x64_bf16 v[236:239], v[16:19], a[56:63], %[ix]\n\t" \
    "s_or_b64 vcc, vcc, s[62:63]\n\t" "s_cbranch_scc1 31f\n\t" "32:\n\t" "s_waitcnt lgkmcnt(10)\n\t" "v_smfmac_f32_16x16x64_bf16 v[232:235], v[20:23], a[64:71], %[ix]\n\t" \
    "v_smfmac_f32_16x16x64_bf16 v[236:239], v[20:23], v[176:183], %[ix]\n\t" "s_waitcnt lgkmcnt(4)\n\t" "s_barrier\n\t" XT(88, 89) "ds_read_b32 v225, v227\n\t" \
    "v_smfmac_f32_16x16x64_bf16 v[232:235], v[24:27], a[72:79], %[ix]\n\t" "ds_read_b128 v[176:179], %[l0] offset:12288\n\t" "ds_read_b128 v[180:183], %[l0] offset:13312\n\t" \
    "v_smfmac_f32_16x16x64_bf16 v[236:239], v[24:27], a[80:87], %[ix]\n\t" "ds_read_b128 v[248:251], %[l1] offset:14336\n\t" "ds_read_b128 v[252:255], %[l1] offset:2048\n\t" \
    "v_smfmac_f32_16x16x64_bf16 v[232:235], v[28:31], a[88:95], %[ix]\n\t" "ds_read_b128 v[16:19], %[l1] offset:6144\n\t" "ds_read_b128 v[20:23], %[l1] offset:15360\n\t" \
    "v_smfmac_f32_16x16x64_bf16 v[236:239], v[28:31], v[184:191], %[ix]\n\t" "ds_read_b128 v[24:27], %[l1] offset:3072\n\t" "ds_read_b128 v[184:187], %[l0] offset:14336\n\t" \
    "v_smfmac_f32_16x16x64_bf16 v[232:235], v[240:243], a[96:103], %[ix]\n\t" "ds_read_b128 v[188:191], %[l0] offset:15360\n\t" "ds_read_b128 v[28:31], %[l1] offset:7168\n\t" \
    "v_smfmac_f32_16x16x64_bf16 v[236:239], v[240:243], a[104:111], %[ix]\n\t" "s_waitcnt lgkmcnt(10)\n\t" "v_cmp_eq_u32 vcc, %[rt], v225\n\t" \
    "v_smfmac_f32_16x16x64_bf16 v[232:235], v[244:247], a[112:119], %[ix]\n\t" "s_cbranch_vccnz 33f\n\t" \
    "v_smfmac_f32_16x16x64_bf16 v[236:239], v[244:247], v[160:167], %[ix]\n\t" "ds_read_b128 v[160:163], %[l0] offset:16384\n\t" \
    "ds_read_b128 v[164:167], %[l0] offset:17408\n\t" "s_waitcnt lgkmcnt(9)\n\t" "v_smfmac_f32_16x16x64_bf16 v[232:235], v[248:251], a[120:127], %[ix]\n\t" \
    "v_smfmac_f32_16x16x64_bf16 v[236:239], v[248:251], a[128:135], %[ix]\n\t" "s_waitcnt lgkmcnt(8)\n\t" \
    "v_smfmac_f32_16x16x64_bf16 v[232:235], v[252:255], a[136:143], %[ix]\n\t" "v_smfmac_f32_16x16x64_bf16 v[236:239], v[252:255], v[168:175], %[ix]\n\t" \
    "ds_read_b128 v[168:171], %[l0] offset:18432\n\t" "ds_read_b128 v[172:175], %[l0] offset:19456\n\t" "s_waitcnt lgkmcnt(9)\n\t" \
    "v_smfmac_f32_16x16x64_bf16 v[232:235], v[16:19], a[144:151], %[ix]\n\t" "v_smfmac_f32_16x16x64_bf16 v[236:239], v[16:19], a[152:159], %[ix]\n\t" \
    "s_waitcnt lgkmcnt(8)\n\t" "v_smfmac_f32_16x16x64_bf16 v[232:235], v[20:23], a[160:167], %[ix]\n\t" \
    "v_smfmac_f32_16x16x64_bf16 v[236:239], v[20:23], v[176:183], %[ix]\n\t" "ds_read_b128 v[176:179], %[l0] offset:20480\n\t" \
    "ds_read_b128 v[180:183], %[l0] offset:21504\n\t" "s_waitcnt lgkmcnt(9)\n\t" "v_smfmac_f32_16x16x64_bf16 v[232:235], v[24:27], a[168:175], %[ix]\n\t" \
    "v_smfmac_f32_16x16x64_bf16 v[236:239], v[24:27], a[176:183], %[ix]\n\t" "s_waitcnt lgkmcnt(6)\n\t" \
    "v_smfmac_f32_16x16x64_bf16 v[232:235], v[28:31], a[184:191], %[ix]\n\t" "v_smfmac_f32_16x16x64_bf16 v[236:239], v[28:31], v[184:191], %[ix]\n\t" \
    "ds_read_b128 v[184:187], %[l0] offset:22528\n\t" "ds_read_b128 v[188:191], %[l0] offset:23552\n\t" XT(90, 91) "s_nop 4\n\t" "v_add_f32 v232, v232, v233\n\t" \
    "v_add_f32 v234, v234, v235\n\t" "v_add_f32 v236, v236, v237\n\t" "v_add_f32 v238, v238, v239\n\t" "v_cvt_pk_bf16_f32 v236, v232, v236\n\t" \
    "v_cvt_pk_bf16_f32 v237, v234, v238\n\t" "ds_write2_b32 %[vw], v236, v237 offset1:68\n\t" "s_waitcnt lgkmcnt(0)\n\t" "s_barrier\n\t" XT(92, 93) \
    "ds_read_b128 v[240:243], %[vr] offset:0\n\t" "ds_read_b128 v[244:247], %[vr] offset:64\n\t" "ds_read_b128 v[248:251], %[vr] offset:128\n\t" \
    "ds_read_b128 v[252:255], %[vr] offset:192\n\t" "s_mov_b32 s61, %[ub]\n\t" "s_waitcnt lgkmcnt(3)\n\t" \
    "v_smfmac_f32_16x16x64_bf16 v[192:195], v[240:243], v[32:39], %[ix]\n\t" "v_smfmac_f32_16x16x64_bf16 v[196:199], v[240:243], v[48:55], %[ix]\n\t" \
    "s_waitcnt lgkmcnt(2)\n\t" "v_smfmac_f32_16x16x64_bf16 v[192:195], v[244:247], v[40:47], %[ix]\n\t" \
    "v_smfmac_f32_16x16x64_bf16 v[196:199], v[244:247], v[56:63], %[ix]\n\t" "s_waitcnt lgkmcnt(1)\n\t" \
    "v_smfmac_f32_16x16x64_bf16 v[192:195], v[248:251], a[192:199], %[ix]\n\t" "v_smfmac_f32_16x16x64_bf16 v[196:199], v[248:251], a[200:207], %[ix]\n\t" \
    "s_waitcnt lgkmcnt(0)\n\t" "v_smfmac_f32_16x16x64_bf16 v[192:195], v[252:255], v[160:167], %[ix]\n\t" "ds_read_b128 v[160:163], %[l0] offset:24576\n\t" \
    "ds_read_b128 v[164:167], %[l0] offset:25600\n\t" "v_smfmac_f32_16x16x64_bf16 v[196:199], v[252:255], v[168:175], %[ix]\n\t" \
    "ds_read_b128 v[168:171], %[l0] offset:26624\n\t" "ds_read_b128 v[172:175], %[l0] offset:27648\n\t" \
    "v_smfmac_f32_16x16x64_bf16 v[200:203], v[240:243], v[64:71], %[ix]\n\t" "s_nop 1\n\t" "v_smfmac_f32_16x16x64_bf16 v[204:207], v[240:243], v[80:87], %[ix]\n\t" \
    "v_cvt_pk_bf16_f32 v224, v192, v194\n\t" "v_cvt_pk_bf16_f32 v225, v193, v195\n\t" "v_smfmac_f32_16x16x64_bf16 v[200:203], v[244:247], v[72:79], %[ix]\n\t" \
    "v_cvt_pk_bf16_f32 v226, v196, v198\n\t" "v_cvt_pk_bf16_f32 v227, v197, v199\n\t" "v_smfmac_f32_16x16x64_bf16 v[204:207], v[244:247], v[88:95], %[ix]\n\t" "s_nop 0\n\t" \
    "v_permlane32_swap_b32 v224, v226\n\t" "v_smfmac_f32_16x16x64_bf16 v[200:203], v[248:251], a[208:215], %[ix]\n\t" "v_permlane32_swap_b32 v225, v227\n\t" "s_nop 0\n\t" \
    "v_smfmac_f32_16x16x64_bf16 v[204:207], v[248:251], a[216:223], %[ix]\n\t" "v_permlane16_swap_b32 v224, v225\n\t" "v_permlane16_swap_b32 v226, v227\n\t" \
    "v_smfmac_f32_16x16x64_bf16 v[200:203], v[252:255], v[176:183], %[ix]\n\t" "ds_read_b128 v[176:179], %[l0] offset:28672\n\t" \
    "buffer_store_dwordx4 v[224:227], %[uo], %[rs], s61 offen sc1\n\t" "s_xor_b32 s61, s61, 0x40000\n\t" XT(94, 95) \
    "v_smfmac_f32_16x16x64_bf16 v[204:207], v[252:255], v[184:191], %[ix]\n\t" "ds_read_b128 v[180:183], %[l0] offset:29696\n\t" \
    "ds_read_b128 v[184:187], %[l0] offset:30720\n\t" "v_smfmac_f32_16x16x64_bf16 v[208:211], v[240:243], v[96:103], %[ix]\n\t" \
    "ds_read_b128 v[188:191], %[l0] offset:31744\n\t" "s_nop 0\n\t" "v_smfmac_f32_16x16x64_bf16 v[212:215], v[240:243], v[112:119], %[ix]\n\t" \
    "v_cvt_pk_bf16_f32 v228, v200, v202\n\t" "v_cvt_pk_bf16_f32 v229, v201, v203\n\t" "v_smfmac_f32_16x16x64_bf16 v[208:211], v[244:247], v[104:111], %[ix]\n\t" \
    "v_cvt_pk_bf16_f32 v230, v204, v206\n\t" "v_cvt_pk_bf16_f32 v231, v205, v207\n\t" "v_smfmac_f32_16x16x64_bf16 v[212:215], v[244:247], v[120:127], %[ix]\n\t" "s_nop 0\n\t" \
    "v_permlane32_swap_b32 v228, v230\n\t" "v_smfmac_f32_16x16x64_bf16 v[208:211], v[248:251], a[224:231], %[ix]\n\t" "v_permlane32_swap_b32 v229, v231\n\t" "s_nop 0\n\t" \
    "v_smfmac_f32_16x16x64_bf16 v[212:215], v[248:251], a[232:239], %[ix]\n\t" "v_permlane16_swap_b32 v228, v229\n\t" "v_permlane16_swap_b32 v230, v231\n\t" \
    "s_waitcnt lgkmcnt(6)\n\t" "v_smfmac_f32_16x16x64_bf16 v[208:211], v[252:255], v[160:167], %[ix]\n\t" "buffer_store_dwordx4 v[228:231], %[uo], %[rs], s61 offen sc1\n\t" \
    "s_xor_b32 s61, s61, 0xc0000\n\t" "s_waitcnt lgkmcnt(4)\n\t" "v_smfmac_f32_16x16x64_bf16 v[212:215], v[252:255], v[168:175], %[ix]\n\t" "s_nop 1\n\t" \
    "v_smfmac_f32_16x16x64_bf16 v[216:219], v[240:243], v[128:135], %[ix]\n\t" "s_nop 1\n\t" "v_smfmac_f32_16x16x64_bf16 v[220:223], v[240:243], v[144:151], %[ix]\n\t" \
    "v_cvt_pk_bf16_f32 v224, v208, v210\n\t" "v_cvt_pk_bf16_f32 v225, v209, v211\n\t" "v_smfmac_f32_16x16x64_bf16 v[216:219], v[244:247], v[136:143], %[ix]\n\t" \
    "v_cvt_pk_bf16_f32 v226, v212, v214\n\t" "v_cvt_pk_bf16_f32 v227, v213, v215\n\t" "v_smfmac_f32_16x16x64_bf16 v[220:223], v[244:247], v[152:159], %[ix]\n\t" "s_nop 0\n\t" \
    "v_permlane32_swap_b32 v224, v226\n\t" "v_smfmac_f32_16x16x64_bf16 v[216:219], v[248:251], a[240:247], %[ix]\n\t" "v_permlane32_swap_b32 v225, v227\n\t" "s_nop 0\n\t" \
    "v_smfmac_f32_16x16x64_bf16 v[220:223], v[248:251], a[248:255], %[ix]\n\t" "v_permlane16_swap_b32 v224, v225\n\t" "v_permlane16_swap_b32 v226, v227\n\t" \
    "s_waitcnt lgkmcnt(2)\n\t" "v_smfmac_f32_16x16x64_bf16 v[216:219], v[252:255], v[176:183], %[ix]\n\t" "buffer_store_dwordx4 v[224:227], %[uo], %[rs], s61 offen sc1\n\t" \
    "s_xor_b32 s61, s61, 0x40000\n\t" "s_waitcnt lgkmcnt(0)\n\t" "v_smfmac_f32_16x16x64_bf16 v[220:223], v[252:255], v[184:191], %[ix]\n\t" "s_nop 5\n\t" \
    "v_cvt_pk_bf16_f32 v228, v216, v218\n\t" "v_cvt_pk_bf16_f32 v229, v217, v219\n\t" "v_cvt_pk_bf16_f32 v230, v220, v222\n\t" "v_cvt_pk_bf16_f32 v231, v221, v223\n\t" \
    "s_nop 0\n\t" "v_permlane32_swap_b32 v228, v230\n\t" "v_permlane32_swap_b32 v229, v231\n\t" "s_nop 1\n\t" "v_permlane16_swap_b32 v228, v229\n\t" \
    "v_permlane16_swap_b32 v230, v231\n\t" "buffer_store_dwordx4 v[228:231], %[uo], %[rs], s61 offen sc1\n\t" "s_nop 1\n\t" XT(96, 97) "s_branch 99f\n\t" "31:\n\t" \
    "ds_read_b128 v[228:231], %[l1] offset:8192\n\t" "s_waitcnt lgkmcnt(0)\n\t" "v_max3_u32 v225, v228, v229, v230\n\t" "v_max_u32 v225, v225, v231\n\t" \
    "ds_read_b128 v[228:231], %[l1] offset:9216\n\t" "s_waitcnt lgkmcnt(0)\n\t" "v_max3_u32 v225, v225, v228, v229\n\t" "v_max3_u32 v225, v225, v230, v231\n\t" \
    "ds_read_b128 v[228:231], %[l1] offset:10240\n\t" "s_waitcnt lgkmcnt(0)\n\t" "v_max3_u32 v225, v225, v228, v229\n\t" "v_max3_u32 v225, v225, v230, v231\n\t" \
    "ds_read_b128 v[228:231], %[l1] offset:11264\n\t" "s_waitcnt lgkmcnt(0)\n\t" "v_max3_u32 v225, v225, v228, v229\n\t" "v_max3_u32 v225, v225, v230, v231\n\t" \
    "v_cmp_eq_u32 vcc, -1, v225\n\t" "s_cbranch_vccz 32b\n\t" "s_cmp_eq_u32 %[rt], 1\n\t" "s_cbranch_scc1 32b\n\t" "v_mov_b32 v225, %[rt]\n\t" "ds_write_b32 v227, v225\n\t" \
    "s_waitcnt lgkmcnt(0)\n\t" "s_branch 32b\n\t" "33:\n\t" "s_sub_u32 %[rt], %[rt], 1\n\t" "s_waitcnt lgkmcnt(0)\n\t" XCNT(100) "34:\n\t" \
    "buffer_load_dwordx4 v[240:243], %[mo], %[rs], %[sb] offen sc1\n\t" "buffer_load_dwordx4 v[244:247], v224, %[rs], %[sb] offen sc1\n\t" \
    "buffer_load_dwordx4 v[248:251], %[mo], %[rs], s60 offen sc1\n\t" "buffer_load_dwordx4 v[252:255], v224, %[rs], s60 offen sc1\n\t" "s_waitcnt vmcnt(0)\n\t" \
    "v_max_u32 v225, v240, v241\n\t" "v_max3_u32 v225, v225, v242, v243\n\t" "v_max3_u32 v225, v225, v244, v245\n\t" "v_max3_u32 v225, v225, v246, v247\n\t" \
    "v_max3_u32 v225, v225, v248, v249\n\t" "v_max3_u32 v225, v225, v250, v251\n\t" "v_max3_u32 v225, v225, v252, v253\n\t" "v_max3_u32 v225, v225, v254, v255\n\t" \
    "v_cmp_eq_u32 vcc, -1, v225\n\t" "s_cbranch_vccz 35f\n\t" "s_cmp_eq_u32 %[bd], 0\n\t" "s_cbranch_scc1 35f\n\t" "s_sub_u32 %[bd], %[bd], 1\n\t" XCNT(101) \
    "s_branch 34b\n\t" "35:\n\t" "ds_read_b128 v[160:163], %[l0] offset:0\n\t" "ds_read_b128 v[164:167], %[l0] offset:1024\n\t" \
    "ds_read_b128 v[168:171], %[l0] offset:2048\n\t" "ds_read_b128 v[172:175], %[l0] offset:3072\n\t" "ds_read_b128 v[176:179], %[l0] offset:4096\n\t" \
    "ds_read_b128 v[180:183], %[l0] offset:5120\n\t" "ds_read_b128 v[184:187], %[l0] offset:6144\n\t" "ds_read_b128 v[188:191], %[l0] offset:7168\n\t" \
    "v_mov_b64 v[232:233], 0\n\t" "v_mov_b64 v[234:235], 0\n\t" "v_mov_b64 v[236:237], 0\n\t" "v_mov_b64 v[238:239], 0\n\t" "s_barrier\n\t" "s_branch 30b\n\t" "93:\n\t" \
    XT(80, 81) "s_barrier\n\t" XT(82, 83) "buffer_load_dwordx4 v[240:243], %[mo], %[rs], %[sb] offen sc1\n\t" "v_mov_b64 v[232:233], 0\n\t" "v_mov_b64 v[234:235], 0\n\t" \
    "v_mov_b64 v[236:237], 0\n\t" "v_mov_b64 v[238:239], 0\n\t" "v_mov_b64 v[192:193], 0\n\t" "v_mov_b64 v[194:195], 0\n\t" "v_mov_b64 v[196:197], 0\n\t" \
    "v_mov_b64 v[198:199], 0\n\t" "buffer_load_dwordx4 v[244:247], v224, %[rs], %[sb] offen sc1\n\t" "ds_read_b128 v[160:163], %[l0] offset:0\n\t" \
    "ds_read_b128 v[164:167], %[l0] offset:1024\n\t" "ds_read_b128 v[168:171], %[l0] offset:2048\n\t" "ds_read_b128 v[172:175], %[l0] offset:3072\n\t" "s_nop 3\n\t" \
    "buffer_load_dwordx4 v[248:251], %[mo], %[rs], s60 offen sc1\n\t" "ds_read_b128 v[176:179], %[l0] offset:4096\n\t" "ds_read_b128 v[180:183], %[l0] offset:5120\n\t" \
    "ds_read_b128 v[184:187], %[l0] offset:6144\n\t" "ds_read_b128 v[188:191], %[l0] offset:7168\n\t" "s_nop 3\n\t" \
    "buffer_load_dwordx4 v[252:255], v224, %[rs], s60 offen sc1\n\t" "v_mov_b64 v[200:201], 0\n\t" "v_mov_b64 v[202:203], 0\n\t" "v_mov_b64 v[204:205], 0\n\t" \
    "v_mov_b64 v[206:207], 0\n\t" "v_mov_b64 v[208:209], 0\n\t" "v_mov_b64 v[210:211], 0\n\t" "v_mov_b64 v[212:213], 0\n\t" "v_mov_b64 v[214:215], 0\n\t" \
    "v_mov_b64 v[216:217], 0\n\t" "v_mov_b64 v[218:219], 0\n\t" "v_mov_b64 v[220:221], 0\n\t" "v_mov_b64 v[222:223], 0\n\t" "v_mov_b32 v227, 160512\n\t" "40:\n\t" \
    "s_waitcnt vmcnt(3)\n\t" XT(84, 85) "v_smfmac_f32_16x16x64_bf16 v[232:235], v[240:243], a[0:7], %[ix]\n\t" "ds_write_b128 %[l1], v[240:243] offset:12288\n\t" \
    "v_smfmac_f32_16x16x64_bf16 v[236:239], v[240:243], a[8:15], %[ix]\n\t" "s_waitcnt vmcnt(2)\n\t" "v_smfmac_f32_16x16x64_bf16 v[232:235], v[244:247], a[16:23], %[ix]\n\t" \
    "ds_write_b128 %[l1], v[244:247] offset:13312\n\t" "s_waitcnt lgkmcnt(8)\n\t" "v_smfmac_f32_16x16x64_bf16 v[236:239], v[244:247], v[160:167], %[ix]\n\t" \
    "s_waitcnt lgkmcnt(0)\n\t" "s_barrier\n\t" XT(86, 87) "ds_read_b128 v[16:19], %[l1] offset:0\n\t" "ds_read_b128 v[20:23], %[l1] offset:4096\n\t" \
    "ds_read_b128 v[24:27], %[l1] offset:8192\n\t" "s_waitcnt vmcnt(1)\n\t" "v_smfmac_f32_16x16x64_bf16 v[232:235], v[248:251], a[24:31], %[ix]\n\t" \
    "ds_write_b128 %[l1], v[248:251] offset:14336\n\t" "v_smfmac_f32_16x16x64_bf16 v[236:239], v[248:251], a[32:39], %[ix]\n\t" "ds_read_b128 v[28:31], %[l1] offset:1024\n\t" \
    "ds_read_b128 v[240:243], %[l1] offset:5120\n\t" "ds_read_b128 v[244:247], %[l1] offset:9216\n\t" "s_waitcnt vmcnt(0)\n\t" \
    "v_smfmac_f32_16x16x64_bf16 v[232:235], v[252:255], a[40:47], %[ix]\n\t" "ds_write_b128 %[l1], v[252:255] offset:15360\n\t" \
    "v_smfmac_f32_16x16x64_bf16 v[236:239], v[252:255], v[168:175], %[ix]\n\t" "ds_read_b128 v[160:163], %[l0] offset:8192\n\t" \
    "ds_read_b128 v[164:167], %[l0] offset:9216\n\t" "ds_read_b128 v[168:171], %[l0] offset:10240\n\t" "ds_read_b128 v[172:175], %[l0] offset:11264\n\t" "s_nop 1\n\t" \
    "v_cmp_u_f32 vcc, v232, v233\n\t" "v_cmp_u_f32 s[62:63], v234, v235\n\t" "s_waitcnt lgkmcnt(11)\n\t" \
    "v_smfmac_f32_16x16x64_bf16 v[232:235], v[16:19], a[48:55], %[ix]\n\t" "v_smfmac_f32_16x16x64_bf16 v[236:239], v[16:19], a[56:63], %[ix]\n\t" \
    "s_or_b64 vcc, vcc, s[62:63]\n\t" "s_cbranch_scc1 41f\n\t" "42:\n\t" "s_waitcnt lgkmcnt(10)\n\t" "v_smfmac_f32_16x16x64_bf16 v[232:235], v[20:23], a[64:71], %[ix]\n\t" \
    "v_smfmac_f32_16x16x64_bf16 v[236:239], v[20:23], v[176:183], %[ix]\n\t" "s_waitcnt lgkmcnt(4)\n\t" "s_barrier\n\t" XT(88, 89) "ds_read_b32 v225, v227\n\t" \
    "v_smfmac_f32_16x16x64_bf16 v[232:235], v[24:27], a[72:79], %[ix]\n\t" "ds_read_b128 v[176:179], %[l0] offset:12288\n\t" "ds_read_b128 v[180:183], %[l0] offset:13312\n\t" \
    "v_smfmac_f32_16x16x64_bf16 v[236:239], v[24:27], a[80:87], %[ix]\n\t" "ds_read_b128 v[248:251], %[l1] offset:2048\n\t" "ds_read_b128 v[252:255], %[l1] offset:6144\n\t" \
    "v_smfmac_f32_16x16x64_bf16 v[232:235], v[28:31], a[88:95], %[ix]\n\t" "ds_read_b128 v[16:19], %[l1] offset:10240\n\t" "ds_read_b128 v[20:23], %[l1] offset:3072\n\t" \
    "v_smfmac_f32_16x16x64_bf16 v[236:239], v[28:31], v[184:191], %[ix]\n\t" "ds_read_b128 v[24:27], %[l1] offset:7168\n\t" "ds_read_b128 v[184:187], %[l0] offset:14336\n\t" \
    "v_smfmac_f32_16x16x64_bf16 v[232:235], v[240:243], a[96:103], %[ix]\n\t" "ds_read_b128 v[188:191], %[l0] offset:15360\n\t" \
    "ds_read_b128 v[28:31], %[l1] offset:11264\n\t" "v_smfmac_f32_16x16x64_bf16 v[236:239], v[240:243], a[104:111], %[ix]\n\t" "s_waitcnt lgkmcnt(10)\n\t" \
    "v_cmp_eq_u32 vcc, %[rt], v225\n\t" "v_smfmac_f32_16x16x64_bf16 v[232:235], v[244:247], a[112:119], %[ix]\n\t" "s_cbranch_vccnz 43f\n\t" \
    "v_smfmac_f32_16x16x64_bf16 v[236:239], v[244:247], v[160:167], %[ix]\n\t" "ds_read_b128 v[160:163], %[l0] offset:16384\n\t" \
    "ds_read_b128 v[164:167], %[l0] offset:17408\n\t" "s_waitcnt lgkmcnt(9)\n\t" "v_smfmac_f32_16x16x64_bf16 v[232:235], v[248:251], a[120:127], %[ix]\n\t" \
    "v_smfmac_f32_16x16x64_bf16 v[236:239], v[248:251], a[128:135], %[ix]\n\t" "s_waitcnt lgkmcnt(8)\n\t" \
    "v_smfmac_f32_16x16x64_bf16 v[232:235], v[252:255], a[136:143], %[ix]\n\t" "v_smfmac_f32_16x16x64_bf16 v[236:239], v[252:255], v[168:175], %[ix]\n\t" \
    "ds_read_b128 v[168:171], %[l0] offset:18432\n\t" "ds_read_b128 v[172:175], %[l0] offset:19456\n\t" "s_waitcnt lgkmcnt(9)\n\t" \
    "v_smfmac_f32_16x16x64_bf16 v[232:235], v[16:19], a[144:151], %[ix]\n\t" "v_smfmac_f32_16x16x64_bf16 v[236:239], v[16:19], a[152:159], %[ix]\n\t" \
    "s_waitcnt lgkmcnt(8)\n\t" "v_smfmac_f32_16x16x64_bf16 v[232:235], v[20:23], a[160:167], %[ix]\n\t" \
    "v_smfmac_f32_16x16x64_bf16 v[236:239], v[20:23], v[176:183], %[ix]\n\t" "ds_read_b128 v[176:179], %[l0] offset:20480\n\t" \
    "ds_read_b128 v[180:183], %[l0] offset:21504\n\t" "s_waitcnt lgkmcnt(9)\n\t" "v_smfmac_f32_16x16x64_bf16 v[232:235], v[24:27], a[168:175], %[ix]\n\t" \
    "v_smfmac_f32_16x16x64_bf16 v[236:239], v[24:27], a[176:183], %[ix]\n\t" "s_waitcnt lgkmcnt(6)\n\t" \
    "v_smfmac_f32_16x16x64_bf16 v[232:235], v[28:31], a[184:191], %[ix]\n\t" "v_smfmac_f32_16x16x64_bf16 v[236:239], v[28:31], v[184:191], %[ix]\n\t" \
    "ds_read_b128 v[184:187], %[l0] offset:22528\n\t" "ds_read_b128 v[188:191], %[l0] offset:23552\n\t" XT(90, 91) "s_nop 4\n\t" "v_add_f32 v232, v232, v233\n\t" \
    "v_add_f32 v234, v234, v235\n\t" "v_add_f32 v236, v236, v237\n\t" "v_add_f32 v238, v238, v239\n\t" "v_cvt_pk_bf16_f32 v236, v232, v236\n\t" \
    "v_cvt_pk_bf16_f32 v237, v234, v238\n\t" "ds_write2_b32 %[vw], v236, v237 offset1:68\n\t" "s_waitcnt lgkmcnt(0)\n\t" "s_barrier\n\t" XT(92, 93) \
    "ds_read_b128 v[240:243], %[vr] offset:0\n\t" "ds_read_b128 v[244:247], %[vr] offset:64\n\t" "ds_read_b128 v[248:251], %[vr] offset:128\n\t" \
    "ds_read_b128 v[252:255], %[vr] offset:192\n\t" "s_mov_b32 s61, %[ub]\n\t" "s_waitcnt lgkmcnt(3)\n\t" \
    "v_smfmac_f32_16x16x64_bf16 v[192:195], v[240:243], v[32:39], %[ix]\n\t" "v_smfmac_f32_16x16x64_bf16 v[196:199], v[240:243], v[48:55], %[ix]\n\t" \
    "s_waitcnt lgkmcnt(2)\n\t" "v_smfmac_f32_16x16x64_bf16 v[192:195], v[244:247], v[40:47], %[ix]\n\t" \
    "v_smfmac_f32_16x16x64_bf16 v[196:199], v[244:247], v[56:63], %[ix]\n\t" "s_waitcnt lgkmcnt(1)\n\t" \
    "v_smfmac_f32_16x16x64_bf16 v[192:195], v[248:251], a[192:199], %[ix]\n\t" "v_smfmac_f32_16x16x64_bf16 v[196:199], v[248:251], a[200:207], %[ix]\n\t" \
    "s_waitcnt lgkmcnt(0)\n\t" "v_smfmac_f32_16x16x64_bf16 v[192:195], v[252:255], v[160:167], %[ix]\n\t" "ds_read_b128 v[160:163], %[l0] offset:24576\n\t" \
    "ds_read_b128 v[164:167], %[l0] offset:25600\n\t" "v_smfmac_f32_16x16x64_bf16 v[196:199], v[252:255], v[168:175], %[ix]\n\t" \
    "ds_read_b128 v[168:171], %[l0] offset:26624\n\t" "ds_read_b128 v[172:175], %[l0] offset:27648\n\t" \
    "v_smfmac_f32_16x16x64_bf16 v[200:203], v[240:243], v[64:71], %[ix]\n\t" "s_nop 1\n\t" "v_smfmac_f32_16x16x64_bf16 v[204:207], v[240:243], v[80:87], %[ix]\n\t" \
    "v_cvt_pk_bf16_f32 v224, v192, v194\n\t" "v_cvt_pk_bf16_f32 v225, v193, v195\n\t" "v_smfmac_f32_16x16x64_bf16 v[200:203], v[244:247], v[72:79], %[ix]\n\t" \
    "v_cvt_pk_bf16_f32 v226, v196, v198\n\t" "v_cvt_pk_bf16_f32 v227, v197, v199\n\t" "v_smfmac_f32_16x16x64_bf16 v[204:207], v[244:247], v[88:95], %[ix]\n\t" "s_nop 0\n\t" \
    "v_permlane32_swap_b32 v224, v226\n\t" "v_smfmac_f32_16x16x64_bf16 v[200:203], v[248:251], a[208:215], %[ix]\n\t" "v_permlane32_swap_b32 v225, v227\n\t" "s_nop 0\n\t" \
    "v_smfmac_f32_16x16x64_bf16 v[204:207], v[248:251], a[216:223], %[ix]\n\t" "v_permlane16_swap_b32 v224, v225\n\t" "v_permlane16_swap_b32 v226, v227\n\t" \
    "v_smfmac_f32_16x16x64_bf16 v[200:203], v[252:255], v[176:183], %[ix]\n\t" "ds_read_b128 v[176:179], %[l0] offset:28672\n\t" \
    "buffer_store_dwordx4 v[224:227], %[uo], %[rs], s61 offen sc1\n\t" "s_xor_b32 s61, s61, 0x40000\n\t" XT(94, 95) \
    "v_smfmac_f32_16x16x64_bf16 v[204:207], v[252:255], v[184:191], %[ix]\n\t" "ds_read_b128 v[180:183], %[l0] offset:29696\n\t" \
    "ds_read_b128 v[184:187], %[l0] offset:30720\n\t" "v_smfmac_f32_16x16x64_bf16 v[208:211], v[240:243], v[96:103], %[ix]\n\t" \
    "ds_read_b128 v[188:191], %[l0] offset:31744\n\t" "s_nop 0\n\t" "v_smfmac_f32_16x16x64_bf16 v[212:215], v[240:243], v[112:119], %[ix]\n\t" \
    "v_cvt_pk_bf16_f32 v228, v200, v202\n\t" "v_cvt_pk_bf16_f32 v229, v201, v203\n\t" "v_smfmac_f32_16x16x64_bf16 v[208:211], v[244:247], v[104:111], %[ix]\n\t" \
    "v_cvt_pk_bf16_f32 v230, v204, v206\n\t" "v_cvt_pk_bf16_f32 v231, v205, v207\n\t" "v_smfmac_f32_16x16x64_bf16 v[212:215], v[244:247], v[120:127], %[ix]\n\t" "s_nop 0\n\t" \
    "v_permlane32_swap_b32 v228, v230\n\t" "v_smfmac_f32_16x16x64_bf16 v[208:211], v[248:251], a[224:231], %[ix]\n\t" "v_permlane32_swap_b32 v229, v231\n\t" "s_nop 0\n\t" \
    "v_smfmac_f32_16x16x64_bf16 v[212:215], v[248:251], a[232:239], %[ix]\n\t" "v_permlane16_swap_b32 v228, v229\n\t" "v_permlane16_swap_b32 v230, v231\n\t" \
    "s_waitcnt lgkmcnt(6)\n\t" "v_smfmac_f32_16x16x64_bf16 v[208:211], v[252:255], v[160:167], %[ix]\n\t" "buffer_store_dwordx4 v[228:231], %[uo], %[rs], s61 offen sc1\n\t" \
    "s_xor_b32 s61, s61, 0xc0000\n\t" "s_waitcnt lgkmcnt(4)\n\t" "v_smfmac_f32_16x16x64_bf16 v[212:215], v[252:255], v[168:175], %[ix]\n\t" "s_nop 1\n\t" \
    "v_smfmac_f32_16x16x64_bf16 v[216:219], v[240:243], v[128:135], %[ix]\n\t" "s_nop 1\n\t" "v_smfmac_f32_16x16x64_bf16 v[220:223], v[240:243], v[144:151], %[ix]\n\t" \
    "v_cvt_pk_bf16_f32 v224, v208, v210\n\t" "v_cvt_pk_bf16_f32 v225, v209, v211\n\t" "v_smfmac_f32_16x16x64_bf16 v[216:219], v[244:247], v[136:143], %[ix]\n\t" \
    "v_cvt_pk_bf16_f32 v226, v212, v214\n\t" "v_cvt_pk_bf16_f32 v227, v213, v215\n\t" "v_smfmac_f32_16x16x64_bf16 v[220:223], v[244:247], v[152:159], %[ix]\n\t" "s_nop 0\n\t" \
    "v_permlane32_swap_b32 v224, v226\n\t" "v_smfmac_f32_16x16x64_bf16 v[216:219], v[248:251], a[240:247], %[ix]\n\t" "v_permlane32_swap_b32 v225, v227\n\t" "s_nop 0\n\t" \
    "v_smfmac_f32_16x16x64_bf16 v[220:223], v[248:251], a[248:255], %[ix]\n\t" "v_permlane16_swap_b32 v224, v225\n\t" "v_permlane16_swap_b32 v226, v227\n\t" \
    "s_waitcnt lgkmcnt(2)\n\t" "v_smfmac_f32_16x16x64_bf16 v[216:219], v[252:255], v[176:183], %[ix]\n\t" "buffer_store_dwordx4 v[224:227], %[uo], %[rs], s61 offen sc1\n\t" \
    "s_xor_b32 s61, s61, 0x40000\n\t" "s_waitcnt lgkmcnt(0)\n\t" "v_smfmac_f32_16x16x64_bf16 v[220:223], v[252:255], v[184:191], %[ix]\n\t" "s_nop 5\n\t" \
    "v_cvt_pk_bf16_f32 v228, v216, v218\n\t" "v_cvt_pk_bf16_f32 v229, v217, v219\n\t" "v_cvt_pk_bf16_f32 v230, v220, v222\n\t" "v_cvt_pk_bf16_f32 v231, v221, v223\n\t" \
    "s_nop 0\n\t" "v_permlane32_swap_b32 v228, v230\n\t" "v_permlane32_swap_b32 v229, v231\n\t" "s_nop 1\n\t" "v_permlane16_swap_b32 v228, v229\n\t" \
    "v_permlane16_swap_b32 v230, v231\n\t" "buffer_store_dwordx4 v[228:231], %[uo], %[rs], s61 offen sc1\n\t" "s_nop 1\n\t" XT(96, 97) "s_branch 99f\n\t" "41:\n\t" \
    "ds_read_b128 v[228:231], %[l1] offset:12288\n\t" "s_waitcnt lgkmcnt(0)\n\t" "v_max3_u32 v225, v228, v229, v230\n\t" "v_max_u32 v225, v225, v231\n\t" \
    "ds_read_b128 v[228:231], %[l1] offset:13312\n\t" "s_waitcnt lgkmcnt(0)\n\t" "v_max3_u32 v225, v225, v228, v229\n\t" "v_max3_u32 v225, v225, v230, v231\n\t" \
    "ds_read_b128 v[228:231], %[l1] offset:14336\n\t" "s_waitcnt lgkmcnt(0)\n\t" "v_max3_u32 v225, v225, v228, v229\n\t" "v_max3_u32 v225, v225, v230, v231\n\t" \
    "ds_read_b128 v[228:231], %[l1] offset:15360\n\t" "s_waitcnt lgkmcnt(0)\n\t" "v_max3_u32 v225, v225, v228, v229\n\t" "v_max3_u32 v225, v225, v230, v231\n\t" \
    "v_cmp_eq_u32 vcc, -1, v225\n\t" "s_cbranch_vccz 42b\n\t" "s_cmp_eq_u32 %[rt], 1\n\t" "s_cbranch_scc1 42b\n\t" "v_mov_b32 v225, %[rt]\n\t" "ds_write_b32 v227, v225\n\t" \
    "s_waitcnt lgkmcnt(0)\n\t" "s_branch 42b\n\t" "43:\n\t" "s_sub_u32 %[rt], %[rt], 1\n\t" "s_waitcnt lgkmcnt(0)\n\t" XCNT(100) "44:\n\t" \
    "buffer_load_dwordx4 v[240:243], %[mo], %[rs], %[sb] offen sc1\n\t" "buffer_load_dwordx4 v[244:247], v224, %[rs], %[sb] offen sc1\n\t" \
    "buffer_load_dwordx4 v[248:251], %[mo], %[rs], s60 offen sc1\n\t" "buffer_load_dwordx4 v[252:255], v224, %[rs], s60 offen sc1\n\t" "s_waitcnt vmcnt(0)\n\t" \
    "v_max_u32 v225, v240, v241\n\t" "v_max3_u32 v225, v225, v242, v243\n\t" "v_max3_u32 v225, v225, v244, v245\n\t" "v_max3_u32 v225, v225, v246, v247\n\t" \
    "v_max3_u32 v225, v225, v248, v249\n\t" "v_max3_u32 v225, v225, v250, v251\n\t" "v_max3_u32 v225, v225, v252, v253\n\t" "v_max3_u32 v225, v225, v254, v255\n\t" \
    "v_cmp_eq_u32 vcc, -1, v225\n\t" "s_cbranch_vccz 45f\n\t" "s_cmp_eq_u32 %[bd], 0\n\t" "s_cbranch_scc1 45f\n\t" "s_sub_u32 %[bd], %[bd], 1\n\t" XCNT(101) \
    "s_branch 44b\n\t" "45:\n\t" "ds_read_b128 v[160:163], %[l0] offset:0\n\t" "ds_read_b128 v[164:167], %[l0] offset:1024\n\t" \
    "ds_read_b128 v[168:171], %[l0] offset:2048\n\t" "ds_read_b128 v[172:175], %[l0] offset:3072\n\t" "ds_read_b128 v[176:179], %[l0] offset:4096\n\t" \
    "ds_read_b128 v[180:183], %[l0] offset:5120\n\t" "ds_read_b128 v[184:187], %[l0] offset:6144\n\t" "ds_read_b128 v[188:191], %[l0] offset:7168\n\t" \
    "v_mov_b64 v[232:233], 0\n\t" "v_mov_b64 v[234:235], 0\n\t" "v_mov_b64 v[236:237], 0\n\t" "v_mov_b64 v[238:239], 0\n\t" "s_barrier\n\t" "s_branch 40b\n\t" "99:\n\t"
#define Y_STEP \
    "v_xor_b32 v128, 0, %[u0]\n\t" "v_xor_b32 v132, 0, %[pm]\n\t" "v_xor_b32 v129, 2048, %[u0]\n\t" "v_xor_b32 v133, 1024, %[pm]\n\t" "v_xor_b32 v130, 4096, %[u0]\n\t" \
    "v_xor_b32 v134, 2048, %[pm]\n\t" "v_xor_b32 v131, 6144, %[u0]\n\t" "v_xor_b32 v135, 3072, %[pm]\n\t" "s_getpc_b64 s[62:63]\n\t" "11:\n\t" "s_memtime s[64:65]\n\t" \
    "s_waitcnt lgkmcnt(0)\n\t" "s_sub_u32 s61, %[tu], s64\n\t" "s_cmp_gt_i32 s61, 124\n\t" "s_cbranch_scc1 11b\n\t" "s_max_i32 s61, s61, 0\n\t" "s_and_b32 s61, s61, 124\n\t" \
    "s_sub_u32 s61, 12f-11b, s61\n\t" "s_add_u32 s62, s62, s61\n\t" "s_addc_u32 s63, s63, 0\n\t" "s_setpc_b64 s[62:63]\n\t" ".rept 31\n\t" "s_nop 0\n\t" ".endr\n\t" "12:\n\t" \
    XT(90, 91) "buffer_load_dwordx4 v[144:147], v128, %[rs], %[ub] offen offset:0 sc1\n\t" "buffer_load_dwordx4 v[148:151], v128, %[rs], %[ub] offen offset:1024 sc1\n\t" \
    "buffer_load_dwordx4 v[152:155], v129, %[rs], %[ub] offen offset:0 sc1\n\t" "buffer_load_dwordx4 v[156:159], v129, %[rs], %[ub] offen offset:1024 sc1\n\t" \
    "s_cmp_lg_u32 %[late], 0\n\t" "s_cbranch_scc1 14f\n\t" "s_getpc_b64 s[62:63]\n\t" "13:\n\t" "s_memtime s[64:65]\n\t" "s_waitcnt lgkmcnt(0)\n\t" \
    "s_sub_u32 s61, %[tg], s64\n\t" "s_cmp_gt_i32 s61, 124\n\t" "s_cbranch_scc1 13b\n\t" "s_max_i32 s61, s61, 0\n\t" "s_and_b32 s61, s61, 124\n\t" \
    "s_sub_u32 s61, 14f-13b, s61\n\t" "s_add_u32 s62, s62, s61\n\t" "s_addc_u32 s63, s63, 0\n\t" "s_setpc_b64 s[62:63]\n\t" ".rept 31\n\t" "s_nop 0\n\t" ".endr\n\t" "14:\n\t" \
    XT(92, 93) "20:\n\t" "buffer_load_dwordx4 v[160:163], v130, %[rs], %[ub] offen offset:0 sc1\n\t" \
    "buffer_load_dwordx4 v[164:167], v130, %[rs], %[ub] offen offset:1024 sc1\n\t" "buffer_load_dwordx4 v[192:195], %[x0], %[rs], %[pb] offen offset:0 sc1\n\t" \
    "buffer_load_dwordx4 v[196:199], %[x0], %[rs], %[pb] offen offset:1024 sc1\n\t" "buffer_load_dwordx4 v[200:203], %[x0], %[rs], %[pb] offen offset:2048 sc1\n\t" \
    "buffer_load_dwordx4 v[204:207], %[x0], %[rs], %[pb] offen offset:3072 sc1\n\t" "buffer_load_dwordx2 v[176:177], v132, %[rs], %[pb] offen offset:0 sc1\n\t" \
    "buffer_load_dwordx2 v[178:179], v132, %[rs], %[pb] offen offset:128 sc1\n\t" "v_mov_b64 v[120:121], 0\n\t" "v_mov_b64 v[122:123], 0\n\t" \
    "buffer_load_dwordx4 v[208:211], %[x1], %[rs], %[pb] offen offset:0 sc1\n\t" "buffer_load_dwordx4 v[212:215], %[x1], %[rs], %[pb] offen offset:1024 sc1\n\t" \
    "buffer_load_dwordx4 v[216:219], %[x1], %[rs], %[pb] offen offset:2048 sc1\n\t" "buffer_load_dwordx4 v[220:223], %[x1], %[rs], %[pb] offen offset:3072 sc1\n\t" \
    "buffer_load_dwordx2 v[180:181], v133, %[rs], %[pb] offen offset:0 sc1\n\t" "buffer_load_dwordx2 v[182:183], v133, %[rs], %[pb] offen offset:128 sc1\n\t" \
    "buffer_load_dwordx4 v[224:227], %[x2], %[rs], %[pb] offen offset:0 sc1\n\t" "buffer_load_dwordx4 v[228:231], %[x2], %[rs], %[pb] offen offset:1024 sc1\n\t" \
    "buffer_load_dwordx4 v[232:235], %[x2], %[rs], %[pb] offen offset:2048 sc1\n\t" "buffer_load_dwordx4 v[236:239], %[x2], %[rs], %[pb] offen offset:3072 sc1\n\t" \
    "buffer_load_dwordx2 v[184:185], v134, %[rs], %[pb] offen offset:0 sc1\n\t" "buffer_load_dwordx2 v[186:187], v134, %[rs], %[pb] offen offset:128 sc1\n\t" \
    "buffer_load_dwordx4 v[240:243], %[x3], %[rs], %[pb] offen offset:0 sc1\n\t" "buffer_load_dwordx4 v[244:247], %[x3], %[rs], %[pb] offen offset:1024 sc1\n\t" \
    "buffer_load_dwordx4 v[248:251], %[x3], %[rs], %[pb] offen offset:2048 sc1\n\t" "buffer_load_dwordx4 v[252:255], %[x3], %[rs], %[pb] offen offset:3072 sc1\n\t" \
    "buffer_load_dwordx4 v[168:171], v131, %[rs], %[ub] offen offset:0 sc1\n\t" "buffer_load_dwordx4 v[172:175], v131, %[rs], %[ub] offen offset:1024 sc1\n\t" \
    "buffer_load_dwordx2 v[188:189], v135, %[rs], %[pb] offen offset:0 sc1\n\t" "buffer_load_dwordx2 v[190:191], v135, %[rs], %[pb] offen offset:128 sc1\n\t" \
    "buffer_load_dword %[sv], %[so], %[rs], %[pb] offen sc1\n\t" "v_mov_b32 v127, -1\n\t" "s_waitcnt vmcnt(26)\n\t" XT(94, 95) \
    "v_smfmac_f32_16x16x64_bf16 v[120:123], v[192:195], a[0:7], %[ix]\n\t" "s_waitcnt vmcnt(25)\n\t" "v_smfmac_f32_16x16x64_bf16 v[120:123], v[196:199], a[8:15], %[ix]\n\t" \
    "s_waitcnt vmcnt(24)\n\t" "v_smfmac_f32_16x16x64_bf16 v[120:123], v[200:203], a[16:23], %[ix]\n\t" "s_waitcnt vmcnt(23)\n\t" \
    "v_smfmac_f32_16x16x64_bf16 v[120:123], v[204:207], a[24:31], %[ix]\n\t" "s_waitcnt vmcnt(21)\n\t" \
    "v_smfmac_f32_16x16x64_bf16 v[120:123], v[176:179], v[144:151], %[ix]\n\t" "s_waitcnt vmcnt(20)\n\t" \
    "v_smfmac_f32_16x16x64_bf16 v[120:123], v[208:211], a[32:39], %[ix]\n\t" "s_waitcnt vmcnt(19)\n\t" \
    "v_smfmac_f32_16x16x64_bf16 v[120:123], v[212:215], a[40:47], %[ix]\n\t" "s_waitcnt vmcnt(18)\n\t" \
    "v_smfmac_f32_16x16x64_bf16 v[120:123], v[216:219], a[48:55], %[ix]\n\t" "s_waitcnt vmcnt(17)\n\t" \
    "v_smfmac_f32_16x16x64_bf16 v[120:123], v[220:223], a[56:63], %[ix]\n\t" "s_waitcnt vmcnt(15)\n\t" \
    "v_smfmac_f32_16x16x64_bf16 v[120:123], v[180:183], v[152:159], %[ix]\n\t" "s_waitcnt vmcnt(14)\n\t" \
    "v_smfmac_f32_16x16x64_bf16 v[120:123], v[224:227], a[64:71], %[ix]\n\t" "s_waitcnt vmcnt(13)\n\t" \
    "v_smfmac_f32_16x16x64_bf16 v[120:123], v[228:231], a[72:79], %[ix]\n\t" "s_waitcnt vmcnt(12)\n\t" \
    "v_smfmac_f32_16x16x64_bf16 v[120:123], v[232:235], a[80:87], %[ix]\n\t" "s_waitcnt vmcnt(11)\n\t" \
    "v_smfmac_f32_16x16x64_bf16 v[120:123], v[236:239], a[88:95], %[ix]\n\t" "s_waitcnt vmcnt(9)\n\t" \
    "v_smfmac_f32_16x16x64_bf16 v[120:123], v[184:187], v[160:167], %[ix]\n\t" "s_waitcnt vmcnt(8)\n\t" \
    "v_smfmac_f32_16x16x64_bf16 v[120:123], v[240:243], a[96:103], %[ix]\n\t" "s_waitcnt vmcnt(7)\n\t" \
    "v_smfmac_f32_16x16x64_bf16 v[120:123], v[244:247], a[104:111], %[ix]\n\t" "s_waitcnt vmcnt(6)\n\t" \
    "v_smfmac_f32_16x16x64_bf16 v[120:123], v[248:251], a[112:119], %[ix]\n\t" "s_waitcnt vmcnt(5)\n\t" \
    "v_smfmac_f32_16x16x64_bf16 v[120:123], v[252:255], a[120:127], %[ix]\n\t" "s_waitcnt vmcnt(1)\n\t" \
    "v_smfmac_f32_16x16x64_bf16 v[120:123], v[188:191], v[168:175], %[ix]\n\t" XT(82, 83) "s_nop 7\n\t" "v_add_f32 v120, v120, v121\n\t" "v_add_f32 v122, v122, v123\n\t" \
    "v_cvt_pk_bf16_f32 v124, v120, v122\n\t" "v_cmp_u_f32 vcc, v120, v122\n\t" "s_cbranch_vccnz 30f\n\t" "21:\n\t" \
    "v_mov_b32_dpp v125, v124 quad_perm:[1,0,3,2] row_mask:0xf bank_mask:0xf\n\t" "v_perm_b32 v124, v125, v124, %[ps]\n\t" "v_min_u32 v124, -2, v124\n\t" \
    "buffer_store_dword v124, %[mw], %[rs], %[mb] offen sc1\n\t" XT(84, 85) "buffer_store_dword v127, %[mw], %[rs], %[mr] offen sc1\n\t" "s_memtime s[64:65]\n\t" \
    "s_barrier\n\t" XT(98, 99) "s_waitcnt lgkmcnt(0)\n\t" "s_and_b32 %[bad], s64, -2\n\t" "s_branch 40f\n\t" "30:\n\t" "s_cmp_lg_u32 %[late], 0\n\t" "s_cbranch_scc1 32f\n\t" \
    XCNT(100) "s_mov_b32 %[bad], 1\n\t" "s_waitcnt vmcnt(0)\n\t" "s_branch 40f\n\t" "32:\n\t" "v_max_u32 v126, v144, v145\n\t" "v_max3_u32 v126, v126, v146, v147\n\t" \
    "v_max3_u32 v126, v126, v148, v149\n\t" "v_max3_u32 v126, v126, v150, v151\n\t" "v_max3_u32 v126, v126, v152, v153\n\t" "v_max3_u32 v126, v126, v154, v155\n\t" \
    "v_max3_u32 v126, v126, v156, v157\n\t" "v_max3_u32 v126, v126, v158, v159\n\t" "v_max3_u32 v126, v126, v160, v161\n\t" "v_max3_u32 v126, v126, v162, v163\n\t" \
    "v_max3_u32 v126, v126, v164, v165\n\t" "v_max3_u32 v126, v126, v166, v167\n\t" "v_max3_u32 v126, v126, v168, v169\n\t" "v_max3_u32 v126, v126, v170, v171\n\t" \
    "v_max3_u32 v126, v126, v172, v173\n\t" "v_max3_u32 v126, v126, v174, v175\n\t" UCNT "v_max3_u32 v126, v126, v176, v177\n\t" "v_max3_u32 v126, v126, v178, v179\n\t" \
    "v_max3_u32 v126, v126, v180, v181\n\t" "v_max3_u32 v126, v126, v182, v183\n\t" "v_max3_u32 v126, v126, v184, v185\n\t" "v_max3_u32 v126, v126, v186, v187\n\t" \
    "v_max3_u32 v126, v126, v188, v189\n\t" "v_max3_u32 v126, v126, v190, v191\n\t" "v_max3_u32 v126, v126, v192, v193\n\t" "v_max3_u32 v126, v126, v194, v195\n\t" \
    "v_max3_u32 v126, v126, v196, v197\n\t" "v_max3_u32 v126, v126, v198, v199\n\t" "v_max3_u32 v126, v126, v200, v201\n\t" "v_max3_u32 v126, v126, v202, v203\n\t" \
    "v_max3_u32 v126, v126, v204, v205\n\t" "v_max3_u32 v126, v126, v206, v207\n\t" "v_max3_u32 v126, v126, v208, v209\n\t" "v_max3_u32 v126, v126, v210, v211\n\t" \
    "v_max3_u32 v126, v126, v212, v213\n\t" "v_max3_u32 v126, v126, v214, v215\n\t" "v_max3_u32 v126, v126, v216, v217\n\t" "v_max3_u32 v126, v126, v218, v219\n\t" \
    "v_max3_u32 v126, v126, v220, v221\n\t" "v_max3_u32 v126, v126, v222, v223\n\t" "v_max3_u32 v126, v126, v224, v225\n\t" "v_max3_u32 v126, v126, v226, v227\n\t" \
    "v_max3_u32 v126, v126, v228, v229\n\t" "v_max3_u32 v126, v126, v230, v231\n\t" "v_max3_u32 v126, v126, v232, v233\n\t" "v_max3_u32 v126, v126, v234, v235\n\t" \
    "v_max3_u32 v126, v126, v236, v237\n\t" "v_max3_u32 v126, v126, v238, v239\n\t" "v_max3_u32 v126, v126, v240, v241\n\t" "v_max3_u32 v126, v126, v242, v243\n\t" \
    "v_max3_u32 v126, v126, v244, v245\n\t" "v_max3_u32 v126, v126, v246, v247\n\t" "v_max3_u32 v126, v126, v248, v249\n\t" "v_max3_u32 v126, v126, v250, v251\n\t" \
    "v_max3_u32 v126, v126, v252, v253\n\t" "v_max3_u32 v126, v126, v254, v255\n\t" "v_cmp_eq_u32 vcc, -1, v126\n\t" "s_cbranch_vccz 21b\n\t" "s_cmp_eq_u32 %[bd], 0\n\t" \
    "s_cbranch_scc1 21b\n\t" XCNT(100) "s_mov_b32 %[bad], 1\n\t" "s_waitcnt vmcnt(0)\n\t" "40:\n\t"
#define G_LOOP \
    "v_xor_b32 v72, 0, %[xo]\n\t" "v_add_u32 v76, 0x10000, v72\n\t" "v_xor_b32 v88, 0, %[xs]\n\t" "v_add_u32 v88, 0, v88\n\t" "v_xor_b32 v73, 64, %[xo]\n\t" \
    "v_add_u32 v77, 0x10000, v73\n\t" "v_xor_b32 v89, 16, %[xs]\n\t" "v_add_u32 v89, 2048, v89\n\t" "v_xor_b32 v74, 128, %[xo]\n\t" "v_add_u32 v78, 0x10000, v74\n\t" \
    "v_xor_b32 v90, 32, %[xs]\n\t" "v_add_u32 v90, 4096, v90\n\t" "v_xor_b32 v75, 192, %[xo]\n\t" "v_add_u32 v79, 0x10000, v75\n\t" "v_xor_b32 v91, 48, %[xs]\n\t" \
    "v_add_u32 v91, 6144, v91\n\t" "v_xor_b32 v92, 64, %[xs]\n\t" "v_add_u32 v92, 8192, v92\n\t" "v_xor_b32 v93, 80, %[xs]\n\t" "v_add_u32 v93, 10240, v93\n\t" \
    "v_xor_b32 v94, 96, %[xs]\n\t" "v_add_u32 v94, 12288, v94\n\t" "v_xor_b32 v95, 112, %[xs]\n\t" "v_add_u32 v95, 14336, v95\n\t" "v_mov_b64 v[80:81], -1\n\t" \
    "v_mov_b64 v[82:83], -1\n\t" "v_mov_b32 v96, 2.0\n\t" "s_mov_b32 s60, 0\n\t" "s_mov_b32 s62, 0\n\t" "s_waitcnt vmcnt(0)\n\t" "s_barrier\n\t" \
    "ds_read_b128 v[100:103], v72 offset:0\n\t" "ds_read_b128 v[104:107], v72 offset:32768\n\t" "ds_read_b128 v[108:111], v73 offset:0\n\t" \
    "ds_read_b128 v[112:115], v73 offset:32768\n\t" "ds_read_b128 v[116:119], v74 offset:0\n\t" "10:\n\t" XT(80, 81) "s_waitcnt lgkmcnt(4)\n\t" \
    "v_mfma_f32_16x16x32_bf16 v[32:35], a[0:3], v[100:103], 0\n\t" "ds_read_b128 v[120:123], v74 offset:32768\n\t" "s_add_u32 s61, s60, 1\n\t" "s_and_b32 s61, s61, 63\n\t" \
    "v_mfma_f32_16x16x32_bf16 v[36:39], a[4:7], v[100:103], 0\n\t" "s_lshl_b32 s61, s61, 16\n\t" "s_add_u32 s63, %[lw], 65536\n\t" "s_waitcnt lgkmcnt(4)\n\t" \
    "v_mfma_f32_16x16x32_bf16 v[40:43], a[0:3], v[104:107], 0\n\t" "ds_read_b128 v[124:127], v75 offset:0\n\t" "v_mfma_f32_16x16x32_bf16 v[44:47], a[4:7], v[104:107], 0\n\t" \
    "s_waitcnt lgkmcnt(4)\n\t" "v_mfma_f32_16x16x32_bf16 v[32:35], a[8:11], v[108:111], v[32:35]\n\t" "ds_read_b128 v[128:131], v75 offset:32768\n\t" \
    "v_cvt_pk_bf16_f32 v64, v48, v49\n\t" "v_mfma_f32_16x16x32_bf16 v[36:39], a[12:15], v[108:111], v[36:39]\n\t" "v_cvt_pk_bf16_f32 v65, v50, v51\n\t" \
    "v_cvt_pk_bf16_f32 v66, v52, v53\n\t" "s_waitcnt lgkmcnt(4)\n\t" "v_mfma_f32_16x16x32_bf16 v[40:43], a[8:11], v[112:115], v[40:43]\n\t" \
    "ds_read_b128 v[100:103], v72 offset:256\n\t" "v_cvt_pk_bf16_f32 v67, v54, v55\n\t" "v_mfma_f32_16x16x32_bf16 v[44:47], a[12:15], v[112:115], v[44:47]\n\t" \
    "v_cvt_pk_bf16_f32 v68, v56, v57\n\t" "v_cvt_pk_bf16_f32 v69, v58, v59\n\t" "s_waitcnt lgkmcnt(4)\n\t" \
    "v_mfma_f32_16x16x32_bf16 v[32:35], a[16:19], v[116:119], v[32:35]\n\t" "ds_read_b128 v[104:107], v72 offset:33024\n\t" "v_cvt_pk_bf16_f32 v70, v60, v61\n\t" \
    "v_mfma_f32_16x16x32_bf16 v[36:39], a[20:23], v[116:119], v[36:39]\n\t" "v_cvt_pk_bf16_f32 v71, v62, v63\n\t" \
    "buffer_store_dwordx4 v[64:67], v96, %[rs], s62 offen offset:0 sc1\n\t" "s_waitcnt lgkmcnt(4)\n\t" "v_mfma_f32_16x16x32_bf16 v[40:43], a[16:19], v[120:123], v[40:43]\n\t" \
    "ds_read_b128 v[108:111], v73 offset:256\n\t" "v_mfma_f32_16x16x32_bf16 v[44:47], a[20:23], v[120:123], v[44:47]\n\t" "s_waitcnt lgkmcnt(4)\n\t" \
    "v_mfma_f32_16x16x32_bf16 v[32:35], a[24:27], v[124:127], v[32:35]\n\t" "ds_read_b128 v[112:115], v73 offset:33024\n\t" \
    "v_mfma_f32_16x16x32_bf16 v[36:39], a[28:31], v[124:127], v[36:39]\n\t" "buffer_store_dwordx4 v[68:71], v96, %[rs], s62 offen offset:256 sc1\n\t" \
    "s_waitcnt lgkmcnt(4)\n\t" "v_mfma_f32_16x16x32_bf16 v[40:43], a[24:27], v[128:131], v[40:43]\n\t" "ds_read_b128 v[116:119], v74 offset:256\n\t" "s_mov_b32 m0, s63\n\t" \
    "v_mfma_f32_16x16x32_bf16 v[44:47], a[28:31], v[128:131], v[44:47]\n\t" "s_add_u32 s63, s63, 0x800\n\t" "buffer_load_dwordx4 v88, %[rx], s61 offen lds\n\t" \
    "s_waitcnt lgkmcnt(4)\n\t" "v_mfma_f32_16x16x32_bf16 v[32:35], a[32:35], v[100:103], v[32:35]\n\t" "ds_read_b128 v[120:123], v74 offset:33024\n\t" \
    "v_mfma_f32_16x16x32_bf16 v[36:39], a[36:39], v[100:103], v[36:39]\n\t" "buffer_load_dwordx4 v88, %[rx], s61 offen offset:1024 lds\n\t" "s_waitcnt lgkmcnt(4)\n\t" \
    "v_mfma_f32_16x16x32_bf16 v[40:43], a[32:35], v[104:107], v[40:43]\n\t" "ds_read_b128 v[124:127], v75 offset:256\n\t" "s_mov_b32 m0, s63\n\t" \
    "v_mfma_f32_16x16x32_bf16 v[44:47], a[36:39], v[104:107], v[44:47]\n\t" "s_add_u32 s63, s63, 0x800\n\t" "buffer_load_dwordx4 v89, %[rx], s61 offen lds\n\t" \
    "s_waitcnt lgkmcnt(4)\n\t" "v_mfma_f32_16x16x32_bf16 v[32:35], a[40:43], v[108:111], v[32:35]\n\t" "ds_read_b128 v[128:131], v75 offset:33024\n\t" \
    "v_mfma_f32_16x16x32_bf16 v[36:39], a[44:47], v[108:111], v[36:39]\n\t" "buffer_load_dwordx4 v89, %[rx], s61 offen offset:1024 lds\n\t" "s_waitcnt lgkmcnt(4)\n\t" \
    "v_mfma_f32_16x16x32_bf16 v[40:43], a[40:43], v[112:115], v[40:43]\n\t" "ds_read_b128 v[100:103], v72 offset:512\n\t" "s_mov_b32 m0, s63\n\t" \
    "v_mfma_f32_16x16x32_bf16 v[44:47], a[44:47], v[112:115], v[44:47]\n\t" "s_add_u32 s63, s63, 0x800\n\t" "buffer_load_dwordx4 v90, %[rx], s61 offen lds\n\t" \
    "s_waitcnt lgkmcnt(4)\n\t" "v_mfma_f32_16x16x32_bf16 v[32:35], a[48:51], v[116:119], v[32:35]\n\t" "ds_read_b128 v[104:107], v72 offset:33280\n\t" \
    "v_mfma_f32_16x16x32_bf16 v[36:39], a[52:55], v[116:119], v[36:39]\n\t" "buffer_load_dwordx4 v90, %[rx], s61 offen offset:1024 lds\n\t" "s_waitcnt lgkmcnt(4)\n\t" \
    "v_mfma_f32_16x16x32_bf16 v[40:43], a[48:51], v[120:123], v[40:43]\n\t" "ds_read_b128 v[108:111], v73 offset:512\n\t" "s_mov_b32 m0, s63\n\t" \
    "v_mfma_f32_16x16x32_bf16 v[44:47], a[52:55], v[120:123], v[44:47]\n\t" "s_add_u32 s63, s63, 0x800\n\t" "buffer_load_dwordx4 v91, %[rx], s61 offen lds\n\t" \
    "s_waitcnt lgkmcnt(4)\n\t" "v_mfma_f32_16x16x32_bf16 v[32:35], a[56:59], v[124:127], v[32:35]\n\t" "ds_read_b128 v[112:115], v73 offset:33280\n\t" \
    "v_mfma_f32_16x16x32_bf16 v[36:39], a[60:63], v[124:127], v[36:39]\n\t" "buffer_load_dwordx4 v91, %[rx], s61 offen offset:1024 lds\n\t" "s_waitcnt lgkmcnt(4)\n\t" \
    "v_mfma_f32_16x16x32_bf16 v[40:43], a[56:59], v[128:131], v[40:43]\n\t" "ds_read_b128 v[116:119], v74 offset:512\n\t" "s_mov_b32 m0, s63\n\t" \
    "v_mfma_f32_16x16x32_bf16 v[44:47], a[60:63], v[128:131], v[44:47]\n\t" "s_add_u32 s63, s63, 0x800\n\t" "buffer_load_dwordx4 v92, %[rx], s61 offen lds\n\t" \
    "s_waitcnt lgkmcnt(4)\n\t" "v_mfma_f32_16x16x32_bf16 v[32:35], a[64:67], v[100:103], v[32:35]\n\t" "ds_read_b128 v[120:123], v74 offset:33280\n\t" \
    "v_mfma_f32_16x16x32_bf16 v[36:39], a[68:71], v[100:103], v[36:39]\n\t" "buffer_load_dwordx4 v92, %[rx], s61 offen offset:1024 lds\n\t" "s_waitcnt lgkmcnt(4)\n\t" \
    "v_mfma_f32_16x16x32_bf16 v[40:43], a[64:67], v[104:107], v[40:43]\n\t" "ds_read_b128 v[124:127], v75 offset:512\n\t" "s_mov_b32 m0, s63\n\t" \
    "v_mfma_f32_16x16x32_bf16 v[44:47], a[68:71], v[104:107], v[44:47]\n\t" "s_add_u32 s63, s63, 0x800\n\t" "buffer_load_dwordx4 v93, %[rx], s61 offen lds\n\t" \
    "s_waitcnt lgkmcnt(4)\n\t" "v_mfma_f32_16x16x32_bf16 v[32:35], a[72:75], v[108:111], v[32:35]\n\t" "ds_read_b128 v[128:131], v75 offset:33280\n\t" \
    "v_mfma_f32_16x16x32_bf16 v[36:39], a[76:79], v[108:111], v[36:39]\n\t" "buffer_load_dwordx4 v93, %[rx], s61 offen offset:1024 lds\n\t" "s_waitcnt lgkmcnt(4)\n\t" \
    "v_mfma_f32_16x16x32_bf16 v[40:43], a[72:75], v[112:115], v[40:43]\n\t" "ds_read_b128 v[100:103], v72 offset:768\n\t" "s_mov_b32 m0, s63\n\t" \
    "v_mfma_f32_16x16x32_bf16 v[44:47], a[76:79], v[112:115], v[44:47]\n\t" "s_add_u32 s63, s63, 0x800\n\t" "buffer_load_dwordx4 v94, %[rx], s61 offen lds\n\t" \
    "s_waitcnt lgkmcnt(4)\n\t" "v_mfma_f32_16x16x32_bf16 v[32:35], a[80:83], v[116:119], v[32:35]\n\t" "ds_read_b128 v[104:107], v72 offset:33536\n\t" \
    "v_mfma_f32_16x16x32_bf16 v[36:39], a[84:87], v[116:119], v[36:39]\n\t" "buffer_load_dwordx4 v94, %[rx], s61 offen offset:1024 lds\n\t" "s_waitcnt lgkmcnt(4)\n\t" \
    "v_mfma_f32_16x16x32_bf16 v[40:43], a[80:83], v[120:123], v[40:43]\n\t" "ds_read_b128 v[108:111], v73 offset:768\n\t" "s_mov_b32 m0, s63\n\t" \
    "v_mfma_f32_16x16x32_bf16 v[44:47], a[84:87], v[120:123], v[44:47]\n\t" "s_add_u32 s63, s63, 0x800\n\t" "buffer_load_dwordx4 v95, %[rx], s61 offen lds\n\t" \
    "s_waitcnt lgkmcnt(4)\n\t" "v_mfma_f32_16x16x32_bf16 v[32:35], a[88:91], v[124:127], v[32:35]\n\t" "ds_read_b128 v[112:115], v73 offset:33536\n\t" \
    "v_mfma_f32_16x16x32_bf16 v[36:39], a[92:95], v[124:127], v[36:39]\n\t" "buffer_load_dwordx4 v95, %[rx], s61 offen offset:1024 lds\n\t" "s_waitcnt lgkmcnt(4)\n\t" \
    "v_mfma_f32_16x16x32_bf16 v[40:43], a[88:91], v[128:131], v[40:43]\n\t" "ds_read_b128 v[116:119], v74 offset:768\n\t" \
    "v_mfma_f32_16x16x32_bf16 v[44:47], a[92:95], v[128:131], v[44:47]\n\t" "s_waitcnt lgkmcnt(4)\n\t" "v_mfma_f32_16x16x32_bf16 v[32:35], a[96:99], v[100:103], v[32:35]\n\t" \
    "ds_read_b128 v[120:123], v74 offset:33536\n\t" "s_lshr_b32 s64, s60, 2\n\t" "s_xor_b32 s64, s64, %[xp]\n\t" \
    "v_mfma_f32_16x16x32_bf16 v[36:39], a[100:103], v[100:103], v[36:39]\n\t" "s_and_b32 s64, s64, 1\n\t" "s_lshl_b32 s64, s64, 13\n\t" "s_waitcnt lgkmcnt(4)\n\t" \
    "v_mfma_f32_16x16x32_bf16 v[40:43], a[96:99], v[104:107], v[40:43]\n\t" "ds_read_b128 v[124:127], v75 offset:768\n\t" "s_lshl_b32 s62, s60, 19\n\t" \
    "v_mfma_f32_16x16x32_bf16 v[44:47], a[100:103], v[104:107], v[44:47]\n\t" "s_add_u32 s62, s62, s64\n\t" "s_add_u32 s62, s62, %[ob]\n\t" "s_waitcnt lgkmcnt(4)\n\t" \
    "v_mfma_f32_16x16x32_bf16 v[32:35], a[104:107], v[108:111], v[32:35]\n\t" "ds_read_b128 v[128:131], v75 offset:33536\n\t" \
    "v_mfma_f32_16x16x32_bf16 v[36:39], a[108:111], v[108:111], v[36:39]\n\t" "s_waitcnt lgkmcnt(4)\n\t" \
    "v_mfma_f32_16x16x32_bf16 v[40:43], a[104:107], v[112:115], v[40:43]\n\t" "ds_read_b128 v[100:103], v72 offset:1024\n\t" "s_add_u32 s64, s60, 1\n\t" \
    "v_mfma_f32_16x16x32_bf16 v[44:47], a[108:111], v[112:115], v[44:47]\n\t" "s_lshl_b32 s65, s64, 20\n\t" "s_lshr_b32 s64, s64, 1\n\t" "s_waitcnt lgkmcnt(4)\n\t" \
    "v_mfma_f32_16x16x32_bf16 v[32:35], a[112:115], v[116:119], v[32:35]\n\t" "ds_read_b128 v[104:107], v72 offset:33792\n\t" "s_xor_b32 s64, s64, %[xp]\n\t" \
    "v_mfma_f32_16x16x32_bf16 v[36:39], a[116:119], v[116:119], v[36:39]\n\t" "s_and_b32 s64, s64, 1\n\t" "s_lshl_b32 s64, s64, 13\n\t" "s_waitcnt lgkmcnt(4)\n\t" \
    "v_mfma_f32_16x16x32_bf16 v[40:43], a[112:115], v[120:123], v[40:43]\n\t" "ds_read_b128 v[108:111], v73 offset:1024\n\t" \
    "v_mfma_f32_16x16x32_bf16 v[44:47], a[116:119], v[120:123], v[44:47]\n\t" "s_add_u32 s66, s65, s64\n\t" "s_waitcnt lgkmcnt(4)\n\t" \
    "v_mfma_f32_16x16x32_bf16 v[32:35], a[120:123], v[124:127], v[32:35]\n\t" "ds_read_b128 v[112:115], v73 offset:33792\n\t" \
    "v_mfma_f32_16x16x32_bf16 v[36:39], a[124:127], v[124:127], v[36:39]\n\t" "s_waitcnt lgkmcnt(4)\n\t" \
    "v_mfma_f32_16x16x32_bf16 v[40:43], a[120:123], v[128:131], v[40:43]\n\t" "ds_read_b128 v[116:119], v74 offset:1024\n\t" \
    "v_mfma_f32_16x16x32_bf16 v[44:47], a[124:127], v[128:131], v[44:47]\n\t" "s_waitcnt lgkmcnt(4)\n\t" \
    "v_mfma_f32_16x16x32_bf16 v[32:35], a[128:131], v[100:103], v[32:35]\n\t" "ds_read_b128 v[120:123], v74 offset:33792\n\t" \
    "v_mfma_f32_16x16x32_bf16 v[36:39], a[132:135], v[100:103], v[36:39]\n\t" "s_waitcnt lgkmcnt(4)\n\t" \
    "v_mfma_f32_16x16x32_bf16 v[40:43], a[128:131], v[104:107], v[40:43]\n\t" "ds_read_b128 v[124:127], v75 offset:1024\n\t" \
    "v_mfma_f32_16x16x32_bf16 v[44:47], a[132:135], v[104:107], v[44:47]\n\t" "s_waitcnt lgkmcnt(4)\n\t" \
    "v_mfma_f32_16x16x32_bf16 v[32:35], a[136:139], v[108:111], v[32:35]\n\t" "ds_read_b128 v[128:131], v75 offset:33792\n\t" "s_waitcnt vmcnt(16)\n\t" "s_barrier\n\t" \
    "v_mfma_f32_16x16x32_bf16 v[36:39], a[140:143], v[108:111], v[36:39]\n\t" "v_add_u32 v98, s60, %[at]\n\t" "buffer_store_dword v98, %[fo], %[rc], %[fl] offen sc1\n\t" \
    "s_waitcnt lgkmcnt(4)\n\t" "v_mfma_f32_16x16x32_bf16 v[40:43], a[136:139], v[112:115], v[40:43]\n\t" "ds_read_b128 v[100:103], v72 offset:1280\n\t" \
    "v_mfma_f32_16x16x32_bf16 v[44:47], a[140:143], v[112:115], v[44:47]\n\t" "s_waitcnt lgkmcnt(4)\n\t" \
    "v_mfma_f32_16x16x32_bf16 v[32:35], a[144:147], v[116:119], v[32:35]\n\t" "ds_read_b128 v[104:107], v72 offset:34048\n\t" \
    "v_mfma_f32_16x16x32_bf16 v[36:39], a[148:151], v[116:119], v[36:39]\n\t" "s_waitcnt lgkmcnt(4)\n\t" \
    "v_mfma_f32_16x16x32_bf16 v[40:43], a[144:147], v[120:123], v[40:43]\n\t" "ds_read_b128 v[108:111], v73 offset:1280\n\t" \
    "v_mfma_f32_16x16x32_bf16 v[44:47], a[148:151], v[120:123], v[44:47]\n\t" "s_waitcnt lgkmcnt(4)\n\t" \
    "v_mfma_f32_16x16x32_bf16 v[32:35], a[152:155], v[124:127], v[32:35]\n\t" "ds_read_b128 v[112:115], v73 offset:34048\n\t" \
    "v_mfma_f32_16x16x32_bf16 v[36:39], a[156:159], v[124:127], v[36:39]\n\t" "buffer_store_dwordx4 v[80:83], %[uo], %[rs], s66 offen offset:0 sc1\n\t" \
    "s_waitcnt lgkmcnt(4)\n\t" "v_mfma_f32_16x16x32_bf16 v[40:43], a[152:155], v[128:131], v[40:43]\n\t" "ds_read_b128 v[116:119], v74 offset:1280\n\t" \
    "v_mfma_f32_16x16x32_bf16 v[44:47], a[156:159], v[128:131], v[44:47]\n\t" "s_waitcnt lgkmcnt(4)\n\t" \
    "v_mfma_f32_16x16x32_bf16 v[32:35], a[160:163], v[100:103], v[32:35]\n\t" "ds_read_b128 v[120:123], v74 offset:34048\n\t" \
    "v_mfma_f32_16x16x32_bf16 v[36:39], a[164:167], v[100:103], v[36:39]\n\t" "s_waitcnt lgkmcnt(4)\n\t" \
    "v_mfma_f32_16x16x32_bf16 v[40:43], a[160:163], v[104:107], v[40:43]\n\t" "ds_read_b128 v[124:127], v75 offset:1280\n\t" \
    "v_mfma_f32_16x16x32_bf16 v[44:47], a[164:167], v[104:107], v[44:47]\n\t" "s_waitcnt lgkmcnt(4)\n\t" \
    "v_mfma_f32_16x16x32_bf16 v[32:35], a[168:171], v[108:111], v[32:35]\n\t" "ds_read_b128 v[128:131], v75 offset:34048\n\t" \
    "v_mfma_f32_16x16x32_bf16 v[36:39], a[172:175], v[108:111], v[36:39]\n\t" "s_waitcnt lgkmcnt(4)\n\t" \
    "v_mfma_f32_16x16x32_bf16 v[40:43], a[168:171], v[112:115], v[40:43]\n\t" "ds_read_b128 v[100:103], v72 offset:1536\n\t" \
    "v_mfma_f32_16x16x32_bf16 v[44:47], a[172:175], v[112:115], v[44:47]\n\t" "buffer_store_dwordx4 v[80:83], %[uo], %[rs], s66 offen offset:1024 sc1\n\t" \
    "s_waitcnt lgkmcnt(4)\n\t" "v_mfma_f32_16x16x32_bf16 v[32:35], a[176:179], v[116:119], v[32:35]\n\t" "ds_read_b128 v[104:107], v72 offset:34304\n\t" \
    "v_mfma_f32_16x16x32_bf16 v[36:39], a[180:183], v[116:119], v[36:39]\n\t" "s_waitcnt lgkmcnt(4)\n\t" \
    "v_mfma_f32_16x16x32_bf16 v[40:43], a[176:179], v[120:123], v[40:43]\n\t" "ds_read_b128 v[108:111], v73 offset:1536\n\t" \
    "v_mfma_f32_16x16x32_bf16 v[44:47], a[180:183], v[120:123], v[44:47]\n\t" "s_waitcnt lgkmcnt(4)\n\t" \
    "v_mfma_f32_16x16x32_bf16 v[32:35], a[184:187], v[124:127], v[32:35]\n\t" "ds_read_b128 v[112:115], v73 offset:34304\n\t" \
    "v_mfma_f32_16x16x32_bf16 v[36:39], a[188:191], v[124:127], v[36:39]\n\t" "s_waitcnt lgkmcnt(4)\n\t" \
    "v_mfma_f32_16x16x32_bf16 v[40:43], a[184:187], v[128:131], v[40:43]\n\t" "ds_read_b128 v[116:119], v74 offset:1536\n\t" \
    "v_mfma_f32_16x16x32_bf16 v[44:47], a[188:191], v[128:131], v[44:47]\n\t" "s_waitcnt lgkmcnt(4)\n\t" \
    "v_mfma_f32_16x16x32_bf16 v[32:35], a[192:195], v[100:103], v[32:35]\n\t" "ds_read_b128 v[120:123], v74 offset:34304\n\t" \
    "v_mfma_f32_16x16x32_bf16 v[36:39], a[196:199], v[100:103], v[36:39]\n\t" "buffer_store_dwordx4 v[80:83], %[uo], %[rs], s66 offen offset:2048 sc1\n\t" \
    "s_waitcnt lgkmcnt(4)\n\t" "v_mfma_f32_16x16x32_bf16 v[40:43], a[192:195], v[104:107], v[40:43]\n\t" "ds_read_b128 v[124:127], v75 offset:1536\n\t" \
    "v_mfma_f32_16x16x32_bf16 v[44:47], a[196:199], v[104:107], v[44:47]\n\t" "s_waitcnt lgkmcnt(4)\n\t" \
    "v_mfma_f32_16x16x32_bf16 v[32:35], a[200:203], v[108:111], v[32:35]\n\t" "ds_read_b128 v[128:131], v75 offset:34304\n\t" \
    "v_mfma_f32_16x16x32_bf16 v[36:39], a[204:207], v[108:111], v[36:39]\n\t" "s_waitcnt lgkmcnt(4)\n\t" \
    "v_mfma_f32_16x16x32_bf16 v[40:43], a[200:203], v[112:115], v[40:43]\n\t" "ds_read_b128 v[100:103], v72 offset:1792\n\t" \
    "v_mfma_f32_16x16x32_bf16 v[44:47], a[204:207], v[112:115], v[44:47]\n\t" "s_waitcnt lgkmcnt(4)\n\t" \
    "v_mfma_f32_16x16x32_bf16 v[32:35], a[208:211], v[116:119], v[32:35]\n\t" "ds_read_b128 v[104:107], v72 offset:34560\n\t" \
    "v_mfma_f32_16x16x32_bf16 v[36:39], a[212:215], v[116:119], v[36:39]\n\t" "s_waitcnt lgkmcnt(4)\n\t" \
    "v_mfma_f32_16x16x32_bf16 v[40:43], a[208:211], v[120:123], v[40:43]\n\t" "ds_read_b128 v[108:111], v73 offset:1792\n\t" \
    "v_mfma_f32_16x16x32_bf16 v[44:47], a[212:215], v[120:123], v[44:47]\n\t" "buffer_store_dwordx4 v[80:83], %[uo], %[rs], s66 offen offset:3072 sc1\n\t" \
    "s_waitcnt lgkmcnt(4)\n\t" "v_mfma_f32_16x16x32_bf16 v[32:35], a[216:219], v[124:127], v[32:35]\n\t" "ds_read_b128 v[112:115], v73 offset:34560\n\t" \
    "v_mfma_f32_16x16x32_bf16 v[36:39], a[220:223], v[124:127], v[36:39]\n\t" "s_waitcnt lgkmcnt(4)\n\t" \
    "v_mfma_f32_16x16x32_bf16 v[40:43], a[216:219], v[128:131], v[40:43]\n\t" "ds_read_b128 v[116:119], v74 offset:1792\n\t" \
    "v_mfma_f32_16x16x32_bf16 v[44:47], a[220:223], v[128:131], v[44:47]\n\t" "s_waitcnt lgkmcnt(4)\n\t" \
    "v_mfma_f32_16x16x32_bf16 v[32:35], a[224:227], v[100:103], v[32:35]\n\t" "ds_read_b128 v[120:123], v74 offset:34560\n\t" \
    "v_mfma_f32_16x16x32_bf16 v[36:39], a[228:231], v[100:103], v[36:39]\n\t" "s_waitcnt lgkmcnt(4)\n\t" \
    "v_mfma_f32_16x16x32_bf16 v[40:43], a[224:227], v[104:107], v[40:43]\n\t" "ds_read_b128 v[124:127], v75 offset:1792\n\t" \
    "v_mfma_f32_16x16x32_bf16 v[44:47], a[228:231], v[104:107], v[44:47]\n\t" "s_waitcnt lgkmcnt(4)\n\t" \
    "v_mfma_f32_16x16x32_bf16 v[32:35], a[232:235], v[108:111], v[32:35]\n\t" "ds_read_b128 v[128:131], v75 offset:34560\n\t" \
    "v_mfma_f32_16x16x32_bf16 v[36:39], a[236:239], v[108:111], v[36:39]\n\t" "s_waitcnt lgkmcnt(4)\n\t" \
    "v_mfma_f32_16x16x32_bf16 v[40:43], a[232:235], v[112:115], v[40:43]\n\t" "v_mfma_f32_16x16x32_bf16 v[44:47], a[236:239], v[112:115], v[44:47]\n\t" \
    "s_waitcnt lgkmcnt(3)\n\t" "v_mfma_f32_16x16x32_bf16 v[32:35], a[240:243], v[116:119], v[32:35]\n\t" \
    "v_mfma_f32_16x16x32_bf16 v[36:39], a[244:247], v[116:119], v[36:39]\n\t" "s_waitcnt vmcnt(5) lgkmcnt(0)\n\t" "s_barrier\n\t" XT(82, 83) \
    "v_mfma_f32_16x16x32_bf16 v[40:43], a[240:243], v[120:123], v[40:43]\n\t" "ds_read_b128 v[100:103], v76 offset:0\n\t" \
    "v_mfma_f32_16x16x32_bf16 v[44:47], a[244:247], v[120:123], v[44:47]\n\t" "v_mfma_f32_16x16x32_bf16 v[32:35], a[248:251], v[124:127], v[32:35]\n\t" \
    "ds_read_b128 v[104:107], v76 offset:32768\n\t" "v_mfma_f32_16x16x32_bf16 v[36:39], a[252:255], v[124:127], v[36:39]\n\t" "ds_read_b128 v[108:111], v77 offset:0\n\t" \
    "v_mfma_f32_16x16x32_bf16 v[40:43], a[248:251], v[128:131], v[40:43]\n\t" "ds_read_b128 v[112:115], v77 offset:32768\n\t" "ds_read_b128 v[116:119], v78 offset:0\n\t" \
    "v_mfma_f32_16x16x32_bf16 v[44:47], a[252:255], v[128:131], v[44:47]\n\t" "s_add_u32 s60, s60, 1\n\t" GSUM "s_cmp_eq_u32 s60, 1\n\t" "s_cbranch_scc1 20f\n\t" "11:\n\t" \
    XT(80, 81) "s_waitcnt lgkmcnt(4)\n\t" "v_mfma_f32_16x16x32_bf16 v[48:51], a[0:3], v[100:103], 0\n\t" "ds_read_b128 v[120:123], v78 offset:32768\n\t" \
    "s_add_u32 s61, s60, 1\n\t" "s_and_b32 s61, s61, 63\n\t" "v_mfma_f32_16x16x32_bf16 v[52:55], a[4:7], v[100:103], 0\n\t" "s_lshl_b32 s61, s61, 16\n\t" \
    "s_add_u32 s63, %[lw], 0\n\t" "s_waitcnt lgkmcnt(4)\n\t" "v_mfma_f32_16x16x32_bf16 v[56:59], a[0:3], v[104:107], 0\n\t" "ds_read_b128 v[124:127], v79 offset:0\n\t" \
    "v_mfma_f32_16x16x32_bf16 v[60:63], a[4:7], v[104:107], 0\n\t" "s_waitcnt lgkmcnt(4)\n\t" "v_mfma_f32_16x16x32_bf16 v[48:51], a[8:11], v[108:111], v[48:51]\n\t" \
    "ds_read_b128 v[128:131], v79 offset:32768\n\t" "v_cvt_pk_bf16_f32 v64, v32, v33\n\t" "v_mfma_f32_16x16x32_bf16 v[52:55], a[12:15], v[108:111], v[52:55]\n\t" \
    "v_cvt_pk_bf16_f32 v65, v34, v35\n\t" "v_cvt_pk_bf16_f32 v66, v36, v37\n\t" "s_waitcnt lgkmcnt(4)\n\t" \
    "v_mfma_f32_16x16x32_bf16 v[56:59], a[8:11], v[112:115], v[56:59]\n\t" "ds_read_b128 v[100:103], v76 offset:256\n\t" "v_cvt_pk_bf16_f32 v67, v38, v39\n\t" \
    "v_mfma_f32_16x16x32_bf16 v[60:63], a[12:15], v[112:115], v[60:63]\n\t" "v_cvt_pk_bf16_f32 v68, v40, v41\n\t" "v_cvt_pk_bf16_f32 v69, v42, v43\n\t" \
    "s_waitcnt lgkmcnt(4)\n\t" "v_mfma_f32_16x16x32_bf16 v[48:51], a[16:19], v[116:119], v[48:51]\n\t" "ds_read_b128 v[104:107], v76 offset:33024\n\t" \
    "v_cvt_pk_bf16_f32 v70, v44, v45\n\t" "v_mfma_f32_16x16x32_bf16 v[52:55], a[20:23], v[116:119], v[52:55]\n\t" "v_cvt_pk_bf16_f32 v71, v46, v47\n\t" \
    "buffer_store_dwordx4 v[64:67], v96, %[rs], s62 offen offset:0 sc1\n\t" "s_waitcnt lgkmcnt(4)\n\t" "v_mfma_f32_16x16x32_bf16 v[56:59], a[16:19], v[120:123], v[56:59]\n\t" \
    "ds_read_b128 v[108:111], v77 offset:256\n\t" "v_mfma_f32_16x16x32_bf16 v[60:63], a[20:23], v[120:123], v[60:63]\n\t" "s_waitcnt lgkmcnt(4)\n\t" \
    "v_mfma_f32_16x16x32_bf16 v[48:51], a[24:27], v[124:127], v[48:51]\n\t" "ds_read_b128 v[112:115], v77 offset:33024\n\t" \
    "v_mfma_f32_16x16x32_bf16 v[52:55], a[28:31], v[124:127], v[52:55]\n\t" "buffer_store_dwordx4 v[68:71], v96, %[rs], s62 offen offset:256 sc1\n\t" \
    "s_waitcnt lgkmcnt(4)\n\t" "v_mfma_f32_16x16x32_bf16 v[56:59], a[24:27], v[128:131], v[56:59]\n\t" "ds_read_b128 v[116:119], v78 offset:256\n\t" "s_mov_b32 m0, s63\n\t" \
    "v_mfma_f32_16x16x32_bf16 v[60:63], a[28:31], v[128:131], v[60:63]\n\t" "s_add_u32 s63, s63, 0x800\n\t" "buffer_load_dwordx4 v88, %[rx], s61 offen lds\n\t" \
    "s_waitcnt lgkmcnt(4)\n\t" "v_mfma_f32_16x16x32_bf16 v[48:51], a[32:35], v[100:103], v[48:51]\n\t" "ds_read_b128 v[120:123], v78 offset:33024\n\t" \
    "v_mfma_f32_16x16x32_bf16 v[52:55], a[36:39], v[100:103], v[52:55]\n\t" "buffer_load_dwordx4 v88, %[rx], s61 offen offset:1024 lds\n\t" "s_waitcnt lgkmcnt(4)\n\t" \
    "v_mfma_f32_16x16x32_bf16 v[56:59], a[32:35], v[104:107], v[56:59]\n\t" "ds_read_b128 v[124:127], v79 offset:256\n\t" "s_mov_b32 m0, s63\n\t" \
    "v_mfma_f32_16x16x32_bf16 v[60:63], a[36:39], v[104:107], v[60:63]\n\t" "s_add_u32 s63, s63, 0x800\n\t" "buffer_load_dwordx4 v89, %[rx], s61 offen lds\n\t" \
    "s_waitcnt lgkmcnt(4)\n\t" "v_mfma_f32_16x16x32_bf16 v[48:51], a[40:43], v[108:111], v[48:51]\n\t" "ds_read_b128 v[128:131], v79 offset:33024\n\t" \
    "v_mfma_f32_16x16x32_bf16 v[52:55], a[44:47], v[108:111], v[52:55]\n\t" "buffer_load_dwordx4 v89, %[rx], s61 offen offset:1024 lds\n\t" "s_waitcnt lgkmcnt(4)\n\t" \
    "v_mfma_f32_16x16x32_bf16 v[56:59], a[40:43], v[112:115], v[56:59]\n\t" "ds_read_b128 v[100:103], v76 offset:512\n\t" "s_mov_b32 m0, s63\n\t" \
    "v_mfma_f32_16x16x32_bf16 v[60:63], a[44:47], v[112:115], v[60:63]\n\t" "s_add_u32 s63, s63, 0x800\n\t" "buffer_load_dwordx4 v90, %[rx], s61 offen lds\n\t" \
    "s_waitcnt lgkmcnt(4)\n\t" "v_mfma_f32_16x16x32_bf16 v[48:51], a[48:51], v[116:119], v[48:51]\n\t" "ds_read_b128 v[104:107], v76 offset:33280\n\t" \
    "v_mfma_f32_16x16x32_bf16 v[52:55], a[52:55], v[116:119], v[52:55]\n\t" "buffer_load_dwordx4 v90, %[rx], s61 offen offset:1024 lds\n\t" "s_waitcnt lgkmcnt(4)\n\t" \
    "v_mfma_f32_16x16x32_bf16 v[56:59], a[48:51], v[120:123], v[56:59]\n\t" "ds_read_b128 v[108:111], v77 offset:512\n\t" "s_mov_b32 m0, s63\n\t" \
    "v_mfma_f32_16x16x32_bf16 v[60:63], a[52:55], v[120:123], v[60:63]\n\t" "s_add_u32 s63, s63, 0x800\n\t" "buffer_load_dwordx4 v91, %[rx], s61 offen lds\n\t" \
    "s_waitcnt lgkmcnt(4)\n\t" "v_mfma_f32_16x16x32_bf16 v[48:51], a[56:59], v[124:127], v[48:51]\n\t" "ds_read_b128 v[112:115], v77 offset:33280\n\t" \
    "v_mfma_f32_16x16x32_bf16 v[52:55], a[60:63], v[124:127], v[52:55]\n\t" "buffer_load_dwordx4 v91, %[rx], s61 offen offset:1024 lds\n\t" "s_waitcnt lgkmcnt(4)\n\t" \
    "v_mfma_f32_16x16x32_bf16 v[56:59], a[56:59], v[128:131], v[56:59]\n\t" "ds_read_b128 v[116:119], v78 offset:512\n\t" "s_mov_b32 m0, s63\n\t" \
    "v_mfma_f32_16x16x32_bf16 v[60:63], a[60:63], v[128:131], v[60:63]\n\t" "s_add_u32 s63, s63, 0x800\n\t" "buffer_load_dwordx4 v92, %[rx], s61 offen lds\n\t" \
    "s_waitcnt lgkmcnt(4)\n\t" "v_mfma_f32_16x16x32_bf16 v[48:51], a[64:67], v[100:103], v[48:51]\n\t" "ds_read_b128 v[120:123], v78 offset:33280\n\t" \
    "v_mfma_f32_16x16x32_bf16 v[52:55], a[68:71], v[100:103], v[52:55]\n\t" "buffer_load_dwordx4 v92, %[rx], s61 offen offset:1024 lds\n\t" "s_waitcnt lgkmcnt(4)\n\t" \
    "v_mfma_f32_16x16x32_bf16 v[56:59], a[64:67], v[104:107], v[56:59]\n\t" "ds_read_b128 v[124:127], v79 offset:512\n\t" "s_mov_b32 m0, s63\n\t" \
    "v_mfma_f32_16x16x32_bf16 v[60:63], a[68:71], v[104:107], v[60:63]\n\t" "s_add_u32 s63, s63, 0x800\n\t" "buffer_load_dwordx4 v93, %[rx], s61 offen lds\n\t" \
    "s_waitcnt lgkmcnt(4)\n\t" "v_mfma_f32_16x16x32_bf16 v[48:51], a[72:75], v[108:111], v[48:51]\n\t" "ds_read_b128 v[128:131], v79 offset:33280\n\t" \
    "v_mfma_f32_16x16x32_bf16 v[52:55], a[76:79], v[108:111], v[52:55]\n\t" "buffer_load_dwordx4 v93, %[rx], s61 offen offset:1024 lds\n\t" "s_waitcnt lgkmcnt(4)\n\t" \
    "v_mfma_f32_16x16x32_bf16 v[56:59], a[72:75], v[112:115], v[56:59]\n\t" "ds_read_b128 v[100:103], v76 offset:768\n\t" "s_mov_b32 m0, s63\n\t" \
    "v_mfma_f32_16x16x32_bf16 v[60:63], a[76:79], v[112:115], v[60:63]\n\t" "s_add_u32 s63, s63, 0x800\n\t" "buffer_load_dwordx4 v94, %[rx], s61 offen lds\n\t" \
    "s_waitcnt lgkmcnt(4)\n\t" "v_mfma_f32_16x16x32_bf16 v[48:51], a[80:83], v[116:119], v[48:51]\n\t" "ds_read_b128 v[104:107], v76 offset:33536\n\t" \
    "v_mfma_f32_16x16x32_bf16 v[52:55], a[84:87], v[116:119], v[52:55]\n\t" "buffer_load_dwordx4 v94, %[rx], s61 offen offset:1024 lds\n\t" "s_waitcnt lgkmcnt(4)\n\t" \
    "v_mfma_f32_16x16x32_bf16 v[56:59], a[80:83], v[120:123], v[56:59]\n\t" "ds_read_b128 v[108:111], v77 offset:768\n\t" "s_mov_b32 m0, s63\n\t" \
    "v_mfma_f32_16x16x32_bf16 v[60:63], a[84:87], v[120:123], v[60:63]\n\t" "s_add_u32 s63, s63, 0x800\n\t" "buffer_load_dwordx4 v95, %[rx], s61 offen lds\n\t" \
    "s_waitcnt lgkmcnt(4)\n\t" "v_mfma_f32_16x16x32_bf16 v[48:51], a[88:91], v[124:127], v[48:51]\n\t" "ds_read_b128 v[112:115], v77 offset:33536\n\t" \
    "v_mfma_f32_16x16x32_bf16 v[52:55], a[92:95], v[124:127], v[52:55]\n\t" "buffer_load_dwordx4 v95, %[rx], s61 offen offset:1024 lds\n\t" "s_waitcnt lgkmcnt(4)\n\t" \
    "v_mfma_f32_16x16x32_bf16 v[56:59], a[88:91], v[128:131], v[56:59]\n\t" "ds_read_b128 v[116:119], v78 offset:768\n\t" \
    "v_mfma_f32_16x16x32_bf16 v[60:63], a[92:95], v[128:131], v[60:63]\n\t" "s_waitcnt lgkmcnt(4)\n\t" "v_mfma_f32_16x16x32_bf16 v[48:51], a[96:99], v[100:103], v[48:51]\n\t" \
    "ds_read_b128 v[120:123], v78 offset:33536\n\t" "s_lshr_b32 s64, s60, 2\n\t" "s_xor_b32 s64, s64, %[xp]\n\t" \
    "v_mfma_f32_16x16x32_bf16 v[52:55], a[100:103], v[100:103], v[52:55]\n\t" "s_and_b32 s64, s64, 1\n\t" "s_lshl_b32 s64, s64, 13\n\t" "s_waitcnt lgkmcnt(4)\n\t" \
    "v_mfma_f32_16x16x32_bf16 v[56:59], a[96:99], v[104:107], v[56:59]\n\t" "ds_read_b128 v[124:127], v79 offset:768\n\t" "s_lshl_b32 s62, s60, 19\n\t" \
    "v_mfma_f32_16x16x32_bf16 v[60:63], a[100:103], v[104:107], v[60:63]\n\t" "s_add_u32 s62, s62, s64\n\t" "s_add_u32 s62, s62, %[ob]\n\t" "s_waitcnt lgkmcnt(4)\n\t" \
    "v_mfma_f32_16x16x32_bf16 v[48:51], a[104:107], v[108:111], v[48:51]\n\t" "ds_read_b128 v[128:131], v79 offset:33536\n\t" \
    "v_mfma_f32_16x16x32_bf16 v[52:55], a[108:111], v[108:111], v[52:55]\n\t" "s_waitcnt lgkmcnt(4)\n\t" \
    "v_mfma_f32_16x16x32_bf16 v[56:59], a[104:107], v[112:115], v[56:59]\n\t" "ds_read_b128 v[100:103], v76 offset:1024\n\t" "s_add_u32 s64, s60, 1\n\t" \
    "v_mfma_f32_16x16x32_bf16 v[60:63], a[108:111], v[112:115], v[60:63]\n\t" "s_lshl_b32 s65, s64, 20\n\t" "s_lshr_b32 s64, s64, 1\n\t" "s_waitcnt lgkmcnt(4)\n\t" \
    "v_mfma_f32_16x16x32_bf16 v[48:51], a[112:115], v[116:119], v[48:51]\n\t" "ds_read_b128 v[104:107], v76 offset:33792\n\t" "s_xor_b32 s64, s64, %[xp]\n\t" \
    "v_mfma_f32_16x16x32_bf16 v[52:55], a[116:119], v[116:119], v[52:55]\n\t" "s_and_b32 s64, s64, 1\n\t" "s_lshl_b32 s64, s64, 13\n\t" "s_waitcnt lgkmcnt(4)\n\t" \
    "v_mfma_f32_16x16x32_bf16 v[56:59], a[112:115], v[120:123], v[56:59]\n\t" "ds_read_b128 v[108:111], v77 offset:1024\n\t" \
    "v_mfma_f32_16x16x32_bf16 v[60:63], a[116:119], v[120:123], v[60:63]\n\t" "s_add_u32 s66, s65, s64\n\t" "s_waitcnt lgkmcnt(4)\n\t" \
    "v_mfma_f32_16x16x32_bf16 v[48:51], a[120:123], v[124:127], v[48:51]\n\t" "ds_read_b128 v[112:115], v77 offset:33792\n\t" \
    "v_mfma_f32_16x16x32_bf16 v[52:55], a[124:127], v[124:127], v[52:55]\n\t" "s_waitcnt lgkmcnt(4)\n\t" \
    "v_mfma_f32_16x16x32_bf16 v[56:59], a[120:123], v[128:131], v[56:59]\n\t" "ds_read_b128 v[116:119], v78 offset:1024\n\t" \
    "v_mfma_f32_16x16x32_bf16 v[60:63], a[124:127], v[128:131], v[60:63]\n\t" "s_waitcnt lgkmcnt(4)\n\t" \
    "v_mfma_f32_16x16x32_bf16 v[48:51], a[128:131], v[100:103], v[48:51]\n\t" "ds_read_b128 v[120:123], v78 offset:33792\n\t" \
    "v_mfma_f32_16x16x32_bf16 v[52:55], a[132:135], v[100:103], v[52:55]\n\t" "s_waitcnt lgkmcnt(4)\n\t" \
    "v_mfma_f32_16x16x32_bf16 v[56:59], a[128:131], v[104:107], v[56:59]\n\t" "ds_read_b128 v[124:127], v79 offset:1024\n\t" \
    "v_mfma_f32_16x16x32_bf16 v[60:63], a[132:135], v[104:107], v[60:63]\n\t" "s_waitcnt lgkmcnt(4)\n\t" \
    "v_mfma_f32_16x16x32_bf16 v[48:51], a[136:139], v[108:111], v[48:51]\n\t" "ds_read_b128 v[128:131], v79 offset:33792\n\t" "s_waitcnt vmcnt(16)\n\t" "s_barrier\n\t" \
    "v_mfma_f32_16x16x32_bf16 v[52:55], a[140:143], v[108:111], v[52:55]\n\t" "v_add_u32 v98, s60, %[at]\n\t" "buffer_store_dword v98, %[fo], %[rc], %[fl] offen sc1\n\t" \
    "s_waitcnt lgkmcnt(4)\n\t" "v_mfma_f32_16x16x32_bf16 v[56:59], a[136:139], v[112:115], v[56:59]\n\t" "ds_read_b128 v[100:103], v76 offset:1280\n\t" \
    "v_mfma_f32_16x16x32_bf16 v[60:63], a[140:143], v[112:115], v[60:63]\n\t" "s_waitcnt lgkmcnt(4)\n\t" \
    "v_mfma_f32_16x16x32_bf16 v[48:51], a[144:147], v[116:119], v[48:51]\n\t" "ds_read_b128 v[104:107], v76 offset:34048\n\t" \
    "v_mfma_f32_16x16x32_bf16 v[52:55], a[148:151], v[116:119], v[52:55]\n\t" "s_waitcnt lgkmcnt(4)\n\t" \
    "v_mfma_f32_16x16x32_bf16 v[56:59], a[144:147], v[120:123], v[56:59]\n\t" "ds_read_b128 v[108:111], v77 offset:1280\n\t" \
    "v_mfma_f32_16x16x32_bf16 v[60:63], a[148:151], v[120:123], v[60:63]\n\t" "s_waitcnt lgkmcnt(4)\n\t" \
    "v_mfma_f32_16x16x32_bf16 v[48:51], a[152:155], v[124:127], v[48:51]\n\t" "ds_read_b128 v[112:115], v77 offset:34048\n\t" \
    "v_mfma_f32_16x16x32_bf16 v[52:55], a[156:159], v[124:127], v[52:55]\n\t" "buffer_store_dwordx4 v[80:83], %[uo], %[rs], s66 offen offset:0 sc1\n\t" \
    "s_waitcnt lgkmcnt(4)\n\t" "v_mfma_f32_16x16x32_bf16 v[56:59], a[152:155], v[128:131], v[56:59]\n\t" "ds_read_b128 v[116:119], v78 offset:1280\n\t" \
    "v_mfma_f32_16x16x32_bf16 v[60:63], a[156:159], v[128:131], v[60:63]\n\t" "s_waitcnt lgkmcnt(4)\n\t" \
    "v_mfma_f32_16x16x32_bf16 v[48:51], a[160:163], v[100:103], v[48:51]\n\t" "ds_read_b128 v[120:123], v78 offset:34048\n\t" \
    "v_mfma_f32_16x16x32_bf16 v[52:55], a[164:167], v[100:103], v[52:55]\n\t" "s_waitcnt lgkmcnt(4)\n\t" \
    "v_mfma_f32_16x16x32_bf16 v[56:59], a[160:163], v[104:107], v[56:59]\n\t" "ds_read_b128 v[124:127], v79 offset:1280\n\t" \
    "v_mfma_f32_16x16x32_bf16 v[60:63], a[164:167], v[104:107], v[60:63]\n\t" "s_waitcnt lgkmcnt(4)\n\t" \
    "v_mfma_f32_16x16x32_bf16 v[48:51], a[168:171], v[108:111], v[48:51]\n\t" "ds_read_b128 v[128:131], v79 offset:34048\n\t" \
    "v_mfma_f32_16x16x32_bf16 v[52:55], a[172:175], v[108:111], v[52:55]\n\t" "s_waitcnt lgkmcnt(4)\n\t" \
    "v_mfma_f32_16x16x32_bf16 v[56:59], a[168:171], v[112:115], v[56:59]\n\t" "ds_read_b128 v[100:103], v76 offset:1536\n\t" \
    "v_mfma_f32_16x16x32_bf16 v[60:63], a[172:175], v[112:115], v[60:63]\n\t" "buffer_store_dwordx4 v[80:83], %[uo], %[rs], s66 offen offset:1024 sc1\n\t" \
    "s_waitcnt lgkmcnt(4)\n\t" "v_mfma_f32_16x16x32_bf16 v[48:51], a[176:179], v[116:119], v[48:51]\n\t" "ds_read_b128 v[104:107], v76 offset:34304\n\t" \
    "v_mfma_f32_16x16x32_bf16 v[52:55], a[180:183], v[116:119], v[52:55]\n\t" "s_waitcnt lgkmcnt(4)\n\t" \
    "v_mfma_f32_16x16x32_bf16 v[56:59], a[176:179], v[120:123], v[56:59]\n\t" "ds_read_b128 v[108:111], v77 offset:1536\n\t" \
    "v_mfma_f32_16x16x32_bf16 v[60:63], a[180:183], v[120:123], v[60:63]\n\t" "s_waitcnt lgkmcnt(4)\n\t" \
    "v_mfma_f32_16x16x32_bf16 v[48:51], a[184:187], v[124:127], v[48:51]\n\t" "ds_read_b128 v[112:115], v77 offset:34304\n\t" \
    "v_mfma_f32_16x16x32_bf16 v[52:55], a[188:191], v[124:127], v[52:55]\n\t" "s_waitcnt lgkmcnt(4)\n\t" \
    "v_mfma_f32_16x16x32_bf16 v[56:59], a[184:187], v[128:131], v[56:59]\n\t" "ds_read_b128 v[116:119], v78 offset:1536\n\t" \
    "v_mfma_f32_16x16x32_bf16 v[60:63], a[188:191], v[128:131], v[60:63]\n\t" "s_waitcnt lgkmcnt(4)\n\t" \
    "v_mfma_f32_16x16x32_bf16 v[48:51], a[192:195], v[100:103], v[48:51]\n\t" "ds_read_b128 v[120:123], v78 offset:34304\n\t" \
    "v_mfma_f32_16x16x32_bf16 v[52:55], a[196:199], v[100:103], v[52:55]\n\t" "buffer_store_dwordx4 v[80:83], %[uo], %[rs], s66 offen offset:2048 sc1\n\t" \
    "s_waitcnt lgkmcnt(4)\n\t" "v_mfma_f32_16x16x32_bf16 v[56:59], a[192:195], v[104:107], v[56:59]\n\t" "ds_read_b128 v[124:127], v79 offset:1536\n\t" \
    "v_mfma_f32_16x16x32_bf16 v[60:63], a[196:199], v[104:107], v[60:63]\n\t" "s_waitcnt lgkmcnt(4)\n\t" \
    "v_mfma_f32_16x16x32_bf16 v[48:51], a[200:203], v[108:111], v[48:51]\n\t" "ds_read_b128 v[128:131], v79 offset:34304\n\t" \
    "v_mfma_f32_16x16x32_bf16 v[52:55], a[204:207], v[108:111], v[52:55]\n\t" "s_waitcnt lgkmcnt(4)\n\t" \
    "v_mfma_f32_16x16x32_bf16 v[56:59], a[200:203], v[112:115], v[56:59]\n\t" "ds_read_b128 v[100:103], v76 offset:1792\n\t" \
    "v_mfma_f32_16x16x32_bf16 v[60:63], a[204:207], v[112:115], v[60:63]\n\t" "s_waitcnt lgkmcnt(4)\n\t" \
    "v_mfma_f32_16x16x32_bf16 v[48:51], a[208:211], v[116:119], v[48:51]\n\t" "ds_read_b128 v[104:107], v76 offset:34560\n\t" \
    "v_mfma_f32_16x16x32_bf16 v[52:55], a[212:215], v[116:119], v[52:55]\n\t" "s_waitcnt lgkmcnt(4)\n\t" \
    "v_mfma_f32_16x16x32_bf16 v[56:59], a[208:211], v[120:123], v[56:59]\n\t" "ds_read_b128 v[108:111], v77 offset:1792\n\t" \
    "v_mfma_f32_16x16x32_bf16 v[60:63], a[212:215], v[120:123], v[60:63]\n\t" "buffer_store_dwordx4 v[80:83], %[uo], %[rs], s66 offen offset:3072 sc1\n\t" \
    "s_waitcnt lgkmcnt(4)\n\t" "v_mfma_f32_16x16x32_bf16 v[48:51], a[216:219], v[124:127], v[48:51]\n\t" "ds_read_b128 v[112:115], v77 offset:34560\n\t" \
    "v_mfma_f32_16x16x32_bf16 v[52:55], a[220:223], v[124:127], v[52:55]\n\t" "s_waitcnt lgkmcnt(4)\n\t" \
    "v_mfma_f32_16x16x32_bf16 v[56:59], a[216:219], v[128:131], v[56:59]\n\t" "ds_read_b128 v[116:119], v78 offset:1792\n\t" \
    "v_mfma_f32_16x16x32_bf16 v[60:63], a[220:223], v[128:131], v[60:63]\n\t" "s_waitcnt lgkmcnt(4)\n\t" \
    "v_mfma_f32_16x16x32_bf16 v[48:51], a[224:227], v[100:103], v[48:51]\n\t" "ds_read_b128 v[120:123], v78 offset:34560\n\t" \
    "v_mfma_f32_16x16x32_bf16 v[52:55], a[228:231], v[100:103], v[52:55]\n\t" "s_waitcnt lgkmcnt(4)\n\t" \
    "v_mfma_f32_16x16x32_bf16 v[56:59], a[224:227], v[104:107], v[56:59]\n\t" "ds_read_b128 v[124:127], v79 offset:1792\n\t" \
    "v_mfma_f32_16x16x32_bf16 v[60:63], a[228:231], v[104:107], v[60:63]\n\t" "s_waitcnt lgkmcnt(4)\n\t" \
    "v_mfma_f32_16x16x32_bf16 v[48:51], a[232:235], v[108:111], v[48:51]\n\t" "ds_read_b128 v[128:131], v79 offset:34560\n\t" \
    "v_mfma_f32_16x16x32_bf16 v[52:55], a[236:239], v[108:111], v[52:55]\n\t" "s_waitcnt lgkmcnt(4)\n\t" \
    "v_mfma_f32_16x16x32_bf16 v[56:59], a[232:235], v[112:115], v[56:59]\n\t" "v_mfma_f32_16x16x32_bf16 v[60:63], a[236:239], v[112:115], v[60:63]\n\t" \
    "s_waitcnt lgkmcnt(3)\n\t" "v_mfma_f32_16x16x32_bf16 v[48:51], a[240:243], v[116:119], v[48:51]\n\t" \
    "v_mfma_f32_16x16x32_bf16 v[52:55], a[244:247], v[116:119], v[52:55]\n\t" "s_waitcnt vmcnt(5) lgkmcnt(0)\n\t" "s_barrier\n\t" XT(82, 83) \
    "v_mfma_f32_16x16x32_bf16 v[56:59], a[240:243], v[120:123], v[56:59]\n\t" "ds_read_b128 v[100:103], v72 offset:0\n\t" \
    "v_mfma_f32_16x16x32_bf16 v[60:63], a[244:247], v[120:123], v[60:63]\n\t" "v_mfma_f32_16x16x32_bf16 v[48:51], a[248:251], v[124:127], v[48:51]\n\t" \
    "ds_read_b128 v[104:107], v72 offset:32768\n\t" "v_mfma_f32_16x16x32_bf16 v[52:55], a[252:255], v[124:127], v[52:55]\n\t" "ds_read_b128 v[108:111], v73 offset:0\n\t" \
    "v_mfma_f32_16x16x32_bf16 v[56:59], a[248:251], v[128:131], v[56:59]\n\t" "ds_read_b128 v[112:115], v73 offset:32768\n\t" "ds_read_b128 v[116:119], v74 offset:0\n\t" \
    "v_mfma_f32_16x16x32_bf16 v[60:63], a[252:255], v[128:131], v[60:63]\n\t" "s_add_u32 s60, s60, 1\n\t" GSUM "s_cmp_lt_u32 s60, 64\n\t" "s_cbranch_scc1 10b\n\t" \
    "s_nop 7\n\t" "v_cvt_pk_bf16_f32 v64, v48, v49\n\t" "v_cvt_pk_bf16_f32 v65, v50, v51\n\t" "v_cvt_pk_bf16_f32 v66, v52, v53\n\t" "v_cvt_pk_bf16_f32 v67, v54, v55\n\t" \
    "v_cvt_pk_bf16_f32 v68, v56, v57\n\t" "v_cvt_pk_bf16_f32 v69, v58, v59\n\t" "v_cvt_pk_bf16_f32 v70, v60, v61\n\t" "v_cvt_pk_bf16_f32 v71, v62, v63\n\t" \
    "buffer_store_dwordx4 v[64:67], v96, %[rs], s62 offen offset:0 sc1\n\t" "buffer_store_dwordx4 v[68:71], v96, %[rs], s62 offen offset:256 sc1\n\t" "s_waitcnt vmcnt(0)\n\t" \
    "s_barrier\n\t" "v_add_u32 v98, s60, %[at]\n\t" "buffer_store_dword v98, %[fo], %[rc], %[fl] offen sc1\n\t" "s_waitcnt lgkmcnt(0)\n\t" "s_branch 30f\n\t" "20:\n\t" \
    "v_mov_b32 v96, %[so]\n\t" "s_nop 7\n\t" "v_cvt_pk_bf16_f32 v64, v32, v33\n\t" "v_cvt_pk_bf16_f32 v65, v34, v35\n\t" "v_cvt_pk_bf16_f32 v66, v36, v37\n\t" \
    "v_cvt_pk_bf16_f32 v67, v38, v39\n\t" "v_cvt_pk_bf16_f32 v68, v40, v41\n\t" "v_cvt_pk_bf16_f32 v69, v42, v43\n\t" "v_cvt_pk_bf16_f32 v70, v44, v45\n\t" \
    "v_cvt_pk_bf16_f32 v71, v46, v47\n\t" "buffer_store_dwordx4 v[64:67], v96, %[rs], s62 offen offset:0 sc1\n\t" \
    "buffer_store_dwordx4 v[68:71], v96, %[rs], s62 offen offset:256 sc1\n\t" "s_waitcnt vmcnt(0)\n\t" "s_barrier\n\t" "v_add_u32 v98, s60, %[at]\n\t" \
    "buffer_store_dword v98, %[fo], %[rc], %[fl] offen sc1\n\t" "s_branch 11b\n\t" "30:\n\t"
#define ZC_LOOP \
    "s_mov_b32 s60, 0\n\t" "s_mov_b32 s61, 0\n\t" "s_mov_b32 s62, 0\n\t" "s_mov_b32 s63, 0x7f000000\n\t" "s_cmp_ge_u32 s61, %[z0]\n\t" "s_cbranch_scc1 52f\n\t" "51:\n\t" \
    "buffer_load_dword v120, %[fo], %[rc], %[fv] offen sc1\n\t" "buffer_load_dword v121, %[fo], %[rc], %[vd] offen sc1\n\t" "s_waitcnt vmcnt(0)\n\t" \
    "v_readfirstlane_b32 s61, v120\n\t" "v_readfirstlane_b32 s66, v121\n\t" "s_nop 3\n\t" "s_cmp_ge_u32 s61, %[z0]\n\t" "s_cbranch_scc1 52f\n\t" "s_bitcmp1_b32 s66, 1\n\t" \
    "s_cbranch_scc1 90f\n\t" "s_cmp_eq_u32 %[bd], 0\n\t" "s_cbranch_scc1 52f\n\t" "s_sub_u32 %[bd], %[bd], 1\n\t" "s_sleep 2\n\t" "s_branch 51b\n\t" "52:\n\t" \
    "s_lshr_b32 s66, s60, 2\n\t" "s_xor_b32 s66, s66, %[xp]\n\t" "s_and_b32 s66, s66, 1\n\t" "s_lshl_b32 s66, s66, 13\n\t" "s_lshl_b32 s64, s60, 19\n\t" \
    "s_add_u32 s64, s64, s66\n\t" "s_add_u32 s64, s64, %[vb]\n\t" "buffer_load_dwordx4 v[16:19], %[ao], %[rs], s64 offen offset:0 sc1\n\t" \
    "buffer_load_dwordx4 v[20:23], %[ao], %[rs], s64 offen offset:256 sc1\n\t" "buffer_load_dwordx4 v[24:27], %[ao], %[rs], s64 offen offset:2048 sc1\n\t" \
    "buffer_load_dwordx4 v[28:31], %[ao], %[rs], s64 offen offset:2304 sc1\n\t" "buffer_load_dwordx4 v[32:35], %[a1], %[rs], s64 offen offset:0 sc1\n\t" \
    "buffer_load_dwordx4 v[36:39], %[a1], %[rs], s64 offen offset:256 sc1\n\t" "buffer_load_dwordx4 v[40:43], %[a1], %[rs], s64 offen offset:2048 sc1\n\t" \
    "buffer_load_dwordx4 v[44:47], %[a1], %[rs], s64 offen offset:2304 sc1\n\t" "s_waitcnt vmcnt(0)\n\t" "10:\n\t" XT(80, 81) "s_add_u32 s66, s62, %[rg]\n\t" \
    "s_cmp_lt_u32 s66, s60\n\t" "s_cbranch_scc1 60f\n\t" "63:\n\t" "s_add_u32 s66, s60, 1\n\t" "s_cmp_gt_u32 s61, s66\n\t" "s_cbranch_scc0 40f\n\t" \
    "v_mfma_f32_16x16x32_bf16 v[80:83], v[16:19], a[0:3], 0\n\t" "v_mfma_f32_16x16x32_bf16 v[84:87], v[20:23], a[0:3], 0\n\t" \
    "v_mfma_f32_16x16x32_bf16 v[80:83], v[24:27], a[4:7], v[80:83]\n\t" "v_mfma_f32_16x16x32_bf16 v[84:87], v[28:31], a[4:7], v[84:87]\n\t" \
    "v_mfma_f32_16x16x32_bf16 v[80:83], v[32:35], a[8:11], v[80:83]\n\t" "v_cvt_pk_bf16_f32 v112, v96, v97\n\t" \
    "v_mfma_f32_16x16x32_bf16 v[84:87], v[36:39], a[8:11], v[84:87]\n\t" "v_cvt_pk_bf16_f32 v113, v98, v99\n\t" \
    "v_mfma_f32_16x16x32_bf16 v[80:83], v[40:43], a[12:15], v[80:83]\n\t" "v_cvt_pk_bf16_f32 v114, v100, v101\n\t" \
    "v_mfma_f32_16x16x32_bf16 v[84:87], v[44:47], a[12:15], v[84:87]\n\t" "v_cvt_pk_bf16_f32 v115, v102, v103\n\t" \
    "v_mfma_f32_16x16x32_bf16 v[88:91], v[16:19], a[16:19], 0\n\t" "v_cvt_pk_bf16_f32 v116, v104, v105\n\t" "v_mfma_f32_16x16x32_bf16 v[92:95], v[20:23], a[16:19], 0\n\t" \
    "v_cvt_pk_bf16_f32 v117, v106, v107\n\t" "v_mfma_f32_16x16x32_bf16 v[88:91], v[24:27], a[20:23], v[88:91]\n\t" \
    "buffer_store_dwordx4 v[112:115], %[z1], %[rs], s63 offen sc1\n\t" "v_mfma_f32_16x16x32_bf16 v[92:95], v[28:31], a[20:23], v[92:95]\n\t" \
    "v_mfma_f32_16x16x32_bf16 v[88:91], v[32:35], a[24:27], v[88:91]\n\t" "v_cvt_pk_bf16_f32 v118, v108, v109\n\t" \
    "v_mfma_f32_16x16x32_bf16 v[92:95], v[36:39], a[24:27], v[92:95]\n\t" "v_cvt_pk_bf16_f32 v119, v110, v111\n\t" \
    "v_mfma_f32_16x16x32_bf16 v[88:91], v[40:43], a[28:31], v[88:91]\n\t" "buffer_store_dwordx4 v[116:119], %[z2], %[rs], s63 offen sc1\n\t" "s_and_b32 s63, s60, %[rm]\n\t" \
    "v_mfma_f32_16x16x32_bf16 v[92:95], v[44:47], a[28:31], v[92:95]\n\t" "s_lshl_b32 s63, s63, 22\n\t" "s_add_u32 s63, s63, %[zb]\n\t" \
    "v_mfma_f32_16x16x32_bf16 v[96:99], v[16:19], a[32:35], 0\n\t" "s_add_u32 s65, s60, 1\n\t" "s_lshr_b32 s66, s65, 2\n\t" \
    "v_mfma_f32_16x16x32_bf16 v[100:103], v[20:23], a[32:35], 0\n\t" "s_xor_b32 s66, s66, %[xp]\n\t" "s_and_b32 s66, s66, 1\n\t" \
    "v_mfma_f32_16x16x32_bf16 v[96:99], v[24:27], a[36:39], v[96:99]\n\t" "s_lshl_b32 s66, s66, 13\n\t" "s_lshl_b32 s64, s65, 19\n\t" \
    "v_mfma_f32_16x16x32_bf16 v[100:103], v[28:31], a[36:39], v[100:103]\n\t" "s_add_u32 s64, s64, s66\n\t" "s_add_u32 s64, s64, %[vb]\n\t" \
    "v_mfma_f32_16x16x32_bf16 v[96:99], v[32:35], a[40:43], v[96:99]\n\t" "buffer_load_dwordx4 v[48:51], %[ao], %[rs], s64 offen offset:0 sc1\n\t" \
    "v_cvt_pk_bf16_f32 v112, v80, v81\n\t" "v_mfma_f32_16x16x32_bf16 v[100:103], v[36:39], a[40:43], v[100:103]\n\t" "v_cvt_pk_bf16_f32 v113, v82, v83\n\t" \
    "v_mfma_f32_16x16x32_bf16 v[96:99], v[40:43], a[44:47], v[96:99]\n\t" "buffer_load_dwordx4 v[52:55], %[ao], %[rs], s64 offen offset:256 sc1\n\t" \
    "v_cvt_pk_bf16_f32 v114, v84, v85\n\t" "v_mfma_f32_16x16x32_bf16 v[100:103], v[44:47], a[44:47], v[100:103]\n\t" "v_cvt_pk_bf16_f32 v115, v86, v87\n\t" \
    "v_mfma_f32_16x16x32_bf16 v[104:107], v[16:19], a[48:51], 0\n\t" "buffer_load_dwordx4 v[56:59], %[ao], %[rs], s64 offen offset:2048 sc1\n\t" \
    "v_cvt_pk_bf16_f32 v116, v88, v89\n\t" "v_mfma_f32_16x16x32_bf16 v[108:111], v[20:23], a[48:51], 0\n\t" "v_cvt_pk_bf16_f32 v117, v90, v91\n\t" \
    "v_mfma_f32_16x16x32_bf16 v[104:107], v[24:27], a[52:55], v[104:107]\n\t" "buffer_load_dwordx4 v[60:63], %[ao], %[rs], s64 offen offset:2304 sc1\n\t" \
    "v_mfma_f32_16x16x32_bf16 v[108:111], v[28:31], a[52:55], v[108:111]\n\t" "buffer_store_dwordx4 v[112:115], %[z1], %[rs], s63 offen sc1\n\t" \
    "v_mfma_f32_16x16x32_bf16 v[104:107], v[32:35], a[56:59], v[104:107]\n\t" "buffer_load_dwordx4 v[64:67], %[a1], %[rs], s64 offen offset:0 sc1\n\t" \
    "v_cvt_pk_bf16_f32 v118, v92, v93\n\t" "v_mfma_f32_16x16x32_bf16 v[108:111], v[36:39], a[56:59], v[108:111]\n\t" "v_cvt_pk_bf16_f32 v119, v94, v95\n\t" \
    "v_mfma_f32_16x16x32_bf16 v[104:107], v[40:43], a[60:63], v[104:107]\n\t" "buffer_load_dwordx4 v[68:71], %[a1], %[rs], s64 offen offset:256 sc1\n\t" \
    "buffer_store_dwordx4 v[116:119], %[z2], %[rs], s63 offen sc1\n\t" "v_mfma_f32_16x16x32_bf16 v[108:111], v[44:47], a[60:63], v[108:111]\n\t" \
    "s_add_u32 s63, s63, 0x20000\n\t" "v_mfma_f32_16x16x32_bf16 v[80:83], v[16:19], a[64:67], 0\n\t" \
    "buffer_load_dwordx4 v[72:75], %[a1], %[rs], s64 offen offset:2048 sc1\n\t" "v_mfma_f32_16x16x32_bf16 v[84:87], v[20:23], a[64:67], 0\n\t" \
    "v_mfma_f32_16x16x32_bf16 v[80:83], v[24:27], a[68:71], v[80:83]\n\t" "buffer_load_dwordx4 v[76:79], %[a1], %[rs], s64 offen offset:2304 sc1\n\t" \
    "v_mfma_f32_16x16x32_bf16 v[84:87], v[28:31], a[68:71], v[84:87]\n\t" "v_mfma_f32_16x16x32_bf16 v[80:83], v[32:35], a[72:75], v[80:83]\n\t" \
    "buffer_load_dword v120, %[fo], %[rc], %[fv] offen sc1\n\t" "v_cvt_pk_bf16_f32 v112, v96, v97\n\t" "v_mfma_f32_16x16x32_bf16 v[84:87], v[36:39], a[72:75], v[84:87]\n\t" \
    "v_cvt_pk_bf16_f32 v113, v98, v99\n\t" "v_mfma_f32_16x16x32_bf16 v[80:83], v[40:43], a[76:79], v[80:83]\n\t" "buffer_load_dword v121, %[fo], %[rc], %[fs] offen sc1\n\t" \
    "v_mfma_f32_16x16x32_bf16 v[84:87], v[44:47], a[76:79], v[84:87]\n\t" "v_cvt_pk_bf16_f32 v114, v100, v101\n\t" "v_cvt_pk_bf16_f32 v115, v102, v103\n\t" \
    "v_mfma_f32_16x16x32_bf16 v[88:91], v[16:19], a[80:83], 0\n\t" "v_cvt_pk_bf16_f32 v116, v104, v105\n\t" "v_mfma_f32_16x16x32_bf16 v[92:95], v[20:23], a[80:83], 0\n\t" \
    "v_cvt_pk_bf16_f32 v117, v106, v107\n\t" "v_mfma_f32_16x16x32_bf16 v[88:91], v[24:27], a[84:87], v[88:91]\n\t" \
    "buffer_store_dwordx4 v[112:115], %[z1], %[rs], s63 offen sc1\n\t" "v_mfma_f32_16x16x32_bf16 v[92:95], v[28:31], a[84:87], v[92:95]\n\t" \
    "v_mfma_f32_16x16x32_bf16 v[88:91], v[32:35], a[88:91], v[88:91]\n\t" "v_cvt_pk_bf16_f32 v118, v108, v109\n\t" \
    "v_mfma_f32_16x16x32_bf16 v[92:95], v[36:39], a[88:91], v[92:95]\n\t" "v_cvt_pk_bf16_f32 v119, v110, v111\n\t" \
    "v_mfma_f32_16x16x32_bf16 v[88:91], v[40:43], a[92:95], v[88:91]\n\t" "buffer_store_dwordx4 v[116:119], %[z2], %[rs], s63 offen sc1\n\t" \
    "v_mfma_f32_16x16x32_bf16 v[92:95], v[44:47], a[92:95], v[92:95]\n\t" "s_add_u32 s63, s63, 0x20000\n\t" "v_mfma_f32_16x16x32_bf16 v[96:99], v[16:19], a[96:99], 0\n\t" \
    "v_mfma_f32_16x16x32_bf16 v[100:103], v[20:23], a[96:99], 0\n\t" "v_mfma_f32_16x16x32_bf16 v[96:99], v[24:27], a[100:103], v[96:99]\n\t" \
    "v_mfma_f32_16x16x32_bf16 v[100:103], v[28:31], a[100:103], v[100:103]\n\t" "v_mfma_f32_16x16x32_bf16 v[96:99], v[32:35], a[104:107], v[96:99]\n\t" \
    "v_cvt_pk_bf16_f32 v112, v80, v81\n\t" "v_mfma_f32_16x16x32_bf16 v[100:103], v[36:39], a[104:107], v[100:103]\n\t" "v_cvt_pk_bf16_f32 v113, v82, v83\n\t" \
    "v_mfma_f32_16x16x32_bf16 v[96:99], v[40:43], a[108:111], v[96:99]\n\t" "v_cvt_pk_bf16_f32 v114, v84, v85\n\t" \
    "v_mfma_f32_16x16x32_bf16 v[100:103], v[44:47], a[108:111], v[100:103]\n\t" "v_cvt_pk_bf16_f32 v115, v86, v87\n\t" \
    "v_mfma_f32_16x16x32_bf16 v[104:107], v[16:19], a[112:115], 0\n\t" "v_cvt_pk_bf16_f32 v116, v88, v89\n\t" \
    "v_mfma_f32_16x16x32_bf16 v[108:111], v[20:23], a[112:115], 0\n\t" "v_cvt_pk_bf16_f32 v117, v90, v91\n\t" \
    "v_mfma_f32_16x16x32_bf16 v[104:107], v[24:27], a[116:119], v[104:107]\n\t" "buffer_store_dwordx4 v[112:115], %[z1], %[rs], s63 offen sc1\n\t" \
    "v_mfma_f32_16x16x32_bf16 v[108:111], v[28:31], a[116:119], v[108:111]\n\t" "v_mfma_f32_16x16x32_bf16 v[104:107], v[32:35], a[120:123], v[104:107]\n\t" \
    "v_cvt_pk_bf16_f32 v118, v92, v93\n\t" "v_mfma_f32_16x16x32_bf16 v[108:111], v[36:39], a[120:123], v[108:111]\n\t" "v_cvt_pk_bf16_f32 v119, v94, v95\n\t" \
    "v_mfma_f32_16x16x32_bf16 v[104:107], v[40:43], a[124:127], v[104:107]\n\t" "buffer_store_dwordx4 v[116:119], %[z2], %[rs], s63 offen sc1\n\t" \
    "v_mfma_f32_16x16x32_bf16 v[108:111], v[44:47], a[124:127], v[108:111]\n\t" "s_add_u32 s63, s63, 0x20000\n\t" \
    "v_mfma_f32_16x16x32_bf16 v[80:83], v[16:19], a[128:131], 0\n\t" "v_mfma_f32_16x16x32_bf16 v[84:87], v[20:23], a[128:131], 0\n\t" \
    "v_mfma_f32_16x16x32_bf16 v[80:83], v[24:27], a[132:135], v[80:83]\n\t" "v_mfma_f32_16x16x32_bf16 v[84:87], v[28:31], a[132:135], v[84:87]\n\t" \
    "v_mfma_f32_16x16x32_bf16 v[80:83], v[32:35], a[136:139], v[80:83]\n\t" "v_cvt_pk_bf16_f32 v112, v96, v97\n\t" \
    "v_mfma_f32_16x16x32_bf16 v[84:87], v[36:39], a[136:139], v[84:87]\n\t" "v_cvt_pk_bf16_f32 v113, v98, v99\n\t" \
    "v_mfma_f32_16x16x32_bf16 v[80:83], v[40:43], a[140:143], v[80:83]\n\t" "v_cvt_pk_bf16_f32 v114, v100, v101\n\t" \
    "v_mfma_f32_16x16x32_bf16 v[84:87], v[44:47], a[140:143], v[84:87]\n\t" "v_cvt_pk_bf16_f32 v115, v102, v103\n\t" \
    "v_mfma_f32_16x16x32_bf16 v[88:91], v[16:19], a[144:147], 0\n\t" "v_cvt_pk_bf16_f32 v116, v104, v105\n\t" "v_mfma_f32_16x16x32_bf16 v[92:95], v[20:23], a[144:147], 0\n\t" \
    "v_cvt_pk_bf16_f32 v117, v106, v107\n\t" "v_mfma_f32_16x16x32_bf16 v[88:91], v[24:27], a[148:151], v[88:91]\n\t" \
    "buffer_store_dwordx4 v[112:115], %[z1], %[rs], s63 offen sc1\n\t" "v_mfma_f32_16x16x32_bf16 v[92:95], v[28:31], a[148:151], v[92:95]\n\t" \
    "v_mfma_f32_16x16x32_bf16 v[88:91], v[32:35], a[152:155], v[88:91]\n\t" "v_cvt_pk_bf16_f32 v118, v108, v109\n\t" \
    "v_mfma_f32_16x16x32_bf16 v[92:95], v[36:39], a[152:155], v[92:95]\n\t" "v_cvt_pk_bf16_f32 v119, v110, v111\n\t" \
    "v_mfma_f32_16x16x32_bf16 v[88:91], v[40:43], a[156:159], v[88:91]\n\t" "buffer_store_dwordx4 v[116:119], %[z2], %[rs], s63 offen sc1\n\t" \
    "v_mfma_f32_16x16x32_bf16 v[92:95], v[44:47], a[156:159], v[92:95]\n\t" "s_add_u32 s63, s63, 0x20000\n\t" "v_mfma_f32_16x16x32_bf16 v[96:99], v[16:19], a[160:163], 0\n\t" \
    "v_mfma_f32_16x16x32_bf16 v[100:103], v[20:23], a[160:163], 0\n\t" "v_mfma_f32_16x16x32_bf16 v[96:99], v[24:27], a[164:167], v[96:99]\n\t" \
    "v_mfma_f32_16x16x32_bf16 v[100:103], v[28:31], a[164:167], v[100:103]\n\t" "v_mfma_f32_16x16x32_bf16 v[96:99], v[32:35], a[168:171], v[96:99]\n\t" \
    "v_cvt_pk_bf16_f32 v112, v80, v81\n\t" "v_mfma_f32_16x16x32_bf16 v[100:103], v[36:39], a[168:171], v[100:103]\n\t" "v_cvt_pk_bf16_f32 v113, v82, v83\n\t" \
    "v_mfma_f32_16x16x32_bf16 v[96:99], v[40:43], a[172:175], v[96:99]\n\t" "v_cvt_pk_bf16_f32 v114, v84, v85\n\t" \
    "v_mfma_f32_16x16x32_bf16 v[100:103], v[44:47], a[172:175], v[100:103]\n\t" "v_cvt_pk_bf16_f32 v115, v86, v87\n\t" \
    "v_mfma_f32_16x16x32_bf16 v[104:107], v[16:19], a[176:179], 0\n\t" "v_cvt_pk_bf16_f32 v116, v88, v89\n\t" \
    "v_mfma_f32_16x16x32_bf16 v[108:111], v[20:23], a[176:179], 0\n\t" "v_cvt_pk_bf16_f32 v117, v90, v91\n\t" \
    "v_mfma_f32_16x16x32_bf16 v[104:107], v[24:27], a[180:183], v[104:107]\n\t" "buffer_store_dwordx4 v[112:115], %[z1], %[rs], s63 offen sc1\n\t" \
    "v_mfma_f32_16x16x32_bf16 v[108:111], v[28:31], a[180:183], v[108:111]\n\t" "v_mfma_f32_16x16x32_bf16 v[104:107], v[32:35], a[184:187], v[104:107]\n\t" \
    "v_cvt_pk_bf16_f32 v118, v92, v93\n\t" "v_mfma_f32_16x16x32_bf16 v[108:111], v[36:39], a[184:187], v[108:111]\n\t" "v_cvt_pk_bf16_f32 v119, v94, v95\n\t" \
    "v_mfma_f32_16x16x32_bf16 v[104:107], v[40:43], a[188:191], v[104:107]\n\t" "buffer_store_dwordx4 v[116:119], %[z2], %[rs], s63 offen sc1\n\t" \
    "v_mfma_f32_16x16x32_bf16 v[108:111], v[44:47], a[188:191], v[108:111]\n\t" "s_add_u32 s63, s63, 0x20000\n\t" \
    "v_mfma_f32_16x16x32_bf16 v[80:83], v[16:19], a[192:195], 0\n\t" "v_mfma_f32_16x16x32_bf16 v[84:87], v[20:23], a[192:195], 0\n\t" \
    "v_mfma_f32_16x16x32_bf16 v[80:83], v[24:27], a[196:199], v[80:83]\n\t" "v_mfma_f32_16x16x32_bf16 v[84:87], v[28:31], a[196:199], v[84:87]\n\t" \
    "v_mfma_f32_16x16x32_bf16 v[80:83], v[32:35], a[200:203], v[80:83]\n\t" "v_cvt_pk_bf16_f32 v112, v96, v97\n\t" \
    "v_mfma_f32_16x16x32_bf16 v[84:87], v[36:39], a[200:203], v[84:87]\n\t" "v_cvt_pk_bf16_f32 v113, v98, v99\n\t" \
    "v_mfma_f32_16x16x32_bf16 v[80:83], v[40:43], a[204:207], v[80:83]\n\t" "v_cvt_pk_bf16_f32 v114, v100, v101\n\t" \
    "v_mfma_f32_16x16x32_bf16 v[84:87], v[44:47], a[204:207], v[84:87]\n\t" "v_cvt_pk_bf16_f32 v115, v102, v103\n\t" \
    "v_mfma_f32_16x16x32_bf16 v[88:91], v[16:19], a[208:211], 0\n\t" "v_cvt_pk_bf16_f32 v116, v104, v105\n\t" "v_mfma_f32_16x16x32_bf16 v[92:95], v[20:23], a[208:211], 0\n\t" \
    "v_cvt_pk_bf16_f32 v117, v106, v107\n\t" "v_mfma_f32_16x16x32_bf16 v[88:91], v[24:27], a[212:215], v[88:91]\n\t" \
    "buffer_store_dwordx4 v[112:115], %[z1], %[rs], s63 offen sc1\n\t" "v_mfma_f32_16x16x32_bf16 v[92:95], v[28:31], a[212:215], v[92:95]\n\t" \
    "v_mfma_f32_16x16x32_bf16 v[88:91], v[32:35], a[216:219], v[88:91]\n\t" "v_cvt_pk_bf16_f32 v118, v108, v109\n\t" \
    "v_mfma_f32_16x16x32_bf16 v[92:95], v[36:39], a[216:219], v[92:95]\n\t" "v_cvt_pk_bf16_f32 v119, v110, v111\n\t" \
    "v_mfma_f32_16x16x32_bf16 v[88:91], v[40:43], a[220:223], v[88:91]\n\t" "buffer_store_dwordx4 v[116:119], %[z2], %[rs], s63 offen sc1\n\t" \
    "v_mfma_f32_16x16x32_bf16 v[92:95], v[44:47], a[220:223], v[92:95]\n\t" "s_add_u32 s63, s63, 0x20000\n\t" "v_mfma_f32_16x16x32_bf16 v[96:99], v[16:19], a[224:227], 0\n\t" \
    "v_mfma_f32_16x16x32_bf16 v[100:103], v[20:23], a[224:227], 0\n\t" "v_mfma_f32_16x16x32_bf16 v[96:99], v[24:27], a[228:231], v[96:99]\n\t" \
    "v_mfma_f32_16x16x32_bf16 v[100:103], v[28:31], a[228:231], v[100:103]\n\t" "v_mfma_f32_16x16x32_bf16 v[96:99], v[32:35], a[232:235], v[96:99]\n\t" \
    "v_cvt_pk_bf16_f32 v112, v80, v81\n\t" "v_mfma_f32_16x16x32_bf16 v[100:103], v[36:39], a[232:235], v[100:103]\n\t" "v_cvt_pk_bf16_f32 v113, v82, v83\n\t" \
    "v_mfma_f32_16x16x32_bf16 v[96:99], v[40:43], a[236:239], v[96:99]\n\t" "v_cvt_pk_bf16_f32 v114, v84, v85\n\t" \
    "v_mfma_f32_16x16x32_bf16 v[100:103], v[44:47], a[236:239], v[100:103]\n\t" "v_cvt_pk_bf16_f32 v115, v86, v87\n\t" \
    "v_mfma_f32_16x16x32_bf16 v[104:107], v[16:19], a[240:243], 0\n\t" "v_cvt_pk_bf16_f32 v116, v88, v89\n\t" \
    "v_mfma_f32_16x16x32_bf16 v[108:111], v[20:23], a[240:243], 0\n\t" "v_cvt_pk_bf16_f32 v117, v90, v91\n\t" \
    "v_mfma_f32_16x16x32_bf16 v[104:107], v[24:27], a[244:247], v[104:107]\n\t" "buffer_store_dwordx4 v[112:115], %[z1], %[rs], s63 offen sc1\n\t" \
    "v_mfma_f32_16x16x32_bf16 v[108:111], v[28:31], a[244:247], v[108:111]\n\t" "v_mfma_f32_16x16x32_bf16 v[104:107], v[32:35], a[248:251], v[104:107]\n\t" \
    "v_cvt_pk_bf16_f32 v118, v92, v93\n\t" "v_cvt_pk_bf16_f32 v119, v94, v95\n\t" "s_waitcnt vmcnt(11)\n\t" "v_readfirstlane_b32 s66, v120\n\t" \
    "v_mfma_f32_16x16x32_bf16 v[108:111], v[36:39], a[248:251], v[108:111]\n\t" "v_readfirstlane_b32 s67, v121\n\t" "v_mov_b32 v122, s60\n\t" \
    "buffer_store_dword v122, %[fo], %[rc], %[fz] offen sc1\n\t" "v_mfma_f32_16x16x32_bf16 v[104:107], v[40:43], a[252:255], v[104:107]\n\t" \
    "buffer_store_dwordx4 v[116:119], %[z2], %[rs], s63 offen sc1\n\t" "s_add_u32 s63, s63, 0x20000\n\t" "s_max_u32 s61, s61, s66\n\t" \
    "v_mfma_f32_16x16x32_bf16 v[108:111], v[44:47], a[252:255], v[108:111]\n\t" "s_max_u32 s62, s62, s67\n\t" "s_add_u32 s60, s60, 1\n\t" ZSUM "23:\n\t" XT(80, 81) \
    "s_add_u32 s66, s62, %[rg]\n\t" "s_cmp_lt_u32 s66, s60\n\t" "s_cbranch_scc1 70f\n\t" "73:\n\t" "s_add_u32 s66, s60, 1\n\t" "s_cmp_gt_u32 s61, s66\n\t" \
    "s_cbranch_scc0 41f\n\t" "v_mfma_f32_16x16x32_bf16 v[80:83], v[48:51], a[0:3], 0\n\t" "v_mfma_f32_16x16x32_bf16 v[84:87], v[52:55], a[0:3], 0\n\t" \
    "v_mfma_f32_16x16x32_bf16 v[80:83], v[56:59], a[4:7], v[80:83]\n\t" "v_mfma_f32_16x16x32_bf16 v[84:87], v[60:63], a[4:7], v[84:87]\n\t" \
    "v_mfma_f32_16x16x32_bf16 v[80:83], v[64:67], a[8:11], v[80:83]\n\t" "v_cvt_pk_bf16_f32 v112, v96, v97\n\t" \
    "v_mfma_f32_16x16x32_bf16 v[84:87], v[68:71], a[8:11], v[84:87]\n\t" "v_cvt_pk_bf16_f32 v113, v98, v99\n\t" \
    "v_mfma_f32_16x16x32_bf16 v[80:83], v[72:75], a[12:15], v[80:83]\n\t" "v_cvt_pk_bf16_f32 v114, v100, v101\n\t" \
    "v_mfma_f32_16x16x32_bf16 v[84:87], v[76:79], a[12:15], v[84:87]\n\t" "v_cvt_pk_bf16_f32 v115, v102, v103\n\t" \
    "v_mfma_f32_16x16x32_bf16 v[88:91], v[48:51], a[16:19], 0\n\t" "v_cvt_pk_bf16_f32 v116, v104, v105\n\t" "v_mfma_f32_16x16x32_bf16 v[92:95], v[52:55], a[16:19], 0\n\t" \
    "v_cvt_pk_bf16_f32 v117, v106, v107\n\t" "v_mfma_f32_16x16x32_bf16 v[88:91], v[56:59], a[20:23], v[88:91]\n\t" \
    "buffer_store_dwordx4 v[112:115], %[z1], %[rs], s63 offen sc1\n\t" "v_mfma_f32_16x16x32_bf16 v[92:95], v[60:63], a[20:23], v[92:95]\n\t" \
    "v_mfma_f32_16x16x32_bf16 v[88:91], v[64:67], a[24:27], v[88:91]\n\t" "v_cvt_pk_bf16_f32 v118, v108, v109\n\t" \
    "v_mfma_f32_16x16x32_bf16 v[92:95], v[68:71], a[24:27], v[92:95]\n\t" "v_cvt_pk_bf16_f32 v119, v110, v111\n\t" \
    "v_mfma_f32_16x16x32_bf16 v[88:91], v[72:75], a[28:31], v[88:91]\n\t" "buffer_store_dwordx4 v[116:119], %[z2], %[rs], s63 offen sc1\n\t" "s_and_b32 s63, s60, %[rm]\n\t" \
    "v_mfma_f32_16x16x32_bf16 v[92:95], v[76:79], a[28:31], v[92:95]\n\t" "s_lshl_b32 s63, s63, 22\n\t" "s_add_u32 s63, s63, %[zb]\n\t" \
    "v_mfma_f32_16x16x32_bf16 v[96:99], v[48:51], a[32:35], 0\n\t" "s_add_u32 s65, s60, 1\n\t" "s_lshr_b32 s66, s65, 2\n\t" \
    "v_mfma_f32_16x16x32_bf16 v[100:103], v[52:55], a[32:35], 0\n\t" "s_xor_b32 s66, s66, %[xp]\n\t" "s_and_b32 s66, s66, 1\n\t" \
    "v_mfma_f32_16x16x32_bf16 v[96:99], v[56:59], a[36:39], v[96:99]\n\t" "s_lshl_b32 s66, s66, 13\n\t" "s_lshl_b32 s64, s65, 19\n\t" \
    "v_mfma_f32_16x16x32_bf16 v[100:103], v[60:63], a[36:39], v[100:103]\n\t" "s_add_u32 s64, s64, s66\n\t" "s_add_u32 s64, s64, %[vb]\n\t" \
    "v_mfma_f32_16x16x32_bf16 v[96:99], v[64:67], a[40:43], v[96:99]\n\t" "buffer_load_dwordx4 v[16:19], %[ao], %[rs], s64 offen offset:0 sc1\n\t" \
    "v_cvt_pk_bf16_f32 v112, v80, v81\n\t" "v_mfma_f32_16x16x32_bf16 v[100:103], v[68:71], a[40:43], v[100:103]\n\t" "v_cvt_pk_bf16_f32 v113, v82, v83\n\t" \
    "v_mfma_f32_16x16x32_bf16 v[96:99], v[72:75], a[44:47], v[96:99]\n\t" "buffer_load_dwordx4 v[20:23], %[ao], %[rs], s64 offen offset:256 sc1\n\t" \
    "v_cvt_pk_bf16_f32 v114, v84, v85\n\t" "v_mfma_f32_16x16x32_bf16 v[100:103], v[76:79], a[44:47], v[100:103]\n\t" "v_cvt_pk_bf16_f32 v115, v86, v87\n\t" \
    "v_mfma_f32_16x16x32_bf16 v[104:107], v[48:51], a[48:51], 0\n\t" "buffer_load_dwordx4 v[24:27], %[ao], %[rs], s64 offen offset:2048 sc1\n\t" \
    "v_cvt_pk_bf16_f32 v116, v88, v89\n\t" "v_mfma_f32_16x16x32_bf16 v[108:111], v[52:55], a[48:51], 0\n\t" "v_cvt_pk_bf16_f32 v117, v90, v91\n\t" \
    "v_mfma_f32_16x16x32_bf16 v[104:107], v[56:59], a[52:55], v[104:107]\n\t" "buffer_load_dwordx4 v[28:31], %[ao], %[rs], s64 offen offset:2304 sc1\n\t" \
    "v_mfma_f32_16x16x32_bf16 v[108:111], v[60:63], a[52:55], v[108:111]\n\t" "buffer_store_dwordx4 v[112:115], %[z1], %[rs], s63 offen sc1\n\t" \
    "v_mfma_f32_16x16x32_bf16 v[104:107], v[64:67], a[56:59], v[104:107]\n\t" "buffer_load_dwordx4 v[32:35], %[a1], %[rs], s64 offen offset:0 sc1\n\t" \
    "v_cvt_pk_bf16_f32 v118, v92, v93\n\t" "v_mfma_f32_16x16x32_bf16 v[108:111], v[68:71], a[56:59], v[108:111]\n\t" "v_cvt_pk_bf16_f32 v119, v94, v95\n\t" \
    "v_mfma_f32_16x16x32_bf16 v[104:107], v[72:75], a[60:63], v[104:107]\n\t" "buffer_load_dwordx4 v[36:39], %[a1], %[rs], s64 offen offset:256 sc1\n\t" \
    "buffer_store_dwordx4 v[116:119], %[z2], %[rs], s63 offen sc1\n\t" "v_mfma_f32_16x16x32_bf16 v[108:111], v[76:79], a[60:63], v[108:111]\n\t" \
    "s_add_u32 s63, s63, 0x20000\n\t" "v_mfma_f32_16x16x32_bf16 v[80:83], v[48:51], a[64:67], 0\n\t" \
    "buffer_load_dwordx4 v[40:43], %[a1], %[rs], s64 offen offset:2048 sc1\n\t" "v_mfma_f32_16x16x32_bf16 v[84:87], v[52:55], a[64:67], 0\n\t" \
    "v_mfma_f32_16x16x32_bf16 v[80:83], v[56:59], a[68:71], v[80:83]\n\t" "buffer_load_dwordx4 v[44:47], %[a1], %[rs], s64 offen offset:2304 sc1\n\t" \
    "v_mfma_f32_16x16x32_bf16 v[84:87], v[60:63], a[68:71], v[84:87]\n\t" "v_mfma_f32_16x16x32_bf16 v[80:83], v[64:67], a[72:75], v[80:83]\n\t" \
    "buffer_load_dword v120, %[fo], %[rc], %[fv] offen sc1\n\t" "v_cvt_pk_bf16_f32 v112, v96, v97\n\t" "v_mfma_f32_16x16x32_bf16 v[84:87], v[68:71], a[72:75], v[84:87]\n\t" \
    "v_cvt_pk_bf16_f32 v113, v98, v99\n\t" "v_mfma_f32_16x16x32_bf16 v[80:83], v[72:75], a[76:79], v[80:83]\n\t" "buffer_load_dword v121, %[fo], %[rc], %[fs] offen sc1\n\t" \
    "v_mfma_f32_16x16x32_bf16 v[84:87], v[76:79], a[76:79], v[84:87]\n\t" "v_cvt_pk_bf16_f32 v114, v100, v101\n\t" "v_cvt_pk_bf16_f32 v115, v102, v103\n\t" \
    "v_mfma_f32_16x16x32_bf16 v[88:91], v[48:51], a[80:83], 0\n\t" "v_cvt_pk_bf16_f32 v116, v104, v105\n\t" "v_mfma_f32_16x16x32_bf16 v[92:95], v[52:55], a[80:83], 0\n\t" \
    "v_cvt_pk_bf16_f32 v117, v106, v107\n\t" "v_mfma_f32_16x16x32_bf16 v[88:91], v[56:59], a[84:87], v[88:91]\n\t" \
    "buffer_store_dwordx4 v[112:115], %[z1], %[rs], s63 offen sc1\n\t" "v_mfma_f32_16x16x32_bf16 v[92:95], v[60:63], a[84:87], v[92:95]\n\t" \
    "v_mfma_f32_16x16x32_bf16 v[88:91], v[64:67], a[88:91], v[88:91]\n\t" "v_cvt_pk_bf16_f32 v118, v108, v109\n\t" \
    "v_mfma_f32_16x16x32_bf16 v[92:95], v[68:71], a[88:91], v[92:95]\n\t" "v_cvt_pk_bf16_f32 v119, v110, v111\n\t" \
    "v_mfma_f32_16x16x32_bf16 v[88:91], v[72:75], a[92:95], v[88:91]\n\t" "buffer_store_dwordx4 v[116:119], %[z2], %[rs], s63 offen sc1\n\t" \
    "v_mfma_f32_16x16x32_bf16 v[92:95], v[76:79], a[92:95], v[92:95]\n\t" "s_add_u32 s63, s63, 0x20000\n\t" "v_mfma_f32_16x16x32_bf16 v[96:99], v[48:51], a[96:99], 0\n\t" \
    "v_mfma_f32_16x16x32_bf16 v[100:103], v[52:55], a[96:99], 0\n\t" "v_mfma_f32_16x16x32_bf16 v[96:99], v[56:59], a[100:103], v[96:99]\n\t" \
    "v_mfma_f32_16x16x32_bf16 v[100:103], v[60:63], a[100:103], v[100:103]\n\t" "v_mfma_f32_16x16x32_bf16 v[96:99], v[64:67], a[104:107], v[96:99]\n\t" \
    "v_cvt_pk_bf16_f32 v112, v80, v81\n\t" "v_mfma_f32_16x16x32_bf16 v[100:103], v[68:71], a[104:107], v[100:103]\n\t" "v_cvt_pk_bf16_f32 v113, v82, v83\n\t" \
    "v_mfma_f32_16x16x32_bf16 v[96:99], v[72:75], a[108:111], v[96:99]\n\t" "v_cvt_pk_bf16_f32 v114, v84, v85\n\t" \
    "v_mfma_f32_16x16x32_bf16 v[100:103], v[76:79], a[108:111], v[100:103]\n\t" "v_cvt_pk_bf16_f32 v115, v86, v87\n\t" \
    "v_mfma_f32_16x16x32_bf16 v[104:107], v[48:51], a[112:115], 0\n\t" "v_cvt_pk_bf16_f32 v116, v88, v89\n\t" \
    "v_mfma_f32_16x16x32_bf16 v[108:111], v[52:55], a[112:115], 0\n\t" "v_cvt_pk_bf16_f32 v117, v90, v91\n\t" \
    "v_mfma_f32_16x16x32_bf16 v[104:107], v[56:59], a[116:119], v[104:107]\n\t" "buffer_store_dwordx4 v[112:115], %[z1], %[rs], s63 offen sc1\n\t" \
    "v_mfma_f32_16x16x32_bf16 v[108:111], v[60:63], a[116:119], v[108:111]\n\t" "v_mfma_f32_16x16x32_bf16 v[104:107], v[64:67], a[120:123], v[104:107]\n\t" \
    "v_cvt_pk_bf16_f32 v118, v92, v93\n\t" "v_mfma_f32_16x16x32_bf16 v[108:111], v[68:71], a[120:123], v[108:111]\n\t" "v_cvt_pk_bf16_f32 v119, v94, v95\n\t" \
    "v_mfma_f32_16x16x32_bf16 v[104:107], v[72:75], a[124:127], v[104:107]\n\t" "buffer_store_dwordx4 v[116:119], %[z2], %[rs], s63 offen sc1\n\t" \
    "v_mfma_f32_16x16x32_bf16 v[108:111], v[76:79], a[124:127], v[108:111]\n\t" "s_add_u32 s63, s63, 0x20000\n\t" \
    "v_mfma_f32_16x16x32_bf16 v[80:83], v[48:51], a[128:131], 0\n\t" "v_mfma_f32_16x16x32_bf16 v[84:87], v[52:55], a[128:131], 0\n\t" \
    "v_mfma_f32_16x16x32_bf16 v[80:83], v[56:59], a[132:135], v[80:83]\n\t" "v_mfma_f32_16x16x32_bf16 v[84:87], v[60:63], a[132:135], v[84:87]\n\t" \
    "v_mfma_f32_16x16x32_bf16 v[80:83], v[64:67], a[136:139], v[80:83]\n\t" "v_cvt_pk_bf16_f32 v112, v96, v97\n\t" \
    "v_mfma_f32_16x16x32_bf16 v[84:87], v[68:71], a[136:139], v[84:87]\n\t" "v_cvt_pk_bf16_f32 v113, v98, v99\n\t" \
    "v_mfma_f32_16x16x32_bf16 v[80:83], v[72:75], a[140:143], v[80:83]\n\t" "v_cvt_pk_bf16_f32 v114, v100, v101\n\t" \
    "v_mfma_f32_16x16x32_bf16 v[84:87], v[76:79], a[140:143], v[84:87]\n\t" "v_cvt_pk_bf16_f32 v115, v102, v103\n\t" \
    "v_mfma_f32_16x16x32_bf16 v[88:91], v[48:51], a[144:147], 0\n\t" "v_cvt_pk_bf16_f32 v116, v104, v105\n\t" "v_mfma_f32_16x16x32_bf16 v[92:95], v[52:55], a[144:147], 0\n\t" \
    "v_cvt_pk_bf16_f32 v117, v106, v107\n\t" "v_mfma_f32_16x16x32_bf16 v[88:91], v[56:59], a[148:151], v[88:91]\n\t" \
    "buffer_store_dwordx4 v[112:115], %[z1], %[rs], s63 offen sc1\n\t" "v_mfma_f32_16x16x32_bf16 v[92:95], v[60:63], a[148:151], v[92:95]\n\t" \
    "v_mfma_f32_16x16x32_bf16 v[88:91], v[64:67], a[152:155], v[88:91]\n\t" "v_cvt_pk_bf16_f32 v118, v108, v109\n\t" \
    "v_mfma_f32_16x16x32_bf16 v[92:95], v[68:71], a[152:155], v[92:95]\n\t" "v_cvt_pk_bf16_f32 v119, v110, v111\n\t" \
    "v_mfma_f32_16x16x32_bf16 v[88:91], v[72:75], a[156:159], v[88:91]\n\t" "buffer_store_dwordx4 v[116:119], %[z2], %[rs], s63 offen sc1\n\t" \
    "v_mfma_f32_16x16x32_bf16 v[92:95], v[76:79], a[156:159], v[92:95]\n\t" "s_add_u32 s63, s63, 0x20000\n\t" "v_mfma_f32_16x16x32_bf16 v[96:99], v[48:51], a[160:163], 0\n\t" \
    "v_mfma_f32_16x16x32_bf16 v[100:103], v[52:55], a[160:163], 0\n\t" "v_mfma_f32_16x16x32_bf16 v[96:99], v[56:59], a[164:167], v[96:99]\n\t" \
    "v_mfma_f32_16x16x32_bf16 v[100:103], v[60:63], a[164:167], v[100:103]\n\t" "v_mfma_f32_16x16x32_bf16 v[96:99], v[64:67], a[168:171], v[96:99]\n\t" \
    "v_cvt_pk_bf16_f32 v112, v80, v81\n\t" "v_mfma_f32_16x16x32_bf16 v[100:103], v[68:71], a[168:171], v[100:103]\n\t" "v_cvt_pk_bf16_f32 v113, v82, v83\n\t" \
    "v_mfma_f32_16x16x32_bf16 v[96:99], v[72:75], a[172:175], v[96:99]\n\t" "v_cvt_pk_bf16_f32 v114, v84, v85\n\t" \
    "v_mfma_f32_16x16x32_bf16 v[100:103], v[76:79], a[172:175], v[100:103]\n\t" "v_cvt_pk_bf16_f32 v115, v86, v87\n\t" \
    "v_mfma_f32_16x16x32_bf16 v[104:107], v[48:51], a[176:179], 0\n\t" "v_cvt_pk_bf16_f32 v116, v88, v89\n\t" \
    "v_mfma_f32_16x16x32_bf16 v[108:111], v[52:55], a[176:179], 0\n\t" "v_cvt_pk_bf16_f32 v117, v90, v91\n\t" \
    "v_mfma_f32_16x16x32_bf16 v[104:107], v[56:59], a[180:183], v[104:107]\n\t" "buffer_store_dwordx4 v[112:115], %[z1], %[rs], s63 offen sc1\n\t" \
    "v_mfma_f32_16x16x32_bf16 v[108:111], v[60:63], a[180:183], v[108:111]\n\t" "v_mfma_f32_16x16x32_bf16 v[104:107], v[64:67], a[184:187], v[104:107]\n\t" \
    "v_cvt_pk_bf16_f32 v118, v92, v93\n\t" "v_mfma_f32_16x16x32_bf16 v[108:111], v[68:71], a[184:187], v[108:111]\n\t" "v_cvt_pk_bf16_f32 v119, v94, v95\n\t" \
    "v_mfma_f32_16x16x32_bf16 v[104:107], v[72:75], a[188:191], v[104:107]\n\t" "buffer_store_dwordx4 v[116:119], %[z2], %[rs], s63 offen sc1\n\t" \
    "v_mfma_f32_16x16x32_bf16 v[108:111], v[76:79], a[188:191], v[108:111]\n\t" "s_add_u32 s63, s63, 0x20000\n\t" \
    "v_mfma_f32_16x16x32_bf16 v[80:83], v[48:51], a[192:195], 0\n\t" "v_mfma_f32_16x16x32_bf16 v[84:87], v[52:55], a[192:195], 0\n\t" \
    "v_mfma_f32_16x16x32_bf16 v[80:83], v[56:59], a[196:199], v[80:83]\n\t" "v_mfma_f32_16x16x32_bf16 v[84:87], v[60:63], a[196:199], v[84:87]\n\t" \
    "v_mfma_f32_16x16x32_bf16 v[80:83], v[64:67], a[200:203], v[80:83]\n\t" "v_cvt_pk_bf16_f32 v112, v96, v97\n\t" \
    "v_mfma_f32_16x16x32_bf16 v[84:87], v[68:71], a[200:203], v[84:87]\n\t" "v_cvt_pk_bf16_f32 v113, v98, v99\n\t" \
    "v_mfma_f32_16x16x32_bf16 v[80:83], v[72:75], a[204:207], v[80:83]\n\t" "v_cvt_pk_bf16_f32 v114, v100, v101\n\t" \
    "v_mfma_f32_16x16x32_bf16 v[84:87], v[76:79], a[204:207], v[84:87]\n\t" "v_cvt_pk_bf16_f32 v115, v102, v103\n\t" \
    "v_mfma_f32_16x16x32_bf16 v[88:91], v[48:51], a[208:211], 0\n\t" "v_cvt_pk_bf16_f32 v116, v104, v105\n\t" "v_mfma_f32_16x16x32_bf16 v[92:95], v[52:55], a[208:211], 0\n\t" \
    "v_cvt_pk_bf16_f32 v117, v106, v107\n\t" "v_mfma_f32_16x16x32_bf16 v[88:91], v[56:59], a[212:215], v[88:91]\n\t" \
    "buffer_store_dwordx4 v[112:115], %[z1], %[rs], s63 offen sc1\n\t" "v_mfma_f32_16x16x32_bf16 v[92:95], v[60:63], a[212:215], v[92:95]\n\t" \
    "v_mfma_f32_16x16x32_bf16 v[88:91], v[64:67], a[216:219], v[88:91]\n\t" "v_cvt_pk_bf16_f32 v118, v108, v109\n\t" \
    "v_mfma_f32_16x16x32_bf16 v[92:95], v[68:71], a[216:219], v[92:95]\n\t" "v_cvt_pk_bf16_f32 v119, v110, v111\n\t" \
    "v_mfma_f32_16x16x32_bf16 v[88:91], v[72:75], a[220:223], v[88:91]\n\t" "buffer_store_dwordx4 v[116:119], %[z2], %[rs], s63 offen sc1\n\t" \
    "v_mfma_f32_16x16x32_bf16 v[92:95], v[76:79], a[220:223], v[92:95]\n\t" "s_add_u32 s63, s63, 0x20000\n\t" "v_mfma_f32_16x16x32_bf16 v[96:99], v[48:51], a[224:227], 0\n\t" \
    "v_mfma_f32_16x16x32_bf16 v[100:103], v[52:55], a[224:227], 0\n\t" "v_mfma_f32_16x16x32_bf16 v[96:99], v[56:59], a[228:231], v[96:99]\n\t" \
    "v_mfma_f32_16x16x32_bf16 v[100:103], v[60:63], a[228:231], v[100:103]\n\t" "v_mfma_f32_16x16x32_bf16 v[96:99], v[64:67], a[232:235], v[96:99]\n\t" \
    "v_cvt_pk_bf16_f32 v112, v80, v81\n\t" "v_mfma_f32_16x16x32_bf16 v[100:103], v[68:71], a[232:235], v[100:103]\n\t" "v_cvt_pk_bf16_f32 v113, v82, v83\n\t" \
    "v_mfma_f32_16x16x32_bf16 v[96:99], v[72:75], a[236:239], v[96:99]\n\t" "v_cvt_pk_bf16_f32 v114, v84, v85\n\t" \
    "v_mfma_f32_16x16x32_bf16 v[100:103], v[76:79], a[236:239], v[100:103]\n\t" "v_cvt_pk_bf16_f32 v115, v86, v87\n\t" \
    "v_mfma_f32_16x16x32_bf16 v[104:107], v[48:51], a[240:243], 0\n\t" "v_cvt_pk_bf16_f32 v116, v88, v89\n\t" \
    "v_mfma_f32_16x16x32_bf16 v[108:111], v[52:55], a[240:243], 0\n\t" "v_cvt_pk_bf16_f32 v117, v90, v91\n\t" \
    "v_mfma_f32_16x16x32_bf16 v[104:107], v[56:59], a[244:247], v[104:107]\n\t" "buffer_store_dwordx4 v[112:115], %[z1], %[rs], s63 offen sc1\n\t" \
    "v_mfma_f32_16x16x32_bf16 v[108:111], v[60:63], a[244:247], v[108:111]\n\t" "v_mfma_f32_16x16x32_bf16 v[104:107], v[64:67], a[248:251], v[104:107]\n\t" \
    "v_cvt_pk_bf16_f32 v118, v92, v93\n\t" "v_cvt_pk_bf16_f32 v119, v94, v95\n\t" "s_waitcnt vmcnt(11)\n\t" "v_readfirstlane_b32 s66, v120\n\t" \
    "v_mfma_f32_16x16x32_bf16 v[108:111], v[68:71], a[248:251], v[108:111]\n\t" "v_readfirstlane_b32 s67, v121\n\t" "v_mov_b32 v122, s60\n\t" \
    "buffer_store_dword v122, %[fo], %[rc], %[fz] offen sc1\n\t" "v_mfma_f32_16x16x32_bf16 v[104:107], v[72:75], a[252:255], v[104:107]\n\t" \
    "buffer_store_dwordx4 v[116:119], %[z2], %[rs], s63 offen sc1\n\t" "s_add_u32 s63, s63, 0x20000\n\t" "s_max_u32 s61, s61, s66\n\t" \
    "v_mfma_f32_16x16x32_bf16 v[108:111], v[76:79], a[252:255], v[108:111]\n\t" "s_max_u32 s62, s62, s67\n\t" "s_add_u32 s60, s60, 1\n\t" ZSUM "33:\n\t" "s_branch 10b\n\t" \
    "40:\n\t" "v_mfma_f32_16x16x32_bf16 v[80:83], v[16:19], a[0:3], 0\n\t" "v_mfma_f32_16x16x32_bf16 v[84:87], v[20:23], a[0:3], 0\n\t" \
    "v_mfma_f32_16x16x32_bf16 v[80:83], v[24:27], a[4:7], v[80:83]\n\t" "v_mfma_f32_16x16x32_bf16 v[84:87], v[28:31], a[4:7], v[84:87]\n\t" \
    "v_mfma_f32_16x16x32_bf16 v[80:83], v[32:35], a[8:11], v[80:83]\n\t" "v_cvt_pk_bf16_f32 v112, v96, v97\n\t" \
    "v_mfma_f32_16x16x32_bf16 v[84:87], v[36:39], a[8:11], v[84:87]\n\t" "v_cvt_pk_bf16_f32 v113, v98, v99\n\t" \
    "v_mfma_f32_16x16x32_bf16 v[80:83], v[40:43], a[12:15], v[80:83]\n\t" "v_cvt_pk_bf16_f32 v114, v100, v101\n\t" \
    "v_mfma_f32_16x16x32_bf16 v[84:87], v[44:47], a[12:15], v[84:87]\n\t" "v_cvt_pk_bf16_f32 v115, v102, v103\n\t" \
    "v_mfma_f32_16x16x32_bf16 v[88:91], v[16:19], a[16:19], 0\n\t" "v_cvt_pk_bf16_f32 v116, v104, v105\n\t" "v_mfma_f32_16x16x32_bf16 v[92:95], v[20:23], a[16:19], 0\n\t" \
    "v_cvt_pk_bf16_f32 v117, v106, v107\n\t" "v_mfma_f32_16x16x32_bf16 v[88:91], v[24:27], a[20:23], v[88:91]\n\t" \
    "buffer_store_dwordx4 v[112:115], %[z1], %[rs], s63 offen sc1\n\t" "v_mfma_f32_16x16x32_bf16 v[92:95], v[28:31], a[20:23], v[92:95]\n\t" \
    "v_mfma_f32_16x16x32_bf16 v[88:91], v[32:35], a[24:27], v[88:91]\n\t" "v_cvt_pk_bf16_f32 v118, v108, v109\n\t" \
    "v_mfma_f32_16x16x32_bf16 v[92:95], v[36:39], a[24:27], v[92:95]\n\t" "v_cvt_pk_bf16_f32 v119, v110, v111\n\t" \
    "v_mfma_f32_16x16x32_bf16 v[88:91], v[40:43], a[28:31], v[88:91]\n\t" "buffer_store_dwordx4 v[116:119], %[z2], %[rs], s63 offen sc1\n\t" "s_and_b32 s63, s60, %[rm]\n\t" \
    "v_mfma_f32_16x16x32_bf16 v[92:95], v[44:47], a[28:31], v[92:95]\n\t" "s_lshl_b32 s63, s63, 22\n\t" "s_add_u32 s63, s63, %[zb]\n\t" \
    "v_mfma_f32_16x16x32_bf16 v[96:99], v[16:19], a[32:35], 0\n\t" "buffer_load_dword v120, %[fo], %[rc], %[fv] offen sc1\n\t" \
    "v_mfma_f32_16x16x32_bf16 v[100:103], v[20:23], a[32:35], 0\n\t" "v_mfma_f32_16x16x32_bf16 v[96:99], v[24:27], a[36:39], v[96:99]\n\t" \
    "buffer_load_dword v121, %[fo], %[rc], %[fs] offen sc1\n\t" "v_mfma_f32_16x16x32_bf16 v[100:103], v[28:31], a[36:39], v[100:103]\n\t" \
    "v_mfma_f32_16x16x32_bf16 v[96:99], v[32:35], a[40:43], v[96:99]\n\t" "v_cvt_pk_bf16_f32 v112, v80, v81\n\t" \
    "v_mfma_f32_16x16x32_bf16 v[100:103], v[36:39], a[40:43], v[100:103]\n\t" "v_cvt_pk_bf16_f32 v113, v82, v83\n\t" \
    "v_mfma_f32_16x16x32_bf16 v[96:99], v[40:43], a[44:47], v[96:99]\n\t" "v_cvt_pk_bf16_f32 v114, v84, v85\n\t" \
    "v_mfma_f32_16x16x32_bf16 v[100:103], v[44:47], a[44:47], v[100:103]\n\t" "v_cvt_pk_bf16_f32 v115, v86, v87\n\t" \
    "v_mfma_f32_16x16x32_bf16 v[104:107], v[16:19], a[48:51], 0\n\t" "v_cvt_pk_bf16_f32 v116, v88, v89\n\t" "v_mfma_f32_16x16x32_bf16 v[108:111], v[20:23], a[48:51], 0\n\t" \
    "v_cvt_pk_bf16_f32 v117, v90, v91\n\t" "v_mfma_f32_16x16x32_bf16 v[104:107], v[24:27], a[52:55], v[104:107]\n\t" \
    "buffer_store_dwordx4 v[112:115], %[z1], %[rs], s63 offen sc1\n\t" "v_mfma_f32_16x16x32_bf16 v[108:111], v[28:31], a[52:55], v[108:111]\n\t" \
    "v_mfma_f32_16x16x32_bf16 v[104:107], v[32:35], a[56:59], v[104:107]\n\t" "v_cvt_pk_bf16_f32 v118, v92, v93\n\t" \
    "v_mfma_f32_16x16x32_bf16 v[108:111], v[36:39], a[56:59], v[108:111]\n\t" "v_cvt_pk_bf16_f32 v119, v94, v95\n\t" \
    "v_mfma_f32_16x16x32_bf16 v[104:107], v[40:43], a[60:63], v[104:107]\n\t" "buffer_store_dwordx4 v[116:119], %[z2], %[rs], s63 offen sc1\n\t" \
    "v_mfma_f32_16x16x32_bf16 v[108:111], v[44:47], a[60:63], v[108:111]\n\t" "s_add_u32 s63, s63, 0x20000\n\t" "v_mfma_f32_16x16x32_bf16 v[80:83], v[16:19], a[64:67], 0\n\t" \
    "s_add_u32 s65, s60, 1\n\t" "s_lshr_b32 s66, s65, 2\n\t" "v_mfma_f32_16x16x32_bf16 v[84:87], v[20:23], a[64:67], 0\n\t" "s_xor_b32 s66, s66, %[xp]\n\t" \
    "s_and_b32 s66, s66, 1\n\t" "v_mfma_f32_16x16x32_bf16 v[80:83], v[24:27], a[68:71], v[80:83]\n\t" "v_mfma_f32_16x16x32_bf16 v[84:87], v[28:31], a[68:71], v[84:87]\n\t" \
    "v_mfma_f32_16x16x32_bf16 v[80:83], v[32:35], a[72:75], v[80:83]\n\t" "v_cvt_pk_bf16_f32 v112, v96, v97\n\t" \
    "v_mfma_f32_16x16x32_bf16 v[84:87], v[36:39], a[72:75], v[84:87]\n\t" "v_cvt_pk_bf16_f32 v113, v98, v99\n\t" \
    "v_mfma_f32_16x16x32_bf16 v[80:83], v[40:43], a[76:79], v[80:83]\n\t" "v_cvt_pk_bf16_f32 v114, v100, v101\n\t" \
    "v_mfma_f32_16x16x32_bf16 v[84:87], v[44:47], a[76:79], v[84:87]\n\t" "v_cvt_pk_bf16_f32 v115, v102, v103\n\t" \
    "v_mfma_f32_16x16x32_bf16 v[88:91], v[16:19], a[80:83], 0\n\t" "v_cvt_pk_bf16_f32 v116, v104, v105\n\t" "v_mfma_f32_16x16x32_bf16 v[92:95], v[20:23], a[80:83], 0\n\t" \
    "v_cvt_pk_bf16_f32 v117, v106, v107\n\t" "v_mfma_f32_16x16x32_bf16 v[88:91], v[24:27], a[84:87], v[88:91]\n\t" \
    "buffer_store_dwordx4 v[112:115], %[z1], %[rs], s63 offen sc1\n\t" "v_mfma_f32_16x16x32_bf16 v[92:95], v[28:31], a[84:87], v[92:95]\n\t" \
    "v_mfma_f32_16x16x32_bf16 v[88:91], v[32:35], a[88:91], v[88:91]\n\t" "v_cvt_pk_bf16_f32 v118, v108, v109\n\t" \
    "v_mfma_f32_16x16x32_bf16 v[92:95], v[36:39], a[88:91], v[92:95]\n\t" "v_cvt_pk_bf16_f32 v119, v110, v111\n\t" \
    "v_mfma_f32_16x16x32_bf16 v[88:91], v[40:43], a[92:95], v[88:91]\n\t" "buffer_store_dwordx4 v[116:119], %[z2], %[rs], s63 offen sc1\n\t" \
    "v_mfma_f32_16x16x32_bf16 v[92:95], v[44:47], a[92:95], v[92:95]\n\t" "s_add_u32 s63, s63, 0x20000\n\t" "v_mfma_f32_16x16x32_bf16 v[96:99], v[16:19], a[96:99], 0\n\t" \
    "s_lshl_b32 s66, s66, 13\n\t" "s_lshl_b32 s64, s65, 19\n\t" "v_mfma_f32_16x16x32_bf16 v[100:103], v[20:23], a[96:99], 0\n\t" "s_add_u32 s64, s64, s66\n\t" \
    "s_add_u32 s64, s64, %[vb]\n\t" "v_mfma_f32_16x16x32_bf16 v[96:99], v[24:27], a[100:103], v[96:99]\n\t" \
    "v_mfma_f32_16x16x32_bf16 v[100:103], v[28:31], a[100:103], v[100:103]\n\t" "v_mfma_f32_16x16x32_bf16 v[96:99], v[32:35], a[104:107], v[96:99]\n\t" \
    "v_cvt_pk_bf16_f32 v112, v80, v81\n\t" "v_mfma_f32_16x16x32_bf16 v[100:103], v[36:39], a[104:107], v[100:103]\n\t" "v_cvt_pk_bf16_f32 v113, v82, v83\n\t" \
    "v_mfma_f32_16x16x32_bf16 v[96:99], v[40:43], a[108:111], v[96:99]\n\t" "v_cvt_pk_bf16_f32 v114, v84, v85\n\t" \
    "v_mfma_f32_16x16x32_bf16 v[100:103], v[44:47], a[108:111], v[100:103]\n\t" "v_cvt_pk_bf16_f32 v115, v86, v87\n\t" \
    "v_mfma_f32_16x16x32_bf16 v[104:107], v[16:19], a[112:115], 0\n\t" "v_cvt_pk_bf16_f32 v116, v88, v89\n\t" \
    "v_mfma_f32_16x16x32_bf16 v[108:111], v[20:23], a[112:115], 0\n\t" "v_cvt_pk_bf16_f32 v117, v90, v91\n\t" \
    "v_mfma_f32_16x16x32_bf16 v[104:107], v[24:27], a[116:119], v[104:107]\n\t" "buffer_store_dwordx4 v[112:115], %[z1], %[rs], s63 offen sc1\n\t" \
    "v_mfma_f32_16x16x32_bf16 v[108:111], v[28:31], a[116:119], v[108:111]\n\t" "v_mfma_f32_16x16x32_bf16 v[104:107], v[32:35], a[120:123], v[104:107]\n\t" \
    "v_cvt_pk_bf16_f32 v118, v92, v93\n\t" "v_mfma_f32_16x16x32_bf16 v[108:111], v[36:39], a[120:123], v[108:111]\n\t" "v_cvt_pk_bf16_f32 v119, v94, v95\n\t" \
    "v_mfma_f32_16x16x32_bf16 v[104:107], v[40:43], a[124:127], v[104:107]\n\t" "buffer_store_dwordx4 v[116:119], %[z2], %[rs], s63 offen sc1\n\t" \
    "v_mfma_f32_16x16x32_bf16 v[108:111], v[44:47], a[124:127], v[108:111]\n\t" "s_add_u32 s63, s63, 0x20000\n\t" \
    "v_mfma_f32_16x16x32_bf16 v[80:83], v[16:19], a[128:131], 0\n\t" "s_waitcnt vmcnt(6)\n\t" "v_readfirstlane_b32 s66, v120\n\t" "v_readfirstlane_b32 s67, v121\n\t" \
    "v_mfma_f32_16x16x32_bf16 v[84:87], v[20:23], a[128:131], 0\n\t" "v_mov_b32 v122, s60\n\t" "buffer_store_dword v122, %[fo], %[rc], %[fz] offen sc1\n\t" \
    "v_mfma_f32_16x16x32_bf16 v[80:83], v[24:27], a[132:135], v[80:83]\n\t" "s_max_u32 s61, s61, s66\n\t" "s_max_u32 s62, s62, s67\n\t" "s_cmp_gt_u32 s61, s65\n\t" \
    "v_mfma_f32_16x16x32_bf16 v[84:87], v[28:31], a[132:135], v[84:87]\n\t" "s_cselect_b32 s65, 1, 0\n\t" "s_cselect_b32 s64, s64, 0x7f000000\n\t" \
    "v_mfma_f32_16x16x32_bf16 v[80:83], v[32:35], a[136:139], v[80:83]\n\t" "buffer_load_dwordx4 v[48:51], %[ao], %[rs], s64 offen offset:0 sc1\n\t" \
    "v_cvt_pk_bf16_f32 v112, v96, v97\n\t" "v_mfma_f32_16x16x32_bf16 v[84:87], v[36:39], a[136:139], v[84:87]\n\t" "v_cvt_pk_bf16_f32 v113, v98, v99\n\t" \
    "v_mfma_f32_16x16x32_bf16 v[80:83], v[40:43], a[140:143], v[80:83]\n\t" "buffer_load_dwordx4 v[52:55], %[ao], %[rs], s64 offen offset:256 sc1\n\t" \
    "v_cvt_pk_bf16_f32 v114, v100, v101\n\t" "v_mfma_f32_16x16x32_bf16 v[84:87], v[44:47], a[140:143], v[84:87]\n\t" "v_cvt_pk_bf16_f32 v115, v102, v103\n\t" \
    "v_mfma_f32_16x16x32_bf16 v[88:91], v[16:19], a[144:147], 0\n\t" "buffer_load_dwordx4 v[56:59], %[ao], %[rs], s64 offen offset:2048 sc1\n\t" \
    "v_cvt_pk_bf16_f32 v116, v104, v105\n\t" "v_mfma_f32_16x16x32_bf16 v[92:95], v[20:23], a[144:147], 0\n\t" "v_cvt_pk_bf16_f32 v117, v106, v107\n\t" \
    "v_mfma_f32_16x16x32_bf16 v[88:91], v[24:27], a[148:151], v[88:91]\n\t" "buffer_load_dwordx4 v[60:63], %[ao], %[rs], s64 offen offset:2304 sc1\n\t" \
    "v_mfma_f32_16x16x32_bf16 v[92:95], v[28:31], a[148:151], v[92:95]\n\t" "buffer_store_dwordx4 v[112:115], %[z1], %[rs], s63 offen sc1\n\t" \
    "v_mfma_f32_16x16x32_bf16 v[88:91], v[32:35], a[152:155], v[88:91]\n\t" "buffer_load_dwordx4 v[64:67], %[a1], %[rs], s64 offen offset:0 sc1\n\t" \
    "v_cvt_pk_bf16_f32 v118, v108, v109\n\t" "v_mfma_f32_16x16x32_bf16 v[92:95], v[36:39], a[152:155], v[92:95]\n\t" "v_cvt_pk_bf16_f32 v119, v110, v111\n\t" \
    "v_mfma_f32_16x16x32_bf16 v[88:91], v[40:43], a[156:159], v[88:91]\n\t" "buffer_load_dwordx4 v[68:71], %[a1], %[rs], s64 offen offset:256 sc1\n\t" \
    "buffer_store_dwordx4 v[116:119], %[z2], %[rs], s63 offen sc1\n\t" "v_mfma_f32_16x16x32_bf16 v[92:95], v[44:47], a[156:159], v[92:95]\n\t" \
    "s_add_u32 s63, s63, 0x20000\n\t" "v_mfma_f32_16x16x32_bf16 v[96:99], v[16:19], a[160:163], 0\n\t" \
    "buffer_load_dwordx4 v[72:75], %[a1], %[rs], s64 offen offset:2048 sc1\n\t" "v_mfma_f32_16x16x32_bf16 v[100:103], v[20:23], a[160:163], 0\n\t" \
    "v_mfma_f32_16x16x32_bf16 v[96:99], v[24:27], a[164:167], v[96:99]\n\t" "buffer_load_dwordx4 v[76:79], %[a1], %[rs], s64 offen offset:2304 sc1\n\t" \
    "v_mfma_f32_16x16x32_bf16 v[100:103], v[28:31], a[164:167], v[100:103]\n\t" "v_mfma_f32_16x16x32_bf16 v[96:99], v[32:35], a[168:171], v[96:99]\n\t" \
    "v_cvt_pk_bf16_f32 v112, v80, v81\n\t" "v_mfma_f32_16x16x32_bf16 v[100:103], v[36:39], a[168:171], v[100:103]\n\t" "v_cvt_pk_bf16_f32 v113, v82, v83\n\t" \
    "v_mfma_f32_16x16x32_bf16 v[96:99], v[40:43], a[172:175], v[96:99]\n\t" "v_cvt_pk_bf16_f32 v114, v84, v85\n\t" \
    "v_mfma_f32_16x16x32_bf16 v[100:103], v[44:47], a[172:175], v[100:103]\n\t" "v_cvt_pk_bf16_f32 v115, v86, v87\n\t" \
    "v_mfma_f32_16x16x32_bf16 v[104:107], v[16:19], a[176:179], 0\n\t" "v_cvt_pk_bf16_f32 v116, v88, v89\n\t" \
    "v_mfma_f32_16x16x32_bf16 v[108:111], v[20:23], a[176:179], 0\n\t" "v_cvt_pk_bf16_f32 v117, v90, v91\n\t" \
    "v_mfma_f32_16x16x32_bf16 v[104:107], v[24:27], a[180:183], v[104:107]\n\t" "buffer_store_dwordx4 v[112:115], %[z1], %[rs], s63 offen sc1\n\t" \
    "v_mfma_f32_16x16x32_bf16 v[108:111], v[28:31], a[180:183], v[108:111]\n\t" "v_mfma_f32_16x16x32_bf16 v[104:107], v[32:35], a[184:187], v[104:107]\n\t" \
    "v_cvt_pk_bf16_f32 v118, v92, v93\n\t" "v_mfma_f32_16x16x32_bf16 v[108:111], v[36:39], a[184:187], v[108:111]\n\t" "v_cvt_pk_bf16_f32 v119, v94, v95\n\t" \
    "v_mfma_f32_16x16x32_bf16 v[104:107], v[40:43], a[188:191], v[104:107]\n\t" "buffer_store_dwordx4 v[116:119], %[z2], %[rs], s63 offen sc1\n\t" \
    "v_mfma_f32_16x16x32_bf16 v[108:111], v[44:47], a[188:191], v[108:111]\n\t" "s_add_u32 s63, s63, 0x20000\n\t" \
    "v_mfma_f32_16x16x32_bf16 v[80:83], v[16:19], a[192:195], 0\n\t" "v_mfma_f32_16x16x32_bf16 v[84:87], v[20:23], a[192:195], 0\n\t" \
    "v_mfma_f32_16x16x32_bf16 v[80:83], v[24:27], a[196:199], v[80:83]\n\t" "v_mfma_f32_16x16x32_bf16 v[84:87], v[28:31], a[196:199], v[84:87]\n\t" \
    "v_mfma_f32_16x16x32_bf16 v[80:83], v[32:35], a[200:203], v[80:83]\n\t" "v_cvt_pk_bf16_f32 v112, v96, v97\n\t" \
    "v_mfma_f32_16x16x32_bf16 v[84:87], v[36:39], a[200:203], v[84:87]\n\t" "v_cvt_pk_bf16_f32 v113, v98, v99\n\t" \
    "v_mfma_f32_16x16x32_bf16 v[80:83], v[40:43], a[204:207], v[80:83]\n\t" "v_cvt_pk_bf16_f32 v114, v100, v101\n\t" \
    "v_mfma_f32_16x16x32_bf16 v[84:87], v[44:47], a[204:207], v[84:87]\n\t" "v_cvt_pk_bf16_f32 v115, v102, v103\n\t" \
    "v_mfma_f32_16x16x32_bf16 v[88:91], v[16:19], a[208:211], 0\n\t" "v_cvt_pk_bf16_f32 v116, v104, v105\n\t" "v_mfma_f32_16x16x32_bf16 v[92:95], v[20:23], a[208:211], 0\n\t" \
    "v_cvt_pk_bf16_f32 v117, v106, v107\n\t" "v_mfma_f32_16x16x32_bf16 v[88:91], v[24:27], a[212:215], v[88:91]\n\t" \
    "buffer_store_dwordx4 v[112:115], %[z1], %[rs], s63 offen sc1\n\t" "v_mfma_f32_16x16x32_bf16 v[92:95], v[28:31], a[212:215], v[92:95]\n\t" \
    "v_mfma_f32_16x16x32_bf16 v[88:91], v[32:35], a[216:219], v[88:91]\n\t" "v_cvt_pk_bf16_f32 v118, v108, v109\n\t" \
    "v_mfma_f32_16x16x32_bf16 v[92:95], v[36:39], a[216:219], v[92:95]\n\t" "v_cvt_pk_bf16_f32 v119, v110, v111\n\t" \
    "v_mfma_f32_16x16x32_bf16 v[88:91], v[40:43], a[220:223], v[88:91]\n\t" "buffer_store_dwordx4 v[116:119], %[z2], %[rs], s63 offen sc1\n\t" \
    "v_mfma_f32_16x16x32_bf16 v[92:95], v[44:47], a[220:223], v[92:95]\n\t" "s_add_u32 s63, s63, 0x20000\n\t" "v_mfma_f32_16x16x32_bf16 v[96:99], v[16:19], a[224:227], 0\n\t" \
    "v_mfma_f32_16x16x32_bf16 v[100:103], v[20:23], a[224:227], 0\n\t" "v_mfma_f32_16x16x32_bf16 v[96:99], v[24:27], a[228:231], v[96:99]\n\t" \
    "v_mfma_f32_16x16x32_bf16 v[100:103], v[28:31], a[228:231], v[100:103]\n\t" "v_mfma_f32_16x16x32_bf16 v[96:99], v[32:35], a[232:235], v[96:99]\n\t" \
    "v_cvt_pk_bf16_f32 v112, v80, v81\n\t" "v_mfma_f32_16x16x32_bf16 v[100:103], v[36:39], a[232:235], v[100:103]\n\t" "v_cvt_pk_bf16_f32 v113, v82, v83\n\t" \
    "v_mfma_f32_16x16x32_bf16 v[96:99], v[40:43], a[236:239], v[96:99]\n\t" "v_cvt_pk_bf16_f32 v114, v84, v85\n\t" \
    "v_mfma_f32_16x16x32_bf16 v[100:103], v[44:47], a[236:239], v[100:103]\n\t" "v_cvt_pk_bf16_f32 v115, v86, v87\n\t" \
    "v_mfma_f32_16x16x32_bf16 v[104:107], v[16:19], a[240:243], 0\n\t" "v_cvt_pk_bf16_f32 v116, v88, v89\n\t" \
    "v_mfma_f32_16x16x32_bf16 v[108:111], v[20:23], a[240:243], 0\n\t" "v_cvt_pk_bf16_f32 v117, v90, v91\n\t" \
    "v_mfma_f32_16x16x32_bf16 v[104:107], v[24:27], a[244:247], v[104:107]\n\t" "buffer_store_dwordx4 v[112:115], %[z1], %[rs], s63 offen sc1\n\t" \
    "v_mfma_f32_16x16x32_bf16 v[108:111], v[28:31], a[244:247], v[108:111]\n\t" "v_mfma_f32_16x16x32_bf16 v[104:107], v[32:35], a[248:251], v[104:107]\n\t" \
    "v_cvt_pk_bf16_f32 v118, v92, v93\n\t" "v_mfma_f32_16x16x32_bf16 v[108:111], v[36:39], a[248:251], v[108:111]\n\t" "v_cvt_pk_bf16_f32 v119, v94, v95\n\t" \
    "v_mfma_f32_16x16x32_bf16 v[104:107], v[40:43], a[252:255], v[104:107]\n\t" "buffer_store_dwordx4 v[116:119], %[z2], %[rs], s63 offen sc1\n\t" \
    "s_add_u32 s63, s63, 0x20000\n\t" "s_add_u32 s60, s60, 1\n\t" "v_mfma_f32_16x16x32_bf16 v[108:111], v[44:47], a[252:255], v[108:111]\n\t" "s_cmp_eq_u32 s65, 0\n\t" \
    "s_waitcnt vmcnt(6)\n\t" ZSUM "s_cbranch_scc0 23b\n\t" "s_branch 20f\n\t" "41:\n\t" "v_mfma_f32_16x16x32_bf16 v[80:83], v[48:51], a[0:3], 0\n\t" \
    "v_mfma_f32_16x16x32_bf16 v[84:87], v[52:55], a[0:3], 0\n\t" "v_mfma_f32_16x16x32_bf16 v[80:83], v[56:59], a[4:7], v[80:83]\n\t" \
    "v_mfma_f32_16x16x32_bf16 v[84:87], v[60:63], a[4:7], v[84:87]\n\t" "v_mfma_f32_16x16x32_bf16 v[80:83], v[64:67], a[8:11], v[80:83]\n\t" \
    "v_cvt_pk_bf16_f32 v112, v96, v97\n\t" "v_mfma_f32_16x16x32_bf16 v[84:87], v[68:71], a[8:11], v[84:87]\n\t" "v_cvt_pk_bf16_f32 v113, v98, v99\n\t" \
    "v_mfma_f32_16x16x32_bf16 v[80:83], v[72:75], a[12:15], v[80:83]\n\t" "v_cvt_pk_bf16_f32 v114, v100, v101\n\t" \
    "v_mfma_f32_16x16x32_bf16 v[84:87], v[76:79], a[12:15], v[84:87]\n\t" "v_cvt_pk_bf16_f32 v115, v102, v103\n\t" \
    "v_mfma_f32_16x16x32_bf16 v[88:91], v[48:51], a[16:19], 0\n\t" "v_cvt_pk_bf16_f32 v116, v104, v105\n\t" "v_mfma_f32_16x16x32_bf16 v[92:95], v[52:55], a[16:19], 0\n\t" \
    "v_cvt_pk_bf16_f32 v117, v106, v107\n\t" "v_mfma_f32_16x16x32_bf16 v[88:91], v[56:59], a[20:23], v[88:91]\n\t" \
    "buffer_store_dwordx4 v[112:115], %[z1], %[rs], s63 offen sc1\n\t" "v_mfma_f32_16x16x32_bf16 v[92:95], v[60:63], a[20:23], v[92:95]\n\t" \
    "v_mfma_f32_16x16x32_bf16 v[88:91], v[64:67], a[24:27], v[88:91]\n\t" "v_cvt_pk_bf16_f32 v118, v108, v109\n\t" \
    "v_mfma_f32_16x16x32_bf16 v[92:95], v[68:71], a[24:27], v[92:95]\n\t" "v_cvt_pk_bf16_f32 v119, v110, v111\n\t" \
    "v_mfma_f32_16x16x32_bf16 v[88:91], v[72:75], a[28:31], v[88:91]\n\t" "buffer_store_dwordx4 v[116:119], %[z2], %[rs], s63 offen sc1\n\t" "s_and_b32 s63, s60, %[rm]\n\t" \
    "v_mfma_f32_16x16x32_bf16 v[92:95], v[76:79], a[28:31], v[92:95]\n\t" "s_lshl_b32 s63, s63, 22\n\t" "s_add_u32 s63, s63, %[zb]\n\t" \
    "v_mfma_f32_16x16x32_bf16 v[96:99], v[48:51], a[32:35], 0\n\t" "buffer_load_dword v120, %[fo], %[rc], %[fv] offen sc1\n\t" \
    "v_mfma_f32_16x16x32_bf16 v[100:103], v[52:55], a[32:35], 0\n\t" "v_mfma_f32_16x16x32_bf16 v[96:99], v[56:59], a[36:39], v[96:99]\n\t" \
    "buffer_load_dword v121, %[fo], %[rc], %[fs] offen sc1\n\t" "v_mfma_f32_16x16x32_bf16 v[100:103], v[60:63], a[36:39], v[100:103]\n\t" \
    "v_mfma_f32_16x16x32_bf16 v[96:99], v[64:67], a[40:43], v[96:99]\n\t" "v_cvt_pk_bf16_f32 v112, v80, v81\n\t" \
    "v_mfma_f32_16x16x32_bf16 v[100:103], v[68:71], a[40:43], v[100:103]\n\t" "v_cvt_pk_bf16_f32 v113, v82, v83\n\t" \
    "v_mfma_f32_16x16x32_bf16 v[96:99], v[72:75], a[44:47], v[96:99]\n\t" "v_cvt_pk_bf16_f32 v114, v84, v85\n\t" \
    "v_mfma_f32_16x16x32_bf16 v[100:103], v[76:79], a[44:47], v[100:103]\n\t" "v_cvt_pk_bf16_f32 v115, v86, v87\n\t" \
    "v_mfma_f32_16x16x32_bf16 v[104:107], v[48:51], a[48:51], 0\n\t" "v_cvt_pk_bf16_f32 v116, v88, v89\n\t" "v_mfma_f32_16x16x32_bf16 v[108:111], v[52:55], a[48:51], 0\n\t" \
    "v_cvt_pk_bf16_f32 v117, v90, v91\n\t" "v_mfma_f32_16x16x32_bf16 v[104:107], v[56:59], a[52:55], v[104:107]\n\t" \
    "buffer_store_dwordx4 v[112:115], %[z1], %[rs], s63 offen sc1\n\t" "v_mfma_f32_16x16x32_bf16 v[108:111], v[60:63], a[52:55], v[108:111]\n\t" \
    "v_mfma_f32_16x16x32_bf16 v[104:107], v[64:67], a[56:59], v[104:107]\n\t" "v_cvt_pk_bf16_f32 v118, v92, v93\n\t" \
    "v_mfma_f32_16x16x32_bf16 v[108:111], v[68:71], a[56:59], v[108:111]\n\t" "v_cvt_pk_bf16_f32 v119, v94, v95\n\t" \
    "v_mfma_f32_16x16x32_bf16 v[104:107], v[72:75], a[60:63], v[104:107]\n\t" "buffer_store_dwordx4 v[116:119], %[z2], %[rs], s63 offen sc1\n\t" \
    "v_mfma_f32_16x16x32_bf16 v[108:111], v[76:79], a[60:63], v[108:111]\n\t" "s_add_u32 s63, s63, 0x20000\n\t" "v_mfma_f32_16x16x32_bf16 v[80:83], v[48:51], a[64:67], 0\n\t" \
    "s_add_u32 s65, s60, 1\n\t" "s_lshr_b32 s66, s65, 2\n\t" "v_mfma_f32_16x16x32_bf16 v[84:87], v[52:55], a[64:67], 0\n\t" "s_xor_b32 s66, s66, %[xp]\n\t" \
    "s_and_b32 s66, s66, 1\n\t" "v_mfma_f32_16x16x32_bf16 v[80:83], v[56:59], a[68:71], v[80:83]\n\t" "v_mfma_f32_16x16x32_bf16 v[84:87], v[60:63], a[68:71], v[84:87]\n\t" \
    "v_mfma_f32_16x16x32_bf16 v[80:83], v[64:67], a[72:75], v[80:83]\n\t" "v_cvt_pk_bf16_f32 v112, v96, v97\n\t" \
    "v_mfma_f32_16x16x32_bf16 v[84:87], v[68:71], a[72:75], v[84:87]\n\t" "v_cvt_pk_bf16_f32 v113, v98, v99\n\t" \
    "v_mfma_f32_16x16x32_bf16 v[80:83], v[72:75], a[76:79], v[80:83]\n\t" "v_cvt_pk_bf16_f32 v114, v100, v101\n\t" \
    "v_mfma_f32_16x16x32_bf16 v[84:87], v[76:79], a[76:79], v[84:87]\n\t" "v_cvt_pk_bf16_f32 v115, v102, v103\n\t" \
    "v_mfma_f32_16x16x32_bf16 v[88:91], v[48:51], a[80:83], 0\n\t" "v_cvt_pk_bf16_f32 v116, v104, v105\n\t" "v_mfma_f32_16x16x32_bf16 v[92:95], v[52:55], a[80:83], 0\n\t" \
    "v_cvt_pk_bf16_f32 v117, v106, v107\n\t" "v_mfma_f32_16x16x32_bf16 v[88:91], v[56:59], a[84:87], v[88:91]\n\t" \
    "buffer_store_dwordx4 v[112:115], %[z1], %[rs], s63 offen sc1\n\t" "v_mfma_f32_16x16x32_bf16 v[92:95], v[60:63], a[84:87], v[92:95]\n\t" \
    "v_mfma_f32_16x16x32_bf16 v[88:91], v[64:67], a[88:91], v[88:91]\n\t" "v_cvt_pk_bf16_f32 v118, v108, v109\n\t" \
    "v_mfma_f32_16x16x32_bf16 v[92:95], v[68:71], a[88:91], v[92:95]\n\t" "v_cvt_pk_bf16_f32 v119, v110, v111\n\t" \
    "v_mfma_f32_16x16x32_bf16 v[88:91], v[72:75], a[92:95], v[88:91]\n\t" "buffer_store_dwordx4 v[116:119], %[z2], %[rs], s63 offen sc1\n\t" \
    "v_mfma_f32_16x16x32_bf16 v[92:95], v[76:79], a[92:95], v[92:95]\n\t" "s_add_u32 s63, s63, 0x20000\n\t" "v_mfma_f32_16x16x32_bf16 v[96:99], v[48:51], a[96:99], 0\n\t" \
    "s_lshl_b32 s66, s66, 13\n\t" "s_lshl_b32 s64, s65, 19\n\t" "v_mfma_f32_16x16x32_bf16 v[100:103], v[52:55], a[96:99], 0\n\t" "s_add_u32 s64, s64, s66\n\t" \
    "s_add_u32 s64, s64, %[vb]\n\t" "v_mfma_f32_16x16x32_bf16 v[96:99], v[56:59], a[100:103], v[96:99]\n\t" \
    "v_mfma_f32_16x16x32_bf16 v[100:103], v[60:63], a[100:103], v[100:103]\n\t" "v_mfma_f32_16x16x32_bf16 v[96:99], v[64:67], a[104:107], v[96:99]\n\t" \
    "v_cvt_pk_bf16_f32 v112, v80, v81\n\t" "v_mfma_f32_16x16x32_bf16 v[100:103], v[68:71], a[104:107], v[100:103]\n\t" "v_cvt_pk_bf16_f32 v113, v82, v83\n\t" \
    "v_mfma_f32_16x16x32_bf16 v[96:99], v[72:75], a[108:111], v[96:99]\n\t" "v_cvt_pk_bf16_f32 v114, v84, v85\n\t" \
    "v_mfma_f32_16x16x32_bf16 v[100:103], v[76:79], a[108:111], v[100:103]\n\t" "v_cvt_pk_bf16_f32 v115, v86, v87\n\t" \
    "v_mfma_f32_16x16x32_bf16 v[104:107], v[48:51], a[112:115], 0\n\t" "v_cvt_pk_bf16_f32 v116, v88, v89\n\t" \
    "v_mfma_f32_16x16x32_bf16 v[108:111], v[52:55], a[112:115], 0\n\t" "v_cvt_pk_bf16_f32 v117, v90, v91\n\t" \
    "v_mfma_f32_16x16x32_bf16 v[104:107], v[56:59], a[116:119], v[104:107]\n\t" "buffer_store_dwordx4 v[112:115], %[z1], %[rs], s63 offen sc1\n\t" \
    "v_mfma_f32_16x16x32_bf16 v[108:111], v[60:63], a[116:119], v[108:111]\n\t" "v_mfma_f32_16x16x32_bf16 v[104:107], v[64:67], a[120:123], v[104:107]\n\t" \
    "v_cvt_pk_bf16_f32 v118, v92, v93\n\t" "v_mfma_f32_16x16x32_bf16 v[108:111], v[68:71], a[120:123], v[108:111]\n\t" "v_cvt_pk_bf16_f32 v119, v94, v95\n\t" \
    "v_mfma_f32_16x16x32_bf16 v[104:107], v[72:75], a[124:127], v[104:107]\n\t" "buffer_store_dwordx4 v[116:119], %[z2], %[rs], s63 offen sc1\n\t" \
    "v_mfma_f32_16x16x32_bf16 v[108:111], v[76:79], a[124:127], v[108:111]\n\t" "s_add_u32 s63, s63, 0x20000\n\t" \
    "v_mfma_f32_16x16x32_bf16 v[80:83], v[48:51], a[128:131], 0\n\t" "s_waitcnt vmcnt(6)\n\t" "v_readfirstlane_b32 s66, v120\n\t" "v_readfirstlane_b32 s67, v121\n\t" \
    "v_mfma_f32_16x16x32_bf16 v[84:87], v[52:55], a[128:131], 0\n\t" "v_mov_b32 v122, s60\n\t" "buffer_store_dword v122, %[fo], %[rc], %[fz] offen sc1\n\t" \
    "v_mfma_f32_16x16x32_bf16 v[80:83], v[56:59], a[132:135], v[80:83]\n\t" "s_max_u32 s61, s61, s66\n\t" "s_max_u32 s62, s62, s67\n\t" "s_cmp_gt_u32 s61, s65\n\t" \
    "v_mfma_f32_16x16x32_bf16 v[84:87], v[60:63], a[132:135], v[84:87]\n\t" "s_cselect_b32 s65, 1, 0\n\t" "s_cselect_b32 s64, s64, 0x7f000000\n\t" \
    "v_mfma_f32_16x16x32_bf16 v[80:83], v[64:67], a[136:139], v[80:83]\n\t" "buffer_load_dwordx4 v[16:19], %[ao], %[rs], s64 offen offset:0 sc1\n\t" \
    "v_cvt_pk_bf16_f32 v112, v96, v97\n\t" "v_mfma_f32_16x16x32_bf16 v[84:87], v[68:71], a[136:139], v[84:87]\n\t" "v_cvt_pk_bf16_f32 v113, v98, v99\n\t" \
    "v_mfma_f32_16x16x32_bf16 v[80:83], v[72:75], a[140:143], v[80:83]\n\t" "buffer_load_dwordx4 v[20:23], %[ao], %[rs], s64 offen offset:256 sc1\n\t" \
    "v_cvt_pk_bf16_f32 v114, v100, v101\n\t" "v_mfma_f32_16x16x32_bf16 v[84:87], v[76:79], a[140:143], v[84:87]\n\t" "v_cvt_pk_bf16_f32 v115, v102, v103\n\t" \
    "v_mfma_f32_16x16x32_bf16 v[88:91], v[48:51], a[144:147], 0\n\t" "buffer_load_dwordx4 v[24:27], %[ao], %[rs], s64 offen offset:2048 sc1\n\t" \
    "v_cvt_pk_bf16_f32 v116, v104, v105\n\t" "v_mfma_f32_16x16x32_bf16 v[92:95], v[52:55], a[144:147], 0\n\t" "v_cvt_pk_bf16_f32 v117, v106, v107\n\t" \
    "v_mfma_f32_16x16x32_bf16 v[88:91], v[56:59], a[148:151], v[88:91]\n\t" "buffer_load_dwordx4 v[28:31], %[ao], %[rs], s64 offen offset:2304 sc1\n\t" \
    "v_mfma_f32_16x16x32_bf16 v[92:95], v[60:63], a[148:151], v[92:95]\n\t" "buffer_store_dwordx4 v[112:115], %[z1], %[rs], s63 offen sc1\n\t" \
    "v_mfma_f32_16x16x32_bf16 v[88:91], v[64:67], a[152:155], v[88:91]\n\t" "buffer_load_dwordx4 v[32:35], %[a1], %[rs], s64 offen offset:0 sc1\n\t" \
    "v_cvt_pk_bf16_f32 v118, v108, v109\n\t" "v_mfma_f32_16x16x32_bf16 v[92:95], v[68:71], a[152:155], v[92:95]\n\t" "v_cvt_pk_bf16_f32 v119, v110, v111\n\t" \
    "v_mfma_f32_16x16x32_bf16 v[88:91], v[72:75], a[156:159], v[88:91]\n\t" "buffer_load_dwordx4 v[36:39], %[a1], %[rs], s64 offen offset:256 sc1\n\t" \
    "buffer_store_dwordx4 v[116:119], %[z2], %[rs], s63 offen sc1\n\t" "v_mfma_f32_16x16x32_bf16 v[92:95], v[76:79], a[156:159], v[92:95]\n\t" \
    "s_add_u32 s63, s63, 0x20000\n\t" "v_mfma_f32_16x16x32_bf16 v[96:99], v[48:51], a[160:163], 0\n\t" \
    "buffer_load_dwordx4 v[40:43], %[a1], %[rs], s64 offen offset:2048 sc1\n\t" "v_mfma_f32_16x16x32_bf16 v[100:103], v[52:55], a[160:163], 0\n\t" \
    "v_mfma_f32_16x16x32_bf16 v[96:99], v[56:59], a[164:167], v[96:99]\n\t" "buffer_load_dwordx4 v[44:47], %[a1], %[rs], s64 offen offset:2304 sc1\n\t" \
    "v_mfma_f32_16x16x32_bf16 v[100:103], v[60:63], a[164:167], v[100:103]\n\t" "v_mfma_f32_16x16x32_bf16 v[96:99], v[64:67], a[168:171], v[96:99]\n\t" \
    "v_cvt_pk_bf16_f32 v112, v80, v81\n\t" "v_mfma_f32_16x16x32_bf16 v[100:103], v[68:71], a[168:171], v[100:103]\n\t" "v_cvt_pk_bf16_f32 v113, v82, v83\n\t" \
    "v_mfma_f32_16x16x32_bf16 v[96:99], v[72:75], a[172:175], v[96:99]\n\t" "v_cvt_pk_bf16_f32 v114, v84, v85\n\t" \
    "v_mfma_f32_16x16x32_bf16 v[100:103], v[76:79], a[172:175], v[100:103]\n\t" "v_cvt_pk_bf16_f32 v115, v86, v87\n\t" \
    "v_mfma_f32_16x16x32_bf16 v[104:107], v[48:51], a[176:179], 0\n\t" "v_cvt_pk_bf16_f32 v116, v88, v89\n\t" \
    "v_mfma_f32_16x16x32_bf16 v[108:111], v[52:55], a[176:179], 0\n\t" "v_cvt_pk_bf16_f32 v117, v90, v91\n\t" \
    "v_mfma_f32_16x16x32_bf16 v[104:107], v[56:59], a[180:183], v[104:107]\n\t" "buffer_store_dwordx4 v[112:115], %[z1], %[rs], s63 offen sc1\n\t" \
    "v_mfma_f32_16x16x32_bf16 v[108:111], v[60:63], a[180:183], v[108:111]\n\t" "v_mfma_f32_16x16x32_bf16 v[104:107], v[64:67], a[184:187], v[104:107]\n\t" \
    "v_cvt_pk_bf16_f32 v118, v92, v93\n\t" "v_mfma_f32_16x16x32_bf16 v[108:111], v[68:71], a[184:187], v[108:111]\n\t" "v_cvt_pk_bf16_f32 v119, v94, v95\n\t" \
    "v_mfma_f32_16x16x32_bf16 v[104:107], v[72:75], a[188:191], v[104:107]\n\t" "buffer_store_dwordx4 v[116:119], %[z2], %[rs], s63 offen sc1\n\t" \
    "v_mfma_f32_16x16x32_bf16 v[108:111], v[76:79], a[188:191], v[108:111]\n\t" "s_add_u32 s63, s63, 0x20000\n\t" \
    "v_mfma_f32_16x16x32_bf16 v[80:83], v[48:51], a[192:195], 0\n\t" "v_mfma_f32_16x16x32_bf16 v[84:87], v[52:55], a[192:195], 0\n\t" \
    "v_mfma_f32_16x16x32_bf16 v[80:83], v[56:59], a[196:199], v[80:83]\n\t" "v_mfma_f32_16x16x32_bf16 v[84:87], v[60:63], a[196:199], v[84:87]\n\t" \
    "v_mfma_f32_16x16x32_bf16 v[80:83], v[64:67], a[200:203], v[80:83]\n\t" "v_cvt_pk_bf16_f32 v112, v96, v97\n\t" \
    "v_mfma_f32_16x16x32_bf16 v[84:87], v[68:71], a[200:203], v[84:87]\n\t" "v_cvt_pk_bf16_f32 v113, v98, v99\n\t" \
    "v_mfma_f32_16x16x32_bf16 v[80:83], v[72:75], a[204:207], v[80:83]\n\t" "v_cvt_pk_bf16_f32 v114, v100, v101\n\t" \
    "v_mfma_f32_16x16x32_bf16 v[84:87], v[76:79], a[204:207], v[84:87]\n\t" "v_cvt_pk_bf16_f32 v115, v102, v103\n\t" \
    "v_mfma_f32_16x16x32_bf16 v[88:91], v[48:51], a[208:211], 0\n\t" "v_cvt_pk_bf16_f32 v116, v104, v105\n\t" "v_mfma_f32_16x16x32_bf16 v[92:95], v[52:55], a[208:211], 0\n\t" \
    "v_cvt_pk_bf16_f32 v117, v106, v107\n\t" "v_mfma_f32_16x16x32_bf16 v[88:91], v[56:59], a[212:215], v[88:91]\n\t" \
    "buffer_store_dwordx4 v[112:115], %[z1], %[rs], s63 offen sc1\n\t" "v_mfma_f32_16x16x32_bf16 v[92:95], v[60:63], a[212:215], v[92:95]\n\t" \
    "v_mfma_f32_16x16x32_bf16 v[88:91], v[64:67], a[216:219], v[88:91]\n\t" "v_cvt_pk_bf16_f32 v118, v108, v109\n\t" \
    "v_mfma_f32_16x16x32_bf16 v[92:95], v[68:71], a[216:219], v[92:95]\n\t" "v_cvt_pk_bf16_f32 v119, v110, v111\n\t" \
    "v_mfma_f32_16x16x32_bf16 v[88:91], v[72:75], a[220:223], v[88:91]\n\t" "buffer_store_dwordx4 v[116:119], %[z2], %[rs], s63 offen sc1\n\t" \
    "v_mfma_f32_16x16x32_bf16 v[92:95], v[76:79], a[220:223], v[92:95]\n\t" "s_add_u32 s63, s63, 0x20000\n\t" "v_mfma_f32_16x16x32_bf16 v[96:99], v[48:51], a[224:227], 0\n\t" \
    "v_mfma_f32_16x16x32_bf16 v[100:103], v[52:55], a[224:227], 0\n\t" "v_mfma_f32_16x16x32_bf16 v[96:99], v[56:59], a[228:231], v[96:99]\n\t" \
    "v_mfma_f32_16x16x32_bf16 v[100:103], v[60:63], a[228:231], v[100:103]\n\t" "v_mfma_f32_16x16x32_bf16 v[96:99], v[64:67], a[232:235], v[96:99]\n\t" \
    "v_cvt_pk_bf16_f32 v112, v80, v81\n\t" "v_mfma_f32_16x16x32_bf16 v[100:103], v[68:71], a[232:235], v[100:103]\n\t" "v_cvt_pk_bf16_f32 v113, v82, v83\n\t" \
    "v_mfma_f32_16x16x32_bf16 v[96:99], v[72:75], a[236:239], v[96:99]\n\t" "v_cvt_pk_bf16_f32 v114, v84, v85\n\t" \
    "v_mfma_f32_16x16x32_bf16 v[100:103], v[76:79], a[236:239], v[100:103]\n\t" "v_cvt_pk_bf16_f32 v115, v86, v87\n\t" \
    "v_mfma_f32_16x16x32_bf16 v[104:107], v[48:51], a[240:243], 0\n\t" "v_cvt_pk_bf16_f32 v116, v88, v89\n\t" \
    "v_mfma_f32_16x16x32_bf16 v[108:111], v[52:55], a[240:243], 0\n\t" "v_cvt_pk_bf16_f32 v117, v90, v91\n\t" \
    "v_mfma_f32_16x16x32_bf16 v[104:107], v[56:59], a[244:247], v[104:107]\n\t" "buffer_store_dwordx4 v[112:115], %[z1], %[rs], s63 offen sc1\n\t" \
    "v_mfma_f32_16x16x32_bf16 v[108:111], v[60:63], a[244:247], v[108:111]\n\t" "v_mfma_f32_16x16x32_bf16 v[104:107], v[64:67], a[248:251], v[104:107]\n\t" \
    "v_cvt_pk_bf16_f32 v118, v92, v93\n\t" "v_mfma_f32_16x16x32_bf16 v[108:111], v[68:71], a[248:251], v[108:111]\n\t" "v_cvt_pk_bf16_f32 v119, v94, v95\n\t" \
    "v_mfma_f32_16x16x32_bf16 v[104:107], v[72:75], a[252:255], v[104:107]\n\t" "buffer_store_dwordx4 v[116:119], %[z2], %[rs], s63 offen sc1\n\t" \
    "s_add_u32 s63, s63, 0x20000\n\t" "s_add_u32 s60, s60, 1\n\t" "v_mfma_f32_16x16x32_bf16 v[108:111], v[76:79], a[252:255], v[108:111]\n\t" "s_cmp_eq_u32 s65, 0\n\t" \
    "s_waitcnt vmcnt(6)\n\t" ZSUM "s_cbranch_scc0 33b\n\t" "s_branch 30f\n\t" "20:\n\t" "s_nop 7\n\t" "v_cvt_pk_bf16_f32 v112, v96, v97\n\t" \
    "v_cvt_pk_bf16_f32 v113, v98, v99\n\t" "v_cvt_pk_bf16_f32 v114, v100, v101\n\t" "v_cvt_pk_bf16_f32 v115, v102, v103\n\t" "v_cvt_pk_bf16_f32 v116, v104, v105\n\t" \
    "v_cvt_pk_bf16_f32 v117, v106, v107\n\t" "v_cvt_pk_bf16_f32 v118, v108, v109\n\t" "v_cvt_pk_bf16_f32 v119, v110, v111\n\t" \
    "buffer_store_dwordx4 v[112:115], %[z1], %[rs], s63 offen sc1\n\t" "buffer_store_dwordx4 v[116:119], %[z2], %[rs], s63 offen sc1\n\t" \
    "buffer_load_dword v120, %[fo], %[rc], %[fv] offen sc1\n\t" "s_waitcnt vmcnt(0)\n\t" "v_readfirstlane_b32 s61, v120\n\t" "s_lshr_b32 s66, s60, 2\n\t" \
    "s_xor_b32 s66, s66, %[xp]\n\t" "s_and_b32 s66, s66, 1\n\t" "s_lshl_b32 s66, s66, 13\n\t" "s_lshl_b32 s64, s60, 19\n\t" "s_add_u32 s64, s64, s66\n\t" \
    "s_add_u32 s64, s64, %[vb]\n\t" "s_cmp_gt_u32 s61, s60\n\t" "s_cbranch_scc0 24f\n\t" "s_cmp_eq_u32 s60, 64\n\t" "s_cbranch_scc1 24f\n\t" \
    "buffer_load_dwordx4 v[48:51], %[ao], %[rs], s64 offen offset:0 sc1\n\t" "buffer_load_dwordx4 v[52:55], %[ao], %[rs], s64 offen offset:256 sc1\n\t" \
    "buffer_load_dwordx4 v[56:59], %[ao], %[rs], s64 offen offset:2048 sc1\n\t" "buffer_load_dwordx4 v[60:63], %[ao], %[rs], s64 offen offset:2304 sc1\n\t" \
    "buffer_load_dwordx4 v[64:67], %[a1], %[rs], s64 offen offset:0 sc1\n\t" "buffer_load_dwordx4 v[68:71], %[a1], %[rs], s64 offen offset:256 sc1\n\t" \
    "buffer_load_dwordx4 v[72:75], %[a1], %[rs], s64 offen offset:2048 sc1\n\t" "buffer_load_dwordx4 v[76:79], %[a1], %[rs], s64 offen offset:2304 sc1\n\t" \
    "v_mov_b32 v122, s60\n\t" "buffer_store_dword v122, %[fo], %[rc], %[fz] offen sc1\n\t" "s_waitcnt vmcnt(1)\n\t" "s_branch 23b\n\t" "24:\n\t" "v_mov_b32 v122, s60\n\t" \
    "buffer_store_dword v122, %[fo], %[rc], %[fz] offen sc1\n\t" "s_cmp_eq_u32 s60, 64\n\t" "s_cbranch_scc1 90f\n\t" "s_add_u32 s67, s60, 1\n\t" "s_cmp_ge_u32 s61, s67\n\t" \
    "s_cbranch_scc1 22f\n\t" "21:\n\t" "buffer_load_dword v120, %[fo], %[rc], %[fv] offen sc1\n\t" "buffer_load_dword v121, %[fo], %[rc], %[vd] offen sc1\n\t" \
    "s_waitcnt vmcnt(0)\n\t" "v_readfirstlane_b32 s61, v120\n\t" "v_readfirstlane_b32 s66, v121\n\t" "s_nop 3\n\t" "s_cmp_ge_u32 s61, s67\n\t" "s_cbranch_scc1 22f\n\t" \
    "s_bitcmp1_b32 s66, 1\n\t" "s_cbranch_scc1 90f\n\t" "s_cmp_eq_u32 %[bd], 0\n\t" "s_cbranch_scc1 22f\n\t" "s_sub_u32 %[bd], %[bd], 1\n\t" "s_sleep 2\n\t" \
    "s_branch 21b\n\t" "22:\n\t" "s_lshr_b32 s66, s60, 2\n\t" "s_xor_b32 s66, s66, %[xp]\n\t" "s_and_b32 s66, s66, 1\n\t" "s_lshl_b32 s66, s66, 13\n\t" \
    "s_lshl_b32 s64, s60, 19\n\t" "s_add_u32 s64, s64, s66\n\t" "s_add_u32 s64, s64, %[vb]\n\t" "buffer_load_dwordx4 v[48:51], %[ao], %[rs], s64 offen offset:0 sc1\n\t" \
    "buffer_load_dwordx4 v[52:55], %[ao], %[rs], s64 offen offset:256 sc1\n\t" "buffer_load_dwordx4 v[56:59], %[ao], %[rs], s64 offen offset:2048 sc1\n\t" \
    "buffer_load_dwordx4 v[60:63], %[ao], %[rs], s64 offen offset:2304 sc1\n\t" "buffer_load_dwordx4 v[64:67], %[a1], %[rs], s64 offen offset:0 sc1\n\t" \
    "buffer_load_dwordx4 v[68:71], %[a1], %[rs], s64 offen offset:256 sc1\n\t" "buffer_load_dwordx4 v[72:75], %[a1], %[rs], s64 offen offset:2048 sc1\n\t" \
    "buffer_load_dwordx4 v[76:79], %[a1], %[rs], s64 offen offset:2304 sc1\n\t" "s_waitcnt vmcnt(0)\n\t" "s_branch 23b\n\t" "60:\n\t" "s_sub_u32 s67, s60, %[rg]\n\t" \
    "s_cmp_ge_u32 s62, s67\n\t" "s_cbranch_scc1 62f\n\t" "61:\n\t" "buffer_load_dword v120, %[fo], %[rc], %[fs] offen sc1\n\t" \
    "buffer_load_dword v121, %[fo], %[rc], %[vd] offen sc1\n\t" "s_waitcnt vmcnt(0)\n\t" "v_readfirstlane_b32 s62, v120\n\t" "v_readfirstlane_b32 s66, v121\n\t" "s_nop 3\n\t" \
    "s_cmp_ge_u32 s62, s67\n\t" "s_cbranch_scc1 62f\n\t" "s_bitcmp1_b32 s66, 1\n\t" "s_cbranch_scc1 90f\n\t" "s_cmp_eq_u32 %[bd], 0\n\t" "s_cbranch_scc1 62f\n\t" \
    "s_sub_u32 %[bd], %[bd], 1\n\t" "s_sleep 2\n\t" "s_branch 61b\n\t" "62:\n\t" "s_branch 63b\n\t" "30:\n\t" "s_nop 7\n\t" "v_cvt_pk_bf16_f32 v112, v96, v97\n\t" \
    "v_cvt_pk_bf16_f32 v113, v98, v99\n\t" "v_cvt_pk_bf16_f32 v114, v100, v101\n\t" "v_cvt_pk_bf16_f32 v115, v102, v103\n\t" "v_cvt_pk_bf16_f32 v116, v104, v105\n\t" \
    "v_cvt_pk_bf16_f32 v117, v106, v107\n\t" "v_cvt_pk_bf16_f32 v118, v108, v109\n\t" "v_cvt_pk_bf16_f32 v119, v110, v111\n\t" \
    "buffer_store_dwordx4 v[112:115], %[z1], %[rs], s63 offen sc1\n\t" "buffer_store_dwordx4 v[116:119], %[z2], %[rs], s63 offen sc1\n\t" \
    "buffer_load_dword v120, %[fo], %[rc], %[fv] offen sc1\n\t" "s_waitcnt vmcnt(0)\n\t" "v_readfirstlane_b32 s61, v120\n\t" "s_lshr_b32 s66, s60, 2\n\t" \
    "s_xor_b32 s66, s66, %[xp]\n\t" "s_and_b32 s66, s66, 1\n\t" "s_lshl_b32 s66, s66, 13\n\t" "s_lshl_b32 s64, s60, 19\n\t" "s_add_u32 s64, s64, s66\n\t" \
    "s_add_u32 s64, s64, %[vb]\n\t" "s_cmp_gt_u32 s61, s60\n\t" "s_cbranch_scc0 34f\n\t" "s_cmp_eq_u32 s60, 64\n\t" "s_cbranch_scc1 34f\n\t" \
    "buffer_load_dwordx4 v[16:19], %[ao], %[rs], s64 offen offset:0 sc1\n\t" "buffer_load_dwordx4 v[20:23], %[ao], %[rs], s64 offen offset:256 sc1\n\t" \
    "buffer_load_dwordx4 v[24:27], %[ao], %[rs], s64 offen offset:2048 sc1\n\t" "buffer_load_dwordx4 v[28:31], %[ao], %[rs], s64 offen offset:2304 sc1\n\t" \
    "buffer_load_dwordx4 v[32:35], %[a1], %[rs], s64 offen offset:0 sc1\n\t" "buffer_load_dwordx4 v[36:39], %[a1], %[rs], s64 offen offset:256 sc1\n\t" \
    "buffer_load_dwordx4 v[40:43], %[a1], %[rs], s64 offen offset:2048 sc1\n\t" "buffer_load_dwordx4 v[44:47], %[a1], %[rs], s64 offen offset:2304 sc1\n\t" \
    "v_mov_b32 v122, s60\n\t" "buffer_store_dword v122, %[fo], %[rc], %[fz] offen sc1\n\t" "s_waitcnt vmcnt(1)\n\t" "s_branch 33b\n\t" "34:\n\t" "v_mov_b32 v122, s60\n\t" \
    "buffer_store_dword v122, %[fo], %[rc], %[fz] offen sc1\n\t" "s_cmp_eq_u32 s60, 64\n\t" "s_cbranch_scc1 90f\n\t" "s_add_u32 s67, s60, 1\n\t" "s_cmp_ge_u32 s61, s67\n\t" \
    "s_cbranch_scc1 32f\n\t" "31:\n\t" "buffer_load_dword v120, %[fo], %[rc], %[fv] offen sc1\n\t" "buffer_load_dword v121, %[fo], %[rc], %[vd] offen sc1\n\t" \
    "s_waitcnt vmcnt(0)\n\t" "v_readfirstlane_b32 s61, v120\n\t" "v_readfirstlane_b32 s66, v121\n\t" "s_nop 3\n\t" "s_cmp_ge_u32 s61, s67\n\t" "s_cbranch_scc1 32f\n\t" \
    "s_bitcmp1_b32 s66, 1\n\t" "s_cbranch_scc1 90f\n\t" "s_cmp_eq_u32 %[bd], 0\n\t" "s_cbranch_scc1 32f\n\t" "s_sub_u32 %[bd], %[bd], 1\n\t" "s_sleep 2\n\t" \
    "s_branch 31b\n\t" "32:\n\t" "s_lshr_b32 s66, s60, 2\n\t" "s_xor_b32 s66, s66, %[xp]\n\t" "s_and_b32 s66, s66, 1\n\t" "s_lshl_b32 s66, s66, 13\n\t" \
    "s_lshl_b32 s64, s60, 19\n\t" "s_add_u32 s64, s64, s66\n\t" "s_add_u32 s64, s64, %[vb]\n\t" "buffer_load_dwordx4 v[16:19], %[ao], %[rs], s64 offen offset:0 sc1\n\t" \
    "buffer_load_dwordx4 v[20:23], %[ao], %[rs], s64 offen offset:256 sc1\n\t" "buffer_load_dwordx4 v[24:27], %[ao], %[rs], s64 offen offset:2048 sc1\n\t" \
    "buffer_load_dwordx4 v[28:31], %[ao], %[rs], s64 offen offset:2304 sc1\n\t" "buffer_load_dwordx4 v[32:35], %[a1], %[rs], s64 offen offset:0 sc1\n\t" \
    "buffer_load_dwordx4 v[36:39], %[a1], %[rs], s64 offen offset:256 sc1\n\t" "buffer_load_dwordx4 v[40:43], %[a1], %[rs], s64 offen offset:2048 sc1\n\t" \
    "buffer_load_dwordx4 v[44:47], %[a1], %[rs], s64 offen offset:2304 sc1\n\t" "s_waitcnt vmcnt(0)\n\t" "s_branch 33b\n\t" "70:\n\t" "s_sub_u32 s67, s60, %[rg]\n\t" \
    "s_cmp_ge_u32 s62, s67\n\t" "s_cbranch_scc1 72f\n\t" "71:\n\t" "buffer_load_dword v120, %[fo], %[rc], %[fs] offen sc1\n\t" \
    "buffer_load_dword v121, %[fo], %[rc], %[vd] offen sc1\n\t" "s_waitcnt vmcnt(0)\n\t" "v_readfirstlane_b32 s62, v120\n\t" "v_readfirstlane_b32 s66, v121\n\t" "s_nop 3\n\t" \
    "s_cmp_ge_u32 s62, s67\n\t" "s_cbranch_scc1 72f\n\t" "s_bitcmp1_b32 s66, 1\n\t" "s_cbranch_scc1 90f\n\t" "s_cmp_eq_u32 %[bd], 0\n\t" "s_cbranch_scc1 72f\n\t" \
    "s_sub_u32 %[bd], %[bd], 1\n\t" "s_sleep 2\n\t" "s_branch 71b\n\t" "72:\n\t" "s_branch 73b\n\t" "90:\n\t"

#define T_DQ 1390
#define T_DV 1290
#define T_RQ 700
#define T_RV 640
#define T_M1 100
#define T_DY 580
#define T_DX 1240
#define T_MIN 35
#define T_PEN 280
#define T_SAFE 80
#define T_RETRY 700
#define T_POLL 1
#define T_KS 0
#define T_TS 0
#define T_VLAG 0
#define T_U0 340
constexpr int T_OVH = 118, T_NEAR = 460;
constexpr unsigned NS(int ns) { return (unsigned)(ns * 64 / 10); }
struct Clk { unsigned cyc, g, pc, pg, k1, k2; };
DEV void mark(Clk& k) {
  unsigned long long c, r[8];
  asm volatile("s_memtime %0\n\ts_memrealtime %1\n\ts_nop 2\n\ts_memrealtime %2\n\ts_nop 2\n\ts_memrealtime %3\n\ts_nop 2\n\ts_memrealtime %4\n\ts_nop 2\n\t"
               "s_memrealtime %5\n\ts_nop 2\n\ts_memrealtime %6\n\ts_nop 2\n\ts_memrealtime %7\n\ts_waitcnt lgkmcnt(0)"
               : "=s"(c), "=s"(r[0]), "=s"(r[1]), "=s"(r[2]), "=s"(r[3]), "=s"(r[4]), "=s"(r[5]), "=s"(r[6]) :: "memory");
  const unsigned a = (unsigned)r[0];
  unsigned n = 0;
#pragma unroll
  for (int i = 1; i < 7; ++i) asm("s_cmp_eq_u32 %1, %2\n\ts_addc_u32 %0, %0, 0" : "+s"(n) : "s"((unsigned)r[i]), "s"(a) : "scc");
  k.g = (a + 4u) << 6; k.cyc = (unsigned)c + 12u + 16u * n;
}
DEV void anchor(Clk& k, bool any = false) {
  mark(k);
  const unsigned dg_ = k.g - k.pg;
  const float dc = (float)(k.cyc - k.pc), dg = (float)dg_;
  unsigned k1, k2;
  asm volatile("s_nop 0\n\tv_readfirstlane_b32 %0, %2\n\tv_readfirstlane_b32 %1, %3\n\ts_nop 3\n\ts_min_u32 %0, %0, 0x80000\n\ts_min_u32 %1, %1, 0x8000"
               : "=&s"(k1), "=&s"(k2) : "v"((unsigned)(dg * __builtin_amdgcn_rcpf(dc) * 65536.f)), "v"((unsigned)(dc * __builtin_amdgcn_rcpf(dg) * 65536.f)) : "scc");
  if (any) { k.k1 = k1; k.k2 = k2; }
  else asm("s_cmp_lt_u32 %4, %5\n\ts_cselect_b32 %2, %2, %0\n\ts_cselect_b32 %3, %3, %1\n\ts_add_u32 %0, %0, %2\n\ts_lshr_b32 %0, %0, 1\n\ts_add_u32 %1, %1, %3\n\ts_lshr_b32 %1, %1, 1"
           : "+s"(k.k1), "+s"(k.k2), "+s"(k1), "+s"(k2) : "s"(dg_ - NS(1400)), "n"(NS(800)) : "scc");
  k.pc = k.cyc; k.pg = k.g;
}
DEV unsigned to_cyc(const Clk& k, unsigned g) {
  unsigned d;
  asm("s_max_i32 %0, %1, 0xffff8000\n\ts_min_i32 %0, %0, 0x8000\n\ts_mul_i32 %0, %0, %2\n\ts_ashr_i32 %0, %0, 16" : "=&s"(d) : "s"(g - k.g), "s"(k.k2) : "scc");
  return k.cyc + d - T_OVH;
}
DEV void wait_cyc(unsigned t) { asm volatile(TIMED_WAIT :: "s"(t) : CL_S); }
DEV void close_in(Clk& k, unsigned t) {
  if ((int)(t - (unsigned)__builtin_amdgcn_s_memtime()) > T_NEAR + 60) { wait_cyc(t - T_NEAR); mark(k); }
}
DEV bool slow_head(const u16* q, int h) { return ((((unsigned)(size_t)q >> 8) + h) & 3) == 1; }
DEV int turn(int h, int w) { return ((h >> 3) ^ (w & 1)) & 3; }
DEV void hold(const Ctx& c, int ticks) {
#pragma unroll 1
  while ((unsigned)__builtin_amdgcn_s_memrealtime() - c.t0 < (unsigned)ticks) __builtin_amdgcn_s_sleep(4);
}
DEV int z_mix(int y) { return (y >> 4) | ((y & 1) << 2); }
DEV int u_slot(int s, int para) { return R_U + s * MB + ((para ^ (s >> 1)) & 1) * 8192; }
DEV unsigned ask_stamps(Ctx& c, int par, int slot) { return __builtin_amdgcn_raw_buffer_load_b32(c.rs, amap(24576, par) + fresh_lane() * 128, slot, AUX_SC1); }
DEV unsigned latest(Ctx& c, const Clk& k, int par, int slot, unsigned v) {
  if (__builtin_expect(!allok(v), 0)) {
#pragma unroll 1
    do {
      __builtin_amdgcn_s_sleep(T_POLL);
      asm volatile("" ::: "memory");
      const unsigned t = __builtin_amdgcn_raw_buffer_load_b32(c.rs, v == SENT ? amap(24576, par) + fresh_lane() * 128 : 1 << 30, slot, AUX_SC1);
      v = v == SENT ? t : v;
    } while (!allok(v) && !spend(c));
  }
  int d = (int)(v - k.g);
  d = max(d, __builtin_amdgcn_update_dpp(d, d, 0x128, 0xf, 0xf, true));
  d = max(d, __builtin_amdgcn_update_dpp(d, d, 0x124, 0xf, 0xf, true));
  d = max(d, __builtin_amdgcn_update_dpp(d, d, 0x122, 0xf, 0xf, true));
  d = max(d, __builtin_amdgcn_update_dpp(d, d, 0x121, 0xf, 0xf, true));
  unsigned m;
  asm("s_max_i32 %0, %1, %2\n\ts_max_i32 %0, %0, %3\n\ts_max_i32 %0, %0, %4" : "=&s"(m)
      : "s"(__builtin_amdgcn_readlane(d, 0)), "s"(__builtin_amdgcn_readlane(d, 16)), "s"(__builtin_amdgcn_readlane(d, 32)), "s"(__builtin_amdgcn_readlane(d, 48)) : "scc");
  return k.g + m;
}
struct Plan { unsigned gx, dx; };
DEV unsigned smax(unsigned a, unsigned b) { return (int)(a - b) > 0 ? a : b; }
DEV unsigned when_p(const Plan& t, unsigned last) {
  const unsigned gy = t.gx + t.dx + NS(T_M1);
  return (int)(last + NS(T_MIN) - gy) > 0 ? smax(last + NS(T_PEN), gy + NS(T_RETRY)) : gy;
}
DEV void advance(Plan& t, unsigned last) {
  const unsigned gy = t.gx + t.dx + NS(T_M1), dur = last - t.gx, gp = when_p(t, last);
  const unsigned nx = gp + NS(T_DY) + (gp != gy ? NS(T_SAFE) : 0u);
  t.dx = dur < t.dx ? (7u * t.dx + dur) >> 3 : dur < t.dx + NS(80) ? (t.dx + dur) >> 1 : t.dx + NS(4);
  t.gx = nx;
}
DEV unsigned next_go(Ctx& c, Clk& k, Plan& t, int par, int slot, unsigned v) {
  SCHED();
  anchor(k);
  SCHED();
  advance(t, latest(c, k, par, slot, v));
  return to_cyc(k, t.gx);
}

DEV constexpr int qk_r(int j) { return j < 4 ? 0 : 1 + (j - 4) % 3; }
DEV constexpr int qk_i(int j) { return j < 4 ? j : (j - 4) / 3; }
DEV constexpr int qk_upto(int u) { return u < 6 ? u + 7 : u < 10 ? 12 + (u - 4) / 2 : u < 12 ? 15 : 16; }
DEV void qk_role(const Params& p, Ctx& c, int h) {
  constexpr int WL = 0, MEMS = 131072, KXL = 147456, QKX = 155648, QROW = 272;
  c.budget = SPIN_CAP;
  const int w = c.w;
  Clk k = {0u, 0u, 0u, 0u, 0u, 0u};
  anchor(k, true);
  v8u wa[32], wh[16];
  claim(c, 0);
  hold(c, QK_AT - (slow_head(p.Wq, h) ? SLOW_LEAD : 0));
  {
    const int lane = fresh_lane(), g = lane >> 4, cc = lane & 15;
    const rsrc_t rq = mkrs(p.Wq, 1024 * 4096 * 2), rk = mkrs(p.Wk, 1024 * 4096 * 2);
    int vo[4];
#pragma unroll
    for (int i = 0; i < 4; ++i) vo[i] = opq((256 * w + 64 * (i ^ turn(h, w)) + krow_lane(g)) * 8192 + (h * 128 + 8 * cc) * 2);
    v4u row[16][8];
    auto request = [&](int u) {
#pragma unroll
      for (int r = 0; r < 8; ++r) row[u][r] = ld16w(u < 8 ? rq : rk, vo[(u >> 1) & 3], krow(4 * (u & 1) + (r >> 1), r & 1) * 8192);
    };
#pragma unroll
    for (int u = 0; u < qk_upto(-1); ++u) request(u);
    SCHED();
    const int sw = MEMS + w * 8192 + g * 256, lw = opq(WL + w * 32768 + lane * 16);
    int sr[2];
#pragma unroll
    for (int t = 0; t < 2; ++t) { const int i = (2 * cc + t) & 7; sr[t] = opq(i * 1024 + g * 256 + ((4 * w + (cc >> 2) + 2 * i) & 15) * 16); }
    sfor<16>([&](auto U) __attribute__((always_inline)) {
      constexpr int u = U.value;
#pragma unroll
      for (int i = 0; i < 8; ++i) {
        v4u f;
#pragma unroll
        for (int r = 0; r < 4; ++r) f[r] = prm(row[u][2 * r + 1][i >> 1], row[u][2 * r][i >> 1], i & 1);
        sts16(c.L, sw + i * 1024 + ((cc + 2 * i) & 15) * 16, f);
      }
      SCHED();
      if (qk_upto(u) > qk_upto(u - 1)) request(qk_upto(u - 1));
      BAR();
      v4u in[4][2];
#pragma unroll
      for (int t = 1; t >= 0; --t)
#pragma unroll
        for (int r = 0; r < 4; ++r) in[r][t] = lds16(c.L, sr[t] + MEMS + ((w + r) & 3) * 8192);
      SCHED();
#pragma unroll
      for (int j = 0; j < 16; ++j) {
        if (qk_i(j) != ((u >> 1) & 3)) continue;
#pragma unroll
        for (int t = 1; t >= 0; --t) {
          const v4u o = in[qk_r(j)][t];
          if (u >= 8 && t == 1) sts16(c.L, lw + j * 2048 + (u & 1) * 1024, o);
          else {
            v8u& d = u < 8 ? wa[2 * j + t] : wh[j];
#pragma unroll
            for (int r = 0; r < 4; ++r) d[4 * (u & 1) + r] = o[r];
#define M_(r) asm volatile("" : "+" r(d))
            if (u & 1) { if (u < 8) AT_ACC(M_, 2 * j + t); else AT_HOME(M_, j); }
#undef M_
          }
        }
      }
      BAR();
    });
  }
  STAMP(0);
  BAR();
  const unsigned vote = wait_verdict(c);
  const int par = vote_b(vote), para = vote_a(vote);
  const int lane = fresh_lane(), g = lane >> 4, cc = lane & 15;
  const int moff = opq(amap(w * 4096 + lane * 16, par) + (turn(h, w) & 1) * 1024), m2 = R_MEM + (turn(h, w) & 2) * 1024;
  unsigned tries = 1u << 12;
  *(volatile LAS unsigned*)(c.L + 160512) = 0u;
  LGKM(0);
  const int idx = opq((lane & 1) ? 0xEEEE : 0x4444);
  TICK_DECL;
  const int l0 = opq(WL + w * 32768 + lane * 16), l1 = opq(MEMS + lane * 16);
  const int qw = opq(QKX + (2 * w + (cc >> 3)) * 256 + g * 64 + (cc & 1) * 16 + ((cc >> 1) & 3) * 4), kw = opq(QKX + (8 + 2 * g) * QROW + (32 * w + 2 * cc) * 2);
#define QK_PINS PIN_ACC0(wa); PIN_ACC1(wa); PIN_HOME(wh)
#define QK_IN [l0] "v"(l0), [l1] "v"(l1), [mo] "v"(moff), [ix] "v"(idx), [qw] "v"(qw), [kw] "v"(kw), [rs] "s"(c.rs), [sb] "s"(m2 + (s & 3) * MEM_SLOT)
#define QK_CL CL_S, CL_16_32, CL_160_200, CL_200_216, CL_216_256
  if (w) RAWBAR();
  if (w == 3) {
    const int hq = opq(lane ? 1 << 30 : amap(24576 + h * 128, par));
    anchor(k, true);
    unsigned tg = k.cyc;
    Plan tt = {k.g - NS(100000), NS(T_DX)};
    for (int s = 0; s < NSEG; ++s) {
      const int pb = R_P + (s & 3) * P_SLOT;
      QK_PINS;
      asm volatile(QK_STEP3 : [bd] "+s"(c.budget), [rt] "+s"(tries) : QK_IN, [hq] "v"(hq), [pb] "s"(pb), [tg] "s"(tg), [ca] "s"(k.cyc - T_DQ), [k1] "s"(k.k1), [ga] "s"(k.g), [dr] "n"(T_RQ), [pr] "s"(R_P + ((s + 1) & 3) * P_SLOT),
                     [kn] "s"(R_KX - 8 * MEMS + xblk(min(s + 1, NSEG - 1), h, para)) : QK_CL);
      QK_PINS;
      LOG(1, s, tg);
      const unsigned sv = ask_stamps(c, par, pb);
      SCHED();
      anchor(k);
      RAWBAR();
      SCHED();
      advance(tt, latest(c, k, par, pb, sv));
      tg = to_cyc(k, tt.gx);
      LOG(2, s, k.cyc); LOG(3, s, k.g); LOG(4, s, tt.dx);
      st4s(mkrs((const void*)c.ctrl, CTRL_BYTES), hq & (1 << 30), C_FLAG + (F_S + h) * 128, (unsigned)s + 1u);
      TICK_SUM(10);
      close_in(k, tg);
      tg = to_cyc(k, tt.gx);
    }
  } else {
    const int kx0 = opq(KXL + g * 512 + cc * 16), kmrd = opq(QKX + g * 16 + (8 + (cc & 7)) * QROW);
    unsigned seen = NOFLAG;
    if (w == 0) {
      const int prow = (2 * g + (cc & 1)) * 2 + ((cc >> 1) & 1);
      const int px = opq(amap((h >> 1) * 1024 + ((2 * (h & 1) + (cc >> 3)) * 16 + prow) * 16 + ((cc >> 2) & 1) * 8, par));
      const int pm = opq(cc < 8 ? amap(16384 + h * 128 + prow * 8 + (cc >> 2) * 4, par) : 1 << 30);
      const int ps = opq((cc & 1) ? 0x03020706 : 0x05040100);
      for (int s = 0; s < NSEG; ++s) {
        if (seen < (unsigned)(s + 1 + 256 * c.att)) seen = wait_flag(c, F_K + h, s + 1 + 256 * c.att);
        QK_PINS;
        asm volatile(QK_STEP0 : [bd] "+s"(c.budget), [rt] "+s"(tries), [sq] "+s"(seen)
                     : QK_IN, [kx] "v"(kx0), [km] "v"(kmrd), [px] "v"(px), [pm] "v"(pm), [ps] "v"(ps), [pb] "s"(R_P + (s & 3) * P_SLOT), [pr] "s"(R_P + ((s + 1) & 3) * P_SLOT),
                       [kb] "s"(R_KX - KXL + xblk(s, h, para)), [fq] "s"(c.ctrl + (C_FLAG + (F_K + h) * 128) / 4) : QK_CL);
        QK_PINS;
        LITE(s); LOGS(0, s);
        TICK_SUM(10);
      }
      RAWBAR();
    } else {
      const int qt = w - 1;
      const int px = opq(amap(qt ? 16384 + h * 1024 + lane * 16 : h * 512 + (g >> 1) * 256 + cc * 16 + (g & 1) * 8, para));
      const int pm = opq(g < 2 ? amap(49152 + (qt * 32 + h) * 256 + cc * 16 + g * 8, para) : 1 << 30);
      const int ps = opq(cc - 4 * g), hq = opq(g * 512 + (16 * qt + cc) * 16);
#define QK_TOK(STEP) for (int s = 0; s < NSEG; ++s) { QK_PINS; \
        asm volatile(STEP : [bd] "+s"(c.budget), [rt] "+s"(tries), [sq] "+s"(seen) \
                     : QK_IN, [kx] "v"(kx0), [km] "v"(kmrd), [px] "v"(px), [pm] "v"(pm), [ps] "v"(ps), [hq] "v"(hq), [qb] "s"(R_QX + xblk(s, h, para)), [tb] "s"(R_PT + (s & 7) * PT_SLOT), \
                       [tr] "s"(R_PT + ((s + 2) & 7) * PT_SLOT), [sn] "s"(s + 1), [fq] "s"(c.ctrl + (C_FLAG + (F_Q + h) * 128) / 4) : QK_CL); \
        QK_PINS; TICK_SUM(10); }
      if (w == 1) { QK_TOK(QK_STEP1) } else { QK_TOK(QK_STEP2) }
    }
  }
  TICK_OUT();
  if (c.budget == 0u) __hip_atomic_store(c.ctrl + C_TMO / 4, 1u, RLX_AGENT);
}

DEV void shuffle_cols(Ctx& c, int scratch, const v4u (&rows)[8], v4u (&o)[8]) {
  const int g = c.lane >> 4, cc = c.lane & 15;
#pragma unroll
  for (int i = 0; i < 8; ++i) {
    v4u f;
#pragma unroll
    for (int r = 0; r < 4; ++r) f[r] = prm(rows[2 * r + 1][i >> 1], rows[2 * r][i >> 1], i & 1);
    sts16(c.L, scratch + (8 * cc + i) * 64 + ((g ^ (cc & 3)) << 4), f);
  }
  asm volatile("s_waitcnt lgkmcnt(0)" ::: "memory");
#pragma unroll
  for (int i = 0; i < 8; ++i) { const int col = 16 * i + cc; o[i] = lds16(c.L, scratch + col * 64 + ((g ^ ((col >> 3) & 3)) << 4)); }
  asm volatile("s_waitcnt lgkmcnt(0)" ::: "memory");
}

DEV void vo_role(const Params& p, Ctx& c, int h) {
  constexpr int WL = 0, MEMS = 131072, VM = 147456, VROW = 272;
  c.budget = SPIN_CAP;
  const int w = c.w, lane = c.lane = fresh_lane(), g = lane >> 4, cc = lane & 15;
  Clk k = {0u, 0u, 0u, 0u, 0u, 0u};
  anchor(k, true);
  v8u wa[32], wh[16];
  claim(c, 0);
  hold(c, VO_AT);
  const rsrc_t rwo = mkrs(p.Wo, 4096 * 1024 * 2);
  const int voo = (h * 128 + 4 * g) * 2048 + (256 * ((cc >> 3) ^ (h >> 3)) + 64 * w + 8 * (cc & 7)) * 2;
  v4u buf[3][8];
  auto request_o = [&](int u) {
#pragma unroll
    for (int r = 0; r < 8; ++r) buf[u % 3][r] = ld16w(rwo, voo ^ ((u >> 2) * 1024), (32 * (u & 3) + 16 * (r >> 2) + (r & 3)) * 2048);
  };
  {
    const rsrc_t rw = mkrs(p.Wv, 1024 * 4096 * 2);
    const int vo = (256 * w + krow_lane(g)) * 8192 + (h * 128 + 8 * cc) * 2;
    v4u vrow[8][8];
    auto request = [&](int u) {
#pragma unroll
      for (int r = 0; r < 8; ++r) vrow[u][r] = ld16w(rw, vo, (64 * ((u >> 1) ^ turn(h, w)) + krow(4 * (u & 1) + (r >> 1), r & 1)) * 8192);
    };
#pragma unroll
    for (int u = 0; u < 5; ++u) request(u);
    SCHED();
    const int wr_ = MEMS + w * 8192 + g * 256;
    int sr[2];
#pragma unroll
    for (int t = 0; t < 2; ++t) { const int i = (2 * cc + t) & 7; sr[t] = opq(i * 1024 + g * 256 + ((4 * w + (cc >> 2) + 2 * i) & 15) * 16); }
#pragma unroll
    for (int u = 0; u < 8; ++u) {
#pragma unroll
      for (int i = 0; i < 8; ++i) {
        v4u f;
#pragma unroll
        for (int r = 0; r < 4; ++r) f[r] = prm(vrow[u][2 * r + 1][i >> 1], vrow[u][2 * r][i >> 1], i & 1);
        sts16(c.L, wr_ + i * 1024 + ((cc + 2 * i) & 15) * 16, f);
      }
      SCHED();
      if (u + 5 < 8) request(u + 5); else if (u < 6) request_o(u - 3);
      BAR();
      v4u in[4][2];
#pragma unroll
      for (int t = 1; t >= 0; --t)
#pragma unroll
        for (int r = 0; r < 4; ++r) in[r][t] = lds16(c.L, sr[t] + MEMS + ((w + r) & 3) * 8192);
      SCHED();
#pragma unroll
      for (int j = 0; j < 16; ++j) {
        if (qk_i(j) != (u >> 1)) continue;
#pragma unroll
        for (int t = 1; t >= 0; --t) {
          const v4u o = in[qk_r(j)][t];
          if (t == 1 && (j & 1)) sts16(c.L, WL + (w * 16 + (j >> 1)) * 2048 + (u & 1) * 1024 + lane * 16, o);
          else {
            const int n = 3 * (j >> 1) + ((j & 1) ? 2 : t);
#pragma unroll
            for (int r = 0; r < 4; ++r) wa[n][4 * (u & 1) + r] = o[r];
#define M_(r) asm volatile("" : "+" r(wa[n]))
            if (u & 1) AT_ACC(M_, n);
#undef M_
          }
        }
      }
      BAR();
    }
  }
#pragma unroll
  for (int u = 0; u < 8; ++u) {
    v4u o[8];
    shuffle_cols(c, MEMS + w * 8192, buf[u % 3], o);
    SCHED();
    if (u + 3 < 8) request_o(u + 3);
#pragma unroll
    for (int i = 0; i < 4; ++i) {
      const int pr = 4 * (u >> 2) + i, ch = u & 3;
      const v8u f = {o[2 * i][0], o[2 * i + 1][0], o[2 * i][1], o[2 * i + 1][1], o[2 * i][2], o[2 * i + 1][2], o[2 * i][3], o[2 * i + 1][3]};
      if (ch < 2) {
        wh[2 * pr + ch] = f;
#define M_(r) asm volatile("" : "+" r(wh[2 * pr + ch]))
        AT_HOME(M_, 2 * pr + ch);
#undef M_
      } else if (ch == 2) {
        wa[24 + pr] = f;
#define M_(r) asm volatile("" : "+" r(wa[24 + pr]))
        AT_ACC(M_, 24 + pr);
#undef M_
      } else {
        sts16(c.L, WL + (w * 16 + 8 + pr) * 2048 + lane * 16, lo4(f));
        sts16(c.L, WL + (w * 16 + 8 + pr) * 2048 + 1024 + lane * 16, hi4(f));
      }
    }
    SCHED();
  }
  BAR();
  STAMP(0);
  const unsigned vote = wait_verdict(c);
  const int par = vote_b(vote), para = vote_a(vote);
  const int moff = opq(amap(w * 4096 + lane * 16, par) + (turn(h, w) & 1) * 1024), m2 = R_MEM + (turn(h, w) & 2) * 1024;
  unsigned tries = 1u << 12;
  *(volatile LAS unsigned*)(c.L + 160512) = 0u;
  LGKM(0);
  const int idx = opq((lane & 1) ? 0xEEEE : 0x4444);
  const int u_off = opq((4 * w + g) * 16384 + (h ^ (8 * g)) * 256 + cc * 16);
  const int hq = opq((lane | w) ? 1 << 30 : amap(24576 + (32 + h) * 128, par));
  const int l0 = opq(WL + w * 32768 + lane * 16), l1 = opq(MEMS + lane * 16);
  const int vmw = opq(VM + 2 * g * VROW + (32 * w + 2 * cc) * 2);
  const int vmr = opq(VM + (cc >> 1) * VROW + g * 16);
  TICK_DECL;
  anchor(k, true);
  unsigned tg = k.cyc;
  Plan tt = {k.g - NS(100000), NS(T_DX)};
  for (int s = 0; s < NSEG; ++s) {
    const int pb = R_P + (s & 3) * P_SLOT;
    PIN_ACC0(wa); PIN_ACC1(wa); PIN_HOME(wh);
    asm volatile(VO_STEP
        : [bd] "+s"(c.budget), [rt] "+s"(tries)
        : [l0] "v"(l0), [l1] "v"(l1), [mo] "v"(moff), [ix] "v"(idx), [vw] "v"(vmw), [vr] "v"(vmr), [uo] "v"(u_off), [w] "s"(w), [rs] "s"(c.rs),
          [sb] "s"(m2 + (s & 3) * MEM_SLOT), [ub] "s"(u_slot(s, para) + (h >> 3) * 16 * 16384), [tg] "s"(tg + T_VLAG), [hq] "v"(hq), [pb] "s"(pb),
          [ca] "s"(k.cyc - T_DV), [k1] "s"(k.k1), [ga] "s"(k.g), [dr] "n"(T_RV), [pr] "s"(R_P + ((s + 1) & 3) * P_SLOT)
        : CL_S, CL_16_32, CL_160_200, CL_200_216, CL_216_256);
    PIN_ACC0(wa); PIN_ACC1(wa); PIN_HOME(wh);
    if (w == 0) {
      LITE(s);
      LOG(1, s, tg);
      tg = next_go(c, k, tt, par, pb, ask_stamps(c, par, pb));
      LOG(2, s, k.cyc); LOG(3, s, k.g);
    }
    TICK_SUM(10);
    if (w == 0) { close_in(k, tg); tg = to_cyc(k, tt.gx); }
  }
  TICK_OUT();
  if (c.budget == 0u) __hip_atomic_store(c.ctrl + C_TMO / 4, 1u, RLX_AGENT);
}

DEV void y_role(const Params& p, Ctx& c, int y) {
  constexpr int ZR = 0, UL = 131072, CW = 155648;
  c.budget = SPIN_CAP;
  const int w = c.w, lane = c.lane = fresh_lane(), g = lane >> 4, cc = lane & 15;
  const v4u sent4 = {SENT, SENT, SENT, SENT};
  STAMP(2);
  Clk k = {0u, 0u, 0u, 0u, 0u, 0u};
  anchor(k, true);
  unsigned m0 = 0;
  if (w == 0) {
    unsigned best[2] = {~0u, ~0u};
    st4s(c.rs, y * 16384 + (lane >> 5) * 8192 + (lane & 31) * 4, R_U, SENT);
#pragma unroll 1
    for (int n = 0; n < 12; ++n) {
#pragma unroll
      for (int r = 0; r < 2; ++r) {
        VMWAIT(0);
        const unsigned t0 = (unsigned)__builtin_amdgcn_s_memtime();
        LGKM(0);
        st4s(c.rs, y * 16384 + (lane & 31) * 4, R_U + r * 8192, SENT);
        VMWAIT(0);
        const unsigned t1 = (unsigned)__builtin_amdgcn_s_memtime();
        LGKM(0);
        best[r] = min(best[r], t1 - t0);
      }
      if (n + 1 >= PROBES && min(best[0], best[1]) < 600u && max(best[0], best[1]) > min(best[0], best[1]) + 128u) break;
    }
    STAMP(5);
    claim(c, CL_VOTED | (best[1] < best[0] ? CL_A : 0u));
    m0 = *(const GAS unsigned*)(p.mem0 + (lane >> 3) * 1024 + 16 * y + 2 * (lane & 7));
    m0 = m0 < SENT - 1u ? m0 : SENT - 1u;
    if (lane < 8) *(LAS unsigned*)(c.L + CW + lane * 4) = 0u;
    const unsigned v = wait_vote(c);
    STAMP(7);
    const int oa = y * 16384 + vote_a(v) * 8192 + lane * 16, ob = amap(y * 3072 + lane * 16, vote_b(v));
#pragma unroll
    for (int i = 0; i < 8; ++i) st16s(c.rs, oa + (i & 3) * 1024, R_U + (i >> 2) * 4096, sent4);
#pragma unroll
    for (int i = 0; i < 3; ++i) st16s(c.rs, amap(y * 3072 + i * 1024 + lane * 16, vote_b(v)), R_MEM, sent4);
#pragma unroll
    for (int i = 0; i < 8; ++i) st16s(c.rs, oa + (i & 3) * 1024, R_PT + (i >> 2) * 4096, sent4);
    (void)ob;
    VMWAIT(0);
    if (lane == 0) __hip_atomic_fetch_add(c.ctrl + (C_CLAIM + c.att * 1024) / 4 + c.slot, CL_FILLED, RLX_AGENT);
    STAMP(6);
  }
  BAR();
  const unsigned vote = wait_verdict(c);
  const int par = vote_b(vote), para = vote_a(vote);
  const int mo = amap(y * 256 + lane * 4, par);
  if (w == 0) st4s(c.rs, mo, R_MEM, m0);
  STAMP(0);
  const int rot = y >> 4;
  volatile LAS unsigned* const GO = (volatile LAS unsigned*)(c.L + CW);
  TICK_DECL;
  if (w == 0) {
    int xo[4];
#pragma unroll
    for (int i = 0; i < 4; ++i) xo[i] = amap((i ^ rot) * 4096 + lane * 16, par);
    const int pmo = amap(16384 + rot * 1024 + 2 * g * 128 + cc * 8, par), psel = (cc & 1) ? 0x03020706 : 0x05040100;
    constexpr int lead = T_U0, late = 0;
    anchor(k, true);
    Plan tt = {k.g - NS(100000), NS(T_DX)};
    unsigned done = k.cyc;
    const int so = amap(24576, par) + lane * 128;
    const int idx = opq((lane & 1) ? 0xEEEE : 0x4444);
    const int uo = y * 16384 + lane * 16;
    const int mw = amap(y * 256 + (2 * g + (cc & 1)) * 32 + (cc >> 1) * 4, par);
    for (int s = 0; s < NSEG - 1; ++s) {
      if (s == 0) RAWBAR();
      const int za = ZR + (s & 3) * 32768 + rot * 8192 + lane * 16;
      v32u z0, z1, z2, z3;
      asm volatile(
          "ds_read_b128 a[0:3], %[z0] offset:0\n\t" "ds_read_b128 a[4:7], %[z0] offset:1024\n\t" "ds_read_b128 a[8:11], %[z0] offset:2048\n\t" "ds_read_b128 a[12:15], %[z0] offset:3072\n\t"
          "ds_read_b128 a[16:19], %[z0] offset:4096\n\t" "ds_read_b128 a[20:23], %[z0] offset:5120\n\t" "ds_read_b128 a[24:27], %[z0] offset:6144\n\t" "ds_read_b128 a[28:31], %[z0] offset:7168\n\t"
          "ds_read_b128 a[32:35], %[z1] offset:0\n\t" "ds_read_b128 a[36:39], %[z1] offset:1024\n\t" "ds_read_b128 a[40:43], %[z1] offset:2048\n\t" "ds_read_b128 a[44:47], %[z1] offset:3072\n\t"
          "ds_read_b128 a[48:51], %[z1] offset:4096\n\t" "ds_read_b128 a[52:55], %[z1] offset:5120\n\t" "ds_read_b128 a[56:59], %[z1] offset:6144\n\t" "ds_read_b128 a[60:63], %[z1] offset:7168\n\t"
          "ds_read_b128 a[64:67], %[z2] offset:0\n\t" "ds_read_b128 a[68:71], %[z2] offset:1024\n\t" "ds_read_b128 a[72:75], %[z2] offset:2048\n\t" "ds_read_b128 a[76:79], %[z2] offset:3072\n\t"
          "ds_read_b128 a[80:83], %[z2] offset:4096\n\t" "ds_read_b128 a[84:87], %[z2] offset:5120\n\t" "ds_read_b128 a[88:91], %[z2] offset:6144\n\t" "ds_read_b128 a[92:95], %[z2] offset:7168\n\t"
          "ds_read_b128 a[96:99], %[z3] offset:0\n\t" "ds_read_b128 a[100:103], %[z3] offset:1024\n\t" "ds_read_b128 a[104:107], %[z3] offset:2048\n\t" "ds_read_b128 a[108:111], %[z3] offset:3072\n\t"
          "ds_read_b128 a[112:115], %[z3] offset:4096\n\t" "ds_read_b128 a[116:119], %[z3] offset:5120\n\t" "ds_read_b128 a[120:123], %[z3] offset:6144\n\t" "ds_read_b128 a[124:127], %[z3] offset:7168\n\t"
          "s_waitcnt lgkmcnt(0)"
          : "={a[0:31]}"(z0), "={a[32:63]}"(z1), "={a[64:95]}"(z2), "={a[96:127]}"(z3) : [z0] "v"(za), [z1] "v"(za ^ 8192), [z2] "v"(za ^ 16384), [z3] "v"(za ^ 24576) : "memory");
      const int pb = R_P + (s & 3) * P_SLOT;
      v32u uu;
      const int ub = u_slot(s, para);
      TICK(0);
      anchor(k);
      TICK(8);
      unsigned gp = tt.gx + tt.dx + NS(T_M1), lt = late, sv = SENT;
      unsigned bad = (int)(done - to_cyc(k, tt.gx)) > T_OVH;
      TICK(4);
      LOG(2, s, k.cyc); LOG(3, s, k.g);
#pragma unroll 1
      for (;;) {
        if (bad) {
          const bool all = allok(sv);
          const unsigned last = latest(c, k, par, pb, sv);
          if (!all) mark(k);
          gp = when_p(tt, last);
          lt = 1;
        }
        close_in(k, to_cyc(k, gp) - (lt ? 96 : lead));
        const unsigned tg = to_cyc(k, gp);
        if (!bad) LOG(1, s, tg);
        asm volatile(Y_STEP
          : "=&{v[144:175]}"(uu), [bad] "=&s"(bad), [sv] "=&v"(sv)
          : [x0] "v"(xo[0]), [x1] "v"(xo[1]), [x2] "v"(xo[2]), [x3] "v"(xo[3]), [pm] "v"(pmo), [u0] "v"(uo + (rot ^ (y & 3)) * 2048), [mw] "v"(mw), [ps] "v"(psel), [ix] "v"(idx),
            [rs] "s"(c.rs), [pb] "s"(pb), [ub] "s"(ub), [mb] "s"(R_MEM + ((s + 1) & 3) * MEM_SLOT), [mr] "s"(R_MEM + ((s + 3) & 3) * MEM_SLOT), [tu] "s"(tg - (lt ? 96 : lead)), [tg] "s"(tg - 32), [late] "s"(lt),
            [so] "v"(so), [bd] "s"(c.budget),
            "{a[0:31]}"(z0), "{a[32:63]}"(z1), "{a[64:95]}"(z2), "{a[96:127]}"(z3)
          : CL_S, CL_120_128, CL_176_200, CL_200_216, CL_216_256);
        bad = __builtin_amdgcn_readfirstlane(bad);
        if (bad != 1u) break;
        spend(c);
      }
      done = bad;
      SCHED();
      const int ul = opq(UL + rot * 2048 + lane * 16);
#pragma unroll
      for (int i = 0; i < 8; ++i) sts16(c.L, (ul ^ ((i >> 1) * 2048)) + (i & 1) * 1024, v4u{uu[4 * i], uu[4 * i + 1], uu[4 * i + 2], uu[4 * i + 3]});
      asm volatile("" ::: "memory");
      *GO = s + 1;
      asm volatile("s_waitcnt vmcnt(2)" : "+v"(sv));
      advance(tt, latest(c, k, par, pb, sv));
      TICK(3);
      LITE(s); LOG(0, s, done);
      TICK_LDS(CW + 256);
    }
    {
      anchor(k);
      wait_cyc(to_cyc(k, tt.gx + tt.dx + NS(T_M1)));
      const int ub = u_slot(NSEG - 1, para);
      v4u u[8];
#pragma unroll 1
      for (;;) {
        asm volatile("" ::: "memory");
#pragma unroll
        for (int i = 0; i < 8; ++i) u[i] = ld16s(c.rs, uo + (i & 3) * 1024, ub + (i >> 2) * 4096);
        SCHED();
        unsigned t = 0;
#pragma unroll
        for (int i = 0; i < 8; ++i) t = umax(t, top(u[i]));
        if (allok(t) || spend(c)) break;
      }
      RAWBAR();
#pragma unroll
      for (int i = 0; i < 8; ++i) sts16(c.L, UL + (i ^ (2 * (y & 3))) * 1024 + lane * 16, u[i]);
      asm volatile("" ::: "memory");
      *GO = NSEG;
    }
    TICK_DUMP(CW + 256);
  } else if (w < 3) {
    const rsrc_t ro = mkrs(p.out, 2048 * 2048 * 2);
    auto tokens = [&](auto QT) {
      constexpr int qt = QT.value, NX = qt ? 32 : 16;
      RAWBAR();
      for (int s = 0; s < NSEG; ++s) {
        const int tb = R_PT + (s & 7) * PT_SLOT + para * 8192;
        int ua[4], za[4], xb[4], mb[4];
#pragma unroll
        for (int i = 0; i < 4; ++i) {
          ua[i] = opq(UL + (i ^ rot) * 2048 + lane * 16);
          za[i] = opq(ZR + (s & 3) * 32768 + (i ^ rot) * 8192 + (qt ? lane * 16 : (g >> 1) * 1024 + (g & 1) * 512 + cc * 16));
          xb[i] = tb + amap(qt * 16384 + (i ^ rot) * (qt ? 8192 : 4096), 0);
          mb[i] = tb + amap(49152 + qt * 8192, 0) + (i ^ rot) * 2048;
        }
        SCHED();
        RAWBAR();
        if (TOK_WAIT) __builtin_amdgcn_s_sleep(TOK_WAIT);
        TICK(0);
        f32x4 a;
#pragma unroll 1
        for (;;) {
          asm volatile("" ::: "memory");
          v4u pt[NX + 8];
#pragma unroll
          for (int i = 0; i < NX; ++i) pt[i] = ld16s(c.rs, (i & 3) * 1024 + lane * 16, qt ? xb[i >> 3] + ((i >> 2) & 1) * 4096 : xb[i >> 2]);
#pragma unroll
          for (int i = 0; i < 8; ++i) pt[NX + i] = ld16s(c.rs, (i & 1) * 1024 + lane * 16, mb[i >> 1]);
          SCHED();
          TICK(1);
#pragma unroll 1
          while (__builtin_amdgcn_readfirstlane(*GO) < (unsigned)s + 1u) { if (spend(c)) break; }
          asm volatile("" ::: "memory");
          f32x4 d[2] = {f32x4{0.f, 0.f, 0.f, 0.f}, f32x4{0.f, 0.f, 0.f, 0.f}};
          v4u fa[8];
          auto frag = [&](int i) {
            if (i >= NX) fa[i & 7] = lds16(c.L, ua[(i - NX) >> 1] + (i & 1) * 1024);
            else if (qt) fa[i & 7] = lds16(c.L, za[i >> 3] + (i & 7) * 1024);
            else {
              const v2u lo = *(LAS v2u*)(c.L + za[i >> 2] + (i & 3) * 2048), hi = *(LAS v2u*)(c.L + za[i >> 2] + (i & 3) * 2048 + 256);
              fa[i & 7] = v4u{lo.x, lo.y, hi.x, hi.y};
            }
          };
#pragma unroll
          for (int i = 0; i < 7; ++i) frag(i);
          SCHED();
#pragma unroll
          for (int i = 0; i < NX + 8; ++i) {
            if (i + 7 < NX + 8) frag(i + 7);
            d[i & 1] = mfma16(fa[i & 7], pt[i], d[i & 1]);
            SCHED();
          }
          a = d[0] + d[1];
          if (!__any(a[0] != a[0])) break;
          unsigned t = 0;
#pragma unroll
          for (int i = 0; i < NX + 8; ++i) t = umax(t, top(pt[i]));
          if (allok(t) || spend(c)) break;
        }
        __builtin_amdgcn_raw_buffer_store_b64(v2u{pk(a[0], a[1]), pk(a[2], a[3])}, ro, (16 * qt + cc) * 4096 + (16 * y + 4 * g) * 2, s * 32 * 4096, AUX_NT);
        TICK(2);
        TICK_SUM(3);
      }
    };
    if (w == 1) tokens(IC<0>{}); else tokens(IC<1>{});
  } else {
    unsigned seenZ = NOFLAG;
    const rsrc_t rc = mkrs((const void*)c.ctrl, CTRL_BYTES);
    const int fo = lane < 32 ? lane * 128 : 1 << 30;
    __builtin_amdgcn_s_setprio(3);
    auto least = [&](unsigned v) {
      v |= lane < 32 ? 0u : ~0u;
#pragma unroll
      for (int o = 1; o < 32; o <<= 1) v = min(v, (unsigned)__shfl_xor((int)v, o));
      seenZ = umax(seenZ, __builtin_amdgcn_readfirstlane(v));
    };
    auto await = [&](unsigned need) {
#pragma unroll 1
      while (seenZ < need) {
        asm volatile("" ::: "memory");
        least(__builtin_amdgcn_raw_buffer_load_b32(rc, fo, C_FLAG + (F_Z + (y >> 4) * 32) * 128, AUX_SC1));
        if (seenZ >= need || spend(c)) break;
        __builtin_amdgcn_s_sleep(4);
      }
      __builtin_amdgcn_fence(__ATOMIC_ACQUIRE, "wavefront");
    };
    for (int n = 0; n < NSEG; ++n) {
      TICK(0);
      await(n + 1);
      TICK(1);
      v4u z[32];
      int za[8];
#pragma unroll
      for (int i = 0; i < 8; ++i) za[i] = opq(ZR + (n & 3) * 32768 + (i ^ z_mix(y)) * 1024 + lane * 16);
      unsigned look;
#pragma unroll
      for (int i = 0; i < 32 + Z_WIN; ++i) {
        if (i < 32) z[i] = ld16z(c.rs, (i & 3) * 1024 + lane * 16, R_Z + (n & (ZRING - 1)) * Z_SLOT + y * 65536 + ((para ^ (y >> 5)) & 1) * 8192 + amap((i >> 2) * 4096, 0));
        if (i == 32) { look = __builtin_amdgcn_raw_buffer_load_b32(rc, seenZ < (unsigned)n + 2u ? fo : 1 << 30, C_FLAG + (F_Z + (y >> 4) * 32) * 128, AUX_SC1); TICK(4); }
        SCHED();
        if (i >= Z_WIN) sts16(c.L, za[(i - Z_WIN) & 7] + ((i - Z_WIN) >> 3) * 8192, z[i - Z_WIN]);
        else asm volatile("s_nop " STR(Z_GAP));
        if (i == 30 + Z_WIN) { TICK(3); }
        SCHED();
      }
      least(look);
      if (n == 0) await(PRIME);
      TICK(2);
      { const int s = n; (void)s; TICK_SUM(3); }
      BAR();
    }
    RAWBAR();
  }
  TICK_OUT();
  if (c.budget == 0u) __hip_atomic_store(c.ctrl + C_TMO / 4, 1u, RLX_AGENT);
}

#define MF1A(d, a, b) asm volatile(MF16 "%0, %1, %2, %0" : "+v"(d) : "a"(a), "v"(b))
#define MF32A(d, a, b) asm volatile("v_mfma_f32_32x32x16_bf16 %0, %1, %2, %0" : "+v"(d) : "a"(a), "v"(b))
#define MF32AZ(d, a, b) asm volatile("v_mfma_f32_32x32x16_bf16 %0, %1, %2, 0" : "=&v"(d) : "a"(a), "v"(b))
#define MF1B(d, a, b) asm volatile(MF16 "%0, %1, %2, %0" : "+v"(d) : "v"(a), "a"(b))
#define MF1BZ(d, a, b) asm volatile(MF16 "%0, %1, %2, 0" : "=&v"(d) : "v"(a), "a"(b))
#define MF1AZ(d, a, b) asm volatile(MF16 "%0, %1, %2, 0" : "=&v"(d) : "a"(a), "v"(b))
DEV void g_role(const Params& p, Ctx& c, int mat, int h, int gi) {
  constexpr int EX = 131072;
  c.budget = SPIN_CAP;
  const int w = c.w, lane = c.lane = fresh_lane(), g = lane >> 4, cc = lane & 15;
  if (mat < 2) {
    unsigned t = 0;
#pragma unroll
    for (int i = 0; i < 3; ++i) t += __builtin_amdgcn_raw_buffer_load_b32(mkrs(p.Wv, 8 * MB), (256 * w + (lane >> 1)) * 8192 + h * 256 + (lane & 1) * 128, (2 + 2 * i + mat) * 32 * 8192, 0);
    if (mat == 0) {
      hold(c, X_AT);
#pragma unroll
      for (int i = 0; i < 4; ++i) t += __builtin_amdgcn_raw_buffer_load_b32(mkrs(p.x, 4 * MB), ((4 * h + w) * 64 + lane) * 128, i * MB, 0);
    }
    asm volatile("" :: "v"(t));
    claim(c, 0);
    hold(c, (mat ? GK_AT : GQ_AT) - (slow_head(p.Wq, h) ? SLOW_LEAD : 0));
  }
  const rsrc_t rx = mkrs(p.x, 2048 * 2048);
  const int xs = w * 16384 + ((lane * 16) ^ ((w & 1) << 7));

  v32u wa[8];
  {
    const rsrc_t rw = mkrs(mat == 0 ? p.Wq : mat == 1 ? p.Wk : p.Wv, 1024 * 4096 * 2);
    const int vo = (256 * w + 8 * g) * 8192 + (h * 128 + 8 * cc) * 2;
    v4u row[8][8];
    auto request = [&](int u) {
#pragma unroll
      for (int r = 0; r < 8; ++r) row[u][r] = ld16w(rw, vo, (32 * u + r) * 8192);
    };
#pragma unroll
    for (int u = 0; u < 2; ++u) request(u);
#pragma unroll
    for (int u = 0; u < 16; ++u) dma16(rx, c.L + w * 16384 + u * 1024, (xs ^ ((u >> 1) << 4)) + u * 1024);
#pragma unroll
    for (int u = 2; u < 6; ++u) request(u);
    SCHED();
    const int sw_ = EX + w * 8192 + g * 256;
    const int sr0 = opq(EX + (cc & 3) * 1024 + g * 256 + ((4 * w + (cc >> 2) + 4 * cc) & 15) * 16), sr1 = sr0 + 4096;
#pragma unroll
    for (int u = 0; u < 8; ++u) {
#pragma unroll
      for (int i = 0; i < 8; ++i) {
        v4u f;
#pragma unroll
        for (int r = 0; r < 4; ++r) f[r] = prm(row[u][2 * r + 1][i >> 1], row[u][2 * r][i >> 1], i & 1);
        sts16(c.L, sw_ + i * 1024 + ((cc + 4 * i) & 15) * 16, f);
      }
      SCHED();
      if (u + 6 < 8) request(u + 6);
      if (u == 7 && mat == 2) claim(c, 0);
      BAR();
      v8u in[4];
#pragma unroll
      for (int q = 0; q < 4; ++q) in[q] = cat(lds16(c.L, sr0 + q * 8192), lds16(c.L, sr1 + q * 8192));
      SCHED();
#pragma unroll
      for (int q = 0; q < 4; ++q) {
        v8u f = in[q];
#define M_(r) asm volatile("" : "+" r(f))
        AT_ACC(M_, 8 * q + u);
#undef M_
#pragma unroll
        for (int r = 0; r < 8; ++r) wa[2 * q + (u >> 2)][8 * (u & 3) + r] = f[r];
      }
      BAR();
    }
  }
  STAMP(0);
  const int xpar = vote_a(wait_vote(c)) ^ (mat >> 1);
  TICK_DECL;
  asm volatile(G_LOOP
      :: [xo] "v"(cc * 2048 + ((g ^ cc) << 4)), [xs] "v"(xs), [so] "v"((4 * w + g) * 512 + cc * 16), [fo] "v"((w | lane) ? 1 << 30 : 0),
         [uo] "v"(mat == 1 ? R_U + (2 * h + (w >> 1)) * 16384 + (w & 1) * 4096 + lane * 16 : 1 << 30), [at] "v"(mat == 1 ? 256 * c.att : 0),
         [rs] "s"(c.rs), [rx] "s"(rx), [rc] "s"(mkrs((const void*)c.ctrl, CTRL_BYTES)), [fl] "s"(C_FLAG + (mat * 32 + h) * 128), [xp] "s"(xpar),
         [ob] "s"((mat == 1 ? R_KX : R_QX) + h * 16384), [lw] "s"(w * 16384), GTRACE ACC_IN(wa)
      : CL_S, CL_16_32, CL_32_100);
}

DEV void zc_role(const Params& p, Ctx& c, int h) {
  c.budget = SPIN_CAP;
  const int w = c.w, lane = c.lane = fresh_lane(), g = lane >> 4, cc = lane & 15, h2 = lane >> 5, r32 = lane & 31;
  v32u wa[8];
  hold(c, ZC_AT);
  {
    const rsrc_t rw = mkrs(p.Wo, 4096 * 1024 * 2);
    const int vo = (h * 128 + 8 * g) * 2048 + (256 * w + 8 * cc) * 2;
    v4u row[8][8];
    auto request = [&](int u) {
#pragma unroll
      for (int r = 0; r < 8; ++r) row[u][r] = ld16w(rw, vo, (32 * (u & 3) + r) * 2048 + (u >> 2) * 256);
    };
#pragma unroll
    for (int u = 0; u < 2; ++u) request(u);
    SCHED();
    claim(c, 0);
    SCHED();
#pragma unroll
    for (int u = 2; u < 6; ++u) request(u);
    const int sw_ = w * 8192 + g * 256, sr_ = w * 8192 + (cc & 7) * 1024 + g * 256;
    v8u two[8];
#pragma unroll
    for (int u = 0; u < 8; ++u) {
#pragma unroll
      for (int i = 0; i < 8; ++i) {
        v4u f;
#pragma unroll
        for (int r = 0; r < 4; ++r) f[r] = prm(row[u][2 * r + 1][i >> 1], row[u][2 * r][i >> 1], i & 1);
        sts16(c.L, sw_ + i * 1024 + ((cc + 2 * i) & 15) * 16, f);
      }
      SCHED();
      if (u + 6 < 8) request(u + 6);
      LGKM(0);
#pragma unroll
      for (int q = 0; q < 8; ++q) {
        const v4u o = lds16(c.L, sr_ + ((2 * q + (cc >> 3) + 2 * cc) & 15) * 16);
#pragma unroll
        for (int r = 0; r < 4; ++r) two[q][4 * (u & 1) + r] = o[r];
      }
      SCHED();
      if (u & 1) {
#pragma unroll
        for (int q = 0; q < 8; ++q) {
          const int n = 2 * (8 * (u >> 2) + q) + ((u >> 1) & 1);
#define M_(r) asm volatile("" : "+" r(two[q]))
          AT_ACC(M_, n);
#undef M_
#pragma unroll
          for (int r = 0; r < 8; ++r) wa[n >> 2][8 * (n & 3) + r] = two[q][r];
        }
      }
      LGKM(0);
      SCHED();
    }
  }
  STAMP(0);
  BAR();
  const int para = vote_a(wait_vote(c));
  const int ao = g * 512 + cc * 16, zo = 16 * w * 65536 + lane * 16 + ((para ^ (w >> 1)) & 1) * 8192;
  TICK_DECL;
  asm volatile(ZC_LOOP
      : [bd] "+s"(c.budget)
      : [ao] "v"(ao), [a1] "v"(ao + 4096), [z1] "v"(zo + amap((h ^ z_mix(16 * w)) * 1024, 0)), [z2] "v"(zo + 65536 + amap((h ^ z_mix(16 * w + 1)) * 1024, 0)), [fo] "v"(lane ? 1 << 30 : 0),
        [rs] "s"(c.rs), [rc] "s"(mkrs((const void*)c.ctrl, CTRL_BYTES)), [fv] "s"(C_FLAG + (F_V + h) * 128), [fs] "s"(C_FLAG + (F_S + h) * 128),
        [fz] "s"(C_FLAG + (F_Z + w * 32 + h) * 128), [xp] "s"(para ^ 1), [vb] "s"(R_QX + h * 16384), [vd] "s"(C_VERDICT + c.att * 128), [z0] "n"(ZC_PRIME), [zb] "n"(R_Z), [rg] "n"(ZRING - 2), [rm] "n"(ZRING - 1), GTRACE ACC_IN(wa)
      : CL_S, CL_16_32, CL_32_100);
  if (c.budget == 0u) __hip_atomic_store(c.ctrl + C_TMO / 4, 1u, RLX_AGENT);
}

DEV void conv_role(const Params& p, Ctx& c, int cg, int far) {
  constexpr int FS = 4160, PL = 1280, UO = 4 * FS, WS = UO + 8 * PL;
  const int w = c.w, lane = c.lane = fresh_lane(), tid = w * 64 + lane, g = lane >> 4, cc = lane & 15;
  const int d0 = 8 * cg + 2 * w;
  const int pg = ((far ^ (cg >> 5)) & 1) * 8192;
  auto crow = [](int ch) { return (ch >> 1) * 16384 + (ch & 1) * 4096; };
  const int de = (cg >> 3) * 64 + (tid & 7) * 8, t0 = (cg & 7) * 256 + (tid >> 3) * 8;
  auto group_sync = [&](int phase) {
    gu32* cnt = c.ctrl + (C_CONV + (cg >> 3) * 16 + phase * 8) / 4;
    VMWAIT(0);
    BAR();
    if (w == 0) {
      if (lane == 0) __hip_atomic_fetch_add(cnt, 1u, RLX_AGENT);
#pragma unroll 1
      while (__builtin_amdgcn_readfirstlane(__hip_atomic_load(cnt, RLX_AGENT)) < 8u) { if (spend(c)) break; __builtin_amdgcn_s_sleep(2); }
    }
    BAR();
  };
  v4u fr[2][4], ur[2][4], qr[8];
  unsigned up[2][4];
  const int so = cc * 64 + g * 16;
  const unsigned dd = __builtin_amdgcn_raw_buffer_load_b32(mkrs(p.ssmD, 2048), d0 * 2, 0, CONV_AUX);
  BAR();
  {
    const rsrc_t rk = mkrs(p.bk, 2048 * 2048), rv = mkrs(p.bv, 2048 * 2048);
    v4u kr[11], vr[8], sh[4];
#pragma unroll
    for (int i = 0; i < 4; ++i) sh[i] = ld16c(mkrs(p.shift, 8192), de * 8 + i * 16, 0);
#pragma unroll
    for (int i = 0; i < 11; ++i) kr[i] = ld16c(rk, (t0 + i - 3) * 2048 + de * 2, 0);
#pragma unroll
    for (int i = 0; i < 8; ++i) vr[i] = ld16c(rv, (t0 + i) * 2048 + de * 2, 0);
#pragma unroll
    for (int i = 0; i < 8; ++i) fr[i >> 2][i & 3] = ld16c(mkrs(p.ssmk, 4 * MB), lane * 16 + (i & 3) * 1024, (d0 + (i >> 2)) * 4096);
    SCHED();
    if (lane < 16) sts16(c.L, w * WS + (lane >> 2) * FS + (lane & 3) * 16, v4u{0, 0, 0, 0});
#pragma unroll
    for (int i = 0; i < 2; ++i) sts16(c.L, w * WS + UO + (4 * i + g) * PL + cc * 16, v4u{0, 0, 0, 0});
#pragma unroll
    for (int ch = 0; ch < 8; ++ch) {
      const unsigned sa = sh[ch >> 1][(ch & 1) * 2], sb = sh[ch >> 1][(ch & 1) * 2 + 1];
      const float s0 = blo(sa), s1 = bhi(sa), s2 = blo(sb), s3 = bhi(sb);
      float kk[11], o[8];
#pragma unroll
      for (int i = 0; i < 11; ++i) kk[i] = (ch & 1) ? bhi(kr[i][ch >> 1]) : blo(kr[i][ch >> 1]);
#pragma unroll
      for (int i = 0; i < 8; ++i) o[i] = fmaf(kk[i + 3], s0, fmaf(kk[i + 2], s1, fmaf(kk[i + 1], s2, kk[i] * s3))) * ((ch & 1) ? bhi(vr[i][ch >> 1]) : blo(vr[i][ch >> 1]));
      st16s(c.rs, de * 8192 + t0 * 2, R_KVT + pg + crow(ch), v4u{pk(o[0], o[1]), pk(o[2], o[3]), pk(o[4], o[5]), pk(o[6], o[7])});
    }
    group_sync(0);
#pragma unroll
    for (int i = 0; i < 8; ++i) {
      const int b = R_KVT + pg + d0 * 8192 + (i >> 2) * 4096 + (i & 3) * 1024;
      ur[i >> 2][i & 3] = ld16s(c.rs, so, b);
      up[i >> 2][i & 3] = __builtin_amdgcn_raw_buffer_load_b32(c.rs, (i & 3) == 0 && lane == 0 ? 1 << 30 : so, b - 4, AUX_SC1);
    }
#pragma unroll
    for (int i = 0; i < 8; ++i) qr[i] = ld16c(mkrs(p.bq, 4 * MB), (t0 + i) * 2048 + de * 2, 0);
  }
  STAMP(5);
  const int fw = opq(w * WS + 48 + lane * 16), uw = opq(w * WS + UO + g * PL + cc * 16);
  const int fa = opq(w * WS + (cc & 3) * FS + (3 + (cc >> 2) - g) * 16);
  sfor<2>([&](auto CH) __attribute__((always_inline)) {
    constexpr int ch = CH.value;
    SCHED();
#pragma unroll
    for (int i = 0; i < 4; ++i) {
      const v4u f = fr[ch][i], a = ur[ch][i];
      sts16(c.L, fw + 3 * FS + i * 1024, f);
      sts8(c.L, fw + FS + i * 1024 + 8, v2u{f[0], f[1]}); sts8(c.L, fw + FS + i * 1024 + 16, v2u{f[2], f[3]});
#pragma unroll
      for (int r = 0; r < 2; ++r) {
        const int o = fw + 2 * r * FS + i * 1024 + 12 - 8 * r;
        *(LAS unsigned*)(c.L + o) = f[0]; sts8(c.L, o + 4, v2u{f[1], f[2]}); *(LAS unsigned*)(c.L + o + 12) = f[3];
      }
      v4u r0, r1;
#pragma unroll
      for (int r = 0; r < 4; ++r) {
        r0[r] = __builtin_amdgcn_alignbyte(a[3 - r], a[3 - r], 2);
        r1[r] = __builtin_amdgcn_perm(r < 3 ? a[2 - r] : up[ch][i], a[3 - r], 0x07060100u);
      }
      sts16(c.L, uw + (16 + 16 * i) * 16, r0);
      sts16(c.L, uw + 4 * PL + (16 + 16 * i) * 16, r1);
    }
    f32x4 acc[4][2];
#pragma unroll
    for (int i = 0; i < 8; ++i) acc[i >> 1][i & 1] = f32x4{0.f, 0.f, 0.f, 0.f};
    v4u A[2][4], B[2][8];
    auto frag = [&](int d, int n) {
      constexpr int order[12] = {0, 4, 5, 6, 7, 8, 9, 10, 11, 1, 2, 3};
      const int x = order[n];
      if (x < 4) A[d & 1][x] = lds16(c.L, fa + (d + 16 * x) * 64);
      else B[d & 1][x - 4] = lds16(c.L, uw + ((x - 4) & 1) * 4 * PL + (16 + 16 * ((x - 4) >> 1) - d) * 16);
    };
    asm volatile("" ::: "memory");
#pragma unroll
    for (int n = 0; n < 12; ++n) frag(0, n);
    SCHED();
    sfor<16>([&](auto D) __attribute__((always_inline)) {
      constexpr int d = D.value;
      int n = 0;
#pragma unroll
      for (int k = 0; k < 4; ++k)
#pragma unroll
        for (int m = 0; m + k < 4; ++m)
#pragma unroll
          for (int e = 0; e < 2; ++e) {
            acc[m + k][e] = mfma16(A[d & 1][k], B[d & 1][2 * m + e], acc[m + k][e]);
            if (d < 15 && n < 12) frag(d + 1, n);
            ++n;
            SCHED();
          }
    });
    const float dv = ch ? bhi(dd) : blo(dd);
#pragma unroll
    for (int q = 0; q < 4; ++q) {
      v4u z;
#pragma unroll
      for (int i = 0; i < 4; ++i) {
        const unsigned yy = pk(acc[q][1][i], acc[q][0][i]), ud = pk(blo(ur[ch][q][i]) * dv, bhi(ur[ch][q][i]) * dv);
        z[i] = pk(blo(yy) + blo(ud), bhi(yy) + bhi(ud));
      }
      st16s(c.rs, so + q * 1024, R_YT + pg + d0 * 8192 + ch * 4096, z);
    }
  });
  STAMP(6);
  {
    const rsrc_t ro = mkrs(p.out, 2048 * 2048 * 2);
    v4u yv[8];
    group_sync(1);
#pragma unroll
    for (int ch = 0; ch < 8; ++ch) yv[ch] = ld16s(c.rs, de * 8192 + t0 * 2, R_YT + pg + crow(ch));
#pragma unroll
    for (int i = 0; i < 8; ++i) {
      float o[8];
#pragma unroll
      for (int ch = 0; ch < 8; ++ch) o[ch] = ((ch & 1) ? bhi(qr[i][ch >> 1]) : blo(qr[i][ch >> 1])) * ((i & 1) ? bhi(yv[ch][i >> 1]) : blo(yv[ch][i >> 1]));
      __builtin_amdgcn_raw_buffer_store_b128(v4u{pk(o[0], o[1]), pk(o[2], o[3]), pk(o[4], o[5]), pk(o[6], o[7])}, ro, (t0 + i) * 4096 + (1024 + de) * 2, 0, AUX_NT);
    }
  }
  if (c.budget == 0u) __hip_atomic_store(c.ctrl + C_TMO / 4, 1u, RLX_AGENT);
}

extern "C" __global__ void __launch_bounds__(256, 1) mk_mega(Params p) {
  extern __shared__ __attribute__((aligned(16))) unsigned char lds_raw[];
  Ctx c;
  c.ctrl = (gu32*)p.ctrl;
  c.rs = mkrs(p.data, R_END);
  c.L = (lds_t)lds_raw;
  c.w = __builtin_amdgcn_readfirstlane(threadIdx.x >> 6);
  c.lane = threadIdx.x & 63;
  c.t0 = (unsigned)__builtin_amdgcn_s_memrealtime();
  c.b = (int)(blockIdx.x & 255u);
  c.att = (int)(blockIdx.x >> 8);
  c.ask = 0;
  if (c.w == 1) {
    unsigned long long base; unsigned len, t;
    asm volatile("s_getpc_b64 s[60:61]\n1:\n\ts_sub_u32 s60, s60, 1b-mk_mega\n\ts_subb_u32 s61, s61, 0\n\ts_mov_b64 %0, s[60:61]\n\ts_mov_b32 %1, mk_tail-mk_mega"
                 : "=s"(base), "=s"(len) :: "s60", "s61", "scc");
    t = __builtin_amdgcn_raw_buffer_load_b32(mkrs((const void*)base, len), (c.lane * 32 + (c.b >> 3)) * 128, 0, 0);
    asm volatile("" :: "v"(t));
  }
  if (c.w == 0 && c.att == 0) {
    c.ask = __builtin_amdgcn_readfirstlane(__hip_atomic_load(c.ctrl + C_ECHO / 4 + c.b, RLX_AGENT)) + 1u;
    if (c.lane == 0) __hip_atomic_store(c.ctrl + (C_ASK + c.b * 64) / 4, c.ask, RLX_AGENT);
  }
  c.slot = (c.att ? (c.b & 7) : (int)((unsigned)__builtin_amdgcn_s_getreg(20 | (3 << 11)) & 7u)) * 32 + (c.b >> 3);
  if (c.b == SERVER && c.w == 3) serve(c);
  const int x = c.slot >> 5, t = c.slot & 31;
  if (x < 4) {
    if (t < 8) qk_role(p, c, x * 8 + t);
    else if (t < 16) vo_role(p, c, x * 8 + t - 8);
    else y_role(p, c, x * 16 + t - 16);
  } else {
    if (t < 24) g_role(p, c, t >> 3, (x - 4) * 8 + (t & 7), t);
    else zc_role(p, c, (x - 4) * 8 + t - 24);
    STAMP(2);
    c.budget = SPIN_CAP;
    conv_role(p, c, (x - 4) * 32 + t, vote_a(wait_verdict(c)) ^ 1);
  }
  STAMP(3);
}

}

extern "C" void kernel_launch(void* const* d_in, const int* in_sizes, int n_in, void* d_out, int out_size, void* d_ws, size_t ws_size,
                              hipStream_t stream) {
  using namespace mk;
  (void)in_sizes; (void)n_in; (void)out_size;
  Params p;
  p.x = (const u16*)d_in[0]; p.mem0 = (const u16*)d_in[1];
  p.Wq = (const u16*)d_in[2]; p.Wk = (const u16*)d_in[3]; p.Wv = (const u16*)d_in[4]; p.Wo = (const u16*)d_in[5];
  p.bq = (const u16*)d_in[7]; p.bk = (const u16*)d_in[8]; p.bv = (const u16*)d_in[9];
  p.shift = (const u16*)d_in[10]; p.ssmk = (const u16*)d_in[11]; p.ssmD = (const u16*)d_in[12];
  p.out = (u16*)d_out;
  p.ctrl = (unsigned char*)d_ws;
  p.data = data_base(d_ws, ws_size);
  (void)hipFuncSetAttribute((const void*)mk_mega, hipFuncAttributeMaxDynamicSharedMemorySize, LDS_BYTES);
  hipLaunchKernelGGL(mk_mega, dim3(256), dim3(256), LDS_BYTES, stream, p);
}
```

```cpp
#include <hip/hip_runtime.h>
#include <hip/hip_bf16.h>

namespace mk {

#define GAS __attribute__((address_space(1)))
#define LAS __attribute__((address_space(3)))
#define DEV __device__ __forceinline__
#define RLX_AGENT __ATOMIC_RELAXED, __HIP_MEMORY_SCOPE_AGENT
typedef unsigned short u16;
typedef unsigned v2u __attribute__((ext_vector_type(2)));
typedef unsigned v4u __attribute__((ext_vector_type(4)));
typedef unsigned v8u __attribute__((ext_vector_type(8)));
typedef unsigned v16u __attribute__((ext_vector_type(16)));
typedef unsigned v32u __attribute__((ext_vector_type(32)));
typedef float f32x2 __attribute__((ext_vector_type(2)));
typedef float f32x4 __attribute__((ext_vector_type(4)));
typedef float f32x16 __attribute__((ext_vector_type(16)));
typedef __bf16 bf16x2 __attribute__((ext_vector_type(2)));
typedef __bf16 bf16x8 __attribute__((ext_vector_type(8)));
typedef __bf16 bf16x16 __attribute__((ext_vector_type(16)));
typedef GAS unsigned gu32;
typedef __amdgpu_buffer_rsrc_t rsrc_t;
typedef LAS unsigned char* lds_t;

#define XCNT(r) ""
#define UCNT ""
#define CL_T
#define XT(a, b) ""
#define LMARK ""
#define GSUM ""
#define ZSUM ""
#define GTRACE
#define MIXED 1
#define NOFLAG 0
#define TOK_KICK 1
#define ZRING 16
#define Z_AT 16
#define Z_GAP 4
#define STR_(x) #x
#define STR(x) STR_(x)
#define GQ_AT 600
#define GK_AT 300
#define PULL_AT 800
#define QK_AT 100
#define SERVER 194
#define SLOW_LEAD 100
#define PULL_DEPTH 1
#define PRIME 2
#define X_AT 150
#define VO_AT 250
#define ZC_AT 0
#define CONV_AUX 2
#define W_AUX 0
#define ZC_PRIME 1
#define CODE_PULL 1
#define PROBES 2
#define Z_WIN 20
#define TOK_WAIT 0
#define Z_AUX 0
#define OUT_AUX 2
#define OUT_AUX2 2
#define URING 8
constexpr int NSEG = 64;
constexpr unsigned SENT = 0xFFFFFFFFu;
constexpr int AUX_SC1 = 16, AUX_NT = 2;
constexpr int T_LAG = 61;
constexpr int G_AHEAD = 4;
constexpr unsigned SPIN_CAP = 1u << 21;
constexpr int LDS_BYTES = 163840;

constexpr int C_ECHO = 0, C_CLAIM = 1024, C_VERDICT = 3072, C_TMO = 3328, C_CONV = 3456, C_VOTE = 3840, C_FLAG = 4096, C_ASK = C_FLAG + 256 * 128;
constexpr int F_Q = 0, F_K = 32, F_V = 64, F_S = 96, F_Z = 128;
constexpr int CTRL_BYTES = C_ASK + 256 * 64;
constexpr int MB = 1 << 20;
constexpr int R_U = 136 * MB;
constexpr int R_MEM = 2 * MB, MEM_SLOT = 32768;
constexpr int R_P = R_MEM + 4 * MEM_SLOT, P_SLOT = 65536;
constexpr int R_PT = 4 * MB, PT_SLOT = 131072;
constexpr int R_QX = 8 * MB, R_KX = 40 * MB, R_Z = 72 * MB, Z_SLOT = 4 * MB, R_KVT = R_KX, R_YT = R_KX + 8 * MB, R_END = (136 + URING) * MB;

constexpr size_t R_DIAG = 0;
#define TOP 1
static inline unsigned char* data_base(void* ws, size_t size) {
  return (unsigned char*)((TOP ? (size_t)ws + size - R_END - R_DIAG : (size_t)ws + CTRL_BYTES + 2 * MB - 1) & ~(size_t)(2 * MB - 1));
}
struct Params {
  const u16 *x, *mem0, *Wq, *Wk, *Wv, *Wo, *bq, *bk, *bv, *shift, *ssmk, *ssmD;
  u16* out;
  unsigned char* ctrl;
  unsigned char* data;
};

DEV rsrc_t mkrs(const void* p, unsigned bytes) { return __builtin_amdgcn_make_buffer_rsrc((void*)p, 0, bytes, 0x00020000); }
DEV v4u ld16s(rsrc_t rs, int v, int s) { return __builtin_amdgcn_raw_buffer_load_b128(rs, v, s, AUX_SC1); }
DEV v2u ld8s(rsrc_t rs, int v, int s) { return __builtin_amdgcn_raw_buffer_load_b64(rs, v, s, AUX_SC1); }
DEV v4u ld16(rsrc_t rs, int v, int s) { return __builtin_amdgcn_raw_buffer_load_b128(rs, v, s, 0); }
DEV v4u ld16z(rsrc_t rs, int v, int s) { return __builtin_amdgcn_raw_buffer_load_b128(rs, v, s, AUX_SC1 | Z_AUX); }
DEV v4u ld16c(rsrc_t rs, int v, int s) { return __builtin_amdgcn_raw_buffer_load_b128(rs, v, s, CONV_AUX); }
DEV v4u ld16w(rsrc_t rs, int v, int s) { return __builtin_amdgcn_raw_buffer_load_b128(rs, v, s, W_AUX); }
DEV v2u ld8(rsrc_t rs, int v, int s) { return __builtin_amdgcn_raw_buffer_load_b64(rs, v, s, 0); }
DEV void dma16(rsrc_t rs, lds_t l, int v) {
#if __HIP_DEVICE_COMPILE__
  __builtin_amdgcn_raw_ptr_buffer_load_lds(rs, (LAS void*)l, 16, v, 0, 0, 0);
#endif
}
DEV void st16s(rsrc_t rs, int v, int s, v4u d) { __builtin_amdgcn_raw_buffer_store_b128(d, rs, v, s, AUX_SC1); }
DEV void st8s(rsrc_t rs, int v, int s, v2u d) { __builtin_amdgcn_raw_buffer_store_b64(d, rs, v, s, AUX_SC1); }
DEV void st4s(rsrc_t rs, int v, int s, unsigned d) { __builtin_amdgcn_raw_buffer_store_b32(d, rs, v, s, AUX_SC1); }
DEV int amap(int o, int par) { return ((o >> 13) << 14) | (par << 13) | (o & 8191); }
DEV int xblk(int s, int h, int par) { return (s * 32 + h) * 16384 + ((par ^ (s >> 2)) & 1) * 8192; }
DEV unsigned umax(unsigned a, unsigned b) { return a > b ? a : b; }
DEV unsigned top(v4u v) { return umax(umax(v.x, v.y), umax(v.z, v.w)); }
DEV unsigned pk(float a, float b) { bf16x2 v = {(__bf16)a, (__bf16)b}; return __builtin_bit_cast(unsigned, v); }
DEV unsigned pks(float a, float b) { unsigned u = pk(a, b); return u < SENT - 1u ? u : SENT - 1u; }
DEV float blo(unsigned u) { return __builtin_bit_cast(float, u << 16); }
DEV float bhi(unsigned u) { return __builtin_bit_cast(float, u & 0xFFFF0000u); }
DEV float rb(float a) { return blo(pk(a, a)); }
DEV unsigned fu(float f) { return __builtin_bit_cast(unsigned, f); }
DEV float uf(unsigned u) { return __builtin_bit_cast(float, u); }
DEV f32x4 mfma16(v4u a, v4u b, f32x4 c) {
  return __builtin_amdgcn_mfma_f32_16x16x32_bf16(__builtin_bit_cast(bf16x8, a), __builtin_bit_cast(bf16x8, b), c, 0, 0, 0);
}
DEV f32x16 mfma32(v4u a, v4u b, f32x16 c) {
  return __builtin_amdgcn_mfma_f32_32x32x16_bf16(__builtin_bit_cast(bf16x8, a), __builtin_bit_cast(bf16x8, b), c, 0, 0, 0);
}
DEV f32x4 smf(v4u a, v8u b, f32x4 c, int idx) {
  return __builtin_amdgcn_smfmac_f32_16x16x64_bf16(__builtin_bit_cast(bf16x8, a), __builtin_bit_cast(bf16x16, b), c, idx, 0, 0);
}
DEV v8u cat(v4u a, v4u b) { return __builtin_shufflevector(a, b, 0, 1, 2, 3, 4, 5, 6, 7); }
DEV v4u lo4(v8u a) { return __builtin_shufflevector(a, a, 0, 1, 2, 3); }
DEV v4u hi4(v8u a) { return __builtin_shufflevector(a, a, 4, 5, 6, 7); }
DEV v4u lds16(lds_t L, int o) { return *(LAS v4u*)(L + o); }
DEV void sts16(lds_t L, int o, v4u v) { *(LAS v4u*)(L + o) = v; }
DEV void sts8(lds_t L, int o, v2u v) { *(LAS v2u*)(L + o) = v; }
#define SCHED() __builtin_amdgcn_sched_barrier(0)
#define PIN_A(x) asm volatile("" : "+a"(x))
#define PIN_V(x) asm volatile("" : "+v"(x))
DEV int opq(int v) { asm volatile("" : "+v"(v)); return v; }
DEV int fresh_lane() {
  int l; asm volatile("v_mbcnt_lo_u32_b32 %0, -1, 0\n\tv_mbcnt_hi_u32_b32 %0, -1, %0" : "=v"(l)); return l;
}
DEV f32x4 fresh_zero() {
  f32x2 a, b;
  asm volatile("v_mov_b64 %0, 0" : "=v"(a));
  asm volatile("v_mov_b64 %0, 0" : "=v"(b));
  return __builtin_shufflevector(a, b, 0, 1, 2, 3);
}
DEV unsigned fresh_sent() { unsigned z; asm volatile("v_mov_b32 %0, -1" : "=v"(z)); return z; }
#define SMF "v_smfmac_f32_16x16x64_bf16 "
#define MF16 "v_mfma_f32_16x16x32_bf16 "
#define SMF1(cls, d, a, b) asm volatile(SMF "%0, %1, %2, %3" : "+v"(d) : "v"(a), cls(b), "v"(idx))
#define SMF1F(cls, lo, hi, d, a, b) asm volatile(SMF "v[" #lo ":" #hi "], %1, %2, %3" : "+{v[" #lo ":" #hi "]}"(d) : "v"(a), cls(b), "v"(idx))
#define AT_HOME(M, j) do { const int j_ = (j); if (j_ == 0) M("{v[32:39]}"); if (j_ == 1) M("{v[40:47]}"); if (j_ == 2) M("{v[48:55]}"); if (j_ == 3) M("{v[56:63]}"); if (j_ == 4) M("{v[64:71]}"); if (j_ == 5) M("{v[72:79]}"); if (j_ == 6) M("{v[80:87]}"); if (j_ == 7) M("{v[88:95]}"); if (j_ == 8) M("{v[96:103]}"); if (j_ == 9) M("{v[104:111]}"); if (j_ == 10) M("{v[112:119]}"); if (j_ == 11) M("{v[120:127]}"); if (j_ == 12) M("{v[128:135]}"); if (j_ == 13) M("{v[136:143]}"); if (j_ == 14) M("{v[144:151]}"); if (j_ == 15) M("{v[152:159]}"); } while (0)
#define MF1(d, a, b) asm volatile(MF16 "%0, %1, %2, %0" : "+v"(d) : "v"(a), "v"(b))
#define MF1Z(d, a, b) asm volatile(MF16 "%0, %1, %2, 0" : "=&v"(d) : "v"(a), "v"(b))
#define LGKM(n) asm volatile("s_waitcnt lgkmcnt(" #n ")" ::: "memory")
#define RAWBAR() do { __builtin_amdgcn_s_barrier(); asm volatile("" ::: "memory"); } while (0)
DEV void settle3(f32x4& s0, f32x4& s1, f32x4& s2) { asm volatile("s_nop 7" : "+v"(s0), "+v"(s1), "+v"(s2)); }
DEV void settle4(f32x4 (&d)[4]) { asm volatile("s_nop 7" : "+v"(d[0]), "+v"(d[1]), "+v"(d[2]), "+v"(d[3])); }
#define BAR() do { asm volatile("s_waitcnt lgkmcnt(0)" ::: "memory"); __builtin_amdgcn_s_barrier(); asm volatile("" ::: "memory"); } while (0)
#define VMWAIT(n) asm volatile("s_waitcnt vmcnt(" #n ")" ::: "memory")
DEV float add1(float a, float b) {
  float o; asm("v_add_f32 %0, %1, %2" : "=v"(o) : "v"(a), "v"(b)); return o;
}
DEV float xmax(float v) {
  auto r = __builtin_amdgcn_permlane32_swap(fu(v), fu(v), false, false); v = fmaxf(uf(r[0]), uf(r[1]));
  auto q = __builtin_amdgcn_permlane16_swap(fu(v), fu(v), false, false); return fmaxf(uf(q[0]), uf(q[1]));
}
DEV float xsum(float v) {
  auto r = __builtin_amdgcn_permlane32_swap(fu(v), fu(v), false, false); v = uf(r[0]) + uf(r[1]);
  auto q = __builtin_amdgcn_permlane16_swap(fu(v), fu(v), false, false); return uf(q[0]) + uf(q[1]);
}

DEV float ror8(float v) { return uf(__builtin_amdgcn_update_dpp(0u, fu(v), 0x128, 0xf, 0xf, true)); }
DEV float fmax2(float a, float b) { return __builtin_amdgcn_fmed3f(a, b, __builtin_inff()); }
DEV float fmax3(float a, float b, float c) { float r; asm("v_max3_f32 %0, %1, %2, %3" : "=v"(r) : "v"(a), "v"(b), "v"(c)); return r; }
DEV bool allok(unsigned t) { return __all(t != SENT); }

template <int N> struct IC { static constexpr int value = N; };
template <int N, class F> DEV void sfor(F&& f) { if constexpr (N > 0) { sfor<N - 1>(f); f(IC<N - 1>{}); } }
struct Ctx {
  gu32* ctrl;
  rsrc_t rs;
  lds_t L;
  int w, lane;
  int b, slot, att;
  unsigned ask;
  unsigned t0;
  unsigned budget;
};
DEV bool spend(Ctx& c) {
  const unsigned old = c.budget;
  asm volatile("s_cmp_lg_u32 %0, 0\n\ts_subb_u32 %0, %0, 0" : "+s"(c.budget) :: "scc");
  return old == 0u;
}
DEV unsigned wait_flag(Ctx& c, int flag, unsigned need) {
  gu32* f = c.ctrl + (C_FLAG + flag * 128) / 4;
  unsigned v;
#pragma unroll 1
  for (;;) {
    v = __builtin_amdgcn_readfirstlane(__hip_atomic_load(f, RLX_AGENT));
    if (v >= need || spend(c)) break;
    __builtin_amdgcn_s_sleep(2);
  }
  __builtin_amdgcn_fence(__ATOMIC_ACQUIRE, "wavefront");
  return v;
}
DEV void post_flag(Ctx& c, int flag, unsigned v, int off = 0) {
  st4s(mkrs((const void*)c.ctrl, CTRL_BYTES), (c.w | off | fresh_lane()) ? 1 << 30 : 0, C_FLAG + flag * 128, v);
}
constexpr unsigned CL_A = 1u << 16, CL_VOTED = 1u << 24, CL_FILLED = 1u << 28;
DEV void claim(Ctx& c, unsigned votes) {
  if (c.w != 0) return;
  if (c.att == 0) {
#pragma unroll 1
    while (__builtin_amdgcn_readfirstlane(__hip_atomic_load(c.ctrl + C_ECHO / 4 + c.b, RLX_AGENT)) != c.ask) {
      if (spend(c)) break;
      __builtin_amdgcn_s_sleep(2);
    }
  }
  if (fresh_lane() == 0) __hip_atomic_fetch_add(c.ctrl + (C_CLAIM + c.att * 1024) / 4 + c.slot, 1u + votes, RLX_AGENT);
}
DEV unsigned claim_look(Ctx& c) { return __hip_atomic_load(c.ctrl + C_ECHO / 4 + c.b, RLX_AGENT); }
DEV bool claim_try(Ctx& c, unsigned look) {
  const bool ok = c.att || __builtin_amdgcn_readfirstlane(look) == c.ask;
  if (ok && c.w == 0 && fresh_lane() == 0) __hip_atomic_fetch_add(c.ctrl + (C_CLAIM + c.att * 1024) / 4 + c.slot, 1u, RLX_AGENT);
  return ok;
}
DEV unsigned wait_verdict(Ctx& c) {
  unsigned v;
#pragma unroll 1
  for (;;) {
    v = __builtin_amdgcn_readfirstlane(__hip_atomic_load(c.ctrl + (C_VERDICT + c.att * 128) / 4, RLX_AGENT));
    if ((v & 1u) || spend(c)) break;
    __builtin_amdgcn_s_sleep(2);
  }
  __builtin_amdgcn_fence(__ATOMIC_ACQUIRE, "wavefront");
  if ((v & 3u) == 3u && c.att == 0) {
    asm volatile("s_waitcnt vmcnt(0) lgkmcnt(0)\n\ts_barrier\n\ts_mov_b64 s[0:1], %0\n\ts_mov_b32 s2, %1\n\tv_mov_b32 v0, %2\n\t"
                 "s_getpc_b64 s[4:5]\n1:\n\ts_sub_u32 s4, s4, 1b-mk_mega\n\ts_subb_u32 s5, s5, 0\n\ts_setpc_b64 s[4:5]"
                 :: "s"(__builtin_amdgcn_kernarg_segment_ptr()), "s"(c.b + 256), "v"(c.w * 64 + fresh_lane()) : "s0", "s1", "s2", "s4", "s5", "v0", "scc", "memory");
    __builtin_unreachable();
  }
  return v;
}
DEV unsigned wait_vote(Ctx& c) {
  unsigned v;
#pragma unroll 1
  for (;;) {
    v = __builtin_amdgcn_readfirstlane(__hip_atomic_load(c.ctrl + C_VOTE / 4, RLX_AGENT));
    if ((v & 1u) || spend(c)) break;
    __builtin_amdgcn_s_sleep(2);
  }
  return v;
}
DEV void serve(Ctx& c) {
  c.budget = SPIN_CAP;
  const int lane = fresh_lane();
  const rsrc_t rc = mkrs((const void*)c.ctrl, CTRL_BYTES);
  if (c.att == 0) {
#pragma unroll
    for (int i = C_CLAIM / 1024; i < C_ASK / 1024; ++i) st16s(rc, lane * 16, i * 1024, v4u{0, 0, 0, 0});
    VMWAIT(0);
  }
  unsigned vote = c.att ? wait_vote(c) : 0u;
  bool echo = c.att == 0, first = true;
  v4u last = {0u, 0u, 0u, 0u};
#pragma unroll 1
  for (;;) {
    asm volatile("" ::: "memory");
    const v4u cl = ld16s(rc, lane * 16, C_CLAIM + c.att * 1024);
    if (echo) {
      v4u e;
#pragma unroll
      for (int i = 0; i < 4; ++i) e[i] = __builtin_amdgcn_raw_buffer_load_b32(rc, (lane * 4 + i) * 64, C_ASK, AUX_SC1);
      if (first || __any(e[0] != last[0] || e[1] != last[1] || e[2] != last[2] || e[3] != last[3])) st16s(rc, lane * 16, C_ECHO, e);
      last = e; first = false;
    }
    unsigned n = 0, one = 1;
    int va = 0, nv = 0, nf = 0;
#pragma unroll
    for (int i = 0; i < 4; ++i) {
      n += cl[i] & 0xFFFFu; one &= (cl[i] & 0xFFFFu) == 1u;
      va += __popcll(__ballot(cl[i] & CL_A)); nv += __popcll(__ballot(cl[i] & CL_VOTED)); nf += __popcll(__ballot(cl[i] & CL_FILLED));
    }
#pragma unroll
    for (int o = 1; o < 64; o <<= 1) n += (unsigned)__shfl_xor((int)n, o);
    const bool all = __builtin_amdgcn_readfirstlane(n) >= 256u, good = __all(one);
    echo = echo && !all;
    if (!vote && (all || max(va, nv - va) > 32)) {
      vote = va > 32 ? 5u : 9u;
      if (lane == 0) __hip_atomic_store(c.ctrl + C_VOTE / 4, vote, RLX_AGENT);
    }
    if (all && (!good || nf >= 64)) {
      if (lane == 0) __hip_atomic_store(c.ctrl + (C_VERDICT + c.att * 128) / 4, vote | (good ? 0u : 2u), RLX_AGENT);
      break;
    }
    if (spend(c)) break;
  }
}
DEV int vote_a(unsigned v) { return (int)((v >> 2) & 1u); }
DEV int vote_b(unsigned v) { return (int)((v >> 3) & 1u); }

#define STAMP(slot) do {} while (0)
#define TICK_DECL
#define LOG(i, s, v) do {} while (0)
#define LOGS(i, s) do {} while (0)
#define LITE(s) do {} while (0)
#define TICK(i) do {} while (0)
#define TICK_SUM(n) do {} while (0)
#define TICK_LDS(b) do {} while (0)
#define TICK_DUMP(b) do {} while (0)
#define TICK_OUT() do {} while (0)

DEV constexpr int krow(int rho8, int i) { return 32 * (rho8 >> 2) + 8 * (rho8 & 1) + 2 * ((rho8 >> 1) & 1) + i; }
DEV int krow_lane(int g) { return 16 * (g >> 1) + 4 * (g & 1); }
DEV unsigned prm(unsigned hi, unsigned lo, int odd) { return __builtin_amdgcn_perm(hi, lo, odd ? 0x07060302u : 0x05040100u); }

#define AT_ACC(M, n) do { const int n_ = (n); if (n_ == 0) M("{a[0:7]}"); if (n_ == 1) M("{a[8:15]}"); if (n_ == 2) M("{a[16:23]}"); if (n_ == 3) M("{a[24:31]}"); if (n_ == 4) M("{a[32:39]}"); if (n_ == 5) M("{a[40:47]}"); if (n_ == 6) M("{a[48:55]}"); if (n_ == 7) M("{a[56:63]}"); if (n_ == 8) M("{a[64:71]}"); if (n_ == 9) M("{a[72:79]}"); if (n_ == 10) M("{a[80:87]}"); if (n_ == 11) M("{a[88:95]}"); if (n_ == 12) M("{a[96:103]}"); if (n_ == 13) M("{a[104:111]}"); if (n_ == 14) M("{a[112:119]}"); if (n_ == 15) M("{a[120:127]}"); if (n_ == 16) M("{a[128:135]}"); if (n_ == 17) M("{a[136:143]}"); if (n_ == 18) M("{a[144:151]}"); if (n_ == 19) M("{a[152:159]}"); if (n_ == 20) M("{a[160:167]}"); if (n_ == 21) M("{a[168:175]}"); if (n_ == 22) M("{a[176:183]}"); if (n_ == 23) M("{a[184:191]}"); if (n_ == 24) M("{a[192:199]}"); if (n_ == 25) M("{a[200:207]}"); if (n_ == 26) M("{a[208:215]}"); if (n_ == 27) M("{a[216:223]}"); if (n_ == 28) M("{a[224:231]}"); if (n_ == 29) M("{a[232:239]}"); if (n_ == 30) M("{a[240:247]}"); if (n_ == 31) M("{a[248:255]}"); } while (0)
#define PIN_ACC0(x) asm volatile("" :: "{a[0:7]}"(x[0]), "{a[8:15]}"(x[1]), "{a[16:23]}"(x[2]), "{a[24:31]}"(x[3]), "{a[32:39]}"(x[4]), "{a[40:47]}"(x[5]), "{a[48:55]}"(x[6]), "{a[56:63]}"(x[7]), "{a[64:71]}"(x[8]), "{a[72:79]}"(x[9]), "{a[80:87]}"(x[10]), "{a[88:95]}"(x[11]), "{a[96:103]}"(x[12]), "{a[104:111]}"(x[13]), "{a[112:119]}"(x[14]), "{a[120:127]}"(x[15]))
#define PIN_ACC1(x) asm volatile("" :: "{a[128:135]}"(x[16]), "{a[136:143]}"(x[17]), "{a[144:151]}"(x[18]), "{a[152:159]}"(x[19]), "{a[160:167]}"(x[20]), "{a[168:175]}"(x[21]), "{a[176:183]}"(x[22]), "{a[184:191]}"(x[23]), "{a[192:199]}"(x[24]), "{a[200:207]}"(x[25]), "{a[208:215]}"(x[26]), "{a[216:223]}"(x[27]), "{a[224:231]}"(x[28]), "{a[232:239]}"(x[29]), "{a[240:247]}"(x[30]), "{a[248:255]}"(x[31]))
#define PIN_HOME(x) asm volatile("" :: "{v[32:39]}"(x[0]), "{v[40:47]}"(x[1]), "{v[48:55]}"(x[2]), "{v[56:63]}"(x[3]), "{v[64:71]}"(x[4]), "{v[72:79]}"(x[5]), "{v[80:87]}"(x[6]), "{v[88:95]}"(x[7]), "{v[96:103]}"(x[8]), "{v[104:111]}"(x[9]), "{v[112:119]}"(x[10]), "{v[120:127]}"(x[11]), "{v[128:135]}"(x[12]), "{v[136:143]}"(x[13]), "{v[144:151]}"(x[14]), "{v[152:159]}"(x[15]))
#define CL_32_100 "v32", "v33", "v34", "v35", "v36", "v37", "v38", "v39", "v40", "v41", "v42", "v43", "v44", "v45", "v46", "v47", "v48", "v49", "v50", "v51", "v52", "v53", "v54", "v55", "v56", "v57", "v58", "v59", "v60", "v61", "v62", "v63", "v64", "v65", "v66", "v67", "v68", "v69", "v70", "v71", "v72", "v73", "v74", "v75", "v76", "v77", "v78", "v79", "v80", "v81", "v82", "v83", "v84", "v85", "v86", "v87", "v88", "v89", "v90", "v91", "v92", "v93", "v94", "v95", "v96", "v97", "v98", "v99", "v100", "v101", "v102", "v103", "v104", "v105", "v106", "v107", "v108", "v109", "v110", "v111", "v112", "v113", "v114", "v115", "v116", "v117", "v118", "v119", "v120", "v121", "v122", "v123", "v124", "v125", "v126", "v127", "v128", "v129", "v130", "v131"
#define ACC_IN(x) "{a[0:31]}"(x[0]), "{a[32:63]}"(x[1]), "{a[64:95]}"(x[2]), "{a[96:127]}"(x[3]), "{a[128:159]}"(x[4]), "{a[160:191]}"(x[5]), "{a[192:223]}"(x[6]), "{a[224:255]}"(x[7])
#define CL_16_32 "v16", "v17", "v18", "v19", "v20", "v21", "v22", "v23", "v24", "v25", "v26", "v27", "v28", "v29", "v30", "v31"
#define CL_120_128 "v120", "v121", "v122", "v123", "v124", "v125", "v126", "v127", "v128", "v129", "v130", "v131", "v132", "v133", "v134", "v135"
#define CL_160_200 "v160", "v161", "v162", "v163", "v164", "v165", "v166", "v167", "v168", "v169", "v170", "v171", "v172", "v173", "v174", "v175", "v176", "v177", "v178", "v179", "v180", "v181", "v182", "v183", "v184", "v185", "v186", "v187", "v188", "v189", "v190", "v191", "v192", "v193", "v194", "v195", "v196", "v197", "v198", "v199"
#define CL_176_200 "v176", "v177", "v178", "v179", "v180", "v181", "v182", "v183", "v184", "v185", "v186", "v187", "v188", "v189", "v190", "v191", "v192", "v193", "v194", "v195", "v196", "v197", "v198", "v199"
#define CL_200_216 "v200", "v201", "v202", "v203", "v204", "v205", "v206", "v207", "v208", "v209", "v210", "v211", "v212", "v213", "v214", "v215"
#define CL_216_256 "v216", "v217", "v218", "v219", "v220", "v221", "v222", "v223", "v224", "v225", "v226", "v227", "v228", "v229", "v230", "v231", "v232", "v233", "v234", "v235", "v236", "v237", "v238", "v239", "v240", "v241", "v242", "v243", "v244", "v245", "v246", "v247", "v248", "v249", "v250", "v251", "v252", "v253", "v254", "v255"
#define CL_S "memory", "vcc", "scc", "s60", "s61", "s62", "s63", "s64", "s65", "s66", "s67", "s68" CL_T
#define TIMED_WAIT \
    "s_getpc_b64 s[62:63]\n\t" "1:\n\t" "s_memtime s[64:65]\n\t" "s_waitcnt lgkmcnt(0)\n\t" "s_sub_u32 s61, %0, s64\n\t" "s_cmp_gt_i32 s61, 124\n\t" "s_cbranch_scc1 1b\n\t" \
    "s_max_i32 s61, s61, 0\n\t" "s_and_b32 s61, s61, 124\n\t" "s_sub_u32 s61, 2f-1b, s61\n\t" "s_add_u32 s62, s62, s61\n\t" "s_addc_u32 s63, s63, 0\n\t" \
    "s_setpc_b64 s[62:63]\n\t" ".rept 31\n\t" "s_nop 0\n\t" ".endr\n\t" "2:\n\t"
#define QK_STEP0 \
    "v_xor_b32 v216, 0x400, %[mo]\n\t" "s_xor_b32 s60, %[sb], 0x800\n\t" "s_mov_b32 s66, 0x3e0293ee\n\t" "s_mov_b32 s67, 0x3e0293ee\n\t" XT(80, 81) \
    "s_add_u32 s61, %[kb], 0x1000\n\t" "buffer_load_dwordx4 v[16:19], %[kx], %[rs], %[kb] offen offset:0 sc1\n\t" \
    "buffer_load_dwordx4 v[20:23], %[kx], %[rs], %[kb] offen offset:2048 sc1\n\t" "buffer_load_dwordx4 v[24:27], %[kx], %[rs], s61 offen offset:0 sc1\n\t" \
    "buffer_load_dwordx4 v[28:31], %[kx], %[rs], s61 offen offset:2048 sc1\n\t" "buffer_load_dwordx4 v[200:203], %[kx], %[rs], %[kb] offen offset:256 sc1\n\t" \
    "buffer_load_dwordx4 v[204:207], %[kx], %[rs], %[kb] offen offset:2304 sc1\n\t" "buffer_load_dwordx4 v[208:211], %[kx], %[rs], s61 offen offset:256 sc1\n\t" \
    "buffer_load_dwordx4 v[212:215], %[kx], %[rs], s61 offen offset:2304 sc1\n\t" "s_barrier\n\t" "s_waitcnt vmcnt(7)\n\t" "ds_write_b128 %[kx], v[16:19] offset:0\n\t" \
    "s_waitcnt vmcnt(6)\n\t" "ds_write_b128 %[kx], v[20:23] offset:2048\n\t" "s_waitcnt vmcnt(5)\n\t" "ds_write_b128 %[kx], v[24:27] offset:4096\n\t" "s_waitcnt vmcnt(4)\n\t" \
    "ds_write_b128 %[kx], v[28:31] offset:6144\n\t" "s_waitcnt vmcnt(3)\n\t" "ds_write_b128 %[kx], v[200:203] offset:256\n\t" "s_waitcnt vmcnt(2)\n\t" \
    "ds_write_b128 %[kx], v[204:207] offset:2304\n\t" "s_waitcnt vmcnt(1)\n\t" "ds_write_b128 %[kx], v[208:211] offset:4352\n\t" "s_waitcnt vmcnt(0)\n\t" \
    "ds_write_b128 %[kx], v[212:215] offset:6400\n\t" "s_waitcnt lgkmcnt(0)\n\t" "v_mov_b64 v[208:209], 0\n\t" "v_mov_b64 v[210:211], 0\n\t" "v_mov_b64 v[212:213], 0\n\t" \
    "v_mov_b64 v[214:215], 0\n\t" XT(98, 99) "s_barrier\n\t" XT(82, 83) "buffer_load_dwordx4 v[240:243], %[mo], %[rs], %[sb] offen sc1\n\t" "v_mov_b64 v[224:225], 0\n\t" \
    "v_mov_b64 v[226:227], 0\n\t" "v_mov_b64 v[228:229], 0\n\t" "v_mov_b64 v[230:231], 0\n\t" "v_mov_b64 v[232:233], 0\n\t" "v_mov_b64 v[234:235], 0\n\t" \
    "v_mov_b64 v[236:237], 0\n\t" "v_mov_b64 v[238:239], 0\n\t" "buffer_load_dwordx4 v[244:247], v216, %[rs], %[sb] offen sc1\n\t" \
    "ds_read_b128 v[160:163], %[l0] offset:0\n\t" "ds_read_b128 v[164:167], %[l0] offset:1024\n\t" "ds_read_b128 v[168:171], %[l0] offset:2048\n\t" \
    "ds_read_b128 v[172:175], %[l0] offset:3072\n\t" "s_nop 3\n\t" "buffer_load_dwordx4 v[248:251], %[mo], %[rs], s60 offen sc1\n\t" \
    "ds_read_b128 v[176:179], %[l0] offset:4096\n\t" "ds_read_b128 v[180:183], %[l0] offset:5120\n\t" "ds_read_b128 v[184:187], %[l0] offset:6144\n\t" \
    "ds_read_b128 v[188:191], %[l0] offset:7168\n\t" "s_nop 3\n\t" "buffer_load_dwordx4 v[252:255], v216, %[rs], s60 offen sc1\n\t" "v_mov_b32 v219, 160512\n\t" \
    "v_mov_b64 v[220:221], -1\n\t" "v_mov_b64 v[222:223], -1\n\t" "v_mov_b32 v217, 0\n\t" "buffer_store_dwordx2 v[220:221], %[px], %[rs], %[pr] offen sc1\n\t" \
    "buffer_store_dword v220, %[pm], %[rs], %[pr] offen sc1\n\t" "global_load_dword v217, v217, %[fq] sc1\n\t" "10:\n\t" "s_waitcnt vmcnt(6)\n\t" XT(84, 85) \
    "v_smfmac_f32_16x16x64_bf16 v[224:227], v[240:243], a[0:7], %[ix]\n\t" "ds_write_b128 %[l1], v[240:243] offset:0\n\t" \
    "v_smfmac_f32_16x16x64_bf16 v[228:231], v[240:243], a[8:15], %[ix]\n\t" "v_smfmac_f32_16x16x64_bf16 v[232:235], v[240:243], v[32:39], %[ix]\n\t" \
    "s_waitcnt lgkmcnt(7)\n\t" "v_smfmac_f32_16x16x64_bf16 v[236:239], v[240:243], v[160:167], %[ix]\n\t" "ds_read_b128 v[160:163], %[l0] offset:8192\n\t" \
    "ds_read_b128 v[164:167], %[l0] offset:9216\n\t" "s_waitcnt vmcnt(5)\n\t" "v_smfmac_f32_16x16x64_bf16 v[224:227], v[244:247], a[16:23], %[ix]\n\t" \
    "ds_write_b128 %[l1], v[244:247] offset:1024\n\t" "v_smfmac_f32_16x16x64_bf16 v[228:231], v[244:247], a[24:31], %[ix]\n\t" \
    "v_smfmac_f32_16x16x64_bf16 v[232:235], v[244:247], v[40:47], %[ix]\n\t" "s_waitcnt lgkmcnt(8)\n\t" \
    "v_smfmac_f32_16x16x64_bf16 v[236:239], v[244:247], v[168:175], %[ix]\n\t" "ds_read_b128 v[168:171], %[l0] offset:10240\n\t" \
    "ds_read_b128 v[172:175], %[l0] offset:11264\n\t" "s_waitcnt vmcnt(4)\n\t" "v_smfmac_f32_16x16x64_bf16 v[224:227], v[248:251], a[32:39], %[ix]\n\t" \
    "ds_write_b128 %[l1], v[248:251] offset:2048\n\t" "v_smfmac_f32_16x16x64_bf16 v[228:231], v[248:251], a[40:47], %[ix]\n\t" "s_waitcnt lgkmcnt(3)\n\t" "s_barrier\n\t" \
    XT(86, 87) "v_smfmac_f32_16x16x64_bf16 v[232:235], v[248:251], v[48:55], %[ix]\n\t" "ds_read_b128 v[192:195], %[l1] offset:4096\n\t" \
    "ds_read_b128 v[196:199], %[l1] offset:8192\n\t" "v_smfmac_f32_16x16x64_bf16 v[236:239], v[248:251], v[176:183], %[ix]\n\t" \
    "ds_read_b128 v[176:179], %[l0] offset:12288\n\t" "ds_read_b128 v[180:183], %[l0] offset:13312\n\t" "s_waitcnt vmcnt(3)\n\t" \
    "v_smfmac_f32_16x16x64_bf16 v[224:227], v[252:255], a[48:55], %[ix]\n\t" "ds_write_b128 %[l1], v[252:255] offset:3072\n\t" \
    "v_smfmac_f32_16x16x64_bf16 v[228:231], v[252:255], a[56:63], %[ix]\n\t" "ds_read_b128 v[240:243], %[l1] offset:12288\n\t" \
    "ds_read_b128 v[244:247], %[l1] offset:5120\n\t" "v_smfmac_f32_16x16x64_bf16 v[232:235], v[252:255], v[56:63], %[ix]\n\t" "s_nop 1\n\t" \
    "v_smfmac_f32_16x16x64_bf16 v[236:239], v[252:255], v[184:191], %[ix]\n\t" "ds_read_b128 v[184:187], %[l0] offset:14336\n\t" "v_cmp_u_f32 vcc, v224, v225\n\t" \
    "v_cmp_u_f32 s[62:63], v226, v227\n\t" "s_waitcnt lgkmcnt(7)\n\t" "v_smfmac_f32_16x16x64_bf16 v[224:227], v[192:195], a[64:71], %[ix]\n\t" \
    "ds_read_b128 v[188:191], %[l0] offset:15360\n\t" "v_smfmac_f32_16x16x64_bf16 v[228:231], v[192:195], a[72:79], %[ix]\n\t" "s_or_b64 vcc, vcc, s[62:63]\n\t" \
    "s_cbranch_scc1 11f\n\t" "12:\n\t" "v_smfmac_f32_16x16x64_bf16 v[232:235], v[192:195], v[64:71], %[ix]\n\t" "s_waitcnt lgkmcnt(4)\n\t" "s_barrier\n\t" XT(88, 89) \
    "ds_read_b32 v218, v219\n\t" "v_smfmac_f32_16x16x64_bf16 v[236:239], v[192:195], v[160:167], %[ix]\n\t" "ds_read_b128 v[160:163], %[l0] offset:16384\n\t" \
    "ds_read_b128 v[164:167], %[l0] offset:17408\n\t" "v_smfmac_f32_16x16x64_bf16 v[224:227], v[196:199], a[80:87], %[ix]\n\t" \
    "ds_read_b128 v[248:251], %[l1] offset:9216\n\t" "ds_read_b128 v[252:255], %[l1] offset:13312\n\t" \
    "v_smfmac_f32_16x16x64_bf16 v[228:231], v[196:199], a[88:95], %[ix]\n\t" "ds_read_b128 v[192:195], %[l1] offset:6144\n\t" \
    "v_smfmac_f32_16x16x64_bf16 v[232:235], v[196:199], v[72:79], %[ix]\n\t" "v_smfmac_f32_16x16x64_bf16 v[236:239], v[196:199], v[168:175], %[ix]\n\t" \
    "s_waitcnt lgkmcnt(5)\n\t" "v_cmp_eq_u32 vcc, %[rt], v218\n\t" "v_smfmac_f32_16x16x64_bf16 v[224:227], v[240:243], a[96:103], %[ix]\n\t" "s_cbranch_vccnz 13f\n\t" \
    "ds_read_b128 v[168:171], %[l0] offset:18432\n\t" "v_smfmac_f32_16x16x64_bf16 v[228:231], v[240:243], a[104:111], %[ix]\n\t" \
    "ds_read_b128 v[172:175], %[l0] offset:19456\n\t" "ds_read_b128 v[196:199], %[l1] offset:10240\n\t" \
    "v_smfmac_f32_16x16x64_bf16 v[232:235], v[240:243], v[80:87], %[ix]\n\t" "v_smfmac_f32_16x16x64_bf16 v[236:239], v[240:243], v[176:183], %[ix]\n\t" \
    "ds_read_b128 v[176:179], %[l0] offset:20480\n\t" "ds_read_b128 v[180:183], %[l0] offset:21504\n\t" \
    "v_smfmac_f32_16x16x64_bf16 v[224:227], v[244:247], a[112:119], %[ix]\n\t" "ds_read_b128 v[240:243], %[l1] offset:14336\n\t" \
    "v_smfmac_f32_16x16x64_bf16 v[228:231], v[244:247], a[120:127], %[ix]\n\t" "v_smfmac_f32_16x16x64_bf16 v[232:235], v[244:247], v[88:95], %[ix]\n\t" \
    "v_smfmac_f32_16x16x64_bf16 v[236:239], v[244:247], v[184:191], %[ix]\n\t" "ds_read_b128 v[184:187], %[l0] offset:22528\n\t" \
    "ds_read_b128 v[188:191], %[l0] offset:23552\n\t" "s_waitcnt lgkmcnt(10)\n\t" "v_smfmac_f32_16x16x64_bf16 v[224:227], v[248:251], a[128:135], %[ix]\n\t" \
    "ds_read_b128 v[244:247], %[l1] offset:7168\n\t" "v_smfmac_f32_16x16x64_bf16 v[228:231], v[248:251], a[136:143], %[ix]\n\t" \
    "v_smfmac_f32_16x16x64_bf16 v[232:235], v[248:251], v[96:103], %[ix]\n\t" "v_smfmac_f32_16x16x64_bf16 v[236:239], v[248:251], v[160:167], %[ix]\n\t" \
    "ds_read_b128 v[160:163], %[l0] offset:24576\n\t" "ds_read_b128 v[164:167], %[l0] offset:25600\n\t" "s_waitcnt lgkmcnt(12)\n\t" \
    "v_smfmac_f32_16x16x64_bf16 v[224:227], v[252:255], a[144:151], %[ix]\n\t" "ds_read_b128 v[248:251], %[l1] offset:11264\n\t" \
    "v_smfmac_f32_16x16x64_bf16 v[228:231], v[252:255], a[152:159], %[ix]\n\t" "v_smfmac_f32_16x16x64_bf16 v[232:235], v[252:255], v[104:111], %[ix]\n\t" \
    "s_waitcnt lgkmcnt(10)\n\t" "v_smfmac_f32_16x16x64_bf16 v[236:239], v[252:255], v[168:175], %[ix]\n\t" "ds_read_b128 v[168:171], %[l0] offset:26624\n\t" \
    "ds_read_b128 v[172:175], %[l0] offset:27648\n\t" "v_smfmac_f32_16x16x64_bf16 v[224:227], v[192:195], a[160:167], %[ix]\n\t" \
    "ds_read_b128 v[252:255], %[l1] offset:15360\n\t" "v_smfmac_f32_16x16x64_bf16 v[228:231], v[192:195], a[168:175], %[ix]\n\t" "s_waitcnt lgkmcnt(12)\n\t" \
    "v_smfmac_f32_16x16x64_bf16 v[224:227], v[196:199], a[176:183], %[ix]\n\t" "v_smfmac_f32_16x16x64_bf16 v[228:231], v[196:199], a[184:191], %[ix]\n\t" \
    "s_waitcnt lgkmcnt(9)\n\t" "v_smfmac_f32_16x16x64_bf16 v[224:227], v[240:243], a[192:199], %[ix]\n\t" \
    "v_smfmac_f32_16x16x64_bf16 v[228:231], v[240:243], a[200:207], %[ix]\n\t" "s_waitcnt vmcnt(0)\n\t" "v_readfirstlane_b32 s62, v217\n\t" "s_waitcnt lgkmcnt(6)\n\t" \
    "v_smfmac_f32_16x16x64_bf16 v[224:227], v[244:247], a[208:215], %[ix]\n\t" "v_smfmac_f32_16x16x64_bf16 v[228:231], v[244:247], a[216:223], %[ix]\n\t" \
    "s_max_u32 %[sq], %[sq], s62\n\t" "s_waitcnt lgkmcnt(3)\n\t" "v_smfmac_f32_16x16x64_bf16 v[224:227], v[248:251], a[224:231], %[ix]\n\t" \
    "v_smfmac_f32_16x16x64_bf16 v[228:231], v[248:251], a[232:239], %[ix]\n\t" "s_waitcnt lgkmcnt(0)\n\t" \
    "v_smfmac_f32_16x16x64_bf16 v[224:227], v[252:255], a[240:247], %[ix]\n\t" "s_nop 1\n\t" "v_smfmac_f32_16x16x64_bf16 v[228:231], v[252:255], a[248:255], %[ix]\n\t" \
    XT(90, 91) "s_nop 1\n\t" "v_smfmac_f32_16x16x64_bf16 v[232:235], v[192:195], v[112:119], %[ix]\n\t" "s_nop 1\n\t" \
    "v_smfmac_f32_16x16x64_bf16 v[236:239], v[192:195], v[176:183], %[ix]\n\t" "ds_read_b128 v[176:179], %[l0] offset:28672\n\t" "v_add_f32 v224, v224, v225\n\t" \
    "v_add_f32 v226, v226, v227\n\t" "v_smfmac_f32_16x16x64_bf16 v[232:235], v[196:199], v[120:127], %[ix]\n\t" "ds_read_b128 v[180:183], %[l0] offset:29696\n\t" \
    "v_add_f32 v228, v228, v229\n\t" "v_add_f32 v230, v230, v231\n\t" "v_smfmac_f32_16x16x64_bf16 v[236:239], v[196:199], v[184:191], %[ix]\n\t" \
    "ds_read_b128 v[184:187], %[l0] offset:30720\n\t" "v_cvt_pk_bf16_f32 v218, v224, v228\n\t" "v_cvt_pk_bf16_f32 v219, v226, v230\n\t" \
    "v_smfmac_f32_16x16x64_bf16 v[232:235], v[240:243], v[128:135], %[ix]\n\t" "ds_write2_b32 %[qw], v218, v219 offset1:8\n\t" \
    "ds_read_b128 v[188:191], %[l0] offset:31744\n\t" "v_smfmac_f32_16x16x64_bf16 v[236:239], v[240:243], v[160:167], %[ix]\n\t" \
    "ds_read_b128 v[192:195], %[kx] offset:4352\n\t" "ds_read_b128 v[196:199], %[kx] offset:6400\n\t" \
    "v_smfmac_f32_16x16x64_bf16 v[232:235], v[244:247], v[136:143], %[ix]\n\t" "v_smfmac_f32_16x16x64_bf16 v[236:239], v[244:247], v[168:175], %[ix]\n\t" \
    "s_waitcnt lgkmcnt(3)\n\t" "s_barrier\n\t" "v_smfmac_f32_16x16x64_bf16 v[232:235], v[248:251], v[144:151], %[ix]\n\t" "ds_read_b128 v[216:219], %[l1] offset:24576\n\t" \
    "ds_read_b128 v[220:223], %[l1] offset:25600\n\t" "v_smfmac_f32_16x16x64_bf16 v[236:239], v[248:251], v[176:183], %[ix]\n\t" \
    "v_smfmac_f32_16x16x64_bf16 v[232:235], v[252:255], v[152:159], %[ix]\n\t" "s_waitcnt lgkmcnt(4)\n\t" \
    "v_smfmac_f32_16x16x64_bf16 v[236:239], v[252:255], v[184:191], %[ix]\n\t" XT(92, 93) "s_waitcnt lgkmcnt(1)\n\t" \
    "v_smfmac_f32_16x16x64_bf16 v[208:211], v[216:219], v[16:23], %[ix]\n\t" "s_nop 1\n\t" "v_smfmac_f32_16x16x64_bf16 v[212:215], v[216:219], v[200:207], %[ix]\n\t" \
    "s_waitcnt lgkmcnt(0)\n\t" "s_nop 0\n\t" "v_add_f32 v232, v232, v233\n\t" "v_add_f32 v234, v234, v235\n\t" "v_add_f32 v236, v236, v237\n\t" \
    "v_add_f32 v238, v238, v239\n\t" "v_cvt_pk_bf16_f32 v232, v232, v236\n\t" "v_cvt_pk_bf16_f32 v233, v234, v238\n\t" "ds_write2_b32 %[kw], v232, v233 offset1:68\n\t" \
    "v_smfmac_f32_16x16x64_bf16 v[208:211], v[220:223], v[24:31], %[ix]\n\t" "v_smfmac_f32_16x16x64_bf16 v[212:215], v[220:223], v[192:199], %[ix]\n\t" \
    "v_mov_b64 v[224:225], 0\n\t" "v_mov_b64 v[226:227], 0\n\t" "s_waitcnt lgkmcnt(0)\n\t" "s_barrier\n\t" XT(94, 95) "ds_read_b128 v[160:163], %[km] offset:0\n\t" \
    "ds_read_b128 v[164:167], %[km] offset:64\n\t" "ds_read_b128 v[168:171], %[km] offset:128\n\t" "ds_read_b128 v[172:175], %[km] offset:192\n\t" \
    "v_add_f32 v178, v208, v209\n\t" "v_add_f32 v179, v210, v211\n\t" "v_add_f32 v180, v212, v213\n\t" "v_add_f32 v181, v214, v215\n\t" "v_max_f32 v182, v178, v180\n\t" \
    "v_max_f32 v183, v179, v181\n\t" "s_nop 0\n\t" "v_max_f32_dpp v182, v182, v182 row_ror:8 row_mask:0xf bank_mask:0xf\n\t" \
    "v_max_f32_dpp v183, v183, v183 row_ror:8 row_mask:0xf bank_mask:0xf\n\t" "s_nop 0\n\t" "v_max_f32_dpp v182, v182, v182 row_ror:4 row_mask:0xf bank_mask:0xf\n\t" \
    "v_max_f32_dpp v183, v183, v183 row_ror:4 row_mask:0xf bank_mask:0xf\n\t" "s_nop 0\n\t" "v_max_f32_dpp v182, v182, v182 row_ror:2 row_mask:0xf bank_mask:0xf\n\t" \
    "v_max_f32_dpp v183, v183, v183 row_ror:2 row_mask:0xf bank_mask:0xf\n\t" "s_nop 0\n\t" "v_max_f32_dpp v182, v182, v182 row_ror:1 row_mask:0xf bank_mask:0xf\n\t" \
    "v_max_f32_dpp v183, v183, v183 row_ror:1 row_mask:0xf bank_mask:0xf\n\t" "s_waitcnt lgkmcnt(2)\n\t" \
    "v_smfmac_f32_16x16x64_bf16 v[224:227], v[216:219], v[160:167], %[ix]\n\t" "s_waitcnt lgkmcnt(0)\n\t" \
    "v_smfmac_f32_16x16x64_bf16 v[224:227], v[220:223], v[168:175], %[ix]\n\t" "s_nop 7\n\t" "v_add_f32 v176, v224, v225\n\t" "v_add_f32 v177, v226, v227\n\t" \
    "v_max_f32 v182, v176, v182\n\t" "v_max_f32 v183, v177, v183\n\t" "s_nop 0\n\t" "v_max_f32_dpp v182, v182, v182 row_ror:4 row_mask:0xf bank_mask:0xf\n\t" \
    "v_max_f32_dpp v183, v183, v183 row_ror:4 row_mask:0xf bank_mask:0xf\n\t" "s_nop 0\n\t" "v_max_f32_dpp v182, v182, v182 row_ror:2 row_mask:0xf bank_mask:0xf\n\t" \
    "v_max_f32_dpp v183, v183, v183 row_ror:2 row_mask:0xf bank_mask:0xf\n\t" "s_nop 0\n\t" "v_max_f32_dpp v182, v182, v182 row_ror:1 row_mask:0xf bank_mask:0xf\n\t" \
    "v_max_f32_dpp v183, v183, v183 row_ror:1 row_mask:0xf bank_mask:0xf\n\t" "v_pk_add_f32 v[178:179], v[178:179], v[182:183] neg_lo:[0,1] neg_hi:[0,1]\n\t" \
    "v_pk_add_f32 v[180:181], v[180:181], v[182:183] neg_lo:[0,1] neg_hi:[0,1]\n\t" "v_pk_add_f32 v[176:177], v[176:177], v[182:183] neg_lo:[0,1] neg_hi:[0,1]\n\t" \
    "v_pk_mul_f32 v[178:179], v[178:179], s[66:67]\n\t" "v_pk_mul_f32 v[180:181], v[180:181], s[66:67]\n\t" "v_pk_mul_f32 v[176:177], v[176:177], s[66:67]\n\t" \
    "v_exp_f32 v178, v178\n\t" "v_exp_f32 v179, v179\n\t" "v_exp_f32 v180, v180\n\t" "v_exp_f32 v181, v181\n\t" "v_exp_f32 v176, v176\n\t" \
    "v_pk_add_f32 v[182:183], v[178:179], v[180:181]\n\t" "v_exp_f32 v177, v177\n\t" "s_nop 0\n\t" "v_add_f32_dpp v182, v182, v182 row_ror:8 row_mask:0xf bank_mask:0xf\n\t" \
    "v_add_f32_dpp v183, v183, v183 row_ror:8 row_mask:0xf bank_mask:0xf\n\t" "v_add_f32 v182, v182, v176\n\t" "v_add_f32 v183, v183, v177\n\t" "s_nop 0\n\t" \
    "v_add_f32_dpp v182, v182, v182 row_ror:4 row_mask:0xf bank_mask:0xf\n\t" "v_add_f32_dpp v183, v183, v183 row_ror:4 row_mask:0xf bank_mask:0xf\n\t" "s_nop 0\n\t" \
    "v_add_f32_dpp v182, v182, v182 row_ror:2 row_mask:0xf bank_mask:0xf\n\t" "v_add_f32_dpp v183, v183, v183 row_ror:2 row_mask:0xf bank_mask:0xf\n\t" "s_nop 0\n\t" \
    "v_add_f32_dpp v182, v182, v182 row_ror:1 row_mask:0xf bank_mask:0xf\n\t" "v_add_f32_dpp v183, v183, v183 row_ror:1 row_mask:0xf bank_mask:0xf\n\t" \
    "v_rcp_f32 v182, v182\n\t" "v_rcp_f32 v183, v183\n\t" "s_nop 0\n\t" "v_pk_mul_f32 v[176:177], v[176:177], v[182:183]\n\t" \
    "v_pk_mul_f32 v[178:179], v[178:179], v[182:183]\n\t" "v_pk_mul_f32 v[180:181], v[180:181], v[182:183]\n\t" "v_cvt_pk_bf16_f32 v176, v176, v177\n\t" \
    "v_cvt_pk_bf16_f32 v178, v178, v179\n\t" "v_cvt_pk_bf16_f32 v180, v180, v181\n\t" "v_mov_b32_dpp v177, v176 quad_perm:[1,0,3,2] row_mask:0xf bank_mask:0xf\n\t" \
    "v_mov_b32_dpp v179, v178 quad_perm:[1,0,3,2] row_mask:0xf bank_mask:0xf\n\t" "v_mov_b32_dpp v181, v180 quad_perm:[1,0,3,2] row_mask:0xf bank_mask:0xf\n\t" \
    "v_perm_b32 v184, v179, v178, %[ps]\n\t" "v_perm_b32 v185, v181, v180, %[ps]\n\t" "buffer_store_dwordx2 v[184:185], %[px], %[rs], %[pb] offen sc1\n\t" \
    "v_perm_b32 v186, v177, v176, %[ps]\n\t" "buffer_store_dword v186, %[pm], %[rs], %[pb] offen sc1\n\t" XT(96, 97) LMARK "s_branch 99f\n\t" "11:\n\t" \
    "ds_read_b128 v[220:223], %[l1] offset:0\n\t" "s_waitcnt lgkmcnt(0)\n\t" "v_max3_u32 v218, v220, v221, v222\n\t" "v_max_u32 v218, v218, v223\n\t" \
    "ds_read_b128 v[220:223], %[l1] offset:1024\n\t" "s_waitcnt lgkmcnt(0)\n\t" "v_max3_u32 v218, v218, v220, v221\n\t" "v_max3_u32 v218, v218, v222, v223\n\t" \
    "ds_read_b128 v[220:223], %[l1] offset:2048\n\t" "s_waitcnt lgkmcnt(0)\n\t" "v_max3_u32 v218, v218, v220, v221\n\t" "v_max3_u32 v218, v218, v222, v223\n\t" \
    "ds_read_b128 v[220:223], %[l1] offset:3072\n\t" "s_waitcnt lgkmcnt(0)\n\t" "v_max3_u32 v218, v218, v220, v221\n\t" "v_max3_u32 v218, v218, v222, v223\n\t" \
    "v_cmp_eq_u32 vcc, -1, v218\n\t" "s_cbranch_vccz 12b\n\t" "s_cmp_eq_u32 %[rt], 1\n\t" "s_cbranch_scc1 12b\n\t" "v_mov_b32 v218, %[rt]\n\t" "ds_write_b32 v219, v218\n\t" \
    "s_waitcnt lgkmcnt(0)\n\t" "s_branch 12b\n\t" "13:\n\t" "s_sub_u32 %[rt], %[rt], 1\n\t" "s_waitcnt lgkmcnt(0)\n\t" XCNT(100) "14:\n\t" \
    "buffer_load_dwordx4 v[240:243], %[mo], %[rs], %[sb] offen sc1\n\t" "buffer_load_dwordx4 v[244:247], v216, %[rs], %[sb] offen sc1\n\t" \
    "buffer_load_dwordx4 v[248:251], %[mo], %[rs], s60 offen sc1\n\t" "buffer_load_dwordx4 v[252:255], v216, %[rs], s60 offen sc1\n\t" "s_waitcnt vmcnt(0)\n\t" \
    "v_max_u32 v218, v240, v241\n\t" "v_max3_u32 v218, v218, v242, v243\n\t" "v_max3_u32 v218, v218, v244, v245\n\t" "v_max3_u32 v218, v218, v246, v247\n\t" \
    "v_max3_u32 v218, v218, v248, v249\n\t" "v_max3_u32 v218, v218, v250, v251\n\t" "v_max3_u32 v218, v218, v252, v253\n\t" "v_max3_u32 v218, v218, v254, v255\n\t" \
    "v_cmp_eq_u32 vcc, -1, v218\n\t" "s_cbranch_vccz 15f\n\t" "s_cmp_eq_u32 %[bd], 0\n\t" "s_cbranch_scc1 15f\n\t" "s_sub_u32 %[bd], %[bd], 1\n\t" XCNT(101) \
    "s_branch 14b\n\t" "15:\n\t" "ds_read_b128 v[160:163], %[l0] offset:0\n\t" "ds_read_b128 v[164:167], %[l0] offset:1024\n\t" \
    "ds_read_b128 v[168:171], %[l0] offset:2048\n\t" "ds_read_b128 v[172:175], %[l0] offset:3072\n\t" "ds_read_b128 v[176:179], %[l0] offset:4096\n\t" \
    "ds_read_b128 v[180:183], %[l0] offset:5120\n\t" "ds_read_b128 v[184:187], %[l0] offset:6144\n\t" "ds_read_b128 v[188:191], %[l0] offset:7168\n\t" \
    "v_mov_b64 v[224:225], 0\n\t" "v_mov_b64 v[226:227], 0\n\t" "v_mov_b64 v[228:229], 0\n\t" "v_mov_b64 v[230:231], 0\n\t" "v_mov_b64 v[232:233], 0\n\t" \
    "v_mov_b64 v[234:235], 0\n\t" "v_mov_b64 v[236:237], 0\n\t" "v_mov_b64 v[238:239], 0\n\t" "s_barrier\n\t" "s_branch 10b\n\t" "99:\n\t"
#define QK_STEP1 \
    "v_xor_b32 v216, 0x400, %[mo]\n\t" "s_xor_b32 s60, %[sb], 0x800\n\t" "s_add_u32 s61, %[qb], 0x1000\n\t" "s_mov_b32 s66, 0x3e0293ee\n\t" "s_mov_b32 s67, 0x3e0293ee\n\t" \
    XT(80, 81) XT(98, 99) "s_barrier\n\t" XT(82, 83) "buffer_load_dwordx4 v[240:243], %[mo], %[rs], %[sb] offen sc1\n\t" "v_mov_b64 v[224:225], 0\n\t" \
    "v_mov_b64 v[226:227], 0\n\t" "v_mov_b64 v[228:229], 0\n\t" "v_mov_b64 v[230:231], 0\n\t" "v_mov_b64 v[232:233], 0\n\t" "v_mov_b64 v[234:235], 0\n\t" \
    "v_mov_b64 v[236:237], 0\n\t" "v_mov_b64 v[238:239], 0\n\t" "buffer_load_dwordx4 v[244:247], v216, %[rs], %[sb] offen sc1\n\t" \
    "ds_read_b128 v[160:163], %[l0] offset:0\n\t" "ds_read_b128 v[164:167], %[l0] offset:1024\n\t" "ds_read_b128 v[168:171], %[l0] offset:2048\n\t" \
    "ds_read_b128 v[172:175], %[l0] offset:3072\n\t" "s_nop 3\n\t" "buffer_load_dwordx4 v[248:251], %[mo], %[rs], s60 offen sc1\n\t" \
    "ds_read_b128 v[176:179], %[l0] offset:4096\n\t" "ds_read_b128 v[180:183], %[l0] offset:5120\n\t" "ds_read_b128 v[184:187], %[l0] offset:6144\n\t" \
    "ds_read_b128 v[188:191], %[l0] offset:7168\n\t" "s_nop 3\n\t" "buffer_load_dwordx4 v[252:255], v216, %[rs], s60 offen sc1\n\t" "v_mov_b32 v219, 160512\n\t" \
    "v_mov_b64 v[220:221], -1\n\t" "v_mov_b64 v[222:223], -1\n\t" "buffer_store_dwordx2 v[220:221], %[px], %[rs], %[tr] offen sc1\n\t" \
    "buffer_store_dwordx2 v[220:221], %[pm], %[rs], %[tr] offen sc1\n\t" "v_mov_b32 v208, 0xd028965f\n\t" "v_mov_b32 v209, 0xff800000\n\t" "s_mov_b32 s62, 0\n\t" \
    "s_mov_b32 s63, -1\n\t" "v_mov_b32 v210, 0\n\t" "v_cmp_gt_i32 vcc, 0, %[ps]\n\t" "v_cndmask_b32 v204, 0, v209, s[62:63]\n\t" "s_nop 0\n\t" \
    "v_cndmask_b32 v200, 0, v208, vcc\n\t" "v_cmp_gt_i32 vcc, 1, %[ps]\n\t" "v_cndmask_b32 v205, 0, v209, s[62:63]\n\t" "s_nop 0\n\t" "v_cndmask_b32 v201, 0, v208, vcc\n\t" \
    "v_cmp_gt_i32 vcc, 2, %[ps]\n\t" "v_cndmask_b32 v206, 0, v209, s[62:63]\n\t" "s_nop 0\n\t" "v_cndmask_b32 v202, 0, v208, vcc\n\t" "v_cmp_gt_i32 vcc, 3, %[ps]\n\t" \
    "v_cndmask_b32 v207, 0, v209, s[62:63]\n\t" "s_nop 0\n\t" "v_cndmask_b32 v203, 0, v208, vcc\n\t" "20:\n\t" "s_waitcnt vmcnt(5)\n\t" XT(84, 85) \
    "v_smfmac_f32_16x16x64_bf16 v[224:227], v[240:243], a[0:7], %[ix]\n\t" "ds_write_b128 %[l1], v[240:243] offset:4096\n\t" \
    "v_smfmac_f32_16x16x64_bf16 v[228:231], v[240:243], a[8:15], %[ix]\n\t" "v_smfmac_f32_16x16x64_bf16 v[232:235], v[240:243], v[32:39], %[ix]\n\t" \
    "s_waitcnt lgkmcnt(7)\n\t" "v_smfmac_f32_16x16x64_bf16 v[236:239], v[240:243], v[160:167], %[ix]\n\t" "ds_read_b128 v[160:163], %[l0] offset:8192\n\t" \
    "ds_read_b128 v[164:167], %[l0] offset:9216\n\t" "s_waitcnt vmcnt(4)\n\t" "v_smfmac_f32_16x16x64_bf16 v[224:227], v[244:247], a[16:23], %[ix]\n\t" \
    "ds_write_b128 %[l1], v[244:247] offset:5120\n\t" "v_smfmac_f32_16x16x64_bf16 v[228:231], v[244:247], a[24:31], %[ix]\n\t" \
    "v_smfmac_f32_16x16x64_bf16 v[232:235], v[244:247], v[40:47], %[ix]\n\t" "s_waitcnt lgkmcnt(8)\n\t" \
    "v_smfmac_f32_16x16x64_bf16 v[236:239], v[244:247], v[168:175], %[ix]\n\t" "ds_read_b128 v[168:171], %[l0] offset:10240\n\t" \
    "ds_read_b128 v[172:175], %[l0] offset:11264\n\t" "s_waitcnt vmcnt(3)\n\t" "v_smfmac_f32_16x16x64_bf16 v[224:227], v[248:251], a[32:39], %[ix]\n\t" \
    "ds_write_b128 %[l1], v[248:251] offset:6144\n\t" "v_smfmac_f32_16x16x64_bf16 v[228:231], v[248:251], a[40:47], %[ix]\n\t" "s_waitcnt lgkmcnt(3)\n\t" "s_barrier\n\t" \
    XT(86, 87) "v_smfmac_f32_16x16x64_bf16 v[232:235], v[248:251], v[48:55], %[ix]\n\t" "ds_read_b128 v[192:195], %[l1] offset:8192\n\t" \
    "ds_read_b128 v[196:199], %[l1] offset:12288\n\t" "v_smfmac_f32_16x16x64_bf16 v[236:239], v[248:251], v[176:183], %[ix]\n\t" \
    "ds_read_b128 v[176:179], %[l0] offset:12288\n\t" "ds_read_b128 v[180:183], %[l0] offset:13312\n\t" "s_waitcnt vmcnt(2)\n\t" \
    "v_smfmac_f32_16x16x64_bf16 v[224:227], v[252:255], a[48:55], %[ix]\n\t" "ds_write_b128 %[l1], v[252:255] offset:7168\n\t" \
    "v_smfmac_f32_16x16x64_bf16 v[228:231], v[252:255], a[56:63], %[ix]\n\t" "ds_read_b128 v[240:243], %[l1] offset:0\n\t" "ds_read_b128 v[244:247], %[l1] offset:9216\n\t" \
    "v_smfmac_f32_16x16x64_bf16 v[232:235], v[252:255], v[56:63], %[ix]\n\t" "s_nop 1\n\t" "v_smfmac_f32_16x16x64_bf16 v[236:239], v[252:255], v[184:191], %[ix]\n\t" \
    "ds_read_b128 v[184:187], %[l0] offset:14336\n\t" "v_cmp_u_f32 vcc, v224, v225\n\t" "v_cmp_u_f32 s[62:63], v226, v227\n\t" "s_waitcnt lgkmcnt(7)\n\t" \
    "v_smfmac_f32_16x16x64_bf16 v[224:227], v[192:195], a[64:71], %[ix]\n\t" "ds_read_b128 v[188:191], %[l0] offset:15360\n\t" \
    "v_smfmac_f32_16x16x64_bf16 v[228:231], v[192:195], a[72:79], %[ix]\n\t" "s_or_b64 vcc, vcc, s[62:63]\n\t" "s_cbranch_scc1 21f\n\t" "22:\n\t" \
    "v_smfmac_f32_16x16x64_bf16 v[232:235], v[192:195], v[64:71], %[ix]\n\t" "s_waitcnt lgkmcnt(4)\n\t" "s_barrier\n\t" XT(88, 89) "ds_read_b32 v218, v219\n\t" \
    "v_smfmac_f32_16x16x64_bf16 v[236:239], v[192:195], v[160:167], %[ix]\n\t" "ds_read_b128 v[160:163], %[l0] offset:16384\n\t" \
    "ds_read_b128 v[164:167], %[l0] offset:17408\n\t" "v_smfmac_f32_16x16x64_bf16 v[224:227], v[196:199], a[80:87], %[ix]\n\t" \
    "ds_read_b128 v[248:251], %[l1] offset:13312\n\t" "ds_read_b128 v[252:255], %[l1] offset:1024\n\t" \
    "v_smfmac_f32_16x16x64_bf16 v[228:231], v[196:199], a[88:95], %[ix]\n\t" "ds_read_b128 v[192:195], %[l1] offset:10240\n\t" \
    "v_smfmac_f32_16x16x64_bf16 v[232:235], v[196:199], v[72:79], %[ix]\n\t" "v_smfmac_f32_16x16x64_bf16 v[236:239], v[196:199], v[168:175], %[ix]\n\t" \
    "s_waitcnt lgkmcnt(5)\n\t" "v_cmp_eq_u32 vcc, %[rt], v218\n\t" "v_smfmac_f32_16x16x64_bf16 v[224:227], v[240:243], a[96:103], %[ix]\n\t" "s_cbranch_vccnz 23f\n\t" \
    "ds_read_b128 v[168:171], %[l0] offset:18432\n\t" "v_smfmac_f32_16x16x64_bf16 v[228:231], v[240:243], a[104:111], %[ix]\n\t" \
    "ds_read_b128 v[172:175], %[l0] offset:19456\n\t" "ds_read_b128 v[196:199], %[l1] offset:14336\n\t" \
    "v_smfmac_f32_16x16x64_bf16 v[232:235], v[240:243], v[80:87], %[ix]\n\t" "buffer_load_dwordx4 v[16:19], %[hq], %[rs], %[qb] offen offset:0 sc1\n\t" \
    "v_smfmac_f32_16x16x64_bf16 v[236:239], v[240:243], v[176:183], %[ix]\n\t" "ds_read_b128 v[176:179], %[l0] offset:20480\n\t" \
    "ds_read_b128 v[180:183], %[l0] offset:21504\n\t" "v_smfmac_f32_16x16x64_bf16 v[224:227], v[244:247], a[112:119], %[ix]\n\t" \
    "ds_read_b128 v[240:243], %[l1] offset:2048\n\t" "v_smfmac_f32_16x16x64_bf16 v[228:231], v[244:247], a[120:127], %[ix]\n\t" \
    "v_smfmac_f32_16x16x64_bf16 v[232:235], v[244:247], v[88:95], %[ix]\n\t" "buffer_load_dwordx4 v[20:23], %[hq], %[rs], %[qb] offen offset:2048 sc1\n\t" \
    "v_smfmac_f32_16x16x64_bf16 v[236:239], v[244:247], v[184:191], %[ix]\n\t" "ds_read_b128 v[184:187], %[l0] offset:22528\n\t" \
    "ds_read_b128 v[188:191], %[l0] offset:23552\n\t" "s_waitcnt lgkmcnt(10)\n\t" "v_smfmac_f32_16x16x64_bf16 v[224:227], v[248:251], a[128:135], %[ix]\n\t" \
    "ds_read_b128 v[244:247], %[l1] offset:11264\n\t" "v_smfmac_f32_16x16x64_bf16 v[228:231], v[248:251], a[136:143], %[ix]\n\t" \
    "v_smfmac_f32_16x16x64_bf16 v[232:235], v[248:251], v[96:103], %[ix]\n\t" "buffer_load_dwordx4 v[24:27], %[hq], %[rs], s61 offen offset:0 sc1\n\t" \
    "v_smfmac_f32_16x16x64_bf16 v[236:239], v[248:251], v[160:167], %[ix]\n\t" "ds_read_b128 v[160:163], %[l0] offset:24576\n\t" \
    "ds_read_b128 v[164:167], %[l0] offset:25600\n\t" "s_waitcnt lgkmcnt(12)\n\t" "v_smfmac_f32_16x16x64_bf16 v[224:227], v[252:255], a[144:151], %[ix]\n\t" \
    "ds_read_b128 v[248:251], %[l1] offset:15360\n\t" "v_smfmac_f32_16x16x64_bf16 v[228:231], v[252:255], a[152:159], %[ix]\n\t" \
    "v_smfmac_f32_16x16x64_bf16 v[232:235], v[252:255], v[104:111], %[ix]\n\t" "buffer_load_dwordx4 v[28:31], %[hq], %[rs], s61 offen offset:2048 sc1\n\t" \
    "s_waitcnt lgkmcnt(10)\n\t" "v_smfmac_f32_16x16x64_bf16 v[236:239], v[252:255], v[168:175], %[ix]\n\t" "ds_read_b128 v[168:171], %[l0] offset:26624\n\t" \
    "ds_read_b128 v[172:175], %[l0] offset:27648\n\t" "v_smfmac_f32_16x16x64_bf16 v[224:227], v[192:195], a[160:167], %[ix]\n\t" \
    "ds_read_b128 v[252:255], %[l1] offset:3072\n\t" "v_smfmac_f32_16x16x64_bf16 v[228:231], v[192:195], a[168:175], %[ix]\n\t" "s_waitcnt lgkmcnt(12)\n\t" \
    "v_smfmac_f32_16x16x64_bf16 v[224:227], v[196:199], a[176:183], %[ix]\n\t" "v_smfmac_f32_16x16x64_bf16 v[228:231], v[196:199], a[184:191], %[ix]\n\t" \
    "s_waitcnt lgkmcnt(9)\n\t" "v_smfmac_f32_16x16x64_bf16 v[224:227], v[240:243], a[192:199], %[ix]\n\t" \
    "v_smfmac_f32_16x16x64_bf16 v[228:231], v[240:243], a[200:207], %[ix]\n\t" "s_waitcnt lgkmcnt(6)\n\t" \
    "v_smfmac_f32_16x16x64_bf16 v[224:227], v[244:247], a[208:215], %[ix]\n\t" "v_smfmac_f32_16x16x64_bf16 v[228:231], v[244:247], a[216:223], %[ix]\n\t" \
    "s_waitcnt vmcnt(4)\n\t" "s_waitcnt lgkmcnt(3)\n\t" "v_smfmac_f32_16x16x64_bf16 v[224:227], v[248:251], a[224:231], %[ix]\n\t" \
    "v_smfmac_f32_16x16x64_bf16 v[228:231], v[248:251], a[232:239], %[ix]\n\t" "s_waitcnt lgkmcnt(0)\n\t" \
    "v_smfmac_f32_16x16x64_bf16 v[224:227], v[252:255], a[240:247], %[ix]\n\t" "s_nop 1\n\t" "v_smfmac_f32_16x16x64_bf16 v[228:231], v[252:255], a[248:255], %[ix]\n\t" \
    XT(90, 91) "s_nop 1\n\t" "v_smfmac_f32_16x16x64_bf16 v[232:235], v[192:195], v[112:119], %[ix]\n\t" "s_nop 1\n\t" \
    "v_smfmac_f32_16x16x64_bf16 v[236:239], v[192:195], v[176:183], %[ix]\n\t" "ds_read_b128 v[176:179], %[l0] offset:28672\n\t" "v_add_f32 v224, v224, v225\n\t" \
    "v_add_f32 v226, v226, v227\n\t" "v_smfmac_f32_16x16x64_bf16 v[232:235], v[196:199], v[120:127], %[ix]\n\t" "ds_read_b128 v[180:183], %[l0] offset:29696\n\t" \
    "v_add_f32 v228, v228, v229\n\t" "v_add_f32 v230, v230, v231\n\t" "v_smfmac_f32_16x16x64_bf16 v[236:239], v[196:199], v[184:191], %[ix]\n\t" \
    "ds_read_b128 v[184:187], %[l0] offset:30720\n\t" "v_cvt_pk_bf16_f32 v218, v224, v228\n\t" "v_cvt_pk_bf16_f32 v219, v226, v230\n\t" \
    "v_smfmac_f32_16x16x64_bf16 v[232:235], v[240:243], v[128:135], %[ix]\n\t" "ds_write2_b32 %[qw], v218, v219 offset1:8\n\t" \
    "ds_read_b128 v[188:191], %[l0] offset:31744\n\t" "v_smfmac_f32_16x16x64_bf16 v[236:239], v[240:243], v[160:167], %[ix]\n\t" \
    "v_smfmac_f32_16x16x64_bf16 v[232:235], v[244:247], v[136:143], %[ix]\n\t" "v_smfmac_f32_16x16x64_bf16 v[236:239], v[244:247], v[168:175], %[ix]\n\t" \
    "s_waitcnt lgkmcnt(1)\n\t" "s_barrier\n\t" "v_smfmac_f32_16x16x64_bf16 v[232:235], v[248:251], v[144:151], %[ix]\n\t" \
    "v_smfmac_f32_16x16x64_bf16 v[236:239], v[248:251], v[176:183], %[ix]\n\t" "v_smfmac_f32_16x16x64_bf16 v[232:235], v[252:255], v[152:159], %[ix]\n\t" \
    "s_waitcnt lgkmcnt(0)\n\t" "v_smfmac_f32_16x16x64_bf16 v[236:239], v[252:255], v[184:191], %[ix]\n\t" XT(92, 93) "s_nop 6\n\t" "v_add_f32 v232, v232, v233\n\t" \
    "v_add_f32 v234, v234, v235\n\t" "v_add_f32 v236, v236, v237\n\t" "v_add_f32 v238, v238, v239\n\t" "v_cvt_pk_bf16_f32 v232, v232, v236\n\t" \
    "v_cvt_pk_bf16_f32 v233, v234, v238\n\t" "ds_write2_b32 %[kw], v232, v233 offset1:68\n\t" "s_waitcnt lgkmcnt(0)\n\t" "s_barrier\n\t" XT(94, 95) \
    "s_cmp_ge_u32 %[sq], %[sn]\n\t" "s_cbranch_scc1 277f\n\t" "275:\n\t" "global_load_dword v240, v210, %[fq] sc1\n\t" "s_waitcnt vmcnt(0)\n\t" \
    "v_readfirstlane_b32 %[sq], v240\n\t" "s_nop 3\n\t" "s_cmp_ge_u32 %[sq], %[sn]\n\t" "s_cbranch_scc1 276f\n\t" "s_cmp_eq_u32 %[bd], 0\n\t" "s_cbranch_scc1 276f\n\t" \
    "s_sub_u32 %[bd], %[bd], 1\n\t" "s_sleep 2\n\t" "s_branch 275b\n\t" "276:\n\t" "buffer_load_dwordx4 v[16:19], %[hq], %[rs], %[qb] offen offset:0 sc1\n\t" \
    "buffer_load_dwordx4 v[20:23], %[hq], %[rs], %[qb] offen offset:2048 sc1\n\t" "buffer_load_dwordx4 v[24:27], %[hq], %[rs], s61 offen offset:0 sc1\n\t" \
    "buffer_load_dwordx4 v[28:31], %[hq], %[rs], s61 offen offset:2048 sc1\n\t" "277:\n\t" "s_nop 3\n\t" "ds_read_b128 v[160:163], %[kx] offset:0\n\t" \
    "ds_read_b128 v[164:167], %[kx] offset:2048\n\t" "ds_read_b128 v[168:171], %[kx] offset:4096\n\t" "ds_read_b128 v[172:175], %[kx] offset:6144\n\t" \
    "ds_read_b128 v[224:227], %[km] offset:0\n\t" "ds_read_b128 v[228:231], %[km] offset:64\n\t" "ds_read_b128 v[232:235], %[km] offset:128\n\t" \
    "ds_read_b128 v[236:239], %[km] offset:192\n\t" "s_waitcnt vmcnt(0)\n\t" "global_load_dword v240, v210, %[fq] sc1\n\t" "s_waitcnt lgkmcnt(7)\n\t" \
    "v_mfma_f32_16x16x32_bf16 v[248:251], v[160:163], v[16:19], v[200:203]\n\t" "s_waitcnt lgkmcnt(6)\n\t" \
    "v_mfma_f32_16x16x32_bf16 v[248:251], v[164:167], v[20:23], v[248:251]\n\t" "s_waitcnt lgkmcnt(5)\n\t" \
    "v_mfma_f32_16x16x32_bf16 v[248:251], v[168:171], v[24:27], v[248:251]\n\t" "s_waitcnt lgkmcnt(4)\n\t" \
    "v_mfma_f32_16x16x32_bf16 v[248:251], v[172:175], v[28:31], v[248:251]\n\t" "s_waitcnt lgkmcnt(3)\n\t" \
    "v_mfma_f32_16x16x32_bf16 v[244:247], v[224:227], v[16:19], v[204:207]\n\t" "s_waitcnt lgkmcnt(2)\n\t" \
    "v_mfma_f32_16x16x32_bf16 v[244:247], v[228:231], v[20:23], v[244:247]\n\t" "s_waitcnt lgkmcnt(1)\n\t" \
    "v_mfma_f32_16x16x32_bf16 v[244:247], v[232:235], v[24:27], v[244:247]\n\t" "s_waitcnt lgkmcnt(0)\n\t" \
    "v_mfma_f32_16x16x32_bf16 v[244:247], v[236:239], v[28:31], v[244:247]\n\t" "s_nop 3\n\t" "v_max3_f32 v242, v248, v249, v250\n\t" "v_max_f32 v242, v242, v251\n\t" \
    "s_nop 1\n\t" "v_max3_f32 v242, v242, v244, v245\n\t" "v_max3_f32 v242, v242, v246, v247\n\t" "v_mov_b32 v243, v242\n\t" "s_nop 1\n\t" \
    "v_permlane32_swap_b32 v242, v243\n\t" "v_max_f32 v242, v242, v243\n\t" "v_mov_b32 v243, v242\n\t" "s_nop 1\n\t" "v_permlane16_swap_b32 v242, v243\n\t" \
    "v_max_f32 v242, v242, v243\n\t" "v_pk_add_f32 v[244:245], v[244:245], v[242:243] op_sel_hi:[1,0] neg_lo:[0,1] neg_hi:[0,1]\n\t" \
    "v_pk_add_f32 v[246:247], v[246:247], v[242:243] op_sel_hi:[1,0] neg_lo:[0,1] neg_hi:[0,1]\n\t" \
    "v_pk_add_f32 v[248:249], v[248:249], v[242:243] op_sel_hi:[1,0] neg_lo:[0,1] neg_hi:[0,1]\n\t" \
    "v_pk_add_f32 v[250:251], v[250:251], v[242:243] op_sel_hi:[1,0] neg_lo:[0,1] neg_hi:[0,1]\n\t" "v_pk_mul_f32 v[244:245], v[244:245], s[66:67]\n\t" \
    "v_pk_mul_f32 v[246:247], v[246:247], s[66:67]\n\t" "v_pk_mul_f32 v[248:249], v[248:249], s[66:67]\n\t" "v_pk_mul_f32 v[250:251], v[250:251], s[66:67]\n\t" \
    "v_exp_f32 v244, v244\n\t" "v_exp_f32 v245, v245\n\t" "v_exp_f32 v246, v246\n\t" "v_exp_f32 v247, v247\n\t" "v_exp_f32 v248, v248\n\t" "v_exp_f32 v249, v249\n\t" \
    "v_exp_f32 v250, v250\n\t" "v_exp_f32 v251, v251\n\t" "v_pk_add_f32 v[242:243], v[244:245], v[246:247]\n\t" "v_pk_add_f32 v[242:243], v[242:243], v[248:249]\n\t" \
    "v_pk_add_f32 v[242:243], v[242:243], v[250:251]\n\t" "v_add_f32 v242, v242, v243\n\t" "v_mov_b32 v243, v242\n\t" "s_nop 1\n\t" "v_permlane32_swap_b32 v242, v243\n\t" \
    "v_add_f32 v242, v242, v243\n\t" "v_mov_b32 v243, v242\n\t" "s_nop 1\n\t" "v_permlane16_swap_b32 v242, v243\n\t" "v_add_f32 v242, v242, v243\n\t" \
    "v_rcp_f32 v242, v242\n\t" "s_nop 0\n\t" "v_pk_mul_f32 v[244:245], v[244:245], v[242:243] op_sel_hi:[1,0]\n\t" \
    "v_pk_mul_f32 v[246:247], v[246:247], v[242:243] op_sel_hi:[1,0]\n\t" "v_pk_mul_f32 v[248:249], v[248:249], v[242:243] op_sel_hi:[1,0]\n\t" \
    "v_pk_mul_f32 v[250:251], v[250:251], v[242:243] op_sel_hi:[1,0]\n\t" "v_cvt_pk_bf16_f32 v162, v244, v245\n\t" "v_cvt_pk_bf16_f32 v163, v246, v247\n\t" \
    "v_cvt_pk_bf16_f32 v164, v248, v249\n\t" "v_cvt_pk_bf16_f32 v165, v250, v251\n\t" "s_barrier\n\t" "buffer_store_dwordx2 v[164:165], %[px], %[rs], %[tb] offen sc1\n\t" \
    "buffer_store_dwordx2 v[162:163], %[pm], %[rs], %[tb] offen sc1\n\t" "s_waitcnt vmcnt(2)\n\t" "v_readfirstlane_b32 s62, v240\n\t" "s_nop 3\n\t" \
    "s_max_u32 %[sq], %[sq], s62\n\t" XT(96, 97) "s_branch 99f\n\t" "21:\n\t" "ds_read_b128 v[220:223], %[l1] offset:4096\n\t" "s_waitcnt lgkmcnt(0)\n\t" \
    "v_max3_u32 v218, v220, v221, v222\n\t" "v_max_u32 v218, v218, v223\n\t" "ds_read_b128 v[220:223], %[l1] offset:5120\n\t" "s_waitcnt lgkmcnt(0)\n\t" \
    "v_max3_u32 v218, v218, v220, v221\n\t" "v_max3_u32 v218, v218, v222, v223\n\t" "ds_read_b128 v[220:223], %[l1] offset:6144\n\t" "s_waitcnt lgkmcnt(0)\n\t" \
    "v_max3_u32 v218, v218, v220, v221\n\t" "v_max3_u32 v218, v218, v222, v223\n\t" "ds_read_b128 v[220:223], %[l1] offset:7168\n\t" "s_waitcnt lgkmcnt(0)\n\t" \
    "v_max3_u32 v218, v218, v220, v221\n\t" "v_max3_u32 v218, v218, v222, v223\n\t" "v_cmp_eq_u32 vcc, -1, v218\n\t" "s_cbranch_vccz 22b\n\t" "s_cmp_eq_u32 %[rt], 1\n\t" \
    "s_cbranch_scc1 22b\n\t" "v_mov_b32 v218, %[rt]\n\t" "ds_write_b32 v219, v218\n\t" "s_waitcnt lgkmcnt(0)\n\t" "s_branch 22b\n\t" "23:\n\t" "s_sub_u32 %[rt], %[rt], 1\n\t" \
    "s_waitcnt lgkmcnt(0)\n\t" XCNT(100) "24:\n\t" "buffer_load_dwordx4 v[240:243], %[mo], %[rs], %[sb] offen sc1\n\t" \
    "buffer_load_dwordx4 v[244:247], v216, %[rs], %[sb] offen sc1\n\t" "buffer_load_dwordx4 v[248:251], %[mo], %[rs], s60 offen sc1\n\t" \
    "buffer_load_dwordx4 v[252:255], v216, %[rs], s60 offen sc1\n\t" "s_waitcnt vmcnt(0)\n\t" "v_max_u32 v218, v240, v241\n\t" "v_max3_u32 v218, v218, v242, v243\n\t" \
    "v_max3_u32 v218, v218, v244, v245\n\t" "v_max3_u32 v218, v218, v246, v247\n\t" "v_max3_u32 v218, v218, v248, v249\n\t" "v_max3_u32 v218, v218, v250, v251\n\t" \
    "v_max3_u32 v218, v218, v252, v253\n\t" "v_max3_u32 v218, v218, v254, v255\n\t" "v_cmp_eq_u32 vcc, -1, v218\n\t" "s_cbranch_vccz 25f\n\t" "s_cmp_eq_u32 %[bd], 0\n\t" \
    "s_cbranch_scc1 25f\n\t" "s_sub_u32 %[bd], %[bd], 1\n\t" XCNT(101) "s_branch 24b\n\t" "25:\n\t" "ds_read_b128 v[160:163], %[l0] offset:0\n\t" \
    "ds_read_b128 v[164:167], %[l0] offset:1024\n\t" "ds_read_b128 v[168:171], %[l0] offset:2048\n\t" "ds_read_b128 v[172:175], %[l0] offset:3072\n\t" \
    "ds_read_b128 v[176:179], %[l0] offset:4096\n\t" "ds_read_b128 v[180:183], %[l0] offset:5120\n\t" "ds_read_b128 v[184:187], %[l0] offset:6144\n\t" \
    "ds_read_b128 v[188:191], %[l0] offset:7168\n\t" "v_mov_b64 v[224:225], 0\n\t" "v_mov_b64 v[226:227], 0\n\t" "v_mov_b64 v[228:229], 0\n\t" "v_mov_b64 v[230:231], 0\n\t" \
    "v_mov_b64 v[232:233], 0\n\t" "v_mov_b64 v[234:235], 0\n\t" "v_mov_b64 v[236:237], 0\n\t" "v_mov_b64 v[238:239], 0\n\t" "s_barrier\n\t" "s_branch 20b\n\t" "99:\n\t"
#define QK_STEP2 \
    "v_xor_b32 v216, 0x400, %[mo]\n\t" "s_xor_b32 s60, %[sb], 0x800\n\t" "s_add_u32 s61, %[qb], 0x1000\n\t" "s_mov_b32 s66, 0x3e0293ee\n\t" "s_mov_b32 s67, 0x3e0293ee\n\t" \
    XT(80, 81) XT(98, 99) "s_barrier\n\t" XT(82, 83) "buffer_load_dwordx4 v[240:243], %[mo], %[rs], %[sb] offen sc1\n\t" "v_mov_b64 v[224:225], 0\n\t" \
    "v_mov_b64 v[226:227], 0\n\t" "v_mov_b64 v[228:229], 0\n\t" "v_mov_b64 v[230:231], 0\n\t" "v_mov_b64 v[232:233], 0\n\t" "v_mov_b64 v[234:235], 0\n\t" \
    "v_mov_b64 v[236:237], 0\n\t" "v_mov_b64 v[238:239], 0\n\t" "buffer_load_dwordx4 v[244:247], v216, %[rs], %[sb] offen sc1\n\t" \
    "ds_read_b128 v[160:163], %[l0] offset:0\n\t" "ds_read_b128 v[164:167], %[l0] offset:1024\n\t" "ds_read_b128 v[168:171], %[l0] offset:2048\n\t" \
    "ds_read_b128 v[172:175], %[l0] offset:3072\n\t" "s_nop 3\n\t" "buffer_load_dwordx4 v[248:251], %[mo], %[rs], s60 offen sc1\n\t" \
    "ds_read_b128 v[176:179], %[l0] offset:4096\n\t" "ds_read_b128 v[180:183], %[l0] offset:5120\n\t" "ds_read_b128 v[184:187], %[l0] offset:6144\n\t" \
    "ds_read_b128 v[188:191], %[l0] offset:7168\n\t" "s_nop 3\n\t" "buffer_load_dwordx4 v[252:255], v216, %[rs], s60 offen sc1\n\t" "v_mov_b32 v219, 160512\n\t" \
    "v_mov_b64 v[220:221], -1\n\t" "v_mov_b64 v[222:223], -1\n\t" "buffer_store_dwordx4 v[220:223], %[px], %[rs], %[tr] offen sc1\n\t" \
    "buffer_store_dwordx2 v[220:221], %[pm], %[rs], %[tr] offen sc1\n\t" "v_mov_b32 v208, 0xd028965f\n\t" "v_mov_b32 v209, 0xff800000\n\t" "s_mov_b32 s62, 0\n\t" \
    "s_mov_b32 s63, -1\n\t" "v_mov_b32 v210, 0\n\t" "v_cmp_gt_i32 vcc, 0, %[ps]\n\t" "v_cndmask_b32 v204, 0, v209, s[62:63]\n\t" "s_nop 0\n\t" \
    "v_cndmask_b32 v200, 0, v208, vcc\n\t" "v_cmp_gt_i32 vcc, 1, %[ps]\n\t" "v_cndmask_b32 v205, 0, v209, s[62:63]\n\t" "s_nop 0\n\t" "v_cndmask_b32 v201, 0, v208, vcc\n\t" \
    "v_cmp_gt_i32 vcc, 2, %[ps]\n\t" "v_cndmask_b32 v206, 0, v209, s[62:63]\n\t" "s_nop 0\n\t" "v_cndmask_b32 v202, 0, v208, vcc\n\t" "v_cmp_gt_i32 vcc, 3, %[ps]\n\t" \
    "v_cndmask_b32 v207, 0, v209, s[62:63]\n\t" "s_nop 0\n\t" "v_cndmask_b32 v203, 0, v208, vcc\n\t" "30:\n\t" "s_waitcnt vmcnt(5)\n\t" XT(84, 85) \
    "v_smfmac_f32_16x16x64_bf16 v[224:227], v[240:243], a[0:7], %[ix]\n\t" "ds_write_b128 %[l1], v[240:243] offset:8192\n\t" \
    "v_smfmac_f32_16x16x64_bf16 v[228:231], v[240:243], a[8:15], %[ix]\n\t" "v_smfmac_f32_16x16x64_bf16 v[232:235], v[240:243], v[32:39], %[ix]\n\t" \
    "s_waitcnt lgkmcnt(7)\n\t" "v_smfmac_f32_16x16x64_bf16 v[236:239], v[240:243], v[160:167], %[ix]\n\t" "ds_read_b128 v[160:163], %[l0] offset:8192\n\t" \
    "ds_read_b128 v[164:167], %[l0] offset:9216\n\t" "s_waitcnt vmcnt(4)\n\t" "v_smfmac_f32_16x16x64_bf16 v[224:227], v[244:247], a[16:23], %[ix]\n\t" \
    "ds_write_b128 %[l1], v[244:247] offset:9216\n\t" "v_smfmac_f32_16x16x64_bf16 v[228:231], v[244:247], a[24:31], %[ix]\n\t" \
    "v_smfmac_f32_16x16x64_bf16 v[232:235], v[244:247], v[40:47], %[ix]\n\t" "s_waitcnt lgkmcnt(8)\n\t" \
    "v_smfmac_f32_16x16x64_bf16 v[236:239], v[244:247], v[168:175], %[ix]\n\t" "ds_read_b128 v[168:171], %[l0] offset:10240\n\t" \
    "ds_read_b128 v[172:175], %[l0] offset:11264\n\t" "s_waitcnt vmcnt(3)\n\t" "v_smfmac_f32_16x16x64_bf16 v[224:227], v[248:251], a[32:39], %[ix]\n\t" \
    "ds_write_b128 %[l1], v[248:251] offset:10240\n\t" "v_smfmac_f32_16x16x64_bf16 v[228:231], v[248:251], a[40:47], %[ix]\n\t" "s_waitcnt lgkmcnt(3)\n\t" "s_barrier\n\t" \
    XT(86, 87) "v_smfmac_f32_16x16x64_bf16 v[232:235], v[248:251], v[48:55], %[ix]\n\t" "ds_read_b128 v[192:195], %[l1] offset:12288\n\t" \
    "ds_read_b128 v[196:199], %[l1] offset:0\n\t" "v_smfmac_f32_16x16x64_bf16 v[236:239], v[248:251], v[176:183], %[ix]\n\t" "ds_read_b128 v[176:179], %[l0] offset:12288\n\t" \
    "ds_read_b128 v[180:183], %[l0] offset:13312\n\t" "s_waitcnt vmcnt(2)\n\t" "v_smfmac_f32_16x16x64_bf16 v[224:227], v[252:255], a[48:55], %[ix]\n\t" \
    "ds_write_b128 %[l1], v[252:255] offset:11264\n\t" "v_smfmac_f32_16x16x64_bf16 v[228:231], v[252:255], a[56:63], %[ix]\n\t" \
    "ds_read_b128 v[240:243], %[l1] offset:4096\n\t" "ds_read_b128 v[244:247], %[l1] offset:13312\n\t" \
    "v_smfmac_f32_16x16x64_bf16 v[232:235], v[252:255], v[56:63], %[ix]\n\t" "s_nop 1\n\t" "v_smfmac_f32_16x16x64_bf16 v[236:239], v[252:255], v[184:191], %[ix]\n\t" \
    "ds_read_b128 v[184:187], %[l0] offset:14336\n\t" "v_cmp_u_f32 vcc, v224, v225\n\t" "v_cmp_u_f32 s[62:63], v226, v227\n\t" "s_waitcnt lgkmcnt(7)\n\t" \
    "v_smfmac_f32_16x16x64_bf16 v[224:227], v[192:195], a[64:71], %[ix]\n\t" "ds_read_b128 v[188:191], %[l0] offset:15360\n\t" \
    "v_smfmac_f32_16x16x64_bf16 v[228:231], v[192:195], a[72:79], %[ix]\n\t" "s_or_b64 vcc, vcc, s[62:63]\n\t" "s_cbranch_scc1 31f\n\t" "32:\n\t" \
    "v_smfmac_f32_16x16x64_bf16 v[232:235], v[192:195], v[64:71], %[ix]\n\t" "s_waitcnt lgkmcnt(4)\n\t" "s_barrier\n\t" XT(88, 89) "ds_read_b32 v218, v219\n\t" \
    "v_smfmac_f32_16x16x64_bf16 v[236:239], v[192:195], v[160:167], %[ix]\n\t" "ds_read_b128 v[160:163], %[l0] offset:16384\n\t" \
    "ds_read_b128 v[164:167], %[l0] offset:17408\n\t" "v_smfmac_f32_16x16x64_bf16 v[224:227], v[196:199], a[80:87], %[ix]\n\t" \
    "ds_read_b128 v[248:251], %[l1] offset:1024\n\t" "ds_read_b128 v[252:255], %[l1] offset:5120\n\t" "v_smfmac_f32_16x16x64_bf16 v[228:231], v[196:199], a[88:95], %[ix]\n\t" \
    "ds_read_b128 v[192:195], %[l1] offset:14336\n\t" "v_smfmac_f32_16x16x64_bf16 v[232:235], v[196:199], v[72:79], %[ix]\n\t" \
    "v_smfmac_f32_16x16x64_bf16 v[236:239], v[196:199], v[168:175], %[ix]\n\t" "s_waitcnt lgkmcnt(5)\n\t" "v_cmp_eq_u32 vcc, %[rt], v218\n\t" \
    "v_smfmac_f32_16x16x64_bf16 v[224:227], v[240:243], a[96:103], %[ix]\n\t" "s_cbranch_vccnz 33f\n\t" "ds_read_b128 v[168:171], %[l0] offset:18432\n\t" \
    "v_smfmac_f32_16x16x64_bf16 v[228:231], v[240:243], a[104:111], %[ix]\n\t" "ds_read_b128 v[172:175], %[l0] offset:19456\n\t" \
    "ds_read_b128 v[196:199], %[l1] offset:2048\n\t" "v_smfmac_f32_16x16x64_bf16 v[232:235], v[240:243], v[80:87], %[ix]\n\t" \
    "buffer_load_dwordx4 v[16:19], %[hq], %[rs], %[qb] offen offset:0 sc1\n\t" "v_smfmac_f32_16x16x64_bf16 v[236:239], v[240:243], v[176:183], %[ix]\n\t" \
    "ds_read_b128 v[176:179], %[l0] offset:20480\n\t" "ds_read_b128 v[180:183], %[l0] offset:21504\n\t" \
    "v_smfmac_f32_16x16x64_bf16 v[224:227], v[244:247], a[112:119], %[ix]\n\t" "ds_read_b128 v[240:243], %[l1] offset:6144\n\t" \
    "v_smfmac_f32_16x16x64_bf16 v[228:231], v[244:247], a[120:127], %[ix]\n\t" "v_smfmac_f32_16x16x64_bf16 v[232:235], v[244:247], v[88:95], %[ix]\n\t" \
    "buffer_load_dwordx4 v[20:23], %[hq], %[rs], %[qb] offen offset:2048 sc1\n\t" "v_smfmac_f32_16x16x64_bf16 v[236:239], v[244:247], v[184:191], %[ix]\n\t" \
    "ds_read_b128 v[184:187], %[l0] offset:22528\n\t" "ds_read_b128 v[188:191], %[l0] offset:23552\n\t" "s_waitcnt lgkmcnt(10)\n\t" \
    "v_smfmac_f32_16x16x64_bf16 v[224:227], v[248:251], a[128:135], %[ix]\n\t" "ds_read_b128 v[244:247], %[l1] offset:15360\n\t" \
    "v_smfmac_f32_16x16x64_bf16 v[228:231], v[248:251], a[136:143], %[ix]\n\t" "v_smfmac_f32_16x16x64_bf16 v[232:235], v[248:251], v[96:103], %[ix]\n\t" \
    "buffer_load_dwordx4 v[24:27], %[hq], %[rs], s61 offen offset:0 sc1\n\t" "v_smfmac_f32_16x16x64_bf16 v[236:239], v[248:251], v[160:167], %[ix]\n\t" \
    "ds_read_b128 v[160:163], %[l0] offset:24576\n\t" "ds_read_b128 v[164:167], %[l0] offset:25600\n\t" "s_waitcnt lgkmcnt(12)\n\t" \
    "v_smfmac_f32_16x16x64_bf16 v[224:227], v[252:255], a[144:151], %[ix]\n\t" "ds_read_b128 v[248:251], %[l1] offset:3072\n\t" \
    "v_smfmac_f32_16x16x64_bf16 v[228:231], v[252:255], a[152:159], %[ix]\n\t" "v_smfmac_f32_16x16x64_bf16 v[232:235], v[252:255], v[104:111], %[ix]\n\t" \
    "buffer_load_dwordx4 v[28:31], %[hq], %[rs], s61 offen offset:2048 sc1\n\t" "s_waitcnt lgkmcnt(10)\n\t" \
    "v_smfmac_f32_16x16x64_bf16 v[236:239], v[252:255], v[168:175], %[ix]\n\t" "ds_read_b128 v[168:171], %[l0] offset:26624\n\t" \
    "ds_read_b128 v[172:175], %[l0] offset:27648\n\t" "v_smfmac_f32_16x16x64_bf16 v[224:227], v[192:195], a[160:167], %[ix]\n\t" \
    "ds_read_b128 v[252:255], %[l1] offset:7168\n\t" "v_smfmac_f32_16x16x64_bf16 v[228:231], v[192:195], a[168:175], %[ix]\n\t" "s_waitcnt lgkmcnt(12)\n\t" \
    "v_smfmac_f32_16x16x64_bf16 v[224:227], v[196:199], a[176:183], %[ix]\n\t" "v_smfmac_f32_16x16x64_bf16 v[228:231], v[196:199], a[184:191], %[ix]\n\t" \
    "s_waitcnt lgkmcnt(9)\n\t" "v_smfmac_f32_16x16x64_bf16 v[224:227], v[240:243], a[192:199], %[ix]\n\t" \
    "v_smfmac_f32_16x16x64_bf16 v[228:231], v[240:243], a[200:207], %[ix]\n\t" "s_waitcnt lgkmcnt(6)\n\t" \
    "v_smfmac_f32_16x16x64_bf16 v[224:227], v[244:247], a[208:215], %[ix]\n\t" "v_smfmac_f32_16x16x64_bf16 v[228:231], v[244:247], a[216:223], %[ix]\n\t" \
    "s_waitcnt vmcnt(4)\n\t" "s_waitcnt lgkmcnt(3)\n\t" "v_smfmac_f32_16x16x64_bf16 v[224:227], v[248:251], a[224:231], %[ix]\n\t" \
    "v_smfmac_f32_16x16x64_bf16 v[228:231], v[248:251], a[232:239], %[ix]\n\t" "s_waitcnt lgkmcnt(0)\n\t" \
    "v_smfmac_f32_16x16x64_bf16 v[224:227], v[252:255], a[240:247], %[ix]\n\t" "s_nop 1\n\t" "v_smfmac_f32_16x16x64_bf16 v[228:231], v[252:255], a[248:255], %[ix]\n\t" \
    XT(90, 91) "s_nop 1\n\t" "v_smfmac_f32_16x16x64_bf16 v[232:235], v[192:195], v[112:119], %[ix]\n\t" "s_nop 1\n\t" \
    "v_smfmac_f32_16x16x64_bf16 v[236:239], v[192:195], v[176:183], %[ix]\n\t" "ds_read_b128 v[176:179], %[l0] offset:28672\n\t" "v_add_f32 v224, v224, v225\n\t" \
    "v_add_f32 v226, v226, v227\n\t" "v_smfmac_f32_16x16x64_bf16 v[232:235], v[196:199], v[120:127], %[ix]\n\t" "ds_read_b128 v[180:183], %[l0] offset:29696\n\t" \
    "v_add_f32 v228, v228, v229\n\t" "v_add_f32 v230, v230, v231\n\t" "v_smfmac_f32_16x16x64_bf16 v[236:239], v[196:199], v[184:191], %[ix]\n\t" \
    "ds_read_b128 v[184:187], %[l0] offset:30720\n\t" "v_cvt_pk_bf16_f32 v218, v224, v228\n\t" "v_cvt_pk_bf16_f32 v219, v226, v230\n\t" \
    "v_smfmac_f32_16x16x64_bf16 v[232:235], v[240:243], v[128:135], %[ix]\n\t" "ds_write2_b32 %[qw], v218, v219 offset1:8\n\t" \
    "ds_read_b128 v[188:191], %[l0] offset:31744\n\t" "v_smfmac_f32_16x16x64_bf16 v[236:239], v[240:243], v[160:167], %[ix]\n\t" \
    "v_smfmac_f32_16x16x64_bf16 v[232:235], v[244:247], v[136:143], %[ix]\n\t" "v_smfmac_f32_16x16x64_bf16 v[236:239], v[244:247], v[168:175], %[ix]\n\t" \
    "s_waitcnt lgkmcnt(1)\n\t" "s_barrier\n\t" "v_smfmac_f32_16x16x64_bf16 v[232:235], v[248:251], v[144:151], %[ix]\n\t" \
    "v_smfmac_f32_16x16x64_bf16 v[236:239], v[248:251], v[176:183], %[ix]\n\t" "v_smfmac_f32_16x16x64_bf16 v[232:235], v[252:255], v[152:159], %[ix]\n\t" \
    "s_waitcnt lgkmcnt(0)\n\t" "v_smfmac_f32_16x16x64_bf16 v[236:239], v[252:255], v[184:191], %[ix]\n\t" XT(92, 93) "s_nop 6\n\t" "v_add_f32 v232, v232, v233\n\t" \
    "v_add_f32 v234, v234, v235\n\t" "v_add_f32 v236, v236, v237\n\t" "v_add_f32 v238, v238, v239\n\t" "v_cvt_pk_bf16_f32 v232, v232, v236\n\t" \
    "v_cvt_pk_bf16_f32 v233, v234, v238\n\t" "ds_write2_b32 %[kw], v232, v233 offset1:68\n\t" "s_waitcnt lgkmcnt(0)\n\t" "s_barrier\n\t" XT(94, 95) \
    "s_cmp_ge_u32 %[sq], %[sn]\n\t" "s_cbranch_scc1 377f\n\t" "375:\n\t" "global_load_dword v240, v210, %[fq] sc1\n\t" "s_waitcnt vmcnt(0)\n\t" \
    "v_readfirstlane_b32 %[sq], v240\n\t" "s_nop 3\n\t" "s_cmp_ge_u32 %[sq], %[sn]\n\t" "s_cbranch_scc1 376f\n\t" "s_cmp_eq_u32 %[bd], 0\n\t" "s_cbranch_scc1 376f\n\t" \
    "s_sub_u32 %[bd], %[bd], 1\n\t" "s_sleep 2\n\t" "s_branch 375b\n\t" "376:\n\t" "buffer_load_dwordx4 v[16:19], %[hq], %[rs], %[qb] offen offset:0 sc1\n\t" \
    "buffer_load_dwordx4 v[20:23], %[hq], %[rs], %[qb] offen offset:2048 sc1\n\t" "buffer_load_dwordx4 v[24:27], %[hq], %[rs], s61 offen offset:0 sc1\n\t" \
    "buffer_load_dwordx4 v[28:31], %[hq], %[rs], s61 offen offset:2048 sc1\n\t" "377:\n\t" "s_nop 3\n\t" "ds_read_b128 v[160:163], %[kx] offset:0\n\t" \
    "ds_read_b128 v[164:167], %[kx] offset:2048\n\t" "ds_read_b128 v[168:171], %[kx] offset:4096\n\t" "ds_read_b128 v[172:175], %[kx] offset:6144\n\t" \
    "ds_read_b128 v[176:179], %[kx] offset:256\n\t" "ds_read_b128 v[180:183], %[kx] offset:2304\n\t" "ds_read_b128 v[184:187], %[kx] offset:4352\n\t" \
    "ds_read_b128 v[188:191], %[kx] offset:6400\n\t" "ds_read_b128 v[224:227], %[km] offset:0\n\t" "ds_read_b128 v[228:231], %[km] offset:64\n\t" \
    "ds_read_b128 v[232:235], %[km] offset:128\n\t" "ds_read_b128 v[236:239], %[km] offset:192\n\t" "s_waitcnt vmcnt(0)\n\t" "global_load_dword v240, v210, %[fq] sc1\n\t" \
    "s_waitcnt lgkmcnt(11)\n\t" "v_mfma_f32_16x16x32_bf16 v[248:251], v[160:163], v[16:19], 0\n\t" "s_waitcnt lgkmcnt(10)\n\t" \
    "v_mfma_f32_16x16x32_bf16 v[248:251], v[164:167], v[20:23], v[248:251]\n\t" "s_waitcnt lgkmcnt(9)\n\t" \
    "v_mfma_f32_16x16x32_bf16 v[248:251], v[168:171], v[24:27], v[248:251]\n\t" "s_waitcnt lgkmcnt(8)\n\t" \
    "v_mfma_f32_16x16x32_bf16 v[248:251], v[172:175], v[28:31], v[248:251]\n\t" "s_waitcnt lgkmcnt(7)\n\t" \
    "v_mfma_f32_16x16x32_bf16 v[252:255], v[176:179], v[16:19], v[200:203]\n\t" "s_waitcnt lgkmcnt(6)\n\t" \
    "v_mfma_f32_16x16x32_bf16 v[252:255], v[180:183], v[20:23], v[252:255]\n\t" "s_waitcnt lgkmcnt(5)\n\t" \
    "v_mfma_f32_16x16x32_bf16 v[252:255], v[184:187], v[24:27], v[252:255]\n\t" "s_waitcnt lgkmcnt(4)\n\t" \
    "v_mfma_f32_16x16x32_bf16 v[252:255], v[188:191], v[28:31], v[252:255]\n\t" "s_waitcnt lgkmcnt(3)\n\t" \
    "v_mfma_f32_16x16x32_bf16 v[244:247], v[224:227], v[16:19], v[204:207]\n\t" "s_waitcnt lgkmcnt(2)\n\t" \
    "v_mfma_f32_16x16x32_bf16 v[244:247], v[228:231], v[20:23], v[244:247]\n\t" "s_waitcnt lgkmcnt(1)\n\t" \
    "v_mfma_f32_16x16x32_bf16 v[244:247], v[232:235], v[24:27], v[244:247]\n\t" "s_waitcnt lgkmcnt(0)\n\t" \
    "v_mfma_f32_16x16x32_bf16 v[244:247], v[236:239], v[28:31], v[244:247]\n\t" "s_nop 3\n\t" "v_max3_f32 v242, v248, v249, v250\n\t" "v_max3_f32 v242, v242, v251, v252\n\t" \
    "v_max3_f32 v242, v242, v253, v254\n\t" "v_max_f32 v242, v242, v255\n\t" "v_max3_f32 v242, v242, v244, v245\n\t" "v_max3_f32 v242, v242, v246, v247\n\t" \
    "v_mov_b32 v243, v242\n\t" "s_nop 1\n\t" "v_permlane32_swap_b32 v242, v243\n\t" "v_max_f32 v242, v242, v243\n\t" "v_mov_b32 v243, v242\n\t" "s_nop 1\n\t" \
    "v_permlane16_swap_b32 v242, v243\n\t" "v_max_f32 v242, v242, v243\n\t" "v_pk_add_f32 v[244:245], v[244:245], v[242:243] op_sel_hi:[1,0] neg_lo:[0,1] neg_hi:[0,1]\n\t" \
    "v_pk_add_f32 v[246:247], v[246:247], v[242:243] op_sel_hi:[1,0] neg_lo:[0,1] neg_hi:[0,1]\n\t" \
    "v_pk_add_f32 v[248:249], v[248:249], v[242:243] op_sel_hi:[1,0] neg_lo:[0,1] neg_hi:[0,1]\n\t" \
    "v_pk_add_f32 v[250:251], v[250:251], v[242:243] op_sel_hi:[1,0] neg_lo:[0,1] neg_hi:[0,1]\n\t" \
    "v_pk_add_f32 v[252:253], v[252:253], v[242:243] op_sel_hi:[1,0] neg_lo:[0,1] neg_hi:[0,1]\n\t" \
    "v_pk_add_f32 v[254:255], v[254:255], v[242:243] op_sel_hi:[1,0] neg_lo:[0,1] neg_hi:[0,1]\n\t" "v_pk_mul_f32 v[244:245], v[244:245], s[66:67]\n\t" \
    "v_pk_mul_f32 v[246:247], v[246:247], s[66:67]\n\t" "v_pk_mul_f32 v[248:249], v[248:249], s[66:67]\n\t" "v_pk_mul_f32 v[250:251], v[250:251], s[66:67]\n\t" \
    "v_pk_mul_f32 v[252:253], v[252:253], s[66:67]\n\t" "v_pk_mul_f32 v[254:255], v[254:255], s[66:67]\n\t" "v_exp_f32 v244, v244\n\t" "v_exp_f32 v245, v245\n\t" \
    "v_exp_f32 v246, v246\n\t" "v_exp_f32 v247, v247\n\t" "v_exp_f32 v248, v248\n\t" "v_exp_f32 v249, v249\n\t" "v_exp_f32 v250, v250\n\t" "v_exp_f32 v251, v251\n\t" \
    "v_exp_f32 v252, v252\n\t" "v_exp_f32 v253, v253\n\t" "v_exp_f32 v254, v254\n\t" "v_exp_f32 v255, v255\n\t" "v_pk_add_f32 v[242:243], v[244:245], v[246:247]\n\t" \
    "v_pk_add_f32 v[242:243], v[242:243], v[248:249]\n\t" "v_pk_add_f32 v[242:243], v[242:243], v[250:251]\n\t" "v_pk_add_f32 v[242:243], v[242:243], v[252:253]\n\t" \
    "v_pk_add_f32 v[242:243], v[242:243], v[254:255]\n\t" "v_add_f32 v242, v242, v243\n\t" "v_mov_b32 v243, v242\n\t" "s_nop 1\n\t" "v_permlane32_swap_b32 v242, v243\n\t" \
    "v_add_f32 v242, v242, v243\n\t" "v_mov_b32 v243, v242\n\t" "s_nop 1\n\t" "v_permlane16_swap_b32 v242, v243\n\t" "v_add_f32 v242, v242, v243\n\t" \
    "v_rcp_f32 v242, v242\n\t" "s_nop 0\n\t" "v_pk_mul_f32 v[244:245], v[244:245], v[242:243] op_sel_hi:[1,0]\n\t" \
    "v_pk_mul_f32 v[246:247], v[246:247], v[242:243] op_sel_hi:[1,0]\n\t" "v_pk_mul_f32 v[248:249], v[248:249], v[242:243] op_sel_hi:[1,0]\n\t" \
    "v_pk_mul_f32 v[250:251], v[250:251], v[242:243] op_sel_hi:[1,0]\n\t" "v_pk_mul_f32 v[252:253], v[252:253], v[242:243] op_sel_hi:[1,0]\n\t" \
    "v_pk_mul_f32 v[254:255], v[254:255], v[242:243] op_sel_hi:[1,0]\n\t" "v_cvt_pk_bf16_f32 v162, v244, v245\n\t" "v_cvt_pk_bf16_f32 v163, v246, v247\n\t" \
    "v_cvt_pk_bf16_f32 v164, v248, v249\n\t" "v_cvt_pk_bf16_f32 v165, v250, v251\n\t" "v_cvt_pk_bf16_f32 v166, v252, v253\n\t" "v_cvt_pk_bf16_f32 v167, v254, v255\n\t" \
    "s_barrier\n\t" "buffer_store_dwordx4 v[164:167], %[px], %[rs], %[tb] offen sc1\n\t" "buffer_store_dwordx2 v[162:163], %[pm], %[rs], %[tb] offen sc1\n\t" \
    "s_waitcnt vmcnt(2)\n\t" "v_readfirstlane_b32 s62, v240\n\t" "s_nop 3\n\t" "s_max_u32 %[sq], %[sq], s62\n\t" XT(96, 97) "s_branch 99f\n\t" "31:\n\t" \
    "ds_read_b128 v[220:223], %[l1] offset:8192\n\t" "s_waitcnt lgkmcnt(0)\n\t" "v_max3_u32 v218, v220, v221, v222\n\t" "v_max_u32 v218, v218, v223\n\t" \
    "ds_read_b128 v[220:223], %[l1] offset:9216\n\t" "s_waitcnt lgkmcnt(0)\n\t" "v_max3_u32 v218, v218, v220, v221\n\t" "v_max3_u32 v218, v218, v222, v223\n\t" \
    "ds_read_b128 v[220:223], %[l1] offset:10240\n\t" "s_waitcnt lgkmcnt(0)\n\t" "v_max3_u32 v218, v218, v220, v221\n\t" "v_max3_u32 v218, v218, v222, v223\n\t" \
    "ds_read_b128 v[220:223], %[l1] offset:11264\n\t" "s_waitcnt lgkmcnt(0)\n\t" "v_max3_u32 v218, v218, v220, v221\n\t" "v_max3_u32 v218, v218, v222, v223\n\t" \
    "v_cmp_eq_u32 vcc, -1, v218\n\t" "s_cbranch_vccz 32b\n\t" "s_cmp_eq_u32 %[rt], 1\n\t" "s_cbranch_scc1 32b\n\t" "v_mov_b32 v218, %[rt]\n\t" "ds_write_b32 v219, v218\n\t" \
    "s_waitcnt lgkmcnt(0)\n\t" "s_branch 32b\n\t" "33:\n\t" "s_sub_u32 %[rt], %[rt], 1\n\t" "s_waitcnt lgkmcnt(0)\n\t" XCNT(100) "34:\n\t" \
    "buffer_load_dwordx4 v[240:243], %[mo], %[rs], %[sb] offen sc1\n\t" "buffer_load_dwordx4 v[244:247], v216, %[rs], %[sb] offen sc1\n\t" \
    "buffer_load_dwordx4 v[248:251], %[mo], %[rs], s60 offen sc1\n\t" "buffer_load_dwordx4 v[252:255], v216, %[rs], s60 offen sc1\n\t" "s_waitcnt vmcnt(0)\n\t" \
    "v_max_u32 v218, v240, v241\n\t" "v_max3_u32 v218, v218, v242, v243\n\t" "v_max3_u32 v218, v218, v244, v245\n\t" "v_max3_u32 v218, v218, v246, v247\n\t" \
    "v_max3_u32 v218, v218, v248, v249\n\t" "v_max3_u32 v218, v218, v250, v251\n\t" "v_max3_u32 v218, v218, v252, v253\n\t" "v_max3_u32 v218, v218, v254, v255\n\t" \
    "v_cmp_eq_u32 vcc, -1, v218\n\t" "s_cbranch_vccz 35f\n\t" "s_cmp_eq_u32 %[bd], 0\n\t" "s_cbranch_scc1 35f\n\t" "s_sub_u32 %[bd], %[bd], 1\n\t" XCNT(101) \
    "s_branch 34b\n\t" "35:\n\t" "ds_read_b128 v[160:163], %[l0] offset:0\n\t" "ds_read_b128 v[164:167], %[l0] offset:1024\n\t" \
    "ds_read_b128 v[168:171], %[l0] offset:2048\n\t" "ds_read_b128 v[172:175], %[l0] offset:3072\n\t" "ds_read_b128 v[176:179], %[l0] offset:4096\n\t" \
    "ds_read_b128 v[180:183], %[l0] offset:5120\n\t" "ds_read_b128 v[184:187], %[l0] offset:6144\n\t" "ds_read_b128 v[188:191], %[l0] offset:7168\n\t" \
    "v_mov_b64 v[224:225], 0\n\t" "v_mov_b64 v[226:227], 0\n\t" "v_mov_b64 v[228:229], 0\n\t" "v_mov_b64 v[230:231], 0\n\t" "v_mov_b64 v[232:233], 0\n\t" \
    "v_mov_b64 v[234:235], 0\n\t" "v_mov_b64 v[236:237], 0\n\t" "v_mov_b64 v[238:239], 0\n\t" "s_barrier\n\t" "s_branch 30b\n\t" "99:\n\t"
#define QK_STEP3 \
    "v_xor_b32 v216, 0x400, %[mo]\n\t" "s_xor_b32 s60, %[sb], 0x800\n\t" "s_mov_b32 s67, %[pb]\n\t" XT(80, 81) "s_getpc_b64 s[62:63]\n\t" "48:\n\t" "s_memtime s[64:65]\n\t" \
    "s_waitcnt lgkmcnt(0)\n\t" "s_sub_u32 s61, %[tg], s64\n\t" "s_cmp_gt_i32 s61, 124\n\t" "s_cbranch_scc1 48b\n\t" "s_max_i32 s61, s61, 0\n\t" "s_and_b32 s61, s61, 124\n\t" \
    "s_sub_u32 s61, 49f-48b, s61\n\t" "s_add_u32 s62, s62, s61\n\t" "s_addc_u32 s63, s63, 0\n\t" "s_setpc_b64 s[62:63]\n\t" ".rept 31\n\t" "s_nop 0\n\t" ".endr\n\t" "49:\n\t" \
    XT(98, 99) "s_barrier\n\t" XT(82, 83) "buffer_load_dwordx4 v[240:243], %[mo], %[rs], %[sb] offen sc1\n\t" "v_mov_b64 v[224:225], 0\n\t" "v_mov_b64 v[226:227], 0\n\t" \
    "v_mov_b64 v[228:229], 0\n\t" "v_mov_b64 v[230:231], 0\n\t" "v_mov_b64 v[232:233], 0\n\t" "v_mov_b64 v[234:235], 0\n\t" "v_mov_b64 v[236:237], 0\n\t" \
    "v_mov_b64 v[238:239], 0\n\t" "buffer_load_dwordx4 v[244:247], v216, %[rs], %[sb] offen sc1\n\t" "ds_read_b128 v[160:163], %[l0] offset:0\n\t" \
    "ds_read_b128 v[164:167], %[l0] offset:1024\n\t" "ds_read_b128 v[168:171], %[l0] offset:2048\n\t" "ds_read_b128 v[172:175], %[l0] offset:3072\n\t" "s_nop 3\n\t" \
    "buffer_load_dwordx4 v[248:251], %[mo], %[rs], s60 offen sc1\n\t" "ds_read_b128 v[176:179], %[l0] offset:4096\n\t" "ds_read_b128 v[180:183], %[l0] offset:5120\n\t" \
    "ds_read_b128 v[184:187], %[l0] offset:6144\n\t" "ds_read_b128 v[188:191], %[l0] offset:7168\n\t" "s_nop 3\n\t" \
    "buffer_load_dwordx4 v[252:255], v216, %[rs], s60 offen sc1\n\t" "v_mov_b32 v219, 160512\n\t" "v_mov_b32 v220, -1\n\t" \
    "buffer_store_dword v220, %[hq], %[rs], %[pr] offen sc1\n\t" "40:\n\t" "s_waitcnt vmcnt(4)\n\t" XT(84, 85) \
    "v_smfmac_f32_16x16x64_bf16 v[224:227], v[240:243], a[0:7], %[ix]\n\t" "ds_write_b128 %[l1], v[240:243] offset:12288\n\t" \
    "v_smfmac_f32_16x16x64_bf16 v[228:231], v[240:243], a[8:15], %[ix]\n\t" "v_smfmac_f32_16x16x64_bf16 v[232:235], v[240:243], v[32:39], %[ix]\n\t" \
    "s_waitcnt lgkmcnt(7)\n\t" "v_smfmac_f32_16x16x64_bf16 v[236:239], v[240:243], v[160:167], %[ix]\n\t" "ds_read_b128 v[160:163], %[l0] offset:8192\n\t" \
    "ds_read_b128 v[164:167], %[l0] offset:9216\n\t" "s_waitcnt vmcnt(3)\n\t" "v_smfmac_f32_16x16x64_bf16 v[224:227], v[244:247], a[16:23], %[ix]\n\t" \
    "ds_write_b128 %[l1], v[244:247] offset:13312\n\t" "v_smfmac_f32_16x16x64_bf16 v[228:231], v[244:247], a[24:31], %[ix]\n\t" \
    "v_smfmac_f32_16x16x64_bf16 v[232:235], v[244:247], v[40:47], %[ix]\n\t" "s_waitcnt lgkmcnt(8)\n\t" \
    "v_smfmac_f32_16x16x64_bf16 v[236:239], v[244:247], v[168:175], %[ix]\n\t" "ds_read_b128 v[168:171], %[l0] offset:10240\n\t" \
    "ds_read_b128 v[172:175], %[l0] offset:11264\n\t" "s_waitcnt vmcnt(2)\n\t" "v_smfmac_f32_16x16x64_bf16 v[224:227], v[248:251], a[32:39], %[ix]\n\t" \
    "ds_write_b128 %[l1], v[248:251] offset:14336\n\t" "v_smfmac_f32_16x16x64_bf16 v[228:231], v[248:251], a[40:47], %[ix]\n\t" "s_waitcnt lgkmcnt(3)\n\t" "s_barrier\n\t" \
    XT(86, 87) "v_smfmac_f32_16x16x64_bf16 v[232:235], v[248:251], v[48:55], %[ix]\n\t" "ds_read_b128 v[192:195], %[l1] offset:0\n\t" \
    "ds_read_b128 v[196:199], %[l1] offset:4096\n\t" "v_smfmac_f32_16x16x64_bf16 v[236:239], v[248:251], v[176:183], %[ix]\n\t" \
    "ds_read_b128 v[176:179], %[l0] offset:12288\n\t" "ds_read_b128 v[180:183], %[l0] offset:13312\n\t" "s_waitcnt vmcnt(1)\n\t" \
    "v_smfmac_f32_16x16x64_bf16 v[224:227], v[252:255], a[48:55], %[ix]\n\t" "ds_write_b128 %[l1], v[252:255] offset:15360\n\t" \
    "v_smfmac_f32_16x16x64_bf16 v[228:231], v[252:255], a[56:63], %[ix]\n\t" "ds_read_b128 v[240:243], %[l1] offset:8192\n\t" "ds_read_b128 v[244:247], %[l1] offset:1024\n\t" \
    "v_smfmac_f32_16x16x64_bf16 v[232:235], v[252:255], v[56:63], %[ix]\n\t" "s_nop 1\n\t" "v_smfmac_f32_16x16x64_bf16 v[236:239], v[252:255], v[184:191], %[ix]\n\t" \
    "ds_read_b128 v[184:187], %[l0] offset:14336\n\t" "v_cmp_u_f32 vcc, v224, v225\n\t" "v_cmp_u_f32 s[62:63], v226, v227\n\t" "s_waitcnt lgkmcnt(7)\n\t" \
    "v_smfmac_f32_16x16x64_bf16 v[224:227], v[192:195], a[64:71], %[ix]\n\t" "ds_read_b128 v[188:191], %[l0] offset:15360\n\t" \
    "v_smfmac_f32_16x16x64_bf16 v[228:231], v[192:195], a[72:79], %[ix]\n\t" "s_or_b64 vcc, vcc, s[62:63]\n\t" "s_cbranch_scc1 41f\n\t" "42:\n\t" \
    "v_smfmac_f32_16x16x64_bf16 v[232:235], v[192:195], v[64:71], %[ix]\n\t" "s_waitcnt lgkmcnt(4)\n\t" "s_barrier\n\t" XT(88, 89) "ds_read_b32 v218, v219\n\t" \
    "v_smfmac_f32_16x16x64_bf16 v[236:239], v[192:195], v[160:167], %[ix]\n\t" "ds_read_b128 v[160:163], %[l0] offset:16384\n\t" \
    "ds_read_b128 v[164:167], %[l0] offset:17408\n\t" "v_smfmac_f32_16x16x64_bf16 v[224:227], v[196:199], a[80:87], %[ix]\n\t" \
    "ds_read_b128 v[248:251], %[l1] offset:5120\n\t" "ds_read_b128 v[252:255], %[l1] offset:9216\n\t" "v_smfmac_f32_16x16x64_bf16 v[228:231], v[196:199], a[88:95], %[ix]\n\t" \
    "s_memtime s[64:65]\n\t" "ds_read_b128 v[192:195], %[l1] offset:2048\n\t" "v_smfmac_f32_16x16x64_bf16 v[232:235], v[196:199], v[72:79], %[ix]\n\t" \
    "v_smfmac_f32_16x16x64_bf16 v[236:239], v[196:199], v[168:175], %[ix]\n\t" "s_waitcnt lgkmcnt(5)\n\t" "v_cmp_eq_u32 vcc, %[rt], v218\n\t" \
    "v_smfmac_f32_16x16x64_bf16 v[224:227], v[240:243], a[96:103], %[ix]\n\t" "s_cbranch_vccnz 43f\n\t" "ds_read_b128 v[168:171], %[l0] offset:18432\n\t" \
    "v_smfmac_f32_16x16x64_bf16 v[228:231], v[240:243], a[104:111], %[ix]\n\t" "ds_read_b128 v[172:175], %[l0] offset:19456\n\t" \
    "ds_read_b128 v[196:199], %[l1] offset:6144\n\t" "v_smfmac_f32_16x16x64_bf16 v[232:235], v[240:243], v[80:87], %[ix]\n\t" "s_sub_u32 s62, s64, %[ca]\n\t" \
    "s_mul_hi_u32 s63, s62, %[k1]\n\t" "v_smfmac_f32_16x16x64_bf16 v[236:239], v[240:243], v[176:183], %[ix]\n\t" "s_mul_i32 s62, s62, %[k1]\n\t" \
    "s_lshr_b64 s[62:63], s[62:63], 16\n\t" "v_smfmac_f32_16x16x64_bf16 v[224:227], v[244:247], a[112:119], %[ix]\n\t" "s_add_u32 s62, s62, %[ga]\n\t" \
    "s_bitset0_b32 s62, 0\n\t" "v_smfmac_f32_16x16x64_bf16 v[228:231], v[244:247], a[120:127], %[ix]\n\t" "v_mov_b32 v217, s62\n\t" \
    "buffer_store_dword v217, %[hq], %[rs], s67 offen sc1\n\t" "v_smfmac_f32_16x16x64_bf16 v[232:235], v[244:247], v[88:95], %[ix]\n\t" \
    "ds_read_b128 v[176:179], %[l0] offset:20480\n\t" "ds_read_b128 v[180:183], %[l0] offset:21504\n\t" \
    "v_smfmac_f32_16x16x64_bf16 v[236:239], v[244:247], v[184:191], %[ix]\n\t" "ds_read_b128 v[240:243], %[l1] offset:10240\n\t" \
    "ds_read_b128 v[184:187], %[l0] offset:22528\n\t" "s_waitcnt lgkmcnt(9)\n\t" "v_smfmac_f32_16x16x64_bf16 v[224:227], v[248:251], a[128:135], %[ix]\n\t" \
    "ds_read_b128 v[188:191], %[l0] offset:23552\n\t" "ds_read_b128 v[244:247], %[l1] offset:3072\n\t" \
    "v_smfmac_f32_16x16x64_bf16 v[228:231], v[248:251], a[136:143], %[ix]\n\t" "v_lshlrev_b32 v16, 3, %[l1]\n\t" "buffer_load_dword v17, v16, %[rs], %[kn] offen sc1\n\t" \
    "v_smfmac_f32_16x16x64_bf16 v[232:235], v[248:251], v[96:103], %[ix]\n\t" "v_smfmac_f32_16x16x64_bf16 v[236:239], v[248:251], v[160:167], %[ix]\n\t" \
    "ds_read_b128 v[160:163], %[l0] offset:24576\n\t" "ds_read_b128 v[164:167], %[l0] offset:25600\n\t" "s_waitcnt lgkmcnt(12)\n\t" \
    "v_smfmac_f32_16x16x64_bf16 v[224:227], v[252:255], a[144:151], %[ix]\n\t" "ds_read_b128 v[248:251], %[l1] offset:7168\n\t" \
    "v_smfmac_f32_16x16x64_bf16 v[228:231], v[252:255], a[152:159], %[ix]\n\t" "v_smfmac_f32_16x16x64_bf16 v[232:235], v[252:255], v[104:111], %[ix]\n\t" \
    "s_waitcnt lgkmcnt(10)\n\t" "v_smfmac_f32_16x16x64_bf16 v[236:239], v[252:255], v[168:175], %[ix]\n\t" "ds_read_b128 v[168:171], %[l0] offset:26624\n\t" \
    "ds_read_b128 v[172:175], %[l0] offset:27648\n\t" "v_smfmac_f32_16x16x64_bf16 v[224:227], v[192:195], a[160:167], %[ix]\n\t" \
    "ds_read_b128 v[252:255], %[l1] offset:11264\n\t" "v_smfmac_f32_16x16x64_bf16 v[228:231], v[192:195], a[168:175], %[ix]\n\t" "s_waitcnt lgkmcnt(12)\n\t" \
    "v_smfmac_f32_16x16x64_bf16 v[224:227], v[196:199], a[176:183], %[ix]\n\t" "v_smfmac_f32_16x16x64_bf16 v[228:231], v[196:199], a[184:191], %[ix]\n\t" \
    "s_waitcnt lgkmcnt(9)\n\t" "v_smfmac_f32_16x16x64_bf16 v[224:227], v[240:243], a[192:199], %[ix]\n\t" \
    "v_smfmac_f32_16x16x64_bf16 v[228:231], v[240:243], a[200:207], %[ix]\n\t" "s_waitcnt lgkmcnt(6)\n\t" \
    "v_smfmac_f32_16x16x64_bf16 v[224:227], v[244:247], a[208:215], %[ix]\n\t" "v_smfmac_f32_16x16x64_bf16 v[228:231], v[244:247], a[216:223], %[ix]\n\t" \
    "s_waitcnt lgkmcnt(3)\n\t" "v_smfmac_f32_16x16x64_bf16 v[224:227], v[248:251], a[224:231], %[ix]\n\t" \
    "v_smfmac_f32_16x16x64_bf16 v[228:231], v[248:251], a[232:239], %[ix]\n\t" "s_waitcnt lgkmcnt(0)\n\t" \
    "v_smfmac_f32_16x16x64_bf16 v[224:227], v[252:255], a[240:247], %[ix]\n\t" "s_nop 1\n\t" "v_smfmac_f32_16x16x64_bf16 v[228:231], v[252:255], a[248:255], %[ix]\n\t" \
    XT(90, 91) "s_nop 1\n\t" "v_smfmac_f32_16x16x64_bf16 v[232:235], v[192:195], v[112:119], %[ix]\n\t" "s_nop 1\n\t" \
    "v_smfmac_f32_16x16x64_bf16 v[236:239], v[192:195], v[176:183], %[ix]\n\t" "ds_read_b128 v[176:179], %[l0] offset:28672\n\t" "v_add_f32 v224, v224, v225\n\t" \
    "v_add_f32 v226, v226, v227\n\t" "v_smfmac_f32_16x16x64_bf16 v[232:235], v[196:199], v[120:127], %[ix]\n\t" "ds_read_b128 v[180:183], %[l0] offset:29696\n\t" \
    "v_add_f32 v228, v228, v229\n\t" "v_add_f32 v230, v230, v231\n\t" "v_smfmac_f32_16x16x64_bf16 v[236:239], v[196:199], v[184:191], %[ix]\n\t" \
    "ds_read_b128 v[184:187], %[l0] offset:30720\n\t" "v_cvt_pk_bf16_f32 v218, v224, v228\n\t" "v_cvt_pk_bf16_f32 v219, v226, v230\n\t" \
    "v_smfmac_f32_16x16x64_bf16 v[232:235], v[240:243], v[128:135], %[ix]\n\t" "ds_write2_b32 %[qw], v218, v219 offset1:8\n\t" \
    "ds_read_b128 v[188:191], %[l0] offset:31744\n\t" "v_smfmac_f32_16x16x64_bf16 v[236:239], v[240:243], v[160:167], %[ix]\n\t" \
    "v_smfmac_f32_16x16x64_bf16 v[232:235], v[244:247], v[136:143], %[ix]\n\t" "v_smfmac_f32_16x16x64_bf16 v[236:239], v[244:247], v[168:175], %[ix]\n\t" \
    "s_waitcnt lgkmcnt(1)\n\t" "s_barrier\n\t" "v_smfmac_f32_16x16x64_bf16 v[232:235], v[248:251], v[144:151], %[ix]\n\t" \
    "v_smfmac_f32_16x16x64_bf16 v[236:239], v[248:251], v[176:183], %[ix]\n\t" "v_smfmac_f32_16x16x64_bf16 v[232:235], v[252:255], v[152:159], %[ix]\n\t" \
    "s_waitcnt lgkmcnt(0)\n\t" "v_smfmac_f32_16x16x64_bf16 v[236:239], v[252:255], v[184:191], %[ix]\n\t" XT(92, 93) "s_nop 6\n\t" "v_add_f32 v232, v232, v233\n\t" \
    "v_add_f32 v234, v234, v235\n\t" "v_add_f32 v236, v236, v237\n\t" "v_add_f32 v238, v238, v239\n\t" "v_cvt_pk_bf16_f32 v232, v232, v236\n\t" \
    "v_cvt_pk_bf16_f32 v233, v234, v238\n\t" "ds_write2_b32 %[kw], v232, v233 offset1:68\n\t" "s_waitcnt lgkmcnt(0)\n\t" "s_barrier\n\t" XT(94, 95) XT(96, 97) \
    "s_branch 99f\n\t" "41:\n\t" "ds_read_b128 v[220:223], %[l1] offset:12288\n\t" "s_waitcnt lgkmcnt(0)\n\t" "v_max3_u32 v218, v220, v221, v222\n\t" \
    "v_max_u32 v218, v218, v223\n\t" "ds_read_b128 v[220:223], %[l1] offset:13312\n\t" "s_waitcnt lgkmcnt(0)\n\t" "v_max3_u32 v218, v218, v220, v221\n\t" \
    "v_max3_u32 v218, v218, v222, v223\n\t" "ds_read_b128 v[220:223], %[l1] offset:14336\n\t" "s_waitcnt lgkmcnt(0)\n\t" "v_max3_u32 v218, v218, v220, v221\n\t" \
    "v_max3_u32 v218, v218, v222, v223\n\t" "ds_read_b128 v[220:223], %[l1] offset:15360\n\t" "s_waitcnt lgkmcnt(0)\n\t" "v_max3_u32 v218, v218, v220, v221\n\t" \
    "v_max3_u32 v218, v218, v222, v223\n\t" "v_cmp_eq_u32 vcc, -1, v218\n\t" "s_cbranch_vccz 42b\n\t" "s_cmp_eq_u32 %[rt], 1\n\t" "s_cbranch_scc1 42b\n\t" \
    "v_mov_b32 v218, %[rt]\n\t" "ds_write_b32 v219, v218\n\t" "s_waitcnt lgkmcnt(0)\n\t" "s_branch 42b\n\t" "43:\n\t" "s_sub_u32 %[rt], %[rt], 1\n\t" \
    "s_waitcnt lgkmcnt(0)\n\t" XCNT(100) "44:\n\t" "buffer_load_dwordx4 v[240:243], %[mo], %[rs], %[sb] offen sc1\n\t" \
    "buffer_load_dwordx4 v[244:247], v216, %[rs], %[sb] offen sc1\n\t" "buffer_load_dwordx4 v[248:251], %[mo], %[rs], s60 offen sc1\n\t" \
    "buffer_load_dwordx4 v[252:255], v216, %[rs], s60 offen sc1\n\t" "s_waitcnt vmcnt(0)\n\t" "v_max_u32 v218, v240, v241\n\t" "v_max3_u32 v218, v218, v242, v243\n\t" \
    "v_max3_u32 v218, v218, v244, v245\n\t" "v_max3_u32 v218, v218, v246, v247\n\t" "v_max3_u32 v218, v218, v248, v249\n\t" "v_max3_u32 v218, v218, v250, v251\n\t" \
    "v_max3_u32 v218, v218, v252, v253\n\t" "v_max3_u32 v218, v218, v254, v255\n\t" "v_cmp_eq_u32 vcc, -1, v218\n\t" "s_cbranch_vccz 45f\n\t" "s_cmp_eq_u32 %[bd], 0\n\t" \
    "s_cbranch_scc1 45f\n\t" "s_sub_u32 %[bd], %[bd], 1\n\t" XCNT(101) "s_branch 44b\n\t" "45:\n\t" "ds_read_b128 v[160:163], %[l0] offset:0\n\t" \
    "ds_read_b128 v[164:167], %[l0] offset:1024\n\t" "ds_read_b128 v[168:171], %[l0] offset:2048\n\t" "ds_read_b128 v[172:175], %[l0] offset:3072\n\t" \
    "ds_read_b128 v[176:179], %[l0] offset:4096\n\t" "ds_read_b128 v[180:183], %[l0] offset:5120\n\t" "ds_read_b128 v[184:187], %[l0] offset:6144\n\t" \
    "ds_read_b128 v[188:191], %[l0] offset:7168\n\t" "v_mov_b64 v[224:225], 0\n\t" "v_mov_b64 v[226:227], 0\n\t" "v_mov_b64 v[228:229], 0\n\t" "v_mov_b64 v[230:231], 0\n\t" \
    "v_mov_b64 v[232:233], 0\n\t" "v_mov_b64 v[234:235], 0\n\t" "v_mov_b64 v[236:237], 0\n\t" "v_mov_b64 v[238:239], 0\n\t" "s_barrier\n\t" "s_memtime s[64:65]\n\t" \
    "s_waitcnt lgkmcnt(0)\n\t" "s_add_u32 s64, s64, %[dr]\n\t" "s_sub_u32 s62, s64, %[ca]\n\t" "s_mul_hi_u32 s63, s62, %[k1]\n\t" "s_mul_i32 s62, s62, %[k1]\n\t" \
    "s_lshr_b64 s[62:63], s[62:63], 16\n\t" "s_add_u32 s62, s62, %[ga]\n\t" "s_bitset0_b32 s62, 0\n\t" "v_mov_b32 v218, s62\n\t" \
    "buffer_store_dword v218, %[hq], %[rs], s67 offen sc1\n\t" "s_mov_b32 s67, 0x7f000000\n\t" "s_branch 40b\n\t" "99:\n\t" "mk_tail:\n\t"
#define VO_STEP \
    "v_xor_b32 v224, 0x400, %[mo]\n\t" "s_xor_b32 s60, %[sb], 0x800\n\t" "s_mov_b32 s67, %[pb]\n\t" "s_cmp_eq_u32 %[w], 1\n\t" "s_cbranch_scc1 91f\n\t" \
    "s_cmp_eq_u32 %[w], 2\n\t" "s_cbranch_scc1 92f\n\t" "s_cmp_eq_u32 %[w], 3\n\t" "s_cbranch_scc1 93f\n\t" XT(80, 81) "s_getpc_b64 s[62:63]\n\t" "18:\n\t" \
    "s_memtime s[64:65]\n\t" "s_waitcnt lgkmcnt(0)\n\t" "s_sub_u32 s61, %[tg], s64\n\t" "s_cmp_gt_i32 s61, 124\n\t" "s_cbranch_scc1 18b\n\t" "s_max_i32 s61, s61, 0\n\t" \
    "s_and_b32 s61, s61, 124\n\t" "s_sub_u32 s61, 19f-18b, s61\n\t" "s_add_u32 s62, s62, s61\n\t" "s_addc_u32 s63, s63, 0\n\t" "s_setpc_b64 s[62:63]\n\t" ".rept 31\n\t" \
    "s_nop 0\n\t" ".endr\n\t" "19:\n\t" "s_barrier\n\t" XT(82, 83) "buffer_load_dwordx4 v[240:243], %[mo], %[rs], %[sb] offen sc1\n\t" "v_mov_b64 v[232:233], 0\n\t" \
    "v_mov_b64 v[234:235], 0\n\t" "v_mov_b64 v[236:237], 0\n\t" "v_mov_b64 v[238:239], 0\n\t" "v_mov_b64 v[192:193], 0\n\t" "v_mov_b64 v[194:195], 0\n\t" \
    "v_mov_b64 v[196:197], 0\n\t" "v_mov_b64 v[198:199], 0\n\t" "buffer_load_dwordx4 v[244:247], v224, %[rs], %[sb] offen sc1\n\t" \
    "ds_read_b128 v[160:163], %[l0] offset:0\n\t" "ds_read_b128 v[164:167], %[l0] offset:1024\n\t" "ds_read_b128 v[168:171], %[l0] offset:2048\n\t" \
    "ds_read_b128 v[172:175], %[l0] offset:3072\n\t" "s_nop 3\n\t" "buffer_load_dwordx4 v[248:251], %[mo], %[rs], s60 offen sc1\n\t" \
    "ds_read_b128 v[176:179], %[l0] offset:4096\n\t" "ds_read_b128 v[180:183], %[l0] offset:5120\n\t" "ds_read_b128 v[184:187], %[l0] offset:6144\n\t" \
    "ds_read_b128 v[188:191], %[l0] offset:7168\n\t" "s_nop 3\n\t" "buffer_load_dwordx4 v[252:255], v224, %[rs], s60 offen sc1\n\t" "v_mov_b64 v[200:201], 0\n\t" \
    "v_mov_b64 v[202:203], 0\n\t" "v_mov_b64 v[204:205], 0\n\t" "v_mov_b64 v[206:207], 0\n\t" "v_mov_b64 v[208:209], 0\n\t" "v_mov_b64 v[210:211], 0\n\t" \
    "v_mov_b64 v[212:213], 0\n\t" "v_mov_b64 v[214:215], 0\n\t" "v_mov_b64 v[216:217], 0\n\t" "v_mov_b64 v[218:219], 0\n\t" "v_mov_b64 v[220:221], 0\n\t" \
    "v_mov_b64 v[222:223], 0\n\t" "v_mov_b32 v226, -1\n\t" "buffer_store_dword v226, %[hq], %[rs], %[pr] offen sc1\n\t" "v_mov_b32 v227, 160512\n\t" "10:\n\t" \
    "s_waitcnt vmcnt(4)\n\t" XT(84, 85) "v_smfmac_f32_16x16x64_bf16 v[232:235], v[240:243], a[0:7], %[ix]\n\t" "ds_write_b128 %[l1], v[240:243] offset:0\n\t" \
    "v_smfmac_f32_16x16x64_bf16 v[236:239], v[240:243], a[8:15], %[ix]\n\t" "s_waitcnt vmcnt(3)\n\t" "v_smfmac_f32_16x16x64_bf16 v[232:235], v[244:247], a[16:23], %[ix]\n\t" \
    "ds_write_b128 %[l1], v[244:247] offset:1024\n\t" "s_waitcnt lgkmcnt(8)\n\t" "v_smfmac_f32_16x16x64_bf16 v[236:239], v[244:247], v[160:167], %[ix]\n\t" \
    "s_waitcnt lgkmcnt(0)\n\t" "s_barrier\n\t" XT(86, 87) "ds_read_b128 v[16:19], %[l1] offset:4096\n\t" "ds_read_b128 v[20:23], %[l1] offset:8192\n\t" \
    "ds_read_b128 v[24:27], %[l1] offset:12288\n\t" "s_waitcnt vmcnt(2)\n\t" "v_smfmac_f32_16x16x64_bf16 v[232:235], v[248:251], a[24:31], %[ix]\n\t" \
    "ds_write_b128 %[l1], v[248:251] offset:2048\n\t" "v_smfmac_f32_16x16x64_bf16 v[236:239], v[248:251], a[32:39], %[ix]\n\t" "ds_read_b128 v[28:31], %[l1] offset:5120\n\t" \
    "ds_read_b128 v[240:243], %[l1] offset:9216\n\t" "ds_read_b128 v[244:247], %[l1] offset:13312\n\t" "s_waitcnt vmcnt(1)\n\t" \
    "v_smfmac_f32_16x16x64_bf16 v[232:235], v[252:255], a[40:47], %[ix]\n\t" "ds_write_b128 %[l1], v[252:255] offset:3072\n\t" \
    "v_smfmac_f32_16x16x64_bf16 v[236:239], v[252:255], v[168:175], %[ix]\n\t" "ds_read_b128 v[160:163], %[l0] offset:8192\n\t" \
    "ds_read_b128 v[164:167], %[l0] offset:9216\n\t" "ds_read_b128 v[168:171], %[l0] offset:10240\n\t" "ds_read_b128 v[172:175], %[l0] offset:11264\n\t" "s_nop 1\n\t" \
    "v_cmp_u_f32 vcc, v232, v233\n\t" "v_cmp_u_f32 s[62:63], v234, v235\n\t" "s_waitcnt lgkmcnt(11)\n\t" \
    "v_smfmac_f32_16x16x64_bf16 v[232:235], v[16:19], a[48:55], %[ix]\n\t" "v_smfmac_f32_16x16x64_bf16 v[236:239], v[16:19], a[56:63], %[ix]\n\t" \
    "s_or_b64 vcc, vcc, s[62:63]\n\t" "s_cbranch_scc1 11f\n\t" "12:\n\t" "s_waitcnt lgkmcnt(10)\n\t" "v_smfmac_f32_16x16x64_bf16 v[232:235], v[20:23], a[64:71], %[ix]\n\t" \
    "v_smfmac_f32_16x16x64_bf16 v[236:239], v[20:23], v[176:183], %[ix]\n\t" "s_waitcnt lgkmcnt(4)\n\t" "s_barrier\n\t" XT(88, 89) "ds_read_b32 v225, v227\n\t" \
    "v_smfmac_f32_16x16x64_bf16 v[232:235], v[24:27], a[72:79], %[ix]\n\t" "ds_read_b128 v[176:179], %[l0] offset:12288\n\t" "ds_read_b128 v[180:183], %[l0] offset:13312\n\t" \
    "v_smfmac_f32_16x16x64_bf16 v[236:239], v[24:27], a[80:87], %[ix]\n\t" "ds_read_b128 v[248:251], %[l1] offset:6144\n\t" "ds_read_b128 v[252:255], %[l1] offset:10240\n\t" \
    "v_smfmac_f32_16x16x64_bf16 v[232:235], v[28:31], a[88:95], %[ix]\n\t" "ds_read_b128 v[16:19], %[l1] offset:14336\n\t" "ds_read_b128 v[20:23], %[l1] offset:7168\n\t" \
    "v_smfmac_f32_16x16x64_bf16 v[236:239], v[28:31], v[184:191], %[ix]\n\t" "s_memtime s[64:65]\n\t" "ds_read_b128 v[24:27], %[l1] offset:11264\n\t" \
    "v_smfmac_f32_16x16x64_bf16 v[232:235], v[240:243], a[96:103], %[ix]\n\t" "ds_read_b128 v[184:187], %[l0] offset:14336\n\t" \
    "ds_read_b128 v[188:191], %[l0] offset:15360\n\t" "v_smfmac_f32_16x16x64_bf16 v[236:239], v[240:243], a[104:111], %[ix]\n\t" "s_waitcnt lgkmcnt(9)\n\t" \
    "v_cmp_eq_u32 vcc, %[rt], v225\n\t" "v_smfmac_f32_16x16x64_bf16 v[232:235], v[244:247], a[112:119], %[ix]\n\t" "s_cbranch_vccnz 13f\n\t" \
    "ds_read_b128 v[28:31], %[l1] offset:15360\n\t" "v_smfmac_f32_16x16x64_bf16 v[236:239], v[244:247], v[160:167], %[ix]\n\t" \
    "ds_read_b128 v[160:163], %[l0] offset:16384\n\t" "ds_read_b128 v[164:167], %[l0] offset:17408\n\t" "s_waitcnt lgkmcnt(9)\n\t" \
    "v_smfmac_f32_16x16x64_bf16 v[232:235], v[248:251], a[120:127], %[ix]\n\t" "s_sub_u32 s62, s64, %[ca]\n\t" "s_mul_hi_u32 s63, s62, %[k1]\n\t" \
    "v_smfmac_f32_16x16x64_bf16 v[236:239], v[248:251], a[128:135], %[ix]\n\t" "s_mul_i32 s62, s62, %[k1]\n\t" "s_lshr_b64 s[62:63], s[62:63], 16\n\t" \
    "s_waitcnt lgkmcnt(8)\n\t" "v_smfmac_f32_16x16x64_bf16 v[232:235], v[252:255], a[136:143], %[ix]\n\t" "s_add_u32 s62, s62, %[ga]\n\t" "s_bitset0_b32 s62, 0\n\t" \
    "v_smfmac_f32_16x16x64_bf16 v[236:239], v[252:255], v[168:175], %[ix]\n\t" "v_mov_b32 v226, s62\n\t" "buffer_store_dword v226, %[hq], %[rs], s67 offen sc1\n\t" \
    "s_waitcnt lgkmcnt(7)\n\t" "v_smfmac_f32_16x16x64_bf16 v[232:235], v[16:19], a[144:151], %[ix]\n\t" "ds_read_b128 v[168:171], %[l0] offset:18432\n\t" \
    "ds_read_b128 v[172:175], %[l0] offset:19456\n\t" "v_smfmac_f32_16x16x64_bf16 v[236:239], v[16:19], a[152:159], %[ix]\n\t" "s_waitcnt lgkmcnt(8)\n\t" \
    "v_smfmac_f32_16x16x64_bf16 v[232:235], v[20:23], a[160:167], %[ix]\n\t" "v_smfmac_f32_16x16x64_bf16 v[236:239], v[20:23], v[176:183], %[ix]\n\t" \
    "ds_read_b128 v[176:179], %[l0] offset:20480\n\t" "ds_read_b128 v[180:183], %[l0] offset:21504\n\t" "s_waitcnt lgkmcnt(9)\n\t" \
    "v_smfmac_f32_16x16x64_bf16 v[232:235], v[24:27], a[168:175], %[ix]\n\t" "v_smfmac_f32_16x16x64_bf16 v[236:239], v[24:27], a[176:183], %[ix]\n\t" \
    "s_waitcnt lgkmcnt(6)\n\t" "v_smfmac_f32_16x16x64_bf16 v[232:235], v[28:31], a[184:191], %[ix]\n\t" \
    "v_smfmac_f32_16x16x64_bf16 v[236:239], v[28:31], v[184:191], %[ix]\n\t" "ds_read_b128 v[184:187], %[l0] offset:22528\n\t" \
    "ds_read_b128 v[188:191], %[l0] offset:23552\n\t" XT(90, 91) "s_nop 4\n\t" "v_add_f32 v232, v232, v233\n\t" "v_add_f32 v234, v234, v235\n\t" \
    "v_add_f32 v236, v236, v237\n\t" "v_add_f32 v238, v238, v239\n\t" "v_cvt_pk_bf16_f32 v236, v232, v236\n\t" "v_cvt_pk_bf16_f32 v237, v234, v238\n\t" \
    "ds_write2_b32 %[vw], v236, v237 offset1:68\n\t" "s_waitcnt lgkmcnt(0)\n\t" "s_barrier\n\t" XT(92, 93) "ds_read_b128 v[240:243], %[vr] offset:0\n\t" \
    "ds_read_b128 v[244:247], %[vr] offset:64\n\t" "ds_read_b128 v[248:251], %[vr] offset:128\n\t" "ds_read_b128 v[252:255], %[vr] offset:192\n\t" "s_mov_b32 s61, %[ub]\n\t" \
    "s_waitcnt lgkmcnt(3)\n\t" "v_smfmac_f32_16x16x64_bf16 v[192:195], v[240:243], v[32:39], %[ix]\n\t" \
    "v_smfmac_f32_16x16x64_bf16 v[196:199], v[240:243], v[48:55], %[ix]\n\t" "s_waitcnt lgkmcnt(2)\n\t" \
    "v_smfmac_f32_16x16x64_bf16 v[192:195], v[244:247], v[40:47], %[ix]\n\t" "v_smfmac_f32_16x16x64_bf16 v[196:199], v[244:247], v[56:63], %[ix]\n\t" \
    "s_waitcnt lgkmcnt(1)\n\t" "v_smfmac_f32_16x16x64_bf16 v[192:195], v[248:251], a[192:199], %[ix]\n\t" \
    "v_smfmac_f32_16x16x64_bf16 v[196:199], v[248:251], a[200:207], %[ix]\n\t" "s_waitcnt lgkmcnt(0)\n\t" \
    "v_smfmac_f32_16x16x64_bf16 v[192:195], v[252:255], v[160:167], %[ix]\n\t" "ds_read_b128 v[160:163], %[l0] offset:24576\n\t" \
    "ds_read_b128 v[164:167], %[l0] offset:25600\n\t" "v_smfmac_f32_16x16x64_bf16 v[196:199], v[252:255], v[168:175], %[ix]\n\t" \
    "ds_read_b128 v[168:171], %[l0] offset:26624\n\t" "ds_read_b128 v[172:175], %[l0] offset:27648\n\t" \
    "v_smfmac_f32_16x16x64_bf16 v[200:203], v[240:243], v[64:71], %[ix]\n\t" "s_nop 1\n\t" "v_smfmac_f32_16x16x64_bf16 v[204:207], v[240:243], v[80:87], %[ix]\n\t" \
    "v_cvt_pk_bf16_f32 v224, v192, v194\n\t" "v_cvt_pk_bf16_f32 v225, v193, v195\n\t" "v_smfmac_f32_16x16x64_bf16 v[200:203], v[244:247], v[72:79], %[ix]\n\t" \
    "v_cvt_pk_bf16_f32 v226, v196, v198\n\t" "v_cvt_pk_bf16_f32 v227, v197, v199\n\t" "v_smfmac_f32_16x16x64_bf16 v[204:207], v[244:247], v[88:95], %[ix]\n\t" "s_nop 0\n\t" \
    "v_permlane32_swap_b32 v224, v226\n\t" "v_smfmac_f32_16x16x64_bf16 v[200:203], v[248:251], a[208:215], %[ix]\n\t" "v_permlane32_swap_b32 v225, v227\n\t" "s_nop 0\n\t" \
    "v_smfmac_f32_16x16x64_bf16 v[204:207], v[248:251], a[216:223], %[ix]\n\t" "v_permlane16_swap_b32 v224, v225\n\t" "v_permlane16_swap_b32 v226, v227\n\t" \
    "v_smfmac_f32_16x16x64_bf16 v[200:203], v[252:255], v[176:183], %[ix]\n\t" "ds_read_b128 v[176:179], %[l0] offset:28672\n\t" \
    "buffer_store_dwordx4 v[224:227], %[uo], %[rs], s61 offen sc1\n\t" "s_xor_b32 s61, s61, 0x40000\n\t" XT(94, 95) \
    "v_smfmac_f32_16x16x64_bf16 v[204:207], v[252:255], v[184:191], %[ix]\n\t" "ds_read_b128 v[180:183], %[l0] offset:29696\n\t" \
    "ds_read_b128 v[184:187], %[l0] offset:30720\n\t" "v_smfmac_f32_16x16x64_bf16 v[208:211], v[240:243], v[96:103], %[ix]\n\t" \
    "ds_read_b128 v[188:191], %[l0] offset:31744\n\t" "s_nop 0\n\t" "v_smfmac_f32_16x16x64_bf16 v[212:215], v[240:243], v[112:119], %[ix]\n\t" \
    "v_cvt_pk_bf16_f32 v228, v200, v202\n\t" "v_cvt_pk_bf16_f32 v229, v201, v203\n\t" "v_smfmac_f32_16x16x64_bf16 v[208:211], v[244:247], v[104:111], %[ix]\n\t" \
    "v_cvt_pk_bf16_f32 v230, v204, v206\n\t" "v_cvt_pk_bf16_f32 v231, v205, v207\n\t" "v_smfmac_f32_16x16x64_bf16 v[212:215], v[244:247], v[120:127], %[ix]\n\t" "s_nop 0\n\t" \
    "v_permlane32_swap_b32 v228, v230\n\t" "v_smfmac_f32_16x16x64_bf16 v[208:211], v[248:251], a[224:231], %[ix]\n\t" "v_permlane32_swap_b32 v229, v231\n\t" "s_nop 0\n\t" \
    "v_smfmac_f32_16x16x64_bf16 v[212:215], v[248:251], a[232:239], %[ix]\n\t" "v_permlane16_swap_b32 v228, v229\n\t" "v_permlane16_swap_b32 v230, v231\n\t" \
    "s_waitcnt lgkmcnt(6)\n\t" "v_smfmac_f32_16x16x64_bf16 v[208:211], v[252:255], v[160:167], %[ix]\n\t" "buffer_store_dwordx4 v[228:231], %[uo], %[rs], s61 offen sc1\n\t" \
    "s_xor_b32 s61, s61, 0xc0000\n\t" "s_waitcnt lgkmcnt(4)\n\t" "v_smfmac_f32_16x16x64_bf16 v[212:215], v[252:255], v[168:175], %[ix]\n\t" "s_nop 1\n\t" \
    "v_smfmac_f32_16x16x64_bf16 v[216:219], v[240:243], v[128:135], %[ix]\n\t" "s_nop 1\n\t" "v_smfmac_f32_16x16x64_bf16 v[220:223], v[240:243], v[144:151], %[ix]\n\t" \
    "v_cvt_pk_bf16_f32 v224, v208, v210\n\t" "v_cvt_pk_bf16_f32 v225, v209, v211\n\t" "v_smfmac_f32_16x16x64_bf16 v[216:219], v[244:247], v[136:143], %[ix]\n\t" \
    "v_cvt_pk_bf16_f32 v226, v212, v214\n\t" "v_cvt_pk_bf16_f32 v227, v213, v215\n\t" "v_smfmac_f32_16x16x64_bf16 v[220:223], v[244:247], v[152:159], %[ix]\n\t" "s_nop 0\n\t" \
    "v_permlane32_swap_b32 v224, v226\n\t" "v_smfmac_f32_16x16x64_bf16 v[216:219], v[248:251], a[240:247], %[ix]\n\t" "v_permlane32_swap_b32 v225, v227\n\t" "s_nop 0\n\t" \
    "v_smfmac_f32_16x16x64_bf16 v[220:223], v[248:251], a[248:255], %[ix]\n\t" "v_permlane16_swap_b32 v224, v225\n\t" "v_permlane16_swap_b32 v226, v227\n\t" \
    "s_waitcnt lgkmcnt(2)\n\t" "v_smfmac_f32_16x16x64_bf16 v[216:219], v[252:255], v[176:183], %[ix]\n\t" "buffer_store_dwordx4 v[224:227], %[uo], %[rs], s61 offen sc1\n\t" \
    "s_xor_b32 s61, s61, 0x40000\n\t" "s_waitcnt lgkmcnt(0)\n\t" "v_smfmac_f32_16x16x64_bf16 v[220:223], v[252:255], v[184:191], %[ix]\n\t" "s_nop 5\n\t" \
    "v_cvt_pk_bf16_f32 v228, v216, v218\n\t" "v_cvt_pk_bf16_f32 v229, v217, v219\n\t" "v_cvt_pk_bf16_f32 v230, v220, v222\n\t" "v_cvt_pk_bf16_f32 v231, v221, v223\n\t" \
    "s_nop 0\n\t" "v_permlane32_swap_b32 v228, v230\n\t" "v_permlane32_swap_b32 v229, v231\n\t" "s_nop 1\n\t" "v_permlane16_swap_b32 v228, v229\n\t" \
    "v_permlane16_swap_b32 v230, v231\n\t" "buffer_store_dwordx4 v[228:231], %[uo], %[rs], s61 offen sc1\n\t" "s_nop 1\n\t" XT(96, 97) "s_branch 99f\n\t" "11:\n\t" \
    "ds_read_b128 v[228:231], %[l1] offset:0\n\t" "s_waitcnt lgkmcnt(0)\n\t" "v_max3_u32 v225, v228, v229, v230\n\t" "v_max_u32 v225, v225, v231\n\t" \
    "ds_read_b128 v[228:231], %[l1] offset:1024\n\t" "s_waitcnt lgkmcnt(0)\n\t" "v_max3_u32 v225, v225, v228, v229\n\t" "v_max3_u32 v225, v225, v230, v231\n\t" \
    "ds_read_b128 v[228:231], %[l1] offset:2048\n\t" "s_waitcnt lgkmcnt(0)\n\t" "v_max3_u32 v225, v225, v228, v229\n\t" "v_max3_u32 v225, v225, v230, v231\n\t" \
    "ds_read_b128 v[228:231], %[l1] offset:3072\n\t" "s_waitcnt lgkmcnt(0)\n\t" "v_max3_u32 v225, v225, v228, v229\n\t" "v_max3_u32 v225, v225, v230, v231\n\t" \
    "v_cmp_eq_u32 vcc, -1, v225\n\t" "s_cbranch_vccz 12b\n\t" "s_cmp_eq_u32 %[rt], 1\n\t" "s_cbranch_scc1 12b\n\t" "v_mov_b32 v225, %[rt]\n\t" "ds_write_b32 v227, v225\n\t" \
    "s_waitcnt lgkmcnt(0)\n\t" "s_branch 12b\n\t" "13:\n\t" "s_sub_u32 %[rt], %[rt], 1\n\t" "s_waitcnt lgkmcnt(0)\n\t" XCNT(100) "14:\n\t" \
    "buffer_load_dwordx4 v[240:243], %[mo], %[rs], %[sb] offen sc1\n\t" "buffer_load_dwordx4 v[244:247], v224, %[rs], %[sb] offen sc1\n\t" \
    "buffer_load_dwordx4 v[248:251], %[mo], %[rs], s60 offen sc1\n\t" "buffer_load_dwordx4 v[252:255], v224, %[rs], s60 offen sc1\n\t" "s_waitcnt vmcnt(0)\n\t" \
    "v_max_u32 v225, v240, v241\n\t" "v_max3_u32 v225, v225, v242, v243\n\t" "v_max3_u32 v225, v225, v244, v245\n\t" "v_max3_u32 v225, v225, v246, v247\n\t" \
    "v_max3_u32 v225, v225, v248, v249\n\t" "v_max3_u32 v225, v225, v250, v251\n\t" "v_max3_u32 v225, v225, v252, v253\n\t" "v_max3_u32 v225, v225, v254, v255\n\t" \
    "v_cmp_eq_u32 vcc, -1, v225\n\t" "s_cbranch_vccz 15f\n\t" "s_cmp_eq_u32 %[bd], 0\n\t" "s_cbranch_scc1 15f\n\t" "s_sub_u32 %[bd], %[bd], 1\n\t" XCNT(101) \
    "s_branch 14b\n\t" "15:\n\t" "ds_read_b128 v[160:163], %[l0] offset:0\n\t" "ds_read_b128 v[164:167], %[l0] offset:1024\n\t" \
    "ds_read_b128 v[168:171], %[l0] offset:2048\n\t" "ds_read_b128 v[172:175], %[l0] offset:3072\n\t" "ds_read_b128 v[176:179], %[l0] offset:4096\n\t" \
    "ds_read_b128 v[180:183], %[l0] offset:5120\n\t" "ds_read_b128 v[184:187], %[l0] offset:6144\n\t" "ds_read_b128 v[188:191], %[l0] offset:7168\n\t" \
    "v_mov_b64 v[232:233], 0\n\t" "v_mov_b64 v[234:235], 0\n\t" "v_mov_b64 v[236:237], 0\n\t" "v_mov_b64 v[238:239], 0\n\t" "s_barrier\n\t" "s_memtime s[64:65]\n\t" \
    "s_waitcnt lgkmcnt(0)\n\t" "s_add_u32 s64, s64, %[dr]\n\t" "s_sub_u32 s62, s64, %[ca]\n\t" "s_mul_hi_u32 s63, s62, %[k1]\n\t" "s_mul_i32 s62, s62, %[k1]\n\t" \
    "s_lshr_b64 s[62:63], s[62:63], 16\n\t" "s_add_u32 s62, s62, %[ga]\n\t" "s_bitset0_b32 s62, 0\n\t" "v_mov_b32 v225, s62\n\t" \
    "buffer_store_dword v225, %[hq], %[rs], s67 offen sc1\n\t" "s_mov_b32 s67, 0x7f000000\n\t" "s_branch 10b\n\t" "91:\n\t" XT(80, 81) "s_barrier\n\t" XT(82, 83) \
    "buffer_load_dwordx4 v[240:243], %[mo], %[rs], %[sb] offen sc1\n\t" "v_mov_b64 v[232:233], 0\n\t" "v_mov_b64 v[234:235], 0\n\t" "v_mov_b64 v[236:237], 0\n\t" \
    "v_mov_b64 v[238:239], 0\n\t" "v_mov_b64 v[192:193], 0\n\t" "v_mov_b64 v[194:195], 0\n\t" "v_mov_b64 v[196:197], 0\n\t" "v_mov_b64 v[198:199], 0\n\t" \
    "buffer_load_dwordx4 v[244:247], v224, %[rs], %[sb] offen sc1\n\t" "ds_read_b128 v[160:163], %[l0] offset:0\n\t" "ds_read_b128 v[164:167], %[l0] offset:1024\n\t" \
    "ds_read_b128 v[168:171], %[l0] offset:2048\n\t" "ds_read_b128 v[172:175], %[l0] offset:3072\n\t" "s_nop 3\n\t" \
    "buffer_load_dwordx4 v[248:251], %[mo], %[rs], s60 offen sc1\n\t" "ds_read_b128 v[176:179], %[l0] offset:4096\n\t" "ds_read_b128 v[180:183], %[l0] offset:5120\n\t" \
    "ds_read_b128 v[184:187], %[l0] offset:6144\n\t" "ds_read_b128 v[188:191], %[l0] offset:7168\n\t" "s_nop 3\n\t" \
    "buffer_load_dwordx4 v[252:255], v224, %[rs], s60 offen sc1\n\t" "v_mov_b64 v[200:201], 0\n\t" "v_mov_b64 v[202:203], 0\n\t" "v_mov_b64 v[204:205], 0\n\t" \
    "v_mov_b64 v[206:207], 0\n\t" "v_mov_b64 v[208:209], 0\n\t" "v_mov_b64 v[210:211], 0\n\t" "v_mov_b64 v[212:213], 0\n\t" "v_mov_b64 v[214:215], 0\n\t" \
    "v_mov_b64 v[216:217], 0\n\t" "v_mov_b64 v[218:219], 0\n\t" "v_mov_b64 v[220:221], 0\n\t" "v_mov_b64 v[222:223], 0\n\t" "v_mov_b32 v227, 160512\n\t" "20:\n\t" \
    "s_waitcnt vmcnt(3)\n\t" XT(84, 85) "v_smfmac_f32_16x16x64_bf16 v[232:235], v[240:243], a[0:7], %[ix]\n\t" "ds_write_b128 %[l1], v[240:243] offset:4096\n\t" \
    "v_smfmac_f32_16x16x64_bf16 v[236:239], v[240:243], a[8:15], %[ix]\n\t" "s_waitcnt vmcnt(2)\n\t" "v_smfmac_f32_16x16x64_bf16 v[232:235], v[244:247], a[16:23], %[ix]\n\t" \
    "ds_write_b128 %[l1], v[244:247] offset:5120\n\t" "s_waitcnt lgkmcnt(8)\n\t" "v_smfmac_f32_16x16x64_bf16 v[236:239], v[244:247], v[160:167], %[ix]\n\t" \
    "s_waitcnt lgkmcnt(0)\n\t" "s_barrier\n\t" XT(86, 87) "ds_read_b128 v[16:19], %[l1] offset:8192\n\t" "ds_read_b128 v[20:23], %[l1] offset:12288\n\t" \
    "ds_read_b128 v[24:27], %[l1] offset:0\n\t" "s_waitcnt vmcnt(1)\n\t" "v_smfmac_f32_16x16x64_bf16 v[232:235], v[248:251], a[24:31], %[ix]\n\t" \
    "ds_write_b128 %[l1], v[248:251] offset:6144\n\t" "v_smfmac_f32_16x16x64_bf16 v[236:239], v[248:251], a[32:39], %[ix]\n\t" "ds_read_b128 v[28:31], %[l1] offset:9216\n\t" \
    "ds_read_b128 v[240:243], %[l1] offset:13312\n\t" "ds_read_b128 v[244:247], %[l1] offset:1024\n\t" "s_waitcnt vmcnt(0)\n\t" \
    "v_smfmac_f32_16x16x64_bf16 v[232:235], v[252:255], a[40:47], %[ix]\n\t" "ds_write_b128 %[l1], v[252:255] offset:7168\n\t" \
    "v_smfmac_f32_16x16x64_bf16 v[236:239], v[252:255], v[168:175], %[ix]\n\t" "ds_read_b128 v[160:163], %[l0] offset:8192\n\t" \
    "ds_read_b128 v[164:167], %[l0] offset:9216\n\t" "ds_read_b128 v[168:171], %[l0] offset:10240\n\t" "ds_read_b128 v[172:175], %[l0] offset:11264\n\t" "s_nop 1\n\t" \
    "v_cmp_u_f32 vcc, v232, v233\n\t" "v_cmp_u_f32 s[62:63], v234, v235\n\t" "s_waitcnt lgkmcnt(11)\n\t" \
    "v_smfmac_f32_16x16x64_bf16 v[232:235], v[16:19], a[48:55], %[ix]\n\t" "v_smfmac_f32_16x16x64_bf16 v[236:239], v[16:19], a[56:63], %[ix]\n\t" \
    "s_or_b64 vcc, vcc, s[62:63]\n\t" "s_cbranch_scc1 21f\n\t" "22:\n\t" "s_waitcnt lgkmcnt(10)\n\t" "v_smfmac_f32_16x16x64_bf16 v[232:235], v[20:23], a[64:71], %[ix]\n\t" \
    "v_smfmac_f32_16x16x64_bf16 v[236:239], v[20:23], v[176:183], %[ix]\n\t" "s_waitcnt lgkmcnt(4)\n\t" "s_barrier\n\t" XT(88, 89) "ds_read_b32 v225, v227\n\t" \
    "v_smfmac_f32_16x16x64_bf16 v[232:235], v[24:27], a[72:79], %[ix]\n\t" "ds_read_b128 v[176:179], %[l0] offset:12288\n\t" "ds_read_b128 v[180:183], %[l0] offset:13312\n\t" \
    "v_smfmac_f32_16x16x64_bf16 v[236:239], v[24:27], a[80:87], %[ix]\n\t" "ds_read_b128 v[248:251], %[l1] offset:10240\n\t" "ds_read_b128 v[252:255], %[l1] offset:14336\n\t" \
    "v_smfmac_f32_16x16x64_bf16 v[232:235], v[28:31], a[88:95], %[ix]\n\t" "ds_read_b128 v[16:19], %[l1] offset:2048\n\t" "ds_read_b128 v[20:23], %[l1] offset:11264\n\t" \
    "v_smfmac_f32_16x16x64_bf16 v[236:239], v[28:31], v[184:191], %[ix]\n\t" "ds_read_b128 v[24:27], %[l1] offset:15360\n\t" "ds_read_b128 v[184:187], %[l0] offset:14336\n\t" \
    "v_smfmac_f32_16x16x64_bf16 v[232:235], v[240:243], a[96:103], %[ix]\n\t" "ds_read_b128 v[188:191], %[l0] offset:15360\n\t" "ds_read_b128 v[28:31], %[l1] offset:3072\n\t" \
    "v_smfmac_f32_16x16x64_bf16 v[236:239], v[240:243], a[104:111], %[ix]\n\t" "s_waitcnt lgkmcnt(10)\n\t" "v_cmp_eq_u32 vcc, %[rt], v225\n\t" \
    "v_smfmac_f32_16x16x64_bf16 v[232:235], v[244:247], a[112:119], %[ix]\n\t" "s_cbranch_vccnz 23f\n\t" \
    "v_smfmac_f32_16x16x64_bf16 v[236:239], v[244:247], v[160:167], %[ix]\n\t" "ds_read_b128 v[160:163], %[l0] offset:16384\n\t" \
    "ds_read_b128 v[164:167], %[l0] offset:17408\n\t" "s_waitcnt lgkmcnt(9)\n\t" "v_smfmac_f32_16x16x64_bf16 v[232:235], v[248:251], a[120:127], %[ix]\n\t" \
    "v_smfmac_f32_16x16x64_bf16 v[236:239], v[248:251], a[128:135], %[ix]\n\t" "s_waitcnt lgkmcnt(8)\n\t" \
    "v_smfmac_f32_16x16x64_bf16 v[232:235], v[252:255], a[136:143], %[ix]\n\t" "v_smfmac_f32_16x16x64_bf16 v[236:239], v[252:255], v[168:175], %[ix]\n\t" \
    "ds_read_b128 v[168:171], %[l0] offset:18432\n\t" "ds_read_b128 v[172:175], %[l0] offset:19456\n\t" "s_waitcnt lgkmcnt(9)\n\t" \
    "v_smfmac_f32_16x16x64_bf16 v[232:235], v[16:19], a[144:151], %[ix]\n\t" "v_smfmac_f32_16x16x64_bf16 v[236:239], v[16:19], a[152:159], %[ix]\n\t" \
    "s_waitcnt lgkmcnt(8)\n\t" "v_smfmac_f32_16x16x64_bf16 v[232:235], v[20:23], a[160:167], %[ix]\n\t" \
    "v_smfmac_f32_16x16x64_bf16 v[236:239], v[20:23], v[176:183], %[ix]\n\t" "ds_read_b128 v[176:179], %[l0] offset:20480\n\t" \
    "ds_read_b128 v[180:183], %[l0] offset:21504\n\t" "s_waitcnt lgkmcnt(9)\n\t" "v_smfmac_f32_16x16x64_bf16 v[232:235], v[24:27], a[168:175], %[ix]\n\t" \
    "v_smfmac_f32_16x16x64_bf16 v[236:239], v[24:27], a[176:183], %[ix]\n\t" "s_waitcnt lgkmcnt(6)\n\t" \
    "v_smfmac_f32_16x16x64_bf16 v[232:235], v[28:31], a[184:191], %[ix]\n\t" "v_smfmac_f32_16x16x64_bf16 v[236:239], v[28:31], v[184:191], %[ix]\n\t" \
    "ds_read_b128 v[184:187], %[l0] offset:22528\n\t" "ds_read_b128 v[188:191], %[l0] offset:23552\n\t" XT(90, 91) "s_nop 4\n\t" "v_add_f32 v232, v232, v233\n\t" \
    "v_add_f32 v234, v234, v235\n\t" "v_add_f32 v236, v236, v237\n\t" "v_add_f32 v238, v238, v239\n\t" "v_cvt_pk_bf16_f32 v236, v232, v236\n\t" \
    "v_cvt_pk_bf16_f32 v237, v234, v238\n\t" "ds_write2_b32 %[vw], v236, v237 offset1:68\n\t" "s_waitcnt lgkmcnt(0)\n\t" "s_barrier\n\t" XT(92, 93) \
    "ds_read_b128 v[240:243], %[vr] offset:0\n\t" "ds_read_b128 v[244:247], %[vr] offset:64\n\t" "ds_read_b128 v[248:251], %[vr] offset:128\n\t" \
    "ds_read_b128 v[252:255], %[vr] offset:192\n\t" "s_mov_b32 s61, %[ub]\n\t" "s_waitcnt lgkmcnt(3)\n\t" \
    "v_smfmac_f32_16x16x64_bf16 v[192:195], v[240:243], v[32:39], %[ix]\n\t" "v_smfmac_f32_16x16x64_bf16 v[196:199], v[240:243], v[48:55], %[ix]\n\t" \
    "s_waitcnt lgkmcnt(2)\n\t" "v_smfmac_f32_16x16x64_bf16 v[192:195], v[244:247], v[40:47], %[ix]\n\t" \
    "v_smfmac_f32_16x16x64_bf16 v[196:199], v[244:247], v[56:63], %[ix]\n\t" "s_waitcnt lgkmcnt(1)\n\t" \
    "v_smfmac_f32_16x16x64_bf16 v[192:195], v[248:251], a[192:199], %[ix]\n\t" "v_smfmac_f32_16x16x64_bf16 v[196:199], v[248:251], a[200:207], %[ix]\n\t" \
    "s_waitcnt lgkmcnt(0)\n\t" "v_smfmac_f32_16x16x64_bf16 v[192:195], v[252:255], v[160:167], %[ix]\n\t" "ds_read_b128 v[160:163], %[l0] offset:24576\n\t" \
    "ds_read_b128 v[164:167], %[l0] offset:25600\n\t" "v_smfmac_f32_16x16x64_bf16 v[196:199], v[252:255], v[168:175], %[ix]\n\t" \
    "ds_read_b128 v[168:171], %[l0] offset:26624\n\t" "ds_read_b128 v[172:175], %[l0] offset:27648\n\t" \
    "v_smfmac_f32_16x16x64_bf16 v[200:203], v[240:243], v[64:71], %[ix]\n\t" "s_nop 1\n\t" "v_smfmac_f32_16x16x64_bf16 v[204:207], v[240:243], v[80:87], %[ix]\n\t" \
    "v_cvt_pk_bf16_f32 v224, v192, v194\n\t" "v_cvt_pk_bf16_f32 v225, v193, v195\n\t" "v_smfmac_f32_16x16x64_bf16 v[200:203], v[244:247], v[72:79], %[ix]\n\t" \
    "v_cvt_pk_bf16_f32 v226, v196, v198\n\t" "v_cvt_pk_bf16_f32 v227, v197, v199\n\t" "v_smfmac_f32_16x16x64_bf16 v[204:207], v[244:247], v[88:95], %[ix]\n\t" "s_nop 0\n\t" \
    "v_permlane32_swap_b32 v224, v226\n\t" "v_smfmac_f32_16x16x64_bf16 v[200:203], v[248:251], a[208:215], %[ix]\n\t" "v_permlane32_swap_b32 v225, v227\n\t" "s_nop 0\n\t" \
    "v_smfmac_f32_16x16x64_bf16 v[204:207], v[248:251], a[216:223], %[ix]\n\t" "v_permlane16_swap_b32 v224, v225\n\t" "v_permlane16_swap_b32 v226, v227\n\t" \
    "v_smfmac_f32_16x16x64_bf16 v[200:203], v[252:255], v[176:183], %[ix]\n\t" "ds_read_b128 v[176:179], %[l0] offset:28672\n\t" \
    "buffer_store_dwordx4 v[224:227], %[uo], %[rs], s61 offen sc1\n\t" "s_xor_b32 s61, s61, 0x40000\n\t" XT(94, 95) \
    "v_smfmac_f32_16x16x64_bf16 v[204:207], v[252:255], v[184:191], %[ix]\n\t" "ds_read_b128 v[180:183], %[l0] offset:29696\n\t" \
    "ds_read_b128 v[184:187], %[l0] offset:30720\n\t" "v_smfmac_f32_16x16x64_bf16 v[208:211], v[240:243], v[96:103], %[ix]\n\t" \
    "ds_read_b128 v[188:191], %[l0] offset:31744\n\t" "s_nop 0\n\t" "v_smfmac_f32_16x16x64_bf16 v[212:215], v[240:243], v[112:119], %[ix]\n\t" \
    "v_cvt_pk_bf16_f32 v228, v200, v202\n\t" "v_cvt_pk_bf16_f32 v229, v201, v203\n\t" "v_smfmac_f32_16x16x64_bf16 v[208:211], v[244:247], v[104:111], %[ix]\n\t" \
    "v_cvt_pk_bf16_f32 v230, v204, v206\n\t" "v_cvt_pk_bf16_f32 v231, v205, v207\n\t" "v_smfmac_f32_16x16x64_bf16 v[212:215], v[244:247], v[120:127], %[ix]\n\t" "s_nop 0\n\t" \
    "v_permlane32_swap_b32 v228, v230\n\t" "v_smfmac_f32_16x16x64_bf16 v[208:211], v[248:251], a[224:231], %[ix]\n\t" "v_permlane32_swap_b32 v229, v231\n\t" "s_nop 0\n\t" \
    "v_smfmac_f32_16x16x64_bf16 v[212:215], v[248:251], a[232:239], %[ix]\n\t" "v_permlane16_swap_b32 v228, v229\n\t" "v_permlane16_swap_b32 v230, v231\n\t" \
    "s_waitcnt lgkmcnt(6)\n\t" "v_smfmac_f32_16x16x64_bf16 v[208:211], v[252:255], v[160:167], %[ix]\n\t" "buffer_store_dwordx4 v[228:231], %[uo], %[rs], s61 offen sc1\n\t" \
    "s_xor_b32 s61, s61, 0xc0000\n\t" "s_waitcnt lgkmcnt(4)\n\t" "v_smfmac_f32_16x16x64_bf16 v[212:215], v[252:255], v[168:175], %[ix]\n\t" "s_nop 1\n\t" \
    "v_smfmac_f32_16x16x64_bf16 v[216:219], v[240:243], v[128:135], %[ix]\n\t" "s_nop 1\n\t" "v_smfmac_f32_16x16x64_bf16 v[220:223], v[240:243], v[144:151], %[ix]\n\t" \
    "v_cvt_pk_bf16_f32 v224, v208, v210\n\t" "v_cvt_pk_bf16_f32 v225, v209, v211\n\t" "v_smfmac_f32_16x16x64_bf16 v[216:219], v[244:247], v[136:143], %[ix]\n\t" \
    "v_cvt_pk_bf16_f32 v226, v212, v214\n\t" "v_cvt_pk_bf16_f32 v227, v213, v215\n\t" "v_smfmac_f32_16x16x64_bf16 v[220:223], v[244:247], v[152:159], %[ix]\n\t" "s_nop 0\n\t" \
    "v_permlane32_swap_b32 v224, v226\n\t" "v_smfmac_f32_16x16x64_bf16 v[216:219], v[248:251], a[240:247], %[ix]\n\t" "v_permlane32_swap_b32 v225, v227\n\t" "s_nop 0\n\t" \
    "v_smfmac_f32_16x16x64_bf16 v[220:223], v[248:251], a[248:255], %[ix]\n\t" "v_permlane16_swap_b32 v224, v225\n\t" "v_permlane16_swap_b32 v226, v227\n\t" \
    "s_waitcnt lgkmcnt(2)\n\t" "v_smfmac_f32_16x16x64_bf16 v[216:219], v[252:255], v[176:183], %[ix]\n\t" "buffer_store_dwordx4 v[224:227], %[uo], %[rs], s61 offen sc1\n\t" \
    "s_xor_b32 s61, s61, 0x40000\n\t" "s_waitcnt lgkmcnt(0)\n\t" "v_smfmac_f32_16x16x64_bf16 v[220:223], v[252:255], v[184:191], %[ix]\n\t" "s_nop 5\n\t" \
    "v_cvt_pk_bf16_f32 v228, v216, v218\n\t" "v_cvt_pk_bf16_f32 v229, v217, v219\n\t" "v_cvt_pk_bf16_f32 v230, v220, v222\n\t" "v_cvt_pk_bf16_f32 v231, v221, v223\n\t" \
    "s_nop 0\n\t" "v_permlane32_swap_b32 v228, v230\n\t" "v_permlane32_swap_b32 v229, v231\n\t" "s_nop 1\n\t" "v_permlane16_swap_b32 v228, v229\n\t" \
    "v_permlane16_swap_b32 v230, v231\n\t" "buffer_store_dwordx4 v[228:231], %[uo], %[rs], s61 offen sc1\n\t" "s_nop 1\n\t" XT(96, 97) "v_mov_b64 v[16:17], -1\n\t" \
    "v_mov_b64 v[18:19], -1\n\t" "s_barrier\n\t" "buffer_store_dwordx4 v[16:19], %[l1], %[rs], %[ur] offen offset:0 sc1\n\t" \
    "buffer_store_dwordx4 v[16:19], %[l1], %[rs], %[ur] offen offset:1024 sc1\n\t" "buffer_store_dwordx4 v[16:19], %[l1], %[rs], %[ur] offen offset:2048 sc1\n\t" \
    "buffer_store_dwordx4 v[16:19], %[l1], %[rs], %[ur] offen offset:3072 sc1\n\t" "s_sub_u32 s61, %[ur], 0x1000\n\t" \
    "buffer_store_dwordx4 v[16:19], %[l1], %[rs], s61 offen offset:0 sc1\n\t" "buffer_store_dwordx4 v[16:19], %[l1], %[rs], s61 offen offset:1024 sc1\n\t" \
    "buffer_store_dwordx4 v[16:19], %[l1], %[rs], s61 offen offset:2048 sc1\n\t" "buffer_store_dwordx4 v[16:19], %[l1], %[rs], s61 offen offset:3072 sc1\n\t" \
    "s_branch 99f\n\t" "21:\n\t" "ds_read_b128 v[228:231], %[l1] offset:4096\n\t" "s_waitcnt lgkmcnt(0)\n\t" "v_max3_u32 v225, v228, v229, v230\n\t" \
    "v_max_u32 v225, v225, v231\n\t" "ds_read_b128 v[228:231], %[l1] offset:5120\n\t" "s_waitcnt lgkmcnt(0)\n\t" "v_max3_u32 v225, v225, v228, v229\n\t" \
    "v_max3_u32 v225, v225, v230, v231\n\t" "ds_read_b128 v[228:231], %[l1] offset:6144\n\t" "s_waitcnt lgkmcnt(0)\n\t" "v_max3_u32 v225, v225, v228, v229\n\t" \
    "v_max3_u32 v225, v225, v230, v231\n\t" "ds_read_b128 v[228:231], %[l1] offset:7168\n\t" "s_waitcnt lgkmcnt(0)\n\t" "v_max3_u32 v225, v225, v228, v229\n\t" \
    "v_max3_u32 v225, v225, v230, v231\n\t" "v_cmp_eq_u32 vcc, -1, v225\n\t" "s_cbranch_vccz 22b\n\t" "s_cmp_eq_u32 %[rt], 1\n\t" "s_cbranch_scc1 22b\n\t" \
    "v_mov_b32 v225, %[rt]\n\t" "ds_write_b32 v227, v225\n\t" "s_waitcnt lgkmcnt(0)\n\t" "s_branch 22b\n\t" "23:\n\t" "s_sub_u32 %[rt], %[rt], 1\n\t" \
    "s_waitcnt lgkmcnt(0)\n\t" XCNT(100) "24:\n\t" "buffer_load_dwordx4 v[240:243], %[mo], %[rs], %[sb] offen sc1\n\t" \
    "buffer_load_dwordx4 v[244:247], v224, %[rs], %[sb] offen sc1\n\t" "buffer_load_dwordx4 v[248:251], %[mo], %[rs], s60 offen sc1\n\t" \
    "buffer_load_dwordx4 v[252:255], v224, %[rs], s60 offen sc1\n\t" "s_waitcnt vmcnt(0)\n\t" "v_max_u32 v225, v240, v241\n\t" "v_max3_u32 v225, v225, v242, v243\n\t" \
    "v_max3_u32 v225, v225, v244, v245\n\t" "v_max3_u32 v225, v225, v246, v247\n\t" "v_max3_u32 v225, v225, v248, v249\n\t" "v_max3_u32 v225, v225, v250, v251\n\t" \
    "v_max3_u32 v225, v225, v252, v253\n\t" "v_max3_u32 v225, v225, v254, v255\n\t" "v_cmp_eq_u32 vcc, -1, v225\n\t" "s_cbranch_vccz 25f\n\t" "s_cmp_eq_u32 %[bd], 0\n\t" \
    "s_cbranch_scc1 25f\n\t" "s_sub_u32 %[bd], %[bd], 1\n\t" XCNT(101) "s_branch 24b\n\t" "25:\n\t" "ds_read_b128 v[160:163], %[l0] offset:0\n\t" \
    "ds_read_b128 v[164:167], %[l0] offset:1024\n\t" "ds_read_b128 v[168:171], %[l0] offset:2048\n\t" "ds_read_b128 v[172:175], %[l0] offset:3072\n\t" \
    "ds_read_b128 v[176:179], %[l0] offset:4096\n\t" "ds_read_b128 v[180:183], %[l0] offset:5120\n\t" "ds_read_b128 v[184:187], %[l0] offset:6144\n\t" \
    "ds_read_b128 v[188:191], %[l0] offset:7168\n\t" "v_mov_b64 v[232:233], 0\n\t" "v_mov_b64 v[234:235], 0\n\t" "v_mov_b64 v[236:237], 0\n\t" "v_mov_b64 v[238:239], 0\n\t" \
    "s_barrier\n\t" "s_branch 20b\n\t" "92:\n\t" XT(80, 81) "s_barrier\n\t" XT(82, 83) "buffer_load_dwordx4 v[240:243], %[mo], %[rs], %[sb] offen sc1\n\t" \
    "v_mov_b64 v[232:233], 0\n\t" "v_mov_b64 v[234:235], 0\n\t" "v_mov_b64 v[236:237], 0\n\t" "v_mov_b64 v[238:239], 0\n\t" "v_mov_b64 v[192:193], 0\n\t" \
    "v_mov_b64 v[194:195], 0\n\t" "v_mov_b64 v[196:197], 0\n\t" "v_mov_b64 v[198:199], 0\n\t" "buffer_load_dwordx4 v[244:247], v224, %[rs], %[sb] offen sc1\n\t" \
    "ds_read_b128 v[160:163], %[l0] offset:0\n\t" "ds_read_b128 v[164:167], %[l0] offset:1024\n\t" "ds_read_b128 v[168:171], %[l0] offset:2048\n\t" \
    "ds_read_b128 v[172:175], %[l0] offset:3072\n\t" "s_nop 3\n\t" "buffer_load_dwordx4 v[248:251], %[mo], %[rs], s60 offen sc1\n\t" \
    "ds_read_b128 v[176:179], %[l0] offset:4096\n\t" "ds_read_b128 v[180:183], %[l0] offset:5120\n\t" "ds_read_b128 v[184:187], %[l0] offset:6144\n\t" \
    "ds_read_b128 v[188:191], %[l0] offset:7168\n\t" "s_nop 3\n\t" "buffer_load_dwordx4 v[252:255], v224, %[rs], s60 offen sc1\n\t" "v_mov_b64 v[200:201], 0\n\t" \
    "v_mov_b64 v[202:203], 0\n\t" "v_mov_b64 v[204:205], 0\n\t" "v_mov_b64 v[206:207], 0\n\t" "v_mov_b64 v[208:209], 0\n\t" "v_mov_b64 v[210:211], 0\n\t" \
    "v_mov_b64 v[212:213], 0\n\t" "v_mov_b64 v[214:215], 0\n\t" "v_mov_b64 v[216:217], 0\n\t" "v_mov_b64 v[218:219], 0\n\t" "v_mov_b64 v[220:221], 0\n\t" \
    "v_mov_b64 v[222:223], 0\n\t" "v_mov_b32 v227, 160512\n\t" "30:\n\t" "s_waitcnt vmcnt(3)\n\t" XT(84, 85) \
    "v_smfmac_f32_16x16x64_bf16 v[232:235], v[240:243], a[0:7], %[ix]\n\t" "ds_write_b128 %[l1], v[240:243] offset:8192\n\t" \
    "v_smfmac_f32_16x16x64_bf16 v[236:239], v[240:243], a[8:15], %[ix]\n\t" "s_waitcnt vmcnt(2)\n\t" "v_smfmac_f32_16x16x64_bf16 v[232:235], v[244:247], a[16:23], %[ix]\n\t" \
    "ds_write_b128 %[l1], v[244:247] offset:9216\n\t" "s_waitcnt lgkmcnt(8)\n\t" "v_smfmac_f32_16x16x64_bf16 v[236:239], v[244:247], v[160:167], %[ix]\n\t" \
    "s_waitcnt lgkmcnt(0)\n\t" "s_barrier\n\t" XT(86, 87) "ds_read_b128 v[16:19], %[l1] offset:12288\n\t" "ds_read_b128 v[20:23], %[l1] offset:0\n\t" \
    "ds_read_b128 v[24:27], %[l1] offset:4096\n\t" "s_waitcnt vmcnt(1)\n\t" "v_smfmac_f32_16x16x64_bf16 v[232:235], v[248:251], a[24:31], %[ix]\n\t" \
    "ds_write_b128 %[l1], v[248:251] offset:10240\n\t" "v_smfmac_f32_16x16x64_bf16 v[236:239], v[248:251], a[32:39], %[ix]\n\t" \
    "ds_read_b128 v[28:31], %[l1] offset:13312\n\t" "ds_read_b128 v[240:243], %[l1] offset:1024\n\t" "ds_read_b128 v[244:247], %[l1] offset:5120\n\t" "s_waitcnt vmcnt(0)\n\t" \
    "v_smfmac_f32_16x16x64_bf16 v[232:235], v[252:255], a[40:47], %[ix]\n\t" "ds_write_b128 %[l1], v[252:255] offset:11264\n\t" \
    "v_smfmac_f32_16x16x64_bf16 v[236:239], v[252:255], v[168:175], %[ix]\n\t" "ds_read_b128 v[160:163], %[l0] offset:8192\n\t" \
    "ds_read_b128 v[164:167], %[l0] offset:9216\n\t" "ds_read_b128 v[168:171], %[l0] offset:10240\n\t" "ds_read_b128 v[172:175], %[l0] offset:11264\n\t" "s_nop 1\n\t" \
    "v_cmp_u_f32 vcc, v232, v233\n\t" "v_cmp_u_f32 s[62:63], v234, v235\n\t" "s_waitcnt lgkmcnt(11)\n\t" \
    "v_smfmac_f32_16x16x64_bf16 v[232:235], v[16:19], a[48:55], %[ix]\n\t" "v_smfmac_f32_16x16x64_bf16 v[236:239], v[16:19], a[56:63], %[ix]\n\t" \
    "s_or_b64 vcc, vcc, s[62:63]\n\t" "s_cbranch_scc1 31f\n\t" "32:\n\t" "s_waitcnt lgkmcnt(10)\n\t" "v_smfmac_f32_16x16x64_bf16 v[232:235], v[20:23], a[64:71], %[ix]\n\t" \
    "v_smfmac_f32_16x16x64_bf16 v[236:239], v[20:23], v[176:183], %[ix]\n\t" "s_waitcnt lgkmcnt(4)\n\t" "s_barrier\n\t" XT(88, 89) "ds_read_b32 v225, v227\n\t" \
    "v_smfmac_f32_16x16x64_bf16 v[232:235], v[24:27], a[72:79], %[ix]\n\t" "ds_read_b128 v[176:179], %[l0] offset:12288\n\t" "ds_read_b128 v[180:183], %[l0] offset:13312\n\t" \
    "v_smfmac_f32_16x16x64_bf16 v[236:239], v[24:27], a[80:87], %[ix]\n\t" "ds_read_b128 v[248:251], %[l1] offset:14336\n\t" "ds_read_b128 v[252:255], %[l1] offset:2048\n\t" \
    "v_smfmac_f32_16x16x64_bf16 v[232:235], v[28:31], a[88:95], %[ix]\n\t" "ds_read_b128 v[16:19], %[l1] offset:6144\n\t" "ds_read_b128 v[20:23], %[l1] offset:15360\n\t" \
    "v_smfmac_f32_16x16x64_bf16 v[236:239], v[28:31], v[184:191], %[ix]\n\t" "ds_read_b128 v[24:27], %[l1] offset:3072\n\t" "ds_read_b128 v[184:187], %[l0] offset:14336\n\t" \
    "v_smfmac_f32_16x16x64_bf16 v[232:235], v[240:243], a[96:103], %[ix]\n\t" "ds_read_b128 v[188:191], %[l0] offset:15360\n\t" "ds_read_b128 v[28:31], %[l1] offset:7168\n\t" \
    "v_smfmac_f32_16x16x64_bf16 v[236:239], v[240:243], a[104:111], %[ix]\n\t" "s_waitcnt lgkmcnt(10)\n\t" "v_cmp_eq_u32 vcc, %[rt], v225\n\t" \
    "v_smfmac_f32_16x16x64_bf16 v[232:235], v[244:247], a[112:119], %[ix]\n\t" "s_cbranch_vccnz 33f\n\t" \
    "v_smfmac_f32_16x16x64_bf16 v[236:239], v[244:247], v[160:167], %[ix]\n\t" "ds_read_b128 v[160:163], %[l0] offset:16384\n\t" \
    "ds_read_b128 v[164:167], %[l0] offset:17408\n\t" "s_waitcnt lgkmcnt(9)\n\t" "v_smfmac_f32_16x16x64_bf16 v[232:235], v[248:251], a[120:127], %[ix]\n\t" \
    "v_smfmac_f32_16x16x64_bf16 v[236:239], v[248:251], a[128:135], %[ix]\n\t" "s_waitcnt lgkmcnt(8)\n\t" \
    "v_smfmac_f32_16x16x64_bf16 v[232:235], v[252:255], a[136:143], %[ix]\n\t" "v_smfmac_f32_16x16x64_bf16 v[236:239], v[252:255], v[168:175], %[ix]\n\t" \
    "ds_read_b128 v[168:171], %[l0] offset:18432\n\t" "ds_read_b128 v[172:175], %[l0] offset:19456\n\t" "s_waitcnt lgkmcnt(9)\n\t" \
    "v_smfmac_f32_16x16x64_bf16 v[232:235], v[16:19], a[144:151], %[ix]\n\t" "v_smfmac_f32_16x16x64_bf16 v[236:239], v[16:19], a[152:159], %[ix]\n\t" \
    "s_waitcnt lgkmcnt(8)\n\t" "v_smfmac_f32_16x16x64_bf16 v[232:235], v[20:23], a[160:167], %[ix]\n\t" \
    "v_smfmac_f32_16x16x64_bf16 v[236:239], v[20:23], v[176:183], %[ix]\n\t" "ds_read_b128 v[176:179], %[l0] offset:20480\n\t" \
    "ds_read_b128 v[180:183], %[l0] offset:21504\n\t" "s_waitcnt lgkmcnt(9)\n\t" "v_smfmac_f32_16x16x64_bf16 v[232:235], v[24:27], a[168:175], %[ix]\n\t" \
    "v_smfmac_f32_16x16x64_bf16 v[236:239], v[24:27], a[176:183], %[ix]\n\t" "s_waitcnt lgkmcnt(6)\n\t" \
    "v_smfmac_f32_16x16x64_bf16 v[232:235], v[28:31], a[184:191], %[ix]\n\t" "v_smfmac_f32_16x16x64_bf16 v[236:239], v[28:31], v[184:191], %[ix]\n\t" \
    "ds_read_b128 v[184:187], %[l0] offset:22528\n\t" "ds_read_b128 v[188:191], %[l0] offset:23552\n\t" XT(90, 91) "s_nop 4\n\t" "v_add_f32 v232, v232, v233\n\t" \
    "v_add_f32 v234, v234, v235\n\t" "v_add_f32 v236, v236, v237\n\t" "v_add_f32 v238, v238, v239\n\t" "v_cvt_pk_bf16_f32 v236, v232, v236\n\t" \
    "v_cvt_pk_bf16_f32 v237, v234, v238\n\t" "ds_write2_b32 %[vw], v236, v237 offset1:68\n\t" "s_waitcnt lgkmcnt(0)\n\t" "s_barrier\n\t" XT(92, 93) \
    "ds_read_b128 v[240:243], %[vr] offset:0\n\t" "ds_read_b128 v[244:247], %[vr] offset:64\n\t" "ds_read_b128 v[248:251], %[vr] offset:128\n\t" \
    "ds_read_b128 v[252:255], %[vr] offset:192\n\t" "s_mov_b32 s61, %[ub]\n\t" "s_waitcnt lgkmcnt(3)\n\t" \
    "v_smfmac_f32_16x16x64_bf16 v[192:195], v[240:243], v[32:39], %[ix]\n\t" "v_smfmac_f32_16x16x64_bf16 v[196:199], v[240:243], v[48:55], %[ix]\n\t" \
    "s_waitcnt lgkmcnt(2)\n\t" "v_smfmac_f32_16x16x64_bf16 v[192:195], v[244:247], v[40:47], %[ix]\n\t" \
    "v_smfmac_f32_16x16x64_bf16 v[196:199], v[244:247], v[56:63], %[ix]\n\t" "s_waitcnt lgkmcnt(1)\n\t" \
    "v_smfmac_f32_16x16x64_bf16 v[192:195], v[248:251], a[192:199], %[ix]\n\t" "v_smfmac_f32_16x16x64_bf16 v[196:199], v[248:251], a[200:207], %[ix]\n\t" \
    "s_waitcnt lgkmcnt(0)\n\t" "v_smfmac_f32_16x16x64_bf16 v[192:195], v[252:255], v[160:167], %[ix]\n\t" "ds_read_b128 v[160:163], %[l0] offset:24576\n\t" \
    "ds_read_b128 v[164:167], %[l0] offset:25600\n\t" "v_smfmac_f32_16x16x64_bf16 v[196:199], v[252:255], v[168:175], %[ix]\n\t" \
    "ds_read_b128 v[168:171], %[l0] offset:26624\n\t" "ds_read_b128 v[172:175], %[l0] offset:27648\n\t" \
    "v_smfmac_f32_16x16x64_bf16 v[200:203], v[240:243], v[64:71], %[ix]\n\t" "s_nop 1\n\t" "v_smfmac_f32_16x16x64_bf16 v[204:207], v[240:243], v[80:87], %[ix]\n\t" \
    "v_cvt_pk_bf16_f32 v224, v192, v194\n\t" "v_cvt_pk_bf16_f32 v225, v193, v195\n\t" "v_smfmac_f32_16x16x64_bf16 v[200:203], v[244:247], v[72:79], %[ix]\n\t" \
    "v_cvt_pk_bf16_f32 v226, v196, v198\n\t" "v_cvt_pk_bf16_f32 v227, v197, v199\n\t" "v_smfmac_f32_16x16x64_bf16 v[204:207], v[244:247], v[88:95], %[ix]\n\t" "s_nop 0\n\t" \
    "v_permlane32_swap_b32 v224, v226\n\t" "v_smfmac_f32_16x16x64_bf16 v[200:203], v[248:251], a[208:215], %[ix]\n\t" "v_permlane32_swap_b32 v225, v227\n\t" "s_nop 0\n\t" \
    "v_smfmac_f32_16x16x64_bf16 v[204:207], v[248:251], a[216:223], %[ix]\n\t" "v_permlane16_swap_b32 v224, v225\n\t" "v_permlane16_swap_b32 v226, v227\n\t" \
    "v_smfmac_f32_16x16x64_bf16 v[200:203], v[252:255], v[176:183], %[ix]\n\t" "ds_read_b128 v[176:179], %[l0] offset:28672\n\t" \
    "buffer_store_dwordx4 v[224:227], %[uo], %[rs], s61 offen sc1\n\t" "s_xor_b32 s61, s61, 0x40000\n\t" XT(94, 95) \
    "v_smfmac_f32_16x16x64_bf16 v[204:207], v[252:255], v[184:191], %[ix]\n\t" "ds_read_b128 v[180:183], %[l0] offset:29696\n\t" \
    "ds_read_b128 v[184:187], %[l0] offset:30720\n\t" "v_smfmac_f32_16x16x64_bf16 v[208:211], v[240:243], v[96:103], %[ix]\n\t" \
    "ds_read_b128 v[188:191], %[l0] offset:31744\n\t" "s_nop 0\n\t" "v_smfmac_f32_16x16x64_bf16 v[212:215], v[240:243], v[112:119], %[ix]\n\t" \
    "v_cvt_pk_bf16_f32 v228, v200, v202\n\t" "v_cvt_pk_bf16_f32 v229, v201, v203\n\t" "v_smfmac_f32_16x16x64_bf16 v[208:211], v[244:247], v[104:111], %[ix]\n\t" \
    "v_cvt_pk_bf16_f32 v230, v204, v206\n\t" "v_cvt_pk_bf16_f32 v231, v205, v207\n\t" "v_smfmac_f32_16x16x64_bf16 v[212:215], v[244:247], v[120:127], %[ix]\n\t" "s_nop 0\n\t" \
    "v_permlane32_swap_b32 v228, v230\n\t" "v_smfmac_f32_16x16x64_bf16 v[208:211], v[248:251], a[224:231], %[ix]\n\t" "v_permlane32_swap_b32 v229, v231\n\t" "s_nop 0\n\t" \
    "v_smfmac_f32_16x16x64_bf16 v[212:215], v[248:251], a[232:239], %[ix]\n\t" "v_permlane16_swap_b32 v228, v229\n\t" "v_permlane16_swap_b32 v230, v231\n\t" \
    "s_waitcnt lgkmcnt(6)\n\t" "v_smfmac_f32_16x16x64_bf16 v[208:211], v[252:255], v[160:167], %[ix]\n\t" "buffer_store_dwordx4 v[228:231], %[uo], %[rs], s61 offen sc1\n\t" \
    "s_xor_b32 s61, s61, 0xc0000\n\t" "s_waitcnt lgkmcnt(4)\n\t" "v_smfmac_f32_16x16x64_bf16 v[212:215], v[252:255], v[168:175], %[ix]\n\t" "s_nop 1\n\t" \
    "v_smfmac_f32_16x16x64_bf16 v[216:219], v[240:243], v[128:135], %[ix]\n\t" "s_nop 1\n\t" "v_smfmac_f32_16x16x64_bf16 v[220:223], v[240:243], v[144:151], %[ix]\n\t" \
    "v_cvt_pk_bf16_f32 v224, v208, v210\n\t" "v_cvt_pk_bf16_f32 v225, v209, v211\n\t" "v_smfmac_f32_16x16x64_bf16 v[216:219], v[244:247], v[136:143], %[ix]\n\t" \
    "v_cvt_pk_bf16_f32 v226, v212, v214\n\t" "v_cvt_pk_bf16_f32 v227, v213, v215\n\t" "v_smfmac_f32_16x16x64_bf16 v[220:223], v[244:247], v[152:159], %[ix]\n\t" "s_nop 0\n\t" \
    "v_permlane32_swap_b32 v224, v226\n\t" "v_smfmac_f32_16x16x64_bf16 v[216:219], v[248:251], a[240:247], %[ix]\n\t" "v_permlane32_swap_b32 v225, v227\n\t" "s_nop 0\n\t" \
    "v_smfmac_f32_16x16x64_bf16 v[220:223], v[248:251], a[248:255], %[ix]\n\t" "v_permlane16_swap_b32 v224, v225\n\t" "v_permlane16_swap_b32 v226, v227\n\t" \
    "s_waitcnt lgkmcnt(2)\n\t" "v_smfmac_f32_16x16x64_bf16 v[216:219], v[252:255], v[176:183], %[ix]\n\t" "buffer_store_dwordx4 v[224:227], %[uo], %[rs], s61 offen sc1\n\t" \
    "s_xor_b32 s61, s61, 0x40000\n\t" "s_waitcnt lgkmcnt(0)\n\t" "v_smfmac_f32_16x16x64_bf16 v[220:223], v[252:255], v[184:191], %[ix]\n\t" "s_nop 5\n\t" \
    "v_cvt_pk_bf16_f32 v228, v216, v218\n\t" "v_cvt_pk_bf16_f32 v229, v217, v219\n\t" "v_cvt_pk_bf16_f32 v230, v220, v222\n\t" "v_cvt_pk_bf16_f32 v231, v221, v223\n\t" \
    "s_nop 0\n\t" "v_permlane32_swap_b32 v228, v230\n\t" "v_permlane32_swap_b32 v229, v231\n\t" "s_nop 1\n\t" "v_permlane16_swap_b32 v228, v229\n\t" \
    "v_permlane16_swap_b32 v230, v231\n\t" "buffer_store_dwordx4 v[228:231], %[uo], %[rs], s61 offen sc1\n\t" "s_nop 1\n\t" XT(96, 97) "v_mov_b64 v[16:17], -1\n\t" \
    "v_mov_b64 v[18:19], -1\n\t" "s_barrier\n\t" "buffer_store_dwordx4 v[16:19], %[l1], %[rs], %[ur] offen offset:0 sc1\n\t" \
    "buffer_store_dwordx4 v[16:19], %[l1], %[rs], %[ur] offen offset:1024 sc1\n\t" "buffer_store_dwordx4 v[16:19], %[l1], %[rs], %[ur] offen offset:2048 sc1\n\t" \
    "buffer_store_dwordx4 v[16:19], %[l1], %[rs], %[ur] offen offset:3072 sc1\n\t" "s_branch 99f\n\t" "31:\n\t" "ds_read_b128 v[228:231], %[l1] offset:8192\n\t" \
    "s_waitcnt lgkmcnt(0)\n\t" "v_max3_u32 v225, v228, v229, v230\n\t" "v_max_u32 v225, v225, v231\n\t" "ds_read_b128 v[228:231], %[l1] offset:9216\n\t" \
    "s_waitcnt lgkmcnt(0)\n\t" "v_max3_u32 v225, v225, v228, v229\n\t" "v_max3_u32 v225, v225, v230, v231\n\t" "ds_read_b128 v[228:231], %[l1] offset:10240\n\t" \
    "s_waitcnt lgkmcnt(0)\n\t" "v_max3_u32 v225, v225, v228, v229\n\t" "v_max3_u32 v225, v225, v230, v231\n\t" "ds_read_b128 v[228:231], %[l1] offset:11264\n\t" \
    "s_waitcnt lgkmcnt(0)\n\t" "v_max3_u32 v225, v225, v228, v229\n\t" "v_max3_u32 v225, v225, v230, v231\n\t" "v_cmp_eq_u32 vcc, -1, v225\n\t" "s_cbranch_vccz 32b\n\t" \
    "s_cmp_eq_u32 %[rt], 1\n\t" "s_cbranch_scc1 32b\n\t" "v_mov_b32 v225, %[rt]\n\t" "ds_write_b32 v227, v225\n\t" "s_waitcnt lgkmcnt(0)\n\t" "s_branch 32b\n\t" "33:\n\t" \
    "s_sub_u32 %[rt], %[rt], 1\n\t" "s_waitcnt lgkmcnt(0)\n\t" XCNT(100) "34:\n\t" "buffer_load_dwordx4 v[240:243], %[mo], %[rs], %[sb] offen sc1\n\t" \
    "buffer_load_dwordx4 v[244:247], v224, %[rs], %[sb] offen sc1\n\t" "buffer_load_dwordx4 v[248:251], %[mo], %[rs], s60 offen sc1\n\t" \
    "buffer_load_dwordx4 v[252:255], v224, %[rs], s60 offen sc1\n\t" "s_waitcnt vmcnt(0)\n\t" "v_max_u32 v225, v240, v241\n\t" "v_max3_u32 v225, v225, v242, v243\n\t" \
    "v_max3_u32 v225, v225, v244, v245\n\t" "v_max3_u32 v225, v225, v246, v247\n\t" "v_max3_u32 v225, v225, v248, v249\n\t" "v_max3_u32 v225, v225, v250, v251\n\t" \
    "v_max3_u32 v225, v225, v252, v253\n\t" "v_max3_u32 v225, v225, v254, v255\n\t" "v_cmp_eq_u32 vcc, -1, v225\n\t" "s_cbranch_vccz 35f\n\t" "s_cmp_eq_u32 %[bd], 0\n\t" \
    "s_cbranch_scc1 35f\n\t" "s_sub_u32 %[bd], %[bd], 1\n\t" XCNT(101) "s_branch 34b\n\t" "35:\n\t" "ds_read_b128 v[160:163], %[l0] offset:0\n\t" \
    "ds_read_b128 v[164:167], %[l0] offset:1024\n\t" "ds_read_b128 v[168:171], %[l0] offset:2048\n\t" "ds_read_b128 v[172:175], %[l0] offset:3072\n\t" \
    "ds_read_b128 v[176:179], %[l0] offset:4096\n\t" "ds_read_b128 v[180:183], %[l0] offset:5120\n\t" "ds_read_b128 v[184:187], %[l0] offset:6144\n\t" \
    "ds_read_b128 v[188:191], %[l0] offset:7168\n\t" "v_mov_b64 v[232:233], 0\n\t" "v_mov_b64 v[234:235], 0\n\t" "v_mov_b64 v[236:237], 0\n\t" "v_mov_b64 v[238:239], 0\n\t" \
    "s_barrier\n\t" "s_branch 30b\n\t" "93:\n\t" XT(80, 81) "s_barrier\n\t" XT(82, 83) "buffer_load_dwordx4 v[240:243], %[mo], %[rs], %[sb] offen sc1\n\t" \
    "v_mov_b64 v[232:233], 0\n\t" "v_mov_b64 v[234:235], 0\n\t" "v_mov_b64 v[236:237], 0\n\t" "v_mov_b64 v[238:239], 0\n\t" "v_mov_b64 v[192:193], 0\n\t" \
    "v_mov_b64 v[194:195], 0\n\t" "v_mov_b64 v[196:197], 0\n\t" "v_mov_b64 v[198:199], 0\n\t" "buffer_load_dwordx4 v[244:247], v224, %[rs], %[sb] offen sc1\n\t" \
    "ds_read_b128 v[160:163], %[l0] offset:0\n\t" "ds_read_b128 v[164:167], %[l0] offset:1024\n\t" "ds_read_b128 v[168:171], %[l0] offset:2048\n\t" \
    "ds_read_b128 v[172:175], %[l0] offset:3072\n\t" "s_nop 3\n\t" "buffer_load_dwordx4 v[248:251], %[mo], %[rs], s60 offen sc1\n\t" \
    "ds_read_b128 v[176:179], %[l0] offset:4096\n\t" "ds_read_b128 v[180:183], %[l0] offset:5120\n\t" "ds_read_b128 v[184:187], %[l0] offset:6144\n\t" \
    "ds_read_b128 v[188:191], %[l0] offset:7168\n\t" "s_nop 3\n\t" "buffer_load_dwordx4 v[252:255], v224, %[rs], s60 offen sc1\n\t" "v_mov_b64 v[200:201], 0\n\t" \
    "v_mov_b64 v[202:203], 0\n\t" "v_mov_b64 v[204:205], 0\n\t" "v_mov_b64 v[206:207], 0\n\t" "v_mov_b64 v[208:209], 0\n\t" "v_mov_b64 v[210:211], 0\n\t" \
    "v_mov_b64 v[212:213], 0\n\t" "v_mov_b64 v[214:215], 0\n\t" "v_mov_b64 v[216:217], 0\n\t" "v_mov_b64 v[218:219], 0\n\t" "v_mov_b64 v[220:221], 0\n\t" \
    "v_mov_b64 v[222:223], 0\n\t" "v_mov_b32 v227, 160512\n\t" "40:\n\t" "s_waitcnt vmcnt(3)\n\t" XT(84, 85) \
    "v_smfmac_f32_16x16x64_bf16 v[232:235], v[240:243], a[0:7], %[ix]\n\t" "ds_write_b128 %[l1], v[240:243] offset:12288\n\t" \
    "v_smfmac_f32_16x16x64_bf16 v[236:239], v[240:243], a[8:15], %[ix]\n\t" "s_waitcnt vmcnt(2)\n\t" "v_smfmac_f32_16x16x64_bf16 v[232:235], v[244:247], a[16:23], %[ix]\n\t" \
    "ds_write_b128 %[l1], v[244:247] offset:13312\n\t" "s_waitcnt lgkmcnt(8)\n\t" "v_smfmac_f32_16x16x64_bf16 v[236:239], v[244:247], v[160:167], %[ix]\n\t" \
    "s_waitcnt lgkmcnt(0)\n\t" "s_barrier\n\t" XT(86, 87) "ds_read_b128 v[16:19], %[l1] offset:0\n\t" "ds_read_b128 v[20:23], %[l1] offset:4096\n\t" \
    "ds_read_b128 v[24:27], %[l1] offset:8192\n\t" "s_waitcnt vmcnt(1)\n\t" "v_smfmac_f32_16x16x64_bf16 v[232:235], v[248:251], a[24:31], %[ix]\n\t" \
    "ds_write_b128 %[l1], v[248:251] offset:14336\n\t" "v_smfmac_f32_16x16x64_bf16 v[236:239], v[248:251], a[32:39], %[ix]\n\t" "ds_read_b128 v[28:31], %[l1] offset:1024\n\t" \
    "ds_read_b128 v[240:243], %[l1] offset:5120\n\t" "ds_read_b128 v[244:247], %[l1] offset:9216\n\t" "s_waitcnt vmcnt(0)\n\t" \
    "v_smfmac_f32_16x16x64_bf16 v[232:235], v[252:255], a[40:47], %[ix]\n\t" "ds_write_b128 %[l1], v[252:255] offset:15360\n\t" \
    "v_smfmac_f32_16x16x64_bf16 v[236:239], v[252:255], v[168:175], %[ix]\n\t" "ds_read_b128 v[160:163], %[l0] offset:8192\n\t" \
    "ds_read_b128 v[164:167], %[l0] offset:9216\n\t" "ds_read_b128 v[168:171], %[l0] offset:10240\n\t" "ds_read_b128 v[172:175], %[l0] offset:11264\n\t" "s_nop 1\n\t" \
    "v_cmp_u_f32 vcc, v232, v233\n\t" "v_cmp_u_f32 s[62:63], v234, v235\n\t" "s_waitcnt lgkmcnt(11)\n\t" \
    "v_smfmac_f32_16x16x64_bf16 v[232:235], v[16:19], a[48:55], %[ix]\n\t" "v_smfmac_f32_16x16x64_bf16 v[236:239], v[16:19], a[56:63], %[ix]\n\t" \
    "s_or_b64 vcc, vcc, s[62:63]\n\t" "s_cbranch_scc1 41f\n\t" "42:\n\t" "s_waitcnt lgkmcnt(10)\n\t" "v_smfmac_f32_16x16x64_bf16 v[232:235], v[20:23], a[64:71], %[ix]\n\t" \
    "v_smfmac_f32_16x16x64_bf16 v[236:239], v[20:23], v[176:183], %[ix]\n\t" "s_waitcnt lgkmcnt(4)\n\t" "s_barrier\n\t" XT(88, 89) "ds_read_b32 v225, v227\n\t" \
    "v_smfmac_f32_16x16x64_bf16 v[232:235], v[24:27], a[72:79], %[ix]\n\t" "ds_read_b128 v[176:179], %[l0] offset:12288\n\t" "ds_read_b128 v[180:183], %[l0] offset:13312\n\t" \
    "v_smfmac_f32_16x16x64_bf16 v[236:239], v[24:27], a[80:87], %[ix]\n\t" "ds_read_b128 v[248:251], %[l1] offset:2048\n\t" "ds_read_b128 v[252:255], %[l1] offset:6144\n\t" \
    "v_smfmac_f32_16x16x64_bf16 v[232:235], v[28:31], a[88:95], %[ix]\n\t" "ds_read_b128 v[16:19], %[l1] offset:10240\n\t" "ds_read_b128 v[20:23], %[l1] offset:3072\n\t" \
    "v_smfmac_f32_16x16x64_bf16 v[236:239], v[28:31], v[184:191], %[ix]\n\t" "ds_read_b128 v[24:27], %[l1] offset:7168\n\t" "ds_read_b128 v[184:187], %[l0] offset:14336\n\t" \
    "v_smfmac_f32_16x16x64_bf16 v[232:235], v[240:243], a[96:103], %[ix]\n\t" "ds_read_b128 v[188:191], %[l0] offset:15360\n\t" \
    "ds_read_b128 v[28:31], %[l1] offset:11264\n\t" "v_smfmac_f32_16x16x64_bf16 v[236:239], v[240:243], a[104:111], %[ix]\n\t" "s_waitcnt lgkmcnt(10)\n\t" \
    "v_cmp_eq_u32 vcc, %[rt], v225\n\t" "v_smfmac_f32_16x16x64_bf16 v[232:235], v[244:247], a[112:119], %[ix]\n\t" "s_cbranch_vccnz 43f\n\t" \
    "v_smfmac_f32_16x16x64_bf16 v[236:239], v[244:247], v[160:167], %[ix]\n\t" "ds_read_b128 v[160:163], %[l0] offset:16384\n\t" \
    "ds_read_b128 v[164:167], %[l0] offset:17408\n\t" "s_waitcnt lgkmcnt(9)\n\t" "v_smfmac_f32_16x16x64_bf16 v[232:235], v[248:251], a[120:127], %[ix]\n\t" \
    "v_smfmac_f32_16x16x64_bf16 v[236:239], v[248:251], a[128:135], %[ix]\n\t" "s_waitcnt lgkmcnt(8)\n\t" \
    "v_smfmac_f32_16x16x64_bf16 v[232:235], v[252:255], a[136:143], %[ix]\n\t" "v_smfmac_f32_16x16x64_bf16 v[236:239], v[252:255], v[168:175], %[ix]\n\t" \
    "ds_read_b128 v[168:171], %[l0] offset:18432\n\t" "ds_read_b128 v[172:175], %[l0] offset:19456\n\t" "s_waitcnt lgkmcnt(9)\n\t" \
    "v_smfmac_f32_16x16x64_bf16 v[232:235], v[16:19], a[144:151], %[ix]\n\t" "v_smfmac_f32_16x16x64_bf16 v[236:239], v[16:19], a[152:159], %[ix]\n\t" \
    "s_waitcnt lgkmcnt(8)\n\t" "v_smfmac_f32_16x16x64_bf16 v[232:235], v[20:23], a[160:167], %[ix]\n\t" \
    "v_smfmac_f32_16x16x64_bf16 v[236:239], v[20:23], v[176:183], %[ix]\n\t" "ds_read_b128 v[176:179], %[l0] offset:20480\n\t" \
    "ds_read_b128 v[180:183], %[l0] offset:21504\n\t" "s_waitcnt lgkmcnt(9)\n\t" "v_smfmac_f32_16x16x64_bf16 v[232:235], v[24:27], a[168:175], %[ix]\n\t" \
    "v_smfmac_f32_16x16x64_bf16 v[236:239], v[24:27], a[176:183], %[ix]\n\t" "s_waitcnt lgkmcnt(6)\n\t" \
    "v_smfmac_f32_16x16x64_bf16 v[232:235], v[28:31], a[184:191], %[ix]\n\t" "v_smfmac_f32_16x16x64_bf16 v[236:239], v[28:31], v[184:191], %[ix]\n\t" \
    "ds_read_b128 v[184:187], %[l0] offset:22528\n\t" "ds_read_b128 v[188:191], %[l0] offset:23552\n\t" XT(90, 91) "s_nop 4\n\t" "v_add_f32 v232, v232, v233\n\t" \
    "v_add_f32 v234, v234, v235\n\t" "v_add_f32 v236, v236, v237\n\t" "v_add_f32 v238, v238, v239\n\t" "v_cvt_pk_bf16_f32 v236, v232, v236\n\t" \
    "v_cvt_pk_bf16_f32 v237, v234, v238\n\t" "ds_write2_b32 %[vw], v236, v237 offset1:68\n\t" "s_waitcnt lgkmcnt(0)\n\t" "s_barrier\n\t" XT(92, 93) \
    "ds_read_b128 v[240:243], %[vr] offset:0\n\t" "ds_read_b128 v[244:247], %[vr] offset:64\n\t" "ds_read_b128 v[248:251], %[vr] offset:128\n\t" \
    "ds_read_b128 v[252:255], %[vr] offset:192\n\t" "s_mov_b32 s61, %[ub]\n\t" "s_waitcnt lgkmcnt(3)\n\t" \
    "v_smfmac_f32_16x16x64_bf16 v[192:195], v[240:243], v[32:39], %[ix]\n\t" "v_smfmac_f32_16x16x64_bf16 v[196:199], v[240:243], v[48:55], %[ix]\n\t" \
    "s_waitcnt lgkmcnt(2)\n\t" "v_smfmac_f32_16x16x64_bf16 v[192:195], v[244:247], v[40:47], %[ix]\n\t" \
    "v_smfmac_f32_16x16x64_bf16 v[196:199], v[244:247], v[56:63], %[ix]\n\t" "s_waitcnt lgkmcnt(1)\n\t" \
    "v_smfmac_f32_16x16x64_bf16 v[192:195], v[248:251], a[192:199], %[ix]\n\t" "v_smfmac_f32_16x16x64_bf16 v[196:199], v[248:251], a[200:207], %[ix]\n\t" \
    "s_waitcnt lgkmcnt(0)\n\t" "v_smfmac_f32_16x16x64_bf16 v[192:195], v[252:255], v[160:167], %[ix]\n\t" "ds_read_b128 v[160:163], %[l0] offset:24576\n\t" \
    "ds_read_b128 v[164:167], %[l0] offset:25600\n\t" "v_smfmac_f32_16x16x64_bf16 v[196:199], v[252:255], v[168:175], %[ix]\n\t" \
    "ds_read_b128 v[168:171], %[l0] offset:26624\n\t" "ds_read_b128 v[172:175], %[l0] offset:27648\n\t" \
    "v_smfmac_f32_16x16x64_bf16 v[200:203], v[240:243], v[64:71], %[ix]\n\t" "s_nop 1\n\t" "v_smfmac_f32_16x16x64_bf16 v[204:207], v[240:243], v[80:87], %[ix]\n\t" \
    "v_cvt_pk_bf16_f32 v224, v192, v194\n\t" "v_cvt_pk_bf16_f32 v225, v193, v195\n\t" "v_smfmac_f32_16x16x64_bf16 v[200:203], v[244:247], v[72:79], %[ix]\n\t" \
    "v_cvt_pk_bf16_f32 v226, v196, v198\n\t" "v_cvt_pk_bf16_f32 v227, v197, v199\n\t" "v_smfmac_f32_16x16x64_bf16 v[204:207], v[244:247], v[88:95], %[ix]\n\t" "s_nop 0\n\t" \
    "v_permlane32_swap_b32 v224, v226\n\t" "v_smfmac_f32_16x16x64_bf16 v[200:203], v[248:251], a[208:215], %[ix]\n\t" "v_permlane32_swap_b32 v225, v227\n\t" "s_nop 0\n\t" \
    "v_smfmac_f32_16x16x64_bf16 v[204:207], v[248:251], a[216:223], %[ix]\n\t" "v_permlane16_swap_b32 v224, v225\n\t" "v_permlane16_swap_b32 v226, v227\n\t" \
    "v_smfmac_f32_16x16x64_bf16 v[200:203], v[252:255], v[176:183], %[ix]\n\t" "ds_read_b128 v[176:179], %[l0] offset:28672\n\t" \
    "buffer_store_dwordx4 v[224:227], %[uo], %[rs], s61 offen sc1\n\t" "s_xor_b32 s61, s61, 0x40000\n\t" XT(94, 95) \
    "v_smfmac_f32_16x16x64_bf16 v[204:207], v[252:255], v[184:191], %[ix]\n\t" "ds_read_b128 v[180:183], %[l0] offset:29696\n\t" \
    "ds_read_b128 v[184:187], %[l0] offset:30720\n\t" "v_smfmac_f32_16x16x64_bf16 v[208:211], v[240:243], v[96:103], %[ix]\n\t" \
    "ds_read_b128 v[188:191], %[l0] offset:31744\n\t" "s_nop 0\n\t" "v_smfmac_f32_16x16x64_bf16 v[212:215], v[240:243], v[112:119], %[ix]\n\t" \
    "v_cvt_pk_bf16_f32 v228, v200, v202\n\t" "v_cvt_pk_bf16_f32 v229, v201, v203\n\t" "v_smfmac_f32_16x16x64_bf16 v[208:211], v[244:247], v[104:111], %[ix]\n\t" \
    "v_cvt_pk_bf16_f32 v230, v204, v206\n\t" "v_cvt_pk_bf16_f32 v231, v205, v207\n\t" "v_smfmac_f32_16x16x64_bf16 v[212:215], v[244:247], v[120:127], %[ix]\n\t" "s_nop 0\n\t" \
    "v_permlane32_swap_b32 v228, v230\n\t" "v_smfmac_f32_16x16x64_bf16 v[208:211], v[248:251], a[224:231], %[ix]\n\t" "v_permlane32_swap_b32 v229, v231\n\t" "s_nop 0\n\t" \
    "v_smfmac_f32_16x16x64_bf16 v[212:215], v[248:251], a[232:239], %[ix]\n\t" "v_permlane16_swap_b32 v228, v229\n\t" "v_permlane16_swap_b32 v230, v231\n\t" \
    "s_waitcnt lgkmcnt(6)\n\t" "v_smfmac_f32_16x16x64_bf16 v[208:211], v[252:255], v[160:167], %[ix]\n\t" "buffer_store_dwordx4 v[228:231], %[uo], %[rs], s61 offen sc1\n\t" \
    "s_xor_b32 s61, s61, 0xc0000\n\t" "s_waitcnt lgkmcnt(4)\n\t" "v_smfmac_f32_16x16x64_bf16 v[212:215], v[252:255], v[168:175], %[ix]\n\t" "s_nop 1\n\t" \
    "v_smfmac_f32_16x16x64_bf16 v[216:219], v[240:243], v[128:135], %[ix]\n\t" "s_nop 1\n\t" "v_smfmac_f32_16x16x64_bf16 v[220:223], v[240:243], v[144:151], %[ix]\n\t" \
    "v_cvt_pk_bf16_f32 v224, v208, v210\n\t" "v_cvt_pk_bf16_f32 v225, v209, v211\n\t" "v_smfmac_f32_16x16x64_bf16 v[216:219], v[244:247], v[136:143], %[ix]\n\t" \
    "v_cvt_pk_bf16_f32 v226, v212, v214\n\t" "v_cvt_pk_bf16_f32 v227, v213, v215\n\t" "v_smfmac_f32_16x16x64_bf16 v[220:223], v[244:247], v[152:159], %[ix]\n\t" "s_nop 0\n\t" \
    "v_permlane32_swap_b32 v224, v226\n\t" "v_smfmac_f32_16x16x64_bf16 v[216:219], v[248:251], a[240:247], %[ix]\n\t" "v_permlane32_swap_b32 v225, v227\n\t" "s_nop 0\n\t" \
    "v_smfmac_f32_16x16x64_bf16 v[220:223], v[248:251], a[248:255], %[ix]\n\t" "v_permlane16_swap_b32 v224, v225\n\t" "v_permlane16_swap_b32 v226, v227\n\t" \
    "s_waitcnt lgkmcnt(2)\n\t" "v_smfmac_f32_16x16x64_bf16 v[216:219], v[252:255], v[176:183], %[ix]\n\t" "buffer_store_dwordx4 v[224:227], %[uo], %[rs], s61 offen sc1\n\t" \
    "s_xor_b32 s61, s61, 0x40000\n\t" "s_waitcnt lgkmcnt(0)\n\t" "v_smfmac_f32_16x16x64_bf16 v[220:223], v[252:255], v[184:191], %[ix]\n\t" "s_nop 5\n\t" \
    "v_cvt_pk_bf16_f32 v228, v216, v218\n\t" "v_cvt_pk_bf16_f32 v229, v217, v219\n\t" "v_cvt_pk_bf16_f32 v230, v220, v222\n\t" "v_cvt_pk_bf16_f32 v231, v221, v223\n\t" \
    "s_nop 0\n\t" "v_permlane32_swap_b32 v228, v230\n\t" "v_permlane32_swap_b32 v229, v231\n\t" "s_nop 1\n\t" "v_permlane16_swap_b32 v228, v229\n\t" \
    "v_permlane16_swap_b32 v230, v231\n\t" "buffer_store_dwordx4 v[228:231], %[uo], %[rs], s61 offen sc1\n\t" "s_nop 1\n\t" XT(96, 97) "v_mov_b64 v[16:17], -1\n\t" \
    "v_mov_b64 v[18:19], -1\n\t" "s_barrier\n\t" "buffer_store_dwordx4 v[16:19], %[l1], %[rs], %[ur] offen offset:0 sc1\n\t" \
    "buffer_store_dwordx4 v[16:19], %[l1], %[rs], %[ur] offen offset:1024 sc1\n\t" "buffer_store_dwordx4 v[16:19], %[l1], %[rs], %[ur] offen offset:2048 sc1\n\t" \
    "buffer_store_dwordx4 v[16:19], %[l1], %[rs], %[ur] offen offset:3072 sc1\n\t" "s_branch 99f\n\t" "41:\n\t" "ds_read_b128 v[228:231], %[l1] offset:12288\n\t" \
    "s_waitcnt lgkmcnt(0)\n\t" "v_max3_u32 v225, v228, v229, v230\n\t" "v_max_u32 v225, v225, v231\n\t" "ds_read_b128 v[228:231], %[l1] offset:13312\n\t" \
    "s_waitcnt lgkmcnt(0)\n\t" "v_max3_u32 v225, v225, v228, v229\n\t" "v_max3_u32 v225, v225, v230, v231\n\t" "ds_read_b128 v[228:231], %[l1] offset:14336\n\t" \
    "s_waitcnt lgkmcnt(0)\n\t" "v_max3_u32 v225, v225, v228, v229\n\t" "v_max3_u32 v225, v225, v230, v231\n\t" "ds_read_b128 v[228:231], %[l1] offset:15360\n\t" \
    "s_waitcnt lgkmcnt(0)\n\t" "v_max3_u32 v225, v225, v228, v229\n\t" "v_max3_u32 v225, v225, v230, v231\n\t" "v_cmp_eq_u32 vcc, -1, v225\n\t" "s_cbranch_vccz 42b\n\t" \
    "s_cmp_eq_u32 %[rt], 1\n\t" "s_cbranch_scc1 42b\n\t" "v_mov_b32 v225, %[rt]\n\t" "ds_write_b32 v227, v225\n\t" "s_waitcnt lgkmcnt(0)\n\t" "s_branch 42b\n\t" "43:\n\t" \
    "s_sub_u32 %[rt], %[rt], 1\n\t" "s_waitcnt lgkmcnt(0)\n\t" XCNT(100) "44:\n\t" "buffer_load_dwordx4 v[240:243], %[mo], %[rs], %[sb] offen sc1\n\t" \
    "buffer_load_dwordx4 v[244:247], v224, %[rs], %[sb] offen sc1\n\t" "buffer_load_dwordx4 v[248:251], %[mo], %[rs], s60 offen sc1\n\t" \
    "buffer_load_dwordx4 v[252:255], v224, %[rs], s60 offen sc1\n\t" "s_waitcnt vmcnt(0)\n\t" "v_max_u32 v225, v240, v241\n\t" "v_max3_u32 v225, v225, v242, v243\n\t" \
    "v_max3_u32 v225, v225, v244, v245\n\t" "v_max3_u32 v225, v225, v246, v247\n\t" "v_max3_u32 v225, v225, v248, v249\n\t" "v_max3_u32 v225, v225, v250, v251\n\t" \
    "v_max3_u32 v225, v225, v252, v253\n\t" "v_max3_u32 v225, v225, v254, v255\n\t" "v_cmp_eq_u32 vcc, -1, v225\n\t" "s_cbranch_vccz 45f\n\t" "s_cmp_eq_u32 %[bd], 0\n\t" \
    "s_cbranch_scc1 45f\n\t" "s_sub_u32 %[bd], %[bd], 1\n\t" XCNT(101) "s_branch 44b\n\t" "45:\n\t" "ds_read_b128 v[160:163], %[l0] offset:0\n\t" \
    "ds_read_b128 v[164:167], %[l0] offset:1024\n\t" "ds_read_b128 v[168:171], %[l0] offset:2048\n\t" "ds_read_b128 v[172:175], %[l0] offset:3072\n\t" \
    "ds_read_b128 v[176:179], %[l0] offset:4096\n\t" "ds_read_b128 v[180:183], %[l0] offset:5120\n\t" "ds_read_b128 v[184:187], %[l0] offset:6144\n\t" \
    "ds_read_b128 v[188:191], %[l0] offset:7168\n\t" "v_mov_b64 v[232:233], 0\n\t" "v_mov_b64 v[234:235], 0\n\t" "v_mov_b64 v[236:237], 0\n\t" "v_mov_b64 v[238:239], 0\n\t" \
    "s_barrier\n\t" "s_branch 40b\n\t" "99:\n\t"
#define Y_STEP \
    "v_xor_b32 v128, 0, %[u0]\n\t" "v_xor_b32 v132, 0, %[pm]\n\t" "v_xor_b32 v129, 2048, %[u0]\n\t" "v_xor_b32 v133, 1024, %[pm]\n\t" "v_xor_b32 v130, 4096, %[u0]\n\t" \
    "v_xor_b32 v134, 2048, %[pm]\n\t" "v_xor_b32 v131, 6144, %[u0]\n\t" "v_xor_b32 v135, 3072, %[pm]\n\t" "s_getpc_b64 s[62:63]\n\t" "11:\n\t" "s_memtime s[64:65]\n\t" \
    "s_waitcnt lgkmcnt(0)\n\t" "s_sub_u32 s61, %[tu], s64\n\t" "s_cmp_gt_i32 s61, 124\n\t" "s_cbranch_scc1 11b\n\t" "s_max_i32 s61, s61, 0\n\t" "s_and_b32 s61, s61, 124\n\t" \
    "s_sub_u32 s61, 12f-11b, s61\n\t" "s_add_u32 s62, s62, s61\n\t" "s_addc_u32 s63, s63, 0\n\t" "s_setpc_b64 s[62:63]\n\t" ".rept 31\n\t" "s_nop 0\n\t" ".endr\n\t" "12:\n\t" \
    XT(90, 91) "buffer_load_dwordx4 v[144:147], v128, %[rs], %[ub] offen offset:0 sc1\n\t" "buffer_load_dwordx4 v[148:151], v128, %[rs], %[ub] offen offset:1024 sc1\n\t" \
    "buffer_load_dwordx4 v[152:155], v129, %[rs], %[ub] offen offset:0 sc1\n\t" "buffer_load_dwordx4 v[156:159], v129, %[rs], %[ub] offen offset:1024 sc1\n\t" \
    "s_cmp_lg_u32 %[late], 0\n\t" "s_cbranch_scc1 14f\n\t" "s_getpc_b64 s[62:63]\n\t" "13:\n\t" "s_memtime s[64:65]\n\t" "s_waitcnt lgkmcnt(0)\n\t" \
    "s_sub_u32 s61, %[tg], s64\n\t" "s_cmp_gt_i32 s61, 124\n\t" "s_cbranch_scc1 13b\n\t" "s_max_i32 s61, s61, 0\n\t" "s_and_b32 s61, s61, 124\n\t" \
    "s_sub_u32 s61, 14f-13b, s61\n\t" "s_add_u32 s62, s62, s61\n\t" "s_addc_u32 s63, s63, 0\n\t" "s_setpc_b64 s[62:63]\n\t" ".rept 31\n\t" "s_nop 0\n\t" ".endr\n\t" "14:\n\t" \
    XT(92, 93) "20:\n\t" "buffer_load_dwordx4 v[160:163], v130, %[rs], %[ub] offen offset:0 sc1\n\t" \
    "buffer_load_dwordx4 v[164:167], v130, %[rs], %[ub] offen offset:1024 sc1\n\t" "buffer_load_dwordx4 v[192:195], %[x0], %[rs], %[pb] offen offset:0 sc1\n\t" \
    "buffer_load_dwordx4 v[196:199], %[x0], %[rs], %[pb] offen offset:1024 sc1\n\t" "buffer_load_dwordx4 v[200:203], %[x0], %[rs], %[pb] offen offset:2048 sc1\n\t" \
    "buffer_load_dwordx4 v[204:207], %[x0], %[rs], %[pb] offen offset:3072 sc1\n\t" "buffer_load_dwordx2 v[176:177], v132, %[rs], %[pb] offen offset:0 sc1\n\t" \
    "buffer_load_dwordx2 v[178:179], v132, %[rs], %[pb] offen offset:128 sc1\n\t" "v_mov_b64 v[120:121], 0\n\t" "v_mov_b64 v[122:123], 0\n\t" \
    "buffer_load_dwordx4 v[208:211], %[x1], %[rs], %[pb] offen offset:0 sc1\n\t" "buffer_load_dwordx4 v[212:215], %[x1], %[rs], %[pb] offen offset:1024 sc1\n\t" \
    "buffer_load_dwordx4 v[216:219], %[x1], %[rs], %[pb] offen offset:2048 sc1\n\t" "buffer_load_dwordx4 v[220:223], %[x1], %[rs], %[pb] offen offset:3072 sc1\n\t" \
    "buffer_load_dwordx2 v[180:181], v133, %[rs], %[pb] offen offset:0 sc1\n\t" "buffer_load_dwordx2 v[182:183], v133, %[rs], %[pb] offen offset:128 sc1\n\t" \
    "buffer_load_dwordx4 v[224:227], %[x2], %[rs], %[pb] offen offset:0 sc1\n\t" "buffer_load_dwordx4 v[228:231], %[x2], %[rs], %[pb] offen offset:1024 sc1\n\t" \
    "buffer_load_dwordx4 v[232:235], %[x2], %[rs], %[pb] offen offset:2048 sc1\n\t" "buffer_load_dwordx4 v[236:239], %[x2], %[rs], %[pb] offen offset:3072 sc1\n\t" \
    "buffer_load_dwordx2 v[184:185], v134, %[rs], %[pb] offen offset:0 sc1\n\t" "buffer_load_dwordx2 v[186:187], v134, %[rs], %[pb] offen offset:128 sc1\n\t" \
    "buffer_load_dwordx4 v[240:243], %[x3], %[rs], %[pb] offen offset:0 sc1\n\t" "buffer_load_dwordx4 v[244:247], %[x3], %[rs], %[pb] offen offset:1024 sc1\n\t" \
    "buffer_load_dwordx4 v[248:251], %[x3], %[rs], %[pb] offen offset:2048 sc1\n\t" "buffer_load_dwordx4 v[252:255], %[x3], %[rs], %[pb] offen offset:3072 sc1\n\t" \
    "buffer_load_dwordx4 v[168:171], v131, %[rs], %[ub] offen offset:0 sc1\n\t" "buffer_load_dwordx4 v[172:175], v131, %[rs], %[ub] offen offset:1024 sc1\n\t" \
    "buffer_load_dwordx2 v[188:189], v135, %[rs], %[pb] offen offset:0 sc1\n\t" "buffer_load_dwordx2 v[190:191], v135, %[rs], %[pb] offen offset:128 sc1\n\t" \
    "buffer_load_dword %[sv], %[so], %[rs], %[pb] offen sc1\n\t" "v_mov_b32 v127, -1\n\t" "s_waitcnt vmcnt(26)\n\t" XT(94, 95) \
    "v_smfmac_f32_16x16x64_bf16 v[120:123], v[192:195], a[0:7], %[ix]\n\t" "s_waitcnt vmcnt(25)\n\t" "v_smfmac_f32_16x16x64_bf16 v[120:123], v[196:199], a[8:15], %[ix]\n\t" \
    "s_waitcnt vmcnt(24)\n\t" "v_smfmac_f32_16x16x64_bf16 v[120:123], v[200:203], a[16:23], %[ix]\n\t" "s_waitcnt vmcnt(23)\n\t" \
    "v_smfmac_f32_16x16x64_bf16 v[120:123], v[204:207], a[24:31], %[ix]\n\t" "s_waitcnt vmcnt(21)\n\t" \
    "v_smfmac_f32_16x16x64_bf16 v[120:123], v[176:179], v[144:151], %[ix]\n\t" "s_waitcnt vmcnt(20)\n\t" \
    "v_smfmac_f32_16x16x64_bf16 v[120:123], v[208:211], a[32:39], %[ix]\n\t" "s_waitcnt vmcnt(19)\n\t" \
    "v_smfmac_f32_16x16x64_bf16 v[120:123], v[212:215], a[40:47], %[ix]\n\t" "s_waitcnt vmcnt(18)\n\t" \
    "v_smfmac_f32_16x16x64_bf16 v[120:123], v[216:219], a[48:55], %[ix]\n\t" "s_waitcnt vmcnt(17)\n\t" \
    "v_smfmac_f32_16x16x64_bf16 v[120:123], v[220:223], a[56:63], %[ix]\n\t" "s_waitcnt vmcnt(15)\n\t" \
    "v_smfmac_f32_16x16x64_bf16 v[120:123], v[180:183], v[152:159], %[ix]\n\t" "s_waitcnt vmcnt(14)\n\t" \
    "v_smfmac_f32_16x16x64_bf16 v[120:123], v[224:227], a[64:71], %[ix]\n\t" "s_waitcnt vmcnt(13)\n\t" \
    "v_smfmac_f32_16x16x64_bf16 v[120:123], v[228:231], a[72:79], %[ix]\n\t" "s_waitcnt vmcnt(12)\n\t" \
    "v_smfmac_f32_16x16x64_bf16 v[120:123], v[232:235], a[80:87], %[ix]\n\t" "s_waitcnt vmcnt(11)\n\t" \
    "v_smfmac_f32_16x16x64_bf16 v[120:123], v[236:239], a[88:95], %[ix]\n\t" "s_waitcnt vmcnt(9)\n\t" \
    "v_smfmac_f32_16x16x64_bf16 v[120:123], v[184:187], v[160:167], %[ix]\n\t" "s_waitcnt vmcnt(8)\n\t" \
    "v_smfmac_f32_16x16x64_bf16 v[120:123], v[240:243], a[96:103], %[ix]\n\t" "s_waitcnt vmcnt(7)\n\t" \
    "v_smfmac_f32_16x16x64_bf16 v[120:123], v[244:247], a[104:111], %[ix]\n\t" "s_waitcnt vmcnt(6)\n\t" \
    "v_smfmac_f32_16x16x64_bf16 v[120:123], v[248:251], a[112:119], %[ix]\n\t" "s_waitcnt vmcnt(5)\n\t" \
    "v_smfmac_f32_16x16x64_bf16 v[120:123], v[252:255], a[120:127], %[ix]\n\t" "s_waitcnt vmcnt(1)\n\t" \
    "v_smfmac_f32_16x16x64_bf16 v[120:123], v[188:191], v[168:175], %[ix]\n\t" XT(82, 83) "s_nop 7\n\t" "v_add_f32 v120, v120, v121\n\t" "v_add_f32 v122, v122, v123\n\t" \
    "v_cvt_pk_bf16_f32 v124, v120, v122\n\t" "v_cmp_u_f32 vcc, v120, v122\n\t" "s_cbranch_vccnz 30f\n\t" "21:\n\t" \
    "v_mov_b32_dpp v125, v124 quad_perm:[1,0,3,2] row_mask:0xf bank_mask:0xf\n\t" "v_perm_b32 v124, v125, v124, %[ps]\n\t" "v_min_u32 v124, -2, v124\n\t" \
    "buffer_store_dword v124, %[mw], %[rs], %[mb] offen sc1\n\t" XT(84, 85) "buffer_store_dword v127, %[mw], %[rs], %[mr] offen sc1\n\t" "s_memtime s[64:65]\n\t" \
    "s_barrier\n\t" XT(98, 99) "s_waitcnt lgkmcnt(0)\n\t" "s_and_b32 %[bad], s64, -2\n\t" "s_branch 40f\n\t" "30:\n\t" "s_cmp_lg_u32 %[late], 0\n\t" "s_cbranch_scc1 32f\n\t" \
    XCNT(100) "s_mov_b32 %[bad], 1\n\t" "s_waitcnt vmcnt(0)\n\t" "s_branch 40f\n\t" "32:\n\t" "v_max_u32 v126, v144, v145\n\t" "v_max3_u32 v126, v126, v146, v147\n\t" \
    "v_max3_u32 v126, v126, v148, v149\n\t" "v_max3_u32 v126, v126, v150, v151\n\t" "v_max3_u32 v126, v126, v152, v153\n\t" "v_max3_u32 v126, v126, v154, v155\n\t" \
    "v_max3_u32 v126, v126, v156, v157\n\t" "v_max3_u32 v126, v126, v158, v159\n\t" "v_max3_u32 v126, v126, v160, v161\n\t" "v_max3_u32 v126, v126, v162, v163\n\t" \
    "v_max3_u32 v126, v126, v164, v165\n\t" "v_max3_u32 v126, v126, v166, v167\n\t" "v_max3_u32 v126, v126, v168, v169\n\t" "v_max3_u32 v126, v126, v170, v171\n\t" \
    "v_max3_u32 v126, v126, v172, v173\n\t" "v_max3_u32 v126, v126, v174, v175\n\t" UCNT "v_max3_u32 v126, v126, v176, v177\n\t" "v_max3_u32 v126, v126, v178, v179\n\t" \
    "v_max3_u32 v126, v126, v180, v181\n\t" "v_max3_u32 v126, v126, v182, v183\n\t" "v_max3_u32 v126, v126, v184, v185\n\t" "v_max3_u32 v126, v126, v186, v187\n\t" \
    "v_max3_u32 v126, v126, v188, v189\n\t" "v_max3_u32 v126, v126, v190, v191\n\t" "v_max3_u32 v126, v126, v192, v193\n\t" "v_max3_u32 v126, v126, v194, v195\n\t" \
    "v_max3_u32 v126, v126, v196, v197\n\t" "v_max3_u32 v126, v126, v198, v199\n\t" "v_max3_u32 v126, v126, v200, v201\n\t" "v_max3_u32 v126, v126, v202, v203\n\t" \
    "v_max3_u32 v126, v126, v204, v205\n\t" "v_max3_u32 v126, v126, v206, v207\n\t" "v_max3_u32 v126, v126, v208, v209\n\t" "v_max3_u32 v126, v126, v210, v211\n\t" \
    "v_max3_u32 v126, v126, v212, v213\n\t" "v_max3_u32 v126, v126, v214, v215\n\t" "v_max3_u32 v126, v126, v216, v217\n\t" "v_max3_u32 v126, v126, v218, v219\n\t" \
    "v_max3_u32 v126, v126, v220, v221\n\t" "v_max3_u32 v126, v126, v222, v223\n\t" "v_max3_u32 v126, v126, v224, v225\n\t" "v_max3_u32 v126, v126, v226, v227\n\t" \
    "v_max3_u32 v126, v126, v228, v229\n\t" "v_max3_u32 v126, v126, v230, v231\n\t" "v_max3_u32 v126, v126, v232, v233\n\t" "v_max3_u32 v126, v126, v234, v235\n\t" \
    "v_max3_u32 v126, v126, v236, v237\n\t" "v_max3_u32 v126, v126, v238, v239\n\t" "v_max3_u32 v126, v126, v240, v241\n\t" "v_max3_u32 v126, v126, v242, v243\n\t" \
    "v_max3_u32 v126, v126, v244, v245\n\t" "v_max3_u32 v126, v126, v246, v247\n\t" "v_max3_u32 v126, v126, v248, v249\n\t" "v_max3_u32 v126, v126, v250, v251\n\t" \
    "v_max3_u32 v126, v126, v252, v253\n\t" "v_max3_u32 v126, v126, v254, v255\n\t" "v_cmp_eq_u32 vcc, -1, v126\n\t" "s_cbranch_vccz 21b\n\t" "s_cmp_eq_u32 %[bd], 0\n\t" \
    "s_cbranch_scc1 21b\n\t" XCNT(100) "s_mov_b32 %[bad], 1\n\t" "s_waitcnt vmcnt(0)\n\t" "40:\n\t"
#define G_LOOP \
    "v_xor_b32 v72, 0, %[xo]\n\t" "v_add_u32 v76, 0x10000, v72\n\t" "v_xor_b32 v88, 0, %[xs]\n\t" "v_add_u32 v88, 0, v88\n\t" "v_xor_b32 v73, 64, %[xo]\n\t" \
    "v_add_u32 v77, 0x10000, v73\n\t" "v_xor_b32 v89, 16, %[xs]\n\t" "v_add_u32 v89, 2048, v89\n\t" "v_xor_b32 v74, 128, %[xo]\n\t" "v_add_u32 v78, 0x10000, v74\n\t" \
    "v_xor_b32 v90, 32, %[xs]\n\t" "v_add_u32 v90, 4096, v90\n\t" "v_xor_b32 v75, 192, %[xo]\n\t" "v_add_u32 v79, 0x10000, v75\n\t" "v_xor_b32 v91, 48, %[xs]\n\t" \
    "v_add_u32 v91, 6144, v91\n\t" "v_xor_b32 v92, 64, %[xs]\n\t" "v_add_u32 v92, 8192, v92\n\t" "v_xor_b32 v93, 80, %[xs]\n\t" "v_add_u32 v93, 10240, v93\n\t" \
    "v_xor_b32 v94, 96, %[xs]\n\t" "v_add_u32 v94, 12288, v94\n\t" "v_xor_b32 v95, 112, %[xs]\n\t" "v_add_u32 v95, 14336, v95\n\t" "v_mov_b32 v96, 2.0\n\t" \
    "s_mov_b32 s60, 0\n\t" "s_mov_b32 s62, 0\n\t" "s_waitcnt vmcnt(0)\n\t" "s_barrier\n\t" "ds_read_b128 v[100:103], v72 offset:0\n\t" \
    "ds_read_b128 v[104:107], v72 offset:32768\n\t" "ds_read_b128 v[108:111], v73 offset:0\n\t" "ds_read_b128 v[112:115], v73 offset:32768\n\t" \
    "ds_read_b128 v[116:119], v74 offset:0\n\t" "10:\n\t" XT(80, 81) "s_waitcnt lgkmcnt(4)\n\t" "v_mfma_f32_16x16x32_bf16 v[32:35], a[0:3], v[100:103], 0\n\t" \
    "ds_read_b128 v[120:123], v74 offset:32768\n\t" "s_add_u32 s61, s60, 1\n\t" "s_and_b32 s61, s61, 63\n\t" "v_mfma_f32_16x16x32_bf16 v[36:39], a[4:7], v[100:103], 0\n\t" \
    "s_lshl_b32 s61, s61, 16\n\t" "s_add_u32 s63, %[lw], 65536\n\t" "s_waitcnt lgkmcnt(4)\n\t" "v_mfma_f32_16x16x32_bf16 v[40:43], a[0:3], v[104:107], 0\n\t" \
    "ds_read_b128 v[124:127], v75 offset:0\n\t" "v_mfma_f32_16x16x32_bf16 v[44:47], a[4:7], v[104:107], 0\n\t" "s_waitcnt lgkmcnt(4)\n\t" \
    "v_mfma_f32_16x16x32_bf16 v[32:35], a[8:11], v[108:111], v[32:35]\n\t" "ds_read_b128 v[128:131], v75 offset:32768\n\t" "v_cvt_pk_bf16_f32 v64, v48, v49\n\t" \
    "v_mfma_f32_16x16x32_bf16 v[36:39], a[12:15], v[108:111], v[36:39]\n\t" "v_cvt_pk_bf16_f32 v65, v50, v51\n\t" "v_cvt_pk_bf16_f32 v66, v52, v53\n\t" \
    "s_waitcnt lgkmcnt(4)\n\t" "v_mfma_f32_16x16x32_bf16 v[40:43], a[8:11], v[112:115], v[40:43]\n\t" "ds_read_b128 v[100:103], v72 offset:256\n\t" \
    "v_cvt_pk_bf16_f32 v67, v54, v55\n\t" "v_mfma_f32_16x16x32_bf16 v[44:47], a[12:15], v[112:115], v[44:47]\n\t" "v_cvt_pk_bf16_f32 v68, v56, v57\n\t" \
    "v_cvt_pk_bf16_f32 v69, v58, v59\n\t" "s_waitcnt lgkmcnt(4)\n\t" "v_mfma_f32_16x16x32_bf16 v[32:35], a[16:19], v[116:119], v[32:35]\n\t" \
    "ds_read_b128 v[104:107], v72 offset:33024\n\t" "v_cvt_pk_bf16_f32 v70, v60, v61\n\t" "v_mfma_f32_16x16x32_bf16 v[36:39], a[20:23], v[116:119], v[36:39]\n\t" \
    "v_cvt_pk_bf16_f32 v71, v62, v63\n\t" "buffer_store_dwordx4 v[64:67], v96, %[rs], s62 offen offset:0 sc1\n\t" "s_waitcnt lgkmcnt(4)\n\t" \
    "v_mfma_f32_16x16x32_bf16 v[40:43], a[16:19], v[120:123], v[40:43]\n\t" "ds_read_b128 v[108:111], v73 offset:256\n\t" \
    "v_mfma_f32_16x16x32_bf16 v[44:47], a[20:23], v[120:123], v[44:47]\n\t" "s_waitcnt lgkmcnt(4)\n\t" "v_mfma_f32_16x16x32_bf16 v[32:35], a[24:27], v[124:127], v[32:35]\n\t" \
    "ds_read_b128 v[112:115], v73 offset:33024\n\t" "v_mfma_f32_16x16x32_bf16 v[36:39], a[28:31], v[124:127], v[36:39]\n\t" \
    "buffer_store_dwordx4 v[68:71], v96, %[rs], s62 offen offset:256 sc1\n\t" "s_waitcnt lgkmcnt(4)\n\t" \
    "v_mfma_f32_16x16x32_bf16 v[40:43], a[24:27], v[128:131], v[40:43]\n\t" "ds_read_b128 v[116:119], v74 offset:256\n\t" "s_mov_b32 m0, s63\n\t" \
    "v_mfma_f32_16x16x32_bf16 v[44:47], a[28:31], v[128:131], v[44:47]\n\t" "s_add_u32 s63, s63, 0x800\n\t" "buffer_load_dwordx4 v88, %[rx], s61 offen lds\n\t" \
    "s_waitcnt lgkmcnt(4)\n\t" "v_mfma_f32_16x16x32_bf16 v[32:35], a[32:35], v[100:103], v[32:35]\n\t" "ds_read_b128 v[120:123], v74 offset:33024\n\t" \
    "v_mfma_f32_16x16x32_bf16 v[36:39], a[36:39], v[100:103], v[36:39]\n\t" "buffer_load_dwordx4 v88, %[rx], s61 offen offset:1024 lds\n\t" "s_waitcnt lgkmcnt(4)\n\t" \
    "v_mfma_f32_16x16x32_bf16 v[40:43], a[32:35], v[104:107], v[40:43]\n\t" "ds_read_b128 v[124:127], v75 offset:256\n\t" "s_mov_b32 m0, s63\n\t" \
    "v_mfma_f32_16x16x32_bf16 v[44:47], a[36:39], v[104:107], v[44:47]\n\t" "s_add_u32 s63, s63, 0x800\n\t" "buffer_load_dwordx4 v89, %[rx], s61 offen lds\n\t" \
    "s_waitcnt lgkmcnt(4)\n\t" "v_mfma_f32_16x16x32_bf16 v[32:35], a[40:43], v[108:111], v[32:35]\n\t" "ds_read_b128 v[128:131], v75 offset:33024\n\t" \
    "v_mfma_f32_16x16x32_bf16 v[36:39], a[44:47], v[108:111], v[36:39]\n\t" "buffer_load_dwordx4 v89, %[rx], s61 offen offset:1024 lds\n\t" "s_waitcnt lgkmcnt(4)\n\t" \
    "v_mfma_f32_16x16x32_bf16 v[40:43], a[40:43], v[112:115], v[40:43]\n\t" "ds_read_b128 v[100:103], v72 offset:512\n\t" "s_mov_b32 m0, s63\n\t" \
    "v_mfma_f32_16x16x32_bf16 v[44:47], a[44:47], v[112:115], v[44:47]\n\t" "s_add_u32 s63, s63, 0x800\n\t" "buffer_load_dwordx4 v90, %[rx], s61 offen lds\n\t" \
    "s_waitcnt lgkmcnt(4)\n\t" "v_mfma_f32_16x16x32_bf16 v[32:35], a[48:51], v[116:119], v[32:35]\n\t" "ds_read_b128 v[104:107], v72 offset:33280\n\t" \
    "v_mfma_f32_16x16x32_bf16 v[36:39], a[52:55], v[116:119], v[36:39]\n\t" "buffer_load_dwordx4 v90, %[rx], s61 offen offset:1024 lds\n\t" "s_waitcnt lgkmcnt(4)\n\t" \
    "v_mfma_f32_16x16x32_bf16 v[40:43], a[48:51], v[120:123], v[40:43]\n\t" "ds_read_b128 v[108:111], v73 offset:512\n\t" "s_mov_b32 m0, s63\n\t" \
    "v_mfma_f32_16x16x32_bf16 v[44:47], a[52:55], v[120:123], v[44:47]\n\t" "s_add_u32 s63, s63, 0x800\n\t" "buffer_load_dwordx4 v91, %[rx], s61 offen lds\n\t" \
    "s_waitcnt lgkmcnt(4)\n\t" "v_mfma_f32_16x16x32_bf16 v[32:35], a[56:59], v[124:127], v[32:35]\n\t" "ds_read_b128 v[112:115], v73 offset:33280\n\t" \
    "v_mfma_f32_16x16x32_bf16 v[36:39], a[60:63], v[124:127], v[36:39]\n\t" "buffer_load_dwordx4 v91, %[rx], s61 offen offset:1024 lds\n\t" "s_waitcnt lgkmcnt(4)\n\t" \
    "v_mfma_f32_16x16x32_bf16 v[40:43], a[56:59], v[128:131], v[40:43]\n\t" "ds_read_b128 v[116:119], v74 offset:512\n\t" "s_mov_b32 m0, s63\n\t" \
    "v_mfma_f32_16x16x32_bf16 v[44:47], a[60:63], v[128:131], v[44:47]\n\t" "s_add_u32 s63, s63, 0x800\n\t" "buffer_load_dwordx4 v92, %[rx], s61 offen lds\n\t" \
    "s_waitcnt lgkmcnt(4)\n\t" "v_mfma_f32_16x16x32_bf16 v[32:35], a[64:67], v[100:103], v[32:35]\n\t" "ds_read_b128 v[120:123], v74 offset:33280\n\t" \
    "v_mfma_f32_16x16x32_bf16 v[36:39], a[68:71], v[100:103], v[36:39]\n\t" "buffer_load_dwordx4 v92, %[rx], s61 offen offset:1024 lds\n\t" "s_waitcnt lgkmcnt(4)\n\t" \
    "v_mfma_f32_16x16x32_bf16 v[40:43], a[64:67], v[104:107], v[40:43]\n\t" "ds_read_b128 v[124:127], v75 offset:512\n\t" "s_mov_b32 m0, s63\n\t" \
    "v_mfma_f32_16x16x32_bf16 v[44:47], a[68:71], v[104:107], v[44:47]\n\t" "s_add_u32 s63, s63, 0x800\n\t" "buffer_load_dwordx4 v93, %[rx], s61 offen lds\n\t" \
    "s_waitcnt lgkmcnt(4)\n\t" "v_mfma_f32_16x16x32_bf16 v[32:35], a[72:75], v[108:111], v[32:35]\n\t" "ds_read_b128 v[128:131], v75 offset:33280\n\t" \
    "v_mfma_f32_16x16x32_bf16 v[36:39], a[76:79], v[108:111], v[36:39]\n\t" "buffer_load_dwordx4 v93, %[rx], s61 offen offset:1024 lds\n\t" "s_waitcnt lgkmcnt(4)\n\t" \
    "v_mfma_f32_16x16x32_bf16 v[40:43], a[72:75], v[112:115], v[40:43]\n\t" "ds_read_b128 v[100:103], v72 offset:768\n\t" "s_mov_b32 m0, s63\n\t" \
    "v_mfma_f32_16x16x32_bf16 v[44:47], a[76:79], v[112:115], v[44:47]\n\t" "s_add_u32 s63, s63, 0x800\n\t" "buffer_load_dwordx4 v94, %[rx], s61 offen lds\n\t" \
    "s_waitcnt lgkmcnt(4)\n\t" "v_mfma_f32_16x16x32_bf16 v[32:35], a[80:83], v[116:119], v[32:35]\n\t" "ds_read_b128 v[104:107], v72 offset:33536\n\t" \
    "v_mfma_f32_16x16x32_bf16 v[36:39], a[84:87], v[116:119], v[36:39]\n\t" "buffer_load_dwordx4 v94, %[rx], s61 offen offset:1024 lds\n\t" "s_waitcnt lgkmcnt(4)\n\t" \
    "v_mfma_f32_16x16x32_bf16 v[40:43], a[80:83], v[120:123], v[40:43]\n\t" "ds_read_b128 v[108:111], v73 offset:768\n\t" "s_mov_b32 m0, s63\n\t" \
    "v_mfma_f32_16x16x32_bf16 v[44:47], a[84:87], v[120:123], v[44:47]\n\t" "s_add_u32 s63, s63, 0x800\n\t" "buffer_load_dwordx4 v95, %[rx], s61 offen lds\n\t" \
    "s_waitcnt lgkmcnt(4)\n\t" "v_mfma_f32_16x16x32_bf16 v[32:35], a[88:91], v[124:127], v[32:35]\n\t" "ds_read_b128 v[112:115], v73 offset:33536\n\t" \
    "v_mfma_f32_16x16x32_bf16 v[36:39], a[92:95], v[124:127], v[36:39]\n\t" "buffer_load_dwordx4 v95, %[rx], s61 offen offset:1024 lds\n\t" "s_waitcnt lgkmcnt(4)\n\t" \
    "v_mfma_f32_16x16x32_bf16 v[40:43], a[88:91], v[128:131], v[40:43]\n\t" "ds_read_b128 v[116:119], v74 offset:768\n\t" \
    "v_mfma_f32_16x16x32_bf16 v[44:47], a[92:95], v[128:131], v[44:47]\n\t" "s_waitcnt lgkmcnt(4)\n\t" "v_mfma_f32_16x16x32_bf16 v[32:35], a[96:99], v[100:103], v[32:35]\n\t" \
    "ds_read_b128 v[120:123], v74 offset:33536\n\t" "s_lshr_b32 s64, s60, 2\n\t" "s_xor_b32 s64, s64, %[xp]\n\t" \
    "v_mfma_f32_16x16x32_bf16 v[36:39], a[100:103], v[100:103], v[36:39]\n\t" "s_and_b32 s64, s64, 1\n\t" "s_lshl_b32 s64, s64, 13\n\t" "s_waitcnt lgkmcnt(4)\n\t" \
    "v_mfma_f32_16x16x32_bf16 v[40:43], a[96:99], v[104:107], v[40:43]\n\t" "ds_read_b128 v[124:127], v75 offset:768\n\t" "s_lshl_b32 s62, s60, 19\n\t" \
    "v_mfma_f32_16x16x32_bf16 v[44:47], a[100:103], v[104:107], v[44:47]\n\t" "s_add_u32 s62, s62, s64\n\t" "s_add_u32 s62, s62, %[ob]\n\t" "s_waitcnt lgkmcnt(4)\n\t" \
    "v_mfma_f32_16x16x32_bf16 v[32:35], a[104:107], v[108:111], v[32:35]\n\t" "ds_read_b128 v[128:131], v75 offset:33536\n\t" \
    "v_mfma_f32_16x16x32_bf16 v[36:39], a[108:111], v[108:111], v[36:39]\n\t" "s_waitcnt lgkmcnt(4)\n\t" \
    "v_mfma_f32_16x16x32_bf16 v[40:43], a[104:107], v[112:115], v[40:43]\n\t" "ds_read_b128 v[100:103], v72 offset:1024\n\t" \
    "v_mfma_f32_16x16x32_bf16 v[44:47], a[108:111], v[112:115], v[44:47]\n\t" "s_waitcnt lgkmcnt(4)\n\t" \
    "v_mfma_f32_16x16x32_bf16 v[32:35], a[112:115], v[116:119], v[32:35]\n\t" "ds_read_b128 v[104:107], v72 offset:33792\n\t" \
    "v_mfma_f32_16x16x32_bf16 v[36:39], a[116:119], v[116:119], v[36:39]\n\t" "s_waitcnt lgkmcnt(4)\n\t" \
    "v_mfma_f32_16x16x32_bf16 v[40:43], a[112:115], v[120:123], v[40:43]\n\t" "ds_read_b128 v[108:111], v73 offset:1024\n\t" \
    "v_mfma_f32_16x16x32_bf16 v[44:47], a[116:119], v[120:123], v[44:47]\n\t" "s_waitcnt lgkmcnt(4)\n\t" \
    "v_mfma_f32_16x16x32_bf16 v[32:35], a[120:123], v[124:127], v[32:35]\n\t" "ds_read_b128 v[112:115], v73 offset:33792\n\t" \
    "v_mfma_f32_16x16x32_bf16 v[36:39], a[124:127], v[124:127], v[36:39]\n\t" "s_waitcnt lgkmcnt(4)\n\t" \
    "v_mfma_f32_16x16x32_bf16 v[40:43], a[120:123], v[128:131], v[40:43]\n\t" "ds_read_b128 v[116:119], v74 offset:1024\n\t" \
    "v_mfma_f32_16x16x32_bf16 v[44:47], a[124:127], v[128:131], v[44:47]\n\t" "s_waitcnt lgkmcnt(4)\n\t" \
    "v_mfma_f32_16x16x32_bf16 v[32:35], a[128:131], v[100:103], v[32:35]\n\t" "ds_read_b128 v[120:123], v74 offset:33792\n\t" \
    "v_mfma_f32_16x16x32_bf16 v[36:39], a[132:135], v[100:103], v[36:39]\n\t" "s_waitcnt lgkmcnt(4)\n\t" \
    "v_mfma_f32_16x16x32_bf16 v[40:43], a[128:131], v[104:107], v[40:43]\n\t" "ds_read_b128 v[124:127], v75 offset:1024\n\t" \
    "v_mfma_f32_16x16x32_bf16 v[44:47], a[132:135], v[104:107], v[44:47]\n\t" "s_waitcnt lgkmcnt(4)\n\t" \
    "v_mfma_f32_16x16x32_bf16 v[32:35], a[136:139], v[108:111], v[32:35]\n\t" "ds_read_b128 v[128:131], v75 offset:33792\n\t" "s_waitcnt vmcnt(16)\n\t" "s_barrier\n\t" \
    "v_mfma_f32_16x16x32_bf16 v[36:39], a[140:143], v[108:111], v[36:39]\n\t" "v_mov_b32 v98, s60\n\t" "buffer_store_dword v98, %[fo], %[rc], %[fl] offen sc1\n\t" \
    "s_waitcnt lgkmcnt(4)\n\t" "v_mfma_f32_16x16x32_bf16 v[40:43], a[136:139], v[112:115], v[40:43]\n\t" "ds_read_b128 v[100:103], v72 offset:1280\n\t" \
    "v_mfma_f32_16x16x32_bf16 v[44:47], a[140:143], v[112:115], v[44:47]\n\t" "s_waitcnt lgkmcnt(4)\n\t" \
    "v_mfma_f32_16x16x32_bf16 v[32:35], a[144:147], v[116:119], v[32:35]\n\t" "ds_read_b128 v[104:107], v72 offset:34048\n\t" \
    "v_mfma_f32_16x16x32_bf16 v[36:39], a[148:151], v[116:119], v[36:39]\n\t" "s_waitcnt lgkmcnt(4)\n\t" \
    "v_mfma_f32_16x16x32_bf16 v[40:43], a[144:147], v[120:123], v[40:43]\n\t" "ds_read_b128 v[108:111], v73 offset:1280\n\t" \
    "v_mfma_f32_16x16x32_bf16 v[44:47], a[148:151], v[120:123], v[44:47]\n\t" "s_waitcnt lgkmcnt(4)\n\t" \
    "v_mfma_f32_16x16x32_bf16 v[32:35], a[152:155], v[124:127], v[32:35]\n\t" "ds_read_b128 v[112:115], v73 offset:34048\n\t" \
    "v_mfma_f32_16x16x32_bf16 v[36:39], a[156:159], v[124:127], v[36:39]\n\t" "s_waitcnt lgkmcnt(4)\n\t" \
    "v_mfma_f32_16x16x32_bf16 v[40:43], a[152:155], v[128:131], v[40:43]\n\t" "ds_read_b128 v[116:119], v74 offset:1280\n\t" \
    "v_mfma_f32_16x16x32_bf16 v[44:47], a[156:159], v[128:131], v[44:47]\n\t" "s_waitcnt lgkmcnt(4)\n\t" \
    "v_mfma_f32_16x16x32_bf16 v[32:35], a[160:163], v[100:103], v[32:35]\n\t" "ds_read_b128 v[120:123], v74 offset:34048\n\t" \
    "v_mfma_f32_16x16x32_bf16 v[36:39], a[164:167], v[100:103], v[36:39]\n\t" "s_waitcnt lgkmcnt(4)\n\t" \
    "v_mfma_f32_16x16x32_bf16 v[40:43], a[160:163], v[104:107], v[40:43]\n\t" "ds_read_b128 v[124:127], v75 offset:1280\n\t" \
    "v_mfma_f32_16x16x32_bf16 v[44:47], a[164:167], v[104:107], v[44:47]\n\t" "s_waitcnt lgkmcnt(4)\n\t" \
    "v_mfma_f32_16x16x32_bf16 v[32:35], a[168:171], v[108:111], v[32:35]\n\t" "ds_read_b128 v[128:131], v75 offset:34048\n\t" \
    "v_mfma_f32_16x16x32_bf16 v[36:39], a[172:175], v[108:111], v[36:39]\n\t" "s_waitcnt lgkmcnt(4)\n\t" \
    "v_mfma_f32_16x16x32_bf16 v[40:43], a[168:171], v[112:115], v[40:43]\n\t" "ds_read_b128 v[100:103], v72 offset:1536\n\t" \
    "v_mfma_f32_16x16x32_bf16 v[44:47], a[172:175], v[112:115], v[44:47]\n\t" "s_waitcnt lgkmcnt(4)\n\t" \
    "v_mfma_f32_16x16x32_bf16 v[32:35], a[176:179], v[116:119], v[32:35]\n\t" "ds_read_b128 v[104:107], v72 offset:34304\n\t" \
    "v_mfma_f32_16x16x32_bf16 v[36:39], a[180:183], v[116:119], v[36:39]\n\t" "s_waitcnt lgkmcnt(4)\n\t" \
    "v_mfma_f32_16x16x32_bf16 v[40:43], a[176:179], v[120:123], v[40:43]\n\t" "ds_read_b128 v[108:111], v73 offset:1536\n\t" \
    "v_mfma_f32_16x16x32_bf16 v[44:47], a[180:183], v[120:123], v[44:47]\n\t" "s_waitcnt lgkmcnt(4)\n\t" \
    "v_mfma_f32_16x16x32_bf16 v[32:35], a[184:187], v[124:127], v[32:35]\n\t" "ds_read_b128 v[112:115], v73 offset:34304\n\t" \
    "v_mfma_f32_16x16x32_bf16 v[36:39], a[188:191], v[124:127], v[36:39]\n\t" "s_waitcnt lgkmcnt(4)\n\t" \
    "v_mfma_f32_16x16x32_bf16 v[40:43], a[184:187], v[128:131], v[40:43]\n\t" "ds_read_b128 v[116:119], v74 offset:1536\n\t" \
    "v_mfma_f32_16x16x32_bf16 v[44:47], a[188:191], v[128:131], v[44:47]\n\t" "s_waitcnt lgkmcnt(4)\n\t" \
    "v_mfma_f32_16x16x32_bf16 v[32:35], a[192:195], v[100:103], v[32:35]\n\t" "ds_read_b128 v[120:123], v74 offset:34304\n\t" \
    "v_mfma_f32_16x16x32_bf16 v[36:39], a[196:199], v[100:103], v[36:39]\n\t" "s_waitcnt lgkmcnt(4)\n\t" \
    "v_mfma_f32_16x16x32_bf16 v[40:43], a[192:195], v[104:107], v[40:43]\n\t" "ds_read_b128 v[124:127], v75 offset:1536\n\t" \
    "v_mfma_f32_16x16x32_bf16 v[44:47], a[196:199], v[104:107], v[44:47]\n\t" "s_waitcnt lgkmcnt(4)\n\t" \
    "v_mfma_f32_16x16x32_bf16 v[32:35], a[200:203], v[108:111], v[32:35]\n\t" "ds_read_b128 v[128:131], v75 offset:34304\n\t" \
    "v_mfma_f32_16x16x32_bf16 v[36:39], a[204:207], v[108:111], v[36:39]\n\t" "s_waitcnt lgkmcnt(4)\n\t" \
    "v_mfma_f32_16x16x32_bf16 v[40:43], a[200:203], v[112:115], v[40:43]\n\t" "ds_read_b128 v[100:103], v72 offset:1792\n\t" \
    "v_mfma_f32_16x16x32_bf16 v[44:47], a[204:207], v[112:115], v[44:47]\n\t" "s_waitcnt lgkmcnt(4)\n\t" \
    "v_mfma_f32_16x16x32_bf16 v[32:35], a[208:211], v[116:119], v[32:35]\n\t" "ds_read_b128 v[104:107], v72 offset:34560\n\t" \
    "v_mfma_f32_16x16x32_bf16 v[36:39], a[212:215], v[116:119], v[36:39]\n\t" "s_waitcnt lgkmcnt(4)\n\t" \
    "v_mfma_f32_16x16x32_bf16 v[40:43], a[208:211], v[120:123], v[40:43]\n\t" "ds_read_b128 v[108:111], v73 offset:1792\n\t" \
    "v_mfma_f32_16x16x32_bf16 v[44:47], a[212:215], v[120:123], v[44:47]\n\t" "s_waitcnt lgkmcnt(4)\n\t" \
    "v_mfma_f32_16x16x32_bf16 v[32:35], a[216:219], v[124:127], v[32:35]\n\t" "ds_read_b128 v[112:115], v73 offset:34560\n\t" \
    "v_mfma_f32_16x16x32_bf16 v[36:39], a[220:223], v[124:127], v[36:39]\n\t" "s_waitcnt lgkmcnt(4)\n\t" \
    "v_mfma_f32_16x16x32_bf16 v[40:43], a[216:219], v[128:131], v[40:43]\n\t" "ds_read_b128 v[116:119], v74 offset:1792\n\t" \
    "v_mfma_f32_16x16x32_bf16 v[44:47], a[220:223], v[128:131], v[44:47]\n\t" "s_waitcnt lgkmcnt(4)\n\t" \
    "v_mfma_f32_16x16x32_bf16 v[32:35], a[224:227], v[100:103], v[32:35]\n\t" "ds_read_b128 v[120:123], v74 offset:34560\n\t" \
    "v_mfma_f32_16x16x32_bf16 v[36:39], a[228:231], v[100:103], v[36:39]\n\t" "s_waitcnt lgkmcnt(4)\n\t" \
    "v_mfma_f32_16x16x32_bf16 v[40:43], a[224:227], v[104:107], v[40:43]\n\t" "ds_read_b128 v[124:127], v75 offset:1792\n\t" \
    "v_mfma_f32_16x16x32_bf16 v[44:47], a[228:231], v[104:107], v[44:47]\n\t" "s_waitcnt lgkmcnt(4)\n\t" \
    "v_mfma_f32_16x16x32_bf16 v[32:35], a[232:235], v[108:111], v[32:35]\n\t" "ds_read_b128 v[128:131], v75 offset:34560\n\t" \
    "v_mfma_f32_16x16x32_bf16 v[36:39], a[236:239], v[108:111], v[36:39]\n\t" "s_waitcnt lgkmcnt(4)\n\t" \
    "v_mfma_f32_16x16x32_bf16 v[40:43], a[232:235], v[112:115], v[40:43]\n\t" "v_mfma_f32_16x16x32_bf16 v[44:47], a[236:239], v[112:115], v[44:47]\n\t" \
    "s_waitcnt lgkmcnt(3)\n\t" "v_mfma_f32_16x16x32_bf16 v[32:35], a[240:243], v[116:119], v[32:35]\n\t" \
    "v_mfma_f32_16x16x32_bf16 v[36:39], a[244:247], v[116:119], v[36:39]\n\t" "s_waitcnt vmcnt(1) lgkmcnt(0)\n\t" "s_barrier\n\t" XT(82, 83) \
    "v_mfma_f32_16x16x32_bf16 v[40:43], a[240:243], v[120:123], v[40:43]\n\t" "ds_read_b128 v[100:103], v76 offset:0\n\t" \
    "v_mfma_f32_16x16x32_bf16 v[44:47], a[244:247], v[120:123], v[44:47]\n\t" "v_mfma_f32_16x16x32_bf16 v[32:35], a[248:251], v[124:127], v[32:35]\n\t" \
    "ds_read_b128 v[104:107], v76 offset:32768\n\t" "v_mfma_f32_16x16x32_bf16 v[36:39], a[252:255], v[124:127], v[36:39]\n\t" "ds_read_b128 v[108:111], v77 offset:0\n\t" \
    "v_mfma_f32_16x16x32_bf16 v[40:43], a[248:251], v[128:131], v[40:43]\n\t" "ds_read_b128 v[112:115], v77 offset:32768\n\t" "ds_read_b128 v[116:119], v78 offset:0\n\t" \
    "v_mfma_f32_16x16x32_bf16 v[44:47], a[252:255], v[128:131], v[44:47]\n\t" "s_add_u32 s60, s60, 1\n\t" GSUM "s_cmp_eq_u32 s60, 1\n\t" "s_cbranch_scc1 20f\n\t" "11:\n\t" \
    XT(80, 81) "s_waitcnt lgkmcnt(4)\n\t" "v_mfma_f32_16x16x32_bf16 v[48:51], a[0:3], v[100:103], 0\n\t" "ds_read_b128 v[120:123], v78 offset:32768\n\t" \
    "s_add_u32 s61, s60, 1\n\t" "s_and_b32 s61, s61, 63\n\t" "v_mfma_f32_16x16x32_bf16 v[52:55], a[4:7], v[100:103], 0\n\t" "s_lshl_b32 s61, s61, 16\n\t" \
    "s_add_u32 s63, %[lw], 0\n\t" "s_waitcnt lgkmcnt(4)\n\t" "v_mfma_f32_16x16x32_bf16 v[56:59], a[0:3], v[104:107], 0\n\t" "ds_read_b128 v[124:127], v79 offset:0\n\t" \
    "v_mfma_f32_16x16x32_bf16 v[60:63], a[4:7], v[104:107], 0\n\t" "s_waitcnt lgkmcnt(4)\n\t" "v_mfma_f32_16x16x32_bf16 v[48:51], a[8:11], v[108:111], v[48:51]\n\t" \
    "ds_read_b128 v[128:131], v79 offset:32768\n\t" "v_cvt_pk_bf16_f32 v64, v32, v33\n\t" "v_mfma_f32_16x16x32_bf16 v[52:55], a[12:15], v[108:111], v[52:55]\n\t" \
    "v_cvt_pk_bf16_f32 v65, v34, v35\n\t" "v_cvt_pk_bf16_f32 v66, v36, v37\n\t" "s_waitcnt lgkmcnt(4)\n\t" \
    "v_mfma_f32_16x16x32_bf16 v[56:59], a[8:11], v[112:115], v[56:59]\n\t" "ds_read_b128 v[100:103], v76 offset:256\n\t" "v_cvt_pk_bf16_f32 v67, v38, v39\n\t" \
    "v_mfma_f32_16x16x32_bf16 v[60:63], a[12:15], v[112:115], v[60:63]\n\t" "v_cvt_pk_bf16_f32 v68, v40, v41\n\t" "v_cvt_pk_bf16_f32 v69, v42, v43\n\t" \
    "s_waitcnt lgkmcnt(4)\n\t" "v_mfma_f32_16x16x32_bf16 v[48:51], a[16:19], v[116:119], v[48:51]\n\t" "ds_read_b128 v[104:107], v76 offset:33024\n\t" \
    "v_cvt_pk_bf16_f32 v70, v44, v45\n\t" "v_mfma_f32_16x16x32_bf16 v[52:55], a[20:23], v[116:119], v[52:55]\n\t" "v_cvt_pk_bf16_f32 v71, v46, v47\n\t" \
    "buffer_store_dwordx4 v[64:67], v96, %[rs], s62 offen offset:0 sc1\n\t" "s_waitcnt lgkmcnt(4)\n\t" "v_mfma_f32_16x16x32_bf16 v[56:59], a[16:19], v[120:123], v[56:59]\n\t" \
    "ds_read_b128 v[108:111], v77 offset:256\n\t" "v_mfma_f32_16x16x32_bf16 v[60:63], a[20:23], v[120:123], v[60:63]\n\t" "s_waitcnt lgkmcnt(4)\n\t" \
    "v_mfma_f32_16x16x32_bf16 v[48:51], a[24:27], v[124:127], v[48:51]\n\t" "ds_read_b128 v[112:115], v77 offset:33024\n\t" \
    "v_mfma_f32_16x16x32_bf16 v[52:55], a[28:31], v[124:127], v[52:55]\n\t" "buffer_store_dwordx4 v[68:71], v96, %[rs], s62 offen offset:256 sc1\n\t" \
    "s_waitcnt lgkmcnt(4)\n\t" "v_mfma_f32_16x16x32_bf16 v[56:59], a[24:27], v[128:131], v[56:59]\n\t" "ds_read_b128 v[116:119], v78 offset:256\n\t" "s_mov_b32 m0, s63\n\t" \
    "v_mfma_f32_16x16x32_bf16 v[60:63], a[28:31], v[128:131], v[60:63]\n\t" "s_add_u32 s63, s63, 0x800\n\t" "buffer_load_dwordx4 v88, %[rx], s61 offen lds\n\t" \
    "s_waitcnt lgkmcnt(4)\n\t" "v_mfma_f32_16x16x32_bf16 v[48:51], a[32:35], v[100:103], v[48:51]\n\t" "ds_read_b128 v[120:123], v78 offset:33024\n\t" \
    "v_mfma_f32_16x16x32_bf16 v[52:55], a[36:39], v[100:103], v[52:55]\n\t" "buffer_load_dwordx4 v88, %[rx], s61 offen offset:1024 lds\n\t" "s_waitcnt lgkmcnt(4)\n\t" \
    "v_mfma_f32_16x16x32_bf16 v[56:59], a[32:35], v[104:107], v[56:59]\n\t" "ds_read_b128 v[124:127], v79 offset:256\n\t" "s_mov_b32 m0, s63\n\t" \
    "v_mfma_f32_16x16x32_bf16 v[60:63], a[36:39], v[104:107], v[60:63]\n\t" "s_add_u32 s63, s63, 0x800\n\t" "buffer_load_dwordx4 v89, %[rx], s61 offen lds\n\t" \
    "s_waitcnt lgkmcnt(4)\n\t" "v_mfma_f32_16x16x32_bf16 v[48:51], a[40:43], v[108:111], v[48:51]\n\t" "ds_read_b128 v[128:131], v79 offset:33024\n\t" \
    "v_mfma_f32_16x16x32_bf16 v[52:55], a[44:47], v[108:111], v[52:55]\n\t" "buffer_load_dwordx4 v89, %[rx], s61 offen offset:1024 lds\n\t" "s_waitcnt lgkmcnt(4)\n\t" \
    "v_mfma_f32_16x16x32_bf16 v[56:59], a[40:43], v[112:115], v[56:59]\n\t" "ds_read_b128 v[100:103], v76 offset:512\n\t" "s_mov_b32 m0, s63\n\t" \
    "v_mfma_f32_16x16x32_bf16 v[60:63], a[44:47], v[112:115], v[60:63]\n\t" "s_add_u32 s63, s63, 0x800\n\t" "buffer_load_dwordx4 v90, %[rx], s61 offen lds\n\t" \
    "s_waitcnt lgkmcnt(4)\n\t" "v_mfma_f32_16x16x32_bf16 v[48:51], a[48:51], v[116:119], v[48:51]\n\t" "ds_read_b128 v[104:107], v76 offset:33280\n\t" \
    "v_mfma_f32_16x16x32_bf16 v[52:55], a[52:55], v[116:119], v[52:55]\n\t" "buffer_load_dwordx4 v90, %[rx], s61 offen offset:1024 lds\n\t" "s_waitcnt lgkmcnt(4)\n\t" \
    "v_mfma_f32_16x16x32_bf16 v[56:59], a[48:51], v[120:123], v[56:59]\n\t" "ds_read_b128 v[108:111], v77 offset:512\n\t" "s_mov_b32 m0, s63\n\t" \
    "v_mfma_f32_16x16x32_bf16 v[60:63], a[52:55], v[120:123], v[60:63]\n\t" "s_add_u32 s63, s63, 0x800\n\t" "buffer_load_dwordx4 v91, %[rx], s61 offen lds\n\t" \
    "s_waitcnt lgkmcnt(4)\n\t" "v_mfma_f32_16x16x32_bf16 v[48:51], a[56:59], v[124:127], v[48:51]\n\t" "ds_read_b128 v[112:115], v77 offset:33280\n\t" \
    "v_mfma_f32_16x16x32_bf16 v[52:55], a[60:63], v[124:127], v[52:55]\n\t" "buffer_load_dwordx4 v91, %[rx], s61 offen offset:1024 lds\n\t" "s_waitcnt lgkmcnt(4)\n\t" \
    "v_mfma_f32_16x16x32_bf16 v[56:59], a[56:59], v[128:131], v[56:59]\n\t" "ds_read_b128 v[116:119], v78 offset:512\n\t" "s_mov_b32 m0, s63\n\t" \
    "v_mfma_f32_16x16x32_bf16 v[60:63], a[60:63], v[128:131], v[60:63]\n\t" "s_add_u32 s63, s63, 0x800\n\t" "buffer_load_dwordx4 v92, %[rx], s61 offen lds\n\t" \
    "s_waitcnt lgkmcnt(4)\n\t" "v_mfma_f32_16x16x32_bf16 v[48:51], a[64:67], v[100:103], v[48:51]\n\t" "ds_read_b128 v[120:123], v78 offset:33280\n\t" \
    "v_mfma_f32_16x16x32_bf16 v[52:55], a[68:71], v[100:103], v[52:55]\n\t" "buffer_load_dwordx4 v92, %[rx], s61 offen offset:1024 lds\n\t" "s_waitcnt lgkmcnt(4)\n\t" \
    "v_mfma_f32_16x16x32_bf16 v[56:59], a[64:67], v[104:107], v[56:59]\n\t" "ds_read_b128 v[124:127], v79 offset:512\n\t" "s_mov_b32 m0, s63\n\t" \
    "v_mfma_f32_16x16x32_bf16 v[60:63], a[68:71], v[104:107], v[60:63]\n\t" "s_add_u32 s63, s63, 0x800\n\t" "buffer_load_dwordx4 v93, %[rx], s61 offen lds\n\t" \
    "s_waitcnt lgkmcnt(4)\n\t" "v_mfma_f32_16x16x32_bf16 v[48:51], a[72:75], v[108:111], v[48:51]\n\t" "ds_read_b128 v[128:131], v79 offset:33280\n\t" \
    "v_mfma_f32_16x16x32_bf16 v[52:55], a[76:79], v[108:111], v[52:55]\n\t" "buffer_load_dwordx4 v93, %[rx], s61 offen offset:1024 lds\n\t" "s_waitcnt lgkmcnt(4)\n\t" \
    "v_mfma_f32_16x16x32_bf16 v[56:59], a[72:75], v[112:115], v[56:59]\n\t" "ds_read_b128 v[100:103], v76 offset:768\n\t" "s_mov_b32 m0, s63\n\t" \
    "v_mfma_f32_16x16x32_bf16 v[60:63], a[76:79], v[112:115], v[60:63]\n\t" "s_add_u32 s63, s63, 0x800\n\t" "buffer_load_dwordx4 v94, %[rx], s61 offen lds\n\t" \
    "s_waitcnt lgkmcnt(4)\n\t" "v_mfma_f32_16x16x32_bf16 v[48:51], a[80:83], v[116:119], v[48:51]\n\t" "ds_read_b128 v[104:107], v76 offset:33536\n\t" \
    "v_mfma_f32_16x16x32_bf16 v[52:55], a[84:87], v[116:119], v[52:55]\n\t" "buffer_load_dwordx4 v94, %[rx], s61 offen offset:1024 lds\n\t" "s_waitcnt lgkmcnt(4)\n\t" \
    "v_mfma_f32_16x16x32_bf16 v[56:59], a[80:83], v[120:123], v[56:59]\n\t" "ds_read_b128 v[108:111], v77 offset:768\n\t" "s_mov_b32 m0, s63\n\t" \
    "v_mfma_f32_16x16x32_bf16 v[60:63], a[84:87], v[120:123], v[60:63]\n\t" "s_add_u32 s63, s63, 0x800\n\t" "buffer_load_dwordx4 v95, %[rx], s61 offen lds\n\t" \
    "s_waitcnt lgkmcnt(4)\n\t" "v_mfma_f32_16x16x32_bf16 v[48:51], a[88:91], v[124:127], v[48:51]\n\t" "ds_read_b128 v[112:115], v77 offset:33536\n\t" \
    "v_mfma_f32_16x16x32_bf16 v[52:55], a[92:95], v[124:127], v[52:55]\n\t" "buffer_load_dwordx4 v95, %[rx], s61 offen offset:1024 lds\n\t" "s_waitcnt lgkmcnt(4)\n\t" \
    "v_mfma_f32_16x16x32_bf16 v[56:59], a[88:91], v[128:131], v[56:59]\n\t" "ds_read_b128 v[116:119], v78 offset:768\n\t" \
    "v_mfma_f32_16x16x32_bf16 v[60:63], a[92:95], v[128:131], v[60:63]\n\t" "s_waitcnt lgkmcnt(4)\n\t" "v_mfma_f32_16x16x32_bf16 v[48:51], a[96:99], v[100:103], v[48:51]\n\t" \
    "ds_read_b128 v[120:123], v78 offset:33536\n\t" "s_lshr_b32 s64, s60, 2\n\t" "s_xor_b32 s64, s64, %[xp]\n\t" \
    "v_mfma_f32_16x16x32_bf16 v[52:55], a[100:103], v[100:103], v[52:55]\n\t" "s_and_b32 s64, s64, 1\n\t" "s_lshl_b32 s64, s64, 13\n\t" "s_waitcnt lgkmcnt(4)\n\t" \
    "v_mfma_f32_16x16x32_bf16 v[56:59], a[96:99], v[104:107], v[56:59]\n\t" "ds_read_b128 v[124:127], v79 offset:768\n\t" "s_lshl_b32 s62, s60, 19\n\t" \
    "v_mfma_f32_16x16x32_bf16 v[60:63], a[100:103], v[104:107], v[60:63]\n\t" "s_add_u32 s62, s62, s64\n\t" "s_add_u32 s62, s62, %[ob]\n\t" "s_waitcnt lgkmcnt(4)\n\t" \
    "v_mfma_f32_16x16x32_bf16 v[48:51], a[104:107], v[108:111], v[48:51]\n\t" "ds_read_b128 v[128:131], v79 offset:33536\n\t" \
    "v_mfma_f32_16x16x32_bf16 v[52:55], a[108:111], v[108:111], v[52:55]\n\t" "s_waitcnt lgkmcnt(4)\n\t" \
    "v_mfma_f32_16x16x32_bf16 v[56:59], a[104:107], v[112:115], v[56:59]\n\t" "ds_read_b128 v[100:103], v76 offset:1024\n\t" \
    "v_mfma_f32_16x16x32_bf16 v[60:63], a[108:111], v[112:115], v[60:63]\n\t" "s_waitcnt lgkmcnt(4)\n\t" \
    "v_mfma_f32_16x16x32_bf16 v[48:51], a[112:115], v[116:119], v[48:51]\n\t" "ds_read_b128 v[104:107], v76 offset:33792\n\t" \
    "v_mfma_f32_16x16x32_bf16 v[52:55], a[116:119], v[116:119], v[52:55]\n\t" "s_waitcnt lgkmcnt(4)\n\t" \
    "v_mfma_f32_16x16x32_bf16 v[56:59], a[112:115], v[120:123], v[56:59]\n\t" "ds_read_b128 v[108:111], v77 offset:1024\n\t" \
    "v_mfma_f32_16x16x32_bf16 v[60:63], a[116:119], v[120:123], v[60:63]\n\t" "s_waitcnt lgkmcnt(4)\n\t" \
    "v_mfma_f32_16x16x32_bf16 v[48:51], a[120:123], v[124:127], v[48:51]\n\t" "ds_read_b128 v[112:115], v77 offset:33792\n\t" \
    "v_mfma_f32_16x16x32_bf16 v[52:55], a[124:127], v[124:127], v[52:55]\n\t" "s_waitcnt lgkmcnt(4)\n\t" \
    "v_mfma_f32_16x16x32_bf16 v[56:59], a[120:123], v[128:131], v[56:59]\n\t" "ds_read_b128 v[116:119], v78 offset:1024\n\t" \
    "v_mfma_f32_16x16x32_bf16 v[60:63], a[124:127], v[128:131], v[60:63]\n\t" "s_waitcnt lgkmcnt(4)\n\t" \
    "v_mfma_f32_16x16x32_bf16 v[48:51], a[128:131], v[100:103], v[48:51]\n\t" "ds_read_b128 v[120:123], v78 offset:33792\n\t" \
    "v_mfma_f32_16x16x32_bf16 v[52:55], a[132:135], v[100:103], v[52:55]\n\t" "s_waitcnt lgkmcnt(4)\n\t" \
    "v_mfma_f32_16x16x32_bf16 v[56:59], a[128:131], v[104:107], v[56:59]\n\t" "ds_read_b128 v[124:127], v79 offset:1024\n\t" \
    "v_mfma_f32_16x16x32_bf16 v[60:63], a[132:135], v[104:107], v[60:63]\n\t" "s_waitcnt lgkmcnt(4)\n\t" \
    "v_mfma_f32_16x16x32_bf16 v[48:51], a[136:139], v[108:111], v[48:51]\n\t" "ds_read_b128 v[128:131], v79 offset:33792\n\t" "s_waitcnt vmcnt(16)\n\t" "s_barrier\n\t" \
    "v_mfma_f32_16x16x32_bf16 v[52:55], a[140:143], v[108:111], v[52:55]\n\t" "v_mov_b32 v98, s60\n\t" "buffer_store_dword v98, %[fo], %[rc], %[fl] offen sc1\n\t" \
    "s_waitcnt lgkmcnt(4)\n\t" "v_mfma_f32_16x16x32_bf16 v[56:59], a[136:139], v[112:115], v[56:59]\n\t" "ds_read_b128 v[100:103], v76 offset:1280\n\t" \
    "v_mfma_f32_16x16x32_bf16 v[60:63], a[140:143], v[112:115], v[60:63]\n\t" "s_waitcnt lgkmcnt(4)\n\t" \
    "v_mfma_f32_16x16x32_bf16 v[48:51], a[144:147], v[116:119], v[48:51]\n\t" "ds_read_b128 v[104:107], v76 offset:34048\n\t" \
    "v_mfma_f32_16x16x32_bf16 v[52:55], a[148:151], v[116:119], v[52:55]\n\t" "s_waitcnt lgkmcnt(4)\n\t" \
    "v_mfma_f32_16x16x32_bf16 v[56:59], a[144:147], v[120:123], v[56:59]\n\t" "ds_read_b128 v[108:111], v77 offset:1280\n\t" \
    "v_mfma_f32_16x16x32_bf16 v[60:63], a[148:151], v[120:123], v[60:63]\n\t" "s_waitcnt lgkmcnt(4)\n\t" \
    "v_mfma_f32_16x16x32_bf16 v[48:51], a[152:155], v[124:127], v[48:51]\n\t" "ds_read_b128 v[112:115], v77 offset:34048\n\t" \
    "v_mfma_f32_16x16x32_bf16 v[52:55], a[156:159], v[124:127], v[52:55]\n\t" "s_waitcnt lgkmcnt(4)\n\t" \
    "v_mfma_f32_16x16x32_bf16 v[56:59], a[152:155], v[128:131], v[56:59]\n\t" "ds_read_b128 v[116:119], v78 offset:1280\n\t" \
    "v_mfma_f32_16x16x32_bf16 v[60:63], a[156:159], v[128:131], v[60:63]\n\t" "s_waitcnt lgkmcnt(4)\n\t" \
    "v_mfma_f32_16x16x32_bf16 v[48:51], a[160:163], v[100:103], v[48:51]\n\t" "ds_read_b128 v[120:123], v78 offset:34048\n\t" \
    "v_mfma_f32_16x16x32_bf16 v[52:55], a[164:167], v[100:103], v[52:55]\n\t" "s_waitcnt lgkmcnt(4)\n\t" \
    "v_mfma_f32_16x16x32_bf16 v[56:59], a[160:163], v[104:107], v[56:59]\n\t" "ds_read_b128 v[124:127], v79 offset:1280\n\t" \
    "v_mfma_f32_16x16x32_bf16 v[60:63], a[164:167], v[104:107], v[60:63]\n\t" "s_waitcnt lgkmcnt(4)\n\t" \
    "v_mfma_f32_16x16x32_bf16 v[48:51], a[168:171], v[108:111], v[48:51]\n\t" "ds_read_b128 v[128:131], v79 offset:34048\n\t" \
    "v_mfma_f32_16x16x32_bf16 v[52:55], a[172:175], v[108:111], v[52:55]\n\t" "s_waitcnt lgkmcnt(4)\n\t" \
    "v_mfma_f32_16x16x32_bf16 v[56:59], a[168:171], v[112:115], v[56:59]\n\t" "ds_read_b128 v[100:103], v76 offset:1536\n\t" \
    "v_mfma_f32_16x16x32_bf16 v[60:63], a[172:175], v[112:115], v[60:63]\n\t" "s_waitcnt lgkmcnt(4)\n\t" \
    "v_mfma_f32_16x16x32_bf16 v[48:51], a[176:179], v[116:119], v[48:51]\n\t" "ds_read_b128 v[104:107], v76 offset:34304\n\t" \
    "v_mfma_f32_16x16x32_bf16 v[52:55], a[180:183], v[116:119], v[52:55]\n\t" "s_waitcnt lgkmcnt(4)\n\t" \
    "v_mfma_f32_16x16x32_bf16 v[56:59], a[176:179], v[120:123], v[56:59]\n\t" "ds_read_b128 v[108:111], v77 offset:1536\n\t" \
    "v_mfma_f32_16x16x32_bf16 v[60:63], a[180:183], v[120:123], v[60:63]\n\t" "s_waitcnt lgkmcnt(4)\n\t" \
    "v_mfma_f32_16x16x32_bf16 v[48:51], a[184:187], v[124:127], v[48:51]\n\t" "ds_read_b128 v[112:115], v77 offset:34304\n\t" \
    "v_mfma_f32_16x16x32_bf16 v[52:55], a[188:191], v[124:127], v[52:55]\n\t" "s_waitcnt lgkmcnt(4)\n\t" \
    "v_mfma_f32_16x16x32_bf16 v[56:59], a[184:187], v[128:131], v[56:59]\n\t" "ds_read_b128 v[116:119], v78 offset:1536\n\t" \
    "v_mfma_f32_16x16x32_bf16 v[60:63], a[188:191], v[128:131], v[60:63]\n\t" "s_waitcnt lgkmcnt(4)\n\t" \
    "v_mfma_f32_16x16x32_bf16 v[48:51], a[192:195], v[100:103], v[48:51]\n\t" "ds_read_b128 v[120:123], v78 offset:34304\n\t" \
    "v_mfma_f32_16x16x32_bf16 v[52:55], a[196:199], v[100:103], v[52:55]\n\t" "s_waitcnt lgkmcnt(4)\n\t" \
    "v_mfma_f32_16x16x32_bf16 v[56:59], a[192:195], v[104:107], v[56:59]\n\t" "ds_read_b128 v[124:127], v79 offset:1536\n\t" \
    "v_mfma_f32_16x16x32_bf16 v[60:63], a[196:199], v[104:107], v[60:63]\n\t" "s_waitcnt lgkmcnt(4)\n\t" \
    "v_mfma_f32_16x16x32_bf16 v[48:51], a[200:203], v[108:111], v[48:51]\n\t" "ds_read_b128 v[128:131], v79 offset:34304\n\t" \
    "v_mfma_f32_16x16x32_bf16 v[52:55], a[204:207], v[108:111], v[52:55]\n\t" "s_waitcnt lgkmcnt(4)\n\t" \
    "v_mfma_f32_16x16x32_bf16 v[56:59], a[200:203], v[112:115], v[56:59]\n\t" "ds_read_b128 v[100:103], v76 offset:1792\n\t" \
    "v_mfma_f32_16x16x32_bf16 v[60:63], a[204:207], v[112:115], v[60:63]\n\t" "s_waitcnt lgkmcnt(4)\n\t" \
    "v_mfma_f32_16x16x32_bf16 v[48:51], a[208:211], v[116:119], v[48:51]\n\t" "ds_read_b128 v[104:107], v76 offset:34560\n\t" \
    "v_mfma_f32_16x16x32_bf16 v[52:55], a[212:215], v[116:119], v[52:55]\n\t" "s_waitcnt lgkmcnt(4)\n\t" \
    "v_mfma_f32_16x16x32_bf16 v[56:59], a[208:211], v[120:123], v[56:59]\n\t" "ds_read_b128 v[108:111], v77 offset:1792\n\t" \
    "v_mfma_f32_16x16x32_bf16 v[60:63], a[212:215], v[120:123], v[60:63]\n\t" "s_waitcnt lgkmcnt(4)\n\t" \
    "v_mfma_f32_16x16x32_bf16 v[48:51], a[216:219], v[124:127], v[48:51]\n\t" "ds_read_b128 v[112:115], v77 offset:34560\n\t" \
    "v_mfma_f32_16x16x32_bf16 v[52:55], a[220:223], v[124:127], v[52:55]\n\t" "s_waitcnt lgkmcnt(4)\n\t" \
    "v_mfma_f32_16x16x32_bf16 v[56:59], a[216:219], v[128:131], v[56:59]\n\t" "ds_read_b128 v[116:119], v78 offset:1792\n\t" \
    "v_mfma_f32_16x16x32_bf16 v[60:63], a[220:223], v[128:131], v[60:63]\n\t" "s_waitcnt lgkmcnt(4)\n\t" \
    "v_mfma_f32_16x16x32_bf16 v[48:51], a[224:227], v[100:103], v[48:51]\n\t" "ds_read_b128 v[120:123], v78 offset:34560\n\t" \
    "v_mfma_f32_16x16x32_bf16 v[52:55], a[228:231], v[100:103], v[52:55]\n\t" "s_waitcnt lgkmcnt(4)\n\t" \
    "v_mfma_f32_16x16x32_bf16 v[56:59], a[224:227], v[104:107], v[56:59]\n\t" "ds_read_b128 v[124:127], v79 offset:1792\n\t" \
    "v_mfma_f32_16x16x32_bf16 v[60:63], a[228:231], v[104:107], v[60:63]\n\t" "s_waitcnt lgkmcnt(4)\n\t" \
    "v_mfma_f32_16x16x32_bf16 v[48:51], a[232:235], v[108:111], v[48:51]\n\t" "ds_read_b128 v[128:131], v79 offset:34560\n\t" \
    "v_mfma_f32_16x16x32_bf16 v[52:55], a[236:239], v[108:111], v[52:55]\n\t" "s_waitcnt lgkmcnt(4)\n\t" \
    "v_mfma_f32_16x16x32_bf16 v[56:59], a[232:235], v[112:115], v[56:59]\n\t" "v_mfma_f32_16x16x32_bf16 v[60:63], a[236:239], v[112:115], v[60:63]\n\t" \
    "s_waitcnt lgkmcnt(3)\n\t" "v_mfma_f32_16x16x32_bf16 v[48:51], a[240:243], v[116:119], v[48:51]\n\t" \
    "v_mfma_f32_16x16x32_bf16 v[52:55], a[244:247], v[116:119], v[52:55]\n\t" "s_waitcnt vmcnt(1) lgkmcnt(0)\n\t" "s_barrier\n\t" XT(82, 83) \
    "v_mfma_f32_16x16x32_bf16 v[56:59], a[240:243], v[120:123], v[56:59]\n\t" "ds_read_b128 v[100:103], v72 offset:0\n\t" \
    "v_mfma_f32_16x16x32_bf16 v[60:63], a[244:247], v[120:123], v[60:63]\n\t" "v_mfma_f32_16x16x32_bf16 v[48:51], a[248:251], v[124:127], v[48:51]\n\t" \
    "ds_read_b128 v[104:107], v72 offset:32768\n\t" "v_mfma_f32_16x16x32_bf16 v[52:55], a[252:255], v[124:127], v[52:55]\n\t" "ds_read_b128 v[108:111], v73 offset:0\n\t" \
    "v_mfma_f32_16x16x32_bf16 v[56:59], a[248:251], v[128:131], v[56:59]\n\t" "ds_read_b128 v[112:115], v73 offset:32768\n\t" "ds_read_b128 v[116:119], v74 offset:0\n\t" \
    "v_mfma_f32_16x16x32_bf16 v[60:63], a[252:255], v[128:131], v[60:63]\n\t" "s_add_u32 s60, s60, 1\n\t" GSUM "s_cmp_lt_u32 s60, 64\n\t" "s_cbranch_scc1 10b\n\t" \
    "s_nop 7\n\t" "v_cvt_pk_bf16_f32 v64, v48, v49\n\t" "v_cvt_pk_bf16_f32 v65, v50, v51\n\t" "v_cvt_pk_bf16_f32 v66, v52, v53\n\t" "v_cvt_pk_bf16_f32 v67, v54, v55\n\t" \
    "v_cvt_pk_bf16_f32 v68, v56, v57\n\t" "v_cvt_pk_bf16_f32 v69, v58, v59\n\t" "v_cvt_pk_bf16_f32 v70, v60, v61\n\t" "v_cvt_pk_bf16_f32 v71, v62, v63\n\t" \
    "buffer_store_dwordx4 v[64:67], v96, %[rs], s62 offen offset:0 sc1\n\t" "buffer_store_dwordx4 v[68:71], v96, %[rs], s62 offen offset:256 sc1\n\t" "s_waitcnt vmcnt(0)\n\t" \
    "s_barrier\n\t" "v_mov_b32 v98, s60\n\t" "buffer_store_dword v98, %[fo], %[rc], %[fl] offen sc1\n\t" "s_waitcnt lgkmcnt(0)\n\t" "s_branch 30f\n\t" "20:\n\t" \
    "v_mov_b32 v96, %[so]\n\t" "s_nop 7\n\t" "v_cvt_pk_bf16_f32 v64, v32, v33\n\t" "v_cvt_pk_bf16_f32 v65, v34, v35\n\t" "v_cvt_pk_bf16_f32 v66, v36, v37\n\t" \
    "v_cvt_pk_bf16_f32 v67, v38, v39\n\t" "v_cvt_pk_bf16_f32 v68, v40, v41\n\t" "v_cvt_pk_bf16_f32 v69, v42, v43\n\t" "v_cvt_pk_bf16_f32 v70, v44, v45\n\t" \
    "v_cvt_pk_bf16_f32 v71, v46, v47\n\t" "buffer_store_dwordx4 v[64:67], v96, %[rs], s62 offen offset:0 sc1\n\t" \
    "buffer_store_dwordx4 v[68:71], v96, %[rs], s62 offen offset:256 sc1\n\t" "s_waitcnt vmcnt(0)\n\t" "s_barrier\n\t" "v_mov_b32 v98, s60\n\t" \
    "buffer_store_dword v98, %[fo], %[rc], %[fl] offen sc1\n\t" "s_branch 11b\n\t" "30:\n\t"
#define ZC_LOOP \
    "s_mov_b32 s60, 0\n\t" "s_mov_b32 s61, 0\n\t" "s_mov_b32 s62, 0\n\t" "s_mov_b32 s63, 0x7f000000\n\t" "s_cmp_ge_u32 s61, %[z0]\n\t" "s_cbranch_scc1 52f\n\t" "51:\n\t" \
    "buffer_load_dword v120, %[fo], %[rc], %[fv] offen sc1\n\t" "buffer_load_dword v121, %[fo], %[rc], %[vd] offen sc1\n\t" "s_waitcnt vmcnt(0)\n\t" \
    "v_readfirstlane_b32 s61, v120\n\t" "v_readfirstlane_b32 s66, v121\n\t" "s_nop 3\n\t" "s_cmp_ge_u32 s61, %[z0]\n\t" "s_cbranch_scc1 52f\n\t" "s_bitcmp1_b32 s66, 1\n\t" \
    "s_cbranch_scc1 90f\n\t" "s_cmp_eq_u32 %[bd], 0\n\t" "s_cbranch_scc1 52f\n\t" "s_sub_u32 %[bd], %[bd], 1\n\t" "s_sleep 2\n\t" "s_branch 51b\n\t" "52:\n\t" \
    "s_lshr_b32 s66, s60, 2\n\t" "s_xor_b32 s66, s66, %[xp]\n\t" "s_and_b32 s66, s66, 1\n\t" "s_lshl_b32 s66, s66, 13\n\t" "s_lshl_b32 s64, s60, 19\n\t" \
    "s_add_u32 s64, s64, s66\n\t" "s_add_u32 s64, s64, %[vb]\n\t" "buffer_load_dwordx4 v[16:19], %[ao], %[rs], s64 offen offset:0 sc1\n\t" \
    "buffer_load_dwordx4 v[20:23], %[ao], %[rs], s64 offen offset:256 sc1\n\t" "buffer_load_dwordx4 v[24:27], %[ao], %[rs], s64 offen offset:2048 sc1\n\t" \
    "buffer_load_dwordx4 v[28:31], %[ao], %[rs], s64 offen offset:2304 sc1\n\t" "buffer_load_dwordx4 v[32:35], %[a1], %[rs], s64 offen offset:0 sc1\n\t" \
    "buffer_load_dwordx4 v[36:39], %[a1], %[rs], s64 offen offset:256 sc1\n\t" "buffer_load_dwordx4 v[40:43], %[a1], %[rs], s64 offen offset:2048 sc1\n\t" \
    "buffer_load_dwordx4 v[44:47], %[a1], %[rs], s64 offen offset:2304 sc1\n\t" "s_waitcnt vmcnt(0)\n\t" "10:\n\t" XT(80, 81) "s_add_u32 s66, s62, %[rg]\n\t" \
    "s_cmp_lt_u32 s66, s60\n\t" "s_cbranch_scc1 60f\n\t" "63:\n\t" "s_add_u32 s66, s60, 1\n\t" "s_cmp_gt_u32 s61, s66\n\t" "s_cbranch_scc0 40f\n\t" \
    "v_mfma_f32_16x16x32_bf16 v[80:83], v[16:19], a[0:3], 0\n\t" "v_mfma_f32_16x16x32_bf16 v[84:87], v[20:23], a[0:3], 0\n\t" \
    "v_mfma_f32_16x16x32_bf16 v[80:83], v[24:27], a[4:7], v[80:83]\n\t" "v_mfma_f32_16x16x32_bf16 v[84:87], v[28:31], a[4:7], v[84:87]\n\t" \
    "v_mfma_f32_16x16x32_bf16 v[80:83], v[32:35], a[8:11], v[80:83]\n\t" "v_cvt_pk_bf16_f32 v112, v96, v97\n\t" \
    "v_mfma_f32_16x16x32_bf16 v[84:87], v[36:39], a[8:11], v[84:87]\n\t" "v_cvt_pk_bf16_f32 v113, v98, v99\n\t" \
    "v_mfma_f32_16x16x32_bf16 v[80:83], v[40:43], a[12:15], v[80:83]\n\t" "v_cvt_pk_bf16_f32 v114, v100, v101\n\t" \
    "v_mfma_f32_16x16x32_bf16 v[84:87], v[44:47], a[12:15], v[84:87]\n\t" "v_cvt_pk_bf16_f32 v115, v102, v103\n\t" \
    "v_mfma_f32_16x16x32_bf16 v[88:91], v[16:19], a[16:19], 0\n\t" "v_cvt_pk_bf16_f32 v116, v104, v105\n\t" "v_mfma_f32_16x16x32_bf16 v[92:95], v[20:23], a[16:19], 0\n\t" \
    "v_cvt_pk_bf16_f32 v117, v106, v107\n\t" "v_mfma_f32_16x16x32_bf16 v[88:91], v[24:27], a[20:23], v[88:91]\n\t" \
    "buffer_store_dwordx4 v[112:115], %[z1], %[rs], s63 offen sc1\n\t" "v_mfma_f32_16x16x32_bf16 v[92:95], v[28:31], a[20:23], v[92:95]\n\t" \
    "v_mfma_f32_16x16x32_bf16 v[88:91], v[32:35], a[24:27], v[88:91]\n\t" "v_cvt_pk_bf16_f32 v118, v108, v109\n\t" \
    "v_mfma_f32_16x16x32_bf16 v[92:95], v[36:39], a[24:27], v[92:95]\n\t" "v_cvt_pk_bf16_f32 v119, v110, v111\n\t" \
    "v_mfma_f32_16x16x32_bf16 v[88:91], v[40:43], a[28:31], v[88:91]\n\t" "buffer_store_dwordx4 v[116:119], %[z2], %[rs], s63 offen sc1\n\t" "s_and_b32 s63, s60, %[rm]\n\t" \
    "v_mfma_f32_16x16x32_bf16 v[92:95], v[44:47], a[28:31], v[92:95]\n\t" "s_lshl_b32 s63, s63, 22\n\t" "s_add_u32 s63, s63, %[zb]\n\t" \
    "v_mfma_f32_16x16x32_bf16 v[96:99], v[16:19], a[32:35], 0\n\t" "s_add_u32 s65, s60, 1\n\t" "s_lshr_b32 s66, s65, 2\n\t" \
    "v_mfma_f32_16x16x32_bf16 v[100:103], v[20:23], a[32:35], 0\n\t" "s_xor_b32 s66, s66, %[xp]\n\t" "s_and_b32 s66, s66, 1\n\t" \
    "v_mfma_f32_16x16x32_bf16 v[96:99], v[24:27], a[36:39], v[96:99]\n\t" "s_lshl_b32 s66, s66, 13\n\t" "s_lshl_b32 s64, s65, 19\n\t" \
    "v_mfma_f32_16x16x32_bf16 v[100:103], v[28:31], a[36:39], v[100:103]\n\t" "s_add_u32 s64, s64, s66\n\t" "s_add_u32 s64, s64, %[vb]\n\t" \
    "v_mfma_f32_16x16x32_bf16 v[96:99], v[32:35], a[40:43], v[96:99]\n\t" "buffer_load_dwordx4 v[48:51], %[ao], %[rs], s64 offen offset:0 sc1\n\t" \
    "v_cvt_pk_bf16_f32 v112, v80, v81\n\t" "v_mfma_f32_16x16x32_bf16 v[100:103], v[36:39], a[40:43], v[100:103]\n\t" "v_cvt_pk_bf16_f32 v113, v82, v83\n\t" \
    "v_mfma_f32_16x16x32_bf16 v[96:99], v[40:43], a[44:47], v[96:99]\n\t" "buffer_load_dwordx4 v[52:55], %[ao], %[rs], s64 offen offset:256 sc1\n\t" \
    "v_cvt_pk_bf16_f32 v114, v84, v85\n\t" "v_mfma_f32_16x16x32_bf16 v[100:103], v[44:47], a[44:47], v[100:103]\n\t" "v_cvt_pk_bf16_f32 v115, v86, v87\n\t" \
    "v_mfma_f32_16x16x32_bf16 v[104:107], v[16:19], a[48:51], 0\n\t" "buffer_load_dwordx4 v[56:59], %[ao], %[rs], s64 offen offset:2048 sc1\n\t" \
    "v_cvt_pk_bf16_f32 v116, v88, v89\n\t" "v_mfma_f32_16x16x32_bf16 v[108:111], v[20:23], a[48:51], 0\n\t" "v_cvt_pk_bf16_f32 v117, v90, v91\n\t" \
    "v_mfma_f32_16x16x32_bf16 v[104:107], v[24:27], a[52:55], v[104:107]\n\t" "buffer_load_dwordx4 v[60:63], %[ao], %[rs], s64 offen offset:2304 sc1\n\t" \
    "v_mfma_f32_16x16x32_bf16 v[108:111], v[28:31], a[52:55], v[108:111]\n\t" "buffer_store_dwordx4 v[112:115], %[z1], %[rs], s63 offen sc1\n\t" \
    "v_mfma_f32_16x16x32_bf16 v[104:107], v[32:35], a[56:59], v[104:107]\n\t" "buffer_load_dwordx4 v[64:67], %[a1], %[rs], s64 offen offset:0 sc1\n\t" \
    "v_cvt_pk_bf16_f32 v118, v92, v93\n\t" "v_mfma_f32_16x16x32_bf16 v[108:111], v[36:39], a[56:59], v[108:111]\n\t" "v_cvt_pk_bf16_f32 v119, v94, v95\n\t" \
    "v_mfma_f32_16x16x32_bf16 v[104:107], v[40:43], a[60:63], v[104:107]\n\t" "buffer_load_dwordx4 v[68:71], %[a1], %[rs], s64 offen offset:256 sc1\n\t" \
    "buffer_store_dwordx4 v[116:119], %[z2], %[rs], s63 offen sc1\n\t" "v_mfma_f32_16x16x32_bf16 v[108:111], v[44:47], a[60:63], v[108:111]\n\t" \
    "s_add_u32 s63, s63, 0x20000\n\t" "v_mfma_f32_16x16x32_bf16 v[80:83], v[16:19], a[64:67], 0\n\t" \
    "buffer_load_dwordx4 v[72:75], %[a1], %[rs], s64 offen offset:2048 sc1\n\t" "v_mfma_f32_16x16x32_bf16 v[84:87], v[20:23], a[64:67], 0\n\t" \
    "v_mfma_f32_16x16x32_bf16 v[80:83], v[24:27], a[68:71], v[80:83]\n\t" "buffer_load_dwordx4 v[76:79], %[a1], %[rs], s64 offen offset:2304 sc1\n\t" \
    "v_mfma_f32_16x16x32_bf16 v[84:87], v[28:31], a[68:71], v[84:87]\n\t" "v_mfma_f32_16x16x32_bf16 v[80:83], v[32:35], a[72:75], v[80:83]\n\t" \
    "buffer_load_dword v120, %[fo], %[rc], %[fv] offen sc1\n\t" "v_cvt_pk_bf16_f32 v112, v96, v97\n\t" "v_mfma_f32_16x16x32_bf16 v[84:87], v[36:39], a[72:75], v[84:87]\n\t" \
    "v_cvt_pk_bf16_f32 v113, v98, v99\n\t" "v_mfma_f32_16x16x32_bf16 v[80:83], v[40:43], a[76:79], v[80:83]\n\t" "buffer_load_dword v121, %[fo], %[rc], %[fs] offen sc1\n\t" \
    "v_mfma_f32_16x16x32_bf16 v[84:87], v[44:47], a[76:79], v[84:87]\n\t" "v_cvt_pk_bf16_f32 v114, v100, v101\n\t" "v_cvt_pk_bf16_f32 v115, v102, v103\n\t" \
    "v_mfma_f32_16x16x32_bf16 v[88:91], v[16:19], a[80:83], 0\n\t" "v_cvt_pk_bf16_f32 v116, v104, v105\n\t" "v_mfma_f32_16x16x32_bf16 v[92:95], v[20:23], a[80:83], 0\n\t" \
    "v_cvt_pk_bf16_f32 v117, v106, v107\n\t" "v_mfma_f32_16x16x32_bf16 v[88:91], v[24:27], a[84:87], v[88:91]\n\t" \
    "buffer_store_dwordx4 v[112:115], %[z1], %[rs], s63 offen sc1\n\t" "v_mfma_f32_16x16x32_bf16 v[92:95], v[28:31], a[84:87], v[92:95]\n\t" \
    "v_mfma_f32_16x16x32_bf16 v[88:91], v[32:35], a[88:91], v[88:91]\n\t" "v_cvt_pk_bf16_f32 v118, v108, v109\n\t" \
    "v_mfma_f32_16x16x32_bf16 v[92:95], v[36:39], a[88:91], v[92:95]\n\t" "v_cvt_pk_bf16_f32 v119, v110, v111\n\t" \
    "v_mfma_f32_16x16x32_bf16 v[88:91], v[40:43], a[92:95], v[88:91]\n\t" "buffer_store_dwordx4 v[116:119], %[z2], %[rs], s63 offen sc1\n\t" \
    "v_mfma_f32_16x16x32_bf16 v[92:95], v[44:47], a[92:95], v[92:95]\n\t" "s_add_u32 s63, s63, 0x20000\n\t" "v_mfma_f32_16x16x32_bf16 v[96:99], v[16:19], a[96:99], 0\n\t" \
    "v_mfma_f32_16x16x32_bf16 v[100:103], v[20:23], a[96:99], 0\n\t" "v_mfma_f32_16x16x32_bf16 v[96:99], v[24:27], a[100:103], v[96:99]\n\t" \
    "v_mfma_f32_16x16x32_bf16 v[100:103], v[28:31], a[100:103], v[100:103]\n\t" "v_mfma_f32_16x16x32_bf16 v[96:99], v[32:35], a[104:107], v[96:99]\n\t" \
    "v_cvt_pk_bf16_f32 v112, v80, v81\n\t" "v_mfma_f32_16x16x32_bf16 v[100:103], v[36:39], a[104:107], v[100:103]\n\t" "v_cvt_pk_bf16_f32 v113, v82, v83\n\t" \
    "v_mfma_f32_16x16x32_bf16 v[96:99], v[40:43], a[108:111], v[96:99]\n\t" "v_cvt_pk_bf16_f32 v114, v84, v85\n\t" \
    "v_mfma_f32_16x16x32_bf16 v[100:103], v[44:47], a[108:111], v[100:103]\n\t" "v_cvt_pk_bf16_f32 v115, v86, v87\n\t" \
    "v_mfma_f32_16x16x32_bf16 v[104:107], v[16:19], a[112:115], 0\n\t" "v_cvt_pk_bf16_f32 v116, v88, v89\n\t" \
    "v_mfma_f32_16x16x32_bf16 v[108:111], v[20:23], a[112:115], 0\n\t" "v_cvt_pk_bf16_f32 v117, v90, v91\n\t" \
    "v_mfma_f32_16x16x32_bf16 v[104:107], v[24:27], a[116:119], v[104:107]\n\t" "buffer_store_dwordx4 v[112:115], %[z1], %[rs], s63 offen sc1\n\t" \
    "v_mfma_f32_16x16x32_bf16 v[108:111], v[28:31], a[116:119], v[108:111]\n\t" "v_mfma_f32_16x16x32_bf16 v[104:107], v[32:35], a[120:123], v[104:107]\n\t" \
    "v_cvt_pk_bf16_f32 v118, v92, v93\n\t" "v_mfma_f32_16x16x32_bf16 v[108:111], v[36:39], a[120:123], v[108:111]\n\t" "v_cvt_pk_bf16_f32 v119, v94, v95\n\t" \
    "v_mfma_f32_16x16x32_bf16 v[104:107], v[40:43], a[124:127], v[104:107]\n\t" "buffer_store_dwordx4 v[116:119], %[z2], %[rs], s63 offen sc1\n\t" \
    "v_mfma_f32_16x16x32_bf16 v[108:111], v[44:47], a[124:127], v[108:111]\n\t" "s_add_u32 s63, s63, 0x20000\n\t" \
    "v_mfma_f32_16x16x32_bf16 v[80:83], v[16:19], a[128:131], 0\n\t" "v_mfma_f32_16x16x32_bf16 v[84:87], v[20:23], a[128:131], 0\n\t" \
    "v_mfma_f32_16x16x32_bf16 v[80:83], v[24:27], a[132:135], v[80:83]\n\t" "v_mfma_f32_16x16x32_bf16 v[84:87], v[28:31], a[132:135], v[84:87]\n\t" \
    "v_mfma_f32_16x16x32_bf16 v[80:83], v[32:35], a[136:139], v[80:83]\n\t" "v_cvt_pk_bf16_f32 v112, v96, v97\n\t" \
    "v_mfma_f32_16x16x32_bf16 v[84:87], v[36:39], a[136:139], v[84:87]\n\t" "v_cvt_pk_bf16_f32 v113, v98, v99\n\t" \
    "v_mfma_f32_16x16x32_bf16 v[80:83], v[40:43], a[140:143], v[80:83]\n\t" "v_cvt_pk_bf16_f32 v114, v100, v101\n\t" \
    "v_mfma_f32_16x16x32_bf16 v[84:87], v[44:47], a[140:143], v[84:87]\n\t" "v_cvt_pk_bf16_f32 v115, v102, v103\n\t" \
    "v_mfma_f32_16x16x32_bf16 v[88:91], v[16:19], a[144:147], 0\n\t" "v_cvt_pk_bf16_f32 v116, v104, v105\n\t" "v_mfma_f32_16x16x32_bf16 v[92:95], v[20:23], a[144:147], 0\n\t" \
    "v_cvt_pk_bf16_f32 v117, v106, v107\n\t" "v_mfma_f32_16x16x32_bf16 v[88:91], v[24:27], a[148:151], v[88:91]\n\t" \
    "buffer_store_dwordx4 v[112:115], %[z1], %[rs], s63 offen sc1\n\t" "v_mfma_f32_16x16x32_bf16 v[92:95], v[28:31], a[148:151], v[92:95]\n\t" \
    "v_mfma_f32_16x16x32_bf16 v[88:91], v[32:35], a[152:155], v[88:91]\n\t" "v_cvt_pk_bf16_f32 v118, v108, v109\n\t" \
    "v_mfma_f32_16x16x32_bf16 v[92:95], v[36:39], a[152:155], v[92:95]\n\t" "v_cvt_pk_bf16_f32 v119, v110, v111\n\t" \
    "v_mfma_f32_16x16x32_bf16 v[88:91], v[40:43], a[156:159], v[88:91]\n\t" "buffer_store_dwordx4 v[116:119], %[z2], %[rs], s63 offen sc1\n\t" \
    "v_mfma_f32_16x16x32_bf16 v[92:95], v[44:47], a[156:159], v[92:95]\n\t" "s_add_u32 s63, s63, 0x20000\n\t" "v_mfma_f32_16x16x32_bf16 v[96:99], v[16:19], a[160:163], 0\n\t" \
    "v_mfma_f32_16x16x32_bf16 v[100:103], v[20:23], a[160:163], 0\n\t" "v_mfma_f32_16x16x32_bf16 v[96:99], v[24:27], a[164:167], v[96:99]\n\t" \
    "v_mfma_f32_16x16x32_bf16 v[100:103], v[28:31], a[164:167], v[100:103]\n\t" "v_mfma_f32_16x16x32_bf16 v[96:99], v[32:35], a[168:171], v[96:99]\n\t" \
    "v_cvt_pk_bf16_f32 v112, v80, v81\n\t" "v_mfma_f32_16x16x32_bf16 v[100:103], v[36:39], a[168:171], v[100:103]\n\t" "v_cvt_pk_bf16_f32 v113, v82, v83\n\t" \
    "v_mfma_f32_16x16x32_bf16 v[96:99], v[40:43], a[172:175], v[96:99]\n\t" "v_cvt_pk_bf16_f32 v114, v84, v85\n\t" \
    "v_mfma_f32_16x16x32_bf16 v[100:103], v[44:47], a[172:175], v[100:103]\n\t" "v_cvt_pk_bf16_f32 v115, v86, v87\n\t" \
    "v_mfma_f32_16x16x32_bf16 v[104:107], v[16:19], a[176:179], 0\n\t" "v_cvt_pk_bf16_f32 v116, v88, v89\n\t" \
    "v_mfma_f32_16x16x32_bf16 v[108:111], v[20:23], a[176:179], 0\n\t" "v_cvt_pk_bf16_f32 v117, v90, v91\n\t" \
    "v_mfma_f32_16x16x32_bf16 v[104:107], v[24:27], a[180:183], v[104:107]\n\t" "buffer_store_dwordx4 v[112:115], %[z1], %[rs], s63 offen sc1\n\t" \
    "v_mfma_f32_16x16x32_bf16 v[108:111], v[28:31], a[180:183], v[108:111]\n\t" "v_mfma_f32_16x16x32_bf16 v[104:107], v[32:35], a[184:187], v[104:107]\n\t" \
    "v_cvt_pk_bf16_f32 v118, v92, v93\n\t" "v_mfma_f32_16x16x32_bf16 v[108:111], v[36:39], a[184:187], v[108:111]\n\t" "v_cvt_pk_bf16_f32 v119, v94, v95\n\t" \
    "v_mfma_f32_16x16x32_bf16 v[104:107], v[40:43], a[188:191], v[104:107]\n\t" "buffer_store_dwordx4 v[116:119], %[z2], %[rs], s63 offen sc1\n\t" \
    "v_mfma_f32_16x16x32_bf16 v[108:111], v[44:47], a[188:191], v[108:111]\n\t" "s_add_u32 s63, s63, 0x20000\n\t" \
    "v_mfma_f32_16x16x32_bf16 v[80:83], v[16:19], a[192:195], 0\n\t" "v_mfma_f32_16x16x32_bf16 v[84:87], v[20:23], a[192:195], 0\n\t" \
    "v_mfma_f32_16x16x32_bf16 v[80:83], v[24:27], a[196:199], v[80:83]\n\t" "v_mfma_f32_16x16x32_bf16 v[84:87], v[28:31], a[196:199], v[84:87]\n\t" \
    "v_mfma_f32_16x16x32_bf16 v[80:83], v[32:35], a[200:203], v[80:83]\n\t" "v_cvt_pk_bf16_f32 v112, v96, v97\n\t" \
    "v_mfma_f32_16x16x32_bf16 v[84:87], v[36:39], a[200:203], v[84:87]\n\t" "v_cvt_pk_bf16_f32 v113, v98, v99\n\t" \
    "v_mfma_f32_16x16x32_bf16 v[80:83], v[40:43], a[204:207], v[80:83]\n\t" "v_cvt_pk_bf16_f32 v114, v100, v101\n\t" \
    "v_mfma_f32_16x16x32_bf16 v[84:87], v[44:47], a[204:207], v[84:87]\n\t" "v_cvt_pk_bf16_f32 v115, v102, v103\n\t" \
    "v_mfma_f32_16x16x32_bf16 v[88:91], v[16:19], a[208:211], 0\n\t" "v_cvt_pk_bf16_f32 v116, v104, v105\n\t" "v_mfma_f32_16x16x32_bf16 v[92:95], v[20:23], a[208:211], 0\n\t" \
    "v_cvt_pk_bf16_f32 v117, v106, v107\n\t" "v_mfma_f32_16x16x32_bf16 v[88:91], v[24:27], a[212:215], v[88:91]\n\t" \
    "buffer_store_dwordx4 v[112:115], %[z1], %[rs], s63 offen sc1\n\t" "v_mfma_f32_16x16x32_bf16 v[92:95], v[28:31], a[212:215], v[92:95]\n\t" \
    "v_mfma_f32_16x16x32_bf16 v[88:91], v[32:35], a[216:219], v[88:91]\n\t" "v_cvt_pk_bf16_f32 v118, v108, v109\n\t" \
    "v_mfma_f32_16x16x32_bf16 v[92:95], v[36:39], a[216:219], v[92:95]\n\t" "v_cvt_pk_bf16_f32 v119, v110, v111\n\t" \
    "v_mfma_f32_16x16x32_bf16 v[88:91], v[40:43], a[220:223], v[88:91]\n\t" "buffer_store_dwordx4 v[116:119], %[z2], %[rs], s63 offen sc1\n\t" \
    "v_mfma_f32_16x16x32_bf16 v[92:95], v[44:47], a[220:223], v[92:95]\n\t" "s_add_u32 s63, s63, 0x20000\n\t" "v_mfma_f32_16x16x32_bf16 v[96:99], v[16:19], a[224:227], 0\n\t" \
    "v_mfma_f32_16x16x32_bf16 v[100:103], v[20:23], a[224:227], 0\n\t" "v_mfma_f32_16x16x32_bf16 v[96:99], v[24:27], a[228:231], v[96:99]\n\t" \
    "v_mfma_f32_16x16x32_bf16 v[100:103], v[28:31], a[228:231], v[100:103]\n\t" "v_mfma_f32_16x16x32_bf16 v[96:99], v[32:35], a[232:235], v[96:99]\n\t" \
    "v_cvt_pk_bf16_f32 v112, v80, v81\n\t" "v_mfma_f32_16x16x32_bf16 v[100:103], v[36:39], a[232:235], v[100:103]\n\t" "v_cvt_pk_bf16_f32 v113, v82, v83\n\t" \
    "v_mfma_f32_16x16x32_bf16 v[96:99], v[40:43], a[236:239], v[96:99]\n\t" "v_cvt_pk_bf16_f32 v114, v84, v85\n\t" \
    "v_mfma_f32_16x16x32_bf16 v[100:103], v[44:47], a[236:239], v[100:103]\n\t" "v_cvt_pk_bf16_f32 v115, v86, v87\n\t" \
    "v_mfma_f32_16x16x32_bf16 v[104:107], v[16:19], a[240:243], 0\n\t" "v_cvt_pk_bf16_f32 v116, v88, v89\n\t" \
    "v_mfma_f32_16x16x32_bf16 v[108:111], v[20:23], a[240:243], 0\n\t" "v_cvt_pk_bf16_f32 v117, v90, v91\n\t" \
    "v_mfma_f32_16x16x32_bf16 v[104:107], v[24:27], a[244:247], v[104:107]\n\t" "buffer_store_dwordx4 v[112:115], %[z1], %[rs], s63 offen sc1\n\t" \
    "v_mfma_f32_16x16x32_bf16 v[108:111], v[28:31], a[244:247], v[108:111]\n\t" "v_mfma_f32_16x16x32_bf16 v[104:107], v[32:35], a[248:251], v[104:107]\n\t" \
    "v_cvt_pk_bf16_f32 v118, v92, v93\n\t" "v_cvt_pk_bf16_f32 v119, v94, v95\n\t" "s_waitcnt vmcnt(11)\n\t" "v_readfirstlane_b32 s66, v120\n\t" \
    "v_mfma_f32_16x16x32_bf16 v[108:111], v[36:39], a[248:251], v[108:111]\n\t" "v_readfirstlane_b32 s67, v121\n\t" "v_mov_b32 v122, s60\n\t" \
    "buffer_store_dword v122, %[fo], %[rc], %[fz] offen sc1\n\t" "v_mfma_f32_16x16x32_bf16 v[104:107], v[40:43], a[252:255], v[104:107]\n\t" \
    "buffer_store_dwordx4 v[116:119], %[z2], %[rs], s63 offen sc1\n\t" "s_add_u32 s63, s63, 0x20000\n\t" "s_max_u32 s61, s61, s66\n\t" \
    "v_mfma_f32_16x16x32_bf16 v[108:111], v[44:47], a[252:255], v[108:111]\n\t" "s_max_u32 s62, s62, s67\n\t" "s_add_u32 s60, s60, 1\n\t" ZSUM "23:\n\t" XT(80, 81) \
    "s_add_u32 s66, s62, %[rg]\n\t" "s_cmp_lt_u32 s66, s60\n\t" "s_cbranch_scc1 70f\n\t" "73:\n\t" "s_add_u32 s66, s60, 1\n\t" "s_cmp_gt_u32 s61, s66\n\t" \
    "s_cbranch_scc0 41f\n\t" "v_mfma_f32_16x16x32_bf16 v[80:83], v[48:51], a[0:3], 0\n\t" "v_mfma_f32_16x16x32_bf16 v[84:87], v[52:55], a[0:3], 0\n\t" \
    "v_mfma_f32_16x16x32_bf16 v[80:83], v[56:59], a[4:7], v[80:83]\n\t" "v_mfma_f32_16x16x32_bf16 v[84:87], v[60:63], a[4:7], v[84:87]\n\t" \
    "v_mfma_f32_16x16x32_bf16 v[80:83], v[64:67], a[8:11], v[80:83]\n\t" "v_cvt_pk_bf16_f32 v112, v96, v97\n\t" \
    "v_mfma_f32_16x16x32_bf16 v[84:87], v[68:71], a[8:11], v[84:87]\n\t" "v_cvt_pk_bf16_f32 v113, v98, v99\n\t" \
    "v_mfma_f32_16x16x32_bf16 v[80:83], v[72:75], a[12:15], v[80:83]\n\t" "v_cvt_pk_bf16_f32 v114, v100, v101\n\t" \
    "v_mfma_f32_16x16x32_bf16 v[84:87], v[76:79], a[12:15], v[84:87]\n\t" "v_cvt_pk_bf16_f32 v115, v102, v103\n\t" \
    "v_mfma_f32_16x16x32_bf16 v[88:91], v[48:51], a[16:19], 0\n\t" "v_cvt_pk_bf16_f32 v116, v104, v105\n\t" "v_mfma_f32_16x16x32_bf16 v[92:95], v[52:55], a[16:19], 0\n\t" \
    "v_cvt_pk_bf16_f32 v117, v106, v107\n\t" "v_mfma_f32_16x16x32_bf16 v[88:91], v[56:59], a[20:23], v[88:91]\n\t" \
    "buffer_store_dwordx4 v[112:115], %[z1], %[rs], s63 offen sc1\n\t" "v_mfma_f32_16x16x32_bf16 v[92:95], v[60:63], a[20:23], v[92:95]\n\t" \
    "v_mfma_f32_16x16x32_bf16 v[88:91], v[64:67], a[24:27], v[88:91]\n\t" "v_cvt_pk_bf16_f32 v118, v108, v109\n\t" \
    "v_mfma_f32_16x16x32_bf16 v[92:95], v[68:71], a[24:27], v[92:95]\n\t" "v_cvt_pk_bf16_f32 v119, v110, v111\n\t" \
    "v_mfma_f32_16x16x32_bf16 v[88:91], v[72:75], a[28:31], v[88:91]\n\t" "buffer_store_dwordx4 v[116:119], %[z2], %[rs], s63 offen sc1\n\t" "s_and_b32 s63, s60, %[rm]\n\t" \
    "v_mfma_f32_16x16x32_bf16 v[92:95], v[76:79], a[28:31], v[92:95]\n\t" "s_lshl_b32 s63, s63, 22\n\t" "s_add_u32 s63, s63, %[zb]\n\t" \
    "v_mfma_f32_16x16x32_bf16 v[96:99], v[48:51], a[32:35], 0\n\t" "s_add_u32 s65, s60, 1\n\t" "s_lshr_b32 s66, s65, 2\n\t" \
    "v_mfma_f32_16x16x32_bf16 v[100:103], v[52:55], a[32:35], 0\n\t" "s_xor_b32 s66, s66, %[xp]\n\t" "s_and_b32 s66, s66, 1\n\t" \
    "v_mfma_f32_16x16x32_bf16 v[96:99], v[56:59], a[36:39], v[96:99]\n\t" "s_lshl_b32 s66, s66, 13\n\t" "s_lshl_b32 s64, s65, 19\n\t" \
    "v_mfma_f32_16x16x32_bf16 v[100:103], v[60:63], a[36:39], v[100:103]\n\t" "s_add_u32 s64, s64, s66\n\t" "s_add_u32 s64, s64, %[vb]\n\t" \
    "v_mfma_f32_16x16x32_bf16 v[96:99], v[64:67], a[40:43], v[96:99]\n\t" "buffer_load_dwordx4 v[16:19], %[ao], %[rs], s64 offen offset:0 sc1\n\t" \
    "v_cvt_pk_bf16_f32 v112, v80, v81\n\t" "v_mfma_f32_16x16x32_bf16 v[100:103], v[68:71], a[40:43], v[100:103]\n\t" "v_cvt_pk_bf16_f32 v113, v82, v83\n\t" \
    "v_mfma_f32_16x16x32_bf16 v[96:99], v[72:75], a[44:47], v[96:99]\n\t" "buffer_load_dwordx4 v[20:23], %[ao], %[rs], s64 offen offset:256 sc1\n\t" \
    "v_cvt_pk_bf16_f32 v114, v84, v85\n\t" "v_mfma_f32_16x16x32_bf16 v[100:103], v[76:79], a[44:47], v[100:103]\n\t" "v_cvt_pk_bf16_f32 v115, v86, v87\n\t" \
    "v_mfma_f32_16x16x32_bf16 v[104:107], v[48:51], a[48:51], 0\n\t" "buffer_load_dwordx4 v[24:27], %[ao], %[rs], s64 offen offset:2048 sc1\n\t" \
    "v_cvt_pk_bf16_f32 v116, v88, v89\n\t" "v_mfma_f32_16x16x32_bf16 v[108:111], v[52:55], a[48:51], 0\n\t" "v_cvt_pk_bf16_f32 v117, v90, v91\n\t" \
    "v_mfma_f32_16x16x32_bf16 v[104:107], v[56:59], a[52:55], v[104:107]\n\t" "buffer_load_dwordx4 v[28:31], %[ao], %[rs], s64 offen offset:2304 sc1\n\t" \
    "v_mfma_f32_16x16x32_bf16 v[108:111], v[60:63], a[52:55], v[108:111]\n\t" "buffer_store_dwordx4 v[112:115], %[z1], %[rs], s63 offen sc1\n\t" \
    "v_mfma_f32_16x16x32_bf16 v[104:107], v[64:67], a[56:59], v[104:107]\n\t" "buffer_load_dwordx4 v[32:35], %[a1], %[rs], s64 offen offset:0 sc1\n\t" \
    "v_cvt_pk_bf16_f32 v118, v92, v93\n\t" "v_mfma_f32_16x16x32_bf16 v[108:111], v[68:71], a[56:59], v[108:111]\n\t" "v_cvt_pk_bf16_f32 v119, v94, v95\n\t" \
    "v_mfma_f32_16x16x32_bf16 v[104:107], v[72:75], a[60:63], v[104:107]\n\t" "buffer_load_dwordx4 v[36:39], %[a1], %[rs], s64 offen offset:256 sc1\n\t" \
    "buffer_store_dwordx4 v[116:119], %[z2], %[rs], s63 offen sc1\n\t" "v_mfma_f32_16x16x32_bf16 v[108:111], v[76:79], a[60:63], v[108:111]\n\t" \
    "s_add_u32 s63, s63, 0x20000\n\t" "v_mfma_f32_16x16x32_bf16 v[80:83], v[48:51], a[64:67], 0\n\t" \
    "buffer_load_dwordx4 v[40:43], %[a1], %[rs], s64 offen offset:2048 sc1\n\t" "v_mfma_f32_16x16x32_bf16 v[84:87], v[52:55], a[64:67], 0\n\t" \
    "v_mfma_f32_16x16x32_bf16 v[80:83], v[56:59], a[68:71], v[80:83]\n\t" "buffer_load_dwordx4 v[44:47], %[a1], %[rs], s64 offen offset:2304 sc1\n\t" \
    "v_mfma_f32_16x16x32_bf16 v[84:87], v[60:63], a[68:71], v[84:87]\n\t" "v_mfma_f32_16x16x32_bf16 v[80:83], v[64:67], a[72:75], v[80:83]\n\t" \
    "buffer_load_dword v120, %[fo], %[rc], %[fv] offen sc1\n\t" "v_cvt_pk_bf16_f32 v112, v96, v97\n\t" "v_mfma_f32_16x16x32_bf16 v[84:87], v[68:71], a[72:75], v[84:87]\n\t" \
    "v_cvt_pk_bf16_f32 v113, v98, v99\n\t" "v_mfma_f32_16x16x32_bf16 v[80:83], v[72:75], a[76:79], v[80:83]\n\t" "buffer_load_dword v121, %[fo], %[rc], %[fs] offen sc1\n\t" \
    "v_mfma_f32_16x16x32_bf16 v[84:87], v[76:79], a[76:79], v[84:87]\n\t" "v_cvt_pk_bf16_f32 v114, v100, v101\n\t" "v_cvt_pk_bf16_f32 v115, v102, v103\n\t" \
    "v_mfma_f32_16x16x32_bf16 v[88:91], v[48:51], a[80:83], 0\n\t" "v_cvt_pk_bf16_f32 v116, v104, v105\n\t" "v_mfma_f32_16x16x32_bf16 v[92:95], v[52:55], a[80:83], 0\n\t" \
    "v_cvt_pk_bf16_f32 v117, v106, v107\n\t" "v_mfma_f32_16x16x32_bf16 v[88:91], v[56:59], a[84:87], v[88:91]\n\t" \
    "buffer_store_dwordx4 v[112:115], %[z1], %[rs], s63 offen sc1\n\t" "v_mfma_f32_16x16x32_bf16 v[92:95], v[60:63], a[84:87], v[92:95]\n\t" \
    "v_mfma_f32_16x16x32_bf16 v[88:91], v[64:67], a[88:91], v[88:91]\n\t" "v_cvt_pk_bf16_f32 v118, v108, v109\n\t" \
    "v_mfma_f32_16x16x32_bf16 v[92:95], v[68:71], a[88:91], v[92:95]\n\t" "v_cvt_pk_bf16_f32 v119, v110, v111\n\t" \
    "v_mfma_f32_16x16x32_bf16 v[88:91], v[72:75], a[92:95], v[88:91]\n\t" "buffer_store_dwordx4 v[116:119], %[z2], %[rs], s63 offen sc1\n\t" \
    "v_mfma_f32_16x16x32_bf16 v[92:95], v[76:79], a[92:95], v[92:95]\n\t" "s_add_u32 s63, s63, 0x20000\n\t" "v_mfma_f32_16x16x32_bf16 v[96:99], v[48:51], a[96:99], 0\n\t" \
    "v_mfma_f32_16x16x32_bf16 v[100:103], v[52:55], a[96:99], 0\n\t" "v_mfma_f32_16x16x32_bf16 v[96:99], v[56:59], a[100:103], v[96:99]\n\t" \
    "v_mfma_f32_16x16x32_bf16 v[100:103], v[60:63], a[100:103], v[100:103]\n\t" "v_mfma_f32_16x16x32_bf16 v[96:99], v[64:67], a[104:107], v[96:99]\n\t" \
    "v_cvt_pk_bf16_f32 v112, v80, v81\n\t" "v_mfma_f32_16x16x32_bf16 v[100:103], v[68:71], a[104:107], v[100:103]\n\t" "v_cvt_pk_bf16_f32 v113, v82, v83\n\t" \
    "v_mfma_f32_16x16x32_bf16 v[96:99], v[72:75], a[108:111], v[96:99]\n\t" "v_cvt_pk_bf16_f32 v114, v84, v85\n\t" \
    "v_mfma_f32_16x16x32_bf16 v[100:103], v[76:79], a[108:111], v[100:103]\n\t" "v_cvt_pk_bf16_f32 v115, v86, v87\n\t" \
    "v_mfma_f32_16x16x32_bf16 v[104:107], v[48:51], a[112:115], 0\n\t" "v_cvt_pk_bf16_f32 v116, v88, v89\n\t" \
    "v_mfma_f32_16x16x32_bf16 v[108:111], v[52:55], a[112:115], 0\n\t" "v_cvt_pk_bf16_f32 v117, v90, v91\n\t" \
    "v_mfma_f32_16x16x32_bf16 v[104:107], v[56:59], a[116:119], v[104:107]\n\t" "buffer_store_dwordx4 v[112:115], %[z1], %[rs], s63 offen sc1\n\t" \
    "v_mfma_f32_16x16x32_bf16 v[108:111], v[60:63], a[116:119], v[108:111]\n\t" "v_mfma_f32_16x16x32_bf16 v[104:107], v[64:67], a[120:123], v[104:107]\n\t" \
    "v_cvt_pk_bf16_f32 v118, v92, v93\n\t" "v_mfma_f32_16x16x32_bf16 v[108:111], v[68:71], a[120:123], v[108:111]\n\t" "v_cvt_pk_bf16_f32 v119, v94, v95\n\t" \
    "v_mfma_f32_16x16x32_bf16 v[104:107], v[72:75], a[124:127], v[104:107]\n\t" "buffer_store_dwordx4 v[116:119], %[z2], %[rs], s63 offen sc1\n\t" \
    "v_mfma_f32_16x16x32_bf16 v[108:111], v[76:79], a[124:127], v[108:111]\n\t" "s_add_u32 s63, s63, 0x20000\n\t" \
    "v_mfma_f32_16x16x32_bf16 v[80:83], v[48:51], a[128:131], 0\n\t" "v_mfma_f32_16x16x32_bf16 v[84:87], v[52:55], a[128:131], 0\n\t" \
    "v_mfma_f32_16x16x32_bf16 v[80:83], v[56:59], a[132:135], v[80:83]\n\t" "v_mfma_f32_16x16x32_bf16 v[84:87], v[60:63], a[132:135], v[84:87]\n\t" \
    "v_mfma_f32_16x16x32_bf16 v[80:83], v[64:67], a[136:139], v[80:83]\n\t" "v_cvt_pk_bf16_f32 v112, v96, v97\n\t" \
    "v_mfma_f32_16x16x32_bf16 v[84:87], v[68:71], a[136:139], v[84:87]\n\t" "v_cvt_pk_bf16_f32 v113, v98, v99\n\t" \
    "v_mfma_f32_16x16x32_bf16 v[80:83], v[72:75], a[140:143], v[80:83]\n\t" "v_cvt_pk_bf16_f32 v114, v100, v101\n\t" \
    "v_mfma_f32_16x16x32_bf16 v[84:87], v[76:79], a[140:143], v[84:87]\n\t" "v_cvt_pk_bf16_f32 v115, v102, v103\n\t" \
    "v_mfma_f32_16x16x32_bf16 v[88:91], v[48:51], a[144:147], 0\n\t" "v_cvt_pk_bf16_f32 v116, v104, v105\n\t" "v_mfma_f32_16x16x32_bf16 v[92:95], v[52:55], a[144:147], 0\n\t" \
    "v_cvt_pk_bf16_f32 v117, v106, v107\n\t" "v_mfma_f32_16x16x32_bf16 v[88:91], v[56:59], a[148:151], v[88:91]\n\t" \
    "buffer_store_dwordx4 v[112:115], %[z1], %[rs], s63 offen sc1\n\t" "v_mfma_f32_16x16x32_bf16 v[92:95], v[60:63], a[148:151], v[92:95]\n\t" \
    "v_mfma_f32_16x16x32_bf16 v[88:91], v[64:67], a[152:155], v[88:91]\n\t" "v_cvt_pk_bf16_f32 v118, v108, v109\n\t" \
    "v_mfma_f32_16x16x32_bf16 v[92:95], v[68:71], a[152:155], v[92:95]\n\t" "v_cvt_pk_bf16_f32 v119, v110, v111\n\t" \
    "v_mfma_f32_16x16x32_bf16 v[88:91], v[72:75], a[156:159], v[88:91]\n\t" "buffer_store_dwordx4 v[116:119], %[z2], %[rs], s63 offen sc1\n\t" \
    "v_mfma_f32_16x16x32_bf16 v[92:95], v[76:79], a[156:159], v[92:95]\n\t" "s_add_u32 s63, s63, 0x20000\n\t" "v_mfma_f32_16x16x32_bf16 v[96:99], v[48:51], a[160:163], 0\n\t" \
    "v_mfma_f32_16x16x32_bf16 v[100:103], v[52:55], a[160:163], 0\n\t" "v_mfma_f32_16x16x32_bf16 v[96:99], v[56:59], a[164:167], v[96:99]\n\t" \
    "v_mfma_f32_16x16x32_bf16 v[100:103], v[60:63], a[164:167], v[100:103]\n\t" "v_mfma_f32_16x16x32_bf16 v[96:99], v[64:67], a[168:171], v[96:99]\n\t" \
    "v_cvt_pk_bf16_f32 v112, v80, v81\n\t" "v_mfma_f32_16x16x32_bf16 v[100:103], v[68:71], a[168:171], v[100:103]\n\t" "v_cvt_pk_bf16_f32 v113, v82, v83\n\t" \
    "v_mfma_f32_16x16x32_bf16 v[96:99], v[72:75], a[172:175], v[96:99]\n\t" "v_cvt_pk_bf16_f32 v114, v84, v85\n\t" \
    "v_mfma_f32_16x16x32_bf16 v[100:103], v[76:79], a[172:175], v[100:103]\n\t" "v_cvt_pk_bf16_f32 v115, v86, v87\n\t" \
    "v_mfma_f32_16x16x32_bf16 v[104:107], v[48:51], a[176:179], 0\n\t" "v_cvt_pk_bf16_f32 v116, v88, v89\n\t" \
    "v_mfma_f32_16x16x32_bf16 v[108:111], v[52:55], a[176:179], 0\n\t" "v_cvt_pk_bf16_f32 v117, v90, v91\n\t" \
    "v_mfma_f32_16x16x32_bf16 v[104:107], v[56:59], a[180:183], v[104:107]\n\t" "buffer_store_dwordx4 v[112:115], %[z1], %[rs], s63 offen sc1\n\t" \
    "v_mfma_f32_16x16x32_bf16 v[108:111], v[60:63], a[180:183], v[108:111]\n\t" "v_mfma_f32_16x16x32_bf16 v[104:107], v[64:67], a[184:187], v[104:107]\n\t" \
    "v_cvt_pk_bf16_f32 v118, v92, v93\n\t" "v_mfma_f32_16x16x32_bf16 v[108:111], v[68:71], a[184:187], v[108:111]\n\t" "v_cvt_pk_bf16_f32 v119, v94, v95\n\t" \
    "v_mfma_f32_16x16x32_bf16 v[104:107], v[72:75], a[188:191], v[104:107]\n\t" "buffer_store_dwordx4 v[116:119], %[z2], %[rs], s63 offen sc1\n\t" \
    "v_mfma_f32_16x16x32_bf16 v[108:111], v[76:79], a[188:191], v[108:111]\n\t" "s_add_u32 s63, s63, 0x20000\n\t" \
    "v_mfma_f32_16x16x32_bf16 v[80:83], v[48:51], a[192:195], 0\n\t" "v_mfma_f32_16x16x32_bf16 v[84:87], v[52:55], a[192:195], 0\n\t" \
    "v_mfma_f32_16x16x32_bf16 v[80:83], v[56:59], a[196:199], v[80:83]\n\t" "v_mfma_f32_16x16x32_bf16 v[84:87], v[60:63], a[196:199], v[84:87]\n\t" \
    "v_mfma_f32_16x16x32_bf16 v[80:83], v[64:67], a[200:203], v[80:83]\n\t" "v_cvt_pk_bf16_f32 v112, v96, v97\n\t" \
    "v_mfma_f32_16x16x32_bf16 v[84:87], v[68:71], a[200:203], v[84:87]\n\t" "v_cvt_pk_bf16_f32 v113, v98, v99\n\t" \
    "v_mfma_f32_16x16x32_bf16 v[80:83], v[72:75], a[204:207], v[80:83]\n\t" "v_cvt_pk_bf16_f32 v114, v100, v101\n\t" \
    "v_mfma_f32_16x16x32_bf16 v[84:87], v[76:79], a[204:207], v[84:87]\n\t" "v_cvt_pk_bf16_f32 v115, v102, v103\n\t" \
    "v_mfma_f32_16x16x32_bf16 v[88:91], v[48:51], a[208:211], 0\n\t" "v_cvt_pk_bf16_f32 v116, v104, v105\n\t" "v_mfma_f32_16x16x32_bf16 v[92:95], v[52:55], a[208:211], 0\n\t" \
    "v_cvt_pk_bf16_f32 v117, v106, v107\n\t" "v_mfma_f32_16x16x32_bf16 v[88:91], v[56:59], a[212:215], v[88:91]\n\t" \
    "buffer_store_dwordx4 v[112:115], %[z1], %[rs], s63 offen sc1\n\t" "v_mfma_f32_16x16x32_bf16 v[92:95], v[60:63], a[212:215], v[92:95]\n\t" \
    "v_mfma_f32_16x16x32_bf16 v[88:91], v[64:67], a[216:219], v[88:91]\n\t" "v_cvt_pk_bf16_f32 v118, v108, v109\n\t" \
    "v_mfma_f32_16x16x32_bf16 v[92:95], v[68:71], a[216:219], v[92:95]\n\t" "v_cvt_pk_bf16_f32 v119, v110, v111\n\t" \
    "v_mfma_f32_16x16x32_bf16 v[88:91], v[72:75], a[220:223], v[88:91]\n\t" "buffer_store_dwordx4 v[116:119], %[z2], %[rs], s63 offen sc1\n\t" \
    "v_mfma_f32_16x16x32_bf16 v[92:95], v[76:79], a[220:223], v[92:95]\n\t" "s_add_u32 s63, s63, 0x20000\n\t" "v_mfma_f32_16x16x32_bf16 v[96:99], v[48:51], a[224:227], 0\n\t" \
    "v_mfma_f32_16x16x32_bf16 v[100:103], v[52:55], a[224:227], 0\n\t" "v_mfma_f32_16x16x32_bf16 v[96:99], v[56:59], a[228:231], v[96:99]\n\t" \
    "v_mfma_f32_16x16x32_bf16 v[100:103], v[60:63], a[228:231], v[100:103]\n\t" "v_mfma_f32_16x16x32_bf16 v[96:99], v[64:67], a[232:235], v[96:99]\n\t" \
    "v_cvt_pk_bf16_f32 v112, v80, v81\n\t" "v_mfma_f32_16x16x32_bf16 v[100:103], v[68:71], a[232:235], v[100:103]\n\t" "v_cvt_pk_bf16_f32 v113, v82, v83\n\t" \
    "v_mfma_f32_16x16x32_bf16 v[96:99], v[72:75], a[236:239], v[96:99]\n\t" "v_cvt_pk_bf16_f32 v114, v84, v85\n\t" \
    "v_mfma_f32_16x16x32_bf16 v[100:103], v[76:79], a[236:239], v[100:103]\n\t" "v_cvt_pk_bf16_f32 v115, v86, v87\n\t" \
    "v_mfma_f32_16x16x32_bf16 v[104:107], v[48:51], a[240:243], 0\n\t" "v_cvt_pk_bf16_f32 v116, v88, v89\n\t" \
    "v_mfma_f32_16x16x32_bf16 v[108:111], v[52:55], a[240:243], 0\n\t" "v_cvt_pk_bf16_f32 v117, v90, v91\n\t" \
    "v_mfma_f32_16x16x32_bf16 v[104:107], v[56:59], a[244:247], v[104:107]\n\t" "buffer_store_dwordx4 v[112:115], %[z1], %[rs], s63 offen sc1\n\t" \
    "v_mfma_f32_16x16x32_bf16 v[108:111], v[60:63], a[244:247], v[108:111]\n\t" "v_mfma_f32_16x16x32_bf16 v[104:107], v[64:67], a[248:251], v[104:107]\n\t" \
    "v_cvt_pk_bf16_f32 v118, v92, v93\n\t" "v_cvt_pk_bf16_f32 v119, v94, v95\n\t" "s_waitcnt vmcnt(11)\n\t" "v_readfirstlane_b32 s66, v120\n\t" \
    "v_mfma_f32_16x16x32_bf16 v[108:111], v[68:71], a[248:251], v[108:111]\n\t" "v_readfirstlane_b32 s67, v121\n\t" "v_mov_b32 v122, s60\n\t" \
    "buffer_store_dword v122, %[fo], %[rc], %[fz] offen sc1\n\t" "v_mfma_f32_16x16x32_bf16 v[104:107], v[72:75], a[252:255], v[104:107]\n\t" \
    "buffer_store_dwordx4 v[116:119], %[z2], %[rs], s63 offen sc1\n\t" "s_add_u32 s63, s63, 0x20000\n\t" "s_max_u32 s61, s61, s66\n\t" \
    "v_mfma_f32_16x16x32_bf16 v[108:111], v[76:79], a[252:255], v[108:111]\n\t" "s_max_u32 s62, s62, s67\n\t" "s_add_u32 s60, s60, 1\n\t" ZSUM "33:\n\t" "s_branch 10b\n\t" \
    "40:\n\t" "v_mfma_f32_16x16x32_bf16 v[80:83], v[16:19], a[0:3], 0\n\t" "v_mfma_f32_16x16x32_bf16 v[84:87], v[20:23], a[0:3], 0\n\t" \
    "v_mfma_f32_16x16x32_bf16 v[80:83], v[24:27], a[4:7], v[80:83]\n\t" "v_mfma_f32_16x16x32_bf16 v[84:87], v[28:31], a[4:7], v[84:87]\n\t" \
    "v_mfma_f32_16x16x32_bf16 v[80:83], v[32:35], a[8:11], v[80:83]\n\t" "v_cvt_pk_bf16_f32 v112, v96, v97\n\t" \
    "v_mfma_f32_16x16x32_bf16 v[84:87], v[36:39], a[8:11], v[84:87]\n\t" "v_cvt_pk_bf16_f32 v113, v98, v99\n\t" \
    "v_mfma_f32_16x16x32_bf16 v[80:83], v[40:43], a[12:15], v[80:83]\n\t" "v_cvt_pk_bf16_f32 v114, v100, v101\n\t" \
    "v_mfma_f32_16x16x32_bf16 v[84:87], v[44:47], a[12:15], v[84:87]\n\t" "v_cvt_pk_bf16_f32 v115, v102, v103\n\t" \
    "v_mfma_f32_16x16x32_bf16 v[88:91], v[16:19], a[16:19], 0\n\t" "v_cvt_pk_bf16_f32 v116, v104, v105\n\t" "v_mfma_f32_16x16x32_bf16 v[92:95], v[20:23], a[16:19], 0\n\t" \
    "v_cvt_pk_bf16_f32 v117, v106, v107\n\t" "v_mfma_f32_16x16x32_bf16 v[88:91], v[24:27], a[20:23], v[88:91]\n\t" \
    "buffer_store_dwordx4 v[112:115], %[z1], %[rs], s63 offen sc1\n\t" "v_mfma_f32_16x16x32_bf16 v[92:95], v[28:31], a[20:23], v[92:95]\n\t" \
    "v_mfma_f32_16x16x32_bf16 v[88:91], v[32:35], a[24:27], v[88:91]\n\t" "v_cvt_pk_bf16_f32 v118, v108, v109\n\t" \
    "v_mfma_f32_16x16x32_bf16 v[92:95], v[36:39], a[24:27], v[92:95]\n\t" "v_cvt_pk_bf16_f32 v119, v110, v111\n\t" \
    "v_mfma_f32_16x16x32_bf16 v[88:91], v[40:43], a[28:31], v[88:91]\n\t" "buffer_store_dwordx4 v[116:119], %[z2], %[rs], s63 offen sc1\n\t" "s_and_b32 s63, s60, %[rm]\n\t" \
    "v_mfma_f32_16x16x32_bf16 v[92:95], v[44:47], a[28:31], v[92:95]\n\t" "s_lshl_b32 s63, s63, 22\n\t" "s_add_u32 s63, s63, %[zb]\n\t" \
    "v_mfma_f32_16x16x32_bf16 v[96:99], v[16:19], a[32:35], 0\n\t" "buffer_load_dword v120, %[fo], %[rc], %[fv] offen sc1\n\t" \
    "v_mfma_f32_16x16x32_bf16 v[100:103], v[20:23], a[32:35], 0\n\t" "v_mfma_f32_16x16x32_bf16 v[96:99], v[24:27], a[36:39], v[96:99]\n\t" \
    "buffer_load_dword v121, %[fo], %[rc], %[fs] offen sc1\n\t" "v_mfma_f32_16x16x32_bf16 v[100:103], v[28:31], a[36:39], v[100:103]\n\t" \
    "v_mfma_f32_16x16x32_bf16 v[96:99], v[32:35], a[40:43], v[96:99]\n\t" "v_cvt_pk_bf16_f32 v112, v80, v81\n\t" \
    "v_mfma_f32_16x16x32_bf16 v[100:103], v[36:39], a[40:43], v[100:103]\n\t" "v_cvt_pk_bf16_f32 v113, v82, v83\n\t" \
    "v_mfma_f32_16x16x32_bf16 v[96:99], v[40:43], a[44:47], v[96:99]\n\t" "v_cvt_pk_bf16_f32 v114, v84, v85\n\t" \
    "v_mfma_f32_16x16x32_bf16 v[100:103], v[44:47], a[44:47], v[100:103]\n\t" "v_cvt_pk_bf16_f32 v115, v86, v87\n\t" \
    "v_mfma_f32_16x16x32_bf16 v[104:107], v[16:19], a[48:51], 0\n\t" "v_cvt_pk_bf16_f32 v116, v88, v89\n\t" "v_mfma_f32_16x16x32_bf16 v[108:111], v[20:23], a[48:51], 0\n\t" \
    "v_cvt_pk_bf16_f32 v117, v90, v91\n\t" "v_mfma_f32_16x16x32_bf16 v[104:107], v[24:27], a[52:55], v[104:107]\n\t" \
    "buffer_store_dwordx4 v[112:115], %[z1], %[rs], s63 offen sc1\n\t" "v_mfma_f32_16x16x32_bf16 v[108:111], v[28:31], a[52:55], v[108:111]\n\t" \
    "v_mfma_f32_16x16x32_bf16 v[104:107], v[32:35], a[56:59], v[104:107]\n\t" "v_cvt_pk_bf16_f32 v118, v92, v93\n\t" \
    "v_mfma_f32_16x16x32_bf16 v[108:111], v[36:39], a[56:59], v[108:111]\n\t" "v_cvt_pk_bf16_f32 v119, v94, v95\n\t" \
    "v_mfma_f32_16x16x32_bf16 v[104:107], v[40:43], a[60:63], v[104:107]\n\t" "buffer_store_dwordx4 v[116:119], %[z2], %[rs], s63 offen sc1\n\t" \
    "v_mfma_f32_16x16x32_bf16 v[108:111], v[44:47], a[60:63], v[108:111]\n\t" "s_add_u32 s63, s63, 0x20000\n\t" "v_mfma_f32_16x16x32_bf16 v[80:83], v[16:19], a[64:67], 0\n\t" \
    "s_add_u32 s65, s60, 1\n\t" "s_lshr_b32 s66, s65, 2\n\t" "v_mfma_f32_16x16x32_bf16 v[84:87], v[20:23], a[64:67], 0\n\t" "s_xor_b32 s66, s66, %[xp]\n\t" \
    "s_and_b32 s66, s66, 1\n\t" "v_mfma_f32_16x16x32_bf16 v[80:83], v[24:27], a[68:71], v[80:83]\n\t" "v_mfma_f32_16x16x32_bf16 v[84:87], v[28:31], a[68:71], v[84:87]\n\t" \
    "v_mfma_f32_16x16x32_bf16 v[80:83], v[32:35], a[72:75], v[80:83]\n\t" "v_cvt_pk_bf16_f32 v112, v96, v97\n\t" \
    "v_mfma_f32_16x16x32_bf16 v[84:87], v[36:39], a[72:75], v[84:87]\n\t" "v_cvt_pk_bf16_f32 v113, v98, v99\n\t" \
    "v_mfma_f32_16x16x32_bf16 v[80:83], v[40:43], a[76:79], v[80:83]\n\t" "v_cvt_pk_bf16_f32 v114, v100, v101\n\t" \
    "v_mfma_f32_16x16x32_bf16 v[84:87], v[44:47], a[76:79], v[84:87]\n\t" "v_cvt_pk_bf16_f32 v115, v102, v103\n\t" \
    "v_mfma_f32_16x16x32_bf16 v[88:91], v[16:19], a[80:83], 0\n\t" "v_cvt_pk_bf16_f32 v116, v104, v105\n\t" "v_mfma_f32_16x16x32_bf16 v[92:95], v[20:23], a[80:83], 0\n\t" \
    "v_cvt_pk_bf16_f32 v117, v106, v107\n\t" "v_mfma_f32_16x16x32_bf16 v[88:91], v[24:27], a[84:87], v[88:91]\n\t" \
    "buffer_store_dwordx4 v[112:115], %[z1], %[rs], s63 offen sc1\n\t" "v_mfma_f32_16x16x32_bf16 v[92:95], v[28:31], a[84:87], v[92:95]\n\t" \
    "v_mfma_f32_16x16x32_bf16 v[88:91], v[32:35], a[88:91], v[88:91]\n\t" "v_cvt_pk_bf16_f32 v118, v108, v109\n\t" \
    "v_mfma_f32_16x16x32_bf16 v[92:95], v[36:39], a[88:91], v[92:95]\n\t" "v_cvt_pk_bf16_f32 v119, v110, v111\n\t" \
    "v_mfma_f32_16x16x32_bf16 v[88:91], v[40:43], a[92:95], v[88:91]\n\t" "buffer_store_dwordx4 v[116:119], %[z2], %[rs], s63 offen sc1\n\t" \
    "v_mfma_f32_16x16x32_bf16 v[92:95], v[44:47], a[92:95], v[92:95]\n\t" "s_add_u32 s63, s63, 0x20000\n\t" "v_mfma_f32_16x16x32_bf16 v[96:99], v[16:19], a[96:99], 0\n\t" \
    "s_lshl_b32 s66, s66, 13\n\t" "s_lshl_b32 s64, s65, 19\n\t" "v_mfma_f32_16x16x32_bf16 v[100:103], v[20:23], a[96:99], 0\n\t" "s_add_u32 s64, s64, s66\n\t" \
    "s_add_u32 s64, s64, %[vb]\n\t" "v_mfma_f32_16x16x32_bf16 v[96:99], v[24:27], a[100:103], v[96:99]\n\t" \
    "v_mfma_f32_16x16x32_bf16 v[100:103], v[28:31], a[100:103], v[100:103]\n\t" "v_mfma_f32_16x16x32_bf16 v[96:99], v[32:35], a[104:107], v[96:99]\n\t" \
    "v_cvt_pk_bf16_f32 v112, v80, v81\n\t" "v_mfma_f32_16x16x32_bf16 v[100:103], v[36:39], a[104:107], v[100:103]\n\t" "v_cvt_pk_bf16_f32 v113, v82, v83\n\t" \
    "v_mfma_f32_16x16x32_bf16 v[96:99], v[40:43], a[108:111], v[96:99]\n\t" "v_cvt_pk_bf16_f32 v114, v84, v85\n\t" \
    "v_mfma_f32_16x16x32_bf16 v[100:103], v[44:47], a[108:111], v[100:103]\n\t" "v_cvt_pk_bf16_f32 v115, v86, v87\n\t" \
    "v_mfma_f32_16x16x32_bf16 v[104:107], v[16:19], a[112:115], 0\n\t" "v_cvt_pk_bf16_f32 v116, v88, v89\n\t" \
    "v_mfma_f32_16x16x32_bf16 v[108:111], v[20:23], a[112:115], 0\n\t" "v_cvt_pk_bf16_f32 v117, v90, v91\n\t" \
    "v_mfma_f32_16x16x32_bf16 v[104:107], v[24:27], a[116:119], v[104:107]\n\t" "buffer_store_dwordx4 v[112:115], %[z1], %[rs], s63 offen sc1\n\t" \
    "v_mfma_f32_16x16x32_bf16 v[108:111], v[28:31], a[116:119], v[108:111]\n\t" "v_mfma_f32_16x16x32_bf16 v[104:107], v[32:35], a[120:123], v[104:107]\n\t" \
    "v_cvt_pk_bf16_f32 v118, v92, v93\n\t" "v_mfma_f32_16x16x32_bf16 v[108:111], v[36:39], a[120:123], v[108:111]\n\t" "v_cvt_pk_bf16_f32 v119, v94, v95\n\t" \
    "v_mfma_f32_16x16x32_bf16 v[104:107], v[40:43], a[124:127], v[104:107]\n\t" "buffer_store_dwordx4 v[116:119], %[z2], %[rs], s63 offen sc1\n\t" \
    "v_mfma_f32_16x16x32_bf16 v[108:111], v[44:47], a[124:127], v[108:111]\n\t" "s_add_u32 s63, s63, 0x20000\n\t" \
    "v_mfma_f32_16x16x32_bf16 v[80:83], v[16:19], a[128:131], 0\n\t" "s_waitcnt vmcnt(6)\n\t" "v_readfirstlane_b32 s66, v120\n\t" "v_readfirstlane_b32 s67, v121\n\t" \
    "v_mfma_f32_16x16x32_bf16 v[84:87], v[20:23], a[128:131], 0\n\t" "v_mov_b32 v122, s60\n\t" "buffer_store_dword v122, %[fo], %[rc], %[fz] offen sc1\n\t" \
    "v_mfma_f32_16x16x32_bf16 v[80:83], v[24:27], a[132:135], v[80:83]\n\t" "s_max_u32 s61, s61, s66\n\t" "s_max_u32 s62, s62, s67\n\t" "s_cmp_gt_u32 s61, s65\n\t" \
    "v_mfma_f32_16x16x32_bf16 v[84:87], v[28:31], a[132:135], v[84:87]\n\t" "s_cselect_b32 s65, 1, 0\n\t" "s_cselect_b32 s64, s64, 0x7f000000\n\t" \
    "v_mfma_f32_16x16x32_bf16 v[80:83], v[32:35], a[136:139], v[80:83]\n\t" "buffer_load_dwordx4 v[48:51], %[ao], %[rs], s64 offen offset:0 sc1\n\t" \
    "v_cvt_pk_bf16_f32 v112, v96, v97\n\t" "v_mfma_f32_16x16x32_bf16 v[84:87], v[36:39], a[136:139], v[84:87]\n\t" "v_cvt_pk_bf16_f32 v113, v98, v99\n\t" \
    "v_mfma_f32_16x16x32_bf16 v[80:83], v[40:43], a[140:143], v[80:83]\n\t" "buffer_load_dwordx4 v[52:55], %[ao], %[rs], s64 offen offset:256 sc1\n\t" \
    "v_cvt_pk_bf16_f32 v114, v100, v101\n\t" "v_mfma_f32_16x16x32_bf16 v[84:87], v[44:47], a[140:143], v[84:87]\n\t" "v_cvt_pk_bf16_f32 v115, v102, v103\n\t" \
    "v_mfma_f32_16x16x32_bf16 v[88:91], v[16:19], a[144:147], 0\n\t" "buffer_load_dwordx4 v[56:59], %[ao], %[rs], s64 offen offset:2048 sc1\n\t" \
    "v_cvt_pk_bf16_f32 v116, v104, v105\n\t" "v_mfma_f32_16x16x32_bf16 v[92:95], v[20:23], a[144:147], 0\n\t" "v_cvt_pk_bf16_f32 v117, v106, v107\n\t" \
    "v_mfma_f32_16x16x32_bf16 v[88:91], v[24:27], a[148:151], v[88:91]\n\t" "buffer_load_dwordx4 v[60:63], %[ao], %[rs], s64 offen offset:2304 sc1\n\t" \
    "v_mfma_f32_16x16x32_bf16 v[92:95], v[28:31], a[148:151], v[92:95]\n\t" "buffer_store_dwordx4 v[112:115], %[z1], %[rs], s63 offen sc1\n\t" \
    "v_mfma_f32_16x16x32_bf16 v[88:91], v[32:35], a[152:155], v[88:91]\n\t" "buffer_load_dwordx4 v[64:67], %[a1], %[rs], s64 offen offset:0 sc1\n\t" \
    "v_cvt_pk_bf16_f32 v118, v108, v109\n\t" "v_mfma_f32_16x16x32_bf16 v[92:95], v[36:39], a[152:155], v[92:95]\n\t" "v_cvt_pk_bf16_f32 v119, v110, v111\n\t" \
    "v_mfma_f32_16x16x32_bf16 v[88:91], v[40:43], a[156:159], v[88:91]\n\t" "buffer_load_dwordx4 v[68:71], %[a1], %[rs], s64 offen offset:256 sc1\n\t" \
    "buffer_store_dwordx4 v[116:119], %[z2], %[rs], s63 offen sc1\n\t" "v_mfma_f32_16x16x32_bf16 v[92:95], v[44:47], a[156:159], v[92:95]\n\t" \
    "s_add_u32 s63, s63, 0x20000\n\t" "v_mfma_f32_16x16x32_bf16 v[96:99], v[16:19], a[160:163], 0\n\t" \
    "buffer_load_dwordx4 v[72:75], %[a1], %[rs], s64 offen offset:2048 sc1\n\t" "v_mfma_f32_16x16x32_bf16 v[100:103], v[20:23], a[160:163], 0\n\t" \
    "v_mfma_f32_16x16x32_bf16 v[96:99], v[24:27], a[164:167], v[96:99]\n\t" "buffer_load_dwordx4 v[76:79], %[a1], %[rs], s64 offen offset:2304 sc1\n\t" \
    "v_mfma_f32_16x16x32_bf16 v[100:103], v[28:31], a[164:167], v[100:103]\n\t" "v_mfma_f32_16x16x32_bf16 v[96:99], v[32:35], a[168:171], v[96:99]\n\t" \
    "v_cvt_pk_bf16_f32 v112, v80, v81\n\t" "v_mfma_f32_16x16x32_bf16 v[100:103], v[36:39], a[168:171], v[100:103]\n\t" "v_cvt_pk_bf16_f32 v113, v82, v83\n\t" \
    "v_mfma_f32_16x16x32_bf16 v[96:99], v[40:43], a[172:175], v[96:99]\n\t" "v_cvt_pk_bf16_f32 v114, v84, v85\n\t" \
    "v_mfma_f32_16x16x32_bf16 v[100:103], v[44:47], a[172:175], v[100:103]\n\t" "v_cvt_pk_bf16_f32 v115, v86, v87\n\t" \
    "v_mfma_f32_16x16x32_bf16 v[104:107], v[16:19], a[176:179], 0\n\t" "v_cvt_pk_bf16_f32 v116, v88, v89\n\t" \
    "v_mfma_f32_16x16x32_bf16 v[108:111], v[20:23], a[176:179], 0\n\t" "v_cvt_pk_bf16_f32 v117, v90, v91\n\t" \
    "v_mfma_f32_16x16x32_bf16 v[104:107], v[24:27], a[180:183], v[104:107]\n\t" "buffer_store_dwordx4 v[112:115], %[z1], %[rs], s63 offen sc1\n\t" \
    "v_mfma_f32_16x16x32_bf16 v[108:111], v[28:31], a[180:183], v[108:111]\n\t" "v_mfma_f32_16x16x32_bf16 v[104:107], v[32:35], a[184:187], v[104:107]\n\t" \
    "v_cvt_pk_bf16_f32 v118, v92, v93\n\t" "v_mfma_f32_16x16x32_bf16 v[108:111], v[36:39], a[184:187], v[108:111]\n\t" "v_cvt_pk_bf16_f32 v119, v94, v95\n\t" \
    "v_mfma_f32_16x16x32_bf16 v[104:107], v[40:43], a[188:191], v[104:107]\n\t" "buffer_store_dwordx4 v[116:119], %[z2], %[rs], s63 offen sc1\n\t" \
    "v_mfma_f32_16x16x32_bf16 v[108:111], v[44:47], a[188:191], v[108:111]\n\t" "s_add_u32 s63, s63, 0x20000\n\t" \
    "v_mfma_f32_16x16x32_bf16 v[80:83], v[16:19], a[192:195], 0\n\t" "v_mfma_f32_16x16x32_bf16 v[84:87], v[20:23], a[192:195], 0\n\t" \
    "v_mfma_f32_16x16x32_bf16 v[80:83], v[24:27], a[196:199], v[80:83]\n\t" "v_mfma_f32_16x16x32_bf16 v[84:87], v[28:31], a[196:199], v[84:87]\n\t" \
    "v_mfma_f32_16x16x32_bf16 v[80:83], v[32:35], a[200:203], v[80:83]\n\t" "v_cvt_pk_bf16_f32 v112, v96, v97\n\t" \
    "v_mfma_f32_16x16x32_bf16 v[84:87], v[36:39], a[200:203], v[84:87]\n\t" "v_cvt_pk_bf16_f32 v113, v98, v99\n\t" \
    "v_mfma_f32_16x16x32_bf16 v[80:83], v[40:43], a[204:207], v[80:83]\n\t" "v_cvt_pk_bf16_f32 v114, v100, v101\n\t" \
    "v_mfma_f32_16x16x32_bf16 v[84:87], v[44:47], a[204:207], v[84:87]\n\t" "v_cvt_pk_bf16_f32 v115, v102, v103\n\t" \
    "v_mfma_f32_16x16x32_bf16 v[88:91], v[16:19], a[208:211], 0\n\t" "v_cvt_pk_bf16_f32 v116, v104, v105\n\t" "v_mfma_f32_16x16x32_bf16 v[92:95], v[20:23], a[208:211], 0\n\t" \
    "v_cvt_pk_bf16_f32 v117, v106, v107\n\t" "v_mfma_f32_16x16x32_bf16 v[88:91], v[24:27], a[212:215], v[88:91]\n\t" \
    "buffer_store_dwordx4 v[112:115], %[z1], %[rs], s63 offen sc1\n\t" "v_mfma_f32_16x16x32_bf16 v[92:95], v[28:31], a[212:215], v[92:95]\n\t" \
    "v_mfma_f32_16x16x32_bf16 v[88:91], v[32:35], a[216:219], v[88:91]\n\t" "v_cvt_pk_bf16_f32 v118, v108, v109\n\t" \
    "v_mfma_f32_16x16x32_bf16 v[92:95], v[36:39], a[216:219], v[92:95]\n\t" "v_cvt_pk_bf16_f32 v119, v110, v111\n\t" \
    "v_mfma_f32_16x16x32_bf16 v[88:91], v[40:43], a[220:223], v[88:91]\n\t" "buffer_store_dwordx4 v[116:119], %[z2], %[rs], s63 offen sc1\n\t" \
    "v_mfma_f32_16x16x32_bf16 v[92:95], v[44:47], a[220:223], v[92:95]\n\t" "s_add_u32 s63, s63, 0x20000\n\t" "v_mfma_f32_16x16x32_bf16 v[96:99], v[16:19], a[224:227], 0\n\t" \
    "v_mfma_f32_16x16x32_bf16 v[100:103], v[20:23], a[224:227], 0\n\t" "v_mfma_f32_16x16x32_bf16 v[96:99], v[24:27], a[228:231], v[96:99]\n\t" \
    "v_mfma_f32_16x16x32_bf16 v[100:103], v[28:31], a[228:231], v[100:103]\n\t" "v_mfma_f32_16x16x32_bf16 v[96:99], v[32:35], a[232:235], v[96:99]\n\t" \
    "v_cvt_pk_bf16_f32 v112, v80, v81\n\t" "v_mfma_f32_16x16x32_bf16 v[100:103], v[36:39], a[232:235], v[100:103]\n\t" "v_cvt_pk_bf16_f32 v113, v82, v83\n\t" \
    "v_mfma_f32_16x16x32_bf16 v[96:99], v[40:43], a[236:239], v[96:99]\n\t" "v_cvt_pk_bf16_f32 v114, v84, v85\n\t" \
    "v_mfma_f32_16x16x32_bf16 v[100:103], v[44:47], a[236:239], v[100:103]\n\t" "v_cvt_pk_bf16_f32 v115, v86, v87\n\t" \
    "v_mfma_f32_16x16x32_bf16 v[104:107], v[16:19], a[240:243], 0\n\t" "v_cvt_pk_bf16_f32 v116, v88, v89\n\t" \
    "v_mfma_f32_16x16x32_bf16 v[108:111], v[20:23], a[240:243], 0\n\t" "v_cvt_pk_bf16_f32 v117, v90, v91\n\t" \
    "v_mfma_f32_16x16x32_bf16 v[104:107], v[24:27], a[244:247], v[104:107]\n\t" "buffer_store_dwordx4 v[112:115], %[z1], %[rs], s63 offen sc1\n\t" \
    "v_mfma_f32_16x16x32_bf16 v[108:111], v[28:31], a[244:247], v[108:111]\n\t" "v_mfma_f32_16x16x32_bf16 v[104:107], v[32:35], a[248:251], v[104:107]\n\t" \
    "v_cvt_pk_bf16_f32 v118, v92, v93\n\t" "v_mfma_f32_16x16x32_bf16 v[108:111], v[36:39], a[248:251], v[108:111]\n\t" "v_cvt_pk_bf16_f32 v119, v94, v95\n\t" \
    "v_mfma_f32_16x16x32_bf16 v[104:107], v[40:43], a[252:255], v[104:107]\n\t" "buffer_store_dwordx4 v[116:119], %[z2], %[rs], s63 offen sc1\n\t" \
    "s_add_u32 s63, s63, 0x20000\n\t" "s_add_u32 s60, s60, 1\n\t" "v_mfma_f32_16x16x32_bf16 v[108:111], v[44:47], a[252:255], v[108:111]\n\t" "s_cmp_eq_u32 s65, 0\n\t" \
    "s_waitcnt vmcnt(6)\n\t" ZSUM "s_cbranch_scc0 23b\n\t" "s_branch 20f\n\t" "41:\n\t" "v_mfma_f32_16x16x32_bf16 v[80:83], v[48:51], a[0:3], 0\n\t" \
    "v_mfma_f32_16x16x32_bf16 v[84:87], v[52:55], a[0:3], 0\n\t" "v_mfma_f32_16x16x32_bf16 v[80:83], v[56:59], a[4:7], v[80:83]\n\t" \
    "v_mfma_f32_16x16x32_bf16 v[84:87], v[60:63], a[4:7], v[84:87]\n\t" "v_mfma_f32_16x16x32_bf16 v[80:83], v[64:67], a[8:11], v[80:83]\n\t" \
    "v_cvt_pk_bf16_f32 v112, v96, v97\n\t" "v_mfma_f32_16x16x32_bf16 v[84:87], v[68:71], a[8:11], v[84:87]\n\t" "v_cvt_pk_bf16_f32 v113, v98, v99\n\t" \
    "v_mfma_f32_16x16x32_bf16 v[80:83], v[72:75], a[12:15], v[80:83]\n\t" "v_cvt_pk_bf16_f32 v114, v100, v101\n\t" \
    "v_mfma_f32_16x16x32_bf16 v[84:87], v[76:79], a[12:15], v[84:87]\n\t" "v_cvt_pk_bf16_f32 v115, v102, v103\n\t" \
    "v_mfma_f32_16x16x32_bf16 v[88:91], v[48:51], a[16:19], 0\n\t" "v_cvt_pk_bf16_f32 v116, v104, v105\n\t" "v_mfma_f32_16x16x32_bf16 v[92:95], v[52:55], a[16:19], 0\n\t" \
    "v_cvt_pk_bf16_f32 v117, v106, v107\n\t" "v_mfma_f32_16x16x32_bf16 v[88:91], v[56:59], a[20:23], v[88:91]\n\t" \
    "buffer_store_dwordx4 v[112:115], %[z1], %[rs], s63 offen sc1\n\t" "v_mfma_f32_16x16x32_bf16 v[92:95], v[60:63], a[20:23], v[92:95]\n\t" \
    "v_mfma_f32_16x16x32_bf16 v[88:91], v[64:67], a[24:27], v[88:91]\n\t" "v_cvt_pk_bf16_f32 v118, v108, v109\n\t" \
    "v_mfma_f32_16x16x32_bf16 v[92:95], v[68:71], a[24:27], v[92:95]\n\t" "v_cvt_pk_bf16_f32 v119, v110, v111\n\t" \
    "v_mfma_f32_16x16x32_bf16 v[88:91], v[72:75], a[28:31], v[88:91]\n\t" "buffer_store_dwordx4 v[116:119], %[z2], %[rs], s63 offen sc1\n\t" "s_and_b32 s63, s60, %[rm]\n\t" \
    "v_mfma_f32_16x16x32_bf16 v[92:95], v[76:79], a[28:31], v[92:95]\n\t" "s_lshl_b32 s63, s63, 22\n\t" "s_add_u32 s63, s63, %[zb]\n\t" \
    "v_mfma_f32_16x16x32_bf16 v[96:99], v[48:51], a[32:35], 0\n\t" "buffer_load_dword v120, %[fo], %[rc], %[fv] offen sc1\n\t" \
    "v_mfma_f32_16x16x32_bf16 v[100:103], v[52:55], a[32:35], 0\n\t" "v_mfma_f32_16x16x32_bf16 v[96:99], v[56:59], a[36:39], v[96:99]\n\t" \
    "buffer_load_dword v121, %[fo], %[rc], %[fs] offen sc1\n\t" "v_mfma_f32_16x16x32_bf16 v[100:103], v[60:63], a[36:39], v[100:103]\n\t" \
    "v_mfma_f32_16x16x32_bf16 v[96:99], v[64:67], a[40:43], v[96:99]\n\t" "v_cvt_pk_bf16_f32 v112, v80, v81\n\t" \
    "v_mfma_f32_16x16x32_bf16 v[100:103], v[68:71], a[40:43], v[100:103]\n\t" "v_cvt_pk_bf16_f32 v113, v82, v83\n\t" \
    "v_mfma_f32_16x16x32_bf16 v[96:99], v[72:75], a[44:47], v[96:99]\n\t" "v_cvt_pk_bf16_f32 v114, v84, v85\n\t" \
    "v_mfma_f32_16x16x32_bf16 v[100:103], v[76:79], a[44:47], v[100:103]\n\t" "v_cvt_pk_bf16_f32 v115, v86, v87\n\t" \
    "v_mfma_f32_16x16x32_bf16 v[104:107], v[48:51], a[48:51], 0\n\t" "v_cvt_pk_bf16_f32 v116, v88, v89\n\t" "v_mfma_f32_16x16x32_bf16 v[108:111], v[52:55], a[48:51], 0\n\t" \
    "v_cvt_pk_bf16_f32 v117, v90, v91\n\t" "v_mfma_f32_16x16x32_bf16 v[104:107], v[56:59], a[52:55], v[104:107]\n\t" \
    "buffer_store_dwordx4 v[112:115], %[z1], %[rs], s63 offen sc1\n\t" "v_mfma_f32_16x16x32_bf16 v[108:111], v[60:63], a[52:55], v[108:111]\n\t" \
    "v_mfma_f32_16x16x32_bf16 v[104:107], v[64:67], a[56:59], v[104:107]\n\t" "v_cvt_pk_bf16_f32 v118, v92, v93\n\t" \
    "v_mfma_f32_16x16x32_bf16 v[108:111], v[68:71], a[56:59], v[108:111]\n\t" "v_cvt_pk_bf16_f32 v119, v94, v95\n\t" \
    "v_mfma_f32_16x16x32_bf16 v[104:107], v[72:75], a[60:63], v[104:107]\n\t" "buffer_store_dwordx4 v[116:119], %[z2], %[rs], s63 offen sc1\n\t" \
    "v_mfma_f32_16x16x32_bf16 v[108:111], v[76:79], a[60:63], v[108:111]\n\t" "s_add_u32 s63, s63, 0x20000\n\t" "v_mfma_f32_16x16x32_bf16 v[80:83], v[48:51], a[64:67], 0\n\t" \
    "s_add_u32 s65, s60, 1\n\t" "s_lshr_b32 s66, s65, 2\n\t" "v_mfma_f32_16x16x32_bf16 v[84:87], v[52:55], a[64:67], 0\n\t" "s_xor_b32 s66, s66, %[xp]\n\t" \
    "s_and_b32 s66, s66, 1\n\t" "v_mfma_f32_16x16x32_bf16 v[80:83], v[56:59], a[68:71], v[80:83]\n\t" "v_mfma_f32_16x16x32_bf16 v[84:87], v[60:63], a[68:71], v[84:87]\n\t" \
    "v_mfma_f32_16x16x32_bf16 v[80:83], v[64:67], a[72:75], v[80:83]\n\t" "v_cvt_pk_bf16_f32 v112, v96, v97\n\t" \
    "v_mfma_f32_16x16x32_bf16 v[84:87], v[68:71], a[72:75], v[84:87]\n\t" "v_cvt_pk_bf16_f32 v113, v98, v99\n\t" \
    "v_mfma_f32_16x16x32_bf16 v[80:83], v[72:75], a[76:79], v[80:83]\n\t" "v_cvt_pk_bf16_f32 v114, v100, v101\n\t" \
    "v_mfma_f32_16x16x32_bf16 v[84:87], v[76:79], a[76:79], v[84:87]\n\t" "v_cvt_pk_bf16_f32 v115, v102, v103\n\t" \
    "v_mfma_f32_16x16x32_bf16 v[88:91], v[48:51], a[80:83], 0\n\t" "v_cvt_pk_bf16_f32 v116, v104, v105\n\t" "v_mfma_f32_16x16x32_bf16 v[92:95], v[52:55], a[80:83], 0\n\t" \
    "v_cvt_pk_bf16_f32 v117, v106, v107\n\t" "v_mfma_f32_16x16x32_bf16 v[88:91], v[56:59], a[84:87], v[88:91]\n\t" \
    "buffer_store_dwordx4 v[112:115], %[z1], %[rs], s63 offen sc1\n\t" "v_mfma_f32_16x16x32_bf16 v[92:95], v[60:63], a[84:87], v[92:95]\n\t" \
    "v_mfma_f32_16x16x32_bf16 v[88:91], v[64:67], a[88:91], v[88:91]\n\t" "v_cvt_pk_bf16_f32 v118, v108, v109\n\t" \
    "v_mfma_f32_16x16x32_bf16 v[92:95], v[68:71], a[88:91], v[92:95]\n\t" "v_cvt_pk_bf16_f32 v119, v110, v111\n\t" \
    "v_mfma_f32_16x16x32_bf16 v[88:91], v[72:75], a[92:95], v[88:91]\n\t" "buffer_store_dwordx4 v[116:119], %[z2], %[rs], s63 offen sc1\n\t" \
    "v_mfma_f32_16x16x32_bf16 v[92:95], v[76:79], a[92:95], v[92:95]\n\t" "s_add_u32 s63, s63, 0x20000\n\t" "v_mfma_f32_16x16x32_bf16 v[96:99], v[48:51], a[96:99], 0\n\t" \
    "s_lshl_b32 s66, s66, 13\n\t" "s_lshl_b32 s64, s65, 19\n\t" "v_mfma_f32_16x16x32_bf16 v[100:103], v[52:55], a[96:99], 0\n\t" "s_add_u32 s64, s64, s66\n\t" \
    "s_add_u32 s64, s64, %[vb]\n\t" "v_mfma_f32_16x16x32_bf16 v[96:99], v[56:59], a[100:103], v[96:99]\n\t" \
    "v_mfma_f32_16x16x32_bf16 v[100:103], v[60:63], a[100:103], v[100:103]\n\t" "v_mfma_f32_16x16x32_bf16 v[96:99], v[64:67], a[104:107], v[96:99]\n\t" \
    "v_cvt_pk_bf16_f32 v112, v80, v81\n\t" "v_mfma_f32_16x16x32_bf16 v[100:103], v[68:71], a[104:107], v[100:103]\n\t" "v_cvt_pk_bf16_f32 v113, v82, v83\n\t" \
    "v_mfma_f32_16x16x32_bf16 v[96:99], v[72:75], a[108:111], v[96:99]\n\t" "v_cvt_pk_bf16_f32 v114, v84, v85\n\t" \
    "v_mfma_f32_16x16x32_bf16 v[100:103], v[76:79], a[108:111], v[100:103]\n\t" "v_cvt_pk_bf16_f32 v115, v86, v87\n\t" \
    "v_mfma_f32_16x16x32_bf16 v[104:107], v[48:51], a[112:115], 0\n\t" "v_cvt_pk_bf16_f32 v116, v88, v89\n\t" \
    "v_mfma_f32_16x16x32_bf16 v[108:111], v[52:55], a[112:115], 0\n\t" "v_cvt_pk_bf16_f32 v117, v90, v91\n\t" \
    "v_mfma_f32_16x16x32_bf16 v[104:107], v[56:59], a[116:119], v[104:107]\n\t" "buffer_store_dwordx4 v[112:115], %[z1], %[rs], s63 offen sc1\n\t" \
    "v_mfma_f32_16x16x32_bf16 v[108:111], v[60:63], a[116:119], v[108:111]\n\t" "v_mfma_f32_16x16x32_bf16 v[104:107], v[64:67], a[120:123], v[104:107]\n\t" \
    "v_cvt_pk_bf16_f32 v118, v92, v93\n\t" "v_mfma_f32_16x16x32_bf16 v[108:111], v[68:71], a[120:123], v[108:111]\n\t" "v_cvt_pk_bf16_f32 v119, v94, v95\n\t" \
    "v_mfma_f32_16x16x32_bf16 v[104:107], v[72:75], a[124:127], v[104:107]\n\t" "buffer_store_dwordx4 v[116:119], %[z2], %[rs], s63 offen sc1\n\t" \
    "v_mfma_f32_16x16x32_bf16 v[108:111], v[76:79], a[124:127], v[108:111]\n\t" "s_add_u32 s63, s63, 0x20000\n\t" \
    "v_mfma_f32_16x16x32_bf16 v[80:83], v[48:51], a[128:131], 0\n\t" "s_waitcnt vmcnt(6)\n\t" "v_readfirstlane_b32 s66, v120\n\t" "v_readfirstlane_b32 s67, v121\n\t" \
    "v_mfma_f32_16x16x32_bf16 v[84:87], v[52:55], a[128:131], 0\n\t" "v_mov_b32 v122, s60\n\t" "buffer_store_dword v122, %[fo], %[rc], %[fz] offen sc1\n\t" \
    "v_mfma_f32_16x16x32_bf16 v[80:83], v[56:59], a[132:135], v[80:83]\n\t" "s_max_u32 s61, s61, s66\n\t" "s_max_u32 s62, s62, s67\n\t" "s_cmp_gt_u32 s61, s65\n\t" \
    "v_mfma_f32_16x16x32_bf16 v[84:87], v[60:63], a[132:135], v[84:87]\n\t" "s_cselect_b32 s65, 1, 0\n\t" "s_cselect_b32 s64, s64, 0x7f000000\n\t" \
    "v_mfma_f32_16x16x32_bf16 v[80:83], v[64:67], a[136:139], v[80:83]\n\t" "buffer_load_dwordx4 v[16:19], %[ao], %[rs], s64 offen offset:0 sc1\n\t" \
    "v_cvt_pk_bf16_f32 v112, v96, v97\n\t" "v_mfma_f32_16x16x32_bf16 v[84:87], v[68:71], a[136:139], v[84:87]\n\t" "v_cvt_pk_bf16_f32 v113, v98, v99\n\t" \
    "v_mfma_f32_16x16x32_bf16 v[80:83], v[72:75], a[140:143], v[80:83]\n\t" "buffer_load_dwordx4 v[20:23], %[ao], %[rs], s64 offen offset:256 sc1\n\t" \
    "v_cvt_pk_bf16_f32 v114, v100, v101\n\t" "v_mfma_f32_16x16x32_bf16 v[84:87], v[76:79], a[140:143], v[84:87]\n\t" "v_cvt_pk_bf16_f32 v115, v102, v103\n\t" \
    "v_mfma_f32_16x16x32_bf16 v[88:91], v[48:51], a[144:147], 0\n\t" "buffer_load_dwordx4 v[24:27], %[ao], %[rs], s64 offen offset:2048 sc1\n\t" \
    "v_cvt_pk_bf16_f32 v116, v104, v105\n\t" "v_mfma_f32_16x16x32_bf16 v[92:95], v[52:55], a[144:147], 0\n\t" "v_cvt_pk_bf16_f32 v117, v106, v107\n\t" \
    "v_mfma_f32_16x16x32_bf16 v[88:91], v[56:59], a[148:151], v[88:91]\n\t" "buffer_load_dwordx4 v[28:31], %[ao], %[rs], s64 offen offset:2304 sc1\n\t" \
    "v_mfma_f32_16x16x32_bf16 v[92:95], v[60:63], a[148:151], v[92:95]\n\t" "buffer_store_dwordx4 v[112:115], %[z1], %[rs], s63 offen sc1\n\t" \
    "v_mfma_f32_16x16x32_bf16 v[88:91], v[64:67], a[152:155], v[88:91]\n\t" "buffer_load_dwordx4 v[32:35], %[a1], %[rs], s64 offen offset:0 sc1\n\t" \
    "v_cvt_pk_bf16_f32 v118, v108, v109\n\t" "v_mfma_f32_16x16x32_bf16 v[92:95], v[68:71], a[152:155], v[92:95]\n\t" "v_cvt_pk_bf16_f32 v119, v110, v111\n\t" \
    "v_mfma_f32_16x16x32_bf16 v[88:91], v[72:75], a[156:159], v[88:91]\n\t" "buffer_load_dwordx4 v[36:39], %[a1], %[rs], s64 offen offset:256 sc1\n\t" \
    "buffer_store_dwordx4 v[116:119], %[z2], %[rs], s63 offen sc1\n\t" "v_mfma_f32_16x16x32_bf16 v[92:95], v[76:79], a[156:159], v[92:95]\n\t" \
    "s_add_u32 s63, s63, 0x20000\n\t" "v_mfma_f32_16x16x32_bf16 v[96:99], v[48:51], a[160:163], 0\n\t" \
    "buffer_load_dwordx4 v[40:43], %[a1], %[rs], s64 offen offset:2048 sc1\n\t" "v_mfma_f32_16x16x32_bf16 v[100:103], v[52:55], a[160:163], 0\n\t" \
    "v_mfma_f32_16x16x32_bf16 v[96:99], v[56:59], a[164:167], v[96:99]\n\t" "buffer_load_dwordx4 v[44:47], %[a1], %[rs], s64 offen offset:2304 sc1\n\t" \
    "v_mfma_f32_16x16x32_bf16 v[100:103], v[60:63], a[164:167], v[100:103]\n\t" "v_mfma_f32_16x16x32_bf16 v[96:99], v[64:67], a[168:171], v[96:99]\n\t" \
    "v_cvt_pk_bf16_f32 v112, v80, v81\n\t" "v_mfma_f32_16x16x32_bf16 v[100:103], v[68:71], a[168:171], v[100:103]\n\t" "v_cvt_pk_bf16_f32 v113, v82, v83\n\t" \
    "v_mfma_f32_16x16x32_bf16 v[96:99], v[72:75], a[172:175], v[96:99]\n\t" "v_cvt_pk_bf16_f32 v114, v84, v85\n\t" \
    "v_mfma_f32_16x16x32_bf16 v[100:103], v[76:79], a[172:175], v[100:103]\n\t" "v_cvt_pk_bf16_f32 v115, v86, v87\n\t" \
    "v_mfma_f32_16x16x32_bf16 v[104:107], v[48:51], a[176:179], 0\n\t" "v_cvt_pk_bf16_f32 v116, v88, v89\n\t" \
    "v_mfma_f32_16x16x32_bf16 v[108:111], v[52:55], a[176:179], 0\n\t" "v_cvt_pk_bf16_f32 v117, v90, v91\n\t" \
    "v_mfma_f32_16x16x32_bf16 v[104:107], v[56:59], a[180:183], v[104:107]\n\t" "buffer_store_dwordx4 v[112:115], %[z1], %[rs], s63 offen sc1\n\t" \
    "v_mfma_f32_16x16x32_bf16 v[108:111], v[60:63], a[180:183], v[108:111]\n\t" "v_mfma_f32_16x16x32_bf16 v[104:107], v[64:67], a[184:187], v[104:107]\n\t" \
    "v_cvt_pk_bf16_f32 v118, v92, v93\n\t" "v_mfma_f32_16x16x32_bf16 v[108:111], v[68:71], a[184:187], v[108:111]\n\t" "v_cvt_pk_bf16_f32 v119, v94, v95\n\t" \
    "v_mfma_f32_16x16x32_bf16 v[104:107], v[72:75], a[188:191], v[104:107]\n\t" "buffer_store_dwordx4 v[116:119], %[z2], %[rs], s63 offen sc1\n\t" \
    "v_mfma_f32_16x16x32_bf16 v[108:111], v[76:79], a[188:191], v[108:111]\n\t" "s_add_u32 s63, s63, 0x20000\n\t" \
    "v_mfma_f32_16x16x32_bf16 v[80:83], v[48:51], a[192:195], 0\n\t" "v_mfma_f32_16x16x32_bf16 v[84:87], v[52:55], a[192:195], 0\n\t" \
    "v_mfma_f32_16x16x32_bf16 v[80:83], v[56:59], a[196:199], v[80:83]\n\t" "v_mfma_f32_16x16x32_bf16 v[84:87], v[60:63], a[196:199], v[84:87]\n\t" \
    "v_mfma_f32_16x16x32_bf16 v[80:83], v[64:67], a[200:203], v[80:83]\n\t" "v_cvt_pk_bf16_f32 v112, v96, v97\n\t" \
    "v_mfma_f32_16x16x32_bf16 v[84:87], v[68:71], a[200:203], v[84:87]\n\t" "v_cvt_pk_bf16_f32 v113, v98, v99\n\t" \
    "v_mfma_f32_16x16x32_bf16 v[80:83], v[72:75], a[204:207], v[80:83]\n\t" "v_cvt_pk_bf16_f32 v114, v100, v101\n\t" \
    "v_mfma_f32_16x16x32_bf16 v[84:87], v[76:79], a[204:207], v[84:87]\n\t" "v_cvt_pk_bf16_f32 v115, v102, v103\n\t" \
    "v_mfma_f32_16x16x32_bf16 v[88:91], v[48:51], a[208:211], 0\n\t" "v_cvt_pk_bf16_f32 v116, v104, v105\n\t" "v_mfma_f32_16x16x32_bf16 v[92:95], v[52:55], a[208:211], 0\n\t" \
    "v_cvt_pk_bf16_f32 v117, v106, v107\n\t" "v_mfma_f32_16x16x32_bf16 v[88:91], v[56:59], a[212:215], v[88:91]\n\t" \
    "buffer_store_dwordx4 v[112:115], %[z1], %[rs], s63 offen sc1\n\t" "v_mfma_f32_16x16x32_bf16 v[92:95], v[60:63], a[212:215], v[92:95]\n\t" \
    "v_mfma_f32_16x16x32_bf16 v[88:91], v[64:67], a[216:219], v[88:91]\n\t" "v_cvt_pk_bf16_f32 v118, v108, v109\n\t" \
    "v_mfma_f32_16x16x32_bf16 v[92:95], v[68:71], a[216:219], v[92:95]\n\t" "v_cvt_pk_bf16_f32 v119, v110, v111\n\t" \
    "v_mfma_f32_16x16x32_bf16 v[88:91], v[72:75], a[220:223], v[88:91]\n\t" "buffer_store_dwordx4 v[116:119], %[z2], %[rs], s63 offen sc1\n\t" \
    "v_mfma_f32_16x16x32_bf16 v[92:95], v[76:79], a[220:223], v[92:95]\n\t" "s_add_u32 s63, s63, 0x20000\n\t" "v_mfma_f32_16x16x32_bf16 v[96:99], v[48:51], a[224:227], 0\n\t" \
    "v_mfma_f32_16x16x32_bf16 v[100:103], v[52:55], a[224:227], 0\n\t" "v_mfma_f32_16x16x32_bf16 v[96:99], v[56:59], a[228:231], v[96:99]\n\t" \
    "v_mfma_f32_16x16x32_bf16 v[100:103], v[60:63], a[228:231], v[100:103]\n\t" "v_mfma_f32_16x16x32_bf16 v[96:99], v[64:67], a[232:235], v[96:99]\n\t" \
    "v_cvt_pk_bf16_f32 v112, v80, v81\n\t" "v_mfma_f32_16x16x32_bf16 v[100:103], v[68:71], a[232:235], v[100:103]\n\t" "v_cvt_pk_bf16_f32 v113, v82, v83\n\t" \
    "v_mfma_f32_16x16x32_bf16 v[96:99], v[72:75], a[236:239], v[96:99]\n\t" "v_cvt_pk_bf16_f32 v114, v84, v85\n\t" \
    "v_mfma_f32_16x16x32_bf16 v[100:103], v[76:79], a[236:239], v[100:103]\n\t" "v_cvt_pk_bf16_f32 v115, v86, v87\n\t" \
    "v_mfma_f32_16x16x32_bf16 v[104:107], v[48:51], a[240:243], 0\n\t" "v_cvt_pk_bf16_f32 v116, v88, v89\n\t" \
    "v_mfma_f32_16x16x32_bf16 v[108:111], v[52:55], a[240:243], 0\n\t" "v_cvt_pk_bf16_f32 v117, v90, v91\n\t" \
    "v_mfma_f32_16x16x32_bf16 v[104:107], v[56:59], a[244:247], v[104:107]\n\t" "buffer_store_dwordx4 v[112:115], %[z1], %[rs], s63 offen sc1\n\t" \
    "v_mfma_f32_16x16x32_bf16 v[108:111], v[60:63], a[244:247], v[108:111]\n\t" "v_mfma_f32_16x16x32_bf16 v[104:107], v[64:67], a[248:251], v[104:107]\n\t" \
    "v_cvt_pk_bf16_f32 v118, v92, v93\n\t" "v_mfma_f32_16x16x32_bf16 v[108:111], v[68:71], a[248:251], v[108:111]\n\t" "v_cvt_pk_bf16_f32 v119, v94, v95\n\t" \
    "v_mfma_f32_16x16x32_bf16 v[104:107], v[72:75], a[252:255], v[104:107]\n\t" "buffer_store_dwordx4 v[116:119], %[z2], %[rs], s63 offen sc1\n\t" \
    "s_add_u32 s63, s63, 0x20000\n\t" "s_add_u32 s60, s60, 1\n\t" "v_mfma_f32_16x16x32_bf16 v[108:111], v[76:79], a[252:255], v[108:111]\n\t" "s_cmp_eq_u32 s65, 0\n\t" \
    "s_waitcnt vmcnt(6)\n\t" ZSUM "s_cbranch_scc0 33b\n\t" "s_branch 30f\n\t" "20:\n\t" "s_nop 7\n\t" "v_cvt_pk_bf16_f32 v112, v96, v97\n\t" \
    "v_cvt_pk_bf16_f32 v113, v98, v99\n\t" "v_cvt_pk_bf16_f32 v114, v100, v101\n\t" "v_cvt_pk_bf16_f32 v115, v102, v103\n\t" "v_cvt_pk_bf16_f32 v116, v104, v105\n\t" \
    "v_cvt_pk_bf16_f32 v117, v106, v107\n\t" "v_cvt_pk_bf16_f32 v118, v108, v109\n\t" "v_cvt_pk_bf16_f32 v119, v110, v111\n\t" \
    "buffer_store_dwordx4 v[112:115], %[z1], %[rs], s63 offen sc1\n\t" "buffer_store_dwordx4 v[116:119], %[z2], %[rs], s63 offen sc1\n\t" \
    "buffer_load_dword v120, %[fo], %[rc], %[fv] offen sc1\n\t" "s_waitcnt vmcnt(0)\n\t" "v_readfirstlane_b32 s61, v120\n\t" "s_lshr_b32 s66, s60, 2\n\t" \
    "s_xor_b32 s66, s66, %[xp]\n\t" "s_and_b32 s66, s66, 1\n\t" "s_lshl_b32 s66, s66, 13\n\t" "s_lshl_b32 s64, s60, 19\n\t" "s_add_u32 s64, s64, s66\n\t" \
    "s_add_u32 s64, s64, %[vb]\n\t" "s_cmp_gt_u32 s61, s60\n\t" "s_cbranch_scc0 24f\n\t" "s_cmp_eq_u32 s60, 64\n\t" "s_cbranch_scc1 24f\n\t" \
    "buffer_load_dwordx4 v[48:51], %[ao], %[rs], s64 offen offset:0 sc1\n\t" "buffer_load_dwordx4 v[52:55], %[ao], %[rs], s64 offen offset:256 sc1\n\t" \
    "buffer_load_dwordx4 v[56:59], %[ao], %[rs], s64 offen offset:2048 sc1\n\t" "buffer_load_dwordx4 v[60:63], %[ao], %[rs], s64 offen offset:2304 sc1\n\t" \
    "buffer_load_dwordx4 v[64:67], %[a1], %[rs], s64 offen offset:0 sc1\n\t" "buffer_load_dwordx4 v[68:71], %[a1], %[rs], s64 offen offset:256 sc1\n\t" \
    "buffer_load_dwordx4 v[72:75], %[a1], %[rs], s64 offen offset:2048 sc1\n\t" "buffer_load_dwordx4 v[76:79], %[a1], %[rs], s64 offen offset:2304 sc1\n\t" \
    "v_mov_b32 v122, s60\n\t" "buffer_store_dword v122, %[fo], %[rc], %[fz] offen sc1\n\t" "s_waitcnt vmcnt(1)\n\t" "s_branch 23b\n\t" "24:\n\t" "v_mov_b32 v122, s60\n\t" \
    "buffer_store_dword v122, %[fo], %[rc], %[fz] offen sc1\n\t" "s_cmp_eq_u32 s60, 64\n\t" "s_cbranch_scc1 90f\n\t" "s_add_u32 s67, s60, 1\n\t" "s_cmp_ge_u32 s61, s67\n\t" \
    "s_cbranch_scc1 22f\n\t" "21:\n\t" "buffer_load_dword v120, %[fo], %[rc], %[fv] offen sc1\n\t" "buffer_load_dword v121, %[fo], %[rc], %[vd] offen sc1\n\t" \
    "s_waitcnt vmcnt(0)\n\t" "v_readfirstlane_b32 s61, v120\n\t" "v_readfirstlane_b32 s66, v121\n\t" "s_nop 3\n\t" "s_cmp_ge_u32 s61, s67\n\t" "s_cbranch_scc1 22f\n\t" \
    "s_bitcmp1_b32 s66, 1\n\t" "s_cbranch_scc1 90f\n\t" "s_cmp_eq_u32 %[bd], 0\n\t" "s_cbranch_scc1 22f\n\t" "s_sub_u32 %[bd], %[bd], 1\n\t" "s_sleep 2\n\t" \
    "s_branch 21b\n\t" "22:\n\t" "s_lshr_b32 s66, s60, 2\n\t" "s_xor_b32 s66, s66, %[xp]\n\t" "s_and_b32 s66, s66, 1\n\t" "s_lshl_b32 s66, s66, 13\n\t" \
    "s_lshl_b32 s64, s60, 19\n\t" "s_add_u32 s64, s64, s66\n\t" "s_add_u32 s64, s64, %[vb]\n\t" "buffer_load_dwordx4 v[48:51], %[ao], %[rs], s64 offen offset:0 sc1\n\t" \
    "buffer_load_dwordx4 v[52:55], %[ao], %[rs], s64 offen offset:256 sc1\n\t" "buffer_load_dwordx4 v[56:59], %[ao], %[rs], s64 offen offset:2048 sc1\n\t" \
    "buffer_load_dwordx4 v[60:63], %[ao], %[rs], s64 offen offset:2304 sc1\n\t" "buffer_load_dwordx4 v[64:67], %[a1], %[rs], s64 offen offset:0 sc1\n\t" \
    "buffer_load_dwordx4 v[68:71], %[a1], %[rs], s64 offen offset:256 sc1\n\t" "buffer_load_dwordx4 v[72:75], %[a1], %[rs], s64 offen offset:2048 sc1\n\t" \
    "buffer_load_dwordx4 v[76:79], %[a1], %[rs], s64 offen offset:2304 sc1\n\t" "s_waitcnt vmcnt(0)\n\t" "s_branch 23b\n\t" "60:\n\t" "s_sub_u32 s67, s60, %[rg]\n\t" \
    "s_cmp_ge_u32 s62, s67\n\t" "s_cbranch_scc1 62f\n\t" "61:\n\t" "buffer_load_dword v120, %[fo], %[rc], %[fs] offen sc1\n\t" \
    "buffer_load_dword v121, %[fo], %[rc], %[vd] offen sc1\n\t" "s_waitcnt vmcnt(0)\n\t" "v_readfirstlane_b32 s62, v120\n\t" "v_readfirstlane_b32 s66, v121\n\t" "s_nop 3\n\t" \
    "s_cmp_ge_u32 s62, s67\n\t" "s_cbranch_scc1 62f\n\t" "s_bitcmp1_b32 s66, 1\n\t" "s_cbranch_scc1 90f\n\t" "s_cmp_eq_u32 %[bd], 0\n\t" "s_cbranch_scc1 62f\n\t" \
    "s_sub_u32 %[bd], %[bd], 1\n\t" "s_sleep 2\n\t" "s_branch 61b\n\t" "62:\n\t" "s_branch 63b\n\t" "30:\n\t" "s_nop 7\n\t" "v_cvt_pk_bf16_f32 v112, v96, v97\n\t" \
    "v_cvt_pk_bf16_f32 v113, v98, v99\n\t" "v_cvt_pk_bf16_f32 v114, v100, v101\n\t" "v_cvt_pk_bf16_f32 v115, v102, v103\n\t" "v_cvt_pk_bf16_f32 v116, v104, v105\n\t" \
    "v_cvt_pk_bf16_f32 v117, v106, v107\n\t" "v_cvt_pk_bf16_f32 v118, v108, v109\n\t" "v_cvt_pk_bf16_f32 v119, v110, v111\n\t" \
    "buffer_store_dwordx4 v[112:115], %[z1], %[rs], s63 offen sc1\n\t" "buffer_store_dwordx4 v[116:119], %[z2], %[rs], s63 offen sc1\n\t" \
    "buffer_load_dword v120, %[fo], %[rc], %[fv] offen sc1\n\t" "s_waitcnt vmcnt(0)\n\t" "v_readfirstlane_b32 s61, v120\n\t" "s_lshr_b32 s66, s60, 2\n\t" \
    "s_xor_b32 s66, s66, %[xp]\n\t" "s_and_b32 s66, s66, 1\n\t" "s_lshl_b32 s66, s66, 13\n\t" "s_lshl_b32 s64, s60, 19\n\t" "s_add_u32 s64, s64, s66\n\t" \
    "s_add_u32 s64, s64, %[vb]\n\t" "s_cmp_gt_u32 s61, s60\n\t" "s_cbranch_scc0 34f\n\t" "s_cmp_eq_u32 s60, 64\n\t" "s_cbranch_scc1 34f\n\t" \
    "buffer_load_dwordx4 v[16:19], %[ao], %[rs], s64 offen offset:0 sc1\n\t" "buffer_load_dwordx4 v[20:23], %[ao], %[rs], s64 offen offset:256 sc1\n\t" \
    "buffer_load_dwordx4 v[24:27], %[ao], %[rs], s64 offen offset:2048 sc1\n\t" "buffer_load_dwordx4 v[28:31], %[ao], %[rs], s64 offen offset:2304 sc1\n\t" \
    "buffer_load_dwordx4 v[32:35], %[a1], %[rs], s64 offen offset:0 sc1\n\t" "buffer_load_dwordx4 v[36:39], %[a1], %[rs], s64 offen offset:256 sc1\n\t" \
    "buffer_load_dwordx4 v[40:43], %[a1], %[rs], s64 offen offset:2048 sc1\n\t" "buffer_load_dwordx4 v[44:47], %[a1], %[rs], s64 offen offset:2304 sc1\n\t" \
    "v_mov_b32 v122, s60\n\t" "buffer_store_dword v122, %[fo], %[rc], %[fz] offen sc1\n\t" "s_waitcnt vmcnt(1)\n\t" "s_branch 33b\n\t" "34:\n\t" "v_mov_b32 v122, s60\n\t" \
    "buffer_store_dword v122, %[fo], %[rc], %[fz] offen sc1\n\t" "s_cmp_eq_u32 s60, 64\n\t" "s_cbranch_scc1 90f\n\t" "s_add_u32 s67, s60, 1\n\t" "s_cmp_ge_u32 s61, s67\n\t" \
    "s_cbranch_scc1 32f\n\t" "31:\n\t" "buffer_load_dword v120, %[fo], %[rc], %[fv] offen sc1\n\t" "buffer_load_dword v121, %[fo], %[rc], %[vd] offen sc1\n\t" \
    "s_waitcnt vmcnt(0)\n\t" "v_readfirstlane_b32 s61, v120\n\t" "v_readfirstlane_b32 s66, v121\n\t" "s_nop 3\n\t" "s_cmp_ge_u32 s61, s67\n\t" "s_cbranch_scc1 32f\n\t" \
    "s_bitcmp1_b32 s66, 1\n\t" "s_cbranch_scc1 90f\n\t" "s_cmp_eq_u32 %[bd], 0\n\t" "s_cbranch_scc1 32f\n\t" "s_sub_u32 %[bd], %[bd], 1\n\t" "s_sleep 2\n\t" \
    "s_branch 31b\n\t" "32:\n\t" "s_lshr_b32 s66, s60, 2\n\t" "s_xor_b32 s66, s66, %[xp]\n\t" "s_and_b32 s66, s66, 1\n\t" "s_lshl_b32 s66, s66, 13\n\t" \
    "s_lshl_b32 s64, s60, 19\n\t" "s_add_u32 s64, s64, s66\n\t" "s_add_u32 s64, s64, %[vb]\n\t" "buffer_load_dwordx4 v[16:19], %[ao], %[rs], s64 offen offset:0 sc1\n\t" \
    "buffer_load_dwordx4 v[20:23], %[ao], %[rs], s64 offen offset:256 sc1\n\t" "buffer_load_dwordx4 v[24:27], %[ao], %[rs], s64 offen offset:2048 sc1\n\t" \
    "buffer_load_dwordx4 v[28:31], %[ao], %[rs], s64 offen offset:2304 sc1\n\t" "buffer_load_dwordx4 v[32:35], %[a1], %[rs], s64 offen offset:0 sc1\n\t" \
    "buffer_load_dwordx4 v[36:39], %[a1], %[rs], s64 offen offset:256 sc1\n\t" "buffer_load_dwordx4 v[40:43], %[a1], %[rs], s64 offen offset:2048 sc1\n\t" \
    "buffer_load_dwordx4 v[44:47], %[a1], %[rs], s64 offen offset:2304 sc1\n\t" "s_waitcnt vmcnt(0)\n\t" "s_branch 33b\n\t" "70:\n\t" "s_sub_u32 s67, s60, %[rg]\n\t" \
    "s_cmp_ge_u32 s62, s67\n\t" "s_cbranch_scc1 72f\n\t" "71:\n\t" "buffer_load_dword v120, %[fo], %[rc], %[fs] offen sc1\n\t" \
    "buffer_load_dword v121, %[fo], %[rc], %[vd] offen sc1\n\t" "s_waitcnt vmcnt(0)\n\t" "v_readfirstlane_b32 s62, v120\n\t" "v_readfirstlane_b32 s66, v121\n\t" "s_nop 3\n\t" \
    "s_cmp_ge_u32 s62, s67\n\t" "s_cbranch_scc1 72f\n\t" "s_bitcmp1_b32 s66, 1\n\t" "s_cbranch_scc1 90f\n\t" "s_cmp_eq_u32 %[bd], 0\n\t" "s_cbranch_scc1 72f\n\t" \
    "s_sub_u32 %[bd], %[bd], 1\n\t" "s_sleep 2\n\t" "s_branch 71b\n\t" "72:\n\t" "s_branch 73b\n\t" "90:\n\t"

#define T_DQ 1390
#define T_DV 1290
#define T_RQ 700
#define T_RV 640
#define T_M1 100
#define T_DY 580
#define T_DX 1240
#define T_MIN 35
#define T_PEN 280
#define T_SAFE 80
#define T_RETRY 700
#define T_POLL 1
#define T_KS 0
#define T_TS 0
#define T_CLR 250
#define T_VLAG 0
#define T_U0 340
constexpr int T_OVH = 118, T_NEAR = 460;
constexpr unsigned NS(int ns) { return (unsigned)(ns * 64 / 10); }
struct Clk { unsigned cyc, g, pc, pg, k1, k2; };
DEV void mark(Clk& k) {
  unsigned long long c, r[8];
  asm volatile("s_memtime %0\n\ts_memrealtime %1\n\ts_nop 2\n\ts_memrealtime %2\n\ts_nop 2\n\ts_memrealtime %3\n\ts_nop 2\n\ts_memrealtime %4\n\ts_nop 2\n\t"
               "s_memrealtime %5\n\ts_nop 2\n\ts_memrealtime %6\n\ts_nop 2\n\ts_memrealtime %7\n\ts_waitcnt lgkmcnt(0)"
               : "=s"(c), "=s"(r[0]), "=s"(r[1]), "=s"(r[2]), "=s"(r[3]), "=s"(r[4]), "=s"(r[5]), "=s"(r[6]) :: "memory");
  const unsigned a = (unsigned)r[0];
  unsigned n = 0;
#pragma unroll
  for (int i = 1; i < 7; ++i) asm("s_cmp_eq_u32 %1, %2\n\ts_addc_u32 %0, %0, 0" : "+s"(n) : "s"((unsigned)r[i]), "s"(a) : "scc");
  k.g = (a + 4u) << 6; k.cyc = (unsigned)c + 12u + 16u * n;
}
DEV void anchor(Clk& k, bool any = false) {
  mark(k);
  const unsigned dg_ = k.g - k.pg;
  const float dc = (float)(k.cyc - k.pc), dg = (float)dg_;
  unsigned k1, k2;
  asm volatile("s_nop 0\n\tv_readfirstlane_b32 %0, %2\n\tv_readfirstlane_b32 %1, %3\n\ts_nop 3\n\ts_min_u32 %0, %0, 0x80000\n\ts_min_u32 %1, %1, 0x8000"
               : "=&s"(k1), "=&s"(k2) : "v"((unsigned)(dg * __builtin_amdgcn_rcpf(dc) * 65536.f)), "v"((unsigned)(dc * __builtin_amdgcn_rcpf(dg) * 65536.f)) : "scc");
  if (any) { k.k1 = k1; k.k2 = k2; }
  else asm("s_cmp_lt_u32 %4, %5\n\ts_cselect_b32 %2, %2, %0\n\ts_cselect_b32 %3, %3, %1\n\ts_add_u32 %0, %0, %2\n\ts_lshr_b32 %0, %0, 1\n\ts_add_u32 %1, %1, %3\n\ts_lshr_b32 %1, %1, 1"
           : "+s"(k.k1), "+s"(k.k2), "+s"(k1), "+s"(k2) : "s"(dg_ - NS(1400)), "n"(NS(800)) : "scc");
  k.pc = k.cyc; k.pg = k.g;
}
DEV unsigned to_cyc(const Clk& k, unsigned g) {
  unsigned d;
  asm("s_max_i32 %0, %1, 0xffff8000\n\ts_min_i32 %0, %0, 0x8000\n\ts_mul_i32 %0, %0, %2\n\ts_ashr_i32 %0, %0, 16" : "=&s"(d) : "s"(g - k.g), "s"(k.k2) : "scc");
  return k.cyc + d - T_OVH;
}
DEV void wait_cyc(unsigned t) { asm volatile(TIMED_WAIT :: "s"(t) : CL_S); }
DEV void close_in(Clk& k, unsigned t) {
  if ((int)(t - (unsigned)__builtin_amdgcn_s_memtime()) > T_NEAR + 60) { wait_cyc(t - T_NEAR); mark(k); }
}
DEV bool slow_head(const u16* q, int h) { return ((((unsigned)(size_t)q >> 8) + h) & 3) == 1; }
DEV int turn(int h, int w) { return ((h >> 3) ^ (w & 1)) & 3; }
DEV void hold(const Ctx& c, int ticks) {
#pragma unroll 1
  while ((unsigned)__builtin_amdgcn_s_memrealtime() - c.t0 < (unsigned)ticks) __builtin_amdgcn_s_sleep(4);
}
DEV int z_mix(int y) { return (y >> 4) | ((y & 1) << 2); }
DEV int u_slot(int s, int para) { return R_U + (s & (URING == 64 ? 127 : URING - 1)) * MB + ((para ^ (s >> 1)) & 1) * 8192; }
DEV unsigned ask_stamps(Ctx& c, int par, int slot) { return __builtin_amdgcn_raw_buffer_load_b32(c.rs, amap(24576, par) + fresh_lane() * 128, slot, AUX_SC1); }
DEV unsigned latest(Ctx& c, const Clk& k, int par, int slot, unsigned v) {
  if (__builtin_expect(!allok(v), 0)) {
#pragma unroll 1
    do {
      __builtin_amdgcn_s_sleep(T_POLL);
      asm volatile("" ::: "memory");
      const unsigned t = __builtin_amdgcn_raw_buffer_load_b32(c.rs, v == SENT ? amap(24576, par) + fresh_lane() * 128 : 1 << 30, slot, AUX_SC1);
      v = v == SENT ? t : v;
    } while (!allok(v) && !spend(c));
  }
  int d = (int)(v - k.g);
  d = max(d, __builtin_amdgcn_update_dpp(d, d, 0x128, 0xf, 0xf, true));
  d = max(d, __builtin_amdgcn_update_dpp(d, d, 0x124, 0xf, 0xf, true));
  d = max(d, __builtin_amdgcn_update_dpp(d, d, 0x122, 0xf, 0xf, true));
  d = max(d, __builtin_amdgcn_update_dpp(d, d, 0x121, 0xf, 0xf, true));
  unsigned m;
  asm("s_max_i32 %0, %1, %2\n\ts_max_i32 %0, %0, %3\n\ts_max_i32 %0, %0, %4" : "=&s"(m)
      : "s"(__builtin_amdgcn_readlane(d, 0)), "s"(__builtin_amdgcn_readlane(d, 16)), "s"(__builtin_amdgcn_readlane(d, 32)), "s"(__builtin_amdgcn_readlane(d, 48)) : "scc");
  return k.g + m;
}
struct Plan { unsigned gx, dx; };
DEV unsigned smax(unsigned a, unsigned b) { return (int)(a - b) > 0 ? a : b; }
DEV unsigned when_p(const Plan& t, unsigned last) {
  const unsigned gy = t.gx + t.dx + NS(T_M1);
  return (int)(last + NS(T_MIN) - gy) > 0 ? smax(last + NS(T_PEN), gy + NS(T_RETRY)) : gy;
}
DEV void advance(Plan& t, unsigned last) {
  const unsigned gy = t.gx + t.dx + NS(T_M1), dur = last - t.gx, gp = when_p(t, last);
  const unsigned nx = gp + NS(T_DY) + (gp != gy ? NS(T_SAFE) : 0u);
  t.dx = dur < t.dx ? (7u * t.dx + dur) >> 3 : dur < t.dx + NS(80) ? (t.dx + dur) >> 1 : t.dx + NS(4);
  t.gx = nx;
}
DEV unsigned next_go(Ctx& c, Clk& k, Plan& t, int par, int slot, unsigned v) {
  SCHED();
  anchor(k);
  SCHED();
  advance(t, latest(c, k, par, slot, v));
  return to_cyc(k, t.gx);
}

DEV constexpr int qk_r(int j) { return j < 4 ? 0 : 1 + (j - 4) % 3; }
DEV constexpr int qk_i(int j) { return j < 4 ? j : (j - 4) / 3; }
DEV constexpr int qk_upto(int u) { return u < 6 ? u + 7 : u < 10 ? 12 + (u - 4) / 2 : u < 12 ? 15 : 16; }
DEV void qk_role(const Params& p, Ctx& c, int h) {
  constexpr int WL = 0, MEMS = 131072, KXL = 147456, QKX = 155648, QROW = 272;
  c.budget = SPIN_CAP;
  const int w = c.w;
  Clk k = {0u, 0u, 0u, 0u, 0u, 0u};
  anchor(k, true);
  v8u wa[32], wh[16];
  unsigned look = 0;
  bool claimed = false;
  hold(c, QK_AT - (slow_head(p.Wq, h) ? SLOW_LEAD : 0));
  {
    const int lane = fresh_lane(), g = lane >> 4, cc = lane & 15;
    const rsrc_t rq = mkrs(p.Wq, 1024 * 4096 * 2), rk = mkrs(p.Wk, 1024 * 4096 * 2);
    int vo[4];
#pragma unroll
    for (int i = 0; i < 4; ++i) vo[i] = opq((256 * w + 64 * (i ^ turn(h, w)) + krow_lane(g)) * 8192 + (h * 128 + 8 * cc) * 2);
    v4u row[16][8];
    auto request = [&](int u) {
#pragma unroll
      for (int r = 0; r < 8; ++r) row[u][r] = ld16w(u < 8 ? rq : rk, vo[(u >> 1) & 3], krow(4 * (u & 1) + (r >> 1), r & 1) * 8192);
    };
#pragma unroll
    for (int u = 0; u < qk_upto(-1); ++u) request(u);
    SCHED();
    const int sw = MEMS + w * 8192 + g * 256, lw = opq(WL + w * 32768 + lane * 16);
    int sr[2];
#pragma unroll
    for (int t = 0; t < 2; ++t) { const int i = (2 * cc + t) & 7; sr[t] = opq(i * 1024 + g * 256 + ((4 * w + (cc >> 2) + 2 * i) & 15) * 16); }
    sfor<16>([&](auto U) __attribute__((always_inline)) {
      constexpr int u = U.value;
#pragma unroll
      for (int i = 0; i < 8; ++i) {
        v4u f;
#pragma unroll
        for (int r = 0; r < 4; ++r) f[r] = prm(row[u][2 * r + 1][i >> 1], row[u][2 * r][i >> 1], i & 1);
        sts16(c.L, sw + i * 1024 + ((cc + 2 * i) & 15) * 16, f);
      }
      SCHED();
      if (u == 8) claimed = claim_try(c, look);
      if (u == 2) look = claim_look(c);
      if (qk_upto(u) > qk_upto(u - 1)) request(qk_upto(u - 1));
      if (u == 15 && !claimed) claim(c, 0);
      BAR();
      v4u in[4][2];
#pragma unroll
      for (int t = 1; t >= 0; --t)
#pragma unroll
        for (int r = 0; r < 4; ++r) in[r][t] = lds16(c.L, sr[t] + MEMS + ((w + r) & 3) * 8192);
      SCHED();
#pragma unroll
      for (int j = 0; j < 16; ++j) {
        if (qk_i(j) != ((u >> 1) & 3)) continue;
#pragma unroll
        for (int t = 1; t >= 0; --t) {
          const v4u o = in[qk_r(j)][t];
          if (u >= 8 && t == 1) sts16(c.L, lw + j * 2048 + (u & 1) * 1024, o);
          else {
            v8u& d = u < 8 ? wa[2 * j + t] : wh[j];
#pragma unroll
            for (int r = 0; r < 4; ++r) d[4 * (u & 1) + r] = o[r];
#define M_(r) asm volatile("" : "+" r(d))
            if (u & 1) { if (u < 8) AT_ACC(M_, 2 * j + t); else AT_HOME(M_, j); }
#undef M_
          }
        }
      }
      BAR();
    });
  }
  STAMP(0);
  BAR();
  const unsigned vote = wait_verdict(c);
  const int par = vote_b(vote), para = vote_a(vote);
  const int lane = fresh_lane(), g = lane >> 4, cc = lane & 15;
  const int moff = opq(amap(w * 4096 + lane * 16, par) + (turn(h, w) & 1) * 1024), m2 = R_MEM + (turn(h, w) & 2) * 1024;
  unsigned tries = 1u << 12;
  *(volatile LAS unsigned*)(c.L + 160512) = 0u;
  LGKM(0);
  const int idx = opq((lane & 1) ? 0xEEEE : 0x4444);
  TICK_DECL;
  const int l0 = opq(WL + w * 32768 + lane * 16), l1 = opq(MEMS + lane * 16);
  const int qw = opq(QKX + (2 * w + (cc >> 3)) * 256 + g * 64 + (cc & 1) * 16 + ((cc >> 1) & 3) * 4), kw = opq(QKX + (8 + 2 * g) * QROW + (32 * w + 2 * cc) * 2);
#define QK_PINS PIN_ACC0(wa); PIN_ACC1(wa); PIN_HOME(wh)
#define QK_IN [l0] "v"(l0), [l1] "v"(l1), [mo] "v"(moff), [ix] "v"(idx), [qw] "v"(qw), [kw] "v"(kw), [rs] "s"(c.rs), [sb] "s"(m2 + (s & 3) * MEM_SLOT)
#define QK_CL CL_S, CL_16_32, CL_160_200, CL_200_216, CL_216_256
  if (w) RAWBAR();
  if (w == 3) {
    const int hq = opq(lane ? 1 << 30 : amap(24576 + h * 128, par));
    anchor(k, true);
    unsigned tg = k.cyc;
    Plan tt = {k.g - NS(100000), NS(T_DX)};
    for (int s = 0; s < NSEG; ++s) {
      const int pb = R_P + (s & 3) * P_SLOT;
      QK_PINS;
      asm volatile(QK_STEP3 : [bd] "+s"(c.budget), [rt] "+s"(tries) : QK_IN, [hq] "v"(hq), [pb] "s"(pb), [tg] "s"(tg), [ca] "s"(k.cyc - T_DQ), [k1] "s"(k.k1), [ga] "s"(k.g), [dr] "n"(T_RQ), [pr] "s"(R_P + ((s + 1) & 3) * P_SLOT),
                     [kn] "s"(R_KX - 8 * MEMS + xblk(min(s + 1, NSEG - 1), h, para)) : QK_CL);
      QK_PINS;
      LOG(1, s, tg);
      const unsigned sv = ask_stamps(c, par, pb);
      SCHED();
      anchor(k);
      RAWBAR();
      SCHED();
      advance(tt, latest(c, k, par, pb, sv));
      tg = to_cyc(k, tt.gx);
      LOG(2, s, k.cyc); LOG(3, s, k.g); LOG(4, s, tt.dx);
      st4s(mkrs((const void*)c.ctrl, CTRL_BYTES), hq & (1 << 30), C_FLAG + (F_S + h) * 128, (unsigned)s + 1u);
      TICK_SUM(10);
      close_in(k, tg);
      tg = to_cyc(k, tt.gx);
    }
  } else {
    const int kx0 = opq(KXL + g * 512 + cc * 16), kmrd = opq(QKX + g * 16 + (8 + (cc & 7)) * QROW);
    unsigned seen = NOFLAG;
    if (w == 0) {
      const int prow = (2 * g + (cc & 1)) * 2 + ((cc >> 1) & 1);
      const int px = opq(amap((h >> 1) * 1024 + ((2 * (h & 1) + (cc >> 3)) * 16 + prow) * 16 + ((cc >> 2) & 1) * 8, par));
      const int pm = opq(cc < 8 ? amap(16384 + h * 128 + prow * 8 + (cc >> 2) * 4, par) : 1 << 30);
      const int ps = opq((cc & 1) ? 0x03020706 : 0x05040100);
      for (int s = 0; s < NSEG; ++s) {
        if (seen < (unsigned)s + 1u) seen = wait_flag(c, F_K + h, s + 1);
        QK_PINS;
        asm volatile(QK_STEP0 : [bd] "+s"(c.budget), [rt] "+s"(tries), [sq] "+s"(seen)
                     : QK_IN, [kx] "v"(kx0), [km] "v"(kmrd), [px] "v"(px), [pm] "v"(pm), [ps] "v"(ps), [pb] "s"(R_P + (s & 3) * P_SLOT), [pr] "s"(R_P + ((s + 1) & 3) * P_SLOT),
                       [kb] "s"(R_KX - KXL + xblk(s, h, para)), [fq] "s"(c.ctrl + (C_FLAG + (F_K + h) * 128) / 4) : QK_CL);
        QK_PINS;
        LITE(s); LOGS(0, s);
        TICK_SUM(10);
      }
      RAWBAR();
    } else {
      const int qt = w - 1;
      const int px = opq(amap(qt ? 16384 + h * 1024 + lane * 16 : h * 512 + (g >> 1) * 256 + cc * 16 + (g & 1) * 8, para));
      const int pm = opq(g < 2 ? amap(49152 + (qt * 32 + h) * 256 + cc * 16 + g * 8, para) : 1 << 30);
      const int ps = opq(cc - 4 * g), hq = opq(g * 512 + (16 * qt + cc) * 16);
#define QK_TOK(STEP) for (int s = 0; s < NSEG; ++s) { QK_PINS; \
        asm volatile(STEP : [bd] "+s"(c.budget), [rt] "+s"(tries), [sq] "+s"(seen) \
                     : QK_IN, [kx] "v"(kx0), [km] "v"(kmrd), [px] "v"(px), [pm] "v"(pm), [ps] "v"(ps), [hq] "v"(hq), [qb] "s"(R_QX + xblk(s, h, para)), [tb] "s"(R_PT + (s & 7) * PT_SLOT), \
                       [tr] "s"(R_PT + ((s + 2) & 7) * PT_SLOT), [sn] "s"(s + 1), [fq] "s"(c.ctrl + (C_FLAG + (F_Q + h) * 128) / 4) : QK_CL); \
        QK_PINS; TICK_SUM(10); }
      if (w == 1) { QK_TOK(QK_STEP1) } else { QK_TOK(QK_STEP2) }
    }
  }
  TICK_OUT();
  if (c.budget == 0u) __hip_atomic_store(c.ctrl + C_TMO / 4, 1u, RLX_AGENT);
}

DEV void shuffle_cols(Ctx& c, int scratch, const v4u (&rows)[8], v4u (&o)[8]) {
  const int g = c.lane >> 4, cc = c.lane & 15;
#pragma unroll
  for (int i = 0; i < 8; ++i) {
    v4u f;
#pragma unroll
    for (int r = 0; r < 4; ++r) f[r] = prm(rows[2 * r + 1][i >> 1], rows[2 * r][i >> 1], i & 1);
    sts16(c.L, scratch + (8 * cc + i) * 64 + ((g ^ (cc & 3)) << 4), f);
  }
  asm volatile("s_waitcnt lgkmcnt(0)" ::: "memory");
#pragma unroll
  for (int i = 0; i < 8; ++i) { const int col = 16 * i + cc; o[i] = lds16(c.L, scratch + col * 64 + ((g ^ ((col >> 3) & 3)) << 4)); }
  asm volatile("s_waitcnt lgkmcnt(0)" ::: "memory");
}

DEV void vo_role(const Params& p, Ctx& c, int h) {
  constexpr int WL = 0, MEMS = 131072, VM = 147456, VROW = 272;
  c.budget = SPIN_CAP;
  const int w = c.w, lane = c.lane = fresh_lane(), g = lane >> 4, cc = lane & 15;
  Clk k = {0u, 0u, 0u, 0u, 0u, 0u};
  anchor(k, true);
  v8u wa[32], wh[16];
  unsigned look = 0;
  bool claimed = false;
  hold(c, VO_AT);
  const rsrc_t rwo = mkrs(p.Wo, 4096 * 1024 * 2);
  const int voo = (h * 128 + 4 * g) * 2048 + (256 * ((cc >> 3) ^ (h >> 3)) + 64 * w + 8 * (cc & 7)) * 2;
  v4u buf[3][8];
  auto request_o = [&](int u) {
#pragma unroll
    for (int r = 0; r < 8; ++r) buf[u % 3][r] = ld16w(rwo, voo ^ ((u >> 2) * 1024), (32 * (u & 3) + 16 * (r >> 2) + (r & 3)) * 2048);
  };
  {
    const rsrc_t rw = mkrs(p.Wv, 1024 * 4096 * 2);
    const int vo = (256 * w + krow_lane(g)) * 8192 + (h * 128 + 8 * cc) * 2;
    v4u vrow[8][8];
    auto request = [&](int u) {
#pragma unroll
      for (int r = 0; r < 8; ++r) vrow[u][r] = ld16w(rw, vo, (64 * ((u >> 1) ^ turn(h, w)) + krow(4 * (u & 1) + (r >> 1), r & 1)) * 8192);
    };
#pragma unroll
    for (int u = 0; u < 5; ++u) request(u);
    SCHED();
    const int wr_ = MEMS + w * 8192 + g * 256;
    int sr[2];
#pragma unroll
    for (int t = 0; t < 2; ++t) { const int i = (2 * cc + t) & 7; sr[t] = opq(i * 1024 + g * 256 + ((4 * w + (cc >> 2) + 2 * i) & 15) * 16); }
#pragma unroll
    for (int u = 0; u < 8; ++u) {
#pragma unroll
      for (int i = 0; i < 8; ++i) {
        v4u f;
#pragma unroll
        for (int r = 0; r < 4; ++r) f[r] = prm(vrow[u][2 * r + 1][i >> 1], vrow[u][2 * r][i >> 1], i & 1);
        sts16(c.L, wr_ + i * 1024 + ((cc + 2 * i) & 15) * 16, f);
      }
      SCHED();
      if (u == 7) claimed = claim_try(c, look);
      if (u == 2) look = claim_look(c);
      if (u + 5 < 8) request(u + 5); else if (u < 6) request_o(u - 3);
      BAR();
      v4u in[4][2];
#pragma unroll
      for (int t = 1; t >= 0; --t)
#pragma unroll
        for (int r = 0; r < 4; ++r) in[r][t] = lds16(c.L, sr[t] + MEMS + ((w + r) & 3) * 8192);
      SCHED();
#pragma unroll
      for (int j = 0; j < 16; ++j) {
        if (qk_i(j) != (u >> 1)) continue;
#pragma unroll
        for (int t = 1; t >= 0; --t) {
          const v4u o = in[qk_r(j)][t];
          if (t == 1 && (j & 1)) sts16(c.L, WL + (w * 16 + (j >> 1)) * 2048 + (u & 1) * 1024 + lane * 16, o);
          else {
            const int n = 3 * (j >> 1) + ((j & 1) ? 2 : t);
#pragma unroll
            for (int r = 0; r < 4; ++r) wa[n][4 * (u & 1) + r] = o[r];
#define M_(r) asm volatile("" : "+" r(wa[n]))
            if (u & 1) AT_ACC(M_, n);
#undef M_
          }
        }
      }
      BAR();
    }
  }
#pragma unroll
  for (int u = 0; u < 8; ++u) {
    v4u o[8];
    shuffle_cols(c, MEMS + w * 8192, buf[u % 3], o);
    SCHED();
    if (u + 3 < 8) request_o(u + 3);
#pragma unroll
    for (int i = 0; i < 4; ++i) {
      const int pr = 4 * (u >> 2) + i, ch = u & 3;
      const v8u f = {o[2 * i][0], o[2 * i + 1][0], o[2 * i][1], o[2 * i + 1][1], o[2 * i][2], o[2 * i + 1][2], o[2 * i][3], o[2 * i + 1][3]};
      if (ch < 2) {
        wh[2 * pr + ch] = f;
#define M_(r) asm volatile("" : "+" r(wh[2 * pr + ch]))
        AT_HOME(M_, 2 * pr + ch);
#undef M_
      } else if (ch == 2) {
        wa[24 + pr] = f;
#define M_(r) asm volatile("" : "+" r(wa[24 + pr]))
        AT_ACC(M_, 24 + pr);
#undef M_
      } else {
        sts16(c.L, WL + (w * 16 + 8 + pr) * 2048 + lane * 16, lo4(f));
        sts16(c.L, WL + (w * 16 + 8 + pr) * 2048 + 1024 + lane * 16, hi4(f));
      }
    }
    SCHED();
  }
  if (!claimed) claim(c, 0);
  BAR();
  STAMP(0);
  const unsigned vote = wait_verdict(c);
  const int par = vote_b(vote), para = vote_a(vote);
  const int moff = opq(amap(w * 4096 + lane * 16, par) + (turn(h, w) & 1) * 1024), m2 = R_MEM + (turn(h, w) & 2) * 1024;
  unsigned tries = 1u << 12;
  *(volatile LAS unsigned*)(c.L + 160512) = 0u;
  LGKM(0);
  const int idx = opq((lane & 1) ? 0xEEEE : 0x4444);
  const int u_off = opq((4 * w + g) * 16384 + (h ^ (8 * g)) * 256 + cc * 16);
  const int hq = opq((lane | w) ? 1 << 30 : amap(24576 + (32 + h) * 128, par));
  const int l0 = opq(WL + w * 32768 + lane * 16), l1 = opq(MEMS + lane * 16);
  const int vmw = opq(VM + 2 * g * VROW + (32 * w + 2 * cc) * 2);
  const int vmr = opq(VM + (cc >> 1) * VROW + g * 16);
  TICK_DECL;
  anchor(k, true);
  unsigned tg = k.cyc;
  Plan tt = {k.g - NS(100000), NS(T_DX)};
  for (int s = 0; s < NSEG; ++s) {
    const int pb = R_P + (s & 3) * P_SLOT;
    PIN_ACC0(wa); PIN_ACC1(wa); PIN_HOME(wh);
    asm volatile(VO_STEP
        : [bd] "+s"(c.budget), [rt] "+s"(tries)
        : [l0] "v"(l0), [l1] "v"(l1), [mo] "v"(moff), [ix] "v"(idx), [vw] "v"(vmw), [vr] "v"(vmr), [uo] "v"(u_off), [w] "s"(w), [rs] "s"(c.rs),
          [sb] "s"(m2 + (s & 3) * MEM_SLOT), [ub] "s"(u_slot(s, para) + (h >> 3) * 16 * 16384), [ur] "s"(u_slot(s + 2, para) + (2 * h + (w >> 1)) * 16384 + (w & 1) * 4096 - MEMS), [tg] "s"(tg + T_VLAG), [hq] "v"(hq), [pb] "s"(pb),
          [ca] "s"(k.cyc - T_DV), [k1] "s"(k.k1), [ga] "s"(k.g), [dr] "n"(T_RV), [pr] "s"(R_P + ((s + 1) & 3) * P_SLOT)
        : CL_S, CL_16_32, CL_160_200, CL_200_216, CL_216_256);
    PIN_ACC0(wa); PIN_ACC1(wa); PIN_HOME(wh);
    if (w == 0) {
      LITE(s);
      LOG(1, s, tg);
      const unsigned sv = ask_stamps(c, par, pb);
      SCHED();
      anchor(k);
      unsigned gc = tt.gx + tt.dx + NS(T_M1 + T_CLR);
      if (__builtin_expect((int)(gc - k.g) < 0, 0)) gc = when_p(tt, latest(c, k, par, pb, sv)) + NS(T_CLR);
      wait_cyc(to_cyc(k, gc));
      RAWBAR();
      SCHED();
      advance(tt, latest(c, k, par, pb, sv));
      tg = to_cyc(k, tt.gx);
      LOG(2, s, k.cyc); LOG(3, s, k.g);
    }
    TICK_SUM(10);
    if (w == 0) { close_in(k, tg); tg = to_cyc(k, tt.gx); }
  }
  TICK_OUT();
  if (c.budget == 0u) __hip_atomic_store(c.ctrl + C_TMO / 4, 1u, RLX_AGENT);
}

DEV void y_role(const Params& p, Ctx& c, int y) {
  constexpr int ZR = 0, UL = 131072, CW = 155648;
  c.budget = SPIN_CAP;
  const int w = c.w, lane = c.lane = fresh_lane(), g = lane >> 4, cc = lane & 15;
  const v4u sent4 = {SENT, SENT, SENT, SENT};
  STAMP(2);
  Clk k = {0u, 0u, 0u, 0u, 0u, 0u};
  anchor(k, true);
  unsigned m0 = 0;
  if (w == 0) {
    unsigned best[2] = {~0u, ~0u};
    st4s(c.rs, y * 16384 + (lane >> 5) * 8192 + (lane & 31) * 4, R_U, SENT);
#pragma unroll 1
    for (int n = 0; n < 12; ++n) {
#pragma unroll
      for (int r = 0; r < 2; ++r) {
        VMWAIT(0);
        const unsigned t0 = (unsigned)__builtin_amdgcn_s_memtime();
        LGKM(0);
        st4s(c.rs, y * 16384 + (lane & 31) * 4, R_U + r * 8192, SENT);
        VMWAIT(0);
        const unsigned t1 = (unsigned)__builtin_amdgcn_s_memtime();
        LGKM(0);
        best[r] = min(best[r], t1 - t0);
      }
      if (n + 1 >= PROBES && min(best[0], best[1]) < 600u && max(best[0], best[1]) > min(best[0], best[1]) + 128u) break;
    }
    STAMP(5);
    claim(c, CL_VOTED | (best[1] < best[0] ? CL_A : 0u));
    m0 = *(const GAS unsigned*)(p.mem0 + (lane >> 3) * 1024 + 16 * y + 2 * (lane & 7));
    m0 = m0 < SENT - 1u ? m0 : SENT - 1u;
    if (lane < 8) *(LAS unsigned*)(c.L + CW + lane * 4) = 0u;
    const unsigned v = wait_vote(c);
    STAMP(7);
    const int oa = y * 16384 + vote_a(v) * 8192 + lane * 16, ob = amap(y * 3072 + lane * 16, vote_b(v));
#pragma unroll
    for (int i = 0; i < 16; ++i) st16s(c.rs, oa + (i & 3) * 1024, R_U + (i >> 3) * MB + ((i >> 2) & 1) * 4096, sent4);
#pragma unroll
    for (int i = 0; i < 3; ++i) st16s(c.rs, amap(y * 3072 + i * 1024 + lane * 16, vote_b(v)), R_MEM, sent4);
#pragma unroll
    for (int i = 0; i < 8; ++i) st16s(c.rs, oa + (i & 3) * 1024, R_PT + (i >> 2) * 4096, sent4);
    (void)ob;
    VMWAIT(0);
    if (lane == 0) __hip_atomic_fetch_add(c.ctrl + (C_CLAIM + c.att * 1024) / 4 + c.slot, CL_FILLED, RLX_AGENT);
    STAMP(6);
  }
  BAR();
  const unsigned vote = wait_verdict(c);
  const int par = vote_b(vote), para = vote_a(vote);
  const int mo = amap(y * 256 + lane * 4, par);
  if (w == 0) st4s(c.rs, mo, R_MEM, m0);
  STAMP(0);
  const int rot = y >> 4;
  volatile LAS unsigned* const GO = (volatile LAS unsigned*)(c.L + CW);
  TICK_DECL;
  if (w == 0) {
    int xo[4];
#pragma unroll
    for (int i = 0; i < 4; ++i) xo[i] = amap((i ^ rot) * 4096 + lane * 16, par);
    const int pmo = amap(16384 + rot * 1024 + 2 * g * 128 + cc * 8, par), psel = (cc & 1) ? 0x03020706 : 0x05040100;
    constexpr int lead = T_U0, late = 0;
    anchor(k, true);
    Plan tt = {k.g - NS(100000), NS(T_DX)};
    unsigned done = k.cyc;
    const int so = amap(24576, par) + lane * 128;
    const int idx = opq((lane & 1) ? 0xEEEE : 0x4444);
    const int uo = y * 16384 + lane * 16;
    const int mw = amap(y * 256 + (2 * g + (cc & 1)) * 32 + (cc >> 1) * 4, par);
    for (int s = 0; s < NSEG - 1; ++s) {
      if (s == 0) RAWBAR();
      const int za = ZR + (s & 3) * 32768 + rot * 8192 + lane * 16;
      v32u z0, z1, z2, z3;
      asm volatile(
          "ds_read_b128 a[0:3], %[z0] offset:0\n\t" "ds_read_b128 a[4:7], %[z0] offset:1024\n\t" "ds_read_b128 a[8:11], %[z0] offset:2048\n\t" "ds_read_b128 a[12:15], %[z0] offset:3072\n\t"
          "ds_read_b128 a[16:19], %[z0] offset:4096\n\t" "ds_read_b128 a[20:23], %[z0] offset:5120\n\t" "ds_read_b128 a[24:27], %[z0] offset:6144\n\t" "ds_read_b128 a[28:31], %[z0] offset:7168\n\t"
          "ds_read_b128 a[32:35], %[z1] offset:0\n\t" "ds_read_b128 a[36:39], %[z1] offset:1024\n\t" "ds_read_b128 a[40:43], %[z1] offset:2048\n\t" "ds_read_b128 a[44:47], %[z1] offset:3072\n\t"
          "ds_read_b128 a[48:51], %[z1] offset:4096\n\t" "ds_read_b128 a[52:55], %[z1] offset:5120\n\t" "ds_read_b128 a[56:59], %[z1] offset:6144\n\t" "ds_read_b128 a[60:63], %[z1] offset:7168\n\t"
          "ds_read_b128 a[64:67], %[z2] offset:0\n\t" "ds_read_b128 a[68:71], %[z2] offset:1024\n\t" "ds_read_b128 a[72:75], %[z2] offset:2048\n\t" "ds_read_b128 a[76:79], %[z2] offset:3072\n\t"
          "ds_read_b128 a[80:83], %[z2] offset:4096\n\t" "ds_read_b128 a[84:87], %[z2] offset:5120\n\t" "ds_read_b128 a[88:91], %[z2] offset:6144\n\t" "ds_read_b128 a[92:95], %[z2] offset:7168\n\t"
          "ds_read_b128 a[96:99], %[z3] offset:0\n\t" "ds_read_b128 a[100:103], %[z3] offset:1024\n\t" "ds_read_b128 a[104:107], %[z3] offset:2048\n\t" "ds_read_b128 a[108:111], %[z3] offset:3072\n\t"
          "ds_read_b128 a[112:115], %[z3] offset:4096\n\t" "ds_read_b128 a[116:119], %[z3] offset:5120\n\t" "ds_read_b128 a[120:123], %[z3] offset:6144\n\t" "ds_read_b128 a[124:127], %[z3] offset:7168\n\t"
          "s_waitcnt lgkmcnt(0)"
          : "={a[0:31]}"(z0), "={a[32:63]}"(z1), "={a[64:95]}"(z2), "={a[96:127]}"(z3) : [z0] "v"(za), [z1] "v"(za ^ 8192), [z2] "v"(za ^ 16384), [z3] "v"(za ^ 24576) : "memory");
      const int pb = R_P + (s & 3) * P_SLOT;
      v32u uu;
      const int ub = u_slot(s, para);
      TICK(0);
      anchor(k);
      TICK(8);
      unsigned gp = tt.gx + tt.dx + NS(T_M1), lt = late, sv = SENT;
      unsigned bad = (int)(done - to_cyc(k, tt.gx)) > T_OVH;
      TICK(4);
      LOG(2, s, k.cyc); LOG(3, s, k.g);
#pragma unroll 1
      for (;;) {
        if (bad) {
          const bool all = allok(sv);
          const unsigned last = latest(c, k, par, pb, sv);
          if (!all) mark(k);
          gp = when_p(tt, last);
          lt = 1;
        }
        close_in(k, to_cyc(k, gp) - (lt ? 96 : lead));
        const unsigned tg = to_cyc(k, gp);
        if (!bad) LOG(1, s, tg);
        asm volatile(Y_STEP
          : "=&{v[144:175]}"(uu), [bad] "=&s"(bad), [sv] "=&v"(sv)
          : [x0] "v"(xo[0]), [x1] "v"(xo[1]), [x2] "v"(xo[2]), [x3] "v"(xo[3]), [pm] "v"(pmo), [u0] "v"(uo + (rot ^ (y & 3)) * 2048), [mw] "v"(mw), [ps] "v"(psel), [ix] "v"(idx),
            [rs] "s"(c.rs), [pb] "s"(pb), [ub] "s"(ub), [mb] "s"(R_MEM + ((s + 1) & 3) * MEM_SLOT), [mr] "s"(R_MEM + ((s + 3) & 3) * MEM_SLOT), [tu] "s"(tg - (lt ? 96 : lead)), [tg] "s"(tg - 32), [late] "s"(lt),
            [so] "v"(so), [bd] "s"(c.budget),
            "{a[0:31]}"(z0), "{a[32:63]}"(z1), "{a[64:95]}"(z2), "{a[96:127]}"(z3)
          : CL_S, CL_120_128, CL_176_200, CL_200_216, CL_216_256);
        bad = __builtin_amdgcn_readfirstlane(bad);
        if (bad != 1u) break;
        spend(c);
      }
      done = bad;
      SCHED();
      const int ul = opq(UL + rot * 2048 + lane * 16);
#pragma unroll
      for (int i = 0; i < 8; ++i) sts16(c.L, (ul ^ ((i >> 1) * 2048)) + (i & 1) * 1024, v4u{uu[4 * i], uu[4 * i + 1], uu[4 * i + 2], uu[4 * i + 3]});
      asm volatile("" ::: "memory");
      *GO = s + 1;
      asm volatile("s_waitcnt vmcnt(2)" : "+v"(sv));
      advance(tt, latest(c, k, par, pb, sv));
      TICK(3);
      LITE(s); LOG(0, s, done);
      TICK_LDS(CW + 256);
    }
    {
      anchor(k);
      wait_cyc(to_cyc(k, tt.gx + tt.dx + NS(T_M1)));
      const int ub = u_slot(NSEG - 1, para);
      v4u u[8];
#pragma unroll 1
      for (;;) {
        asm volatile("" ::: "memory");
#pragma unroll
        for (int i = 0; i < 8; ++i) u[i] = ld16s(c.rs, uo + (i & 3) * 1024, ub + (i >> 2) * 4096);
        SCHED();
        unsigned t = 0;
#pragma unroll
        for (int i = 0; i < 8; ++i) t = umax(t, top(u[i]));
        if (allok(t) || spend(c)) break;
      }
      RAWBAR();
#pragma unroll
      for (int i = 0; i < 8; ++i) sts16(c.L, UL + (i ^ (2 * (y & 3))) * 1024 + lane * 16, u[i]);
      asm volatile("" ::: "memory");
      *GO = NSEG;
    }
    TICK_DUMP(CW + 256);
  } else if (w < 3) {
    const rsrc_t ro = mkrs(p.out, 2048 * 2048 * 2);
    auto tokens = [&](auto QT) {
      constexpr int qt = QT.value, NX = qt ? 32 : 16;
      RAWBAR();
      for (int s = 0; s < NSEG; ++s) {
        const int tb = R_PT + (s & 7) * PT_SLOT + para * 8192;
        int ua[4], za[4], xb[4], mb[4];
#pragma unroll
        for (int i = 0; i < 4; ++i) {
          ua[i] = opq(UL + (i ^ rot) * 2048 + lane * 16);
          za[i] = opq(ZR + (s & 3) * 32768 + (i ^ rot) * 8192 + (qt ? lane * 16 : (g >> 1) * 1024 + (g & 1) * 512 + cc * 16));
          xb[i] = tb + amap(qt * 16384 + (i ^ rot) * (qt ? 8192 : 4096), 0);
          mb[i] = tb + amap(49152 + qt * 8192, 0) + (i ^ rot) * 2048;
        }
        SCHED();
        RAWBAR();
        if (TOK_WAIT) __builtin_amdgcn_s_sleep(TOK_WAIT);
        TICK(0);
        f32x4 a;
#pragma unroll 1
        for (;;) {
          asm volatile("" ::: "memory");
          v4u pt[NX + 8];
#pragma unroll
          for (int i = 0; i < NX; ++i) pt[i] = ld16s(c.rs, (i & 3) * 1024 + lane * 16, qt ? xb[i >> 3] + ((i >> 2) & 1) * 4096 : xb[i >> 2]);
#pragma unroll
          for (int i = 0; i < 8; ++i) pt[NX + i] = ld16s(c.rs, (i & 1) * 1024 + lane * 16, mb[i >> 1]);
          SCHED();
          TICK(1);
#pragma unroll 1
          while (__builtin_amdgcn_readfirstlane(*GO) < (unsigned)s + 1u) { if (spend(c)) break; }
          asm volatile("" ::: "memory");
          f32x4 d[2] = {f32x4{0.f, 0.f, 0.f, 0.f}, f32x4{0.f, 0.f, 0.f, 0.f}};
          v4u fa[8];
          auto frag = [&](int i) {
            if (i >= NX) fa[i & 7] = lds16(c.L, ua[(i - NX) >> 1] + (i & 1) * 1024);
            else if (qt) fa[i & 7] = lds16(c.L, za[i >> 3] + (i & 7) * 1024);
            else {
              const v2u lo = *(LAS v2u*)(c.L + za[i >> 2] + (i & 3) * 2048), hi = *(LAS v2u*)(c.L + za[i >> 2] + (i & 3) * 2048 + 256);
              fa[i & 7] = v4u{lo.x, lo.y, hi.x, hi.y};
            }
          };
#pragma unroll
          for (int i = 0; i < 7; ++i) frag(i);
          SCHED();
#pragma unroll
          for (int i = 0; i < NX + 8; ++i) {
            if (i + 7 < NX + 8) frag(i + 7);
            d[i & 1] = mfma16(fa[i & 7], pt[i], d[i & 1]);
            SCHED();
          }
          a = d[0] + d[1];
          if (!__any(a[0] != a[0])) break;
          unsigned t = 0;
#pragma unroll
          for (int i = 0; i < NX + 8; ++i) t = umax(t, top(pt[i]));
          if (allok(t) || spend(c)) break;
        }
        __builtin_amdgcn_raw_buffer_store_b64(v2u{pk(a[0], a[1]), pk(a[2], a[3])}, ro, (16 * qt + cc) * 4096 + (16 * y + 4 * g) * 2, s * 32 * 4096, OUT_AUX);
        TICK(2);
        TICK_SUM(3);
      }
    };
    if (w == 1) tokens(IC<0>{}); else tokens(IC<1>{});
  } else {
    unsigned seenZ = NOFLAG;
    const rsrc_t rc = mkrs((const void*)c.ctrl, CTRL_BYTES);
    const int fo = lane < 32 ? lane * 128 : 1 << 30;
    __builtin_amdgcn_s_setprio(3);
    auto least = [&](unsigned v) {
      v |= lane < 32 ? 0u : ~0u;
#pragma unroll
      for (int o = 1; o < 32; o <<= 1) v = min(v, (unsigned)__shfl_xor((int)v, o));
      seenZ = umax(seenZ, __builtin_amdgcn_readfirstlane(v));
    };
    auto await = [&](unsigned need) {
#pragma unroll 1
      while (seenZ < need) {
        asm volatile("" ::: "memory");
        least(__builtin_amdgcn_raw_buffer_load_b32(rc, fo, C_FLAG + (F_Z + (y >> 4) * 32) * 128, AUX_SC1));
        if (seenZ >= need || spend(c)) break;
        __builtin_amdgcn_s_sleep(4);
      }
      __builtin_amdgcn_fence(__ATOMIC_ACQUIRE, "wavefront");
    };
    for (int n = 0; n < NSEG; ++n) {
      TICK(0);
      await(n + 1);
      TICK(1);
      v4u z[32];
      int za[8];
#pragma unroll
      for (int i = 0; i < 8; ++i) za[i] = opq(ZR + (n & 3) * 32768 + (i ^ z_mix(y)) * 1024 + lane * 16);
      unsigned look;
#pragma unroll
      for (int i = 0; i < 32 + Z_WIN; ++i) {
        if (i < 32) z[i] = ld16z(c.rs, (i & 3) * 1024 + lane * 16, R_Z + (n & (ZRING - 1)) * Z_SLOT + y * 65536 + ((para ^ (y >> 5)) & 1) * 8192 + amap((i >> 2) * 4096, 0));
        if (i == 32) { look = __builtin_amdgcn_raw_buffer_load_b32(rc, seenZ < (unsigned)n + 2u ? fo : 1 << 30, C_FLAG + (F_Z + (y >> 4) * 32) * 128, AUX_SC1); TICK(4); }
        SCHED();
        if (i >= Z_WIN) sts16(c.L, za[(i - Z_WIN) & 7] + ((i - Z_WIN) >> 3) * 8192, z[i - Z_WIN]);
        else asm volatile("s_nop " STR(Z_GAP));
        if (i == 30 + Z_WIN) { TICK(3); }
        SCHED();
      }
      least(look);
      if (n == 0) await(PRIME);
      TICK(2);
      { const int s = n; (void)s; TICK_SUM(3); }
      BAR();
    }
    RAWBAR();
  }
  TICK_OUT();
  if (c.budget == 0u) __hip_atomic_store(c.ctrl + C_TMO / 4, 1u, RLX_AGENT);
}

#define MF1A(d, a, b) asm volatile(MF16 "%0, %1, %2, %0" : "+v"(d) : "a"(a), "v"(b))
#define MF32A(d, a, b) asm volatile("v_mfma_f32_32x32x16_bf16 %0, %1, %2, %0" : "+v"(d) : "a"(a), "v"(b))
#define MF32AZ(d, a, b) asm volatile("v_mfma_f32_32x32x16_bf16 %0, %1, %2, 0" : "=&v"(d) : "a"(a), "v"(b))
#define MF1B(d, a, b) asm volatile(MF16 "%0, %1, %2, %0" : "+v"(d) : "v"(a), "a"(b))
#define MF1BZ(d, a, b) asm volatile(MF16 "%0, %1, %2, 0" : "=&v"(d) : "v"(a), "a"(b))
#define MF1AZ(d, a, b) asm volatile(MF16 "%0, %1, %2, 0" : "=&v"(d) : "a"(a), "v"(b))
DEV void g_role(const Params& p, Ctx& c, int mat, int h, int gi) {
  constexpr int EX = 131072;
  c.budget = SPIN_CAP;
  const int w = c.w, lane = c.lane = fresh_lane(), g = lane >> 4, cc = lane & 15;
  unsigned t = 0, look = 0;
  if (mat < 2) {
#pragma unroll
    for (int i = 0; i < 3; ++i) t += __builtin_amdgcn_raw_buffer_load_b32(mkrs(p.Wv, 8 * MB), (256 * w + (lane >> 1)) * 8192 + h * 256 + (lane & 1) * 128, (2 + 2 * i + mat) * 32 * 8192, 0);
    if (mat == 0) {
      hold(c, X_AT);
#pragma unroll
      for (int i = 0; i < 4; ++i) t += __builtin_amdgcn_raw_buffer_load_b32(mkrs(p.x, 4 * MB), ((4 * h + w) * 64 + lane) * 128, i * MB, 0);
    }
    if (mat == 0) { asm volatile("" :: "v"(t)); claim(c, 0); }
    hold(c, (mat ? GK_AT : GQ_AT) - (slow_head(p.Wq, h) ? SLOW_LEAD : 0));
  }
  const rsrc_t rx = mkrs(p.x, 2048 * 2048);
  const int xs = w * 16384 + ((lane * 16) ^ ((w & 1) << 7));

  v32u wa[8];
  {
    const rsrc_t rw = mkrs(mat == 0 ? p.Wq : mat == 1 ? p.Wk : p.Wv, 1024 * 4096 * 2);
    const int vo = (256 * w + 8 * g) * 8192 + (h * 128 + 8 * cc) * 2;
    v4u row[8][8];
    auto request = [&](int u) {
#pragma unroll
      for (int r = 0; r < 8; ++r) row[u][r] = ld16w(rw, vo, (32 * u + r) * 8192);
    };
#pragma unroll
    for (int u = 0; u < 2; ++u) request(u);
#pragma unroll
    for (int u = 0; u < 16; ++u) dma16(rx, c.L + w * 16384 + u * 1024, (xs ^ ((u >> 1) << 4)) + u * 1024);
#pragma unroll
    for (int u = 2; u < 6; ++u) request(u);
    SCHED();
    const int sw_ = EX + w * 8192 + g * 256;
    const int sr0 = opq(EX + (cc & 3) * 1024 + g * 256 + ((4 * w + (cc >> 2) + 4 * cc) & 15) * 16), sr1 = sr0 + 4096;
#pragma unroll
    for (int u = 0; u < 8; ++u) {
#pragma unroll
      for (int i = 0; i < 8; ++i) {
        v4u f;
#pragma unroll
        for (int r = 0; r < 4; ++r) f[r] = prm(row[u][2 * r + 1][i >> 1], row[u][2 * r][i >> 1], i & 1);
        sts16(c.L, sw_ + i * 1024 + ((cc + 4 * i) & 15) * 16, f);
      }
      SCHED();
      if (u == 1) look = claim_look(c);
      if (u + 6 < 8) request(u + 6);
      if (u == 7 && mat != 0 && !claim_try(c, look)) claim(c, 0);
      BAR();
      v8u in[4];
#pragma unroll
      for (int q = 0; q < 4; ++q) in[q] = cat(lds16(c.L, sr0 + q * 8192), lds16(c.L, sr1 + q * 8192));
      SCHED();
#pragma unroll
      for (int q = 0; q < 4; ++q) {
        v8u f = in[q];
#define M_(r) asm volatile("" : "+" r(f))
        AT_ACC(M_, 8 * q + u);
#undef M_
#pragma unroll
        for (int r = 0; r < 8; ++r) wa[2 * q + (u >> 2)][8 * (u & 3) + r] = f[r];
      }
      BAR();
    }
  }
  asm volatile("" :: "v"(t));
  STAMP(0);
  const int xpar = vote_a(wait_vote(c)) ^ (mat >> 1);
  TICK_DECL;
  asm volatile(G_LOOP
      :: [xo] "v"(cc * 2048 + ((g ^ cc) << 4)), [xs] "v"(xs), [so] "v"((4 * w + g) * 512 + cc * 16), [fo] "v"((w | lane) ? 1 << 30 : 0),
         [rs] "s"(c.rs), [rx] "s"(rx), [rc] "s"(mkrs((const void*)c.ctrl, CTRL_BYTES)), [fl] "s"(C_FLAG + (mat * 32 + h) * 128), [xp] "s"(xpar),
         [ob] "s"((mat == 1 ? R_KX : R_QX) + h * 16384), [lw] "s"(w * 16384), GTRACE ACC_IN(wa)
      : CL_S, CL_16_32, CL_32_100);
}

DEV void zc_role(const Params& p, Ctx& c, int h) {
  c.budget = SPIN_CAP;
  const int w = c.w, lane = c.lane = fresh_lane(), g = lane >> 4, cc = lane & 15, h2 = lane >> 5, r32 = lane & 31;
  v32u wa[8];
  hold(c, ZC_AT);
  {
    const rsrc_t rw = mkrs(p.Wo, 4096 * 1024 * 2);
    const int vo = (h * 128 + 8 * g) * 2048 + (256 * w + 8 * cc) * 2;
    v4u row[8][8];
    auto request = [&](int u) {
#pragma unroll
      for (int r = 0; r < 8; ++r) row[u][r] = ld16w(rw, vo, (32 * (u & 3) + r) * 2048 + (u >> 2) * 256);
    };
#pragma unroll
    for (int u = 0; u < 2; ++u) request(u);
    SCHED();
    claim(c, 0);
    SCHED();
#pragma unroll
    for (int u = 2; u < 6; ++u) request(u);
    const int sw_ = w * 8192 + g * 256, sr_ = w * 8192 + (cc & 7) * 1024 + g * 256;
    v8u two[8];
#pragma unroll
    for (int u = 0; u < 8; ++u) {
#pragma unroll
      for (int i = 0; i < 8; ++i) {
        v4u f;
#pragma unroll
        for (int r = 0; r < 4; ++r) f[r] = prm(row[u][2 * r + 1][i >> 1], row[u][2 * r][i >> 1], i & 1);
        sts16(c.L, sw_ + i * 1024 + ((cc + 2 * i) & 15) * 16, f);
      }
      SCHED();
      if (u + 6 < 8) request(u + 6);
      LGKM(0);
#pragma unroll
      for (int q = 0; q < 8; ++q) {
        const v4u o = lds16(c.L, sr_ + ((2 * q + (cc >> 3) + 2 * cc) & 15) * 16);
#pragma unroll
        for (int r = 0; r < 4; ++r) two[q][4 * (u & 1) + r] = o[r];
      }
      SCHED();
      if (u & 1) {
#pragma unroll
        for (int q = 0; q < 8; ++q) {
          const int n = 2 * (8 * (u >> 2) + q) + ((u >> 1) & 1);
#define M_(r) asm volatile("" : "+" r(two[q]))
          AT_ACC(M_, n);
#undef M_
#pragma unroll
          for (int r = 0; r < 8; ++r) wa[n >> 2][8 * (n & 3) + r] = two[q][r];
        }
      }
      LGKM(0);
      SCHED();
    }
  }
  STAMP(0);
  BAR();
  const int para = vote_a(wait_vote(c));
  const int ao = g * 512 + cc * 16, zo = 16 * w * 65536 + lane * 16 + ((para ^ (w >> 1)) & 1) * 8192;
  TICK_DECL;
  asm volatile(ZC_LOOP
      : [bd] "+s"(c.budget)
      : [ao] "v"(ao), [a1] "v"(ao + 4096), [z1] "v"(zo + amap((h ^ z_mix(16 * w)) * 1024, 0)), [z2] "v"(zo + 65536 + amap((h ^ z_mix(16 * w + 1)) * 1024, 0)), [fo] "v"(lane ? 1 << 30 : 0),
        [rs] "s"(c.rs), [rc] "s"(mkrs((const void*)c.ctrl, CTRL_BYTES)), [fv] "s"(C_FLAG + (F_V + h) * 128), [fs] "s"(C_FLAG + (F_S + h) * 128),
        [fz] "s"(C_FLAG + (F_Z + w * 32 + h) * 128), [xp] "s"(para ^ 1), [vb] "s"(R_QX + h * 16384), [vd] "s"(C_VERDICT + c.att * 128), [z0] "n"(ZC_PRIME), [zb] "n"(R_Z), [rg] "n"(ZRING - 2), [rm] "n"(ZRING - 1), GTRACE ACC_IN(wa)
      : CL_S, CL_16_32, CL_32_100);
  if (c.budget == 0u) __hip_atomic_store(c.ctrl + C_TMO / 4, 1u, RLX_AGENT);
}

DEV void conv_role(const Params& p, Ctx& c, int cg, int far) {
  constexpr int FS = 4160, PL = 1280, UO = 4 * FS, WS = UO + 8 * PL;
  const int w = c.w, lane = c.lane = fresh_lane(), tid = w * 64 + lane, g = lane >> 4, cc = lane & 15;
  const int d0 = 8 * cg + 2 * w;
  const int pg = ((far ^ (cg >> 5)) & 1) * 8192;
  auto crow = [](int ch) { return (ch >> 1) * 16384 + (ch & 1) * 4096; };
  const int de = (cg >> 3) * 64 + (tid & 7) * 8, t0 = (cg & 7) * 256 + (tid >> 3) * 8;
  auto group_sync = [&](int phase) {
    gu32* cnt = c.ctrl + (C_CONV + (cg >> 3) * 16 + phase * 8) / 4;
    VMWAIT(0);
    BAR();
    if (w == 0) {
      if (lane == 0) __hip_atomic_fetch_add(cnt, 1u, RLX_AGENT);
#pragma unroll 1
      while (__builtin_amdgcn_readfirstlane(__hip_atomic_load(cnt, RLX_AGENT)) < 8u) { if (spend(c)) break; __builtin_amdgcn_s_sleep(2); }
    }
    BAR();
  };
  v4u fr[2][4], ur[2][4], qr[8];
  unsigned up[2][4];
  const int so = cc * 64 + g * 16;
  const unsigned dd = __builtin_amdgcn_raw_buffer_load_b32(mkrs(p.ssmD, 2048), d0 * 2, 0, CONV_AUX);
  BAR();
  {
    const rsrc_t rk = mkrs(p.bk, 2048 * 2048), rv = mkrs(p.bv, 2048 * 2048);
    v4u kr[11], vr[8], sh[4];
#pragma unroll
    for (int i = 0; i < 4; ++i) sh[i] = ld16c(mkrs(p.shift, 8192), de * 8 + i * 16, 0);
#pragma unroll
    for (int i = 0; i < 11; ++i) kr[i] = ld16c(rk, (t0 + i - 3) * 2048 + de * 2, 0);
#pragma unroll
    for (int i = 0; i < 8; ++i) vr[i] = ld16c(rv, (t0 + i) * 2048 + de * 2, 0);
#pragma unroll
    for (int i = 0; i < 8; ++i) fr[i >> 2][i & 3] = ld16c(mkrs(p.ssmk, 4 * MB), lane * 16 + (i & 3) * 1024, (d0 + (i >> 2)) * 4096);
    SCHED();
    if (lane < 16) sts16(c.L, w * WS + (lane >> 2) * FS + (lane & 3) * 16, v4u{0, 0, 0, 0});
#pragma unroll
    for (int i = 0; i < 2; ++i) sts16(c.L, w * WS + UO + (4 * i + g) * PL + cc * 16, v4u{0, 0, 0, 0});
#pragma unroll
    for (int ch = 0; ch < 8; ++ch) {
      const unsigned sa = sh[ch >> 1][(ch & 1) * 2], sb = sh[ch >> 1][(ch & 1) * 2 + 1];
      const float s0 = blo(sa), s1 = bhi(sa), s2 = blo(sb), s3 = bhi(sb);
      float kk[11], o[8];
#pragma unroll
      for (int i = 0; i < 11; ++i) kk[i] = (ch & 1) ? bhi(kr[i][ch >> 1]) : blo(kr[i][ch >> 1]);
#pragma unroll
      for (int i = 0; i < 8; ++i) o[i] = fmaf(kk[i + 3], s0, fmaf(kk[i + 2], s1, fmaf(kk[i + 1], s2, kk[i] * s3))) * ((ch & 1) ? bhi(vr[i][ch >> 1]) : blo(vr[i][ch >> 1]));
      st16s(c.rs, de * 8192 + t0 * 2, R_KVT + pg + crow(ch), v4u{pk(o[0], o[1]), pk(o[2], o[3]), pk(o[4], o[5]), pk(o[6], o[7])});
    }
    group_sync(0);
#pragma unroll
    for (int i = 0; i < 8; ++i) {
      const int b = R_KVT + pg + d0 * 8192 + (i >> 2) * 4096 + (i & 3) * 1024;
      ur[i >> 2][i & 3] = ld16s(c.rs, so, b);
      up[i >> 2][i & 3] = __builtin_amdgcn_raw_buffer_load_b32(c.rs, (i & 3) == 0 && lane == 0 ? 1 << 30 : so, b - 4, AUX_SC1);
    }
#pragma unroll
    for (int i = 0; i < 8; ++i) qr[i] = ld16c(mkrs(p.bq, 4 * MB), (t0 + i) * 2048 + de * 2, 0);
  }
  STAMP(5);
  const int fw = opq(w * WS + 48 + lane * 16), uw = opq(w * WS + UO + g * PL + cc * 16);
  const int fa = opq(w * WS + (cc & 3) * FS + (3 + (cc >> 2) - g) * 16);
  sfor<2>([&](auto CH) __attribute__((always_inline)) {
    constexpr int ch = CH.value;
    SCHED();
#pragma unroll
    for (int i = 0; i < 4; ++i) {
      const v4u f = fr[ch][i], a = ur[ch][i];
      sts16(c.L, fw + 3 * FS + i * 1024, f);
      sts8(c.L, fw + FS + i * 1024 + 8, v2u{f[0], f[1]}); sts8(c.L, fw + FS + i * 1024 + 16, v2u{f[2], f[3]});
#pragma unroll
      for (int r = 0; r < 2; ++r) {
        const int o = fw + 2 * r * FS + i * 1024 + 12 - 8 * r;
        *(LAS unsigned*)(c.L + o) = f[0]; sts8(c.L, o + 4, v2u{f[1], f[2]}); *(LAS unsigned*)(c.L + o + 12) = f[3];
      }
      v4u r0, r1;
#pragma unroll
      for (int r = 0; r < 4; ++r) {
        r0[r] = __builtin_amdgcn_alignbyte(a[3 - r], a[3 - r], 2);
        r1[r] = __builtin_amdgcn_perm(r < 3 ? a[2 - r] : up[ch][i], a[3 - r], 0x07060100u);
      }
      sts16(c.L, uw + (16 + 16 * i) * 16, r0);
      sts16(c.L, uw + 4 * PL + (16 + 16 * i) * 16, r1);
    }
    f32x4 acc[4][2];
#pragma unroll
    for (int i = 0; i < 8; ++i) acc[i >> 1][i & 1] = f32x4{0.f, 0.f, 0.f, 0.f};
    v4u A[2][4], B[2][8];
    auto frag = [&](int d, int n) {
      constexpr int order[12] = {0, 4, 5, 6, 7, 8, 9, 10, 11, 1, 2, 3};
      const int x = order[n];
      if (x < 4) A[d & 1][x] = lds16(c.L, fa + (d + 16 * x) * 64);
      else B[d & 1][x - 4] = lds16(c.L, uw + ((x - 4) & 1) * 4 * PL + (16 + 16 * ((x - 4) >> 1) - d) * 16);
    };
    asm volatile("" ::: "memory");
#pragma unroll
    for (int n = 0; n < 12; ++n) frag(0, n);
    SCHED();
    sfor<16>([&](auto D) __attribute__((always_inline)) {
      constexpr int d = D.value;
      int n = 0;
#pragma unroll
      for (int k = 0; k < 4; ++k)
#pragma unroll
        for (int m = 0; m + k < 4; ++m)
#pragma unroll
          for (int e = 0; e < 2; ++e) {
            acc[m + k][e] = mfma16(A[d & 1][k], B[d & 1][2 * m + e], acc[m + k][e]);
            if (d < 15 && n < 12) frag(d + 1, n);
            ++n;
            SCHED();
          }
    });
    const float dv = ch ? bhi(dd) : blo(dd);
#pragma unroll
    for (int q = 0; q < 4; ++q) {
      v4u z;
#pragma unroll
      for (int i = 0; i < 4; ++i) {
        const unsigned yy = pk(acc[q][1][i], acc[q][0][i]), ud = pk(blo(ur[ch][q][i]) * dv, bhi(ur[ch][q][i]) * dv);
        z[i] = pk(blo(yy) + blo(ud), bhi(yy) + bhi(ud));
      }
      st16s(c.rs, so + q * 1024, R_YT + pg + d0 * 8192 + ch * 4096, z);
    }
  });
  STAMP(6);
  {
    const rsrc_t ro = mkrs(p.out, 2048 * 2048 * 2);
    v4u yv[8];
    group_sync(1);
#pragma unroll
    for (int ch = 0; ch < 8; ++ch) yv[ch] = ld16s(c.rs, de * 8192 + t0 * 2, R_YT + pg + crow(ch));
#pragma unroll
    for (int i = 0; i < 8; ++i) {
      float o[8];
#pragma unroll
      for (int ch = 0; ch < 8; ++ch) o[ch] = ((ch & 1) ? bhi(qr[i][ch >> 1]) : blo(qr[i][ch >> 1])) * ((i & 1) ? bhi(yv[ch][i >> 1]) : blo(yv[ch][i >> 1]));
      __builtin_amdgcn_raw_buffer_store_b128(v4u{pk(o[0], o[1]), pk(o[2], o[3]), pk(o[4], o[5]), pk(o[6], o[7])}, ro, (t0 + i) * 4096 + (1024 + de) * 2, 0, OUT_AUX2);
    }
  }
  if (c.budget == 0u) __hip_atomic_store(c.ctrl + C_TMO / 4, 1u, RLX_AGENT);
}

extern "C" __global__ void __launch_bounds__(256, 1) mk_mega(Params p) {
  extern __shared__ __attribute__((aligned(16))) unsigned char lds_raw[];
  Ctx c;
  c.ctrl = (gu32*)p.ctrl;
  c.rs = mkrs(p.data, R_END);
  c.L = (lds_t)lds_raw;
  c.w = __builtin_amdgcn_readfirstlane(threadIdx.x >> 6);
  c.lane = threadIdx.x & 63;
  c.t0 = (unsigned)__builtin_amdgcn_s_memrealtime();
  c.b = (int)(blockIdx.x & 255u);
  c.att = (int)(blockIdx.x >> 8);
  c.ask = 0;
  if (c.w == 1) {
    unsigned long long base; unsigned len, t;
    asm volatile("s_getpc_b64 s[60:61]\n1:\n\ts_sub_u32 s60, s60, 1b-mk_mega\n\ts_subb_u32 s61, s61, 0\n\ts_mov_b64 %0, s[60:61]\n\ts_mov_b32 %1, mk_tail-mk_mega"
                 : "=s"(base), "=s"(len) :: "s60", "s61", "scc");
    t = __builtin_amdgcn_raw_buffer_load_b32(mkrs((const void*)base, len), (c.lane * 32 + (c.b >> 3)) * 128, 0, 0);
    asm volatile("" :: "v"(t));
  }
  if (c.w == 0 && c.att == 0) {
    c.ask = __builtin_amdgcn_readfirstlane(__hip_atomic_load(c.ctrl + C_ECHO / 4 + c.b, RLX_AGENT)) + 1u;
    if (c.lane == 0) __hip_atomic_store(c.ctrl + (C_ASK + c.b * 64) / 4, c.ask, RLX_AGENT);
  }
  c.slot = (c.att ? (c.b & 7) : (int)((unsigned)__builtin_amdgcn_s_getreg(20 | (3 << 11)) & 7u)) * 32 + (c.b >> 3);
  if (c.b == SERVER && c.w == 3) serve(c);
  const int x = c.slot >> 5, t = c.slot & 31;
  if (x < 4) {
    if (t < 8) qk_role(p, c, x * 8 + t);
    else if (t < 16) vo_role(p, c, x * 8 + t - 8);
    else y_role(p, c, x * 16 + t - 16);
  } else {
    if (t < 24) g_role(p, c, t >> 3, (x - 4) * 8 + (t & 7), t);
    else zc_role(p, c, (x - 4) * 8 + t - 24);
    STAMP(2);
    c.budget = SPIN_CAP;
    conv_role(p, c, (x - 4) * 32 + t, vote_a(wait_verdict(c)) ^ 1);
  }
  STAMP(3);
}

}

extern "C" void kernel_launch(void* const* d_in, const int* in_sizes, int n_in, void* d_out, int out_size, void* d_ws, size_t ws_size,
                              hipStream_t stream) {
  using namespace mk;
  (void)in_sizes; (void)n_in; (void)out_size;
  Params p;
  p.x = (const u16*)d_in[0]; p.mem0 = (const u16*)d_in[1];
  p.Wq = (const u16*)d_in[2]; p.Wk = (const u16*)d_in[3]; p.Wv = (const u16*)d_in[4]; p.Wo = (const u16*)d_in[5];
  p.bq = (const u16*)d_in[7]; p.bk = (const u16*)d_in[8]; p.bv = (const u16*)d_in[9];
  p.shift = (const u16*)d_in[10]; p.ssmk = (const u16*)d_in[11]; p.ssmD = (const u16*)d_in[12];
  p.out = (u16*)d_out;
  p.ctrl = (unsigned char*)d_ws;
  p.data = data_base(d_ws, ws_size);
  (void)hipFuncSetAttribute((const void*)mk_mega, hipFuncAttributeMaxDynamicSharedMemorySize, LDS_BYTES);
  hipLaunchKernelGGL(mk_mega, dim3(256), dim3(256), LDS_BYTES, stream, p);
}
```

```cpp
#include <hip/hip_runtime.h>
#include <hip/hip_bf16.h>

namespace mk {

#define GAS __attribute__((address_space(1)))
#define LAS __attribute__((address_space(3)))
#define DEV __device__ __forceinline__
#define RLX_AGENT __ATOMIC_RELAXED, __HIP_MEMORY_SCOPE_AGENT
typedef unsigned short u16;
typedef unsigned v2u __attribute__((ext_vector_type(2)));
typedef unsigned v4u __attribute__((ext_vector_type(4)));
typedef unsigned v8u __attribute__((ext_vector_type(8)));
typedef unsigned v16u __attribute__((ext_vector_type(16)));
typedef unsigned v32u __attribute__((ext_vector_type(32)));
typedef float f32x2 __attribute__((ext_vector_type(2)));
typedef float f32x4 __attribute__((ext_vector_type(4)));
typedef float f32x16 __attribute__((ext_vector_type(16)));
typedef __bf16 bf16x2 __attribute__((ext_vector_type(2)));
typedef __bf16 bf16x8 __attribute__((ext_vector_type(8)));
typedef __bf16 bf16x16 __attribute__((ext_vector_type(16)));
typedef GAS unsigned gu32;
typedef __amdgpu_buffer_rsrc_t rsrc_t;
typedef LAS unsigned char* lds_t;

#define XCNT(r) ""
#define UCNT ""
#define CL_T
#define XT(a, b) ""
#define LMARK ""
#define GSUM ""
#define ZSUM ""
#define GTRACE
#define MIXED 1
#define NOFLAG 0
#define TOK_KICK 1
#define ZRING 16
#define Z_AT 16
#define Z_GAP 4
#define STR_(x) #x
#define STR(x) STR_(x)
#define GQ_AT 500
#define GK_AT 300
#define PULL_AT 800
#define QK_AT 0
#define SERVER 194
#define SLOW_LEAD 100
#define PULL_DEPTH 1
#define PRIME 2
#define X_AT 400
#define VO_AT 200
#define ZC_AT 0
#define CONV_AUX 2
#define W_AUX 0
#define ZC_PRIME 1
#define CODE_PULL 1
#define PROBES 2
#define Z_WIN 20
#define TOK_WAIT 0
#define Z_AUX 0
#define OUT_AUX 2
#define OUT_AUX2 2
#define URING 8
#define YH_AT 0
#define G_DEEP 1
#define AGAIN 0
#define PRE 0
#define YH 1
#define HELP_O 0
#define HELP_V 1
#define SLOW_V 1
#define K_FIRST 1
#define FLIP 1
#define YH_STEP 300
constexpr int NSEG = 64;
constexpr unsigned SENT = 0xFFFFFFFFu;
constexpr int AUX_SC1 = 16, AUX_NT = 2;
constexpr int T_LAG = 61;
constexpr int G_AHEAD = 4;
constexpr unsigned SPIN_CAP = 1u << 21;
constexpr int LDS_BYTES = 163840;

constexpr int C_ECHO = 0, C_CLAIM = 1024, C_VERDICT = 3072, C_TMO = 3328, C_CONV = 3456, C_VOTE = 3840, C_FLAG = 4096, C_ASK = C_FLAG + 256 * 128;
constexpr int F_Q = 0, F_K = 32, F_V = 64, F_S = 96, F_Z = 128;
constexpr int CTRL_BYTES = C_ASK + 256 * 64;
constexpr int MB = 1 << 20;
constexpr int R_U = 136 * MB;
constexpr int R_MEM = 2 * MB, MEM_SLOT = 32768;
constexpr int R_P = R_MEM + 4 * MEM_SLOT, P_SLOT = 65536;
constexpr int R_PT = 4 * MB, PT_SLOT = 131072;
constexpr int R_QX = 8 * MB, R_KX = 40 * MB, R_Z = 72 * MB, Z_SLOT = 4 * MB, R_KVT = R_KX, R_YT = R_KX + 8 * MB, R_END = (136 + URING) * MB;

constexpr size_t R_DIAG = 0;
#define TOP 1
static inline unsigned char* data_base(void* ws, size_t size) {
  return (unsigned char*)((TOP ? (size_t)ws + size - R_END - R_DIAG : (size_t)ws + CTRL_BYTES + 2 * MB - 1) & ~(size_t)(2 * MB - 1));
}
struct Params {
  const u16 *x, *mem0, *Wq, *Wk, *Wv, *Wo, *bq, *bk, *bv, *shift, *ssmk, *ssmD;
  u16* out;
  unsigned char* ctrl;
  unsigned char* data;
};

DEV rsrc_t mkrs(const void* p, unsigned bytes) { return __builtin_amdgcn_make_buffer_rsrc((void*)p, 0, bytes, 0x00020000); }
DEV v4u ld16s(rsrc_t rs, int v, int s) { return __builtin_amdgcn_raw_buffer_load_b128(rs, v, s, AUX_SC1); }
DEV v2u ld8s(rsrc_t rs, int v, int s) { return __builtin_amdgcn_raw_buffer_load_b64(rs, v, s, AUX_SC1); }
DEV v4u ld16(rsrc_t rs, int v, int s) { return __builtin_amdgcn_raw_buffer_load_b128(rs, v, s, 0); }
DEV v4u ld16z(rsrc_t rs, int v, int s) { return __builtin_amdgcn_raw_buffer_load_b128(rs, v, s, AUX_SC1 | Z_AUX); }
DEV v4u ld16c(rsrc_t rs, int v, int s) { return __builtin_amdgcn_raw_buffer_load_b128(rs, v, s, CONV_AUX); }
template <int A> DEV v4u ld16w(rsrc_t rs, int v, int s) { return __builtin_amdgcn_raw_buffer_load_b128(rs, v, s, A); }
DEV v2u ld8(rsrc_t rs, int v, int s) { return __builtin_amdgcn_raw_buffer_load_b64(rs, v, s, 0); }
DEV void dma16(rsrc_t rs, lds_t l, int v) {
#if __HIP_DEVICE_COMPILE__
  __builtin_amdgcn_raw_ptr_buffer_load_lds(rs, (LAS void*)l, 16, v, 0, 0, 0);
#endif
}
DEV void st16s(rsrc_t rs, int v, int s, v4u d) { __builtin_amdgcn_raw_buffer_store_b128(d, rs, v, s, AUX_SC1); }
DEV void st8s(rsrc_t rs, int v, int s, v2u d) { __builtin_amdgcn_raw_buffer_store_b64(d, rs, v, s, AUX_SC1); }
DEV void st4s(rsrc_t rs, int v, int s, unsigned d) { __builtin_amdgcn_raw_buffer_store_b32(d, rs, v, s, AUX_SC1); }
DEV int amap(int o, int par) { return ((o >> 13) << 14) | (par << 13) | (o & 8191); }
DEV int xblk(int s, int h, int par) { return (s * 32 + h) * 16384 + ((par ^ (s >> 2)) & 1) * 8192; }
DEV unsigned umax(unsigned a, unsigned b) { return a > b ? a : b; }
DEV unsigned top(v4u v) { return umax(umax(v.x, v.y), umax(v.z, v.w)); }
DEV unsigned pk(float a, float b) { bf16x2 v = {(__bf16)a, (__bf16)b}; return __builtin_bit_cast(unsigned, v); }
DEV unsigned pks(float a, float b) { unsigned u = pk(a, b); return u < SENT - 1u ? u : SENT - 1u; }
DEV float blo(unsigned u) { return __builtin_bit_cast(float, u << 16); }
DEV float bhi(unsigned u) { return __builtin_bit_cast(float, u & 0xFFFF0000u); }
DEV float rb(float a) { return blo(pk(a, a)); }
DEV unsigned fu(float f) { return __builtin_bit_cast(unsigned, f); }
DEV float uf(unsigned u) { return __builtin_bit_cast(float, u); }
DEV f32x4 mfma16(v4u a, v4u b, f32x4 c) {
  return __builtin_amdgcn_mfma_f32_16x16x32_bf16(__builtin_bit_cast(bf16x8, a), __builtin_bit_cast(bf16x8, b), c, 0, 0, 0);
}
DEV f32x16 mfma32(v4u a, v4u b, f32x16 c) {
  return __builtin_amdgcn_mfma_f32_32x32x16_bf16(__builtin_bit_cast(bf16x8, a), __builtin_bit_cast(bf16x8, b), c, 0, 0, 0);
}
DEV f32x4 smf(v4u a, v8u b, f32x4 c, int idx) {
  return __builtin_amdgcn_smfmac_f32_16x16x64_bf16(__builtin_bit_cast(bf16x8, a), __builtin_bit_cast(bf16x16, b), c, idx, 0, 0);
}
DEV v8u cat(v4u a, v4u b) { return __builtin_shufflevector(a, b, 0, 1, 2, 3, 4, 5, 6, 7); }
DEV v4u lo4(v8u a) { return __builtin_shufflevector(a, a, 0, 1, 2, 3); }
DEV v4u hi4(v8u a) { return __builtin_shufflevector(a, a, 4, 5, 6, 7); }
DEV v4u lds16(lds_t L, int o) { return *(LAS v4u*)(L + o); }
DEV void sts16(lds_t L, int o, v4u v) { *(LAS v4u*)(L + o) = v; }
DEV void sts8(lds_t L, int o, v2u v) { *(LAS v2u*)(L + o) = v; }
#define SCHED() __builtin_amdgcn_sched_barrier(0)
#define PIN_A(x) asm volatile("" : "+a"(x))
#define PIN_V(x) asm volatile("" : "+v"(x))
DEV int opq(int v) { asm volatile("" : "+v"(v)); return v; }
DEV int fresh_lane() {
  int l; asm volatile("v_mbcnt_lo_u32_b32 %0, -1, 0\n\tv_mbcnt_hi_u32_b32 %0, -1, %0" : "=v"(l)); return l;
}
DEV f32x4 fresh_zero() {
  f32x2 a, b;
  asm volatile("v_mov_b64 %0, 0" : "=v"(a));
  asm volatile("v_mov_b64 %0, 0" : "=v"(b));
  return __builtin_shufflevector(a, b, 0, 1, 2, 3);
}
DEV unsigned fresh_sent() { unsigned z; asm volatile("v_mov_b32 %0, -1" : "=v"(z)); return z; }
#define SMF "v_smfmac_f32_16x16x64_bf16 "
#define MF16 "v_mfma_f32_16x16x32_bf16 "
#define SMF1(cls, d, a, b) asm volatile(SMF "%0, %1, %2, %3" : "+v"(d) : "v"(a), cls(b), "v"(idx))
#define SMF1F(cls, lo, hi, d, a, b) asm volatile(SMF "v[" #lo ":" #hi "], %1, %2, %3" : "+{v[" #lo ":" #hi "]}"(d) : "v"(a), cls(b), "v"(idx))
#define AT_HOME(M, j) do { const int j_ = (j); if (j_ == 0) M("{v[32:39]}"); if (j_ == 1) M("{v[40:47]}"); if (j_ == 2) M("{v[48:55]}"); if (j_ == 3) M("{v[56:63]}"); if (j_ == 4) M("{v[64:71]}"); if (j_ == 5) M("{v[72:79]}"); if (j_ == 6) M("{v[80:87]}"); if (j_ == 7) M("{v[88:95]}"); if (j_ == 8) M("{v[96:103]}"); if (j_ == 9) M("{v[104:111]}"); if (j_ == 10) M("{v[112:119]}"); if (j_ == 11) M("{v[120:127]}"); if (j_ == 12) M("{v[128:135]}"); if (j_ == 13) M("{v[136:143]}"); if (j_ == 14) M("{v[144:151]}"); if (j_ == 15) M("{v[152:159]}"); } while (0)
#define MF1(d, a, b) asm volatile(MF16 "%0, %1, %2, %0" : "+v"(d) : "v"(a), "v"(b))
#define MF1Z(d, a, b) asm volatile(MF16 "%0, %1, %2, 0" : "=&v"(d) : "v"(a), "v"(b))
#define LGKM(n) asm volatile("s_waitcnt lgkmcnt(" #n ")" ::: "memory")
#define RAWBAR() do { __builtin_amdgcn_s_barrier(); asm volatile("" ::: "memory"); } while (0)
DEV void settle3(f32x4& s0, f32x4& s1, f32x4& s2) { asm volatile("s_nop 7" : "+v"(s0), "+v"(s1), "+v"(s2)); }
DEV void settle4(f32x4 (&d)[4]) { asm volatile("s_nop 7" : "+v"(d[0]), "+v"(d[1]), "+v"(d[2]), "+v"(d[3])); }
#define BAR() do { asm volatile("s_waitcnt lgkmcnt(0)" ::: "memory"); __builtin_amdgcn_s_barrier(); asm volatile("" ::: "memory"); } while (0)
#define VMWAIT(n) asm volatile("s_waitcnt vmcnt(" #n ")" ::: "memory")
DEV float add1(float a, float b) {
  float o; asm("v_add_f32 %0, %1, %2" : "=v"(o) : "v"(a), "v"(b)); return o;
}
DEV float xmax(float v) {
  auto r = __builtin_amdgcn_permlane32_swap(fu(v), fu(v), false, false); v = fmaxf(uf(r[0]), uf(r[1]));
  auto q = __builtin_amdgcn_permlane16_swap(fu(v), fu(v), false, false); return fmaxf(uf(q[0]), uf(q[1]));
}
DEV float xsum(float v) {
  auto r = __builtin_amdgcn_permlane32_swap(fu(v), fu(v), false, false); v = uf(r[0]) + uf(r[1]);
  auto q = __builtin_amdgcn_permlane16_swap(fu(v), fu(v), false, false); return uf(q[0]) + uf(q[1]);
}

DEV float ror8(float v) { return uf(__builtin_amdgcn_update_dpp(0u, fu(v), 0x128, 0xf, 0xf, true)); }
DEV float fmax2(float a, float b) { return __builtin_amdgcn_fmed3f(a, b, __builtin_inff()); }
DEV float fmax3(float a, float b, float c) { float r; asm("v_max3_f32 %0, %1, %2, %3" : "=v"(r) : "v"(a), "v"(b), "v"(c)); return r; }
DEV bool allok(unsigned t) { return __all(t != SENT); }

template <int N> struct IC { static constexpr int value = N; };
template <int N, class F> DEV void sfor(F&& f) { if constexpr (N > 0) { sfor<N - 1>(f); f(IC<N - 1>{}); } }
struct Ctx {
  gu32* ctrl;
  rsrc_t rs;
  lds_t L;
  int w, lane;
  int b, slot, att;
  unsigned ask;
  unsigned t0;
  unsigned budget;
};
DEV bool spend(Ctx& c) {
  const unsigned old = c.budget;
  asm volatile("s_cmp_lg_u32 %0, 0\n\ts_subb_u32 %0, %0, 0" : "+s"(c.budget) :: "scc");
  return old == 0u;
}
DEV unsigned wait_flag(Ctx& c, int flag, unsigned need) {
  gu32* f = c.ctrl + (C_FLAG + flag * 128) / 4;
  unsigned v;
#pragma unroll 1
  for (;;) {
    v = __builtin_amdgcn_readfirstlane(__hip_atomic_load(f, RLX_AGENT));
    if (v >= need || spend(c)) break;
    __builtin_amdgcn_s_sleep(2);
  }
  __builtin_amdgcn_fence(__ATOMIC_ACQUIRE, "wavefront");
  return v;
}
DEV void post_flag(Ctx& c, int flag, unsigned v, int off = 0) {
  st4s(mkrs((const void*)c.ctrl, CTRL_BYTES), (c.w | off | fresh_lane()) ? 1 << 30 : 0, C_FLAG + flag * 128, v);
}
constexpr unsigned CL_A = 1u << 16, CL_VOTED = 1u << 24, CL_FILLED = 1u << 28;
DEV unsigned claim_look(Ctx& c) { return __hip_atomic_load(c.ctrl + C_ECHO / 4 + c.b, RLX_AGENT); }
DEV void ask(Ctx& c, unsigned first) {
  c.ask = __builtin_amdgcn_readfirstlane(first) + 1u;
  st4s(mkrs((const void*)c.ctrl, CTRL_BYTES), (c.w | c.att | fresh_lane()) ? 1 << 30 : 0, C_ASK + c.b * 64, c.ask);
}
DEV void claim(Ctx& c, unsigned votes) {
  if (c.w != 0) return;
  if (c.att == 0) {
#pragma unroll 1
    while (__builtin_amdgcn_readfirstlane(__hip_atomic_load(c.ctrl + C_ECHO / 4 + c.b, RLX_AGENT)) != c.ask) {
      if (spend(c)) break;
      __builtin_amdgcn_s_sleep(2);
    }
  }
  if (fresh_lane() == 0) __hip_atomic_fetch_add(c.ctrl + (C_CLAIM + c.att * 1024) / 4 + c.slot, 1u + votes, RLX_AGENT);
}
DEV bool claim_try(Ctx& c, unsigned look) {
  const bool ok = c.att || __builtin_amdgcn_readfirstlane(look) == c.ask;
  if (ok && c.w == 0 && fresh_lane() == 0) __hip_atomic_fetch_add(c.ctrl + (C_CLAIM + c.att * 1024) / 4 + c.slot, 1u, RLX_AGENT);
  return ok;
}
DEV unsigned wait_verdict(Ctx& c) {
  unsigned v;
#pragma unroll 1
  for (;;) {
    v = __builtin_amdgcn_readfirstlane(__hip_atomic_load(c.ctrl + (C_VERDICT + c.att * 128) / 4, RLX_AGENT));
    if ((v & 1u) || spend(c)) break;
    __builtin_amdgcn_s_sleep(2);
  }
  __builtin_amdgcn_fence(__ATOMIC_ACQUIRE, "wavefront");
  if ((v & 3u) == 3u && c.att == 0) {
    asm volatile("s_waitcnt vmcnt(0) lgkmcnt(0)\n\ts_barrier\n\ts_mov_b64 s[0:1], %0\n\ts_mov_b32 s2, %1\n\tv_mov_b32 v0, %2\n\t"
                 "s_getpc_b64 s[4:5]\n1:\n\ts_sub_u32 s4, s4, 1b-mk_mega\n\ts_subb_u32 s5, s5, 0\n\ts_setpc_b64 s[4:5]"
                 :: "s"(__builtin_amdgcn_kernarg_segment_ptr()), "s"(c.b + 256), "v"(c.w * 64 + fresh_lane()) : "s0", "s1", "s2", "s4", "s5", "v0", "scc", "memory");
    __builtin_unreachable();
  }
  return v;
}
DEV unsigned wait_vote(Ctx& c) {
  unsigned v;
#pragma unroll 1
  for (;;) {
    v = __builtin_amdgcn_readfirstlane(__hip_atomic_load(c.ctrl + C_VOTE / 4, RLX_AGENT));
    if ((v & 1u) || spend(c)) break;
    __builtin_amdgcn_s_sleep(2);
  }
  return v;
}
DEV void serve(Ctx& c) {
  c.budget = SPIN_CAP;
  const int lane = fresh_lane();
  const rsrc_t rc = mkrs((const void*)c.ctrl, CTRL_BYTES);
  if (c.att == 0) {
#pragma unroll
    for (int i = C_CLAIM / 1024; i < C_ASK / 1024; ++i) st16s(rc, lane * 16, i * 1024, v4u{0, 0, 0, 0});
    VMWAIT(0);
  }
  unsigned vote = c.att ? wait_vote(c) : 0u;
  bool echo = c.att == 0, first = true;
  v4u last = {0u, 0u, 0u, 0u};
#pragma unroll 1
  for (;;) {
    asm volatile("" ::: "memory");
    const v4u cl = ld16s(rc, lane * 16, C_CLAIM + c.att * 1024);
    if (echo) {
      v4u e;
#pragma unroll
      for (int i = 0; i < 4; ++i) e[i] = __builtin_amdgcn_raw_buffer_load_b32(rc, (lane * 4 + i) * 64, C_ASK, AUX_SC1);
      if (first || __any(e[0] != last[0] || e[1] != last[1] || e[2] != last[2] || e[3] != last[3])) st16s(rc, lane * 16, C_ECHO, e);
      last = e; first = false;
    }
    unsigned n = 0, one = 1;
    int va = 0, nv = 0, nf = 0;
#pragma unroll
    for (int i = 0; i < 4; ++i) {
      n += cl[i] & 0xFFFFu; one &= (cl[i] & 0xFFFFu) == 1u;
      va += __popcll(__ballot(cl[i] & CL_A)); nv += __popcll(__ballot(cl[i] & CL_VOTED)); nf += __popcll(__ballot(cl[i] & CL_FILLED));
    }
#pragma unroll
    for (int o = 1; o < 64; o <<= 1) n += (unsigned)__shfl_xor((int)n, o);
    const bool all = __builtin_amdgcn_readfirstlane(n) >= 256u, good = __all(one);
    echo = echo && !all;
    if (!vote && (all || max(va, nv - va) > 20)) {
      vote = 2 * va > nv ? 5u : 9u;
      if (lane == 0) __hip_atomic_store(c.ctrl + C_VOTE / 4, vote, RLX_AGENT);
    }
    if (all && (!good || nf >= 64)) {
      if (lane == 0) __hip_atomic_store(c.ctrl + (C_VERDICT + c.att * 128) / 4, vote | (good ? 0u : 2u), RLX_AGENT);
      break;
    }
    if (spend(c)) break;
  }
}
DEV int vote_a(unsigned v) { return (int)((v >> 2) & 1u); }
DEV int vote_b(unsigned v) { return (int)((v >> 3) & 1u); }

#define STAMP(slot) do {} while (0)
#define TICK_DECL
#define LOG(i, s, v) do {} while (0)
#define LOGS(i, s) do {} while (0)
#define LITE(s) do {} while (0)
#define TICK(i) do {} while (0)
#define TICK_SUM(n) do {} while (0)
#define TICK_LDS(b) do {} while (0)
#define TICK_DUMP(b) do {} while (0)
#define TICK_OUT() do {} while (0)

DEV constexpr int krow(int rho8, int i) { return 32 * (rho8 >> 2) + 8 * (rho8 & 1) + 2 * ((rho8 >> 1) & 1) + i; }
DEV int krow_lane(int g) { return 16 * (g >> 1) + 4 * (g & 1); }
DEV unsigned prm(unsigned hi, unsigned lo, int odd) { return __builtin_amdgcn_perm(hi, lo, odd ? 0x07060302u : 0x05040100u); }

#define AT_ACC(M, n) do { const int n_ = (n); if (n_ == 0) M("{a[0:7]}"); if (n_ == 1) M("{a[8:15]}"); if (n_ == 2) M("{a[16:23]}"); if (n_ == 3) M("{a[24:31]}"); if (n_ == 4) M("{a[32:39]}"); if (n_ == 5) M("{a[40:47]}"); if (n_ == 6) M("{a[48:55]}"); if (n_ == 7) M("{a[56:63]}"); if (n_ == 8) M("{a[64:71]}"); if (n_ == 9) M("{a[72:79]}"); if (n_ == 10) M("{a[80:87]}"); if (n_ == 11) M("{a[88:95]}"); if (n_ == 12) M("{a[96:103]}"); if (n_ == 13) M("{a[104:111]}"); if (n_ == 14) M("{a[112:119]}"); if (n_ == 15) M("{a[120:127]}"); if (n_ == 16) M("{a[128:135]}"); if (n_ == 17) M("{a[136:143]}"); if (n_ == 18) M("{a[144:151]}"); if (n_ == 19) M("{a[152:159]}"); if (n_ == 20) M("{a[160:167]}"); if (n_ == 21) M("{a[168:175]}"); if (n_ == 22) M("{a[176:183]}"); if (n_ == 23) M("{a[184:191]}"); if (n_ == 24) M("{a[192:199]}"); if (n_ == 25) M("{a[200:207]}"); if (n_ == 26) M("{a[208:215]}"); if (n_ == 27) M("{a[216:223]}"); if (n_ == 28) M("{a[224:231]}"); if (n_ == 29) M("{a[232:239]}"); if (n_ == 30) M("{a[240:247]}"); if (n_ == 31) M("{a[248:255]}"); } while (0)
#define PIN_ACC0(x) asm volatile("" :: "{a[0:7]}"(x[0]), "{a[8:15]}"(x[1]), "{a[16:23]}"(x[2]), "{a[24:31]}"(x[3]), "{a[32:39]}"(x[4]), "{a[40:47]}"(x[5]), "{a[48:55]}"(x[6]), "{a[56:63]}"(x[7]), "{a[64:71]}"(x[8]), "{a[72:79]}"(x[9]), "{a[80:87]}"(x[10]), "{a[88:95]}"(x[11]), "{a[96:103]}"(x[12]), "{a[104:111]}"(x[13]), "{a[112:119]}"(x[14]), "{a[120:127]}"(x[15]))
#define PIN_ACC1(x) asm volatile("" :: "{a[128:135]}"(x[16]), "{a[136:143]}"(x[17]), "{a[144:151]}"(x[18]), "{a[152:159]}"(x[19]), "{a[160:167]}"(x[20]), "{a[168:175]}"(x[21]), "{a[176:183]}"(x[22]), "{a[184:191]}"(x[23]), "{a[192:199]}"(x[24]), "{a[200:207]}"(x[25]), "{a[208:215]}"(x[26]), "{a[216:223]}"(x[27]), "{a[224:231]}"(x[28]), "{a[232:239]}"(x[29]), "{a[240:247]}"(x[30]), "{a[248:255]}"(x[31]))
#define PIN_HOME(x) asm volatile("" :: "{v[32:39]}"(x[0]), "{v[40:47]}"(x[1]), "{v[48:55]}"(x[2]), "{v[56:63]}"(x[3]), "{v[64:71]}"(x[4]), "{v[72:79]}"(x[5]), "{v[80:87]}"(x[6]), "{v[88:95]}"(x[7]), "{v[96:103]}"(x[8]), "{v[104:111]}"(x[9]), "{v[112:119]}"(x[10]), "{v[120:127]}"(x[11]), "{v[128:135]}"(x[12]), "{v[136:143]}"(x[13]), "{v[144:151]}"(x[14]), "{v[152:159]}"(x[15]))
#define CL_32_100 "v32", "v33", "v34", "v35", "v36", "v37", "v38", "v39", "v40", "v41", "v42", "v43", "v44", "v45", "v46", "v47", "v48", "v49", "v50", "v51", "v52", "v53", "v54", "v55", "v56", "v57", "v58", "v59", "v60", "v61", "v62", "v63", "v64", "v65", "v66", "v67", "v68", "v69", "v70", "v71", "v72", "v73", "v74", "v75", "v76", "v77", "v78", "v79", "v80", "v81", "v82", "v83", "v84", "v85", "v86", "v87", "v88", "v89", "v90", "v91", "v92", "v93", "v94", "v95", "v96", "v97", "v98", "v99", "v100", "v101", "v102", "v103", "v104", "v105", "v106", "v107", "v108", "v109", "v110", "v111", "v112", "v113", "v114", "v115", "v116", "v117", "v118", "v119", "v120", "v121", "v122", "v123", "v124", "v125", "v126", "v127", "v128", "v129", "v130", "v131"
#define ACC_IN(x) "{a[0:31]}"(x[0]), "{a[32:63]}"(x[1]), "{a[64:95]}"(x[2]), "{a[96:127]}"(x[3]), "{a[128:159]}"(x[4]), "{a[160:191]}"(x[5]), "{a[192:223]}"(x[6]), "{a[224:255]}"(x[7])
#define CL_16_32 "v16", "v17", "v18", "v19", "v20", "v21", "v22", "v23", "v24", "v25", "v26", "v27", "v28", "v29", "v30", "v31"
#define CL_120_128 "v120", "v121", "v122", "v123", "v124", "v125", "v126", "v127", "v128", "v129", "v130", "v131", "v132", "v133", "v134", "v135"
#define CL_160_200 "v160", "v161", "v162", "v163", "v164", "v165", "v166", "v167", "v168", "v169", "v170", "v171", "v172", "v173", "v174", "v175", "v176", "v177", "v178", "v179", "v180", "v181", "v182", "v183", "v184", "v185", "v186", "v187", "v188", "v189", "v190", "v191", "v192", "v193", "v194", "v195", "v196", "v197", "v198", "v199"
#define CL_176_200 "v176", "v177", "v178", "v179", "v180", "v181", "v182", "v183", "v184", "v185", "v186", "v187", "v188", "v189", "v190", "v191", "v192", "v193", "v194", "v195", "v196", "v197", "v198", "v199"
#define CL_200_216 "v200", "v201", "v202", "v203", "v204", "v205", "v206", "v207", "v208", "v209", "v210", "v211", "v212", "v213", "v214", "v215"
#define CL_216_256 "v216", "v217", "v218", "v219", "v220", "v221", "v222", "v223", "v224", "v225", "v226", "v227", "v228", "v229", "v230", "v231", "v232", "v233", "v234", "v235", "v236", "v237", "v238", "v239", "v240", "v241", "v242", "v243", "v244", "v245", "v246", "v247", "v248", "v249", "v250", "v251", "v252", "v253", "v254", "v255"
#define CL_S "memory", "vcc", "scc", "s60", "s61", "s62", "s63", "s64", "s65", "s66", "s67", "s68" CL_T
#define TIMED_WAIT \
    "s_getpc_b64 s[62:63]\n\t" "1:\n\t" "s_memtime s[64:65]\n\t" "s_waitcnt lgkmcnt(0)\n\t" "s_sub_u32 s61, %0, s64\n\t" "s_cmp_gt_i32 s61, 124\n\t" "s_cbranch_scc1 1b\n\t" \
    "s_max_i32 s61, s61, 0\n\t" "s_and_b32 s61, s61, 124\n\t" "s_sub_u32 s61, 2f-1b, s61\n\t" "s_add_u32 s62, s62, s61\n\t" "s_addc_u32 s63, s63, 0\n\t" \
    "s_setpc_b64 s[62:63]\n\t" ".rept 31\n\t" "s_nop 0\n\t" ".endr\n\t" "2:\n\t"
#define QK_STEP0 \
    ".p2align 6\n\t" "v_xor_b32 v216, 0x400, %[mo]\n\t" "s_xor_b32 s60, %[sb], 0x800\n\t" "s_mov_b32 s66, 0x3e0293ee\n\t" "s_mov_b32 s67, 0x3e0293ee\n\t" XT(80, 81) \
    "s_add_u32 s61, %[kb], 0x1000\n\t" "buffer_load_dwordx4 v[16:19], %[kx], %[rs], %[kb] offen offset:0 sc1\n\t" \
    "buffer_load_dwordx4 v[20:23], %[kx], %[rs], %[kb] offen offset:2048 sc1\n\t" "buffer_load_dwordx4 v[24:27], %[kx], %[rs], s61 offen offset:0 sc1\n\t" \
    "buffer_load_dwordx4 v[28:31], %[kx], %[rs], s61 offen offset:2048 sc1\n\t" "buffer_load_dwordx4 v[200:203], %[kx], %[rs], %[kb] offen offset:256 sc1\n\t" \
    "buffer_load_dwordx4 v[204:207], %[kx], %[rs], %[kb] offen offset:2304 sc1\n\t" "buffer_load_dwordx4 v[208:211], %[kx], %[rs], s61 offen offset:256 sc1\n\t" \
    "buffer_load_dwordx4 v[212:215], %[kx], %[rs], s61 offen offset:2304 sc1\n\t" "s_barrier\n\t" "s_waitcnt vmcnt(7)\n\t" "ds_write_b128 %[kx], v[16:19] offset:0\n\t" \
    "s_waitcnt vmcnt(6)\n\t" "ds_write_b128 %[kx], v[20:23] offset:2048\n\t" "s_waitcnt vmcnt(5)\n\t" "ds_write_b128 %[kx], v[24:27] offset:4096\n\t" "s_waitcnt vmcnt(4)\n\t" \
    "ds_write_b128 %[kx], v[28:31] offset:6144\n\t" "s_waitcnt vmcnt(3)\n\t" "ds_write_b128 %[kx], v[200:203] offset:256\n\t" "s_waitcnt vmcnt(2)\n\t" \
    "ds_write_b128 %[kx], v[204:207] offset:2304\n\t" "s_waitcnt vmcnt(1)\n\t" "ds_write_b128 %[kx], v[208:211] offset:4352\n\t" "s_waitcnt vmcnt(0)\n\t" \
    "ds_write_b128 %[kx], v[212:215] offset:6400\n\t" "s_waitcnt lgkmcnt(0)\n\t" "v_mov_b64 v[208:209], 0\n\t" "v_mov_b64 v[210:211], 0\n\t" "v_mov_b64 v[212:213], 0\n\t" \
    "v_mov_b64 v[214:215], 0\n\t" XT(98, 99) "s_barrier\n\t" XT(82, 83) "buffer_load_dwordx4 v[240:243], %[mo], %[rs], %[sb] offen sc1\n\t" "v_mov_b64 v[224:225], 0\n\t" \
    "v_mov_b64 v[226:227], 0\n\t" "v_mov_b64 v[228:229], 0\n\t" "v_mov_b64 v[230:231], 0\n\t" "v_mov_b64 v[232:233], 0\n\t" "v_mov_b64 v[234:235], 0\n\t" \
    "v_mov_b64 v[236:237], 0\n\t" "v_mov_b64 v[238:239], 0\n\t" "buffer_load_dwordx4 v[244:247], v216, %[rs], %[sb] offen sc1\n\t" \
    "ds_read_b128 v[160:163], %[l0] offset:0\n\t" "ds_read_b128 v[164:167], %[l0] offset:1024\n\t" "ds_read_b128 v[168:171], %[l0] offset:2048\n\t" \
    "ds_read_b128 v[172:175], %[l0] offset:3072\n\t" "s_nop 3\n\t" "buffer_load_dwordx4 v[248:251], %[mo], %[rs], s60 offen sc1\n\t" \
    "ds_read_b128 v[176:179], %[l0] offset:4096\n\t" "ds_read_b128 v[180:183], %[l0] offset:5120\n\t" "ds_read_b128 v[184:187], %[l0] offset:6144\n\t" \
    "ds_read_b128 v[188:191], %[l0] offset:7168\n\t" "s_nop 3\n\t" "buffer_load_dwordx4 v[252:255], v216, %[rs], s60 offen sc1\n\t" "v_mov_b32 v219, 160512\n\t" \
    "v_mov_b64 v[220:221], -1\n\t" "v_mov_b64 v[222:223], -1\n\t" "v_mov_b32 v217, 0\n\t" "buffer_store_dwordx2 v[220:221], %[px], %[rs], %[pr] offen sc1\n\t" \
    "buffer_store_dword v220, %[pm], %[rs], %[pr] offen sc1\n\t" "global_load_dword v217, v217, %[fq] sc1\n\t" "10:\n\t" "s_waitcnt vmcnt(6)\n\t" XT(84, 85) \
    "v_smfmac_f32_16x16x64_bf16 v[224:227], v[240:243], a[0:7], %[ix]\n\t" "ds_write_b128 %[l1], v[240:243] offset:0\n\t" \
    "v_smfmac_f32_16x16x64_bf16 v[228:231], v[240:243], a[8:15], %[ix]\n\t" "v_smfmac_f32_16x16x64_bf16 v[232:235], v[240:243], v[32:39], %[ix]\n\t" \
    "s_waitcnt lgkmcnt(7)\n\t" "v_smfmac_f32_16x16x64_bf16 v[236:239], v[240:243], v[160:167], %[ix]\n\t" "ds_read_b128 v[160:163], %[l0] offset:8192\n\t" \
    "ds_read_b128 v[164:167], %[l0] offset:9216\n\t" "s_waitcnt vmcnt(5)\n\t" "v_smfmac_f32_16x16x64_bf16 v[224:227], v[244:247], a[16:23], %[ix]\n\t" \
    "ds_write_b128 %[l1], v[244:247] offset:1024\n\t" "v_smfmac_f32_16x16x64_bf16 v[228:231], v[244:247], a[24:31], %[ix]\n\t" \
    "v_smfmac_f32_16x16x64_bf16 v[232:235], v[244:247], v[40:47], %[ix]\n\t" "s_waitcnt lgkmcnt(8)\n\t" \
    "v_smfmac_f32_16x16x64_bf16 v[236:239], v[244:247], v[168:175], %[ix]\n\t" "ds_read_b128 v[168:171], %[l0] offset:10240\n\t" \
    "ds_read_b128 v[172:175], %[l0] offset:11264\n\t" "s_waitcnt vmcnt(4)\n\t" "v_smfmac_f32_16x16x64_bf16 v[224:227], v[248:251], a[32:39], %[ix]\n\t" \
    "ds_write_b128 %[l1], v[248:251] offset:2048\n\t" "v_smfmac_f32_16x16x64_bf16 v[228:231], v[248:251], a[40:47], %[ix]\n\t" "s_waitcnt lgkmcnt(3)\n\t" "s_barrier\n\t" \
    XT(86, 87) "v_smfmac_f32_16x16x64_bf16 v[232:235], v[248:251], v[48:55], %[ix]\n\t" "ds_read_b128 v[192:195], %[l1] offset:4096\n\t" \
    "ds_read_b128 v[196:199], %[l1] offset:8192\n\t" "v_smfmac_f32_16x16x64_bf16 v[236:239], v[248:251], v[176:183], %[ix]\n\t" \
    "ds_read_b128 v[176:179], %[l0] offset:12288\n\t" "ds_read_b128 v[180:183], %[l0] offset:13312\n\t" "s_waitcnt vmcnt(3)\n\t" \
    "v_smfmac_f32_16x16x64_bf16 v[224:227], v[252:255], a[48:55], %[ix]\n\t" "ds_write_b128 %[l1], v[252:255] offset:3072\n\t" \
    "v_smfmac_f32_16x16x64_bf16 v[228:231], v[252:255], a[56:63], %[ix]\n\t" "ds_read_b128 v[240:243], %[l1] offset:12288\n\t" \
    "ds_read_b128 v[244:247], %[l1] offset:5120\n\t" "v_smfmac_f32_16x16x64_bf16 v[232:235], v[252:255], v[56:63], %[ix]\n\t" "s_nop 1\n\t" \
    "v_smfmac_f32_16x16x64_bf16 v[236:239], v[252:255], v[184:191], %[ix]\n\t" "ds_read_b128 v[184:187], %[l0] offset:14336\n\t" "v_cmp_u_f32 vcc, v224, v225\n\t" \
    "v_cmp_u_f32 s[62:63], v226, v227\n\t" "s_waitcnt lgkmcnt(7)\n\t" "v_smfmac_f32_16x16x64_bf16 v[224:227], v[192:195], a[64:71], %[ix]\n\t" \
    "ds_read_b128 v[188:191], %[l0] offset:15360\n\t" "v_smfmac_f32_16x16x64_bf16 v[228:231], v[192:195], a[72:79], %[ix]\n\t" "s_or_b64 vcc, vcc, s[62:63]\n\t" \
    "s_cbranch_scc1 11f\n\t" "12:\n\t" "v_smfmac_f32_16x16x64_bf16 v[232:235], v[192:195], v[64:71], %[ix]\n\t" "s_waitcnt lgkmcnt(4)\n\t" "s_barrier\n\t" XT(88, 89) \
    "ds_read_b32 v218, v219\n\t" "v_smfmac_f32_16x16x64_bf16 v[236:239], v[192:195], v[160:167], %[ix]\n\t" "ds_read_b128 v[160:163], %[l0] offset:16384\n\t" \
    "ds_read_b128 v[164:167], %[l0] offset:17408\n\t" "v_smfmac_f32_16x16x64_bf16 v[224:227], v[196:199], a[80:87], %[ix]\n\t" \
    "ds_read_b128 v[248:251], %[l1] offset:9216\n\t" "ds_read_b128 v[252:255], %[l1] offset:13312\n\t" \
    "v_smfmac_f32_16x16x64_bf16 v[228:231], v[196:199], a[88:95], %[ix]\n\t" "ds_read_b128 v[192:195], %[l1] offset:6144\n\t" \
    "v_smfmac_f32_16x16x64_bf16 v[232:235], v[196:199], v[72:79], %[ix]\n\t" "v_smfmac_f32_16x16x64_bf16 v[236:239], v[196:199], v[168:175], %[ix]\n\t" \
    "s_waitcnt lgkmcnt(5)\n\t" "v_cmp_eq_u32 vcc, %[rt], v218\n\t" "v_smfmac_f32_16x16x64_bf16 v[224:227], v[240:243], a[96:103], %[ix]\n\t" "s_cbranch_vccnz 13f\n\t" \
    "ds_read_b128 v[168:171], %[l0] offset:18432\n\t" "v_smfmac_f32_16x16x64_bf16 v[228:231], v[240:243], a[104:111], %[ix]\n\t" \
    "ds_read_b128 v[172:175], %[l0] offset:19456\n\t" "ds_read_b128 v[196:199], %[l1] offset:10240\n\t" \
    "v_smfmac_f32_16x16x64_bf16 v[232:235], v[240:243], v[80:87], %[ix]\n\t" "v_smfmac_f32_16x16x64_bf16 v[236:239], v[240:243], v[176:183], %[ix]\n\t" \
    "ds_read_b128 v[176:179], %[l0] offset:20480\n\t" "ds_read_b128 v[180:183], %[l0] offset:21504\n\t" \
    "v_smfmac_f32_16x16x64_bf16 v[224:227], v[244:247], a[112:119], %[ix]\n\t" "ds_read_b128 v[240:243], %[l1] offset:14336\n\t" \
    "v_smfmac_f32_16x16x64_bf16 v[228:231], v[244:247], a[120:127], %[ix]\n\t" "v_smfmac_f32_16x16x64_bf16 v[232:235], v[244:247], v[88:95], %[ix]\n\t" \
    "v_smfmac_f32_16x16x64_bf16 v[236:239], v[244:247], v[184:191], %[ix]\n\t" "ds_read_b128 v[184:187], %[l0] offset:22528\n\t" \
    "ds_read_b128 v[188:191], %[l0] offset:23552\n\t" "s_waitcnt lgkmcnt(10)\n\t" "v_smfmac_f32_16x16x64_bf16 v[224:227], v[248:251], a[128:135], %[ix]\n\t" \
    "ds_read_b128 v[244:247], %[l1] offset:7168\n\t" "v_smfmac_f32_16x16x64_bf16 v[228:231], v[248:251], a[136:143], %[ix]\n\t" \
    "v_smfmac_f32_16x16x64_bf16 v[232:235], v[248:251], v[96:103], %[ix]\n\t" "v_smfmac_f32_16x16x64_bf16 v[236:239], v[248:251], v[160:167], %[ix]\n\t" \
    "ds_read_b128 v[160:163], %[l0] offset:24576\n\t" "ds_read_b128 v[164:167], %[l0] offset:25600\n\t" "s_waitcnt lgkmcnt(12)\n\t" \
    "v_smfmac_f32_16x16x64_bf16 v[224:227], v[252:255], a[144:151], %[ix]\n\t" "ds_read_b128 v[248:251], %[l1] offset:11264\n\t" \
    "v_smfmac_f32_16x16x64_bf16 v[228:231], v[252:255], a[152:159], %[ix]\n\t" "v_smfmac_f32_16x16x64_bf16 v[232:235], v[252:255], v[104:111], %[ix]\n\t" \
    "s_waitcnt lgkmcnt(10)\n\t" "v_smfmac_f32_16x16x64_bf16 v[236:239], v[252:255], v[168:175], %[ix]\n\t" "ds_read_b128 v[168:171], %[l0] offset:26624\n\t" \
    "ds_read_b128 v[172:175], %[l0] offset:27648\n\t" "v_smfmac_f32_16x16x64_bf16 v[224:227], v[192:195], a[160:167], %[ix]\n\t" \
    "ds_read_b128 v[252:255], %[l1] offset:15360\n\t" "v_smfmac_f32_16x16x64_bf16 v[228:231], v[192:195], a[168:175], %[ix]\n\t" "s_waitcnt lgkmcnt(12)\n\t" \
    "v_smfmac_f32_16x16x64_bf16 v[224:227], v[196:199], a[176:183], %[ix]\n\t" "v_smfmac_f32_16x16x64_bf16 v[228:231], v[196:199], a[184:191], %[ix]\n\t" \
    "s_waitcnt lgkmcnt(9)\n\t" "v_smfmac_f32_16x16x64_bf16 v[224:227], v[240:243], a[192:199], %[ix]\n\t" \
    "v_smfmac_f32_16x16x64_bf16 v[228:231], v[240:243], a[200:207], %[ix]\n\t" "s_waitcnt vmcnt(0)\n\t" "v_readfirstlane_b32 s62, v217\n\t" "s_waitcnt lgkmcnt(6)\n\t" \
    "v_smfmac_f32_16x16x64_bf16 v[224:227], v[244:247], a[208:215], %[ix]\n\t" "v_smfmac_f32_16x16x64_bf16 v[228:231], v[244:247], a[216:223], %[ix]\n\t" \
    "s_max_u32 %[sq], %[sq], s62\n\t" "s_waitcnt lgkmcnt(3)\n\t" "v_smfmac_f32_16x16x64_bf16 v[224:227], v[248:251], a[224:231], %[ix]\n\t" \
    "v_smfmac_f32_16x16x64_bf16 v[228:231], v[248:251], a[232:239], %[ix]\n\t" "s_waitcnt lgkmcnt(0)\n\t" \
    "v_smfmac_f32_16x16x64_bf16 v[224:227], v[252:255], a[240:247], %[ix]\n\t" "s_nop 1\n\t" "v_smfmac_f32_16x16x64_bf16 v[228:231], v[252:255], a[248:255], %[ix]\n\t" \
    XT(90, 91) "s_nop 1\n\t" "v_smfmac_f32_16x16x64_bf16 v[232:235], v[192:195], v[112:119], %[ix]\n\t" "s_nop 1\n\t" \
    "v_smfmac_f32_16x16x64_bf16 v[236:239], v[192:195], v[176:183], %[ix]\n\t" "ds_read_b128 v[176:179], %[l0] offset:28672\n\t" "v_add_f32 v224, v224, v225\n\t" \
    "v_add_f32 v226, v226, v227\n\t" "v_smfmac_f32_16x16x64_bf16 v[232:235], v[196:199], v[120:127], %[ix]\n\t" "ds_read_b128 v[180:183], %[l0] offset:29696\n\t" \
    "v_add_f32 v228, v228, v229\n\t" "v_add_f32 v230, v230, v231\n\t" "v_smfmac_f32_16x16x64_bf16 v[236:239], v[196:199], v[184:191], %[ix]\n\t" \
    "ds_read_b128 v[184:187], %[l0] offset:30720\n\t" "v_cvt_pk_bf16_f32 v218, v224, v228\n\t" "v_cvt_pk_bf16_f32 v219, v226, v230\n\t" \
    "v_smfmac_f32_16x16x64_bf16 v[232:235], v[240:243], v[128:135], %[ix]\n\t" "ds_write2_b32 %[qw], v218, v219 offset1:8\n\t" \
    "ds_read_b128 v[188:191], %[l0] offset:31744\n\t" "v_smfmac_f32_16x16x64_bf16 v[236:239], v[240:243], v[160:167], %[ix]\n\t" \
    "ds_read_b128 v[192:195], %[kx] offset:4352\n\t" "ds_read_b128 v[196:199], %[kx] offset:6400\n\t" \
    "v_smfmac_f32_16x16x64_bf16 v[232:235], v[244:247], v[136:143], %[ix]\n\t" "v_smfmac_f32_16x16x64_bf16 v[236:239], v[244:247], v[168:175], %[ix]\n\t" \
    "s_waitcnt lgkmcnt(3)\n\t" "s_barrier\n\t" "v_smfmac_f32_16x16x64_bf16 v[232:235], v[248:251], v[144:151], %[ix]\n\t" "ds_read_b128 v[216:219], %[l1] offset:24576\n\t" \
    "ds_read_b128 v[220:223], %[l1] offset:25600\n\t" "v_smfmac_f32_16x16x64_bf16 v[236:239], v[248:251], v[176:183], %[ix]\n\t" \
    "v_smfmac_f32_16x16x64_bf16 v[232:235], v[252:255], v[152:159], %[ix]\n\t" "s_waitcnt lgkmcnt(4)\n\t" \
    "v_smfmac_f32_16x16x64_bf16 v[236:239], v[252:255], v[184:191], %[ix]\n\t" XT(92, 93) "s_waitcnt lgkmcnt(1)\n\t" \
    "v_smfmac_f32_16x16x64_bf16 v[208:211], v[216:219], v[16:23], %[ix]\n\t" "s_nop 1\n\t" "v_smfmac_f32_16x16x64_bf16 v[212:215], v[216:219], v[200:207], %[ix]\n\t" \
    "s_waitcnt lgkmcnt(0)\n\t" "s_nop 0\n\t" "v_add_f32 v232, v232, v233\n\t" "v_add_f32 v234, v234, v235\n\t" "v_add_f32 v236, v236, v237\n\t" \
    "v_add_f32 v238, v238, v239\n\t" "v_cvt_pk_bf16_f32 v232, v232, v236\n\t" "v_cvt_pk_bf16_f32 v233, v234, v238\n\t" "ds_write2_b32 %[kw], v232, v233 offset1:68\n\t" \
    "v_smfmac_f32_16x16x64_bf16 v[208:211], v[220:223], v[24:31], %[ix]\n\t" "v_smfmac_f32_16x16x64_bf16 v[212:215], v[220:223], v[192:199], %[ix]\n\t" \
    "v_mov_b64 v[224:225], 0\n\t" "v_mov_b64 v[226:227], 0\n\t" "s_waitcnt lgkmcnt(0)\n\t" "s_barrier\n\t" XT(94, 95) "ds_read_b128 v[160:163], %[km] offset:0\n\t" \
    "ds_read_b128 v[164:167], %[km] offset:64\n\t" "ds_read_b128 v[168:171], %[km] offset:128\n\t" "ds_read_b128 v[172:175], %[km] offset:192\n\t" \
    "v_add_f32 v178, v208, v209\n\t" "v_add_f32 v179, v210, v211\n\t" "v_add_f32 v180, v212, v213\n\t" "v_add_f32 v181, v214, v215\n\t" "v_max_f32 v182, v178, v180\n\t" \
    "v_max_f32 v183, v179, v181\n\t" "s_nop 0\n\t" "v_max_f32_dpp v182, v182, v182 row_ror:8 row_mask:0xf bank_mask:0xf\n\t" \
    "v_max_f32_dpp v183, v183, v183 row_ror:8 row_mask:0xf bank_mask:0xf\n\t" "s_nop 0\n\t" "v_max_f32_dpp v182, v182, v182 row_ror:4 row_mask:0xf bank_mask:0xf\n\t" \
    "v_max_f32_dpp v183, v183, v183 row_ror:4 row_mask:0xf bank_mask:0xf\n\t" "s_nop 0\n\t" "v_max_f32_dpp v182, v182, v182 row_ror:2 row_mask:0xf bank_mask:0xf\n\t" \
    "v_max_f32_dpp v183, v183, v183 row_ror:2 row_mask:0xf bank_mask:0xf\n\t" "s_nop 0\n\t" "v_max_f32_dpp v182, v182, v182 row_ror:1 row_mask:0xf bank_mask:0xf\n\t" \
    "v_max_f32_dpp v183, v183, v183 row_ror:1 row_mask:0xf bank_mask:0xf\n\t" "s_waitcnt lgkmcnt(2)\n\t" \
    "v_smfmac_f32_16x16x64_bf16 v[224:227], v[216:219], v[160:167], %[ix]\n\t" "s_waitcnt lgkmcnt(0)\n\t" \
    "v_smfmac_f32_16x16x64_bf16 v[224:227], v[220:223], v[168:175], %[ix]\n\t" "s_nop 7\n\t" "v_add_f32 v176, v224, v225\n\t" "v_add_f32 v177, v226, v227\n\t" \
    "v_max_f32 v182, v176, v182\n\t" "v_max_f32 v183, v177, v183\n\t" "s_nop 0\n\t" "v_max_f32_dpp v182, v182, v182 row_ror:4 row_mask:0xf bank_mask:0xf\n\t" \
    "v_max_f32_dpp v183, v183, v183 row_ror:4 row_mask:0xf bank_mask:0xf\n\t" "s_nop 0\n\t" "v_max_f32_dpp v182, v182, v182 row_ror:2 row_mask:0xf bank_mask:0xf\n\t" \
    "v_max_f32_dpp v183, v183, v183 row_ror:2 row_mask:0xf bank_mask:0xf\n\t" "s_nop 0\n\t" "v_max_f32_dpp v182, v182, v182 row_ror:1 row_mask:0xf bank_mask:0xf\n\t" \
    "v_max_f32_dpp v183, v183, v183 row_ror:1 row_mask:0xf bank_mask:0xf\n\t" "v_pk_add_f32 v[178:179], v[178:179], v[182:183] neg_lo:[0,1] neg_hi:[0,1]\n\t" \
    "v_pk_add_f32 v[180:181], v[180:181], v[182:183] neg_lo:[0,1] neg_hi:[0,1]\n\t" "v_pk_add_f32 v[176:177], v[176:177], v[182:183] neg_lo:[0,1] neg_hi:[0,1]\n\t" \
    "v_pk_mul_f32 v[178:179], v[178:179], s[66:67]\n\t" "v_pk_mul_f32 v[180:181], v[180:181], s[66:67]\n\t" "v_pk_mul_f32 v[176:177], v[176:177], s[66:67]\n\t" \
    "v_exp_f32 v178, v178\n\t" "v_exp_f32 v179, v179\n\t" "v_exp_f32 v180, v180\n\t" "v_exp_f32 v181, v181\n\t" "v_exp_f32 v176, v176\n\t" \
    "v_pk_add_f32 v[182:183], v[178:179], v[180:181]\n\t" "v_exp_f32 v177, v177\n\t" "s_nop 0\n\t" "v_add_f32_dpp v182, v182, v182 row_ror:8 row_mask:0xf bank_mask:0xf\n\t" \
    "v_add_f32_dpp v183, v183, v183 row_ror:8 row_mask:0xf bank_mask:0xf\n\t" "v_add_f32 v182, v182, v176\n\t" "v_add_f32 v183, v183, v177\n\t" "s_nop 0\n\t" \
    "v_add_f32_dpp v182, v182, v182 row_ror:4 row_mask:0xf bank_mask:0xf\n\t" "v_add_f32_dpp v183, v183, v183 row_ror:4 row_mask:0xf bank_mask:0xf\n\t" "s_nop 0\n\t" \
    "v_add_f32_dpp v182, v182, v182 row_ror:2 row_mask:0xf bank_mask:0xf\n\t" "v_add_f32_dpp v183, v183, v183 row_ror:2 row_mask:0xf bank_mask:0xf\n\t" "s_nop 0\n\t" \
    "v_add_f32_dpp v182, v182, v182 row_ror:1 row_mask:0xf bank_mask:0xf\n\t" "v_add_f32_dpp v183, v183, v183 row_ror:1 row_mask:0xf bank_mask:0xf\n\t" \
    "v_rcp_f32 v182, v182\n\t" "v_rcp_f32 v183, v183\n\t" "s_nop 0\n\t" "v_pk_mul_f32 v[176:177], v[176:177], v[182:183]\n\t" \
    "v_pk_mul_f32 v[178:179], v[178:179], v[182:183]\n\t" "v_pk_mul_f32 v[180:181], v[180:181], v[182:183]\n\t" "v_cvt_pk_bf16_f32 v176, v176, v177\n\t" \
    "v_cvt_pk_bf16_f32 v178, v178, v179\n\t" "v_cvt_pk_bf16_f32 v180, v180, v181\n\t" "v_mov_b32_dpp v177, v176 quad_perm:[1,0,3,2] row_mask:0xf bank_mask:0xf\n\t" \
    "v_mov_b32_dpp v179, v178 quad_perm:[1,0,3,2] row_mask:0xf bank_mask:0xf\n\t" "v_mov_b32_dpp v181, v180 quad_perm:[1,0,3,2] row_mask:0xf bank_mask:0xf\n\t" \
    "v_perm_b32 v184, v179, v178, %[ps]\n\t" "v_perm_b32 v185, v181, v180, %[ps]\n\t" "buffer_store_dwordx2 v[184:185], %[px], %[rs], %[pb] offen sc1\n\t" \
    "v_perm_b32 v186, v177, v176, %[ps]\n\t" "buffer_store_dword v186, %[pm], %[rs], %[pb] offen sc1\n\t" XT(96, 97) LMARK "s_branch 99f\n\t" "11:\n\t" \
    "ds_read_b128 v[220:223], %[l1] offset:0\n\t" "s_waitcnt lgkmcnt(0)\n\t" "v_max3_u32 v218, v220, v221, v222\n\t" "v_max_u32 v218, v218, v223\n\t" \
    "ds_read_b128 v[220:223], %[l1] offset:1024\n\t" "s_waitcnt lgkmcnt(0)\n\t" "v_max3_u32 v218, v218, v220, v221\n\t" "v_max3_u32 v218, v218, v222, v223\n\t" \
    "ds_read_b128 v[220:223], %[l1] offset:2048\n\t" "s_waitcnt lgkmcnt(0)\n\t" "v_max3_u32 v218, v218, v220, v221\n\t" "v_max3_u32 v218, v218, v222, v223\n\t" \
    "ds_read_b128 v[220:223], %[l1] offset:3072\n\t" "s_waitcnt lgkmcnt(0)\n\t" "v_max3_u32 v218, v218, v220, v221\n\t" "v_max3_u32 v218, v218, v222, v223\n\t" \
    "v_cmp_eq_u32 vcc, -1, v218\n\t" "s_cbranch_vccz 12b\n\t" "s_cmp_eq_u32 %[rt], 1\n\t" "s_cbranch_scc1 12b\n\t" "v_mov_b32 v218, %[rt]\n\t" "ds_write_b32 v219, v218\n\t" \
    "s_waitcnt lgkmcnt(0)\n\t" "s_branch 12b\n\t" "13:\n\t" "s_sub_u32 %[rt], %[rt], 1\n\t" "s_waitcnt lgkmcnt(0)\n\t" XCNT(100) "14:\n\t" \
    "buffer_load_dwordx4 v[240:243], %[mo], %[rs], %[sb] offen sc1\n\t" "buffer_load_dwordx4 v[244:247], v216, %[rs], %[sb] offen sc1\n\t" \
    "buffer_load_dwordx4 v[248:251], %[mo], %[rs], s60 offen sc1\n\t" "buffer_load_dwordx4 v[252:255], v216, %[rs], s60 offen sc1\n\t" "s_waitcnt vmcnt(0)\n\t" \
    "v_max_u32 v218, v240, v241\n\t" "v_max3_u32 v218, v218, v242, v243\n\t" "v_max3_u32 v218, v218, v244, v245\n\t" "v_max3_u32 v218, v218, v246, v247\n\t" \
    "v_max3_u32 v218, v218, v248, v249\n\t" "v_max3_u32 v218, v218, v250, v251\n\t" "v_max3_u32 v218, v218, v252, v253\n\t" "v_max3_u32 v218, v218, v254, v255\n\t" \
    "v_cmp_eq_u32 vcc, -1, v218\n\t" "s_cbranch_vccz 15f\n\t" "s_cmp_eq_u32 %[bd], 0\n\t" "s_cbranch_scc1 15f\n\t" "s_sub_u32 %[bd], %[bd], 1\n\t" XCNT(101) \
    "s_branch 14b\n\t" "15:\n\t" "ds_read_b128 v[160:163], %[l0] offset:0\n\t" "ds_read_b128 v[164:167], %[l0] offset:1024\n\t" \
    "ds_read_b128 v[168:171], %[l0] offset:2048\n\t" "ds_read_b128 v[172:175], %[l0] offset:3072\n\t" "ds_read_b128 v[176:179], %[l0] offset:4096\n\t" \
    "ds_read_b128 v[180:183], %[l0] offset:5120\n\t" "ds_read_b128 v[184:187], %[l0] offset:6144\n\t" "ds_read_b128 v[188:191], %[l0] offset:7168\n\t" \
    "v_mov_b64 v[224:225], 0\n\t" "v_mov_b64 v[226:227], 0\n\t" "v_mov_b64 v[228:229], 0\n\t" "v_mov_b64 v[230:231], 0\n\t" "v_mov_b64 v[232:233], 0\n\t" \
    "v_mov_b64 v[234:235], 0\n\t" "v_mov_b64 v[236:237], 0\n\t" "v_mov_b64 v[238:239], 0\n\t" "s_barrier\n\t" "s_branch 10b\n\t" "99:\n\t"
#define QK_STEP1 \
    ".p2align 6\n\t" "v_xor_b32 v216, 0x400, %[mo]\n\t" "s_xor_b32 s60, %[sb], 0x800\n\t" "s_add_u32 s61, %[qb], 0x1000\n\t" "s_mov_b32 s66, 0x3e0293ee\n\t" \
    "s_mov_b32 s67, 0x3e0293ee\n\t" XT(80, 81) XT(98, 99) "s_barrier\n\t" XT(82, 83) "buffer_load_dwordx4 v[240:243], %[mo], %[rs], %[sb] offen sc1\n\t" \
    "v_mov_b64 v[224:225], 0\n\t" "v_mov_b64 v[226:227], 0\n\t" "v_mov_b64 v[228:229], 0\n\t" "v_mov_b64 v[230:231], 0\n\t" "v_mov_b64 v[232:233], 0\n\t" \
    "v_mov_b64 v[234:235], 0\n\t" "v_mov_b64 v[236:237], 0\n\t" "v_mov_b64 v[238:239], 0\n\t" "buffer_load_dwordx4 v[244:247], v216, %[rs], %[sb] offen sc1\n\t" \
    "ds_read_b128 v[160:163], %[l0] offset:0\n\t" "ds_read_b128 v[164:167], %[l0] offset:1024\n\t" "ds_read_b128 v[168:171], %[l0] offset:2048\n\t" \
    "ds_read_b128 v[172:175], %[l0] offset:3072\n\t" "s_nop 3\n\t" "buffer_load_dwordx4 v[248:251], %[mo], %[rs], s60 offen sc1\n\t" \
    "ds_read_b128 v[176:179], %[l0] offset:4096\n\t" "ds_read_b128 v[180:183], %[l0] offset:5120\n\t" "ds_read_b128 v[184:187], %[l0] offset:6144\n\t" \
    "ds_read_b128 v[188:191], %[l0] offset:7168\n\t" "s_nop 3\n\t" "buffer_load_dwordx4 v[252:255], v216, %[rs], s60 offen sc1\n\t" "v_mov_b32 v219, 160512\n\t" \
    "v_mov_b64 v[220:221], -1\n\t" "v_mov_b64 v[222:223], -1\n\t" "buffer_store_dwordx2 v[220:221], %[px], %[rs], %[tr] offen sc1\n\t" \
    "buffer_store_dwordx2 v[220:221], %[pm], %[rs], %[tr] offen sc1\n\t" "v_mov_b32 v208, 0xd028965f\n\t" "v_mov_b32 v209, 0xff800000\n\t" "s_mov_b32 s62, 0\n\t" \
    "s_mov_b32 s63, -1\n\t" "v_mov_b32 v210, 0\n\t" "v_cmp_gt_i32 vcc, 0, %[ps]\n\t" "v_cndmask_b32 v204, 0, v209, s[62:63]\n\t" "s_nop 0\n\t" \
    "v_cndmask_b32 v200, 0, v208, vcc\n\t" "v_cmp_gt_i32 vcc, 1, %[ps]\n\t" "v_cndmask_b32 v205, 0, v209, s[62:63]\n\t" "s_nop 0\n\t" "v_cndmask_b32 v201, 0, v208, vcc\n\t" \
    "v_cmp_gt_i32 vcc, 2, %[ps]\n\t" "v_cndmask_b32 v206, 0, v209, s[62:63]\n\t" "s_nop 0\n\t" "v_cndmask_b32 v202, 0, v208, vcc\n\t" "v_cmp_gt_i32 vcc, 3, %[ps]\n\t" \
    "v_cndmask_b32 v207, 0, v209, s[62:63]\n\t" "s_nop 0\n\t" "v_cndmask_b32 v203, 0, v208, vcc\n\t" "20:\n\t" "s_waitcnt vmcnt(5)\n\t" XT(84, 85) \
    "v_smfmac_f32_16x16x64_bf16 v[224:227], v[240:243], a[0:7], %[ix]\n\t" "ds_write_b128 %[l1], v[240:243] offset:4096\n\t" \
    "v_smfmac_f32_16x16x64_bf16 v[228:231], v[240:243], a[8:15], %[ix]\n\t" "v_smfmac_f32_16x16x64_bf16 v[232:235], v[240:243], v[32:39], %[ix]\n\t" \
    "s_waitcnt lgkmcnt(7)\n\t" "v_smfmac_f32_16x16x64_bf16 v[236:239], v[240:243], v[160:167], %[ix]\n\t" "ds_read_b128 v[160:163], %[l0] offset:8192\n\t" \
    "ds_read_b128 v[164:167], %[l0] offset:9216\n\t" "s_waitcnt vmcnt(4)\n\t" "v_smfmac_f32_16x16x64_bf16 v[224:227], v[244:247], a[16:23], %[ix]\n\t" \
    "ds_write_b128 %[l1], v[244:247] offset:5120\n\t" "v_smfmac_f32_16x16x64_bf16 v[228:231], v[244:247], a[24:31], %[ix]\n\t" \
    "v_smfmac_f32_16x16x64_bf16 v[232:235], v[244:247], v[40:47], %[ix]\n\t" "s_waitcnt lgkmcnt(8)\n\t" \
    "v_smfmac_f32_16x16x64_bf16 v[236:239], v[244:247], v[168:175], %[ix]\n\t" "ds_read_b128 v[168:171], %[l0] offset:10240\n\t" \
    "ds_read_b128 v[172:175], %[l0] offset:11264\n\t" "s_waitcnt vmcnt(3)\n\t" "v_smfmac_f32_16x16x64_bf16 v[224:227], v[248:251], a[32:39], %[ix]\n\t" \
    "ds_write_b128 %[l1], v[248:251] offset:6144\n\t" "v_smfmac_f32_16x16x64_bf16 v[228:231], v[248:251], a[40:47], %[ix]\n\t" "s_waitcnt lgkmcnt(3)\n\t" "s_barrier\n\t" \
    XT(86, 87) "v_smfmac_f32_16x16x64_bf16 v[232:235], v[248:251], v[48:55], %[ix]\n\t" "ds_read_b128 v[192:195], %[l1] offset:8192\n\t" \
    "ds_read_b128 v[196:199], %[l1] offset:12288\n\t" "v_smfmac_f32_16x16x64_bf16 v[236:239], v[248:251], v[176:183], %[ix]\n\t" \
    "ds_read_b128 v[176:179], %[l0] offset:12288\n\t" "ds_read_b128 v[180:183], %[l0] offset:13312\n\t" "s_waitcnt vmcnt(2)\n\t" \
    "v_smfmac_f32_16x16x64_bf16 v[224:227], v[252:255], a[48:55], %[ix]\n\t" "ds_write_b128 %[l1], v[252:255] offset:7168\n\t" \
    "v_smfmac_f32_16x16x64_bf16 v[228:231], v[252:255], a[56:63], %[ix]\n\t" "ds_read_b128 v[240:243], %[l1] offset:0\n\t" "ds_read_b128 v[244:247], %[l1] offset:9216\n\t" \
    "v_smfmac_f32_16x16x64_bf16 v[232:235], v[252:255], v[56:63], %[ix]\n\t" "s_nop 1\n\t" "v_smfmac_f32_16x16x64_bf16 v[236:239], v[252:255], v[184:191], %[ix]\n\t" \
    "ds_read_b128 v[184:187], %[l0] offset:14336\n\t" "v_cmp_u_f32 vcc, v224, v225\n\t" "v_cmp_u_f32 s[62:63], v226, v227\n\t" "s_waitcnt lgkmcnt(7)\n\t" \
    "v_smfmac_f32_16x16x64_bf16 v[224:227], v[192:195], a[64:71], %[ix]\n\t" "ds_read_b128 v[188:191], %[l0] offset:15360\n\t" \
    "v_smfmac_f32_16x16x64_bf16 v[228:231], v[192:195], a[72:79], %[ix]\n\t" "s_or_b64 vcc, vcc, s[62:63]\n\t" "s_cbranch_scc1 21f\n\t" "22:\n\t" \
    "v_smfmac_f32_16x16x64_bf16 v[232:235], v[192:195], v[64:71], %[ix]\n\t" "s_waitcnt lgkmcnt(4)\n\t" "s_barrier\n\t" XT(88, 89) "ds_read_b32 v218, v219\n\t" \
    "v_smfmac_f32_16x16x64_bf16 v[236:239], v[192:195], v[160:167], %[ix]\n\t" "ds_read_b128 v[160:163], %[l0] offset:16384\n\t" \
    "ds_read_b128 v[164:167], %[l0] offset:17408\n\t" "v_smfmac_f32_16x16x64_bf16 v[224:227], v[196:199], a[80:87], %[ix]\n\t" \
    "ds_read_b128 v[248:251], %[l1] offset:13312\n\t" "ds_read_b128 v[252:255], %[l1] offset:1024\n\t" \
    "v_smfmac_f32_16x16x64_bf16 v[228:231], v[196:199], a[88:95], %[ix]\n\t" "ds_read_b128 v[192:195], %[l1] offset:10240\n\t" \
    "v_smfmac_f32_16x16x64_bf16 v[232:235], v[196:199], v[72:79], %[ix]\n\t" "v_smfmac_f32_16x16x64_bf16 v[236:239], v[196:199], v[168:175], %[ix]\n\t" \
    "s_waitcnt lgkmcnt(5)\n\t" "v_cmp_eq_u32 vcc, %[rt], v218\n\t" "v_smfmac_f32_16x16x64_bf16 v[224:227], v[240:243], a[96:103], %[ix]\n\t" "s_cbranch_vccnz 23f\n\t" \
    "ds_read_b128 v[168:171], %[l0] offset:18432\n\t" "v_smfmac_f32_16x16x64_bf16 v[228:231], v[240:243], a[104:111], %[ix]\n\t" \
    "ds_read_b128 v[172:175], %[l0] offset:19456\n\t" "ds_read_b128 v[196:199], %[l1] offset:14336\n\t" \
    "v_smfmac_f32_16x16x64_bf16 v[232:235], v[240:243], v[80:87], %[ix]\n\t" "buffer_load_dwordx4 v[16:19], %[hq], %[rs], %[qb] offen offset:0 sc1\n\t" \
    "v_smfmac_f32_16x16x64_bf16 v[236:239], v[240:243], v[176:183], %[ix]\n\t" "ds_read_b128 v[176:179], %[l0] offset:20480\n\t" \
    "ds_read_b128 v[180:183], %[l0] offset:21504\n\t" "v_smfmac_f32_16x16x64_bf16 v[224:227], v[244:247], a[112:119], %[ix]\n\t" \
    "ds_read_b128 v[240:243], %[l1] offset:2048\n\t" "v_smfmac_f32_16x16x64_bf16 v[228:231], v[244:247], a[120:127], %[ix]\n\t" \
    "v_smfmac_f32_16x16x64_bf16 v[232:235], v[244:247], v[88:95], %[ix]\n\t" "buffer_load_dwordx4 v[20:23], %[hq], %[rs], %[qb] offen offset:2048 sc1\n\t" \
    "v_smfmac_f32_16x16x64_bf16 v[236:239], v[244:247], v[184:191], %[ix]\n\t" "ds_read_b128 v[184:187], %[l0] offset:22528\n\t" \
    "ds_read_b128 v[188:191], %[l0] offset:23552\n\t" "s_waitcnt lgkmcnt(10)\n\t" "v_smfmac_f32_16x16x64_bf16 v[224:227], v[248:251], a[128:135], %[ix]\n\t" \
    "ds_read_b128 v[244:247], %[l1] offset:11264\n\t" "v_smfmac_f32_16x16x64_bf16 v[228:231], v[248:251], a[136:143], %[ix]\n\t" \
    "v_smfmac_f32_16x16x64_bf16 v[232:235], v[248:251], v[96:103], %[ix]\n\t" "buffer_load_dwordx4 v[24:27], %[hq], %[rs], s61 offen offset:0 sc1\n\t" \
    "v_smfmac_f32_16x16x64_bf16 v[236:239], v[248:251], v[160:167], %[ix]\n\t" "ds_read_b128 v[160:163], %[l0] offset:24576\n\t" \
    "ds_read_b128 v[164:167], %[l0] offset:25600\n\t" "s_waitcnt lgkmcnt(12)\n\t" "v_smfmac_f32_16x16x64_bf16 v[224:227], v[252:255], a[144:151], %[ix]\n\t" \
    "ds_read_b128 v[248:251], %[l1] offset:15360\n\t" "v_smfmac_f32_16x16x64_bf16 v[228:231], v[252:255], a[152:159], %[ix]\n\t" \
    "v_smfmac_f32_16x16x64_bf16 v[232:235], v[252:255], v[104:111], %[ix]\n\t" "buffer_load_dwordx4 v[28:31], %[hq], %[rs], s61 offen offset:2048 sc1\n\t" \
    "s_waitcnt lgkmcnt(10)\n\t" "v_smfmac_f32_16x16x64_bf16 v[236:239], v[252:255], v[168:175], %[ix]\n\t" "ds_read_b128 v[168:171], %[l0] offset:26624\n\t" \
    "ds_read_b128 v[172:175], %[l0] offset:27648\n\t" "v_smfmac_f32_16x16x64_bf16 v[224:227], v[192:195], a[160:167], %[ix]\n\t" \
    "ds_read_b128 v[252:255], %[l1] offset:3072\n\t" "v_smfmac_f32_16x16x64_bf16 v[228:231], v[192:195], a[168:175], %[ix]\n\t" "s_waitcnt lgkmcnt(12)\n\t" \
    "v_smfmac_f32_16x16x64_bf16 v[224:227], v[196:199], a[176:183], %[ix]\n\t" "v_smfmac_f32_16x16x64_bf16 v[228:231], v[196:199], a[184:191], %[ix]\n\t" \
    "s_waitcnt lgkmcnt(9)\n\t" "v_smfmac_f32_16x16x64_bf16 v[224:227], v[240:243], a[192:199], %[ix]\n\t" \
    "v_smfmac_f32_16x16x64_bf16 v[228:231], v[240:243], a[200:207], %[ix]\n\t" "s_waitcnt lgkmcnt(6)\n\t" \
    "v_smfmac_f32_16x16x64_bf16 v[224:227], v[244:247], a[208:215], %[ix]\n\t" "v_smfmac_f32_16x16x64_bf16 v[228:231], v[244:247], a[216:223], %[ix]\n\t" \
    "s_waitcnt vmcnt(4)\n\t" "s_waitcnt lgkmcnt(3)\n\t" "v_smfmac_f32_16x16x64_bf16 v[224:227], v[248:251], a[224:231], %[ix]\n\t" \
    "v_smfmac_f32_16x16x64_bf16 v[228:231], v[248:251], a[232:239], %[ix]\n\t" "s_waitcnt lgkmcnt(0)\n\t" \
    "v_smfmac_f32_16x16x64_bf16 v[224:227], v[252:255], a[240:247], %[ix]\n\t" "s_nop 1\n\t" "v_smfmac_f32_16x16x64_bf16 v[228:231], v[252:255], a[248:255], %[ix]\n\t" \
    XT(90, 91) "s_nop 1\n\t" "v_smfmac_f32_16x16x64_bf16 v[232:235], v[192:195], v[112:119], %[ix]\n\t" "s_nop 1\n\t" \
    "v_smfmac_f32_16x16x64_bf16 v[236:239], v[192:195], v[176:183], %[ix]\n\t" "ds_read_b128 v[176:179], %[l0] offset:28672\n\t" "v_add_f32 v224, v224, v225\n\t" \
    "v_add_f32 v226, v226, v227\n\t" "v_smfmac_f32_16x16x64_bf16 v[232:235], v[196:199], v[120:127], %[ix]\n\t" "ds_read_b128 v[180:183], %[l0] offset:29696\n\t" \
    "v_add_f32 v228, v228, v229\n\t" "v_add_f32 v230, v230, v231\n\t" "v_smfmac_f32_16x16x64_bf16 v[236:239], v[196:199], v[184:191], %[ix]\n\t" \
    "ds_read_b128 v[184:187], %[l0] offset:30720\n\t" "v_cvt_pk_bf16_f32 v218, v224, v228\n\t" "v_cvt_pk_bf16_f32 v219, v226, v230\n\t" \
    "v_smfmac_f32_16x16x64_bf16 v[232:235], v[240:243], v[128:135], %[ix]\n\t" "ds_write2_b32 %[qw], v218, v219 offset1:8\n\t" \
    "ds_read_b128 v[188:191], %[l0] offset:31744\n\t" "v_smfmac_f32_16x16x64_bf16 v[236:239], v[240:243], v[160:167], %[ix]\n\t" \
    "v_smfmac_f32_16x16x64_bf16 v[232:235], v[244:247], v[136:143], %[ix]\n\t" "v_smfmac_f32_16x16x64_bf16 v[236:239], v[244:247], v[168:175], %[ix]\n\t" \
    "s_waitcnt lgkmcnt(1)\n\t" "s_barrier\n\t" "v_smfmac_f32_16x16x64_bf16 v[232:235], v[248:251], v[144:151], %[ix]\n\t" \
    "v_smfmac_f32_16x16x64_bf16 v[236:239], v[248:251], v[176:183], %[ix]\n\t" "v_smfmac_f32_16x16x64_bf16 v[232:235], v[252:255], v[152:159], %[ix]\n\t" \
    "s_waitcnt lgkmcnt(0)\n\t" "v_smfmac_f32_16x16x64_bf16 v[236:239], v[252:255], v[184:191], %[ix]\n\t" XT(92, 93) "s_nop 6\n\t" "v_add_f32 v232, v232, v233\n\t" \
    "v_add_f32 v234, v234, v235\n\t" "v_add_f32 v236, v236, v237\n\t" "v_add_f32 v238, v238, v239\n\t" "v_cvt_pk_bf16_f32 v232, v232, v236\n\t" \
    "v_cvt_pk_bf16_f32 v233, v234, v238\n\t" "ds_write2_b32 %[kw], v232, v233 offset1:68\n\t" "s_waitcnt lgkmcnt(0)\n\t" "s_barrier\n\t" XT(94, 95) \
    "s_cmp_ge_u32 %[sq], %[sn]\n\t" "s_cbranch_scc1 277f\n\t" "275:\n\t" "global_load_dword v240, v210, %[fq] sc1\n\t" "s_waitcnt vmcnt(0)\n\t" \
    "v_readfirstlane_b32 %[sq], v240\n\t" "s_nop 3\n\t" "s_cmp_ge_u32 %[sq], %[sn]\n\t" "s_cbranch_scc1 276f\n\t" "s_cmp_eq_u32 %[bd], 0\n\t" "s_cbranch_scc1 276f\n\t" \
    "s_sub_u32 %[bd], %[bd], 1\n\t" "s_sleep 2\n\t" "s_branch 275b\n\t" "276:\n\t" "buffer_load_dwordx4 v[16:19], %[hq], %[rs], %[qb] offen offset:0 sc1\n\t" \
    "buffer_load_dwordx4 v[20:23], %[hq], %[rs], %[qb] offen offset:2048 sc1\n\t" "buffer_load_dwordx4 v[24:27], %[hq], %[rs], s61 offen offset:0 sc1\n\t" \
    "buffer_load_dwordx4 v[28:31], %[hq], %[rs], s61 offen offset:2048 sc1\n\t" "277:\n\t" "s_nop 3\n\t" "ds_read_b128 v[160:163], %[kx] offset:0\n\t" \
    "ds_read_b128 v[164:167], %[kx] offset:2048\n\t" "ds_read_b128 v[168:171], %[kx] offset:4096\n\t" "ds_read_b128 v[172:175], %[kx] offset:6144\n\t" \
    "ds_read_b128 v[224:227], %[km] offset:0\n\t" "ds_read_b128 v[228:231], %[km] offset:64\n\t" "ds_read_b128 v[232:235], %[km] offset:128\n\t" \
    "ds_read_b128 v[236:239], %[km] offset:192\n\t" "s_waitcnt vmcnt(0)\n\t" "global_load_dword v240, v210, %[fq] sc1\n\t" "s_waitcnt lgkmcnt(7)\n\t" \
    "v_mfma_f32_16x16x32_bf16 v[248:251], v[160:163], v[16:19], v[200:203]\n\t" "s_waitcnt lgkmcnt(6)\n\t" \
    "v_mfma_f32_16x16x32_bf16 v[248:251], v[164:167], v[20:23], v[248:251]\n\t" "s_waitcnt lgkmcnt(5)\n\t" \
    "v_mfma_f32_16x16x32_bf16 v[248:251], v[168:171], v[24:27], v[248:251]\n\t" "s_waitcnt lgkmcnt(4)\n\t" \
    "v_mfma_f32_16x16x32_bf16 v[248:251], v[172:175], v[28:31], v[248:251]\n\t" "s_waitcnt lgkmcnt(3)\n\t" \
    "v_mfma_f32_16x16x32_bf16 v[244:247], v[224:227], v[16:19], v[204:207]\n\t" "s_waitcnt lgkmcnt(2)\n\t" \
    "v_mfma_f32_16x16x32_bf16 v[244:247], v[228:231], v[20:23], v[244:247]\n\t" "s_waitcnt lgkmcnt(1)\n\t" \
    "v_mfma_f32_16x16x32_bf16 v[244:247], v[232:235], v[24:27], v[244:247]\n\t" "s_waitcnt lgkmcnt(0)\n\t" \
    "v_mfma_f32_16x16x32_bf16 v[244:247], v[236:239], v[28:31], v[244:247]\n\t" "s_nop 3\n\t" "v_max3_f32 v242, v248, v249, v250\n\t" "v_max_f32 v242, v242, v251\n\t" \
    "s_nop 1\n\t" "v_max3_f32 v242, v242, v244, v245\n\t" "v_max3_f32 v242, v242, v246, v247\n\t" "v_mov_b32 v243, v242\n\t" "s_nop 1\n\t" \
    "v_permlane32_swap_b32 v242, v243\n\t" "v_max_f32 v242, v242, v243\n\t" "v_mov_b32 v243, v242\n\t" "s_nop 1\n\t" "v_permlane16_swap_b32 v242, v243\n\t" \
    "v_max_f32 v242, v242, v243\n\t" "v_pk_add_f32 v[244:245], v[244:245], v[242:243] op_sel_hi:[1,0] neg_lo:[0,1] neg_hi:[0,1]\n\t" \
    "v_pk_add_f32 v[246:247], v[246:247], v[242:243] op_sel_hi:[1,0] neg_lo:[0,1] neg_hi:[0,1]\n\t" \
    "v_pk_add_f32 v[248:249], v[248:249], v[242:243] op_sel_hi:[1,0] neg_lo:[0,1] neg_hi:[0,1]\n\t" \
    "v_pk_add_f32 v[250:251], v[250:251], v[242:243] op_sel_hi:[1,0] neg_lo:[0,1] neg_hi:[0,1]\n\t" "v_pk_mul_f32 v[244:245], v[244:245], s[66:67]\n\t" \
    "v_pk_mul_f32 v[246:247], v[246:247], s[66:67]\n\t" "v_pk_mul_f32 v[248:249], v[248:249], s[66:67]\n\t" "v_pk_mul_f32 v[250:251], v[250:251], s[66:67]\n\t" \
    "v_exp_f32 v244, v244\n\t" "v_exp_f32 v245, v245\n\t" "v_exp_f32 v246, v246\n\t" "v_exp_f32 v247, v247\n\t" "v_exp_f32 v248, v248\n\t" "v_exp_f32 v249, v249\n\t" \
    "v_exp_f32 v250, v250\n\t" "v_exp_f32 v251, v251\n\t" "v_pk_add_f32 v[242:243], v[244:245], v[246:247]\n\t" "v_pk_add_f32 v[242:243], v[242:243], v[248:249]\n\t" \
    "v_pk_add_f32 v[242:243], v[242:243], v[250:251]\n\t" "v_add_f32 v242, v242, v243\n\t" "v_mov_b32 v243, v242\n\t" "s_nop 1\n\t" "v_permlane32_swap_b32 v242, v243\n\t" \
    "v_add_f32 v242, v242, v243\n\t" "v_mov_b32 v243, v242\n\t" "s_nop 1\n\t" "v_permlane16_swap_b32 v242, v243\n\t" "v_add_f32 v242, v242, v243\n\t" \
    "v_rcp_f32 v242, v242\n\t" "s_nop 0\n\t" "v_pk_mul_f32 v[244:245], v[244:245], v[242:243] op_sel_hi:[1,0]\n\t" \
    "v_pk_mul_f32 v[246:247], v[246:247], v[242:243] op_sel_hi:[1,0]\n\t" "v_pk_mul_f32 v[248:249], v[248:249], v[242:243] op_sel_hi:[1,0]\n\t" \
    "v_pk_mul_f32 v[250:251], v[250:251], v[242:243] op_sel_hi:[1,0]\n\t" "v_cvt_pk_bf16_f32 v162, v244, v245\n\t" "v_cvt_pk_bf16_f32 v163, v246, v247\n\t" \
    "v_cvt_pk_bf16_f32 v164, v248, v249\n\t" "v_cvt_pk_bf16_f32 v165, v250, v251\n\t" "buffer_store_dwordx2 v[164:165], %[px], %[rs], %[tb] offen sc1\n\t" \
    "buffer_store_dwordx2 v[162:163], %[pm], %[rs], %[tb] offen sc1\n\t" "s_waitcnt vmcnt(2)\n\t" "v_readfirstlane_b32 s62, v240\n\t" "s_nop 3\n\t" \
    "s_max_u32 %[sq], %[sq], s62\n\t" "s_barrier\n\t" XT(96, 97) "s_branch 99f\n\t" "21:\n\t" "ds_read_b128 v[220:223], %[l1] offset:4096\n\t" "s_waitcnt lgkmcnt(0)\n\t" \
    "v_max3_u32 v218, v220, v221, v222\n\t" "v_max_u32 v218, v218, v223\n\t" "ds_read_b128 v[220:223], %[l1] offset:5120\n\t" "s_waitcnt lgkmcnt(0)\n\t" \
    "v_max3_u32 v218, v218, v220, v221\n\t" "v_max3_u32 v218, v218, v222, v223\n\t" "ds_read_b128 v[220:223], %[l1] offset:6144\n\t" "s_waitcnt lgkmcnt(0)\n\t" \
    "v_max3_u32 v218, v218, v220, v221\n\t" "v_max3_u32 v218, v218, v222, v223\n\t" "ds_read_b128 v[220:223], %[l1] offset:7168\n\t" "s_waitcnt lgkmcnt(0)\n\t" \
    "v_max3_u32 v218, v218, v220, v221\n\t" "v_max3_u32 v218, v218, v222, v223\n\t" "v_cmp_eq_u32 vcc, -1, v218\n\t" "s_cbranch_vccz 22b\n\t" "s_cmp_eq_u32 %[rt], 1\n\t" \
    "s_cbranch_scc1 22b\n\t" "v_mov_b32 v218, %[rt]\n\t" "ds_write_b32 v219, v218\n\t" "s_waitcnt lgkmcnt(0)\n\t" "s_branch 22b\n\t" "23:\n\t" "s_sub_u32 %[rt], %[rt], 1\n\t" \
    "s_waitcnt lgkmcnt(0)\n\t" XCNT(100) "24:\n\t" "buffer_load_dwordx4 v[240:243], %[mo], %[rs], %[sb] offen sc1\n\t" \
    "buffer_load_dwordx4 v[244:247], v216, %[rs], %[sb] offen sc1\n\t" "buffer_load_dwordx4 v[248:251], %[mo], %[rs], s60 offen sc1\n\t" \
    "buffer_load_dwordx4 v[252:255], v216, %[rs], s60 offen sc1\n\t" "s_waitcnt vmcnt(0)\n\t" "v_max_u32 v218, v240, v241\n\t" "v_max3_u32 v218, v218, v242, v243\n\t" \
    "v_max3_u32 v218, v218, v244, v245\n\t" "v_max3_u32 v218, v218, v246, v247\n\t" "v_max3_u32 v218, v218, v248, v249\n\t" "v_max3_u32 v218, v218, v250, v251\n\t" \
    "v_max3_u32 v218, v218, v252, v253\n\t" "v_max3_u32 v218, v218, v254, v255\n\t" "v_cmp_eq_u32 vcc, -1, v218\n\t" "s_cbranch_vccz 25f\n\t" "s_cmp_eq_u32 %[bd], 0\n\t" \
    "s_cbranch_scc1 25f\n\t" "s_sub_u32 %[bd], %[bd], 1\n\t" XCNT(101) "s_branch 24b\n\t" "25:\n\t" "ds_read_b128 v[160:163], %[l0] offset:0\n\t" \
    "ds_read_b128 v[164:167], %[l0] offset:1024\n\t" "ds_read_b128 v[168:171], %[l0] offset:2048\n\t" "ds_read_b128 v[172:175], %[l0] offset:3072\n\t" \
    "ds_read_b128 v[176:179], %[l0] offset:4096\n\t" "ds_read_b128 v[180:183], %[l0] offset:5120\n\t" "ds_read_b128 v[184:187], %[l0] offset:6144\n\t" \
    "ds_read_b128 v[188:191], %[l0] offset:7168\n\t" "v_mov_b64 v[224:225], 0\n\t" "v_mov_b64 v[226:227], 0\n\t" "v_mov_b64 v[228:229], 0\n\t" "v_mov_b64 v[230:231], 0\n\t" \
    "v_mov_b64 v[232:233], 0\n\t" "v_mov_b64 v[234:235], 0\n\t" "v_mov_b64 v[236:237], 0\n\t" "v_mov_b64 v[238:239], 0\n\t" "s_barrier\n\t" "s_branch 20b\n\t" "99:\n\t"
#define QK_STEP2 \
    ".p2align 6\n\t" "v_xor_b32 v216, 0x400, %[mo]\n\t" "s_xor_b32 s60, %[sb], 0x800\n\t" "s_add_u32 s61, %[qb], 0x1000\n\t" "s_mov_b32 s66, 0x3e0293ee\n\t" \
    "s_mov_b32 s67, 0x3e0293ee\n\t" XT(80, 81) XT(98, 99) "s_barrier\n\t" XT(82, 83) "buffer_load_dwordx4 v[240:243], %[mo], %[rs], %[sb] offen sc1\n\t" \
    "v_mov_b64 v[224:225], 0\n\t" "v_mov_b64 v[226:227], 0\n\t" "v_mov_b64 v[228:229], 0\n\t" "v_mov_b64 v[230:231], 0\n\t" "v_mov_b64 v[232:233], 0\n\t" \
    "v_mov_b64 v[234:235], 0\n\t" "v_mov_b64 v[236:237], 0\n\t" "v_mov_b64 v[238:239], 0\n\t" "buffer_load_dwordx4 v[244:247], v216, %[rs], %[sb] offen sc1\n\t" \
    "ds_read_b128 v[160:163], %[l0] offset:0\n\t" "ds_read_b128 v[164:167], %[l0] offset:1024\n\t" "ds_read_b128 v[168:171], %[l0] offset:2048\n\t" \
    "ds_read_b128 v[172:175], %[l0] offset:3072\n\t" "s_nop 3\n\t" "buffer_load_dwordx4 v[248:251], %[mo], %[rs], s60 offen sc1\n\t" \
    "ds_read_b128 v[176:179], %[l0] offset:4096\n\t" "ds_read_b128 v[180:183], %[l0] offset:5120\n\t" "ds_read_b128 v[184:187], %[l0] offset:6144\n\t" \
    "ds_read_b128 v[188:191], %[l0] offset:7168\n\t" "s_nop 3\n\t" "buffer_load_dwordx4 v[252:255], v216, %[rs], s60 offen sc1\n\t" "v_mov_b32 v219, 160512\n\t" \
    "v_mov_b64 v[220:221], -1\n\t" "v_mov_b64 v[222:223], -1\n\t" "buffer_store_dwordx4 v[220:223], %[px], %[rs], %[tr] offen sc1\n\t" \
    "buffer_store_dwordx2 v[220:221], %[pm], %[rs], %[tr] offen sc1\n\t" "v_mov_b32 v208, 0xd028965f\n\t" "v_mov_b32 v209, 0xff800000\n\t" "s_mov_b32 s62, 0\n\t" \
    "s_mov_b32 s63, -1\n\t" "v_mov_b32 v210, 0\n\t" "v_cmp_gt_i32 vcc, 0, %[ps]\n\t" "v_cndmask_b32 v204, 0, v209, s[62:63]\n\t" "s_nop 0\n\t" \
    "v_cndmask_b32 v200, 0, v208, vcc\n\t" "v_cmp_gt_i32 vcc, 1, %[ps]\n\t" "v_cndmask_b32 v205, 0, v209, s[62:63]\n\t" "s_nop 0\n\t" "v_cndmask_b32 v201, 0, v208, vcc\n\t" \
    "v_cmp_gt_i32 vcc, 2, %[ps]\n\t" "v_cndmask_b32 v206, 0, v209, s[62:63]\n\t" "s_nop 0\n\t" "v_cndmask_b32 v202, 0, v208, vcc\n\t" "v_cmp_gt_i32 vcc, 3, %[ps]\n\t" \
    "v_cndmask_b32 v207, 0, v209, s[62:63]\n\t" "s_nop 0\n\t" "v_cndmask_b32 v203, 0, v208, vcc\n\t" "30:\n\t" "s_waitcnt vmcnt(5)\n\t" XT(84, 85) \
    "v_smfmac_f32_16x16x64_bf16 v[224:227], v[240:243], a[0:7], %[ix]\n\t" "ds_write_b128 %[l1], v[240:243] offset:8192\n\t" \
    "v_smfmac_f32_16x16x64_bf16 v[228:231], v[240:243], a[8:15], %[ix]\n\t" "v_smfmac_f32_16x16x64_bf16 v[232:235], v[240:243], v[32:39], %[ix]\n\t" \
    "s_waitcnt lgkmcnt(7)\n\t" "v_smfmac_f32_16x16x64_bf16 v[236:239], v[240:243], v[160:167], %[ix]\n\t" "ds_read_b128 v[160:163], %[l0] offset:8192\n\t" \
    "ds_read_b128 v[164:167], %[l0] offset:9216\n\t" "s_waitcnt vmcnt(4)\n\t" "v_smfmac_f32_16x16x64_bf16 v[224:227], v[244:247], a[16:23], %[ix]\n\t" \
    "ds_write_b128 %[l1], v[244:247] offset:9216\n\t" "v_smfmac_f32_16x16x64_bf16 v[228:231], v[244:247], a[24:31], %[ix]\n\t" \
    "v_smfmac_f32_16x16x64_bf16 v[232:235], v[244:247], v[40:47], %[ix]\n\t" "s_waitcnt lgkmcnt(8)\n\t" \
    "v_smfmac_f32_16x16x64_bf16 v[236:239], v[244:247], v[168:175], %[ix]\n\t" "ds_read_b128 v[168:171], %[l0] offset:10240\n\t" \
    "ds_read_b128 v[172:175], %[l0] offset:11264\n\t" "s_waitcnt vmcnt(3)\n\t" "v_smfmac_f32_16x16x64_bf16 v[224:227], v[248:251], a[32:39], %[ix]\n\t" \
    "ds_write_b128 %[l1], v[248:251] offset:10240\n\t" "v_smfmac_f32_16x16x64_bf16 v[228:231], v[248:251], a[40:47], %[ix]\n\t" "s_waitcnt lgkmcnt(3)\n\t" "s_barrier\n\t" \
    XT(86, 87) "v_smfmac_f32_16x16x64_bf16 v[232:235], v[248:251], v[48:55], %[ix]\n\t" "ds_read_b128 v[192:195], %[l1] offset:12288\n\t" \
    "ds_read_b128 v[196:199], %[l1] offset:0\n\t" "v_smfmac_f32_16x16x64_bf16 v[236:239], v[248:251], v[176:183], %[ix]\n\t" "ds_read_b128 v[176:179], %[l0] offset:12288\n\t" \
    "ds_read_b128 v[180:183], %[l0] offset:13312\n\t" "s_waitcnt vmcnt(2)\n\t" "v_smfmac_f32_16x16x64_bf16 v[224:227], v[252:255], a[48:55], %[ix]\n\t" \
    "ds_write_b128 %[l1], v[252:255] offset:11264\n\t" "v_smfmac_f32_16x16x64_bf16 v[228:231], v[252:255], a[56:63], %[ix]\n\t" \
    "ds_read_b128 v[240:243], %[l1] offset:4096\n\t" "ds_read_b128 v[244:247], %[l1] offset:13312\n\t" \
    "v_smfmac_f32_16x16x64_bf16 v[232:235], v[252:255], v[56:63], %[ix]\n\t" "s_nop 1\n\t" "v_smfmac_f32_16x16x64_bf16 v[236:239], v[252:255], v[184:191], %[ix]\n\t" \
    "ds_read_b128 v[184:187], %[l0] offset:14336\n\t" "v_cmp_u_f32 vcc, v224, v225\n\t" "v_cmp_u_f32 s[62:63], v226, v227\n\t" "s_waitcnt lgkmcnt(7)\n\t" \
    "v_smfmac_f32_16x16x64_bf16 v[224:227], v[192:195], a[64:71], %[ix]\n\t" "ds_read_b128 v[188:191], %[l0] offset:15360\n\t" \
    "v_smfmac_f32_16x16x64_bf16 v[228:231], v[192:195], a[72:79], %[ix]\n\t" "s_or_b64 vcc, vcc, s[62:63]\n\t" "s_cbranch_scc1 31f\n\t" "32:\n\t" \
    "v_smfmac_f32_16x16x64_bf16 v[232:235], v[192:195], v[64:71], %[ix]\n\t" "s_waitcnt lgkmcnt(4)\n\t" "s_barrier\n\t" XT(88, 89) "ds_read_b32 v218, v219\n\t" \
    "v_smfmac_f32_16x16x64_bf16 v[236:239], v[192:195], v[160:167], %[ix]\n\t" "ds_read_b128 v[160:163], %[l0] offset:16384\n\t" \
    "ds_read_b128 v[164:167], %[l0] offset:17408\n\t" "v_smfmac_f32_16x16x64_bf16 v[224:227], v[196:199], a[80:87], %[ix]\n\t" \
    "ds_read_b128 v[248:251], %[l1] offset:1024\n\t" "ds_read_b128 v[252:255], %[l1] offset:5120\n\t" "v_smfmac_f32_16x16x64_bf16 v[228:231], v[196:199], a[88:95], %[ix]\n\t" \
    "ds_read_b128 v[192:195], %[l1] offset:14336\n\t" "v_smfmac_f32_16x16x64_bf16 v[232:235], v[196:199], v[72:79], %[ix]\n\t" \
    "v_smfmac_f32_16x16x64_bf16 v[236:239], v[196:199], v[168:175], %[ix]\n\t" "s_waitcnt lgkmcnt(5)\n\t" "v_cmp_eq_u32 vcc, %[rt], v218\n\t" \
    "v_smfmac_f32_16x16x64_bf16 v[224:227], v[240:243], a[96:103], %[ix]\n\t" "s_cbranch_vccnz 33f\n\t" "ds_read_b128 v[168:171], %[l0] offset:18432\n\t" \
    "v_smfmac_f32_16x16x64_bf16 v[228:231], v[240:243], a[104:111], %[ix]\n\t" "ds_read_b128 v[172:175], %[l0] offset:19456\n\t" \
    "ds_read_b128 v[196:199], %[l1] offset:2048\n\t" "v_smfmac_f32_16x16x64_bf16 v[232:235], v[240:243], v[80:87], %[ix]\n\t" \
    "buffer_load_dwordx4 v[16:19], %[hq], %[rs], %[qb] offen offset:0 sc1\n\t" "v_smfmac_f32_16x16x64_bf16 v[236:239], v[240:243], v[176:183], %[ix]\n\t" \
    "ds_read_b128 v[176:179], %[l0] offset:20480\n\t" "ds_read_b128 v[180:183], %[l0] offset:21504\n\t" \
    "v_smfmac_f32_16x16x64_bf16 v[224:227], v[244:247], a[112:119], %[ix]\n\t" "ds_read_b128 v[240:243], %[l1] offset:6144\n\t" \
    "v_smfmac_f32_16x16x64_bf16 v[228:231], v[244:247], a[120:127], %[ix]\n\t" "v_smfmac_f32_16x16x64_bf16 v[232:235], v[244:247], v[88:95], %[ix]\n\t" \
    "buffer_load_dwordx4 v[20:23], %[hq], %[rs], %[qb] offen offset:2048 sc1\n\t" "v_smfmac_f32_16x16x64_bf16 v[236:239], v[244:247], v[184:191], %[ix]\n\t" \
    "ds_read_b128 v[184:187], %[l0] offset:22528\n\t" "ds_read_b128 v[188:191], %[l0] offset:23552\n\t" "s_waitcnt lgkmcnt(10)\n\t" \
    "v_smfmac_f32_16x16x64_bf16 v[224:227], v[248:251], a[128:135], %[ix]\n\t" "ds_read_b128 v[244:247], %[l1] offset:15360\n\t" \
    "v_smfmac_f32_16x16x64_bf16 v[228:231], v[248:251], a[136:143], %[ix]\n\t" "v_smfmac_f32_16x16x64_bf16 v[232:235], v[248:251], v[96:103], %[ix]\n\t" \
    "buffer_load_dwordx4 v[24:27], %[hq], %[rs], s61 offen offset:0 sc1\n\t" "v_smfmac_f32_16x16x64_bf16 v[236:239], v[248:251], v[160:167], %[ix]\n\t" \
    "ds_read_b128 v[160:163], %[l0] offset:24576\n\t" "ds_read_b128 v[164:167], %[l0] offset:25600\n\t" "s_waitcnt lgkmcnt(12)\n\t" \
    "v_smfmac_f32_16x16x64_bf16 v[224:227], v[252:255], a[144:151], %[ix]\n\t" "ds_read_b128 v[248:251], %[l1] offset:3072\n\t" \
    "v_smfmac_f32_16x16x64_bf16 v[228:231], v[252:255], a[152:159], %[ix]\n\t" "v_smfmac_f32_16x16x64_bf16 v[232:235], v[252:255], v[104:111], %[ix]\n\t" \
    "buffer_load_dwordx4 v[28:31], %[hq], %[rs], s61 offen offset:2048 sc1\n\t" "s_waitcnt lgkmcnt(10)\n\t" \
    "v_smfmac_f32_16x16x64_bf16 v[236:239], v[252:255], v[168:175], %[ix]\n\t" "ds_read_b128 v[168:171], %[l0] offset:26624\n\t" \
    "ds_read_b128 v[172:175], %[l0] offset:27648\n\t" "v_smfmac_f32_16x16x64_bf16 v[224:227], v[192:195], a[160:167], %[ix]\n\t" \
    "ds_read_b128 v[252:255], %[l1] offset:7168\n\t" "v_smfmac_f32_16x16x64_bf16 v[228:231], v[192:195], a[168:175], %[ix]\n\t" "s_waitcnt lgkmcnt(12)\n\t" \
    "v_smfmac_f32_16x16x64_bf16 v[224:227], v[196:199], a[176:183], %[ix]\n\t" "v_smfmac_f32_16x16x64_bf16 v[228:231], v[196:199], a[184:191], %[ix]\n\t" \
    "s_waitcnt lgkmcnt(9)\n\t" "v_smfmac_f32_16x16x64_bf16 v[224:227], v[240:243], a[192:199], %[ix]\n\t" \
    "v_smfmac_f32_16x16x64_bf16 v[228:231], v[240:243], a[200:207], %[ix]\n\t" "s_waitcnt lgkmcnt(6)\n\t" \
    "v_smfmac_f32_16x16x64_bf16 v[224:227], v[244:247], a[208:215], %[ix]\n\t" "v_smfmac_f32_16x16x64_bf16 v[228:231], v[244:247], a[216:223], %[ix]\n\t" \
    "s_waitcnt vmcnt(4)\n\t" "s_waitcnt lgkmcnt(3)\n\t" "v_smfmac_f32_16x16x64_bf16 v[224:227], v[248:251], a[224:231], %[ix]\n\t" \
    "v_smfmac_f32_16x16x64_bf16 v[228:231], v[248:251], a[232:239], %[ix]\n\t" "s_waitcnt lgkmcnt(0)\n\t" \
    "v_smfmac_f32_16x16x64_bf16 v[224:227], v[252:255], a[240:247], %[ix]\n\t" "s_nop 1\n\t" "v_smfmac_f32_16x16x64_bf16 v[228:231], v[252:255], a[248:255], %[ix]\n\t" \
    XT(90, 91) "s_nop 1\n\t" "v_smfmac_f32_16x16x64_bf16 v[232:235], v[192:195], v[112:119], %[ix]\n\t" "s_nop 1\n\t" \
    "v_smfmac_f32_16x16x64_bf16 v[236:239], v[192:195], v[176:183], %[ix]\n\t" "ds_read_b128 v[176:179], %[l0] offset:28672\n\t" "v_add_f32 v224, v224, v225\n\t" \
    "v_add_f32 v226, v226, v227\n\t" "v_smfmac_f32_16x16x64_bf16 v[232:235], v[196:199], v[120:127], %[ix]\n\t" "ds_read_b128 v[180:183], %[l0] offset:29696\n\t" \
    "v_add_f32 v228, v228, v229\n\t" "v_add_f32 v230, v230, v231\n\t" "v_smfmac_f32_16x16x64_bf16 v[236:239], v[196:199], v[184:191], %[ix]\n\t" \
    "ds_read_b128 v[184:187], %[l0] offset:30720\n\t" "v_cvt_pk_bf16_f32 v218, v224, v228\n\t" "v_cvt_pk_bf16_f32 v219, v226, v230\n\t" \
    "v_smfmac_f32_16x16x64_bf16 v[232:235], v[240:243], v[128:135], %[ix]\n\t" "ds_write2_b32 %[qw], v218, v219 offset1:8\n\t" \
    "ds_read_b128 v[188:191], %[l0] offset:31744\n\t" "v_smfmac_f32_16x16x64_bf16 v[236:239], v[240:243], v[160:167], %[ix]\n\t" \
    "v_smfmac_f32_16x16x64_bf16 v[232:235], v[244:247], v[136:143], %[ix]\n\t" "v_smfmac_f32_16x16x64_bf16 v[236:239], v[244:247], v[168:175], %[ix]\n\t" \
    "s_waitcnt lgkmcnt(1)\n\t" "s_barrier\n\t" "v_smfmac_f32_16x16x64_bf16 v[232:235], v[248:251], v[144:151], %[ix]\n\t" \
    "v_smfmac_f32_16x16x64_bf16 v[236:239], v[248:251], v[176:183], %[ix]\n\t" "v_smfmac_f32_16x16x64_bf16 v[232:235], v[252:255], v[152:159], %[ix]\n\t" \
    "s_waitcnt lgkmcnt(0)\n\t" "v_smfmac_f32_16x16x64_bf16 v[236:239], v[252:255], v[184:191], %[ix]\n\t" XT(92, 93) "s_nop 6\n\t" "v_add_f32 v232, v232, v233\n\t" \
    "v_add_f32 v234, v234, v235\n\t" "v_add_f32 v236, v236, v237\n\t" "v_add_f32 v238, v238, v239\n\t" "v_cvt_pk_bf16_f32 v232, v232, v236\n\t" \
    "v_cvt_pk_bf16_f32 v233, v234, v238\n\t" "ds_write2_b32 %[kw], v232, v233 offset1:68\n\t" "s_waitcnt lgkmcnt(0)\n\t" "s_barrier\n\t" XT(94, 95) \
    "s_cmp_ge_u32 %[sq], %[sn]\n\t" "s_cbranch_scc1 377f\n\t" "375:\n\t" "global_load_dword v240, v210, %[fq] sc1\n\t" "s_waitcnt vmcnt(0)\n\t" \
    "v_readfirstlane_b32 %[sq], v240\n\t" "s_nop 3\n\t" "s_cmp_ge_u32 %[sq], %[sn]\n\t" "s_cbranch_scc1 376f\n\t" "s_cmp_eq_u32 %[bd], 0\n\t" "s_cbranch_scc1 376f\n\t" \
    "s_sub_u32 %[bd], %[bd], 1\n\t" "s_sleep 2\n\t" "s_branch 375b\n\t" "376:\n\t" "buffer_load_dwordx4 v[16:19], %[hq], %[rs], %[qb] offen offset:0 sc1\n\t" \
    "buffer_load_dwordx4 v[20:23], %[hq], %[rs], %[qb] offen offset:2048 sc1\n\t" "buffer_load_dwordx4 v[24:27], %[hq], %[rs], s61 offen offset:0 sc1\n\t" \
    "buffer_load_dwordx4 v[28:31], %[hq], %[rs], s61 offen offset:2048 sc1\n\t" "377:\n\t" "s_nop 3\n\t" "ds_read_b128 v[160:163], %[kx] offset:0\n\t" \
    "ds_read_b128 v[164:167], %[kx] offset:2048\n\t" "ds_read_b128 v[168:171], %[kx] offset:4096\n\t" "ds_read_b128 v[172:175], %[kx] offset:6144\n\t" \
    "ds_read_b128 v[176:179], %[kx] offset:256\n\t" "ds_read_b128 v[180:183], %[kx] offset:2304\n\t" "ds_read_b128 v[184:187], %[kx] offset:4352\n\t" \
    "ds_read_b128 v[188:191], %[kx] offset:6400\n\t" "ds_read_b128 v[224:227], %[km] offset:0\n\t" "ds_read_b128 v[228:231], %[km] offset:64\n\t" \
    "ds_read_b128 v[232:235], %[km] offset:128\n\t" "ds_read_b128 v[236:239], %[km] offset:192\n\t" "s_waitcnt vmcnt(0)\n\t" "global_load_dword v240, v210, %[fq] sc1\n\t" \
    "s_waitcnt lgkmcnt(11)\n\t" "v_mfma_f32_16x16x32_bf16 v[248:251], v[160:163], v[16:19], 0\n\t" "s_waitcnt lgkmcnt(10)\n\t" \
    "v_mfma_f32_16x16x32_bf16 v[248:251], v[164:167], v[20:23], v[248:251]\n\t" "s_waitcnt lgkmcnt(9)\n\t" \
    "v_mfma_f32_16x16x32_bf16 v[248:251], v[168:171], v[24:27], v[248:251]\n\t" "s_waitcnt lgkmcnt(8)\n\t" \
    "v_mfma_f32_16x16x32_bf16 v[248:251], v[172:175], v[28:31], v[248:251]\n\t" "s_waitcnt lgkmcnt(7)\n\t" \
    "v_mfma_f32_16x16x32_bf16 v[252:255], v[176:179], v[16:19], v[200:203]\n\t" "s_waitcnt lgkmcnt(6)\n\t" \
    "v_mfma_f32_16x16x32_bf16 v[252:255], v[180:183], v[20:23], v[252:255]\n\t" "s_waitcnt lgkmcnt(5)\n\t" \
    "v_mfma_f32_16x16x32_bf16 v[252:255], v[184:187], v[24:27], v[252:255]\n\t" "s_waitcnt lgkmcnt(4)\n\t" \
    "v_mfma_f32_16x16x32_bf16 v[252:255], v[188:191], v[28:31], v[252:255]\n\t" "s_waitcnt lgkmcnt(3)\n\t" \
    "v_mfma_f32_16x16x32_bf16 v[244:247], v[224:227], v[16:19], v[204:207]\n\t" "s_waitcnt lgkmcnt(2)\n\t" \
    "v_mfma_f32_16x16x32_bf16 v[244:247], v[228:231], v[20:23], v[244:247]\n\t" "s_waitcnt lgkmcnt(1)\n\t" \
    "v_mfma_f32_16x16x32_bf16 v[244:247], v[232:235], v[24:27], v[244:247]\n\t" "s_waitcnt lgkmcnt(0)\n\t" \
    "v_mfma_f32_16x16x32_bf16 v[244:247], v[236:239], v[28:31], v[244:247]\n\t" "s_nop 3\n\t" "v_max3_f32 v242, v248, v249, v250\n\t" "v_max3_f32 v242, v242, v251, v252\n\t" \
    "v_max3_f32 v242, v242, v253, v254\n\t" "v_max_f32 v242, v242, v255\n\t" "v_max3_f32 v242, v242, v244, v245\n\t" "v_max3_f32 v242, v242, v246, v247\n\t" \
    "v_mov_b32 v243, v242\n\t" "s_nop 1\n\t" "v_permlane32_swap_b32 v242, v243\n\t" "v_max_f32 v242, v242, v243\n\t" "v_mov_b32 v243, v242\n\t" "s_nop 1\n\t" \
    "v_permlane16_swap_b32 v242, v243\n\t" "v_max_f32 v242, v242, v243\n\t" "v_pk_add_f32 v[244:245], v[244:245], v[242:243] op_sel_hi:[1,0] neg_lo:[0,1] neg_hi:[0,1]\n\t" \
    "v_pk_add_f32 v[246:247], v[246:247], v[242:243] op_sel_hi:[1,0] neg_lo:[0,1] neg_hi:[0,1]\n\t" \
    "v_pk_add_f32 v[248:249], v[248:249], v[242:243] op_sel_hi:[1,0] neg_lo:[0,1] neg_hi:[0,1]\n\t" \
    "v_pk_add_f32 v[250:251], v[250:251], v[242:243] op_sel_hi:[1,0] neg_lo:[0,1] neg_hi:[0,1]\n\t" \
    "v_pk_add_f32 v[252:253], v[252:253], v[242:243] op_sel_hi:[1,0] neg_lo:[0,1] neg_hi:[0,1]\n\t" \
    "v_pk_add_f32 v[254:255], v[254:255], v[242:243] op_sel_hi:[1,0] neg_lo:[0,1] neg_hi:[0,1]\n\t" "v_pk_mul_f32 v[244:245], v[244:245], s[66:67]\n\t" \
    "v_pk_mul_f32 v[246:247], v[246:247], s[66:67]\n\t" "v_pk_mul_f32 v[248:249], v[248:249], s[66:67]\n\t" "v_pk_mul_f32 v[250:251], v[250:251], s[66:67]\n\t" \
    "v_pk_mul_f32 v[252:253], v[252:253], s[66:67]\n\t" "v_pk_mul_f32 v[254:255], v[254:255], s[66:67]\n\t" "v_exp_f32 v244, v244\n\t" "v_exp_f32 v245, v245\n\t" \
    "v_exp_f32 v246, v246\n\t" "v_exp_f32 v247, v247\n\t" "v_exp_f32 v248, v248\n\t" "v_exp_f32 v249, v249\n\t" "v_exp_f32 v250, v250\n\t" "v_exp_f32 v251, v251\n\t" \
    "v_exp_f32 v252, v252\n\t" "v_exp_f32 v253, v253\n\t" "v_exp_f32 v254, v254\n\t" "v_exp_f32 v255, v255\n\t" "v_pk_add_f32 v[242:243], v[244:245], v[246:247]\n\t" \
    "v_pk_add_f32 v[242:243], v[242:243], v[248:249]\n\t" "v_pk_add_f32 v[242:243], v[242:243], v[250:251]\n\t" "v_pk_add_f32 v[242:243], v[242:243], v[252:253]\n\t" \
    "v_pk_add_f32 v[242:243], v[242:243], v[254:255]\n\t" "v_add_f32 v242, v242, v243\n\t" "v_mov_b32 v243, v242\n\t" "s_nop 1\n\t" "v_permlane32_swap_b32 v242, v243\n\t" \
    "v_add_f32 v242, v242, v243\n\t" "v_mov_b32 v243, v242\n\t" "s_nop 1\n\t" "v_permlane16_swap_b32 v242, v243\n\t" "v_add_f32 v242, v242, v243\n\t" \
    "v_rcp_f32 v242, v242\n\t" "s_nop 0\n\t" "v_pk_mul_f32 v[244:245], v[244:245], v[242:243] op_sel_hi:[1,0]\n\t" \
    "v_pk_mul_f32 v[246:247], v[246:247], v[242:243] op_sel_hi:[1,0]\n\t" "v_pk_mul_f32 v[248:249], v[248:249], v[242:243] op_sel_hi:[1,0]\n\t" \
    "v_pk_mul_f32 v[250:251], v[250:251], v[242:243] op_sel_hi:[1,0]\n\t" "v_pk_mul_f32 v[252:253], v[252:253], v[242:243] op_sel_hi:[1,0]\n\t" \
    "v_pk_mul_f32 v[254:255], v[254:255], v[242:243] op_sel_hi:[1,0]\n\t" "v_cvt_pk_bf16_f32 v162, v244, v245\n\t" "v_cvt_pk_bf16_f32 v163, v246, v247\n\t" \
    "v_cvt_pk_bf16_f32 v164, v248, v249\n\t" "v_cvt_pk_bf16_f32 v165, v250, v251\n\t" "v_cvt_pk_bf16_f32 v166, v252, v253\n\t" "v_cvt_pk_bf16_f32 v167, v254, v255\n\t" \
    "buffer_store_dwordx4 v[164:167], %[px], %[rs], %[tb] offen sc1\n\t" "buffer_store_dwordx2 v[162:163], %[pm], %[rs], %[tb] offen sc1\n\t" "s_waitcnt vmcnt(2)\n\t" \
    "v_readfirstlane_b32 s62, v240\n\t" "s_nop 3\n\t" "s_max_u32 %[sq], %[sq], s62\n\t" "s_barrier\n\t" XT(96, 97) "s_branch 99f\n\t" "31:\n\t" \
    "ds_read_b128 v[220:223], %[l1] offset:8192\n\t" "s_waitcnt lgkmcnt(0)\n\t" "v_max3_u32 v218, v220, v221, v222\n\t" "v_max_u32 v218, v218, v223\n\t" \
    "ds_read_b128 v[220:223], %[l1] offset:9216\n\t" "s_waitcnt lgkmcnt(0)\n\t" "v_max3_u32 v218, v218, v220, v221\n\t" "v_max3_u32 v218, v218, v222, v223\n\t" \
    "ds_read_b128 v[220:223], %[l1] offset:10240\n\t" "s_waitcnt lgkmcnt(0)\n\t" "v_max3_u32 v218, v218, v220, v221\n\t" "v_max3_u32 v218, v218, v222, v223\n\t" \
    "ds_read_b128 v[220:223], %[l1] offset:11264\n\t" "s_waitcnt lgkmcnt(0)\n\t" "v_max3_u32 v218, v218, v220, v221\n\t" "v_max3_u32 v218, v218, v222, v223\n\t" \
    "v_cmp_eq_u32 vcc, -1, v218\n\t" "s_cbranch_vccz 32b\n\t" "s_cmp_eq_u32 %[rt], 1\n\t" "s_cbranch_scc1 32b\n\t" "v_mov_b32 v218, %[rt]\n\t" "ds_write_b32 v219, v218\n\t" \
    "s_waitcnt lgkmcnt(0)\n\t" "s_branch 32b\n\t" "33:\n\t" "s_sub_u32 %[rt], %[rt], 1\n\t" "s_waitcnt lgkmcnt(0)\n\t" XCNT(100) "34:\n\t" \
    "buffer_load_dwordx4 v[240:243], %[mo], %[rs], %[sb] offen sc1\n\t" "buffer_load_dwordx4 v[244:247], v216, %[rs], %[sb] offen sc1\n\t" \
    "buffer_load_dwordx4 v[248:251], %[mo], %[rs], s60 offen sc1\n\t" "buffer_load_dwordx4 v[252:255], v216, %[rs], s60 offen sc1\n\t" "s_waitcnt vmcnt(0)\n\t" \
    "v_max_u32 v218, v240, v241\n\t" "v_max3_u32 v218, v218, v242, v243\n\t" "v_max3_u32 v218, v218, v244, v245\n\t" "v_max3_u32 v218, v218, v246, v247\n\t" \
    "v_max3_u32 v218, v218, v248, v249\n\t" "v_max3_u32 v218, v218, v250, v251\n\t" "v_max3_u32 v218, v218, v252, v253\n\t" "v_max3_u32 v218, v218, v254, v255\n\t" \
    "v_cmp_eq_u32 vcc, -1, v218\n\t" "s_cbranch_vccz 35f\n\t" "s_cmp_eq_u32 %[bd], 0\n\t" "s_cbranch_scc1 35f\n\t" "s_sub_u32 %[bd], %[bd], 1\n\t" XCNT(101) \
    "s_branch 34b\n\t" "35:\n\t" "ds_read_b128 v[160:163], %[l0] offset:0\n\t" "ds_read_b128 v[164:167], %[l0] offset:1024\n\t" \
    "ds_read_b128 v[168:171], %[l0] offset:2048\n\t" "ds_read_b128 v[172:175], %[l0] offset:3072\n\t" "ds_read_b128 v[176:179], %[l0] offset:4096\n\t" \
    "ds_read_b128 v[180:183], %[l0] offset:5120\n\t" "ds_read_b128 v[184:187], %[l0] offset:6144\n\t" "ds_read_b128 v[188:191], %[l0] offset:7168\n\t" \
    "v_mov_b64 v[224:225], 0\n\t" "v_mov_b64 v[226:227], 0\n\t" "v_mov_b64 v[228:229], 0\n\t" "v_mov_b64 v[230:231], 0\n\t" "v_mov_b64 v[232:233], 0\n\t" \
    "v_mov_b64 v[234:235], 0\n\t" "v_mov_b64 v[236:237], 0\n\t" "v_mov_b64 v[238:239], 0\n\t" "s_barrier\n\t" "s_branch 30b\n\t" "99:\n\t"
#define QK_STEP3 \
    ".p2align 6\n\t" "v_xor_b32 v216, 0x400, %[mo]\n\t" "s_xor_b32 s60, %[sb], 0x800\n\t" "s_mov_b32 s67, %[pb]\n\t" XT(80, 81) "s_getpc_b64 s[62:63]\n\t" "48:\n\t" \
    "s_memtime s[64:65]\n\t" "s_waitcnt lgkmcnt(0)\n\t" "s_sub_u32 s61, %[tg], s64\n\t" "s_cmp_gt_i32 s61, 124\n\t" "s_cbranch_scc1 48b\n\t" "s_max_i32 s61, s61, 0\n\t" \
    "s_and_b32 s61, s61, 124\n\t" "s_sub_u32 s61, 49f-48b, s61\n\t" "s_add_u32 s62, s62, s61\n\t" "s_addc_u32 s63, s63, 0\n\t" "s_setpc_b64 s[62:63]\n\t" ".rept 31\n\t" \
    "s_nop 0\n\t" ".endr\n\t" "49:\n\t" XT(98, 99) "s_barrier\n\t" XT(82, 83) "buffer_load_dwordx4 v[240:243], %[mo], %[rs], %[sb] offen sc1\n\t" \
    "v_mov_b64 v[224:225], 0\n\t" "v_mov_b64 v[226:227], 0\n\t" "v_mov_b64 v[228:229], 0\n\t" "v_mov_b64 v[230:231], 0\n\t" "v_mov_b64 v[232:233], 0\n\t" \
    "v_mov_b64 v[234:235], 0\n\t" "v_mov_b64 v[236:237], 0\n\t" "v_mov_b64 v[238:239], 0\n\t" "buffer_load_dwordx4 v[244:247], v216, %[rs], %[sb] offen sc1\n\t" \
    "ds_read_b128 v[160:163], %[l0] offset:0\n\t" "ds_read_b128 v[164:167], %[l0] offset:1024\n\t" "ds_read_b128 v[168:171], %[l0] offset:2048\n\t" \
    "ds_read_b128 v[172:175], %[l0] offset:3072\n\t" "s_nop 3\n\t" "buffer_load_dwordx4 v[248:251], %[mo], %[rs], s60 offen sc1\n\t" \
    "ds_read_b128 v[176:179], %[l0] offset:4096\n\t" "ds_read_b128 v[180:183], %[l0] offset:5120\n\t" "ds_read_b128 v[184:187], %[l0] offset:6144\n\t" \
    "ds_read_b128 v[188:191], %[l0] offset:7168\n\t" "s_nop 3\n\t" "buffer_load_dwordx4 v[252:255], v216, %[rs], s60 offen sc1\n\t" "v_mov_b32 v219, 160512\n\t" \
    "v_mov_b32 v220, -1\n\t" "buffer_store_dword v220, %[hq], %[rs], %[pr] offen sc1\n\t" "40:\n\t" "s_waitcnt vmcnt(4)\n\t" XT(84, 85) \
    "v_smfmac_f32_16x16x64_bf16 v[224:227], v[240:243], a[0:7], %[ix]\n\t" "ds_write_b128 %[l1], v[240:243] offset:12288\n\t" \
    "v_smfmac_f32_16x16x64_bf16 v[228:231], v[240:243], a[8:15], %[ix]\n\t" "v_smfmac_f32_16x16x64_bf16 v[232:235], v[240:243], v[32:39], %[ix]\n\t" \
    "s_waitcnt lgkmcnt(7)\n\t" "v_smfmac_f32_16x16x64_bf16 v[236:239], v[240:243], v[160:167], %[ix]\n\t" "ds_read_b128 v[160:163], %[l0] offset:8192\n\t" \
    "ds_read_b128 v[164:167], %[l0] offset:9216\n\t" "s_waitcnt vmcnt(3)\n\t" "v_smfmac_f32_16x16x64_bf16 v[224:227], v[244:247], a[16:23], %[ix]\n\t" \
    "ds_write_b128 %[l1], v[244:247] offset:13312\n\t" "v_smfmac_f32_16x16x64_bf16 v[228:231], v[244:247], a[24:31], %[ix]\n\t" \
    "v_smfmac_f32_16x16x64_bf16 v[232:235], v[244:247], v[40:47], %[ix]\n\t" "s_waitcnt lgkmcnt(8)\n\t" \
    "v_smfmac_f32_16x16x64_bf16 v[236:239], v[244:247], v[168:175], %[ix]\n\t" "ds_read_b128 v[168:171], %[l0] offset:10240\n\t" \
    "ds_read_b128 v[172:175], %[l0] offset:11264\n\t" "s_waitcnt vmcnt(2)\n\t" "v_smfmac_f32_16x16x64_bf16 v[224:227], v[248:251], a[32:39], %[ix]\n\t" \
    "ds_write_b128 %[l1], v[248:251] offset:14336\n\t" "v_smfmac_f32_16x16x64_bf16 v[228:231], v[248:251], a[40:47], %[ix]\n\t" "s_waitcnt lgkmcnt(3)\n\t" "s_barrier\n\t" \
    XT(86, 87) "v_smfmac_f32_16x16x64_bf16 v[232:235], v[248:251], v[48:55], %[ix]\n\t" "ds_read_b128 v[192:195], %[l1] offset:0\n\t" \
    "ds_read_b128 v[196:199], %[l1] offset:4096\n\t" "v_smfmac_f32_16x16x64_bf16 v[236:239], v[248:251], v[176:183], %[ix]\n\t" \
    "ds_read_b128 v[176:179], %[l0] offset:12288\n\t" "ds_read_b128 v[180:183], %[l0] offset:13312\n\t" "s_waitcnt vmcnt(1)\n\t" \
    "v_smfmac_f32_16x16x64_bf16 v[224:227], v[252:255], a[48:55], %[ix]\n\t" "ds_write_b128 %[l1], v[252:255] offset:15360\n\t" \
    "v_smfmac_f32_16x16x64_bf16 v[228:231], v[252:255], a[56:63], %[ix]\n\t" "ds_read_b128 v[240:243], %[l1] offset:8192\n\t" "ds_read_b128 v[244:247], %[l1] offset:1024\n\t" \
    "v_smfmac_f32_16x16x64_bf16 v[232:235], v[252:255], v[56:63], %[ix]\n\t" "s_nop 1\n\t" "v_smfmac_f32_16x16x64_bf16 v[236:239], v[252:255], v[184:191], %[ix]\n\t" \
    "ds_read_b128 v[184:187], %[l0] offset:14336\n\t" "v_cmp_u_f32 vcc, v224, v225\n\t" "v_cmp_u_f32 s[62:63], v226, v227\n\t" "s_waitcnt lgkmcnt(7)\n\t" \
    "v_smfmac_f32_16x16x64_bf16 v[224:227], v[192:195], a[64:71], %[ix]\n\t" "ds_read_b128 v[188:191], %[l0] offset:15360\n\t" \
    "v_smfmac_f32_16x16x64_bf16 v[228:231], v[192:195], a[72:79], %[ix]\n\t" "s_or_b64 vcc, vcc, s[62:63]\n\t" "s_cbranch_scc1 41f\n\t" "42:\n\t" \
    "v_smfmac_f32_16x16x64_bf16 v[232:235], v[192:195], v[64:71], %[ix]\n\t" "s_waitcnt lgkmcnt(4)\n\t" "s_barrier\n\t" XT(88, 89) "ds_read_b32 v218, v219\n\t" \
    "v_smfmac_f32_16x16x64_bf16 v[236:239], v[192:195], v[160:167], %[ix]\n\t" "s_memtime s[64:65]\n\t" "ds_read_b128 v[160:163], %[l0] offset:16384\n\t" \
    "v_smfmac_f32_16x16x64_bf16 v[224:227], v[196:199], a[80:87], %[ix]\n\t" "ds_read_b128 v[164:167], %[l0] offset:17408\n\t" \
    "ds_read_b128 v[248:251], %[l1] offset:5120\n\t" "v_smfmac_f32_16x16x64_bf16 v[228:231], v[196:199], a[88:95], %[ix]\n\t" "ds_read_b128 v[252:255], %[l1] offset:9216\n\t" \
    "ds_read_b128 v[192:195], %[l1] offset:2048\n\t" "v_smfmac_f32_16x16x64_bf16 v[232:235], v[196:199], v[72:79], %[ix]\n\t" "s_sub_u32 s62, s64, %[ca]\n\t" \
    "s_mul_hi_u32 s63, s62, %[k1]\n\t" "s_mul_i32 s62, s62, %[k1]\n\t" "v_smfmac_f32_16x16x64_bf16 v[236:239], v[196:199], v[168:175], %[ix]\n\t" "s_waitcnt lgkmcnt(5)\n\t" \
    "v_cmp_eq_u32 vcc, %[rt], v218\n\t" "s_lshr_b64 s[62:63], s[62:63], 16\n\t" "s_add_u32 s62, s62, %[ga]\n\t" "s_bitset0_b32 s62, 0\n\t" \
    "v_smfmac_f32_16x16x64_bf16 v[224:227], v[240:243], a[96:103], %[ix]\n\t" "s_cbranch_vccnz 43f\n\t" "ds_read_b128 v[168:171], %[l0] offset:18432\n\t" \
    "v_smfmac_f32_16x16x64_bf16 v[228:231], v[240:243], a[104:111], %[ix]\n\t" "ds_read_b128 v[172:175], %[l0] offset:19456\n\t" "v_mov_b32 v217, s62\n\t" \
    "buffer_store_dword v217, %[hq], %[rs], s67 offen sc1\n\t" "v_smfmac_f32_16x16x64_bf16 v[232:235], v[240:243], v[80:87], %[ix]\n\t" \
    "ds_read_b128 v[196:199], %[l1] offset:6144\n\t" "v_smfmac_f32_16x16x64_bf16 v[236:239], v[240:243], v[176:183], %[ix]\n\t" \
    "ds_read_b128 v[176:179], %[l0] offset:20480\n\t" "ds_read_b128 v[180:183], %[l0] offset:21504\n\t" \
    "v_smfmac_f32_16x16x64_bf16 v[224:227], v[244:247], a[112:119], %[ix]\n\t" "ds_read_b128 v[240:243], %[l1] offset:10240\n\t" \
    "v_smfmac_f32_16x16x64_bf16 v[228:231], v[244:247], a[120:127], %[ix]\n\t" "v_smfmac_f32_16x16x64_bf16 v[232:235], v[244:247], v[88:95], %[ix]\n\t" \
    "v_smfmac_f32_16x16x64_bf16 v[236:239], v[244:247], v[184:191], %[ix]\n\t" "ds_read_b128 v[184:187], %[l0] offset:22528\n\t" \
    "ds_read_b128 v[188:191], %[l0] offset:23552\n\t" "s_waitcnt lgkmcnt(10)\n\t" "v_smfmac_f32_16x16x64_bf16 v[224:227], v[248:251], a[128:135], %[ix]\n\t" \
    "ds_read_b128 v[244:247], %[l1] offset:3072\n\t" "v_smfmac_f32_16x16x64_bf16 v[228:231], v[248:251], a[136:143], %[ix]\n\t" "v_lshlrev_b32 v16, 3, %[l1]\n\t" \
    "buffer_load_dword v17, v16, %[rs], %[kn] offen sc1\n\t" "v_smfmac_f32_16x16x64_bf16 v[232:235], v[248:251], v[96:103], %[ix]\n\t" \
    "v_smfmac_f32_16x16x64_bf16 v[236:239], v[248:251], v[160:167], %[ix]\n\t" "ds_read_b128 v[160:163], %[l0] offset:24576\n\t" \
    "ds_read_b128 v[164:167], %[l0] offset:25600\n\t" "s_waitcnt lgkmcnt(12)\n\t" "v_smfmac_f32_16x16x64_bf16 v[224:227], v[252:255], a[144:151], %[ix]\n\t" \
    "ds_read_b128 v[248:251], %[l1] offset:7168\n\t" "v_smfmac_f32_16x16x64_bf16 v[228:231], v[252:255], a[152:159], %[ix]\n\t" \
    "v_smfmac_f32_16x16x64_bf16 v[232:235], v[252:255], v[104:111], %[ix]\n\t" "s_waitcnt lgkmcnt(10)\n\t" \
    "v_smfmac_f32_16x16x64_bf16 v[236:239], v[252:255], v[168:175], %[ix]\n\t" "ds_read_b128 v[168:171], %[l0] offset:26624\n\t" \
    "ds_read_b128 v[172:175], %[l0] offset:27648\n\t" "v_smfmac_f32_16x16x64_bf16 v[224:227], v[192:195], a[160:167], %[ix]\n\t" \
    "ds_read_b128 v[252:255], %[l1] offset:11264\n\t" "v_smfmac_f32_16x16x64_bf16 v[228:231], v[192:195], a[168:175], %[ix]\n\t" "s_waitcnt lgkmcnt(12)\n\t" \
    "v_smfmac_f32_16x16x64_bf16 v[224:227], v[196:199], a[176:183], %[ix]\n\t" "v_smfmac_f32_16x16x64_bf16 v[228:231], v[196:199], a[184:191], %[ix]\n\t" \
    "s_waitcnt lgkmcnt(9)\n\t" "v_smfmac_f32_16x16x64_bf16 v[224:227], v[240:243], a[192:199], %[ix]\n\t" \
    "v_smfmac_f32_16x16x64_bf16 v[228:231], v[240:243], a[200:207], %[ix]\n\t" "s_waitcnt lgkmcnt(6)\n\t" \
    "v_smfmac_f32_16x16x64_bf16 v[224:227], v[244:247], a[208:215], %[ix]\n\t" "v_smfmac_f32_16x16x64_bf16 v[228:231], v[244:247], a[216:223], %[ix]\n\t" \
    "s_waitcnt lgkmcnt(3)\n\t" "v_smfmac_f32_16x16x64_bf16 v[224:227], v[248:251], a[224:231], %[ix]\n\t" \
    "v_smfmac_f32_16x16x64_bf16 v[228:231], v[248:251], a[232:239], %[ix]\n\t" "s_waitcnt lgkmcnt(0)\n\t" \
    "v_smfmac_f32_16x16x64_bf16 v[224:227], v[252:255], a[240:247], %[ix]\n\t" "s_nop 1\n\t" "v_smfmac_f32_16x16x64_bf16 v[228:231], v[252:255], a[248:255], %[ix]\n\t" \
    XT(90, 91) "s_nop 1\n\t" "v_smfmac_f32_16x16x64_bf16 v[232:235], v[192:195], v[112:119], %[ix]\n\t" "s_nop 1\n\t" \
    "v_smfmac_f32_16x16x64_bf16 v[236:239], v[192:195], v[176:183], %[ix]\n\t" "ds_read_b128 v[176:179], %[l0] offset:28672\n\t" "v_add_f32 v224, v224, v225\n\t" \
    "v_add_f32 v226, v226, v227\n\t" "v_smfmac_f32_16x16x64_bf16 v[232:235], v[196:199], v[120:127], %[ix]\n\t" "ds_read_b128 v[180:183], %[l0] offset:29696\n\t" \
    "v_add_f32 v228, v228, v229\n\t" "v_add_f32 v230, v230, v231\n\t" "v_smfmac_f32_16x16x64_bf16 v[236:239], v[196:199], v[184:191], %[ix]\n\t" \
    "ds_read_b128 v[184:187], %[l0] offset:30720\n\t" "v_cvt_pk_bf16_f32 v218, v224, v228\n\t" "v_cvt_pk_bf16_f32 v219, v226, v230\n\t" \
    "v_smfmac_f32_16x16x64_bf16 v[232:235], v[240:243], v[128:135], %[ix]\n\t" "ds_write2_b32 %[qw], v218, v219 offset1:8\n\t" \
    "ds_read_b128 v[188:191], %[l0] offset:31744\n\t" "v_smfmac_f32_16x16x64_bf16 v[236:239], v[240:243], v[160:167], %[ix]\n\t" \
    "v_smfmac_f32_16x16x64_bf16 v[232:235], v[244:247], v[136:143], %[ix]\n\t" "v_smfmac_f32_16x16x64_bf16 v[236:239], v[244:247], v[168:175], %[ix]\n\t" \
    "s_waitcnt lgkmcnt(1)\n\t" "s_barrier\n\t" "v_smfmac_f32_16x16x64_bf16 v[232:235], v[248:251], v[144:151], %[ix]\n\t" \
    "v_smfmac_f32_16x16x64_bf16 v[236:239], v[248:251], v[176:183], %[ix]\n\t" "v_smfmac_f32_16x16x64_bf16 v[232:235], v[252:255], v[152:159], %[ix]\n\t" \
    "s_waitcnt lgkmcnt(0)\n\t" "v_smfmac_f32_16x16x64_bf16 v[236:239], v[252:255], v[184:191], %[ix]\n\t" XT(92, 93) "s_nop 6\n\t" "v_add_f32 v232, v232, v233\n\t" \
    "v_add_f32 v234, v234, v235\n\t" "v_add_f32 v236, v236, v237\n\t" "v_add_f32 v238, v238, v239\n\t" "v_cvt_pk_bf16_f32 v232, v232, v236\n\t" \
    "v_cvt_pk_bf16_f32 v233, v234, v238\n\t" "ds_write2_b32 %[kw], v232, v233 offset1:68\n\t" "s_waitcnt lgkmcnt(0)\n\t" "s_barrier\n\t" XT(94, 95) XT(96, 97) \
    "s_branch 99f\n\t" "41:\n\t" "ds_read_b128 v[220:223], %[l1] offset:12288\n\t" "s_waitcnt lgkmcnt(0)\n\t" "v_max3_u32 v218, v220, v221, v222\n\t" \
    "v_max_u32 v218, v218, v223\n\t" "ds_read_b128 v[220:223], %[l1] offset:13312\n\t" "s_waitcnt lgkmcnt(0)\n\t" "v_max3_u32 v218, v218, v220, v221\n\t" \
    "v_max3_u32 v218, v218, v222, v223\n\t" "ds_read_b128 v[220:223], %[l1] offset:14336\n\t" "s_waitcnt lgkmcnt(0)\n\t" "v_max3_u32 v218, v218, v220, v221\n\t" \
    "v_max3_u32 v218, v218, v222, v223\n\t" "ds_read_b128 v[220:223], %[l1] offset:15360\n\t" "s_waitcnt lgkmcnt(0)\n\t" "v_max3_u32 v218, v218, v220, v221\n\t" \
    "v_max3_u32 v218, v218, v222, v223\n\t" "v_cmp_eq_u32 vcc, -1, v218\n\t" "s_cbranch_vccz 42b\n\t" "s_cmp_eq_u32 %[rt], 1\n\t" "s_cbranch_scc1 42b\n\t" \
    "v_mov_b32 v218, %[rt]\n\t" "ds_write_b32 v219, v218\n\t" "s_waitcnt lgkmcnt(0)\n\t" "s_branch 42b\n\t" "43:\n\t" "s_sub_u32 %[rt], %[rt], 1\n\t" \
    "s_waitcnt lgkmcnt(0)\n\t" XCNT(100) "44:\n\t" "buffer_load_dwordx4 v[240:243], %[mo], %[rs], %[sb] offen sc1\n\t" \
    "buffer_load_dwordx4 v[244:247], v216, %[rs], %[sb] offen sc1\n\t" "buffer_load_dwordx4 v[248:251], %[mo], %[rs], s60 offen sc1\n\t" \
    "buffer_load_dwordx4 v[252:255], v216, %[rs], s60 offen sc1\n\t" "s_waitcnt vmcnt(0)\n\t" "v_max_u32 v218, v240, v241\n\t" "v_max3_u32 v218, v218, v242, v243\n\t" \
    "v_max3_u32 v218, v218, v244, v245\n\t" "v_max3_u32 v218, v218, v246, v247\n\t" "v_max3_u32 v218, v218, v248, v249\n\t" "v_max3_u32 v218, v218, v250, v251\n\t" \
    "v_max3_u32 v218, v218, v252, v253\n\t" "v_max3_u32 v218, v218, v254, v255\n\t" "v_cmp_eq_u32 vcc, -1, v218\n\t" "s_cbranch_vccz 45f\n\t" "s_cmp_eq_u32 %[bd], 0\n\t" \
    "s_cbranch_scc1 45f\n\t" "s_sub_u32 %[bd], %[bd], 1\n\t" XCNT(101) "s_branch 44b\n\t" "45:\n\t" "ds_read_b128 v[160:163], %[l0] offset:0\n\t" \
    "ds_read_b128 v[164:167], %[l0] offset:1024\n\t" "ds_read_b128 v[168:171], %[l0] offset:2048\n\t" "ds_read_b128 v[172:175], %[l0] offset:3072\n\t" \
    "ds_read_b128 v[176:179], %[l0] offset:4096\n\t" "ds_read_b128 v[180:183], %[l0] offset:5120\n\t" "ds_read_b128 v[184:187], %[l0] offset:6144\n\t" \
    "ds_read_b128 v[188:191], %[l0] offset:7168\n\t" "v_mov_b64 v[224:225], 0\n\t" "v_mov_b64 v[226:227], 0\n\t" "v_mov_b64 v[228:229], 0\n\t" "v_mov_b64 v[230:231], 0\n\t" \
    "v_mov_b64 v[232:233], 0\n\t" "v_mov_b64 v[234:235], 0\n\t" "v_mov_b64 v[236:237], 0\n\t" "v_mov_b64 v[238:239], 0\n\t" "s_barrier\n\t" "s_memtime s[64:65]\n\t" \
    "s_waitcnt lgkmcnt(0)\n\t" "s_add_u32 s64, s64, %[dr]\n\t" "s_sub_u32 s62, s64, %[ca]\n\t" "s_mul_hi_u32 s63, s62, %[k1]\n\t" "s_mul_i32 s62, s62, %[k1]\n\t" \
    "s_lshr_b64 s[62:63], s[62:63], 16\n\t" "s_add_u32 s62, s62, %[ga]\n\t" "s_bitset0_b32 s62, 0\n\t" "v_mov_b32 v218, s62\n\t" \
    "buffer_store_dword v218, %[hq], %[rs], s67 offen sc1\n\t" "s_mov_b32 s67, 0x7f000000\n\t" "s_branch 40b\n\t" "99:\n\t" "mk_tail:\n\t"
#define VO_STEP \
    ".p2align 6\n\t" "v_xor_b32 v224, 0x400, %[mo]\n\t" "s_xor_b32 s60, %[sb], 0x800\n\t" "s_mov_b32 s67, %[pb]\n\t" "s_cmp_eq_u32 %[w], 1\n\t" "s_cbranch_scc1 91f\n\t" \
    "s_cmp_eq_u32 %[w], 2\n\t" "s_cbranch_scc1 92f\n\t" "s_cmp_eq_u32 %[w], 3\n\t" "s_cbranch_scc1 93f\n\t" XT(80, 81) "s_getpc_b64 s[62:63]\n\t" "18:\n\t" \
    "s_memtime s[64:65]\n\t" "s_waitcnt lgkmcnt(0)\n\t" "s_sub_u32 s61, %[tg], s64\n\t" "s_cmp_gt_i32 s61, 124\n\t" "s_cbranch_scc1 18b\n\t" "s_max_i32 s61, s61, 0\n\t" \
    "s_and_b32 s61, s61, 124\n\t" "s_sub_u32 s61, 19f-18b, s61\n\t" "s_add_u32 s62, s62, s61\n\t" "s_addc_u32 s63, s63, 0\n\t" "s_setpc_b64 s[62:63]\n\t" ".rept 31\n\t" \
    "s_nop 0\n\t" ".endr\n\t" "19:\n\t" "s_barrier\n\t" XT(82, 83) "buffer_load_dwordx4 v[240:243], %[mo], %[rs], %[sb] offen sc1\n\t" "v_mov_b64 v[232:233], 0\n\t" \
    "v_mov_b64 v[234:235], 0\n\t" "v_mov_b64 v[236:237], 0\n\t" "v_mov_b64 v[238:239], 0\n\t" "v_mov_b64 v[192:193], 0\n\t" "v_mov_b64 v[194:195], 0\n\t" \
    "v_mov_b64 v[196:197], 0\n\t" "v_mov_b64 v[198:199], 0\n\t" "buffer_load_dwordx4 v[244:247], v224, %[rs], %[sb] offen sc1\n\t" \
    "ds_read_b128 v[160:163], %[l0] offset:0\n\t" "ds_read_b128 v[164:167], %[l0] offset:1024\n\t" "ds_read_b128 v[168:171], %[l0] offset:2048\n\t" \
    "ds_read_b128 v[172:175], %[l0] offset:3072\n\t" "s_nop 3\n\t" "buffer_load_dwordx4 v[248:251], %[mo], %[rs], s60 offen sc1\n\t" \
    "ds_read_b128 v[176:179], %[l0] offset:4096\n\t" "ds_read_b128 v[180:183], %[l0] offset:5120\n\t" "ds_read_b128 v[184:187], %[l0] offset:6144\n\t" \
    "ds_read_b128 v[188:191], %[l0] offset:7168\n\t" "s_nop 3\n\t" "buffer_load_dwordx4 v[252:255], v224, %[rs], s60 offen sc1\n\t" "v_mov_b64 v[200:201], 0\n\t" \
    "v_mov_b64 v[202:203], 0\n\t" "v_mov_b64 v[204:205], 0\n\t" "v_mov_b64 v[206:207], 0\n\t" "v_mov_b64 v[208:209], 0\n\t" "v_mov_b64 v[210:211], 0\n\t" \
    "v_mov_b64 v[212:213], 0\n\t" "v_mov_b64 v[214:215], 0\n\t" "v_mov_b64 v[216:217], 0\n\t" "v_mov_b64 v[218:219], 0\n\t" "v_mov_b64 v[220:221], 0\n\t" \
    "v_mov_b64 v[222:223], 0\n\t" "v_mov_b32 v226, -1\n\t" "buffer_store_dword v226, %[hq], %[rs], %[pr] offen sc1\n\t" "v_mov_b32 v227, 160512\n\t" "10:\n\t" \
    "s_waitcnt vmcnt(4)\n\t" XT(84, 85) "v_smfmac_f32_16x16x64_bf16 v[232:235], v[240:243], a[0:7], %[ix]\n\t" "ds_write_b128 %[l1], v[240:243] offset:0\n\t" \
    "v_smfmac_f32_16x16x64_bf16 v[236:239], v[240:243], a[8:15], %[ix]\n\t" "s_waitcnt vmcnt(3)\n\t" "v_smfmac_f32_16x16x64_bf16 v[232:235], v[244:247], a[16:23], %[ix]\n\t" \
    "ds_write_b128 %[l1], v[244:247] offset:1024\n\t" "s_waitcnt lgkmcnt(8)\n\t" "v_smfmac_f32_16x16x64_bf16 v[236:239], v[244:247], v[160:167], %[ix]\n\t" \
    "s_waitcnt lgkmcnt(0)\n\t" "s_barrier\n\t" XT(86, 87) "ds_read_b128 v[16:19], %[l1] offset:4096\n\t" "ds_read_b128 v[20:23], %[l1] offset:8192\n\t" \
    "ds_read_b128 v[24:27], %[l1] offset:12288\n\t" "s_waitcnt vmcnt(2)\n\t" "v_smfmac_f32_16x16x64_bf16 v[232:235], v[248:251], a[24:31], %[ix]\n\t" \
    "ds_write_b128 %[l1], v[248:251] offset:2048\n\t" "v_smfmac_f32_16x16x64_bf16 v[236:239], v[248:251], a[32:39], %[ix]\n\t" "ds_read_b128 v[28:31], %[l1] offset:5120\n\t" \
    "ds_read_b128 v[240:243], %[l1] offset:9216\n\t" "ds_read_b128 v[244:247], %[l1] offset:13312\n\t" "s_waitcnt vmcnt(1)\n\t" \
    "v_smfmac_f32_16x16x64_bf16 v[232:235], v[252:255], a[40:47], %[ix]\n\t" "ds_write_b128 %[l1], v[252:255] offset:3072\n\t" \
    "v_smfmac_f32_16x16x64_bf16 v[236:239], v[252:255], v[168:175], %[ix]\n\t" "ds_read_b128 v[160:163], %[l0] offset:8192\n\t" \
    "ds_read_b128 v[164:167], %[l0] offset:9216\n\t" "ds_read_b128 v[168:171], %[l0] offset:10240\n\t" "ds_read_b128 v[172:175], %[l0] offset:11264\n\t" "s_nop 1\n\t" \
    "v_cmp_u_f32 vcc, v232, v233\n\t" "v_cmp_u_f32 s[62:63], v234, v235\n\t" "s_waitcnt lgkmcnt(11)\n\t" \
    "v_smfmac_f32_16x16x64_bf16 v[232:235], v[16:19], a[48:55], %[ix]\n\t" "v_smfmac_f32_16x16x64_bf16 v[236:239], v[16:19], a[56:63], %[ix]\n\t" \
    "s_or_b64 vcc, vcc, s[62:63]\n\t" "s_cbranch_scc1 11f\n\t" "12:\n\t" "s_waitcnt lgkmcnt(10)\n\t" "v_smfmac_f32_16x16x64_bf16 v[232:235], v[20:23], a[64:71], %[ix]\n\t" \
    "v_smfmac_f32_16x16x64_bf16 v[236:239], v[20:23], v[176:183], %[ix]\n\t" "s_waitcnt lgkmcnt(4)\n\t" "s_barrier\n\t" XT(88, 89) "ds_read_b32 v225, v227\n\t" \
    "v_smfmac_f32_16x16x64_bf16 v[232:235], v[24:27], a[72:79], %[ix]\n\t" "ds_read_b128 v[176:179], %[l0] offset:12288\n\t" "ds_read_b128 v[180:183], %[l0] offset:13312\n\t" \
    "v_smfmac_f32_16x16x64_bf16 v[236:239], v[24:27], a[80:87], %[ix]\n\t" "ds_read_b128 v[248:251], %[l1] offset:6144\n\t" "ds_read_b128 v[252:255], %[l1] offset:10240\n\t" \
    "v_smfmac_f32_16x16x64_bf16 v[232:235], v[28:31], a[88:95], %[ix]\n\t" "ds_read_b128 v[16:19], %[l1] offset:14336\n\t" "ds_read_b128 v[20:23], %[l1] offset:7168\n\t" \
    "v_smfmac_f32_16x16x64_bf16 v[236:239], v[28:31], v[184:191], %[ix]\n\t" "s_memtime s[64:65]\n\t" "ds_read_b128 v[24:27], %[l1] offset:11264\n\t" \
    "v_smfmac_f32_16x16x64_bf16 v[232:235], v[240:243], a[96:103], %[ix]\n\t" "ds_read_b128 v[184:187], %[l0] offset:14336\n\t" \
    "ds_read_b128 v[188:191], %[l0] offset:15360\n\t" "v_smfmac_f32_16x16x64_bf16 v[236:239], v[240:243], a[104:111], %[ix]\n\t" "s_waitcnt lgkmcnt(9)\n\t" \
    "v_cmp_eq_u32 vcc, %[rt], v225\n\t" "v_smfmac_f32_16x16x64_bf16 v[232:235], v[244:247], a[112:119], %[ix]\n\t" "s_cbranch_vccnz 13f\n\t" \
    "ds_read_b128 v[28:31], %[l1] offset:15360\n\t" "v_smfmac_f32_16x16x64_bf16 v[236:239], v[244:247], v[160:167], %[ix]\n\t" \
    "ds_read_b128 v[160:163], %[l0] offset:16384\n\t" "ds_read_b128 v[164:167], %[l0] offset:17408\n\t" "s_waitcnt lgkmcnt(9)\n\t" \
    "v_smfmac_f32_16x16x64_bf16 v[232:235], v[248:251], a[120:127], %[ix]\n\t" "s_sub_u32 s62, s64, %[ca]\n\t" "s_mul_hi_u32 s63, s62, %[k1]\n\t" \
    "v_smfmac_f32_16x16x64_bf16 v[236:239], v[248:251], a[128:135], %[ix]\n\t" "s_mul_i32 s62, s62, %[k1]\n\t" "s_lshr_b64 s[62:63], s[62:63], 16\n\t" \
    "s_waitcnt lgkmcnt(8)\n\t" "v_smfmac_f32_16x16x64_bf16 v[232:235], v[252:255], a[136:143], %[ix]\n\t" "s_add_u32 s62, s62, %[ga]\n\t" "s_bitset0_b32 s62, 0\n\t" \
    "v_smfmac_f32_16x16x64_bf16 v[236:239], v[252:255], v[168:175], %[ix]\n\t" "v_mov_b32 v226, s62\n\t" "buffer_store_dword v226, %[hq], %[rs], s67 offen sc1\n\t" \
    "s_waitcnt lgkmcnt(7)\n\t" "v_smfmac_f32_16x16x64_bf16 v[232:235], v[16:19], a[144:151], %[ix]\n\t" "ds_read_b128 v[168:171], %[l0] offset:18432\n\t" \
    "ds_read_b128 v[172:175], %[l0] offset:19456\n\t" "v_smfmac_f32_16x16x64_bf16 v[236:239], v[16:19], a[152:159], %[ix]\n\t" "s_waitcnt lgkmcnt(8)\n\t" \
    "v_smfmac_f32_16x16x64_bf16 v[232:235], v[20:23], a[160:167], %[ix]\n\t" "v_smfmac_f32_16x16x64_bf16 v[236:239], v[20:23], v[176:183], %[ix]\n\t" \
    "ds_read_b128 v[176:179], %[l0] offset:20480\n\t" "ds_read_b128 v[180:183], %[l0] offset:21504\n\t" "s_waitcnt lgkmcnt(9)\n\t" \
    "v_smfmac_f32_16x16x64_bf16 v[232:235], v[24:27], a[168:175], %[ix]\n\t" "v_smfmac_f32_16x16x64_bf16 v[236:239], v[24:27], a[176:183], %[ix]\n\t" \
    "s_waitcnt lgkmcnt(6)\n\t" "v_smfmac_f32_16x16x64_bf16 v[232:235], v[28:31], a[184:191], %[ix]\n\t" \
    "v_smfmac_f32_16x16x64_bf16 v[236:239], v[28:31], v[184:191], %[ix]\n\t" "ds_read_b128 v[184:187], %[l0] offset:22528\n\t" \
    "ds_read_b128 v[188:191], %[l0] offset:23552\n\t" XT(90, 91) "s_nop 4\n\t" "v_add_f32 v232, v232, v233\n\t" "v_add_f32 v234, v234, v235\n\t" \
    "v_add_f32 v236, v236, v237\n\t" "v_add_f32 v238, v238, v239\n\t" "v_cvt_pk_bf16_f32 v236, v232, v236\n\t" "v_cvt_pk_bf16_f32 v237, v234, v238\n\t" \
    "ds_write2_b32 %[vw], v236, v237 offset1:68\n\t" "s_waitcnt lgkmcnt(0)\n\t" "s_barrier\n\t" XT(92, 93) "ds_read_b128 v[240:243], %[vr] offset:0\n\t" \
    "ds_read_b128 v[244:247], %[vr] offset:64\n\t" "ds_read_b128 v[248:251], %[vr] offset:128\n\t" "ds_read_b128 v[252:255], %[vr] offset:192\n\t" "s_mov_b32 s61, %[ub]\n\t" \
    "s_waitcnt lgkmcnt(3)\n\t" "v_smfmac_f32_16x16x64_bf16 v[192:195], v[240:243], v[32:39], %[ix]\n\t" \
    "v_smfmac_f32_16x16x64_bf16 v[196:199], v[240:243], v[48:55], %[ix]\n\t" "s_waitcnt lgkmcnt(2)\n\t" \
    "v_smfmac_f32_16x16x64_bf16 v[192:195], v[244:247], v[40:47], %[ix]\n\t" "v_smfmac_f32_16x16x64_bf16 v[196:199], v[244:247], v[56:63], %[ix]\n\t" \
    "s_waitcnt lgkmcnt(1)\n\t" "v_smfmac_f32_16x16x64_bf16 v[192:195], v[248:251], a[192:199], %[ix]\n\t" \
    "v_smfmac_f32_16x16x64_bf16 v[196:199], v[248:251], a[200:207], %[ix]\n\t" "s_waitcnt lgkmcnt(0)\n\t" \
    "v_smfmac_f32_16x16x64_bf16 v[192:195], v[252:255], v[160:167], %[ix]\n\t" "ds_read_b128 v[160:163], %[l0] offset:24576\n\t" \
    "ds_read_b128 v[164:167], %[l0] offset:25600\n\t" "v_smfmac_f32_16x16x64_bf16 v[196:199], v[252:255], v[168:175], %[ix]\n\t" \
    "ds_read_b128 v[168:171], %[l0] offset:26624\n\t" "ds_read_b128 v[172:175], %[l0] offset:27648\n\t" \
    "v_smfmac_f32_16x16x64_bf16 v[200:203], v[240:243], v[64:71], %[ix]\n\t" "s_nop 1\n\t" "v_smfmac_f32_16x16x64_bf16 v[204:207], v[240:243], v[80:87], %[ix]\n\t" \
    "v_cvt_pk_bf16_f32 v224, v192, v194\n\t" "v_cvt_pk_bf16_f32 v225, v193, v195\n\t" "v_smfmac_f32_16x16x64_bf16 v[200:203], v[244:247], v[72:79], %[ix]\n\t" \
    "v_cvt_pk_bf16_f32 v226, v196, v198\n\t" "v_cvt_pk_bf16_f32 v227, v197, v199\n\t" "v_smfmac_f32_16x16x64_bf16 v[204:207], v[244:247], v[88:95], %[ix]\n\t" "s_nop 0\n\t" \
    "v_permlane32_swap_b32 v224, v226\n\t" "v_smfmac_f32_16x16x64_bf16 v[200:203], v[248:251], a[208:215], %[ix]\n\t" "v_permlane32_swap_b32 v225, v227\n\t" "s_nop 0\n\t" \
    "v_smfmac_f32_16x16x64_bf16 v[204:207], v[248:251], a[216:223], %[ix]\n\t" "v_permlane16_swap_b32 v224, v225\n\t" "v_permlane16_swap_b32 v226, v227\n\t" \
    "v_smfmac_f32_16x16x64_bf16 v[200:203], v[252:255], v[176:183], %[ix]\n\t" "ds_read_b128 v[176:179], %[l0] offset:28672\n\t" \
    "buffer_store_dwordx4 v[224:227], %[uo], %[rs], s61 offen sc1\n\t" "s_xor_b32 s61, s61, 0x40000\n\t" XT(94, 95) \
    "v_smfmac_f32_16x16x64_bf16 v[204:207], v[252:255], v[184:191], %[ix]\n\t" "ds_read_b128 v[180:183], %[l0] offset:29696\n\t" \
    "ds_read_b128 v[184:187], %[l0] offset:30720\n\t" "v_smfmac_f32_16x16x64_bf16 v[208:211], v[240:243], v[96:103], %[ix]\n\t" \
    "ds_read_b128 v[188:191], %[l0] offset:31744\n\t" "s_nop 0\n\t" "v_smfmac_f32_16x16x64_bf16 v[212:215], v[240:243], v[112:119], %[ix]\n\t" \
    "v_cvt_pk_bf16_f32 v228, v200, v202\n\t" "v_cvt_pk_bf16_f32 v229, v201, v203\n\t" "v_smfmac_f32_16x16x64_bf16 v[208:211], v[244:247], v[104:111], %[ix]\n\t" \
    "v_cvt_pk_bf16_f32 v230, v204, v206\n\t" "v_cvt_pk_bf16_f32 v231, v205, v207\n\t" "v_smfmac_f32_16x16x64_bf16 v[212:215], v[244:247], v[120:127], %[ix]\n\t" "s_nop 0\n\t" \
    "v_permlane32_swap_b32 v228, v230\n\t" "v_smfmac_f32_16x16x64_bf16 v[208:211], v[248:251], a[224:231], %[ix]\n\t" "v_permlane32_swap_b32 v229, v231\n\t" "s_nop 0\n\t" \
    "v_smfmac_f32_16x16x64_bf16 v[212:215], v[248:251], a[232:239], %[ix]\n\t" "v_permlane16_swap_b32 v228, v229\n\t" "v_permlane16_swap_b32 v230, v231\n\t" \
    "s_waitcnt lgkmcnt(6)\n\t" "v_smfmac_f32_16x16x64_bf16 v[208:211], v[252:255], v[160:167], %[ix]\n\t" "buffer_store_dwordx4 v[228:231], %[uo], %[rs], s61 offen sc1\n\t" \
    "s_xor_b32 s61, s61, 0xc0000\n\t" "s_waitcnt lgkmcnt(4)\n\t" "v_smfmac_f32_16x16x64_bf16 v[212:215], v[252:255], v[168:175], %[ix]\n\t" "s_nop 1\n\t" \
    "v_smfmac_f32_16x16x64_bf16 v[216:219], v[240:243], v[128:135], %[ix]\n\t" "s_nop 1\n\t" "v_smfmac_f32_16x16x64_bf16 v[220:223], v[240:243], v[144:151], %[ix]\n\t" \
    "v_cvt_pk_bf16_f32 v224, v208, v210\n\t" "v_cvt_pk_bf16_f32 v225, v209, v211\n\t" "v_smfmac_f32_16x16x64_bf16 v[216:219], v[244:247], v[136:143], %[ix]\n\t" \
    "v_cvt_pk_bf16_f32 v226, v212, v214\n\t" "v_cvt_pk_bf16_f32 v227, v213, v215\n\t" "v_smfmac_f32_16x16x64_bf16 v[220:223], v[244:247], v[152:159], %[ix]\n\t" "s_nop 0\n\t" \
    "v_permlane32_swap_b32 v224, v226\n\t" "v_smfmac_f32_16x16x64_bf16 v[216:219], v[248:251], a[240:247], %[ix]\n\t" "v_permlane32_swap_b32 v225, v227\n\t" "s_nop 0\n\t" \
    "v_smfmac_f32_16x16x64_bf16 v[220:223], v[248:251], a[248:255], %[ix]\n\t" "v_permlane16_swap_b32 v224, v225\n\t" "v_permlane16_swap_b32 v226, v227\n\t" \
    "s_waitcnt lgkmcnt(2)\n\t" "v_smfmac_f32_16x16x64_bf16 v[216:219], v[252:255], v[176:183], %[ix]\n\t" "buffer_store_dwordx4 v[224:227], %[uo], %[rs], s61 offen sc1\n\t" \
    "s_xor_b32 s61, s61, 0x40000\n\t" "s_waitcnt lgkmcnt(0)\n\t" "v_smfmac_f32_16x16x64_bf16 v[220:223], v[252:255], v[184:191], %[ix]\n\t" "s_nop 5\n\t" \
    "v_cvt_pk_bf16_f32 v228, v216, v218\n\t" "v_cvt_pk_bf16_f32 v229, v217, v219\n\t" "v_cvt_pk_bf16_f32 v230, v220, v222\n\t" "v_cvt_pk_bf16_f32 v231, v221, v223\n\t" \
    "s_nop 0\n\t" "v_permlane32_swap_b32 v228, v230\n\t" "v_permlane32_swap_b32 v229, v231\n\t" "s_nop 1\n\t" "v_permlane16_swap_b32 v228, v229\n\t" \
    "v_permlane16_swap_b32 v230, v231\n\t" "buffer_store_dwordx4 v[228:231], %[uo], %[rs], s61 offen sc1\n\t" "s_nop 1\n\t" XT(96, 97) "s_branch 99f\n\t" "11:\n\t" \
    "ds_read_b128 v[228:231], %[l1] offset:0\n\t" "s_waitcnt lgkmcnt(0)\n\t" "v_max3_u32 v225, v228, v229, v230\n\t" "v_max_u32 v225, v225, v231\n\t" \
    "ds_read_b128 v[228:231], %[l1] offset:1024\n\t" "s_waitcnt lgkmcnt(0)\n\t" "v_max3_u32 v225, v225, v228, v229\n\t" "v_max3_u32 v225, v225, v230, v231\n\t" \
    "ds_read_b128 v[228:231], %[l1] offset:2048\n\t" "s_waitcnt lgkmcnt(0)\n\t" "v_max3_u32 v225, v225, v228, v229\n\t" "v_max3_u32 v225, v225, v230, v231\n\t" \
    "ds_read_b128 v[228:231], %[l1] offset:3072\n\t" "s_waitcnt lgkmcnt(0)\n\t" "v_max3_u32 v225, v225, v228, v229\n\t" "v_max3_u32 v225, v225, v230, v231\n\t" \
    "v_cmp_eq_u32 vcc, -1, v225\n\t" "s_cbranch_vccz 12b\n\t" "s_cmp_eq_u32 %[rt], 1\n\t" "s_cbranch_scc1 12b\n\t" "v_mov_b32 v225, %[rt]\n\t" "ds_write_b32 v227, v225\n\t" \
    "s_waitcnt lgkmcnt(0)\n\t" "s_branch 12b\n\t" "13:\n\t" "s_sub_u32 %[rt], %[rt], 1\n\t" "s_waitcnt lgkmcnt(0)\n\t" XCNT(100) "14:\n\t" \
    "buffer_load_dwordx4 v[240:243], %[mo], %[rs], %[sb] offen sc1\n\t" "buffer_load_dwordx4 v[244:247], v224, %[rs], %[sb] offen sc1\n\t" \
    "buffer_load_dwordx4 v[248:251], %[mo], %[rs], s60 offen sc1\n\t" "buffer_load_dwordx4 v[252:255], v224, %[rs], s60 offen sc1\n\t" "s_waitcnt vmcnt(0)\n\t" \
    "v_max_u32 v225, v240, v241\n\t" "v_max3_u32 v225, v225, v242, v243\n\t" "v_max3_u32 v225, v225, v244, v245\n\t" "v_max3_u32 v225, v225, v246, v247\n\t" \
    "v_max3_u32 v225, v225, v248, v249\n\t" "v_max3_u32 v225, v225, v250, v251\n\t" "v_max3_u32 v225, v225, v252, v253\n\t" "v_max3_u32 v225, v225, v254, v255\n\t" \
    "v_cmp_eq_u32 vcc, -1, v225\n\t" "s_cbranch_vccz 15f\n\t" "s_cmp_eq_u32 %[bd], 0\n\t" "s_cbranch_scc1 15f\n\t" "s_sub_u32 %[bd], %[bd], 1\n\t" XCNT(101) \
    "s_branch 14b\n\t" "15:\n\t" "ds_read_b128 v[160:163], %[l0] offset:0\n\t" "ds_read_b128 v[164:167], %[l0] offset:1024\n\t" \
    "ds_read_b128 v[168:171], %[l0] offset:2048\n\t" "ds_read_b128 v[172:175], %[l0] offset:3072\n\t" "ds_read_b128 v[176:179], %[l0] offset:4096\n\t" \
    "ds_read_b128 v[180:183], %[l0] offset:5120\n\t" "ds_read_b128 v[184:187], %[l0] offset:6144\n\t" "ds_read_b128 v[188:191], %[l0] offset:7168\n\t" \
    "v_mov_b64 v[232:233], 0\n\t" "v_mov_b64 v[234:235], 0\n\t" "v_mov_b64 v[236:237], 0\n\t" "v_mov_b64 v[238:239], 0\n\t" "s_barrier\n\t" "s_memtime s[64:65]\n\t" \
    "s_waitcnt lgkmcnt(0)\n\t" "s_add_u32 s64, s64, %[dr]\n\t" "s_sub_u32 s62, s64, %[ca]\n\t" "s_mul_hi_u32 s63, s62, %[k1]\n\t" "s_mul_i32 s62, s62, %[k1]\n\t" \
    "s_lshr_b64 s[62:63], s[62:63], 16\n\t" "s_add_u32 s62, s62, %[ga]\n\t" "s_bitset0_b32 s62, 0\n\t" "v_mov_b32 v225, s62\n\t" \
    "buffer_store_dword v225, %[hq], %[rs], s67 offen sc1\n\t" "s_mov_b32 s67, 0x7f000000\n\t" "s_branch 10b\n\t" "91:\n\t" XT(80, 81) "s_barrier\n\t" XT(82, 83) \
    "buffer_load_dwordx4 v[240:243], %[mo], %[rs], %[sb] offen sc1\n\t" "v_mov_b64 v[232:233], 0\n\t" "v_mov_b64 v[234:235], 0\n\t" "v_mov_b64 v[236:237], 0\n\t" \
    "v_mov_b64 v[238:239], 0\n\t" "v_mov_b64 v[192:193], 0\n\t" "v_mov_b64 v[194:195], 0\n\t" "v_mov_b64 v[196:197], 0\n\t" "v_mov_b64 v[198:199], 0\n\t" \
    "buffer_load_dwordx4 v[244:247], v224, %[rs], %[sb] offen sc1\n\t" "ds_read_b128 v[160:163], %[l0] offset:0\n\t" "ds_read_b128 v[164:167], %[l0] offset:1024\n\t" \
    "ds_read_b128 v[168:171], %[l0] offset:2048\n\t" "ds_read_b128 v[172:175], %[l0] offset:3072\n\t" "s_nop 3\n\t" \
    "buffer_load_dwordx4 v[248:251], %[mo], %[rs], s60 offen sc1\n\t" "ds_read_b128 v[176:179], %[l0] offset:4096\n\t" "ds_read_b128 v[180:183], %[l0] offset:5120\n\t" \
    "ds_read_b128 v[184:187], %[l0] offset:6144\n\t" "ds_read_b128 v[188:191], %[l0] offset:7168\n\t" "s_nop 3\n\t" \
    "buffer_load_dwordx4 v[252:255], v224, %[rs], s60 offen sc1\n\t" "v_mov_b64 v[200:201], 0\n\t" "v_mov_b64 v[202:203], 0\n\t" "v_mov_b64 v[204:205], 0\n\t" \
    "v_mov_b64 v[206:207], 0\n\t" "v_mov_b64 v[208:209], 0\n\t" "v_mov_b64 v[210:211], 0\n\t" "v_mov_b64 v[212:213], 0\n\t" "v_mov_b64 v[214:215], 0\n\t" \
    "v_mov_b64 v[216:217], 0\n\t" "v_mov_b64 v[218:219], 0\n\t" "v_mov_b64 v[220:221], 0\n\t" "v_mov_b64 v[222:223], 0\n\t" "v_mov_b32 v227, 160512\n\t" "20:\n\t" \
    "s_waitcnt vmcnt(3)\n\t" XT(84, 85) "v_smfmac_f32_16x16x64_bf16 v[232:235], v[240:243], a[0:7], %[ix]\n\t" "ds_write_b128 %[l1], v[240:243] offset:4096\n\t" \
    "v_smfmac_f32_16x16x64_bf16 v[236:239], v[240:243], a[8:15], %[ix]\n\t" "s_waitcnt vmcnt(2)\n\t" "v_smfmac_f32_16x16x64_bf16 v[232:235], v[244:247], a[16:23], %[ix]\n\t" \
    "ds_write_b128 %[l1], v[244:247] offset:5120\n\t" "s_waitcnt lgkmcnt(8)\n\t" "v_smfmac_f32_16x16x64_bf16 v[236:239], v[244:247], v[160:167], %[ix]\n\t" \
    "s_waitcnt lgkmcnt(0)\n\t" "s_barrier\n\t" XT(86, 87) "ds_read_b128 v[16:19], %[l1] offset:8192\n\t" "ds_read_b128 v[20:23], %[l1] offset:12288\n\t" \
    "ds_read_b128 v[24:27], %[l1] offset:0\n\t" "s_waitcnt vmcnt(1)\n\t" "v_smfmac_f32_16x16x64_bf16 v[232:235], v[248:251], a[24:31], %[ix]\n\t" \
    "ds_write_b128 %[l1], v[248:251] offset:6144\n\t" "v_smfmac_f32_16x16x64_bf16 v[236:239], v[248:251], a[32:39], %[ix]\n\t" "ds_read_b128 v[28:31], %[l1] offset:9216\n\t" \
    "ds_read_b128 v[240:243], %[l1] offset:13312\n\t" "ds_read_b128 v[244:247], %[l1] offset:1024\n\t" "s_waitcnt vmcnt(0)\n\t" \
    "v_smfmac_f32_16x16x64_bf16 v[232:235], v[252:255], a[40:47], %[ix]\n\t" "ds_write_b128 %[l1], v[252:255] offset:7168\n\t" \
    "v_smfmac_f32_16x16x64_bf16 v[236:239], v[252:255], v[168:175], %[ix]\n\t" "ds_read_b128 v[160:163], %[l0] offset:8192\n\t" \
    "ds_read_b128 v[164:167], %[l0] offset:9216\n\t" "ds_read_b128 v[168:171], %[l0] offset:10240\n\t" "ds_read_b128 v[172:175], %[l0] offset:11264\n\t" "s_nop 1\n\t" \
    "v_cmp_u_f32 vcc, v232, v233\n\t" "v_cmp_u_f32 s[62:63], v234, v235\n\t" "s_waitcnt lgkmcnt(11)\n\t" \
    "v_smfmac_f32_16x16x64_bf16 v[232:235], v[16:19], a[48:55], %[ix]\n\t" "v_smfmac_f32_16x16x64_bf16 v[236:239], v[16:19], a[56:63], %[ix]\n\t" \
    "s_or_b64 vcc, vcc, s[62:63]\n\t" "s_cbranch_scc1 21f\n\t" "22:\n\t" "s_waitcnt lgkmcnt(10)\n\t" "v_smfmac_f32_16x16x64_bf16 v[232:235], v[20:23], a[64:71], %[ix]\n\t" \
    "v_smfmac_f32_16x16x64_bf16 v[236:239], v[20:23], v[176:183], %[ix]\n\t" "s_waitcnt lgkmcnt(4)\n\t" "s_barrier\n\t" XT(88, 89) "ds_read_b32 v225, v227\n\t" \
    "v_smfmac_f32_16x16x64_bf16 v[232:235], v[24:27], a[72:79], %[ix]\n\t" "ds_read_b128 v[176:179], %[l0] offset:12288\n\t" "ds_read_b128 v[180:183], %[l0] offset:13312\n\t" \
    "v_smfmac_f32_16x16x64_bf16 v[236:239], v[24:27], a[80:87], %[ix]\n\t" "ds_read_b128 v[248:251], %[l1] offset:10240\n\t" "ds_read_b128 v[252:255], %[l1] offset:14336\n\t" \
    "v_smfmac_f32_16x16x64_bf16 v[232:235], v[28:31], a[88:95], %[ix]\n\t" "ds_read_b128 v[16:19], %[l1] offset:2048\n\t" "ds_read_b128 v[20:23], %[l1] offset:11264\n\t" \
    "v_smfmac_f32_16x16x64_bf16 v[236:239], v[28:31], v[184:191], %[ix]\n\t" "ds_read_b128 v[24:27], %[l1] offset:15360\n\t" "ds_read_b128 v[184:187], %[l0] offset:14336\n\t" \
    "v_smfmac_f32_16x16x64_bf16 v[232:235], v[240:243], a[96:103], %[ix]\n\t" "ds_read_b128 v[188:191], %[l0] offset:15360\n\t" "ds_read_b128 v[28:31], %[l1] offset:3072\n\t" \
    "v_smfmac_f32_16x16x64_bf16 v[236:239], v[240:243], a[104:111], %[ix]\n\t" "s_waitcnt lgkmcnt(10)\n\t" "v_cmp_eq_u32 vcc, %[rt], v225\n\t" \
    "v_smfmac_f32_16x16x64_bf16 v[232:235], v[244:247], a[112:119], %[ix]\n\t" "s_cbranch_vccnz 23f\n\t" \
    "v_smfmac_f32_16x16x64_bf16 v[236:239], v[244:247], v[160:167], %[ix]\n\t" "ds_read_b128 v[160:163], %[l0] offset:16384\n\t" \
    "ds_read_b128 v[164:167], %[l0] offset:17408\n\t" "s_waitcnt lgkmcnt(9)\n\t" "v_smfmac_f32_16x16x64_bf16 v[232:235], v[248:251], a[120:127], %[ix]\n\t" \
    "v_smfmac_f32_16x16x64_bf16 v[236:239], v[248:251], a[128:135], %[ix]\n\t" "s_waitcnt lgkmcnt(8)\n\t" \
    "v_smfmac_f32_16x16x64_bf16 v[232:235], v[252:255], a[136:143], %[ix]\n\t" "v_smfmac_f32_16x16x64_bf16 v[236:239], v[252:255], v[168:175], %[ix]\n\t" \
    "ds_read_b128 v[168:171], %[l0] offset:18432\n\t" "ds_read_b128 v[172:175], %[l0] offset:19456\n\t" "s_waitcnt lgkmcnt(9)\n\t" \
    "v_smfmac_f32_16x16x64_bf16 v[232:235], v[16:19], a[144:151], %[ix]\n\t" "v_smfmac_f32_16x16x64_bf16 v[236:239], v[16:19], a[152:159], %[ix]\n\t" \
    "s_waitcnt lgkmcnt(8)\n\t" "v_smfmac_f32_16x16x64_bf16 v[232:235], v[20:23], a[160:167], %[ix]\n\t" \
    "v_smfmac_f32_16x16x64_bf16 v[236:239], v[20:23], v[176:183], %[ix]\n\t" "ds_read_b128 v[176:179], %[l0] offset:20480\n\t" \
    "ds_read_b128 v[180:183], %[l0] offset:21504\n\t" "s_waitcnt lgkmcnt(9)\n\t" "v_smfmac_f32_16x16x64_bf16 v[232:235], v[24:27], a[168:175], %[ix]\n\t" \
    "v_smfmac_f32_16x16x64_bf16 v[236:239], v[24:27], a[176:183], %[ix]\n\t" "s_waitcnt lgkmcnt(6)\n\t" \
    "v_smfmac_f32_16x16x64_bf16 v[232:235], v[28:31], a[184:191], %[ix]\n\t" "v_smfmac_f32_16x16x64_bf16 v[236:239], v[28:31], v[184:191], %[ix]\n\t" \
    "ds_read_b128 v[184:187], %[l0] offset:22528\n\t" "ds_read_b128 v[188:191], %[l0] offset:23552\n\t" XT(90, 91) "s_nop 4\n\t" "v_add_f32 v232, v232, v233\n\t" \
    "v_add_f32 v234, v234, v235\n\t" "v_add_f32 v236, v236, v237\n\t" "v_add_f32 v238, v238, v239\n\t" "v_cvt_pk_bf16_f32 v236, v232, v236\n\t" \
    "v_cvt_pk_bf16_f32 v237, v234, v238\n\t" "ds_write2_b32 %[vw], v236, v237 offset1:68\n\t" "s_waitcnt lgkmcnt(0)\n\t" "s_barrier\n\t" XT(92, 93) \
    "ds_read_b128 v[240:243], %[vr] offset:0\n\t" "ds_read_b128 v[244:247], %[vr] offset:64\n\t" "ds_read_b128 v[248:251], %[vr] offset:128\n\t" \
    "ds_read_b128 v[252:255], %[vr] offset:192\n\t" "s_mov_b32 s61, %[ub]\n\t" "s_waitcnt lgkmcnt(3)\n\t" \
    "v_smfmac_f32_16x16x64_bf16 v[192:195], v[240:243], v[32:39], %[ix]\n\t" "v_smfmac_f32_16x16x64_bf16 v[196:199], v[240:243], v[48:55], %[ix]\n\t" \
    "s_waitcnt lgkmcnt(2)\n\t" "v_smfmac_f32_16x16x64_bf16 v[192:195], v[244:247], v[40:47], %[ix]\n\t" \
    "v_smfmac_f32_16x16x64_bf16 v[196:199], v[244:247], v[56:63], %[ix]\n\t" "s_waitcnt lgkmcnt(1)\n\t" \
    "v_smfmac_f32_16x16x64_bf16 v[192:195], v[248:251], a[192:199], %[ix]\n\t" "v_smfmac_f32_16x16x64_bf16 v[196:199], v[248:251], a[200:207], %[ix]\n\t" \
    "s_waitcnt lgkmcnt(0)\n\t" "v_smfmac_f32_16x16x64_bf16 v[192:195], v[252:255], v[160:167], %[ix]\n\t" "ds_read_b128 v[160:163], %[l0] offset:24576\n\t" \
    "ds_read_b128 v[164:167], %[l0] offset:25600\n\t" "v_smfmac_f32_16x16x64_bf16 v[196:199], v[252:255], v[168:175], %[ix]\n\t" \
    "ds_read_b128 v[168:171], %[l0] offset:26624\n\t" "ds_read_b128 v[172:175], %[l0] offset:27648\n\t" \
    "v_smfmac_f32_16x16x64_bf16 v[200:203], v[240:243], v[64:71], %[ix]\n\t" "s_nop 1\n\t" "v_smfmac_f32_16x16x64_bf16 v[204:207], v[240:243], v[80:87], %[ix]\n\t" \
    "v_cvt_pk_bf16_f32 v224, v192, v194\n\t" "v_cvt_pk_bf16_f32 v225, v193, v195\n\t" "v_smfmac_f32_16x16x64_bf16 v[200:203], v[244:247], v[72:79], %[ix]\n\t" \
    "v_cvt_pk_bf16_f32 v226, v196, v198\n\t" "v_cvt_pk_bf16_f32 v227, v197, v199\n\t" "v_smfmac_f32_16x16x64_bf16 v[204:207], v[244:247], v[88:95], %[ix]\n\t" "s_nop 0\n\t" \
    "v_permlane32_swap_b32 v224, v226\n\t" "v_smfmac_f32_16x16x64_bf16 v[200:203], v[248:251], a[208:215], %[ix]\n\t" "v_permlane32_swap_b32 v225, v227\n\t" "s_nop 0\n\t" \
    "v_smfmac_f32_16x16x64_bf16 v[204:207], v[248:251], a[216:223], %[ix]\n\t" "v_permlane16_swap_b32 v224, v225\n\t" "v_permlane16_swap_b32 v226, v227\n\t" \
    "v_smfmac_f32_16x16x64_bf16 v[200:203], v[252:255], v[176:183], %[ix]\n\t" "ds_read_b128 v[176:179], %[l0] offset:28672\n\t" \
    "buffer_store_dwordx4 v[224:227], %[uo], %[rs], s61 offen sc1\n\t" "s_xor_b32 s61, s61, 0x40000\n\t" XT(94, 95) \
    "v_smfmac_f32_16x16x64_bf16 v[204:207], v[252:255], v[184:191], %[ix]\n\t" "ds_read_b128 v[180:183], %[l0] offset:29696\n\t" \
    "ds_read_b128 v[184:187], %[l0] offset:30720\n\t" "v_smfmac_f32_16x16x64_bf16 v[208:211], v[240:243], v[96:103], %[ix]\n\t" \
    "ds_read_b128 v[188:191], %[l0] offset:31744\n\t" "s_nop 0\n\t" "v_smfmac_f32_16x16x64_bf16 v[212:215], v[240:243], v[112:119], %[ix]\n\t" \
    "v_cvt_pk_bf16_f32 v228, v200, v202\n\t" "v_cvt_pk_bf16_f32 v229, v201, v203\n\t" "v_smfmac_f32_16x16x64_bf16 v[208:211], v[244:247], v[104:111], %[ix]\n\t" \
    "v_cvt_pk_bf16_f32 v230, v204, v206\n\t" "v_cvt_pk_bf16_f32 v231, v205, v207\n\t" "v_smfmac_f32_16x16x64_bf16 v[212:215], v[244:247], v[120:127], %[ix]\n\t" "s_nop 0\n\t" \
    "v_permlane32_swap_b32 v228, v230\n\t" "v_smfmac_f32_16x16x64_bf16 v[208:211], v[248:251], a[224:231], %[ix]\n\t" "v_permlane32_swap_b32 v229, v231\n\t" "s_nop 0\n\t" \
    "v_smfmac_f32_16x16x64_bf16 v[212:215], v[248:251], a[232:239], %[ix]\n\t" "v_permlane16_swap_b32 v228, v229\n\t" "v_permlane16_swap_b32 v230, v231\n\t" \
    "s_waitcnt lgkmcnt(6)\n\t" "v_smfmac_f32_16x16x64_bf16 v[208:211], v[252:255], v[160:167], %[ix]\n\t" "buffer_store_dwordx4 v[228:231], %[uo], %[rs], s61 offen sc1\n\t" \
    "s_xor_b32 s61, s61, 0xc0000\n\t" "s_waitcnt lgkmcnt(4)\n\t" "v_smfmac_f32_16x16x64_bf16 v[212:215], v[252:255], v[168:175], %[ix]\n\t" "s_nop 1\n\t" \
    "v_smfmac_f32_16x16x64_bf16 v[216:219], v[240:243], v[128:135], %[ix]\n\t" "s_nop 1\n\t" "v_smfmac_f32_16x16x64_bf16 v[220:223], v[240:243], v[144:151], %[ix]\n\t" \
    "v_cvt_pk_bf16_f32 v224, v208, v210\n\t" "v_cvt_pk_bf16_f32 v225, v209, v211\n\t" "v_smfmac_f32_16x16x64_bf16 v[216:219], v[244:247], v[136:143], %[ix]\n\t" \
    "v_cvt_pk_bf16_f32 v226, v212, v214\n\t" "v_cvt_pk_bf16_f32 v227, v213, v215\n\t" "v_smfmac_f32_16x16x64_bf16 v[220:223], v[244:247], v[152:159], %[ix]\n\t" "s_nop 0\n\t" \
    "v_permlane32_swap_b32 v224, v226\n\t" "v_smfmac_f32_16x16x64_bf16 v[216:219], v[248:251], a[240:247], %[ix]\n\t" "v_permlane32_swap_b32 v225, v227\n\t" "s_nop 0\n\t" \
    "v_smfmac_f32_16x16x64_bf16 v[220:223], v[248:251], a[248:255], %[ix]\n\t" "v_permlane16_swap_b32 v224, v225\n\t" "v_permlane16_swap_b32 v226, v227\n\t" \
    "s_waitcnt lgkmcnt(2)\n\t" "v_smfmac_f32_16x16x64_bf16 v[216:219], v[252:255], v[176:183], %[ix]\n\t" "buffer_store_dwordx4 v[224:227], %[uo], %[rs], s61 offen sc1\n\t" \
    "s_xor_b32 s61, s61, 0x40000\n\t" "s_waitcnt lgkmcnt(0)\n\t" "v_smfmac_f32_16x16x64_bf16 v[220:223], v[252:255], v[184:191], %[ix]\n\t" "s_nop 5\n\t" \
    "v_cvt_pk_bf16_f32 v228, v216, v218\n\t" "v_cvt_pk_bf16_f32 v229, v217, v219\n\t" "v_cvt_pk_bf16_f32 v230, v220, v222\n\t" "v_cvt_pk_bf16_f32 v231, v221, v223\n\t" \
    "s_nop 0\n\t" "v_permlane32_swap_b32 v228, v230\n\t" "v_permlane32_swap_b32 v229, v231\n\t" "s_nop 1\n\t" "v_permlane16_swap_b32 v228, v229\n\t" \
    "v_permlane16_swap_b32 v230, v231\n\t" "buffer_store_dwordx4 v[228:231], %[uo], %[rs], s61 offen sc1\n\t" "s_nop 1\n\t" XT(96, 97) "v_mov_b64 v[16:17], -1\n\t" \
    "v_mov_b64 v[18:19], -1\n\t" "s_barrier\n\t" "buffer_store_dwordx4 v[16:19], %[l1], %[rs], %[ur] offen offset:0 sc1\n\t" \
    "buffer_store_dwordx4 v[16:19], %[l1], %[rs], %[ur] offen offset:1024 sc1\n\t" "buffer_store_dwordx4 v[16:19], %[l1], %[rs], %[ur] offen offset:2048 sc1\n\t" \
    "buffer_store_dwordx4 v[16:19], %[l1], %[rs], %[ur] offen offset:3072 sc1\n\t" "s_sub_u32 s61, %[ur], 0x1000\n\t" \
    "buffer_store_dwordx4 v[16:19], %[l1], %[rs], s61 offen offset:0 sc1\n\t" "buffer_store_dwordx4 v[16:19], %[l1], %[rs], s61 offen offset:1024 sc1\n\t" "s_branch 99f\n\t" \
    "21:\n\t" "ds_read_b128 v[228:231], %[l1] offset:4096\n\t" "s_waitcnt lgkmcnt(0)\n\t" "v_max3_u32 v225, v228, v229, v230\n\t" "v_max_u32 v225, v225, v231\n\t" \
    "ds_read_b128 v[228:231], %[l1] offset:5120\n\t" "s_waitcnt lgkmcnt(0)\n\t" "v_max3_u32 v225, v225, v228, v229\n\t" "v_max3_u32 v225, v225, v230, v231\n\t" \
    "ds_read_b128 v[228:231], %[l1] offset:6144\n\t" "s_waitcnt lgkmcnt(0)\n\t" "v_max3_u32 v225, v225, v228, v229\n\t" "v_max3_u32 v225, v225, v230, v231\n\t" \
    "ds_read_b128 v[228:231], %[l1] offset:7168\n\t" "s_waitcnt lgkmcnt(0)\n\t" "v_max3_u32 v225, v225, v228, v229\n\t" "v_max3_u32 v225, v225, v230, v231\n\t" \
    "v_cmp_eq_u32 vcc, -1, v225\n\t" "s_cbranch_vccz 22b\n\t" "s_cmp_eq_u32 %[rt], 1\n\t" "s_cbranch_scc1 22b\n\t" "v_mov_b32 v225, %[rt]\n\t" "ds_write_b32 v227, v225\n\t" \
    "s_waitcnt lgkmcnt(0)\n\t" "s_branch 22b\n\t" "23:\n\t" "s_sub_u32 %[rt], %[rt], 1\n\t" "s_waitcnt lgkmcnt(0)\n\t" XCNT(100) "24:\n\t" \
    "buffer_load_dwordx4 v[240:243], %[mo], %[rs], %[sb] offen sc1\n\t" "buffer_load_dwordx4 v[244:247], v224, %[rs], %[sb] offen sc1\n\t" \
    "buffer_load_dwordx4 v[248:251], %[mo], %[rs], s60 offen sc1\n\t" "buffer_load_dwordx4 v[252:255], v224, %[rs], s60 offen sc1\n\t" "s_waitcnt vmcnt(0)\n\t" \
    "v_max_u32 v225, v240, v241\n\t" "v_max3_u32 v225, v225, v242, v243\n\t" "v_max3_u32 v225, v225, v244, v245\n\t" "v_max3_u32 v225, v225, v246, v247\n\t" \
    "v_max3_u32 v225, v225, v248, v249\n\t" "v_max3_u32 v225, v225, v250, v251\n\t" "v_max3_u32 v225, v225, v252, v253\n\t" "v_max3_u32 v225, v225, v254, v255\n\t" \
    "v_cmp_eq_u32 vcc, -1, v225\n\t" "s_cbranch_vccz 25f\n\t" "s_cmp_eq_u32 %[bd], 0\n\t" "s_cbranch_scc1 25f\n\t" "s_sub_u32 %[bd], %[bd], 1\n\t" XCNT(101) \
    "s_branch 24b\n\t" "25:\n\t" "ds_read_b128 v[160:163], %[l0] offset:0\n\t" "ds_read_b128 v[164:167], %[l0] offset:1024\n\t" \
    "ds_read_b128 v[168:171], %[l0] offset:2048\n\t" "ds_read_b128 v[172:175], %[l0] offset:3072\n\t" "ds_read_b128 v[176:179], %[l0] offset:4096\n\t" \
    "ds_read_b128 v[180:183], %[l0] offset:5120\n\t" "ds_read_b128 v[184:187], %[l0] offset:6144\n\t" "ds_read_b128 v[188:191], %[l0] offset:7168\n\t" \
    "v_mov_b64 v[232:233], 0\n\t" "v_mov_b64 v[234:235], 0\n\t" "v_mov_b64 v[236:237], 0\n\t" "v_mov_b64 v[238:239], 0\n\t" "s_barrier\n\t" "s_branch 20b\n\t" "92:\n\t" \
    XT(80, 81) "s_barrier\n\t" XT(82, 83) "buffer_load_dwordx4 v[240:243], %[mo], %[rs], %[sb] offen sc1\n\t" "v_mov_b64 v[232:233], 0\n\t" "v_mov_b64 v[234:235], 0\n\t" \
    "v_mov_b64 v[236:237], 0\n\t" "v_mov_b64 v[238:239], 0\n\t" "v_mov_b64 v[192:193], 0\n\t" "v_mov_b64 v[194:195], 0\n\t" "v_mov_b64 v[196:197], 0\n\t" \
    "v_mov_b64 v[198:199], 0\n\t" "buffer_load_dwordx4 v[244:247], v224, %[rs], %[sb] offen sc1\n\t" "ds_read_b128 v[160:163], %[l0] offset:0\n\t" \
    "ds_read_b128 v[164:167], %[l0] offset:1024\n\t" "ds_read_b128 v[168:171], %[l0] offset:2048\n\t" "ds_read_b128 v[172:175], %[l0] offset:3072\n\t" "s_nop 3\n\t" \
    "buffer_load_dwordx4 v[248:251], %[mo], %[rs], s60 offen sc1\n\t" "ds_read_b128 v[176:179], %[l0] offset:4096\n\t" "ds_read_b128 v[180:183], %[l0] offset:5120\n\t" \
    "ds_read_b128 v[184:187], %[l0] offset:6144\n\t" "ds_read_b128 v[188:191], %[l0] offset:7168\n\t" "s_nop 3\n\t" \
    "buffer_load_dwordx4 v[252:255], v224, %[rs], s60 offen sc1\n\t" "v_mov_b64 v[200:201], 0\n\t" "v_mov_b64 v[202:203], 0\n\t" "v_mov_b64 v[204:205], 0\n\t" \
    "v_mov_b64 v[206:207], 0\n\t" "v_mov_b64 v[208:209], 0\n\t" "v_mov_b64 v[210:211], 0\n\t" "v_mov_b64 v[212:213], 0\n\t" "v_mov_b64 v[214:215], 0\n\t" \
    "v_mov_b64 v[216:217], 0\n\t" "v_mov_b64 v[218:219], 0\n\t" "v_mov_b64 v[220:221], 0\n\t" "v_mov_b64 v[222:223], 0\n\t" "v_mov_b32 v227, 160512\n\t" "30:\n\t" \
    "s_waitcnt vmcnt(3)\n\t" XT(84, 85) "v_smfmac_f32_16x16x64_bf16 v[232:235], v[240:243], a[0:7], %[ix]\n\t" "ds_write_b128 %[l1], v[240:243] offset:8192\n\t" \
    "v_smfmac_f32_16x16x64_bf16 v[236:239], v[240:243], a[8:15], %[ix]\n\t" "s_waitcnt vmcnt(2)\n\t" "v_smfmac_f32_16x16x64_bf16 v[232:235], v[244:247], a[16:23], %[ix]\n\t" \
    "ds_write_b128 %[l1], v[244:247] offset:9216\n\t" "s_waitcnt lgkmcnt(8)\n\t" "v_smfmac_f32_16x16x64_bf16 v[236:239], v[244:247], v[160:167], %[ix]\n\t" \
    "s_waitcnt lgkmcnt(0)\n\t" "s_barrier\n\t" XT(86, 87) "ds_read_b128 v[16:19], %[l1] offset:12288\n\t" "ds_read_b128 v[20:23], %[l1] offset:0\n\t" \
    "ds_read_b128 v[24:27], %[l1] offset:4096\n\t" "s_waitcnt vmcnt(1)\n\t" "v_smfmac_f32_16x16x64_bf16 v[232:235], v[248:251], a[24:31], %[ix]\n\t" \
    "ds_write_b128 %[l1], v[248:251] offset:10240\n\t" "v_smfmac_f32_16x16x64_bf16 v[236:239], v[248:251], a[32:39], %[ix]\n\t" \
    "ds_read_b128 v[28:31], %[l1] offset:13312\n\t" "ds_read_b128 v[240:243], %[l1] offset:1024\n\t" "ds_read_b128 v[244:247], %[l1] offset:5120\n\t" "s_waitcnt vmcnt(0)\n\t" \
    "v_smfmac_f32_16x16x64_bf16 v[232:235], v[252:255], a[40:47], %[ix]\n\t" "ds_write_b128 %[l1], v[252:255] offset:11264\n\t" \
    "v_smfmac_f32_16x16x64_bf16 v[236:239], v[252:255], v[168:175], %[ix]\n\t" "ds_read_b128 v[160:163], %[l0] offset:8192\n\t" \
    "ds_read_b128 v[164:167], %[l0] offset:9216\n\t" "ds_read_b128 v[168:171], %[l0] offset:10240\n\t" "ds_read_b128 v[172:175], %[l0] offset:11264\n\t" "s_nop 1\n\t" \
    "v_cmp_u_f32 vcc, v232, v233\n\t" "v_cmp_u_f32 s[62:63], v234, v235\n\t" "s_waitcnt lgkmcnt(11)\n\t" \
    "v_smfmac_f32_16x16x64_bf16 v[232:235], v[16:19], a[48:55], %[ix]\n\t" "v_smfmac_f32_16x16x64_bf16 v[236:239], v[16:19], a[56:63], %[ix]\n\t" \
    "s_or_b64 vcc, vcc, s[62:63]\n\t" "s_cbranch_scc1 31f\n\t" "32:\n\t" "s_waitcnt lgkmcnt(10)\n\t" "v_smfmac_f32_16x16x64_bf16 v[232:235], v[20:23], a[64:71], %[ix]\n\t" \
    "v_smfmac_f32_16x16x64_bf16 v[236:239], v[20:23], v[176:183], %[ix]\n\t" "s_waitcnt lgkmcnt(4)\n\t" "s_barrier\n\t" XT(88, 89) "ds_read_b32 v225, v227\n\t" \
    "v_smfmac_f32_16x16x64_bf16 v[232:235], v[24:27], a[72:79], %[ix]\n\t" "ds_read_b128 v[176:179], %[l0] offset:12288\n\t" "ds_read_b128 v[180:183], %[l0] offset:13312\n\t" \
    "v_smfmac_f32_16x16x64_bf16 v[236:239], v[24:27], a[80:87], %[ix]\n\t" "ds_read_b128 v[248:251], %[l1] offset:14336\n\t" "ds_read_b128 v[252:255], %[l1] offset:2048\n\t" \
    "v_smfmac_f32_16x16x64_bf16 v[232:235], v[28:31], a[88:95], %[ix]\n\t" "ds_read_b128 v[16:19], %[l1] offset:6144\n\t" "ds_read_b128 v[20:23], %[l1] offset:15360\n\t" \
    "v_smfmac_f32_16x16x64_bf16 v[236:239], v[28:31], v[184:191], %[ix]\n\t" "ds_read_b128 v[24:27], %[l1] offset:3072\n\t" "ds_read_b128 v[184:187], %[l0] offset:14336\n\t" \
    "v_smfmac_f32_16x16x64_bf16 v[232:235], v[240:243], a[96:103], %[ix]\n\t" "ds_read_b128 v[188:191], %[l0] offset:15360\n\t" "ds_read_b128 v[28:31], %[l1] offset:7168\n\t" \
    "v_smfmac_f32_16x16x64_bf16 v[236:239], v[240:243], a[104:111], %[ix]\n\t" "s_waitcnt lgkmcnt(10)\n\t" "v_cmp_eq_u32 vcc, %[rt], v225\n\t" \
    "v_smfmac_f32_16x16x64_bf16 v[232:235], v[244:247], a[112:119], %[ix]\n\t" "s_cbranch_vccnz 33f\n\t" \
    "v_smfmac_f32_16x16x64_bf16 v[236:239], v[244:247], v[160:167], %[ix]\n\t" "ds_read_b128 v[160:163], %[l0] offset:16384\n\t" \
    "ds_read_b128 v[164:167], %[l0] offset:17408\n\t" "s_waitcnt lgkmcnt(9)\n\t" "v_smfmac_f32_16x16x64_bf16 v[232:235], v[248:251], a[120:127], %[ix]\n\t" \
    "v_smfmac_f32_16x16x64_bf16 v[236:239], v[248:251], a[128:135], %[ix]\n\t" "s_waitcnt lgkmcnt(8)\n\t" \
    "v_smfmac_f32_16x16x64_bf16 v[232:235], v[252:255], a[136:143], %[ix]\n\t" "v_smfmac_f32_16x16x64_bf16 v[236:239], v[252:255], v[168:175], %[ix]\n\t" \
    "ds_read_b128 v[168:171], %[l0] offset:18432\n\t" "ds_read_b128 v[172:175], %[l0] offset:19456\n\t" "s_waitcnt lgkmcnt(9)\n\t" \
    "v_smfmac_f32_16x16x64_bf16 v[232:235], v[16:19], a[144:151], %[ix]\n\t" "v_smfmac_f32_16x16x64_bf16 v[236:239], v[16:19], a[152:159], %[ix]\n\t" \
    "s_waitcnt lgkmcnt(8)\n\t" "v_smfmac_f32_16x16x64_bf16 v[232:235], v[20:23], a[160:167], %[ix]\n\t" \
    "v_smfmac_f32_16x16x64_bf16 v[236:239], v[20:23], v[176:183], %[ix]\n\t" "ds_read_b128 v[176:179], %[l0] offset:20480\n\t" \
    "ds_read_b128 v[180:183], %[l0] offset:21504\n\t" "s_waitcnt lgkmcnt(9)\n\t" "v_smfmac_f32_16x16x64_bf16 v[232:235], v[24:27], a[168:175], %[ix]\n\t" \
    "v_smfmac_f32_16x16x64_bf16 v[236:239], v[24:27], a[176:183], %[ix]\n\t" "s_waitcnt lgkmcnt(6)\n\t" \
    "v_smfmac_f32_16x16x64_bf16 v[232:235], v[28:31], a[184:191], %[ix]\n\t" "v_smfmac_f32_16x16x64_bf16 v[236:239], v[28:31], v[184:191], %[ix]\n\t" \
    "ds_read_b128 v[184:187], %[l0] offset:22528\n\t" "ds_read_b128 v[188:191], %[l0] offset:23552\n\t" XT(90, 91) "s_nop 4\n\t" "v_add_f32 v232, v232, v233\n\t" \
    "v_add_f32 v234, v234, v235\n\t" "v_add_f32 v236, v236, v237\n\t" "v_add_f32 v238, v238, v239\n\t" "v_cvt_pk_bf16_f32 v236, v232, v236\n\t" \
    "v_cvt_pk_bf16_f32 v237, v234, v238\n\t" "ds_write2_b32 %[vw], v236, v237 offset1:68\n\t" "s_waitcnt lgkmcnt(0)\n\t" "s_barrier\n\t" XT(92, 93) \
    "ds_read_b128 v[240:243], %[vr] offset:0\n\t" "ds_read_b128 v[244:247], %[vr] offset:64\n\t" "ds_read_b128 v[248:251], %[vr] offset:128\n\t" \
    "ds_read_b128 v[252:255], %[vr] offset:192\n\t" "s_mov_b32 s61, %[ub]\n\t" "s_waitcnt lgkmcnt(3)\n\t" \
    "v_smfmac_f32_16x16x64_bf16 v[192:195], v[240:243], v[32:39], %[ix]\n\t" "v_smfmac_f32_16x16x64_bf16 v[196:199], v[240:243], v[48:55], %[ix]\n\t" \
    "s_waitcnt lgkmcnt(2)\n\t" "v_smfmac_f32_16x16x64_bf16 v[192:195], v[244:247], v[40:47], %[ix]\n\t" \
    "v_smfmac_f32_16x16x64_bf16 v[196:199], v[244:247], v[56:63], %[ix]\n\t" "s_waitcnt lgkmcnt(1)\n\t" \
    "v_smfmac_f32_16x16x64_bf16 v[192:195], v[248:251], a[192:199], %[ix]\n\t" "v_smfmac_f32_16x16x64_bf16 v[196:199], v[248:251], a[200:207], %[ix]\n\t" \
    "s_waitcnt lgkmcnt(0)\n\t" "v_smfmac_f32_16x16x64_bf16 v[192:195], v[252:255], v[160:167], %[ix]\n\t" "ds_read_b128 v[160:163], %[l0] offset:24576\n\t" \
    "ds_read_b128 v[164:167], %[l0] offset:25600\n\t" "v_smfmac_f32_16x16x64_bf16 v[196:199], v[252:255], v[168:175], %[ix]\n\t" \
    "ds_read_b128 v[168:171], %[l0] offset:26624\n\t" "ds_read_b128 v[172:175], %[l0] offset:27648\n\t" \
    "v_smfmac_f32_16x16x64_bf16 v[200:203], v[240:243], v[64:71], %[ix]\n\t" "s_nop 1\n\t" "v_smfmac_f32_16x16x64_bf16 v[204:207], v[240:243], v[80:87], %[ix]\n\t" \
    "v_cvt_pk_bf16_f32 v224, v192, v194\n\t" "v_cvt_pk_bf16_f32 v225, v193, v195\n\t" "v_smfmac_f32_16x16x64_bf16 v[200:203], v[244:247], v[72:79], %[ix]\n\t" \
    "v_cvt_pk_bf16_f32 v226, v196, v198\n\t" "v_cvt_pk_bf16_f32 v227, v197, v199\n\t" "v_smfmac_f32_16x16x64_bf16 v[204:207], v[244:247], v[88:95], %[ix]\n\t" "s_nop 0\n\t" \
    "v_permlane32_swap_b32 v224, v226\n\t" "v_smfmac_f32_16x16x64_bf16 v[200:203], v[248:251], a[208:215], %[ix]\n\t" "v_permlane32_swap_b32 v225, v227\n\t" "s_nop 0\n\t" \
    "v_smfmac_f32_16x16x64_bf16 v[204:207], v[248:251], a[216:223], %[ix]\n\t" "v_permlane16_swap_b32 v224, v225\n\t" "v_permlane16_swap_b32 v226, v227\n\t" \
    "v_smfmac_f32_16x16x64_bf16 v[200:203], v[252:255], v[176:183], %[ix]\n\t" "ds_read_b128 v[176:179], %[l0] offset:28672\n\t" \
    "buffer_store_dwordx4 v[224:227], %[uo], %[rs], s61 offen sc1\n\t" "s_xor_b32 s61, s61, 0x40000\n\t" XT(94, 95) \
    "v_smfmac_f32_16x16x64_bf16 v[204:207], v[252:255], v[184:191], %[ix]\n\t" "ds_read_b128 v[180:183], %[l0] offset:29696\n\t" \
    "ds_read_b128 v[184:187], %[l0] offset:30720\n\t" "v_smfmac_f32_16x16x64_bf16 v[208:211], v[240:243], v[96:103], %[ix]\n\t" \
    "ds_read_b128 v[188:191], %[l0] offset:31744\n\t" "s_nop 0\n\t" "v_smfmac_f32_16x16x64_bf16 v[212:215], v[240:243], v[112:119], %[ix]\n\t" \
    "v_cvt_pk_bf16_f32 v228, v200, v202\n\t" "v_cvt_pk_bf16_f32 v229, v201, v203\n\t" "v_smfmac_f32_16x16x64_bf16 v[208:211], v[244:247], v[104:111], %[ix]\n\t" \
    "v_cvt_pk_bf16_f32 v230, v204, v206\n\t" "v_cvt_pk_bf16_f32 v231, v205, v207\n\t" "v_smfmac_f32_16x16x64_bf16 v[212:215], v[244:247], v[120:127], %[ix]\n\t" "s_nop 0\n\t" \
    "v_permlane32_swap_b32 v228, v230\n\t" "v_smfmac_f32_16x16x64_bf16 v[208:211], v[248:251], a[224:231], %[ix]\n\t" "v_permlane32_swap_b32 v229, v231\n\t" "s_nop 0\n\t" \
    "v_smfmac_f32_16x16x64_bf16 v[212:215], v[248:251], a[232:239], %[ix]\n\t" "v_permlane16_swap_b32 v228, v229\n\t" "v_permlane16_swap_b32 v230, v231\n\t" \
    "s_waitcnt lgkmcnt(6)\n\t" "v_smfmac_f32_16x16x64_bf16 v[208:211], v[252:255], v[160:167], %[ix]\n\t" "buffer_store_dwordx4 v[228:231], %[uo], %[rs], s61 offen sc1\n\t" \
    "s_xor_b32 s61, s61, 0xc0000\n\t" "s_waitcnt lgkmcnt(4)\n\t" "v_smfmac_f32_16x16x64_bf16 v[212:215], v[252:255], v[168:175], %[ix]\n\t" "s_nop 1\n\t" \
    "v_smfmac_f32_16x16x64_bf16 v[216:219], v[240:243], v[128:135], %[ix]\n\t" "s_nop 1\n\t" "v_smfmac_f32_16x16x64_bf16 v[220:223], v[240:243], v[144:151], %[ix]\n\t" \
    "v_cvt_pk_bf16_f32 v224, v208, v210\n\t" "v_cvt_pk_bf16_f32 v225, v209, v211\n\t" "v_smfmac_f32_16x16x64_bf16 v[216:219], v[244:247], v[136:143], %[ix]\n\t" \
    "v_cvt_pk_bf16_f32 v226, v212, v214\n\t" "v_cvt_pk_bf16_f32 v227, v213, v215\n\t" "v_smfmac_f32_16x16x64_bf16 v[220:223], v[244:247], v[152:159], %[ix]\n\t" "s_nop 0\n\t" \
    "v_permlane32_swap_b32 v224, v226\n\t" "v_smfmac_f32_16x16x64_bf16 v[216:219], v[248:251], a[240:247], %[ix]\n\t" "v_permlane32_swap_b32 v225, v227\n\t" "s_nop 0\n\t" \
    "v_smfmac_f32_16x16x64_bf16 v[220:223], v[248:251], a[248:255], %[ix]\n\t" "v_permlane16_swap_b32 v224, v225\n\t" "v_permlane16_swap_b32 v226, v227\n\t" \
    "s_waitcnt lgkmcnt(2)\n\t" "v_smfmac_f32_16x16x64_bf16 v[216:219], v[252:255], v[176:183], %[ix]\n\t" "buffer_store_dwordx4 v[224:227], %[uo], %[rs], s61 offen sc1\n\t" \
    "s_xor_b32 s61, s61, 0x40000\n\t" "s_waitcnt lgkmcnt(0)\n\t" "v_smfmac_f32_16x16x64_bf16 v[220:223], v[252:255], v[184:191], %[ix]\n\t" "s_nop 5\n\t" \
    "v_cvt_pk_bf16_f32 v228, v216, v218\n\t" "v_cvt_pk_bf16_f32 v229, v217, v219\n\t" "v_cvt_pk_bf16_f32 v230, v220, v222\n\t" "v_cvt_pk_bf16_f32 v231, v221, v223\n\t" \
    "s_nop 0\n\t" "v_permlane32_swap_b32 v228, v230\n\t" "v_permlane32_swap_b32 v229, v231\n\t" "s_nop 1\n\t" "v_permlane16_swap_b32 v228, v229\n\t" \
    "v_permlane16_swap_b32 v230, v231\n\t" "buffer_store_dwordx4 v[228:231], %[uo], %[rs], s61 offen sc1\n\t" "s_nop 1\n\t" XT(96, 97) "v_mov_b64 v[16:17], -1\n\t" \
    "v_mov_b64 v[18:19], -1\n\t" "s_barrier\n\t" "buffer_store_dwordx4 v[16:19], %[l1], %[rs], %[ur] offen offset:0 sc1\n\t" \
    "buffer_store_dwordx4 v[16:19], %[l1], %[rs], %[ur] offen offset:1024 sc1\n\t" "buffer_store_dwordx4 v[16:19], %[l1], %[rs], %[ur] offen offset:2048 sc1\n\t" \
    "buffer_store_dwordx4 v[16:19], %[l1], %[rs], %[ur] offen offset:3072 sc1\n\t" "s_sub_u32 s61, %[ur], 0x4000\n\t" \
    "buffer_store_dwordx4 v[16:19], %[l1], %[rs], s61 offen offset:2048 sc1\n\t" "s_branch 99f\n\t" "31:\n\t" "ds_read_b128 v[228:231], %[l1] offset:8192\n\t" \
    "s_waitcnt lgkmcnt(0)\n\t" "v_max3_u32 v225, v228, v229, v230\n\t" "v_max_u32 v225, v225, v231\n\t" "ds_read_b128 v[228:231], %[l1] offset:9216\n\t" \
    "s_waitcnt lgkmcnt(0)\n\t" "v_max3_u32 v225, v225, v228, v229\n\t" "v_max3_u32 v225, v225, v230, v231\n\t" "ds_read_b128 v[228:231], %[l1] offset:10240\n\t" \
    "s_waitcnt lgkmcnt(0)\n\t" "v_max3_u32 v225, v225, v228, v229\n\t" "v_max3_u32 v225, v225, v230, v231\n\t" "ds_read_b128 v[228:231], %[l1] offset:11264\n\t" \
    "s_waitcnt lgkmcnt(0)\n\t" "v_max3_u32 v225, v225, v228, v229\n\t" "v_max3_u32 v225, v225, v230, v231\n\t" "v_cmp_eq_u32 vcc, -1, v225\n\t" "s_cbranch_vccz 32b\n\t" \
    "s_cmp_eq_u32 %[rt], 1\n\t" "s_cbranch_scc1 32b\n\t" "v_mov_b32 v225, %[rt]\n\t" "ds_write_b32 v227, v225\n\t" "s_waitcnt lgkmcnt(0)\n\t" "s_branch 32b\n\t" "33:\n\t" \
    "s_sub_u32 %[rt], %[rt], 1\n\t" "s_waitcnt lgkmcnt(0)\n\t" XCNT(100) "34:\n\t" "buffer_load_dwordx4 v[240:243], %[mo], %[rs], %[sb] offen sc1\n\t" \
    "buffer_load_dwordx4 v[244:247], v224, %[rs], %[sb] offen sc1\n\t" "buffer_load_dwordx4 v[248:251], %[mo], %[rs], s60 offen sc1\n\t" \
    "buffer_load_dwordx4 v[252:255], v224, %[rs], s60 offen sc1\n\t" "s_waitcnt vmcnt(0)\n\t" "v_max_u32 v225, v240, v241\n\t" "v_max3_u32 v225, v225, v242, v243\n\t" \
    "v_max3_u32 v225, v225, v244, v245\n\t" "v_max3_u32 v225, v225, v246, v247\n\t" "v_max3_u32 v225, v225, v248, v249\n\t" "v_max3_u32 v225, v225, v250, v251\n\t" \
    "v_max3_u32 v225, v225, v252, v253\n\t" "v_max3_u32 v225, v225, v254, v255\n\t" "v_cmp_eq_u32 vcc, -1, v225\n\t" "s_cbranch_vccz 35f\n\t" "s_cmp_eq_u32 %[bd], 0\n\t" \
    "s_cbranch_scc1 35f\n\t" "s_sub_u32 %[bd], %[bd], 1\n\t" XCNT(101) "s_branch 34b\n\t" "35:\n\t" "ds_read_b128 v[160:163], %[l0] offset:0\n\t" \
    "ds_read_b128 v[164:167], %[l0] offset:1024\n\t" "ds_read_b128 v[168:171], %[l0] offset:2048\n\t" "ds_read_b128 v[172:175], %[l0] offset:3072\n\t" \
    "ds_read_b128 v[176:179], %[l0] offset:4096\n\t" "ds_read_b128 v[180:183], %[l0] offset:5120\n\t" "ds_read_b128 v[184:187], %[l0] offset:6144\n\t" \
    "ds_read_b128 v[188:191], %[l0] offset:7168\n\t" "v_mov_b64 v[232:233], 0\n\t" "v_mov_b64 v[234:235], 0\n\t" "v_mov_b64 v[236:237], 0\n\t" "v_mov_b64 v[238:239], 0\n\t" \
    "s_barrier\n\t" "s_branch 30b\n\t" "93:\n\t" XT(80, 81) "s_barrier\n\t" XT(82, 83) "buffer_load_dwordx4 v[240:243], %[mo], %[rs], %[sb] offen sc1\n\t" \
    "v_mov_b64 v[232:233], 0\n\t" "v_mov_b64 v[234:235], 0\n\t" "v_mov_b64 v[236:237], 0\n\t" "v_mov_b64 v[238:239], 0\n\t" "v_mov_b64 v[192:193], 0\n\t" \
    "v_mov_b64 v[194:195], 0\n\t" "v_mov_b64 v[196:197], 0\n\t" "v_mov_b64 v[198:199], 0\n\t" "buffer_load_dwordx4 v[244:247], v224, %[rs], %[sb] offen sc1\n\t" \
    "ds_read_b128 v[160:163], %[l0] offset:0\n\t" "ds_read_b128 v[164:167], %[l0] offset:1024\n\t" "ds_read_b128 v[168:171], %[l0] offset:2048\n\t" \
    "ds_read_b128 v[172:175], %[l0] offset:3072\n\t" "s_nop 3\n\t" "buffer_load_dwordx4 v[248:251], %[mo], %[rs], s60 offen sc1\n\t" \
    "ds_read_b128 v[176:179], %[l0] offset:4096\n\t" "ds_read_b128 v[180:183], %[l0] offset:5120\n\t" "ds_read_b128 v[184:187], %[l0] offset:6144\n\t" \
    "ds_read_b128 v[188:191], %[l0] offset:7168\n\t" "s_nop 3\n\t" "buffer_load_dwordx4 v[252:255], v224, %[rs], s60 offen sc1\n\t" "v_mov_b64 v[200:201], 0\n\t" \
    "v_mov_b64 v[202:203], 0\n\t" "v_mov_b64 v[204:205], 0\n\t" "v_mov_b64 v[206:207], 0\n\t" "v_mov_b64 v[208:209], 0\n\t" "v_mov_b64 v[210:211], 0\n\t" \
    "v_mov_b64 v[212:213], 0\n\t" "v_mov_b64 v[214:215], 0\n\t" "v_mov_b64 v[216:217], 0\n\t" "v_mov_b64 v[218:219], 0\n\t" "v_mov_b64 v[220:221], 0\n\t" \
    "v_mov_b64 v[222:223], 0\n\t" "v_mov_b32 v227, 160512\n\t" "40:\n\t" "s_waitcnt vmcnt(3)\n\t" XT(84, 85) \
    "v_smfmac_f32_16x16x64_bf16 v[232:235], v[240:243], a[0:7], %[ix]\n\t" "ds_write_b128 %[l1], v[240:243] offset:12288\n\t" \
    "v_smfmac_f32_16x16x64_bf16 v[236:239], v[240:243], a[8:15], %[ix]\n\t" "s_waitcnt vmcnt(2)\n\t" "v_smfmac_f32_16x16x64_bf16 v[232:235], v[244:247], a[16:23], %[ix]\n\t" \
    "ds_write_b128 %[l1], v[244:247] offset:13312\n\t" "s_waitcnt lgkmcnt(8)\n\t" "v_smfmac_f32_16x16x64_bf16 v[236:239], v[244:247], v[160:167], %[ix]\n\t" \
    "s_waitcnt lgkmcnt(0)\n\t" "s_barrier\n\t" XT(86, 87) "ds_read_b128 v[16:19], %[l1] offset:0\n\t" "ds_read_b128 v[20:23], %[l1] offset:4096\n\t" \
    "ds_read_b128 v[24:27], %[l1] offset:8192\n\t" "s_waitcnt vmcnt(1)\n\t" "v_smfmac_f32_16x16x64_bf16 v[232:235], v[248:251], a[24:31], %[ix]\n\t" \
    "ds_write_b128 %[l1], v[248:251] offset:14336\n\t" "v_smfmac_f32_16x16x64_bf16 v[236:239], v[248:251], a[32:39], %[ix]\n\t" "ds_read_b128 v[28:31], %[l1] offset:1024\n\t" \
    "ds_read_b128 v[240:243], %[l1] offset:5120\n\t" "ds_read_b128 v[244:247], %[l1] offset:9216\n\t" "s_waitcnt vmcnt(0)\n\t" \
    "v_smfmac_f32_16x16x64_bf16 v[232:235], v[252:255], a[40:47], %[ix]\n\t" "ds_write_b128 %[l1], v[252:255] offset:15360\n\t" \
    "v_smfmac_f32_16x16x64_bf16 v[236:239], v[252:255], v[168:175], %[ix]\n\t" "ds_read_b128 v[160:163], %[l0] offset:8192\n\t" \
    "ds_read_b128 v[164:167], %[l0] offset:9216\n\t" "ds_read_b128 v[168:171], %[l0] offset:10240\n\t" "ds_read_b128 v[172:175], %[l0] offset:11264\n\t" "s_nop 1\n\t" \
    "v_cmp_u_f32 vcc, v232, v233\n\t" "v_cmp_u_f32 s[62:63], v234, v235\n\t" "s_waitcnt lgkmcnt(11)\n\t" \
    "v_smfmac_f32_16x16x64_bf16 v[232:235], v[16:19], a[48:55], %[ix]\n\t" "v_smfmac_f32_16x16x64_bf16 v[236:239], v[16:19], a[56:63], %[ix]\n\t" \
    "s_or_b64 vcc, vcc, s[62:63]\n\t" "s_cbranch_scc1 41f\n\t" "42:\n\t" "s_waitcnt lgkmcnt(10)\n\t" "v_smfmac_f32_16x16x64_bf16 v[232:235], v[20:23], a[64:71], %[ix]\n\t" \
    "v_smfmac_f32_16x16x64_bf16 v[236:239], v[20:23], v[176:183], %[ix]\n\t" "s_waitcnt lgkmcnt(4)\n\t" "s_barrier\n\t" XT(88, 89) "ds_read_b32 v225, v227\n\t" \
    "v_smfmac_f32_16x16x64_bf16 v[232:235], v[24:27], a[72:79], %[ix]\n\t" "ds_read_b128 v[176:179], %[l0] offset:12288\n\t" "ds_read_b128 v[180:183], %[l0] offset:13312\n\t" \
    "v_smfmac_f32_16x16x64_bf16 v[236:239], v[24:27], a[80:87], %[ix]\n\t" "ds_read_b128 v[248:251], %[l1] offset:2048\n\t" "ds_read_b128 v[252:255], %[l1] offset:6144\n\t" \
    "v_smfmac_f32_16x16x64_bf16 v[232:235], v[28:31], a[88:95], %[ix]\n\t" "ds_read_b128 v[16:19], %[l1] offset:10240\n\t" "ds_read_b128 v[20:23], %[l1] offset:3072\n\t" \
    "v_smfmac_f32_16x16x64_bf16 v[236:239], v[28:31], v[184:191], %[ix]\n\t" "ds_read_b128 v[24:27], %[l1] offset:7168\n\t" "ds_read_b128 v[184:187], %[l0] offset:14336\n\t" \
    "v_smfmac_f32_16x16x64_bf16 v[232:235], v[240:243], a[96:103], %[ix]\n\t" "ds_read_b128 v[188:191], %[l0] offset:15360\n\t" \
    "ds_read_b128 v[28:31], %[l1] offset:11264\n\t" "v_smfmac_f32_16x16x64_bf16 v[236:239], v[240:243], a[104:111], %[ix]\n\t" "s_waitcnt lgkmcnt(10)\n\t" \
    "v_cmp_eq_u32 vcc, %[rt], v225\n\t" "v_smfmac_f32_16x16x64_bf16 v[232:235], v[244:247], a[112:119], %[ix]\n\t" "s_cbranch_vccnz 43f\n\t" \
    "v_smfmac_f32_16x16x64_bf16 v[236:239], v[244:247], v[160:167], %[ix]\n\t" "ds_read_b128 v[160:163], %[l0] offset:16384\n\t" \
    "ds_read_b128 v[164:167], %[l0] offset:17408\n\t" "s_waitcnt lgkmcnt(9)\n\t" "v_smfmac_f32_16x16x64_bf16 v[232:235], v[248:251], a[120:127], %[ix]\n\t" \
    "v_smfmac_f32_16x16x64_bf16 v[236:239], v[248:251], a[128:135], %[ix]\n\t" "s_waitcnt lgkmcnt(8)\n\t" \
    "v_smfmac_f32_16x16x64_bf16 v[232:235], v[252:255], a[136:143], %[ix]\n\t" "v_smfmac_f32_16x16x64_bf16 v[236:239], v[252:255], v[168:175], %[ix]\n\t" \
    "ds_read_b128 v[168:171], %[l0] offset:18432\n\t" "ds_read_b128 v[172:175], %[l0] offset:19456\n\t" "s_waitcnt lgkmcnt(9)\n\t" \
    "v_smfmac_f32_16x16x64_bf16 v[232:235], v[16:19], a[144:151], %[ix]\n\t" "v_smfmac_f32_16x16x64_bf16 v[236:239], v[16:19], a[152:159], %[ix]\n\t" \
    "s_waitcnt lgkmcnt(8)\n\t" "v_smfmac_f32_16x16x64_bf16 v[232:235], v[20:23], a[160:167], %[ix]\n\t" \
    "v_smfmac_f32_16x16x64_bf16 v[236:239], v[20:23], v[176:183], %[ix]\n\t" "ds_read_b128 v[176:179], %[l0] offset:20480\n\t" \
    "ds_read_b128 v[180:183], %[l0] offset:21504\n\t" "s_waitcnt lgkmcnt(9)\n\t" "v_smfmac_f32_16x16x64_bf16 v[232:235], v[24:27], a[168:175], %[ix]\n\t" \
    "v_smfmac_f32_16x16x64_bf16 v[236:239], v[24:27], a[176:183], %[ix]\n\t" "s_waitcnt lgkmcnt(6)\n\t" \
    "v_smfmac_f32_16x16x64_bf16 v[232:235], v[28:31], a[184:191], %[ix]\n\t" "v_smfmac_f32_16x16x64_bf16 v[236:239], v[28:31], v[184:191], %[ix]\n\t" \
    "ds_read_b128 v[184:187], %[l0] offset:22528\n\t" "ds_read_b128 v[188:191], %[l0] offset:23552\n\t" XT(90, 91) "s_nop 4\n\t" "v_add_f32 v232, v232, v233\n\t" \
    "v_add_f32 v234, v234, v235\n\t" "v_add_f32 v236, v236, v237\n\t" "v_add_f32 v238, v238, v239\n\t" "v_cvt_pk_bf16_f32 v236, v232, v236\n\t" \
    "v_cvt_pk_bf16_f32 v237, v234, v238\n\t" "ds_write2_b32 %[vw], v236, v237 offset1:68\n\t" "s_waitcnt lgkmcnt(0)\n\t" "s_barrier\n\t" XT(92, 93) \
    "ds_read_b128 v[240:243], %[vr] offset:0\n\t" "ds_read_b128 v[244:247], %[vr] offset:64\n\t" "ds_read_b128 v[248:251], %[vr] offset:128\n\t" \
    "ds_read_b128 v[252:255], %[vr] offset:192\n\t" "s_mov_b32 s61, %[ub]\n\t" "s_waitcnt lgkmcnt(3)\n\t" \
    "v_smfmac_f32_16x16x64_bf16 v[192:195], v[240:243], v[32:39], %[ix]\n\t" "v_smfmac_f32_16x16x64_bf16 v[196:199], v[240:243], v[48:55], %[ix]\n\t" \
    "s_waitcnt lgkmcnt(2)\n\t" "v_smfmac_f32_16x16x64_bf16 v[192:195], v[244:247], v[40:47], %[ix]\n\t" \
    "v_smfmac_f32_16x16x64_bf16 v[196:199], v[244:247], v[56:63], %[ix]\n\t" "s_waitcnt lgkmcnt(1)\n\t" \
    "v_smfmac_f32_16x16x64_bf16 v[192:195], v[248:251], a[192:199], %[ix]\n\t" "v_smfmac_f32_16x16x64_bf16 v[196:199], v[248:251], a[200:207], %[ix]\n\t" \
    "s_waitcnt lgkmcnt(0)\n\t" "v_smfmac_f32_16x16x64_bf16 v[192:195], v[252:255], v[160:167], %[ix]\n\t" "ds_read_b128 v[160:163], %[l0] offset:24576\n\t" \
    "ds_read_b128 v[164:167], %[l0] offset:25600\n\t" "v_smfmac_f32_16x16x64_bf16 v[196:199], v[252:255], v[168:175], %[ix]\n\t" \
    "ds_read_b128 v[168:171], %[l0] offset:26624\n\t" "ds_read_b128 v[172:175], %[l0] offset:27648\n\t" \
    "v_smfmac_f32_16x16x64_bf16 v[200:203], v[240:243], v[64:71], %[ix]\n\t" "s_nop 1\n\t" "v_smfmac_f32_16x16x64_bf16 v[204:207], v[240:243], v[80:87], %[ix]\n\t" \
    "v_cvt_pk_bf16_f32 v224, v192, v194\n\t" "v_cvt_pk_bf16_f32 v225, v193, v195\n\t" "v_smfmac_f32_16x16x64_bf16 v[200:203], v[244:247], v[72:79], %[ix]\n\t" \
    "v_cvt_pk_bf16_f32 v226, v196, v198\n\t" "v_cvt_pk_bf16_f32 v227, v197, v199\n\t" "v_smfmac_f32_16x16x64_bf16 v[204:207], v[244:247], v[88:95], %[ix]\n\t" "s_nop 0\n\t" \
    "v_permlane32_swap_b32 v224, v226\n\t" "v_smfmac_f32_16x16x64_bf16 v[200:203], v[248:251], a[208:215], %[ix]\n\t" "v_permlane32_swap_b32 v225, v227\n\t" "s_nop 0\n\t" \
    "v_smfmac_f32_16x16x64_bf16 v[204:207], v[248:251], a[216:223], %[ix]\n\t" "v_permlane16_swap_b32 v224, v225\n\t" "v_permlane16_swap_b32 v226, v227\n\t" \
    "v_smfmac_f32_16x16x64_bf16 v[200:203], v[252:255], v[176:183], %[ix]\n\t" "ds_read_b128 v[176:179], %[l0] offset:28672\n\t" \
    "buffer_store_dwordx4 v[224:227], %[uo], %[rs], s61 offen sc1\n\t" "s_xor_b32 s61, s61, 0x40000\n\t" XT(94, 95) \
    "v_smfmac_f32_16x16x64_bf16 v[204:207], v[252:255], v[184:191], %[ix]\n\t" "ds_read_b128 v[180:183], %[l0] offset:29696\n\t" \
    "ds_read_b128 v[184:187], %[l0] offset:30720\n\t" "v_smfmac_f32_16x16x64_bf16 v[208:211], v[240:243], v[96:103], %[ix]\n\t" \
    "ds_read_b128 v[188:191], %[l0] offset:31744\n\t" "s_nop 0\n\t" "v_smfmac_f32_16x16x64_bf16 v[212:215], v[240:243], v[112:119], %[ix]\n\t" \
    "v_cvt_pk_bf16_f32 v228, v200, v202\n\t" "v_cvt_pk_bf16_f32 v229, v201, v203\n\t" "v_smfmac_f32_16x16x64_bf16 v[208:211], v[244:247], v[104:111], %[ix]\n\t" \
    "v_cvt_pk_bf16_f32 v230, v204, v206\n\t" "v_cvt_pk_bf16_f32 v231, v205, v207\n\t" "v_smfmac_f32_16x16x64_bf16 v[212:215], v[244:247], v[120:127], %[ix]\n\t" "s_nop 0\n\t" \
    "v_permlane32_swap_b32 v228, v230\n\t" "v_smfmac_f32_16x16x64_bf16 v[208:211], v[248:251], a[224:231], %[ix]\n\t" "v_permlane32_swap_b32 v229, v231\n\t" "s_nop 0\n\t" \
    "v_smfmac_f32_16x16x64_bf16 v[212:215], v[248:251], a[232:239], %[ix]\n\t" "v_permlane16_swap_b32 v228, v229\n\t" "v_permlane16_swap_b32 v230, v231\n\t" \
    "s_waitcnt lgkmcnt(6)\n\t" "v_smfmac_f32_16x16x64_bf16 v[208:211], v[252:255], v[160:167], %[ix]\n\t" "buffer_store_dwordx4 v[228:231], %[uo], %[rs], s61 offen sc1\n\t" \
    "s_xor_b32 s61, s61, 0xc0000\n\t" "s_waitcnt lgkmcnt(4)\n\t" "v_smfmac_f32_16x16x64_bf16 v[212:215], v[252:255], v[168:175], %[ix]\n\t" "s_nop 1\n\t" \
    "v_smfmac_f32_16x16x64_bf16 v[216:219], v[240:243], v[128:135], %[ix]\n\t" "s_nop 1\n\t" "v_smfmac_f32_16x16x64_bf16 v[220:223], v[240:243], v[144:151], %[ix]\n\t" \
    "v_cvt_pk_bf16_f32 v224, v208, v210\n\t" "v_cvt_pk_bf16_f32 v225, v209, v211\n\t" "v_smfmac_f32_16x16x64_bf16 v[216:219], v[244:247], v[136:143], %[ix]\n\t" \
    "v_cvt_pk_bf16_f32 v226, v212, v214\n\t" "v_cvt_pk_bf16_f32 v227, v213, v215\n\t" "v_smfmac_f32_16x16x64_bf16 v[220:223], v[244:247], v[152:159], %[ix]\n\t" "s_nop 0\n\t" \
    "v_permlane32_swap_b32 v224, v226\n\t" "v_smfmac_f32_16x16x64_bf16 v[216:219], v[248:251], a[240:247], %[ix]\n\t" "v_permlane32_swap_b32 v225, v227\n\t" "s_nop 0\n\t" \
    "v_smfmac_f32_16x16x64_bf16 v[220:223], v[248:251], a[248:255], %[ix]\n\t" "v_permlane16_swap_b32 v224, v225\n\t" "v_permlane16_swap_b32 v226, v227\n\t" \
    "s_waitcnt lgkmcnt(2)\n\t" "v_smfmac_f32_16x16x64_bf16 v[216:219], v[252:255], v[176:183], %[ix]\n\t" "buffer_store_dwordx4 v[224:227], %[uo], %[rs], s61 offen sc1\n\t" \
    "s_xor_b32 s61, s61, 0x40000\n\t" "s_waitcnt lgkmcnt(0)\n\t" "v_smfmac_f32_16x16x64_bf16 v[220:223], v[252:255], v[184:191], %[ix]\n\t" "s_nop 5\n\t" \
    "v_cvt_pk_bf16_f32 v228, v216, v218\n\t" "v_cvt_pk_bf16_f32 v229, v217, v219\n\t" "v_cvt_pk_bf16_f32 v230, v220, v222\n\t" "v_cvt_pk_bf16_f32 v231, v221, v223\n\t" \
    "s_nop 0\n\t" "v_permlane32_swap_b32 v228, v230\n\t" "v_permlane32_swap_b32 v229, v231\n\t" "s_nop 1\n\t" "v_permlane16_swap_b32 v228, v229\n\t" \
    "v_permlane16_swap_b32 v230, v231\n\t" "buffer_store_dwordx4 v[228:231], %[uo], %[rs], s61 offen sc1\n\t" "s_nop 1\n\t" XT(96, 97) "v_mov_b64 v[16:17], -1\n\t" \
    "v_mov_b64 v[18:19], -1\n\t" "s_barrier\n\t" "buffer_store_dwordx4 v[16:19], %[l1], %[rs], %[ur] offen offset:0 sc1\n\t" \
    "buffer_store_dwordx4 v[16:19], %[l1], %[rs], %[ur] offen offset:1024 sc1\n\t" "buffer_store_dwordx4 v[16:19], %[l1], %[rs], %[ur] offen offset:2048 sc1\n\t" \
    "buffer_store_dwordx4 v[16:19], %[l1], %[rs], %[ur] offen offset:3072 sc1\n\t" "s_sub_u32 s61, %[ur], 0x5000\n\t" \
    "buffer_store_dwordx4 v[16:19], %[l1], %[rs], s61 offen offset:3072 sc1\n\t" "s_branch 99f\n\t" "41:\n\t" "ds_read_b128 v[228:231], %[l1] offset:12288\n\t" \
    "s_waitcnt lgkmcnt(0)\n\t" "v_max3_u32 v225, v228, v229, v230\n\t" "v_max_u32 v225, v225, v231\n\t" "ds_read_b128 v[228:231], %[l1] offset:13312\n\t" \
    "s_waitcnt lgkmcnt(0)\n\t" "v_max3_u32 v225, v225, v228, v229\n\t" "v_max3_u32 v225, v225, v230, v231\n\t" "ds_read_b128 v[228:231], %[l1] offset:14336\n\t" \
    "s_waitcnt lgkmcnt(0)\n\t" "v_max3_u32 v225, v225, v228, v229\n\t" "v_max3_u32 v225, v225, v230, v231\n\t" "ds_read_b128 v[228:231], %[l1] offset:15360\n\t" \
    "s_waitcnt lgkmcnt(0)\n\t" "v_max3_u32 v225, v225, v228, v229\n\t" "v_max3_u32 v225, v225, v230, v231\n\t" "v_cmp_eq_u32 vcc, -1, v225\n\t" "s_cbranch_vccz 42b\n\t" \
    "s_cmp_eq_u32 %[rt], 1\n\t" "s_cbranch_scc1 42b\n\t" "v_mov_b32 v225, %[rt]\n\t" "ds_write_b32 v227, v225\n\t" "s_waitcnt lgkmcnt(0)\n\t" "s_branch 42b\n\t" "43:\n\t" \
    "s_sub_u32 %[rt], %[rt], 1\n\t" "s_waitcnt lgkmcnt(0)\n\t" XCNT(100) "44:\n\t" "buffer_load_dwordx4 v[240:243], %[mo], %[rs], %[sb] offen sc1\n\t" \
    "buffer_load_dwordx4 v[244:247], v224, %[rs], %[sb] offen sc1\n\t" "buffer_load_dwordx4 v[248:251], %[mo], %[rs], s60 offen sc1\n\t" \
    "buffer_load_dwordx4 v[252:255], v224, %[rs], s60 offen sc1\n\t" "s_waitcnt vmcnt(0)\n\t" "v_max_u32 v225, v240, v241\n\t" "v_max3_u32 v225, v225, v242, v243\n\t" \
    "v_max3_u32 v225, v225, v244, v245\n\t" "v_max3_u32 v225, v225, v246, v247\n\t" "v_max3_u32 v225, v225, v248, v249\n\t" "v_max3_u32 v225, v225, v250, v251\n\t" \
    "v_max3_u32 v225, v225, v252, v253\n\t" "v_max3_u32 v225, v225, v254, v255\n\t" "v_cmp_eq_u32 vcc, -1, v225\n\t" "s_cbranch_vccz 45f\n\t" "s_cmp_eq_u32 %[bd], 0\n\t" \
    "s_cbranch_scc1 45f\n\t" "s_sub_u32 %[bd], %[bd], 1\n\t" XCNT(101) "s_branch 44b\n\t" "45:\n\t" "ds_read_b128 v[160:163], %[l0] offset:0\n\t" \
    "ds_read_b128 v[164:167], %[l0] offset:1024\n\t" "ds_read_b128 v[168:171], %[l0] offset:2048\n\t" "ds_read_b128 v[172:175], %[l0] offset:3072\n\t" \
    "ds_read_b128 v[176:179], %[l0] offset:4096\n\t" "ds_read_b128 v[180:183], %[l0] offset:5120\n\t" "ds_read_b128 v[184:187], %[l0] offset:6144\n\t" \
    "ds_read_b128 v[188:191], %[l0] offset:7168\n\t" "v_mov_b64 v[232:233], 0\n\t" "v_mov_b64 v[234:235], 0\n\t" "v_mov_b64 v[236:237], 0\n\t" "v_mov_b64 v[238:239], 0\n\t" \
    "s_barrier\n\t" "s_branch 40b\n\t" "99:\n\t"
#define Y_STEP \
    ".p2align 6\n\t" "v_xor_b32 v128, 0, %[u0]\n\t" "v_xor_b32 v132, 0, %[pm]\n\t" "v_xor_b32 v129, 2048, %[u0]\n\t" "v_xor_b32 v133, 1024, %[pm]\n\t" \
    "v_xor_b32 v130, 4096, %[u0]\n\t" "v_xor_b32 v134, 2048, %[pm]\n\t" "v_xor_b32 v131, 6144, %[u0]\n\t" "v_xor_b32 v135, 3072, %[pm]\n\t" "s_getpc_b64 s[62:63]\n\t" \
    "11:\n\t" "s_memtime s[64:65]\n\t" "s_waitcnt lgkmcnt(0)\n\t" "s_sub_u32 s61, %[tu], s64\n\t" "s_cmp_gt_i32 s61, 124\n\t" "s_cbranch_scc1 11b\n\t" \
    "s_max_i32 s61, s61, 0\n\t" "s_and_b32 s61, s61, 124\n\t" "s_sub_u32 s61, 12f-11b, s61\n\t" "s_add_u32 s62, s62, s61\n\t" "s_addc_u32 s63, s63, 0\n\t" \
    "s_setpc_b64 s[62:63]\n\t" ".rept 31\n\t" "s_nop 0\n\t" ".endr\n\t" "12:\n\t" XT(90, 91) "buffer_load_dwordx4 v[144:147], v128, %[rs], %[ub] offen offset:0 sc1\n\t" \
    "buffer_load_dwordx4 v[148:151], v128, %[rs], %[ub] offen offset:1024 sc1\n\t" "buffer_load_dwordx4 v[152:155], v129, %[rs], %[ub] offen offset:0 sc1\n\t" \
    "buffer_load_dwordx4 v[156:159], v129, %[rs], %[ub] offen offset:1024 sc1\n\t" "s_cmp_lg_u32 %[late], 0\n\t" "s_cbranch_scc1 14f\n\t" "s_getpc_b64 s[62:63]\n\t" "13:\n\t" \
    "s_memtime s[64:65]\n\t" "s_waitcnt lgkmcnt(0)\n\t" "s_sub_u32 s61, %[tg], s64\n\t" "s_cmp_gt_i32 s61, 124\n\t" "s_cbranch_scc1 13b\n\t" "s_max_i32 s61, s61, 0\n\t" \
    "s_and_b32 s61, s61, 124\n\t" "s_sub_u32 s61, 14f-13b, s61\n\t" "s_add_u32 s62, s62, s61\n\t" "s_addc_u32 s63, s63, 0\n\t" "s_setpc_b64 s[62:63]\n\t" ".rept 31\n\t" \
    "s_nop 0\n\t" ".endr\n\t" "14:\n\t" XT(92, 93) "20:\n\t" "buffer_load_dwordx4 v[160:163], v130, %[rs], %[ub] offen offset:0 sc1\n\t" \
    "buffer_load_dwordx4 v[164:167], v130, %[rs], %[ub] offen offset:1024 sc1\n\t" "buffer_load_dwordx4 v[192:195], %[x0], %[rs], %[pb] offen offset:0 sc1\n\t" \
    "buffer_load_dwordx4 v[196:199], %[x0], %[rs], %[pb] offen offset:1024 sc1\n\t" "buffer_load_dwordx4 v[200:203], %[x0], %[rs], %[pb] offen offset:2048 sc1\n\t" \
    "buffer_load_dwordx4 v[204:207], %[x0], %[rs], %[pb] offen offset:3072 sc1\n\t" "buffer_load_dwordx2 v[176:177], v132, %[rs], %[pb] offen offset:0 sc1\n\t" \
    "buffer_load_dwordx2 v[178:179], v132, %[rs], %[pb] offen offset:128 sc1\n\t" "v_mov_b64 v[120:121], 0\n\t" "v_mov_b64 v[122:123], 0\n\t" \
    "buffer_load_dwordx4 v[208:211], %[x1], %[rs], %[pb] offen offset:0 sc1\n\t" "buffer_load_dwordx4 v[212:215], %[x1], %[rs], %[pb] offen offset:1024 sc1\n\t" \
    "buffer_load_dwordx4 v[216:219], %[x1], %[rs], %[pb] offen offset:2048 sc1\n\t" "buffer_load_dwordx4 v[220:223], %[x1], %[rs], %[pb] offen offset:3072 sc1\n\t" \
    "buffer_load_dwordx2 v[180:181], v133, %[rs], %[pb] offen offset:0 sc1\n\t" "buffer_load_dwordx2 v[182:183], v133, %[rs], %[pb] offen offset:128 sc1\n\t" \
    "buffer_load_dwordx4 v[224:227], %[x2], %[rs], %[pb] offen offset:0 sc1\n\t" "buffer_load_dwordx4 v[228:231], %[x2], %[rs], %[pb] offen offset:1024 sc1\n\t" \
    "buffer_load_dwordx4 v[232:235], %[x2], %[rs], %[pb] offen offset:2048 sc1\n\t" "buffer_load_dwordx4 v[236:239], %[x2], %[rs], %[pb] offen offset:3072 sc1\n\t" \
    "buffer_load_dwordx2 v[184:185], v134, %[rs], %[pb] offen offset:0 sc1\n\t" "buffer_load_dwordx2 v[186:187], v134, %[rs], %[pb] offen offset:128 sc1\n\t" \
    "buffer_load_dwordx4 v[240:243], %[x3], %[rs], %[pb] offen offset:0 sc1\n\t" "buffer_load_dwordx4 v[244:247], %[x3], %[rs], %[pb] offen offset:1024 sc1\n\t" \
    "buffer_load_dwordx4 v[248:251], %[x3], %[rs], %[pb] offen offset:2048 sc1\n\t" "buffer_load_dwordx4 v[252:255], %[x3], %[rs], %[pb] offen offset:3072 sc1\n\t" \
    "buffer_load_dwordx4 v[168:171], v131, %[rs], %[ub] offen offset:0 sc1\n\t" "buffer_load_dwordx4 v[172:175], v131, %[rs], %[ub] offen offset:1024 sc1\n\t" \
    "buffer_load_dwordx2 v[188:189], v135, %[rs], %[pb] offen offset:0 sc1\n\t" "buffer_load_dwordx2 v[190:191], v135, %[rs], %[pb] offen offset:128 sc1\n\t" \
    "buffer_load_dword %[sv], %[so], %[rs], %[pb] offen sc1\n\t" "v_mov_b32 v127, -1\n\t" "s_waitcnt vmcnt(26)\n\t" XT(94, 95) \
    "v_smfmac_f32_16x16x64_bf16 v[120:123], v[192:195], a[0:7], %[ix]\n\t" "s_waitcnt vmcnt(25)\n\t" "v_smfmac_f32_16x16x64_bf16 v[120:123], v[196:199], a[8:15], %[ix]\n\t" \
    "s_waitcnt vmcnt(24)\n\t" "v_smfmac_f32_16x16x64_bf16 v[120:123], v[200:203], a[16:23], %[ix]\n\t" "s_waitcnt vmcnt(23)\n\t" \
    "v_smfmac_f32_16x16x64_bf16 v[120:123], v[204:207], a[24:31], %[ix]\n\t" "s_waitcnt vmcnt(21)\n\t" \
    "v_smfmac_f32_16x16x64_bf16 v[120:123], v[176:179], v[144:151], %[ix]\n\t" "s_waitcnt vmcnt(20)\n\t" \
    "v_smfmac_f32_16x16x64_bf16 v[120:123], v[208:211], a[32:39], %[ix]\n\t" "s_waitcnt vmcnt(19)\n\t" \
    "v_smfmac_f32_16x16x64_bf16 v[120:123], v[212:215], a[40:47], %[ix]\n\t" "s_waitcnt vmcnt(18)\n\t" \
    "v_smfmac_f32_16x16x64_bf16 v[120:123], v[216:219], a[48:55], %[ix]\n\t" "s_waitcnt vmcnt(17)\n\t" \
    "v_smfmac_f32_16x16x64_bf16 v[120:123], v[220:223], a[56:63], %[ix]\n\t" "s_waitcnt vmcnt(15)\n\t" \
    "v_smfmac_f32_16x16x64_bf16 v[120:123], v[180:183], v[152:159], %[ix]\n\t" "s_waitcnt vmcnt(14)\n\t" \
    "v_smfmac_f32_16x16x64_bf16 v[120:123], v[224:227], a[64:71], %[ix]\n\t" "s_waitcnt vmcnt(13)\n\t" \
    "v_smfmac_f32_16x16x64_bf16 v[120:123], v[228:231], a[72:79], %[ix]\n\t" "s_waitcnt vmcnt(12)\n\t" \
    "v_smfmac_f32_16x16x64_bf16 v[120:123], v[232:235], a[80:87], %[ix]\n\t" "s_waitcnt vmcnt(11)\n\t" \
    "v_smfmac_f32_16x16x64_bf16 v[120:123], v[236:239], a[88:95], %[ix]\n\t" "s_waitcnt vmcnt(9)\n\t" \
    "v_smfmac_f32_16x16x64_bf16 v[120:123], v[184:187], v[160:167], %[ix]\n\t" "s_waitcnt vmcnt(8)\n\t" \
    "v_smfmac_f32_16x16x64_bf16 v[120:123], v[240:243], a[96:103], %[ix]\n\t" "s_waitcnt vmcnt(7)\n\t" \
    "v_smfmac_f32_16x16x64_bf16 v[120:123], v[244:247], a[104:111], %[ix]\n\t" "s_waitcnt vmcnt(6)\n\t" \
    "v_smfmac_f32_16x16x64_bf16 v[120:123], v[248:251], a[112:119], %[ix]\n\t" "s_waitcnt vmcnt(5)\n\t" \
    "v_smfmac_f32_16x16x64_bf16 v[120:123], v[252:255], a[120:127], %[ix]\n\t" "s_waitcnt vmcnt(1)\n\t" \
    "v_smfmac_f32_16x16x64_bf16 v[120:123], v[188:191], v[168:175], %[ix]\n\t" XT(82, 83) "s_nop 7\n\t" "v_add_f32 v120, v120, v121\n\t" "v_add_f32 v122, v122, v123\n\t" \
    "v_cvt_pk_bf16_f32 v124, v120, v122\n\t" "v_cmp_u_f32 vcc, v120, v122\n\t" "s_cbranch_vccnz 30f\n\t" "21:\n\t" \
    "v_mov_b32_dpp v125, v124 quad_perm:[1,0,3,2] row_mask:0xf bank_mask:0xf\n\t" "v_perm_b32 v124, v125, v124, %[ps]\n\t" "v_min_u32 v124, -2, v124\n\t" \
    "buffer_store_dword v124, %[mw], %[rs], %[mb] offen sc1\n\t" XT(84, 85) "buffer_store_dword v127, %[mw], %[rs], %[mr] offen sc1\n\t" "s_memtime s[64:65]\n\t" \
    "s_barrier\n\t" XT(98, 99) "s_waitcnt lgkmcnt(0)\n\t" "s_and_b32 %[bad], s64, -2\n\t" "s_branch 40f\n\t" "30:\n\t" "s_cmp_lg_u32 %[late], 0\n\t" "s_cbranch_scc1 32f\n\t" \
    XCNT(100) "s_mov_b32 %[bad], 1\n\t" "s_waitcnt vmcnt(0)\n\t" "s_branch 40f\n\t" "32:\n\t" "v_max_u32 v126, v144, v145\n\t" "v_max3_u32 v126, v126, v146, v147\n\t" \
    "v_max3_u32 v126, v126, v148, v149\n\t" "v_max3_u32 v126, v126, v150, v151\n\t" "v_max3_u32 v126, v126, v152, v153\n\t" "v_max3_u32 v126, v126, v154, v155\n\t" \
    "v_max3_u32 v126, v126, v156, v157\n\t" "v_max3_u32 v126, v126, v158, v159\n\t" "v_max3_u32 v126, v126, v160, v161\n\t" "v_max3_u32 v126, v126, v162, v163\n\t" \
    "v_max3_u32 v126, v126, v164, v165\n\t" "v_max3_u32 v126, v126, v166, v167\n\t" "v_max3_u32 v126, v126, v168, v169\n\t" "v_max3_u32 v126, v126, v170, v171\n\t" \
    "v_max3_u32 v126, v126, v172, v173\n\t" "v_max3_u32 v126, v126, v174, v175\n\t" UCNT "v_max3_u32 v126, v126, v176, v177\n\t" "v_max3_u32 v126, v126, v178, v179\n\t" \
    "v_max3_u32 v126, v126, v180, v181\n\t" "v_max3_u32 v126, v126, v182, v183\n\t" "v_max3_u32 v126, v126, v184, v185\n\t" "v_max3_u32 v126, v126, v186, v187\n\t" \
    "v_max3_u32 v126, v126, v188, v189\n\t" "v_max3_u32 v126, v126, v190, v191\n\t" "v_max3_u32 v126, v126, v192, v193\n\t" "v_max3_u32 v126, v126, v194, v195\n\t" \
    "v_max3_u32 v126, v126, v196, v197\n\t" "v_max3_u32 v126, v126, v198, v199\n\t" "v_max3_u32 v126, v126, v200, v201\n\t" "v_max3_u32 v126, v126, v202, v203\n\t" \
    "v_max3_u32 v126, v126, v204, v205\n\t" "v_max3_u32 v126, v126, v206, v207\n\t" "v_max3_u32 v126, v126, v208, v209\n\t" "v_max3_u32 v126, v126, v210, v211\n\t" \
    "v_max3_u32 v126, v126, v212, v213\n\t" "v_max3_u32 v126, v126, v214, v215\n\t" "v_max3_u32 v126, v126, v216, v217\n\t" "v_max3_u32 v126, v126, v218, v219\n\t" \
    "v_max3_u32 v126, v126, v220, v221\n\t" "v_max3_u32 v126, v126, v222, v223\n\t" "v_max3_u32 v126, v126, v224, v225\n\t" "v_max3_u32 v126, v126, v226, v227\n\t" \
    "v_max3_u32 v126, v126, v228, v229\n\t" "v_max3_u32 v126, v126, v230, v231\n\t" "v_max3_u32 v126, v126, v232, v233\n\t" "v_max3_u32 v126, v126, v234, v235\n\t" \
    "v_max3_u32 v126, v126, v236, v237\n\t" "v_max3_u32 v126, v126, v238, v239\n\t" "v_max3_u32 v126, v126, v240, v241\n\t" "v_max3_u32 v126, v126, v242, v243\n\t" \
    "v_max3_u32 v126, v126, v244, v245\n\t" "v_max3_u32 v126, v126, v246, v247\n\t" "v_max3_u32 v126, v126, v248, v249\n\t" "v_max3_u32 v126, v126, v250, v251\n\t" \
    "v_max3_u32 v126, v126, v252, v253\n\t" "v_max3_u32 v126, v126, v254, v255\n\t" "v_cmp_eq_u32 vcc, -1, v126\n\t" "s_cbranch_vccz 21b\n\t" "s_cmp_eq_u32 %[bd], 0\n\t" \
    "s_cbranch_scc1 21b\n\t" XCNT(100) "s_mov_b32 %[bad], 1\n\t" "s_waitcnt vmcnt(0)\n\t" "40:\n\t"
#define G_LOOP \
    ".p2align 6\n\t" "v_xor_b32 v72, 0, %[xo]\n\t" "v_add_u32 v80, 0x10000, v72\n\t" "v_xor_b32 v88, 0, %[xs]\n\t" "v_add_u32 v88, 0, v88\n\t" "v_xor_b32 v73, 64, %[xo]\n\t" \
    "v_add_u32 v81, 0x10000, v73\n\t" "v_xor_b32 v89, 16, %[xs]\n\t" "v_add_u32 v89, 2048, v89\n\t" "v_xor_b32 v74, 128, %[xo]\n\t" "v_add_u32 v82, 0x10000, v74\n\t" \
    "v_xor_b32 v90, 32, %[xs]\n\t" "v_add_u32 v90, 4096, v90\n\t" "v_xor_b32 v75, 192, %[xo]\n\t" "v_add_u32 v83, 0x10000, v75\n\t" "v_xor_b32 v91, 48, %[xs]\n\t" \
    "v_add_u32 v91, 6144, v91\n\t" "v_xor_b32 v76, 256, %[xo]\n\t" "v_add_u32 v84, 0x10000, v76\n\t" "v_xor_b32 v92, 64, %[xs]\n\t" "v_add_u32 v92, 8192, v92\n\t" \
    "v_xor_b32 v77, 320, %[xo]\n\t" "v_add_u32 v85, 0x10000, v77\n\t" "v_xor_b32 v93, 80, %[xs]\n\t" "v_add_u32 v93, 10240, v93\n\t" "v_xor_b32 v78, 384, %[xo]\n\t" \
    "v_add_u32 v86, 0x10000, v78\n\t" "v_xor_b32 v94, 96, %[xs]\n\t" "v_add_u32 v94, 12288, v94\n\t" "v_xor_b32 v79, 448, %[xo]\n\t" "v_add_u32 v87, 0x10000, v79\n\t" \
    "v_xor_b32 v95, 112, %[xs]\n\t" "v_add_u32 v95, 14336, v95\n\t" "v_mov_b32 v96, 2.0\n\t" "s_mov_b32 s60, 0\n\t" "s_mov_b32 s62, 0\n\t" "s_waitcnt vmcnt(0)\n\t" \
    "s_barrier\n\t" "ds_read_b128 v[100:103], v72 offset:0\n\t" "ds_read_b128 v[104:107], v72 offset:32768\n\t" "ds_read_b128 v[108:111], v73 offset:0\n\t" \
    "ds_read_b128 v[112:115], v73 offset:32768\n\t" "ds_read_b128 v[116:119], v74 offset:0\n\t" "10:\n\t" XT(80, 81) "s_waitcnt lgkmcnt(4)\n\t" \
    "v_mfma_f32_16x16x32_bf16 v[32:35], a[0:3], v[100:103], 0\n\t" "ds_read_b128 v[120:123], v74 offset:32768\n\t" "s_add_u32 s61, s60, 1\n\t" "s_and_b32 s61, s61, 63\n\t" \
    "v_mfma_f32_16x16x32_bf16 v[36:39], a[4:7], v[100:103], 0\n\t" "s_lshl_b32 s61, s61, 16\n\t" "s_add_u32 s63, %[lw], 65536\n\t" "s_waitcnt lgkmcnt(4)\n\t" \
    "v_mfma_f32_16x16x32_bf16 v[40:43], a[0:3], v[104:107], 0\n\t" "ds_read_b128 v[124:127], v75 offset:0\n\t" "v_mfma_f32_16x16x32_bf16 v[44:47], a[4:7], v[104:107], 0\n\t" \
    "s_waitcnt lgkmcnt(4)\n\t" "v_mfma_f32_16x16x32_bf16 v[32:35], a[8:11], v[108:111], v[32:35]\n\t" "ds_read_b128 v[128:131], v75 offset:32768\n\t" \
    "v_cvt_pk_bf16_f32 v64, v48, v49\n\t" "v_mfma_f32_16x16x32_bf16 v[36:39], a[12:15], v[108:111], v[36:39]\n\t" "v_cvt_pk_bf16_f32 v65, v50, v51\n\t" \
    "v_cvt_pk_bf16_f32 v66, v52, v53\n\t" "s_waitcnt lgkmcnt(4)\n\t" "v_mfma_f32_16x16x32_bf16 v[40:43], a[8:11], v[112:115], v[40:43]\n\t" \
    "ds_read_b128 v[100:103], v76 offset:0\n\t" "v_cvt_pk_bf16_f32 v67, v54, v55\n\t" "v_mfma_f32_16x16x32_bf16 v[44:47], a[12:15], v[112:115], v[44:47]\n\t" \
    "v_cvt_pk_bf16_f32 v68, v56, v57\n\t" "v_cvt_pk_bf16_f32 v69, v58, v59\n\t" "s_waitcnt lgkmcnt(4)\n\t" \
    "v_mfma_f32_16x16x32_bf16 v[32:35], a[16:19], v[116:119], v[32:35]\n\t" "ds_read_b128 v[104:107], v76 offset:32768\n\t" "v_cvt_pk_bf16_f32 v70, v60, v61\n\t" \
    "v_mfma_f32_16x16x32_bf16 v[36:39], a[20:23], v[116:119], v[36:39]\n\t" "v_cvt_pk_bf16_f32 v71, v62, v63\n\t" \
    "buffer_store_dwordx4 v[64:67], v96, %[rs], s62 offen offset:0 sc1\n\t" "s_waitcnt lgkmcnt(4)\n\t" "v_mfma_f32_16x16x32_bf16 v[40:43], a[16:19], v[120:123], v[40:43]\n\t" \
    "ds_read_b128 v[108:111], v77 offset:0\n\t" "v_mfma_f32_16x16x32_bf16 v[44:47], a[20:23], v[120:123], v[44:47]\n\t" "s_waitcnt lgkmcnt(4)\n\t" \
    "v_mfma_f32_16x16x32_bf16 v[32:35], a[24:27], v[124:127], v[32:35]\n\t" "ds_read_b128 v[112:115], v77 offset:32768\n\t" \
    "v_mfma_f32_16x16x32_bf16 v[36:39], a[28:31], v[124:127], v[36:39]\n\t" "buffer_store_dwordx4 v[68:71], v96, %[rs], s62 offen offset:256 sc1\n\t" \
    "s_waitcnt lgkmcnt(4)\n\t" "v_mfma_f32_16x16x32_bf16 v[40:43], a[24:27], v[128:131], v[40:43]\n\t" "ds_read_b128 v[116:119], v78 offset:0\n\t" "s_mov_b32 m0, s63\n\t" \
    "v_mfma_f32_16x16x32_bf16 v[44:47], a[28:31], v[128:131], v[44:47]\n\t" "s_add_u32 s63, s63, 0x800\n\t" "buffer_load_dwordx4 v88, %[rx], s61 offen lds\n\t" \
    "s_waitcnt lgkmcnt(4)\n\t" "v_mfma_f32_16x16x32_bf16 v[32:35], a[32:35], v[100:103], v[32:35]\n\t" "ds_read_b128 v[120:123], v78 offset:32768\n\t" \
    "v_mfma_f32_16x16x32_bf16 v[36:39], a[36:39], v[100:103], v[36:39]\n\t" "buffer_load_dwordx4 v88, %[rx], s61 offen offset:1024 lds\n\t" "s_waitcnt lgkmcnt(4)\n\t" \
    "v_mfma_f32_16x16x32_bf16 v[40:43], a[32:35], v[104:107], v[40:43]\n\t" "ds_read_b128 v[124:127], v79 offset:0\n\t" "s_mov_b32 m0, s63\n\t" \
    "v_mfma_f32_16x16x32_bf16 v[44:47], a[36:39], v[104:107], v[44:47]\n\t" "s_add_u32 s63, s63, 0x800\n\t" "buffer_load_dwordx4 v89, %[rx], s61 offen lds\n\t" \
    "s_waitcnt lgkmcnt(4)\n\t" "v_mfma_f32_16x16x32_bf16 v[32:35], a[40:43], v[108:111], v[32:35]\n\t" "ds_read_b128 v[128:131], v79 offset:32768\n\t" \
    "v_mfma_f32_16x16x32_bf16 v[36:39], a[44:47], v[108:111], v[36:39]\n\t" "buffer_load_dwordx4 v89, %[rx], s61 offen offset:1024 lds\n\t" "s_waitcnt lgkmcnt(4)\n\t" \
    "v_mfma_f32_16x16x32_bf16 v[40:43], a[40:43], v[112:115], v[40:43]\n\t" "ds_read_b128 v[100:103], v72 offset:512\n\t" "s_mov_b32 m0, s63\n\t" \
    "v_mfma_f32_16x16x32_bf16 v[44:47], a[44:47], v[112:115], v[44:47]\n\t" "s_add_u32 s63, s63, 0x800\n\t" "buffer_load_dwordx4 v90, %[rx], s61 offen lds\n\t" \
    "s_waitcnt lgkmcnt(4)\n\t" "v_mfma_f32_16x16x32_bf16 v[32:35], a[48:51], v[116:119], v[32:35]\n\t" "ds_read_b128 v[104:107], v72 offset:33280\n\t" \
    "v_mfma_f32_16x16x32_bf16 v[36:39], a[52:55], v[116:119], v[36:39]\n\t" "buffer_load_dwordx4 v90, %[rx], s61 offen offset:1024 lds\n\t" "s_waitcnt lgkmcnt(4)\n\t" \
    "v_mfma_f32_16x16x32_bf16 v[40:43], a[48:51], v[120:123], v[40:43]\n\t" "ds_read_b128 v[108:111], v73 offset:512\n\t" "s_mov_b32 m0, s63\n\t" \
    "v_mfma_f32_16x16x32_bf16 v[44:47], a[52:55], v[120:123], v[44:47]\n\t" "s_add_u32 s63, s63, 0x800\n\t" "buffer_load_dwordx4 v91, %[rx], s61 offen lds\n\t" \
    "s_waitcnt lgkmcnt(4)\n\t" "v_mfma_f32_16x16x32_bf16 v[32:35], a[56:59], v[124:127], v[32:35]\n\t" "ds_read_b128 v[112:115], v73 offset:33280\n\t" \
    "v_mfma_f32_16x16x32_bf16 v[36:39], a[60:63], v[124:127], v[36:39]\n\t" "buffer_load_dwordx4 v91, %[rx], s61 offen offset:1024 lds\n\t" "s_waitcnt lgkmcnt(4)\n\t" \
    "v_mfma_f32_16x16x32_bf16 v[40:43], a[56:59], v[128:131], v[40:43]\n\t" "ds_read_b128 v[116:119], v74 offset:512\n\t" "s_mov_b32 m0, s63\n\t" \
    "v_mfma_f32_16x16x32_bf16 v[44:47], a[60:63], v[128:131], v[44:47]\n\t" "s_add_u32 s63, s63, 0x800\n\t" "buffer_load_dwordx4 v92, %[rx], s61 offen lds\n\t" \
    "s_waitcnt lgkmcnt(4)\n\t" "v_mfma_f32_16x16x32_bf16 v[32:35], a[64:67], v[100:103], v[32:35]\n\t" "ds_read_b128 v[120:123], v74 offset:33280\n\t" \
    "v_mfma_f32_16x16x32_bf16 v[36:39], a[68:71], v[100:103], v[36:39]\n\t" "buffer_load_dwordx4 v92, %[rx], s61 offen offset:1024 lds\n\t" "s_waitcnt lgkmcnt(4)\n\t" \
    "v_mfma_f32_16x16x32_bf16 v[40:43], a[64:67], v[104:107], v[40:43]\n\t" "ds_read_b128 v[124:127], v75 offset:512\n\t" "s_mov_b32 m0, s63\n\t" \
    "v_mfma_f32_16x16x32_bf16 v[44:47], a[68:71], v[104:107], v[44:47]\n\t" "s_add_u32 s63, s63, 0x800\n\t" "buffer_load_dwordx4 v93, %[rx], s61 offen lds\n\t" \
    "s_waitcnt lgkmcnt(4)\n\t" "v_mfma_f32_16x16x32_bf16 v[32:35], a[72:75], v[108:111], v[32:35]\n\t" "ds_read_b128 v[128:131], v75 offset:33280\n\t" \
    "v_mfma_f32_16x16x32_bf16 v[36:39], a[76:79], v[108:111], v[36:39]\n\t" "buffer_load_dwordx4 v93, %[rx], s61 offen offset:1024 lds\n\t" "s_waitcnt lgkmcnt(4)\n\t" \
    "v_mfma_f32_16x16x32_bf16 v[40:43], a[72:75], v[112:115], v[40:43]\n\t" "ds_read_b128 v[100:103], v76 offset:512\n\t" "s_mov_b32 m0, s63\n\t" \
    "v_mfma_f32_16x16x32_bf16 v[44:47], a[76:79], v[112:115], v[44:47]\n\t" "s_add_u32 s63, s63, 0x800\n\t" "buffer_load_dwordx4 v94, %[rx], s61 offen lds\n\t" \
    "s_waitcnt lgkmcnt(4)\n\t" "v_mfma_f32_16x16x32_bf16 v[32:35], a[80:83], v[116:119], v[32:35]\n\t" "ds_read_b128 v[104:107], v76 offset:33280\n\t" \
    "v_mfma_f32_16x16x32_bf16 v[36:39], a[84:87], v[116:119], v[36:39]\n\t" "buffer_load_dwordx4 v94, %[rx], s61 offen offset:1024 lds\n\t" "s_waitcnt lgkmcnt(4)\n\t" \
    "v_mfma_f32_16x16x32_bf16 v[40:43], a[80:83], v[120:123], v[40:43]\n\t" "ds_read_b128 v[108:111], v77 offset:512\n\t" "s_mov_b32 m0, s63\n\t" \
    "v_mfma_f32_16x16x32_bf16 v[44:47], a[84:87], v[120:123], v[44:47]\n\t" "s_add_u32 s63, s63, 0x800\n\t" "buffer_load_dwordx4 v95, %[rx], s61 offen lds\n\t" \
    "s_waitcnt lgkmcnt(4)\n\t" "v_mfma_f32_16x16x32_bf16 v[32:35], a[88:91], v[124:127], v[32:35]\n\t" "ds_read_b128 v[112:115], v77 offset:33280\n\t" \
    "v_mfma_f32_16x16x32_bf16 v[36:39], a[92:95], v[124:127], v[36:39]\n\t" "buffer_load_dwordx4 v95, %[rx], s61 offen offset:1024 lds\n\t" "s_waitcnt lgkmcnt(4)\n\t" \
    "v_mfma_f32_16x16x32_bf16 v[40:43], a[88:91], v[128:131], v[40:43]\n\t" "ds_read_b128 v[116:119], v78 offset:512\n\t" \
    "v_mfma_f32_16x16x32_bf16 v[44:47], a[92:95], v[128:131], v[44:47]\n\t" "s_waitcnt lgkmcnt(4)\n\t" "v_mfma_f32_16x16x32_bf16 v[32:35], a[96:99], v[100:103], v[32:35]\n\t" \
    "ds_read_b128 v[120:123], v78 offset:33280\n\t" "s_lshr_b32 s64, s60, 2\n\t" "s_xor_b32 s64, s64, %[xp]\n\t" \
    "v_mfma_f32_16x16x32_bf16 v[36:39], a[100:103], v[100:103], v[36:39]\n\t" "s_and_b32 s64, s64, 1\n\t" "s_lshl_b32 s64, s64, 13\n\t" "s_waitcnt lgkmcnt(4)\n\t" \
    "v_mfma_f32_16x16x32_bf16 v[40:43], a[96:99], v[104:107], v[40:43]\n\t" "ds_read_b128 v[124:127], v79 offset:512\n\t" "s_lshl_b32 s62, s60, 19\n\t" \
    "v_mfma_f32_16x16x32_bf16 v[44:47], a[100:103], v[104:107], v[44:47]\n\t" "s_add_u32 s62, s62, s64\n\t" "s_add_u32 s62, s62, %[ob]\n\t" "s_waitcnt lgkmcnt(4)\n\t" \
    "v_mfma_f32_16x16x32_bf16 v[32:35], a[104:107], v[108:111], v[32:35]\n\t" "ds_read_b128 v[128:131], v79 offset:33280\n\t" \
    "v_mfma_f32_16x16x32_bf16 v[36:39], a[108:111], v[108:111], v[36:39]\n\t" "s_waitcnt lgkmcnt(4)\n\t" \
    "v_mfma_f32_16x16x32_bf16 v[40:43], a[104:107], v[112:115], v[40:43]\n\t" "ds_read_b128 v[100:103], v72 offset:1024\n\t" \
    "v_mfma_f32_16x16x32_bf16 v[44:47], a[108:111], v[112:115], v[44:47]\n\t" "s_waitcnt lgkmcnt(4)\n\t" \
    "v_mfma_f32_16x16x32_bf16 v[32:35], a[112:115], v[116:119], v[32:35]\n\t" "ds_read_b128 v[104:107], v72 offset:33792\n\t" \
    "v_mfma_f32_16x16x32_bf16 v[36:39], a[116:119], v[116:119], v[36:39]\n\t" "s_waitcnt lgkmcnt(4)\n\t" \
    "v_mfma_f32_16x16x32_bf16 v[40:43], a[112:115], v[120:123], v[40:43]\n\t" "ds_read_b128 v[108:111], v73 offset:1024\n\t" \
    "v_mfma_f32_16x16x32_bf16 v[44:47], a[116:119], v[120:123], v[44:47]\n\t" "s_waitcnt lgkmcnt(4)\n\t" \
    "v_mfma_f32_16x16x32_bf16 v[32:35], a[120:123], v[124:127], v[32:35]\n\t" "ds_read_b128 v[112:115], v73 offset:33792\n\t" \
    "v_mfma_f32_16x16x32_bf16 v[36:39], a[124:127], v[124:127], v[36:39]\n\t" "s_waitcnt lgkmcnt(4)\n\t" \
    "v_mfma_f32_16x16x32_bf16 v[40:43], a[120:123], v[128:131], v[40:43]\n\t" "ds_read_b128 v[116:119], v74 offset:1024\n\t" \
    "v_mfma_f32_16x16x32_bf16 v[44:47], a[124:127], v[128:131], v[44:47]\n\t" "s_waitcnt lgkmcnt(4)\n\t" \
    "v_mfma_f32_16x16x32_bf16 v[32:35], a[128:131], v[100:103], v[32:35]\n\t" "ds_read_b128 v[120:123], v74 offset:33792\n\t" \
    "v_mfma_f32_16x16x32_bf16 v[36:39], a[132:135], v[100:103], v[36:39]\n\t" "s_waitcnt lgkmcnt(4)\n\t" \
    "v_mfma_f32_16x16x32_bf16 v[40:43], a[128:131], v[104:107], v[40:43]\n\t" "ds_read_b128 v[124:127], v75 offset:1024\n\t" \
    "v_mfma_f32_16x16x32_bf16 v[44:47], a[132:135], v[104:107], v[44:47]\n\t" "s_waitcnt lgkmcnt(4)\n\t" \
    "v_mfma_f32_16x16x32_bf16 v[32:35], a[136:139], v[108:111], v[32:35]\n\t" "ds_read_b128 v[128:131], v75 offset:33792\n\t" "s_waitcnt vmcnt(16)\n\t" "s_barrier\n\t" \
    "v_mfma_f32_16x16x32_bf16 v[36:39], a[140:143], v[108:111], v[36:39]\n\t" "v_mov_b32 v98, s60\n\t" "buffer_store_dword v98, %[fo], %[rc], %[fl] offen sc1\n\t" \
    "s_waitcnt lgkmcnt(4)\n\t" "v_mfma_f32_16x16x32_bf16 v[40:43], a[136:139], v[112:115], v[40:43]\n\t" "ds_read_b128 v[100:103], v76 offset:1024\n\t" \
    "v_mfma_f32_16x16x32_bf16 v[44:47], a[140:143], v[112:115], v[44:47]\n\t" "s_waitcnt lgkmcnt(4)\n\t" \
    "v_mfma_f32_16x16x32_bf16 v[32:35], a[144:147], v[116:119], v[32:35]\n\t" "ds_read_b128 v[104:107], v76 offset:33792\n\t" \
    "v_mfma_f32_16x16x32_bf16 v[36:39], a[148:151], v[116:119], v[36:39]\n\t" "s_waitcnt lgkmcnt(4)\n\t" \
    "v_mfma_f32_16x16x32_bf16 v[40:43], a[144:147], v[120:123], v[40:43]\n\t" "ds_read_b128 v[108:111], v77 offset:1024\n\t" \
    "v_mfma_f32_16x16x32_bf16 v[44:47], a[148:151], v[120:123], v[44:47]\n\t" "s_waitcnt lgkmcnt(4)\n\t" \
    "v_mfma_f32_16x16x32_bf16 v[32:35], a[152:155], v[124:127], v[32:35]\n\t" "ds_read_b128 v[112:115], v77 offset:33792\n\t" \
    "v_mfma_f32_16x16x32_bf16 v[36:39], a[156:159], v[124:127], v[36:39]\n\t" "s_waitcnt lgkmcnt(4)\n\t" \
    "v_mfma_f32_16x16x32_bf16 v[40:43], a[152:155], v[128:131], v[40:43]\n\t" "ds_read_b128 v[116:119], v78 offset:1024\n\t" \
    "v_mfma_f32_16x16x32_bf16 v[44:47], a[156:159], v[128:131], v[44:47]\n\t" "s_waitcnt lgkmcnt(4)\n\t" \
    "v_mfma_f32_16x16x32_bf16 v[32:35], a[160:163], v[100:103], v[32:35]\n\t" "ds_read_b128 v[120:123], v78 offset:33792\n\t" \
    "v_mfma_f32_16x16x32_bf16 v[36:39], a[164:167], v[100:103], v[36:39]\n\t" "s_waitcnt lgkmcnt(4)\n\t" \
    "v_mfma_f32_16x16x32_bf16 v[40:43], a[160:163], v[104:107], v[40:43]\n\t" "ds_read_b128 v[124:127], v79 offset:1024\n\t" \
    "v_mfma_f32_16x16x32_bf16 v[44:47], a[164:167], v[104:107], v[44:47]\n\t" "s_waitcnt lgkmcnt(4)\n\t" \
    "v_mfma_f32_16x16x32_bf16 v[32:35], a[168:171], v[108:111], v[32:35]\n\t" "ds_read_b128 v[128:131], v79 offset:33792\n\t" \
    "v_mfma_f32_16x16x32_bf16 v[36:39], a[172:175], v[108:111], v[36:39]\n\t" "s_waitcnt lgkmcnt(4)\n\t" \
    "v_mfma_f32_16x16x32_bf16 v[40:43], a[168:171], v[112:115], v[40:43]\n\t" "ds_read_b128 v[100:103], v72 offset:1536\n\t" \
    "v_mfma_f32_16x16x32_bf16 v[44:47], a[172:175], v[112:115], v[44:47]\n\t" "s_waitcnt lgkmcnt(4)\n\t" \
    "v_mfma_f32_16x16x32_bf16 v[32:35], a[176:179], v[116:119], v[32:35]\n\t" "ds_read_b128 v[104:107], v72 offset:34304\n\t" \
    "v_mfma_f32_16x16x32_bf16 v[36:39], a[180:183], v[116:119], v[36:39]\n\t" "s_waitcnt lgkmcnt(4)\n\t" \
    "v_mfma_f32_16x16x32_bf16 v[40:43], a[176:179], v[120:123], v[40:43]\n\t" "ds_read_b128 v[108:111], v73 offset:1536\n\t" \
    "v_mfma_f32_16x16x32_bf16 v[44:47], a[180:183], v[120:123], v[44:47]\n\t" "s_waitcnt lgkmcnt(4)\n\t" \
    "v_mfma_f32_16x16x32_bf16 v[32:35], a[184:187], v[124:127], v[32:35]\n\t" "ds_read_b128 v[112:115], v73 offset:34304\n\t" \
    "v_mfma_f32_16x16x32_bf16 v[36:39], a[188:191], v[124:127], v[36:39]\n\t" "s_waitcnt lgkmcnt(4)\n\t" \
    "v_mfma_f32_16x16x32_bf16 v[40:43], a[184:187], v[128:131], v[40:43]\n\t" "ds_read_b128 v[116:119], v74 offset:1536\n\t" \
    "v_mfma_f32_16x16x32_bf16 v[44:47], a[188:191], v[128:131], v[44:47]\n\t" "s_waitcnt lgkmcnt(4)\n\t" \
    "v_mfma_f32_16x16x32_bf16 v[32:35], a[192:195], v[100:103], v[32:35]\n\t" "ds_read_b128 v[120:123], v74 offset:34304\n\t" \
    "v_mfma_f32_16x16x32_bf16 v[36:39], a[196:199], v[100:103], v[36:39]\n\t" "s_waitcnt lgkmcnt(4)\n\t" \
    "v_mfma_f32_16x16x32_bf16 v[40:43], a[192:195], v[104:107], v[40:43]\n\t" "ds_read_b128 v[124:127], v75 offset:1536\n\t" \
    "v_mfma_f32_16x16x32_bf16 v[44:47], a[196:199], v[104:107], v[44:47]\n\t" "s_waitcnt lgkmcnt(4)\n\t" \
    "v_mfma_f32_16x16x32_bf16 v[32:35], a[200:203], v[108:111], v[32:35]\n\t" "ds_read_b128 v[128:131], v75 offset:34304\n\t" \
    "v_mfma_f32_16x16x32_bf16 v[36:39], a[204:207], v[108:111], v[36:39]\n\t" "s_waitcnt lgkmcnt(4)\n\t" \
    "v_mfma_f32_16x16x32_bf16 v[40:43], a[200:203], v[112:115], v[40:43]\n\t" "ds_read_b128 v[100:103], v76 offset:1536\n\t" \
    "v_mfma_f32_16x16x32_bf16 v[44:47], a[204:207], v[112:115], v[44:47]\n\t" "s_waitcnt lgkmcnt(4)\n\t" \
    "v_mfma_f32_16x16x32_bf16 v[32:35], a[208:211], v[116:119], v[32:35]\n\t" "ds_read_b128 v[104:107], v76 offset:34304\n\t" \
    "v_mfma_f32_16x16x32_bf16 v[36:39], a[212:215], v[116:119], v[36:39]\n\t" "s_waitcnt lgkmcnt(4)\n\t" \
    "v_mfma_f32_16x16x32_bf16 v[40:43], a[208:211], v[120:123], v[40:43]\n\t" "ds_read_b128 v[108:111], v77 offset:1536\n\t" \
    "v_mfma_f32_16x16x32_bf16 v[44:47], a[212:215], v[120:123], v[44:47]\n\t" "s_waitcnt lgkmcnt(4)\n\t" \
    "v_mfma_f32_16x16x32_bf16 v[32:35], a[216:219], v[124:127], v[32:35]\n\t" "ds_read_b128 v[112:115], v77 offset:34304\n\t" \
    "v_mfma_f32_16x16x32_bf16 v[36:39], a[220:223], v[124:127], v[36:39]\n\t" "s_waitcnt lgkmcnt(4)\n\t" \
    "v_mfma_f32_16x16x32_bf16 v[40:43], a[216:219], v[128:131], v[40:43]\n\t" "ds_read_b128 v[116:119], v78 offset:1536\n\t" \
    "v_mfma_f32_16x16x32_bf16 v[44:47], a[220:223], v[128:131], v[44:47]\n\t" "s_waitcnt lgkmcnt(4)\n\t" \
    "v_mfma_f32_16x16x32_bf16 v[32:35], a[224:227], v[100:103], v[32:35]\n\t" "ds_read_b128 v[120:123], v78 offset:34304\n\t" \
    "v_mfma_f32_16x16x32_bf16 v[36:39], a[228:231], v[100:103], v[36:39]\n\t" "s_waitcnt lgkmcnt(4)\n\t" \
    "v_mfma_f32_16x16x32_bf16 v[40:43], a[224:227], v[104:107], v[40:43]\n\t" "ds_read_b128 v[124:127], v79 offset:1536\n\t" \
    "v_mfma_f32_16x16x32_bf16 v[44:47], a[228:231], v[104:107], v[44:47]\n\t" "s_waitcnt lgkmcnt(4)\n\t" \
    "v_mfma_f32_16x16x32_bf16 v[32:35], a[232:235], v[108:111], v[32:35]\n\t" "ds_read_b128 v[128:131], v79 offset:34304\n\t" \
    "v_mfma_f32_16x16x32_bf16 v[36:39], a[236:239], v[108:111], v[36:39]\n\t" "s_waitcnt lgkmcnt(4)\n\t" \
    "v_mfma_f32_16x16x32_bf16 v[40:43], a[232:235], v[112:115], v[40:43]\n\t" "v_mfma_f32_16x16x32_bf16 v[44:47], a[236:239], v[112:115], v[44:47]\n\t" \
    "s_waitcnt lgkmcnt(3)\n\t" "v_mfma_f32_16x16x32_bf16 v[32:35], a[240:243], v[116:119], v[32:35]\n\t" \
    "v_mfma_f32_16x16x32_bf16 v[36:39], a[244:247], v[116:119], v[36:39]\n\t" "s_waitcnt vmcnt(1) lgkmcnt(0)\n\t" "s_barrier\n\t" XT(82, 83) \
    "v_mfma_f32_16x16x32_bf16 v[40:43], a[240:243], v[120:123], v[40:43]\n\t" "ds_read_b128 v[100:103], v80 offset:0\n\t" \
    "v_mfma_f32_16x16x32_bf16 v[44:47], a[244:247], v[120:123], v[44:47]\n\t" "v_mfma_f32_16x16x32_bf16 v[32:35], a[248:251], v[124:127], v[32:35]\n\t" \
    "ds_read_b128 v[104:107], v80 offset:32768\n\t" "v_mfma_f32_16x16x32_bf16 v[36:39], a[252:255], v[124:127], v[36:39]\n\t" "ds_read_b128 v[108:111], v81 offset:0\n\t" \
    "v_mfma_f32_16x16x32_bf16 v[40:43], a[248:251], v[128:131], v[40:43]\n\t" "ds_read_b128 v[112:115], v81 offset:32768\n\t" "ds_read_b128 v[116:119], v82 offset:0\n\t" \
    "v_mfma_f32_16x16x32_bf16 v[44:47], a[252:255], v[128:131], v[44:47]\n\t" "s_add_u32 s60, s60, 1\n\t" GSUM "s_cmp_eq_u32 s60, 1\n\t" "s_cbranch_scc1 20f\n\t" "11:\n\t" \
    XT(80, 81) "s_waitcnt lgkmcnt(4)\n\t" "v_mfma_f32_16x16x32_bf16 v[48:51], a[0:3], v[100:103], 0\n\t" "ds_read_b128 v[120:123], v82 offset:32768\n\t" \
    "s_add_u32 s61, s60, 1\n\t" "s_and_b32 s61, s61, 63\n\t" "v_mfma_f32_16x16x32_bf16 v[52:55], a[4:7], v[100:103], 0\n\t" "s_lshl_b32 s61, s61, 16\n\t" \
    "s_add_u32 s63, %[lw], 0\n\t" "s_waitcnt lgkmcnt(4)\n\t" "v_mfma_f32_16x16x32_bf16 v[56:59], a[0:3], v[104:107], 0\n\t" "ds_read_b128 v[124:127], v83 offset:0\n\t" \
    "v_mfma_f32_16x16x32_bf16 v[60:63], a[4:7], v[104:107], 0\n\t" "s_waitcnt lgkmcnt(4)\n\t" "v_mfma_f32_16x16x32_bf16 v[48:51], a[8:11], v[108:111], v[48:51]\n\t" \
    "ds_read_b128 v[128:131], v83 offset:32768\n\t" "v_cvt_pk_bf16_f32 v64, v32, v33\n\t" "v_mfma_f32_16x16x32_bf16 v[52:55], a[12:15], v[108:111], v[52:55]\n\t" \
    "v_cvt_pk_bf16_f32 v65, v34, v35\n\t" "v_cvt_pk_bf16_f32 v66, v36, v37\n\t" "s_waitcnt lgkmcnt(4)\n\t" \
    "v_mfma_f32_16x16x32_bf16 v[56:59], a[8:11], v[112:115], v[56:59]\n\t" "ds_read_b128 v[100:103], v84 offset:0\n\t" "v_cvt_pk_bf16_f32 v67, v38, v39\n\t" \
    "v_mfma_f32_16x16x32_bf16 v[60:63], a[12:15], v[112:115], v[60:63]\n\t" "v_cvt_pk_bf16_f32 v68, v40, v41\n\t" "v_cvt_pk_bf16_f32 v69, v42, v43\n\t" \
    "s_waitcnt lgkmcnt(4)\n\t" "v_mfma_f32_16x16x32_bf16 v[48:51], a[16:19], v[116:119], v[48:51]\n\t" "ds_read_b128 v[104:107], v84 offset:32768\n\t" \
    "v_cvt_pk_bf16_f32 v70, v44, v45\n\t" "v_mfma_f32_16x16x32_bf16 v[52:55], a[20:23], v[116:119], v[52:55]\n\t" "v_cvt_pk_bf16_f32 v71, v46, v47\n\t" \
    "buffer_store_dwordx4 v[64:67], v96, %[rs], s62 offen offset:0 sc1\n\t" "s_waitcnt lgkmcnt(4)\n\t" "v_mfma_f32_16x16x32_bf16 v[56:59], a[16:19], v[120:123], v[56:59]\n\t" \
    "ds_read_b128 v[108:111], v85 offset:0\n\t" "v_mfma_f32_16x16x32_bf16 v[60:63], a[20:23], v[120:123], v[60:63]\n\t" "s_waitcnt lgkmcnt(4)\n\t" \
    "v_mfma_f32_16x16x32_bf16 v[48:51], a[24:27], v[124:127], v[48:51]\n\t" "ds_read_b128 v[112:115], v85 offset:32768\n\t" \
    "v_mfma_f32_16x16x32_bf16 v[52:55], a[28:31], v[124:127], v[52:55]\n\t" "buffer_store_dwordx4 v[68:71], v96, %[rs], s62 offen offset:256 sc1\n\t" \
    "s_waitcnt lgkmcnt(4)\n\t" "v_mfma_f32_16x16x32_bf16 v[56:59], a[24:27], v[128:131], v[56:59]\n\t" "ds_read_b128 v[116:119], v86 offset:0\n\t" "s_mov_b32 m0, s63\n\t" \
    "v_mfma_f32_16x16x32_bf16 v[60:63], a[28:31], v[128:131], v[60:63]\n\t" "s_add_u32 s63, s63, 0x800\n\t" "buffer_load_dwordx4 v88, %[rx], s61 offen lds\n\t" \
    "s_waitcnt lgkmcnt(4)\n\t" "v_mfma_f32_16x16x32_bf16 v[48:51], a[32:35], v[100:103], v[48:51]\n\t" "ds_read_b128 v[120:123], v86 offset:32768\n\t" \
    "v_mfma_f32_16x16x32_bf16 v[52:55], a[36:39], v[100:103], v[52:55]\n\t" "buffer_load_dwordx4 v88, %[rx], s61 offen offset:1024 lds\n\t" "s_waitcnt lgkmcnt(4)\n\t" \
    "v_mfma_f32_16x16x32_bf16 v[56:59], a[32:35], v[104:107], v[56:59]\n\t" "ds_read_b128 v[124:127], v87 offset:0\n\t" "s_mov_b32 m0, s63\n\t" \
    "v_mfma_f32_16x16x32_bf16 v[60:63], a[36:39], v[104:107], v[60:63]\n\t" "s_add_u32 s63, s63, 0x800\n\t" "buffer_load_dwordx4 v89, %[rx], s61 offen lds\n\t" \
    "s_waitcnt lgkmcnt(4)\n\t" "v_mfma_f32_16x16x32_bf16 v[48:51], a[40:43], v[108:111], v[48:51]\n\t" "ds_read_b128 v[128:131], v87 offset:32768\n\t" \
    "v_mfma_f32_16x16x32_bf16 v[52:55], a[44:47], v[108:111], v[52:55]\n\t" "buffer_load_dwordx4 v89, %[rx], s61 offen offset:1024 lds\n\t" "s_waitcnt lgkmcnt(4)\n\t" \
    "v_mfma_f32_16x16x32_bf16 v[56:59], a[40:43], v[112:115], v[56:59]\n\t" "ds_read_b128 v[100:103], v80 offset:512\n\t" "s_mov_b32 m0, s63\n\t" \
    "v_mfma_f32_16x16x32_bf16 v[60:63], a[44:47], v[112:115], v[60:63]\n\t" "s_add_u32 s63, s63, 0x800\n\t" "buffer_load_dwordx4 v90, %[rx], s61 offen lds\n\t" \
    "s_waitcnt lgkmcnt(4)\n\t" "v_mfma_f32_16x16x32_bf16 v[48:51], a[48:51], v[116:119], v[48:51]\n\t" "ds_read_b128 v[104:107], v80 offset:33280\n\t" \
    "v_mfma_f32_16x16x32_bf16 v[52:55], a[52:55], v[116:119], v[52:55]\n\t" "buffer_load_dwordx4 v90, %[rx], s61 offen offset:1024 lds\n\t" "s_waitcnt lgkmcnt(4)\n\t" \
    "v_mfma_f32_16x16x32_bf16 v[56:59], a[48:51], v[120:123], v[56:59]\n\t" "ds_read_b128 v[108:111], v81 offset:512\n\t" "s_mov_b32 m0, s63\n\t" \
    "v_mfma_f32_16x16x32_bf16 v[60:63], a[52:55], v[120:123], v[60:63]\n\t" "s_add_u32 s63, s63, 0x800\n\t" "buffer_load_dwordx4 v91, %[rx], s61 offen lds\n\t" \
    "s_waitcnt lgkmcnt(4)\n\t" "v_mfma_f32_16x16x32_bf16 v[48:51], a[56:59], v[124:127], v[48:51]\n\t" "ds_read_b128 v[112:115], v81 offset:33280\n\t" \
    "v_mfma_f32_16x16x32_bf16 v[52:55], a[60:63], v[124:127], v[52:55]\n\t" "buffer_load_dwordx4 v91, %[rx], s61 offen offset:1024 lds\n\t" "s_waitcnt lgkmcnt(4)\n\t" \
    "v_mfma_f32_16x16x32_bf16 v[56:59], a[56:59], v[128:131], v[56:59]\n\t" "ds_read_b128 v[116:119], v82 offset:512\n\t" "s_mov_b32 m0, s63\n\t" \
    "v_mfma_f32_16x16x32_bf16 v[60:63], a[60:63], v[128:131], v[60:63]\n\t" "s_add_u32 s63, s63, 0x800\n\t" "buffer_load_dwordx4 v92, %[rx], s61 offen lds\n\t" \
    "s_waitcnt lgkmcnt(4)\n\t" "v_mfma_f32_16x16x32_bf16 v[48:51], a[64:67], v[100:103], v[48:51]\n\t" "ds_read_b128 v[120:123], v82 offset:33280\n\t" \
    "v_mfma_f32_16x16x32_bf16 v[52:55], a[68:71], v[100:103], v[52:55]\n\t" "buffer_load_dwordx4 v92, %[rx], s61 offen offset:1024 lds\n\t" "s_waitcnt lgkmcnt(4)\n\t" \
    "v_mfma_f32_16x16x32_bf16 v[56:59], a[64:67], v[104:107], v[56:59]\n\t" "ds_read_b128 v[124:127], v83 offset:512\n\t" "s_mov_b32 m0, s63\n\t" \
    "v_mfma_f32_16x16x32_bf16 v[60:63], a[68:71], v[104:107], v[60:63]\n\t" "s_add_u32 s63, s63, 0x800\n\t" "buffer_load_dwordx4 v93, %[rx], s61 offen lds\n\t" \
    "s_waitcnt lgkmcnt(4)\n\t" "v_mfma_f32_16x16x32_bf16 v[48:51], a[72:75], v[108:111], v[48:51]\n\t" "ds_read_b128 v[128:131], v83 offset:33280\n\t" \
    "v_mfma_f32_16x16x32_bf16 v[52:55], a[76:79], v[108:111], v[52:55]\n\t" "buffer_load_dwordx4 v93, %[rx], s61 offen offset:1024 lds\n\t" "s_waitcnt lgkmcnt(4)\n\t" \
    "v_mfma_f32_16x16x32_bf16 v[56:59], a[72:75], v[112:115], v[56:59]\n\t" "ds_read_b128 v[100:103], v84 offset:512\n\t" "s_mov_b32 m0, s63\n\t" \
    "v_mfma_f32_16x16x32_bf16 v[60:63], a[76:79], v[112:115], v[60:63]\n\t" "s_add_u32 s63, s63, 0x800\n\t" "buffer_load_dwordx4 v94, %[rx], s61 offen lds\n\t" \
    "s_waitcnt lgkmcnt(4)\n\t" "v_mfma_f32_16x16x32_bf16 v[48:51], a[80:83], v[116:119], v[48:51]\n\t" "ds_read_b128 v[104:107], v84 offset:33280\n\t" \
    "v_mfma_f32_16x16x32_bf16 v[52:55], a[84:87], v[116:119], v[52:55]\n\t" "buffer_load_dwordx4 v94, %[rx], s61 offen offset:1024 lds\n\t" "s_waitcnt lgkmcnt(4)\n\t" \
    "v_mfma_f32_16x16x32_bf16 v[56:59], a[80:83], v[120:123], v[56:59]\n\t" "ds_read_b128 v[108:111], v85 offset:512\n\t" "s_mov_b32 m0, s63\n\t" \
    "v_mfma_f32_16x16x32_bf16 v[60:63], a[84:87], v[120:123], v[60:63]\n\t" "s_add_u32 s63, s63, 0x800\n\t" "buffer_load_dwordx4 v95, %[rx], s61 offen lds\n\t" \
    "s_waitcnt lgkmcnt(4)\n\t" "v_mfma_f32_16x16x32_bf16 v[48:51], a[88:91], v[124:127], v[48:51]\n\t" "ds_read_b128 v[112:115], v85 offset:33280\n\t" \
    "v_mfma_f32_16x16x32_bf16 v[52:55], a[92:95], v[124:127], v[52:55]\n\t" "buffer_load_dwordx4 v95, %[rx], s61 offen offset:1024 lds\n\t" "s_waitcnt lgkmcnt(4)\n\t" \
    "v_mfma_f32_16x16x32_bf16 v[56:59], a[88:91], v[128:131], v[56:59]\n\t" "ds_read_b128 v[116:119], v86 offset:512\n\t" \
    "v_mfma_f32_16x16x32_bf16 v[60:63], a[92:95], v[128:131], v[60:63]\n\t" "s_waitcnt lgkmcnt(4)\n\t" "v_mfma_f32_16x16x32_bf16 v[48:51], a[96:99], v[100:103], v[48:51]\n\t" \
    "ds_read_b128 v[120:123], v86 offset:33280\n\t" "s_lshr_b32 s64, s60, 2\n\t" "s_xor_b32 s64, s64, %[xp]\n\t" \
    "v_mfma_f32_16x16x32_bf16 v[52:55], a[100:103], v[100:103], v[52:55]\n\t" "s_and_b32 s64, s64, 1\n\t" "s_lshl_b32 s64, s64, 13\n\t" "s_waitcnt lgkmcnt(4)\n\t" \
    "v_mfma_f32_16x16x32_bf16 v[56:59], a[96:99], v[104:107], v[56:59]\n\t" "ds_read_b128 v[124:127], v87 offset:512\n\t" "s_lshl_b32 s62, s60, 19\n\t" \
    "v_mfma_f32_16x16x32_bf16 v[60:63], a[100:103], v[104:107], v[60:63]\n\t" "s_add_u32 s62, s62, s64\n\t" "s_add_u32 s62, s62, %[ob]\n\t" "s_waitcnt lgkmcnt(4)\n\t" \
    "v_mfma_f32_16x16x32_bf16 v[48:51], a[104:107], v[108:111], v[48:51]\n\t" "ds_read_b128 v[128:131], v87 offset:33280\n\t" \
    "v_mfma_f32_16x16x32_bf16 v[52:55], a[108:111], v[108:111], v[52:55]\n\t" "s_waitcnt lgkmcnt(4)\n\t" \
    "v_mfma_f32_16x16x32_bf16 v[56:59], a[104:107], v[112:115], v[56:59]\n\t" "ds_read_b128 v[100:103], v80 offset:1024\n\t" \
    "v_mfma_f32_16x16x32_bf16 v[60:63], a[108:111], v[112:115], v[60:63]\n\t" "s_waitcnt lgkmcnt(4)\n\t" \
    "v_mfma_f32_16x16x32_bf16 v[48:51], a[112:115], v[116:119], v[48:51]\n\t" "ds_read_b128 v[104:107], v80 offset:33792\n\t" \
    "v_mfma_f32_16x16x32_bf16 v[52:55], a[116:119], v[116:119], v[52:55]\n\t" "s_waitcnt lgkmcnt(4)\n\t" \
    "v_mfma_f32_16x16x32_bf16 v[56:59], a[112:115], v[120:123], v[56:59]\n\t" "ds_read_b128 v[108:111], v81 offset:1024\n\t" \
    "v_mfma_f32_16x16x32_bf16 v[60:63], a[116:119], v[120:123], v[60:63]\n\t" "s_waitcnt lgkmcnt(4)\n\t" \
    "v_mfma_f32_16x16x32_bf16 v[48:51], a[120:123], v[124:127], v[48:51]\n\t" "ds_read_b128 v[112:115], v81 offset:33792\n\t" \
    "v_mfma_f32_16x16x32_bf16 v[52:55], a[124:127], v[124:127], v[52:55]\n\t" "s_waitcnt lgkmcnt(4)\n\t" \
    "v_mfma_f32_16x16x32_bf16 v[56:59], a[120:123], v[128:131], v[56:59]\n\t" "ds_read_b128 v[116:119], v82 offset:1024\n\t" \
    "v_mfma_f32_16x16x32_bf16 v[60:63], a[124:127], v[128:131], v[60:63]\n\t" "s_waitcnt lgkmcnt(4)\n\t" \
    "v_mfma_f32_16x16x32_bf16 v[48:51], a[128:131], v[100:103], v[48:51]\n\t" "ds_read_b128 v[120:123], v82 offset:33792\n\t" \
    "v_mfma_f32_16x16x32_bf16 v[52:55], a[132:135], v[100:103], v[52:55]\n\t" "s_waitcnt lgkmcnt(4)\n\t" \
    "v_mfma_f32_16x16x32_bf16 v[56:59], a[128:131], v[104:107], v[56:59]\n\t" "ds_read_b128 v[124:127], v83 offset:1024\n\t" \
    "v_mfma_f32_16x16x32_bf16 v[60:63], a[132:135], v[104:107], v[60:63]\n\t" "s_waitcnt lgkmcnt(4)\n\t" \
    "v_mfma_f32_16x16x32_bf16 v[48:51], a[136:139], v[108:111], v[48:51]\n\t" "ds_read_b128 v[128:131], v83 offset:33792\n\t" "s_waitcnt vmcnt(16)\n\t" "s_barrier\n\t" \
    "v_mfma_f32_16x16x32_bf16 v[52:55], a[140:143], v[108:111], v[52:55]\n\t" "v_mov_b32 v98, s60\n\t" "buffer_store_dword v98, %[fo], %[rc], %[fl] offen sc1\n\t" \
    "s_waitcnt lgkmcnt(4)\n\t" "v_mfma_f32_16x16x32_bf16 v[56:59], a[136:139], v[112:115], v[56:59]\n\t" "ds_read_b128 v[100:103], v84 offset:1024\n\t" \
    "v_mfma_f32_16x16x32_bf16 v[60:63], a[140:143], v[112:115], v[60:63]\n\t" "s_waitcnt lgkmcnt(4)\n\t" \
    "v_mfma_f32_16x16x32_bf16 v[48:51], a[144:147], v[116:119], v[48:51]\n\t" "ds_read_b128 v[104:107], v84 offset:33792\n\t" \
    "v_mfma_f32_16x16x32_bf16 v[52:55], a[148:151], v[116:119], v[52:55]\n\t" "s_waitcnt lgkmcnt(4)\n\t" \
    "v_mfma_f32_16x16x32_bf16 v[56:59], a[144:147], v[120:123], v[56:59]\n\t" "ds_read_b128 v[108:111], v85 offset:1024\n\t" \
    "v_mfma_f32_16x16x32_bf16 v[60:63], a[148:151], v[120:123], v[60:63]\n\t" "s_waitcnt lgkmcnt(4)\n\t" \
    "v_mfma_f32_16x16x32_bf16 v[48:51], a[152:155], v[124:127], v[48:51]\n\t" "ds_read_b128 v[112:115], v85 offset:33792\n\t" \
    "v_mfma_f32_16x16x32_bf16 v[52:55], a[156:159], v[124:127], v[52:55]\n\t" "s_waitcnt lgkmcnt(4)\n\t" \
    "v_mfma_f32_16x16x32_bf16 v[56:59], a[152:155], v[128:131], v[56:59]\n\t" "ds_read_b128 v[116:119], v86 offset:1024\n\t" \
    "v_mfma_f32_16x16x32_bf16 v[60:63], a[156:159], v[128:131], v[60:63]\n\t" "s_waitcnt lgkmcnt(4)\n\t" \
    "v_mfma_f32_16x16x32_bf16 v[48:51], a[160:163], v[100:103], v[48:51]\n\t" "ds_read_b128 v[120:123], v86 offset:33792\n\t" \
    "v_mfma_f32_16x16x32_bf16 v[52:55], a[164:167], v[100:103], v[52:55]\n\t" "s_waitcnt lgkmcnt(4)\n\t" \
    "v_mfma_f32_16x16x32_bf16 v[56:59], a[160:163], v[104:107], v[56:59]\n\t" "ds_read_b128 v[124:127], v87 offset:1024\n\t" \
    "v_mfma_f32_16x16x32_bf16 v[60:63], a[164:167], v[104:107], v[60:63]\n\t" "s_waitcnt lgkmcnt(4)\n\t" \
    "v_mfma_f32_16x16x32_bf16 v[48:51], a[168:171], v[108:111], v[48:51]\n\t" "ds_read_b128 v[128:131], v87 offset:33792\n\t" \
    "v_mfma_f32_16x16x32_bf16 v[52:55], a[172:175], v[108:111], v[52:55]\n\t" "s_waitcnt lgkmcnt(4)\n\t" \
    "v_mfma_f32_16x16x32_bf16 v[56:59], a[168:171], v[112:115], v[56:59]\n\t" "ds_read_b128 v[100:103], v80 offset:1536\n\t" \
    "v_mfma_f32_16x16x32_bf16 v[60:63], a[172:175], v[112:115], v[60:63]\n\t" "s_waitcnt lgkmcnt(4)\n\t" \
    "v_mfma_f32_16x16x32_bf16 v[48:51], a[176:179], v[116:119], v[48:51]\n\t" "ds_read_b128 v[104:107], v80 offset:34304\n\t" \
    "v_mfma_f32_16x16x32_bf16 v[52:55], a[180:183], v[116:119], v[52:55]\n\t" "s_waitcnt lgkmcnt(4)\n\t" \
    "v_mfma_f32_16x16x32_bf16 v[56:59], a[176:179], v[120:123], v[56:59]\n\t" "ds_read_b128 v[108:111], v81 offset:1536\n\t" \
    "v_mfma_f32_16x16x32_bf16 v[60:63], a[180:183], v[120:123], v[60:63]\n\t" "s_waitcnt lgkmcnt(4)\n\t" \
    "v_mfma_f32_16x16x32_bf16 v[48:51], a[184:187], v[124:127], v[48:51]\n\t" "ds_read_b128 v[112:115], v81 offset:34304\n\t" \
    "v_mfma_f32_16x16x32_bf16 v[52:55], a[188:191], v[124:127], v[52:55]\n\t" "s_waitcnt lgkmcnt(4)\n\t" \
    "v_mfma_f32_16x16x32_bf16 v[56:59], a[184:187], v[128:131], v[56:59]\n\t" "ds_read_b128 v[116:119], v82 offset:1536\n\t" \
    "v_mfma_f32_16x16x32_bf16 v[60:63], a[188:191], v[128:131], v[60:63]\n\t" "s_waitcnt lgkmcnt(4)\n\t" \
    "v_mfma_f32_16x16x32_bf16 v[48:51], a[192:195], v[100:103], v[48:51]\n\t" "ds_read_b128 v[120:123], v82 offset:34304\n\t" \
    "v_mfma_f32_16x16x32_bf16 v[52:55], a[196:199], v[100:103], v[52:55]\n\t" "s_waitcnt lgkmcnt(4)\n\t" \
    "v_mfma_f32_16x16x32_bf16 v[56:59], a[192:195], v[104:107], v[56:59]\n\t" "ds_read_b128 v[124:127], v83 offset:1536\n\t" \
    "v_mfma_f32_16x16x32_bf16 v[60:63], a[196:199], v[104:107], v[60:63]\n\t" "s_waitcnt lgkmcnt(4)\n\t" \
    "v_mfma_f32_16x16x32_bf16 v[48:51], a[200:203], v[108:111], v[48:51]\n\t" "ds_read_b128 v[128:131], v83 offset:34304\n\t" \
    "v_mfma_f32_16x16x32_bf16 v[52:55], a[204:207], v[108:111], v[52:55]\n\t" "s_waitcnt lgkmcnt(4)\n\t" \
    "v_mfma_f32_16x16x32_bf16 v[56:59], a[200:203], v[112:115], v[56:59]\n\t" "ds_read_b128 v[100:103], v84 offset:1536\n\t" \
    "v_mfma_f32_16x16x32_bf16 v[60:63], a[204:207], v[112:115], v[60:63]\n\t" "s_waitcnt lgkmcnt(4)\n\t" \
    "v_mfma_f32_16x16x32_bf16 v[48:51], a[208:211], v[116:119], v[48:51]\n\t" "ds_read_b128 v[104:107], v84 offset:34304\n\t" \
    "v_mfma_f32_16x16x32_bf16 v[52:55], a[212:215], v[116:119], v[52:55]\n\t" "s_waitcnt lgkmcnt(4)\n\t" \
    "v_mfma_f32_16x16x32_bf16 v[56:59], a[208:211], v[120:123], v[56:59]\n\t" "ds_read_b128 v[108:111], v85 offset:1536\n\t" \
    "v_mfma_f32_16x16x32_bf16 v[60:63], a[212:215], v[120:123], v[60:63]\n\t" "s_waitcnt lgkmcnt(4)\n\t" \
    "v_mfma_f32_16x16x32_bf16 v[48:51], a[216:219], v[124:127], v[48:51]\n\t" "ds_read_b128 v[112:115], v85 offset:34304\n\t" \
    "v_mfma_f32_16x16x32_bf16 v[52:55], a[220:223], v[124:127], v[52:55]\n\t" "s_waitcnt lgkmcnt(4)\n\t" \
    "v_mfma_f32_16x16x32_bf16 v[56:59], a[216:219], v[128:131], v[56:59]\n\t" "ds_read_b128 v[116:119], v86 offset:1536\n\t" \
    "v_mfma_f32_16x16x32_bf16 v[60:63], a[220:223], v[128:131], v[60:63]\n\t" "s_waitcnt lgkmcnt(4)\n\t" \
    "v_mfma_f32_16x16x32_bf16 v[48:51], a[224:227], v[100:103], v[48:51]\n\t" "ds_read_b128 v[120:123], v86 offset:34304\n\t" \
    "v_mfma_f32_16x16x32_bf16 v[52:55], a[228:231], v[100:103], v[52:55]\n\t" "s_waitcnt lgkmcnt(4)\n\t" \
    "v_mfma_f32_16x16x32_bf16 v[56:59], a[224:227], v[104:107], v[56:59]\n\t" "ds_read_b128 v[124:127], v87 offset:1536\n\t" \
    "v_mfma_f32_16x16x32_bf16 v[60:63], a[228:231], v[104:107], v[60:63]\n\t" "s_waitcnt lgkmcnt(4)\n\t" \
    "v_mfma_f32_16x16x32_bf16 v[48:51], a[232:235], v[108:111], v[48:51]\n\t" "ds_read_b128 v[128:131], v87 offset:34304\n\t" \
    "v_mfma_f32_16x16x32_bf16 v[52:55], a[236:239], v[108:111], v[52:55]\n\t" "s_waitcnt lgkmcnt(4)\n\t" \
    "v_mfma_f32_16x16x32_bf16 v[56:59], a[232:235], v[112:115], v[56:59]\n\t" "v_mfma_f32_16x16x32_bf16 v[60:63], a[236:239], v[112:115], v[60:63]\n\t" \
    "s_waitcnt lgkmcnt(3)\n\t" "v_mfma_f32_16x16x32_bf16 v[48:51], a[240:243], v[116:119], v[48:51]\n\t" \
    "v_mfma_f32_16x16x32_bf16 v[52:55], a[244:247], v[116:119], v[52:55]\n\t" "s_waitcnt vmcnt(1) lgkmcnt(0)\n\t" "s_barrier\n\t" XT(82, 83) \
    "v_mfma_f32_16x16x32_bf16 v[56:59], a[240:243], v[120:123], v[56:59]\n\t" "ds_read_b128 v[100:103], v72 offset:0\n\t" \
    "v_mfma_f32_16x16x32_bf16 v[60:63], a[244:247], v[120:123], v[60:63]\n\t" "v_mfma_f32_16x16x32_bf16 v[48:51], a[248:251], v[124:127], v[48:51]\n\t" \
    "ds_read_b128 v[104:107], v72 offset:32768\n\t" "v_mfma_f32_16x16x32_bf16 v[52:55], a[252:255], v[124:127], v[52:55]\n\t" "ds_read_b128 v[108:111], v73 offset:0\n\t" \
    "v_mfma_f32_16x16x32_bf16 v[56:59], a[248:251], v[128:131], v[56:59]\n\t" "ds_read_b128 v[112:115], v73 offset:32768\n\t" "ds_read_b128 v[116:119], v74 offset:0\n\t" \
    "v_mfma_f32_16x16x32_bf16 v[60:63], a[252:255], v[128:131], v[60:63]\n\t" "s_add_u32 s60, s60, 1\n\t" GSUM "s_cmp_lt_u32 s60, 64\n\t" "s_cbranch_scc1 10b\n\t" \
    "s_nop 7\n\t" "v_cvt_pk_bf16_f32 v64, v48, v49\n\t" "v_cvt_pk_bf16_f32 v65, v50, v51\n\t" "v_cvt_pk_bf16_f32 v66, v52, v53\n\t" "v_cvt_pk_bf16_f32 v67, v54, v55\n\t" \
    "v_cvt_pk_bf16_f32 v68, v56, v57\n\t" "v_cvt_pk_bf16_f32 v69, v58, v59\n\t" "v_cvt_pk_bf16_f32 v70, v60, v61\n\t" "v_cvt_pk_bf16_f32 v71, v62, v63\n\t" \
    "buffer_store_dwordx4 v[64:67], v96, %[rs], s62 offen offset:0 sc1\n\t" "buffer_store_dwordx4 v[68:71], v96, %[rs], s62 offen offset:256 sc1\n\t" "s_waitcnt vmcnt(0)\n\t" \
    "s_barrier\n\t" "v_mov_b32 v98, s60\n\t" "buffer_store_dword v98, %[fo], %[rc], %[fl] offen sc1\n\t" "s_waitcnt lgkmcnt(0)\n\t" "s_branch 30f\n\t" "20:\n\t" \
    "v_mov_b32 v96, %[so]\n\t" "s_nop 7\n\t" "v_cvt_pk_bf16_f32 v64, v32, v33\n\t" "v_cvt_pk_bf16_f32 v65, v34, v35\n\t" "v_cvt_pk_bf16_f32 v66, v36, v37\n\t" \
    "v_cvt_pk_bf16_f32 v67, v38, v39\n\t" "v_cvt_pk_bf16_f32 v68, v40, v41\n\t" "v_cvt_pk_bf16_f32 v69, v42, v43\n\t" "v_cvt_pk_bf16_f32 v70, v44, v45\n\t" \
    "v_cvt_pk_bf16_f32 v71, v46, v47\n\t" "buffer_store_dwordx4 v[64:67], v96, %[rs], s62 offen offset:0 sc1\n\t" \
    "buffer_store_dwordx4 v[68:71], v96, %[rs], s62 offen offset:256 sc1\n\t" "s_waitcnt vmcnt(0)\n\t" "s_barrier\n\t" "v_mov_b32 v98, s60\n\t" \
    "buffer_store_dword v98, %[fo], %[rc], %[fl] offen sc1\n\t" "s_branch 11b\n\t" "30:\n\t"
#define ZC_LOOP \
    ".p2align 6\n\t" "s_mov_b32 s60, 0\n\t" "s_mov_b32 s61, 0\n\t" "s_mov_b32 s62, 0\n\t" "s_mov_b32 s63, 0x7f000000\n\t" "s_cmp_ge_u32 s61, %[z0]\n\t" \
    "s_cbranch_scc1 52f\n\t" "51:\n\t" "buffer_load_dword v120, %[fo], %[rc], %[fv] offen sc1\n\t" "buffer_load_dword v121, %[fo], %[rc], %[vd] offen sc1\n\t" \
    "s_waitcnt vmcnt(0)\n\t" "v_readfirstlane_b32 s61, v120\n\t" "v_readfirstlane_b32 s66, v121\n\t" "s_nop 3\n\t" "s_cmp_ge_u32 s61, %[z0]\n\t" "s_cbranch_scc1 52f\n\t" \
    "s_bitcmp1_b32 s66, 1\n\t" "s_cbranch_scc1 90f\n\t" "s_cmp_eq_u32 %[bd], 0\n\t" "s_cbranch_scc1 52f\n\t" "s_sub_u32 %[bd], %[bd], 1\n\t" "s_sleep 2\n\t" \
    "s_branch 51b\n\t" "52:\n\t" "s_lshr_b32 s66, s60, 2\n\t" "s_xor_b32 s66, s66, %[xp]\n\t" "s_and_b32 s66, s66, 1\n\t" "s_lshl_b32 s66, s66, 13\n\t" \
    "s_lshl_b32 s64, s60, 19\n\t" "s_add_u32 s64, s64, s66\n\t" "s_add_u32 s64, s64, %[vb]\n\t" "buffer_load_dwordx4 v[16:19], %[ao], %[rs], s64 offen offset:0 sc1\n\t" \
    "buffer_load_dwordx4 v[20:23], %[ao], %[rs], s64 offen offset:256 sc1\n\t" "buffer_load_dwordx4 v[24:27], %[ao], %[rs], s64 offen offset:2048 sc1\n\t" \
    "buffer_load_dwordx4 v[28:31], %[ao], %[rs], s64 offen offset:2304 sc1\n\t" "buffer_load_dwordx4 v[32:35], %[a1], %[rs], s64 offen offset:0 sc1\n\t" \
    "buffer_load_dwordx4 v[36:39], %[a1], %[rs], s64 offen offset:256 sc1\n\t" "buffer_load_dwordx4 v[40:43], %[a1], %[rs], s64 offen offset:2048 sc1\n\t" \
    "buffer_load_dwordx4 v[44:47], %[a1], %[rs], s64 offen offset:2304 sc1\n\t" "s_waitcnt vmcnt(0)\n\t" "10:\n\t" XT(80, 81) "s_add_u32 s66, s62, %[rg]\n\t" \
    "s_cmp_lt_u32 s66, s60\n\t" "s_cbranch_scc1 60f\n\t" "63:\n\t" "s_add_u32 s66, s60, 1\n\t" "s_cmp_gt_u32 s61, s66\n\t" "s_cbranch_scc0 40f\n\t" \
    "v_mfma_f32_16x16x32_bf16 v[80:83], v[16:19], a[0:3], 0\n\t" "v_mfma_f32_16x16x32_bf16 v[84:87], v[20:23], a[0:3], 0\n\t" \
    "v_mfma_f32_16x16x32_bf16 v[80:83], v[24:27], a[4:7], v[80:83]\n\t" "v_mfma_f32_16x16x32_bf16 v[84:87], v[28:31], a[4:7], v[84:87]\n\t" \
    "v_mfma_f32_16x16x32_bf16 v[80:83], v[32:35], a[8:11], v[80:83]\n\t" "v_cvt_pk_bf16_f32 v112, v96, v97\n\t" \
    "v_mfma_f32_16x16x32_bf16 v[84:87], v[36:39], a[8:11], v[84:87]\n\t" "v_cvt_pk_bf16_f32 v113, v98, v99\n\t" \
    "v_mfma_f32_16x16x32_bf16 v[80:83], v[40:43], a[12:15], v[80:83]\n\t" "v_cvt_pk_bf16_f32 v114, v100, v101\n\t" \
    "v_mfma_f32_16x16x32_bf16 v[84:87], v[44:47], a[12:15], v[84:87]\n\t" "v_cvt_pk_bf16_f32 v115, v102, v103\n\t" \
    "v_mfma_f32_16x16x32_bf16 v[88:91], v[16:19], a[16:19], 0\n\t" "v_cvt_pk_bf16_f32 v116, v104, v105\n\t" "v_mfma_f32_16x16x32_bf16 v[92:95], v[20:23], a[16:19], 0\n\t" \
    "v_cvt_pk_bf16_f32 v117, v106, v107\n\t" "v_mfma_f32_16x16x32_bf16 v[88:91], v[24:27], a[20:23], v[88:91]\n\t" \
    "buffer_store_dwordx4 v[112:115], %[z1], %[rs], s63 offen sc1\n\t" "v_mfma_f32_16x16x32_bf16 v[92:95], v[28:31], a[20:23], v[92:95]\n\t" \
    "v_mfma_f32_16x16x32_bf16 v[88:91], v[32:35], a[24:27], v[88:91]\n\t" "v_cvt_pk_bf16_f32 v118, v108, v109\n\t" \
    "v_mfma_f32_16x16x32_bf16 v[92:95], v[36:39], a[24:27], v[92:95]\n\t" "v_cvt_pk_bf16_f32 v119, v110, v111\n\t" \
    "v_mfma_f32_16x16x32_bf16 v[88:91], v[40:43], a[28:31], v[88:91]\n\t" "buffer_store_dwordx4 v[116:119], %[z2], %[rs], s63 offen sc1\n\t" "s_and_b32 s63, s60, %[rm]\n\t" \
    "v_mfma_f32_16x16x32_bf16 v[92:95], v[44:47], a[28:31], v[92:95]\n\t" "s_lshl_b32 s63, s63, 22\n\t" "s_add_u32 s63, s63, %[zb]\n\t" \
    "v_mfma_f32_16x16x32_bf16 v[96:99], v[16:19], a[32:35], 0\n\t" "s_add_u32 s65, s60, 1\n\t" "s_lshr_b32 s66, s65, 2\n\t" \
    "v_mfma_f32_16x16x32_bf16 v[100:103], v[20:23], a[32:35], 0\n\t" "s_xor_b32 s66, s66, %[xp]\n\t" "s_and_b32 s66, s66, 1\n\t" \
    "v_mfma_f32_16x16x32_bf16 v[96:99], v[24:27], a[36:39], v[96:99]\n\t" "s_lshl_b32 s66, s66, 13\n\t" "s_lshl_b32 s64, s65, 19\n\t" \
    "v_mfma_f32_16x16x32_bf16 v[100:103], v[28:31], a[36:39], v[100:103]\n\t" "s_add_u32 s64, s64, s66\n\t" "s_add_u32 s64, s64, %[vb]\n\t" \
    "v_mfma_f32_16x16x32_bf16 v[96:99], v[32:35], a[40:43], v[96:99]\n\t" "buffer_load_dwordx4 v[48:51], %[ao], %[rs], s64 offen offset:0 sc1\n\t" \
    "v_cvt_pk_bf16_f32 v112, v80, v81\n\t" "v_mfma_f32_16x16x32_bf16 v[100:103], v[36:39], a[40:43], v[100:103]\n\t" "v_cvt_pk_bf16_f32 v113, v82, v83\n\t" \
    "v_mfma_f32_16x16x32_bf16 v[96:99], v[40:43], a[44:47], v[96:99]\n\t" "buffer_load_dwordx4 v[52:55], %[ao], %[rs], s64 offen offset:256 sc1\n\t" \
    "v_cvt_pk_bf16_f32 v114, v84, v85\n\t" "v_mfma_f32_16x16x32_bf16 v[100:103], v[44:47], a[44:47], v[100:103]\n\t" "v_cvt_pk_bf16_f32 v115, v86, v87\n\t" \
    "v_mfma_f32_16x16x32_bf16 v[104:107], v[16:19], a[48:51], 0\n\t" "buffer_load_dwordx4 v[56:59], %[ao], %[rs], s64 offen offset:2048 sc1\n\t" \
    "v_cvt_pk_bf16_f32 v116, v88, v89\n\t" "v_mfma_f32_16x16x32_bf16 v[108:111], v[20:23], a[48:51], 0\n\t" "v_cvt_pk_bf16_f32 v117, v90, v91\n\t" \
    "v_mfma_f32_16x16x32_bf16 v[104:107], v[24:27], a[52:55], v[104:107]\n\t" "buffer_load_dwordx4 v[60:63], %[ao], %[rs], s64 offen offset:2304 sc1\n\t" \
    "v_mfma_f32_16x16x32_bf16 v[108:111], v[28:31], a[52:55], v[108:111]\n\t" "buffer_store_dwordx4 v[112:115], %[z1], %[rs], s63 offen sc1\n\t" \
    "v_mfma_f32_16x16x32_bf16 v[104:107], v[32:35], a[56:59], v[104:107]\n\t" "buffer_load_dwordx4 v[64:67], %[a1], %[rs], s64 offen offset:0 sc1\n\t" \
    "v_cvt_pk_bf16_f32 v118, v92, v93\n\t" "v_mfma_f32_16x16x32_bf16 v[108:111], v[36:39], a[56:59], v[108:111]\n\t" "v_cvt_pk_bf16_f32 v119, v94, v95\n\t" \
    "v_mfma_f32_16x16x32_bf16 v[104:107], v[40:43], a[60:63], v[104:107]\n\t" "buffer_load_dwordx4 v[68:71], %[a1], %[rs], s64 offen offset:256 sc1\n\t" \
    "buffer_store_dwordx4 v[116:119], %[z2], %[rs], s63 offen sc1\n\t" "v_mfma_f32_16x16x32_bf16 v[108:111], v[44:47], a[60:63], v[108:111]\n\t" \
    "s_add_u32 s63, s63, 0x20000\n\t" "v_mfma_f32_16x16x32_bf16 v[80:83], v[16:19], a[64:67], 0\n\t" \
    "buffer_load_dwordx4 v[72:75], %[a1], %[rs], s64 offen offset:2048 sc1\n\t" "v_mfma_f32_16x16x32_bf16 v[84:87], v[20:23], a[64:67], 0\n\t" \
    "v_mfma_f32_16x16x32_bf16 v[80:83], v[24:27], a[68:71], v[80:83]\n\t" "buffer_load_dwordx4 v[76:79], %[a1], %[rs], s64 offen offset:2304 sc1\n\t" \
    "v_mfma_f32_16x16x32_bf16 v[84:87], v[28:31], a[68:71], v[84:87]\n\t" "v_mfma_f32_16x16x32_bf16 v[80:83], v[32:35], a[72:75], v[80:83]\n\t" \
    "buffer_load_dword v120, %[fo], %[rc], %[fv] offen sc1\n\t" "v_cvt_pk_bf16_f32 v112, v96, v97\n\t" "v_mfma_f32_16x16x32_bf16 v[84:87], v[36:39], a[72:75], v[84:87]\n\t" \
    "v_cvt_pk_bf16_f32 v113, v98, v99\n\t" "v_mfma_f32_16x16x32_bf16 v[80:83], v[40:43], a[76:79], v[80:83]\n\t" "buffer_load_dword v121, %[fo], %[rc], %[fs] offen sc1\n\t" \
    "v_mfma_f32_16x16x32_bf16 v[84:87], v[44:47], a[76:79], v[84:87]\n\t" "v_cvt_pk_bf16_f32 v114, v100, v101\n\t" "v_cvt_pk_bf16_f32 v115, v102, v103\n\t" \
    "v_mfma_f32_16x16x32_bf16 v[88:91], v[16:19], a[80:83], 0\n\t" "v_cvt_pk_bf16_f32 v116, v104, v105\n\t" "v_mfma_f32_16x16x32_bf16 v[92:95], v[20:23], a[80:83], 0\n\t" \
    "v_cvt_pk_bf16_f32 v117, v106, v107\n\t" "v_mfma_f32_16x16x32_bf16 v[88:91], v[24:27], a[84:87], v[88:91]\n\t" \
    "buffer_store_dwordx4 v[112:115], %[z1], %[rs], s63 offen sc1\n\t" "v_mfma_f32_16x16x32_bf16 v[92:95], v[28:31], a[84:87], v[92:95]\n\t" \
    "v_mfma_f32_16x16x32_bf16 v[88:91], v[32:35], a[88:91], v[88:91]\n\t" "v_cvt_pk_bf16_f32 v118, v108, v109\n\t" \
    "v_mfma_f32_16x16x32_bf16 v[92:95], v[36:39], a[88:91], v[92:95]\n\t" "v_cvt_pk_bf16_f32 v119, v110, v111\n\t" \
    "v_mfma_f32_16x16x32_bf16 v[88:91], v[40:43], a[92:95], v[88:91]\n\t" "buffer_store_dwordx4 v[116:119], %[z2], %[rs], s63 offen sc1\n\t" \
    "v_mfma_f32_16x16x32_bf16 v[92:95], v[44:47], a[92:95], v[92:95]\n\t" "s_add_u32 s63, s63, 0x20000\n\t" "v_mfma_f32_16x16x32_bf16 v[96:99], v[16:19], a[96:99], 0\n\t" \
    "v_mfma_f32_16x16x32_bf16 v[100:103], v[20:23], a[96:99], 0\n\t" "v_mfma_f32_16x16x32_bf16 v[96:99], v[24:27], a[100:103], v[96:99]\n\t" \
    "v_mfma_f32_16x16x32_bf16 v[100:103], v[28:31], a[100:103], v[100:103]\n\t" "v_mfma_f32_16x16x32_bf16 v[96:99], v[32:35], a[104:107], v[96:99]\n\t" \
    "v_cvt_pk_bf16_f32 v112, v80, v81\n\t" "v_mfma_f32_16x16x32_bf16 v[100:103], v[36:39], a[104:107], v[100:103]\n\t" "v_cvt_pk_bf16_f32 v113, v82, v83\n\t" \
    "v_mfma_f32_16x16x32_bf16 v[96:99], v[40:43], a[108:111], v[96:99]\n\t" "v_cvt_pk_bf16_f32 v114, v84, v85\n\t" \
    "v_mfma_f32_16x16x32_bf16 v[100:103], v[44:47], a[108:111], v[100:103]\n\t" "v_cvt_pk_bf16_f32 v115, v86, v87\n\t" \
    "v_mfma_f32_16x16x32_bf16 v[104:107], v[16:19], a[112:115], 0\n\t" "v_cvt_pk_bf16_f32 v116, v88, v89\n\t" \
    "v_mfma_f32_16x16x32_bf16 v[108:111], v[20:23], a[112:115], 0\n\t" "v_cvt_pk_bf16_f32 v117, v90, v91\n\t" \
    "v_mfma_f32_16x16x32_bf16 v[104:107], v[24:27], a[116:119], v[104:107]\n\t" "buffer_store_dwordx4 v[112:115], %[z1], %[rs], s63 offen sc1\n\t" \
    "v_mfma_f32_16x16x32_bf16 v[108:111], v[28:31], a[116:119], v[108:111]\n\t" "v_mfma_f32_16x16x32_bf16 v[104:107], v[32:35], a[120:123], v[104:107]\n\t" \
    "v_cvt_pk_bf16_f32 v118, v92, v93\n\t" "v_mfma_f32_16x16x32_bf16 v[108:111], v[36:39], a[120:123], v[108:111]\n\t" "v_cvt_pk_bf16_f32 v119, v94, v95\n\t" \
    "v_mfma_f32_16x16x32_bf16 v[104:107], v[40:43], a[124:127], v[104:107]\n\t" "buffer_store_dwordx4 v[116:119], %[z2], %[rs], s63 offen sc1\n\t" \
    "v_mfma_f32_16x16x32_bf16 v[108:111], v[44:47], a[124:127], v[108:111]\n\t" "s_add_u32 s63, s63, 0x20000\n\t" \
    "v_mfma_f32_16x16x32_bf16 v[80:83], v[16:19], a[128:131], 0\n\t" "v_mfma_f32_16x16x32_bf16 v[84:87], v[20:23], a[128:131], 0\n\t" \
    "v_mfma_f32_16x16x32_bf16 v[80:83], v[24:27], a[132:135], v[80:83]\n\t" "v_mfma_f32_16x16x32_bf16 v[84:87], v[28:31], a[132:135], v[84:87]\n\t" \
    "v_mfma_f32_16x16x32_bf16 v[80:83], v[32:35], a[136:139], v[80:83]\n\t" "v_cvt_pk_bf16_f32 v112, v96, v97\n\t" \
    "v_mfma_f32_16x16x32_bf16 v[84:87], v[36:39], a[136:139], v[84:87]\n\t" "v_cvt_pk_bf16_f32 v113, v98, v99\n\t" \
    "v_mfma_f32_16x16x32_bf16 v[80:83], v[40:43], a[140:143], v[80:83]\n\t" "v_cvt_pk_bf16_f32 v114, v100, v101\n\t" \
    "v_mfma_f32_16x16x32_bf16 v[84:87], v[44:47], a[140:143], v[84:87]\n\t" "v_cvt_pk_bf16_f32 v115, v102, v103\n\t" \
    "v_mfma_f32_16x16x32_bf16 v[88:91], v[16:19], a[144:147], 0\n\t" "v_cvt_pk_bf16_f32 v116, v104, v105\n\t" "v_mfma_f32_16x16x32_bf16 v[92:95], v[20:23], a[144:147], 0\n\t" \
    "v_cvt_pk_bf16_f32 v117, v106, v107\n\t" "v_mfma_f32_16x16x32_bf16 v[88:91], v[24:27], a[148:151], v[88:91]\n\t" \
    "buffer_store_dwordx4 v[112:115], %[z1], %[rs], s63 offen sc1\n\t" "v_mfma_f32_16x16x32_bf16 v[92:95], v[28:31], a[148:151], v[92:95]\n\t" \
    "v_mfma_f32_16x16x32_bf16 v[88:91], v[32:35], a[152:155], v[88:91]\n\t" "v_cvt_pk_bf16_f32 v118, v108, v109\n\t" \
    "v_mfma_f32_16x16x32_bf16 v[92:95], v[36:39], a[152:155], v[92:95]\n\t" "v_cvt_pk_bf16_f32 v119, v110, v111\n\t" \
    "v_mfma_f32_16x16x32_bf16 v[88:91], v[40:43], a[156:159], v[88:91]\n\t" "buffer_store_dwordx4 v[116:119], %[z2], %[rs], s63 offen sc1\n\t" \
    "v_mfma_f32_16x16x32_bf16 v[92:95], v[44:47], a[156:159], v[92:95]\n\t" "s_add_u32 s63, s63, 0x20000\n\t" "v_mfma_f32_16x16x32_bf16 v[96:99], v[16:19], a[160:163], 0\n\t" \
    "v_mfma_f32_16x16x32_bf16 v[100:103], v[20:23], a[160:163], 0\n\t" "v_mfma_f32_16x16x32_bf16 v[96:99], v[24:27], a[164:167], v[96:99]\n\t" \
    "v_mfma_f32_16x16x32_bf16 v[100:103], v[28:31], a[164:167], v[100:103]\n\t" "v_mfma_f32_16x16x32_bf16 v[96:99], v[32:35], a[168:171], v[96:99]\n\t" \
    "v_cvt_pk_bf16_f32 v112, v80, v81\n\t" "v_mfma_f32_16x16x32_bf16 v[100:103], v[36:39], a[168:171], v[100:103]\n\t" "v_cvt_pk_bf16_f32 v113, v82, v83\n\t" \
    "v_mfma_f32_16x16x32_bf16 v[96:99], v[40:43], a[172:175], v[96:99]\n\t" "v_cvt_pk_bf16_f32 v114, v84, v85\n\t" \
    "v_mfma_f32_16x16x32_bf16 v[100:103], v[44:47], a[172:175], v[100:103]\n\t" "v_cvt_pk_bf16_f32 v115, v86, v87\n\t" \
    "v_mfma_f32_16x16x32_bf16 v[104:107], v[16:19], a[176:179], 0\n\t" "v_cvt_pk_bf16_f32 v116, v88, v89\n\t" \
    "v_mfma_f32_16x16x32_bf16 v[108:111], v[20:23], a[176:179], 0\n\t" "v_cvt_pk_bf16_f32 v117, v90, v91\n\t" \
    "v_mfma_f32_16x16x32_bf16 v[104:107], v[24:27], a[180:183], v[104:107]\n\t" "buffer_store_dwordx4 v[112:115], %[z1], %[rs], s63 offen sc1\n\t" \
    "v_mfma_f32_16x16x32_bf16 v[108:111], v[28:31], a[180:183], v[108:111]\n\t" "v_mfma_f32_16x16x32_bf16 v[104:107], v[32:35], a[184:187], v[104:107]\n\t" \
    "v_cvt_pk_bf16_f32 v118, v92, v93\n\t" "v_mfma_f32_16x16x32_bf16 v[108:111], v[36:39], a[184:187], v[108:111]\n\t" "v_cvt_pk_bf16_f32 v119, v94, v95\n\t" \
    "v_mfma_f32_16x16x32_bf16 v[104:107], v[40:43], a[188:191], v[104:107]\n\t" "buffer_store_dwordx4 v[116:119], %[z2], %[rs], s63 offen sc1\n\t" \
    "v_mfma_f32_16x16x32_bf16 v[108:111], v[44:47], a[188:191], v[108:111]\n\t" "s_add_u32 s63, s63, 0x20000\n\t" \
    "v_mfma_f32_16x16x32_bf16 v[80:83], v[16:19], a[192:195], 0\n\t" "v_mfma_f32_16x16x32_bf16 v[84:87], v[20:23], a[192:195], 0\n\t" \
    "v_mfma_f32_16x16x32_bf16 v[80:83], v[24:27], a[196:199], v[80:83]\n\t" "v_mfma_f32_16x16x32_bf16 v[84:87], v[28:31], a[196:199], v[84:87]\n\t" \
    "v_mfma_f32_16x16x32_bf16 v[80:83], v[32:35], a[200:203], v[80:83]\n\t" "v_cvt_pk_bf16_f32 v112, v96, v97\n\t" \
    "v_mfma_f32_16x16x32_bf16 v[84:87], v[36:39], a[200:203], v[84:87]\n\t" "v_cvt_pk_bf16_f32 v113, v98, v99\n\t" \
    "v_mfma_f32_16x16x32_bf16 v[80:83], v[40:43], a[204:207], v[80:83]\n\t" "v_cvt_pk_bf16_f32 v114, v100, v101\n\t" \
    "v_mfma_f32_16x16x32_bf16 v[84:87], v[44:47], a[204:207], v[84:87]\n\t" "v_cvt_pk_bf16_f32 v115, v102, v103\n\t" \
    "v_mfma_f32_16x16x32_bf16 v[88:91], v[16:19], a[208:211], 0\n\t" "v_cvt_pk_bf16_f32 v116, v104, v105\n\t" "v_mfma_f32_16x16x32_bf16 v[92:95], v[20:23], a[208:211], 0\n\t" \
    "v_cvt_pk_bf16_f32 v117, v106, v107\n\t" "v_mfma_f32_16x16x32_bf16 v[88:91], v[24:27], a[212:215], v[88:91]\n\t" \
    "buffer_store_dwordx4 v[112:115], %[z1], %[rs], s63 offen sc1\n\t" "v_mfma_f32_16x16x32_bf16 v[92:95], v[28:31], a[212:215], v[92:95]\n\t" \
    "v_mfma_f32_16x16x32_bf16 v[88:91], v[32:35], a[216:219], v[88:91]\n\t" "v_cvt_pk_bf16_f32 v118, v108, v109\n\t" \
    "v_mfma_f32_16x16x32_bf16 v[92:95], v[36:39], a[216:219], v[92:95]\n\t" "v_cvt_pk_bf16_f32 v119, v110, v111\n\t" \
    "v_mfma_f32_16x16x32_bf16 v[88:91], v[40:43], a[220:223], v[88:91]\n\t" "buffer_store_dwordx4 v[116:119], %[z2], %[rs], s63 offen sc1\n\t" \
    "v_mfma_f32_16x16x32_bf16 v[92:95], v[44:47], a[220:223], v[92:95]\n\t" "s_add_u32 s63, s63, 0x20000\n\t" "v_mfma_f32_16x16x32_bf16 v[96:99], v[16:19], a[224:227], 0\n\t" \
    "v_mfma_f32_16x16x32_bf16 v[100:103], v[20:23], a[224:227], 0\n\t" "v_mfma_f32_16x16x32_bf16 v[96:99], v[24:27], a[228:231], v[96:99]\n\t" \
    "v_mfma_f32_16x16x32_bf16 v[100:103], v[28:31], a[228:231], v[100:103]\n\t" "v_mfma_f32_16x16x32_bf16 v[96:99], v[32:35], a[232:235], v[96:99]\n\t" \
    "v_cvt_pk_bf16_f32 v112, v80, v81\n\t" "v_mfma_f32_16x16x32_bf16 v[100:103], v[36:39], a[232:235], v[100:103]\n\t" "v_cvt_pk_bf16_f32 v113, v82, v83\n\t" \
    "v_mfma_f32_16x16x32_bf16 v[96:99], v[40:43], a[236:239], v[96:99]\n\t" "v_cvt_pk_bf16_f32 v114, v84, v85\n\t" \
    "v_mfma_f32_16x16x32_bf16 v[100:103], v[44:47], a[236:239], v[100:103]\n\t" "v_cvt_pk_bf16_f32 v115, v86, v87\n\t" \
    "v_mfma_f32_16x16x32_bf16 v[104:107], v[16:19], a[240:243], 0\n\t" "v_cvt_pk_bf16_f32 v116, v88, v89\n\t" \
    "v_mfma_f32_16x16x32_bf16 v[108:111], v[20:23], a[240:243], 0\n\t" "v_cvt_pk_bf16_f32 v117, v90, v91\n\t" \
    "v_mfma_f32_16x16x32_bf16 v[104:107], v[24:27], a[244:247], v[104:107]\n\t" "buffer_store_dwordx4 v[112:115], %[z1], %[rs], s63 offen sc1\n\t" \
    "v_mfma_f32_16x16x32_bf16 v[108:111], v[28:31], a[244:247], v[108:111]\n\t" "v_mfma_f32_16x16x32_bf16 v[104:107], v[32:35], a[248:251], v[104:107]\n\t" \
    "v_cvt_pk_bf16_f32 v118, v92, v93\n\t" "v_cvt_pk_bf16_f32 v119, v94, v95\n\t" "s_waitcnt vmcnt(11)\n\t" "v_readfirstlane_b32 s66, v120\n\t" \
    "v_mfma_f32_16x16x32_bf16 v[108:111], v[36:39], a[248:251], v[108:111]\n\t" "v_readfirstlane_b32 s67, v121\n\t" "v_mov_b32 v122, s60\n\t" \
    "buffer_store_dword v122, %[fo], %[rc], %[fz] offen sc1\n\t" "v_mfma_f32_16x16x32_bf16 v[104:107], v[40:43], a[252:255], v[104:107]\n\t" \
    "buffer_store_dwordx4 v[116:119], %[z2], %[rs], s63 offen sc1\n\t" "s_add_u32 s63, s63, 0x20000\n\t" "s_max_u32 s61, s61, s66\n\t" \
    "v_mfma_f32_16x16x32_bf16 v[108:111], v[44:47], a[252:255], v[108:111]\n\t" "s_max_u32 s62, s62, s67\n\t" "s_add_u32 s60, s60, 1\n\t" ZSUM "23:\n\t" XT(80, 81) \
    "s_add_u32 s66, s62, %[rg]\n\t" "s_cmp_lt_u32 s66, s60\n\t" "s_cbranch_scc1 70f\n\t" "73:\n\t" "s_add_u32 s66, s60, 1\n\t" "s_cmp_gt_u32 s61, s66\n\t" \
    "s_cbranch_scc0 41f\n\t" "v_mfma_f32_16x16x32_bf16 v[80:83], v[48:51], a[0:3], 0\n\t" "v_mfma_f32_16x16x32_bf16 v[84:87], v[52:55], a[0:3], 0\n\t" \
    "v_mfma_f32_16x16x32_bf16 v[80:83], v[56:59], a[4:7], v[80:83]\n\t" "v_mfma_f32_16x16x32_bf16 v[84:87], v[60:63], a[4:7], v[84:87]\n\t" \
    "v_mfma_f32_16x16x32_bf16 v[80:83], v[64:67], a[8:11], v[80:83]\n\t" "v_cvt_pk_bf16_f32 v112, v96, v97\n\t" \
    "v_mfma_f32_16x16x32_bf16 v[84:87], v[68:71], a[8:11], v[84:87]\n\t" "v_cvt_pk_bf16_f32 v113, v98, v99\n\t" \
    "v_mfma_f32_16x16x32_bf16 v[80:83], v[72:75], a[12:15], v[80:83]\n\t" "v_cvt_pk_bf16_f32 v114, v100, v101\n\t" \
    "v_mfma_f32_16x16x32_bf16 v[84:87], v[76:79], a[12:15], v[84:87]\n\t" "v_cvt_pk_bf16_f32 v115, v102, v103\n\t" \
    "v_mfma_f32_16x16x32_bf16 v[88:91], v[48:51], a[16:19], 0\n\t" "v_cvt_pk_bf16_f32 v116, v104, v105\n\t" "v_mfma_f32_16x16x32_bf16 v[92:95], v[52:55], a[16:19], 0\n\t" \
    "v_cvt_pk_bf16_f32 v117, v106, v107\n\t" "v_mfma_f32_16x16x32_bf16 v[88:91], v[56:59], a[20:23], v[88:91]\n\t" \
    "buffer_store_dwordx4 v[112:115], %[z1], %[rs], s63 offen sc1\n\t" "v_mfma_f32_16x16x32_bf16 v[92:95], v[60:63], a[20:23], v[92:95]\n\t" \
    "v_mfma_f32_16x16x32_bf16 v[88:91], v[64:67], a[24:27], v[88:91]\n\t" "v_cvt_pk_bf16_f32 v118, v108, v109\n\t" \
    "v_mfma_f32_16x16x32_bf16 v[92:95], v[68:71], a[24:27], v[92:95]\n\t" "v_cvt_pk_bf16_f32 v119, v110, v111\n\t" \
    "v_mfma_f32_16x16x32_bf16 v[88:91], v[72:75], a[28:31], v[88:91]\n\t" "buffer_store_dwordx4 v[116:119], %[z2], %[rs], s63 offen sc1\n\t" "s_and_b32 s63, s60, %[rm]\n\t" \
    "v_mfma_f32_16x16x32_bf16 v[92:95], v[76:79], a[28:31], v[92:95]\n\t" "s_lshl_b32 s63, s63, 22\n\t" "s_add_u32 s63, s63, %[zb]\n\t" \
    "v_mfma_f32_16x16x32_bf16 v[96:99], v[48:51], a[32:35], 0\n\t" "s_add_u32 s65, s60, 1\n\t" "s_lshr_b32 s66, s65, 2\n\t" \
    "v_mfma_f32_16x16x32_bf16 v[100:103], v[52:55], a[32:35], 0\n\t" "s_xor_b32 s66, s66, %[xp]\n\t" "s_and_b32 s66, s66, 1\n\t" \
    "v_mfma_f32_16x16x32_bf16 v[96:99], v[56:59], a[36:39], v[96:99]\n\t" "s_lshl_b32 s66, s66, 13\n\t" "s_lshl_b32 s64, s65, 19\n\t" \
    "v_mfma_f32_16x16x32_bf16 v[100:103], v[60:63], a[36:39], v[100:103]\n\t" "s_add_u32 s64, s64, s66\n\t" "s_add_u32 s64, s64, %[vb]\n\t" \
    "v_mfma_f32_16x16x32_bf16 v[96:99], v[64:67], a[40:43], v[96:99]\n\t" "buffer_load_dwordx4 v[16:19], %[ao], %[rs], s64 offen offset:0 sc1\n\t" \
    "v_cvt_pk_bf16_f32 v112, v80, v81\n\t" "v_mfma_f32_16x16x32_bf16 v[100:103], v[68:71], a[40:43], v[100:103]\n\t" "v_cvt_pk_bf16_f32 v113, v82, v83\n\t" \
    "v_mfma_f32_16x16x32_bf16 v[96:99], v[72:75], a[44:47], v[96:99]\n\t" "buffer_load_dwordx4 v[20:23], %[ao], %[rs], s64 offen offset:256 sc1\n\t" \
    "v_cvt_pk_bf16_f32 v114, v84, v85\n\t" "v_mfma_f32_16x16x32_bf16 v[100:103], v[76:79], a[44:47], v[100:103]\n\t" "v_cvt_pk_bf16_f32 v115, v86, v87\n\t" \
    "v_mfma_f32_16x16x32_bf16 v[104:107], v[48:51], a[48:51], 0\n\t" "buffer_load_dwordx4 v[24:27], %[ao], %[rs], s64 offen offset:2048 sc1\n\t" \
    "v_cvt_pk_bf16_f32 v116, v88, v89\n\t" "v_mfma_f32_16x16x32_bf16 v[108:111], v[52:55], a[48:51], 0\n\t" "v_cvt_pk_bf16_f32 v117, v90, v91\n\t" \
    "v_mfma_f32_16x16x32_bf16 v[104:107], v[56:59], a[52:55], v[104:107]\n\t" "buffer_load_dwordx4 v[28:31], %[ao], %[rs], s64 offen offset:2304 sc1\n\t" \
    "v_mfma_f32_16x16x32_bf16 v[108:111], v[60:63], a[52:55], v[108:111]\n\t" "buffer_store_dwordx4 v[112:115], %[z1], %[rs], s63 offen sc1\n\t" \
    "v_mfma_f32_16x16x32_bf16 v[104:107], v[64:67], a[56:59], v[104:107]\n\t" "buffer_load_dwordx4 v[32:35], %[a1], %[rs], s64 offen offset:0 sc1\n\t" \
    "v_cvt_pk_bf16_f32 v118, v92, v93\n\t" "v_mfma_f32_16x16x32_bf16 v[108:111], v[68:71], a[56:59], v[108:111]\n\t" "v_cvt_pk_bf16_f32 v119, v94, v95\n\t" \
    "v_mfma_f32_16x16x32_bf16 v[104:107], v[72:75], a[60:63], v[104:107]\n\t" "buffer_load_dwordx4 v[36:39], %[a1], %[rs], s64 offen offset:256 sc1\n\t" \
    "buffer_store_dwordx4 v[116:119], %[z2], %[rs], s63 offen sc1\n\t" "v_mfma_f32_16x16x32_bf16 v[108:111], v[76:79], a[60:63], v[108:111]\n\t" \
    "s_add_u32 s63, s63, 0x20000\n\t" "v_mfma_f32_16x16x32_bf16 v[80:83], v[48:51], a[64:67], 0\n\t" \
    "buffer_load_dwordx4 v[40:43], %[a1], %[rs], s64 offen offset:2048 sc1\n\t" "v_mfma_f32_16x16x32_bf16 v[84:87], v[52:55], a[64:67], 0\n\t" \
    "v_mfma_f32_16x16x32_bf16 v[80:83], v[56:59], a[68:71], v[80:83]\n\t" "buffer_load_dwordx4 v[44:47], %[a1], %[rs], s64 offen offset:2304 sc1\n\t" \
    "v_mfma_f32_16x16x32_bf16 v[84:87], v[60:63], a[68:71], v[84:87]\n\t" "v_mfma_f32_16x16x32_bf16 v[80:83], v[64:67], a[72:75], v[80:83]\n\t" \
    "buffer_load_dword v120, %[fo], %[rc], %[fv] offen sc1\n\t" "v_cvt_pk_bf16_f32 v112, v96, v97\n\t" "v_mfma_f32_16x16x32_bf16 v[84:87], v[68:71], a[72:75], v[84:87]\n\t" \
    "v_cvt_pk_bf16_f32 v113, v98, v99\n\t" "v_mfma_f32_16x16x32_bf16 v[80:83], v[72:75], a[76:79], v[80:83]\n\t" "buffer_load_dword v121, %[fo], %[rc], %[fs] offen sc1\n\t" \
    "v_mfma_f32_16x16x32_bf16 v[84:87], v[76:79], a[76:79], v[84:87]\n\t" "v_cvt_pk_bf16_f32 v114, v100, v101\n\t" "v_cvt_pk_bf16_f32 v115, v102, v103\n\t" \
    "v_mfma_f32_16x16x32_bf16 v[88:91], v[48:51], a[80:83], 0\n\t" "v_cvt_pk_bf16_f32 v116, v104, v105\n\t" "v_mfma_f32_16x16x32_bf16 v[92:95], v[52:55], a[80:83], 0\n\t" \
    "v_cvt_pk_bf16_f32 v117, v106, v107\n\t" "v_mfma_f32_16x16x32_bf16 v[88:91], v[56:59], a[84:87], v[88:91]\n\t" \
    "buffer_store_dwordx4 v[112:115], %[z1], %[rs], s63 offen sc1\n\t" "v_mfma_f32_16x16x32_bf16 v[92:95], v[60:63], a[84:87], v[92:95]\n\t" \
    "v_mfma_f32_16x16x32_bf16 v[88:91], v[64:67], a[88:91], v[88:91]\n\t" "v_cvt_pk_bf16_f32 v118, v108, v109\n\t" \
    "v_mfma_f32_16x16x32_bf16 v[92:95], v[68:71], a[88:91], v[92:95]\n\t" "v_cvt_pk_bf16_f32 v119, v110, v111\n\t" \
    "v_mfma_f32_16x16x32_bf16 v[88:91], v[72:75], a[92:95], v[88:91]\n\t" "buffer_store_dwordx4 v[116:119], %[z2], %[rs], s63 offen sc1\n\t" \
    "v_mfma_f32_16x16x32_bf16 v[92:95], v[76:79], a[92:95], v[92:95]\n\t" "s_add_u32 s63, s63, 0x20000\n\t" "v_mfma_f32_16x16x32_bf16 v[96:99], v[48:51], a[96:99], 0\n\t" \
    "v_mfma_f32_16x16x32_bf16 v[100:103], v[52:55], a[96:99], 0\n\t" "v_mfma_f32_16x16x32_bf16 v[96:99], v[56:59], a[100:103], v[96:99]\n\t" \
    "v_mfma_f32_16x16x32_bf16 v[100:103], v[60:63], a[100:103], v[100:103]\n\t" "v_mfma_f32_16x16x32_bf16 v[96:99], v[64:67], a[104:107], v[96:99]\n\t" \
    "v_cvt_pk_bf16_f32 v112, v80, v81\n\t" "v_mfma_f32_16x16x32_bf16 v[100:103], v[68:71], a[104:107], v[100:103]\n\t" "v_cvt_pk_bf16_f32 v113, v82, v83\n\t" \
    "v_mfma_f32_16x16x32_bf16 v[96:99], v[72:75], a[108:111], v[96:99]\n\t" "v_cvt_pk_bf16_f32 v114, v84, v85\n\t" \
    "v_mfma_f32_16x16x32_bf16 v[100:103], v[76:79], a[108:111], v[100:103]\n\t" "v_cvt_pk_bf16_f32 v115, v86, v87\n\t" \
    "v_mfma_f32_16x16x32_bf16 v[104:107], v[48:51], a[112:115], 0\n\t" "v_cvt_pk_bf16_f32 v116, v88, v89\n\t" \
    "v_mfma_f32_16x16x32_bf16 v[108:111], v[52:55], a[112:115], 0\n\t" "v_cvt_pk_bf16_f32 v117, v90, v91\n\t" \
    "v_mfma_f32_16x16x32_bf16 v[104:107], v[56:59], a[116:119], v[104:107]\n\t" "buffer_store_dwordx4 v[112:115], %[z1], %[rs], s63 offen sc1\n\t" \
    "v_mfma_f32_16x16x32_bf16 v[108:111], v[60:63], a[116:119], v[108:111]\n\t" "v_mfma_f32_16x16x32_bf16 v[104:107], v[64:67], a[120:123], v[104:107]\n\t" \
    "v_cvt_pk_bf16_f32 v118, v92, v93\n\t" "v_mfma_f32_16x16x32_bf16 v[108:111], v[68:71], a[120:123], v[108:111]\n\t" "v_cvt_pk_bf16_f32 v119, v94, v95\n\t" \
    "v_mfma_f32_16x16x32_bf16 v[104:107], v[72:75], a[124:127], v[104:107]\n\t" "buffer_store_dwordx4 v[116:119], %[z2], %[rs], s63 offen sc1\n\t" \
    "v_mfma_f32_16x16x32_bf16 v[108:111], v[76:79], a[124:127], v[108:111]\n\t" "s_add_u32 s63, s63, 0x20000\n\t" \
    "v_mfma_f32_16x16x32_bf16 v[80:83], v[48:51], a[128:131], 0\n\t" "v_mfma_f32_16x16x32_bf16 v[84:87], v[52:55], a[128:131], 0\n\t" \
    "v_mfma_f32_16x16x32_bf16 v[80:83], v[56:59], a[132:135], v[80:83]\n\t" "v_mfma_f32_16x16x32_bf16 v[84:87], v[60:63], a[132:135], v[84:87]\n\t" \
    "v_mfma_f32_16x16x32_bf16 v[80:83], v[64:67], a[136:139], v[80:83]\n\t" "v_cvt_pk_bf16_f32 v112, v96, v97\n\t" \
    "v_mfma_f32_16x16x32_bf16 v[84:87], v[68:71], a[136:139], v[84:87]\n\t" "v_cvt_pk_bf16_f32 v113, v98, v99\n\t" \
    "v_mfma_f32_16x16x32_bf16 v[80:83], v[72:75], a[140:143], v[80:83]\n\t" "v_cvt_pk_bf16_f32 v114, v100, v101\n\t" \
    "v_mfma_f32_16x16x32_bf16 v[84:87], v[76:79], a[140:143], v[84:87]\n\t" "v_cvt_pk_bf16_f32 v115, v102, v103\n\t" \
    "v_mfma_f32_16x16x32_bf16 v[88:91], v[48:51], a[144:147], 0\n\t" "v_cvt_pk_bf16_f32 v116, v104, v105\n\t" "v_mfma_f32_16x16x32_bf16 v[92:95], v[52:55], a[144:147], 0\n\t" \
    "v_cvt_pk_bf16_f32 v117, v106, v107\n\t" "v_mfma_f32_16x16x32_bf16 v[88:91], v[56:59], a[148:151], v[88:91]\n\t" \
    "buffer_store_dwordx4 v[112:115], %[z1], %[rs], s63 offen sc1\n\t" "v_mfma_f32_16x16x32_bf16 v[92:95], v[60:63], a[148:151], v[92:95]\n\t" \
    "v_mfma_f32_16x16x32_bf16 v[88:91], v[64:67], a[152:155], v[88:91]\n\t" "v_cvt_pk_bf16_f32 v118, v108, v109\n\t" \
    "v_mfma_f32_16x16x32_bf16 v[92:95], v[68:71], a[152:155], v[92:95]\n\t" "v_cvt_pk_bf16_f32 v119, v110, v111\n\t" \
    "v_mfma_f32_16x16x32_bf16 v[88:91], v[72:75], a[156:159], v[88:91]\n\t" "buffer_store_dwordx4 v[116:119], %[z2], %[rs], s63 offen sc1\n\t" \
    "v_mfma_f32_16x16x32_bf16 v[92:95], v[76:79], a[156:159], v[92:95]\n\t" "s_add_u32 s63, s63, 0x20000\n\t" "v_mfma_f32_16x16x32_bf16 v[96:99], v[48:51], a[160:163], 0\n\t" \
    "v_mfma_f32_16x16x32_bf16 v[100:103], v[52:55], a[160:163], 0\n\t" "v_mfma_f32_16x16x32_bf16 v[96:99], v[56:59], a[164:167], v[96:99]\n\t" \
    "v_mfma_f32_16x16x32_bf16 v[100:103], v[60:63], a[164:167], v[100:103]\n\t" "v_mfma_f32_16x16x32_bf16 v[96:99], v[64:67], a[168:171], v[96:99]\n\t" \
    "v_cvt_pk_bf16_f32 v112, v80, v81\n\t" "v_mfma_f32_16x16x32_bf16 v[100:103], v[68:71], a[168:171], v[100:103]\n\t" "v_cvt_pk_bf16_f32 v113, v82, v83\n\t" \
    "v_mfma_f32_16x16x32_bf16 v[96:99], v[72:75], a[172:175], v[96:99]\n\t" "v_cvt_pk_bf16_f32 v114, v84, v85\n\t" \
    "v_mfma_f32_16x16x32_bf16 v[100:103], v[76:79], a[172:175], v[100:103]\n\t" "v_cvt_pk_bf16_f32 v115, v86, v87\n\t" \
    "v_mfma_f32_16x16x32_bf16 v[104:107], v[48:51], a[176:179], 0\n\t" "v_cvt_pk_bf16_f32 v116, v88, v89\n\t" \
    "v_mfma_f32_16x16x32_bf16 v[108:111], v[52:55], a[176:179], 0\n\t" "v_cvt_pk_bf16_f32 v117, v90, v91\n\t" \
    "v_mfma_f32_16x16x32_bf16 v[104:107], v[56:59], a[180:183], v[104:107]\n\t" "buffer_store_dwordx4 v[112:115], %[z1], %[rs], s63 offen sc1\n\t" \
    "v_mfma_f32_16x16x32_bf16 v[108:111], v[60:63], a[180:183], v[108:111]\n\t" "v_mfma_f32_16x16x32_bf16 v[104:107], v[64:67], a[184:187], v[104:107]\n\t" \
    "v_cvt_pk_bf16_f32 v118, v92, v93\n\t" "v_mfma_f32_16x16x32_bf16 v[108:111], v[68:71], a[184:187], v[108:111]\n\t" "v_cvt_pk_bf16_f32 v119, v94, v95\n\t" \
    "v_mfma_f32_16x16x32_bf16 v[104:107], v[72:75], a[188:191], v[104:107]\n\t" "buffer_store_dwordx4 v[116:119], %[z2], %[rs], s63 offen sc1\n\t" \
    "v_mfma_f32_16x16x32_bf16 v[108:111], v[76:79], a[188:191], v[108:111]\n\t" "s_add_u32 s63, s63, 0x20000\n\t" \
    "v_mfma_f32_16x16x32_bf16 v[80:83], v[48:51], a[192:195], 0\n\t" "v_mfma_f32_16x16x32_bf16 v[84:87], v[52:55], a[192:195], 0\n\t" \
    "v_mfma_f32_16x16x32_bf16 v[80:83], v[56:59], a[196:199], v[80:83]\n\t" "v_mfma_f32_16x16x32_bf16 v[84:87], v[60:63], a[196:199], v[84:87]\n\t" \
    "v_mfma_f32_16x16x32_bf16 v[80:83], v[64:67], a[200:203], v[80:83]\n\t" "v_cvt_pk_bf16_f32 v112, v96, v97\n\t" \
    "v_mfma_f32_16x16x32_bf16 v[84:87], v[68:71], a[200:203], v[84:87]\n\t" "v_cvt_pk_bf16_f32 v113, v98, v99\n\t" \
    "v_mfma_f32_16x16x32_bf16 v[80:83], v[72:75], a[204:207], v[80:83]\n\t" "v_cvt_pk_bf16_f32 v114, v100, v101\n\t" \
    "v_mfma_f32_16x16x32_bf16 v[84:87], v[76:79], a[204:207], v[84:87]\n\t" "v_cvt_pk_bf16_f32 v115, v102, v103\n\t" \
    "v_mfma_f32_16x16x32_bf16 v[88:91], v[48:51], a[208:211], 0\n\t" "v_cvt_pk_bf16_f32 v116, v104, v105\n\t" "v_mfma_f32_16x16x32_bf16 v[92:95], v[52:55], a[208:211], 0\n\t" \
    "v_cvt_pk_bf16_f32 v117, v106, v107\n\t" "v_mfma_f32_16x16x32_bf16 v[88:91], v[56:59], a[212:215], v[88:91]\n\t" \
    "buffer_store_dwordx4 v[112:115], %[z1], %[rs], s63 offen sc1\n\t" "v_mfma_f32_16x16x32_bf16 v[92:95], v[60:63], a[212:215], v[92:95]\n\t" \
    "v_mfma_f32_16x16x32_bf16 v[88:91], v[64:67], a[216:219], v[88:91]\n\t" "v_cvt_pk_bf16_f32 v118, v108, v109\n\t" \
    "v_mfma_f32_16x16x32_bf16 v[92:95], v[68:71], a[216:219], v[92:95]\n\t" "v_cvt_pk_bf16_f32 v119, v110, v111\n\t" \
    "v_mfma_f32_16x16x32_bf16 v[88:91], v[72:75], a[220:223], v[88:91]\n\t" "buffer_store_dwordx4 v[116:119], %[z2], %[rs], s63 offen sc1\n\t" \
    "v_mfma_f32_16x16x32_bf16 v[92:95], v[76:79], a[220:223], v[92:95]\n\t" "s_add_u32 s63, s63, 0x20000\n\t" "v_mfma_f32_16x16x32_bf16 v[96:99], v[48:51], a[224:227], 0\n\t" \
    "v_mfma_f32_16x16x32_bf16 v[100:103], v[52:55], a[224:227], 0\n\t" "v_mfma_f32_16x16x32_bf16 v[96:99], v[56:59], a[228:231], v[96:99]\n\t" \
    "v_mfma_f32_16x16x32_bf16 v[100:103], v[60:63], a[228:231], v[100:103]\n\t" "v_mfma_f32_16x16x32_bf16 v[96:99], v[64:67], a[232:235], v[96:99]\n\t" \
    "v_cvt_pk_bf16_f32 v112, v80, v81\n\t" "v_mfma_f32_16x16x32_bf16 v[100:103], v[68:71], a[232:235], v[100:103]\n\t" "v_cvt_pk_bf16_f32 v113, v82, v83\n\t" \
    "v_mfma_f32_16x16x32_bf16 v[96:99], v[72:75], a[236:239], v[96:99]\n\t" "v_cvt_pk_bf16_f32 v114, v84, v85\n\t" \
    "v_mfma_f32_16x16x32_bf16 v[100:103], v[76:79], a[236:239], v[100:103]\n\t" "v_cvt_pk_bf16_f32 v115, v86, v87\n\t" \
    "v_mfma_f32_16x16x32_bf16 v[104:107], v[48:51], a[240:243], 0\n\t" "v_cvt_pk_bf16_f32 v116, v88, v89\n\t" \
    "v_mfma_f32_16x16x32_bf16 v[108:111], v[52:55], a[240:243], 0\n\t" "v_cvt_pk_bf16_f32 v117, v90, v91\n\t" \
    "v_mfma_f32_16x16x32_bf16 v[104:107], v[56:59], a[244:247], v[104:107]\n\t" "buffer_store_dwordx4 v[112:115], %[z1], %[rs], s63 offen sc1\n\t" \
    "v_mfma_f32_16x16x32_bf16 v[108:111], v[60:63], a[244:247], v[108:111]\n\t" "v_mfma_f32_16x16x32_bf16 v[104:107], v[64:67], a[248:251], v[104:107]\n\t" \
    "v_cvt_pk_bf16_f32 v118, v92, v93\n\t" "v_cvt_pk_bf16_f32 v119, v94, v95\n\t" "s_waitcnt vmcnt(11)\n\t" "v_readfirstlane_b32 s66, v120\n\t" \
    "v_mfma_f32_16x16x32_bf16 v[108:111], v[68:71], a[248:251], v[108:111]\n\t" "v_readfirstlane_b32 s67, v121\n\t" "v_mov_b32 v122, s60\n\t" \
    "buffer_store_dword v122, %[fo], %[rc], %[fz] offen sc1\n\t" "v_mfma_f32_16x16x32_bf16 v[104:107], v[72:75], a[252:255], v[104:107]\n\t" \
    "buffer_store_dwordx4 v[116:119], %[z2], %[rs], s63 offen sc1\n\t" "s_add_u32 s63, s63, 0x20000\n\t" "s_max_u32 s61, s61, s66\n\t" \
    "v_mfma_f32_16x16x32_bf16 v[108:111], v[76:79], a[252:255], v[108:111]\n\t" "s_max_u32 s62, s62, s67\n\t" "s_add_u32 s60, s60, 1\n\t" ZSUM "33:\n\t" "s_branch 10b\n\t" \
    "40:\n\t" "v_mfma_f32_16x16x32_bf16 v[80:83], v[16:19], a[0:3], 0\n\t" "v_mfma_f32_16x16x32_bf16 v[84:87], v[20:23], a[0:3], 0\n\t" \
    "v_mfma_f32_16x16x32_bf16 v[80:83], v[24:27], a[4:7], v[80:83]\n\t" "v_mfma_f32_16x16x32_bf16 v[84:87], v[28:31], a[4:7], v[84:87]\n\t" \
    "v_mfma_f32_16x16x32_bf16 v[80:83], v[32:35], a[8:11], v[80:83]\n\t" "v_cvt_pk_bf16_f32 v112, v96, v97\n\t" \
    "v_mfma_f32_16x16x32_bf16 v[84:87], v[36:39], a[8:11], v[84:87]\n\t" "v_cvt_pk_bf16_f32 v113, v98, v99\n\t" \
    "v_mfma_f32_16x16x32_bf16 v[80:83], v[40:43], a[12:15], v[80:83]\n\t" "v_cvt_pk_bf16_f32 v114, v100, v101\n\t" \
    "v_mfma_f32_16x16x32_bf16 v[84:87], v[44:47], a[12:15], v[84:87]\n\t" "v_cvt_pk_bf16_f32 v115, v102, v103\n\t" \
    "v_mfma_f32_16x16x32_bf16 v[88:91], v[16:19], a[16:19], 0\n\t" "v_cvt_pk_bf16_f32 v116, v104, v105\n\t" "v_mfma_f32_16x16x32_bf16 v[92:95], v[20:23], a[16:19], 0\n\t" \
    "v_cvt_pk_bf16_f32 v117, v106, v107\n\t" "v_mfma_f32_16x16x32_bf16 v[88:91], v[24:27], a[20:23], v[88:91]\n\t" \
    "buffer_store_dwordx4 v[112:115], %[z1], %[rs], s63 offen sc1\n\t" "v_mfma_f32_16x16x32_bf16 v[92:95], v[28:31], a[20:23], v[92:95]\n\t" \
    "v_mfma_f32_16x16x32_bf16 v[88:91], v[32:35], a[24:27], v[88:91]\n\t" "v_cvt_pk_bf16_f32 v118, v108, v109\n\t" \
    "v_mfma_f32_16x16x32_bf16 v[92:95], v[36:39], a[24:27], v[92:95]\n\t" "v_cvt_pk_bf16_f32 v119, v110, v111\n\t" \
    "v_mfma_f32_16x16x32_bf16 v[88:91], v[40:43], a[28:31], v[88:91]\n\t" "buffer_store_dwordx4 v[116:119], %[z2], %[rs], s63 offen sc1\n\t" "s_and_b32 s63, s60, %[rm]\n\t" \
    "v_mfma_f32_16x16x32_bf16 v[92:95], v[44:47], a[28:31], v[92:95]\n\t" "s_lshl_b32 s63, s63, 22\n\t" "s_add_u32 s63, s63, %[zb]\n\t" \
    "v_mfma_f32_16x16x32_bf16 v[96:99], v[16:19], a[32:35], 0\n\t" "buffer_load_dword v120, %[fo], %[rc], %[fv] offen sc1\n\t" \
    "v_mfma_f32_16x16x32_bf16 v[100:103], v[20:23], a[32:35], 0\n\t" "v_mfma_f32_16x16x32_bf16 v[96:99], v[24:27], a[36:39], v[96:99]\n\t" \
    "buffer_load_dword v121, %[fo], %[rc], %[fs] offen sc1\n\t" "v_mfma_f32_16x16x32_bf16 v[100:103], v[28:31], a[36:39], v[100:103]\n\t" \
    "v_mfma_f32_16x16x32_bf16 v[96:99], v[32:35], a[40:43], v[96:99]\n\t" "v_cvt_pk_bf16_f32 v112, v80, v81\n\t" \
    "v_mfma_f32_16x16x32_bf16 v[100:103], v[36:39], a[40:43], v[100:103]\n\t" "v_cvt_pk_bf16_f32 v113, v82, v83\n\t" \
    "v_mfma_f32_16x16x32_bf16 v[96:99], v[40:43], a[44:47], v[96:99]\n\t" "v_cvt_pk_bf16_f32 v114, v84, v85\n\t" \
    "v_mfma_f32_16x16x32_bf16 v[100:103], v[44:47], a[44:47], v[100:103]\n\t" "v_cvt_pk_bf16_f32 v115, v86, v87\n\t" \
    "v_mfma_f32_16x16x32_bf16 v[104:107], v[16:19], a[48:51], 0\n\t" "v_cvt_pk_bf16_f32 v116, v88, v89\n\t" "v_mfma_f32_16x16x32_bf16 v[108:111], v[20:23], a[48:51], 0\n\t" \
    "v_cvt_pk_bf16_f32 v117, v90, v91\n\t" "v_mfma_f32_16x16x32_bf16 v[104:107], v[24:27], a[52:55], v[104:107]\n\t" \
    "buffer_store_dwordx4 v[112:115], %[z1], %[rs], s63 offen sc1\n\t" "v_mfma_f32_16x16x32_bf16 v[108:111], v[28:31], a[52:55], v[108:111]\n\t" \
    "v_mfma_f32_16x16x32_bf16 v[104:107], v[32:35], a[56:59], v[104:107]\n\t" "v_cvt_pk_bf16_f32 v118, v92, v93\n\t" \
    "v_mfma_f32_16x16x32_bf16 v[108:111], v[36:39], a[56:59], v[108:111]\n\t" "v_cvt_pk_bf16_f32 v119, v94, v95\n\t" \
    "v_mfma_f32_16x16x32_bf16 v[104:107], v[40:43], a[60:63], v[104:107]\n\t" "buffer_store_dwordx4 v[116:119], %[z2], %[rs], s63 offen sc1\n\t" \
    "v_mfma_f32_16x16x32_bf16 v[108:111], v[44:47], a[60:63], v[108:111]\n\t" "s_add_u32 s63, s63, 0x20000\n\t" "v_mfma_f32_16x16x32_bf16 v[80:83], v[16:19], a[64:67], 0\n\t" \
    "s_add_u32 s65, s60, 1\n\t" "s_lshr_b32 s66, s65, 2\n\t" "v_mfma_f32_16x16x32_bf16 v[84:87], v[20:23], a[64:67], 0\n\t" "s_xor_b32 s66, s66, %[xp]\n\t" \
    "s_and_b32 s66, s66, 1\n\t" "v_mfma_f32_16x16x32_bf16 v[80:83], v[24:27], a[68:71], v[80:83]\n\t" "v_mfma_f32_16x16x32_bf16 v[84:87], v[28:31], a[68:71], v[84:87]\n\t" \
    "v_mfma_f32_16x16x32_bf16 v[80:83], v[32:35], a[72:75], v[80:83]\n\t" "v_cvt_pk_bf16_f32 v112, v96, v97\n\t" \
    "v_mfma_f32_16x16x32_bf16 v[84:87], v[36:39], a[72:75], v[84:87]\n\t" "v_cvt_pk_bf16_f32 v113, v98, v99\n\t" \
    "v_mfma_f32_16x16x32_bf16 v[80:83], v[40:43], a[76:79], v[80:83]\n\t" "v_cvt_pk_bf16_f32 v114, v100, v101\n\t" \
    "v_mfma_f32_16x16x32_bf16 v[84:87], v[44:47], a[76:79], v[84:87]\n\t" "v_cvt_pk_bf16_f32 v115, v102, v103\n\t" \
    "v_mfma_f32_16x16x32_bf16 v[88:91], v[16:19], a[80:83], 0\n\t" "v_cvt_pk_bf16_f32 v116, v104, v105\n\t" "v_mfma_f32_16x16x32_bf16 v[92:95], v[20:23], a[80:83], 0\n\t" \
    "v_cvt_pk_bf16_f32 v117, v106, v107\n\t" "v_mfma_f32_16x16x32_bf16 v[88:91], v[24:27], a[84:87], v[88:91]\n\t" \
    "buffer_store_dwordx4 v[112:115], %[z1], %[rs], s63 offen sc1\n\t" "v_mfma_f32_16x16x32_bf16 v[92:95], v[28:31], a[84:87], v[92:95]\n\t" \
    "v_mfma_f32_16x16x32_bf16 v[88:91], v[32:35], a[88:91], v[88:91]\n\t" "v_cvt_pk_bf16_f32 v118, v108, v109\n\t" \
    "v_mfma_f32_16x16x32_bf16 v[92:95], v[36:39], a[88:91], v[92:95]\n\t" "v_cvt_pk_bf16_f32 v119, v110, v111\n\t" \
    "v_mfma_f32_16x16x32_bf16 v[88:91], v[40:43], a[92:95], v[88:91]\n\t" "buffer_store_dwordx4 v[116:119], %[z2], %[rs], s63 offen sc1\n\t" \
    "v_mfma_f32_16x16x32_bf16 v[92:95], v[44:47], a[92:95], v[92:95]\n\t" "s_add_u32 s63, s63, 0x20000\n\t" "v_mfma_f32_16x16x32_bf16 v[96:99], v[16:19], a[96:99], 0\n\t" \
    "s_lshl_b32 s66, s66, 13\n\t" "s_lshl_b32 s64, s65, 19\n\t" "v_mfma_f32_16x16x32_bf16 v[100:103], v[20:23], a[96:99], 0\n\t" "s_add_u32 s64, s64, s66\n\t" \
    "s_add_u32 s64, s64, %[vb]\n\t" "v_mfma_f32_16x16x32_bf16 v[96:99], v[24:27], a[100:103], v[96:99]\n\t" \
    "v_mfma_f32_16x16x32_bf16 v[100:103], v[28:31], a[100:103], v[100:103]\n\t" "v_mfma_f32_16x16x32_bf16 v[96:99], v[32:35], a[104:107], v[96:99]\n\t" \
    "v_cvt_pk_bf16_f32 v112, v80, v81\n\t" "v_mfma_f32_16x16x32_bf16 v[100:103], v[36:39], a[104:107], v[100:103]\n\t" "v_cvt_pk_bf16_f32 v113, v82, v83\n\t" \
    "v_mfma_f32_16x16x32_bf16 v[96:99], v[40:43], a[108:111], v[96:99]\n\t" "v_cvt_pk_bf16_f32 v114, v84, v85\n\t" \
    "v_mfma_f32_16x16x32_bf16 v[100:103], v[44:47], a[108:111], v[100:103]\n\t" "v_cvt_pk_bf16_f32 v115, v86, v87\n\t" \
    "v_mfma_f32_16x16x32_bf16 v[104:107], v[16:19], a[112:115], 0\n\t" "v_cvt_pk_bf16_f32 v116, v88, v89\n\t" \
    "v_mfma_f32_16x16x32_bf16 v[108:111], v[20:23], a[112:115], 0\n\t" "v_cvt_pk_bf16_f32 v117, v90, v91\n\t" \
    "v_mfma_f32_16x16x32_bf16 v[104:107], v[24:27], a[116:119], v[104:107]\n\t" "buffer_store_dwordx4 v[112:115], %[z1], %[rs], s63 offen sc1\n\t" \
    "v_mfma_f32_16x16x32_bf16 v[108:111], v[28:31], a[116:119], v[108:111]\n\t" "v_mfma_f32_16x16x32_bf16 v[104:107], v[32:35], a[120:123], v[104:107]\n\t" \
    "v_cvt_pk_bf16_f32 v118, v92, v93\n\t" "v_mfma_f32_16x16x32_bf16 v[108:111], v[36:39], a[120:123], v[108:111]\n\t" "v_cvt_pk_bf16_f32 v119, v94, v95\n\t" \
    "v_mfma_f32_16x16x32_bf16 v[104:107], v[40:43], a[124:127], v[104:107]\n\t" "buffer_store_dwordx4 v[116:119], %[z2], %[rs], s63 offen sc1\n\t" \
    "v_mfma_f32_16x16x32_bf16 v[108:111], v[44:47], a[124:127], v[108:111]\n\t" "s_add_u32 s63, s63, 0x20000\n\t" \
    "v_mfma_f32_16x16x32_bf16 v[80:83], v[16:19], a[128:131], 0\n\t" "s_waitcnt vmcnt(6)\n\t" "v_readfirstlane_b32 s66, v120\n\t" "v_readfirstlane_b32 s67, v121\n\t" \
    "v_mfma_f32_16x16x32_bf16 v[84:87], v[20:23], a[128:131], 0\n\t" "v_mov_b32 v122, s60\n\t" "buffer_store_dword v122, %[fo], %[rc], %[fz] offen sc1\n\t" \
    "v_mfma_f32_16x16x32_bf16 v[80:83], v[24:27], a[132:135], v[80:83]\n\t" "s_max_u32 s61, s61, s66\n\t" "s_max_u32 s62, s62, s67\n\t" "s_cmp_gt_u32 s61, s65\n\t" \
    "v_mfma_f32_16x16x32_bf16 v[84:87], v[28:31], a[132:135], v[84:87]\n\t" "s_cselect_b32 s65, 1, 0\n\t" "s_cselect_b32 s64, s64, 0x7f000000\n\t" \
    "v_mfma_f32_16x16x32_bf16 v[80:83], v[32:35], a[136:139], v[80:83]\n\t" "buffer_load_dwordx4 v[48:51], %[ao], %[rs], s64 offen offset:0 sc1\n\t" \
    "v_cvt_pk_bf16_f32 v112, v96, v97\n\t" "v_mfma_f32_16x16x32_bf16 v[84:87], v[36:39], a[136:139], v[84:87]\n\t" "v_cvt_pk_bf16_f32 v113, v98, v99\n\t" \
    "v_mfma_f32_16x16x32_bf16 v[80:83], v[40:43], a[140:143], v[80:83]\n\t" "buffer_load_dwordx4 v[52:55], %[ao], %[rs], s64 offen offset:256 sc1\n\t" \
    "v_cvt_pk_bf16_f32 v114, v100, v101\n\t" "v_mfma_f32_16x16x32_bf16 v[84:87], v[44:47], a[140:143], v[84:87]\n\t" "v_cvt_pk_bf16_f32 v115, v102, v103\n\t" \
    "v_mfma_f32_16x16x32_bf16 v[88:91], v[16:19], a[144:147], 0\n\t" "buffer_load_dwordx4 v[56:59], %[ao], %[rs], s64 offen offset:2048 sc1\n\t" \
    "v_cvt_pk_bf16_f32 v116, v104, v105\n\t" "v_mfma_f32_16x16x32_bf16 v[92:95], v[20:23], a[144:147], 0\n\t" "v_cvt_pk_bf16_f32 v117, v106, v107\n\t" \
    "v_mfma_f32_16x16x32_bf16 v[88:91], v[24:27], a[148:151], v[88:91]\n\t" "buffer_load_dwordx4 v[60:63], %[ao], %[rs], s64 offen offset:2304 sc1\n\t" \
    "v_mfma_f32_16x16x32_bf16 v[92:95], v[28:31], a[148:151], v[92:95]\n\t" "buffer_store_dwordx4 v[112:115], %[z1], %[rs], s63 offen sc1\n\t" \
    "v_mfma_f32_16x16x32_bf16 v[88:91], v[32:35], a[152:155], v[88:91]\n\t" "buffer_load_dwordx4 v[64:67], %[a1], %[rs], s64 offen offset:0 sc1\n\t" \
    "v_cvt_pk_bf16_f32 v118, v108, v109\n\t" "v_mfma_f32_16x16x32_bf16 v[92:95], v[36:39], a[152:155], v[92:95]\n\t" "v_cvt_pk_bf16_f32 v119, v110, v111\n\t" \
    "v_mfma_f32_16x16x32_bf16 v[88:91], v[40:43], a[156:159], v[88:91]\n\t" "buffer_load_dwordx4 v[68:71], %[a1], %[rs], s64 offen offset:256 sc1\n\t" \
    "buffer_store_dwordx4 v[116:119], %[z2], %[rs], s63 offen sc1\n\t" "v_mfma_f32_16x16x32_bf16 v[92:95], v[44:47], a[156:159], v[92:95]\n\t" \
    "s_add_u32 s63, s63, 0x20000\n\t" "v_mfma_f32_16x16x32_bf16 v[96:99], v[16:19], a[160:163], 0\n\t" \
    "buffer_load_dwordx4 v[72:75], %[a1], %[rs], s64 offen offset:2048 sc1\n\t" "v_mfma_f32_16x16x32_bf16 v[100:103], v[20:23], a[160:163], 0\n\t" \
    "v_mfma_f32_16x16x32_bf16 v[96:99], v[24:27], a[164:167], v[96:99]\n\t" "buffer_load_dwordx4 v[76:79], %[a1], %[rs], s64 offen offset:2304 sc1\n\t" \
    "v_mfma_f32_16x16x32_bf16 v[100:103], v[28:31], a[164:167], v[100:103]\n\t" "v_mfma_f32_16x16x32_bf16 v[96:99], v[32:35], a[168:171], v[96:99]\n\t" \
    "v_cvt_pk_bf16_f32 v112, v80, v81\n\t" "v_mfma_f32_16x16x32_bf16 v[100:103], v[36:39], a[168:171], v[100:103]\n\t" "v_cvt_pk_bf16_f32 v113, v82, v83\n\t" \
    "v_mfma_f32_16x16x32_bf16 v[96:99], v[40:43], a[172:175], v[96:99]\n\t" "v_cvt_pk_bf16_f32 v114, v84, v85\n\t" \
    "v_mfma_f32_16x16x32_bf16 v[100:103], v[44:47], a[172:175], v[100:103]\n\t" "v_cvt_pk_bf16_f32 v115, v86, v87\n\t" \
    "v_mfma_f32_16x16x32_bf16 v[104:107], v[16:19], a[176:179], 0\n\t" "v_cvt_pk_bf16_f32 v116, v88, v89\n\t" \
    "v_mfma_f32_16x16x32_bf16 v[108:111], v[20:23], a[176:179], 0\n\t" "v_cvt_pk_bf16_f32 v117, v90, v91\n\t" \
    "v_mfma_f32_16x16x32_bf16 v[104:107], v[24:27], a[180:183], v[104:107]\n\t" "buffer_store_dwordx4 v[112:115], %[z1], %[rs], s63 offen sc1\n\t" \
    "v_mfma_f32_16x16x32_bf16 v[108:111], v[28:31], a[180:183], v[108:111]\n\t" "v_mfma_f32_16x16x32_bf16 v[104:107], v[32:35], a[184:187], v[104:107]\n\t" \
    "v_cvt_pk_bf16_f32 v118, v92, v93\n\t" "v_mfma_f32_16x16x32_bf16 v[108:111], v[36:39], a[184:187], v[108:111]\n\t" "v_cvt_pk_bf16_f32 v119, v94, v95\n\t" \
    "v_mfma_f32_16x16x32_bf16 v[104:107], v[40:43], a[188:191], v[104:107]\n\t" "buffer_store_dwordx4 v[116:119], %[z2], %[rs], s63 offen sc1\n\t" \
    "v_mfma_f32_16x16x32_bf16 v[108:111], v[44:47], a[188:191], v[108:111]\n\t" "s_add_u32 s63, s63, 0x20000\n\t" \
    "v_mfma_f32_16x16x32_bf16 v[80:83], v[16:19], a[192:195], 0\n\t" "v_mfma_f32_16x16x32_bf16 v[84:87], v[20:23], a[192:195], 0\n\t" \
    "v_mfma_f32_16x16x32_bf16 v[80:83], v[24:27], a[196:199], v[80:83]\n\t" "v_mfma_f32_16x16x32_bf16 v[84:87], v[28:31], a[196:199], v[84:87]\n\t" \
    "v_mfma_f32_16x16x32_bf16 v[80:83], v[32:35], a[200:203], v[80:83]\n\t" "v_cvt_pk_bf16_f32 v112, v96, v97\n\t" \
    "v_mfma_f32_16x16x32_bf16 v[84:87], v[36:39], a[200:203], v[84:87]\n\t" "v_cvt_pk_bf16_f32 v113, v98, v99\n\t" \
    "v_mfma_f32_16x16x32_bf16 v[80:83], v[40:43], a[204:207], v[80:83]\n\t" "v_cvt_pk_bf16_f32 v114, v100, v101\n\t" \
    "v_mfma_f32_16x16x32_bf16 v[84:87], v[44:47], a[204:207], v[84:87]\n\t" "v_cvt_pk_bf16_f32 v115, v102, v103\n\t" \
    "v_mfma_f32_16x16x32_bf16 v[88:91], v[16:19], a[208:211], 0\n\t" "v_cvt_pk_bf16_f32 v116, v104, v105\n\t" "v_mfma_f32_16x16x32_bf16 v[92:95], v[20:23], a[208:211], 0\n\t" \
    "v_cvt_pk_bf16_f32 v117, v106, v107\n\t" "v_mfma_f32_16x16x32_bf16 v[88:91], v[24:27], a[212:215], v[88:91]\n\t" \
    "buffer_store_dwordx4 v[112:115], %[z1], %[rs], s63 offen sc1\n\t" "v_mfma_f32_16x16x32_bf16 v[92:95], v[28:31], a[212:215], v[92:95]\n\t" \
    "v_mfma_f32_16x16x32_bf16 v[88:91], v[32:35], a[216:219], v[88:91]\n\t" "v_cvt_pk_bf16_f32 v118, v108, v109\n\t" \
    "v_mfma_f32_16x16x32_bf16 v[92:95], v[36:39], a[216:219], v[92:95]\n\t" "v_cvt_pk_bf16_f32 v119, v110, v111\n\t" \
    "v_mfma_f32_16x16x32_bf16 v[88:91], v[40:43], a[220:223], v[88:91]\n\t" "buffer_store_dwordx4 v[116:119], %[z2], %[rs], s63 offen sc1\n\t" \
    "v_mfma_f32_16x16x32_bf16 v[92:95], v[44:47], a[220:223], v[92:95]\n\t" "s_add_u32 s63, s63, 0x20000\n\t" "v_mfma_f32_16x16x32_bf16 v[96:99], v[16:19], a[224:227], 0\n\t" \
    "v_mfma_f32_16x16x32_bf16 v[100:103], v[20:23], a[224:227], 0\n\t" "v_mfma_f32_16x16x32_bf16 v[96:99], v[24:27], a[228:231], v[96:99]\n\t" \
    "v_mfma_f32_16x16x32_bf16 v[100:103], v[28:31], a[228:231], v[100:103]\n\t" "v_mfma_f32_16x16x32_bf16 v[96:99], v[32:35], a[232:235], v[96:99]\n\t" \
    "v_cvt_pk_bf16_f32 v112, v80, v81\n\t" "v_mfma_f32_16x16x32_bf16 v[100:103], v[36:39], a[232:235], v[100:103]\n\t" "v_cvt_pk_bf16_f32 v113, v82, v83\n\t" \
    "v_mfma_f32_16x16x32_bf16 v[96:99], v[40:43], a[236:239], v[96:99]\n\t" "v_cvt_pk_bf16_f32 v114, v84, v85\n\t" \
    "v_mfma_f32_16x16x32_bf16 v[100:103], v[44:47], a[236:239], v[100:103]\n\t" "v_cvt_pk_bf16_f32 v115, v86, v87\n\t" \
    "v_mfma_f32_16x16x32_bf16 v[104:107], v[16:19], a[240:243], 0\n\t" "v_cvt_pk_bf16_f32 v116, v88, v89\n\t" \
    "v_mfma_f32_16x16x32_bf16 v[108:111], v[20:23], a[240:243], 0\n\t" "v_cvt_pk_bf16_f32 v117, v90, v91\n\t" \
    "v_mfma_f32_16x16x32_bf16 v[104:107], v[24:27], a[244:247], v[104:107]\n\t" "buffer_store_dwordx4 v[112:115], %[z1], %[rs], s63 offen sc1\n\t" \
    "v_mfma_f32_16x16x32_bf16 v[108:111], v[28:31], a[244:247], v[108:111]\n\t" "v_mfma_f32_16x16x32_bf16 v[104:107], v[32:35], a[248:251], v[104:107]\n\t" \
    "v_cvt_pk_bf16_f32 v118, v92, v93\n\t" "v_mfma_f32_16x16x32_bf16 v[108:111], v[36:39], a[248:251], v[108:111]\n\t" "v_cvt_pk_bf16_f32 v119, v94, v95\n\t" \
    "v_mfma_f32_16x16x32_bf16 v[104:107], v[40:43], a[252:255], v[104:107]\n\t" "buffer_store_dwordx4 v[116:119], %[z2], %[rs], s63 offen sc1\n\t" \
    "s_add_u32 s63, s63, 0x20000\n\t" "s_add_u32 s60, s60, 1\n\t" "v_mfma_f32_16x16x32_bf16 v[108:111], v[44:47], a[252:255], v[108:111]\n\t" "s_cmp_eq_u32 s65, 0\n\t" \
    "s_waitcnt vmcnt(6)\n\t" ZSUM "s_cbranch_scc0 23b\n\t" "s_branch 20f\n\t" "41:\n\t" "v_mfma_f32_16x16x32_bf16 v[80:83], v[48:51], a[0:3], 0\n\t" \
    "v_mfma_f32_16x16x32_bf16 v[84:87], v[52:55], a[0:3], 0\n\t" "v_mfma_f32_16x16x32_bf16 v[80:83], v[56:59], a[4:7], v[80:83]\n\t" \
    "v_mfma_f32_16x16x32_bf16 v[84:87], v[60:63], a[4:7], v[84:87]\n\t" "v_mfma_f32_16x16x32_bf16 v[80:83], v[64:67], a[8:11], v[80:83]\n\t" \
    "v_cvt_pk_bf16_f32 v112, v96, v97\n\t" "v_mfma_f32_16x16x32_bf16 v[84:87], v[68:71], a[8:11], v[84:87]\n\t" "v_cvt_pk_bf16_f32 v113, v98, v99\n\t" \
    "v_mfma_f32_16x16x32_bf16 v[80:83], v[72:75], a[12:15], v[80:83]\n\t" "v_cvt_pk_bf16_f32 v114, v100, v101\n\t" \
    "v_mfma_f32_16x16x32_bf16 v[84:87], v[76:79], a[12:15], v[84:87]\n\t" "v_cvt_pk_bf16_f32 v115, v102, v103\n\t" \
    "v_mfma_f32_16x16x32_bf16 v[88:91], v[48:51], a[16:19], 0\n\t" "v_cvt_pk_bf16_f32 v116, v104, v105\n\t" "v_mfma_f32_16x16x32_bf16 v[92:95], v[52:55], a[16:19], 0\n\t" \
    "v_cvt_pk_bf16_f32 v117, v106, v107\n\t" "v_mfma_f32_16x16x32_bf16 v[88:91], v[56:59], a[20:23], v[88:91]\n\t" \
    "buffer_store_dwordx4 v[112:115], %[z1], %[rs], s63 offen sc1\n\t" "v_mfma_f32_16x16x32_bf16 v[92:95], v[60:63], a[20:23], v[92:95]\n\t" \
    "v_mfma_f32_16x16x32_bf16 v[88:91], v[64:67], a[24:27], v[88:91]\n\t" "v_cvt_pk_bf16_f32 v118, v108, v109\n\t" \
    "v_mfma_f32_16x16x32_bf16 v[92:95], v[68:71], a[24:27], v[92:95]\n\t" "v_cvt_pk_bf16_f32 v119, v110, v111\n\t" \
    "v_mfma_f32_16x16x32_bf16 v[88:91], v[72:75], a[28:31], v[88:91]\n\t" "buffer_store_dwordx4 v[116:119], %[z2], %[rs], s63 offen sc1\n\t" "s_and_b32 s63, s60, %[rm]\n\t" \
    "v_mfma_f32_16x16x32_bf16 v[92:95], v[76:79], a[28:31], v[92:95]\n\t" "s_lshl_b32 s63, s63, 22\n\t" "s_add_u32 s63, s63, %[zb]\n\t" \
    "v_mfma_f32_16x16x32_bf16 v[96:99], v[48:51], a[32:35], 0\n\t" "buffer_load_dword v120, %[fo], %[rc], %[fv] offen sc1\n\t" \
    "v_mfma_f32_16x16x32_bf16 v[100:103], v[52:55], a[32:35], 0\n\t" "v_mfma_f32_16x16x32_bf16 v[96:99], v[56:59], a[36:39], v[96:99]\n\t" \
    "buffer_load_dword v121, %[fo], %[rc], %[fs] offen sc1\n\t" "v_mfma_f32_16x16x32_bf16 v[100:103], v[60:63], a[36:39], v[100:103]\n\t" \
    "v_mfma_f32_16x16x32_bf16 v[96:99], v[64:67], a[40:43], v[96:99]\n\t" "v_cvt_pk_bf16_f32 v112, v80, v81\n\t" \
    "v_mfma_f32_16x16x32_bf16 v[100:103], v[68:71], a[40:43], v[100:103]\n\t" "v_cvt_pk_bf16_f32 v113, v82, v83\n\t" \
    "v_mfma_f32_16x16x32_bf16 v[96:99], v[72:75], a[44:47], v[96:99]\n\t" "v_cvt_pk_bf16_f32 v114, v84, v85\n\t" \
    "v_mfma_f32_16x16x32_bf16 v[100:103], v[76:79], a[44:47], v[100:103]\n\t" "v_cvt_pk_bf16_f32 v115, v86, v87\n\t" \
    "v_mfma_f32_16x16x32_bf16 v[104:107], v[48:51], a[48:51], 0\n\t" "v_cvt_pk_bf16_f32 v116, v88, v89\n\t" "v_mfma_f32_16x16x32_bf16 v[108:111], v[52:55], a[48:51], 0\n\t" \
    "v_cvt_pk_bf16_f32 v117, v90, v91\n\t" "v_mfma_f32_16x16x32_bf16 v[104:107], v[56:59], a[52:55], v[104:107]\n\t" \
    "buffer_store_dwordx4 v[112:115], %[z1], %[rs], s63 offen sc1\n\t" "v_mfma_f32_16x16x32_bf16 v[108:111], v[60:63], a[52:55], v[108:111]\n\t" \
    "v_mfma_f32_16x16x32_bf16 v[104:107], v[64:67], a[56:59], v[104:107]\n\t" "v_cvt_pk_bf16_f32 v118, v92, v93\n\t" \
    "v_mfma_f32_16x16x32_bf16 v[108:111], v[68:71], a[56:59], v[108:111]\n\t" "v_cvt_pk_bf16_f32 v119, v94, v95\n\t" \
    "v_mfma_f32_16x16x32_bf16 v[104:107], v[72:75], a[60:63], v[104:107]\n\t" "buffer_store_dwordx4 v[116:119], %[z2], %[rs], s63 offen sc1\n\t" \
    "v_mfma_f32_16x16x32_bf16 v[108:111], v[76:79], a[60:63], v[108:111]\n\t" "s_add_u32 s63, s63, 0x20000\n\t" "v_mfma_f32_16x16x32_bf16 v[80:83], v[48:51], a[64:67], 0\n\t" \
    "s_add_u32 s65, s60, 1\n\t" "s_lshr_b32 s66, s65, 2\n\t" "v_mfma_f32_16x16x32_bf16 v[84:87], v[52:55], a[64:67], 0\n\t" "s_xor_b32 s66, s66, %[xp]\n\t" \
    "s_and_b32 s66, s66, 1\n\t" "v_mfma_f32_16x16x32_bf16 v[80:83], v[56:59], a[68:71], v[80:83]\n\t" "v_mfma_f32_16x16x32_bf16 v[84:87], v[60:63], a[68:71], v[84:87]\n\t" \
    "v_mfma_f32_16x16x32_bf16 v[80:83], v[64:67], a[72:75], v[80:83]\n\t" "v_cvt_pk_bf16_f32 v112, v96, v97\n\t" \
    "v_mfma_f32_16x16x32_bf16 v[84:87], v[68:71], a[72:75], v[84:87]\n\t" "v_cvt_pk_bf16_f32 v113, v98, v99\n\t" \
    "v_mfma_f32_16x16x32_bf16 v[80:83], v[72:75], a[76:79], v[80:83]\n\t" "v_cvt_pk_bf16_f32 v114, v100, v101\n\t" \
    "v_mfma_f32_16x16x32_bf16 v[84:87], v[76:79], a[76:79], v[84:87]\n\t" "v_cvt_pk_bf16_f32 v115, v102, v103\n\t" \
    "v_mfma_f32_16x16x32_bf16 v[88:91], v[48:51], a[80:83], 0\n\t" "v_cvt_pk_bf16_f32 v116, v104, v105\n\t" "v_mfma_f32_16x16x32_bf16 v[92:95], v[52:55], a[80:83], 0\n\t" \
    "v_cvt_pk_bf16_f32 v117, v106, v107\n\t" "v_mfma_f32_16x16x32_bf16 v[88:91], v[56:59], a[84:87], v[88:91]\n\t" \
    "buffer_store_dwordx4 v[112:115], %[z1], %[rs], s63 offen sc1\n\t" "v_mfma_f32_16x16x32_bf16 v[92:95], v[60:63], a[84:87], v[92:95]\n\t" \
    "v_mfma_f32_16x16x32_bf16 v[88:91], v[64:67], a[88:91], v[88:91]\n\t" "v_cvt_pk_bf16_f32 v118, v108, v109\n\t" \
    "v_mfma_f32_16x16x32_bf16 v[92:95], v[68:71], a[88:91], v[92:95]\n\t" "v_cvt_pk_bf16_f32 v119, v110, v111\n\t" \
    "v_mfma_f32_16x16x32_bf16 v[88:91], v[72:75], a[92:95], v[88:91]\n\t" "buffer_store_dwordx4 v[116:119], %[z2], %[rs], s63 offen sc1\n\t" \
    "v_mfma_f32_16x16x32_bf16 v[92:95], v[76:79], a[92:95], v[92:95]\n\t" "s_add_u32 s63, s63, 0x20000\n\t" "v_mfma_f32_16x16x32_bf16 v[96:99], v[48:51], a[96:99], 0\n\t" \
    "s_lshl_b32 s66, s66, 13\n\t" "s_lshl_b32 s64, s65, 19\n\t" "v_mfma_f32_16x16x32_bf16 v[100:103], v[52:55], a[96:99], 0\n\t" "s_add_u32 s64, s64, s66\n\t" \
    "s_add_u32 s64, s64, %[vb]\n\t" "v_mfma_f32_16x16x32_bf16 v[96:99], v[56:59], a[100:103], v[96:99]\n\t" \
    "v_mfma_f32_16x16x32_bf16 v[100:103], v[60:63], a[100:103], v[100:103]\n\t" "v_mfma_f32_16x16x32_bf16 v[96:99], v[64:67], a[104:107], v[96:99]\n\t" \
    "v_cvt_pk_bf16_f32 v112, v80, v81\n\t" "v_mfma_f32_16x16x32_bf16 v[100:103], v[68:71], a[104:107], v[100:103]\n\t" "v_cvt_pk_bf16_f32 v113, v82, v83\n\t" \
    "v_mfma_f32_16x16x32_bf16 v[96:99], v[72:75], a[108:111], v[96:99]\n\t" "v_cvt_pk_bf16_f32 v114, v84, v85\n\t" \
    "v_mfma_f32_16x16x32_bf16 v[100:103], v[76:79], a[108:111], v[100:103]\n\t" "v_cvt_pk_bf16_f32 v115, v86, v87\n\t" \
    "v_mfma_f32_16x16x32_bf16 v[104:107], v[48:51], a[112:115], 0\n\t" "v_cvt_pk_bf16_f32 v116, v88, v89\n\t" \
    "v_mfma_f32_16x16x32_bf16 v[108:111], v[52:55], a[112:115], 0\n\t" "v_cvt_pk_bf16_f32 v117, v90, v91\n\t" \
    "v_mfma_f32_16x16x32_bf16 v[104:107], v[56:59], a[116:119], v[104:107]\n\t" "buffer_store_dwordx4 v[112:115], %[z1], %[rs], s63 offen sc1\n\t" \
    "v_mfma_f32_16x16x32_bf16 v[108:111], v[60:63], a[116:119], v[108:111]\n\t" "v_mfma_f32_16x16x32_bf16 v[104:107], v[64:67], a[120:123], v[104:107]\n\t" \
    "v_cvt_pk_bf16_f32 v118, v92, v93\n\t" "v_mfma_f32_16x16x32_bf16 v[108:111], v[68:71], a[120:123], v[108:111]\n\t" "v_cvt_pk_bf16_f32 v119, v94, v95\n\t" \
    "v_mfma_f32_16x16x32_bf16 v[104:107], v[72:75], a[124:127], v[104:107]\n\t" "buffer_store_dwordx4 v[116:119], %[z2], %[rs], s63 offen sc1\n\t" \
    "v_mfma_f32_16x16x32_bf16 v[108:111], v[76:79], a[124:127], v[108:111]\n\t" "s_add_u32 s63, s63, 0x20000\n\t" \
    "v_mfma_f32_16x16x32_bf16 v[80:83], v[48:51], a[128:131], 0\n\t" "s_waitcnt vmcnt(6)\n\t" "v_readfirstlane_b32 s66, v120\n\t" "v_readfirstlane_b32 s67, v121\n\t" \
    "v_mfma_f32_16x16x32_bf16 v[84:87], v[52:55], a[128:131], 0\n\t" "v_mov_b32 v122, s60\n\t" "buffer_store_dword v122, %[fo], %[rc], %[fz] offen sc1\n\t" \
    "v_mfma_f32_16x16x32_bf16 v[80:83], v[56:59], a[132:135], v[80:83]\n\t" "s_max_u32 s61, s61, s66\n\t" "s_max_u32 s62, s62, s67\n\t" "s_cmp_gt_u32 s61, s65\n\t" \
    "v_mfma_f32_16x16x32_bf16 v[84:87], v[60:63], a[132:135], v[84:87]\n\t" "s_cselect_b32 s65, 1, 0\n\t" "s_cselect_b32 s64, s64, 0x7f000000\n\t" \
    "v_mfma_f32_16x16x32_bf16 v[80:83], v[64:67], a[136:139], v[80:83]\n\t" "buffer_load_dwordx4 v[16:19], %[ao], %[rs], s64 offen offset:0 sc1\n\t" \
    "v_cvt_pk_bf16_f32 v112, v96, v97\n\t" "v_mfma_f32_16x16x32_bf16 v[84:87], v[68:71], a[136:139], v[84:87]\n\t" "v_cvt_pk_bf16_f32 v113, v98, v99\n\t" \
    "v_mfma_f32_16x16x32_bf16 v[80:83], v[72:75], a[140:143], v[80:83]\n\t" "buffer_load_dwordx4 v[20:23], %[ao], %[rs], s64 offen offset:256 sc1\n\t" \
    "v_cvt_pk_bf16_f32 v114, v100, v101\n\t" "v_mfma_f32_16x16x32_bf16 v[84:87], v[76:79], a[140:143], v[84:87]\n\t" "v_cvt_pk_bf16_f32 v115, v102, v103\n\t" \
    "v_mfma_f32_16x16x32_bf16 v[88:91], v[48:51], a[144:147], 0\n\t" "buffer_load_dwordx4 v[24:27], %[ao], %[rs], s64 offen offset:2048 sc1\n\t" \
    "v_cvt_pk_bf16_f32 v116, v104, v105\n\t" "v_mfma_f32_16x16x32_bf16 v[92:95], v[52:55], a[144:147], 0\n\t" "v_cvt_pk_bf16_f32 v117, v106, v107\n\t" \
    "v_mfma_f32_16x16x32_bf16 v[88:91], v[56:59], a[148:151], v[88:91]\n\t" "buffer_load_dwordx4 v[28:31], %[ao], %[rs], s64 offen offset:2304 sc1\n\t" \
    "v_mfma_f32_16x16x32_bf16 v[92:95], v[60:63], a[148:151], v[92:95]\n\t" "buffer_store_dwordx4 v[112:115], %[z1], %[rs], s63 offen sc1\n\t" \
    "v_mfma_f32_16x16x32_bf16 v[88:91], v[64:67], a[152:155], v[88:91]\n\t" "buffer_load_dwordx4 v[32:35], %[a1], %[rs], s64 offen offset:0 sc1\n\t" \
    "v_cvt_pk_bf16_f32 v118, v108, v109\n\t" "v_mfma_f32_16x16x32_bf16 v[92:95], v[68:71], a[152:155], v[92:95]\n\t" "v_cvt_pk_bf16_f32 v119, v110, v111\n\t" \
    "v_mfma_f32_16x16x32_bf16 v[88:91], v[72:75], a[156:159], v[88:91]\n\t" "buffer_load_dwordx4 v[36:39], %[a1], %[rs], s64 offen offset:256 sc1\n\t" \
    "buffer_store_dwordx4 v[116:119], %[z2], %[rs], s63 offen sc1\n\t" "v_mfma_f32_16x16x32_bf16 v[92:95], v[76:79], a[156:159], v[92:95]\n\t" \
    "s_add_u32 s63, s63, 0x20000\n\t" "v_mfma_f32_16x16x32_bf16 v[96:99], v[48:51], a[160:163], 0\n\t" \
    "buffer_load_dwordx4 v[40:43], %[a1], %[rs], s64 offen offset:2048 sc1\n\t" "v_mfma_f32_16x16x32_bf16 v[100:103], v[52:55], a[160:163], 0\n\t" \
    "v_mfma_f32_16x16x32_bf16 v[96:99], v[56:59], a[164:167], v[96:99]\n\t" "buffer_load_dwordx4 v[44:47], %[a1], %[rs], s64 offen offset:2304 sc1\n\t" \
    "v_mfma_f32_16x16x32_bf16 v[100:103], v[60:63], a[164:167], v[100:103]\n\t" "v_mfma_f32_16x16x32_bf16 v[96:99], v[64:67], a[168:171], v[96:99]\n\t" \
    "v_cvt_pk_bf16_f32 v112, v80, v81\n\t" "v_mfma_f32_16x16x32_bf16 v[100:103], v[68:71], a[168:171], v[100:103]\n\t" "v_cvt_pk_bf16_f32 v113, v82, v83\n\t" \
    "v_mfma_f32_16x16x32_bf16 v[96:99], v[72:75], a[172:175], v[96:99]\n\t" "v_cvt_pk_bf16_f32 v114, v84, v85\n\t" \
    "v_mfma_f32_16x16x32_bf16 v[100:103], v[76:79], a[172:175], v[100:103]\n\t" "v_cvt_pk_bf16_f32 v115, v86, v87\n\t" \
    "v_mfma_f32_16x16x32_bf16 v[104:107], v[48:51], a[176:179], 0\n\t" "v_cvt_pk_bf16_f32 v116, v88, v89\n\t" \
    "v_mfma_f32_16x16x32_bf16 v[108:111], v[52:55], a[176:179], 0\n\t" "v_cvt_pk_bf16_f32 v117, v90, v91\n\t" \
    "v_mfma_f32_16x16x32_bf16 v[104:107], v[56:59], a[180:183], v[104:107]\n\t" "buffer_store_dwordx4 v[112:115], %[z1], %[rs], s63 offen sc1\n\t" \
    "v_mfma_f32_16x16x32_bf16 v[108:111], v[60:63], a[180:183], v[108:111]\n\t" "v_mfma_f32_16x16x32_bf16 v[104:107], v[64:67], a[184:187], v[104:107]\n\t" \
    "v_cvt_pk_bf16_f32 v118, v92, v93\n\t" "v_mfma_f32_16x16x32_bf16 v[108:111], v[68:71], a[184:187], v[108:111]\n\t" "v_cvt_pk_bf16_f32 v119, v94, v95\n\t" \
    "v_mfma_f32_16x16x32_bf16 v[104:107], v[72:75], a[188:191], v[104:107]\n\t" "buffer_store_dwordx4 v[116:119], %[z2], %[rs], s63 offen sc1\n\t" \
    "v_mfma_f32_16x16x32_bf16 v[108:111], v[76:79], a[188:191], v[108:111]\n\t" "s_add_u32 s63, s63, 0x20000\n\t" \
    "v_mfma_f32_16x16x32_bf16 v[80:83], v[48:51], a[192:195], 0\n\t" "v_mfma_f32_16x16x32_bf16 v[84:87], v[52:55], a[192:195], 0\n\t" \
    "v_mfma_f32_16x16x32_bf16 v[80:83], v[56:59], a[196:199], v[80:83]\n\t" "v_mfma_f32_16x16x32_bf16 v[84:87], v[60:63], a[196:199], v[84:87]\n\t" \
    "v_mfma_f32_16x16x32_bf16 v[80:83], v[64:67], a[200:203], v[80:83]\n\t" "v_cvt_pk_bf16_f32 v112, v96, v97\n\t" \
    "v_mfma_f32_16x16x32_bf16 v[84:87], v[68:71], a[200:203], v[84:87]\n\t" "v_cvt_pk_bf16_f32 v113, v98, v99\n\t" \
    "v_mfma_f32_16x16x32_bf16 v[80:83], v[72:75], a[204:207], v[80:83]\n\t" "v_cvt_pk_bf16_f32 v114, v100, v101\n\t" \
    "v_mfma_f32_16x16x32_bf16 v[84:87], v[76:79], a[204:207], v[84:87]\n\t" "v_cvt_pk_bf16_f32 v115, v102, v103\n\t" \
    "v_mfma_f32_16x16x32_bf16 v[88:91], v[48:51], a[208:211], 0\n\t" "v_cvt_pk_bf16_f32 v116, v104, v105\n\t" "v_mfma_f32_16x16x32_bf16 v[92:95], v[52:55], a[208:211], 0\n\t" \
    "v_cvt_pk_bf16_f32 v117, v106, v107\n\t" "v_mfma_f32_16x16x32_bf16 v[88:91], v[56:59], a[212:215], v[88:91]\n\t" \
    "buffer_store_dwordx4 v[112:115], %[z1], %[rs], s63 offen sc1\n\t" "v_mfma_f32_16x16x32_bf16 v[92:95], v[60:63], a[212:215], v[92:95]\n\t" \
    "v_mfma_f32_16x16x32_bf16 v[88:91], v[64:67], a[216:219], v[88:91]\n\t" "v_cvt_pk_bf16_f32 v118, v108, v109\n\t" \
    "v_mfma_f32_16x16x32_bf16 v[92:95], v[68:71], a[216:219], v[92:95]\n\t" "v_cvt_pk_bf16_f32 v119, v110, v111\n\t" \
    "v_mfma_f32_16x16x32_bf16 v[88:91], v[72:75], a[220:223], v[88:91]\n\t" "buffer_store_dwordx4 v[116:119], %[z2], %[rs], s63 offen sc1\n\t" \
    "v_mfma_f32_16x16x32_bf16 v[92:95], v[76:79], a[220:223], v[92:95]\n\t" "s_add_u32 s63, s63, 0x20000\n\t" "v_mfma_f32_16x16x32_bf16 v[96:99], v[48:51], a[224:227], 0\n\t" \
    "v_mfma_f32_16x16x32_bf16 v[100:103], v[52:55], a[224:227], 0\n\t" "v_mfma_f32_16x16x32_bf16 v[96:99], v[56:59], a[228:231], v[96:99]\n\t" \
    "v_mfma_f32_16x16x32_bf16 v[100:103], v[60:63], a[228:231], v[100:103]\n\t" "v_mfma_f32_16x16x32_bf16 v[96:99], v[64:67], a[232:235], v[96:99]\n\t" \
    "v_cvt_pk_bf16_f32 v112, v80, v81\n\t" "v_mfma_f32_16x16x32_bf16 v[100:103], v[68:71], a[232:235], v[100:103]\n\t" "v_cvt_pk_bf16_f32 v113, v82, v83\n\t" \
    "v_mfma_f32_16x16x32_bf16 v[96:99], v[72:75], a[236:239], v[96:99]\n\t" "v_cvt_pk_bf16_f32 v114, v84, v85\n\t" \
    "v_mfma_f32_16x16x32_bf16 v[100:103], v[76:79], a[236:239], v[100:103]\n\t" "v_cvt_pk_bf16_f32 v115, v86, v87\n\t" \
    "v_mfma_f32_16x16x32_bf16 v[104:107], v[48:51], a[240:243], 0\n\t" "v_cvt_pk_bf16_f32 v116, v88, v89\n\t" \
    "v_mfma_f32_16x16x32_bf16 v[108:111], v[52:55], a[240:243], 0\n\t" "v_cvt_pk_bf16_f32 v117, v90, v91\n\t" \
    "v_mfma_f32_16x16x32_bf16 v[104:107], v[56:59], a[244:247], v[104:107]\n\t" "buffer_store_dwordx4 v[112:115], %[z1], %[rs], s63 offen sc1\n\t" \
    "v_mfma_f32_16x16x32_bf16 v[108:111], v[60:63], a[244:247], v[108:111]\n\t" "v_mfma_f32_16x16x32_bf16 v[104:107], v[64:67], a[248:251], v[104:107]\n\t" \
    "v_cvt_pk_bf16_f32 v118, v92, v93\n\t" "v_mfma_f32_16x16x32_bf16 v[108:111], v[68:71], a[248:251], v[108:111]\n\t" "v_cvt_pk_bf16_f32 v119, v94, v95\n\t" \
    "v_mfma_f32_16x16x32_bf16 v[104:107], v[72:75], a[252:255], v[104:107]\n\t" "buffer_store_dwordx4 v[116:119], %[z2], %[rs], s63 offen sc1\n\t" \
    "s_add_u32 s63, s63, 0x20000\n\t" "s_add_u32 s60, s60, 1\n\t" "v_mfma_f32_16x16x32_bf16 v[108:111], v[76:79], a[252:255], v[108:111]\n\t" "s_cmp_eq_u32 s65, 0\n\t" \
    "s_waitcnt vmcnt(6)\n\t" ZSUM "s_cbranch_scc0 33b\n\t" "s_branch 30f\n\t" "20:\n\t" "s_nop 7\n\t" "v_cvt_pk_bf16_f32 v112, v96, v97\n\t" \
    "v_cvt_pk_bf16_f32 v113, v98, v99\n\t" "v_cvt_pk_bf16_f32 v114, v100, v101\n\t" "v_cvt_pk_bf16_f32 v115, v102, v103\n\t" "v_cvt_pk_bf16_f32 v116, v104, v105\n\t" \
    "v_cvt_pk_bf16_f32 v117, v106, v107\n\t" "v_cvt_pk_bf16_f32 v118, v108, v109\n\t" "v_cvt_pk_bf16_f32 v119, v110, v111\n\t" \
    "buffer_store_dwordx4 v[112:115], %[z1], %[rs], s63 offen sc1\n\t" "buffer_store_dwordx4 v[116:119], %[z2], %[rs], s63 offen sc1\n\t" \
    "buffer_load_dword v120, %[fo], %[rc], %[fv] offen sc1\n\t" "s_waitcnt vmcnt(0)\n\t" "v_readfirstlane_b32 s61, v120\n\t" "s_lshr_b32 s66, s60, 2\n\t" \
    "s_xor_b32 s66, s66, %[xp]\n\t" "s_and_b32 s66, s66, 1\n\t" "s_lshl_b32 s66, s66, 13\n\t" "s_lshl_b32 s64, s60, 19\n\t" "s_add_u32 s64, s64, s66\n\t" \
    "s_add_u32 s64, s64, %[vb]\n\t" "s_cmp_gt_u32 s61, s60\n\t" "s_cbranch_scc0 24f\n\t" "s_cmp_eq_u32 s60, 64\n\t" "s_cbranch_scc1 24f\n\t" \
    "buffer_load_dwordx4 v[48:51], %[ao], %[rs], s64 offen offset:0 sc1\n\t" "buffer_load_dwordx4 v[52:55], %[ao], %[rs], s64 offen offset:256 sc1\n\t" \
    "buffer_load_dwordx4 v[56:59], %[ao], %[rs], s64 offen offset:2048 sc1\n\t" "buffer_load_dwordx4 v[60:63], %[ao], %[rs], s64 offen offset:2304 sc1\n\t" \
    "buffer_load_dwordx4 v[64:67], %[a1], %[rs], s64 offen offset:0 sc1\n\t" "buffer_load_dwordx4 v[68:71], %[a1], %[rs], s64 offen offset:256 sc1\n\t" \
    "buffer_load_dwordx4 v[72:75], %[a1], %[rs], s64 offen offset:2048 sc1\n\t" "buffer_load_dwordx4 v[76:79], %[a1], %[rs], s64 offen offset:2304 sc1\n\t" \
    "v_mov_b32 v122, s60\n\t" "buffer_store_dword v122, %[fo], %[rc], %[fz] offen sc1\n\t" "s_waitcnt vmcnt(1)\n\t" "s_branch 23b\n\t" "24:\n\t" "v_mov_b32 v122, s60\n\t" \
    "buffer_store_dword v122, %[fo], %[rc], %[fz] offen sc1\n\t" "s_cmp_eq_u32 s60, 64\n\t" "s_cbranch_scc1 90f\n\t" "s_add_u32 s67, s60, 1\n\t" "s_cmp_ge_u32 s61, s67\n\t" \
    "s_cbranch_scc1 22f\n\t" "21:\n\t" "buffer_load_dword v120, %[fo], %[rc], %[fv] offen sc1\n\t" "buffer_load_dword v121, %[fo], %[rc], %[vd] offen sc1\n\t" \
    "s_waitcnt vmcnt(0)\n\t" "v_readfirstlane_b32 s61, v120\n\t" "v_readfirstlane_b32 s66, v121\n\t" "s_nop 3\n\t" "s_cmp_ge_u32 s61, s67\n\t" "s_cbranch_scc1 22f\n\t" \
    "s_bitcmp1_b32 s66, 1\n\t" "s_cbranch_scc1 90f\n\t" "s_cmp_eq_u32 %[bd], 0\n\t" "s_cbranch_scc1 22f\n\t" "s_sub_u32 %[bd], %[bd], 1\n\t" "s_sleep 2\n\t" \
    "s_branch 21b\n\t" "22:\n\t" "s_lshr_b32 s66, s60, 2\n\t" "s_xor_b32 s66, s66, %[xp]\n\t" "s_and_b32 s66, s66, 1\n\t" "s_lshl_b32 s66, s66, 13\n\t" \
    "s_lshl_b32 s64, s60, 19\n\t" "s_add_u32 s64, s64, s66\n\t" "s_add_u32 s64, s64, %[vb]\n\t" "buffer_load_dwordx4 v[48:51], %[ao], %[rs], s64 offen offset:0 sc1\n\t" \
    "buffer_load_dwordx4 v[52:55], %[ao], %[rs], s64 offen offset:256 sc1\n\t" "buffer_load_dwordx4 v[56:59], %[ao], %[rs], s64 offen offset:2048 sc1\n\t" \
    "buffer_load_dwordx4 v[60:63], %[ao], %[rs], s64 offen offset:2304 sc1\n\t" "buffer_load_dwordx4 v[64:67], %[a1], %[rs], s64 offen offset:0 sc1\n\t" \
    "buffer_load_dwordx4 v[68:71], %[a1], %[rs], s64 offen offset:256 sc1\n\t" "buffer_load_dwordx4 v[72:75], %[a1], %[rs], s64 offen offset:2048 sc1\n\t" \
    "buffer_load_dwordx4 v[76:79], %[a1], %[rs], s64 offen offset:2304 sc1\n\t" "s_waitcnt vmcnt(0)\n\t" "s_branch 23b\n\t" "60:\n\t" "s_sub_u32 s67, s60, %[rg]\n\t" \
    "s_cmp_ge_u32 s62, s67\n\t" "s_cbranch_scc1 62f\n\t" "61:\n\t" "buffer_load_dword v120, %[fo], %[rc], %[fs] offen sc1\n\t" \
    "buffer_load_dword v121, %[fo], %[rc], %[vd] offen sc1\n\t" "s_waitcnt vmcnt(0)\n\t" "v_readfirstlane_b32 s62, v120\n\t" "v_readfirstlane_b32 s66, v121\n\t" "s_nop 3\n\t" \
    "s_cmp_ge_u32 s62, s67\n\t" "s_cbranch_scc1 62f\n\t" "s_bitcmp1_b32 s66, 1\n\t" "s_cbranch_scc1 90f\n\t" "s_cmp_eq_u32 %[bd], 0\n\t" "s_cbranch_scc1 62f\n\t" \
    "s_sub_u32 %[bd], %[bd], 1\n\t" "s_sleep 2\n\t" "s_branch 61b\n\t" "62:\n\t" "s_branch 63b\n\t" "30:\n\t" "s_nop 7\n\t" "v_cvt_pk_bf16_f32 v112, v96, v97\n\t" \
    "v_cvt_pk_bf16_f32 v113, v98, v99\n\t" "v_cvt_pk_bf16_f32 v114, v100, v101\n\t" "v_cvt_pk_bf16_f32 v115, v102, v103\n\t" "v_cvt_pk_bf16_f32 v116, v104, v105\n\t" \
    "v_cvt_pk_bf16_f32 v117, v106, v107\n\t" "v_cvt_pk_bf16_f32 v118, v108, v109\n\t" "v_cvt_pk_bf16_f32 v119, v110, v111\n\t" \
    "buffer_store_dwordx4 v[112:115], %[z1], %[rs], s63 offen sc1\n\t" "buffer_store_dwordx4 v[116:119], %[z2], %[rs], s63 offen sc1\n\t" \
    "buffer_load_dword v120, %[fo], %[rc], %[fv] offen sc1\n\t" "s_waitcnt vmcnt(0)\n\t" "v_readfirstlane_b32 s61, v120\n\t" "s_lshr_b32 s66, s60, 2\n\t" \
    "s_xor_b32 s66, s66, %[xp]\n\t" "s_and_b32 s66, s66, 1\n\t" "s_lshl_b32 s66, s66, 13\n\t" "s_lshl_b32 s64, s60, 19\n\t" "s_add_u32 s64, s64, s66\n\t" \
    "s_add_u32 s64, s64, %[vb]\n\t" "s_cmp_gt_u32 s61, s60\n\t" "s_cbranch_scc0 34f\n\t" "s_cmp_eq_u32 s60, 64\n\t" "s_cbranch_scc1 34f\n\t" \
    "buffer_load_dwordx4 v[16:19], %[ao], %[rs], s64 offen offset:0 sc1\n\t" "buffer_load_dwordx4 v[20:23], %[ao], %[rs], s64 offen offset:256 sc1\n\t" \
    "buffer_load_dwordx4 v[24:27], %[ao], %[rs], s64 offen offset:2048 sc1\n\t" "buffer_load_dwordx4 v[28:31], %[ao], %[rs], s64 offen offset:2304 sc1\n\t" \
    "buffer_load_dwordx4 v[32:35], %[a1], %[rs], s64 offen offset:0 sc1\n\t" "buffer_load_dwordx4 v[36:39], %[a1], %[rs], s64 offen offset:256 sc1\n\t" \
    "buffer_load_dwordx4 v[40:43], %[a1], %[rs], s64 offen offset:2048 sc1\n\t" "buffer_load_dwordx4 v[44:47], %[a1], %[rs], s64 offen offset:2304 sc1\n\t" \
    "v_mov_b32 v122, s60\n\t" "buffer_store_dword v122, %[fo], %[rc], %[fz] offen sc1\n\t" "s_waitcnt vmcnt(1)\n\t" "s_branch 33b\n\t" "34:\n\t" "v_mov_b32 v122, s60\n\t" \
    "buffer_store_dword v122, %[fo], %[rc], %[fz] offen sc1\n\t" "s_cmp_eq_u32 s60, 64\n\t" "s_cbranch_scc1 90f\n\t" "s_add_u32 s67, s60, 1\n\t" "s_cmp_ge_u32 s61, s67\n\t" \
    "s_cbranch_scc1 32f\n\t" "31:\n\t" "buffer_load_dword v120, %[fo], %[rc], %[fv] offen sc1\n\t" "buffer_load_dword v121, %[fo], %[rc], %[vd] offen sc1\n\t" \
    "s_waitcnt vmcnt(0)\n\t" "v_readfirstlane_b32 s61, v120\n\t" "v_readfirstlane_b32 s66, v121\n\t" "s_nop 3\n\t" "s_cmp_ge_u32 s61, s67\n\t" "s_cbranch_scc1 32f\n\t" \
    "s_bitcmp1_b32 s66, 1\n\t" "s_cbranch_scc1 90f\n\t" "s_cmp_eq_u32 %[bd], 0\n\t" "s_cbranch_scc1 32f\n\t" "s_sub_u32 %[bd], %[bd], 1\n\t" "s_sleep 2\n\t" \
    "s_branch 31b\n\t" "32:\n\t" "s_lshr_b32 s66, s60, 2\n\t" "s_xor_b32 s66, s66, %[xp]\n\t" "s_and_b32 s66, s66, 1\n\t" "s_lshl_b32 s66, s66, 13\n\t" \
    "s_lshl_b32 s64, s60, 19\n\t" "s_add_u32 s64, s64, s66\n\t" "s_add_u32 s64, s64, %[vb]\n\t" "buffer_load_dwordx4 v[16:19], %[ao], %[rs], s64 offen offset:0 sc1\n\t" \
    "buffer_load_dwordx4 v[20:23], %[ao], %[rs], s64 offen offset:256 sc1\n\t" "buffer_load_dwordx4 v[24:27], %[ao], %[rs], s64 offen offset:2048 sc1\n\t" \
    "buffer_load_dwordx4 v[28:31], %[ao], %[rs], s64 offen offset:2304 sc1\n\t" "buffer_load_dwordx4 v[32:35], %[a1], %[rs], s64 offen offset:0 sc1\n\t" \
    "buffer_load_dwordx4 v[36:39], %[a1], %[rs], s64 offen offset:256 sc1\n\t" "buffer_load_dwordx4 v[40:43], %[a1], %[rs], s64 offen offset:2048 sc1\n\t" \
    "buffer_load_dwordx4 v[44:47], %[a1], %[rs], s64 offen offset:2304 sc1\n\t" "s_waitcnt vmcnt(0)\n\t" "s_branch 33b\n\t" "70:\n\t" "s_sub_u32 s67, s60, %[rg]\n\t" \
    "s_cmp_ge_u32 s62, s67\n\t" "s_cbranch_scc1 72f\n\t" "71:\n\t" "buffer_load_dword v120, %[fo], %[rc], %[fs] offen sc1\n\t" \
    "buffer_load_dword v121, %[fo], %[rc], %[vd] offen sc1\n\t" "s_waitcnt vmcnt(0)\n\t" "v_readfirstlane_b32 s62, v120\n\t" "v_readfirstlane_b32 s66, v121\n\t" "s_nop 3\n\t" \
    "s_cmp_ge_u32 s62, s67\n\t" "s_cbranch_scc1 72f\n\t" "s_bitcmp1_b32 s66, 1\n\t" "s_cbranch_scc1 90f\n\t" "s_cmp_eq_u32 %[bd], 0\n\t" "s_cbranch_scc1 72f\n\t" \
    "s_sub_u32 %[bd], %[bd], 1\n\t" "s_sleep 2\n\t" "s_branch 71b\n\t" "72:\n\t" "s_branch 73b\n\t" "90:\n\t"

#define T_DQ 1420
#define T_DV 1290
#define T_RQ 670
#define T_RV 640
#define T_M1 100
#define T_DY 580
#define T_DX 1240
#define T_MIN 35
#define T_PEN 280
#define T_SAFE 80
#define T_RETRY 700
#define T_POLL 1
#define T_KS 0
#define T_TS 0
#define T_CLR 210
#define T_VLAG 0
#define T_U0 340
constexpr int T_OVH = 118, T_NEAR = 460;
constexpr unsigned NS(int ns) { return (unsigned)(ns * 64 / 10); }
struct Clk { unsigned cyc, g, pc, pg, k1, k2; };
DEV void mark(Clk& k) {
  unsigned long long c, r[8];
  asm volatile("s_memtime %0\n\ts_memrealtime %1\n\ts_nop 2\n\ts_memrealtime %2\n\ts_nop 2\n\ts_memrealtime %3\n\ts_nop 2\n\ts_memrealtime %4\n\ts_nop 2\n\t"
               "s_memrealtime %5\n\ts_nop 2\n\ts_memrealtime %6\n\ts_nop 2\n\ts_memrealtime %7\n\ts_waitcnt lgkmcnt(0)"
               : "=s"(c), "=s"(r[0]), "=s"(r[1]), "=s"(r[2]), "=s"(r[3]), "=s"(r[4]), "=s"(r[5]), "=s"(r[6]) :: "memory");
  const unsigned a = (unsigned)r[0];
  unsigned n = 0;
#pragma unroll
  for (int i = 1; i < 7; ++i) asm("s_cmp_eq_u32 %1, %2\n\ts_addc_u32 %0, %0, 0" : "+s"(n) : "s"((unsigned)r[i]), "s"(a) : "scc");
  k.g = (a + 4u) << 6; k.cyc = (unsigned)c + 12u + 16u * n;
}
DEV void anchor(Clk& k, bool any = false) {
  mark(k);
  const unsigned dg_ = k.g - k.pg;
  const float dc = (float)(k.cyc - k.pc), dg = (float)dg_;
  unsigned k1, k2;
  asm volatile("s_nop 0\n\tv_readfirstlane_b32 %0, %2\n\tv_readfirstlane_b32 %1, %3\n\ts_nop 3\n\ts_min_u32 %0, %0, 0x80000\n\ts_min_u32 %1, %1, 0x8000"
               : "=&s"(k1), "=&s"(k2) : "v"((unsigned)(dg * __builtin_amdgcn_rcpf(dc) * 65536.f)), "v"((unsigned)(dc * __builtin_amdgcn_rcpf(dg) * 65536.f)) : "scc");
  if (any) { k.k1 = k1; k.k2 = k2; }
  else asm("s_cmp_lt_u32 %4, %5\n\ts_cselect_b32 %2, %2, %0\n\ts_cselect_b32 %3, %3, %1\n\ts_add_u32 %0, %0, %2\n\ts_lshr_b32 %0, %0, 1\n\ts_add_u32 %1, %1, %3\n\ts_lshr_b32 %1, %1, 1"
           : "+s"(k.k1), "+s"(k.k2), "+s"(k1), "+s"(k2) : "s"(dg_ - NS(1400)), "n"(NS(800)) : "scc");
  k.pc = k.cyc; k.pg = k.g;
}
DEV unsigned to_cyc(const Clk& k, unsigned g) {
  unsigned d;
  asm("s_max_i32 %0, %1, 0xffff8000\n\ts_min_i32 %0, %0, 0x8000\n\ts_mul_i32 %0, %0, %2\n\ts_ashr_i32 %0, %0, 16" : "=&s"(d) : "s"(g - k.g), "s"(k.k2) : "scc");
  return k.cyc + d - T_OVH;
}
DEV void wait_cyc(unsigned t) { asm volatile(TIMED_WAIT :: "s"(t) : CL_S); }
DEV void close_in(Clk& k, unsigned t) {
  if ((int)(t - (unsigned)__builtin_amdgcn_s_memtime()) > T_NEAR + 60) { wait_cyc(t - T_NEAR); mark(k); }
}
DEV int slow_line(const u16* q, int h, int j, int lane) { return (j * 32 + (lane >> 1)) * 8192 + (h >> 3) * 2048 + (lane & 1) * 1024 + ((3 - (int)((size_t)q >> 7)) & 7) * 128; }
DEV int turn(int h, int w) { return ((h >> 3) ^ (w & 1)) & 3; }
DEV void hold(const Ctx& c, int ticks) {
#pragma unroll 1
  while ((unsigned)__builtin_amdgcn_s_memrealtime() - c.t0 < (unsigned)ticks) __builtin_amdgcn_s_sleep(4);
}
DEV int z_mix(int y) { return (y >> 4) | ((y & 1) << 2); }
DEV int u_slot(int s, int para) { return R_U + (s & (URING == 64 ? 127 : URING - 1)) * MB + ((para ^ (s >> 1)) & 1) * 8192; }
DEV unsigned ask_stamps(Ctx& c, int par, int slot) { return __builtin_amdgcn_raw_buffer_load_b32(c.rs, amap(24576, par) + fresh_lane() * 128, slot, AUX_SC1); }
DEV unsigned latest(Ctx& c, const Clk& k, int par, int slot, unsigned v) {
  if (__builtin_expect(!allok(v), 0)) {
#pragma unroll 1
    do {
      __builtin_amdgcn_s_sleep(T_POLL);
      asm volatile("" ::: "memory");
      const unsigned t = __builtin_amdgcn_raw_buffer_load_b32(c.rs, v == SENT ? amap(24576, par) + fresh_lane() * 128 : 1 << 30, slot, AUX_SC1);
      v = v == SENT ? t : v;
    } while (!allok(v) && !spend(c));
  }
  int d = (int)(v - k.g);
  d = max(d, __builtin_amdgcn_update_dpp(d, d, 0x128, 0xf, 0xf, true));
  d = max(d, __builtin_amdgcn_update_dpp(d, d, 0x124, 0xf, 0xf, true));
  d = max(d, __builtin_amdgcn_update_dpp(d, d, 0x122, 0xf, 0xf, true));
  d = max(d, __builtin_amdgcn_update_dpp(d, d, 0x121, 0xf, 0xf, true));
  unsigned m;
  asm("s_max_i32 %0, %1, %2\n\ts_max_i32 %0, %0, %3\n\ts_max_i32 %0, %0, %4" : "=&s"(m)
      : "s"(__builtin_amdgcn_readlane(d, 0)), "s"(__builtin_amdgcn_readlane(d, 16)), "s"(__builtin_amdgcn_readlane(d, 32)), "s"(__builtin_amdgcn_readlane(d, 48)) : "scc");
  return k.g + m;
}
struct Plan { unsigned gx, dx; };
DEV unsigned smax(unsigned a, unsigned b) { return (int)(a - b) > 0 ? a : b; }
DEV unsigned pick(int d, unsigned a, unsigned b) { unsigned r; asm("s_cmp_gt_i32 %1, 0\n\ts_cselect_b32 %0, %2, %3" : "=s"(r) : "s"(d), "s"(a), "s"(b) : "scc"); return r; }
DEV unsigned when_p(const Plan& t, unsigned last, unsigned more = 0) {
  const unsigned gy = t.gx + t.dx + NS(T_M1), a = last + NS(T_PEN), b = gy + NS(T_RETRY);
  return pick((int)(last + NS(T_MIN) - gy), pick((int)(a - b), a, b) + more, gy);
}
DEV void advance(Plan& t, unsigned last) {
  const int over = (int)(last - t.gx - t.dx);
  t.gx = when_p(t, last, NS(T_SAFE)) + NS(T_DY);
  t.dx += pick(over - (int)NS(80), NS(4), pick(over, (unsigned)(over >> 1), (unsigned)(over >> 3)));
}
DEV unsigned next_go(Ctx& c, Clk& k, Plan& t, int par, int slot, unsigned v) {
  SCHED();
  anchor(k);
  SCHED();
  advance(t, latest(c, k, par, slot, v));
  return to_cyc(k, t.gx);
}

DEV constexpr int qk_r(int j) { return j < 4 ? 0 : 1 + (j - 4) % 3; }
DEV constexpr int qk_i(int j) { return j < 4 ? j : (j - 4) / 3; }
DEV constexpr int qk_upto(int u) { return K_FIRST && u >= 3 ? (u + 3 < 16 ? u + 3 : 16) : (u + 4 < 16 ? u + 4 : 16); }
DEV void qk_role(const Params& p, Ctx& c, int h) {
  constexpr int WL = 0, MEMS = 131072, KXL = 147456, QKX = 155648, QROW = 272;
  c.budget = SPIN_CAP;
  const int w = c.w;
  Clk k = {0u, 0u, 0u, 0u, 0u, 0u};
  anchor(k, true);
  v8u wa[32], wh[16];
  unsigned look = claim_look(c);
  bool claimed = false;
  hold(c, QK_AT);
  {
    const int lane = fresh_lane(), g = lane >> 4, cc = lane & 15;
    const rsrc_t rq = mkrs(p.Wq, 1024 * 4096 * 2), rk = mkrs(p.Wk, 1024 * 4096 * 2);
    int vo[4];
#pragma unroll
    for (int i = 0; i < 4; ++i) vo[i] = opq((256 * w + 64 * (i ^ turn(h, w)) + krow_lane(g)) * 8192 + (h * 128 + 8 * cc) * 2);
    v4u row[16][8];
    auto request = [&](int u) {
#pragma unroll
      for (int r = 0; r < 8; ++r) row[u][r] = ld16w<W_AUX>((u < 8) != K_FIRST ? rq : rk, vo[(u >> 1) & 3], krow(4 * (u & 1) + (r >> 1), r & 1) * 8192);
    };
#pragma unroll
    for (int u = 0; u < qk_upto(-1); ++u) { request(u); if (u == 0) { SCHED(); ask(c, look); SCHED(); } }
    SCHED();
    const int sw = MEMS + w * 8192 + g * 256, lw = opq(WL + w * 32768 + lane * 16);
    int sr[2];
#pragma unroll
    for (int t = 0; t < 2; ++t) { const int i = (2 * cc + t) & 7; sr[t] = opq(i * 1024 + g * 256 + ((4 * w + (cc >> 2) + 2 * i) & 15) * 16); }
    sfor<16>([&](auto U) __attribute__((always_inline)) {
      constexpr int u = U.value;
#pragma unroll
      for (int i = 0; i < 8; ++i) {
        v4u f;
#pragma unroll
        for (int r = 0; r < 4; ++r) f[r] = prm(row[u][2 * r + 1][i >> 1], row[u][2 * r][i >> 1], i & 1);
        sts16(c.L, sw + i * 1024 + ((cc + 2 * i) & 15) * 16, f);
      }
      SCHED();
      if ((u == 5 || u == 10) && !claimed) claimed = claim_try(c, look);
      if (u == 2 || u == 7) look = claim_look(c);
      if (qk_upto(u) > qk_upto(u - 1)) request(qk_upto(u - 1));
      if (u == 15 && !claimed) claim(c, 0);
      BAR();
      v4u in[4][2];
#pragma unroll
      for (int t = 1; t >= 0; --t)
#pragma unroll
        for (int r = 0; r < 4; ++r) in[r][t] = lds16(c.L, sr[t] + MEMS + ((w + r) & 3) * 8192);
      SCHED();
#pragma unroll
      for (int j = 0; j < 16; ++j) {
        if (qk_i(j) != ((u >> 1) & 3)) continue;
#pragma unroll
        for (int t = 1; t >= 0; --t) {
          const v4u o = in[qk_r(j)][t];
          constexpr bool isq = (u < 8) != K_FIRST;
          if (!isq && t == 1) sts16(c.L, lw + j * 2048 + (u & 1) * 1024, o);
          else {
            v8u& d = isq ? wa[2 * j + t] : wh[j];
#pragma unroll
            for (int r = 0; r < 4; ++r) d[4 * (u & 1) + r] = o[r];
#define M_(r) asm volatile("" : "+" r(d))
            if (u & 1) { if (isq) AT_ACC(M_, 2 * j + t); else AT_HOME(M_, j); }
#undef M_
          }
        }
      }
      BAR();
    });
  }
  STAMP(0);
  BAR();
  const unsigned vote = wait_verdict(c);
  const int par = vote_b(vote), para = vote_a(vote);
  const int lane = fresh_lane(), g = lane >> 4, cc = lane & 15;
  const int moff = opq(amap(w * 4096 + lane * 16, par) + (turn(h, w) & 1) * 1024), m2 = R_MEM + (turn(h, w) & 2) * 1024;
  unsigned tries = 1u << 12;
  *(volatile LAS unsigned*)(c.L + 160512) = 0u;
  LGKM(0);
  const int idx = opq((lane & 1) ? 0xEEEE : 0x4444);
  TICK_DECL;
  const int l0 = opq(WL + w * 32768 + lane * 16), l1 = opq(MEMS + lane * 16);
  const int qw = opq(QKX + (2 * w + (cc >> 3)) * 256 + g * 64 + (cc & 1) * 16 + ((cc >> 1) & 3) * 4), kw = opq(QKX + (8 + 2 * g) * QROW + (32 * w + 2 * cc) * 2);
#define QK_PINS PIN_ACC0(wa); PIN_ACC1(wa); PIN_HOME(wh)
#define QK_IN [l0] "v"(l0), [l1] "v"(l1), [mo] "v"(moff), [ix] "v"(idx), [qw] "v"(qw), [kw] "v"(kw), [rs] "s"(c.rs), [sb] "s"(m2 + (s & 3) * MEM_SLOT)
#define QK_CL CL_S, CL_16_32, CL_160_200, CL_200_216, CL_216_256
  if (w) RAWBAR();
  if (w == 3) {
    const int hq = opq(lane ? 1 << 30 : amap(24576 + h * 128, par));
    anchor(k, true);
    unsigned tg = k.cyc;
    Plan tt = {k.g - NS(100000), NS(T_DX)};
    for (int s = 0; s < NSEG; ++s) {
      const int pb = R_P + (s & 3) * P_SLOT;
      QK_PINS;
      asm volatile(QK_STEP3 : [bd] "+s"(c.budget), [rt] "+s"(tries) : QK_IN, [hq] "v"(hq), [pb] "s"(pb), [tg] "s"(tg), [ca] "s"(k.cyc - T_DQ), [k1] "s"(k.k1), [ga] "s"(k.g), [dr] "n"(T_RQ), [pr] "s"(R_P + ((s + 1) & 3) * P_SLOT),
                     [kn] "s"(R_KX - 8 * MEMS + xblk(min(s + 1, NSEG - 1), h, para)) : QK_CL);
      QK_PINS;
      LOG(1, s, tg);
      const unsigned sv = ask_stamps(c, par, pb);
      SCHED();
      anchor(k);
      SCHED();
      advance(tt, latest(c, k, par, pb, sv));
      tg = to_cyc(k, tt.gx);
      RAWBAR();
      LOG(2, s, k.cyc); LOG(3, s, k.g); LOG(4, s, tt.dx);
      st4s(mkrs((const void*)c.ctrl, CTRL_BYTES), hq & (1 << 30), C_FLAG + (F_S + h) * 128, (unsigned)s + 1u);
      TICK_SUM(10);
      close_in(k, tg);
      tg = to_cyc(k, tt.gx);
    }
  } else {
    const int kx0 = opq(KXL + g * 512 + cc * 16), kmrd = opq(QKX + g * 16 + (8 + (cc & 7)) * QROW);
    unsigned seen = NOFLAG;
    if (w == 0) {
      const int prow = (2 * g + (cc & 1)) * 2 + ((cc >> 1) & 1);
      const int px = opq(amap((h >> 1) * 1024 + ((2 * (h & 1) + (cc >> 3)) * 16 + prow) * 16 + ((cc >> 2) & 1) * 8, par));
      const int pm = opq(cc < 8 ? amap(16384 + h * 128 + prow * 8 + (cc >> 2) * 4, par) : 1 << 30);
      const int ps = opq((cc & 1) ? 0x03020706 : 0x05040100);
      for (int s = 0; s < NSEG; ++s) {
        if (seen < (unsigned)s + 1u) seen = wait_flag(c, F_K + h, s + 1);
        QK_PINS;
        asm volatile(QK_STEP0 : [bd] "+s"(c.budget), [rt] "+s"(tries), [sq] "+s"(seen)
                     : QK_IN, [kx] "v"(kx0), [km] "v"(kmrd), [px] "v"(px), [pm] "v"(pm), [ps] "v"(ps), [pb] "s"(R_P + (s & 3) * P_SLOT), [pr] "s"(R_P + ((s + 1) & 3) * P_SLOT),
                       [kb] "s"(R_KX - KXL + xblk(s, h, para)), [fq] "s"(c.ctrl + (C_FLAG + (F_K + h) * 128) / 4) : QK_CL);
        QK_PINS;
        LITE(s); LOGS(0, s);
        TICK_SUM(10);
      }
      RAWBAR();
    } else {
      const int qt = w - 1;
      const int px = opq(amap(qt ? 16384 + h * 1024 + lane * 16 : h * 512 + (g >> 1) * 256 + cc * 16 + (g & 1) * 8, para));
      const int pm = opq(g < 2 ? amap(49152 + (qt * 32 + h) * 256 + cc * 16 + g * 8, para) : 1 << 30);
      const int ps = opq(cc - 4 * g), hq = opq(g * 512 + (16 * qt + cc) * 16);
#define QK_TOK(STEP) for (int s = 0; s < NSEG; ++s) { QK_PINS; \
        asm volatile(STEP : [bd] "+s"(c.budget), [rt] "+s"(tries), [sq] "+s"(seen) \
                     : QK_IN, [kx] "v"(kx0), [km] "v"(kmrd), [px] "v"(px), [pm] "v"(pm), [ps] "v"(ps), [hq] "v"(hq), [qb] "s"(R_QX + xblk(s, h, para)), [tb] "s"(R_PT + (s & 7) * PT_SLOT), \
                       [tr] "s"(R_PT + ((s + 2) & 7) * PT_SLOT), [sn] "s"(s + 1), [fq] "s"(c.ctrl + (C_FLAG + (F_Q + h) * 128) / 4) : QK_CL); \
        QK_PINS; TICK_SUM(10); }
      if (w == 1) { QK_TOK(QK_STEP1) } else { QK_TOK(QK_STEP2) }
    }
  }
  TICK_OUT();
  if (c.budget == 0u) __hip_atomic_store(c.ctrl + C_TMO / 4, 1u, RLX_AGENT);
}

DEV void shuffle_cols(Ctx& c, int scratch, const v4u (&rows)[8], v4u (&o)[8]) {
  const int g = c.lane >> 4, cc = c.lane & 15;
#pragma unroll
  for (int i = 0; i < 8; ++i) {
    v4u f;
#pragma unroll
    for (int r = 0; r < 4; ++r) f[r] = prm(rows[2 * r + 1][i >> 1], rows[2 * r][i >> 1], i & 1);
    sts16(c.L, scratch + (8 * cc + i) * 64 + ((g ^ (cc & 3)) << 4), f);
  }
  asm volatile("s_waitcnt lgkmcnt(0)" ::: "memory");
#pragma unroll
  for (int i = 0; i < 8; ++i) { const int col = 16 * i + cc; o[i] = lds16(c.L, scratch + col * 64 + ((g ^ ((col >> 3) & 3)) << 4)); }
  asm volatile("s_waitcnt lgkmcnt(0)" ::: "memory");
}

DEV void vo_role(const Params& p, Ctx& c, int h) {
  constexpr int WL = 0, MEMS = 131072, VM = 147456, VROW = 272;
  c.budget = SPIN_CAP;
  const int w = c.w, lane = c.lane = fresh_lane(), g = lane >> 4, cc = lane & 15;
  Clk k = {0u, 0u, 0u, 0u, 0u, 0u};
  anchor(k, true);
  v8u wa[32], wh[16];
  ask(c, claim_look(c));
  unsigned look = 0;
  bool claimed = false;
  hold(c, VO_AT);
  const rsrc_t rwo = mkrs(p.Wo, 4096 * 1024 * 2);
  const int voo = (h * 128 + 4 * g) * 2048 + (256 * ((cc >> 3) ^ (h >> 3)) + 64 * w + 8 * (cc & 7)) * 2;
  v4u buf[3][8];
  auto request_o = [&](int u) {
#pragma unroll
    for (int r = 0; r < 8; ++r) buf[u % 3][r] = ld16w<W_AUX>(rwo, voo ^ ((u >> 2) * 1024), (32 * (u & 3) + 16 * (r >> 2) + (r & 3)) * 2048);
  };
  {
    const rsrc_t rw = mkrs(p.Wv, 1024 * 4096 * 2);
    const int vo = (256 * w + krow_lane(g)) * 8192 + (h * 128 + 8 * cc) * 2;
    v4u vrow[8][8];
    auto request = [&](int u) {
#pragma unroll
      for (int r = 0; r < 8; ++r) vrow[u][r] = ld16w<W_AUX>(rw, vo, (64 * ((u >> 1) ^ turn(h, w)) + krow(4 * (u & 1) + (r >> 1), r & 1)) * 8192);
    };
#pragma unroll
    for (int u = 0; u < 3; ++u) request(u);
    SCHED();
    const int wr_ = MEMS + w * 8192 + g * 256;
    int sr[2];
#pragma unroll
    for (int t = 0; t < 2; ++t) { const int i = (2 * cc + t) & 7; sr[t] = opq(i * 1024 + g * 256 + ((4 * w + (cc >> 2) + 2 * i) & 15) * 16); }
#pragma unroll
    for (int u = 0; u < 8; ++u) {
#pragma unroll
      for (int i = 0; i < 8; ++i) {
        v4u f;
#pragma unroll
        for (int r = 0; r < 4; ++r) f[r] = prm(vrow[u][2 * r + 1][i >> 1], vrow[u][2 * r][i >> 1], i & 1);
        sts16(c.L, wr_ + i * 1024 + ((cc + 2 * i) & 15) * 16, f);
      }
      SCHED();
      if (u == 7) claimed = claim_try(c, look);
      if (u == 4) look = claim_look(c);
      if (u + 3 < 8) request(u + 3); else request_o(u - 5);
      BAR();
      v4u in[4][2];
#pragma unroll
      for (int t = 1; t >= 0; --t)
#pragma unroll
        for (int r = 0; r < 4; ++r) in[r][t] = lds16(c.L, sr[t] + MEMS + ((w + r) & 3) * 8192);
      SCHED();
#pragma unroll
      for (int j = 0; j < 16; ++j) {
        if (qk_i(j) != (u >> 1)) continue;
#pragma unroll
        for (int t = 1; t >= 0; --t) {
          const v4u o = in[qk_r(j)][t];
          if (t == 1 && (j & 1)) sts16(c.L, WL + (w * 16 + (j >> 1)) * 2048 + (u & 1) * 1024 + lane * 16, o);
          else {
            const int n = 3 * (j >> 1) + ((j & 1) ? 2 : t);
#pragma unroll
            for (int r = 0; r < 4; ++r) wa[n][4 * (u & 1) + r] = o[r];
#define M_(r) asm volatile("" : "+" r(wa[n]))
            if (u & 1) AT_ACC(M_, n);
#undef M_
          }
        }
      }
      BAR();
    }
  }
#pragma unroll
  for (int u = 0; u < 8; ++u) {
    v4u o[8];
    shuffle_cols(c, MEMS + w * 8192, buf[u % 3], o);
    SCHED();
    if (u + 3 < 8) request_o(u + 3);
#pragma unroll
    for (int i = 0; i < 4; ++i) {
      const int pr = 4 * (u >> 2) + i, ch = u & 3;
      const v8u f = {o[2 * i][0], o[2 * i + 1][0], o[2 * i][1], o[2 * i + 1][1], o[2 * i][2], o[2 * i + 1][2], o[2 * i][3], o[2 * i + 1][3]};
      if (ch < 2) {
        wh[2 * pr + ch] = f;
#define M_(r) asm volatile("" : "+" r(wh[2 * pr + ch]))
        AT_HOME(M_, 2 * pr + ch);
#undef M_
      } else if (ch == 2) {
        wa[24 + pr] = f;
#define M_(r) asm volatile("" : "+" r(wa[24 + pr]))
        AT_ACC(M_, 24 + pr);
#undef M_
      } else {
        sts16(c.L, WL + (w * 16 + 8 + pr) * 2048 + lane * 16, lo4(f));
        sts16(c.L, WL + (w * 16 + 8 + pr) * 2048 + 1024 + lane * 16, hi4(f));
      }
    }
    SCHED();
  }
  if (!claimed) claim(c, 0);
  BAR();
  STAMP(0);
  const unsigned vote = wait_verdict(c);
  const int par = vote_b(vote), para = vote_a(vote);
  const int moff = opq(amap(w * 4096 + lane * 16, par) + (turn(h, w) & 1) * 1024), m2 = R_MEM + (turn(h, w) & 2) * 1024;
  unsigned tries = 1u << 12;
  *(volatile LAS unsigned*)(c.L + 160512) = 0u;
  LGKM(0);
  const int idx = opq((lane & 1) ? 0xEEEE : 0x4444);
  const int u_off = opq((4 * w + g) * 16384 + (h ^ (8 * g)) * 256 + cc * 16);
  const int hq = opq((lane | w) ? 1 << 30 : amap(24576 + (32 + h) * 128, par));
  const int l0 = opq(WL + w * 32768 + lane * 16), l1 = opq(MEMS + lane * 16);
  const int vmw = opq(VM + 2 * g * VROW + (32 * w + 2 * cc) * 2);
  const int vmr = opq(VM + (cc >> 1) * VROW + g * 16);
  TICK_DECL;
  anchor(k, true);
  unsigned tg = k.cyc;
  Plan tt = {k.g - NS(100000), NS(T_DX)};
  for (int s = 0; s < NSEG; ++s) {
    const int pb = R_P + (s & 3) * P_SLOT;
    PIN_ACC0(wa); PIN_ACC1(wa); PIN_HOME(wh);
    asm volatile(VO_STEP
        : [bd] "+s"(c.budget), [rt] "+s"(tries)
        : [l0] "v"(l0), [l1] "v"(l1), [mo] "v"(moff), [ix] "v"(idx), [vw] "v"(vmw), [vr] "v"(vmr), [uo] "v"(u_off), [w] "s"(w), [rs] "s"(c.rs),
          [sb] "s"(m2 + (s & 3) * MEM_SLOT), [ub] "s"(u_slot(s, para) + (h >> 3) * 16 * 16384), [ur] "s"(u_slot(s + 2, para) + (2 * h + (w >> 1)) * 16384 + (w & 1) * 4096 - MEMS), [tg] "s"(tg + T_VLAG), [hq] "v"(hq), [pb] "s"(pb),
          [ca] "s"(k.cyc - T_DV), [k1] "s"(k.k1), [ga] "s"(k.g), [dr] "n"(T_RV), [pr] "s"(R_P + ((s + 1) & 3) * P_SLOT)
        : CL_S, CL_16_32, CL_160_200, CL_200_216, CL_216_256);
    PIN_ACC0(wa); PIN_ACC1(wa); PIN_HOME(wh);
    if (w == 0) {
      LITE(s);
      LOG(1, s, tg);
      const unsigned sv = ask_stamps(c, par, pb);
      SCHED();
      anchor(k);
      const unsigned gc = tt.gx + tt.dx + NS(T_M1 + T_CLR);
      const bool ontime = (int)(gc - k.g) > 0;
      if (ontime) { wait_cyc(to_cyc(k, gc)); RAWBAR(); }
      SCHED();
      advance(tt, latest(c, k, par, pb, sv));
      if (!ontime) { wait_cyc(to_cyc(k, tt.gx - NS(T_DY + T_SAFE - T_CLR))); RAWBAR(); }
      tg = to_cyc(k, tt.gx);
      LOG(2, s, k.cyc); LOG(3, s, k.g);
    }
    TICK_SUM(10);
    if (w == 0) { close_in(k, tg); tg = to_cyc(k, tt.gx); }
  }
  TICK_OUT();
  if (c.budget == 0u) __hip_atomic_store(c.ctrl + C_TMO / 4, 1u, RLX_AGENT);
}

DEV void y_role(const Params& p, Ctx& c, int y) {
  constexpr int ZR = 0, UL = 131072, CW = 155648;
  c.budget = SPIN_CAP;
  const int w = c.w, lane = c.lane = fresh_lane(), g = lane >> 4, cc = lane & 15;
  const v4u sent4 = {SENT, SENT, SENT, SENT};
  STAMP(2);
  Clk k = {0u, 0u, 0u, 0u, 0u, 0u};
  anchor(k, true);
  unsigned m0 = 0;
  if (w == 0) {
    unsigned best[2] = {~0u, ~0u};
    const unsigned first = claim_look(c);
    st4s(c.rs, y * 16384 + (lane >> 5) * 8192 + (lane & 31) * 4, R_U, SENT);
    ask(c, first);
#pragma unroll 1
    for (int n = 0; n < 12; ++n) {
#pragma unroll
      for (int r = 0; r < 2; ++r) {
        VMWAIT(0);
        const unsigned t0 = (unsigned)__builtin_amdgcn_s_memtime();
        LGKM(0);
        st4s(c.rs, y * 16384 + (lane & 31) * 4, R_U + r * 8192, SENT);
        VMWAIT(0);
        const unsigned t1 = (unsigned)__builtin_amdgcn_s_memtime();
        LGKM(0);
        best[r] = min(best[r], t1 - t0);
      }
      if (n + 1 >= PROBES && min(best[0], best[1]) < 600u && max(best[0], best[1]) > min(best[0], best[1]) + 128u) break;
    }
    STAMP(5);
    claim(c, CL_VOTED | (best[1] < best[0] ? CL_A : 0u));
    m0 = *(const GAS unsigned*)(p.mem0 + (lane >> 3) * 1024 + 16 * y + 2 * (lane & 7));
    m0 = m0 < SENT - 1u ? m0 : SENT - 1u;
    if (lane < 8) *(LAS unsigned*)(c.L + CW + lane * 4) = 0u;
    const unsigned v = wait_vote(c);
    STAMP(7);
    const int oa = y * 16384 + vote_a(v) * 8192 + lane * 16, ob = amap(y * 3072 + lane * 16, vote_b(v));
#pragma unroll
    for (int i = 0; i < 16; ++i) st16s(c.rs, oa + (i & 3) * 1024, R_U + (i >> 3) * MB + ((i >> 2) & 1) * 4096, sent4);
#pragma unroll
    for (int i = 0; i < 3; ++i) st16s(c.rs, amap(y * 3072 + i * 1024 + lane * 16, vote_b(v)), R_MEM, sent4);
#pragma unroll
    for (int i = 0; i < 8; ++i) st16s(c.rs, oa + (i & 3) * 1024, R_PT + (i >> 2) * 4096, sent4);
    (void)ob;
    VMWAIT(0);
    if (lane == 0) __hip_atomic_fetch_add(c.ctrl + (C_CLAIM + c.att * 1024) / 4 + c.slot, CL_FILLED, RLX_AGENT);
    STAMP(6);
  } else {
    if (YH && w < 3 && (y & 8) == 0) {
      hold(c, YH_AT);
#pragma unroll
      for (int i = 0; i < 8; ++i) {
        const u16* q = (i < 4) != K_FIRST ? p.Wq : p.Wk;
        if (i == 4) hold(c, YH_AT + YH_STEP);
        m0 += __builtin_amdgcn_raw_buffer_load_b32(mkrs(q, 8 * MB), slow_line(q, y >> 1, (y & 7) + 8 * (i & 3), lane) ^ ((w - 1) * 128), 0, 0);
      }
      asm volatile("" :: "v"(m0));
    }
  }
  BAR();
  const unsigned vote = wait_verdict(c);
  const int par = vote_b(vote), para = vote_a(vote);
  const int mo = amap(y * 256 + lane * 4, par);
  if (w == 0) st4s(c.rs, mo, R_MEM, m0);
  STAMP(0);
  const int rot = y >> 4;
  volatile LAS unsigned* const GO = (volatile LAS unsigned*)(c.L + CW);
  TICK_DECL;
  if (w == 0) {
    int xo[4];
#pragma unroll
    for (int i = 0; i < 4; ++i) xo[i] = amap((i ^ rot) * 4096 + lane * 16, par);
    const int pmo = amap(16384 + rot * 1024 + 2 * g * 128 + cc * 8, par), psel = (cc & 1) ? 0x03020706 : 0x05040100;
    constexpr int lead = T_U0, late = 0;
    anchor(k, true);
    Plan tt = {k.g - NS(100000), NS(T_DX)};
    unsigned done = k.cyc;
    const int so = amap(24576, par) + lane * 128;
    const int idx = opq((lane & 1) ? 0xEEEE : 0x4444);
    const int uo = y * 16384 + lane * 16;
    const int mw = amap(y * 256 + (2 * g + (cc & 1)) * 32 + (cc >> 1) * 4, par);
    for (int s = 0; s < NSEG - 1; ++s) {
      if (s == 0) RAWBAR();
      const int za = ZR + (s & 3) * 32768 + rot * 8192 + lane * 16;
      v32u z0, z1, z2, z3;
      asm volatile(
          "ds_read_b128 a[0:3], %[z0] offset:0\n\t" "ds_read_b128 a[4:7], %[z0] offset:1024\n\t" "ds_read_b128 a[8:11], %[z0] offset:2048\n\t" "ds_read_b128 a[12:15], %[z0] offset:3072\n\t"
          "ds_read_b128 a[16:19], %[z0] offset:4096\n\t" "ds_read_b128 a[20:23], %[z0] offset:5120\n\t" "ds_read_b128 a[24:27], %[z0] offset:6144\n\t" "ds_read_b128 a[28:31], %[z0] offset:7168\n\t"
          "ds_read_b128 a[32:35], %[z1] offset:0\n\t" "ds_read_b128 a[36:39], %[z1] offset:1024\n\t" "ds_read_b128 a[40:43], %[z1] offset:2048\n\t" "ds_read_b128 a[44:47], %[z1] offset:3072\n\t"
          "ds_read_b128 a[48:51], %[z1] offset:4096\n\t" "ds_read_b128 a[52:55], %[z1] offset:5120\n\t" "ds_read_b128 a[56:59], %[z1] offset:6144\n\t" "ds_read_b128 a[60:63], %[z1] offset:7168\n\t"
          "ds_read_b128 a[64:67], %[z2] offset:0\n\t" "ds_read_b128 a[68:71], %[z2] offset:1024\n\t" "ds_read_b128 a[72:75], %[z2] offset:2048\n\t" "ds_read_b128 a[76:79], %[z2] offset:3072\n\t"
          "ds_read_b128 a[80:83], %[z2] offset:4096\n\t" "ds_read_b128 a[84:87], %[z2] offset:5120\n\t" "ds_read_b128 a[88:91], %[z2] offset:6144\n\t" "ds_read_b128 a[92:95], %[z2] offset:7168\n\t"
          "ds_read_b128 a[96:99], %[z3] offset:0\n\t" "ds_read_b128 a[100:103], %[z3] offset:1024\n\t" "ds_read_b128 a[104:107], %[z3] offset:2048\n\t" "ds_read_b128 a[108:111], %[z3] offset:3072\n\t"
          "ds_read_b128 a[112:115], %[z3] offset:4096\n\t" "ds_read_b128 a[116:119], %[z3] offset:5120\n\t" "ds_read_b128 a[120:123], %[z3] offset:6144\n\t" "ds_read_b128 a[124:127], %[z3] offset:7168\n\t"
          "s_waitcnt lgkmcnt(0)"
          : "={a[0:31]}"(z0), "={a[32:63]}"(z1), "={a[64:95]}"(z2), "={a[96:127]}"(z3) : [z0] "v"(za), [z1] "v"(za ^ 8192), [z2] "v"(za ^ 16384), [z3] "v"(za ^ 24576) : "memory");
      const int pb = R_P + (s & 3) * P_SLOT;
      v32u uu;
      const int ub = u_slot(s, para);
      TICK(0);
      anchor(k);
      TICK(8);
      unsigned gp = tt.gx + tt.dx + NS(T_M1), lt = late, sv = SENT;
      unsigned bad = (int)(done - to_cyc(k, tt.gx)) > T_OVH;
      TICK(4);
      LOG(2, s, k.cyc); LOG(3, s, k.g);
#pragma unroll 1
      for (;;) {
        if (bad) {
          const bool all = allok(sv);
          const unsigned last = latest(c, k, par, pb, sv);
          if (!all) mark(k);
          gp = when_p(tt, last);
          lt = 1;
        }
        close_in(k, to_cyc(k, gp) - (lt ? 96 : lead));
        const unsigned tg = to_cyc(k, gp);
        if (!bad) LOG(1, s, tg);
        asm volatile(Y_STEP
          : "=&{v[144:175]}"(uu), [bad] "=&s"(bad), [sv] "=&v"(sv)
          : [x0] "v"(xo[0]), [x1] "v"(xo[1]), [x2] "v"(xo[2]), [x3] "v"(xo[3]), [pm] "v"(pmo), [u0] "v"(uo + (rot ^ (y & 3)) * 2048), [mw] "v"(mw), [ps] "v"(psel), [ix] "v"(idx),
            [rs] "s"(c.rs), [pb] "s"(pb), [ub] "s"(ub), [mb] "s"(R_MEM + ((s + 1) & 3) * MEM_SLOT), [mr] "s"(R_MEM + ((s + 3) & 3) * MEM_SLOT), [tu] "s"(tg - (lt ? 96 : lead)), [tg] "s"(tg - 32), [late] "s"(lt),
            [so] "v"(so), [bd] "s"(c.budget),
            "{a[0:31]}"(z0), "{a[32:63]}"(z1), "{a[64:95]}"(z2), "{a[96:127]}"(z3)
          : CL_S, CL_120_128, CL_176_200, CL_200_216, CL_216_256);
        bad = __builtin_amdgcn_readfirstlane(bad);
        if (bad != 1u) break;
        spend(c);
      }
      done = bad;
      SCHED();
      const int ul = opq(UL + rot * 2048 + lane * 16);
#pragma unroll
      for (int i = 0; i < 8; ++i) sts16(c.L, (ul ^ ((i >> 1) * 2048)) + (i & 1) * 1024, v4u{uu[4 * i], uu[4 * i + 1], uu[4 * i + 2], uu[4 * i + 3]});
      asm volatile("" ::: "memory");
      *GO = s + 1;
      asm volatile("s_waitcnt vmcnt(2)" : "+v"(sv));
      advance(tt, latest(c, k, par, pb, sv));
      TICK(3);
      LITE(s); LOG(0, s, done);
      TICK_LDS(CW + 256);
    }
    {
      anchor(k);
      wait_cyc(to_cyc(k, tt.gx + tt.dx + NS(T_M1)));
      const int ub = u_slot(NSEG - 1, para);
      v4u u[8];
#pragma unroll 1
      for (;;) {
        asm volatile("" ::: "memory");
#pragma unroll
        for (int i = 0; i < 8; ++i) u[i] = ld16s(c.rs, uo + (i & 3) * 1024, ub + (i >> 2) * 4096);
        SCHED();
        unsigned t = 0;
#pragma unroll
        for (int i = 0; i < 8; ++i) t = umax(t, top(u[i]));
        if (allok(t) || spend(c)) break;
      }
      RAWBAR();
#pragma unroll
      for (int i = 0; i < 8; ++i) sts16(c.L, UL + (i ^ (2 * (y & 3))) * 1024 + lane * 16, u[i]);
      asm volatile("" ::: "memory");
      *GO = NSEG;
    }
    TICK_DUMP(CW + 256);
  } else if (w < 3) {
    const rsrc_t ro = mkrs(p.out, 2048 * 2048 * 2);
    auto tokens = [&](auto QT) {
      constexpr int qt = QT.value, NX = qt ? 32 : 16;
      RAWBAR();
      for (int s = 0; s < NSEG; ++s) {
        const int tb = R_PT + (s & 7) * PT_SLOT + para * 8192;
        int ua[4], za[4], xb[4], mb[4];
#pragma unroll
        for (int i = 0; i < 4; ++i) {
          ua[i] = opq(UL + (i ^ rot) * 2048 + lane * 16);
          za[i] = opq(ZR + (s & 3) * 32768 + (i ^ rot) * 8192 + (qt ? lane * 16 : (g >> 1) * 1024 + (g & 1) * 512 + cc * 16));
          xb[i] = tb + amap(qt * 16384 + (i ^ rot) * (qt ? 8192 : 4096), 0);
          mb[i] = tb + amap(49152 + qt * 8192, 0) + (i ^ rot) * 2048;
        }
        SCHED();
        RAWBAR();
        if (TOK_WAIT) __builtin_amdgcn_s_sleep(TOK_WAIT);
        TICK(0);
        f32x4 a;
#pragma unroll 1
        for (;;) {
          asm volatile("" ::: "memory");
          v4u pt[NX + 8];
#pragma unroll
          for (int i = 0; i < NX; ++i) pt[i] = ld16s(c.rs, (i & 3) * 1024 + lane * 16, qt ? xb[i >> 3] + ((i >> 2) & 1) * 4096 : xb[i >> 2]);
#pragma unroll
          for (int i = 0; i < 8; ++i) pt[NX + i] = ld16s(c.rs, (i & 1) * 1024 + lane * 16, mb[i >> 1]);
          SCHED();
          TICK(1);
#pragma unroll 1
          while (__builtin_amdgcn_readfirstlane(*GO) < (unsigned)s + 1u) { if (spend(c)) break; }
          asm volatile("" ::: "memory");
          f32x4 d[2] = {f32x4{0.f, 0.f, 0.f, 0.f}, f32x4{0.f, 0.f, 0.f, 0.f}};
          v4u fa[8];
          auto frag = [&](int i) {
            if (i >= NX) fa[i & 7] = lds16(c.L, ua[(i - NX) >> 1] + (i & 1) * 1024);
            else if (qt) fa[i & 7] = lds16(c.L, za[i >> 3] + (i & 7) * 1024);
            else {
              const v2u lo = *(LAS v2u*)(c.L + za[i >> 2] + (i & 3) * 2048), hi = *(LAS v2u*)(c.L + za[i >> 2] + (i & 3) * 2048 + 256);
              fa[i & 7] = v4u{lo.x, lo.y, hi.x, hi.y};
            }
          };
#pragma unroll
          for (int i = 0; i < 7; ++i) frag(i);
          SCHED();
#pragma unroll
          for (int i = 0; i < NX + 8; ++i) {
            if (i + 7 < NX + 8) frag(i + 7);
            d[i & 1] = mfma16(fa[i & 7], pt[i], d[i & 1]);
            SCHED();
          }
          a = d[0] + d[1];
          if (!__any(a[0] != a[0])) break;
          unsigned t = 0;
#pragma unroll
          for (int i = 0; i < NX + 8; ++i) t = umax(t, top(pt[i]));
          if (allok(t) || spend(c)) break;
        }
        __builtin_amdgcn_raw_buffer_store_b64(v2u{pk(a[0], a[1]), pk(a[2], a[3])}, ro, (16 * qt + cc) * 4096 + (16 * y + 4 * g) * 2, s * 32 * 4096, OUT_AUX);
        TICK(2);
        TICK_SUM(3);
      }
    };
    if (w == 1) tokens(IC<0>{}); else tokens(IC<1>{});
  } else {
    unsigned seenZ = NOFLAG;
    const rsrc_t rc = mkrs((const void*)c.ctrl, CTRL_BYTES);
    const int fo = lane < 32 ? lane * 128 : 1 << 30;
    __builtin_amdgcn_s_setprio(3);
    auto least = [&](unsigned v) {
      v |= lane < 32 ? 0u : ~0u;
#pragma unroll
      for (int o = 1; o < 32; o <<= 1) v = min(v, (unsigned)__shfl_xor((int)v, o));
      seenZ = umax(seenZ, __builtin_amdgcn_readfirstlane(v));
    };
    auto await = [&](unsigned need) {
#pragma unroll 1
      while (seenZ < need) {
        asm volatile("" ::: "memory");
        least(__builtin_amdgcn_raw_buffer_load_b32(rc, fo, C_FLAG + (F_Z + (y >> 4) * 32) * 128, AUX_SC1));
        if (seenZ >= need || spend(c)) break;
        __builtin_amdgcn_s_sleep(4);
      }
      __builtin_amdgcn_fence(__ATOMIC_ACQUIRE, "wavefront");
    };
    for (int n = 0; n < NSEG; ++n) {
      TICK(0);
      await(n + 1);
      TICK(1);
      v4u z[32];
      int za[8];
#pragma unroll
      for (int i = 0; i < 8; ++i) za[i] = opq(ZR + (n & 3) * 32768 + (i ^ z_mix(y)) * 1024 + lane * 16);
      unsigned look;
#pragma unroll
      for (int i = 0; i < 32 + Z_WIN; ++i) {
        if (i < 32) z[i] = ld16z(c.rs, (i & 3) * 1024 + lane * 16, R_Z + (n & (ZRING - 1)) * Z_SLOT + y * 65536 + ((para ^ (y >> 5)) & 1) * 8192 + amap((i >> 2) * 4096, 0));
        if (i == 32) { look = __builtin_amdgcn_raw_buffer_load_b32(rc, seenZ < (unsigned)n + 2u ? fo : 1 << 30, C_FLAG + (F_Z + (y >> 4) * 32) * 128, AUX_SC1); TICK(4); }
        SCHED();
        if (i >= Z_WIN) sts16(c.L, za[(i - Z_WIN) & 7] + ((i - Z_WIN) >> 3) * 8192, z[i - Z_WIN]);
        else asm volatile("s_nop " STR(Z_GAP));
        if (i == 30 + Z_WIN) { TICK(3); }
        SCHED();
      }
      least(look);
      if (n == 0) await(PRIME);
      TICK(2);
      { const int s = n; (void)s; TICK_SUM(3); }
      BAR();
    }
    RAWBAR();
  }
  TICK_OUT();
  if (c.budget == 0u) __hip_atomic_store(c.ctrl + C_TMO / 4, 1u, RLX_AGENT);
}

#define MF1A(d, a, b) asm volatile(MF16 "%0, %1, %2, %0" : "+v"(d) : "a"(a), "v"(b))
#define MF32A(d, a, b) asm volatile("v_mfma_f32_32x32x16_bf16 %0, %1, %2, %0" : "+v"(d) : "a"(a), "v"(b))
#define MF32AZ(d, a, b) asm volatile("v_mfma_f32_32x32x16_bf16 %0, %1, %2, 0" : "=&v"(d) : "a"(a), "v"(b))
#define MF1B(d, a, b) asm volatile(MF16 "%0, %1, %2, %0" : "+v"(d) : "v"(a), "a"(b))
#define MF1BZ(d, a, b) asm volatile(MF16 "%0, %1, %2, 0" : "=&v"(d) : "v"(a), "a"(b))
#define MF1AZ(d, a, b) asm volatile(MF16 "%0, %1, %2, 0" : "=&v"(d) : "a"(a), "v"(b))
DEV void g_role(const Params& p, Ctx& c, int mat, int h, int gi) {
  constexpr int EX = 131072;
  c.budget = SPIN_CAP;
  const int w = c.w, lane = c.lane = fresh_lane(), g = lane >> 4, cc = lane & 15;
  unsigned look = claim_look(c);
  const int flip = FLIP && mat < 2 ? ((h >> 4) & 1) ^ 1 : 0;
  unsigned t[7];
  {
    auto pull = [&](const u16* q, int bytes, int v, int so) { return __builtin_amdgcn_raw_buffer_load_b32(mkrs(q, bytes), v, so, 0); };
    const int all = mat < 2 ? 8 * MB : 0, xq = mat == 0 ? 4 * MB : 0;
    t[6] = pull(p.Wv, SLOW_V * all, slow_line(p.Wv, h, (h & 7) + 8 * w, lane) ^ (mat * 128), 0);
    t[0] = pull(p.x, xq, lane < 16 ? (((h & 7) * 4 + w) * 16 + lane) * 128 : 1 << 30, 0);
#pragma unroll
    for (int i = 0; i < 3; ++i) t[1 + i] = pull(p.Wv, HELP_V * all, (256 * w + 4 * (h & 7) + g) * 8192 + (h >> 3) * 2048 + cc * 128, (2 + 2 * i + mat) * 32 * 8192);
    SCHED();
    if (mat < 2) ask(c, look);
    SCHED();
#pragma unroll
    for (int i = 0; i < 2; ++i) t[4 + i] = pull(p.Wo, HELP_O * all, (h * 128 + (lane >> 1)) * 2048 + w * 512 + 256 + (lane & 1) * 128, (2 * i + mat) * 32 * 2048);
    SCHED();
    if (mat == 0) claim(c, 0);
    hold(c, mat == 0 ? GQ_AT : mat == 1 ? GK_AT : 0);
  }
  const rsrc_t rx = mkrs(p.x, 2048 * 2048);
  const int xs = w * 16384 + ((lane * 16) ^ ((w & 1) << 7));

  v32u wa[8];
  {
    const rsrc_t rw = mkrs(mat == 0 ? p.Wq : mat == 1 ? p.Wk : p.Wv, 1024 * 4096 * 2);
    const int vo0 = opq((256 * w + 128 * flip + 8 * g) * 8192 + (h * 128 + 8 * cc) * 2), vo1 = opq(vo0 ^ (128 * 8192));
    v4u row[8][8];
    auto request = [&](int u) {
#pragma unroll
      for (int r = 0; r < 8; ++r) {
        row[u][r] = ld16w<0>(rw, (u & 4) ? vo1 : vo0, (32 * (u & 3) + r) * 8192);
        if ((r & 3) == 3) { const int n = 2 * u + (r >> 2); dma16(rx, c.L + w * 16384 + n * 1024, (xs ^ ((n >> 1) << 4)) + n * 1024); }
      }
    };
    const unsigned look0 = claim_look(c);
    bool claimed = false;
    request(0);
    SCHED();
    if (mat == 2) ask(c, look);
    SCHED();
    request(1); request(2);
    if (G_DEEP) request(3);
    SCHED();
    const int sw_ = EX + w * 8192 + g * 256;
    const int sr0 = opq(EX + (cc & 3) * 1024 + g * 256 + ((4 * w + (cc >> 2) + 4 * cc) & 15) * 16), sr1 = sr0 + 4096;
#pragma unroll
    for (int u = 0; u < 8; ++u) {
#pragma unroll
      for (int i = 0; i < 8; ++i) {
        v4u f;
#pragma unroll
        for (int r = 0; r < 4; ++r) f[r] = prm(row[u][2 * r + 1][i >> 1], row[u][2 * r][i >> 1], i & 1);
        sts16(c.L, sw_ + i * 1024 + ((cc + 4 * i) & 15) * 16, f);
      }
      SCHED();
      if (u == 0) asm volatile("" :: "v"(t[0]), "v"(t[1]), "v"(t[2]), "v"(t[3]), "v"(t[4]), "v"(t[5]), "v"(t[6]));
      if (u == 0) claimed = mat == 0 || claim_try(c, look0);
      if (u == 4 - G_DEEP) look = claim_look(c);
      if (u + 3 + G_DEEP < 8) request(u + 3 + G_DEEP);
      if (u == 7 && !claimed && !claim_try(c, look)) claim(c, 0);
      BAR();
      v8u in[4];
#pragma unroll
      for (int q = 0; q < 4; ++q) in[q] = cat(lds16(c.L, sr0 + q * 8192), lds16(c.L, sr1 + q * 8192));
      SCHED();
#pragma unroll
      for (int q = 0; q < 4; ++q) {
        v8u f = in[q];
#define M_(r) asm volatile("" : "+" r(f))
        AT_ACC(M_, 8 * q + u);
#undef M_
#pragma unroll
        for (int r = 0; r < 8; ++r) wa[2 * q + (u >> 2)][8 * (u & 3) + r] = f[r];
      }
      BAR();
    }
  }
  STAMP(0);
  const int xpar = vote_a(wait_vote(c)) ^ (mat >> 1);
  TICK_DECL;
  asm volatile(G_LOOP
      :: [xo] "v"(cc * 2048 + flip * 256 + ((g ^ cc) << 4)), [xs] "v"(xs), [so] "v"((4 * w + g) * 512 + cc * 16), [fo] "v"((w | lane) ? 1 << 30 : 0),
         [rs] "s"(c.rs), [rx] "s"(rx), [rc] "s"(mkrs((const void*)c.ctrl, CTRL_BYTES)), [fl] "s"(C_FLAG + (mat * 32 + h) * 128), [xp] "s"(xpar),
         [ob] "s"((mat == 1 ? R_KX : R_QX) + h * 16384), [lw] "s"(w * 16384), GTRACE ACC_IN(wa)
      : CL_S, CL_16_32, CL_32_100);
}

DEV void zc_role(const Params& p, Ctx& c, int h) {
  c.budget = SPIN_CAP;
  const int w = c.w, lane = c.lane = fresh_lane(), g = lane >> 4, cc = lane & 15, h2 = lane >> 5, r32 = lane & 31;
  v32u wa[8];
  const unsigned first = claim_look(c);
  {
    const rsrc_t rw = mkrs(p.Wo, 4096 * 1024 * 2);
    const int vo = (h * 128 + 8 * g) * 2048 + (256 * w + 8 * cc) * 2;
    v4u row[8][8];
    auto request = [&](int u) {
#pragma unroll
      for (int r = 0; r < 8; ++r) row[u][r] = ld16w<0>(rw, vo, (32 * (u & 3) + r) * 2048 + (u >> 2) * 256);
    };
    request(0);
    SCHED();
    ask(c, first);
    SCHED();
    request(1); request(2);
    unsigned look = 0;
    const int sw_ = w * 8192 + g * 256, sr_ = w * 8192 + (cc & 7) * 1024 + g * 256;
    v8u two[8];
#pragma unroll
    for (int u = 0; u < 8; ++u) {
#pragma unroll
      for (int i = 0; i < 8; ++i) {
        v4u f;
#pragma unroll
        for (int r = 0; r < 4; ++r) f[r] = prm(row[u][2 * r + 1][i >> 1], row[u][2 * r][i >> 1], i & 1);
        sts16(c.L, sw_ + i * 1024 + ((cc + 2 * i) & 15) * 16, f);
      }
      SCHED();
      if (u == 4) look = claim_look(c);
      if (u + 3 < 8) request(u + 3);
      if (u == 7 && !claim_try(c, look)) claim(c, 0);
      LGKM(0);
#pragma unroll
      for (int q = 0; q < 8; ++q) {
        const v4u o = lds16(c.L, sr_ + ((2 * q + (cc >> 3) + 2 * cc) & 15) * 16);
#pragma unroll
        for (int r = 0; r < 4; ++r) two[q][4 * (u & 1) + r] = o[r];
      }
      SCHED();
      if (u & 1) {
#pragma unroll
        for (int q = 0; q < 8; ++q) {
          const int n = 2 * (8 * (u >> 2) + q) + ((u >> 1) & 1);
#define M_(r) asm volatile("" : "+" r(two[q]))
          AT_ACC(M_, n);
#undef M_
#pragma unroll
          for (int r = 0; r < 8; ++r) wa[n >> 2][8 * (n & 3) + r] = two[q][r];
        }
      }
      LGKM(0);
      SCHED();
    }
  }
  STAMP(0);
  {
    unsigned t = 0;
#pragma unroll
    for (int i = 0; i < 4; ++i) t += __builtin_amdgcn_raw_buffer_load_b32(mkrs(p.x, 4 * MB), ((4 * h + w) * 64 + lane) * 128, i * MB, 0);
    asm volatile("" :: "v"(t));
  }
  BAR();
  const int para = vote_a(wait_vote(c));
  const int ao = g * 512 + cc * 16, zo = 16 * w * 65536 + lane * 16 + ((para ^ (w >> 1)) & 1) * 8192;
  TICK_DECL;
  asm volatile(ZC_LOOP
      : [bd] "+s"(c.budget)
      : [ao] "v"(ao), [a1] "v"(ao + 4096), [z1] "v"(zo + amap((h ^ z_mix(16 * w)) * 1024, 0)), [z2] "v"(zo + 65536 + amap((h ^ z_mix(16 * w + 1)) * 1024, 0)), [fo] "v"(lane ? 1 << 30 : 0),
        [rs] "s"(c.rs), [rc] "s"(mkrs((const void*)c.ctrl, CTRL_BYTES)), [fv] "s"(C_FLAG + (F_V + h) * 128), [fs] "s"(C_FLAG + (F_S + h) * 128),
        [fz] "s"(C_FLAG + (F_Z + w * 32 + h) * 128), [xp] "s"(para ^ 1), [vb] "s"(R_QX + h * 16384), [vd] "s"(C_VERDICT + c.att * 128), [z0] "n"(ZC_PRIME), [zb] "n"(R_Z), [rg] "n"(ZRING - 2), [rm] "n"(ZRING - 1), GTRACE ACC_IN(wa)
      : CL_S, CL_16_32, CL_32_100);
  if (c.budget == 0u) __hip_atomic_store(c.ctrl + C_TMO / 4, 1u, RLX_AGENT);
}

DEV void conv_role(const Params& p, Ctx& c, int cg, int far) {
  constexpr int FS = 4160, PL = 1280, UO = 4 * FS, WS = UO + 8 * PL;
  const int w = c.w, lane = c.lane = fresh_lane(), tid = w * 64 + lane, g = lane >> 4, cc = lane & 15;
  const int d0 = 8 * cg + 2 * w;
  const int pg = ((far ^ (cg >> 5)) & 1) * 8192;
  auto crow = [](int ch) { return (ch >> 1) * 16384 + (ch & 1) * 4096; };
  const int de = (cg >> 3) * 64 + (tid & 7) * 8, t0 = (cg & 7) * 256 + (tid >> 3) * 8;
  auto group_sync = [&](int phase) {
    gu32* cnt = c.ctrl + (C_CONV + (cg >> 3) * 16 + phase * 8) / 4;
    VMWAIT(0);
    BAR();
    if (w == 0) {
      if (lane == 0) __hip_atomic_fetch_add(cnt, 1u, RLX_AGENT);
#pragma unroll 1
      while (__builtin_amdgcn_readfirstlane(__hip_atomic_load(cnt, RLX_AGENT)) < 8u) { if (spend(c)) break; __builtin_amdgcn_s_sleep(2); }
    }
    BAR();
  };
  v4u fr[2][4], ur[2][4], qr[8];
  unsigned up[2][4];
  const int so = cc * 64 + g * 16;
  const unsigned dd = __builtin_amdgcn_raw_buffer_load_b32(mkrs(p.ssmD, 2048), d0 * 2, 0, CONV_AUX);
  BAR();
  {
    const rsrc_t rk = mkrs(p.bk, 2048 * 2048), rv = mkrs(p.bv, 2048 * 2048);
    v4u kr[11], vr[8], sh[4];
#pragma unroll
    for (int i = 0; i < 4; ++i) sh[i] = ld16c(mkrs(p.shift, 8192), de * 8 + i * 16, 0);
#pragma unroll
    for (int i = 0; i < 11; ++i) kr[i] = ld16c(rk, (t0 + i - 3) * 2048 + de * 2, 0);
#pragma unroll
    for (int i = 0; i < 8; ++i) vr[i] = ld16c(rv, (t0 + i) * 2048 + de * 2, 0);
#pragma unroll
    for (int i = 0; i < 8; ++i) fr[i >> 2][i & 3] = ld16c(mkrs(p.ssmk, 4 * MB), lane * 16 + (i & 3) * 1024, (d0 + (i >> 2)) * 4096);
    SCHED();
    if (lane < 16) sts16(c.L, w * WS + (lane >> 2) * FS + (lane & 3) * 16, v4u{0, 0, 0, 0});
#pragma unroll
    for (int i = 0; i < 2; ++i) sts16(c.L, w * WS + UO + (4 * i + g) * PL + cc * 16, v4u{0, 0, 0, 0});
#pragma unroll
    for (int ch = 0; ch < 8; ++ch) {
      const unsigned sa = sh[ch >> 1][(ch & 1) * 2], sb = sh[ch >> 1][(ch & 1) * 2 + 1];
      const float s0 = blo(sa), s1 = bhi(sa), s2 = blo(sb), s3 = bhi(sb);
      float kk[11], o[8];
#pragma unroll
      for (int i = 0; i < 11; ++i) kk[i] = (ch & 1) ? bhi(kr[i][ch >> 1]) : blo(kr[i][ch >> 1]);
#pragma unroll
      for (int i = 0; i < 8; ++i) o[i] = fmaf(kk[i + 3], s0, fmaf(kk[i + 2], s1, fmaf(kk[i + 1], s2, kk[i] * s3))) * ((ch & 1) ? bhi(vr[i][ch >> 1]) : blo(vr[i][ch >> 1]));
      st16s(c.rs, de * 8192 + t0 * 2, R_KVT + pg + crow(ch), v4u{pk(o[0], o[1]), pk(o[2], o[3]), pk(o[4], o[5]), pk(o[6], o[7])});
    }
    group_sync(0);
#pragma unroll
    for (int i = 0; i < 8; ++i) {
      const int b = R_KVT + pg + d0 * 8192 + (i >> 2) * 4096 + (i & 3) * 1024;
      ur[i >> 2][i & 3] = ld16s(c.rs, so, b);
      up[i >> 2][i & 3] = __builtin_amdgcn_raw_buffer_load_b32(c.rs, (i & 3) == 0 && lane == 0 ? 1 << 30 : so, b - 4, AUX_SC1);
    }
#pragma unroll
    for (int i = 0; i < 8; ++i) qr[i] = ld16c(mkrs(p.bq, 4 * MB), (t0 + i) * 2048 + de * 2, 0);
  }
  STAMP(5);
  const int fw = opq(w * WS + 48 + lane * 16), uw = opq(w * WS + UO + g * PL + cc * 16);
  const int fa = opq(w * WS + (cc & 3) * FS + (3 + (cc >> 2) - g) * 16);
  sfor<2>([&](auto CH) __attribute__((always_inline)) {
    constexpr int ch = CH.value;
    SCHED();
#pragma unroll
    for (int i = 0; i < 4; ++i) {
      const v4u f = fr[ch][i], a = ur[ch][i];
      sts16(c.L, fw + 3 * FS + i * 1024, f);
      sts8(c.L, fw + FS + i * 1024 + 8, v2u{f[0], f[1]}); sts8(c.L, fw + FS + i * 1024 + 16, v2u{f[2], f[3]});
#pragma unroll
      for (int r = 0; r < 2; ++r) {
        const int o = fw + 2 * r * FS + i * 1024 + 12 - 8 * r;
        *(LAS unsigned*)(c.L + o) = f[0]; sts8(c.L, o + 4, v2u{f[1], f[2]}); *(LAS unsigned*)(c.L + o + 12) = f[3];
      }
      v4u r0, r1;
#pragma unroll
      for (int r = 0; r < 4; ++r) {
        r0[r] = __builtin_amdgcn_alignbyte(a[3 - r], a[3 - r], 2);
        r1[r] = __builtin_amdgcn_perm(r < 3 ? a[2 - r] : up[ch][i], a[3 - r], 0x07060100u);
      }
      sts16(c.L, uw + (16 + 16 * i) * 16, r0);
      sts16(c.L, uw + 4 * PL + (16 + 16 * i) * 16, r1);
    }
    f32x4 acc[4][2];
#pragma unroll
    for (int i = 0; i < 8; ++i) acc[i >> 1][i & 1] = f32x4{0.f, 0.f, 0.f, 0.f};
    v4u A[2][4], B[2][8];
    auto frag = [&](int d, int n) {
      constexpr int order[12] = {0, 4, 5, 6, 7, 8, 9, 10, 11, 1, 2, 3};
      const int x = order[n];
      if (x < 4) A[d & 1][x] = lds16(c.L, fa + (d + 16 * x) * 64);
      else B[d & 1][x - 4] = lds16(c.L, uw + ((x - 4) & 1) * 4 * PL + (16 + 16 * ((x - 4) >> 1) - d) * 16);
    };
    asm volatile("" ::: "memory");
#pragma unroll
    for (int n = 0; n < 12; ++n) frag(0, n);
    SCHED();
    sfor<16>([&](auto D) __attribute__((always_inline)) {
      constexpr int d = D.value;
      int n = 0;
#pragma unroll
      for (int k = 0; k < 4; ++k)
#pragma unroll
        for (int m = 0; m + k < 4; ++m)
#pragma unroll
          for (int e = 0; e < 2; ++e) {
            acc[m + k][e] = mfma16(A[d & 1][k], B[d & 1][2 * m + e], acc[m + k][e]);
            if (d < 15 && n < 12) frag(d + 1, n);
            ++n;
            SCHED();
          }
    });
    const float dv = ch ? bhi(dd) : blo(dd);
#pragma unroll
    for (int q = 0; q < 4; ++q) {
      v4u z;
#pragma unroll
      for (int i = 0; i < 4; ++i) {
        const unsigned yy = pk(acc[q][1][i], acc[q][0][i]), ud = pk(blo(ur[ch][q][i]) * dv, bhi(ur[ch][q][i]) * dv);
        z[i] = pk(blo(yy) + blo(ud), bhi(yy) + bhi(ud));
      }
      st16s(c.rs, so + q * 1024, R_YT + pg + d0 * 8192 + ch * 4096, z);
    }
  });
  STAMP(6);
  {
    const rsrc_t ro = mkrs(p.out, 2048 * 2048 * 2);
    v4u yv[8];
    group_sync(1);
#pragma unroll
    for (int ch = 0; ch < 8; ++ch) yv[ch] = ld16s(c.rs, de * 8192 + t0 * 2, R_YT + pg + crow(ch));
#pragma unroll
    for (int i = 0; i < 8; ++i) {
      float o[8];
#pragma unroll
      for (int ch = 0; ch < 8; ++ch) o[ch] = ((ch & 1) ? bhi(qr[i][ch >> 1]) : blo(qr[i][ch >> 1])) * ((i & 1) ? bhi(yv[ch][i >> 1]) : blo(yv[ch][i >> 1]));
      __builtin_amdgcn_raw_buffer_store_b128(v4u{pk(o[0], o[1]), pk(o[2], o[3]), pk(o[4], o[5]), pk(o[6], o[7])}, ro, (t0 + i) * 4096 + (1024 + de) * 2, 0, OUT_AUX2);
    }
  }
  if (c.budget == 0u) __hip_atomic_store(c.ctrl + C_TMO / 4, 1u, RLX_AGENT);
}

extern "C" __global__ void __launch_bounds__(256, 1) mk_mega(Params p) {
  extern __shared__ __attribute__((aligned(16))) unsigned char lds_raw[];
  Ctx c;
  c.ctrl = (gu32*)p.ctrl;
  c.rs = mkrs(p.data, R_END);
  c.L = (lds_t)lds_raw;
  c.w = __builtin_amdgcn_readfirstlane(threadIdx.x >> 6);
  c.lane = threadIdx.x & 63;
  c.t0 = (unsigned)__builtin_amdgcn_s_memrealtime();
  c.b = (int)(blockIdx.x & 255u);
  c.att = (int)(blockIdx.x >> 8);
  c.ask = 0;
  c.slot = (c.att ? (c.b & 7) : (int)((unsigned)__builtin_amdgcn_s_getreg(20 | (3 << 11)) & 7u)) * 32 + (c.b >> 3);
  const int x = c.slot >> 5, t = c.slot & 31;
  if (c.w == 1) {
    asm volatile("s_getpc_b64 s[60:61]\n1:\n\ts_sub_u32 s60, s60, 1b-mk_mega\n\ts_subb_u32 s61, s61, 0\n\ts_mov_b32 s62, mk_tail-mk_mega\n\ts_mov_b32 s63, 0x20000\n\t"
                 "buffer_load_dword v255, %0, s[60:63], 0 offen\n\ts_waitcnt vmcnt(0)" :: "v"((c.lane * 32 + t) * 128) : "s60", "s61", "s62", "s63", "v255", "scc", "memory");
  }
  if (c.b == SERVER && c.w == 3) serve(c);
  if (x < 4) {
    if (t < 8) qk_role(p, c, x * 8 + t);
    else if (t < 16) vo_role(p, c, x * 8 + t - 8);
    else y_role(p, c, x * 16 + t - 16);
  } else {
    if (t < 24) g_role(p, c, t >> 3, (x - 4) * 8 + (t & 7), t);
    else zc_role(p, c, (x - 4) * 8 + t - 24);
    STAMP(2);
    c.budget = SPIN_CAP;
    conv_role(p, c, (x - 4) * 32 + t, vote_a(wait_verdict(c)) ^ 1);
  }
  STAMP(3);
}

}

extern "C" void kernel_launch(void* const* d_in, const int* in_sizes, int n_in, void* d_out, int out_size, void* d_ws, size_t ws_size,
                              hipStream_t stream) {
  using namespace mk;
  (void)in_sizes; (void)n_in; (void)out_size;
  Params p;
  p.x = (const u16*)d_in[0]; p.mem0 = (const u16*)d_in[1];
  p.Wq = (const u16*)d_in[2]; p.Wk = (const u16*)d_in[3]; p.Wv = (const u16*)d_in[4]; p.Wo = (const u16*)d_in[5];
  p.bq = (const u16*)d_in[7]; p.bk = (const u16*)d_in[8]; p.bv = (const u16*)d_in[9];
  p.shift = (const u16*)d_in[10]; p.ssmk = (const u16*)d_in[11]; p.ssmD = (const u16*)d_in[12];
  p.out = (u16*)d_out;
  p.ctrl = (unsigned char*)d_ws;
  p.data = data_base(d_ws, ws_size);
  (void)hipFuncSetAttribute((const void*)mk_mega, hipFuncAttributeMaxDynamicSharedMemorySize, LDS_BYTES);
  hipLaunchKernelGGL(mk_mega, dim3(256), dim3(256), LDS_BYTES, stream, p);
}
```

```cpp
#include <hip/hip_runtime.h>
#include <hip/hip_bf16.h>

namespace mk {

#define GAS __attribute__((address_space(1)))
#define LAS __attribute__((address_space(3)))
#define DEV __device__ __forceinline__
#define RLX_AGENT __ATOMIC_RELAXED, __HIP_MEMORY_SCOPE_AGENT
typedef unsigned short u16;
typedef unsigned v2u __attribute__((ext_vector_type(2)));
typedef unsigned v4u __attribute__((ext_vector_type(4)));
typedef unsigned v8u __attribute__((ext_vector_type(8)));
typedef unsigned v16u __attribute__((ext_vector_type(16)));
typedef unsigned v32u __attribute__((ext_vector_type(32)));
typedef float f32x2 __attribute__((ext_vector_type(2)));
typedef float f32x4 __attribute__((ext_vector_type(4)));
typedef float f32x16 __attribute__((ext_vector_type(16)));
typedef __bf16 bf16x2 __attribute__((ext_vector_type(2)));
typedef __bf16 bf16x8 __attribute__((ext_vector_type(8)));
typedef __bf16 bf16x16 __attribute__((ext_vector_type(16)));
typedef GAS unsigned gu32;
typedef __amdgpu_buffer_rsrc_t rsrc_t;
typedef LAS unsigned char* lds_t;

#define XCNT(r) ""
#define UCNT ""
#define CL_T
#define XT(a, b) ""
#define LMARK ""
#define GSUM ""
#define ZSUM ""
#define GTRACE
#define MIXED 1
#define NOFLAG 0
#define TOK_KICK 1
#define ZRING 16
#define Z_AT 16
#define Z_GAP 4
#define STR_(x) #x
#define STR(x) STR_(x)
#define GQ_AT 500
#define GK_AT 300
#define PULL_AT 800
#define QK_AT 0
#define SERVER 194
#define SLOW_LEAD 100
#define PULL_DEPTH 1
#define PRIME 2
#define X_AT 400
#define VO_AT 200
#define ZC_AT 0
#define CONV_AUX 2
#define W_AUX 0
#define ZC_PRIME 1
#define CODE_PULL 1
#define PROBES 2
#define Z_WIN 20
#define TOK_WAIT 0
#define Z_AUX 0
#define OUT_AUX 2
#define OUT_AUX2 2
#define URING 8
#define YH_AT 0
#define G_DEEP 1
#define AGAIN 0
#define PRE 0
#define YH 1
#define HELP_O 0
#define HELP_V 1
#define SLOW_V 1
#define K_FIRST 1
#define FLIP 1
#define YH_STEP 300
constexpr int NSEG = 64;
constexpr unsigned SENT = 0xFFFFFFFFu;
constexpr int AUX_SC1 = 16, AUX_NT = 2;
constexpr int T_LAG = 61;
constexpr int G_AHEAD = 4;
constexpr unsigned SPIN_CAP = 1u << 21;
constexpr int LDS_BYTES = 163840;

constexpr int C_ECHO = 0, C_CLAIM = 1024, C_VERDICT = 3072, C_TMO = 3328, C_CONV = 3456, C_VOTE = 3840, C_FLAG = 4096, C_ASK = C_FLAG + 256 * 128;
constexpr int F_Q = 0, F_K = 32, F_V = 64, F_S = 96, F_Z = 128;
constexpr int CTRL_BYTES = C_ASK + 256 * 64;
constexpr int MB = 1 << 20;
constexpr int R_U = 136 * MB;
constexpr int R_MEM = 2 * MB, MEM_SLOT = 32768;
constexpr int R_P = R_MEM + 4 * MEM_SLOT, P_SLOT = 65536;
constexpr int R_PT = 4 * MB, PT_SLOT = 131072;
constexpr int R_QX = 8 * MB, R_KX = 40 * MB, R_Z = 72 * MB, Z_SLOT = 4 * MB, R_KVT = R_KX, R_YT = R_KX + 8 * MB, R_END = (136 + URING) * MB;

constexpr size_t R_DIAG = 0;
#define TOP 1
static inline unsigned char* data_base(void* ws, size_t size) {
  return (unsigned char*)((TOP ? (size_t)ws + size - R_END - R_DIAG : (size_t)ws + CTRL_BYTES + 2 * MB - 1) & ~(size_t)(2 * MB - 1));
}
struct Params {
  const u16 *x, *mem0, *Wq, *Wk, *Wv, *Wo, *bq, *bk, *bv, *shift, *ssmk, *ssmD;
  u16* out;
  unsigned char* ctrl;
  unsigned char* data;
};

DEV rsrc_t mkrs(const void* p, unsigned bytes) { return __builtin_amdgcn_make_buffer_rsrc((void*)p, 0, bytes, 0x00020000); }
DEV v4u ld16s(rsrc_t rs, int v, int s) { return __builtin_amdgcn_raw_buffer_load_b128(rs, v, s, AUX_SC1); }
DEV v2u ld8s(rsrc_t rs, int v, int s) { return __builtin_amdgcn_raw_buffer_load_b64(rs, v, s, AUX_SC1); }
DEV v4u ld16(rsrc_t rs, int v, int s) { return __builtin_amdgcn_raw_buffer_load_b128(rs, v, s, 0); }
DEV v4u ld16z(rsrc_t rs, int v, int s) { return __builtin_amdgcn_raw_buffer_load_b128(rs, v, s, AUX_SC1 | Z_AUX); }
DEV v4u ld16c(rsrc_t rs, int v, int s) { return __builtin_amdgcn_raw_buffer_load_b128(rs, v, s, CONV_AUX); }
template <int A> DEV v4u ld16w(rsrc_t rs, int v, int s) { return __builtin_amdgcn_raw_buffer_load_b128(rs, v, s, A); }
DEV v2u ld8(rsrc_t rs, int v, int s) { return __builtin_amdgcn_raw_buffer_load_b64(rs, v, s, 0); }
DEV void dma16(rsrc_t rs, lds_t l, int v) {
#if __HIP_DEVICE_COMPILE__
  __builtin_amdgcn_raw_ptr_buffer_load_lds(rs, (LAS void*)l, 16, v, 0, 0, 0);
#endif
}
DEV void st16s(rsrc_t rs, int v, int s, v4u d) { __builtin_amdgcn_raw_buffer_store_b128(d, rs, v, s, AUX_SC1); }
DEV void st8s(rsrc_t rs, int v, int s, v2u d) { __builtin_amdgcn_raw_buffer_store_b64(d, rs, v, s, AUX_SC1); }
DEV void st4s(rsrc_t rs, int v, int s, unsigned d) { __builtin_amdgcn_raw_buffer_store_b32(d, rs, v, s, AUX_SC1); }
DEV int amap(int o, int par) { return ((o >> 13) << 14) | (par << 13) | (o & 8191); }
DEV int xblk(int s, int h, int par) { return (s * 32 + h) * 16384 + ((par ^ (s >> 2)) & 1) * 8192; }
DEV unsigned umax(unsigned a, unsigned b) { return a > b ? a : b; }
DEV unsigned top(v4u v) { return umax(umax(v.x, v.y), umax(v.z, v.w)); }
DEV unsigned pk(float a, float b) { bf16x2 v = {(__bf16)a, (__bf16)b}; return __builtin_bit_cast(unsigned, v); }
DEV unsigned pks(float a, float b) { unsigned u = pk(a, b); return u < SENT - 1u ? u : SENT - 1u; }
DEV float blo(unsigned u) { return __builtin_bit_cast(float, u << 16); }
DEV float bhi(unsigned u) { return __builtin_bit_cast(float, u & 0xFFFF0000u); }
DEV float rb(float a) { return blo(pk(a, a)); }
DEV unsigned fu(float f) { return __builtin_bit_cast(unsigned, f); }
DEV float uf(unsigned u) { return __builtin_bit_cast(float, u); }
DEV f32x4 mfma16(v4u a, v4u b, f32x4 c) {
  return __builtin_amdgcn_mfma_f32_16x16x32_bf16(__builtin_bit_cast(bf16x8, a), __builtin_bit_cast(bf16x8, b), c, 0, 0, 0);
}
DEV f32x16 mfma32(v4u a, v4u b, f32x16 c) {
  return __builtin_amdgcn_mfma_f32_32x32x16_bf16(__builtin_bit_cast(bf16x8, a), __builtin_bit_cast(bf16x8, b), c, 0, 0, 0);
}
DEV f32x4 smf(v4u a, v8u b, f32x4 c, int idx) {
  return __builtin_amdgcn_smfmac_f32_16x16x64_bf16(__builtin_bit_cast(bf16x8, a), __builtin_bit_cast(bf16x16, b), c, idx, 0, 0);
}
DEV v8u cat(v4u a, v4u b) { return __builtin_shufflevector(a, b, 0, 1, 2, 3, 4, 5, 6, 7); }
DEV v4u lo4(v8u a) { return __builtin_shufflevector(a, a, 0, 1, 2, 3); }
DEV v4u hi4(v8u a) { return __builtin_shufflevector(a, a, 4, 5, 6, 7); }
DEV v4u lds16(lds_t L, int o) { return *(LAS v4u*)(L + o); }
DEV void sts16(lds_t L, int o, v4u v) { *(LAS v4u*)(L + o) = v; }
DEV void sts8(lds_t L, int o, v2u v) { *(LAS v2u*)(L + o) = v; }
#define SCHED() __builtin_amdgcn_sched_barrier(0)
#define PIN_A(x) asm volatile("" : "+a"(x))
#define PIN_V(x) asm volatile("" : "+v"(x))
DEV int opq(int v) { asm volatile("" : "+v"(v)); return v; }
DEV int fresh_lane() {
  int l; asm volatile("v_mbcnt_lo_u32_b32 %0, -1, 0\n\tv_mbcnt_hi_u32_b32 %0, -1, %0" : "=v"(l)); return l;
}
DEV f32x4 fresh_zero() {
  f32x2 a, b;
  asm volatile("v_mov_b64 %0, 0" : "=v"(a));
  asm volatile("v_mov_b64 %0, 0" : "=v"(b));
  return __builtin_shufflevector(a, b, 0, 1, 2, 3);
}
DEV unsigned fresh_sent() { unsigned z; asm volatile("v_mov_b32 %0, -1" : "=v"(z)); return z; }
#define SMF "v_smfmac_f32_16x16x64_bf16 "
#define MF16 "v_mfma_f32_16x16x32_bf16 "
#define SMF1(cls, d, a, b) asm volatile(SMF "%0, %1, %2, %3" : "+v"(d) : "v"(a), cls(b), "v"(idx))
#define SMF1F(cls, lo, hi, d, a, b) asm volatile(SMF "v[" #lo ":" #hi "], %1, %2, %3" : "+{v[" #lo ":" #hi "]}"(d) : "v"(a), cls(b), "v"(idx))
#define AT_HOME(M, j) do { const int j_ = (j); if (j_ == 0) M("{v[32:39]}"); if (j_ == 1) M("{v[40:47]}"); if (j_ == 2) M("{v[48:55]}"); if (j_ == 3) M("{v[56:63]}"); if (j_ == 4) M("{v[64:71]}"); if (j_ == 5) M("{v[72:79]}"); if (j_ == 6) M("{v[80:87]}"); if (j_ == 7) M("{v[88:95]}"); if (j_ == 8) M("{v[96:103]}"); if (j_ == 9) M("{v[104:111]}"); if (j_ == 10) M("{v[112:119]}"); if (j_ == 11) M("{v[120:127]}"); if (j_ == 12) M("{v[128:135]}"); if (j_ == 13) M("{v[136:143]}"); if (j_ == 14) M("{v[144:151]}"); if (j_ == 15) M("{v[152:159]}"); } while (0)
#define MF1(d, a, b) asm volatile(MF16 "%0, %1, %2, %0" : "+v"(d) : "v"(a), "v"(b))
#define MF1Z(d, a, b) asm volatile(MF16 "%0, %1, %2, 0" : "=&v"(d) : "v"(a), "v"(b))
#define LGKM(n) asm volatile("s_waitcnt lgkmcnt(" #n ")" ::: "memory")
#define RAWBAR() do { __builtin_amdgcn_s_barrier(); asm volatile("" ::: "memory"); } while (0)
DEV void settle3(f32x4& s0, f32x4& s1, f32x4& s2) { asm volatile("s_nop 7" : "+v"(s0), "+v"(s1), "+v"(s2)); }
DEV void settle4(f32x4 (&d)[4]) { asm volatile("s_nop 7" : "+v"(d[0]), "+v"(d[1]), "+v"(d[2]), "+v"(d[3])); }
#define BAR() do { asm volatile("s_waitcnt lgkmcnt(0)" ::: "memory"); __builtin_amdgcn_s_barrier(); asm volatile("" ::: "memory"); } while (0)
#define VMWAIT(n) asm volatile("s_waitcnt vmcnt(" #n ")" ::: "memory")
DEV float add1(float a, float b) {
  float o; asm("v_add_f32 %0, %1, %2" : "=v"(o) : "v"(a), "v"(b)); return o;
}
DEV float xmax(float v) {
  auto r = __builtin_amdgcn_permlane32_swap(fu(v), fu(v), false, false); v = fmaxf(uf(r[0]), uf(r[1]));
  auto q = __builtin_amdgcn_permlane16_swap(fu(v), fu(v), false, false); return fmaxf(uf(q[0]), uf(q[1]));
}
DEV float xsum(float v) {
  auto r = __builtin_amdgcn_permlane32_swap(fu(v), fu(v), false, false); v = uf(r[0]) + uf(r[1]);
  auto q = __builtin_amdgcn_permlane16_swap(fu(v), fu(v), false, false); return uf(q[0]) + uf(q[1]);
}

DEV float ror8(float v) { return uf(__builtin_amdgcn_update_dpp(0u, fu(v), 0x128, 0xf, 0xf, true)); }
DEV float fmax2(float a, float b) { return __builtin_amdgcn_fmed3f(a, b, __builtin_inff()); }
DEV float fmax3(float a, float b, float c) { float r; asm("v_max3_f32 %0, %1, %2, %3" : "=v"(r) : "v"(a), "v"(b), "v"(c)); return r; }
DEV bool allok(unsigned t) { return __all(t != SENT); }

template <int N> struct IC { static constexpr int value = N; };
template <int N, class F> DEV void sfor(F&& f) { if constexpr (N > 0) { sfor<N - 1>(f); f(IC<N - 1>{}); } }
struct Ctx {
  gu32* ctrl;
  rsrc_t rs;
  lds_t L;
  int w, lane;
  int b, slot, att;
  unsigned ask;
  unsigned t0;
  unsigned budget;
};
DEV bool spend(Ctx& c) {
  const unsigned old = c.budget;
  asm volatile("s_cmp_lg_u32 %0, 0\n\ts_subb_u32 %0, %0, 0" : "+s"(c.budget) :: "scc");
  return old == 0u;
}
DEV unsigned wait_flag(Ctx& c, int flag, unsigned need) {
  gu32* f = c.ctrl + (C_FLAG + flag * 128) / 4;
  unsigned v;
#pragma unroll 1
  for (;;) {
    v = __builtin_amdgcn_readfirstlane(__hip_atomic_load(f, RLX_AGENT));
    if (v >= need || spend(c)) break;
    __builtin_amdgcn_s_sleep(2);
  }
  __builtin_amdgcn_fence(__ATOMIC_ACQUIRE, "wavefront");
  return v;
}
DEV void post_flag(Ctx& c, int flag, unsigned v, int off = 0) {
  st4s(mkrs((const void*)c.ctrl, CTRL_BYTES), (c.w | off | fresh_lane()) ? 1 << 30 : 0, C_FLAG + flag * 128, v);
}
constexpr unsigned CL_A = 1u << 16, CL_VOTED = 1u << 24, CL_FILLED = 1u << 28;
DEV unsigned claim_look(Ctx& c) { return __hip_atomic_load(c.ctrl + C_ECHO / 4 + c.b, RLX_AGENT); }
DEV void ask(Ctx& c, unsigned first) {
  c.ask = __builtin_amdgcn_readfirstlane(first) + 1u;
  st4s(mkrs((const void*)c.ctrl, CTRL_BYTES), (c.w | c.att | fresh_lane()) ? 1 << 30 : 0, C_ASK + c.b * 64, c.ask);
}
DEV void claim(Ctx& c, unsigned votes) {
  if (c.w != 0) return;
  if (c.att == 0) {
#pragma unroll 1
    while (__builtin_amdgcn_readfirstlane(__hip_atomic_load(c.ctrl + C_ECHO / 4 + c.b, RLX_AGENT)) != c.ask) {
      if (spend(c)) break;
      __builtin_amdgcn_s_sleep(2);
    }
  }
  if (fresh_lane() == 0) __hip_atomic_fetch_add(c.ctrl + (C_CLAIM + c.att * 1024) / 4 + c.slot, 1u + votes, RLX_AGENT);
}
DEV bool claim_try(Ctx& c, unsigned look) {
  const bool ok = c.att || __builtin_amdgcn_readfirstlane(look) == c.ask;
  if (ok && c.w == 0 && fresh_lane() == 0) __hip_atomic_fetch_add(c.ctrl + (C_CLAIM + c.att * 1024) / 4 + c.slot, 1u, RLX_AGENT);
  return ok;
}
DEV unsigned wait_verdict(Ctx& c) {
  unsigned v;
#pragma unroll 1
  for (;;) {
    v = __builtin_amdgcn_readfirstlane(__hip_atomic_load(c.ctrl + (C_VERDICT + c.att * 128) / 4, RLX_AGENT));
    if ((v & 1u) || spend(c)) break;
    __builtin_amdgcn_s_sleep(2);
  }
  __builtin_amdgcn_fence(__ATOMIC_ACQUIRE, "wavefront");
  if ((v & 3u) == 3u && c.att == 0) {
    asm volatile("s_waitcnt vmcnt(0) lgkmcnt(0)\n\ts_barrier\n\ts_mov_b64 s[0:1], %0\n\ts_mov_b32 s2, %1\n\tv_mov_b32 v0, %2\n\t"
                 "s_getpc_b64 s[4:5]\n1:\n\ts_sub_u32 s4, s4, 1b-mk_mega\n\ts_subb_u32 s5, s5, 0\n\ts_setpc_b64 s[4:5]"
                 :: "s"(__builtin_amdgcn_kernarg_segment_ptr()), "s"(c.b + 256), "v"(c.w * 64 + fresh_lane()) : "s0", "s1", "s2", "s4", "s5", "v0", "scc", "memory");
    __builtin_unreachable();
  }
  return v;
}
DEV unsigned wait_vote(Ctx& c) {
  unsigned v;
#pragma unroll 1
  for (;;) {
    v = __builtin_amdgcn_readfirstlane(__hip_atomic_load(c.ctrl + C_VOTE / 4, RLX_AGENT));
    if ((v & 1u) || spend(c)) break;
    __builtin_amdgcn_s_sleep(2);
  }
  return v;
}
DEV void serve(Ctx& c) {
  c.budget = SPIN_CAP;
  const int lane = fresh_lane();
  const rsrc_t rc = mkrs((const void*)c.ctrl, CTRL_BYTES);
  if (c.att == 0) {
#pragma unroll
    for (int i = C_CLAIM / 1024; i < C_ASK / 1024; ++i) st16s(rc, lane * 16, i * 1024, v4u{0, 0, 0, 0});
    VMWAIT(0);
  }
  unsigned vote = c.att ? wait_vote(c) : 0u;
  bool echo = c.att == 0, first = true;
  v4u last = {0u, 0u, 0u, 0u};
#pragma unroll 1
  for (;;) {
    asm volatile("" ::: "memory");
    const v4u cl = ld16s(rc, lane * 16, C_CLAIM + c.att * 1024);
    if (echo) {
      v4u e;
#pragma unroll
      for (int i = 0; i < 4; ++i) e[i] = __builtin_amdgcn_raw_buffer_load_b32(rc, (lane * 4 + i) * 64, C_ASK, AUX_SC1);
      if (first || __any(e[0] != last[0] || e[1] != last[1] || e[2] != last[2] || e[3] != last[3])) st16s(rc, lane * 16, C_ECHO, e);
      last = e; first = false;
    }
    unsigned n = 0, one = 1;
    int va = 0, nv = 0, nf = 0;
#pragma unroll
    for (int i = 0; i < 4; ++i) {
      n += cl[i] & 0xFFFFu; one &= (cl[i] & 0xFFFFu) == 1u;
      va += __popcll(__ballot(cl[i] & CL_A)); nv += __popcll(__ballot(cl[i] & CL_VOTED)); nf += __popcll(__ballot(cl[i] & CL_FILLED));
    }
#pragma unroll
    for (int o = 1; o < 64; o <<= 1) n += (unsigned)__shfl_xor((int)n, o);
    const bool all = __builtin_amdgcn_readfirstlane(n) >= 256u, good = __all(one);
    echo = echo && !all;
    if (!vote && (all || max(va, nv - va) > 20)) {
      vote = 2 * va > nv ? 5u : 9u;
      if (lane == 0) __hip_atomic_store(c.ctrl + C_VOTE / 4, vote, RLX_AGENT);
    }
    if (all && (!good || nf >= 64)) {
      if (lane == 0) __hip_atomic_store(c.ctrl + (C_VERDICT + c.att * 128) / 4, vote | (good ? 0u : 2u), RLX_AGENT);
      break;
    }
    if (spend(c)) break;
  }
}
DEV int vote_a(unsigned v) { return (int)((v >> 2) & 1u); }
DEV int vote_b(unsigned v) { return (int)((v >> 3) & 1u); }

#define STAMP(slot) do {} while (0)
#define TICK_DECL
#define LOG(i, s, v) do {} while (0)
#define LOGS(i, s) do {} while (0)
#define LITE(s) do {} while (0)
#define TICK(i) do {} while (0)
#define TICK_SUM(n) do {} while (0)
#define TICK_LDS(b) do {} while (0)
#define TICK_DUMP(b) do {} while (0)
#define TICK_OUT() do {} while (0)

DEV constexpr int krow(int rho8, int i) { return 32 * (rho8 >> 2) + 8 * (rho8 & 1) + 2 * ((rho8 >> 1) & 1) + i; }
DEV int krow_lane(int g) { return 16 * (g >> 1) + 4 * (g & 1); }
DEV unsigned prm(unsigned hi, unsigned lo, int odd) { return __builtin_amdgcn_perm(hi, lo, odd ? 0x07060302u : 0x05040100u); }

#define AT_ACC(M, n) do { const int n_ = (n); if (n_ == 0) M("{a[0:7]}"); if (n_ == 1) M("{a[8:15]}"); if (n_ == 2) M("{a[16:23]}"); if (n_ == 3) M("{a[24:31]}"); if (n_ == 4) M("{a[32:39]}"); if (n_ == 5) M("{a[40:47]}"); if (n_ == 6) M("{a[48:55]}"); if (n_ == 7) M("{a[56:63]}"); if (n_ == 8) M("{a[64:71]}"); if (n_ == 9) M("{a[72:79]}"); if (n_ == 10) M("{a[80:87]}"); if (n_ == 11) M("{a[88:95]}"); if (n_ == 12) M("{a[96:103]}"); if (n_ == 13) M("{a[104:111]}"); if (n_ == 14) M("{a[112:119]}"); if (n_ == 15) M("{a[120:127]}"); if (n_ == 16) M("{a[128:135]}"); if (n_ == 17) M("{a[136:143]}"); if (n_ == 18) M("{a[144:151]}"); if (n_ == 19) M("{a[152:159]}"); if (n_ == 20) M("{a[160:167]}"); if (n_ == 21) M("{a[168:175]}"); if (n_ == 22) M("{a[176:183]}"); if (n_ == 23) M("{a[184:191]}"); if (n_ == 24) M("{a[192:199]}"); if (n_ == 25) M("{a[200:207]}"); if (n_ == 26) M("{a[208:215]}"); if (n_ == 27) M("{a[216:223]}"); if (n_ == 28) M("{a[224:231]}"); if (n_ == 29) M("{a[232:239]}"); if (n_ == 30) M("{a[240:247]}"); if (n_ == 31) M("{a[248:255]}"); } while (0)
#define PIN_ACC0(x) asm volatile("" :: "{a[0:7]}"(x[0]), "{a[8:15]}"(x[1]), "{a[16:23]}"(x[2]), "{a[24:31]}"(x[3]), "{a[32:39]}"(x[4]), "{a[40:47]}"(x[5]), "{a[48:55]}"(x[6]), "{a[56:63]}"(x[7]), "{a[64:71]}"(x[8]), "{a[72:79]}"(x[9]), "{a[80:87]}"(x[10]), "{a[88:95]}"(x[11]), "{a[96:103]}"(x[12]), "{a[104:111]}"(x[13]), "{a[112:119]}"(x[14]), "{a[120:127]}"(x[15]))
#define PIN_ACC1(x) asm volatile("" :: "{a[128:135]}"(x[16]), "{a[136:143]}"(x[17]), "{a[144:151]}"(x[18]), "{a[152:159]}"(x[19]), "{a[160:167]}"(x[20]), "{a[168:175]}"(x[21]), "{a[176:183]}"(x[22]), "{a[184:191]}"(x[23]), "{a[192:199]}"(x[24]), "{a[200:207]}"(x[25]), "{a[208:215]}"(x[26]), "{a[216:223]}"(x[27]), "{a[224:231]}"(x[28]), "{a[232:239]}"(x[29]), "{a[240:247]}"(x[30]), "{a[248:255]}"(x[31]))
#define PIN_HOME(x) asm volatile("" :: "{v[32:39]}"(x[0]), "{v[40:47]}"(x[1]), "{v[48:55]}"(x[2]), "{v[56:63]}"(x[3]), "{v[64:71]}"(x[4]), "{v[72:79]}"(x[5]), "{v[80:87]}"(x[6]), "{v[88:95]}"(x[7]), "{v[96:103]}"(x[8]), "{v[104:111]}"(x[9]), "{v[112:119]}"(x[10]), "{v[120:127]}"(x[11]), "{v[128:135]}"(x[12]), "{v[136:143]}"(x[13]), "{v[144:151]}"(x[14]), "{v[152:159]}"(x[15]))
#define CL_32_100 "v32", "v33", "v34", "v35", "v36", "v37", "v38", "v39", "v40", "v41", "v42", "v43", "v44", "v45", "v46", "v47", "v48", "v49", "v50", "v51", "v52", "v53", "v54", "v55", "v56", "v57", "v58", "v59", "v60", "v61", "v62", "v63", "v64", "v65", "v66", "v67", "v68", "v69", "v70", "v71", "v72", "v73", "v74", "v75", "v76", "v77", "v78", "v79", "v80", "v81", "v82", "v83", "v84", "v85", "v86", "v87", "v88", "v89", "v90", "v91", "v92", "v93", "v94", "v95", "v96", "v97", "v98", "v99", "v100", "v101", "v102", "v103", "v104", "v105", "v106", "v107", "v108", "v109", "v110", "v111", "v112", "v113", "v114", "v115", "v116", "v117", "v118", "v119", "v120", "v121", "v122", "v123", "v124", "v125", "v126", "v127", "v128", "v129", "v130", "v131"
#define ACC_IN(x) "{a[0:31]}"(x[0]), "{a[32:63]}"(x[1]), "{a[64:95]}"(x[2]), "{a[96:127]}"(x[3]), "{a[128:159]}"(x[4]), "{a[160:191]}"(x[5]), "{a[192:223]}"(x[6]), "{a[224:255]}"(x[7])
#define CL_16_32 "v16", "v17", "v18", "v19", "v20", "v21", "v22", "v23", "v24", "v25", "v26", "v27", "v28", "v29", "v30", "v31"
#define CL_120_128 "v120", "v121", "v122", "v123", "v124", "v125", "v126", "v127", "v128", "v129", "v130", "v131", "v132", "v133", "v134", "v135", "v136", "v137", "v138", "v139"
#define CL_160_200 "v160", "v161", "v162", "v163", "v164", "v165", "v166", "v167", "v168", "v169", "v170", "v171", "v172", "v173", "v174", "v175", "v176", "v177", "v178", "v179", "v180", "v181", "v182", "v183", "v184", "v185", "v186", "v187", "v188", "v189", "v190", "v191", "v192", "v193", "v194", "v195", "v196", "v197", "v198", "v199"
#define CL_176_200 "v176", "v177", "v178", "v179", "v180", "v181", "v182", "v183", "v184", "v185", "v186", "v187", "v188", "v189", "v190", "v191", "v192", "v193", "v194", "v195", "v196", "v197", "v198", "v199"
#define CL_200_216 "v200", "v201", "v202", "v203", "v204", "v205", "v206", "v207", "v208", "v209", "v210", "v211", "v212", "v213", "v214", "v215"
#define CL_216_256 "v216", "v217", "v218", "v219", "v220", "v221", "v222", "v223", "v224", "v225", "v226", "v227", "v228", "v229", "v230", "v231", "v232", "v233", "v234", "v235", "v236", "v237", "v238", "v239", "v240", "v241", "v242", "v243", "v244", "v245", "v246", "v247", "v248", "v249", "v250", "v251", "v252", "v253", "v254", "v255"
#define CL_S "memory", "vcc", "scc", "s60", "s61", "s62", "s63", "s64", "s65", "s66", "s67", "s68" CL_T
#define TIMED_WAIT \
    "s_getpc_b64 s[62:63]\n\t" "1:\n\t" "s_memtime s[64:65]\n\t" "s_waitcnt lgkmcnt(0)\n\t" "s_sub_u32 s61, %0, s64\n\t" "s_cmp_gt_i32 s61, 124\n\t" "s_cbranch_scc1 1b\n\t" \
    "s_max_i32 s61, s61, 0\n\t" "s_and_b32 s61, s61, 124\n\t" "s_sub_u32 s61, 2f-1b, s61\n\t" "s_add_u32 s62, s62, s61\n\t" "s_addc_u32 s63, s63, 0\n\t" \
    "s_setpc_b64 s[62:63]\n\t" ".rept 31\n\t" "s_nop 0\n\t" ".endr\n\t" "2:\n\t"
#define QK_STEP0 \
    ".p2align 6\n\t" "v_xor_b32 v216, 0x400, %[mo]\n\t" "s_xor_b32 s60, %[sb], 0x800\n\t" "s_mov_b32 s66, 0x3e0293ee\n\t" "s_mov_b32 s67, 0x3e0293ee\n\t" XT(80, 81) \
    "s_add_u32 s61, %[kb], 0x1000\n\t" "buffer_load_dwordx4 v[16:19], %[kx], %[rs], %[kb] offen offset:0 sc1\n\t" \
    "buffer_load_dwordx4 v[20:23], %[kx], %[rs], %[kb] offen offset:2048 sc1\n\t" "buffer_load_dwordx4 v[24:27], %[kx], %[rs], s61 offen offset:0 sc1\n\t" \
    "buffer_load_dwordx4 v[28:31], %[kx], %[rs], s61 offen offset:2048 sc1\n\t" "buffer_load_dwordx4 v[200:203], %[kx], %[rs], %[kb] offen offset:256 sc1\n\t" \
    "buffer_load_dwordx4 v[204:207], %[kx], %[rs], %[kb] offen offset:2304 sc1\n\t" "buffer_load_dwordx4 v[208:211], %[kx], %[rs], s61 offen offset:256 sc1\n\t" \
    "buffer_load_dwordx4 v[212:215], %[kx], %[rs], s61 offen offset:2304 sc1\n\t" "s_barrier\n\t" "s_waitcnt vmcnt(7)\n\t" "ds_write_b128 %[kx], v[16:19] offset:0\n\t" \
    "s_waitcnt vmcnt(6)\n\t" "ds_write_b128 %[kx], v[20:23] offset:2048\n\t" "s_waitcnt vmcnt(5)\n\t" "ds_write_b128 %[kx], v[24:27] offset:4096\n\t" "s_waitcnt vmcnt(4)\n\t" \
    "ds_write_b128 %[kx], v[28:31] offset:6144\n\t" "s_waitcnt vmcnt(3)\n\t" "ds_write_b128 %[kx], v[200:203] offset:256\n\t" "s_waitcnt vmcnt(2)\n\t" \
    "ds_write_b128 %[kx], v[204:207] offset:2304\n\t" "s_waitcnt vmcnt(1)\n\t" "ds_write_b128 %[kx], v[208:211] offset:4352\n\t" "s_waitcnt vmcnt(0)\n\t" \
    "ds_write_b128 %[kx], v[212:215] offset:6400\n\t" "s_waitcnt lgkmcnt(0)\n\t" "v_mov_b64 v[208:209], 0\n\t" "v_mov_b64 v[210:211], 0\n\t" "v_mov_b64 v[212:213], 0\n\t" \
    "v_mov_b64 v[214:215], 0\n\t" XT(98, 99) "s_barrier\n\t" XT(82, 83) "buffer_load_dwordx4 v[240:243], %[mo], %[rs], %[sb] offen sc1\n\t" "v_mov_b64 v[224:225], 0\n\t" \
    "v_mov_b64 v[226:227], 0\n\t" "v_mov_b64 v[228:229], 0\n\t" "v_mov_b64 v[230:231], 0\n\t" "v_mov_b64 v[232:233], 0\n\t" "v_mov_b64 v[234:235], 0\n\t" \
    "v_mov_b64 v[236:237], 0\n\t" "v_mov_b64 v[238:239], 0\n\t" "buffer_load_dwordx4 v[244:247], v216, %[rs], %[sb] offen sc1\n\t" \
    "ds_read_b128 v[160:163], %[l0] offset:0\n\t" "ds_read_b128 v[164:167], %[l0] offset:1024\n\t" "ds_read_b128 v[168:171], %[l0] offset:2048\n\t" \
    "ds_read_b128 v[172:175], %[l0] offset:3072\n\t" "s_nop 3\n\t" "buffer_load_dwordx4 v[248:251], %[mo], %[rs], s60 offen sc1\n\t" \
    "ds_read_b128 v[176:179], %[l0] offset:4096\n\t" "ds_read_b128 v[180:183], %[l0] offset:5120\n\t" "ds_read_b128 v[184:187], %[l0] offset:6144\n\t" \
    "ds_read_b128 v[188:191], %[l0] offset:7168\n\t" "s_nop 3\n\t" "buffer_load_dwordx4 v[252:255], v216, %[rs], s60 offen sc1\n\t" "v_mov_b32 v219, 160512\n\t" \
    "v_mov_b64 v[220:221], -1\n\t" "v_mov_b64 v[222:223], -1\n\t" "v_mov_b32 v217, 0\n\t" "buffer_store_dwordx2 v[220:221], %[px], %[rs], %[pr] offen sc1\n\t" \
    "buffer_store_dword v220, %[pm], %[rs], %[pr] offen sc1\n\t" "global_load_dword v217, v217, %[fq] sc1\n\t" "10:\n\t" "s_waitcnt vmcnt(6)\n\t" XT(84, 85) \
    "v_smfmac_f32_16x16x64_bf16 v[224:227], v[240:243], a[0:7], %[ix]\n\t" "ds_write_b128 %[l1], v[240:243] offset:0\n\t" \
    "v_smfmac_f32_16x16x64_bf16 v[228:231], v[240:243], a[8:15], %[ix]\n\t" "v_smfmac_f32_16x16x64_bf16 v[232:235], v[240:243], v[32:39], %[ix]\n\t" \
    "s_waitcnt lgkmcnt(7)\n\t" "v_smfmac_f32_16x16x64_bf16 v[236:239], v[240:243], v[160:167], %[ix]\n\t" "ds_read_b128 v[160:163], %[l0] offset:8192\n\t" \
    "ds_read_b128 v[164:167], %[l0] offset:9216\n\t" "s_waitcnt vmcnt(5)\n\t" "v_smfmac_f32_16x16x64_bf16 v[224:227], v[244:247], a[16:23], %[ix]\n\t" \
    "ds_write_b128 %[l1], v[244:247] offset:1024\n\t" "v_smfmac_f32_16x16x64_bf16 v[228:231], v[244:247], a[24:31], %[ix]\n\t" \
    "v_smfmac_f32_16x16x64_bf16 v[232:235], v[244:247], v[40:47], %[ix]\n\t" "s_waitcnt lgkmcnt(8)\n\t" \
    "v_smfmac_f32_16x16x64_bf16 v[236:239], v[244:247], v[168:175], %[ix]\n\t" "ds_read_b128 v[168:171], %[l0] offset:10240\n\t" \
    "ds_read_b128 v[172:175], %[l0] offset:11264\n\t" "s_waitcnt vmcnt(4)\n\t" "v_smfmac_f32_16x16x64_bf16 v[224:227], v[248:251], a[32:39], %[ix]\n\t" \
    "ds_write_b128 %[l1], v[248:251] offset:2048\n\t" "v_smfmac_f32_16x16x64_bf16 v[228:231], v[248:251], a[40:47], %[ix]\n\t" "s_waitcnt lgkmcnt(3)\n\t" "s_barrier\n\t" \
    XT(86, 87) "v_smfmac_f32_16x16x64_bf16 v[232:235], v[248:251], v[48:55], %[ix]\n\t" "ds_read_b128 v[192:195], %[l1] offset:4096\n\t" \
    "ds_read_b128 v[196:199], %[l1] offset:8192\n\t" "v_smfmac_f32_16x16x64_bf16 v[236:239], v[248:251], v[176:183], %[ix]\n\t" \
    "ds_read_b128 v[176:179], %[l0] offset:12288\n\t" "ds_read_b128 v[180:183], %[l0] offset:13312\n\t" "s_waitcnt vmcnt(3)\n\t" \
    "v_smfmac_f32_16x16x64_bf16 v[224:227], v[252:255], a[48:55], %[ix]\n\t" "ds_write_b128 %[l1], v[252:255] offset:3072\n\t" \
    "v_smfmac_f32_16x16x64_bf16 v[228:231], v[252:255], a[56:63], %[ix]\n\t" "ds_read_b128 v[240:243], %[l1] offset:12288\n\t" \
    "ds_read_b128 v[244:247], %[l1] offset:5120\n\t" "v_smfmac_f32_16x16x64_bf16 v[232:235], v[252:255], v[56:63], %[ix]\n\t" "s_nop 1\n\t" \
    "v_smfmac_f32_16x16x64_bf16 v[236:239], v[252:255], v[184:191], %[ix]\n\t" "ds_read_b128 v[184:187], %[l0] offset:14336\n\t" "v_cmp_u_f32 vcc, v224, v225\n\t" \
    "v_cmp_u_f32 s[62:63], v226, v227\n\t" "s_waitcnt lgkmcnt(7)\n\t" "v_smfmac_f32_16x16x64_bf16 v[224:227], v[192:195], a[64:71], %[ix]\n\t" \
    "ds_read_b128 v[188:191], %[l0] offset:15360\n\t" "v_smfmac_f32_16x16x64_bf16 v[228:231], v[192:195], a[72:79], %[ix]\n\t" "s_or_b64 vcc, vcc, s[62:63]\n\t" \
    "s_cbranch_scc1 11f\n\t" "12:\n\t" "v_smfmac_f32_16x16x64_bf16 v[232:235], v[192:195], v[64:71], %[ix]\n\t" "s_waitcnt lgkmcnt(4)\n\t" "s_barrier\n\t" XT(88, 89) \
    "ds_read_b32 v218, v219\n\t" "v_smfmac_f32_16x16x64_bf16 v[236:239], v[192:195], v[160:167], %[ix]\n\t" "ds_read_b128 v[160:163], %[l0] offset:16384\n\t" \
    "ds_read_b128 v[164:167], %[l0] offset:17408\n\t" "v_smfmac_f32_16x16x64_bf16 v[224:227], v[196:199], a[80:87], %[ix]\n\t" \
    "ds_read_b128 v[248:251], %[l1] offset:9216\n\t" "ds_read_b128 v[252:255], %[l1] offset:13312\n\t" \
    "v_smfmac_f32_16x16x64_bf16 v[228:231], v[196:199], a[88:95], %[ix]\n\t" "ds_read_b128 v[192:195], %[l1] offset:6144\n\t" \
    "v_smfmac_f32_16x16x64_bf16 v[232:235], v[196:199], v[72:79], %[ix]\n\t" "v_smfmac_f32_16x16x64_bf16 v[236:239], v[196:199], v[168:175], %[ix]\n\t" \
    "s_waitcnt lgkmcnt(5)\n\t" "v_cmp_eq_u32 vcc, %[rt], v218\n\t" "v_smfmac_f32_16x16x64_bf16 v[224:227], v[240:243], a[96:103], %[ix]\n\t" "s_cbranch_vccnz 13f\n\t" \
    "ds_read_b128 v[168:171], %[l0] offset:18432\n\t" "v_smfmac_f32_16x16x64_bf16 v[228:231], v[240:243], a[104:111], %[ix]\n\t" \
    "ds_read_b128 v[172:175], %[l0] offset:19456\n\t" "ds_read_b128 v[196:199], %[l1] offset:10240\n\t" \
    "v_smfmac_f32_16x16x64_bf16 v[232:235], v[240:243], v[80:87], %[ix]\n\t" "v_smfmac_f32_16x16x64_bf16 v[236:239], v[240:243], v[176:183], %[ix]\n\t" \
    "ds_read_b128 v[176:179], %[l0] offset:20480\n\t" "ds_read_b128 v[180:183], %[l0] offset:21504\n\t" \
    "v_smfmac_f32_16x16x64_bf16 v[224:227], v[244:247], a[112:119], %[ix]\n\t" "ds_read_b128 v[240:243], %[l1] offset:14336\n\t" \
    "v_smfmac_f32_16x16x64_bf16 v[228:231], v[244:247], a[120:127], %[ix]\n\t" "buffer_load_dword %[pd], %[pa], %[rs], %[kn] offen sc1\n\t" \
    "v_smfmac_f32_16x16x64_bf16 v[232:235], v[244:247], v[88:95], %[ix]\n\t" "v_smfmac_f32_16x16x64_bf16 v[236:239], v[244:247], v[184:191], %[ix]\n\t" \
    "ds_read_b128 v[184:187], %[l0] offset:22528\n\t" "ds_read_b128 v[188:191], %[l0] offset:23552\n\t" "s_waitcnt lgkmcnt(10)\n\t" \
    "v_smfmac_f32_16x16x64_bf16 v[224:227], v[248:251], a[128:135], %[ix]\n\t" "ds_read_b128 v[244:247], %[l1] offset:7168\n\t" \
    "v_smfmac_f32_16x16x64_bf16 v[228:231], v[248:251], a[136:143], %[ix]\n\t" "v_smfmac_f32_16x16x64_bf16 v[232:235], v[248:251], v[96:103], %[ix]\n\t" \
    "v_smfmac_f32_16x16x64_bf16 v[236:239], v[248:251], v[160:167], %[ix]\n\t" "ds_read_b128 v[160:163], %[l0] offset:24576\n\t" \
    "ds_read_b128 v[164:167], %[l0] offset:25600\n\t" "s_waitcnt lgkmcnt(12)\n\t" "v_smfmac_f32_16x16x64_bf16 v[224:227], v[252:255], a[144:151], %[ix]\n\t" \
    "ds_read_b128 v[248:251], %[l1] offset:11264\n\t" "v_smfmac_f32_16x16x64_bf16 v[228:231], v[252:255], a[152:159], %[ix]\n\t" \
    "v_smfmac_f32_16x16x64_bf16 v[232:235], v[252:255], v[104:111], %[ix]\n\t" "s_waitcnt lgkmcnt(10)\n\t" \
    "v_smfmac_f32_16x16x64_bf16 v[236:239], v[252:255], v[168:175], %[ix]\n\t" "ds_read_b128 v[168:171], %[l0] offset:26624\n\t" \
    "ds_read_b128 v[172:175], %[l0] offset:27648\n\t" "v_smfmac_f32_16x16x64_bf16 v[224:227], v[192:195], a[160:167], %[ix]\n\t" \
    "ds_read_b128 v[252:255], %[l1] offset:15360\n\t" "v_smfmac_f32_16x16x64_bf16 v[228:231], v[192:195], a[168:175], %[ix]\n\t" "s_waitcnt lgkmcnt(12)\n\t" \
    "v_smfmac_f32_16x16x64_bf16 v[224:227], v[196:199], a[176:183], %[ix]\n\t" "v_smfmac_f32_16x16x64_bf16 v[228:231], v[196:199], a[184:191], %[ix]\n\t" \
    "s_waitcnt lgkmcnt(9)\n\t" "v_smfmac_f32_16x16x64_bf16 v[224:227], v[240:243], a[192:199], %[ix]\n\t" \
    "v_smfmac_f32_16x16x64_bf16 v[228:231], v[240:243], a[200:207], %[ix]\n\t" "s_waitcnt vmcnt(1)\n\t" "v_readfirstlane_b32 s62, v217\n\t" "s_waitcnt lgkmcnt(6)\n\t" \
    "v_smfmac_f32_16x16x64_bf16 v[224:227], v[244:247], a[208:215], %[ix]\n\t" "v_smfmac_f32_16x16x64_bf16 v[228:231], v[244:247], a[216:223], %[ix]\n\t" \
    "s_max_u32 %[sq], %[sq], s62\n\t" "s_waitcnt lgkmcnt(3)\n\t" "v_smfmac_f32_16x16x64_bf16 v[224:227], v[248:251], a[224:231], %[ix]\n\t" \
    "v_smfmac_f32_16x16x64_bf16 v[228:231], v[248:251], a[232:239], %[ix]\n\t" "s_waitcnt lgkmcnt(0)\n\t" \
    "v_smfmac_f32_16x16x64_bf16 v[224:227], v[252:255], a[240:247], %[ix]\n\t" "s_nop 1\n\t" "v_smfmac_f32_16x16x64_bf16 v[228:231], v[252:255], a[248:255], %[ix]\n\t" \
    XT(90, 91) "s_nop 1\n\t" "v_smfmac_f32_16x16x64_bf16 v[232:235], v[192:195], v[112:119], %[ix]\n\t" "s_nop 1\n\t" \
    "v_smfmac_f32_16x16x64_bf16 v[236:239], v[192:195], v[176:183], %[ix]\n\t" "ds_read_b128 v[176:179], %[l0] offset:28672\n\t" "v_add_f32 v224, v224, v225\n\t" \
    "v_add_f32 v226, v226, v227\n\t" "v_smfmac_f32_16x16x64_bf16 v[232:235], v[196:199], v[120:127], %[ix]\n\t" "ds_read_b128 v[180:183], %[l0] offset:29696\n\t" \
    "v_add_f32 v228, v228, v229\n\t" "v_add_f32 v230, v230, v231\n\t" "v_smfmac_f32_16x16x64_bf16 v[236:239], v[196:199], v[184:191], %[ix]\n\t" \
    "ds_read_b128 v[184:187], %[l0] offset:30720\n\t" "v_cvt_pk_bf16_f32 v218, v224, v228\n\t" "v_cvt_pk_bf16_f32 v219, v226, v230\n\t" \
    "v_smfmac_f32_16x16x64_bf16 v[232:235], v[240:243], v[128:135], %[ix]\n\t" "ds_write2_b32 %[qw], v218, v219 offset1:8\n\t" \
    "ds_read_b128 v[188:191], %[l0] offset:31744\n\t" "v_smfmac_f32_16x16x64_bf16 v[236:239], v[240:243], v[160:167], %[ix]\n\t" \
    "ds_read_b128 v[192:195], %[kx] offset:4352\n\t" "ds_read_b128 v[196:199], %[kx] offset:6400\n\t" \
    "v_smfmac_f32_16x16x64_bf16 v[232:235], v[244:247], v[136:143], %[ix]\n\t" "v_smfmac_f32_16x16x64_bf16 v[236:239], v[244:247], v[168:175], %[ix]\n\t" \
    "s_waitcnt lgkmcnt(3)\n\t" "s_barrier\n\t" "v_smfmac_f32_16x16x64_bf16 v[232:235], v[248:251], v[144:151], %[ix]\n\t" "ds_read_b128 v[216:219], %[l1] offset:24576\n\t" \
    "ds_read_b128 v[220:223], %[l1] offset:25600\n\t" "v_smfmac_f32_16x16x64_bf16 v[236:239], v[248:251], v[176:183], %[ix]\n\t" \
    "v_smfmac_f32_16x16x64_bf16 v[232:235], v[252:255], v[152:159], %[ix]\n\t" "s_waitcnt lgkmcnt(4)\n\t" \
    "v_smfmac_f32_16x16x64_bf16 v[236:239], v[252:255], v[184:191], %[ix]\n\t" XT(92, 93) "s_waitcnt lgkmcnt(1)\n\t" \
    "v_smfmac_f32_16x16x64_bf16 v[208:211], v[216:219], v[16:23], %[ix]\n\t" "s_nop 1\n\t" "v_smfmac_f32_16x16x64_bf16 v[212:215], v[216:219], v[200:207], %[ix]\n\t" \
    "s_waitcnt lgkmcnt(0)\n\t" "s_nop 0\n\t" "v_add_f32 v232, v232, v233\n\t" "v_add_f32 v234, v234, v235\n\t" "v_add_f32 v236, v236, v237\n\t" \
    "v_add_f32 v238, v238, v239\n\t" "v_cvt_pk_bf16_f32 v232, v232, v236\n\t" "v_cvt_pk_bf16_f32 v233, v234, v238\n\t" "ds_write2_b32 %[kw], v232, v233 offset1:68\n\t" \
    "v_smfmac_f32_16x16x64_bf16 v[208:211], v[220:223], v[24:31], %[ix]\n\t" "v_smfmac_f32_16x16x64_bf16 v[212:215], v[220:223], v[192:199], %[ix]\n\t" \
    "v_mov_b64 v[224:225], 0\n\t" "v_mov_b64 v[226:227], 0\n\t" "s_waitcnt lgkmcnt(0)\n\t" "s_barrier\n\t" XT(94, 95) "ds_read_b128 v[160:163], %[km] offset:0\n\t" \
    "ds_read_b128 v[164:167], %[km] offset:64\n\t" "ds_read_b128 v[168:171], %[km] offset:128\n\t" "ds_read_b128 v[172:175], %[km] offset:192\n\t" \
    "v_add_f32 v178, v208, v209\n\t" "v_add_f32 v179, v210, v211\n\t" "v_add_f32 v180, v212, v213\n\t" "v_add_f32 v181, v214, v215\n\t" \
    "v_pk_mul_f32 v[178:179], v[178:179], s[66:67]\n\t" "v_pk_mul_f32 v[180:181], v[180:181], s[66:67]\n\t" "v_max_f32 v182, v178, v180\n\t" "v_max_f32 v183, v179, v181\n\t" \
    "s_nop 0\n\t" "v_max_f32_dpp v182, v182, v182 row_ror:8 row_mask:0xf bank_mask:0xf\n\t" "v_max_f32_dpp v183, v183, v183 row_ror:8 row_mask:0xf bank_mask:0xf\n\t" \
    "s_nop 0\n\t" "s_waitcnt lgkmcnt(2)\n\t" "v_smfmac_f32_16x16x64_bf16 v[224:227], v[216:219], v[160:167], %[ix]\n\t" "s_waitcnt lgkmcnt(0)\n\t" \
    "v_smfmac_f32_16x16x64_bf16 v[224:227], v[220:223], v[168:175], %[ix]\n\t" "s_nop 7\n\t" "v_add_f32 v176, v224, v225\n\t" "v_add_f32 v177, v226, v227\n\t" \
    "v_mul_f32 v176, s66, v176\n\t" "v_mul_f32 v177, s66, v177\n\t" "v_max_f32 v182, v176, v182\n\t" "v_max_f32 v183, v177, v183\n\t" "s_nop 0\n\t" \
    "v_max_f32_dpp v182, v182, v182 row_ror:4 row_mask:0xf bank_mask:0xf\n\t" "v_max_f32_dpp v183, v183, v183 row_ror:4 row_mask:0xf bank_mask:0xf\n\t" "s_nop 0\n\t" \
    "v_max_f32_dpp v182, v182, v182 row_ror:2 row_mask:0xf bank_mask:0xf\n\t" "v_max_f32_dpp v183, v183, v183 row_ror:2 row_mask:0xf bank_mask:0xf\n\t" "s_nop 0\n\t" \
    "v_max_f32_dpp v182, v182, v182 row_ror:1 row_mask:0xf bank_mask:0xf\n\t" "v_max_f32_dpp v183, v183, v183 row_ror:1 row_mask:0xf bank_mask:0xf\n\t" \
    "v_pk_add_f32 v[178:179], v[178:179], v[182:183] neg_lo:[0,1] neg_hi:[0,1]\n\t" "v_pk_add_f32 v[180:181], v[180:181], v[182:183] neg_lo:[0,1] neg_hi:[0,1]\n\t" \
    "v_pk_add_f32 v[176:177], v[176:177], v[182:183] neg_lo:[0,1] neg_hi:[0,1]\n\t" "v_exp_f32 v178, v178\n\t" "v_exp_f32 v179, v179\n\t" "v_exp_f32 v180, v180\n\t" \
    "v_exp_f32 v181, v181\n\t" "v_exp_f32 v176, v176\n\t" "v_pk_add_f32 v[182:183], v[178:179], v[180:181]\n\t" "v_exp_f32 v177, v177\n\t" "s_nop 0\n\t" \
    "v_add_f32_dpp v182, v182, v182 row_ror:8 row_mask:0xf bank_mask:0xf\n\t" "v_add_f32_dpp v183, v183, v183 row_ror:8 row_mask:0xf bank_mask:0xf\n\t" \
    "v_add_f32 v182, v182, v176\n\t" "v_add_f32 v183, v183, v177\n\t" "s_nop 0\n\t" "v_add_f32_dpp v182, v182, v182 row_ror:4 row_mask:0xf bank_mask:0xf\n\t" \
    "v_add_f32_dpp v183, v183, v183 row_ror:4 row_mask:0xf bank_mask:0xf\n\t" "s_nop 0\n\t" "v_add_f32_dpp v182, v182, v182 row_ror:2 row_mask:0xf bank_mask:0xf\n\t" \
    "v_add_f32_dpp v183, v183, v183 row_ror:2 row_mask:0xf bank_mask:0xf\n\t" "s_nop 0\n\t" "v_add_f32_dpp v182, v182, v182 row_ror:1 row_mask:0xf bank_mask:0xf\n\t" \
    "v_add_f32_dpp v183, v183, v183 row_ror:1 row_mask:0xf bank_mask:0xf\n\t" "v_rcp_f32 v182, v182\n\t" "v_rcp_f32 v183, v183\n\t" "s_nop 0\n\t" \
    "v_pk_mul_f32 v[176:177], v[176:177], v[182:183]\n\t" "v_pk_mul_f32 v[178:179], v[178:179], v[182:183]\n\t" "v_pk_mul_f32 v[180:181], v[180:181], v[182:183]\n\t" \
    "v_cvt_pk_bf16_f32 v176, v176, v177\n\t" "v_cvt_pk_bf16_f32 v178, v178, v179\n\t" "v_cvt_pk_bf16_f32 v180, v180, v181\n\t" \
    "v_mov_b32_dpp v177, v176 quad_perm:[1,0,3,2] row_mask:0xf bank_mask:0xf\n\t" "v_mov_b32_dpp v179, v178 quad_perm:[1,0,3,2] row_mask:0xf bank_mask:0xf\n\t" \
    "v_mov_b32_dpp v181, v180 quad_perm:[1,0,3,2] row_mask:0xf bank_mask:0xf\n\t" "v_perm_b32 v184, v179, v178, %[ps]\n\t" "v_perm_b32 v185, v181, v180, %[ps]\n\t" \
    "buffer_store_dwordx2 v[184:185], %[px], %[rs], %[pb] offen sc1\n\t" "v_perm_b32 v186, v177, v176, %[ps]\n\t" "buffer_store_dword v186, %[pm], %[rs], %[pb] offen sc1\n\t" \
    XT(96, 97) LMARK "s_waitcnt vmcnt(2)\n\t" "s_branch 99f\n\t" "11:\n\t" "ds_read_b128 v[220:223], %[l1] offset:0\n\t" "s_waitcnt lgkmcnt(0)\n\t" \
    "v_max3_u32 v218, v220, v221, v222\n\t" "v_max_u32 v218, v218, v223\n\t" "ds_read_b128 v[220:223], %[l1] offset:1024\n\t" "s_waitcnt lgkmcnt(0)\n\t" \
    "v_max3_u32 v218, v218, v220, v221\n\t" "v_max3_u32 v218, v218, v222, v223\n\t" "ds_read_b128 v[220:223], %[l1] offset:2048\n\t" "s_waitcnt lgkmcnt(0)\n\t" \
    "v_max3_u32 v218, v218, v220, v221\n\t" "v_max3_u32 v218, v218, v222, v223\n\t" "ds_read_b128 v[220:223], %[l1] offset:3072\n\t" "s_waitcnt lgkmcnt(0)\n\t" \
    "v_max3_u32 v218, v218, v220, v221\n\t" "v_max3_u32 v218, v218, v222, v223\n\t" "v_cmp_eq_u32 vcc, -1, v218\n\t" "s_cbranch_vccz 12b\n\t" "s_cmp_eq_u32 %[rt], 1\n\t" \
    "s_cbranch_scc1 12b\n\t" "v_mov_b32 v218, %[rt]\n\t" "ds_write_b32 v219, v218\n\t" "s_waitcnt lgkmcnt(0)\n\t" "s_branch 12b\n\t" "13:\n\t" "s_sub_u32 %[rt], %[rt], 1\n\t" \
    "s_waitcnt lgkmcnt(0)\n\t" XCNT(100) "14:\n\t" "buffer_load_dwordx4 v[240:243], %[mo], %[rs], %[sb] offen sc1\n\t" \
    "buffer_load_dwordx4 v[244:247], v216, %[rs], %[sb] offen sc1\n\t" "buffer_load_dwordx4 v[248:251], %[mo], %[rs], s60 offen sc1\n\t" \
    "buffer_load_dwordx4 v[252:255], v216, %[rs], s60 offen sc1\n\t" "s_waitcnt vmcnt(0)\n\t" "v_max_u32 v218, v240, v241\n\t" "v_max3_u32 v218, v218, v242, v243\n\t" \
    "v_max3_u32 v218, v218, v244, v245\n\t" "v_max3_u32 v218, v218, v246, v247\n\t" "v_max3_u32 v218, v218, v248, v249\n\t" "v_max3_u32 v218, v218, v250, v251\n\t" \
    "v_max3_u32 v218, v218, v252, v253\n\t" "v_max3_u32 v218, v218, v254, v255\n\t" "v_cmp_eq_u32 vcc, -1, v218\n\t" "s_cbranch_vccz 15f\n\t" "s_cmp_eq_u32 %[bd], 0\n\t" \
    "s_cbranch_scc1 15f\n\t" "s_sub_u32 %[bd], %[bd], 1\n\t" XCNT(101) "s_branch 14b\n\t" "15:\n\t" "ds_read_b128 v[160:163], %[l0] offset:0\n\t" \
    "ds_read_b128 v[164:167], %[l0] offset:1024\n\t" "ds_read_b128 v[168:171], %[l0] offset:2048\n\t" "ds_read_b128 v[172:175], %[l0] offset:3072\n\t" \
    "ds_read_b128 v[176:179], %[l0] offset:4096\n\t" "ds_read_b128 v[180:183], %[l0] offset:5120\n\t" "ds_read_b128 v[184:187], %[l0] offset:6144\n\t" \
    "ds_read_b128 v[188:191], %[l0] offset:7168\n\t" "v_mov_b64 v[224:225], 0\n\t" "v_mov_b64 v[226:227], 0\n\t" "v_mov_b64 v[228:229], 0\n\t" "v_mov_b64 v[230:231], 0\n\t" \
    "v_mov_b64 v[232:233], 0\n\t" "v_mov_b64 v[234:235], 0\n\t" "v_mov_b64 v[236:237], 0\n\t" "v_mov_b64 v[238:239], 0\n\t" "s_barrier\n\t" "s_branch 10b\n\t" "99:\n\t"
#define QK_STEP1 \
    ".p2align 6\n\t" "v_xor_b32 v216, 0x400, %[mo]\n\t" "s_xor_b32 s60, %[sb], 0x800\n\t" "s_add_u32 s61, %[qb], 0x1000\n\t" "s_mov_b32 s66, 0x3e0293ee\n\t" \
    "s_mov_b32 s67, 0x3e0293ee\n\t" XT(80, 81) XT(98, 99) "s_barrier\n\t" XT(82, 83) "buffer_load_dwordx4 v[240:243], %[mo], %[rs], %[sb] offen sc1\n\t" \
    "v_mov_b64 v[224:225], 0\n\t" "v_mov_b64 v[226:227], 0\n\t" "v_mov_b64 v[228:229], 0\n\t" "v_mov_b64 v[230:231], 0\n\t" "v_mov_b64 v[232:233], 0\n\t" \
    "v_mov_b64 v[234:235], 0\n\t" "v_mov_b64 v[236:237], 0\n\t" "v_mov_b64 v[238:239], 0\n\t" "buffer_load_dwordx4 v[244:247], v216, %[rs], %[sb] offen sc1\n\t" \
    "ds_read_b128 v[160:163], %[l0] offset:0\n\t" "ds_read_b128 v[164:167], %[l0] offset:1024\n\t" "ds_read_b128 v[168:171], %[l0] offset:2048\n\t" \
    "ds_read_b128 v[172:175], %[l0] offset:3072\n\t" "s_nop 3\n\t" "buffer_load_dwordx4 v[248:251], %[mo], %[rs], s60 offen sc1\n\t" \
    "ds_read_b128 v[176:179], %[l0] offset:4096\n\t" "ds_read_b128 v[180:183], %[l0] offset:5120\n\t" "ds_read_b128 v[184:187], %[l0] offset:6144\n\t" \
    "ds_read_b128 v[188:191], %[l0] offset:7168\n\t" "s_nop 3\n\t" "buffer_load_dwordx4 v[252:255], v216, %[rs], s60 offen sc1\n\t" "v_mov_b32 v219, 160512\n\t" \
    "v_mov_b64 v[220:221], -1\n\t" "v_mov_b64 v[222:223], -1\n\t" "buffer_store_dwordx2 v[220:221], %[px], %[rs], %[tr] offen sc1\n\t" \
    "buffer_store_dwordx2 v[220:221], %[pm], %[rs], %[tr] offen sc1\n\t" "v_mov_b32 v208, 0xd028965f\n\t" "v_mov_b32 v209, 0xff800000\n\t" "s_mov_b32 s62, 0\n\t" \
    "s_mov_b32 s63, -1\n\t" "v_mov_b32 v210, 0\n\t" "v_cmp_gt_i32 vcc, 0, %[ps]\n\t" "v_cndmask_b32 v204, 0, v209, s[62:63]\n\t" "s_nop 0\n\t" \
    "v_cndmask_b32 v200, 0, v208, vcc\n\t" "v_cmp_gt_i32 vcc, 1, %[ps]\n\t" "v_cndmask_b32 v205, 0, v209, s[62:63]\n\t" "s_nop 0\n\t" "v_cndmask_b32 v201, 0, v208, vcc\n\t" \
    "v_cmp_gt_i32 vcc, 2, %[ps]\n\t" "v_cndmask_b32 v206, 0, v209, s[62:63]\n\t" "s_nop 0\n\t" "v_cndmask_b32 v202, 0, v208, vcc\n\t" "v_cmp_gt_i32 vcc, 3, %[ps]\n\t" \
    "v_cndmask_b32 v207, 0, v209, s[62:63]\n\t" "s_nop 0\n\t" "v_cndmask_b32 v203, 0, v208, vcc\n\t" "20:\n\t" "s_waitcnt vmcnt(5)\n\t" XT(84, 85) \
    "v_smfmac_f32_16x16x64_bf16 v[224:227], v[240:243], a[0:7], %[ix]\n\t" "ds_write_b128 %[l1], v[240:243] offset:4096\n\t" \
    "v_smfmac_f32_16x16x64_bf16 v[228:231], v[240:243], a[8:15], %[ix]\n\t" "v_smfmac_f32_16x16x64_bf16 v[232:235], v[240:243], v[32:39], %[ix]\n\t" \
    "s_waitcnt lgkmcnt(7)\n\t" "v_smfmac_f32_16x16x64_bf16 v[236:239], v[240:243], v[160:167], %[ix]\n\t" "ds_read_b128 v[160:163], %[l0] offset:8192\n\t" \
    "ds_read_b128 v[164:167], %[l0] offset:9216\n\t" "s_waitcnt vmcnt(4)\n\t" "v_smfmac_f32_16x16x64_bf16 v[224:227], v[244:247], a[16:23], %[ix]\n\t" \
    "ds_write_b128 %[l1], v[244:247] offset:5120\n\t" "v_smfmac_f32_16x16x64_bf16 v[228:231], v[244:247], a[24:31], %[ix]\n\t" \
    "v_smfmac_f32_16x16x64_bf16 v[232:235], v[244:247], v[40:47], %[ix]\n\t" "s_waitcnt lgkmcnt(8)\n\t" \
    "v_smfmac_f32_16x16x64_bf16 v[236:239], v[244:247], v[168:175], %[ix]\n\t" "ds_read_b128 v[168:171], %[l0] offset:10240\n\t" \
    "ds_read_b128 v[172:175], %[l0] offset:11264\n\t" "s_waitcnt vmcnt(3)\n\t" "v_smfmac_f32_16x16x64_bf16 v[224:227], v[248:251], a[32:39], %[ix]\n\t" \
    "ds_write_b128 %[l1], v[248:251] offset:6144\n\t" "v_smfmac_f32_16x16x64_bf16 v[228:231], v[248:251], a[40:47], %[ix]\n\t" "s_waitcnt lgkmcnt(3)\n\t" "s_barrier\n\t" \
    XT(86, 87) "v_smfmac_f32_16x16x64_bf16 v[232:235], v[248:251], v[48:55], %[ix]\n\t" "ds_read_b128 v[192:195], %[l1] offset:8192\n\t" \
    "ds_read_b128 v[196:199], %[l1] offset:12288\n\t" "v_smfmac_f32_16x16x64_bf16 v[236:239], v[248:251], v[176:183], %[ix]\n\t" \
    "ds_read_b128 v[176:179], %[l0] offset:12288\n\t" "ds_read_b128 v[180:183], %[l0] offset:13312\n\t" "s_waitcnt vmcnt(2)\n\t" \
    "v_smfmac_f32_16x16x64_bf16 v[224:227], v[252:255], a[48:55], %[ix]\n\t" "ds_write_b128 %[l1], v[252:255] offset:7168\n\t" \
    "v_smfmac_f32_16x16x64_bf16 v[228:231], v[252:255], a[56:63], %[ix]\n\t" "ds_read_b128 v[240:243], %[l1] offset:0\n\t" "ds_read_b128 v[244:247], %[l1] offset:9216\n\t" \
    "v_smfmac_f32_16x16x64_bf16 v[232:235], v[252:255], v[56:63], %[ix]\n\t" "s_nop 1\n\t" "v_smfmac_f32_16x16x64_bf16 v[236:239], v[252:255], v[184:191], %[ix]\n\t" \
    "ds_read_b128 v[184:187], %[l0] offset:14336\n\t" "v_cmp_u_f32 vcc, v224, v225\n\t" "v_cmp_u_f32 s[62:63], v226, v227\n\t" "s_waitcnt lgkmcnt(7)\n\t" \
    "v_smfmac_f32_16x16x64_bf16 v[224:227], v[192:195], a[64:71], %[ix]\n\t" "ds_read_b128 v[188:191], %[l0] offset:15360\n\t" \
    "v_smfmac_f32_16x16x64_bf16 v[228:231], v[192:195], a[72:79], %[ix]\n\t" "s_or_b64 vcc, vcc, s[62:63]\n\t" "s_cbranch_scc1 21f\n\t" "22:\n\t" \
    "v_smfmac_f32_16x16x64_bf16 v[232:235], v[192:195], v[64:71], %[ix]\n\t" "s_waitcnt lgkmcnt(4)\n\t" "s_barrier\n\t" XT(88, 89) "ds_read_b32 v218, v219\n\t" \
    "v_smfmac_f32_16x16x64_bf16 v[236:239], v[192:195], v[160:167], %[ix]\n\t" "ds_read_b128 v[160:163], %[l0] offset:16384\n\t" \
    "ds_read_b128 v[164:167], %[l0] offset:17408\n\t" "v_smfmac_f32_16x16x64_bf16 v[224:227], v[196:199], a[80:87], %[ix]\n\t" \
    "ds_read_b128 v[248:251], %[l1] offset:13312\n\t" "ds_read_b128 v[252:255], %[l1] offset:1024\n\t" \
    "v_smfmac_f32_16x16x64_bf16 v[228:231], v[196:199], a[88:95], %[ix]\n\t" "ds_read_b128 v[192:195], %[l1] offset:10240\n\t" \
    "v_smfmac_f32_16x16x64_bf16 v[232:235], v[196:199], v[72:79], %[ix]\n\t" "v_smfmac_f32_16x16x64_bf16 v[236:239], v[196:199], v[168:175], %[ix]\n\t" \
    "s_waitcnt lgkmcnt(5)\n\t" "v_cmp_eq_u32 vcc, %[rt], v218\n\t" "v_smfmac_f32_16x16x64_bf16 v[224:227], v[240:243], a[96:103], %[ix]\n\t" "s_cbranch_vccnz 23f\n\t" \
    "ds_read_b128 v[168:171], %[l0] offset:18432\n\t" "v_smfmac_f32_16x16x64_bf16 v[228:231], v[240:243], a[104:111], %[ix]\n\t" \
    "ds_read_b128 v[172:175], %[l0] offset:19456\n\t" "ds_read_b128 v[196:199], %[l1] offset:14336\n\t" \
    "v_smfmac_f32_16x16x64_bf16 v[232:235], v[240:243], v[80:87], %[ix]\n\t" "buffer_load_dwordx4 v[16:19], %[hq], %[rs], %[qb] offen offset:0 sc1\n\t" \
    "v_smfmac_f32_16x16x64_bf16 v[236:239], v[240:243], v[176:183], %[ix]\n\t" "ds_read_b128 v[176:179], %[l0] offset:20480\n\t" \
    "ds_read_b128 v[180:183], %[l0] offset:21504\n\t" "v_smfmac_f32_16x16x64_bf16 v[224:227], v[244:247], a[112:119], %[ix]\n\t" \
    "ds_read_b128 v[240:243], %[l1] offset:2048\n\t" "v_smfmac_f32_16x16x64_bf16 v[228:231], v[244:247], a[120:127], %[ix]\n\t" \
    "v_smfmac_f32_16x16x64_bf16 v[232:235], v[244:247], v[88:95], %[ix]\n\t" "buffer_load_dwordx4 v[20:23], %[hq], %[rs], %[qb] offen offset:2048 sc1\n\t" \
    "v_smfmac_f32_16x16x64_bf16 v[236:239], v[244:247], v[184:191], %[ix]\n\t" "ds_read_b128 v[184:187], %[l0] offset:22528\n\t" \
    "ds_read_b128 v[188:191], %[l0] offset:23552\n\t" "s_waitcnt lgkmcnt(10)\n\t" "v_smfmac_f32_16x16x64_bf16 v[224:227], v[248:251], a[128:135], %[ix]\n\t" \
    "ds_read_b128 v[244:247], %[l1] offset:11264\n\t" "v_smfmac_f32_16x16x64_bf16 v[228:231], v[248:251], a[136:143], %[ix]\n\t" \
    "v_smfmac_f32_16x16x64_bf16 v[232:235], v[248:251], v[96:103], %[ix]\n\t" "buffer_load_dwordx4 v[24:27], %[hq], %[rs], s61 offen offset:0 sc1\n\t" \
    "v_smfmac_f32_16x16x64_bf16 v[236:239], v[248:251], v[160:167], %[ix]\n\t" "ds_read_b128 v[160:163], %[l0] offset:24576\n\t" \
    "ds_read_b128 v[164:167], %[l0] offset:25600\n\t" "s_waitcnt lgkmcnt(12)\n\t" "v_smfmac_f32_16x16x64_bf16 v[224:227], v[252:255], a[144:151], %[ix]\n\t" \
    "ds_read_b128 v[248:251], %[l1] offset:15360\n\t" "v_smfmac_f32_16x16x64_bf16 v[228:231], v[252:255], a[152:159], %[ix]\n\t" \
    "v_smfmac_f32_16x16x64_bf16 v[232:235], v[252:255], v[104:111], %[ix]\n\t" "buffer_load_dwordx4 v[28:31], %[hq], %[rs], s61 offen offset:2048 sc1\n\t" \
    "s_waitcnt lgkmcnt(10)\n\t" "v_smfmac_f32_16x16x64_bf16 v[236:239], v[252:255], v[168:175], %[ix]\n\t" "ds_read_b128 v[168:171], %[l0] offset:26624\n\t" \
    "ds_read_b128 v[172:175], %[l0] offset:27648\n\t" "v_smfmac_f32_16x16x64_bf16 v[224:227], v[192:195], a[160:167], %[ix]\n\t" \
    "ds_read_b128 v[252:255], %[l1] offset:3072\n\t" "v_smfmac_f32_16x16x64_bf16 v[228:231], v[192:195], a[168:175], %[ix]\n\t" "s_waitcnt lgkmcnt(12)\n\t" \
    "v_smfmac_f32_16x16x64_bf16 v[224:227], v[196:199], a[176:183], %[ix]\n\t" "v_smfmac_f32_16x16x64_bf16 v[228:231], v[196:199], a[184:191], %[ix]\n\t" \
    "s_waitcnt lgkmcnt(9)\n\t" "v_smfmac_f32_16x16x64_bf16 v[224:227], v[240:243], a[192:199], %[ix]\n\t" \
    "v_smfmac_f32_16x16x64_bf16 v[228:231], v[240:243], a[200:207], %[ix]\n\t" "s_waitcnt lgkmcnt(6)\n\t" \
    "v_smfmac_f32_16x16x64_bf16 v[224:227], v[244:247], a[208:215], %[ix]\n\t" "v_smfmac_f32_16x16x64_bf16 v[228:231], v[244:247], a[216:223], %[ix]\n\t" \
    "s_waitcnt vmcnt(4)\n\t" "s_waitcnt lgkmcnt(3)\n\t" "v_smfmac_f32_16x16x64_bf16 v[224:227], v[248:251], a[224:231], %[ix]\n\t" \
    "v_smfmac_f32_16x16x64_bf16 v[228:231], v[248:251], a[232:239], %[ix]\n\t" "s_waitcnt lgkmcnt(0)\n\t" \
    "v_smfmac_f32_16x16x64_bf16 v[224:227], v[252:255], a[240:247], %[ix]\n\t" "s_nop 1\n\t" "v_smfmac_f32_16x16x64_bf16 v[228:231], v[252:255], a[248:255], %[ix]\n\t" \
    XT(90, 91) "s_nop 1\n\t" "v_smfmac_f32_16x16x64_bf16 v[232:235], v[192:195], v[112:119], %[ix]\n\t" "s_nop 1\n\t" \
    "v_smfmac_f32_16x16x64_bf16 v[236:239], v[192:195], v[176:183], %[ix]\n\t" "ds_read_b128 v[176:179], %[l0] offset:28672\n\t" "v_add_f32 v224, v224, v225\n\t" \
    "v_add_f32 v226, v226, v227\n\t" "v_smfmac_f32_16x16x64_bf16 v[232:235], v[196:199], v[120:127], %[ix]\n\t" "ds_read_b128 v[180:183], %[l0] offset:29696\n\t" \
    "v_add_f32 v228, v228, v229\n\t" "v_add_f32 v230, v230, v231\n\t" "v_smfmac_f32_16x16x64_bf16 v[236:239], v[196:199], v[184:191], %[ix]\n\t" \
    "ds_read_b128 v[184:187], %[l0] offset:30720\n\t" "v_cvt_pk_bf16_f32 v218, v224, v228\n\t" "v_cvt_pk_bf16_f32 v219, v226, v230\n\t" \
    "v_smfmac_f32_16x16x64_bf16 v[232:235], v[240:243], v[128:135], %[ix]\n\t" "ds_write2_b32 %[qw], v218, v219 offset1:8\n\t" \
    "ds_read_b128 v[188:191], %[l0] offset:31744\n\t" "v_smfmac_f32_16x16x64_bf16 v[236:239], v[240:243], v[160:167], %[ix]\n\t" \
    "v_smfmac_f32_16x16x64_bf16 v[232:235], v[244:247], v[136:143], %[ix]\n\t" "v_smfmac_f32_16x16x64_bf16 v[236:239], v[244:247], v[168:175], %[ix]\n\t" \
    "s_waitcnt lgkmcnt(1)\n\t" "s_barrier\n\t" "v_smfmac_f32_16x16x64_bf16 v[232:235], v[248:251], v[144:151], %[ix]\n\t" \
    "v_smfmac_f32_16x16x64_bf16 v[236:239], v[248:251], v[176:183], %[ix]\n\t" "v_smfmac_f32_16x16x64_bf16 v[232:235], v[252:255], v[152:159], %[ix]\n\t" \
    "s_waitcnt lgkmcnt(0)\n\t" "v_smfmac_f32_16x16x64_bf16 v[236:239], v[252:255], v[184:191], %[ix]\n\t" XT(92, 93) "s_nop 6\n\t" "v_add_f32 v232, v232, v233\n\t" \
    "v_add_f32 v234, v234, v235\n\t" "v_add_f32 v236, v236, v237\n\t" "v_add_f32 v238, v238, v239\n\t" "v_cvt_pk_bf16_f32 v232, v232, v236\n\t" \
    "v_cvt_pk_bf16_f32 v233, v234, v238\n\t" "ds_write2_b32 %[kw], v232, v233 offset1:68\n\t" "s_waitcnt lgkmcnt(0)\n\t" "s_barrier\n\t" XT(94, 95) \
    "s_cmp_ge_u32 %[sq], %[sn]\n\t" "s_cbranch_scc1 277f\n\t" "275:\n\t" "global_load_dword v240, v210, %[fq] sc1\n\t" "s_waitcnt vmcnt(0)\n\t" \
    "v_readfirstlane_b32 %[sq], v240\n\t" "s_nop 3\n\t" "s_cmp_ge_u32 %[sq], %[sn]\n\t" "s_cbranch_scc1 276f\n\t" "s_cmp_eq_u32 %[bd], 0\n\t" "s_cbranch_scc1 276f\n\t" \
    "s_sub_u32 %[bd], %[bd], 1\n\t" "s_sleep 2\n\t" "s_branch 275b\n\t" "276:\n\t" "buffer_load_dwordx4 v[16:19], %[hq], %[rs], %[qb] offen offset:0 sc1\n\t" \
    "buffer_load_dwordx4 v[20:23], %[hq], %[rs], %[qb] offen offset:2048 sc1\n\t" "buffer_load_dwordx4 v[24:27], %[hq], %[rs], s61 offen offset:0 sc1\n\t" \
    "buffer_load_dwordx4 v[28:31], %[hq], %[rs], s61 offen offset:2048 sc1\n\t" "277:\n\t" "s_nop 3\n\t" "ds_read_b128 v[160:163], %[kx] offset:0\n\t" \
    "ds_read_b128 v[164:167], %[kx] offset:2048\n\t" "ds_read_b128 v[168:171], %[kx] offset:4096\n\t" "ds_read_b128 v[172:175], %[kx] offset:6144\n\t" \
    "ds_read_b128 v[224:227], %[km] offset:0\n\t" "ds_read_b128 v[228:231], %[km] offset:64\n\t" "ds_read_b128 v[232:235], %[km] offset:128\n\t" \
    "ds_read_b128 v[236:239], %[km] offset:192\n\t" "s_waitcnt vmcnt(0)\n\t" "global_load_dword v240, v210, %[fq] sc1\n\t" "s_waitcnt lgkmcnt(7)\n\t" \
    "v_mfma_f32_16x16x32_bf16 v[248:251], v[160:163], v[16:19], v[200:203]\n\t" "s_waitcnt lgkmcnt(6)\n\t" \
    "v_mfma_f32_16x16x32_bf16 v[248:251], v[164:167], v[20:23], v[248:251]\n\t" "s_waitcnt lgkmcnt(5)\n\t" \
    "v_mfma_f32_16x16x32_bf16 v[248:251], v[168:171], v[24:27], v[248:251]\n\t" "s_waitcnt lgkmcnt(4)\n\t" \
    "v_mfma_f32_16x16x32_bf16 v[248:251], v[172:175], v[28:31], v[248:251]\n\t" "s_waitcnt lgkmcnt(3)\n\t" \
    "v_mfma_f32_16x16x32_bf16 v[244:247], v[224:227], v[16:19], v[204:207]\n\t" "s_waitcnt lgkmcnt(2)\n\t" \
    "v_mfma_f32_16x16x32_bf16 v[244:247], v[228:231], v[20:23], v[244:247]\n\t" "s_waitcnt lgkmcnt(1)\n\t" \
    "v_mfma_f32_16x16x32_bf16 v[244:247], v[232:235], v[24:27], v[244:247]\n\t" "s_waitcnt lgkmcnt(0)\n\t" \
    "v_mfma_f32_16x16x32_bf16 v[244:247], v[236:239], v[28:31], v[244:247]\n\t" "s_nop 3\n\t" "v_max3_f32 v242, v248, v249, v250\n\t" "v_max_f32 v242, v242, v251\n\t" \
    "s_nop 1\n\t" "v_max3_f32 v242, v242, v244, v245\n\t" "v_max3_f32 v242, v242, v246, v247\n\t" "v_mov_b32 v243, v242\n\t" "s_nop 1\n\t" \
    "v_permlane32_swap_b32 v242, v243\n\t" "v_max_f32 v242, v242, v243\n\t" "v_mov_b32 v243, v242\n\t" "s_nop 1\n\t" "v_permlane16_swap_b32 v242, v243\n\t" \
    "v_max_f32 v242, v242, v243\n\t" "v_pk_add_f32 v[244:245], v[244:245], v[242:243] op_sel_hi:[1,0] neg_lo:[0,1] neg_hi:[0,1]\n\t" \
    "v_pk_add_f32 v[246:247], v[246:247], v[242:243] op_sel_hi:[1,0] neg_lo:[0,1] neg_hi:[0,1]\n\t" \
    "v_pk_add_f32 v[248:249], v[248:249], v[242:243] op_sel_hi:[1,0] neg_lo:[0,1] neg_hi:[0,1]\n\t" \
    "v_pk_add_f32 v[250:251], v[250:251], v[242:243] op_sel_hi:[1,0] neg_lo:[0,1] neg_hi:[0,1]\n\t" "v_pk_mul_f32 v[244:245], v[244:245], s[66:67]\n\t" \
    "v_pk_mul_f32 v[246:247], v[246:247], s[66:67]\n\t" "v_pk_mul_f32 v[248:249], v[248:249], s[66:67]\n\t" "v_pk_mul_f32 v[250:251], v[250:251], s[66:67]\n\t" \
    "v_exp_f32 v244, v244\n\t" "v_exp_f32 v245, v245\n\t" "v_exp_f32 v246, v246\n\t" "v_exp_f32 v247, v247\n\t" "v_exp_f32 v248, v248\n\t" "v_exp_f32 v249, v249\n\t" \
    "v_exp_f32 v250, v250\n\t" "v_exp_f32 v251, v251\n\t" "v_pk_add_f32 v[242:243], v[244:245], v[246:247]\n\t" "v_pk_add_f32 v[242:243], v[242:243], v[248:249]\n\t" \
    "v_pk_add_f32 v[242:243], v[242:243], v[250:251]\n\t" "v_add_f32 v242, v242, v243\n\t" "v_mov_b32 v243, v242\n\t" "s_nop 1\n\t" "v_permlane32_swap_b32 v242, v243\n\t" \
    "v_add_f32 v242, v242, v243\n\t" "v_mov_b32 v243, v242\n\t" "s_nop 1\n\t" "v_permlane16_swap_b32 v242, v243\n\t" "v_add_f32 v242, v242, v243\n\t" \
    "v_rcp_f32 v242, v242\n\t" "s_nop 0\n\t" "v_pk_mul_f32 v[244:245], v[244:245], v[242:243] op_sel_hi:[1,0]\n\t" \
    "v_pk_mul_f32 v[246:247], v[246:247], v[242:243] op_sel_hi:[1,0]\n\t" "v_pk_mul_f32 v[248:249], v[248:249], v[242:243] op_sel_hi:[1,0]\n\t" \
    "v_pk_mul_f32 v[250:251], v[250:251], v[242:243] op_sel_hi:[1,0]\n\t" "v_cvt_pk_bf16_f32 v162, v244, v245\n\t" "v_cvt_pk_bf16_f32 v163, v246, v247\n\t" \
    "v_cvt_pk_bf16_f32 v164, v248, v249\n\t" "v_cvt_pk_bf16_f32 v165, v250, v251\n\t" "buffer_store_dwordx2 v[164:165], %[px], %[rs], %[tb] offen sc1\n\t" \
    "buffer_store_dwordx2 v[162:163], %[pm], %[rs], %[tb] offen sc1\n\t" "s_waitcnt vmcnt(2)\n\t" "v_readfirstlane_b32 s62, v240\n\t" "s_nop 3\n\t" \
    "s_max_u32 %[sq], %[sq], s62\n\t" "s_barrier\n\t" XT(96, 97) "s_branch 99f\n\t" "21:\n\t" "ds_read_b128 v[220:223], %[l1] offset:4096\n\t" "s_waitcnt lgkmcnt(0)\n\t" \
    "v_max3_u32 v218, v220, v221, v222\n\t" "v_max_u32 v218, v218, v223\n\t" "ds_read_b128 v[220:223], %[l1] offset:5120\n\t" "s_waitcnt lgkmcnt(0)\n\t" \
    "v_max3_u32 v218, v218, v220, v221\n\t" "v_max3_u32 v218, v218, v222, v223\n\t" "ds_read_b128 v[220:223], %[l1] offset:6144\n\t" "s_waitcnt lgkmcnt(0)\n\t" \
    "v_max3_u32 v218, v218, v220, v221\n\t" "v_max3_u32 v218, v218, v222, v223\n\t" "ds_read_b128 v[220:223], %[l1] offset:7168\n\t" "s_waitcnt lgkmcnt(0)\n\t" \
    "v_max3_u32 v218, v218, v220, v221\n\t" "v_max3_u32 v218, v218, v222, v223\n\t" "v_cmp_eq_u32 vcc, -1, v218\n\t" "s_cbranch_vccz 22b\n\t" "s_cmp_eq_u32 %[rt], 1\n\t" \
    "s_cbranch_scc1 22b\n\t" "v_mov_b32 v218, %[rt]\n\t" "ds_write_b32 v219, v218\n\t" "s_waitcnt lgkmcnt(0)\n\t" "s_branch 22b\n\t" "23:\n\t" "s_sub_u32 %[rt], %[rt], 1\n\t" \
    "s_waitcnt lgkmcnt(0)\n\t" XCNT(100) "24:\n\t" "buffer_load_dwordx4 v[240:243], %[mo], %[rs], %[sb] offen sc1\n\t" \
    "buffer_load_dwordx4 v[244:247], v216, %[rs], %[sb] offen sc1\n\t" "buffer_load_dwordx4 v[248:251], %[mo], %[rs], s60 offen sc1\n\t" \
    "buffer_load_dwordx4 v[252:255], v216, %[rs], s60 offen sc1\n\t" "s_waitcnt vmcnt(0)\n\t" "v_max_u32 v218, v240, v241\n\t" "v_max3_u32 v218, v218, v242, v243\n\t" \
    "v_max3_u32 v218, v218, v244, v245\n\t" "v_max3_u32 v218, v218, v246, v247\n\t" "v_max3_u32 v218, v218, v248, v249\n\t" "v_max3_u32 v218, v218, v250, v251\n\t" \
    "v_max3_u32 v218, v218, v252, v253\n\t" "v_max3_u32 v218, v218, v254, v255\n\t" "v_cmp_eq_u32 vcc, -1, v218\n\t" "s_cbranch_vccz 25f\n\t" "s_cmp_eq_u32 %[bd], 0\n\t" \
    "s_cbranch_scc1 25f\n\t" "s_sub_u32 %[bd], %[bd], 1\n\t" XCNT(101) "s_branch 24b\n\t" "25:\n\t" "ds_read_b128 v[160:163], %[l0] offset:0\n\t" \
    "ds_read_b128 v[164:167], %[l0] offset:1024\n\t" "ds_read_b128 v[168:171], %[l0] offset:2048\n\t" "ds_read_b128 v[172:175], %[l0] offset:3072\n\t" \
    "ds_read_b128 v[176:179], %[l0] offset:4096\n\t" "ds_read_b128 v[180:183], %[l0] offset:5120\n\t" "ds_read_b128 v[184:187], %[l0] offset:6144\n\t" \
    "ds_read_b128 v[188:191], %[l0] offset:7168\n\t" "v_mov_b64 v[224:225], 0\n\t" "v_mov_b64 v[226:227], 0\n\t" "v_mov_b64 v[228:229], 0\n\t" "v_mov_b64 v[230:231], 0\n\t" \
    "v_mov_b64 v[232:233], 0\n\t" "v_mov_b64 v[234:235], 0\n\t" "v_mov_b64 v[236:237], 0\n\t" "v_mov_b64 v[238:239], 0\n\t" "s_barrier\n\t" "s_branch 20b\n\t" "99:\n\t"
#define QK_STEP2 \
    ".p2align 6\n\t" "v_xor_b32 v216, 0x400, %[mo]\n\t" "s_xor_b32 s60, %[sb], 0x800\n\t" "s_add_u32 s61, %[qb], 0x1000\n\t" "s_mov_b32 s66, 0x3e0293ee\n\t" \
    "s_mov_b32 s67, 0x3e0293ee\n\t" XT(80, 81) XT(98, 99) "s_barrier\n\t" XT(82, 83) "buffer_load_dwordx4 v[240:243], %[mo], %[rs], %[sb] offen sc1\n\t" \
    "v_mov_b64 v[224:225], 0\n\t" "v_mov_b64 v[226:227], 0\n\t" "v_mov_b64 v[228:229], 0\n\t" "v_mov_b64 v[230:231], 0\n\t" "v_mov_b64 v[232:233], 0\n\t" \
    "v_mov_b64 v[234:235], 0\n\t" "v_mov_b64 v[236:237], 0\n\t" "v_mov_b64 v[238:239], 0\n\t" "buffer_load_dwordx4 v[244:247], v216, %[rs], %[sb] offen sc1\n\t" \
    "ds_read_b128 v[160:163], %[l0] offset:0\n\t" "ds_read_b128 v[164:167], %[l0] offset:1024\n\t" "ds_read_b128 v[168:171], %[l0] offset:2048\n\t" \
    "ds_read_b128 v[172:175], %[l0] offset:3072\n\t" "s_nop 3\n\t" "buffer_load_dwordx4 v[248:251], %[mo], %[rs], s60 offen sc1\n\t" \
    "ds_read_b128 v[176:179], %[l0] offset:4096\n\t" "ds_read_b128 v[180:183], %[l0] offset:5120\n\t" "ds_read_b128 v[184:187], %[l0] offset:6144\n\t" \
    "ds_read_b128 v[188:191], %[l0] offset:7168\n\t" "s_nop 3\n\t" "buffer_load_dwordx4 v[252:255], v216, %[rs], s60 offen sc1\n\t" "v_mov_b32 v219, 160512\n\t" \
    "v_mov_b64 v[220:221], -1\n\t" "v_mov_b64 v[222:223], -1\n\t" "buffer_store_dwordx4 v[220:223], %[px], %[rs], %[tr] offen sc1\n\t" \
    "buffer_store_dwordx2 v[220:221], %[pm], %[rs], %[tr] offen sc1\n\t" "v_mov_b32 v208, 0xd028965f\n\t" "v_mov_b32 v209, 0xff800000\n\t" "s_mov_b32 s62, 0\n\t" \
    "s_mov_b32 s63, -1\n\t" "v_mov_b32 v210, 0\n\t" "v_cmp_gt_i32 vcc, 0, %[ps]\n\t" "v_cndmask_b32 v204, 0, v209, s[62:63]\n\t" "s_nop 0\n\t" \
    "v_cndmask_b32 v200, 0, v208, vcc\n\t" "v_cmp_gt_i32 vcc, 1, %[ps]\n\t" "v_cndmask_b32 v205, 0, v209, s[62:63]\n\t" "s_nop 0\n\t" "v_cndmask_b32 v201, 0, v208, vcc\n\t" \
    "v_cmp_gt_i32 vcc, 2, %[ps]\n\t" "v_cndmask_b32 v206, 0, v209, s[62:63]\n\t" "s_nop 0\n\t" "v_cndmask_b32 v202, 0, v208, vcc\n\t" "v_cmp_gt_i32 vcc, 3, %[ps]\n\t" \
    "v_cndmask_b32 v207, 0, v209, s[62:63]\n\t" "s_nop 0\n\t" "v_cndmask_b32 v203, 0, v208, vcc\n\t" "30:\n\t" "s_waitcnt vmcnt(5)\n\t" XT(84, 85) \
    "v_smfmac_f32_16x16x64_bf16 v[224:227], v[240:243], a[0:7], %[ix]\n\t" "ds_write_b128 %[l1], v[240:243] offset:8192\n\t" \
    "v_smfmac_f32_16x16x64_bf16 v[228:231], v[240:243], a[8:15], %[ix]\n\t" "v_smfmac_f32_16x16x64_bf16 v[232:235], v[240:243], v[32:39], %[ix]\n\t" \
    "s_waitcnt lgkmcnt(7)\n\t" "v_smfmac_f32_16x16x64_bf16 v[236:239], v[240:243], v[160:167], %[ix]\n\t" "ds_read_b128 v[160:163], %[l0] offset:8192\n\t" \
    "ds_read_b128 v[164:167], %[l0] offset:9216\n\t" "s_waitcnt vmcnt(4)\n\t" "v_smfmac_f32_16x16x64_bf16 v[224:227], v[244:247], a[16:23], %[ix]\n\t" \
    "ds_write_b128 %[l1], v[244:247] offset:9216\n\t" "v_smfmac_f32_16x16x64_bf16 v[228:231], v[244:247], a[24:31], %[ix]\n\t" \
    "v_smfmac_f32_16x16x64_bf16 v[232:235], v[244:247], v[40:47], %[ix]\n\t" "s_waitcnt lgkmcnt(8)\n\t" \
    "v_smfmac_f32_16x16x64_bf16 v[236:239], v[244:247], v[168:175], %[ix]\n\t" "ds_read_b128 v[168:171], %[l0] offset:10240\n\t" \
    "ds_read_b128 v[172:175], %[l0] offset:11264\n\t" "s_waitcnt vmcnt(3)\n\t" "v_smfmac_f32_16x16x64_bf16 v[224:227], v[248:251], a[32:39], %[ix]\n\t" \
    "ds_write_b128 %[l1], v[248:251] offset:10240\n\t" "v_smfmac_f32_16x16x64_bf16 v[228:231], v[248:251], a[40:47], %[ix]\n\t" "s_waitcnt lgkmcnt(3)\n\t" "s_barrier\n\t" \
    XT(86, 87) "v_smfmac_f32_16x16x64_bf16 v[232:235], v[248:251], v[48:55], %[ix]\n\t" "ds_read_b128 v[192:195], %[l1] offset:12288\n\t" \
    "ds_read_b128 v[196:199], %[l1] offset:0\n\t" "v_smfmac_f32_16x16x64_bf16 v[236:239], v[248:251], v[176:183], %[ix]\n\t" "ds_read_b128 v[176:179], %[l0] offset:12288\n\t" \
    "ds_read_b128 v[180:183], %[l0] offset:13312\n\t" "s_waitcnt vmcnt(2)\n\t" "v_smfmac_f32_16x16x64_bf16 v[224:227], v[252:255], a[48:55], %[ix]\n\t" \
    "ds_write_b128 %[l1], v[252:255] offset:11264\n\t" "v_smfmac_f32_16x16x64_bf16 v[228:231], v[252:255], a[56:63], %[ix]\n\t" \
    "ds_read_b128 v[240:243], %[l1] offset:4096\n\t" "ds_read_b128 v[244:247], %[l1] offset:13312\n\t" \
    "v_smfmac_f32_16x16x64_bf16 v[232:235], v[252:255], v[56:63], %[ix]\n\t" "s_nop 1\n\t" "v_smfmac_f32_16x16x64_bf16 v[236:239], v[252:255], v[184:191], %[ix]\n\t" \
    "ds_read_b128 v[184:187], %[l0] offset:14336\n\t" "v_cmp_u_f32 vcc, v224, v225\n\t" "v_cmp_u_f32 s[62:63], v226, v227\n\t" "s_waitcnt lgkmcnt(7)\n\t" \
    "v_smfmac_f32_16x16x64_bf16 v[224:227], v[192:195], a[64:71], %[ix]\n\t" "ds_read_b128 v[188:191], %[l0] offset:15360\n\t" \
    "v_smfmac_f32_16x16x64_bf16 v[228:231], v[192:195], a[72:79], %[ix]\n\t" "s_or_b64 vcc, vcc, s[62:63]\n\t" "s_cbranch_scc1 31f\n\t" "32:\n\t" \
    "v_smfmac_f32_16x16x64_bf16 v[232:235], v[192:195], v[64:71], %[ix]\n\t" "s_waitcnt lgkmcnt(4)\n\t" "s_barrier\n\t" XT(88, 89) "ds_read_b32 v218, v219\n\t" \
    "v_smfmac_f32_16x16x64_bf16 v[236:239], v[192:195], v[160:167], %[ix]\n\t" "ds_read_b128 v[160:163], %[l0] offset:16384\n\t" \
    "ds_read_b128 v[164:167], %[l0] offset:17408\n\t" "v_smfmac_f32_16x16x64_bf16 v[224:227], v[196:199], a[80:87], %[ix]\n\t" \
    "ds_read_b128 v[248:251], %[l1] offset:1024\n\t" "ds_read_b128 v[252:255], %[l1] offset:5120\n\t" "v_smfmac_f32_16x16x64_bf16 v[228:231], v[196:199], a[88:95], %[ix]\n\t" \
    "ds_read_b128 v[192:195], %[l1] offset:14336\n\t" "v_smfmac_f32_16x16x64_bf16 v[232:235], v[196:199], v[72:79], %[ix]\n\t" \
    "v_smfmac_f32_16x16x64_bf16 v[236:239], v[196:199], v[168:175], %[ix]\n\t" "s_waitcnt lgkmcnt(5)\n\t" "v_cmp_eq_u32 vcc, %[rt], v218\n\t" \
    "v_smfmac_f32_16x16x64_bf16 v[224:227], v[240:243], a[96:103], %[ix]\n\t" "s_cbranch_vccnz 33f\n\t" "ds_read_b128 v[168:171], %[l0] offset:18432\n\t" \
    "v_smfmac_f32_16x16x64_bf16 v[228:231], v[240:243], a[104:111], %[ix]\n\t" "ds_read_b128 v[172:175], %[l0] offset:19456\n\t" \
    "ds_read_b128 v[196:199], %[l1] offset:2048\n\t" "v_smfmac_f32_16x16x64_bf16 v[232:235], v[240:243], v[80:87], %[ix]\n\t" \
    "buffer_load_dwordx4 v[16:19], %[hq], %[rs], %[qb] offen offset:0 sc1\n\t" "v_smfmac_f32_16x16x64_bf16 v[236:239], v[240:243], v[176:183], %[ix]\n\t" \
    "ds_read_b128 v[176:179], %[l0] offset:20480\n\t" "ds_read_b128 v[180:183], %[l0] offset:21504\n\t" \
    "v_smfmac_f32_16x16x64_bf16 v[224:227], v[244:247], a[112:119], %[ix]\n\t" "ds_read_b128 v[240:243], %[l1] offset:6144\n\t" \
    "v_smfmac_f32_16x16x64_bf16 v[228:231], v[244:247], a[120:127], %[ix]\n\t" "v_smfmac_f32_16x16x64_bf16 v[232:235], v[244:247], v[88:95], %[ix]\n\t" \
    "buffer_load_dwordx4 v[20:23], %[hq], %[rs], %[qb] offen offset:2048 sc1\n\t" "v_smfmac_f32_16x16x64_bf16 v[236:239], v[244:247], v[184:191], %[ix]\n\t" \
    "ds_read_b128 v[184:187], %[l0] offset:22528\n\t" "ds_read_b128 v[188:191], %[l0] offset:23552\n\t" "s_waitcnt lgkmcnt(10)\n\t" \
    "v_smfmac_f32_16x16x64_bf16 v[224:227], v[248:251], a[128:135], %[ix]\n\t" "ds_read_b128 v[244:247], %[l1] offset:15360\n\t" \
    "v_smfmac_f32_16x16x64_bf16 v[228:231], v[248:251], a[136:143], %[ix]\n\t" "v_smfmac_f32_16x16x64_bf16 v[232:235], v[248:251], v[96:103], %[ix]\n\t" \
    "buffer_load_dwordx4 v[24:27], %[hq], %[rs], s61 offen offset:0 sc1\n\t" "v_smfmac_f32_16x16x64_bf16 v[236:239], v[248:251], v[160:167], %[ix]\n\t" \
    "ds_read_b128 v[160:163], %[l0] offset:24576\n\t" "ds_read_b128 v[164:167], %[l0] offset:25600\n\t" "s_waitcnt lgkmcnt(12)\n\t" \
    "v_smfmac_f32_16x16x64_bf16 v[224:227], v[252:255], a[144:151], %[ix]\n\t" "ds_read_b128 v[248:251], %[l1] offset:3072\n\t" \
    "v_smfmac_f32_16x16x64_bf16 v[228:231], v[252:255], a[152:159], %[ix]\n\t" "v_smfmac_f32_16x16x64_bf16 v[232:235], v[252:255], v[104:111], %[ix]\n\t" \
    "buffer_load_dwordx4 v[28:31], %[hq], %[rs], s61 offen offset:2048 sc1\n\t" "s_waitcnt lgkmcnt(10)\n\t" \
    "v_smfmac_f32_16x16x64_bf16 v[236:239], v[252:255], v[168:175], %[ix]\n\t" "ds_read_b128 v[168:171], %[l0] offset:26624\n\t" \
    "ds_read_b128 v[172:175], %[l0] offset:27648\n\t" "v_smfmac_f32_16x16x64_bf16 v[224:227], v[192:195], a[160:167], %[ix]\n\t" \
    "ds_read_b128 v[252:255], %[l1] offset:7168\n\t" "v_smfmac_f32_16x16x64_bf16 v[228:231], v[192:195], a[168:175], %[ix]\n\t" "s_waitcnt lgkmcnt(12)\n\t" \
    "v_smfmac_f32_16x16x64_bf16 v[224:227], v[196:199], a[176:183], %[ix]\n\t" "v_smfmac_f32_16x16x64_bf16 v[228:231], v[196:199], a[184:191], %[ix]\n\t" \
    "s_waitcnt lgkmcnt(9)\n\t" "v_smfmac_f32_16x16x64_bf16 v[224:227], v[240:243], a[192:199], %[ix]\n\t" \
    "v_smfmac_f32_16x16x64_bf16 v[228:231], v[240:243], a[200:207], %[ix]\n\t" "s_waitcnt lgkmcnt(6)\n\t" \
    "v_smfmac_f32_16x16x64_bf16 v[224:227], v[244:247], a[208:215], %[ix]\n\t" "v_smfmac_f32_16x16x64_bf16 v[228:231], v[244:247], a[216:223], %[ix]\n\t" \
    "s_waitcnt vmcnt(4)\n\t" "s_waitcnt lgkmcnt(3)\n\t" "v_smfmac_f32_16x16x64_bf16 v[224:227], v[248:251], a[224:231], %[ix]\n\t" \
    "v_smfmac_f32_16x16x64_bf16 v[228:231], v[248:251], a[232:239], %[ix]\n\t" "s_waitcnt lgkmcnt(0)\n\t" \
    "v_smfmac_f32_16x16x64_bf16 v[224:227], v[252:255], a[240:247], %[ix]\n\t" "s_nop 1\n\t" "v_smfmac_f32_16x16x64_bf16 v[228:231], v[252:255], a[248:255], %[ix]\n\t" \
    XT(90, 91) "s_nop 1\n\t" "v_smfmac_f32_16x16x64_bf16 v[232:235], v[192:195], v[112:119], %[ix]\n\t" "s_nop 1\n\t" \
    "v_smfmac_f32_16x16x64_bf16 v[236:239], v[192:195], v[176:183], %[ix]\n\t" "ds_read_b128 v[176:179], %[l0] offset:28672\n\t" "v_add_f32 v224, v224, v225\n\t" \
    "v_add_f32 v226, v226, v227\n\t" "v_smfmac_f32_16x16x64_bf16 v[232:235], v[196:199], v[120:127], %[ix]\n\t" "ds_read_b128 v[180:183], %[l0] offset:29696\n\t" \
    "v_add_f32 v228, v228, v229\n\t" "v_add_f32 v230, v230, v231\n\t" "v_smfmac_f32_16x16x64_bf16 v[236:239], v[196:199], v[184:191], %[ix]\n\t" \
    "ds_read_b128 v[184:187], %[l0] offset:30720\n\t" "v_cvt_pk_bf16_f32 v218, v224, v228\n\t" "v_cvt_pk_bf16_f32 v219, v226, v230\n\t" \
    "v_smfmac_f32_16x16x64_bf16 v[232:235], v[240:243], v[128:135], %[ix]\n\t" "ds_write2_b32 %[qw], v218, v219 offset1:8\n\t" \
    "ds_read_b128 v[188:191], %[l0] offset:31744\n\t" "v_smfmac_f32_16x16x64_bf16 v[236:239], v[240:243], v[160:167], %[ix]\n\t" \
    "v_smfmac_f32_16x16x64_bf16 v[232:235], v[244:247], v[136:143], %[ix]\n\t" "v_smfmac_f32_16x16x64_bf16 v[236:239], v[244:247], v[168:175], %[ix]\n\t" \
    "s_waitcnt lgkmcnt(1)\n\t" "s_barrier\n\t" "v_smfmac_f32_16x16x64_bf16 v[232:235], v[248:251], v[144:151], %[ix]\n\t" \
    "v_smfmac_f32_16x16x64_bf16 v[236:239], v[248:251], v[176:183], %[ix]\n\t" "v_smfmac_f32_16x16x64_bf16 v[232:235], v[252:255], v[152:159], %[ix]\n\t" \
    "s_waitcnt lgkmcnt(0)\n\t" "v_smfmac_f32_16x16x64_bf16 v[236:239], v[252:255], v[184:191], %[ix]\n\t" XT(92, 93) "s_nop 6\n\t" "v_add_f32 v232, v232, v233\n\t" \
    "v_add_f32 v234, v234, v235\n\t" "v_add_f32 v236, v236, v237\n\t" "v_add_f32 v238, v238, v239\n\t" "v_cvt_pk_bf16_f32 v232, v232, v236\n\t" \
    "v_cvt_pk_bf16_f32 v233, v234, v238\n\t" "ds_write2_b32 %[kw], v232, v233 offset1:68\n\t" "s_waitcnt lgkmcnt(0)\n\t" "s_barrier\n\t" XT(94, 95) \
    "s_cmp_ge_u32 %[sq], %[sn]\n\t" "s_cbranch_scc1 377f\n\t" "375:\n\t" "global_load_dword v240, v210, %[fq] sc1\n\t" "s_waitcnt vmcnt(0)\n\t" \
    "v_readfirstlane_b32 %[sq], v240\n\t" "s_nop 3\n\t" "s_cmp_ge_u32 %[sq], %[sn]\n\t" "s_cbranch_scc1 376f\n\t" "s_cmp_eq_u32 %[bd], 0\n\t" "s_cbranch_scc1 376f\n\t" \
    "s_sub_u32 %[bd], %[bd], 1\n\t" "s_sleep 2\n\t" "s_branch 375b\n\t" "376:\n\t" "buffer_load_dwordx4 v[16:19], %[hq], %[rs], %[qb] offen offset:0 sc1\n\t" \
    "buffer_load_dwordx4 v[20:23], %[hq], %[rs], %[qb] offen offset:2048 sc1\n\t" "buffer_load_dwordx4 v[24:27], %[hq], %[rs], s61 offen offset:0 sc1\n\t" \
    "buffer_load_dwordx4 v[28:31], %[hq], %[rs], s61 offen offset:2048 sc1\n\t" "377:\n\t" "s_nop 3\n\t" "ds_read_b128 v[160:163], %[kx] offset:0\n\t" \
    "ds_read_b128 v[164:167], %[kx] offset:2048\n\t" "ds_read_b128 v[168:171], %[kx] offset:4096\n\t" "ds_read_b128 v[172:175], %[kx] offset:6144\n\t" \
    "ds_read_b128 v[176:179], %[kx] offset:256\n\t" "ds_read_b128 v[180:183], %[kx] offset:2304\n\t" "ds_read_b128 v[184:187], %[kx] offset:4352\n\t" \
    "ds_read_b128 v[188:191], %[kx] offset:6400\n\t" "ds_read_b128 v[224:227], %[km] offset:0\n\t" "ds_read_b128 v[228:231], %[km] offset:64\n\t" \
    "ds_read_b128 v[232:235], %[km] offset:128\n\t" "ds_read_b128 v[236:239], %[km] offset:192\n\t" "s_waitcnt vmcnt(0)\n\t" "global_load_dword v240, v210, %[fq] sc1\n\t" \
    "s_waitcnt lgkmcnt(11)\n\t" "v_mfma_f32_16x16x32_bf16 v[248:251], v[160:163], v[16:19], 0\n\t" "s_waitcnt lgkmcnt(10)\n\t" \
    "v_mfma_f32_16x16x32_bf16 v[248:251], v[164:167], v[20:23], v[248:251]\n\t" "s_waitcnt lgkmcnt(9)\n\t" \
    "v_mfma_f32_16x16x32_bf16 v[248:251], v[168:171], v[24:27], v[248:251]\n\t" "s_waitcnt lgkmcnt(8)\n\t" \
    "v_mfma_f32_16x16x32_bf16 v[248:251], v[172:175], v[28:31], v[248:251]\n\t" "s_waitcnt lgkmcnt(7)\n\t" \
    "v_mfma_f32_16x16x32_bf16 v[252:255], v[176:179], v[16:19], v[200:203]\n\t" "s_waitcnt lgkmcnt(6)\n\t" \
    "v_mfma_f32_16x16x32_bf16 v[252:255], v[180:183], v[20:23], v[252:255]\n\t" "s_waitcnt lgkmcnt(5)\n\t" \
    "v_mfma_f32_16x16x32_bf16 v[252:255], v[184:187], v[24:27], v[252:255]\n\t" "s_waitcnt lgkmcnt(4)\n\t" \
    "v_mfma_f32_16x16x32_bf16 v[252:255], v[188:191], v[28:31], v[252:255]\n\t" "s_waitcnt lgkmcnt(3)\n\t" \
    "v_mfma_f32_16x16x32_bf16 v[244:247], v[224:227], v[16:19], v[204:207]\n\t" "s_waitcnt lgkmcnt(2)\n\t" \
    "v_mfma_f32_16x16x32_bf16 v[244:247], v[228:231], v[20:23], v[244:247]\n\t" "s_waitcnt lgkmcnt(1)\n\t" \
    "v_mfma_f32_16x16x32_bf16 v[244:247], v[232:235], v[24:27], v[244:247]\n\t" "s_waitcnt lgkmcnt(0)\n\t" \
    "v_mfma_f32_16x16x32_bf16 v[244:247], v[236:239], v[28:31], v[244:247]\n\t" "s_nop 3\n\t" "v_max3_f32 v242, v248, v249, v250\n\t" "v_max3_f32 v242, v242, v251, v252\n\t" \
    "v_max3_f32 v242, v242, v253, v254\n\t" "v_max_f32 v242, v242, v255\n\t" "v_max3_f32 v242, v242, v244, v245\n\t" "v_max3_f32 v242, v242, v246, v247\n\t" \
    "v_mov_b32 v243, v242\n\t" "s_nop 1\n\t" "v_permlane32_swap_b32 v242, v243\n\t" "v_max_f32 v242, v242, v243\n\t" "v_mov_b32 v243, v242\n\t" "s_nop 1\n\t" \
    "v_permlane16_swap_b32 v242, v243\n\t" "v_max_f32 v242, v242, v243\n\t" "v_pk_add_f32 v[244:245], v[244:245], v[242:243] op_sel_hi:[1,0] neg_lo:[0,1] neg_hi:[0,1]\n\t" \
    "v_pk_add_f32 v[246:247], v[246:247], v[242:243] op_sel_hi:[1,0] neg_lo:[0,1] neg_hi:[0,1]\n\t" \
    "v_pk_add_f32 v[248:249], v[248:249], v[242:243] op_sel_hi:[1,0] neg_lo:[0,1] neg_hi:[0,1]\n\t" \
    "v_pk_add_f32 v[250:251], v[250:251], v[242:243] op_sel_hi:[1,0] neg_lo:[0,1] neg_hi:[0,1]\n\t" \
    "v_pk_add_f32 v[252:253], v[252:253], v[242:243] op_sel_hi:[1,0] neg_lo:[0,1] neg_hi:[0,1]\n\t" \
    "v_pk_add_f32 v[254:255], v[254:255], v[242:243] op_sel_hi:[1,0] neg_lo:[0,1] neg_hi:[0,1]\n\t" "v_pk_mul_f32 v[244:245], v[244:245], s[66:67]\n\t" \
    "v_pk_mul_f32 v[246:247], v[246:247], s[66:67]\n\t" "v_pk_mul_f32 v[248:249], v[248:249], s[66:67]\n\t" "v_pk_mul_f32 v[250:251], v[250:251], s[66:67]\n\t" \
    "v_pk_mul_f32 v[252:253], v[252:253], s[66:67]\n\t" "v_pk_mul_f32 v[254:255], v[254:255], s[66:67]\n\t" "v_exp_f32 v244, v244\n\t" "v_exp_f32 v245, v245\n\t" \
    "v_exp_f32 v246, v246\n\t" "v_exp_f32 v247, v247\n\t" "v_exp_f32 v248, v248\n\t" "v_exp_f32 v249, v249\n\t" "v_exp_f32 v250, v250\n\t" "v_exp_f32 v251, v251\n\t" \
    "v_exp_f32 v252, v252\n\t" "v_exp_f32 v253, v253\n\t" "v_exp_f32 v254, v254\n\t" "v_exp_f32 v255, v255\n\t" "v_pk_add_f32 v[242:243], v[244:245], v[246:247]\n\t" \
    "v_pk_add_f32 v[242:243], v[242:243], v[248:249]\n\t" "v_pk_add_f32 v[242:243], v[242:243], v[250:251]\n\t" "v_pk_add_f32 v[242:243], v[242:243], v[252:253]\n\t" \
    "v_pk_add_f32 v[242:243], v[242:243], v[254:255]\n\t" "v_add_f32 v242, v242, v243\n\t" "v_mov_b32 v243, v242\n\t" "s_nop 1\n\t" "v_permlane32_swap_b32 v242, v243\n\t" \
    "v_add_f32 v242, v242, v243\n\t" "v_mov_b32 v243, v242\n\t" "s_nop 1\n\t" "v_permlane16_swap_b32 v242, v243\n\t" "v_add_f32 v242, v242, v243\n\t" \
    "v_rcp_f32 v242, v242\n\t" "s_nop 0\n\t" "v_pk_mul_f32 v[244:245], v[244:245], v[242:243] op_sel_hi:[1,0]\n\t" \
    "v_pk_mul_f32 v[246:247], v[246:247], v[242:243] op_sel_hi:[1,0]\n\t" "v_pk_mul_f32 v[248:249], v[248:249], v[242:243] op_sel_hi:[1,0]\n\t" \
    "v_pk_mul_f32 v[250:251], v[250:251], v[242:243] op_sel_hi:[1,0]\n\t" "v_pk_mul_f32 v[252:253], v[252:253], v[242:243] op_sel_hi:[1,0]\n\t" \
    "v_pk_mul_f32 v[254:255], v[254:255], v[242:243] op_sel_hi:[1,0]\n\t" "v_cvt_pk_bf16_f32 v162, v244, v245\n\t" "v_cvt_pk_bf16_f32 v163, v246, v247\n\t" \
    "v_cvt_pk_bf16_f32 v164, v248, v249\n\t" "v_cvt_pk_bf16_f32 v165, v250, v251\n\t" "v_cvt_pk_bf16_f32 v166, v252, v253\n\t" "v_cvt_pk_bf16_f32 v167, v254, v255\n\t" \
    "buffer_store_dwordx4 v[164:167], %[px], %[rs], %[tb] offen sc1\n\t" "buffer_store_dwordx2 v[162:163], %[pm], %[rs], %[tb] offen sc1\n\t" "s_waitcnt vmcnt(2)\n\t" \
    "v_readfirstlane_b32 s62, v240\n\t" "s_nop 3\n\t" "s_max_u32 %[sq], %[sq], s62\n\t" "s_barrier\n\t" XT(96, 97) "s_branch 99f\n\t" "31:\n\t" \
    "ds_read_b128 v[220:223], %[l1] offset:8192\n\t" "s_waitcnt lgkmcnt(0)\n\t" "v_max3_u32 v218, v220, v221, v222\n\t" "v_max_u32 v218, v218, v223\n\t" \
    "ds_read_b128 v[220:223], %[l1] offset:9216\n\t" "s_waitcnt lgkmcnt(0)\n\t" "v_max3_u32 v218, v218, v220, v221\n\t" "v_max3_u32 v218, v218, v222, v223\n\t" \
    "ds_read_b128 v[220:223], %[l1] offset:10240\n\t" "s_waitcnt lgkmcnt(0)\n\t" "v_max3_u32 v218, v218, v220, v221\n\t" "v_max3_u32 v218, v218, v222, v223\n\t" \
    "ds_read_b128 v[220:223], %[l1] offset:11264\n\t" "s_waitcnt lgkmcnt(0)\n\t" "v_max3_u32 v218, v218, v220, v221\n\t" "v_max3_u32 v218, v218, v222, v223\n\t" \
    "v_cmp_eq_u32 vcc, -1, v218\n\t" "s_cbranch_vccz 32b\n\t" "s_cmp_eq_u32 %[rt], 1\n\t" "s_cbranch_scc1 32b\n\t" "v_mov_b32 v218, %[rt]\n\t" "ds_write_b32 v219, v218\n\t" \
    "s_waitcnt lgkmcnt(0)\n\t" "s_branch 32b\n\t" "33:\n\t" "s_sub_u32 %[rt], %[rt], 1\n\t" "s_waitcnt lgkmcnt(0)\n\t" XCNT(100) "34:\n\t" \
    "buffer_load_dwordx4 v[240:243], %[mo], %[rs], %[sb] offen sc1\n\t" "buffer_load_dwordx4 v[244:247], v216, %[rs], %[sb] offen sc1\n\t" \
    "buffer_load_dwordx4 v[248:251], %[mo], %[rs], s60 offen sc1\n\t" "buffer_load_dwordx4 v[252:255], v216, %[rs], s60 offen sc1\n\t" "s_waitcnt vmcnt(0)\n\t" \
    "v_max_u32 v218, v240, v241\n\t" "v_max3_u32 v218, v218, v242, v243\n\t" "v_max3_u32 v218, v218, v244, v245\n\t" "v_max3_u32 v218, v218, v246, v247\n\t" \
    "v_max3_u32 v218, v218, v248, v249\n\t" "v_max3_u32 v218, v218, v250, v251\n\t" "v_max3_u32 v218, v218, v252, v253\n\t" "v_max3_u32 v218, v218, v254, v255\n\t" \
    "v_cmp_eq_u32 vcc, -1, v218\n\t" "s_cbranch_vccz 35f\n\t" "s_cmp_eq_u32 %[bd], 0\n\t" "s_cbranch_scc1 35f\n\t" "s_sub_u32 %[bd], %[bd], 1\n\t" XCNT(101) \
    "s_branch 34b\n\t" "35:\n\t" "ds_read_b128 v[160:163], %[l0] offset:0\n\t" "ds_read_b128 v[164:167], %[l0] offset:1024\n\t" \
    "ds_read_b128 v[168:171], %[l0] offset:2048\n\t" "ds_read_b128 v[172:175], %[l0] offset:3072\n\t" "ds_read_b128 v[176:179], %[l0] offset:4096\n\t" \
    "ds_read_b128 v[180:183], %[l0] offset:5120\n\t" "ds_read_b128 v[184:187], %[l0] offset:6144\n\t" "ds_read_b128 v[188:191], %[l0] offset:7168\n\t" \
    "v_mov_b64 v[224:225], 0\n\t" "v_mov_b64 v[226:227], 0\n\t" "v_mov_b64 v[228:229], 0\n\t" "v_mov_b64 v[230:231], 0\n\t" "v_mov_b64 v[232:233], 0\n\t" \
    "v_mov_b64 v[234:235], 0\n\t" "v_mov_b64 v[236:237], 0\n\t" "v_mov_b64 v[238:239], 0\n\t" "s_barrier\n\t" "s_branch 30b\n\t" "99:\n\t"
#define QK_STEP3 \
    ".p2align 6\n\t" "v_xor_b32 v216, 0x400, %[mo]\n\t" "s_xor_b32 s60, %[sb], 0x800\n\t" "s_mov_b32 s67, %[pb]\n\t" XT(80, 81) "s_getpc_b64 s[62:63]\n\t" "48:\n\t" \
    "s_memtime s[64:65]\n\t" "s_waitcnt lgkmcnt(0)\n\t" "s_sub_u32 s61, %[tg], s64\n\t" "s_cmp_gt_i32 s61, 124\n\t" "s_cbranch_scc1 48b\n\t" "s_max_i32 s61, s61, 0\n\t" \
    "s_and_b32 s61, s61, 124\n\t" "s_sub_u32 s61, 49f-48b, s61\n\t" "s_add_u32 s62, s62, s61\n\t" "s_addc_u32 s63, s63, 0\n\t" "s_setpc_b64 s[62:63]\n\t" ".rept 31\n\t" \
    "s_nop 0\n\t" ".endr\n\t" "49:\n\t" XT(98, 99) "s_barrier\n\t" XT(82, 83) "buffer_load_dwordx4 v[240:243], %[mo], %[rs], %[sb] offen sc1\n\t" \
    "v_mov_b64 v[224:225], 0\n\t" "v_mov_b64 v[226:227], 0\n\t" "v_mov_b64 v[228:229], 0\n\t" "v_mov_b64 v[230:231], 0\n\t" "v_mov_b64 v[232:233], 0\n\t" \
    "v_mov_b64 v[234:235], 0\n\t" "v_mov_b64 v[236:237], 0\n\t" "v_mov_b64 v[238:239], 0\n\t" "buffer_load_dwordx4 v[244:247], v216, %[rs], %[sb] offen sc1\n\t" \
    "ds_read_b128 v[160:163], %[l0] offset:0\n\t" "ds_read_b128 v[164:167], %[l0] offset:1024\n\t" "ds_read_b128 v[168:171], %[l0] offset:2048\n\t" \
    "ds_read_b128 v[172:175], %[l0] offset:3072\n\t" "s_nop 3\n\t" "buffer_load_dwordx4 v[248:251], %[mo], %[rs], s60 offen sc1\n\t" \
    "ds_read_b128 v[176:179], %[l0] offset:4096\n\t" "ds_read_b128 v[180:183], %[l0] offset:5120\n\t" "ds_read_b128 v[184:187], %[l0] offset:6144\n\t" \
    "ds_read_b128 v[188:191], %[l0] offset:7168\n\t" "s_nop 3\n\t" "buffer_load_dwordx4 v[252:255], v216, %[rs], s60 offen sc1\n\t" "v_mov_b32 v219, 160512\n\t" \
    "v_mov_b32 v220, -1\n\t" "buffer_store_dword v220, %[hq], %[rs], %[pr] offen sc1\n\t" "40:\n\t" "s_waitcnt vmcnt(4)\n\t" XT(84, 85) \
    "v_smfmac_f32_16x16x64_bf16 v[224:227], v[240:243], a[0:7], %[ix]\n\t" "ds_write_b128 %[l1], v[240:243] offset:12288\n\t" \
    "v_smfmac_f32_16x16x64_bf16 v[228:231], v[240:243], a[8:15], %[ix]\n\t" "v_smfmac_f32_16x16x64_bf16 v[232:235], v[240:243], v[32:39], %[ix]\n\t" \
    "s_waitcnt lgkmcnt(7)\n\t" "v_smfmac_f32_16x16x64_bf16 v[236:239], v[240:243], v[160:167], %[ix]\n\t" "ds_read_b128 v[160:163], %[l0] offset:8192\n\t" \
    "ds_read_b128 v[164:167], %[l0] offset:9216\n\t" "s_waitcnt vmcnt(3)\n\t" "v_smfmac_f32_16x16x64_bf16 v[224:227], v[244:247], a[16:23], %[ix]\n\t" \
    "ds_write_b128 %[l1], v[244:247] offset:13312\n\t" "v_smfmac_f32_16x16x64_bf16 v[228:231], v[244:247], a[24:31], %[ix]\n\t" \
    "v_smfmac_f32_16x16x64_bf16 v[232:235], v[244:247], v[40:47], %[ix]\n\t" "s_waitcnt lgkmcnt(8)\n\t" \
    "v_smfmac_f32_16x16x64_bf16 v[236:239], v[244:247], v[168:175], %[ix]\n\t" "ds_read_b128 v[168:171], %[l0] offset:10240\n\t" \
    "ds_read_b128 v[172:175], %[l0] offset:11264\n\t" "s_waitcnt vmcnt(2)\n\t" "v_smfmac_f32_16x16x64_bf16 v[224:227], v[248:251], a[32:39], %[ix]\n\t" \
    "ds_write_b128 %[l1], v[248:251] offset:14336\n\t" "v_smfmac_f32_16x16x64_bf16 v[228:231], v[248:251], a[40:47], %[ix]\n\t" "s_waitcnt lgkmcnt(3)\n\t" "s_barrier\n\t" \
    XT(86, 87) "v_smfmac_f32_16x16x64_bf16 v[232:235], v[248:251], v[48:55], %[ix]\n\t" "ds_read_b128 v[192:195], %[l1] offset:0\n\t" \
    "ds_read_b128 v[196:199], %[l1] offset:4096\n\t" "v_smfmac_f32_16x16x64_bf16 v[236:239], v[248:251], v[176:183], %[ix]\n\t" \
    "ds_read_b128 v[176:179], %[l0] offset:12288\n\t" "ds_read_b128 v[180:183], %[l0] offset:13312\n\t" "s_waitcnt vmcnt(1)\n\t" \
    "v_smfmac_f32_16x16x64_bf16 v[224:227], v[252:255], a[48:55], %[ix]\n\t" "ds_write_b128 %[l1], v[252:255] offset:15360\n\t" \
    "v_smfmac_f32_16x16x64_bf16 v[228:231], v[252:255], a[56:63], %[ix]\n\t" "ds_read_b128 v[240:243], %[l1] offset:8192\n\t" "ds_read_b128 v[244:247], %[l1] offset:1024\n\t" \
    "v_smfmac_f32_16x16x64_bf16 v[232:235], v[252:255], v[56:63], %[ix]\n\t" "s_nop 1\n\t" "v_smfmac_f32_16x16x64_bf16 v[236:239], v[252:255], v[184:191], %[ix]\n\t" \
    "ds_read_b128 v[184:187], %[l0] offset:14336\n\t" "v_cmp_u_f32 vcc, v224, v225\n\t" "v_cmp_u_f32 s[62:63], v226, v227\n\t" "s_waitcnt lgkmcnt(7)\n\t" \
    "v_smfmac_f32_16x16x64_bf16 v[224:227], v[192:195], a[64:71], %[ix]\n\t" "ds_read_b128 v[188:191], %[l0] offset:15360\n\t" \
    "v_smfmac_f32_16x16x64_bf16 v[228:231], v[192:195], a[72:79], %[ix]\n\t" "s_or_b64 vcc, vcc, s[62:63]\n\t" "s_cbranch_scc1 41f\n\t" "42:\n\t" \
    "v_smfmac_f32_16x16x64_bf16 v[232:235], v[192:195], v[64:71], %[ix]\n\t" "s_waitcnt lgkmcnt(4)\n\t" "s_barrier\n\t" XT(88, 89) "ds_read_b32 v218, v219\n\t" \
    "v_smfmac_f32_16x16x64_bf16 v[236:239], v[192:195], v[160:167], %[ix]\n\t" "s_memtime s[64:65]\n\t" "ds_read_b128 v[160:163], %[l0] offset:16384\n\t" \
    "v_smfmac_f32_16x16x64_bf16 v[224:227], v[196:199], a[80:87], %[ix]\n\t" "ds_read_b128 v[164:167], %[l0] offset:17408\n\t" \
    "ds_read_b128 v[248:251], %[l1] offset:5120\n\t" "v_smfmac_f32_16x16x64_bf16 v[228:231], v[196:199], a[88:95], %[ix]\n\t" "ds_read_b128 v[252:255], %[l1] offset:9216\n\t" \
    "ds_read_b128 v[192:195], %[l1] offset:2048\n\t" "v_smfmac_f32_16x16x64_bf16 v[232:235], v[196:199], v[72:79], %[ix]\n\t" "s_sub_u32 s62, s64, %[ca]\n\t" \
    "s_mul_hi_u32 s63, s62, %[k1]\n\t" "s_mul_i32 s62, s62, %[k1]\n\t" "v_smfmac_f32_16x16x64_bf16 v[236:239], v[196:199], v[168:175], %[ix]\n\t" "s_waitcnt lgkmcnt(5)\n\t" \
    "v_cmp_eq_u32 vcc, %[rt], v218\n\t" "s_lshr_b64 s[62:63], s[62:63], 16\n\t" "s_add_u32 s62, s62, %[ga]\n\t" "s_bitset0_b32 s62, 0\n\t" \
    "v_smfmac_f32_16x16x64_bf16 v[224:227], v[240:243], a[96:103], %[ix]\n\t" "s_cbranch_vccnz 43f\n\t" "ds_read_b128 v[168:171], %[l0] offset:18432\n\t" \
    "v_smfmac_f32_16x16x64_bf16 v[228:231], v[240:243], a[104:111], %[ix]\n\t" "ds_read_b128 v[172:175], %[l0] offset:19456\n\t" "v_mov_b32 v217, s62\n\t" \
    "buffer_store_dword v217, %[hq], %[rs], s67 offen sc1\n\t" "v_smfmac_f32_16x16x64_bf16 v[232:235], v[240:243], v[80:87], %[ix]\n\t" \
    "ds_read_b128 v[196:199], %[l1] offset:6144\n\t" "v_smfmac_f32_16x16x64_bf16 v[236:239], v[240:243], v[176:183], %[ix]\n\t" \
    "ds_read_b128 v[176:179], %[l0] offset:20480\n\t" "ds_read_b128 v[180:183], %[l0] offset:21504\n\t" \
    "v_smfmac_f32_16x16x64_bf16 v[224:227], v[244:247], a[112:119], %[ix]\n\t" "ds_read_b128 v[240:243], %[l1] offset:10240\n\t" \
    "v_smfmac_f32_16x16x64_bf16 v[228:231], v[244:247], a[120:127], %[ix]\n\t" "v_smfmac_f32_16x16x64_bf16 v[232:235], v[244:247], v[88:95], %[ix]\n\t" \
    "v_smfmac_f32_16x16x64_bf16 v[236:239], v[244:247], v[184:191], %[ix]\n\t" "ds_read_b128 v[184:187], %[l0] offset:22528\n\t" \
    "ds_read_b128 v[188:191], %[l0] offset:23552\n\t" "s_waitcnt lgkmcnt(10)\n\t" "v_smfmac_f32_16x16x64_bf16 v[224:227], v[248:251], a[128:135], %[ix]\n\t" \
    "ds_read_b128 v[244:247], %[l1] offset:3072\n\t" "v_smfmac_f32_16x16x64_bf16 v[228:231], v[248:251], a[136:143], %[ix]\n\t" \
    "v_smfmac_f32_16x16x64_bf16 v[232:235], v[248:251], v[96:103], %[ix]\n\t" "v_smfmac_f32_16x16x64_bf16 v[236:239], v[248:251], v[160:167], %[ix]\n\t" \
    "ds_read_b128 v[160:163], %[l0] offset:24576\n\t" "ds_read_b128 v[164:167], %[l0] offset:25600\n\t" "s_waitcnt lgkmcnt(12)\n\t" \
    "v_smfmac_f32_16x16x64_bf16 v[224:227], v[252:255], a[144:151], %[ix]\n\t" "ds_read_b128 v[248:251], %[l1] offset:7168\n\t" \
    "v_smfmac_f32_16x16x64_bf16 v[228:231], v[252:255], a[152:159], %[ix]\n\t" "v_smfmac_f32_16x16x64_bf16 v[232:235], v[252:255], v[104:111], %[ix]\n\t" \
    "s_waitcnt lgkmcnt(10)\n\t" "v_smfmac_f32_16x16x64_bf16 v[236:239], v[252:255], v[168:175], %[ix]\n\t" "ds_read_b128 v[168:171], %[l0] offset:26624\n\t" \
    "ds_read_b128 v[172:175], %[l0] offset:27648\n\t" "v_smfmac_f32_16x16x64_bf16 v[224:227], v[192:195], a[160:167], %[ix]\n\t" \
    "ds_read_b128 v[252:255], %[l1] offset:11264\n\t" "v_smfmac_f32_16x16x64_bf16 v[228:231], v[192:195], a[168:175], %[ix]\n\t" "s_waitcnt lgkmcnt(12)\n\t" \
    "v_smfmac_f32_16x16x64_bf16 v[224:227], v[196:199], a[176:183], %[ix]\n\t" "v_smfmac_f32_16x16x64_bf16 v[228:231], v[196:199], a[184:191], %[ix]\n\t" \
    "s_waitcnt lgkmcnt(9)\n\t" "v_smfmac_f32_16x16x64_bf16 v[224:227], v[240:243], a[192:199], %[ix]\n\t" \
    "v_smfmac_f32_16x16x64_bf16 v[228:231], v[240:243], a[200:207], %[ix]\n\t" "s_waitcnt lgkmcnt(6)\n\t" \
    "v_smfmac_f32_16x16x64_bf16 v[224:227], v[244:247], a[208:215], %[ix]\n\t" "v_smfmac_f32_16x16x64_bf16 v[228:231], v[244:247], a[216:223], %[ix]\n\t" \
    "s_waitcnt lgkmcnt(3)\n\t" "v_smfmac_f32_16x16x64_bf16 v[224:227], v[248:251], a[224:231], %[ix]\n\t" \
    "v_smfmac_f32_16x16x64_bf16 v[228:231], v[248:251], a[232:239], %[ix]\n\t" "s_waitcnt lgkmcnt(0)\n\t" \
    "v_smfmac_f32_16x16x64_bf16 v[224:227], v[252:255], a[240:247], %[ix]\n\t" "s_nop 1\n\t" "v_smfmac_f32_16x16x64_bf16 v[228:231], v[252:255], a[248:255], %[ix]\n\t" \
    XT(90, 91) "s_nop 1\n\t" "v_smfmac_f32_16x16x64_bf16 v[232:235], v[192:195], v[112:119], %[ix]\n\t" "s_nop 1\n\t" \
    "v_smfmac_f32_16x16x64_bf16 v[236:239], v[192:195], v[176:183], %[ix]\n\t" "ds_read_b128 v[176:179], %[l0] offset:28672\n\t" "v_add_f32 v224, v224, v225\n\t" \
    "v_add_f32 v226, v226, v227\n\t" "v_smfmac_f32_16x16x64_bf16 v[232:235], v[196:199], v[120:127], %[ix]\n\t" "ds_read_b128 v[180:183], %[l0] offset:29696\n\t" \
    "v_add_f32 v228, v228, v229\n\t" "v_add_f32 v230, v230, v231\n\t" "v_smfmac_f32_16x16x64_bf16 v[236:239], v[196:199], v[184:191], %[ix]\n\t" \
    "ds_read_b128 v[184:187], %[l0] offset:30720\n\t" "v_cvt_pk_bf16_f32 v218, v224, v228\n\t" "v_cvt_pk_bf16_f32 v219, v226, v230\n\t" \
    "v_smfmac_f32_16x16x64_bf16 v[232:235], v[240:243], v[128:135], %[ix]\n\t" "ds_write2_b32 %[qw], v218, v219 offset1:8\n\t" \
    "ds_read_b128 v[188:191], %[l0] offset:31744\n\t" "v_smfmac_f32_16x16x64_bf16 v[236:239], v[240:243], v[160:167], %[ix]\n\t" \
    "v_smfmac_f32_16x16x64_bf16 v[232:235], v[244:247], v[136:143], %[ix]\n\t" "v_smfmac_f32_16x16x64_bf16 v[236:239], v[244:247], v[168:175], %[ix]\n\t" \
    "s_waitcnt lgkmcnt(1)\n\t" "s_barrier\n\t" "v_smfmac_f32_16x16x64_bf16 v[232:235], v[248:251], v[144:151], %[ix]\n\t" \
    "v_smfmac_f32_16x16x64_bf16 v[236:239], v[248:251], v[176:183], %[ix]\n\t" "v_smfmac_f32_16x16x64_bf16 v[232:235], v[252:255], v[152:159], %[ix]\n\t" \
    "s_waitcnt lgkmcnt(0)\n\t" "v_smfmac_f32_16x16x64_bf16 v[236:239], v[252:255], v[184:191], %[ix]\n\t" XT(92, 93) "s_nop 6\n\t" "v_add_f32 v232, v232, v233\n\t" \
    "v_add_f32 v234, v234, v235\n\t" "v_add_f32 v236, v236, v237\n\t" "v_add_f32 v238, v238, v239\n\t" "v_cvt_pk_bf16_f32 v232, v232, v236\n\t" \
    "v_cvt_pk_bf16_f32 v233, v234, v238\n\t" "ds_write2_b32 %[kw], v232, v233 offset1:68\n\t" "s_waitcnt lgkmcnt(0)\n\t" "s_barrier\n\t" XT(94, 95) XT(96, 97) \
    "s_branch 99f\n\t" "41:\n\t" "ds_read_b128 v[220:223], %[l1] offset:12288\n\t" "s_waitcnt lgkmcnt(0)\n\t" "v_max3_u32 v218, v220, v221, v222\n\t" \
    "v_max_u32 v218, v218, v223\n\t" "ds_read_b128 v[220:223], %[l1] offset:13312\n\t" "s_waitcnt lgkmcnt(0)\n\t" "v_max3_u32 v218, v218, v220, v221\n\t" \
    "v_max3_u32 v218, v218, v222, v223\n\t" "ds_read_b128 v[220:223], %[l1] offset:14336\n\t" "s_waitcnt lgkmcnt(0)\n\t" "v_max3_u32 v218, v218, v220, v221\n\t" \
    "v_max3_u32 v218, v218, v222, v223\n\t" "ds_read_b128 v[220:223], %[l1] offset:15360\n\t" "s_waitcnt lgkmcnt(0)\n\t" "v_max3_u32 v218, v218, v220, v221\n\t" \
    "v_max3_u32 v218, v218, v222, v223\n\t" "v_cmp_eq_u32 vcc, -1, v218\n\t" "s_cbranch_vccz 42b\n\t" "s_cmp_eq_u32 %[rt], 1\n\t" "s_cbranch_scc1 42b\n\t" \
    "v_mov_b32 v218, %[rt]\n\t" "ds_write_b32 v219, v218\n\t" "s_waitcnt lgkmcnt(0)\n\t" "s_branch 42b\n\t" "43:\n\t" "s_sub_u32 %[rt], %[rt], 1\n\t" \
    "s_waitcnt lgkmcnt(0)\n\t" XCNT(100) "44:\n\t" "buffer_load_dwordx4 v[240:243], %[mo], %[rs], %[sb] offen sc1\n\t" \
    "buffer_load_dwordx4 v[244:247], v216, %[rs], %[sb] offen sc1\n\t" "buffer_load_dwordx4 v[248:251], %[mo], %[rs], s60 offen sc1\n\t" \
    "buffer_load_dwordx4 v[252:255], v216, %[rs], s60 offen sc1\n\t" "s_waitcnt vmcnt(0)\n\t" "v_max_u32 v218, v240, v241\n\t" "v_max3_u32 v218, v218, v242, v243\n\t" \
    "v_max3_u32 v218, v218, v244, v245\n\t" "v_max3_u32 v218, v218, v246, v247\n\t" "v_max3_u32 v218, v218, v248, v249\n\t" "v_max3_u32 v218, v218, v250, v251\n\t" \
    "v_max3_u32 v218, v218, v252, v253\n\t" "v_max3_u32 v218, v218, v254, v255\n\t" "v_cmp_eq_u32 vcc, -1, v218\n\t" "s_cbranch_vccz 45f\n\t" "s_cmp_eq_u32 %[bd], 0\n\t" \
    "s_cbranch_scc1 45f\n\t" "s_sub_u32 %[bd], %[bd], 1\n\t" XCNT(101) "s_branch 44b\n\t" "45:\n\t" "ds_read_b128 v[160:163], %[l0] offset:0\n\t" \
    "ds_read_b128 v[164:167], %[l0] offset:1024\n\t" "ds_read_b128 v[168:171], %[l0] offset:2048\n\t" "ds_read_b128 v[172:175], %[l0] offset:3072\n\t" \
    "ds_read_b128 v[176:179], %[l0] offset:4096\n\t" "ds_read_b128 v[180:183], %[l0] offset:5120\n\t" "ds_read_b128 v[184:187], %[l0] offset:6144\n\t" \
    "ds_read_b128 v[188:191], %[l0] offset:7168\n\t" "v_mov_b64 v[224:225], 0\n\t" "v_mov_b64 v[226:227], 0\n\t" "v_mov_b64 v[228:229], 0\n\t" "v_mov_b64 v[230:231], 0\n\t" \
    "v_mov_b64 v[232:233], 0\n\t" "v_mov_b64 v[234:235], 0\n\t" "v_mov_b64 v[236:237], 0\n\t" "v_mov_b64 v[238:239], 0\n\t" "s_barrier\n\t" "s_memtime s[64:65]\n\t" \
    "s_waitcnt lgkmcnt(0)\n\t" "s_add_u32 s64, s64, %[dr]\n\t" "s_sub_u32 s62, s64, %[ca]\n\t" "s_mul_hi_u32 s63, s62, %[k1]\n\t" "s_mul_i32 s62, s62, %[k1]\n\t" \
    "s_lshr_b64 s[62:63], s[62:63], 16\n\t" "s_add_u32 s62, s62, %[ga]\n\t" "s_bitset0_b32 s62, 0\n\t" "v_mov_b32 v218, s62\n\t" \
    "buffer_store_dword v218, %[hq], %[rs], s67 offen sc1\n\t" "s_mov_b32 s67, 0x7f000000\n\t" "s_branch 40b\n\t" "99:\n\t" "mk_tail:\n\t"
#define VO_STEP \
    ".p2align 6\n\t" "v_xor_b32 v224, 0x400, %[mo]\n\t" "s_xor_b32 s60, %[sb], 0x800\n\t" "s_mov_b32 s67, %[pb]\n\t" "s_cmp_eq_u32 %[w], 1\n\t" "s_cbranch_scc1 91f\n\t" \
    "s_cmp_eq_u32 %[w], 2\n\t" "s_cbranch_scc1 92f\n\t" "s_cmp_eq_u32 %[w], 3\n\t" "s_cbranch_scc1 93f\n\t" XT(80, 81) "s_getpc_b64 s[62:63]\n\t" "18:\n\t" \
    "s_memtime s[64:65]\n\t" "s_waitcnt lgkmcnt(0)\n\t" "s_sub_u32 s61, %[tg], s64\n\t" "s_cmp_gt_i32 s61, 124\n\t" "s_cbranch_scc1 18b\n\t" "s_max_i32 s61, s61, 0\n\t" \
    "s_and_b32 s61, s61, 124\n\t" "s_sub_u32 s61, 19f-18b, s61\n\t" "s_add_u32 s62, s62, s61\n\t" "s_addc_u32 s63, s63, 0\n\t" "s_setpc_b64 s[62:63]\n\t" ".rept 31\n\t" \
    "s_nop 0\n\t" ".endr\n\t" "19:\n\t" "s_barrier\n\t" XT(82, 83) "buffer_load_dwordx4 v[240:243], %[mo], %[rs], %[sb] offen sc1\n\t" "v_mov_b64 v[232:233], 0\n\t" \
    "v_mov_b64 v[234:235], 0\n\t" "v_mov_b64 v[236:237], 0\n\t" "v_mov_b64 v[238:239], 0\n\t" "v_mov_b64 v[192:193], 0\n\t" "v_mov_b64 v[194:195], 0\n\t" \
    "v_mov_b64 v[196:197], 0\n\t" "v_mov_b64 v[198:199], 0\n\t" "buffer_load_dwordx4 v[244:247], v224, %[rs], %[sb] offen sc1\n\t" \
    "ds_read_b128 v[160:163], %[l0] offset:0\n\t" "ds_read_b128 v[164:167], %[l0] offset:1024\n\t" "ds_read_b128 v[168:171], %[l0] offset:2048\n\t" \
    "ds_read_b128 v[172:175], %[l0] offset:3072\n\t" "s_nop 3\n\t" "buffer_load_dwordx4 v[248:251], %[mo], %[rs], s60 offen sc1\n\t" \
    "ds_read_b128 v[176:179], %[l0] offset:4096\n\t" "ds_read_b128 v[180:183], %[l0] offset:5120\n\t" "ds_read_b128 v[184:187], %[l0] offset:6144\n\t" \
    "ds_read_b128 v[188:191], %[l0] offset:7168\n\t" "s_nop 3\n\t" "buffer_load_dwordx4 v[252:255], v224, %[rs], s60 offen sc1\n\t" "v_mov_b64 v[200:201], 0\n\t" \
    "v_mov_b64 v[202:203], 0\n\t" "v_mov_b64 v[204:205], 0\n\t" "v_mov_b64 v[206:207], 0\n\t" "v_mov_b64 v[208:209], 0\n\t" "v_mov_b64 v[210:211], 0\n\t" \
    "v_mov_b64 v[212:213], 0\n\t" "v_mov_b64 v[214:215], 0\n\t" "v_mov_b64 v[216:217], 0\n\t" "v_mov_b64 v[218:219], 0\n\t" "v_mov_b64 v[220:221], 0\n\t" \
    "v_mov_b64 v[222:223], 0\n\t" "v_mov_b32 v226, -1\n\t" "buffer_store_dword v226, %[hq], %[rs], %[pr] offen sc1\n\t" "v_mov_b32 v227, 160512\n\t" "10:\n\t" \
    "s_waitcnt vmcnt(4)\n\t" XT(84, 85) "v_smfmac_f32_16x16x64_bf16 v[232:235], v[240:243], a[0:7], %[ix]\n\t" "ds_write_b128 %[l1], v[240:243] offset:0\n\t" \
    "v_smfmac_f32_16x16x64_bf16 v[236:239], v[240:243], a[8:15], %[ix]\n\t" "s_waitcnt vmcnt(3)\n\t" "v_smfmac_f32_16x16x64_bf16 v[232:235], v[244:247], a[16:23], %[ix]\n\t" \
    "ds_write_b128 %[l1], v[244:247] offset:1024\n\t" "s_waitcnt lgkmcnt(8)\n\t" "v_smfmac_f32_16x16x64_bf16 v[236:239], v[244:247], v[160:167], %[ix]\n\t" \
    "s_waitcnt lgkmcnt(0)\n\t" "s_barrier\n\t" XT(86, 87) "ds_read_b128 v[16:19], %[l1] offset:4096\n\t" "ds_read_b128 v[20:23], %[l1] offset:8192\n\t" \
    "ds_read_b128 v[24:27], %[l1] offset:12288\n\t" "s_waitcnt vmcnt(2)\n\t" "v_smfmac_f32_16x16x64_bf16 v[232:235], v[248:251], a[24:31], %[ix]\n\t" \
    "ds_write_b128 %[l1], v[248:251] offset:2048\n\t" "v_smfmac_f32_16x16x64_bf16 v[236:239], v[248:251], a[32:39], %[ix]\n\t" "ds_read_b128 v[28:31], %[l1] offset:5120\n\t" \
    "ds_read_b128 v[240:243], %[l1] offset:9216\n\t" "ds_read_b128 v[244:247], %[l1] offset:13312\n\t" "s_waitcnt vmcnt(1)\n\t" \
    "v_smfmac_f32_16x16x64_bf16 v[232:235], v[252:255], a[40:47], %[ix]\n\t" "ds_write_b128 %[l1], v[252:255] offset:3072\n\t" \
    "v_smfmac_f32_16x16x64_bf16 v[236:239], v[252:255], v[168:175], %[ix]\n\t" "ds_read_b128 v[160:163], %[l0] offset:8192\n\t" \
    "ds_read_b128 v[164:167], %[l0] offset:9216\n\t" "ds_read_b128 v[168:171], %[l0] offset:10240\n\t" "ds_read_b128 v[172:175], %[l0] offset:11264\n\t" "s_nop 1\n\t" \
    "v_cmp_u_f32 vcc, v232, v233\n\t" "v_cmp_u_f32 s[62:63], v234, v235\n\t" "s_waitcnt lgkmcnt(11)\n\t" \
    "v_smfmac_f32_16x16x64_bf16 v[232:235], v[16:19], a[48:55], %[ix]\n\t" "v_smfmac_f32_16x16x64_bf16 v[236:239], v[16:19], a[56:63], %[ix]\n\t" \
    "s_or_b64 vcc, vcc, s[62:63]\n\t" "s_cbranch_scc1 11f\n\t" "12:\n\t" "s_waitcnt lgkmcnt(10)\n\t" "v_smfmac_f32_16x16x64_bf16 v[232:235], v[20:23], a[64:71], %[ix]\n\t" \
    "v_smfmac_f32_16x16x64_bf16 v[236:239], v[20:23], v[176:183], %[ix]\n\t" "s_waitcnt lgkmcnt(4)\n\t" "s_barrier\n\t" XT(88, 89) "ds_read_b32 v225, v227\n\t" \
    "v_smfmac_f32_16x16x64_bf16 v[232:235], v[24:27], a[72:79], %[ix]\n\t" "ds_read_b128 v[176:179], %[l0] offset:12288\n\t" "ds_read_b128 v[180:183], %[l0] offset:13312\n\t" \
    "v_smfmac_f32_16x16x64_bf16 v[236:239], v[24:27], a[80:87], %[ix]\n\t" "ds_read_b128 v[248:251], %[l1] offset:6144\n\t" "ds_read_b128 v[252:255], %[l1] offset:10240\n\t" \
    "v_smfmac_f32_16x16x64_bf16 v[232:235], v[28:31], a[88:95], %[ix]\n\t" "ds_read_b128 v[16:19], %[l1] offset:14336\n\t" "ds_read_b128 v[20:23], %[l1] offset:7168\n\t" \
    "v_smfmac_f32_16x16x64_bf16 v[236:239], v[28:31], v[184:191], %[ix]\n\t" "s_memtime s[64:65]\n\t" "ds_read_b128 v[24:27], %[l1] offset:11264\n\t" \
    "v_smfmac_f32_16x16x64_bf16 v[232:235], v[240:243], a[96:103], %[ix]\n\t" "ds_read_b128 v[184:187], %[l0] offset:14336\n\t" \
    "ds_read_b128 v[188:191], %[l0] offset:15360\n\t" "v_smfmac_f32_16x16x64_bf16 v[236:239], v[240:243], a[104:111], %[ix]\n\t" "s_waitcnt lgkmcnt(9)\n\t" \
    "v_cmp_eq_u32 vcc, %[rt], v225\n\t" "v_smfmac_f32_16x16x64_bf16 v[232:235], v[244:247], a[112:119], %[ix]\n\t" "s_cbranch_vccnz 13f\n\t" \
    "ds_read_b128 v[28:31], %[l1] offset:15360\n\t" "v_smfmac_f32_16x16x64_bf16 v[236:239], v[244:247], v[160:167], %[ix]\n\t" \
    "ds_read_b128 v[160:163], %[l0] offset:16384\n\t" "ds_read_b128 v[164:167], %[l0] offset:17408\n\t" "s_waitcnt lgkmcnt(9)\n\t" \
    "v_smfmac_f32_16x16x64_bf16 v[232:235], v[248:251], a[120:127], %[ix]\n\t" "s_sub_u32 s62, s64, %[ca]\n\t" "s_mul_hi_u32 s63, s62, %[k1]\n\t" \
    "v_smfmac_f32_16x16x64_bf16 v[236:239], v[248:251], a[128:135], %[ix]\n\t" "s_mul_i32 s62, s62, %[k1]\n\t" "s_lshr_b64 s[62:63], s[62:63], 16\n\t" \
    "s_waitcnt lgkmcnt(8)\n\t" "v_smfmac_f32_16x16x64_bf16 v[232:235], v[252:255], a[136:143], %[ix]\n\t" "s_add_u32 s62, s62, %[ga]\n\t" "s_bitset0_b32 s62, 0\n\t" \
    "v_smfmac_f32_16x16x64_bf16 v[236:239], v[252:255], v[168:175], %[ix]\n\t" "v_mov_b32 v226, s62\n\t" "buffer_store_dword v226, %[hq], %[rs], s67 offen sc1\n\t" \
    "s_waitcnt lgkmcnt(7)\n\t" "v_smfmac_f32_16x16x64_bf16 v[232:235], v[16:19], a[144:151], %[ix]\n\t" "ds_read_b128 v[168:171], %[l0] offset:18432\n\t" \
    "ds_read_b128 v[172:175], %[l0] offset:19456\n\t" "v_smfmac_f32_16x16x64_bf16 v[236:239], v[16:19], a[152:159], %[ix]\n\t" "s_waitcnt lgkmcnt(8)\n\t" \
    "v_smfmac_f32_16x16x64_bf16 v[232:235], v[20:23], a[160:167], %[ix]\n\t" "v_smfmac_f32_16x16x64_bf16 v[236:239], v[20:23], v[176:183], %[ix]\n\t" \
    "ds_read_b128 v[176:179], %[l0] offset:20480\n\t" "ds_read_b128 v[180:183], %[l0] offset:21504\n\t" "s_waitcnt lgkmcnt(9)\n\t" \
    "v_smfmac_f32_16x16x64_bf16 v[232:235], v[24:27], a[168:175], %[ix]\n\t" "v_smfmac_f32_16x16x64_bf16 v[236:239], v[24:27], a[176:183], %[ix]\n\t" \
    "s_waitcnt lgkmcnt(6)\n\t" "v_smfmac_f32_16x16x64_bf16 v[232:235], v[28:31], a[184:191], %[ix]\n\t" \
    "v_smfmac_f32_16x16x64_bf16 v[236:239], v[28:31], v[184:191], %[ix]\n\t" "ds_read_b128 v[184:187], %[l0] offset:22528\n\t" \
    "ds_read_b128 v[188:191], %[l0] offset:23552\n\t" XT(90, 91) "s_nop 4\n\t" "v_add_f32 v232, v232, v233\n\t" "v_add_f32 v234, v234, v235\n\t" \
    "v_add_f32 v236, v236, v237\n\t" "v_add_f32 v238, v238, v239\n\t" "v_cvt_pk_bf16_f32 v236, v232, v236\n\t" "v_cvt_pk_bf16_f32 v237, v234, v238\n\t" \
    "ds_write2_b32 %[vw], v236, v237 offset1:68\n\t" "s_waitcnt lgkmcnt(0)\n\t" "s_barrier\n\t" XT(92, 93) "ds_read_b128 v[240:243], %[vr] offset:0\n\t" \
    "ds_read_b128 v[244:247], %[vr] offset:64\n\t" "ds_read_b128 v[248:251], %[vr] offset:128\n\t" "ds_read_b128 v[252:255], %[vr] offset:192\n\t" "s_mov_b32 s61, %[ub]\n\t" \
    "s_waitcnt lgkmcnt(3)\n\t" "v_smfmac_f32_16x16x64_bf16 v[192:195], v[240:243], v[32:39], %[ix]\n\t" \
    "v_smfmac_f32_16x16x64_bf16 v[196:199], v[240:243], v[48:55], %[ix]\n\t" "s_waitcnt lgkmcnt(2)\n\t" \
    "v_smfmac_f32_16x16x64_bf16 v[192:195], v[244:247], v[40:47], %[ix]\n\t" "v_smfmac_f32_16x16x64_bf16 v[196:199], v[244:247], v[56:63], %[ix]\n\t" \
    "s_waitcnt lgkmcnt(1)\n\t" "v_smfmac_f32_16x16x64_bf16 v[192:195], v[248:251], a[192:199], %[ix]\n\t" \
    "v_smfmac_f32_16x16x64_bf16 v[196:199], v[248:251], a[200:207], %[ix]\n\t" "s_waitcnt lgkmcnt(0)\n\t" \
    "v_smfmac_f32_16x16x64_bf16 v[192:195], v[252:255], v[160:167], %[ix]\n\t" "ds_read_b128 v[160:163], %[l0] offset:24576\n\t" \
    "ds_read_b128 v[164:167], %[l0] offset:25600\n\t" "v_smfmac_f32_16x16x64_bf16 v[196:199], v[252:255], v[168:175], %[ix]\n\t" \
    "ds_read_b128 v[168:171], %[l0] offset:26624\n\t" "ds_read_b128 v[172:175], %[l0] offset:27648\n\t" \
    "v_smfmac_f32_16x16x64_bf16 v[200:203], v[240:243], v[64:71], %[ix]\n\t" "s_nop 1\n\t" "v_smfmac_f32_16x16x64_bf16 v[204:207], v[240:243], v[80:87], %[ix]\n\t" \
    "v_cvt_pk_bf16_f32 v224, v192, v194\n\t" "v_cvt_pk_bf16_f32 v225, v193, v195\n\t" "v_smfmac_f32_16x16x64_bf16 v[200:203], v[244:247], v[72:79], %[ix]\n\t" \
    "v_cvt_pk_bf16_f32 v226, v196, v198\n\t" "v_cvt_pk_bf16_f32 v227, v197, v199\n\t" "v_smfmac_f32_16x16x64_bf16 v[204:207], v[244:247], v[88:95], %[ix]\n\t" "s_nop 0\n\t" \
    "v_permlane32_swap_b32 v224, v226\n\t" "v_smfmac_f32_16x16x64_bf16 v[200:203], v[248:251], a[208:215], %[ix]\n\t" "v_permlane32_swap_b32 v225, v227\n\t" "s_nop 0\n\t" \
    "v_smfmac_f32_16x16x64_bf16 v[204:207], v[248:251], a[216:223], %[ix]\n\t" "v_permlane16_swap_b32 v224, v225\n\t" "v_permlane16_swap_b32 v226, v227\n\t" \
    "v_smfmac_f32_16x16x64_bf16 v[200:203], v[252:255], v[176:183], %[ix]\n\t" "ds_read_b128 v[176:179], %[l0] offset:28672\n\t" \
    "buffer_store_dwordx4 v[224:227], %[uo], %[rs], s61 offen sc1\n\t" "s_xor_b32 s61, s61, 0x40000\n\t" XT(94, 95) \
    "v_smfmac_f32_16x16x64_bf16 v[204:207], v[252:255], v[184:191], %[ix]\n\t" "ds_read_b128 v[180:183], %[l0] offset:29696\n\t" \
    "ds_read_b128 v[184:187], %[l0] offset:30720\n\t" "v_smfmac_f32_16x16x64_bf16 v[208:211], v[240:243], v[96:103], %[ix]\n\t" \
    "ds_read_b128 v[188:191], %[l0] offset:31744\n\t" "s_nop 0\n\t" "v_smfmac_f32_16x16x64_bf16 v[212:215], v[240:243], v[112:119], %[ix]\n\t" \
    "v_cvt_pk_bf16_f32 v228, v200, v202\n\t" "v_cvt_pk_bf16_f32 v229, v201, v203\n\t" "v_smfmac_f32_16x16x64_bf16 v[208:211], v[244:247], v[104:111], %[ix]\n\t" \
    "v_cvt_pk_bf16_f32 v230, v204, v206\n\t" "v_cvt_pk_bf16_f32 v231, v205, v207\n\t" "v_smfmac_f32_16x16x64_bf16 v[212:215], v[244:247], v[120:127], %[ix]\n\t" "s_nop 0\n\t" \
    "v_permlane32_swap_b32 v228, v230\n\t" "v_smfmac_f32_16x16x64_bf16 v[208:211], v[248:251], a[224:231], %[ix]\n\t" "v_permlane32_swap_b32 v229, v231\n\t" "s_nop 0\n\t" \
    "v_smfmac_f32_16x16x64_bf16 v[212:215], v[248:251], a[232:239], %[ix]\n\t" "v_permlane16_swap_b32 v228, v229\n\t" "v_permlane16_swap_b32 v230, v231\n\t" \
    "s_waitcnt lgkmcnt(6)\n\t" "v_smfmac_f32_16x16x64_bf16 v[208:211], v[252:255], v[160:167], %[ix]\n\t" "buffer_store_dwordx4 v[228:231], %[uo], %[rs], s61 offen sc1\n\t" \
    "s_xor_b32 s61, s61, 0xc0000\n\t" "s_waitcnt lgkmcnt(4)\n\t" "v_smfmac_f32_16x16x64_bf16 v[212:215], v[252:255], v[168:175], %[ix]\n\t" "s_nop 1\n\t" \
    "v_smfmac_f32_16x16x64_bf16 v[216:219], v[240:243], v[128:135], %[ix]\n\t" "s_nop 1\n\t" "v_smfmac_f32_16x16x64_bf16 v[220:223], v[240:243], v[144:151], %[ix]\n\t" \
    "v_cvt_pk_bf16_f32 v224, v208, v210\n\t" "v_cvt_pk_bf16_f32 v225, v209, v211\n\t" "v_smfmac_f32_16x16x64_bf16 v[216:219], v[244:247], v[136:143], %[ix]\n\t" \
    "v_cvt_pk_bf16_f32 v226, v212, v214\n\t" "v_cvt_pk_bf16_f32 v227, v213, v215\n\t" "v_smfmac_f32_16x16x64_bf16 v[220:223], v[244:247], v[152:159], %[ix]\n\t" "s_nop 0\n\t" \
    "v_permlane32_swap_b32 v224, v226\n\t" "v_smfmac_f32_16x16x64_bf16 v[216:219], v[248:251], a[240:247], %[ix]\n\t" "v_permlane32_swap_b32 v225, v227\n\t" "s_nop 0\n\t" \
    "v_smfmac_f32_16x16x64_bf16 v[220:223], v[248:251], a[248:255], %[ix]\n\t" "v_permlane16_swap_b32 v224, v225\n\t" "v_permlane16_swap_b32 v226, v227\n\t" \
    "s_waitcnt lgkmcnt(2)\n\t" "v_smfmac_f32_16x16x64_bf16 v[216:219], v[252:255], v[176:183], %[ix]\n\t" "buffer_store_dwordx4 v[224:227], %[uo], %[rs], s61 offen sc1\n\t" \
    "s_xor_b32 s61, s61, 0x40000\n\t" "s_waitcnt lgkmcnt(0)\n\t" "v_smfmac_f32_16x16x64_bf16 v[220:223], v[252:255], v[184:191], %[ix]\n\t" "s_nop 5\n\t" \
    "v_cvt_pk_bf16_f32 v228, v216, v218\n\t" "v_cvt_pk_bf16_f32 v229, v217, v219\n\t" "v_cvt_pk_bf16_f32 v230, v220, v222\n\t" "v_cvt_pk_bf16_f32 v231, v221, v223\n\t" \
    "s_nop 0\n\t" "v_permlane32_swap_b32 v228, v230\n\t" "v_permlane32_swap_b32 v229, v231\n\t" "s_nop 1\n\t" "v_permlane16_swap_b32 v228, v229\n\t" \
    "v_permlane16_swap_b32 v230, v231\n\t" "buffer_store_dwordx4 v[228:231], %[uo], %[rs], s61 offen sc1\n\t" "s_nop 1\n\t" XT(96, 97) "s_branch 99f\n\t" "11:\n\t" \
    "ds_read_b128 v[228:231], %[l1] offset:0\n\t" "s_waitcnt lgkmcnt(0)\n\t" "v_max3_u32 v225, v228, v229, v230\n\t" "v_max_u32 v225, v225, v231\n\t" \
    "ds_read_b128 v[228:231], %[l1] offset:1024\n\t" "s_waitcnt lgkmcnt(0)\n\t" "v_max3_u32 v225, v225, v228, v229\n\t" "v_max3_u32 v225, v225, v230, v231\n\t" \
    "ds_read_b128 v[228:231], %[l1] offset:2048\n\t" "s_waitcnt lgkmcnt(0)\n\t" "v_max3_u32 v225, v225, v228, v229\n\t" "v_max3_u32 v225, v225, v230, v231\n\t" \
    "ds_read_b128 v[228:231], %[l1] offset:3072\n\t" "s_waitcnt lgkmcnt(0)\n\t" "v_max3_u32 v225, v225, v228, v229\n\t" "v_max3_u32 v225, v225, v230, v231\n\t" \
    "v_cmp_eq_u32 vcc, -1, v225\n\t" "s_cbranch_vccz 12b\n\t" "s_cmp_eq_u32 %[rt], 1\n\t" "s_cbranch_scc1 12b\n\t" "v_mov_b32 v225, %[rt]\n\t" "ds_write_b32 v227, v225\n\t" \
    "s_waitcnt lgkmcnt(0)\n\t" "s_branch 12b\n\t" "13:\n\t" "s_sub_u32 %[rt], %[rt], 1\n\t" "s_waitcnt lgkmcnt(0)\n\t" XCNT(100) "14:\n\t" \
    "buffer_load_dwordx4 v[240:243], %[mo], %[rs], %[sb] offen sc1\n\t" "buffer_load_dwordx4 v[244:247], v224, %[rs], %[sb] offen sc1\n\t" \
    "buffer_load_dwordx4 v[248:251], %[mo], %[rs], s60 offen sc1\n\t" "buffer_load_dwordx4 v[252:255], v224, %[rs], s60 offen sc1\n\t" "s_waitcnt vmcnt(0)\n\t" \
    "v_max_u32 v225, v240, v241\n\t" "v_max3_u32 v225, v225, v242, v243\n\t" "v_max3_u32 v225, v225, v244, v245\n\t" "v_max3_u32 v225, v225, v246, v247\n\t" \
    "v_max3_u32 v225, v225, v248, v249\n\t" "v_max3_u32 v225, v225, v250, v251\n\t" "v_max3_u32 v225, v225, v252, v253\n\t" "v_max3_u32 v225, v225, v254, v255\n\t" \
    "v_cmp_eq_u32 vcc, -1, v225\n\t" "s_cbranch_vccz 15f\n\t" "s_cmp_eq_u32 %[bd], 0\n\t" "s_cbranch_scc1 15f\n\t" "s_sub_u32 %[bd], %[bd], 1\n\t" XCNT(101) \
    "s_branch 14b\n\t" "15:\n\t" "ds_read_b128 v[160:163], %[l0] offset:0\n\t" "ds_read_b128 v[164:167], %[l0] offset:1024\n\t" \
    "ds_read_b128 v[168:171], %[l0] offset:2048\n\t" "ds_read_b128 v[172:175], %[l0] offset:3072\n\t" "ds_read_b128 v[176:179], %[l0] offset:4096\n\t" \
    "ds_read_b128 v[180:183], %[l0] offset:5120\n\t" "ds_read_b128 v[184:187], %[l0] offset:6144\n\t" "ds_read_b128 v[188:191], %[l0] offset:7168\n\t" \
    "v_mov_b64 v[232:233], 0\n\t" "v_mov_b64 v[234:235], 0\n\t" "v_mov_b64 v[236:237], 0\n\t" "v_mov_b64 v[238:239], 0\n\t" "s_barrier\n\t" "s_memtime s[64:65]\n\t" \
    "s_waitcnt lgkmcnt(0)\n\t" "s_add_u32 s64, s64, %[dr]\n\t" "s_sub_u32 s62, s64, %[ca]\n\t" "s_mul_hi_u32 s63, s62, %[k1]\n\t" "s_mul_i32 s62, s62, %[k1]\n\t" \
    "s_lshr_b64 s[62:63], s[62:63], 16\n\t" "s_add_u32 s62, s62, %[ga]\n\t" "s_bitset0_b32 s62, 0\n\t" "v_mov_b32 v225, s62\n\t" \
    "buffer_store_dword v225, %[hq], %[rs], s67 offen sc1\n\t" "s_mov_b32 s67, 0x7f000000\n\t" "s_branch 10b\n\t" "91:\n\t" XT(80, 81) "s_barrier\n\t" XT(82, 83) \
    "buffer_load_dwordx4 v[240:243], %[mo], %[rs], %[sb] offen sc1\n\t" "v_mov_b64 v[232:233], 0\n\t" "v_mov_b64 v[234:235], 0\n\t" "v_mov_b64 v[236:237], 0\n\t" \
    "v_mov_b64 v[238:239], 0\n\t" "v_mov_b64 v[192:193], 0\n\t" "v_mov_b64 v[194:195], 0\n\t" "v_mov_b64 v[196:197], 0\n\t" "v_mov_b64 v[198:199], 0\n\t" \
    "buffer_load_dwordx4 v[244:247], v224, %[rs], %[sb] offen sc1\n\t" "ds_read_b128 v[160:163], %[l0] offset:0\n\t" "ds_read_b128 v[164:167], %[l0] offset:1024\n\t" \
    "ds_read_b128 v[168:171], %[l0] offset:2048\n\t" "ds_read_b128 v[172:175], %[l0] offset:3072\n\t" "s_nop 3\n\t" \
    "buffer_load_dwordx4 v[248:251], %[mo], %[rs], s60 offen sc1\n\t" "ds_read_b128 v[176:179], %[l0] offset:4096\n\t" "ds_read_b128 v[180:183], %[l0] offset:5120\n\t" \
    "ds_read_b128 v[184:187], %[l0] offset:6144\n\t" "ds_read_b128 v[188:191], %[l0] offset:7168\n\t" "s_nop 3\n\t" \
    "buffer_load_dwordx4 v[252:255], v224, %[rs], s60 offen sc1\n\t" "v_mov_b64 v[200:201], 0\n\t" "v_mov_b64 v[202:203], 0\n\t" "v_mov_b64 v[204:205], 0\n\t" \
    "v_mov_b64 v[206:207], 0\n\t" "v_mov_b64 v[208:209], 0\n\t" "v_mov_b64 v[210:211], 0\n\t" "v_mov_b64 v[212:213], 0\n\t" "v_mov_b64 v[214:215], 0\n\t" \
    "v_mov_b64 v[216:217], 0\n\t" "v_mov_b64 v[218:219], 0\n\t" "v_mov_b64 v[220:221], 0\n\t" "v_mov_b64 v[222:223], 0\n\t" "v_mov_b32 v227, 160512\n\t" "20:\n\t" \
    "s_waitcnt vmcnt(3)\n\t" XT(84, 85) "v_smfmac_f32_16x16x64_bf16 v[232:235], v[240:243], a[0:7], %[ix]\n\t" "ds_write_b128 %[l1], v[240:243] offset:4096\n\t" \
    "v_smfmac_f32_16x16x64_bf16 v[236:239], v[240:243], a[8:15], %[ix]\n\t" "s_waitcnt vmcnt(2)\n\t" "v_smfmac_f32_16x16x64_bf16 v[232:235], v[244:247], a[16:23], %[ix]\n\t" \
    "ds_write_b128 %[l1], v[244:247] offset:5120\n\t" "s_waitcnt lgkmcnt(8)\n\t" "v_smfmac_f32_16x16x64_bf16 v[236:239], v[244:247], v[160:167], %[ix]\n\t" \
    "s_waitcnt lgkmcnt(0)\n\t" "s_barrier\n\t" XT(86, 87) "ds_read_b128 v[16:19], %[l1] offset:8192\n\t" "ds_read_b128 v[20:23], %[l1] offset:12288\n\t" \
    "ds_read_b128 v[24:27], %[l1] offset:0\n\t" "s_waitcnt vmcnt(1)\n\t" "v_smfmac_f32_16x16x64_bf16 v[232:235], v[248:251], a[24:31], %[ix]\n\t" \
    "ds_write_b128 %[l1], v[248:251] offset:6144\n\t" "v_smfmac_f32_16x16x64_bf16 v[236:239], v[248:251], a[32:39], %[ix]\n\t" "ds_read_b128 v[28:31], %[l1] offset:9216\n\t" \
    "ds_read_b128 v[240:243], %[l1] offset:13312\n\t" "ds_read_b128 v[244:247], %[l1] offset:1024\n\t" "s_waitcnt vmcnt(0)\n\t" \
    "v_smfmac_f32_16x16x64_bf16 v[232:235], v[252:255], a[40:47], %[ix]\n\t" "ds_write_b128 %[l1], v[252:255] offset:7168\n\t" \
    "v_smfmac_f32_16x16x64_bf16 v[236:239], v[252:255], v[168:175], %[ix]\n\t" "ds_read_b128 v[160:163], %[l0] offset:8192\n\t" \
    "ds_read_b128 v[164:167], %[l0] offset:9216\n\t" "ds_read_b128 v[168:171], %[l0] offset:10240\n\t" "ds_read_b128 v[172:175], %[l0] offset:11264\n\t" "s_nop 1\n\t" \
    "v_cmp_u_f32 vcc, v232, v233\n\t" "v_cmp_u_f32 s[62:63], v234, v235\n\t" "s_waitcnt lgkmcnt(11)\n\t" \
    "v_smfmac_f32_16x16x64_bf16 v[232:235], v[16:19], a[48:55], %[ix]\n\t" "v_smfmac_f32_16x16x64_bf16 v[236:239], v[16:19], a[56:63], %[ix]\n\t" \
    "s_or_b64 vcc, vcc, s[62:63]\n\t" "s_cbranch_scc1 21f\n\t" "22:\n\t" "s_waitcnt lgkmcnt(10)\n\t" "v_smfmac_f32_16x16x64_bf16 v[232:235], v[20:23], a[64:71], %[ix]\n\t" \
    "v_smfmac_f32_16x16x64_bf16 v[236:239], v[20:23], v[176:183], %[ix]\n\t" "s_waitcnt lgkmcnt(4)\n\t" "s_barrier\n\t" XT(88, 89) "ds_read_b32 v225, v227\n\t" \
    "v_smfmac_f32_16x16x64_bf16 v[232:235], v[24:27], a[72:79], %[ix]\n\t" "ds_read_b128 v[176:179], %[l0] offset:12288\n\t" "ds_read_b128 v[180:183], %[l0] offset:13312\n\t" \
    "v_smfmac_f32_16x16x64_bf16 v[236:239], v[24:27], a[80:87], %[ix]\n\t" "ds_read_b128 v[248:251], %[l1] offset:10240\n\t" "ds_read_b128 v[252:255], %[l1] offset:14336\n\t" \
    "v_smfmac_f32_16x16x64_bf16 v[232:235], v[28:31], a[88:95], %[ix]\n\t" "ds_read_b128 v[16:19], %[l1] offset:2048\n\t" "ds_read_b128 v[20:23], %[l1] offset:11264\n\t" \
    "v_smfmac_f32_16x16x64_bf16 v[236:239], v[28:31], v[184:191], %[ix]\n\t" "ds_read_b128 v[24:27], %[l1] offset:15360\n\t" "ds_read_b128 v[184:187], %[l0] offset:14336\n\t" \
    "v_smfmac_f32_16x16x64_bf16 v[232:235], v[240:243], a[96:103], %[ix]\n\t" "ds_read_b128 v[188:191], %[l0] offset:15360\n\t" "ds_read_b128 v[28:31], %[l1] offset:3072\n\t" \
    "v_smfmac_f32_16x16x64_bf16 v[236:239], v[240:243], a[104:111], %[ix]\n\t" "s_waitcnt lgkmcnt(10)\n\t" "v_cmp_eq_u32 vcc, %[rt], v225\n\t" \
    "v_smfmac_f32_16x16x64_bf16 v[232:235], v[244:247], a[112:119], %[ix]\n\t" "s_cbranch_vccnz 23f\n\t" \
    "v_smfmac_f32_16x16x64_bf16 v[236:239], v[244:247], v[160:167], %[ix]\n\t" "ds_read_b128 v[160:163], %[l0] offset:16384\n\t" \
    "ds_read_b128 v[164:167], %[l0] offset:17408\n\t" "s_waitcnt lgkmcnt(9)\n\t" "v_smfmac_f32_16x16x64_bf16 v[232:235], v[248:251], a[120:127], %[ix]\n\t" \
    "v_smfmac_f32_16x16x64_bf16 v[236:239], v[248:251], a[128:135], %[ix]\n\t" "s_waitcnt lgkmcnt(8)\n\t" \
    "v_smfmac_f32_16x16x64_bf16 v[232:235], v[252:255], a[136:143], %[ix]\n\t" "v_smfmac_f32_16x16x64_bf16 v[236:239], v[252:255], v[168:175], %[ix]\n\t" \
    "ds_read_b128 v[168:171], %[l0] offset:18432\n\t" "ds_read_b128 v[172:175], %[l0] offset:19456\n\t" "s_waitcnt lgkmcnt(9)\n\t" \
    "v_smfmac_f32_16x16x64_bf16 v[232:235], v[16:19], a[144:151], %[ix]\n\t" "v_smfmac_f32_16x16x64_bf16 v[236:239], v[16:19], a[152:159], %[ix]\n\t" \
    "s_waitcnt lgkmcnt(8)\n\t" "v_smfmac_f32_16x16x64_bf16 v[232:235], v[20:23], a[160:167], %[ix]\n\t" \
    "v_smfmac_f32_16x16x64_bf16 v[236:239], v[20:23], v[176:183], %[ix]\n\t" "ds_read_b128 v[176:179], %[l0] offset:20480\n\t" \
    "ds_read_b128 v[180:183], %[l0] offset:21504\n\t" "s_waitcnt lgkmcnt(9)\n\t" "v_smfmac_f32_16x16x64_bf16 v[232:235], v[24:27], a[168:175], %[ix]\n\t" \
    "v_smfmac_f32_16x16x64_bf16 v[236:239], v[24:27], a[176:183], %[ix]\n\t" "s_waitcnt lgkmcnt(6)\n\t" \
    "v_smfmac_f32_16x16x64_bf16 v[232:235], v[28:31], a[184:191], %[ix]\n\t" "v_smfmac_f32_16x16x64_bf16 v[236:239], v[28:31], v[184:191], %[ix]\n\t" \
    "ds_read_b128 v[184:187], %[l0] offset:22528\n\t" "ds_read_b128 v[188:191], %[l0] offset:23552\n\t" XT(90, 91) "s_nop 4\n\t" "v_add_f32 v232, v232, v233\n\t" \
    "v_add_f32 v234, v234, v235\n\t" "v_add_f32 v236, v236, v237\n\t" "v_add_f32 v238, v238, v239\n\t" "v_cvt_pk_bf16_f32 v236, v232, v236\n\t" \
    "v_cvt_pk_bf16_f32 v237, v234, v238\n\t" "ds_write2_b32 %[vw], v236, v237 offset1:68\n\t" "s_waitcnt lgkmcnt(0)\n\t" "s_barrier\n\t" XT(92, 93) \
    "ds_read_b128 v[240:243], %[vr] offset:0\n\t" "ds_read_b128 v[244:247], %[vr] offset:64\n\t" "ds_read_b128 v[248:251], %[vr] offset:128\n\t" \
    "ds_read_b128 v[252:255], %[vr] offset:192\n\t" "s_mov_b32 s61, %[ub]\n\t" "s_waitcnt lgkmcnt(3)\n\t" \
    "v_smfmac_f32_16x16x64_bf16 v[192:195], v[240:243], v[32:39], %[ix]\n\t" "v_smfmac_f32_16x16x64_bf16 v[196:199], v[240:243], v[48:55], %[ix]\n\t" \
    "s_waitcnt lgkmcnt(2)\n\t" "v_smfmac_f32_16x16x64_bf16 v[192:195], v[244:247], v[40:47], %[ix]\n\t" \
    "v_smfmac_f32_16x16x64_bf16 v[196:199], v[244:247], v[56:63], %[ix]\n\t" "s_waitcnt lgkmcnt(1)\n\t" \
    "v_smfmac_f32_16x16x64_bf16 v[192:195], v[248:251], a[192:199], %[ix]\n\t" "v_smfmac_f32_16x16x64_bf16 v[196:199], v[248:251], a[200:207], %[ix]\n\t" \
    "s_waitcnt lgkmcnt(0)\n\t" "v_smfmac_f32_16x16x64_bf16 v[192:195], v[252:255], v[160:167], %[ix]\n\t" "ds_read_b128 v[160:163], %[l0] offset:24576\n\t" \
    "ds_read_b128 v[164:167], %[l0] offset:25600\n\t" "v_smfmac_f32_16x16x64_bf16 v[196:199], v[252:255], v[168:175], %[ix]\n\t" \
    "ds_read_b128 v[168:171], %[l0] offset:26624\n\t" "ds_read_b128 v[172:175], %[l0] offset:27648\n\t" \
    "v_smfmac_f32_16x16x64_bf16 v[200:203], v[240:243], v[64:71], %[ix]\n\t" "s_nop 1\n\t" "v_smfmac_f32_16x16x64_bf16 v[204:207], v[240:243], v[80:87], %[ix]\n\t" \
    "v_cvt_pk_bf16_f32 v224, v192, v194\n\t" "v_cvt_pk_bf16_f32 v225, v193, v195\n\t" "v_smfmac_f32_16x16x64_bf16 v[200:203], v[244:247], v[72:79], %[ix]\n\t" \
    "v_cvt_pk_bf16_f32 v226, v196, v198\n\t" "v_cvt_pk_bf16_f32 v227, v197, v199\n\t" "v_smfmac_f32_16x16x64_bf16 v[204:207], v[244:247], v[88:95], %[ix]\n\t" "s_nop 0\n\t" \
    "v_permlane32_swap_b32 v224, v226\n\t" "v_smfmac_f32_16x16x64_bf16 v[200:203], v[248:251], a[208:215], %[ix]\n\t" "v_permlane32_swap_b32 v225, v227\n\t" "s_nop 0\n\t" \
    "v_smfmac_f32_16x16x64_bf16 v[204:207], v[248:251], a[216:223], %[ix]\n\t" "v_permlane16_swap_b32 v224, v225\n\t" "v_permlane16_swap_b32 v226, v227\n\t" \
    "v_smfmac_f32_16x16x64_bf16 v[200:203], v[252:255], v[176:183], %[ix]\n\t" "ds_read_b128 v[176:179], %[l0] offset:28672\n\t" \
    "buffer_store_dwordx4 v[224:227], %[uo], %[rs], s61 offen sc1\n\t" "s_xor_b32 s61, s61, 0x40000\n\t" XT(94, 95) \
    "v_smfmac_f32_16x16x64_bf16 v[204:207], v[252:255], v[184:191], %[ix]\n\t" "ds_read_b128 v[180:183], %[l0] offset:29696\n\t" \
    "ds_read_b128 v[184:187], %[l0] offset:30720\n\t" "v_smfmac_f32_16x16x64_bf16 v[208:211], v[240:243], v[96:103], %[ix]\n\t" \
    "ds_read_b128 v[188:191], %[l0] offset:31744\n\t" "s_nop 0\n\t" "v_smfmac_f32_16x16x64_bf16 v[212:215], v[240:243], v[112:119], %[ix]\n\t" \
    "v_cvt_pk_bf16_f32 v228, v200, v202\n\t" "v_cvt_pk_bf16_f32 v229, v201, v203\n\t" "v_smfmac_f32_16x16x64_bf16 v[208:211], v[244:247], v[104:111], %[ix]\n\t" \
    "v_cvt_pk_bf16_f32 v230, v204, v206\n\t" "v_cvt_pk_bf16_f32 v231, v205, v207\n\t" "v_smfmac_f32_16x16x64_bf16 v[212:215], v[244:247], v[120:127], %[ix]\n\t" "s_nop 0\n\t" \
    "v_permlane32_swap_b32 v228, v230\n\t" "v_smfmac_f32_16x16x64_bf16 v[208:211], v[248:251], a[224:231], %[ix]\n\t" "v_permlane32_swap_b32 v229, v231\n\t" "s_nop 0\n\t" \
    "v_smfmac_f32_16x16x64_bf16 v[212:215], v[248:251], a[232:239], %[ix]\n\t" "v_permlane16_swap_b32 v228, v229\n\t" "v_permlane16_swap_b32 v230, v231\n\t" \
    "s_waitcnt lgkmcnt(6)\n\t" "v_smfmac_f32_16x16x64_bf16 v[208:211], v[252:255], v[160:167], %[ix]\n\t" "buffer_store_dwordx4 v[228:231], %[uo], %[rs], s61 offen sc1\n\t" \
    "s_xor_b32 s61, s61, 0xc0000\n\t" "s_waitcnt lgkmcnt(4)\n\t" "v_smfmac_f32_16x16x64_bf16 v[212:215], v[252:255], v[168:175], %[ix]\n\t" "s_nop 1\n\t" \
    "v_smfmac_f32_16x16x64_bf16 v[216:219], v[240:243], v[128:135], %[ix]\n\t" "s_nop 1\n\t" "v_smfmac_f32_16x16x64_bf16 v[220:223], v[240:243], v[144:151], %[ix]\n\t" \
    "v_cvt_pk_bf16_f32 v224, v208, v210\n\t" "v_cvt_pk_bf16_f32 v225, v209, v211\n\t" "v_smfmac_f32_16x16x64_bf16 v[216:219], v[244:247], v[136:143], %[ix]\n\t" \
    "v_cvt_pk_bf16_f32 v226, v212, v214\n\t" "v_cvt_pk_bf16_f32 v227, v213, v215\n\t" "v_smfmac_f32_16x16x64_bf16 v[220:223], v[244:247], v[152:159], %[ix]\n\t" "s_nop 0\n\t" \
    "v_permlane32_swap_b32 v224, v226\n\t" "v_smfmac_f32_16x16x64_bf16 v[216:219], v[248:251], a[240:247], %[ix]\n\t" "v_permlane32_swap_b32 v225, v227\n\t" "s_nop 0\n\t" \
    "v_smfmac_f32_16x16x64_bf16 v[220:223], v[248:251], a[248:255], %[ix]\n\t" "v_permlane16_swap_b32 v224, v225\n\t" "v_permlane16_swap_b32 v226, v227\n\t" \
    "s_waitcnt lgkmcnt(2)\n\t" "v_smfmac_f32_16x16x64_bf16 v[216:219], v[252:255], v[176:183], %[ix]\n\t" "buffer_store_dwordx4 v[224:227], %[uo], %[rs], s61 offen sc1\n\t" \
    "s_xor_b32 s61, s61, 0x40000\n\t" "s_waitcnt lgkmcnt(0)\n\t" "v_smfmac_f32_16x16x64_bf16 v[220:223], v[252:255], v[184:191], %[ix]\n\t" "s_nop 5\n\t" \
    "v_cvt_pk_bf16_f32 v228, v216, v218\n\t" "v_cvt_pk_bf16_f32 v229, v217, v219\n\t" "v_cvt_pk_bf16_f32 v230, v220, v222\n\t" "v_cvt_pk_bf16_f32 v231, v221, v223\n\t" \
    "s_nop 0\n\t" "v_permlane32_swap_b32 v228, v230\n\t" "v_permlane32_swap_b32 v229, v231\n\t" "s_nop 1\n\t" "v_permlane16_swap_b32 v228, v229\n\t" \
    "v_permlane16_swap_b32 v230, v231\n\t" "buffer_store_dwordx4 v[228:231], %[uo], %[rs], s61 offen sc1\n\t" "s_nop 1\n\t" XT(96, 97) "v_mov_b64 v[16:17], -1\n\t" \
    "v_mov_b64 v[18:19], -1\n\t" "s_barrier\n\t" "buffer_store_dwordx4 v[16:19], %[l1], %[rs], %[ur] offen offset:0 sc1\n\t" \
    "buffer_store_dwordx4 v[16:19], %[l1], %[rs], %[ur] offen offset:1024 sc1\n\t" "buffer_store_dwordx4 v[16:19], %[l1], %[rs], %[ur] offen offset:2048 sc1\n\t" \
    "buffer_store_dwordx4 v[16:19], %[l1], %[rs], %[ur] offen offset:3072 sc1\n\t" "s_sub_u32 s61, %[ur], 0x1000\n\t" \
    "buffer_store_dwordx4 v[16:19], %[l1], %[rs], s61 offen offset:0 sc1\n\t" "buffer_store_dwordx4 v[16:19], %[l1], %[rs], s61 offen offset:1024 sc1\n\t" "s_branch 99f\n\t" \
    "21:\n\t" "ds_read_b128 v[228:231], %[l1] offset:4096\n\t" "s_waitcnt lgkmcnt(0)\n\t" "v_max3_u32 v225, v228, v229, v230\n\t" "v_max_u32 v225, v225, v231\n\t" \
    "ds_read_b128 v[228:231], %[l1] offset:5120\n\t" "s_waitcnt lgkmcnt(0)\n\t" "v_max3_u32 v225, v225, v228, v229\n\t" "v_max3_u32 v225, v225, v230, v231\n\t" \
    "ds_read_b128 v[228:231], %[l1] offset:6144\n\t" "s_waitcnt lgkmcnt(0)\n\t" "v_max3_u32 v225, v225, v228, v229\n\t" "v_max3_u32 v225, v225, v230, v231\n\t" \
    "ds_read_b128 v[228:231], %[l1] offset:7168\n\t" "s_waitcnt lgkmcnt(0)\n\t" "v_max3_u32 v225, v225, v228, v229\n\t" "v_max3_u32 v225, v225, v230, v231\n\t" \
    "v_cmp_eq_u32 vcc, -1, v225\n\t" "s_cbranch_vccz 22b\n\t" "s_cmp_eq_u32 %[rt], 1\n\t" "s_cbranch_scc1 22b\n\t" "v_mov_b32 v225, %[rt]\n\t" "ds_write_b32 v227, v225\n\t" \
    "s_waitcnt lgkmcnt(0)\n\t" "s_branch 22b\n\t" "23:\n\t" "s_sub_u32 %[rt], %[rt], 1\n\t" "s_waitcnt lgkmcnt(0)\n\t" XCNT(100) "24:\n\t" \
    "buffer_load_dwordx4 v[240:243], %[mo], %[rs], %[sb] offen sc1\n\t" "buffer_load_dwordx4 v[244:247], v224, %[rs], %[sb] offen sc1\n\t" \
    "buffer_load_dwordx4 v[248:251], %[mo], %[rs], s60 offen sc1\n\t" "buffer_load_dwordx4 v[252:255], v224, %[rs], s60 offen sc1\n\t" "s_waitcnt vmcnt(0)\n\t" \
    "v_max_u32 v225, v240, v241\n\t" "v_max3_u32 v225, v225, v242, v243\n\t" "v_max3_u32 v225, v225, v244, v245\n\t" "v_max3_u32 v225, v225, v246, v247\n\t" \
    "v_max3_u32 v225, v225, v248, v249\n\t" "v_max3_u32 v225, v225, v250, v251\n\t" "v_max3_u32 v225, v225, v252, v253\n\t" "v_max3_u32 v225, v225, v254, v255\n\t" \
    "v_cmp_eq_u32 vcc, -1, v225\n\t" "s_cbranch_vccz 25f\n\t" "s_cmp_eq_u32 %[bd], 0\n\t" "s_cbranch_scc1 25f\n\t" "s_sub_u32 %[bd], %[bd], 1\n\t" XCNT(101) \
    "s_branch 24b\n\t" "25:\n\t" "ds_read_b128 v[160:163], %[l0] offset:0\n\t" "ds_read_b128 v[164:167], %[l0] offset:1024\n\t" \
    "ds_read_b128 v[168:171], %[l0] offset:2048\n\t" "ds_read_b128 v[172:175], %[l0] offset:3072\n\t" "ds_read_b128 v[176:179], %[l0] offset:4096\n\t" \
    "ds_read_b128 v[180:183], %[l0] offset:5120\n\t" "ds_read_b128 v[184:187], %[l0] offset:6144\n\t" "ds_read_b128 v[188:191], %[l0] offset:7168\n\t" \
    "v_mov_b64 v[232:233], 0\n\t" "v_mov_b64 v[234:235], 0\n\t" "v_mov_b64 v[236:237], 0\n\t" "v_mov_b64 v[238:239], 0\n\t" "s_barrier\n\t" "s_branch 20b\n\t" "92:\n\t" \
    XT(80, 81) "s_barrier\n\t" XT(82, 83) "buffer_load_dwordx4 v[240:243], %[mo], %[rs], %[sb] offen sc1\n\t" "v_mov_b64 v[232:233], 0\n\t" "v_mov_b64 v[234:235], 0\n\t" \
    "v_mov_b64 v[236:237], 0\n\t" "v_mov_b64 v[238:239], 0\n\t" "v_mov_b64 v[192:193], 0\n\t" "v_mov_b64 v[194:195], 0\n\t" "v_mov_b64 v[196:197], 0\n\t" \
    "v_mov_b64 v[198:199], 0\n\t" "buffer_load_dwordx4 v[244:247], v224, %[rs], %[sb] offen sc1\n\t" "ds_read_b128 v[160:163], %[l0] offset:0\n\t" \
    "ds_read_b128 v[164:167], %[l0] offset:1024\n\t" "ds_read_b128 v[168:171], %[l0] offset:2048\n\t" "ds_read_b128 v[172:175], %[l0] offset:3072\n\t" "s_nop 3\n\t" \
    "buffer_load_dwordx4 v[248:251], %[mo], %[rs], s60 offen sc1\n\t" "ds_read_b128 v[176:179], %[l0] offset:4096\n\t" "ds_read_b128 v[180:183], %[l0] offset:5120\n\t" \
    "ds_read_b128 v[184:187], %[l0] offset:6144\n\t" "ds_read_b128 v[188:191], %[l0] offset:7168\n\t" "s_nop 3\n\t" \
    "buffer_load_dwordx4 v[252:255], v224, %[rs], s60 offen sc1\n\t" "v_mov_b64 v[200:201], 0\n\t" "v_mov_b64 v[202:203], 0\n\t" "v_mov_b64 v[204:205], 0\n\t" \
    "v_mov_b64 v[206:207], 0\n\t" "v_mov_b64 v[208:209], 0\n\t" "v_mov_b64 v[210:211], 0\n\t" "v_mov_b64 v[212:213], 0\n\t" "v_mov_b64 v[214:215], 0\n\t" \
    "v_mov_b64 v[216:217], 0\n\t" "v_mov_b64 v[218:219], 0\n\t" "v_mov_b64 v[220:221], 0\n\t" "v_mov_b64 v[222:223], 0\n\t" "v_mov_b32 v227, 160512\n\t" "30:\n\t" \
    "s_waitcnt vmcnt(3)\n\t" XT(84, 85) "v_smfmac_f32_16x16x64_bf16 v[232:235], v[240:243], a[0:7], %[ix]\n\t" "ds_write_b128 %[l1], v[240:243] offset:8192\n\t" \
    "v_smfmac_f32_16x16x64_bf16 v[236:239], v[240:243], a[8:15], %[ix]\n\t" "s_waitcnt vmcnt(2)\n\t" "v_smfmac_f32_16x16x64_bf16 v[232:235], v[244:247], a[16:23], %[ix]\n\t" \
    "ds_write_b128 %[l1], v[244:247] offset:9216\n\t" "s_waitcnt lgkmcnt(8)\n\t" "v_smfmac_f32_16x16x64_bf16 v[236:239], v[244:247], v[160:167], %[ix]\n\t" \
    "s_waitcnt lgkmcnt(0)\n\t" "s_barrier\n\t" XT(86, 87) "ds_read_b128 v[16:19], %[l1] offset:12288\n\t" "ds_read_b128 v[20:23], %[l1] offset:0\n\t" \
    "ds_read_b128 v[24:27], %[l1] offset:4096\n\t" "s_waitcnt vmcnt(1)\n\t" "v_smfmac_f32_16x16x64_bf16 v[232:235], v[248:251], a[24:31], %[ix]\n\t" \
    "ds_write_b128 %[l1], v[248:251] offset:10240\n\t" "v_smfmac_f32_16x16x64_bf16 v[236:239], v[248:251], a[32:39], %[ix]\n\t" \
    "ds_read_b128 v[28:31], %[l1] offset:13312\n\t" "ds_read_b128 v[240:243], %[l1] offset:1024\n\t" "ds_read_b128 v[244:247], %[l1] offset:5120\n\t" "s_waitcnt vmcnt(0)\n\t" \
    "v_smfmac_f32_16x16x64_bf16 v[232:235], v[252:255], a[40:47], %[ix]\n\t" "ds_write_b128 %[l1], v[252:255] offset:11264\n\t" \
    "v_smfmac_f32_16x16x64_bf16 v[236:239], v[252:255], v[168:175], %[ix]\n\t" "ds_read_b128 v[160:163], %[l0] offset:8192\n\t" \
    "ds_read_b128 v[164:167], %[l0] offset:9216\n\t" "ds_read_b128 v[168:171], %[l0] offset:10240\n\t" "ds_read_b128 v[172:175], %[l0] offset:11264\n\t" "s_nop 1\n\t" \
    "v_cmp_u_f32 vcc, v232, v233\n\t" "v_cmp_u_f32 s[62:63], v234, v235\n\t" "s_waitcnt lgkmcnt(11)\n\t" \
    "v_smfmac_f32_16x16x64_bf16 v[232:235], v[16:19], a[48:55], %[ix]\n\t" "v_smfmac_f32_16x16x64_bf16 v[236:239], v[16:19], a[56:63], %[ix]\n\t" \
    "s_or_b64 vcc, vcc, s[62:63]\n\t" "s_cbranch_scc1 31f\n\t" "32:\n\t" "s_waitcnt lgkmcnt(10)\n\t" "v_smfmac_f32_16x16x64_bf16 v[232:235], v[20:23], a[64:71], %[ix]\n\t" \
    "v_smfmac_f32_16x16x64_bf16 v[236:239], v[20:23], v[176:183], %[ix]\n\t" "s_waitcnt lgkmcnt(4)\n\t" "s_barrier\n\t" XT(88, 89) "ds_read_b32 v225, v227\n\t" \
    "v_smfmac_f32_16x16x64_bf16 v[232:235], v[24:27], a[72:79], %[ix]\n\t" "ds_read_b128 v[176:179], %[l0] offset:12288\n\t" "ds_read_b128 v[180:183], %[l0] offset:13312\n\t" \
    "v_smfmac_f32_16x16x64_bf16 v[236:239], v[24:27], a[80:87], %[ix]\n\t" "ds_read_b128 v[248:251], %[l1] offset:14336\n\t" "ds_read_b128 v[252:255], %[l1] offset:2048\n\t" \
    "v_smfmac_f32_16x16x64_bf16 v[232:235], v[28:31], a[88:95], %[ix]\n\t" "ds_read_b128 v[16:19], %[l1] offset:6144\n\t" "ds_read_b128 v[20:23], %[l1] offset:15360\n\t" \
    "v_smfmac_f32_16x16x64_bf16 v[236:239], v[28:31], v[184:191], %[ix]\n\t" "ds_read_b128 v[24:27], %[l1] offset:3072\n\t" "ds_read_b128 v[184:187], %[l0] offset:14336\n\t" \
    "v_smfmac_f32_16x16x64_bf16 v[232:235], v[240:243], a[96:103], %[ix]\n\t" "ds_read_b128 v[188:191], %[l0] offset:15360\n\t" "ds_read_b128 v[28:31], %[l1] offset:7168\n\t" \
    "v_smfmac_f32_16x16x64_bf16 v[236:239], v[240:243], a[104:111], %[ix]\n\t" "s_waitcnt lgkmcnt(10)\n\t" "v_cmp_eq_u32 vcc, %[rt], v225\n\t" \
    "v_smfmac_f32_16x16x64_bf16 v[232:235], v[244:247], a[112:119], %[ix]\n\t" "s_cbranch_vccnz 33f\n\t" \
    "v_smfmac_f32_16x16x64_bf16 v[236:239], v[244:247], v[160:167], %[ix]\n\t" "ds_read_b128 v[160:163], %[l0] offset:16384\n\t" \
    "ds_read_b128 v[164:167], %[l0] offset:17408\n\t" "s_waitcnt lgkmcnt(9)\n\t" "v_smfmac_f32_16x16x64_bf16 v[232:235], v[248:251], a[120:127], %[ix]\n\t" \
    "v_smfmac_f32_16x16x64_bf16 v[236:239], v[248:251], a[128:135], %[ix]\n\t" "s_waitcnt lgkmcnt(8)\n\t" \
    "v_smfmac_f32_16x16x64_bf16 v[232:235], v[252:255], a[136:143], %[ix]\n\t" "v_smfmac_f32_16x16x64_bf16 v[236:239], v[252:255], v[168:175], %[ix]\n\t" \
    "ds_read_b128 v[168:171], %[l0] offset:18432\n\t" "ds_read_b128 v[172:175], %[l0] offset:19456\n\t" "s_waitcnt lgkmcnt(9)\n\t" \
    "v_smfmac_f32_16x16x64_bf16 v[232:235], v[16:19], a[144:151], %[ix]\n\t" "v_smfmac_f32_16x16x64_bf16 v[236:239], v[16:19], a[152:159], %[ix]\n\t" \
    "s_waitcnt lgkmcnt(8)\n\t" "v_smfmac_f32_16x16x64_bf16 v[232:235], v[20:23], a[160:167], %[ix]\n\t" \
    "v_smfmac_f32_16x16x64_bf16 v[236:239], v[20:23], v[176:183], %[ix]\n\t" "ds_read_b128 v[176:179], %[l0] offset:20480\n\t" \
    "ds_read_b128 v[180:183], %[l0] offset:21504\n\t" "s_waitcnt lgkmcnt(9)\n\t" "v_smfmac_f32_16x16x64_bf16 v[232:235], v[24:27], a[168:175], %[ix]\n\t" \
    "v_smfmac_f32_16x16x64_bf16 v[236:239], v[24:27], a[176:183], %[ix]\n\t" "s_waitcnt lgkmcnt(6)\n\t" \
    "v_smfmac_f32_16x16x64_bf16 v[232:235], v[28:31], a[184:191], %[ix]\n\t" "v_smfmac_f32_16x16x64_bf16 v[236:239], v[28:31], v[184:191], %[ix]\n\t" \
    "ds_read_b128 v[184:187], %[l0] offset:22528\n\t" "ds_read_b128 v[188:191], %[l0] offset:23552\n\t" XT(90, 91) "s_nop 4\n\t" "v_add_f32 v232, v232, v233\n\t" \
    "v_add_f32 v234, v234, v235\n\t" "v_add_f32 v236, v236, v237\n\t" "v_add_f32 v238, v238, v239\n\t" "v_cvt_pk_bf16_f32 v236, v232, v236\n\t" \
    "v_cvt_pk_bf16_f32 v237, v234, v238\n\t" "ds_write2_b32 %[vw], v236, v237 offset1:68\n\t" "s_waitcnt lgkmcnt(0)\n\t" "s_barrier\n\t" XT(92, 93) \
    "ds_read_b128 v[240:243], %[vr] offset:0\n\t" "ds_read_b128 v[244:247], %[vr] offset:64\n\t" "ds_read_b128 v[248:251], %[vr] offset:128\n\t" \
    "ds_read_b128 v[252:255], %[vr] offset:192\n\t" "s_mov_b32 s61, %[ub]\n\t" "s_waitcnt lgkmcnt(3)\n\t" \
    "v_smfmac_f32_16x16x64_bf16 v[192:195], v[240:243], v[32:39], %[ix]\n\t" "v_smfmac_f32_16x16x64_bf16 v[196:199], v[240:243], v[48:55], %[ix]\n\t" \
    "s_waitcnt lgkmcnt(2)\n\t" "v_smfmac_f32_16x16x64_bf16 v[192:195], v[244:247], v[40:47], %[ix]\n\t" \
    "v_smfmac_f32_16x16x64_bf16 v[196:199], v[244:247], v[56:63], %[ix]\n\t" "s_waitcnt lgkmcnt(1)\n\t" \
    "v_smfmac_f32_16x16x64_bf16 v[192:195], v[248:251], a[192:199], %[ix]\n\t" "v_smfmac_f32_16x16x64_bf16 v[196:199], v[248:251], a[200:207], %[ix]\n\t" \
    "s_waitcnt lgkmcnt(0)\n\t" "v_smfmac_f32_16x16x64_bf16 v[192:195], v[252:255], v[160:167], %[ix]\n\t" "ds_read_b128 v[160:163], %[l0] offset:24576\n\t" \
    "ds_read_b128 v[164:167], %[l0] offset:25600\n\t" "v_smfmac_f32_16x16x64_bf16 v[196:199], v[252:255], v[168:175], %[ix]\n\t" \
    "ds_read_b128 v[168:171], %[l0] offset:26624\n\t" "ds_read_b128 v[172:175], %[l0] offset:27648\n\t" \
    "v_smfmac_f32_16x16x64_bf16 v[200:203], v[240:243], v[64:71], %[ix]\n\t" "s_nop 1\n\t" "v_smfmac_f32_16x16x64_bf16 v[204:207], v[240:243], v[80:87], %[ix]\n\t" \
    "v_cvt_pk_bf16_f32 v224, v192, v194\n\t" "v_cvt_pk_bf16_f32 v225, v193, v195\n\t" "v_smfmac_f32_16x16x64_bf16 v[200:203], v[244:247], v[72:79], %[ix]\n\t" \
    "v_cvt_pk_bf16_f32 v226, v196, v198\n\t" "v_cvt_pk_bf16_f32 v227, v197, v199\n\t" "v_smfmac_f32_16x16x64_bf16 v[204:207], v[244:247], v[88:95], %[ix]\n\t" "s_nop 0\n\t" \
    "v_permlane32_swap_b32 v224, v226\n\t" "v_smfmac_f32_16x16x64_bf16 v[200:203], v[248:251], a[208:215], %[ix]\n\t" "v_permlane32_swap_b32 v225, v227\n\t" "s_nop 0\n\t" \
    "v_smfmac_f32_16x16x64_bf16 v[204:207], v[248:251], a[216:223], %[ix]\n\t" "v_permlane16_swap_b32 v224, v225\n\t" "v_permlane16_swap_b32 v226, v227\n\t" \
    "v_smfmac_f32_16x16x64_bf16 v[200:203], v[252:255], v[176:183], %[ix]\n\t" "ds_read_b128 v[176:179], %[l0] offset:28672\n\t" \
    "buffer_store_dwordx4 v[224:227], %[uo], %[rs], s61 offen sc1\n\t" "s_xor_b32 s61, s61, 0x40000\n\t" XT(94, 95) \
    "v_smfmac_f32_16x16x64_bf16 v[204:207], v[252:255], v[184:191], %[ix]\n\t" "ds_read_b128 v[180:183], %[l0] offset:29696\n\t" \
    "ds_read_b128 v[184:187], %[l0] offset:30720\n\t" "v_smfmac_f32_16x16x64_bf16 v[208:211], v[240:243], v[96:103], %[ix]\n\t" \
    "ds_read_b128 v[188:191], %[l0] offset:31744\n\t" "s_nop 0\n\t" "v_smfmac_f32_16x16x64_bf16 v[212:215], v[240:243], v[112:119], %[ix]\n\t" \
    "v_cvt_pk_bf16_f32 v228, v200, v202\n\t" "v_cvt_pk_bf16_f32 v229, v201, v203\n\t" "v_smfmac_f32_16x16x64_bf16 v[208:211], v[244:247], v[104:111], %[ix]\n\t" \
    "v_cvt_pk_bf16_f32 v230, v204, v206\n\t" "v_cvt_pk_bf16_f32 v231, v205, v207\n\t" "v_smfmac_f32_16x16x64_bf16 v[212:215], v[244:247], v[120:127], %[ix]\n\t" "s_nop 0\n\t" \
    "v_permlane32_swap_b32 v228, v230\n\t" "v_smfmac_f32_16x16x64_bf16 v[208:211], v[248:251], a[224:231], %[ix]\n\t" "v_permlane32_swap_b32 v229, v231\n\t" "s_nop 0\n\t" \
    "v_smfmac_f32_16x16x64_bf16 v[212:215], v[248:251], a[232:239], %[ix]\n\t" "v_permlane16_swap_b32 v228, v229\n\t" "v_permlane16_swap_b32 v230, v231\n\t" \
    "s_waitcnt lgkmcnt(6)\n\t" "v_smfmac_f32_16x16x64_bf16 v[208:211], v[252:255], v[160:167], %[ix]\n\t" "buffer_store_dwordx4 v[228:231], %[uo], %[rs], s61 offen sc1\n\t" \
    "s_xor_b32 s61, s61, 0xc0000\n\t" "s_waitcnt lgkmcnt(4)\n\t" "v_smfmac_f32_16x16x64_bf16 v[212:215], v[252:255], v[168:175], %[ix]\n\t" "s_nop 1\n\t" \
    "v_smfmac_f32_16x16x64_bf16 v[216:219], v[240:243], v[128:135], %[ix]\n\t" "s_nop 1\n\t" "v_smfmac_f32_16x16x64_bf16 v[220:223], v[240:243], v[144:151], %[ix]\n\t" \
    "v_cvt_pk_bf16_f32 v224, v208, v210\n\t" "v_cvt_pk_bf16_f32 v225, v209, v211\n\t" "v_smfmac_f32_16x16x64_bf16 v[216:219], v[244:247], v[136:143], %[ix]\n\t" \
    "v_cvt_pk_bf16_f32 v226, v212, v214\n\t" "v_cvt_pk_bf16_f32 v227, v213, v215\n\t" "v_smfmac_f32_16x16x64_bf16 v[220:223], v[244:247], v[152:159], %[ix]\n\t" "s_nop 0\n\t" \
    "v_permlane32_swap_b32 v224, v226\n\t" "v_smfmac_f32_16x16x64_bf16 v[216:219], v[248:251], a[240:247], %[ix]\n\t" "v_permlane32_swap_b32 v225, v227\n\t" "s_nop 0\n\t" \
    "v_smfmac_f32_16x16x64_bf16 v[220:223], v[248:251], a[248:255], %[ix]\n\t" "v_permlane16_swap_b32 v224, v225\n\t" "v_permlane16_swap_b32 v226, v227\n\t" \
    "s_waitcnt lgkmcnt(2)\n\t" "v_smfmac_f32_16x16x64_bf16 v[216:219], v[252:255], v[176:183], %[ix]\n\t" "buffer_store_dwordx4 v[224:227], %[uo], %[rs], s61 offen sc1\n\t" \
    "s_xor_b32 s61, s61, 0x40000\n\t" "s_waitcnt lgkmcnt(0)\n\t" "v_smfmac_f32_16x16x64_bf16 v[220:223], v[252:255], v[184:191], %[ix]\n\t" "s_nop 5\n\t" \
    "v_cvt_pk_bf16_f32 v228, v216, v218\n\t" "v_cvt_pk_bf16_f32 v229, v217, v219\n\t" "v_cvt_pk_bf16_f32 v230, v220, v222\n\t" "v_cvt_pk_bf16_f32 v231, v221, v223\n\t" \
    "s_nop 0\n\t" "v_permlane32_swap_b32 v228, v230\n\t" "v_permlane32_swap_b32 v229, v231\n\t" "s_nop 1\n\t" "v_permlane16_swap_b32 v228, v229\n\t" \
    "v_permlane16_swap_b32 v230, v231\n\t" "buffer_store_dwordx4 v[228:231], %[uo], %[rs], s61 offen sc1\n\t" "s_nop 1\n\t" XT(96, 97) "v_mov_b64 v[16:17], -1\n\t" \
    "v_mov_b64 v[18:19], -1\n\t" "s_barrier\n\t" "buffer_store_dwordx4 v[16:19], %[l1], %[rs], %[ur] offen offset:0 sc1\n\t" \
    "buffer_store_dwordx4 v[16:19], %[l1], %[rs], %[ur] offen offset:1024 sc1\n\t" "buffer_store_dwordx4 v[16:19], %[l1], %[rs], %[ur] offen offset:2048 sc1\n\t" \
    "buffer_store_dwordx4 v[16:19], %[l1], %[rs], %[ur] offen offset:3072 sc1\n\t" "s_sub_u32 s61, %[ur], 0x4000\n\t" \
    "buffer_store_dwordx4 v[16:19], %[l1], %[rs], s61 offen offset:2048 sc1\n\t" "s_branch 99f\n\t" "31:\n\t" "ds_read_b128 v[228:231], %[l1] offset:8192\n\t" \
    "s_waitcnt lgkmcnt(0)\n\t" "v_max3_u32 v225, v228, v229, v230\n\t" "v_max_u32 v225, v225, v231\n\t" "ds_read_b128 v[228:231], %[l1] offset:9216\n\t" \
    "s_waitcnt lgkmcnt(0)\n\t" "v_max3_u32 v225, v225, v228, v229\n\t" "v_max3_u32 v225, v225, v230, v231\n\t" "ds_read_b128 v[228:231], %[l1] offset:10240\n\t" \
    "s_waitcnt lgkmcnt(0)\n\t" "v_max3_u32 v225, v225, v228, v229\n\t" "v_max3_u32 v225, v225, v230, v231\n\t" "ds_read_b128 v[228:231], %[l1] offset:11264\n\t" \
    "s_waitcnt lgkmcnt(0)\n\t" "v_max3_u32 v225, v225, v228, v229\n\t" "v_max3_u32 v225, v225, v230, v231\n\t" "v_cmp_eq_u32 vcc, -1, v225\n\t" "s_cbranch_vccz 32b\n\t" \
    "s_cmp_eq_u32 %[rt], 1\n\t" "s_cbranch_scc1 32b\n\t" "v_mov_b32 v225, %[rt]\n\t" "ds_write_b32 v227, v225\n\t" "s_waitcnt lgkmcnt(0)\n\t" "s_branch 32b\n\t" "33:\n\t" \
    "s_sub_u32 %[rt], %[rt], 1\n\t" "s_waitcnt lgkmcnt(0)\n\t" XCNT(100) "34:\n\t" "buffer_load_dwordx4 v[240:243], %[mo], %[rs], %[sb] offen sc1\n\t" \
    "buffer_load_dwordx4 v[244:247], v224, %[rs], %[sb] offen sc1\n\t" "buffer_load_dwordx4 v[248:251], %[mo], %[rs], s60 offen sc1\n\t" \
    "buffer_load_dwordx4 v[252:255], v224, %[rs], s60 offen sc1\n\t" "s_waitcnt vmcnt(0)\n\t" "v_max_u32 v225, v240, v241\n\t" "v_max3_u32 v225, v225, v242, v243\n\t" \
    "v_max3_u32 v225, v225, v244, v245\n\t" "v_max3_u32 v225, v225, v246, v247\n\t" "v_max3_u32 v225, v225, v248, v249\n\t" "v_max3_u32 v225, v225, v250, v251\n\t" \
    "v_max3_u32 v225, v225, v252, v253\n\t" "v_max3_u32 v225, v225, v254, v255\n\t" "v_cmp_eq_u32 vcc, -1, v225\n\t" "s_cbranch_vccz 35f\n\t" "s_cmp_eq_u32 %[bd], 0\n\t" \
    "s_cbranch_scc1 35f\n\t" "s_sub_u32 %[bd], %[bd], 1\n\t" XCNT(101) "s_branch 34b\n\t" "35:\n\t" "ds_read_b128 v[160:163], %[l0] offset:0\n\t" \
    "ds_read_b128 v[164:167], %[l0] offset:1024\n\t" "ds_read_b128 v[168:171], %[l0] offset:2048\n\t" "ds_read_b128 v[172:175], %[l0] offset:3072\n\t" \
    "ds_read_b128 v[176:179], %[l0] offset:4096\n\t" "ds_read_b128 v[180:183], %[l0] offset:5120\n\t" "ds_read_b128 v[184:187], %[l0] offset:6144\n\t" \
    "ds_read_b128 v[188:191], %[l0] offset:7168\n\t" "v_mov_b64 v[232:233], 0\n\t" "v_mov_b64 v[234:235], 0\n\t" "v_mov_b64 v[236:237], 0\n\t" "v_mov_b64 v[238:239], 0\n\t" \
    "s_barrier\n\t" "s_branch 30b\n\t" "93:\n\t" XT(80, 81) "s_barrier\n\t" XT(82, 83) "buffer_load_dwordx4 v[240:243], %[mo], %[rs], %[sb] offen sc1\n\t" \
    "v_mov_b64 v[232:233], 0\n\t" "v_mov_b64 v[234:235], 0\n\t" "v_mov_b64 v[236:237], 0\n\t" "v_mov_b64 v[238:239], 0\n\t" "v_mov_b64 v[192:193], 0\n\t" \
    "v_mov_b64 v[194:195], 0\n\t" "v_mov_b64 v[196:197], 0\n\t" "v_mov_b64 v[198:199], 0\n\t" "buffer_load_dwordx4 v[244:247], v224, %[rs], %[sb] offen sc1\n\t" \
    "ds_read_b128 v[160:163], %[l0] offset:0\n\t" "ds_read_b128 v[164:167], %[l0] offset:1024\n\t" "ds_read_b128 v[168:171], %[l0] offset:2048\n\t" \
    "ds_read_b128 v[172:175], %[l0] offset:3072\n\t" "s_nop 3\n\t" "buffer_load_dwordx4 v[248:251], %[mo], %[rs], s60 offen sc1\n\t" \
    "ds_read_b128 v[176:179], %[l0] offset:4096\n\t" "ds_read_b128 v[180:183], %[l0] offset:5120\n\t" "ds_read_b128 v[184:187], %[l0] offset:6144\n\t" \
    "ds_read_b128 v[188:191], %[l0] offset:7168\n\t" "s_nop 3\n\t" "buffer_load_dwordx4 v[252:255], v224, %[rs], s60 offen sc1\n\t" "v_mov_b64 v[200:201], 0\n\t" \
    "v_mov_b64 v[202:203], 0\n\t" "v_mov_b64 v[204:205], 0\n\t" "v_mov_b64 v[206:207], 0\n\t" "v_mov_b64 v[208:209], 0\n\t" "v_mov_b64 v[210:211], 0\n\t" \
    "v_mov_b64 v[212:213], 0\n\t" "v_mov_b64 v[214:215], 0\n\t" "v_mov_b64 v[216:217], 0\n\t" "v_mov_b64 v[218:219], 0\n\t" "v_mov_b64 v[220:221], 0\n\t" \
    "v_mov_b64 v[222:223], 0\n\t" "v_mov_b32 v227, 160512\n\t" "40:\n\t" "s_waitcnt vmcnt(3)\n\t" XT(84, 85) \
    "v_smfmac_f32_16x16x64_bf16 v[232:235], v[240:243], a[0:7], %[ix]\n\t" "ds_write_b128 %[l1], v[240:243] offset:12288\n\t" \
    "v_smfmac_f32_16x16x64_bf16 v[236:239], v[240:243], a[8:15], %[ix]\n\t" "s_waitcnt vmcnt(2)\n\t" "v_smfmac_f32_16x16x64_bf16 v[232:235], v[244:247], a[16:23], %[ix]\n\t" \
    "ds_write_b128 %[l1], v[244:247] offset:13312\n\t" "s_waitcnt lgkmcnt(8)\n\t" "v_smfmac_f32_16x16x64_bf16 v[236:239], v[244:247], v[160:167], %[ix]\n\t" \
    "s_waitcnt lgkmcnt(0)\n\t" "s_barrier\n\t" XT(86, 87) "ds_read_b128 v[16:19], %[l1] offset:0\n\t" "ds_read_b128 v[20:23], %[l1] offset:4096\n\t" \
    "ds_read_b128 v[24:27], %[l1] offset:8192\n\t" "s_waitcnt vmcnt(1)\n\t" "v_smfmac_f32_16x16x64_bf16 v[232:235], v[248:251], a[24:31], %[ix]\n\t" \
    "ds_write_b128 %[l1], v[248:251] offset:14336\n\t" "v_smfmac_f32_16x16x64_bf16 v[236:239], v[248:251], a[32:39], %[ix]\n\t" "ds_read_b128 v[28:31], %[l1] offset:1024\n\t" \
    "ds_read_b128 v[240:243], %[l1] offset:5120\n\t" "ds_read_b128 v[244:247], %[l1] offset:9216\n\t" "s_waitcnt vmcnt(0)\n\t" \
    "v_smfmac_f32_16x16x64_bf16 v[232:235], v[252:255], a[40:47], %[ix]\n\t" "ds_write_b128 %[l1], v[252:255] offset:15360\n\t" \
    "v_smfmac_f32_16x16x64_bf16 v[236:239], v[252:255], v[168:175], %[ix]\n\t" "ds_read_b128 v[160:163], %[l0] offset:8192\n\t" \
    "ds_read_b128 v[164:167], %[l0] offset:9216\n\t" "ds_read_b128 v[168:171], %[l0] offset:10240\n\t" "ds_read_b128 v[172:175], %[l0] offset:11264\n\t" "s_nop 1\n\t" \
    "v_cmp_u_f32 vcc, v232, v233\n\t" "v_cmp_u_f32 s[62:63], v234, v235\n\t" "s_waitcnt lgkmcnt(11)\n\t" \
    "v_smfmac_f32_16x16x64_bf16 v[232:235], v[16:19], a[48:55], %[ix]\n\t" "v_smfmac_f32_16x16x64_bf16 v[236:239], v[16:19], a[56:63], %[ix]\n\t" \
    "s_or_b64 vcc, vcc, s[62:63]\n\t" "s_cbranch_scc1 41f\n\t" "42:\n\t" "s_waitcnt lgkmcnt(10)\n\t" "v_smfmac_f32_16x16x64_bf16 v[232:235], v[20:23], a[64:71], %[ix]\n\t" \
    "v_smfmac_f32_16x16x64_bf16 v[236:239], v[20:23], v[176:183], %[ix]\n\t" "s_waitcnt lgkmcnt(4)\n\t" "s_barrier\n\t" XT(88, 89) "ds_read_b32 v225, v227\n\t" \
    "v_smfmac_f32_16x16x64_bf16 v[232:235], v[24:27], a[72:79], %[ix]\n\t" "ds_read_b128 v[176:179], %[l0] offset:12288\n\t" "ds_read_b128 v[180:183], %[l0] offset:13312\n\t" \
    "v_smfmac_f32_16x16x64_bf16 v[236:239], v[24:27], a[80:87], %[ix]\n\t" "ds_read_b128 v[248:251], %[l1] offset:2048\n\t" "ds_read_b128 v[252:255], %[l1] offset:6144\n\t" \
    "v_smfmac_f32_16x16x64_bf16 v[232:235], v[28:31], a[88:95], %[ix]\n\t" "ds_read_b128 v[16:19], %[l1] offset:10240\n\t" "ds_read_b128 v[20:23], %[l1] offset:3072\n\t" \
    "v_smfmac_f32_16x16x64_bf16 v[236:239], v[28:31], v[184:191], %[ix]\n\t" "ds_read_b128 v[24:27], %[l1] offset:7168\n\t" "ds_read_b128 v[184:187], %[l0] offset:14336\n\t" \
    "v_smfmac_f32_16x16x64_bf16 v[232:235], v[240:243], a[96:103], %[ix]\n\t" "ds_read_b128 v[188:191], %[l0] offset:15360\n\t" \
    "ds_read_b128 v[28:31], %[l1] offset:11264\n\t" "v_smfmac_f32_16x16x64_bf16 v[236:239], v[240:243], a[104:111], %[ix]\n\t" "s_waitcnt lgkmcnt(10)\n\t" \
    "v_cmp_eq_u32 vcc, %[rt], v225\n\t" "v_smfmac_f32_16x16x64_bf16 v[232:235], v[244:247], a[112:119], %[ix]\n\t" "s_cbranch_vccnz 43f\n\t" \
    "v_smfmac_f32_16x16x64_bf16 v[236:239], v[244:247], v[160:167], %[ix]\n\t" "ds_read_b128 v[160:163], %[l0] offset:16384\n\t" \
    "ds_read_b128 v[164:167], %[l0] offset:17408\n\t" "s_waitcnt lgkmcnt(9)\n\t" "v_smfmac_f32_16x16x64_bf16 v[232:235], v[248:251], a[120:127], %[ix]\n\t" \
    "v_smfmac_f32_16x16x64_bf16 v[236:239], v[248:251], a[128:135], %[ix]\n\t" "s_waitcnt lgkmcnt(8)\n\t" \
    "v_smfmac_f32_16x16x64_bf16 v[232:235], v[252:255], a[136:143], %[ix]\n\t" "v_smfmac_f32_16x16x64_bf16 v[236:239], v[252:255], v[168:175], %[ix]\n\t" \
    "ds_read_b128 v[168:171], %[l0] offset:18432\n\t" "ds_read_b128 v[172:175], %[l0] offset:19456\n\t" "s_waitcnt lgkmcnt(9)\n\t" \
    "v_smfmac_f32_16x16x64_bf16 v[232:235], v[16:19], a[144:151], %[ix]\n\t" "v_smfmac_f32_16x16x64_bf16 v[236:239], v[16:19], a[152:159], %[ix]\n\t" \
    "s_waitcnt lgkmcnt(8)\n\t" "v_smfmac_f32_16x16x64_bf16 v[232:235], v[20:23], a[160:167], %[ix]\n\t" \
    "v_smfmac_f32_16x16x64_bf16 v[236:239], v[20:23], v[176:183], %[ix]\n\t" "ds_read_b128 v[176:179], %[l0] offset:20480\n\t" \
    "ds_read_b128 v[180:183], %[l0] offset:21504\n\t" "s_waitcnt lgkmcnt(9)\n\t" "v_smfmac_f32_16x16x64_bf16 v[232:235], v[24:27], a[168:175], %[ix]\n\t" \
    "v_smfmac_f32_16x16x64_bf16 v[236:239], v[24:27], a[176:183], %[ix]\n\t" "s_waitcnt lgkmcnt(6)\n\t" \
    "v_smfmac_f32_16x16x64_bf16 v[232:235], v[28:31], a[184:191], %[ix]\n\t" "v_smfmac_f32_16x16x64_bf16 v[236:239], v[28:31], v[184:191], %[ix]\n\t" \
    "ds_read_b128 v[184:187], %[l0] offset:22528\n\t" "ds_read_b128 v[188:191], %[l0] offset:23552\n\t" XT(90, 91) "s_nop 4\n\t" "v_add_f32 v232, v232, v233\n\t" \
    "v_add_f32 v234, v234, v235\n\t" "v_add_f32 v236, v236, v237\n\t" "v_add_f32 v238, v238, v239\n\t" "v_cvt_pk_bf16_f32 v236, v232, v236\n\t" \
    "v_cvt_pk_bf16_f32 v237, v234, v238\n\t" "ds_write2_b32 %[vw], v236, v237 offset1:68\n\t" "s_waitcnt lgkmcnt(0)\n\t" "s_barrier\n\t" XT(92, 93) \
    "ds_read_b128 v[240:243], %[vr] offset:0\n\t" "ds_read_b128 v[244:247], %[vr] offset:64\n\t" "ds_read_b128 v[248:251], %[vr] offset:128\n\t" \
    "ds_read_b128 v[252:255], %[vr] offset:192\n\t" "s_mov_b32 s61, %[ub]\n\t" "s_waitcnt lgkmcnt(3)\n\t" \
    "v_smfmac_f32_16x16x64_bf16 v[192:195], v[240:243], v[32:39], %[ix]\n\t" "v_smfmac_f32_16x16x64_bf16 v[196:199], v[240:243], v[48:55], %[ix]\n\t" \
    "s_waitcnt lgkmcnt(2)\n\t" "v_smfmac_f32_16x16x64_bf16 v[192:195], v[244:247], v[40:47], %[ix]\n\t" \
    "v_smfmac_f32_16x16x64_bf16 v[196:199], v[244:247], v[56:63], %[ix]\n\t" "s_waitcnt lgkmcnt(1)\n\t" \
    "v_smfmac_f32_16x16x64_bf16 v[192:195], v[248:251], a[192:199], %[ix]\n\t" "v_smfmac_f32_16x16x64_bf16 v[196:199], v[248:251], a[200:207], %[ix]\n\t" \
    "s_waitcnt lgkmcnt(0)\n\t" "v_smfmac_f32_16x16x64_bf16 v[192:195], v[252:255], v[160:167], %[ix]\n\t" "ds_read_b128 v[160:163], %[l0] offset:24576\n\t" \
    "ds_read_b128 v[164:167], %[l0] offset:25600\n\t" "v_smfmac_f32_16x16x64_bf16 v[196:199], v[252:255], v[168:175], %[ix]\n\t" \
    "ds_read_b128 v[168:171], %[l0] offset:26624\n\t" "ds_read_b128 v[172:175], %[l0] offset:27648\n\t" \
    "v_smfmac_f32_16x16x64_bf16 v[200:203], v[240:243], v[64:71], %[ix]\n\t" "s_nop 1\n\t" "v_smfmac_f32_16x16x64_bf16 v[204:207], v[240:243], v[80:87], %[ix]\n\t" \
    "v_cvt_pk_bf16_f32 v224, v192, v194\n\t" "v_cvt_pk_bf16_f32 v225, v193, v195\n\t" "v_smfmac_f32_16x16x64_bf16 v[200:203], v[244:247], v[72:79], %[ix]\n\t" \
    "v_cvt_pk_bf16_f32 v226, v196, v198\n\t" "v_cvt_pk_bf16_f32 v227, v197, v199\n\t" "v_smfmac_f32_16x16x64_bf16 v[204:207], v[244:247], v[88:95], %[ix]\n\t" "s_nop 0\n\t" \
    "v_permlane32_swap_b32 v224, v226\n\t" "v_smfmac_f32_16x16x64_bf16 v[200:203], v[248:251], a[208:215], %[ix]\n\t" "v_permlane32_swap_b32 v225, v227\n\t" "s_nop 0\n\t" \
    "v_smfmac_f32_16x16x64_bf16 v[204:207], v[248:251], a[216:223], %[ix]\n\t" "v_permlane16_swap_b32 v224, v225\n\t" "v_permlane16_swap_b32 v226, v227\n\t" \
    "v_smfmac_f32_16x16x64_bf16 v[200:203], v[252:255], v[176:183], %[ix]\n\t" "ds_read_b128 v[176:179], %[l0] offset:28672\n\t" \
    "buffer_store_dwordx4 v[224:227], %[uo], %[rs], s61 offen sc1\n\t" "s_xor_b32 s61, s61, 0x40000\n\t" XT(94, 95) \
    "v_smfmac_f32_16x16x64_bf16 v[204:207], v[252:255], v[184:191], %[ix]\n\t" "ds_read_b128 v[180:183], %[l0] offset:29696\n\t" \
    "ds_read_b128 v[184:187], %[l0] offset:30720\n\t" "v_smfmac_f32_16x16x64_bf16 v[208:211], v[240:243], v[96:103], %[ix]\n\t" \
    "ds_read_b128 v[188:191], %[l0] offset:31744\n\t" "s_nop 0\n\t" "v_smfmac_f32_16x16x64_bf16 v[212:215], v[240:243], v[112:119], %[ix]\n\t" \
    "v_cvt_pk_bf16_f32 v228, v200, v202\n\t" "v_cvt_pk_bf16_f32 v229, v201, v203\n\t" "v_smfmac_f32_16x16x64_bf16 v[208:211], v[244:247], v[104:111], %[ix]\n\t" \
    "v_cvt_pk_bf16_f32 v230, v204, v206\n\t" "v_cvt_pk_bf16_f32 v231, v205, v207\n\t" "v_smfmac_f32_16x16x64_bf16 v[212:215], v[244:247], v[120:127], %[ix]\n\t" "s_nop 0\n\t" \
    "v_permlane32_swap_b32 v228, v230\n\t" "v_smfmac_f32_16x16x64_bf16 v[208:211], v[248:251], a[224:231], %[ix]\n\t" "v_permlane32_swap_b32 v229, v231\n\t" "s_nop 0\n\t" \
    "v_smfmac_f32_16x16x64_bf16 v[212:215], v[248:251], a[232:239], %[ix]\n\t" "v_permlane16_swap_b32 v228, v229\n\t" "v_permlane16_swap_b32 v230, v231\n\t" \
    "s_waitcnt lgkmcnt(6)\n\t" "v_smfmac_f32_16x16x64_bf16 v[208:211], v[252:255], v[160:167], %[ix]\n\t" "buffer_store_dwordx4 v[228:231], %[uo], %[rs], s61 offen sc1\n\t" \
    "s_xor_b32 s61, s61, 0xc0000\n\t" "s_waitcnt lgkmcnt(4)\n\t" "v_smfmac_f32_16x16x64_bf16 v[212:215], v[252:255], v[168:175], %[ix]\n\t" "s_nop 1\n\t" \
    "v_smfmac_f32_16x16x64_bf16 v[216:219], v[240:243], v[128:135], %[ix]\n\t" "s_nop 1\n\t" "v_smfmac_f32_16x16x64_bf16 v[220:223], v[240:243], v[144:151], %[ix]\n\t" \
    "v_cvt_pk_bf16_f32 v224, v208, v210\n\t" "v_cvt_pk_bf16_f32 v225, v209, v211\n\t" "v_smfmac_f32_16x16x64_bf16 v[216:219], v[244:247], v[136:143], %[ix]\n\t" \
    "v_cvt_pk_bf16_f32 v226, v212, v214\n\t" "v_cvt_pk_bf16_f32 v227, v213, v215\n\t" "v_smfmac_f32_16x16x64_bf16 v[220:223], v[244:247], v[152:159], %[ix]\n\t" "s_nop 0\n\t" \
    "v_permlane32_swap_b32 v224, v226\n\t" "v_smfmac_f32_16x16x64_bf16 v[216:219], v[248:251], a[240:247], %[ix]\n\t" "v_permlane32_swap_b32 v225, v227\n\t" "s_nop 0\n\t" \
    "v_smfmac_f32_16x16x64_bf16 v[220:223], v[248:251], a[248:255], %[ix]\n\t" "v_permlane16_swap_b32 v224, v225\n\t" "v_permlane16_swap_b32 v226, v227\n\t" \
    "s_waitcnt lgkmcnt(2)\n\t" "v_smfmac_f32_16x16x64_bf16 v[216:219], v[252:255], v[176:183], %[ix]\n\t" "buffer_store_dwordx4 v[224:227], %[uo], %[rs], s61 offen sc1\n\t" \
    "s_xor_b32 s61, s61, 0x40000\n\t" "s_waitcnt lgkmcnt(0)\n\t" "v_smfmac_f32_16x16x64_bf16 v[220:223], v[252:255], v[184:191], %[ix]\n\t" "s_nop 5\n\t" \
    "v_cvt_pk_bf16_f32 v228, v216, v218\n\t" "v_cvt_pk_bf16_f32 v229, v217, v219\n\t" "v_cvt_pk_bf16_f32 v230, v220, v222\n\t" "v_cvt_pk_bf16_f32 v231, v221, v223\n\t" \
    "s_nop 0\n\t" "v_permlane32_swap_b32 v228, v230\n\t" "v_permlane32_swap_b32 v229, v231\n\t" "s_nop 1\n\t" "v_permlane16_swap_b32 v228, v229\n\t" \
    "v_permlane16_swap_b32 v230, v231\n\t" "buffer_store_dwordx4 v[228:231], %[uo], %[rs], s61 offen sc1\n\t" "s_nop 1\n\t" XT(96, 97) "v_mov_b64 v[16:17], -1\n\t" \
    "v_mov_b64 v[18:19], -1\n\t" "s_barrier\n\t" "buffer_store_dwordx4 v[16:19], %[l1], %[rs], %[ur] offen offset:0 sc1\n\t" \
    "buffer_store_dwordx4 v[16:19], %[l1], %[rs], %[ur] offen offset:1024 sc1\n\t" "buffer_store_dwordx4 v[16:19], %[l1], %[rs], %[ur] offen offset:2048 sc1\n\t" \
    "buffer_store_dwordx4 v[16:19], %[l1], %[rs], %[ur] offen offset:3072 sc1\n\t" "s_sub_u32 s61, %[ur], 0x5000\n\t" \
    "buffer_store_dwordx4 v[16:19], %[l1], %[rs], s61 offen offset:3072 sc1\n\t" "s_branch 99f\n\t" "41:\n\t" "ds_read_b128 v[228:231], %[l1] offset:12288\n\t" \
    "s_waitcnt lgkmcnt(0)\n\t" "v_max3_u32 v225, v228, v229, v230\n\t" "v_max_u32 v225, v225, v231\n\t" "ds_read_b128 v[228:231], %[l1] offset:13312\n\t" \
    "s_waitcnt lgkmcnt(0)\n\t" "v_max3_u32 v225, v225, v228, v229\n\t" "v_max3_u32 v225, v225, v230, v231\n\t" "ds_read_b128 v[228:231], %[l1] offset:14336\n\t" \
    "s_waitcnt lgkmcnt(0)\n\t" "v_max3_u32 v225, v225, v228, v229\n\t" "v_max3_u32 v225, v225, v230, v231\n\t" "ds_read_b128 v[228:231], %[l1] offset:15360\n\t" \
    "s_waitcnt lgkmcnt(0)\n\t" "v_max3_u32 v225, v225, v228, v229\n\t" "v_max3_u32 v225, v225, v230, v231\n\t" "v_cmp_eq_u32 vcc, -1, v225\n\t" "s_cbranch_vccz 42b\n\t" \
    "s_cmp_eq_u32 %[rt], 1\n\t" "s_cbranch_scc1 42b\n\t" "v_mov_b32 v225, %[rt]\n\t" "ds_write_b32 v227, v225\n\t" "s_waitcnt lgkmcnt(0)\n\t" "s_branch 42b\n\t" "43:\n\t" \
    "s_sub_u32 %[rt], %[rt], 1\n\t" "s_waitcnt lgkmcnt(0)\n\t" XCNT(100) "44:\n\t" "buffer_load_dwordx4 v[240:243], %[mo], %[rs], %[sb] offen sc1\n\t" \
    "buffer_load_dwordx4 v[244:247], v224, %[rs], %[sb] offen sc1\n\t" "buffer_load_dwordx4 v[248:251], %[mo], %[rs], s60 offen sc1\n\t" \
    "buffer_load_dwordx4 v[252:255], v224, %[rs], s60 offen sc1\n\t" "s_waitcnt vmcnt(0)\n\t" "v_max_u32 v225, v240, v241\n\t" "v_max3_u32 v225, v225, v242, v243\n\t" \
    "v_max3_u32 v225, v225, v244, v245\n\t" "v_max3_u32 v225, v225, v246, v247\n\t" "v_max3_u32 v225, v225, v248, v249\n\t" "v_max3_u32 v225, v225, v250, v251\n\t" \
    "v_max3_u32 v225, v225, v252, v253\n\t" "v_max3_u32 v225, v225, v254, v255\n\t" "v_cmp_eq_u32 vcc, -1, v225\n\t" "s_cbranch_vccz 45f\n\t" "s_cmp_eq_u32 %[bd], 0\n\t" \
    "s_cbranch_scc1 45f\n\t" "s_sub_u32 %[bd], %[bd], 1\n\t" XCNT(101) "s_branch 44b\n\t" "45:\n\t" "ds_read_b128 v[160:163], %[l0] offset:0\n\t" \
    "ds_read_b128 v[164:167], %[l0] offset:1024\n\t" "ds_read_b128 v[168:171], %[l0] offset:2048\n\t" "ds_read_b128 v[172:175], %[l0] offset:3072\n\t" \
    "ds_read_b128 v[176:179], %[l0] offset:4096\n\t" "ds_read_b128 v[180:183], %[l0] offset:5120\n\t" "ds_read_b128 v[184:187], %[l0] offset:6144\n\t" \
    "ds_read_b128 v[188:191], %[l0] offset:7168\n\t" "v_mov_b64 v[232:233], 0\n\t" "v_mov_b64 v[234:235], 0\n\t" "v_mov_b64 v[236:237], 0\n\t" "v_mov_b64 v[238:239], 0\n\t" \
    "s_barrier\n\t" "s_branch 40b\n\t" "99:\n\t"
#define Y_STEP \
    ".p2align 6\n\t" "v_xor_b32 v128, 0, %[u0]\n\t" "v_xor_b32 v132, 0, %[pm]\n\t" "v_xor_b32 v129, 2048, %[u0]\n\t" "v_xor_b32 v133, 1024, %[pm]\n\t" \
    "v_xor_b32 v130, 4096, %[u0]\n\t" "v_xor_b32 v134, 2048, %[pm]\n\t" "v_xor_b32 v131, 6144, %[u0]\n\t" "v_xor_b32 v135, 3072, %[pm]\n\t" "s_getpc_b64 s[62:63]\n\t" \
    "11:\n\t" "s_memtime s[64:65]\n\t" "s_waitcnt lgkmcnt(0)\n\t" "s_sub_u32 s61, %[tu], s64\n\t" "s_cmp_gt_i32 s61, 124\n\t" "s_cbranch_scc1 11b\n\t" \
    "s_max_i32 s61, s61, 0\n\t" "s_and_b32 s61, s61, 124\n\t" "s_sub_u32 s61, 12f-11b, s61\n\t" "s_add_u32 s62, s62, s61\n\t" "s_addc_u32 s63, s63, 0\n\t" \
    "s_setpc_b64 s[62:63]\n\t" ".rept 31\n\t" "s_nop 0\n\t" ".endr\n\t" "12:\n\t" XT(90, 91) "buffer_load_dwordx4 v[144:147], v128, %[rs], %[ub] offen offset:0 sc1\n\t" \
    "buffer_load_dwordx4 v[148:151], v128, %[rs], %[ub] offen offset:1024 sc1\n\t" "buffer_load_dwordx4 v[152:155], v129, %[rs], %[ub] offen offset:0 sc1\n\t" \
    "buffer_load_dwordx4 v[156:159], v129, %[rs], %[ub] offen offset:1024 sc1\n\t" "s_cmp_lg_u32 %[late], 0\n\t" "s_cbranch_scc1 14f\n\t" "s_getpc_b64 s[62:63]\n\t" "13:\n\t" \
    "s_memtime s[64:65]\n\t" "s_waitcnt lgkmcnt(0)\n\t" "s_sub_u32 s61, %[tg], s64\n\t" "s_cmp_gt_i32 s61, 124\n\t" "s_cbranch_scc1 13b\n\t" "s_max_i32 s61, s61, 0\n\t" \
    "s_and_b32 s61, s61, 124\n\t" "s_sub_u32 s61, 14f-13b, s61\n\t" "s_add_u32 s62, s62, s61\n\t" "s_addc_u32 s63, s63, 0\n\t" "s_setpc_b64 s[62:63]\n\t" ".rept 31\n\t" \
    "s_nop 0\n\t" ".endr\n\t" "14:\n\t" XT(92, 93) "20:\n\t" "buffer_load_dwordx4 v[160:163], v130, %[rs], %[ub] offen offset:0 sc1\n\t" \
    "buffer_load_dwordx4 v[164:167], v130, %[rs], %[ub] offen offset:1024 sc1\n\t" "buffer_load_dwordx4 v[192:195], %[x0], %[rs], %[pb] offen offset:0 sc1\n\t" \
    "buffer_load_dwordx4 v[196:199], %[x0], %[rs], %[pb] offen offset:1024 sc1\n\t" "buffer_load_dwordx4 v[200:203], %[x0], %[rs], %[pb] offen offset:2048 sc1\n\t" \
    "buffer_load_dwordx4 v[204:207], %[x0], %[rs], %[pb] offen offset:3072 sc1\n\t" "buffer_load_dwordx4 v[176:179], v132, %[rs], %[pb] offen sc1\n\t" \
    "v_mov_b64 v[120:121], 0\n\t" "v_mov_b64 v[122:123], 0\n\t" "v_mov_b64 v[136:137], 0\n\t" "v_mov_b64 v[138:139], 0\n\t" \
    "buffer_load_dwordx4 v[208:211], %[x1], %[rs], %[pb] offen offset:0 sc1\n\t" "buffer_load_dwordx4 v[212:215], %[x1], %[rs], %[pb] offen offset:1024 sc1\n\t" \
    "buffer_load_dwordx4 v[216:219], %[x1], %[rs], %[pb] offen offset:2048 sc1\n\t" "buffer_load_dwordx4 v[220:223], %[x1], %[rs], %[pb] offen offset:3072 sc1\n\t" \
    "buffer_load_dwordx4 v[180:183], v133, %[rs], %[pb] offen sc1\n\t" "buffer_load_dwordx4 v[224:227], %[x2], %[rs], %[pb] offen offset:0 sc1\n\t" \
    "buffer_load_dwordx4 v[228:231], %[x2], %[rs], %[pb] offen offset:1024 sc1\n\t" "buffer_load_dwordx4 v[232:235], %[x2], %[rs], %[pb] offen offset:2048 sc1\n\t" \
    "buffer_load_dwordx4 v[236:239], %[x2], %[rs], %[pb] offen offset:3072 sc1\n\t" "buffer_load_dwordx4 v[184:187], v134, %[rs], %[pb] offen sc1\n\t" \
    "buffer_load_dwordx4 v[168:171], v131, %[rs], %[ub] offen offset:0 sc1\n\t" "buffer_load_dwordx4 v[172:175], v131, %[rs], %[ub] offen offset:1024 sc1\n\t" \
    "buffer_load_dwordx4 v[240:243], %[x3], %[rs], %[pb] offen offset:0 sc1\n\t" "buffer_load_dwordx4 v[244:247], %[x3], %[rs], %[pb] offen offset:1024 sc1\n\t" \
    "buffer_load_dwordx4 v[248:251], %[x3], %[rs], %[pb] offen offset:2048 sc1\n\t" "buffer_load_dwordx4 v[252:255], %[x3], %[rs], %[pb] offen offset:3072 sc1\n\t" \
    "buffer_load_dwordx4 v[188:191], v135, %[rs], %[pb] offen sc1\n\t" "buffer_load_dword %[sv], %[so], %[rs], %[pb] offen sc1\n\t" "v_mov_b32 v127, -1\n\t" \
    "s_waitcnt vmcnt(25)\n\t" "v_permlane16_swap_b32 v144, v145\n\t" "v_permlane16_swap_b32 v146, v147\n\t" "v_permlane16_swap_b32 v148, v149\n\t" \
    "v_permlane16_swap_b32 v150, v151\n\t" "v_permlane16_swap_b32 v152, v153\n\t" "v_permlane16_swap_b32 v154, v155\n\t" "v_permlane16_swap_b32 v156, v157\n\t" \
    "v_permlane16_swap_b32 v158, v159\n\t" "s_waitcnt vmcnt(22)\n\t" XT(94, 95) "v_smfmac_f32_16x16x64_bf16 v[136:139], v[192:195], a[0:7], %[ix]\n\t" \
    "s_waitcnt vmcnt(21)\n\t" "v_smfmac_f32_16x16x64_bf16 v[120:123], v[196:199], a[8:15], %[ix]\n\t" "v_permlane16_swap_b32 v160, v161\n\t" "s_waitcnt vmcnt(20)\n\t" \
    "v_smfmac_f32_16x16x64_bf16 v[136:139], v[200:203], a[16:23], %[ix]\n\t" "s_waitcnt vmcnt(19)\n\t" \
    "v_smfmac_f32_16x16x64_bf16 v[120:123], v[204:207], a[24:31], %[ix]\n\t" "v_permlane16_swap_b32 v162, v163\n\t" "s_waitcnt vmcnt(18)\n\t" \
    "v_smfmac_f32_16x16x64_bf16 v[136:139], v[176:179], v[144:151], %[ix]\n\t" "s_waitcnt vmcnt(17)\n\t" \
    "v_smfmac_f32_16x16x64_bf16 v[120:123], v[208:211], a[32:39], %[ix]\n\t" "s_waitcnt vmcnt(16)\n\t" \
    "v_smfmac_f32_16x16x64_bf16 v[136:139], v[212:215], a[40:47], %[ix]\n\t" "v_permlane16_swap_b32 v164, v165\n\t" "s_waitcnt vmcnt(15)\n\t" \
    "v_smfmac_f32_16x16x64_bf16 v[120:123], v[216:219], a[48:55], %[ix]\n\t" "s_waitcnt vmcnt(14)\n\t" \
    "v_smfmac_f32_16x16x64_bf16 v[136:139], v[220:223], a[56:63], %[ix]\n\t" "v_permlane16_swap_b32 v166, v167\n\t" "s_waitcnt vmcnt(13)\n\t" \
    "v_smfmac_f32_16x16x64_bf16 v[120:123], v[180:183], v[152:159], %[ix]\n\t" "s_waitcnt vmcnt(12)\n\t" \
    "v_smfmac_f32_16x16x64_bf16 v[136:139], v[224:227], a[64:71], %[ix]\n\t" "s_waitcnt vmcnt(11)\n\t" \
    "v_smfmac_f32_16x16x64_bf16 v[120:123], v[228:231], a[72:79], %[ix]\n\t" "s_waitcnt vmcnt(10)\n\t" \
    "v_smfmac_f32_16x16x64_bf16 v[136:139], v[232:235], a[80:87], %[ix]\n\t" "s_waitcnt vmcnt(9)\n\t" "v_smfmac_f32_16x16x64_bf16 v[120:123], v[236:239], a[88:95], %[ix]\n\t" \
    "s_waitcnt vmcnt(8)\n\t" "v_smfmac_f32_16x16x64_bf16 v[136:139], v[184:187], v[160:167], %[ix]\n\t" "s_waitcnt vmcnt(5)\n\t" \
    "v_smfmac_f32_16x16x64_bf16 v[120:123], v[240:243], a[96:103], %[ix]\n\t" "v_permlane16_swap_b32 v168, v169\n\t" "v_permlane16_swap_b32 v170, v171\n\t" \
    "s_waitcnt vmcnt(4)\n\t" "v_smfmac_f32_16x16x64_bf16 v[136:139], v[244:247], a[104:111], %[ix]\n\t" "v_permlane16_swap_b32 v172, v173\n\t" \
    "v_permlane16_swap_b32 v174, v175\n\t" "s_waitcnt vmcnt(3)\n\t" "v_smfmac_f32_16x16x64_bf16 v[120:123], v[248:251], a[112:119], %[ix]\n\t" "s_waitcnt vmcnt(2)\n\t" \
    "v_smfmac_f32_16x16x64_bf16 v[120:123], v[252:255], a[120:127], %[ix]\n\t" "s_waitcnt vmcnt(1)\n\t" \
    "v_smfmac_f32_16x16x64_bf16 v[120:123], v[188:191], v[168:175], %[ix]\n\t" XT(82, 83) "v_add_f32 v136, v136, v137\n\t" "v_add_f32 v138, v138, v139\n\t" "s_nop 5\n\t" \
    "v_add_f32 v120, v120, v121\n\t" "v_add_f32 v122, v122, v123\n\t" "v_add_f32 v120, v120, v136\n\t" "v_add_f32 v122, v122, v138\n\t" \
    "v_cvt_pk_bf16_f32 v124, v120, v122\n\t" "v_cmp_u_f32 vcc, v120, v122\n\t" "s_cbranch_vccnz 30f\n\t" "21:\n\t" \
    "v_mov_b32_dpp v125, v124 quad_perm:[1,0,3,2] row_mask:0xf bank_mask:0xf\n\t" "v_perm_b32 v124, v125, v124, %[ps]\n\t" \
    "buffer_store_dword v124, %[mw], %[rs], %[mb] offen sc1\n\t" XT(84, 85) "buffer_store_dword v127, %[mw], %[rs], %[mr] offen sc1\n\t" "s_memtime s[64:65]\n\t" \
    "s_barrier\n\t" XT(98, 99) "v_permlane16_swap_b32 v144, v145\n\t" "v_permlane16_swap_b32 v146, v147\n\t" "v_permlane16_swap_b32 v148, v149\n\t" \
    "v_permlane16_swap_b32 v150, v151\n\t" "v_permlane16_swap_b32 v152, v153\n\t" "v_permlane16_swap_b32 v154, v155\n\t" "v_permlane16_swap_b32 v156, v157\n\t" \
    "v_permlane16_swap_b32 v158, v159\n\t" "v_permlane16_swap_b32 v160, v161\n\t" "v_permlane16_swap_b32 v162, v163\n\t" "v_permlane16_swap_b32 v164, v165\n\t" \
    "v_permlane16_swap_b32 v166, v167\n\t" "v_permlane16_swap_b32 v168, v169\n\t" "v_permlane16_swap_b32 v170, v171\n\t" "v_permlane16_swap_b32 v172, v173\n\t" \
    "v_permlane16_swap_b32 v174, v175\n\t" "s_waitcnt lgkmcnt(0)\n\t" "s_and_b32 %[bad], s64, -2\n\t" "s_branch 40f\n\t" "30:\n\t" "s_cmp_lg_u32 %[late], 0\n\t" \
    "s_cbranch_scc1 32f\n\t" XCNT(100) "s_mov_b32 %[bad], 1\n\t" "s_waitcnt vmcnt(0)\n\t" "s_branch 40f\n\t" "32:\n\t" "v_max_u32 v126, v144, v145\n\t" \
    "v_max3_u32 v126, v126, v146, v147\n\t" "v_max3_u32 v126, v126, v148, v149\n\t" "v_max3_u32 v126, v126, v150, v151\n\t" "v_max3_u32 v126, v126, v152, v153\n\t" \
    "v_max3_u32 v126, v126, v154, v155\n\t" "v_max3_u32 v126, v126, v156, v157\n\t" "v_max3_u32 v126, v126, v158, v159\n\t" "v_max3_u32 v126, v126, v160, v161\n\t" \
    "v_max3_u32 v126, v126, v162, v163\n\t" "v_max3_u32 v126, v126, v164, v165\n\t" "v_max3_u32 v126, v126, v166, v167\n\t" "v_max3_u32 v126, v126, v168, v169\n\t" \
    "v_max3_u32 v126, v126, v170, v171\n\t" "v_max3_u32 v126, v126, v172, v173\n\t" "v_max3_u32 v126, v126, v174, v175\n\t" UCNT "v_max3_u32 v126, v126, v176, v177\n\t" \
    "v_max3_u32 v126, v126, v178, v179\n\t" "v_max3_u32 v126, v126, v180, v181\n\t" "v_max3_u32 v126, v126, v182, v183\n\t" "v_max3_u32 v126, v126, v184, v185\n\t" \
    "v_max3_u32 v126, v126, v186, v187\n\t" "v_max3_u32 v126, v126, v188, v189\n\t" "v_max3_u32 v126, v126, v190, v191\n\t" "v_max3_u32 v126, v126, v192, v193\n\t" \
    "v_max3_u32 v126, v126, v194, v195\n\t" "v_max3_u32 v126, v126, v196, v197\n\t" "v_max3_u32 v126, v126, v198, v199\n\t" "v_max3_u32 v126, v126, v200, v201\n\t" \
    "v_max3_u32 v126, v126, v202, v203\n\t" "v_max3_u32 v126, v126, v204, v205\n\t" "v_max3_u32 v126, v126, v206, v207\n\t" "v_max3_u32 v126, v126, v208, v209\n\t" \
    "v_max3_u32 v126, v126, v210, v211\n\t" "v_max3_u32 v126, v126, v212, v213\n\t" "v_max3_u32 v126, v126, v214, v215\n\t" "v_max3_u32 v126, v126, v216, v217\n\t" \
    "v_max3_u32 v126, v126, v218, v219\n\t" "v_max3_u32 v126, v126, v220, v221\n\t" "v_max3_u32 v126, v126, v222, v223\n\t" "v_max3_u32 v126, v126, v224, v225\n\t" \
    "v_max3_u32 v126, v126, v226, v227\n\t" "v_max3_u32 v126, v126, v228, v229\n\t" "v_max3_u32 v126, v126, v230, v231\n\t" "v_max3_u32 v126, v126, v232, v233\n\t" \
    "v_max3_u32 v126, v126, v234, v235\n\t" "v_max3_u32 v126, v126, v236, v237\n\t" "v_max3_u32 v126, v126, v238, v239\n\t" "v_max3_u32 v126, v126, v240, v241\n\t" \
    "v_max3_u32 v126, v126, v242, v243\n\t" "v_max3_u32 v126, v126, v244, v245\n\t" "v_max3_u32 v126, v126, v246, v247\n\t" "v_max3_u32 v126, v126, v248, v249\n\t" \
    "v_max3_u32 v126, v126, v250, v251\n\t" "v_max3_u32 v126, v126, v252, v253\n\t" "v_max3_u32 v126, v126, v254, v255\n\t" "v_cmp_eq_u32 vcc, -1, v126\n\t" \
    "s_cbranch_vccz 21b\n\t" "s_cmp_eq_u32 %[bd], 0\n\t" "s_cbranch_scc1 21b\n\t" XCNT(100) "s_mov_b32 %[bad], 1\n\t" "s_waitcnt vmcnt(0)\n\t" "40:\n\t"
#define G_LOOP \
    ".p2align 6\n\t" "v_xor_b32 v72, 0, %[xo]\n\t" "v_add_u32 v80, 0x10000, v72\n\t" "v_xor_b32 v88, 0, %[xs]\n\t" "v_add_u32 v88, 0, v88\n\t" "v_xor_b32 v73, 64, %[xo]\n\t" \
    "v_add_u32 v81, 0x10000, v73\n\t" "v_xor_b32 v89, 16, %[xs]\n\t" "v_add_u32 v89, 2048, v89\n\t" "v_xor_b32 v74, 128, %[xo]\n\t" "v_add_u32 v82, 0x10000, v74\n\t" \
    "v_xor_b32 v90, 32, %[xs]\n\t" "v_add_u32 v90, 4096, v90\n\t" "v_xor_b32 v75, 192, %[xo]\n\t" "v_add_u32 v83, 0x10000, v75\n\t" "v_xor_b32 v91, 48, %[xs]\n\t" \
    "v_add_u32 v91, 6144, v91\n\t" "v_xor_b32 v76, 256, %[xo]\n\t" "v_add_u32 v84, 0x10000, v76\n\t" "v_xor_b32 v92, 64, %[xs]\n\t" "v_add_u32 v92, 8192, v92\n\t" \
    "v_xor_b32 v77, 320, %[xo]\n\t" "v_add_u32 v85, 0x10000, v77\n\t" "v_xor_b32 v93, 80, %[xs]\n\t" "v_add_u32 v93, 10240, v93\n\t" "v_xor_b32 v78, 384, %[xo]\n\t" \
    "v_add_u32 v86, 0x10000, v78\n\t" "v_xor_b32 v94, 96, %[xs]\n\t" "v_add_u32 v94, 12288, v94\n\t" "v_xor_b32 v79, 448, %[xo]\n\t" "v_add_u32 v87, 0x10000, v79\n\t" \
    "v_xor_b32 v95, 112, %[xs]\n\t" "v_add_u32 v95, 14336, v95\n\t" "v_mov_b32 v96, 2.0\n\t" "s_mov_b32 s60, 0\n\t" "s_mov_b32 s62, 0\n\t" "s_waitcnt vmcnt(0)\n\t" \
    "s_barrier\n\t" "ds_read_b128 v[100:103], v72 offset:0\n\t" "ds_read_b128 v[104:107], v72 offset:32768\n\t" "ds_read_b128 v[108:111], v73 offset:0\n\t" \
    "ds_read_b128 v[112:115], v73 offset:32768\n\t" "ds_read_b128 v[116:119], v74 offset:0\n\t" "10:\n\t" XT(80, 81) "s_waitcnt lgkmcnt(4)\n\t" \
    "v_mfma_f32_16x16x32_bf16 v[32:35], a[0:3], v[100:103], 0\n\t" "ds_read_b128 v[120:123], v74 offset:32768\n\t" "s_add_u32 s61, s60, 1\n\t" "s_and_b32 s61, s61, 63\n\t" \
    "v_mfma_f32_16x16x32_bf16 v[36:39], a[4:7], v[100:103], 0\n\t" "s_lshl_b32 s61, s61, 16\n\t" "s_add_u32 s63, %[lw], 65536\n\t" "s_waitcnt lgkmcnt(4)\n\t" \
    "v_mfma_f32_16x16x32_bf16 v[40:43], a[0:3], v[104:107], 0\n\t" "ds_read_b128 v[124:127], v75 offset:0\n\t" "v_mfma_f32_16x16x32_bf16 v[44:47], a[4:7], v[104:107], 0\n\t" \
    "s_waitcnt lgkmcnt(4)\n\t" "v_mfma_f32_16x16x32_bf16 v[32:35], a[8:11], v[108:111], v[32:35]\n\t" "ds_read_b128 v[128:131], v75 offset:32768\n\t" \
    "v_cvt_pk_bf16_f32 v64, v48, v49\n\t" "v_mfma_f32_16x16x32_bf16 v[36:39], a[12:15], v[108:111], v[36:39]\n\t" "v_cvt_pk_bf16_f32 v65, v50, v51\n\t" \
    "v_cvt_pk_bf16_f32 v66, v52, v53\n\t" "s_waitcnt lgkmcnt(4)\n\t" "v_mfma_f32_16x16x32_bf16 v[40:43], a[8:11], v[112:115], v[40:43]\n\t" \
    "ds_read_b128 v[100:103], v76 offset:0\n\t" "v_cvt_pk_bf16_f32 v67, v54, v55\n\t" "v_mfma_f32_16x16x32_bf16 v[44:47], a[12:15], v[112:115], v[44:47]\n\t" \
    "v_cvt_pk_bf16_f32 v68, v56, v57\n\t" "v_cvt_pk_bf16_f32 v69, v58, v59\n\t" "s_waitcnt lgkmcnt(4)\n\t" \
    "v_mfma_f32_16x16x32_bf16 v[32:35], a[16:19], v[116:119], v[32:35]\n\t" "ds_read_b128 v[104:107], v76 offset:32768\n\t" "v_cvt_pk_bf16_f32 v70, v60, v61\n\t" \
    "v_mfma_f32_16x16x32_bf16 v[36:39], a[20:23], v[116:119], v[36:39]\n\t" "v_cvt_pk_bf16_f32 v71, v62, v63\n\t" \
    "buffer_store_dwordx4 v[64:67], v96, %[rs], s62 offen offset:0 sc1\n\t" "s_waitcnt lgkmcnt(4)\n\t" "v_mfma_f32_16x16x32_bf16 v[40:43], a[16:19], v[120:123], v[40:43]\n\t" \
    "ds_read_b128 v[108:111], v77 offset:0\n\t" "v_mfma_f32_16x16x32_bf16 v[44:47], a[20:23], v[120:123], v[44:47]\n\t" "s_waitcnt lgkmcnt(4)\n\t" \
    "v_mfma_f32_16x16x32_bf16 v[32:35], a[24:27], v[124:127], v[32:35]\n\t" "ds_read_b128 v[112:115], v77 offset:32768\n\t" \
    "v_mfma_f32_16x16x32_bf16 v[36:39], a[28:31], v[124:127], v[36:39]\n\t" "buffer_store_dwordx4 v[68:71], v96, %[rs], s62 offen offset:256 sc1\n\t" \
    "s_waitcnt lgkmcnt(4)\n\t" "v_mfma_f32_16x16x32_bf16 v[40:43], a[24:27], v[128:131], v[40:43]\n\t" "ds_read_b128 v[116:119], v78 offset:0\n\t" "s_mov_b32 m0, s63\n\t" \
    "v_mfma_f32_16x16x32_bf16 v[44:47], a[28:31], v[128:131], v[44:47]\n\t" "s_add_u32 s63, s63, 0x800\n\t" "buffer_load_dwordx4 v88, %[rx], s61 offen lds\n\t" \
    "s_waitcnt lgkmcnt(4)\n\t" "v_mfma_f32_16x16x32_bf16 v[32:35], a[32:35], v[100:103], v[32:35]\n\t" "ds_read_b128 v[120:123], v78 offset:32768\n\t" \
    "v_mfma_f32_16x16x32_bf16 v[36:39], a[36:39], v[100:103], v[36:39]\n\t" "buffer_load_dwordx4 v88, %[rx], s61 offen offset:1024 lds\n\t" "s_waitcnt lgkmcnt(4)\n\t" \
    "v_mfma_f32_16x16x32_bf16 v[40:43], a[32:35], v[104:107], v[40:43]\n\t" "ds_read_b128 v[124:127], v79 offset:0\n\t" "s_mov_b32 m0, s63\n\t" \
    "v_mfma_f32_16x16x32_bf16 v[44:47], a[36:39], v[104:107], v[44:47]\n\t" "s_add_u32 s63, s63, 0x800\n\t" "buffer_load_dwordx4 v89, %[rx], s61 offen lds\n\t" \
    "s_waitcnt lgkmcnt(4)\n\t" "v_mfma_f32_16x16x32_bf16 v[32:35], a[40:43], v[108:111], v[32:35]\n\t" "ds_read_b128 v[128:131], v79 offset:32768\n\t" \
    "v_mfma_f32_16x16x32_bf16 v[36:39], a[44:47], v[108:111], v[36:39]\n\t" "buffer_load_dwordx4 v89, %[rx], s61 offen offset:1024 lds\n\t" "s_waitcnt lgkmcnt(4)\n\t" \
    "v_mfma_f32_16x16x32_bf16 v[40:43], a[40:43], v[112:115], v[40:43]\n\t" "ds_read_b128 v[100:103], v72 offset:512\n\t" "s_mov_b32 m0, s63\n\t" \
    "v_mfma_f32_16x16x32_bf16 v[44:47], a[44:47], v[112:115], v[44:47]\n\t" "s_add_u32 s63, s63, 0x800\n\t" "buffer_load_dwordx4 v90, %[rx], s61 offen lds\n\t" \
    "s_waitcnt lgkmcnt(4)\n\t" "v_mfma_f32_16x16x32_bf16 v[32:35], a[48:51], v[116:119], v[32:35]\n\t" "ds_read_b128 v[104:107], v72 offset:33280\n\t" \
    "v_mfma_f32_16x16x32_bf16 v[36:39], a[52:55], v[116:119], v[36:39]\n\t" "buffer_load_dwordx4 v90, %[rx], s61 offen offset:1024 lds\n\t" "s_waitcnt lgkmcnt(4)\n\t" \
    "v_mfma_f32_16x16x32_bf16 v[40:43], a[48:51], v[120:123], v[40:43]\n\t" "ds_read_b128 v[108:111], v73 offset:512\n\t" "s_mov_b32 m0, s63\n\t" \
    "v_mfma_f32_16x16x32_bf16 v[44:47], a[52:55], v[120:123], v[44:47]\n\t" "s_add_u32 s63, s63, 0x800\n\t" "buffer_load_dwordx4 v91, %[rx], s61 offen lds\n\t" \
    "s_waitcnt lgkmcnt(4)\n\t" "v_mfma_f32_16x16x32_bf16 v[32:35], a[56:59], v[124:127], v[32:35]\n\t" "ds_read_b128 v[112:115], v73 offset:33280\n\t" \
    "v_mfma_f32_16x16x32_bf16 v[36:39], a[60:63], v[124:127], v[36:39]\n\t" "buffer_load_dwordx4 v91, %[rx], s61 offen offset:1024 lds\n\t" "s_waitcnt lgkmcnt(4)\n\t" \
    "v_mfma_f32_16x16x32_bf16 v[40:43], a[56:59], v[128:131], v[40:43]\n\t" "ds_read_b128 v[116:119], v74 offset:512\n\t" "s_mov_b32 m0, s63\n\t" \
    "v_mfma_f32_16x16x32_bf16 v[44:47], a[60:63], v[128:131], v[44:47]\n\t" "s_add_u32 s63, s63, 0x800\n\t" "buffer_load_dwordx4 v92, %[rx], s61 offen lds\n\t" \
    "s_waitcnt lgkmcnt(4)\n\t" "v_mfma_f32_16x16x32_bf16 v[32:35], a[64:67], v[100:103], v[32:35]\n\t" "ds_read_b128 v[120:123], v74 offset:33280\n\t" \
    "v_mfma_f32_16x16x32_bf16 v[36:39], a[68:71], v[100:103], v[36:39]\n\t" "buffer_load_dwordx4 v92, %[rx], s61 offen offset:1024 lds\n\t" "s_waitcnt lgkmcnt(4)\n\t" \
    "v_mfma_f32_16x16x32_bf16 v[40:43], a[64:67], v[104:107], v[40:43]\n\t" "ds_read_b128 v[124:127], v75 offset:512\n\t" "s_mov_b32 m0, s63\n\t" \
    "v_mfma_f32_16x16x32_bf16 v[44:47], a[68:71], v[104:107], v[44:47]\n\t" "s_add_u32 s63, s63, 0x800\n\t" "buffer_load_dwordx4 v93, %[rx], s61 offen lds\n\t" \
    "s_waitcnt lgkmcnt(4)\n\t" "v_mfma_f32_16x16x32_bf16 v[32:35], a[72:75], v[108:111], v[32:35]\n\t" "ds_read_b128 v[128:131], v75 offset:33280\n\t" \
    "v_mfma_f32_16x16x32_bf16 v[36:39], a[76:79], v[108:111], v[36:39]\n\t" "buffer_load_dwordx4 v93, %[rx], s61 offen offset:1024 lds\n\t" "s_waitcnt lgkmcnt(4)\n\t" \
    "v_mfma_f32_16x16x32_bf16 v[40:43], a[72:75], v[112:115], v[40:43]\n\t" "ds_read_b128 v[100:103], v76 offset:512\n\t" "s_mov_b32 m0, s63\n\t" \
    "v_mfma_f32_16x16x32_bf16 v[44:47], a[76:79], v[112:115], v[44:47]\n\t" "s_add_u32 s63, s63, 0x800\n\t" "buffer_load_dwordx4 v94, %[rx], s61 offen lds\n\t" \
    "s_waitcnt lgkmcnt(4)\n\t" "v_mfma_f32_16x16x32_bf16 v[32:35], a[80:83], v[116:119], v[32:35]\n\t" "ds_read_b128 v[104:107], v76 offset:33280\n\t" \
    "v_mfma_f32_16x16x32_bf16 v[36:39], a[84:87], v[116:119], v[36:39]\n\t" "buffer_load_dwordx4 v94, %[rx], s61 offen offset:1024 lds\n\t" "s_waitcnt lgkmcnt(4)\n\t" \
    "v_mfma_f32_16x16x32_bf16 v[40:43], a[80:83], v[120:123], v[40:43]\n\t" "ds_read_b128 v[108:111], v77 offset:512\n\t" "s_mov_b32 m0, s63\n\t" \
    "v_mfma_f32_16x16x32_bf16 v[44:47], a[84:87], v[120:123], v[44:47]\n\t" "s_add_u32 s63, s63, 0x800\n\t" "buffer_load_dwordx4 v95, %[rx], s61 offen lds\n\t" \
    "s_waitcnt lgkmcnt(4)\n\t" "v_mfma_f32_16x16x32_bf16 v[32:35], a[88:91], v[124:127], v[32:35]\n\t" "ds_read_b128 v[112:115], v77 offset:33280\n\t" \
    "v_mfma_f32_16x16x32_bf16 v[36:39], a[92:95], v[124:127], v[36:39]\n\t" "buffer_load_dwordx4 v95, %[rx], s61 offen offset:1024 lds\n\t" "s_waitcnt lgkmcnt(4)\n\t" \
    "v_mfma_f32_16x16x32_bf16 v[40:43], a[88:91], v[128:131], v[40:43]\n\t" "ds_read_b128 v[116:119], v78 offset:512\n\t" \
    "v_mfma_f32_16x16x32_bf16 v[44:47], a[92:95], v[128:131], v[44:47]\n\t" "s_waitcnt lgkmcnt(4)\n\t" "v_mfma_f32_16x16x32_bf16 v[32:35], a[96:99], v[100:103], v[32:35]\n\t" \
    "ds_read_b128 v[120:123], v78 offset:33280\n\t" "s_lshr_b32 s64, s60, 2\n\t" "s_xor_b32 s64, s64, %[xp]\n\t" \
    "v_mfma_f32_16x16x32_bf16 v[36:39], a[100:103], v[100:103], v[36:39]\n\t" "s_and_b32 s64, s64, 1\n\t" "s_lshl_b32 s64, s64, 13\n\t" "s_waitcnt lgkmcnt(4)\n\t" \
    "v_mfma_f32_16x16x32_bf16 v[40:43], a[96:99], v[104:107], v[40:43]\n\t" "ds_read_b128 v[124:127], v79 offset:512\n\t" "s_lshl_b32 s62, s60, 19\n\t" \
    "v_mfma_f32_16x16x32_bf16 v[44:47], a[100:103], v[104:107], v[44:47]\n\t" "s_add_u32 s62, s62, s64\n\t" "s_add_u32 s62, s62, %[ob]\n\t" "s_waitcnt lgkmcnt(4)\n\t" \
    "v_mfma_f32_16x16x32_bf16 v[32:35], a[104:107], v[108:111], v[32:35]\n\t" "ds_read_b128 v[128:131], v79 offset:33280\n\t" \
    "v_mfma_f32_16x16x32_bf16 v[36:39], a[108:111], v[108:111], v[36:39]\n\t" "s_waitcnt lgkmcnt(4)\n\t" \
    "v_mfma_f32_16x16x32_bf16 v[40:43], a[104:107], v[112:115], v[40:43]\n\t" "ds_read_b128 v[100:103], v72 offset:1024\n\t" \
    "v_mfma_f32_16x16x32_bf16 v[44:47], a[108:111], v[112:115], v[44:47]\n\t" "s_waitcnt lgkmcnt(4)\n\t" \
    "v_mfma_f32_16x16x32_bf16 v[32:35], a[112:115], v[116:119], v[32:35]\n\t" "ds_read_b128 v[104:107], v72 offset:33792\n\t" \
    "v_mfma_f32_16x16x32_bf16 v[36:39], a[116:119], v[116:119], v[36:39]\n\t" "s_waitcnt lgkmcnt(4)\n\t" \
    "v_mfma_f32_16x16x32_bf16 v[40:43], a[112:115], v[120:123], v[40:43]\n\t" "ds_read_b128 v[108:111], v73 offset:1024\n\t" \
    "v_mfma_f32_16x16x32_bf16 v[44:47], a[116:119], v[120:123], v[44:47]\n\t" "s_waitcnt lgkmcnt(4)\n\t" \
    "v_mfma_f32_16x16x32_bf16 v[32:35], a[120:123], v[124:127], v[32:35]\n\t" "ds_read_b128 v[112:115], v73 offset:33792\n\t" \
    "v_mfma_f32_16x16x32_bf16 v[36:39], a[124:127], v[124:127], v[36:39]\n\t" "s_waitcnt lgkmcnt(4)\n\t" \
    "v_mfma_f32_16x16x32_bf16 v[40:43], a[120:123], v[128:131], v[40:43]\n\t" "ds_read_b128 v[116:119], v74 offset:1024\n\t" \
    "v_mfma_f32_16x16x32_bf16 v[44:47], a[124:127], v[128:131], v[44:47]\n\t" "s_waitcnt lgkmcnt(4)\n\t" \
    "v_mfma_f32_16x16x32_bf16 v[32:35], a[128:131], v[100:103], v[32:35]\n\t" "ds_read_b128 v[120:123], v74 offset:33792\n\t" \
    "v_mfma_f32_16x16x32_bf16 v[36:39], a[132:135], v[100:103], v[36:39]\n\t" "s_waitcnt lgkmcnt(4)\n\t" \
    "v_mfma_f32_16x16x32_bf16 v[40:43], a[128:131], v[104:107], v[40:43]\n\t" "ds_read_b128 v[124:127], v75 offset:1024\n\t" \
    "v_mfma_f32_16x16x32_bf16 v[44:47], a[132:135], v[104:107], v[44:47]\n\t" "s_waitcnt lgkmcnt(4)\n\t" \
    "v_mfma_f32_16x16x32_bf16 v[32:35], a[136:139], v[108:111], v[32:35]\n\t" "ds_read_b128 v[128:131], v75 offset:33792\n\t" "s_waitcnt vmcnt(16)\n\t" "s_barrier\n\t" \
    "v_mfma_f32_16x16x32_bf16 v[36:39], a[140:143], v[108:111], v[36:39]\n\t" "v_mov_b32 v98, s60\n\t" "buffer_store_dword v98, %[fo], %[rc], %[fl] offen sc1\n\t" \
    "s_waitcnt lgkmcnt(4)\n\t" "v_mfma_f32_16x16x32_bf16 v[40:43], a[136:139], v[112:115], v[40:43]\n\t" "ds_read_b128 v[100:103], v76 offset:1024\n\t" \
    "v_mfma_f32_16x16x32_bf16 v[44:47], a[140:143], v[112:115], v[44:47]\n\t" "s_waitcnt lgkmcnt(4)\n\t" \
    "v_mfma_f32_16x16x32_bf16 v[32:35], a[144:147], v[116:119], v[32:35]\n\t" "ds_read_b128 v[104:107], v76 offset:33792\n\t" \
    "v_mfma_f32_16x16x32_bf16 v[36:39], a[148:151], v[116:119], v[36:39]\n\t" "s_waitcnt lgkmcnt(4)\n\t" \
    "v_mfma_f32_16x16x32_bf16 v[40:43], a[144:147], v[120:123], v[40:43]\n\t" "ds_read_b128 v[108:111], v77 offset:1024\n\t" \
    "v_mfma_f32_16x16x32_bf16 v[44:47], a[148:151], v[120:123], v[44:47]\n\t" "s_waitcnt lgkmcnt(4)\n\t" \
    "v_mfma_f32_16x16x32_bf16 v[32:35], a[152:155], v[124:127], v[32:35]\n\t" "ds_read_b128 v[112:115], v77 offset:33792\n\t" \
    "v_mfma_f32_16x16x32_bf16 v[36:39], a[156:159], v[124:127], v[36:39]\n\t" "s_waitcnt lgkmcnt(4)\n\t" \
    "v_mfma_f32_16x16x32_bf16 v[40:43], a[152:155], v[128:131], v[40:43]\n\t" "ds_read_b128 v[116:119], v78 offset:1024\n\t" \
    "v_mfma_f32_16x16x32_bf16 v[44:47], a[156:159], v[128:131], v[44:47]\n\t" "s_waitcnt lgkmcnt(4)\n\t" \
    "v_mfma_f32_16x16x32_bf16 v[32:35], a[160:163], v[100:103], v[32:35]\n\t" "ds_read_b128 v[120:123], v78 offset:33792\n\t" \
    "v_mfma_f32_16x16x32_bf16 v[36:39], a[164:167], v[100:103], v[36:39]\n\t" "s_waitcnt lgkmcnt(4)\n\t" \
    "v_mfma_f32_16x16x32_bf16 v[40:43], a[160:163], v[104:107], v[40:43]\n\t" "ds_read_b128 v[124:127], v79 offset:1024\n\t" \
    "v_mfma_f32_16x16x32_bf16 v[44:47], a[164:167], v[104:107], v[44:47]\n\t" "s_waitcnt lgkmcnt(4)\n\t" \
    "v_mfma_f32_16x16x32_bf16 v[32:35], a[168:171], v[108:111], v[32:35]\n\t" "ds_read_b128 v[128:131], v79 offset:33792\n\t" \
    "v_mfma_f32_16x16x32_bf16 v[36:39], a[172:175], v[108:111], v[36:39]\n\t" "s_waitcnt lgkmcnt(4)\n\t" \
    "v_mfma_f32_16x16x32_bf16 v[40:43], a[168:171], v[112:115], v[40:43]\n\t" "ds_read_b128 v[100:103], v72 offset:1536\n\t" \
    "v_mfma_f32_16x16x32_bf16 v[44:47], a[172:175], v[112:115], v[44:47]\n\t" "s_waitcnt lgkmcnt(4)\n\t" \
    "v_mfma_f32_16x16x32_bf16 v[32:35], a[176:179], v[116:119], v[32:35]\n\t" "ds_read_b128 v[104:107], v72 offset:34304\n\t" \
    "v_mfma_f32_16x16x32_bf16 v[36:39], a[180:183], v[116:119], v[36:39]\n\t" "s_waitcnt lgkmcnt(4)\n\t" \
    "v_mfma_f32_16x16x32_bf16 v[40:43], a[176:179], v[120:123], v[40:43]\n\t" "ds_read_b128 v[108:111], v73 offset:1536\n\t" \
    "v_mfma_f32_16x16x32_bf16 v[44:47], a[180:183], v[120:123], v[44:47]\n\t" "s_waitcnt lgkmcnt(4)\n\t" \
    "v_mfma_f32_16x16x32_bf16 v[32:35], a[184:187], v[124:127], v[32:35]\n\t" "ds_read_b128 v[112:115], v73 offset:34304\n\t" \
    "v_mfma_f32_16x16x32_bf16 v[36:39], a[188:191], v[124:127], v[36:39]\n\t" "s_waitcnt lgkmcnt(4)\n\t" \
    "v_mfma_f32_16x16x32_bf16 v[40:43], a[184:187], v[128:131], v[40:43]\n\t" "ds_read_b128 v[116:119], v74 offset:1536\n\t" \
    "v_mfma_f32_16x16x32_bf16 v[44:47], a[188:191], v[128:131], v[44:47]\n\t" "s_waitcnt lgkmcnt(4)\n\t" \
    "v_mfma_f32_16x16x32_bf16 v[32:35], a[192:195], v[100:103], v[32:35]\n\t" "ds_read_b128 v[120:123], v74 offset:34304\n\t" \
    "v_mfma_f32_16x16x32_bf16 v[36:39], a[196:199], v[100:103], v[36:39]\n\t" "s_waitcnt lgkmcnt(4)\n\t" \
    "v_mfma_f32_16x16x32_bf16 v[40:43], a[192:195], v[104:107], v[40:43]\n\t" "ds_read_b128 v[124:127], v75 offset:1536\n\t" \
    "v_mfma_f32_16x16x32_bf16 v[44:47], a[196:199], v[104:107], v[44:47]\n\t" "s_waitcnt lgkmcnt(4)\n\t" \
    "v_mfma_f32_16x16x32_bf16 v[32:35], a[200:203], v[108:111], v[32:35]\n\t" "ds_read_b128 v[128:131], v75 offset:34304\n\t" \
    "v_mfma_f32_16x16x32_bf16 v[36:39], a[204:207], v[108:111], v[36:39]\n\t" "s_waitcnt lgkmcnt(4)\n\t" \
    "v_mfma_f32_16x16x32_bf16 v[40:43], a[200:203], v[112:115], v[40:43]\n\t" "ds_read_b128 v[100:103], v76 offset:1536\n\t" \
    "v_mfma_f32_16x16x32_bf16 v[44:47], a[204:207], v[112:115], v[44:47]\n\t" "s_waitcnt lgkmcnt(4)\n\t" \
    "v_mfma_f32_16x16x32_bf16 v[32:35], a[208:211], v[116:119], v[32:35]\n\t" "ds_read_b128 v[104:107], v76 offset:34304\n\t" \
    "v_mfma_f32_16x16x32_bf16 v[36:39], a[212:215], v[116:119], v[36:39]\n\t" "s_waitcnt lgkmcnt(4)\n\t" \
    "v_mfma_f32_16x16x32_bf16 v[40:43], a[208:211], v[120:123], v[40:43]\n\t" "ds_read_b128 v[108:111], v77 offset:1536\n\t" \
    "v_mfma_f32_16x16x32_bf16 v[44:47], a[212:215], v[120:123], v[44:47]\n\t" "s_waitcnt lgkmcnt(4)\n\t" \
    "v_mfma_f32_16x16x32_bf16 v[32:35], a[216:219], v[124:127], v[32:35]\n\t" "ds_read_b128 v[112:115], v77 offset:34304\n\t" \
    "v_mfma_f32_16x16x32_bf16 v[36:39], a[220:223], v[124:127], v[36:39]\n\t" "s_waitcnt lgkmcnt(4)\n\t" \
    "v_mfma_f32_16x16x32_bf16 v[40:43], a[216:219], v[128:131], v[40:43]\n\t" "ds_read_b128 v[116:119], v78 offset:1536\n\t" \
    "v_mfma_f32_16x16x32_bf16 v[44:47], a[220:223], v[128:131], v[44:47]\n\t" "s_waitcnt lgkmcnt(4)\n\t" \
    "v_mfma_f32_16x16x32_bf16 v[32:35], a[224:227], v[100:103], v[32:35]\n\t" "ds_read_b128 v[120:123], v78 offset:34304\n\t" \
    "v_mfma_f32_16x16x32_bf16 v[36:39], a[228:231], v[100:103], v[36:39]\n\t" "s_waitcnt lgkmcnt(4)\n\t" \
    "v_mfma_f32_16x16x32_bf16 v[40:43], a[224:227], v[104:107], v[40:43]\n\t" "ds_read_b128 v[124:127], v79 offset:1536\n\t" \
    "v_mfma_f32_16x16x32_bf16 v[44:47], a[228:231], v[104:107], v[44:47]\n\t" "s_waitcnt lgkmcnt(4)\n\t" \
    "v_mfma_f32_16x16x32_bf16 v[32:35], a[232:235], v[108:111], v[32:35]\n\t" "ds_read_b128 v[128:131], v79 offset:34304\n\t" \
    "v_mfma_f32_16x16x32_bf16 v[36:39], a[236:239], v[108:111], v[36:39]\n\t" "s_waitcnt lgkmcnt(4)\n\t" \
    "v_mfma_f32_16x16x32_bf16 v[40:43], a[232:235], v[112:115], v[40:43]\n\t" "v_mfma_f32_16x16x32_bf16 v[44:47], a[236:239], v[112:115], v[44:47]\n\t" \
    "s_waitcnt lgkmcnt(3)\n\t" "v_mfma_f32_16x16x32_bf16 v[32:35], a[240:243], v[116:119], v[32:35]\n\t" \
    "v_mfma_f32_16x16x32_bf16 v[36:39], a[244:247], v[116:119], v[36:39]\n\t" "s_waitcnt vmcnt(1) lgkmcnt(0)\n\t" "s_barrier\n\t" XT(82, 83) \
    "v_mfma_f32_16x16x32_bf16 v[40:43], a[240:243], v[120:123], v[40:43]\n\t" "ds_read_b128 v[100:103], v80 offset:0\n\t" \
    "v_mfma_f32_16x16x32_bf16 v[44:47], a[244:247], v[120:123], v[44:47]\n\t" "v_mfma_f32_16x16x32_bf16 v[32:35], a[248:251], v[124:127], v[32:35]\n\t" \
    "ds_read_b128 v[104:107], v80 offset:32768\n\t" "v_mfma_f32_16x16x32_bf16 v[36:39], a[252:255], v[124:127], v[36:39]\n\t" "ds_read_b128 v[108:111], v81 offset:0\n\t" \
    "v_mfma_f32_16x16x32_bf16 v[40:43], a[248:251], v[128:131], v[40:43]\n\t" "ds_read_b128 v[112:115], v81 offset:32768\n\t" "ds_read_b128 v[116:119], v82 offset:0\n\t" \
    "v_mfma_f32_16x16x32_bf16 v[44:47], a[252:255], v[128:131], v[44:47]\n\t" "s_add_u32 s60, s60, 1\n\t" GSUM "s_cmp_eq_u32 s60, 1\n\t" "s_cbranch_scc1 20f\n\t" "11:\n\t" \
    XT(80, 81) "s_waitcnt lgkmcnt(4)\n\t" "v_mfma_f32_16x16x32_bf16 v[48:51], a[0:3], v[100:103], 0\n\t" "ds_read_b128 v[120:123], v82 offset:32768\n\t" \
    "s_add_u32 s61, s60, 1\n\t" "s_and_b32 s61, s61, 63\n\t" "v_mfma_f32_16x16x32_bf16 v[52:55], a[4:7], v[100:103], 0\n\t" "s_lshl_b32 s61, s61, 16\n\t" \
    "s_add_u32 s63, %[lw], 0\n\t" "s_waitcnt lgkmcnt(4)\n\t" "v_mfma_f32_16x16x32_bf16 v[56:59], a[0:3], v[104:107], 0\n\t" "ds_read_b128 v[124:127], v83 offset:0\n\t" \
    "v_mfma_f32_16x16x32_bf16 v[60:63], a[4:7], v[104:107], 0\n\t" "s_waitcnt lgkmcnt(4)\n\t" "v_mfma_f32_16x16x32_bf16 v[48:51], a[8:11], v[108:111], v[48:51]\n\t" \
    "ds_read_b128 v[128:131], v83 offset:32768\n\t" "v_cvt_pk_bf16_f32 v64, v32, v33\n\t" "v_mfma_f32_16x16x32_bf16 v[52:55], a[12:15], v[108:111], v[52:55]\n\t" \
    "v_cvt_pk_bf16_f32 v65, v34, v35\n\t" "v_cvt_pk_bf16_f32 v66, v36, v37\n\t" "s_waitcnt lgkmcnt(4)\n\t" \
    "v_mfma_f32_16x16x32_bf16 v[56:59], a[8:11], v[112:115], v[56:59]\n\t" "ds_read_b128 v[100:103], v84 offset:0\n\t" "v_cvt_pk_bf16_f32 v67, v38, v39\n\t" \
    "v_mfma_f32_16x16x32_bf16 v[60:63], a[12:15], v[112:115], v[60:63]\n\t" "v_cvt_pk_bf16_f32 v68, v40, v41\n\t" "v_cvt_pk_bf16_f32 v69, v42, v43\n\t" \
    "s_waitcnt lgkmcnt(4)\n\t" "v_mfma_f32_16x16x32_bf16 v[48:51], a[16:19], v[116:119], v[48:51]\n\t" "ds_read_b128 v[104:107], v84 offset:32768\n\t" \
    "v_cvt_pk_bf16_f32 v70, v44, v45\n\t" "v_mfma_f32_16x16x32_bf16 v[52:55], a[20:23], v[116:119], v[52:55]\n\t" "v_cvt_pk_bf16_f32 v71, v46, v47\n\t" \
    "buffer_store_dwordx4 v[64:67], v96, %[rs], s62 offen offset:0 sc1\n\t" "s_waitcnt lgkmcnt(4)\n\t" "v_mfma_f32_16x16x32_bf16 v[56:59], a[16:19], v[120:123], v[56:59]\n\t" \
    "ds_read_b128 v[108:111], v85 offset:0\n\t" "v_mfma_f32_16x16x32_bf16 v[60:63], a[20:23], v[120:123], v[60:63]\n\t" "s_waitcnt lgkmcnt(4)\n\t" \
    "v_mfma_f32_16x16x32_bf16 v[48:51], a[24:27], v[124:127], v[48:51]\n\t" "ds_read_b128 v[112:115], v85 offset:32768\n\t" \
    "v_mfma_f32_16x16x32_bf16 v[52:55], a[28:31], v[124:127], v[52:55]\n\t" "buffer_store_dwordx4 v[68:71], v96, %[rs], s62 offen offset:256 sc1\n\t" \
    "s_waitcnt lgkmcnt(4)\n\t" "v_mfma_f32_16x16x32_bf16 v[56:59], a[24:27], v[128:131], v[56:59]\n\t" "ds_read_b128 v[116:119], v86 offset:0\n\t" "s_mov_b32 m0, s63\n\t" \
    "v_mfma_f32_16x16x32_bf16 v[60:63], a[28:31], v[128:131], v[60:63]\n\t" "s_add_u32 s63, s63, 0x800\n\t" "buffer_load_dwordx4 v88, %[rx], s61 offen lds\n\t" \
    "s_waitcnt lgkmcnt(4)\n\t" "v_mfma_f32_16x16x32_bf16 v[48:51], a[32:35], v[100:103], v[48:51]\n\t" "ds_read_b128 v[120:123], v86 offset:32768\n\t" \
    "v_mfma_f32_16x16x32_bf16 v[52:55], a[36:39], v[100:103], v[52:55]\n\t" "buffer_load_dwordx4 v88, %[rx], s61 offen offset:1024 lds\n\t" "s_waitcnt lgkmcnt(4)\n\t" \
    "v_mfma_f32_16x16x32_bf16 v[56:59], a[32:35], v[104:107], v[56:59]\n\t" "ds_read_b128 v[124:127], v87 offset:0\n\t" "s_mov_b32 m0, s63\n\t" \
    "v_mfma_f32_16x16x32_bf16 v[60:63], a[36:39], v[104:107], v[60:63]\n\t" "s_add_u32 s63, s63, 0x800\n\t" "buffer_load_dwordx4 v89, %[rx], s61 offen lds\n\t" \
    "s_waitcnt lgkmcnt(4)\n\t" "v_mfma_f32_16x16x32_bf16 v[48:51], a[40:43], v[108:111], v[48:51]\n\t" "ds_read_b128 v[128:131], v87 offset:32768\n\t" \
    "v_mfma_f32_16x16x32_bf16 v[52:55], a[44:47], v[108:111], v[52:55]\n\t" "buffer_load_dwordx4 v89, %[rx], s61 offen offset:1024 lds\n\t" "s_waitcnt lgkmcnt(4)\n\t" \
    "v_mfma_f32_16x16x32_bf16 v[56:59], a[40:43], v[112:115], v[56:59]\n\t" "ds_read_b128 v[100:103], v80 offset:512\n\t" "s_mov_b32 m0, s63\n\t" \
    "v_mfma_f32_16x16x32_bf16 v[60:63], a[44:47], v[112:115], v[60:63]\n\t" "s_add_u32 s63, s63, 0x800\n\t" "buffer_load_dwordx4 v90, %[rx], s61 offen lds\n\t" \
    "s_waitcnt lgkmcnt(4)\n\t" "v_mfma_f32_16x16x32_bf16 v[48:51], a[48:51], v[116:119], v[48:51]\n\t" "ds_read_b128 v[104:107], v80 offset:33280\n\t" \
    "v_mfma_f32_16x16x32_bf16 v[52:55], a[52:55], v[116:119], v[52:55]\n\t" "buffer_load_dwordx4 v90, %[rx], s61 offen offset:1024 lds\n\t" "s_waitcnt lgkmcnt(4)\n\t" \
    "v_mfma_f32_16x16x32_bf16 v[56:59], a[48:51], v[120:123], v[56:59]\n\t" "ds_read_b128 v[108:111], v81 offset:512\n\t" "s_mov_b32 m0, s63\n\t" \
    "v_mfma_f32_16x16x32_bf16 v[60:63], a[52:55], v[120:123], v[60:63]\n\t" "s_add_u32 s63, s63, 0x800\n\t" "buffer_load_dwordx4 v91, %[rx], s61 offen lds\n\t" \
    "s_waitcnt lgkmcnt(4)\n\t" "v_mfma_f32_16x16x32_bf16 v[48:51], a[56:59], v[124:127], v[48:51]\n\t" "ds_read_b128 v[112:115], v81 offset:33280\n\t" \
    "v_mfma_f32_16x16x32_bf16 v[52:55], a[60:63], v[124:127], v[52:55]\n\t" "buffer_load_dwordx4 v91, %[rx], s61 offen offset:1024 lds\n\t" "s_waitcnt lgkmcnt(4)\n\t" \
    "v_mfma_f32_16x16x32_bf16 v[56:59], a[56:59], v[128:131], v[56:59]\n\t" "ds_read_b128 v[116:119], v82 offset:512\n\t" "s_mov_b32 m0, s63\n\t" \
    "v_mfma_f32_16x16x32_bf16 v[60:63], a[60:63], v[128:131], v[60:63]\n\t" "s_add_u32 s63, s63, 0x800\n\t" "buffer_load_dwordx4 v92, %[rx], s61 offen lds\n\t" \
    "s_waitcnt lgkmcnt(4)\n\t" "v_mfma_f32_16x16x32_bf16 v[48:51], a[64:67], v[100:103], v[48:51]\n\t" "ds_read_b128 v[120:123], v82 offset:33280\n\t" \
    "v_mfma_f32_16x16x32_bf16 v[52:55], a[68:71], v[100:103], v[52:55]\n\t" "buffer_load_dwordx4 v92, %[rx], s61 offen offset:1024 lds\n\t" "s_waitcnt lgkmcnt(4)\n\t" \
    "v_mfma_f32_16x16x32_bf16 v[56:59], a[64:67], v[104:107], v[56:59]\n\t" "ds_read_b128 v[124:127], v83 offset:512\n\t" "s_mov_b32 m0, s63\n\t" \
    "v_mfma_f32_16x16x32_bf16 v[60:63], a[68:71], v[104:107], v[60:63]\n\t" "s_add_u32 s63, s63, 0x800\n\t" "buffer_load_dwordx4 v93, %[rx], s61 offen lds\n\t" \
    "s_waitcnt lgkmcnt(4)\n\t" "v_mfma_f32_16x16x32_bf16 v[48:51], a[72:75], v[108:111], v[48:51]\n\t" "ds_read_b128 v[128:131], v83 offset:33280\n\t" \
    "v_mfma_f32_16x16x32_bf16 v[52:55], a[76:79], v[108:111], v[52:55]\n\t" "buffer_load_dwordx4 v93, %[rx], s61 offen offset:1024 lds\n\t" "s_waitcnt lgkmcnt(4)\n\t" \
    "v_mfma_f32_16x16x32_bf16 v[56:59], a[72:75], v[112:115], v[56:59]\n\t" "ds_read_b128 v[100:103], v84 offset:512\n\t" "s_mov_b32 m0, s63\n\t" \
    "v_mfma_f32_16x16x32_bf16 v[60:63], a[76:79], v[112:115], v[60:63]\n\t" "s_add_u32 s63, s63, 0x800\n\t" "buffer_load_dwordx4 v94, %[rx], s61 offen lds\n\t" \
    "s_waitcnt lgkmcnt(4)\n\t" "v_mfma_f32_16x16x32_bf16 v[48:51], a[80:83], v[116:119], v[48:51]\n\t" "ds_read_b128 v[104:107], v84 offset:33280\n\t" \
    "v_mfma_f32_16x16x32_bf16 v[52:55], a[84:87], v[116:119], v[52:55]\n\t" "buffer_load_dwordx4 v94, %[rx], s61 offen offset:1024 lds\n\t" "s_waitcnt lgkmcnt(4)\n\t" \
    "v_mfma_f32_16x16x32_bf16 v[56:59], a[80:83], v[120:123], v[56:59]\n\t" "ds_read_b128 v[108:111], v85 offset:512\n\t" "s_mov_b32 m0, s63\n\t" \
    "v_mfma_f32_16x16x32_bf16 v[60:63], a[84:87], v[120:123], v[60:63]\n\t" "s_add_u32 s63, s63, 0x800\n\t" "buffer_load_dwordx4 v95, %[rx], s61 offen lds\n\t" \
    "s_waitcnt lgkmcnt(4)\n\t" "v_mfma_f32_16x16x32_bf16 v[48:51], a[88:91], v[124:127], v[48:51]\n\t" "ds_read_b128 v[112:115], v85 offset:33280\n\t" \
    "v_mfma_f32_16x16x32_bf16 v[52:55], a[92:95], v[124:127], v[52:55]\n\t" "buffer_load_dwordx4 v95, %[rx], s61 offen offset:1024 lds\n\t" "s_waitcnt lgkmcnt(4)\n\t" \
    "v_mfma_f32_16x16x32_bf16 v[56:59], a[88:91], v[128:131], v[56:59]\n\t" "ds_read_b128 v[116:119], v86 offset:512\n\t" \
    "v_mfma_f32_16x16x32_bf16 v[60:63], a[92:95], v[128:131], v[60:63]\n\t" "s_waitcnt lgkmcnt(4)\n\t" "v_mfma_f32_16x16x32_bf16 v[48:51], a[96:99], v[100:103], v[48:51]\n\t" \
    "ds_read_b128 v[120:123], v86 offset:33280\n\t" "s_lshr_b32 s64, s60, 2\n\t" "s_xor_b32 s64, s64, %[xp]\n\t" \
    "v_mfma_f32_16x16x32_bf16 v[52:55], a[100:103], v[100:103], v[52:55]\n\t" "s_and_b32 s64, s64, 1\n\t" "s_lshl_b32 s64, s64, 13\n\t" "s_waitcnt lgkmcnt(4)\n\t" \
    "v_mfma_f32_16x16x32_bf16 v[56:59], a[96:99], v[104:107], v[56:59]\n\t" "ds_read_b128 v[124:127], v87 offset:512\n\t" "s_lshl_b32 s62, s60, 19\n\t" \
    "v_mfma_f32_16x16x32_bf16 v[60:63], a[100:103], v[104:107], v[60:63]\n\t" "s_add_u32 s62, s62, s64\n\t" "s_add_u32 s62, s62, %[ob]\n\t" "s_waitcnt lgkmcnt(4)\n\t" \
    "v_mfma_f32_16x16x32_bf16 v[48:51], a[104:107], v[108:111], v[48:51]\n\t" "ds_read_b128 v[128:131], v87 offset:33280\n\t" \
    "v_mfma_f32_16x16x32_bf16 v[52:55], a[108:111], v[108:111], v[52:55]\n\t" "s_waitcnt lgkmcnt(4)\n\t" \
    "v_mfma_f32_16x16x32_bf16 v[56:59], a[104:107], v[112:115], v[56:59]\n\t" "ds_read_b128 v[100:103], v80 offset:1024\n\t" \
    "v_mfma_f32_16x16x32_bf16 v[60:63], a[108:111], v[112:115], v[60:63]\n\t" "s_waitcnt lgkmcnt(4)\n\t" \
    "v_mfma_f32_16x16x32_bf16 v[48:51], a[112:115], v[116:119], v[48:51]\n\t" "ds_read_b128 v[104:107], v80 offset:33792\n\t" \
    "v_mfma_f32_16x16x32_bf16 v[52:55], a[116:119], v[116:119], v[52:55]\n\t" "s_waitcnt lgkmcnt(4)\n\t" \
    "v_mfma_f32_16x16x32_bf16 v[56:59], a[112:115], v[120:123], v[56:59]\n\t" "ds_read_b128 v[108:111], v81 offset:1024\n\t" \
    "v_mfma_f32_16x16x32_bf16 v[60:63], a[116:119], v[120:123], v[60:63]\n\t" "s_waitcnt lgkmcnt(4)\n\t" \
    "v_mfma_f32_16x16x32_bf16 v[48:51], a[120:123], v[124:127], v[48:51]\n\t" "ds_read_b128 v[112:115], v81 offset:33792\n\t" \
    "v_mfma_f32_16x16x32_bf16 v[52:55], a[124:127], v[124:127], v[52:55]\n\t" "s_waitcnt lgkmcnt(4)\n\t" \
    "v_mfma_f32_16x16x32_bf16 v[56:59], a[120:123], v[128:131], v[56:59]\n\t" "ds_read_b128 v[116:119], v82 offset:1024\n\t" \
    "v_mfma_f32_16x16x32_bf16 v[60:63], a[124:127], v[128:131], v[60:63]\n\t" "s_waitcnt lgkmcnt(4)\n\t" \
    "v_mfma_f32_16x16x32_bf16 v[48:51], a[128:131], v[100:103], v[48:51]\n\t" "ds_read_b128 v[120:123], v82 offset:33792\n\t" \
    "v_mfma_f32_16x16x32_bf16 v[52:55], a[132:135], v[100:103], v[52:55]\n\t" "s_waitcnt lgkmcnt(4)\n\t" \
    "v_mfma_f32_16x16x32_bf16 v[56:59], a[128:131], v[104:107], v[56:59]\n\t" "ds_read_b128 v[124:127], v83 offset:1024\n\t" \
    "v_mfma_f32_16x16x32_bf16 v[60:63], a[132:135], v[104:107], v[60:63]\n\t" "s_waitcnt lgkmcnt(4)\n\t" \
    "v_mfma_f32_16x16x32_bf16 v[48:51], a[136:139], v[108:111], v[48:51]\n\t" "ds_read_b128 v[128:131], v83 offset:33792\n\t" "s_waitcnt vmcnt(16)\n\t" "s_barrier\n\t" \
    "v_mfma_f32_16x16x32_bf16 v[52:55], a[140:143], v[108:111], v[52:55]\n\t" "v_mov_b32 v98, s60\n\t" "buffer_store_dword v98, %[fo], %[rc], %[fl] offen sc1\n\t" \
    "s_waitcnt lgkmcnt(4)\n\t" "v_mfma_f32_16x16x32_bf16 v[56:59], a[136:139], v[112:115], v[56:59]\n\t" "ds_read_b128 v[100:103], v84 offset:1024\n\t" \
    "v_mfma_f32_16x16x32_bf16 v[60:63], a[140:143], v[112:115], v[60:63]\n\t" "s_waitcnt lgkmcnt(4)\n\t" \
    "v_mfma_f32_16x16x32_bf16 v[48:51], a[144:147], v[116:119], v[48:51]\n\t" "ds_read_b128 v[104:107], v84 offset:33792\n\t" \
    "v_mfma_f32_16x16x32_bf16 v[52:55], a[148:151], v[116:119], v[52:55]\n\t" "s_waitcnt lgkmcnt(4)\n\t" \
    "v_mfma_f32_16x16x32_bf16 v[56:59], a[144:147], v[120:123], v[56:59]\n\t" "ds_read_b128 v[108:111], v85 offset:1024\n\t" \
    "v_mfma_f32_16x16x32_bf16 v[60:63], a[148:151], v[120:123], v[60:63]\n\t" "s_waitcnt lgkmcnt(4)\n\t" \
    "v_mfma_f32_16x16x32_bf16 v[48:51], a[152:155], v[124:127], v[48:51]\n\t" "ds_read_b128 v[112:115], v85 offset:33792\n\t" \
    "v_mfma_f32_16x16x32_bf16 v[52:55], a[156:159], v[124:127], v[52:55]\n\t" "s_waitcnt lgkmcnt(4)\n\t" \
    "v_mfma_f32_16x16x32_bf16 v[56:59], a[152:155], v[128:131], v[56:59]\n\t" "ds_read_b128 v[116:119], v86 offset:1024\n\t" \
    "v_mfma_f32_16x16x32_bf16 v[60:63], a[156:159], v[128:131], v[60:63]\n\t" "s_waitcnt lgkmcnt(4)\n\t" \
    "v_mfma_f32_16x16x32_bf16 v[48:51], a[160:163], v[100:103], v[48:51]\n\t" "ds_read_b128 v[120:123], v86 offset:33792\n\t" \
    "v_mfma_f32_16x16x32_bf16 v[52:55], a[164:167], v[100:103], v[52:55]\n\t" "s_waitcnt lgkmcnt(4)\n\t" \
    "v_mfma_f32_16x16x32_bf16 v[56:59], a[160:163], v[104:107], v[56:59]\n\t" "ds_read_b128 v[124:127], v87 offset:1024\n\t" \
    "v_mfma_f32_16x16x32_bf16 v[60:63], a[164:167], v[104:107], v[60:63]\n\t" "s_waitcnt lgkmcnt(4)\n\t" \
    "v_mfma_f32_16x16x32_bf16 v[48:51], a[168:171], v[108:111], v[48:51]\n\t" "ds_read_b128 v[128:131], v87 offset:33792\n\t" \
    "v_mfma_f32_16x16x32_bf16 v[52:55], a[172:175], v[108:111], v[52:55]\n\t" "s_waitcnt lgkmcnt(4)\n\t" \
    "v_mfma_f32_16x16x32_bf16 v[56:59], a[168:171], v[112:115], v[56:59]\n\t" "ds_read_b128 v[100:103], v80 offset:1536\n\t" \
    "v_mfma_f32_16x16x32_bf16 v[60:63], a[172:175], v[112:115], v[60:63]\n\t" "s_waitcnt lgkmcnt(4)\n\t" \
    "v_mfma_f32_16x16x32_bf16 v[48:51], a[176:179], v[116:119], v[48:51]\n\t" "ds_read_b128 v[104:107], v80 offset:34304\n\t" \
    "v_mfma_f32_16x16x32_bf16 v[52:55], a[180:183], v[116:119], v[52:55]\n\t" "s_waitcnt lgkmcnt(4)\n\t" \
    "v_mfma_f32_16x16x32_bf16 v[56:59], a[176:179], v[120:123], v[56:59]\n\t" "ds_read_b128 v[108:111], v81 offset:1536\n\t" \
    "v_mfma_f32_16x16x32_bf16 v[60:63], a[180:183], v[120:123], v[60:63]\n\t" "s_waitcnt lgkmcnt(4)\n\t" \
    "v_mfma_f32_16x16x32_bf16 v[48:51], a[184:187], v[124:127], v[48:51]\n\t" "ds_read_b128 v[112:115], v81 offset:34304\n\t" \
    "v_mfma_f32_16x16x32_bf16 v[52:55], a[188:191], v[124:127], v[52:55]\n\t" "s_waitcnt lgkmcnt(4)\n\t" \
    "v_mfma_f32_16x16x32_bf16 v[56:59], a[184:187], v[128:131], v[56:59]\n\t" "ds_read_b128 v[116:119], v82 offset:1536\n\t" \
    "v_mfma_f32_16x16x32_bf16 v[60:63], a[188:191], v[128:131], v[60:63]\n\t" "s_waitcnt lgkmcnt(4)\n\t" \
    "v_mfma_f32_16x16x32_bf16 v[48:51], a[192:195], v[100:103], v[48:51]\n\t" "ds_read_b128 v[120:123], v82 offset:34304\n\t" \
    "v_mfma_f32_16x16x32_bf16 v[52:55], a[196:199], v[100:103], v[52:55]\n\t" "s_waitcnt lgkmcnt(4)\n\t" \
    "v_mfma_f32_16x16x32_bf16 v[56:59], a[192:195], v[104:107], v[56:59]\n\t" "ds_read_b128 v[124:127], v83 offset:1536\n\t" \
    "v_mfma_f32_16x16x32_bf16 v[60:63], a[196:199], v[104:107], v[60:63]\n\t" "s_waitcnt lgkmcnt(4)\n\t" \
    "v_mfma_f32_16x16x32_bf16 v[48:51], a[200:203], v[108:111], v[48:51]\n\t" "ds_read_b128 v[128:131], v83 offset:34304\n\t" \
    "v_mfma_f32_16x16x32_bf16 v[52:55], a[204:207], v[108:111], v[52:55]\n\t" "s_waitcnt lgkmcnt(4)\n\t" \
    "v_mfma_f32_16x16x32_bf16 v[56:59], a[200:203], v[112:115], v[56:59]\n\t" "ds_read_b128 v[100:103], v84 offset:1536\n\t" \
    "v_mfma_f32_16x16x32_bf16 v[60:63], a[204:207], v[112:115], v[60:63]\n\t" "s_waitcnt lgkmcnt(4)\n\t" \
    "v_mfma_f32_16x16x32_bf16 v[48:51], a[208:211], v[116:119], v[48:51]\n\t" "ds_read_b128 v[104:107], v84 offset:34304\n\t" \
    "v_mfma_f32_16x16x32_bf16 v[52:55], a[212:215], v[116:119], v[52:55]\n\t" "s_waitcnt lgkmcnt(4)\n\t" \
    "v_mfma_f32_16x16x32_bf16 v[56:59], a[208:211], v[120:123], v[56:59]\n\t" "ds_read_b128 v[108:111], v85 offset:1536\n\t" \
    "v_mfma_f32_16x16x32_bf16 v[60:63], a[212:215], v[120:123], v[60:63]\n\t" "s_waitcnt lgkmcnt(4)\n\t" \
    "v_mfma_f32_16x16x32_bf16 v[48:51], a[216:219], v[124:127], v[48:51]\n\t" "ds_read_b128 v[112:115], v85 offset:34304\n\t" \
    "v_mfma_f32_16x16x32_bf16 v[52:55], a[220:223], v[124:127], v[52:55]\n\t" "s_waitcnt lgkmcnt(4)\n\t" \
    "v_mfma_f32_16x16x32_bf16 v[56:59], a[216:219], v[128:131], v[56:59]\n\t" "ds_read_b128 v[116:119], v86 offset:1536\n\t" \
    "v_mfma_f32_16x16x32_bf16 v[60:63], a[220:223], v[128:131], v[60:63]\n\t" "s_waitcnt lgkmcnt(4)\n\t" \
    "v_mfma_f32_16x16x32_bf16 v[48:51], a[224:227], v[100:103], v[48:51]\n\t" "ds_read_b128 v[120:123], v86 offset:34304\n\t" \
    "v_mfma_f32_16x16x32_bf16 v[52:55], a[228:231], v[100:103], v[52:55]\n\t" "s_waitcnt lgkmcnt(4)\n\t" \
    "v_mfma_f32_16x16x32_bf16 v[56:59], a[224:227], v[104:107], v[56:59]\n\t" "ds_read_b128 v[124:127], v87 offset:1536\n\t" \
    "v_mfma_f32_16x16x32_bf16 v[60:63], a[228:231], v[104:107], v[60:63]\n\t" "s_waitcnt lgkmcnt(4)\n\t" \
    "v_mfma_f32_16x16x32_bf16 v[48:51], a[232:235], v[108:111], v[48:51]\n\t" "ds_read_b128 v[128:131], v87 offset:34304\n\t" \
    "v_mfma_f32_16x16x32_bf16 v[52:55], a[236:239], v[108:111], v[52:55]\n\t" "s_waitcnt lgkmcnt(4)\n\t" \
    "v_mfma_f32_16x16x32_bf16 v[56:59], a[232:235], v[112:115], v[56:59]\n\t" "v_mfma_f32_16x16x32_bf16 v[60:63], a[236:239], v[112:115], v[60:63]\n\t" \
    "s_waitcnt lgkmcnt(3)\n\t" "v_mfma_f32_16x16x32_bf16 v[48:51], a[240:243], v[116:119], v[48:51]\n\t" \
    "v_mfma_f32_16x16x32_bf16 v[52:55], a[244:247], v[116:119], v[52:55]\n\t" "s_waitcnt vmcnt(1) lgkmcnt(0)\n\t" "s_barrier\n\t" XT(82, 83) \
    "v_mfma_f32_16x16x32_bf16 v[56:59], a[240:243], v[120:123], v[56:59]\n\t" "ds_read_b128 v[100:103], v72 offset:0\n\t" \
    "v_mfma_f32_16x16x32_bf16 v[60:63], a[244:247], v[120:123], v[60:63]\n\t" "v_mfma_f32_16x16x32_bf16 v[48:51], a[248:251], v[124:127], v[48:51]\n\t" \
    "ds_read_b128 v[104:107], v72 offset:32768\n\t" "v_mfma_f32_16x16x32_bf16 v[52:55], a[252:255], v[124:127], v[52:55]\n\t" "ds_read_b128 v[108:111], v73 offset:0\n\t" \
    "v_mfma_f32_16x16x32_bf16 v[56:59], a[248:251], v[128:131], v[56:59]\n\t" "ds_read_b128 v[112:115], v73 offset:32768\n\t" "ds_read_b128 v[116:119], v74 offset:0\n\t" \
    "v_mfma_f32_16x16x32_bf16 v[60:63], a[252:255], v[128:131], v[60:63]\n\t" "s_add_u32 s60, s60, 1\n\t" GSUM "s_cmp_lt_u32 s60, 64\n\t" "s_cbranch_scc1 10b\n\t" \
    "s_nop 7\n\t" "v_cvt_pk_bf16_f32 v64, v48, v49\n\t" "v_cvt_pk_bf16_f32 v65, v50, v51\n\t" "v_cvt_pk_bf16_f32 v66, v52, v53\n\t" "v_cvt_pk_bf16_f32 v67, v54, v55\n\t" \
    "v_cvt_pk_bf16_f32 v68, v56, v57\n\t" "v_cvt_pk_bf16_f32 v69, v58, v59\n\t" "v_cvt_pk_bf16_f32 v70, v60, v61\n\t" "v_cvt_pk_bf16_f32 v71, v62, v63\n\t" \
    "buffer_store_dwordx4 v[64:67], v96, %[rs], s62 offen offset:0 sc1\n\t" "buffer_store_dwordx4 v[68:71], v96, %[rs], s62 offen offset:256 sc1\n\t" "s_waitcnt vmcnt(0)\n\t" \
    "s_barrier\n\t" "v_mov_b32 v98, s60\n\t" "buffer_store_dword v98, %[fo], %[rc], %[fl] offen sc1\n\t" "s_waitcnt lgkmcnt(0)\n\t" "s_branch 30f\n\t" "20:\n\t" \
    "v_mov_b32 v96, %[so]\n\t" "s_nop 7\n\t" "v_cvt_pk_bf16_f32 v64, v32, v33\n\t" "v_cvt_pk_bf16_f32 v65, v34, v35\n\t" "v_cvt_pk_bf16_f32 v66, v36, v37\n\t" \
    "v_cvt_pk_bf16_f32 v67, v38, v39\n\t" "v_cvt_pk_bf16_f32 v68, v40, v41\n\t" "v_cvt_pk_bf16_f32 v69, v42, v43\n\t" "v_cvt_pk_bf16_f32 v70, v44, v45\n\t" \
    "v_cvt_pk_bf16_f32 v71, v46, v47\n\t" "buffer_store_dwordx4 v[64:67], v96, %[rs], s62 offen offset:0 sc1\n\t" \
    "buffer_store_dwordx4 v[68:71], v96, %[rs], s62 offen offset:256 sc1\n\t" "s_waitcnt vmcnt(0)\n\t" "s_barrier\n\t" "v_mov_b32 v98, s60\n\t" \
    "buffer_store_dword v98, %[fo], %[rc], %[fl] offen sc1\n\t" "s_branch 11b\n\t" "30:\n\t"
#define ZC_LOOP \
    ".p2align 6\n\t" "s_mov_b32 s60, 0\n\t" "s_mov_b32 s61, 0\n\t" "s_mov_b32 s62, 0\n\t" "s_mov_b32 s63, 0x7f000000\n\t" "s_cmp_ge_u32 s61, %[z0]\n\t" \
    "s_cbranch_scc1 52f\n\t" "51:\n\t" "buffer_load_dword v120, %[fo], %[rc], %[fv] offen sc1\n\t" "buffer_load_dword v121, %[fo], %[rc], %[vd] offen sc1\n\t" \
    "s_waitcnt vmcnt(0)\n\t" "v_readfirstlane_b32 s61, v120\n\t" "v_readfirstlane_b32 s66, v121\n\t" "s_nop 3\n\t" "s_cmp_ge_u32 s61, %[z0]\n\t" "s_cbranch_scc1 52f\n\t" \
    "s_bitcmp1_b32 s66, 1\n\t" "s_cbranch_scc1 90f\n\t" "s_cmp_eq_u32 %[bd], 0\n\t" "s_cbranch_scc1 52f\n\t" "s_sub_u32 %[bd], %[bd], 1\n\t" "s_sleep 2\n\t" \
    "s_branch 51b\n\t" "52:\n\t" "s_lshr_b32 s66, s60, 2\n\t" "s_xor_b32 s66, s66, %[xp]\n\t" "s_and_b32 s66, s66, 1\n\t" "s_lshl_b32 s66, s66, 13\n\t" \
    "s_lshl_b32 s64, s60, 19\n\t" "s_add_u32 s64, s64, s66\n\t" "s_add_u32 s64, s64, %[vb]\n\t" "buffer_load_dwordx4 v[16:19], %[ao], %[rs], s64 offen offset:0 sc1\n\t" \
    "buffer_load_dwordx4 v[20:23], %[ao], %[rs], s64 offen offset:256 sc1\n\t" "buffer_load_dwordx4 v[24:27], %[ao], %[rs], s64 offen offset:2048 sc1\n\t" \
    "buffer_load_dwordx4 v[28:31], %[ao], %[rs], s64 offen offset:2304 sc1\n\t" "buffer_load_dwordx4 v[32:35], %[a1], %[rs], s64 offen offset:0 sc1\n\t" \
    "buffer_load_dwordx4 v[36:39], %[a1], %[rs], s64 offen offset:256 sc1\n\t" "buffer_load_dwordx4 v[40:43], %[a1], %[rs], s64 offen offset:2048 sc1\n\t" \
    "buffer_load_dwordx4 v[44:47], %[a1], %[rs], s64 offen offset:2304 sc1\n\t" "s_waitcnt vmcnt(0)\n\t" "10:\n\t" XT(80, 81) "s_add_u32 s66, s62, %[rg]\n\t" \
    "s_cmp_lt_u32 s66, s60\n\t" "s_cbranch_scc1 60f\n\t" "63:\n\t" "s_add_u32 s66, s60, 1\n\t" "s_cmp_gt_u32 s61, s66\n\t" "s_cbranch_scc0 40f\n\t" \
    "v_mfma_f32_16x16x32_bf16 v[80:83], v[16:19], a[0:3], 0\n\t" "v_mfma_f32_16x16x32_bf16 v[84:87], v[20:23], a[0:3], 0\n\t" \
    "v_mfma_f32_16x16x32_bf16 v[80:83], v[24:27], a[4:7], v[80:83]\n\t" "v_mfma_f32_16x16x32_bf16 v[84:87], v[28:31], a[4:7], v[84:87]\n\t" \
    "v_mfma_f32_16x16x32_bf16 v[80:83], v[32:35], a[8:11], v[80:83]\n\t" "v_cvt_pk_bf16_f32 v112, v96, v97\n\t" \
    "v_mfma_f32_16x16x32_bf16 v[84:87], v[36:39], a[8:11], v[84:87]\n\t" "v_cvt_pk_bf16_f32 v113, v98, v99\n\t" \
    "v_mfma_f32_16x16x32_bf16 v[80:83], v[40:43], a[12:15], v[80:83]\n\t" "v_cvt_pk_bf16_f32 v114, v100, v101\n\t" \
    "v_mfma_f32_16x16x32_bf16 v[84:87], v[44:47], a[12:15], v[84:87]\n\t" "v_cvt_pk_bf16_f32 v115, v102, v103\n\t" \
    "v_mfma_f32_16x16x32_bf16 v[88:91], v[16:19], a[16:19], 0\n\t" "v_cvt_pk_bf16_f32 v116, v104, v105\n\t" "v_mfma_f32_16x16x32_bf16 v[92:95], v[20:23], a[16:19], 0\n\t" \
    "v_cvt_pk_bf16_f32 v117, v106, v107\n\t" "v_mfma_f32_16x16x32_bf16 v[88:91], v[24:27], a[20:23], v[88:91]\n\t" \
    "buffer_store_dwordx4 v[112:115], %[z1], %[rs], s63 offen sc1\n\t" "v_mfma_f32_16x16x32_bf16 v[92:95], v[28:31], a[20:23], v[92:95]\n\t" \
    "v_mfma_f32_16x16x32_bf16 v[88:91], v[32:35], a[24:27], v[88:91]\n\t" "v_cvt_pk_bf16_f32 v118, v108, v109\n\t" \
    "v_mfma_f32_16x16x32_bf16 v[92:95], v[36:39], a[24:27], v[92:95]\n\t" "v_cvt_pk_bf16_f32 v119, v110, v111\n\t" \
    "v_mfma_f32_16x16x32_bf16 v[88:91], v[40:43], a[28:31], v[88:91]\n\t" "buffer_store_dwordx4 v[116:119], %[z2], %[rs], s63 offen sc1\n\t" "s_and_b32 s63, s60, %[rm]\n\t" \
    "v_mfma_f32_16x16x32_bf16 v[92:95], v[44:47], a[28:31], v[92:95]\n\t" "s_lshl_b32 s63, s63, 22\n\t" "s_add_u32 s63, s63, %[zb]\n\t" \
    "v_mfma_f32_16x16x32_bf16 v[96:99], v[16:19], a[32:35], 0\n\t" "s_add_u32 s65, s60, 1\n\t" "s_lshr_b32 s66, s65, 2\n\t" \
    "v_mfma_f32_16x16x32_bf16 v[100:103], v[20:23], a[32:35], 0\n\t" "s_xor_b32 s66, s66, %[xp]\n\t" "s_and_b32 s66, s66, 1\n\t" \
    "v_mfma_f32_16x16x32_bf16 v[96:99], v[24:27], a[36:39], v[96:99]\n\t" "s_lshl_b32 s66, s66, 13\n\t" "s_lshl_b32 s64, s65, 19\n\t" \
    "v_mfma_f32_16x16x32_bf16 v[100:103], v[28:31], a[36:39], v[100:103]\n\t" "s_add_u32 s64, s64, s66\n\t" "s_add_u32 s64, s64, %[vb]\n\t" \
    "v_mfma_f32_16x16x32_bf16 v[96:99], v[32:35], a[40:43], v[96:99]\n\t" "buffer_load_dwordx4 v[48:51], %[ao], %[rs], s64 offen offset:0 sc1\n\t" \
    "v_cvt_pk_bf16_f32 v112, v80, v81\n\t" "v_mfma_f32_16x16x32_bf16 v[100:103], v[36:39], a[40:43], v[100:103]\n\t" "v_cvt_pk_bf16_f32 v113, v82, v83\n\t" \
    "v_mfma_f32_16x16x32_bf16 v[96:99], v[40:43], a[44:47], v[96:99]\n\t" "buffer_load_dwordx4 v[52:55], %[ao], %[rs], s64 offen offset:256 sc1\n\t" \
    "v_cvt_pk_bf16_f32 v114, v84, v85\n\t" "v_mfma_f32_16x16x32_bf16 v[100:103], v[44:47], a[44:47], v[100:103]\n\t" "v_cvt_pk_bf16_f32 v115, v86, v87\n\t" \
    "v_mfma_f32_16x16x32_bf16 v[104:107], v[16:19], a[48:51], 0\n\t" "buffer_load_dwordx4 v[56:59], %[ao], %[rs], s64 offen offset:2048 sc1\n\t" \
    "v_cvt_pk_bf16_f32 v116, v88, v89\n\t" "v_mfma_f32_16x16x32_bf16 v[108:111], v[20:23], a[48:51], 0\n\t" "v_cvt_pk_bf16_f32 v117, v90, v91\n\t" \
    "v_mfma_f32_16x16x32_bf16 v[104:107], v[24:27], a[52:55], v[104:107]\n\t" "buffer_load_dwordx4 v[60:63], %[ao], %[rs], s64 offen offset:2304 sc1\n\t" \
    "v_mfma_f32_16x16x32_bf16 v[108:111], v[28:31], a[52:55], v[108:111]\n\t" "buffer_store_dwordx4 v[112:115], %[z1], %[rs], s63 offen sc1\n\t" \
    "v_mfma_f32_16x16x32_bf16 v[104:107], v[32:35], a[56:59], v[104:107]\n\t" "buffer_load_dwordx4 v[64:67], %[a1], %[rs], s64 offen offset:0 sc1\n\t" \
    "v_cvt_pk_bf16_f32 v118, v92, v93\n\t" "v_mfma_f32_16x16x32_bf16 v[108:111], v[36:39], a[56:59], v[108:111]\n\t" "v_cvt_pk_bf16_f32 v119, v94, v95\n\t" \
    "v_mfma_f32_16x16x32_bf16 v[104:107], v[40:43], a[60:63], v[104:107]\n\t" "buffer_load_dwordx4 v[68:71], %[a1], %[rs], s64 offen offset:256 sc1\n\t" \
    "buffer_store_dwordx4 v[116:119], %[z2], %[rs], s63 offen sc1\n\t" "v_mfma_f32_16x16x32_bf16 v[108:111], v[44:47], a[60:63], v[108:111]\n\t" \
    "s_add_u32 s63, s63, 0x20000\n\t" "v_mfma_f32_16x16x32_bf16 v[80:83], v[16:19], a[64:67], 0\n\t" \
    "buffer_load_dwordx4 v[72:75], %[a1], %[rs], s64 offen offset:2048 sc1\n\t" "v_mfma_f32_16x16x32_bf16 v[84:87], v[20:23], a[64:67], 0\n\t" \
    "v_mfma_f32_16x16x32_bf16 v[80:83], v[24:27], a[68:71], v[80:83]\n\t" "buffer_load_dwordx4 v[76:79], %[a1], %[rs], s64 offen offset:2304 sc1\n\t" \
    "v_mfma_f32_16x16x32_bf16 v[84:87], v[28:31], a[68:71], v[84:87]\n\t" "v_mfma_f32_16x16x32_bf16 v[80:83], v[32:35], a[72:75], v[80:83]\n\t" \
    "buffer_load_dword v120, %[fo], %[rc], %[fv] offen sc1\n\t" "v_cvt_pk_bf16_f32 v112, v96, v97\n\t" "v_mfma_f32_16x16x32_bf16 v[84:87], v[36:39], a[72:75], v[84:87]\n\t" \
    "v_cvt_pk_bf16_f32 v113, v98, v99\n\t" "v_mfma_f32_16x16x32_bf16 v[80:83], v[40:43], a[76:79], v[80:83]\n\t" "buffer_load_dword v121, %[fo], %[rc], %[fs] offen sc1\n\t" \
    "v_mfma_f32_16x16x32_bf16 v[84:87], v[44:47], a[76:79], v[84:87]\n\t" "v_cvt_pk_bf16_f32 v114, v100, v101\n\t" "v_cvt_pk_bf16_f32 v115, v102, v103\n\t" \
    "v_mfma_f32_16x16x32_bf16 v[88:91], v[16:19], a[80:83], 0\n\t" "v_cvt_pk_bf16_f32 v116, v104, v105\n\t" "v_mfma_f32_16x16x32_bf16 v[92:95], v[20:23], a[80:83], 0\n\t" \
    "v_cvt_pk_bf16_f32 v117, v106, v107\n\t" "v_mfma_f32_16x16x32_bf16 v[88:91], v[24:27], a[84:87], v[88:91]\n\t" \
    "buffer_store_dwordx4 v[112:115], %[z1], %[rs], s63 offen sc1\n\t" "v_mfma_f32_16x16x32_bf16 v[92:95], v[28:31], a[84:87], v[92:95]\n\t" \
    "v_mfma_f32_16x16x32_bf16 v[88:91], v[32:35], a[88:91], v[88:91]\n\t" "v_cvt_pk_bf16_f32 v118, v108, v109\n\t" \
    "v_mfma_f32_16x16x32_bf16 v[92:95], v[36:39], a[88:91], v[92:95]\n\t" "v_cvt_pk_bf16_f32 v119, v110, v111\n\t" \
    "v_mfma_f32_16x16x32_bf16 v[88:91], v[40:43], a[92:95], v[88:91]\n\t" "buffer_store_dwordx4 v[116:119], %[z2], %[rs], s63 offen sc1\n\t" \
    "v_mfma_f32_16x16x32_bf16 v[92:95], v[44:47], a[92:95], v[92:95]\n\t" "s_add_u32 s63, s63, 0x20000\n\t" "v_mfma_f32_16x16x32_bf16 v[96:99], v[16:19], a[96:99], 0\n\t" \
    "v_mfma_f32_16x16x32_bf16 v[100:103], v[20:23], a[96:99], 0\n\t" "v_mfma_f32_16x16x32_bf16 v[96:99], v[24:27], a[100:103], v[96:99]\n\t" \
    "v_mfma_f32_16x16x32_bf16 v[100:103], v[28:31], a[100:103], v[100:103]\n\t" "v_mfma_f32_16x16x32_bf16 v[96:99], v[32:35], a[104:107], v[96:99]\n\t" \
    "v_cvt_pk_bf16_f32 v112, v80, v81\n\t" "v_mfma_f32_16x16x32_bf16 v[100:103], v[36:39], a[104:107], v[100:103]\n\t" "v_cvt_pk_bf16_f32 v113, v82, v83\n\t" \
    "v_mfma_f32_16x16x32_bf16 v[96:99], v[40:43], a[108:111], v[96:99]\n\t" "v_cvt_pk_bf16_f32 v114, v84, v85\n\t" \
    "v_mfma_f32_16x16x32_bf16 v[100:103], v[44:47], a[108:111], v[100:103]\n\t" "v_cvt_pk_bf16_f32 v115, v86, v87\n\t" \
    "v_mfma_f32_16x16x32_bf16 v[104:107], v[16:19], a[112:115], 0\n\t" "v_cvt_pk_bf16_f32 v116, v88, v89\n\t" \
    "v_mfma_f32_16x16x32_bf16 v[108:111], v[20:23], a[112:115], 0\n\t" "v_cvt_pk_bf16_f32 v117, v90, v91\n\t" \
    "v_mfma_f32_16x16x32_bf16 v[104:107], v[24:27], a[116:119], v[104:107]\n\t" "buffer_store_dwordx4 v[112:115], %[z1], %[rs], s63 offen sc1\n\t" \
    "v_mfma_f32_16x16x32_bf16 v[108:111], v[28:31], a[116:119], v[108:111]\n\t" "v_mfma_f32_16x16x32_bf16 v[104:107], v[32:35], a[120:123], v[104:107]\n\t" \
    "v_cvt_pk_bf16_f32 v118, v92, v93\n\t" "v_mfma_f32_16x16x32_bf16 v[108:111], v[36:39], a[120:123], v[108:111]\n\t" "v_cvt_pk_bf16_f32 v119, v94, v95\n\t" \
    "v_mfma_f32_16x16x32_bf16 v[104:107], v[40:43], a[124:127], v[104:107]\n\t" "buffer_store_dwordx4 v[116:119], %[z2], %[rs], s63 offen sc1\n\t" \
    "v_mfma_f32_16x16x32_bf16 v[108:111], v[44:47], a[124:127], v[108:111]\n\t" "s_add_u32 s63, s63, 0x20000\n\t" \
    "v_mfma_f32_16x16x32_bf16 v[80:83], v[16:19], a[128:131], 0\n\t" "v_mfma_f32_16x16x32_bf16 v[84:87], v[20:23], a[128:131], 0\n\t" \
    "v_mfma_f32_16x16x32_bf16 v[80:83], v[24:27], a[132:135], v[80:83]\n\t" "v_mfma_f32_16x16x32_bf16 v[84:87], v[28:31], a[132:135], v[84:87]\n\t" \
    "v_mfma_f32_16x16x32_bf16 v[80:83], v[32:35], a[136:139], v[80:83]\n\t" "v_cvt_pk_bf16_f32 v112, v96, v97\n\t" \
    "v_mfma_f32_16x16x32_bf16 v[84:87], v[36:39], a[136:139], v[84:87]\n\t" "v_cvt_pk_bf16_f32 v113, v98, v99\n\t" \
    "v_mfma_f32_16x16x32_bf16 v[80:83], v[40:43], a[140:143], v[80:83]\n\t" "v_cvt_pk_bf16_f32 v114, v100, v101\n\t" \
    "v_mfma_f32_16x16x32_bf16 v[84:87], v[44:47], a[140:143], v[84:87]\n\t" "v_cvt_pk_bf16_f32 v115, v102, v103\n\t" \
    "v_mfma_f32_16x16x32_bf16 v[88:91], v[16:19], a[144:147], 0\n\t" "v_cvt_pk_bf16_f32 v116, v104, v105\n\t" "v_mfma_f32_16x16x32_bf16 v[92:95], v[20:23], a[144:147], 0\n\t" \
    "v_cvt_pk_bf16_f32 v117, v106, v107\n\t" "v_mfma_f32_16x16x32_bf16 v[88:91], v[24:27], a[148:151], v[88:91]\n\t" \
    "buffer_store_dwordx4 v[112:115], %[z1], %[rs], s63 offen sc1\n\t" "v_mfma_f32_16x16x32_bf16 v[92:95], v[28:31], a[148:151], v[92:95]\n\t" \
    "v_mfma_f32_16x16x32_bf16 v[88:91], v[32:35], a[152:155], v[88:91]\n\t" "v_cvt_pk_bf16_f32 v118, v108, v109\n\t" \
    "v_mfma_f32_16x16x32_bf16 v[92:95], v[36:39], a[152:155], v[92:95]\n\t" "v_cvt_pk_bf16_f32 v119, v110, v111\n\t" \
    "v_mfma_f32_16x16x32_bf16 v[88:91], v[40:43], a[156:159], v[88:91]\n\t" "buffer_store_dwordx4 v[116:119], %[z2], %[rs], s63 offen sc1\n\t" \
    "v_mfma_f32_16x16x32_bf16 v[92:95], v[44:47], a[156:159], v[92:95]\n\t" "s_add_u32 s63, s63, 0x20000\n\t" "v_mfma_f32_16x16x32_bf16 v[96:99], v[16:19], a[160:163], 0\n\t" \
    "v_mfma_f32_16x16x32_bf16 v[100:103], v[20:23], a[160:163], 0\n\t" "v_mfma_f32_16x16x32_bf16 v[96:99], v[24:27], a[164:167], v[96:99]\n\t" \
    "v_mfma_f32_16x16x32_bf16 v[100:103], v[28:31], a[164:167], v[100:103]\n\t" "v_mfma_f32_16x16x32_bf16 v[96:99], v[32:35], a[168:171], v[96:99]\n\t" \
    "v_cvt_pk_bf16_f32 v112, v80, v81\n\t" "v_mfma_f32_16x16x32_bf16 v[100:103], v[36:39], a[168:171], v[100:103]\n\t" "v_cvt_pk_bf16_f32 v113, v82, v83\n\t" \
    "v_mfma_f32_16x16x32_bf16 v[96:99], v[40:43], a[172:175], v[96:99]\n\t" "v_cvt_pk_bf16_f32 v114, v84, v85\n\t" \
    "v_mfma_f32_16x16x32_bf16 v[100:103], v[44:47], a[172:175], v[100:103]\n\t" "v_cvt_pk_bf16_f32 v115, v86, v87\n\t" \
    "v_mfma_f32_16x16x32_bf16 v[104:107], v[16:19], a[176:179], 0\n\t" "v_cvt_pk_bf16_f32 v116, v88, v89\n\t" \
    "v_mfma_f32_16x16x32_bf16 v[108:111], v[20:23], a[176:179], 0\n\t" "v_cvt_pk_bf16_f32 v117, v90, v91\n\t" \
    "v_mfma_f32_16x16x32_bf16 v[104:107], v[24:27], a[180:183], v[104:107]\n\t" "buffer_store_dwordx4 v[112:115], %[z1], %[rs], s63 offen sc1\n\t" \
    "v_mfma_f32_16x16x32_bf16 v[108:111], v[28:31], a[180:183], v[108:111]\n\t" "v_mfma_f32_16x16x32_bf16 v[104:107], v[32:35], a[184:187], v[104:107]\n\t" \
    "v_cvt_pk_bf16_f32 v118, v92, v93\n\t" "v_mfma_f32_16x16x32_bf16 v[108:111], v[36:39], a[184:187], v[108:111]\n\t" "v_cvt_pk_bf16_f32 v119, v94, v95\n\t" \
    "v_mfma_f32_16x16x32_bf16 v[104:107], v[40:43], a[188:191], v[104:107]\n\t" "buffer_store_dwordx4 v[116:119], %[z2], %[rs], s63 offen sc1\n\t" \
    "v_mfma_f32_16x16x32_bf16 v[108:111], v[44:47], a[188:191], v[108:111]\n\t" "s_add_u32 s63, s63, 0x20000\n\t" \
    "v_mfma_f32_16x16x32_bf16 v[80:83], v[16:19], a[192:195], 0\n\t" "v_mfma_f32_16x16x32_bf16 v[84:87], v[20:23], a[192:195], 0\n\t" \
    "v_mfma_f32_16x16x32_bf16 v[80:83], v[24:27], a[196:199], v[80:83]\n\t" "v_mfma_f32_16x16x32_bf16 v[84:87], v[28:31], a[196:199], v[84:87]\n\t" \
    "v_mfma_f32_16x16x32_bf16 v[80:83], v[32:35], a[200:203], v[80:83]\n\t" "v_cvt_pk_bf16_f32 v112, v96, v97\n\t" \
    "v_mfma_f32_16x16x32_bf16 v[84:87], v[36:39], a[200:203], v[84:87]\n\t" "v_cvt_pk_bf16_f32 v113, v98, v99\n\t" \
    "v_mfma_f32_16x16x32_bf16 v[80:83], v[40:43], a[204:207], v[80:83]\n\t" "v_cvt_pk_bf16_f32 v114, v100, v101\n\t" \
    "v_mfma_f32_16x16x32_bf16 v[84:87], v[44:47], a[204:207], v[84:87]\n\t" "v_cvt_pk_bf16_f32 v115, v102, v103\n\t" \
    "v_mfma_f32_16x16x32_bf16 v[88:91], v[16:19], a[208:211], 0\n\t" "v_cvt_pk_bf16_f32 v116, v104, v105\n\t" "v_mfma_f32_16x16x32_bf16 v[92:95], v[20:23], a[208:211], 0\n\t" \
    "v_cvt_pk_bf16_f32 v117, v106, v107\n\t" "v_mfma_f32_16x16x32_bf16 v[88:91], v[24:27], a[212:215], v[88:91]\n\t" \
    "buffer_store_dwordx4 v[112:115], %[z1], %[rs], s63 offen sc1\n\t" "v_mfma_f32_16x16x32_bf16 v[92:95], v[28:31], a[212:215], v[92:95]\n\t" \
    "v_mfma_f32_16x16x32_bf16 v[88:91], v[32:35], a[216:219], v[88:91]\n\t" "v_cvt_pk_bf16_f32 v118, v108, v109\n\t" \
    "v_mfma_f32_16x16x32_bf16 v[92:95], v[36:39], a[216:219], v[92:95]\n\t" "v_cvt_pk_bf16_f32 v119, v110, v111\n\t" \
    "v_mfma_f32_16x16x32_bf16 v[88:91], v[40:43], a[220:223], v[88:91]\n\t" "buffer_store_dwordx4 v[116:119], %[z2], %[rs], s63 offen sc1\n\t" \
    "v_mfma_f32_16x16x32_bf16 v[92:95], v[44:47], a[220:223], v[92:95]\n\t" "s_add_u32 s63, s63, 0x20000\n\t" "v_mfma_f32_16x16x32_bf16 v[96:99], v[16:19], a[224:227], 0\n\t" \
    "v_mfma_f32_16x16x32_bf16 v[100:103], v[20:23], a[224:227], 0\n\t" "v_mfma_f32_16x16x32_bf16 v[96:99], v[24:27], a[228:231], v[96:99]\n\t" \
    "v_mfma_f32_16x16x32_bf16 v[100:103], v[28:31], a[228:231], v[100:103]\n\t" "v_mfma_f32_16x16x32_bf16 v[96:99], v[32:35], a[232:235], v[96:99]\n\t" \
    "v_cvt_pk_bf16_f32 v112, v80, v81\n\t" "v_mfma_f32_16x16x32_bf16 v[100:103], v[36:39], a[232:235], v[100:103]\n\t" "v_cvt_pk_bf16_f32 v113, v82, v83\n\t" \
    "v_mfma_f32_16x16x32_bf16 v[96:99], v[40:43], a[236:239], v[96:99]\n\t" "v_cvt_pk_bf16_f32 v114, v84, v85\n\t" \
    "v_mfma_f32_16x16x32_bf16 v[100:103], v[44:47], a[236:239], v[100:103]\n\t" "v_cvt_pk_bf16_f32 v115, v86, v87\n\t" \
    "v_mfma_f32_16x16x32_bf16 v[104:107], v[16:19], a[240:243], 0\n\t" "v_cvt_pk_bf16_f32 v116, v88, v89\n\t" \
    "v_mfma_f32_16x16x32_bf16 v[108:111], v[20:23], a[240:243], 0\n\t" "v_cvt_pk_bf16_f32 v117, v90, v91\n\t" \
    "v_mfma_f32_16x16x32_bf16 v[104:107], v[24:27], a[244:247], v[104:107]\n\t" "buffer_store_dwordx4 v[112:115], %[z1], %[rs], s63 offen sc1\n\t" \
    "v_mfma_f32_16x16x32_bf16 v[108:111], v[28:31], a[244:247], v[108:111]\n\t" "v_mfma_f32_16x16x32_bf16 v[104:107], v[32:35], a[248:251], v[104:107]\n\t" \
    "v_cvt_pk_bf16_f32 v118, v92, v93\n\t" "v_cvt_pk_bf16_f32 v119, v94, v95\n\t" "s_waitcnt vmcnt(11)\n\t" "v_readfirstlane_b32 s66, v120\n\t" \
    "v_mfma_f32_16x16x32_bf16 v[108:111], v[36:39], a[248:251], v[108:111]\n\t" "v_readfirstlane_b32 s67, v121\n\t" "v_mov_b32 v122, s60\n\t" \
    "buffer_store_dword v122, %[fo], %[rc], %[fz] offen sc1\n\t" "v_mfma_f32_16x16x32_bf16 v[104:107], v[40:43], a[252:255], v[104:107]\n\t" \
    "buffer_store_dwordx4 v[116:119], %[z2], %[rs], s63 offen sc1\n\t" "s_add_u32 s63, s63, 0x20000\n\t" "s_max_u32 s61, s61, s66\n\t" \
    "v_mfma_f32_16x16x32_bf16 v[108:111], v[44:47], a[252:255], v[108:111]\n\t" "s_max_u32 s62, s62, s67\n\t" "s_add_u32 s60, s60, 1\n\t" ZSUM "23:\n\t" XT(80, 81) \
    "s_add_u32 s66, s62, %[rg]\n\t" "s_cmp_lt_u32 s66, s60\n\t" "s_cbranch_scc1 70f\n\t" "73:\n\t" "s_add_u32 s66, s60, 1\n\t" "s_cmp_gt_u32 s61, s66\n\t" \
    "s_cbranch_scc0 41f\n\t" "v_mfma_f32_16x16x32_bf16 v[80:83], v[48:51], a[0:3], 0\n\t" "v_mfma_f32_16x16x32_bf16 v[84:87], v[52:55], a[0:3], 0\n\t" \
    "v_mfma_f32_16x16x32_bf16 v[80:83], v[56:59], a[4:7], v[80:83]\n\t" "v_mfma_f32_16x16x32_bf16 v[84:87], v[60:63], a[4:7], v[84:87]\n\t" \
    "v_mfma_f32_16x16x32_bf16 v[80:83], v[64:67], a[8:11], v[80:83]\n\t" "v_cvt_pk_bf16_f32 v112, v96, v97\n\t" \
    "v_mfma_f32_16x16x32_bf16 v[84:87], v[68:71], a[8:11], v[84:87]\n\t" "v_cvt_pk_bf16_f32 v113, v98, v99\n\t" \
    "v_mfma_f32_16x16x32_bf16 v[80:83], v[72:75], a[12:15], v[80:83]\n\t" "v_cvt_pk_bf16_f32 v114, v100, v101\n\t" \
    "v_mfma_f32_16x16x32_bf16 v[84:87], v[76:79], a[12:15], v[84:87]\n\t" "v_cvt_pk_bf16_f32 v115, v102, v103\n\t" \
    "v_mfma_f32_16x16x32_bf16 v[88:91], v[48:51], a[16:19], 0\n\t" "v_cvt_pk_bf16_f32 v116, v104, v105\n\t" "v_mfma_f32_16x16x32_bf16 v[92:95], v[52:55], a[16:19], 0\n\t" \
    "v_cvt_pk_bf16_f32 v117, v106, v107\n\t" "v_mfma_f32_16x16x32_bf16 v[88:91], v[56:59], a[20:23], v[88:91]\n\t" \
    "buffer_store_dwordx4 v[112:115], %[z1], %[rs], s63 offen sc1\n\t" "v_mfma_f32_16x16x32_bf16 v[92:95], v[60:63], a[20:23], v[92:95]\n\t" \
    "v_mfma_f32_16x16x32_bf16 v[88:91], v[64:67], a[24:27], v[88:91]\n\t" "v_cvt_pk_bf16_f32 v118, v108, v109\n\t" \
    "v_mfma_f32_16x16x32_bf16 v[92:95], v[68:71], a[24:27], v[92:95]\n\t" "v_cvt_pk_bf16_f32 v119, v110, v111\n\t" \
    "v_mfma_f32_16x16x32_bf16 v[88:91], v[72:75], a[28:31], v[88:91]\n\t" "buffer_store_dwordx4 v[116:119], %[z2], %[rs], s63 offen sc1\n\t" "s_and_b32 s63, s60, %[rm]\n\t" \
    "v_mfma_f32_16x16x32_bf16 v[92:95], v[76:79], a[28:31], v[92:95]\n\t" "s_lshl_b32 s63, s63, 22\n\t" "s_add_u32 s63, s63, %[zb]\n\t" \
    "v_mfma_f32_16x16x32_bf16 v[96:99], v[48:51], a[32:35], 0\n\t" "s_add_u32 s65, s60, 1\n\t" "s_lshr_b32 s66, s65, 2\n\t" \
    "v_mfma_f32_16x16x32_bf16 v[100:103], v[52:55], a[32:35], 0\n\t" "s_xor_b32 s66, s66, %[xp]\n\t" "s_and_b32 s66, s66, 1\n\t" \
    "v_mfma_f32_16x16x32_bf16 v[96:99], v[56:59], a[36:39], v[96:99]\n\t" "s_lshl_b32 s66, s66, 13\n\t" "s_lshl_b32 s64, s65, 19\n\t" \
    "v_mfma_f32_16x16x32_bf16 v[100:103], v[60:63], a[36:39], v[100:103]\n\t" "s_add_u32 s64, s64, s66\n\t" "s_add_u32 s64, s64, %[vb]\n\t" \
    "v_mfma_f32_16x16x32_bf16 v[96:99], v[64:67], a[40:43], v[96:99]\n\t" "buffer_load_dwordx4 v[16:19], %[ao], %[rs], s64 offen offset:0 sc1\n\t" \
    "v_cvt_pk_bf16_f32 v112, v80, v81\n\t" "v_mfma_f32_16x16x32_bf16 v[100:103], v[68:71], a[40:43], v[100:103]\n\t" "v_cvt_pk_bf16_f32 v113, v82, v83\n\t" \
    "v_mfma_f32_16x16x32_bf16 v[96:99], v[72:75], a[44:47], v[96:99]\n\t" "buffer_load_dwordx4 v[20:23], %[ao], %[rs], s64 offen offset:256 sc1\n\t" \
    "v_cvt_pk_bf16_f32 v114, v84, v85\n\t" "v_mfma_f32_16x16x32_bf16 v[100:103], v[76:79], a[44:47], v[100:103]\n\t" "v_cvt_pk_bf16_f32 v115, v86, v87\n\t" \
    "v_mfma_f32_16x16x32_bf16 v[104:107], v[48:51], a[48:51], 0\n\t" "buffer_load_dwordx4 v[24:27], %[ao], %[rs], s64 offen offset:2048 sc1\n\t" \
    "v_cvt_pk_bf16_f32 v116, v88, v89\n\t" "v_mfma_f32_16x16x32_bf16 v[108:111], v[52:55], a[48:51], 0\n\t" "v_cvt_pk_bf16_f32 v117, v90, v91\n\t" \
    "v_mfma_f32_16x16x32_bf16 v[104:107], v[56:59], a[52:55], v[104:107]\n\t" "buffer_load_dwordx4 v[28:31], %[ao], %[rs], s64 offen offset:2304 sc1\n\t" \
    "v_mfma_f32_16x16x32_bf16 v[108:111], v[60:63], a[52:55], v[108:111]\n\t" "buffer_store_dwordx4 v[112:115], %[z1], %[rs], s63 offen sc1\n\t" \
    "v_mfma_f32_16x16x32_bf16 v[104:107], v[64:67], a[56:59], v[104:107]\n\t" "buffer_load_dwordx4 v[32:35], %[a1], %[rs], s64 offen offset:0 sc1\n\t" \
    "v_cvt_pk_bf16_f32 v118, v92, v93\n\t" "v_mfma_f32_16x16x32_bf16 v[108:111], v[68:71], a[56:59], v[108:111]\n\t" "v_cvt_pk_bf16_f32 v119, v94, v95\n\t" \
    "v_mfma_f32_16x16x32_bf16 v[104:107], v[72:75], a[60:63], v[104:107]\n\t" "buffer_load_dwordx4 v[36:39], %[a1], %[rs], s64 offen offset:256 sc1\n\t" \
    "buffer_store_dwordx4 v[116:119], %[z2], %[rs], s63 offen sc1\n\t" "v_mfma_f32_16x16x32_bf16 v[108:111], v[76:79], a[60:63], v[108:111]\n\t" \
    "s_add_u32 s63, s63, 0x20000\n\t" "v_mfma_f32_16x16x32_bf16 v[80:83], v[48:51], a[64:67], 0\n\t" \
    "buffer_load_dwordx4 v[40:43], %[a1], %[rs], s64 offen offset:2048 sc1\n\t" "v_mfma_f32_16x16x32_bf16 v[84:87], v[52:55], a[64:67], 0\n\t" \
    "v_mfma_f32_16x16x32_bf16 v[80:83], v[56:59], a[68:71], v[80:83]\n\t" "buffer_load_dwordx4 v[44:47], %[a1], %[rs], s64 offen offset:2304 sc1\n\t" \
    "v_mfma_f32_16x16x32_bf16 v[84:87], v[60:63], a[68:71], v[84:87]\n\t" "v_mfma_f32_16x16x32_bf16 v[80:83], v[64:67], a[72:75], v[80:83]\n\t" \
    "buffer_load_dword v120, %[fo], %[rc], %[fv] offen sc1\n\t" "v_cvt_pk_bf16_f32 v112, v96, v97\n\t" "v_mfma_f32_16x16x32_bf16 v[84:87], v[68:71], a[72:75], v[84:87]\n\t" \
    "v_cvt_pk_bf16_f32 v113, v98, v99\n\t" "v_mfma_f32_16x16x32_bf16 v[80:83], v[72:75], a[76:79], v[80:83]\n\t" "buffer_load_dword v121, %[fo], %[rc], %[fs] offen sc1\n\t" \
    "v_mfma_f32_16x16x32_bf16 v[84:87], v[76:79], a[76:79], v[84:87]\n\t" "v_cvt_pk_bf16_f32 v114, v100, v101\n\t" "v_cvt_pk_bf16_f32 v115, v102, v103\n\t" \
    "v_mfma_f32_16x16x32_bf16 v[88:91], v[48:51], a[80:83], 0\n\t" "v_cvt_pk_bf16_f32 v116, v104, v105\n\t" "v_mfma_f32_16x16x32_bf16 v[92:95], v[52:55], a[80:83], 0\n\t" \
    "v_cvt_pk_bf16_f32 v117, v106, v107\n\t" "v_mfma_f32_16x16x32_bf16 v[88:91], v[56:59], a[84:87], v[88:91]\n\t" \
    "buffer_store_dwordx4 v[112:115], %[z1], %[rs], s63 offen sc1\n\t" "v_mfma_f32_16x16x32_bf16 v[92:95], v[60:63], a[84:87], v[92:95]\n\t" \
    "v_mfma_f32_16x16x32_bf16 v[88:91], v[64:67], a[88:91], v[88:91]\n\t" "v_cvt_pk_bf16_f32 v118, v108, v109\n\t" \
    "v_mfma_f32_16x16x32_bf16 v[92:95], v[68:71], a[88:91], v[92:95]\n\t" "v_cvt_pk_bf16_f32 v119, v110, v111\n\t" \
    "v_mfma_f32_16x16x32_bf16 v[88:91], v[72:75], a[92:95], v[88:91]\n\t" "buffer_store_dwordx4 v[116:119], %[z2], %[rs], s63 offen sc1\n\t" \
    "v_mfma_f32_16x16x32_bf16 v[92:95], v[76:79], a[92:95], v[92:95]\n\t" "s_add_u32 s63, s63, 0x20000\n\t" "v_mfma_f32_16x16x32_bf16 v[96:99], v[48:51], a[96:99], 0\n\t" \
    "v_mfma_f32_16x16x32_bf16 v[100:103], v[52:55], a[96:99], 0\n\t" "v_mfma_f32_16x16x32_bf16 v[96:99], v[56:59], a[100:103], v[96:99]\n\t" \
    "v_mfma_f32_16x16x32_bf16 v[100:103], v[60:63], a[100:103], v[100:103]\n\t" "v_mfma_f32_16x16x32_bf16 v[96:99], v[64:67], a[104:107], v[96:99]\n\t" \
    "v_cvt_pk_bf16_f32 v112, v80, v81\n\t" "v_mfma_f32_16x16x32_bf16 v[100:103], v[68:71], a[104:107], v[100:103]\n\t" "v_cvt_pk_bf16_f32 v113, v82, v83\n\t" \
    "v_mfma_f32_16x16x32_bf16 v[96:99], v[72:75], a[108:111], v[96:99]\n\t" "v_cvt_pk_bf16_f32 v114, v84, v85\n\t" \
    "v_mfma_f32_16x16x32_bf16 v[100:103], v[76:79], a[108:111], v[100:103]\n\t" "v_cvt_pk_bf16_f32 v115, v86, v87\n\t" \
    "v_mfma_f32_16x16x32_bf16 v[104:107], v[48:51], a[112:115], 0\n\t" "v_cvt_pk_bf16_f32 v116, v88, v89\n\t" \
    "v_mfma_f32_16x16x32_bf16 v[108:111], v[52:55], a[112:115], 0\n\t" "v_cvt_pk_bf16_f32 v117, v90, v91\n\t" \
    "v_mfma_f32_16x16x32_bf16 v[104:107], v[56:59], a[116:119], v[104:107]\n\t" "buffer_store_dwordx4 v[112:115], %[z1], %[rs], s63 offen sc1\n\t" \
    "v_mfma_f32_16x16x32_bf16 v[108:111], v[60:63], a[116:119], v[108:111]\n\t" "v_mfma_f32_16x16x32_bf16 v[104:107], v[64:67], a[120:123], v[104:107]\n\t" \
    "v_cvt_pk_bf16_f32 v118, v92, v93\n\t" "v_mfma_f32_16x16x32_bf16 v[108:111], v[68:71], a[120:123], v[108:111]\n\t" "v_cvt_pk_bf16_f32 v119, v94, v95\n\t" \
    "v_mfma_f32_16x16x32_bf16 v[104:107], v[72:75], a[124:127], v[104:107]\n\t" "buffer_store_dwordx4 v[116:119], %[z2], %[rs], s63 offen sc1\n\t" \
    "v_mfma_f32_16x16x32_bf16 v[108:111], v[76:79], a[124:127], v[108:111]\n\t" "s_add_u32 s63, s63, 0x20000\n\t" \
    "v_mfma_f32_16x16x32_bf16 v[80:83], v[48:51], a[128:131], 0\n\t" "v_mfma_f32_16x16x32_bf16 v[84:87], v[52:55], a[128:131], 0\n\t" \
    "v_mfma_f32_16x16x32_bf16 v[80:83], v[56:59], a[132:135], v[80:83]\n\t" "v_mfma_f32_16x16x32_bf16 v[84:87], v[60:63], a[132:135], v[84:87]\n\t" \
    "v_mfma_f32_16x16x32_bf16 v[80:83], v[64:67], a[136:139], v[80:83]\n\t" "v_cvt_pk_bf16_f32 v112, v96, v97\n\t" \
    "v_mfma_f32_16x16x32_bf16 v[84:87], v[68:71], a[136:139], v[84:87]\n\t" "v_cvt_pk_bf16_f32 v113, v98, v99\n\t" \
    "v_mfma_f32_16x16x32_bf16 v[80:83], v[72:75], a[140:143], v[80:83]\n\t" "v_cvt_pk_bf16_f32 v114, v100, v101\n\t" \
    "v_mfma_f32_16x16x32_bf16 v[84:87], v[76:79], a[140:143], v[84:87]\n\t" "v_cvt_pk_bf16_f32 v115, v102, v103\n\t" \
    "v_mfma_f32_16x16x32_bf16 v[88:91], v[48:51], a[144:147], 0\n\t" "v_cvt_pk_bf16_f32 v116, v104, v105\n\t" "v_mfma_f32_16x16x32_bf16 v[92:95], v[52:55], a[144:147], 0\n\t" \
    "v_cvt_pk_bf16_f32 v117, v106, v107\n\t" "v_mfma_f32_16x16x32_bf16 v[88:91], v[56:59], a[148:151], v[88:91]\n\t" \
    "buffer_store_dwordx4 v[112:115], %[z1], %[rs], s63 offen sc1\n\t" "v_mfma_f32_16x16x32_bf16 v[92:95], v[60:63], a[148:151], v[92:95]\n\t" \
    "v_mfma_f32_16x16x32_bf16 v[88:91], v[64:67], a[152:155], v[88:91]\n\t" "v_cvt_pk_bf16_f32 v118, v108, v109\n\t" \
    "v_mfma_f32_16x16x32_bf16 v[92:95], v[68:71], a[152:155], v[92:95]\n\t" "v_cvt_pk_bf16_f32 v119, v110, v111\n\t" \
    "v_mfma_f32_16x16x32_bf16 v[88:91], v[72:75], a[156:159], v[88:91]\n\t" "buffer_store_dwordx4 v[116:119], %[z2], %[rs], s63 offen sc1\n\t" \
    "v_mfma_f32_16x16x32_bf16 v[92:95], v[76:79], a[156:159], v[92:95]\n\t" "s_add_u32 s63, s63, 0x20000\n\t" "v_mfma_f32_16x16x32_bf16 v[96:99], v[48:51], a[160:163], 0\n\t" \
    "v_mfma_f32_16x16x32_bf16 v[100:103], v[52:55], a[160:163], 0\n\t" "v_mfma_f32_16x16x32_bf16 v[96:99], v[56:59], a[164:167], v[96:99]\n\t" \
    "v_mfma_f32_16x16x32_bf16 v[100:103], v[60:63], a[164:167], v[100:103]\n\t" "v_mfma_f32_16x16x32_bf16 v[96:99], v[64:67], a[168:171], v[96:99]\n\t" \
    "v_cvt_pk_bf16_f32 v112, v80, v81\n\t" "v_mfma_f32_16x16x32_bf16 v[100:103], v[68:71], a[168:171], v[100:103]\n\t" "v_cvt_pk_bf16_f32 v113, v82, v83\n\t" \
    "v_mfma_f32_16x16x32_bf16 v[96:99], v[72:75], a[172:175], v[96:99]\n\t" "v_cvt_pk_bf16_f32 v114, v84, v85\n\t" \
    "v_mfma_f32_16x16x32_bf16 v[100:103], v[76:79], a[172:175], v[100:103]\n\t" "v_cvt_pk_bf16_f32 v115, v86, v87\n\t" \
    "v_mfma_f32_16x16x32_bf16 v[104:107], v[48:51], a[176:179], 0\n\t" "v_cvt_pk_bf16_f32 v116, v88, v89\n\t" \
    "v_mfma_f32_16x16x32_bf16 v[108:111], v[52:55], a[176:179], 0\n\t" "v_cvt_pk_bf16_f32 v117, v90, v91\n\t" \
    "v_mfma_f32_16x16x32_bf16 v[104:107], v[56:59], a[180:183], v[104:107]\n\t" "buffer_store_dwordx4 v[112:115], %[z1], %[rs], s63 offen sc1\n\t" \
    "v_mfma_f32_16x16x32_bf16 v[108:111], v[60:63], a[180:183], v[108:111]\n\t" "v_mfma_f32_16x16x32_bf16 v[104:107], v[64:67], a[184:187], v[104:107]\n\t" \
    "v_cvt_pk_bf16_f32 v118, v92, v93\n\t" "v_mfma_f32_16x16x32_bf16 v[108:111], v[68:71], a[184:187], v[108:111]\n\t" "v_cvt_pk_bf16_f32 v119, v94, v95\n\t" \
    "v_mfma_f32_16x16x32_bf16 v[104:107], v[72:75], a[188:191], v[104:107]\n\t" "buffer_store_dwordx4 v[116:119], %[z2], %[rs], s63 offen sc1\n\t" \
    "v_mfma_f32_16x16x32_bf16 v[108:111], v[76:79], a[188:191], v[108:111]\n\t" "s_add_u32 s63, s63, 0x20000\n\t" \
    "v_mfma_f32_16x16x32_bf16 v[80:83], v[48:51], a[192:195], 0\n\t" "v_mfma_f32_16x16x32_bf16 v[84:87], v[52:55], a[192:195], 0\n\t" \
    "v_mfma_f32_16x16x32_bf16 v[80:83], v[56:59], a[196:199], v[80:83]\n\t" "v_mfma_f32_16x16x32_bf16 v[84:87], v[60:63], a[196:199], v[84:87]\n\t" \
    "v_mfma_f32_16x16x32_bf16 v[80:83], v[64:67], a[200:203], v[80:83]\n\t" "v_cvt_pk_bf16_f32 v112, v96, v97\n\t" \
    "v_mfma_f32_16x16x32_bf16 v[84:87], v[68:71], a[200:203], v[84:87]\n\t" "v_cvt_pk_bf16_f32 v113, v98, v99\n\t" \
    "v_mfma_f32_16x16x32_bf16 v[80:83], v[72:75], a[204:207], v[80:83]\n\t" "v_cvt_pk_bf16_f32 v114, v100, v101\n\t" \
    "v_mfma_f32_16x16x32_bf16 v[84:87], v[76:79], a[204:207], v[84:87]\n\t" "v_cvt_pk_bf16_f32 v115, v102, v103\n\t" \
    "v_mfma_f32_16x16x32_bf16 v[88:91], v[48:51], a[208:211], 0\n\t" "v_cvt_pk_bf16_f32 v116, v104, v105\n\t" "v_mfma_f32_16x16x32_bf16 v[92:95], v[52:55], a[208:211], 0\n\t" \
    "v_cvt_pk_bf16_f32 v117, v106, v107\n\t" "v_mfma_f32_16x16x32_bf16 v[88:91], v[56:59], a[212:215], v[88:91]\n\t" \
    "buffer_store_dwordx4 v[112:115], %[z1], %[rs], s63 offen sc1\n\t" "v_mfma_f32_16x16x32_bf16 v[92:95], v[60:63], a[212:215], v[92:95]\n\t" \
    "v_mfma_f32_16x16x32_bf16 v[88:91], v[64:67], a[216:219], v[88:91]\n\t" "v_cvt_pk_bf16_f32 v118, v108, v109\n\t" \
    "v_mfma_f32_16x16x32_bf16 v[92:95], v[68:71], a[216:219], v[92:95]\n\t" "v_cvt_pk_bf16_f32 v119, v110, v111\n\t" \
    "v_mfma_f32_16x16x32_bf16 v[88:91], v[72:75], a[220:223], v[88:91]\n\t" "buffer_store_dwordx4 v[116:119], %[z2], %[rs], s63 offen sc1\n\t" \
    "v_mfma_f32_16x16x32_bf16 v[92:95], v[76:79], a[220:223], v[92:95]\n\t" "s_add_u32 s63, s63, 0x20000\n\t" "v_mfma_f32_16x16x32_bf16 v[96:99], v[48:51], a[224:227], 0\n\t" \
    "v_mfma_f32_16x16x32_bf16 v[100:103], v[52:55], a[224:227], 0\n\t" "v_mfma_f32_16x16x32_bf16 v[96:99], v[56:59], a[228:231], v[96:99]\n\t" \
    "v_mfma_f32_16x16x32_bf16 v[100:103], v[60:63], a[228:231], v[100:103]\n\t" "v_mfma_f32_16x16x32_bf16 v[96:99], v[64:67], a[232:235], v[96:99]\n\t" \
    "v_cvt_pk_bf16_f32 v112, v80, v81\n\t" "v_mfma_f32_16x16x32_bf16 v[100:103], v[68:71], a[232:235], v[100:103]\n\t" "v_cvt_pk_bf16_f32 v113, v82, v83\n\t" \
    "v_mfma_f32_16x16x32_bf16 v[96:99], v[72:75], a[236:239], v[96:99]\n\t" "v_cvt_pk_bf16_f32 v114, v84, v85\n\t" \
    "v_mfma_f32_16x16x32_bf16 v[100:103], v[76:79], a[236:239], v[100:103]\n\t" "v_cvt_pk_bf16_f32 v115, v86, v87\n\t" \
    "v_mfma_f32_16x16x32_bf16 v[104:107], v[48:51], a[240:243], 0\n\t" "v_cvt_pk_bf16_f32 v116, v88, v89\n\t" \
    "v_mfma_f32_16x16x32_bf16 v[108:111], v[52:55], a[240:243], 0\n\t" "v_cvt_pk_bf16_f32 v117, v90, v91\n\t" \
    "v_mfma_f32_16x16x32_bf16 v[104:107], v[56:59], a[244:247], v[104:107]\n\t" "buffer_store_dwordx4 v[112:115], %[z1], %[rs], s63 offen sc1\n\t" \
    "v_mfma_f32_16x16x32_bf16 v[108:111], v[60:63], a[244:247], v[108:111]\n\t" "v_mfma_f32_16x16x32_bf16 v[104:107], v[64:67], a[248:251], v[104:107]\n\t" \
    "v_cvt_pk_bf16_f32 v118, v92, v93\n\t" "v_cvt_pk_bf16_f32 v119, v94, v95\n\t" "s_waitcnt vmcnt(11)\n\t" "v_readfirstlane_b32 s66, v120\n\t" \
    "v_mfma_f32_16x16x32_bf16 v[108:111], v[68:71], a[248:251], v[108:111]\n\t" "v_readfirstlane_b32 s67, v121\n\t" "v_mov_b32 v122, s60\n\t" \
    "buffer_store_dword v122, %[fo], %[rc], %[fz] offen sc1\n\t" "v_mfma_f32_16x16x32_bf16 v[104:107], v[72:75], a[252:255], v[104:107]\n\t" \
    "buffer_store_dwordx4 v[116:119], %[z2], %[rs], s63 offen sc1\n\t" "s_add_u32 s63, s63, 0x20000\n\t" "s_max_u32 s61, s61, s66\n\t" \
    "v_mfma_f32_16x16x32_bf16 v[108:111], v[76:79], a[252:255], v[108:111]\n\t" "s_max_u32 s62, s62, s67\n\t" "s_add_u32 s60, s60, 1\n\t" ZSUM "33:\n\t" "s_branch 10b\n\t" \
    "40:\n\t" "v_mfma_f32_16x16x32_bf16 v[80:83], v[16:19], a[0:3], 0\n\t" "v_mfma_f32_16x16x32_bf16 v[84:87], v[20:23], a[0:3], 0\n\t" \
    "v_mfma_f32_16x16x32_bf16 v[80:83], v[24:27], a[4:7], v[80:83]\n\t" "v_mfma_f32_16x16x32_bf16 v[84:87], v[28:31], a[4:7], v[84:87]\n\t" \
    "v_mfma_f32_16x16x32_bf16 v[80:83], v[32:35], a[8:11], v[80:83]\n\t" "v_cvt_pk_bf16_f32 v112, v96, v97\n\t" \
    "v_mfma_f32_16x16x32_bf16 v[84:87], v[36:39], a[8:11], v[84:87]\n\t" "v_cvt_pk_bf16_f32 v113, v98, v99\n\t" \
    "v_mfma_f32_16x16x32_bf16 v[80:83], v[40:43], a[12:15], v[80:83]\n\t" "v_cvt_pk_bf16_f32 v114, v100, v101\n\t" \
    "v_mfma_f32_16x16x32_bf16 v[84:87], v[44:47], a[12:15], v[84:87]\n\t" "v_cvt_pk_bf16_f32 v115, v102, v103\n\t" \
    "v_mfma_f32_16x16x32_bf16 v[88:91], v[16:19], a[16:19], 0\n\t" "v_cvt_pk_bf16_f32 v116, v104, v105\n\t" "v_mfma_f32_16x16x32_bf16 v[92:95], v[20:23], a[16:19], 0\n\t" \
    "v_cvt_pk_bf16_f32 v117, v106, v107\n\t" "v_mfma_f32_16x16x32_bf16 v[88:91], v[24:27], a[20:23], v[88:91]\n\t" \
    "buffer_store_dwordx4 v[112:115], %[z1], %[rs], s63 offen sc1\n\t" "v_mfma_f32_16x16x32_bf16 v[92:95], v[28:31], a[20:23], v[92:95]\n\t" \
    "v_mfma_f32_16x16x32_bf16 v[88:91], v[32:35], a[24:27], v[88:91]\n\t" "v_cvt_pk_bf16_f32 v118, v108, v109\n\t" \
    "v_mfma_f32_16x16x32_bf16 v[92:95], v[36:39], a[24:27], v[92:95]\n\t" "v_cvt_pk_bf16_f32 v119, v110, v111\n\t" \
    "v_mfma_f32_16x16x32_bf16 v[88:91], v[40:43], a[28:31], v[88:91]\n\t" "buffer_store_dwordx4 v[116:119], %[z2], %[rs], s63 offen sc1\n\t" "s_and_b32 s63, s60, %[rm]\n\t" \
    "v_mfma_f32_16x16x32_bf16 v[92:95], v[44:47], a[28:31], v[92:95]\n\t" "s_lshl_b32 s63, s63, 22\n\t" "s_add_u32 s63, s63, %[zb]\n\t" \
    "v_mfma_f32_16x16x32_bf16 v[96:99], v[16:19], a[32:35], 0\n\t" "buffer_load_dword v120, %[fo], %[rc], %[fv] offen sc1\n\t" \
    "v_mfma_f32_16x16x32_bf16 v[100:103], v[20:23], a[32:35], 0\n\t" "v_mfma_f32_16x16x32_bf16 v[96:99], v[24:27], a[36:39], v[96:99]\n\t" \
    "buffer_load_dword v121, %[fo], %[rc], %[fs] offen sc1\n\t" "v_mfma_f32_16x16x32_bf16 v[100:103], v[28:31], a[36:39], v[100:103]\n\t" \
    "v_mfma_f32_16x16x32_bf16 v[96:99], v[32:35], a[40:43], v[96:99]\n\t" "v_cvt_pk_bf16_f32 v112, v80, v81\n\t" \
    "v_mfma_f32_16x16x32_bf16 v[100:103], v[36:39], a[40:43], v[100:103]\n\t" "v_cvt_pk_bf16_f32 v113, v82, v83\n\t" \
    "v_mfma_f32_16x16x32_bf16 v[96:99], v[40:43], a[44:47], v[96:99]\n\t" "v_cvt_pk_bf16_f32 v114, v84, v85\n\t" \
    "v_mfma_f32_16x16x32_bf16 v[100:103], v[44:47], a[44:47], v[100:103]\n\t" "v_cvt_pk_bf16_f32 v115, v86, v87\n\t" \
    "v_mfma_f32_16x16x32_bf16 v[104:107], v[16:19], a[48:51], 0\n\t" "v_cvt_pk_bf16_f32 v116, v88, v89\n\t" "v_mfma_f32_16x16x32_bf16 v[108:111], v[20:23], a[48:51], 0\n\t" \
    "v_cvt_pk_bf16_f32 v117, v90, v91\n\t" "v_mfma_f32_16x16x32_bf16 v[104:107], v[24:27], a[52:55], v[104:107]\n\t" \
    "buffer_store_dwordx4 v[112:115], %[z1], %[rs], s63 offen sc1\n\t" "v_mfma_f32_16x16x32_bf16 v[108:111], v[28:31], a[52:55], v[108:111]\n\t" \
    "v_mfma_f32_16x16x32_bf16 v[104:107], v[32:35], a[56:59], v[104:107]\n\t" "v_cvt_pk_bf16_f32 v118, v92, v93\n\t" \
    "v_mfma_f32_16x16x32_bf16 v[108:111], v[36:39], a[56:59], v[108:111]\n\t" "v_cvt_pk_bf16_f32 v119, v94, v95\n\t" \
    "v_mfma_f32_16x16x32_bf16 v[104:107], v[40:43], a[60:63], v[104:107]\n\t" "buffer_store_dwordx4 v[116:119], %[z2], %[rs], s63 offen sc1\n\t" \
    "v_mfma_f32_16x16x32_bf16 v[108:111], v[44:47], a[60:63], v[108:111]\n\t" "s_add_u32 s63, s63, 0x20000\n\t" "v_mfma_f32_16x16x32_bf16 v[80:83], v[16:19], a[64:67], 0\n\t" \
    "s_add_u32 s65, s60, 1\n\t" "s_lshr_b32 s66, s65, 2\n\t" "v_mfma_f32_16x16x32_bf16 v[84:87], v[20:23], a[64:67], 0\n\t" "s_xor_b32 s66, s66, %[xp]\n\t" \
    "s_and_b32 s66, s66, 1\n\t" "v_mfma_f32_16x16x32_bf16 v[80:83], v[24:27], a[68:71], v[80:83]\n\t" "v_mfma_f32_16x16x32_bf16 v[84:87], v[28:31], a[68:71], v[84:87]\n\t" \
    "v_mfma_f32_16x16x32_bf16 v[80:83], v[32:35], a[72:75], v[80:83]\n\t" "v_cvt_pk_bf16_f32 v112, v96, v97\n\t" \
    "v_mfma_f32_16x16x32_bf16 v[84:87], v[36:39], a[72:75], v[84:87]\n\t" "v_cvt_pk_bf16_f32 v113, v98, v99\n\t" \
    "v_mfma_f32_16x16x32_bf16 v[80:83], v[40:43], a[76:79], v[80:83]\n\t" "v_cvt_pk_bf16_f32 v114, v100, v101\n\t" \
    "v_mfma_f32_16x16x32_bf16 v[84:87], v[44:47], a[76:79], v[84:87]\n\t" "v_cvt_pk_bf16_f32 v115, v102, v103\n\t" \
    "v_mfma_f32_16x16x32_bf16 v[88:91], v[16:19], a[80:83], 0\n\t" "v_cvt_pk_bf16_f32 v116, v104, v105\n\t" "v_mfma_f32_16x16x32_bf16 v[92:95], v[20:23], a[80:83], 0\n\t" \
    "v_cvt_pk_bf16_f32 v117, v106, v107\n\t" "v_mfma_f32_16x16x32_bf16 v[88:91], v[24:27], a[84:87], v[88:91]\n\t" \
    "buffer_store_dwordx4 v[112:115], %[z1], %[rs], s63 offen sc1\n\t" "v_mfma_f32_16x16x32_bf16 v[92:95], v[28:31], a[84:87], v[92:95]\n\t" \
    "v_mfma_f32_16x16x32_bf16 v[88:91], v[32:35], a[88:91], v[88:91]\n\t" "v_cvt_pk_bf16_f32 v118, v108, v109\n\t" \
    "v_mfma_f32_16x16x32_bf16 v[92:95], v[36:39], a[88:91], v[92:95]\n\t" "v_cvt_pk_bf16_f32 v119, v110, v111\n\t" \
    "v_mfma_f32_16x16x32_bf16 v[88:91], v[40:43], a[92:95], v[88:91]\n\t" "buffer_store_dwordx4 v[116:119], %[z2], %[rs], s63 offen sc1\n\t" \
    "v_mfma_f32_16x16x32_bf16 v[92:95], v[44:47], a[92:95], v[92:95]\n\t" "s_add_u32 s63, s63, 0x20000\n\t" "v_mfma_f32_16x16x32_bf16 v[96:99], v[16:19], a[96:99], 0\n\t" \
    "s_lshl_b32 s66, s66, 13\n\t" "s_lshl_b32 s64, s65, 19\n\t" "v_mfma_f32_16x16x32_bf16 v[100:103], v[20:23], a[96:99], 0\n\t" "s_add_u32 s64, s64, s66\n\t" \
    "s_add_u32 s64, s64, %[vb]\n\t" "v_mfma_f32_16x16x32_bf16 v[96:99], v[24:27], a[100:103], v[96:99]\n\t" \
    "v_mfma_f32_16x16x32_bf16 v[100:103], v[28:31], a[100:103], v[100:103]\n\t" "v_mfma_f32_16x16x32_bf16 v[96:99], v[32:35], a[104:107], v[96:99]\n\t" \
    "v_cvt_pk_bf16_f32 v112, v80, v81\n\t" "v_mfma_f32_16x16x32_bf16 v[100:103], v[36:39], a[104:107], v[100:103]\n\t" "v_cvt_pk_bf16_f32 v113, v82, v83\n\t" \
    "v_mfma_f32_16x16x32_bf16 v[96:99], v[40:43], a[108:111], v[96:99]\n\t" "v_cvt_pk_bf16_f32 v114, v84, v85\n\t" \
    "v_mfma_f32_16x16x32_bf16 v[100:103], v[44:47], a[108:111], v[100:103]\n\t" "v_cvt_pk_bf16_f32 v115, v86, v87\n\t" \
    "v_mfma_f32_16x16x32_bf16 v[104:107], v[16:19], a[112:115], 0\n\t" "v_cvt_pk_bf16_f32 v116, v88, v89\n\t" \
    "v_mfma_f32_16x16x32_bf16 v[108:111], v[20:23], a[112:115], 0\n\t" "v_cvt_pk_bf16_f32 v117, v90, v91\n\t" \
    "v_mfma_f32_16x16x32_bf16 v[104:107], v[24:27], a[116:119], v[104:107]\n\t" "buffer_store_dwordx4 v[112:115], %[z1], %[rs], s63 offen sc1\n\t" \
    "v_mfma_f32_16x16x32_bf16 v[108:111], v[28:31], a[116:119], v[108:111]\n\t" "v_mfma_f32_16x16x32_bf16 v[104:107], v[32:35], a[120:123], v[104:107]\n\t" \
    "v_cvt_pk_bf16_f32 v118, v92, v93\n\t" "v_mfma_f32_16x16x32_bf16 v[108:111], v[36:39], a[120:123], v[108:111]\n\t" "v_cvt_pk_bf16_f32 v119, v94, v95\n\t" \
    "v_mfma_f32_16x16x32_bf16 v[104:107], v[40:43], a[124:127], v[104:107]\n\t" "buffer_store_dwordx4 v[116:119], %[z2], %[rs], s63 offen sc1\n\t" \
    "v_mfma_f32_16x16x32_bf16 v[108:111], v[44:47], a[124:127], v[108:111]\n\t" "s_add_u32 s63, s63, 0x20000\n\t" \
    "v_mfma_f32_16x16x32_bf16 v[80:83], v[16:19], a[128:131], 0\n\t" "s_waitcnt vmcnt(6)\n\t" "v_readfirstlane_b32 s66, v120\n\t" "v_readfirstlane_b32 s67, v121\n\t" \
    "v_mfma_f32_16x16x32_bf16 v[84:87], v[20:23], a[128:131], 0\n\t" "v_mov_b32 v122, s60\n\t" "buffer_store_dword v122, %[fo], %[rc], %[fz] offen sc1\n\t" \
    "v_mfma_f32_16x16x32_bf16 v[80:83], v[24:27], a[132:135], v[80:83]\n\t" "s_max_u32 s61, s61, s66\n\t" "s_max_u32 s62, s62, s67\n\t" "s_cmp_gt_u32 s61, s65\n\t" \
    "v_mfma_f32_16x16x32_bf16 v[84:87], v[28:31], a[132:135], v[84:87]\n\t" "s_cselect_b32 s65, 1, 0\n\t" "s_cselect_b32 s64, s64, 0x7f000000\n\t" \
    "v_mfma_f32_16x16x32_bf16 v[80:83], v[32:35], a[136:139], v[80:83]\n\t" "buffer_load_dwordx4 v[48:51], %[ao], %[rs], s64 offen offset:0 sc1\n\t" \
    "v_cvt_pk_bf16_f32 v112, v96, v97\n\t" "v_mfma_f32_16x16x32_bf16 v[84:87], v[36:39], a[136:139], v[84:87]\n\t" "v_cvt_pk_bf16_f32 v113, v98, v99\n\t" \
    "v_mfma_f32_16x16x32_bf16 v[80:83], v[40:43], a[140:143], v[80:83]\n\t" "buffer_load_dwordx4 v[52:55], %[ao], %[rs], s64 offen offset:256 sc1\n\t" \
    "v_cvt_pk_bf16_f32 v114, v100, v101\n\t" "v_mfma_f32_16x16x32_bf16 v[84:87], v[44:47], a[140:143], v[84:87]\n\t" "v_cvt_pk_bf16_f32 v115, v102, v103\n\t" \
    "v_mfma_f32_16x16x32_bf16 v[88:91], v[16:19], a[144:147], 0\n\t" "buffer_load_dwordx4 v[56:59], %[ao], %[rs], s64 offen offset:2048 sc1\n\t" \
    "v_cvt_pk_bf16_f32 v116, v104, v105\n\t" "v_mfma_f32_16x16x32_bf16 v[92:95], v[20:23], a[144:147], 0\n\t" "v_cvt_pk_bf16_f32 v117, v106, v107\n\t" \
    "v_mfma_f32_16x16x32_bf16 v[88:91], v[24:27], a[148:151], v[88:91]\n\t" "buffer_load_dwordx4 v[60:63], %[ao], %[rs], s64 offen offset:2304 sc1\n\t" \
    "v_mfma_f32_16x16x32_bf16 v[92:95], v[28:31], a[148:151], v[92:95]\n\t" "buffer_store_dwordx4 v[112:115], %[z1], %[rs], s63 offen sc1\n\t" \
    "v_mfma_f32_16x16x32_bf16 v[88:91], v[32:35], a[152:155], v[88:91]\n\t" "buffer_load_dwordx4 v[64:67], %[a1], %[rs], s64 offen offset:0 sc1\n\t" \
    "v_cvt_pk_bf16_f32 v118, v108, v109\n\t" "v_mfma_f32_16x16x32_bf16 v[92:95], v[36:39], a[152:155], v[92:95]\n\t" "v_cvt_pk_bf16_f32 v119, v110, v111\n\t" \
    "v_mfma_f32_16x16x32_bf16 v[88:91], v[40:43], a[156:159], v[88:91]\n\t" "buffer_load_dwordx4 v[68:71], %[a1], %[rs], s64 offen offset:256 sc1\n\t" \
    "buffer_store_dwordx4 v[116:119], %[z2], %[rs], s63 offen sc1\n\t" "v_mfma_f32_16x16x32_bf16 v[92:95], v[44:47], a[156:159], v[92:95]\n\t" \
    "s_add_u32 s63, s63, 0x20000\n\t" "v_mfma_f32_16x16x32_bf16 v[96:99], v[16:19], a[160:163], 0\n\t" \
    "buffer_load_dwordx4 v[72:75], %[a1], %[rs], s64 offen offset:2048 sc1\n\t" "v_mfma_f32_16x16x32_bf16 v[100:103], v[20:23], a[160:163], 0\n\t" \
    "v_mfma_f32_16x16x32_bf16 v[96:99], v[24:27], a[164:167], v[96:99]\n\t" "buffer_load_dwordx4 v[76:79], %[a1], %[rs], s64 offen offset:2304 sc1\n\t" \
    "v_mfma_f32_16x16x32_bf16 v[100:103], v[28:31], a[164:167], v[100:103]\n\t" "v_mfma_f32_16x16x32_bf16 v[96:99], v[32:35], a[168:171], v[96:99]\n\t" \
    "v_cvt_pk_bf16_f32 v112, v80, v81\n\t" "v_mfma_f32_16x16x32_bf16 v[100:103], v[36:39], a[168:171], v[100:103]\n\t" "v_cvt_pk_bf16_f32 v113, v82, v83\n\t" \
    "v_mfma_f32_16x16x32_bf16 v[96:99], v[40:43], a[172:175], v[96:99]\n\t" "v_cvt_pk_bf16_f32 v114, v84, v85\n\t" \
    "v_mfma_f32_16x16x32_bf16 v[100:103], v[44:47], a[172:175], v[100:103]\n\t" "v_cvt_pk_bf16_f32 v115, v86, v87\n\t" \
    "v_mfma_f32_16x16x32_bf16 v[104:107], v[16:19], a[176:179], 0\n\t" "v_cvt_pk_bf16_f32 v116, v88, v89\n\t" \
    "v_mfma_f32_16x16x32_bf16 v[108:111], v[20:23], a[176:179], 0\n\t" "v_cvt_pk_bf16_f32 v117, v90, v91\n\t" \
    "v_mfma_f32_16x16x32_bf16 v[104:107], v[24:27], a[180:183], v[104:107]\n\t" "buffer_store_dwordx4 v[112:115], %[z1], %[rs], s63 offen sc1\n\t" \
    "v_mfma_f32_16x16x32_bf16 v[108:111], v[28:31], a[180:183], v[108:111]\n\t" "v_mfma_f32_16x16x32_bf16 v[104:107], v[32:35], a[184:187], v[104:107]\n\t" \
    "v_cvt_pk_bf16_f32 v118, v92, v93\n\t" "v_mfma_f32_16x16x32_bf16 v[108:111], v[36:39], a[184:187], v[108:111]\n\t" "v_cvt_pk_bf16_f32 v119, v94, v95\n\t" \
    "v_mfma_f32_16x16x32_bf16 v[104:107], v[40:43], a[188:191], v[104:107]\n\t" "buffer_store_dwordx4 v[116:119], %[z2], %[rs], s63 offen sc1\n\t" \
    "v_mfma_f32_16x16x32_bf16 v[108:111], v[44:47], a[188:191], v[108:111]\n\t" "s_add_u32 s63, s63, 0x20000\n\t" \
    "v_mfma_f32_16x16x32_bf16 v[80:83], v[16:19], a[192:195], 0\n\t" "v_mfma_f32_16x16x32_bf16 v[84:87], v[20:23], a[192:195], 0\n\t" \
    "v_mfma_f32_16x16x32_bf16 v[80:83], v[24:27], a[196:199], v[80:83]\n\t" "v_mfma_f32_16x16x32_bf16 v[84:87], v[28:31], a[196:199], v[84:87]\n\t" \
    "v_mfma_f32_16x16x32_bf16 v[80:83], v[32:35], a[200:203], v[80:83]\n\t" "v_cvt_pk_bf16_f32 v112, v96, v97\n\t" \
    "v_mfma_f32_16x16x32_bf16 v[84:87], v[36:39], a[200:203], v[84:87]\n\t" "v_cvt_pk_bf16_f32 v113, v98, v99\n\t" \
    "v_mfma_f32_16x16x32_bf16 v[80:83], v[40:43], a[204:207], v[80:83]\n\t" "v_cvt_pk_bf16_f32 v114, v100, v101\n\t" \
    "v_mfma_f32_16x16x32_bf16 v[84:87], v[44:47], a[204:207], v[84:87]\n\t" "v_cvt_pk_bf16_f32 v115, v102, v103\n\t" \
    "v_mfma_f32_16x16x32_bf16 v[88:91], v[16:19], a[208:211], 0\n\t" "v_cvt_pk_bf16_f32 v116, v104, v105\n\t" "v_mfma_f32_16x16x32_bf16 v[92:95], v[20:23], a[208:211], 0\n\t" \
    "v_cvt_pk_bf16_f32 v117, v106, v107\n\t" "v_mfma_f32_16x16x32_bf16 v[88:91], v[24:27], a[212:215], v[88:91]\n\t" \
    "buffer_store_dwordx4 v[112:115], %[z1], %[rs], s63 offen sc1\n\t" "v_mfma_f32_16x16x32_bf16 v[92:95], v[28:31], a[212:215], v[92:95]\n\t" \
    "v_mfma_f32_16x16x32_bf16 v[88:91], v[32:35], a[216:219], v[88:91]\n\t" "v_cvt_pk_bf16_f32 v118, v108, v109\n\t" \
    "v_mfma_f32_16x16x32_bf16 v[92:95], v[36:39], a[216:219], v[92:95]\n\t" "v_cvt_pk_bf16_f32 v119, v110, v111\n\t" \
    "v_mfma_f32_16x16x32_bf16 v[88:91], v[40:43], a[220:223], v[88:91]\n\t" "buffer_store_dwordx4 v[116:119], %[z2], %[rs], s63 offen sc1\n\t" \
    "v_mfma_f32_16x16x32_bf16 v[92:95], v[44:47], a[220:223], v[92:95]\n\t" "s_add_u32 s63, s63, 0x20000\n\t" "v_mfma_f32_16x16x32_bf16 v[96:99], v[16:19], a[224:227], 0\n\t" \
    "v_mfma_f32_16x16x32_bf16 v[100:103], v[20:23], a[224:227], 0\n\t" "v_mfma_f32_16x16x32_bf16 v[96:99], v[24:27], a[228:231], v[96:99]\n\t" \
    "v_mfma_f32_16x16x32_bf16 v[100:103], v[28:31], a[228:231], v[100:103]\n\t" "v_mfma_f32_16x16x32_bf16 v[96:99], v[32:35], a[232:235], v[96:99]\n\t" \
    "v_cvt_pk_bf16_f32 v112, v80, v81\n\t" "v_mfma_f32_16x16x32_bf16 v[100:103], v[36:39], a[232:235], v[100:103]\n\t" "v_cvt_pk_bf16_f32 v113, v82, v83\n\t" \
    "v_mfma_f32_16x16x32_bf16 v[96:99], v[40:43], a[236:239], v[96:99]\n\t" "v_cvt_pk_bf16_f32 v114, v84, v85\n\t" \
    "v_mfma_f32_16x16x32_bf16 v[100:103], v[44:47], a[236:239], v[100:103]\n\t" "v_cvt_pk_bf16_f32 v115, v86, v87\n\t" \
    "v_mfma_f32_16x16x32_bf16 v[104:107], v[16:19], a[240:243], 0\n\t" "v_cvt_pk_bf16_f32 v116, v88, v89\n\t" \
    "v_mfma_f32_16x16x32_bf16 v[108:111], v[20:23], a[240:243], 0\n\t" "v_cvt_pk_bf16_f32 v117, v90, v91\n\t" \
    "v_mfma_f32_16x16x32_bf16 v[104:107], v[24:27], a[244:247], v[104:107]\n\t" "buffer_store_dwordx4 v[112:115], %[z1], %[rs], s63 offen sc1\n\t" \
    "v_mfma_f32_16x16x32_bf16 v[108:111], v[28:31], a[244:247], v[108:111]\n\t" "v_mfma_f32_16x16x32_bf16 v[104:107], v[32:35], a[248:251], v[104:107]\n\t" \
    "v_cvt_pk_bf16_f32 v118, v92, v93\n\t" "v_mfma_f32_16x16x32_bf16 v[108:111], v[36:39], a[248:251], v[108:111]\n\t" "v_cvt_pk_bf16_f32 v119, v94, v95\n\t" \
    "v_mfma_f32_16x16x32_bf16 v[104:107], v[40:43], a[252:255], v[104:107]\n\t" "buffer_store_dwordx4 v[116:119], %[z2], %[rs], s63 offen sc1\n\t" \
    "s_add_u32 s63, s63, 0x20000\n\t" "s_add_u32 s60, s60, 1\n\t" "v_mfma_f32_16x16x32_bf16 v[108:111], v[44:47], a[252:255], v[108:111]\n\t" "s_cmp_eq_u32 s65, 0\n\t" \
    "s_waitcnt vmcnt(6)\n\t" ZSUM "s_cbranch_scc0 23b\n\t" "s_branch 20f\n\t" "41:\n\t" "v_mfma_f32_16x16x32_bf16 v[80:83], v[48:51], a[0:3], 0\n\t" \
    "v_mfma_f32_16x16x32_bf16 v[84:87], v[52:55], a[0:3], 0\n\t" "v_mfma_f32_16x16x32_bf16 v[80:83], v[56:59], a[4:7], v[80:83]\n\t" \
    "v_mfma_f32_16x16x32_bf16 v[84:87], v[60:63], a[4:7], v[84:87]\n\t" "v_mfma_f32_16x16x32_bf16 v[80:83], v[64:67], a[8:11], v[80:83]\n\t" \
    "v_cvt_pk_bf16_f32 v112, v96, v97\n\t" "v_mfma_f32_16x16x32_bf16 v[84:87], v[68:71], a[8:11], v[84:87]\n\t" "v_cvt_pk_bf16_f32 v113, v98, v99\n\t" \
    "v_mfma_f32_16x16x32_bf16 v[80:83], v[72:75], a[12:15], v[80:83]\n\t" "v_cvt_pk_bf16_f32 v114, v100, v101\n\t" \
    "v_mfma_f32_16x16x32_bf16 v[84:87], v[76:79], a[12:15], v[84:87]\n\t" "v_cvt_pk_bf16_f32 v115, v102, v103\n\t" \
    "v_mfma_f32_16x16x32_bf16 v[88:91], v[48:51], a[16:19], 0\n\t" "v_cvt_pk_bf16_f32 v116, v104, v105\n\t" "v_mfma_f32_16x16x32_bf16 v[92:95], v[52:55], a[16:19], 0\n\t" \
    "v_cvt_pk_bf16_f32 v117, v106, v107\n\t" "v_mfma_f32_16x16x32_bf16 v[88:91], v[56:59], a[20:23], v[88:91]\n\t" \
    "buffer_store_dwordx4 v[112:115], %[z1], %[rs], s63 offen sc1\n\t" "v_mfma_f32_16x16x32_bf16 v[92:95], v[60:63], a[20:23], v[92:95]\n\t" \
    "v_mfma_f32_16x16x32_bf16 v[88:91], v[64:67], a[24:27], v[88:91]\n\t" "v_cvt_pk_bf16_f32 v118, v108, v109\n\t" \
    "v_mfma_f32_16x16x32_bf16 v[92:95], v[68:71], a[24:27], v[92:95]\n\t" "v_cvt_pk_bf16_f32 v119, v110, v111\n\t" \
    "v_mfma_f32_16x16x32_bf16 v[88:91], v[72:75], a[28:31], v[88:91]\n\t" "buffer_store_dwordx4 v[116:119], %[z2], %[rs], s63 offen sc1\n\t" "s_and_b32 s63, s60, %[rm]\n\t" \
    "v_mfma_f32_16x16x32_bf16 v[92:95], v[76:79], a[28:31], v[92:95]\n\t" "s_lshl_b32 s63, s63, 22\n\t" "s_add_u32 s63, s63, %[zb]\n\t" \
    "v_mfma_f32_16x16x32_bf16 v[96:99], v[48:51], a[32:35], 0\n\t" "buffer_load_dword v120, %[fo], %[rc], %[fv] offen sc1\n\t" \
    "v_mfma_f32_16x16x32_bf16 v[100:103], v[52:55], a[32:35], 0\n\t" "v_mfma_f32_16x16x32_bf16 v[96:99], v[56:59], a[36:39], v[96:99]\n\t" \
    "buffer_load_dword v121, %[fo], %[rc], %[fs] offen sc1\n\t" "v_mfma_f32_16x16x32_bf16 v[100:103], v[60:63], a[36:39], v[100:103]\n\t" \
    "v_mfma_f32_16x16x32_bf16 v[96:99], v[64:67], a[40:43], v[96:99]\n\t" "v_cvt_pk_bf16_f32 v112, v80, v81\n\t" \
    "v_mfma_f32_16x16x32_bf16 v[100:103], v[68:71], a[40:43], v[100:103]\n\t" "v_cvt_pk_bf16_f32 v113, v82, v83\n\t" \
    "v_mfma_f32_16x16x32_bf16 v[96:99], v[72:75], a[44:47], v[96:99]\n\t" "v_cvt_pk_bf16_f32 v114, v84, v85\n\t" \
    "v_mfma_f32_16x16x32_bf16 v[100:103], v[76:79], a[44:47], v[100:103]\n\t" "v_cvt_pk_bf16_f32 v115, v86, v87\n\t" \
    "v_mfma_f32_16x16x32_bf16 v[104:107], v[48:51], a[48:51], 0\n\t" "v_cvt_pk_bf16_f32 v116, v88, v89\n\t" "v_mfma_f32_16x16x32_bf16 v[108:111], v[52:55], a[48:51], 0\n\t" \
    "v_cvt_pk_bf16_f32 v117, v90, v91\n\t" "v_mfma_f32_16x16x32_bf16 v[104:107], v[56:59], a[52:55], v[104:107]\n\t" \
    "buffer_store_dwordx4 v[112:115], %[z1], %[rs], s63 offen sc1\n\t" "v_mfma_f32_16x16x32_bf16 v[108:111], v[60:63], a[52:55], v[108:111]\n\t" \
    "v_mfma_f32_16x16x32_bf16 v[104:107], v[64:67], a[56:59], v[104:107]\n\t" "v_cvt_pk_bf16_f32 v118, v92, v93\n\t" \
    "v_mfma_f32_16x16x32_bf16 v[108:111], v[68:71], a[56:59], v[108:111]\n\t" "v_cvt_pk_bf16_f32 v119, v94, v95\n\t" \
    "v_mfma_f32_16x16x32_bf16 v[104:107], v[72:75], a[60:63], v[104:107]\n\t" "buffer_store_dwordx4 v[116:119], %[z2], %[rs], s63 offen sc1\n\t" \
    "v_mfma_f32_16x16x32_bf16 v[108:111], v[76:79], a[60:63], v[108:111]\n\t" "s_add_u32 s63, s63, 0x20000\n\t" "v_mfma_f32_16x16x32_bf16 v[80:83], v[48:51], a[64:67], 0\n\t" \
    "s_add_u32 s65, s60, 1\n\t" "s_lshr_b32 s66, s65, 2\n\t" "v_mfma_f32_16x16x32_bf16 v[84:87], v[52:55], a[64:67], 0\n\t" "s_xor_b32 s66, s66, %[xp]\n\t" \
    "s_and_b32 s66, s66, 1\n\t" "v_mfma_f32_16x16x32_bf16 v[80:83], v[56:59], a[68:71], v[80:83]\n\t" "v_mfma_f32_16x16x32_bf16 v[84:87], v[60:63], a[68:71], v[84:87]\n\t" \
    "v_mfma_f32_16x16x32_bf16 v[80:83], v[64:67], a[72:75], v[80:83]\n\t" "v_cvt_pk_bf16_f32 v112, v96, v97\n\t" \
    "v_mfma_f32_16x16x32_bf16 v[84:87], v[68:71], a[72:75], v[84:87]\n\t" "v_cvt_pk_bf16_f32 v113, v98, v99\n\t" \
    "v_mfma_f32_16x16x32_bf16 v[80:83], v[72:75], a[76:79], v[80:83]\n\t" "v_cvt_pk_bf16_f32 v114, v100, v101\n\t" \
    "v_mfma_f32_16x16x32_bf16 v[84:87], v[76:79], a[76:79], v[84:87]\n\t" "v_cvt_pk_bf16_f32 v115, v102, v103\n\t" \
    "v_mfma_f32_16x16x32_bf16 v[88:91], v[48:51], a[80:83], 0\n\t" "v_cvt_pk_bf16_f32 v116, v104, v105\n\t" "v_mfma_f32_16x16x32_bf16 v[92:95], v[52:55], a[80:83], 0\n\t" \
    "v_cvt_pk_bf16_f32 v117, v106, v107\n\t" "v_mfma_f32_16x16x32_bf16 v[88:91], v[56:59], a[84:87], v[88:91]\n\t" \
    "buffer_store_dwordx4 v[112:115], %[z1], %[rs], s63 offen sc1\n\t" "v_mfma_f32_16x16x32_bf16 v[92:95], v[60:63], a[84:87], v[92:95]\n\t" \
    "v_mfma_f32_16x16x32_bf16 v[88:91], v[64:67], a[88:91], v[88:91]\n\t" "v_cvt_pk_bf16_f32 v118, v108, v109\n\t" \
    "v_mfma_f32_16x16x32_bf16 v[92:95], v[68:71], a[88:91], v[92:95]\n\t" "v_cvt_pk_bf16_f32 v119, v110, v111\n\t" \
    "v_mfma_f32_16x16x32_bf16 v[88:91], v[72:75], a[92:95], v[88:91]\n\t" "buffer_store_dwordx4 v[116:119], %[z2], %[rs], s63 offen sc1\n\t" \
    "v_mfma_f32_16x16x32_bf16 v[92:95], v[76:79], a[92:95], v[92:95]\n\t" "s_add_u32 s63, s63, 0x20000\n\t" "v_mfma_f32_16x16x32_bf16 v[96:99], v[48:51], a[96:99], 0\n\t" \
    "s_lshl_b32 s66, s66, 13\n\t" "s_lshl_b32 s64, s65, 19\n\t" "v_mfma_f32_16x16x32_bf16 v[100:103], v[52:55], a[96:99], 0\n\t" "s_add_u32 s64, s64, s66\n\t" \
    "s_add_u32 s64, s64, %[vb]\n\t" "v_mfma_f32_16x16x32_bf16 v[96:99], v[56:59], a[100:103], v[96:99]\n\t" \
    "v_mfma_f32_16x16x32_bf16 v[100:103], v[60:63], a[100:103], v[100:103]\n\t" "v_mfma_f32_16x16x32_bf16 v[96:99], v[64:67], a[104:107], v[96:99]\n\t" \
    "v_cvt_pk_bf16_f32 v112, v80, v81\n\t" "v_mfma_f32_16x16x32_bf16 v[100:103], v[68:71], a[104:107], v[100:103]\n\t" "v_cvt_pk_bf16_f32 v113, v82, v83\n\t" \
    "v_mfma_f32_16x16x32_bf16 v[96:99], v[72:75], a[108:111], v[96:99]\n\t" "v_cvt_pk_bf16_f32 v114, v84, v85\n\t" \
    "v_mfma_f32_16x16x32_bf16 v[100:103], v[76:79], a[108:111], v[100:103]\n\t" "v_cvt_pk_bf16_f32 v115, v86, v87\n\t" \
    "v_mfma_f32_16x16x32_bf16 v[104:107], v[48:51], a[112:115], 0\n\t" "v_cvt_pk_bf16_f32 v116, v88, v89\n\t" \
    "v_mfma_f32_16x16x32_bf16 v[108:111], v[52:55], a[112:115], 0\n\t" "v_cvt_pk_bf16_f32 v117, v90, v91\n\t" \
    "v_mfma_f32_16x16x32_bf16 v[104:107], v[56:59], a[116:119], v[104:107]\n\t" "buffer_store_dwordx4 v[112:115], %[z1], %[rs], s63 offen sc1\n\t" \
    "v_mfma_f32_16x16x32_bf16 v[108:111], v[60:63], a[116:119], v[108:111]\n\t" "v_mfma_f32_16x16x32_bf16 v[104:107], v[64:67], a[120:123], v[104:107]\n\t" \
    "v_cvt_pk_bf16_f32 v118, v92, v93\n\t" "v_mfma_f32_16x16x32_bf16 v[108:111], v[68:71], a[120:123], v[108:111]\n\t" "v_cvt_pk_bf16_f32 v119, v94, v95\n\t" \
    "v_mfma_f32_16x16x32_bf16 v[104:107], v[72:75], a[124:127], v[104:107]\n\t" "buffer_store_dwordx4 v[116:119], %[z2], %[rs], s63 offen sc1\n\t" \
    "v_mfma_f32_16x16x32_bf16 v[108:111], v[76:79], a[124:127], v[108:111]\n\t" "s_add_u32 s63, s63, 0x20000\n\t" \
    "v_mfma_f32_16x16x32_bf16 v[80:83], v[48:51], a[128:131], 0\n\t" "s_waitcnt vmcnt(6)\n\t" "v_readfirstlane_b32 s66, v120\n\t" "v_readfirstlane_b32 s67, v121\n\t" \
    "v_mfma_f32_16x16x32_bf16 v[84:87], v[52:55], a[128:131], 0\n\t" "v_mov_b32 v122, s60\n\t" "buffer_store_dword v122, %[fo], %[rc], %[fz] offen sc1\n\t" \
    "v_mfma_f32_16x16x32_bf16 v[80:83], v[56:59], a[132:135], v[80:83]\n\t" "s_max_u32 s61, s61, s66\n\t" "s_max_u32 s62, s62, s67\n\t" "s_cmp_gt_u32 s61, s65\n\t" \
    "v_mfma_f32_16x16x32_bf16 v[84:87], v[60:63], a[132:135], v[84:87]\n\t" "s_cselect_b32 s65, 1, 0\n\t" "s_cselect_b32 s64, s64, 0x7f000000\n\t" \
    "v_mfma_f32_16x16x32_bf16 v[80:83], v[64:67], a[136:139], v[80:83]\n\t" "buffer_load_dwordx4 v[16:19], %[ao], %[rs], s64 offen offset:0 sc1\n\t" \
    "v_cvt_pk_bf16_f32 v112, v96, v97\n\t" "v_mfma_f32_16x16x32_bf16 v[84:87], v[68:71], a[136:139], v[84:87]\n\t" "v_cvt_pk_bf16_f32 v113, v98, v99\n\t" \
    "v_mfma_f32_16x16x32_bf16 v[80:83], v[72:75], a[140:143], v[80:83]\n\t" "buffer_load_dwordx4 v[20:23], %[ao], %[rs], s64 offen offset:256 sc1\n\t" \
    "v_cvt_pk_bf16_f32 v114, v100, v101\n\t" "v_mfma_f32_16x16x32_bf16 v[84:87], v[76:79], a[140:143], v[84:87]\n\t" "v_cvt_pk_bf16_f32 v115, v102, v103\n\t" \
    "v_mfma_f32_16x16x32_bf16 v[88:91], v[48:51], a[144:147], 0\n\t" "buffer_load_dwordx4 v[24:27], %[ao], %[rs], s64 offen offset:2048 sc1\n\t" \
    "v_cvt_pk_bf16_f32 v116, v104, v105\n\t" "v_mfma_f32_16x16x32_bf16 v[92:95], v[52:55], a[144:147], 0\n\t" "v_cvt_pk_bf16_f32 v117, v106, v107\n\t" \
    "v_mfma_f32_16x16x32_bf16 v[88:91], v[56:59], a[148:151], v[88:91]\n\t" "buffer_load_dwordx4 v[28:31], %[ao], %[rs], s64 offen offset:2304 sc1\n\t" \
    "v_mfma_f32_16x16x32_bf16 v[92:95], v[60:63], a[148:151], v[92:95]\n\t" "buffer_store_dwordx4 v[112:115], %[z1], %[rs], s63 offen sc1\n\t" \
    "v_mfma_f32_16x16x32_bf16 v[88:91], v[64:67], a[152:155], v[88:91]\n\t" "buffer_load_dwordx4 v[32:35], %[a1], %[rs], s64 offen offset:0 sc1\n\t" \
    "v_cvt_pk_bf16_f32 v118, v108, v109\n\t" "v_mfma_f32_16x16x32_bf16 v[92:95], v[68:71], a[152:155], v[92:95]\n\t" "v_cvt_pk_bf16_f32 v119, v110, v111\n\t" \
    "v_mfma_f32_16x16x32_bf16 v[88:91], v[72:75], a[156:159], v[88:91]\n\t" "buffer_load_dwordx4 v[36:39], %[a1], %[rs], s64 offen offset:256 sc1\n\t" \
    "buffer_store_dwordx4 v[116:119], %[z2], %[rs], s63 offen sc1\n\t" "v_mfma_f32_16x16x32_bf16 v[92:95], v[76:79], a[156:159], v[92:95]\n\t" \
    "s_add_u32 s63, s63, 0x20000\n\t" "v_mfma_f32_16x16x32_bf16 v[96:99], v[48:51], a[160:163], 0\n\t" \
    "buffer_load_dwordx4 v[40:43], %[a1], %[rs], s64 offen offset:2048 sc1\n\t" "v_mfma_f32_16x16x32_bf16 v[100:103], v[52:55], a[160:163], 0\n\t" \
    "v_mfma_f32_16x16x32_bf16 v[96:99], v[56:59], a[164:167], v[96:99]\n\t" "buffer_load_dwordx4 v[44:47], %[a1], %[rs], s64 offen offset:2304 sc1\n\t" \
    "v_mfma_f32_16x16x32_bf16 v[100:103], v[60:63], a[164:167], v[100:103]\n\t" "v_mfma_f32_16x16x32_bf16 v[96:99], v[64:67], a[168:171], v[96:99]\n\t" \
    "v_cvt_pk_bf16_f32 v112, v80, v81\n\t" "v_mfma_f32_16x16x32_bf16 v[100:103], v[68:71], a[168:171], v[100:103]\n\t" "v_cvt_pk_bf16_f32 v113, v82, v83\n\t" \
    "v_mfma_f32_16x16x32_bf16 v[96:99], v[72:75], a[172:175], v[96:99]\n\t" "v_cvt_pk_bf16_f32 v114, v84, v85\n\t" \
    "v_mfma_f32_16x16x32_bf16 v[100:103], v[76:79], a[172:175], v[100:103]\n\t" "v_cvt_pk_bf16_f32 v115, v86, v87\n\t" \
    "v_mfma_f32_16x16x32_bf16 v[104:107], v[48:51], a[176:179], 0\n\t" "v_cvt_pk_bf16_f32 v116, v88, v89\n\t" \
    "v_mfma_f32_16x16x32_bf16 v[108:111], v[52:55], a[176:179], 0\n\t" "v_cvt_pk_bf16_f32 v117, v90, v91\n\t" \
    "v_mfma_f32_16x16x32_bf16 v[104:107], v[56:59], a[180:183], v[104:107]\n\t" "buffer_store_dwordx4 v[112:115], %[z1], %[rs], s63 offen sc1\n\t" \
    "v_mfma_f32_16x16x32_bf16 v[108:111], v[60:63], a[180:183], v[108:111]\n\t" "v_mfma_f32_16x16x32_bf16 v[104:107], v[64:67], a[184:187], v[104:107]\n\t" \
    "v_cvt_pk_bf16_f32 v118, v92, v93\n\t" "v_mfma_f32_16x16x32_bf16 v[108:111], v[68:71], a[184:187], v[108:111]\n\t" "v_cvt_pk_bf16_f32 v119, v94, v95\n\t" \
    "v_mfma_f32_16x16x32_bf16 v[104:107], v[72:75], a[188:191], v[104:107]\n\t" "buffer_store_dwordx4 v[116:119], %[z2], %[rs], s63 offen sc1\n\t" \
    "v_mfma_f32_16x16x32_bf16 v[108:111], v[76:79], a[188:191], v[108:111]\n\t" "s_add_u32 s63, s63, 0x20000\n\t" \
    "v_mfma_f32_16x16x32_bf16 v[80:83], v[48:51], a[192:195], 0\n\t" "v_mfma_f32_16x16x32_bf16 v[84:87], v[52:55], a[192:195], 0\n\t" \
    "v_mfma_f32_16x16x32_bf16 v[80:83], v[56:59], a[196:199], v[80:83]\n\t" "v_mfma_f32_16x16x32_bf16 v[84:87], v[60:63], a[196:199], v[84:87]\n\t" \
    "v_mfma_f32_16x16x32_bf16 v[80:83], v[64:67], a[200:203], v[80:83]\n\t" "v_cvt_pk_bf16_f32 v112, v96, v97\n\t" \
    "v_mfma_f32_16x16x32_bf16 v[84:87], v[68:71], a[200:203], v[84:87]\n\t" "v_cvt_pk_bf16_f32 v113, v98, v99\n\t" \
    "v_mfma_f32_16x16x32_bf16 v[80:83], v[72:75], a[204:207], v[80:83]\n\t" "v_cvt_pk_bf16_f32 v114, v100, v101\n\t" \
    "v_mfma_f32_16x16x32_bf16 v[84:87], v[76:79], a[204:207], v[84:87]\n\t" "v_cvt_pk_bf16_f32 v115, v102, v103\n\t" \
    "v_mfma_f32_16x16x32_bf16 v[88:91], v[48:51], a[208:211], 0\n\t" "v_cvt_pk_bf16_f32 v116, v104, v105\n\t" "v_mfma_f32_16x16x32_bf16 v[92:95], v[52:55], a[208:211], 0\n\t" \
    "v_cvt_pk_bf16_f32 v117, v106, v107\n\t" "v_mfma_f32_16x16x32_bf16 v[88:91], v[56:59], a[212:215], v[88:91]\n\t" \
    "buffer_store_dwordx4 v[112:115], %[z1], %[rs], s63 offen sc1\n\t" "v_mfma_f32_16x16x32_bf16 v[92:95], v[60:63], a[212:215], v[92:95]\n\t" \
    "v_mfma_f32_16x16x32_bf16 v[88:91], v[64:67], a[216:219], v[88:91]\n\t" "v_cvt_pk_bf16_f32 v118, v108, v109\n\t" \
    "v_mfma_f32_16x16x32_bf16 v[92:95], v[68:71], a[216:219], v[92:95]\n\t" "v_cvt_pk_bf16_f32 v119, v110, v111\n\t" \
    "v_mfma_f32_16x16x32_bf16 v[88:91], v[72:75], a[220:223], v[88:91]\n\t" "buffer_store_dwordx4 v[116:119], %[z2], %[rs], s63 offen sc1\n\t" \
    "v_mfma_f32_16x16x32_bf16 v[92:95], v[76:79], a[220:223], v[92:95]\n\t" "s_add_u32 s63, s63, 0x20000\n\t" "v_mfma_f32_16x16x32_bf16 v[96:99], v[48:51], a[224:227], 0\n\t" \
    "v_mfma_f32_16x16x32_bf16 v[100:103], v[52:55], a[224:227], 0\n\t" "v_mfma_f32_16x16x32_bf16 v[96:99], v[56:59], a[228:231], v[96:99]\n\t" \
    "v_mfma_f32_16x16x32_bf16 v[100:103], v[60:63], a[228:231], v[100:103]\n\t" "v_mfma_f32_16x16x32_bf16 v[96:99], v[64:67], a[232:235], v[96:99]\n\t" \
    "v_cvt_pk_bf16_f32 v112, v80, v81\n\t" "v_mfma_f32_16x16x32_bf16 v[100:103], v[68:71], a[232:235], v[100:103]\n\t" "v_cvt_pk_bf16_f32 v113, v82, v83\n\t" \
    "v_mfma_f32_16x16x32_bf16 v[96:99], v[72:75], a[236:239], v[96:99]\n\t" "v_cvt_pk_bf16_f32 v114, v84, v85\n\t" \
    "v_mfma_f32_16x16x32_bf16 v[100:103], v[76:79], a[236:239], v[100:103]\n\t" "v_cvt_pk_bf16_f32 v115, v86, v87\n\t" \
    "v_mfma_f32_16x16x32_bf16 v[104:107], v[48:51], a[240:243], 0\n\t" "v_cvt_pk_bf16_f32 v116, v88, v89\n\t" \
    "v_mfma_f32_16x16x32_bf16 v[108:111], v[52:55], a[240:243], 0\n\t" "v_cvt_pk_bf16_f32 v117, v90, v91\n\t" \
    "v_mfma_f32_16x16x32_bf16 v[104:107], v[56:59], a[244:247], v[104:107]\n\t" "buffer_store_dwordx4 v[112:115], %[z1], %[rs], s63 offen sc1\n\t" \
    "v_mfma_f32_16x16x32_bf16 v[108:111], v[60:63], a[244:247], v[108:111]\n\t" "v_mfma_f32_16x16x32_bf16 v[104:107], v[64:67], a[248:251], v[104:107]\n\t" \
    "v_cvt_pk_bf16_f32 v118, v92, v93\n\t" "v_mfma_f32_16x16x32_bf16 v[108:111], v[68:71], a[248:251], v[108:111]\n\t" "v_cvt_pk_bf16_f32 v119, v94, v95\n\t" \
    "v_mfma_f32_16x16x32_bf16 v[104:107], v[72:75], a[252:255], v[104:107]\n\t" "buffer_store_dwordx4 v[116:119], %[z2], %[rs], s63 offen sc1\n\t" \
    "s_add_u32 s63, s63, 0x20000\n\t" "s_add_u32 s60, s60, 1\n\t" "v_mfma_f32_16x16x32_bf16 v[108:111], v[76:79], a[252:255], v[108:111]\n\t" "s_cmp_eq_u32 s65, 0\n\t" \
    "s_waitcnt vmcnt(6)\n\t" ZSUM "s_cbranch_scc0 33b\n\t" "s_branch 30f\n\t" "20:\n\t" "s_nop 7\n\t" "v_cvt_pk_bf16_f32 v112, v96, v97\n\t" \
    "v_cvt_pk_bf16_f32 v113, v98, v99\n\t" "v_cvt_pk_bf16_f32 v114, v100, v101\n\t" "v_cvt_pk_bf16_f32 v115, v102, v103\n\t" "v_cvt_pk_bf16_f32 v116, v104, v105\n\t" \
    "v_cvt_pk_bf16_f32 v117, v106, v107\n\t" "v_cvt_pk_bf16_f32 v118, v108, v109\n\t" "v_cvt_pk_bf16_f32 v119, v110, v111\n\t" \
    "buffer_store_dwordx4 v[112:115], %[z1], %[rs], s63 offen sc1\n\t" "buffer_store_dwordx4 v[116:119], %[z2], %[rs], s63 offen sc1\n\t" \
    "buffer_load_dword v120, %[fo], %[rc], %[fv] offen sc1\n\t" "s_waitcnt vmcnt(0)\n\t" "v_readfirstlane_b32 s61, v120\n\t" "s_lshr_b32 s66, s60, 2\n\t" \
    "s_xor_b32 s66, s66, %[xp]\n\t" "s_and_b32 s66, s66, 1\n\t" "s_lshl_b32 s66, s66, 13\n\t" "s_lshl_b32 s64, s60, 19\n\t" "s_add_u32 s64, s64, s66\n\t" \
    "s_add_u32 s64, s64, %[vb]\n\t" "s_cmp_gt_u32 s61, s60\n\t" "s_cbranch_scc0 24f\n\t" "s_cmp_eq_u32 s60, 64\n\t" "s_cbranch_scc1 24f\n\t" \
    "buffer_load_dwordx4 v[48:51], %[ao], %[rs], s64 offen offset:0 sc1\n\t" "buffer_load_dwordx4 v[52:55], %[ao], %[rs], s64 offen offset:256 sc1\n\t" \
    "buffer_load_dwordx4 v[56:59], %[ao], %[rs], s64 offen offset:2048 sc1\n\t" "buffer_load_dwordx4 v[60:63], %[ao], %[rs], s64 offen offset:2304 sc1\n\t" \
    "buffer_load_dwordx4 v[64:67], %[a1], %[rs], s64 offen offset:0 sc1\n\t" "buffer_load_dwordx4 v[68:71], %[a1], %[rs], s64 offen offset:256 sc1\n\t" \
    "buffer_load_dwordx4 v[72:75], %[a1], %[rs], s64 offen offset:2048 sc1\n\t" "buffer_load_dwordx4 v[76:79], %[a1], %[rs], s64 offen offset:2304 sc1\n\t" \
    "v_mov_b32 v122, s60\n\t" "buffer_store_dword v122, %[fo], %[rc], %[fz] offen sc1\n\t" "s_waitcnt vmcnt(1)\n\t" "s_branch 23b\n\t" "24:\n\t" "v_mov_b32 v122, s60\n\t" \
    "buffer_store_dword v122, %[fo], %[rc], %[fz] offen sc1\n\t" "s_cmp_eq_u32 s60, 64\n\t" "s_cbranch_scc1 90f\n\t" "s_add_u32 s67, s60, 1\n\t" "s_cmp_ge_u32 s61, s67\n\t" \
    "s_cbranch_scc1 22f\n\t" "21:\n\t" "buffer_load_dword v120, %[fo], %[rc], %[fv] offen sc1\n\t" "buffer_load_dword v121, %[fo], %[rc], %[vd] offen sc1\n\t" \
    "s_waitcnt vmcnt(0)\n\t" "v_readfirstlane_b32 s61, v120\n\t" "v_readfirstlane_b32 s66, v121\n\t" "s_nop 3\n\t" "s_cmp_ge_u32 s61, s67\n\t" "s_cbranch_scc1 22f\n\t" \
    "s_bitcmp1_b32 s66, 1\n\t" "s_cbranch_scc1 90f\n\t" "s_cmp_eq_u32 %[bd], 0\n\t" "s_cbranch_scc1 22f\n\t" "s_sub_u32 %[bd], %[bd], 1\n\t" "s_sleep 2\n\t" \
    "s_branch 21b\n\t" "22:\n\t" "s_lshr_b32 s66, s60, 2\n\t" "s_xor_b32 s66, s66, %[xp]\n\t" "s_and_b32 s66, s66, 1\n\t" "s_lshl_b32 s66, s66, 13\n\t" \
    "s_lshl_b32 s64, s60, 19\n\t" "s_add_u32 s64, s64, s66\n\t" "s_add_u32 s64, s64, %[vb]\n\t" "buffer_load_dwordx4 v[48:51], %[ao], %[rs], s64 offen offset:0 sc1\n\t" \
    "buffer_load_dwordx4 v[52:55], %[ao], %[rs], s64 offen offset:256 sc1\n\t" "buffer_load_dwordx4 v[56:59], %[ao], %[rs], s64 offen offset:2048 sc1\n\t" \
    "buffer_load_dwordx4 v[60:63], %[ao], %[rs], s64 offen offset:2304 sc1\n\t" "buffer_load_dwordx4 v[64:67], %[a1], %[rs], s64 offen offset:0 sc1\n\t" \
    "buffer_load_dwordx4 v[68:71], %[a1], %[rs], s64 offen offset:256 sc1\n\t" "buffer_load_dwordx4 v[72:75], %[a1], %[rs], s64 offen offset:2048 sc1\n\t" \
    "buffer_load_dwordx4 v[76:79], %[a1], %[rs], s64 offen offset:2304 sc1\n\t" "s_waitcnt vmcnt(0)\n\t" "s_branch 23b\n\t" "60:\n\t" "s_sub_u32 s67, s60, %[rg]\n\t" \
    "s_cmp_ge_u32 s62, s67\n\t" "s_cbranch_scc1 62f\n\t" "61:\n\t" "buffer_load_dword v120, %[fo], %[rc], %[fs] offen sc1\n\t" \
    "buffer_load_dword v121, %[fo], %[rc], %[vd] offen sc1\n\t" "s_waitcnt vmcnt(0)\n\t" "v_readfirstlane_b32 s62, v120\n\t" "v_readfirstlane_b32 s66, v121\n\t" "s_nop 3\n\t" \
    "s_cmp_ge_u32 s62, s67\n\t" "s_cbranch_scc1 62f\n\t" "s_bitcmp1_b32 s66, 1\n\t" "s_cbranch_scc1 90f\n\t" "s_cmp_eq_u32 %[bd], 0\n\t" "s_cbranch_scc1 62f\n\t" \
    "s_sub_u32 %[bd], %[bd], 1\n\t" "s_sleep 2\n\t" "s_branch 61b\n\t" "62:\n\t" "s_branch 63b\n\t" "30:\n\t" "s_nop 7\n\t" "v_cvt_pk_bf16_f32 v112, v96, v97\n\t" \
    "v_cvt_pk_bf16_f32 v113, v98, v99\n\t" "v_cvt_pk_bf16_f32 v114, v100, v101\n\t" "v_cvt_pk_bf16_f32 v115, v102, v103\n\t" "v_cvt_pk_bf16_f32 v116, v104, v105\n\t" \
    "v_cvt_pk_bf16_f32 v117, v106, v107\n\t" "v_cvt_pk_bf16_f32 v118, v108, v109\n\t" "v_cvt_pk_bf16_f32 v119, v110, v111\n\t" \
    "buffer_store_dwordx4 v[112:115], %[z1], %[rs], s63 offen sc1\n\t" "buffer_store_dwordx4 v[116:119], %[z2], %[rs], s63 offen sc1\n\t" \
    "buffer_load_dword v120, %[fo], %[rc], %[fv] offen sc1\n\t" "s_waitcnt vmcnt(0)\n\t" "v_readfirstlane_b32 s61, v120\n\t" "s_lshr_b32 s66, s60, 2\n\t" \
    "s_xor_b32 s66, s66, %[xp]\n\t" "s_and_b32 s66, s66, 1\n\t" "s_lshl_b32 s66, s66, 13\n\t" "s_lshl_b32 s64, s60, 19\n\t" "s_add_u32 s64, s64, s66\n\t" \
    "s_add_u32 s64, s64, %[vb]\n\t" "s_cmp_gt_u32 s61, s60\n\t" "s_cbranch_scc0 34f\n\t" "s_cmp_eq_u32 s60, 64\n\t" "s_cbranch_scc1 34f\n\t" \
    "buffer_load_dwordx4 v[16:19], %[ao], %[rs], s64 offen offset:0 sc1\n\t" "buffer_load_dwordx4 v[20:23], %[ao], %[rs], s64 offen offset:256 sc1\n\t" \
    "buffer_load_dwordx4 v[24:27], %[ao], %[rs], s64 offen offset:2048 sc1\n\t" "buffer_load_dwordx4 v[28:31], %[ao], %[rs], s64 offen offset:2304 sc1\n\t" \
    "buffer_load_dwordx4 v[32:35], %[a1], %[rs], s64 offen offset:0 sc1\n\t" "buffer_load_dwordx4 v[36:39], %[a1], %[rs], s64 offen offset:256 sc1\n\t" \
    "buffer_load_dwordx4 v[40:43], %[a1], %[rs], s64 offen offset:2048 sc1\n\t" "buffer_load_dwordx4 v[44:47], %[a1], %[rs], s64 offen offset:2304 sc1\n\t" \
    "v_mov_b32 v122, s60\n\t" "buffer_store_dword v122, %[fo], %[rc], %[fz] offen sc1\n\t" "s_waitcnt vmcnt(1)\n\t" "s_branch 33b\n\t" "34:\n\t" "v_mov_b32 v122, s60\n\t" \
    "buffer_store_dword v122, %[fo], %[rc], %[fz] offen sc1\n\t" "s_cmp_eq_u32 s60, 64\n\t" "s_cbranch_scc1 90f\n\t" "s_add_u32 s67, s60, 1\n\t" "s_cmp_ge_u32 s61, s67\n\t" \
    "s_cbranch_scc1 32f\n\t" "31:\n\t" "buffer_load_dword v120, %[fo], %[rc], %[fv] offen sc1\n\t" "buffer_load_dword v121, %[fo], %[rc], %[vd] offen sc1\n\t" \
    "s_waitcnt vmcnt(0)\n\t" "v_readfirstlane_b32 s61, v120\n\t" "v_readfirstlane_b32 s66, v121\n\t" "s_nop 3\n\t" "s_cmp_ge_u32 s61, s67\n\t" "s_cbranch_scc1 32f\n\t" \
    "s_bitcmp1_b32 s66, 1\n\t" "s_cbranch_scc1 90f\n\t" "s_cmp_eq_u32 %[bd], 0\n\t" "s_cbranch_scc1 32f\n\t" "s_sub_u32 %[bd], %[bd], 1\n\t" "s_sleep 2\n\t" \
    "s_branch 31b\n\t" "32:\n\t" "s_lshr_b32 s66, s60, 2\n\t" "s_xor_b32 s66, s66, %[xp]\n\t" "s_and_b32 s66, s66, 1\n\t" "s_lshl_b32 s66, s66, 13\n\t" \
    "s_lshl_b32 s64, s60, 19\n\t" "s_add_u32 s64, s64, s66\n\t" "s_add_u32 s64, s64, %[vb]\n\t" "buffer_load_dwordx4 v[16:19], %[ao], %[rs], s64 offen offset:0 sc1\n\t" \
    "buffer_load_dwordx4 v[20:23], %[ao], %[rs], s64 offen offset:256 sc1\n\t" "buffer_load_dwordx4 v[24:27], %[ao], %[rs], s64 offen offset:2048 sc1\n\t" \
    "buffer_load_dwordx4 v[28:31], %[ao], %[rs], s64 offen offset:2304 sc1\n\t" "buffer_load_dwordx4 v[32:35], %[a1], %[rs], s64 offen offset:0 sc1\n\t" \
    "buffer_load_dwordx4 v[36:39], %[a1], %[rs], s64 offen offset:256 sc1\n\t" "buffer_load_dwordx4 v[40:43], %[a1], %[rs], s64 offen offset:2048 sc1\n\t" \
    "buffer_load_dwordx4 v[44:47], %[a1], %[rs], s64 offen offset:2304 sc1\n\t" "s_waitcnt vmcnt(0)\n\t" "s_branch 33b\n\t" "70:\n\t" "s_sub_u32 s67, s60, %[rg]\n\t" \
    "s_cmp_ge_u32 s62, s67\n\t" "s_cbranch_scc1 72f\n\t" "71:\n\t" "buffer_load_dword v120, %[fo], %[rc], %[fs] offen sc1\n\t" \
    "buffer_load_dword v121, %[fo], %[rc], %[vd] offen sc1\n\t" "s_waitcnt vmcnt(0)\n\t" "v_readfirstlane_b32 s62, v120\n\t" "v_readfirstlane_b32 s66, v121\n\t" "s_nop 3\n\t" \
    "s_cmp_ge_u32 s62, s67\n\t" "s_cbranch_scc1 72f\n\t" "s_bitcmp1_b32 s66, 1\n\t" "s_cbranch_scc1 90f\n\t" "s_cmp_eq_u32 %[bd], 0\n\t" "s_cbranch_scc1 72f\n\t" \
    "s_sub_u32 %[bd], %[bd], 1\n\t" "s_sleep 2\n\t" "s_branch 71b\n\t" "72:\n\t" "s_branch 73b\n\t" "90:\n\t"

#define T_DQ 1380
#define T_DV 1290
#define T_RQ 670
#define T_RV 640
#define T_M1 100
#define T_DY 570
#define T_DX 1240
#define T_MIN 35
#define T_PEN 280
#define T_SAFE 80
#define T_RETRY 700
#define T_POLL 1
#define T_KS 0
#define T_TS 0
#define T_CLR 210
#define T_VLAG 150
#define T_U0 340
constexpr int T_OVH = 118, T_NEAR = 460;
constexpr unsigned NS(int ns) { return (unsigned)(ns * 64 / 10); }
struct Clk { unsigned cyc, g, pc, pg, k1, k2; };
DEV void mark(Clk& k) {
  unsigned long long c, r[8];
  asm volatile("s_memtime %0\n\ts_memrealtime %1\n\ts_nop 2\n\ts_memrealtime %2\n\ts_nop 2\n\ts_memrealtime %3\n\ts_nop 2\n\ts_memrealtime %4\n\ts_nop 2\n\t"
               "s_memrealtime %5\n\ts_nop 2\n\ts_memrealtime %6\n\ts_nop 2\n\ts_memrealtime %7\n\ts_waitcnt lgkmcnt(0)"
               : "=s"(c), "=s"(r[0]), "=s"(r[1]), "=s"(r[2]), "=s"(r[3]), "=s"(r[4]), "=s"(r[5]), "=s"(r[6]) :: "memory");
  const unsigned a = (unsigned)r[0];
  unsigned n = 0;
#pragma unroll
  for (int i = 1; i < 7; ++i) asm("s_cmp_eq_u32 %1, %2\n\ts_addc_u32 %0, %0, 0" : "+s"(n) : "s"((unsigned)r[i]), "s"(a) : "scc");
  k.g = (a + 4u) << 6; k.cyc = (unsigned)c + 12u + 16u * n;
}
DEV void anchor(Clk& k, bool any = false) {
  mark(k);
  const unsigned dg_ = k.g - k.pg;
  const float dc = (float)(k.cyc - k.pc), dg = (float)dg_;
  unsigned k1, k2;
  asm volatile("s_nop 0\n\tv_readfirstlane_b32 %0, %2\n\tv_readfirstlane_b32 %1, %3\n\ts_nop 3\n\ts_min_u32 %0, %0, 0x80000\n\ts_min_u32 %1, %1, 0x8000"
               : "=&s"(k1), "=&s"(k2) : "v"((unsigned)(dg * __builtin_amdgcn_rcpf(dc) * 65536.f)), "v"((unsigned)(dc * __builtin_amdgcn_rcpf(dg) * 65536.f)) : "scc");
  if (any) { k.k1 = k1; k.k2 = k2; }
  else asm("s_cmp_lt_u32 %4, %5\n\ts_cselect_b32 %2, %2, %0\n\ts_cselect_b32 %3, %3, %1\n\ts_add_u32 %0, %0, %2\n\ts_lshr_b32 %0, %0, 1\n\ts_add_u32 %1, %1, %3\n\ts_lshr_b32 %1, %1, 1"
           : "+s"(k.k1), "+s"(k.k2), "+s"(k1), "+s"(k2) : "s"(dg_ - NS(1400)), "n"(NS(800)) : "scc");
  k.pc = k.cyc; k.pg = k.g;
}
DEV unsigned to_cyc(const Clk& k, unsigned g) {
  unsigned d;
  asm("s_max_i32 %0, %1, 0xffff8000\n\ts_min_i32 %0, %0, 0x8000\n\ts_mul_i32 %0, %0, %2\n\ts_ashr_i32 %0, %0, 16" : "=&s"(d) : "s"(g - k.g), "s"(k.k2) : "scc");
  return k.cyc + d - T_OVH;
}
DEV void wait_cyc(unsigned t) { asm volatile(TIMED_WAIT :: "s"(t) : CL_S); }
DEV void close_in(Clk& k, unsigned t) {
  if ((int)(t - (unsigned)__builtin_amdgcn_s_memtime()) > T_NEAR + 60) { wait_cyc(t - T_NEAR); mark(k); }
}
DEV int slow_line(const u16* q, int h, int j, int lane) { return (j * 32 + (lane >> 1)) * 8192 + (h >> 3) * 2048 + (lane & 1) * 1024 + ((3 - (int)((size_t)q >> 7)) & 7) * 128; }
DEV int turn(int h, int w) { return ((h >> 3) ^ (w & 1)) & 3; }
DEV void hold(const Ctx& c, int ticks) {
#pragma unroll 1
  while ((unsigned)__builtin_amdgcn_s_memrealtime() - c.t0 < (unsigned)ticks) __builtin_amdgcn_s_sleep(4);
}
DEV int z_mix(int y) { return (y >> 4) | ((y & 1) << 2); }
DEV int u_slot(int s, int para) { return R_U + (s & (URING == 64 ? 127 : URING - 1)) * MB + ((para ^ (s >> 1)) & 1) * 8192; }
DEV unsigned ask_stamps(Ctx& c, int par, int slot) { return __builtin_amdgcn_raw_buffer_load_b32(c.rs, amap(24576, par) + fresh_lane() * 128, slot, AUX_SC1); }
DEV unsigned latest(Ctx& c, const Clk& k, int par, int slot, unsigned v) {
  if (__builtin_expect(!allok(v), 0)) {
#pragma unroll 1
    do {
      __builtin_amdgcn_s_sleep(T_POLL);
      asm volatile("" ::: "memory");
      const unsigned t = __builtin_amdgcn_raw_buffer_load_b32(c.rs, v == SENT ? amap(24576, par) + fresh_lane() * 128 : 1 << 30, slot, AUX_SC1);
      v = v == SENT ? t : v;
    } while (!allok(v) && !spend(c));
  }
  int d = (int)(v - k.g);
  d = max(d, __builtin_amdgcn_update_dpp(d, d, 0x128, 0xf, 0xf, true));
  d = max(d, __builtin_amdgcn_update_dpp(d, d, 0x124, 0xf, 0xf, true));
  d = max(d, __builtin_amdgcn_update_dpp(d, d, 0x122, 0xf, 0xf, true));
  d = max(d, __builtin_amdgcn_update_dpp(d, d, 0x121, 0xf, 0xf, true));
  unsigned m;
  asm("s_max_i32 %0, %1, %2\n\ts_max_i32 %0, %0, %3\n\ts_max_i32 %0, %0, %4" : "=&s"(m)
      : "s"(__builtin_amdgcn_readlane(d, 0)), "s"(__builtin_amdgcn_readlane(d, 16)), "s"(__builtin_amdgcn_readlane(d, 32)), "s"(__builtin_amdgcn_readlane(d, 48)) : "scc");
  return k.g + m;
}
struct Plan { unsigned gx, dx; };
DEV unsigned smax(unsigned a, unsigned b) { return (int)(a - b) > 0 ? a : b; }
DEV unsigned pick(int d, unsigned a, unsigned b) { unsigned r; asm("s_cmp_gt_i32 %1, 0\n\ts_cselect_b32 %0, %2, %3" : "=s"(r) : "s"(d), "s"(a), "s"(b) : "scc"); return r; }
DEV unsigned when_p(const Plan& t, unsigned last, unsigned more = 0) {
  const unsigned gy = t.gx + t.dx + NS(T_M1), a = last + NS(T_PEN), b = gy + NS(T_RETRY);
  return pick((int)(last + NS(T_MIN) - gy), pick((int)(a - b), a, b) + more, gy);
}
DEV void advance(Plan& t, unsigned last) {
  const int over = (int)(last - t.gx - t.dx);
  t.gx = when_p(t, last, NS(T_SAFE)) + NS(T_DY);
  t.dx += pick(over - (int)NS(80), NS(4), pick(over, (unsigned)(over >> 1), (unsigned)(over >> 3)));
}
DEV unsigned next_go(Ctx& c, Clk& k, Plan& t, int par, int slot, unsigned v) {
  SCHED();
  anchor(k);
  SCHED();
  advance(t, latest(c, k, par, slot, v));
  return to_cyc(k, t.gx);
}

DEV constexpr int qk_r(int j) { return j < 4 ? 0 : 1 + (j - 4) % 3; }
DEV constexpr int qk_i(int j) { return j < 4 ? j : (j - 4) / 3; }
DEV constexpr int qk_upto(int u) { return K_FIRST && u >= 3 ? (u + 3 < 16 ? u + 3 : 16) : (u + 4 < 16 ? u + 4 : 16); }
DEV void qk_role(const Params& p, Ctx& c, int h) {
  constexpr int WL = 0, MEMS = 131072, KXL = 147456, QKX = 155648, QROW = 272;
  c.budget = SPIN_CAP;
  const int w = c.w;
  Clk k = {0u, 0u, 0u, 0u, 0u, 0u};
  anchor(k, true);
  v8u wa[32], wh[16];
  unsigned look = claim_look(c);
  bool claimed = false;
  hold(c, QK_AT);
  {
    const int lane = fresh_lane(), g = lane >> 4, cc = lane & 15;
    const rsrc_t rq = mkrs(p.Wq, 1024 * 4096 * 2), rk = mkrs(p.Wk, 1024 * 4096 * 2);
    int vo[4];
#pragma unroll
    for (int i = 0; i < 4; ++i) vo[i] = opq((256 * w + 64 * (i ^ turn(h, w)) + krow_lane(g)) * 8192 + (h * 128 + 8 * cc) * 2);
    v4u row[16][8];
    auto request = [&](int u) {
#pragma unroll
      for (int r = 0; r < 8; ++r) row[u][r] = ld16w<W_AUX>((u < 8) != K_FIRST ? rq : rk, vo[(u >> 1) & 3], krow(4 * (u & 1) + (r >> 1), r & 1) * 8192);
    };
#pragma unroll
    for (int u = 0; u < qk_upto(-1); ++u) { request(u); if (u == 0) { SCHED(); ask(c, look); SCHED(); } }
    SCHED();
    const int sw = MEMS + w * 8192 + g * 256, lw = opq(WL + w * 32768 + lane * 16);
    int sr[2];
#pragma unroll
    for (int t = 0; t < 2; ++t) { const int i = (2 * cc + t) & 7; sr[t] = opq(i * 1024 + g * 256 + ((4 * w + (cc >> 2) + 2 * i) & 15) * 16); }
    sfor<16>([&](auto U) __attribute__((always_inline)) {
      constexpr int u = U.value;
#pragma unroll
      for (int i = 0; i < 8; ++i) {
        v4u f;
#pragma unroll
        for (int r = 0; r < 4; ++r) f[r] = prm(row[u][2 * r + 1][i >> 1], row[u][2 * r][i >> 1], i & 1);
        sts16(c.L, sw + i * 1024 + ((cc + 2 * i) & 15) * 16, f);
      }
      SCHED();
      if ((u == 5 || u == 10) && !claimed) claimed = claim_try(c, look);
      if (u == 2 || u == 7) look = claim_look(c);
      if (qk_upto(u) > qk_upto(u - 1)) request(qk_upto(u - 1));
      if (u == 15 && !claimed) claim(c, 0);
      BAR();
      v4u in[4][2];
#pragma unroll
      for (int t = 1; t >= 0; --t)
#pragma unroll
        for (int r = 0; r < 4; ++r) in[r][t] = lds16(c.L, sr[t] + MEMS + ((w + r) & 3) * 8192);
      SCHED();
#pragma unroll
      for (int j = 0; j < 16; ++j) {
        if (qk_i(j) != ((u >> 1) & 3)) continue;
#pragma unroll
        for (int t = 1; t >= 0; --t) {
          const v4u o = in[qk_r(j)][t];
          constexpr bool isq = (u < 8) != K_FIRST;
          if (!isq && t == 1) sts16(c.L, lw + j * 2048 + (u & 1) * 1024, o);
          else {
            v8u& d = isq ? wa[2 * j + t] : wh[j];
#pragma unroll
            for (int r = 0; r < 4; ++r) d[4 * (u & 1) + r] = o[r];
#define M_(r) asm volatile("" : "+" r(d))
            if (u & 1) { if (isq) AT_ACC(M_, 2 * j + t); else AT_HOME(M_, j); }
#undef M_
          }
        }
      }
      BAR();
    });
  }
  STAMP(0);
  BAR();
  const unsigned vote = wait_verdict(c);
  const int par = vote_b(vote), para = vote_a(vote);
  const int lane = fresh_lane(), g = lane >> 4, cc = lane & 15;
  const int moff = opq(amap(w * 4096 + lane * 16, par) + (turn(h, w) & 1) * 1024), m2 = R_MEM + (turn(h, w) & 2) * 1024;
  unsigned tries = 1u << 12;
  *(volatile LAS unsigned*)(c.L + 160512) = 0u;
  LGKM(0);
  const int idx = opq((lane & 1) ? 0xEEEE : 0x4444);
  TICK_DECL;
  const int l0 = opq(WL + w * 32768 + lane * 16), l1 = opq(MEMS + lane * 16);
  const int qw = opq(QKX + (2 * w + (cc >> 3)) * 256 + g * 64 + (cc & 1) * 16 + ((cc >> 1) & 3) * 4), kw = opq(QKX + (8 + 2 * g) * QROW + (32 * w + 2 * cc) * 2);
#define QK_PINS PIN_ACC0(wa); PIN_ACC1(wa); PIN_HOME(wh)
#define QK_IN [l0] "v"(l0), [l1] "v"(l1), [mo] "v"(moff), [ix] "v"(idx), [qw] "v"(qw), [kw] "v"(kw), [rs] "s"(c.rs), [sb] "s"(m2 + (s & 3) * MEM_SLOT)
#define QK_CL CL_S, CL_16_32, CL_160_200, CL_200_216, CL_216_256
  if (w) RAWBAR();
  if (w == 3) {
    const int hq = opq(lane ? 1 << 30 : amap(24576 + h * 128, par));
    anchor(k, true);
    unsigned tg = k.cyc;
    Plan tt = {k.g - NS(100000), NS(T_DX)};
    for (int s = 0; s < NSEG; ++s) {
      const int pb = R_P + (s & 3) * P_SLOT;
      QK_PINS;
      asm volatile(QK_STEP3 : [bd] "+s"(c.budget), [rt] "+s"(tries) : QK_IN, [hq] "v"(hq), [pb] "s"(pb), [tg] "s"(tg), [ca] "s"(k.cyc - T_DQ), [k1] "s"(k.k1), [ga] "s"(k.g), [dr] "n"(T_RQ), [pr] "s"(R_P + ((s + 1) & 3) * P_SLOT) : QK_CL);
      QK_PINS;
      LOG(1, s, tg);
      const unsigned sv = ask_stamps(c, par, pb);
      SCHED();
      anchor(k);
      SCHED();
      advance(tt, latest(c, k, par, pb, sv));
      tg = to_cyc(k, tt.gx);
      RAWBAR();
      LOG(2, s, k.cyc); LOG(3, s, k.g); LOG(4, s, tt.dx);
      st4s(mkrs((const void*)c.ctrl, CTRL_BYTES), hq & (1 << 30), C_FLAG + (F_S + h) * 128, (unsigned)s + 1u);
      TICK_SUM(10);
      close_in(k, tg);
      tg = to_cyc(k, tt.gx);
    }
  } else {
    const int kx0 = opq(KXL + g * 512 + cc * 16), kmrd = opq(QKX + g * 16 + (8 + (cc & 7)) * QROW);
    unsigned seen = NOFLAG;
    if (w == 0) {
      const int prow = (2 * g + (cc & 1)) * 2 + ((cc >> 1) & 1);
      const int px = opq(amap((h >> 1) * 1024 + ((2 * (h & 1) + (cc >> 3)) * 16 + prow) * 16 + ((cc >> 2) & 1) * 8, par));
      const int pm = opq(cc < 8 ? amap(16384 + h * 128 + (2 * g + (cc & 1)) * 16 + (cc & 2) * 4 + (cc & 4), par) : 1 << 30);
      const int ps = opq((cc & 1) ? 0x03020706 : 0x05040100), pa = opq(lane * 128);
      for (int s = 0; s < NSEG; ++s) {
        if (seen < (unsigned)s + 1u) seen = wait_flag(c, F_K + h, s + 1);
        QK_PINS;
        unsigned pd;
        asm volatile(QK_STEP0 : [bd] "+s"(c.budget), [rt] "+s"(tries), [sq] "+s"(seen), [pd] "=&v"(pd)
                     : QK_IN, [kx] "v"(kx0), [km] "v"(kmrd), [px] "v"(px), [pm] "v"(pm), [ps] "v"(ps), [pb] "s"(R_P + (s & 3) * P_SLOT), [pr] "s"(R_P + ((s + 1) & 3) * P_SLOT),
                       [kb] "s"(R_KX - KXL + xblk(s, h, para)), [fq] "s"(c.ctrl + (C_FLAG + (F_K + h) * 128) / 4),
                       [kn] "s"(R_KX + xblk(min(s + 1, NSEG - 1), h, para)), [pa] "v"(pa) : QK_CL);
        QK_PINS;
        LITE(s); LOGS(0, s);
        TICK_SUM(10);
      }
      RAWBAR();
    } else {
      const int qt = w - 1;
      const int px = opq(amap(qt ? 16384 + h * 1024 + lane * 16 : h * 512 + (g >> 1) * 256 + cc * 16 + (g & 1) * 8, para));
      const int pm = opq(g < 2 ? amap(49152 + (qt * 32 + h) * 256 + cc * 16 + g * 8, para) : 1 << 30);
      const int ps = opq(cc - 4 * g), hq = opq(g * 512 + (16 * qt + cc) * 16);
#define QK_TOK(STEP) for (int s = 0; s < NSEG; ++s) { QK_PINS; \
        asm volatile(STEP : [bd] "+s"(c.budget), [rt] "+s"(tries), [sq] "+s"(seen) \
                     : QK_IN, [kx] "v"(kx0), [km] "v"(kmrd), [px] "v"(px), [pm] "v"(pm), [ps] "v"(ps), [hq] "v"(hq), [qb] "s"(R_QX + xblk(s, h, para)), [tb] "s"(R_PT + (s & 7) * PT_SLOT), \
                       [tr] "s"(R_PT + ((s + 2) & 7) * PT_SLOT), [sn] "s"(s + 1), [fq] "s"(c.ctrl + (C_FLAG + (F_Q + h) * 128) / 4) : QK_CL); \
        QK_PINS; TICK_SUM(10); }
      if (w == 1) { QK_TOK(QK_STEP1) } else { QK_TOK(QK_STEP2) }
    }
  }
  TICK_OUT();
  if (c.budget == 0u) __hip_atomic_store(c.ctrl + C_TMO / 4, 1u, RLX_AGENT);
}

DEV void shuffle_cols(Ctx& c, int scratch, const v4u (&rows)[8], v4u (&o)[8]) {
  const int g = c.lane >> 4, cc = c.lane & 15;
#pragma unroll
  for (int i = 0; i < 8; ++i) {
    v4u f;
#pragma unroll
    for (int r = 0; r < 4; ++r) f[r] = prm(rows[2 * r + 1][i >> 1], rows[2 * r][i >> 1], i & 1);
    sts16(c.L, scratch + (8 * cc + i) * 64 + ((g ^ (cc & 3)) << 4), f);
  }
  asm volatile("s_waitcnt lgkmcnt(0)" ::: "memory");
#pragma unroll
  for (int i = 0; i < 8; ++i) { const int col = 16 * i + cc; o[i] = lds16(c.L, scratch + col * 64 + ((g ^ ((col >> 3) & 3)) << 4)); }
  asm volatile("s_waitcnt lgkmcnt(0)" ::: "memory");
}

DEV void vo_role(const Params& p, Ctx& c, int h) {
  constexpr int WL = 0, MEMS = 131072, VM = 147456, VROW = 272;
  c.budget = SPIN_CAP;
  const int w = c.w, lane = c.lane = fresh_lane(), g = lane >> 4, cc = lane & 15;
  Clk k = {0u, 0u, 0u, 0u, 0u, 0u};
  anchor(k, true);
  v8u wa[32], wh[16];
  ask(c, claim_look(c));
  unsigned look = 0;
  bool claimed = false;
  hold(c, VO_AT);
  const rsrc_t rwo = mkrs(p.Wo, 4096 * 1024 * 2);
  const int voo = (h * 128 + 4 * g) * 2048 + (256 * ((cc >> 3) ^ (h >> 3)) + 64 * w + 8 * (cc & 7)) * 2;
  v4u buf[3][8];
  auto request_o = [&](int u) {
#pragma unroll
    for (int r = 0; r < 8; ++r) buf[u % 3][r] = ld16w<W_AUX>(rwo, voo ^ ((u >> 2) * 1024), (32 * (u & 3) + 16 * (r >> 2) + (r & 3)) * 2048);
  };
  {
    const rsrc_t rw = mkrs(p.Wv, 1024 * 4096 * 2);
    const int vo = (256 * w + krow_lane(g)) * 8192 + (h * 128 + 8 * cc) * 2;
    v4u vrow[8][8];
    auto request = [&](int u) {
#pragma unroll
      for (int r = 0; r < 8; ++r) vrow[u][r] = ld16w<W_AUX>(rw, vo, (64 * ((u >> 1) ^ turn(h, w)) + krow(4 * (u & 1) + (r >> 1), r & 1)) * 8192);
    };
#pragma unroll
    for (int u = 0; u < 3; ++u) request(u);
    SCHED();
    const int wr_ = MEMS + w * 8192 + g * 256;
    int sr[2];
#pragma unroll
    for (int t = 0; t < 2; ++t) { const int i = (2 * cc + t) & 7; sr[t] = opq(i * 1024 + g * 256 + ((4 * w + (cc >> 2) + 2 * i) & 15) * 16); }
#pragma unroll
    for (int u = 0; u < 8; ++u) {
#pragma unroll
      for (int i = 0; i < 8; ++i) {
        v4u f;
#pragma unroll
        for (int r = 0; r < 4; ++r) f[r] = prm(vrow[u][2 * r + 1][i >> 1], vrow[u][2 * r][i >> 1], i & 1);
        sts16(c.L, wr_ + i * 1024 + ((cc + 2 * i) & 15) * 16, f);
      }
      SCHED();
      if (u == 7) claimed = claim_try(c, look);
      if (u == 4) look = claim_look(c);
      if (u + 3 < 8) request(u + 3); else request_o(u - 5);
      BAR();
      v4u in[4][2];
#pragma unroll
      for (int t = 1; t >= 0; --t)
#pragma unroll
        for (int r = 0; r < 4; ++r) in[r][t] = lds16(c.L, sr[t] + MEMS + ((w + r) & 3) * 8192);
      SCHED();
#pragma unroll
      for (int j = 0; j < 16; ++j) {
        if (qk_i(j) != (u >> 1)) continue;
#pragma unroll
        for (int t = 1; t >= 0; --t) {
          const v4u o = in[qk_r(j)][t];
          if (t == 1 && (j & 1)) sts16(c.L, WL + (w * 16 + (j >> 1)) * 2048 + (u & 1) * 1024 + lane * 16, o);
          else {
            const int n = 3 * (j >> 1) + ((j & 1) ? 2 : t);
#pragma unroll
            for (int r = 0; r < 4; ++r) wa[n][4 * (u & 1) + r] = o[r];
#define M_(r) asm volatile("" : "+" r(wa[n]))
            if (u & 1) AT_ACC(M_, n);
#undef M_
          }
        }
      }
      BAR();
    }
  }
#pragma unroll
  for (int u = 0; u < 8; ++u) {
    v4u o[8];
    shuffle_cols(c, MEMS + w * 8192, buf[u % 3], o);
    SCHED();
    if (u + 3 < 8) request_o(u + 3);
#pragma unroll
    for (int i = 0; i < 4; ++i) {
      const int pr = 4 * (u >> 2) + i, ch = u & 3;
      const v8u f = {o[2 * i][0], o[2 * i + 1][0], o[2 * i][1], o[2 * i + 1][1], o[2 * i][2], o[2 * i + 1][2], o[2 * i][3], o[2 * i + 1][3]};
      if (ch < 2) {
        wh[2 * pr + ch] = f;
#define M_(r) asm volatile("" : "+" r(wh[2 * pr + ch]))
        AT_HOME(M_, 2 * pr + ch);
#undef M_
      } else if (ch == 2) {
        wa[24 + pr] = f;
#define M_(r) asm volatile("" : "+" r(wa[24 + pr]))
        AT_ACC(M_, 24 + pr);
#undef M_
      } else {
        sts16(c.L, WL + (w * 16 + 8 + pr) * 2048 + lane * 16, lo4(f));
        sts16(c.L, WL + (w * 16 + 8 + pr) * 2048 + 1024 + lane * 16, hi4(f));
      }
    }
    SCHED();
  }
  if (!claimed) claim(c, 0);
  BAR();
  STAMP(0);
  const unsigned vote = wait_verdict(c);
  const int par = vote_b(vote), para = vote_a(vote);
  const int moff = opq(amap(w * 4096 + lane * 16, par) + (turn(h, w) & 1) * 1024), m2 = R_MEM + (turn(h, w) & 2) * 1024;
  unsigned tries = 1u << 12;
  *(volatile LAS unsigned*)(c.L + 160512) = 0u;
  LGKM(0);
  const int idx = opq((lane & 1) ? 0xEEEE : 0x4444);
  const int u_off = opq((4 * w + g) * 16384 + (h ^ (8 * g)) * 256 + cc * 16);
  const int hq = opq((lane | w) ? 1 << 30 : amap(24576 + (32 + h) * 128, par));
  const int l0 = opq(WL + w * 32768 + lane * 16), l1 = opq(MEMS + lane * 16);
  const int vmw = opq(VM + 2 * g * VROW + (32 * w + 2 * cc) * 2);
  const int vmr = opq(VM + (cc >> 1) * VROW + g * 16);
  TICK_DECL;
  anchor(k, true);
  unsigned tg = k.cyc;
  Plan tt = {k.g - NS(100000), NS(T_DX)};
  for (int s = 0; s < NSEG; ++s) {
    const int pb = R_P + (s & 3) * P_SLOT;
    PIN_ACC0(wa); PIN_ACC1(wa); PIN_HOME(wh);
    asm volatile(VO_STEP
        : [bd] "+s"(c.budget), [rt] "+s"(tries)
        : [l0] "v"(l0), [l1] "v"(l1), [mo] "v"(moff), [ix] "v"(idx), [vw] "v"(vmw), [vr] "v"(vmr), [uo] "v"(u_off), [w] "s"(w), [rs] "s"(c.rs),
          [sb] "s"(m2 + (s & 3) * MEM_SLOT), [ub] "s"(u_slot(s, para) + (h >> 3) * 16 * 16384), [ur] "s"(u_slot(s + 2, para) + (2 * h + (w >> 1)) * 16384 + (w & 1) * 4096 - MEMS), [tg] "s"(tg + T_VLAG), [hq] "v"(hq), [pb] "s"(pb),
          [ca] "s"(k.cyc - T_DV), [k1] "s"(k.k1), [ga] "s"(k.g), [dr] "n"(T_RV), [pr] "s"(R_P + ((s + 1) & 3) * P_SLOT)
        : CL_S, CL_16_32, CL_160_200, CL_200_216, CL_216_256);
    PIN_ACC0(wa); PIN_ACC1(wa); PIN_HOME(wh);
    if (w == 0) {
      LITE(s);
      LOG(1, s, tg);
      const unsigned sv = ask_stamps(c, par, pb);
      SCHED();
      anchor(k);
      const unsigned gc = tt.gx + tt.dx + NS(T_M1 + T_CLR);
      const bool ontime = (int)(gc - k.g) > 0;
      if (ontime) { wait_cyc(to_cyc(k, gc)); RAWBAR(); }
      SCHED();
      advance(tt, latest(c, k, par, pb, sv));
      if (!ontime) { wait_cyc(to_cyc(k, tt.gx - NS(T_DY + T_SAFE - T_CLR))); RAWBAR(); }
      tg = to_cyc(k, tt.gx);
      LOG(2, s, k.cyc); LOG(3, s, k.g);
    }
    TICK_SUM(10);
    if (w == 0) { close_in(k, tg); tg = to_cyc(k, tt.gx); }
  }
  TICK_OUT();
  if (c.budget == 0u) __hip_atomic_store(c.ctrl + C_TMO / 4, 1u, RLX_AGENT);
}

DEV void y_role(const Params& p, Ctx& c, int y) {
  constexpr int ZR = 0, UL = 131072, CW = 155648;
  c.budget = SPIN_CAP;
  const int w = c.w, lane = c.lane = fresh_lane(), g = lane >> 4, cc = lane & 15;
  const v4u sent4 = {SENT, SENT, SENT, SENT};
  STAMP(2);
  Clk k = {0u, 0u, 0u, 0u, 0u, 0u};
  anchor(k, true);
  unsigned m0 = 0;
  if (w == 0) {
    unsigned best[2] = {~0u, ~0u};
    const unsigned first = claim_look(c);
    st4s(c.rs, y * 16384 + (lane >> 5) * 8192 + (lane & 31) * 4, R_U, SENT);
    ask(c, first);
#pragma unroll 1
    for (int n = 0; n < 12; ++n) {
#pragma unroll
      for (int r = 0; r < 2; ++r) {
        VMWAIT(0);
        const unsigned t0 = (unsigned)__builtin_amdgcn_s_memtime();
        LGKM(0);
        st4s(c.rs, y * 16384 + (lane & 31) * 4, R_U + r * 8192, SENT);
        VMWAIT(0);
        const unsigned t1 = (unsigned)__builtin_amdgcn_s_memtime();
        LGKM(0);
        best[r] = min(best[r], t1 - t0);
      }
      if (n + 1 >= PROBES && min(best[0], best[1]) < 600u && max(best[0], best[1]) > min(best[0], best[1]) + 128u) break;
    }
    STAMP(5);
    claim(c, CL_VOTED | (best[1] < best[0] ? CL_A : 0u));
    m0 = *(const GAS unsigned*)(p.mem0 + (lane >> 3) * 1024 + 16 * y + 2 * (lane & 7));
    m0 = m0 < SENT - 1u ? m0 : SENT - 1u;
    if (lane < 8) *(LAS unsigned*)(c.L + CW + lane * 4) = 0u;
    const unsigned v = wait_vote(c);
    STAMP(7);
    const int oa = y * 16384 + vote_a(v) * 8192 + lane * 16, ob = amap(y * 3072 + lane * 16, vote_b(v));
#pragma unroll
    for (int i = 0; i < 16; ++i) st16s(c.rs, oa + (i & 3) * 1024, R_U + (i >> 3) * MB + ((i >> 2) & 1) * 4096, sent4);
#pragma unroll
    for (int i = 0; i < 3; ++i) st16s(c.rs, amap(y * 3072 + i * 1024 + lane * 16, vote_b(v)), R_MEM, sent4);
#pragma unroll
    for (int i = 0; i < 8; ++i) st16s(c.rs, oa + (i & 3) * 1024, R_PT + (i >> 2) * 4096, sent4);
    (void)ob;
    VMWAIT(0);
    if (lane == 0) __hip_atomic_fetch_add(c.ctrl + (C_CLAIM + c.att * 1024) / 4 + c.slot, CL_FILLED, RLX_AGENT);
    STAMP(6);
  } else {
    if (YH && w < 3 && (y & 8) == 0) {
      hold(c, YH_AT);
#pragma unroll
      for (int i = 0; i < 8; ++i) {
        const u16* q = (i < 4) != K_FIRST ? p.Wq : p.Wk;
        if (i == 4) hold(c, YH_AT + YH_STEP);
        m0 += __builtin_amdgcn_raw_buffer_load_b32(mkrs(q, 8 * MB), slow_line(q, y >> 1, (y & 7) + 8 * (i & 3), lane) ^ ((w - 1) * 128), 0, 0);
      }
      asm volatile("" :: "v"(m0));
    }
  }
  BAR();
  const unsigned vote = wait_verdict(c);
  const int par = vote_b(vote), para = vote_a(vote);
  const int mo = amap(y * 256 + lane * 4, par);
  if (w == 0) st4s(c.rs, mo, R_MEM, m0);
  STAMP(0);
  const int rot = y >> 4;
  volatile LAS unsigned* const GO = (volatile LAS unsigned*)(c.L + CW);
  TICK_DECL;
  if (w == 0) {
    int xo[4];
#pragma unroll
    for (int i = 0; i < 4; ++i) xo[i] = amap((i ^ rot) * 4096 + lane * 16, par);
    const int pmo = amap(16384 + rot * 1024 + (2 * g + (cc & 1)) * 128 + (cc >> 1) * 16, par), psel = (cc & 1) ? 0x03020706 : 0x05040100;
    constexpr int lead = T_U0, late = 0;
    anchor(k, true);
    Plan tt = {k.g - NS(100000), NS(T_DX)};
    unsigned done = k.cyc;
    const int so = amap(24576, par) + lane * 128;
    const int idx = opq((lane & 1) ? 0xEEEE : 0x4444);
    const int uo = y * 16384 + lane * 16;
    const int mw = amap(y * 256 + (2 * g + (cc & 1)) * 32 + (cc >> 1) * 4, par);
    for (int s = 0; s < NSEG - 1; ++s) {
      if (s == 0) RAWBAR();
      const int za = ZR + (s & 3) * 32768 + rot * 8192 + lane * 16;
      v32u z0, z1, z2, z3;
      asm volatile(
          "ds_read_b128 a[0:3], %[z0] offset:0\n\t" "ds_read_b128 a[4:7], %[z0] offset:1024\n\t" "ds_read_b128 a[8:11], %[z0] offset:2048\n\t" "ds_read_b128 a[12:15], %[z0] offset:3072\n\t"
          "ds_read_b128 a[16:19], %[z0] offset:4096\n\t" "ds_read_b128 a[20:23], %[z0] offset:5120\n\t" "ds_read_b128 a[24:27], %[z0] offset:6144\n\t" "ds_read_b128 a[28:31], %[z0] offset:7168\n\t"
          "ds_read_b128 a[32:35], %[z1] offset:0\n\t" "ds_read_b128 a[36:39], %[z1] offset:1024\n\t" "ds_read_b128 a[40:43], %[z1] offset:2048\n\t" "ds_read_b128 a[44:47], %[z1] offset:3072\n\t"
          "ds_read_b128 a[48:51], %[z1] offset:4096\n\t" "ds_read_b128 a[52:55], %[z1] offset:5120\n\t" "ds_read_b128 a[56:59], %[z1] offset:6144\n\t" "ds_read_b128 a[60:63], %[z1] offset:7168\n\t"
          "ds_read_b128 a[64:67], %[z2] offset:0\n\t" "ds_read_b128 a[68:71], %[z2] offset:1024\n\t" "ds_read_b128 a[72:75], %[z2] offset:2048\n\t" "ds_read_b128 a[76:79], %[z2] offset:3072\n\t"
          "ds_read_b128 a[80:83], %[z2] offset:4096\n\t" "ds_read_b128 a[84:87], %[z2] offset:5120\n\t" "ds_read_b128 a[88:91], %[z2] offset:6144\n\t" "ds_read_b128 a[92:95], %[z2] offset:7168\n\t"
          "ds_read_b128 a[96:99], %[z3] offset:0\n\t" "ds_read_b128 a[100:103], %[z3] offset:1024\n\t" "ds_read_b128 a[104:107], %[z3] offset:2048\n\t" "ds_read_b128 a[108:111], %[z3] offset:3072\n\t"
          "ds_read_b128 a[112:115], %[z3] offset:4096\n\t" "ds_read_b128 a[116:119], %[z3] offset:5120\n\t" "ds_read_b128 a[120:123], %[z3] offset:6144\n\t" "ds_read_b128 a[124:127], %[z3] offset:7168\n\t"
          "s_waitcnt lgkmcnt(0)"
          : "={a[0:31]}"(z0), "={a[32:63]}"(z1), "={a[64:95]}"(z2), "={a[96:127]}"(z3) : [z0] "v"(za), [z1] "v"(za ^ 8192), [z2] "v"(za ^ 16384), [z3] "v"(za ^ 24576) : "memory");
      const int pb = R_P + (s & 3) * P_SLOT;
      v32u uu;
      const int ub = u_slot(s, para);
      TICK(0);
      anchor(k);
      TICK(8);
      unsigned gp = tt.gx + tt.dx + NS(T_M1), lt = late, sv = SENT;
      unsigned bad = (int)(done - to_cyc(k, tt.gx)) > T_OVH;
      TICK(4);
      LOG(2, s, k.cyc); LOG(3, s, k.g);
#pragma unroll 1
      for (;;) {
        if (bad) {
          const bool all = allok(sv);
          const unsigned last = latest(c, k, par, pb, sv);
          if (!all) mark(k);
          gp = when_p(tt, last);
          lt = 1;
        }
        close_in(k, to_cyc(k, gp) - (lt ? 96 : lead));
        const unsigned tg = to_cyc(k, gp);
        if (!bad) LOG(1, s, tg);
        asm volatile(Y_STEP
          : "=&{v[144:175]}"(uu), [bad] "=&s"(bad), [sv] "=&v"(sv)
          : [x0] "v"(xo[0]), [x1] "v"(xo[1]), [x2] "v"(xo[2]), [x3] "v"(xo[3]), [pm] "v"(pmo), [u0] "v"(uo + (rot ^ (y & 3)) * 2048), [mw] "v"(mw), [ps] "v"(psel), [ix] "v"(idx),
            [rs] "s"(c.rs), [pb] "s"(pb), [ub] "s"(ub), [mb] "s"(R_MEM + ((s + 1) & 3) * MEM_SLOT), [mr] "s"(R_MEM + ((s + 3) & 3) * MEM_SLOT), [tu] "s"(tg - (lt ? 96 : lead)), [tg] "s"(tg - 32), [late] "s"(lt),
            [so] "v"(so), [bd] "s"(c.budget),
            "{a[0:31]}"(z0), "{a[32:63]}"(z1), "{a[64:95]}"(z2), "{a[96:127]}"(z3)
          : CL_S, CL_120_128, CL_176_200, CL_200_216, CL_216_256);
        bad = __builtin_amdgcn_readfirstlane(bad);
        if (bad != 1u) break;
        spend(c);
      }
      done = bad;
      SCHED();
      const int ul = opq(UL + rot * 2048 + lane * 16);
#pragma unroll
      for (int i = 0; i < 8; ++i) sts16(c.L, (ul ^ ((i >> 1) * 2048)) + (i & 1) * 1024, v4u{uu[4 * i], uu[4 * i + 1], uu[4 * i + 2], uu[4 * i + 3]});
      asm volatile("" ::: "memory");
      *GO = s + 1;
      asm volatile("s_waitcnt vmcnt(2)" : "+v"(sv));
      advance(tt, latest(c, k, par, pb, sv));
      TICK(3);
      LITE(s); LOG(0, s, done);
      TICK_LDS(CW + 256);
    }
    {
      anchor(k);
      wait_cyc(to_cyc(k, tt.gx + tt.dx + NS(T_M1)));
      const int ub = u_slot(NSEG - 1, para);
      v4u u[8];
#pragma unroll 1
      for (;;) {
        asm volatile("" ::: "memory");
#pragma unroll
        for (int i = 0; i < 8; ++i) u[i] = ld16s(c.rs, uo + (i & 3) * 1024, ub + (i >> 2) * 4096);
        SCHED();
        unsigned t = 0;
#pragma unroll
        for (int i = 0; i < 8; ++i) t = umax(t, top(u[i]));
        if (allok(t) || spend(c)) break;
      }
      RAWBAR();
#pragma unroll
      for (int i = 0; i < 8; ++i) sts16(c.L, UL + (i ^ (2 * (y & 3))) * 1024 + lane * 16, u[i]);
      asm volatile("" ::: "memory");
      *GO = NSEG;
    }
    TICK_DUMP(CW + 256);
  } else if (w < 3) {
    const rsrc_t ro = mkrs(p.out, 2048 * 2048 * 2);
    auto tokens = [&](auto QT) {
      constexpr int qt = QT.value, NX = qt ? 32 : 16;
      RAWBAR();
      for (int s = 0; s < NSEG; ++s) {
        const int tb = R_PT + (s & 7) * PT_SLOT + para * 8192;
        int ua[4], za[4], xb[4], mb[4];
#pragma unroll
        for (int i = 0; i < 4; ++i) {
          ua[i] = opq(UL + (i ^ rot) * 2048 + lane * 16);
          za[i] = opq(ZR + (s & 3) * 32768 + (i ^ rot) * 8192 + (qt ? lane * 16 : (g >> 1) * 1024 + (g & 1) * 512 + cc * 16));
          xb[i] = tb + amap(qt * 16384 + (i ^ rot) * (qt ? 8192 : 4096), 0);
          mb[i] = tb + amap(49152 + qt * 8192, 0) + (i ^ rot) * 2048;
        }
        SCHED();
        RAWBAR();
        if (TOK_WAIT) __builtin_amdgcn_s_sleep(TOK_WAIT);
        TICK(0);
        f32x4 a;
#pragma unroll 1
        for (;;) {
          asm volatile("" ::: "memory");
          v4u pt[NX + 8];
#pragma unroll
          for (int i = 0; i < NX; ++i) pt[i] = ld16s(c.rs, (i & 3) * 1024 + lane * 16, qt ? xb[i >> 3] + ((i >> 2) & 1) * 4096 : xb[i >> 2]);
#pragma unroll
          for (int i = 0; i < 8; ++i) pt[NX + i] = ld16s(c.rs, (i & 1) * 1024 + lane * 16, mb[i >> 1]);
          SCHED();
          TICK(1);
#pragma unroll 1
          while (__builtin_amdgcn_readfirstlane(*GO) < (unsigned)s + 1u) { if (spend(c)) break; }
          asm volatile("" ::: "memory");
          f32x4 d[2] = {f32x4{0.f, 0.f, 0.f, 0.f}, f32x4{0.f, 0.f, 0.f, 0.f}};
          v4u fa[8];
          auto frag = [&](int i) {
            if (i >= NX) fa[i & 7] = lds16(c.L, ua[(i - NX) >> 1] + (i & 1) * 1024);
            else if (qt) fa[i & 7] = lds16(c.L, za[i >> 3] + (i & 7) * 1024);
            else {
              const v2u lo = *(LAS v2u*)(c.L + za[i >> 2] + (i & 3) * 2048), hi = *(LAS v2u*)(c.L + za[i >> 2] + (i & 3) * 2048 + 256);
              fa[i & 7] = v4u{lo.x, lo.y, hi.x, hi.y};
            }
          };
#pragma unroll
          for (int i = 0; i < 7; ++i) frag(i);
          SCHED();
#pragma unroll
          for (int i = 0; i < NX + 8; ++i) {
            if (i + 7 < NX + 8) frag(i + 7);
            d[i & 1] = mfma16(fa[i & 7], pt[i], d[i & 1]);
            SCHED();
          }
          a = d[0] + d[1];
          if (!__any(a[0] != a[0])) break;
          unsigned t = 0;
#pragma unroll
          for (int i = 0; i < NX + 8; ++i) t = umax(t, top(pt[i]));
          if (allok(t) || spend(c)) break;
        }
        __builtin_amdgcn_raw_buffer_store_b64(v2u{pk(a[0], a[1]), pk(a[2], a[3])}, ro, (16 * qt + cc) * 4096 + (16 * y + 4 * g) * 2, s * 32 * 4096, OUT_AUX);
        TICK(2);
        TICK_SUM(3);
      }
    };
    if (w == 1) tokens(IC<0>{}); else tokens(IC<1>{});
  } else {
    unsigned seenZ = NOFLAG;
    const rsrc_t rc = mkrs((const void*)c.ctrl, CTRL_BYTES);
    const int fo = lane < 32 ? lane * 128 : 1 << 30;
    __builtin_amdgcn_s_setprio(3);
    auto least = [&](unsigned v) {
      v |= lane < 32 ? 0u : ~0u;
#pragma unroll
      for (int o = 1; o < 32; o <<= 1) v = min(v, (unsigned)__shfl_xor((int)v, o));
      seenZ = umax(seenZ, __builtin_amdgcn_readfirstlane(v));
    };
    auto await = [&](unsigned need) {
#pragma unroll 1
      while (seenZ < need) {
        asm volatile("" ::: "memory");
        least(__builtin_amdgcn_raw_buffer_load_b32(rc, fo, C_FLAG + (F_Z + (y >> 4) * 32) * 128, AUX_SC1));
        if (seenZ >= need || spend(c)) break;
        __builtin_amdgcn_s_sleep(4);
      }
      __builtin_amdgcn_fence(__ATOMIC_ACQUIRE, "wavefront");
    };
    for (int n = 0; n < NSEG; ++n) {
      TICK(0);
      await(n + 1);
      TICK(1);
      v4u z[32];
      int za[8];
#pragma unroll
      for (int i = 0; i < 8; ++i) za[i] = opq(ZR + (n & 3) * 32768 + (i ^ z_mix(y)) * 1024 + lane * 16);
      unsigned look;
#pragma unroll
      for (int i = 0; i < 32 + Z_WIN; ++i) {
        if (i < 32) z[i] = ld16z(c.rs, (i & 3) * 1024 + lane * 16, R_Z + (n & (ZRING - 1)) * Z_SLOT + y * 65536 + ((para ^ (y >> 5)) & 1) * 8192 + amap((i >> 2) * 4096, 0));
        if (i == 32) { look = __builtin_amdgcn_raw_buffer_load_b32(rc, seenZ < (unsigned)n + 2u ? fo : 1 << 30, C_FLAG + (F_Z + (y >> 4) * 32) * 128, AUX_SC1); TICK(4); }
        SCHED();
        if (i >= Z_WIN) sts16(c.L, za[(i - Z_WIN) & 7] + ((i - Z_WIN) >> 3) * 8192, z[i - Z_WIN]);
        else asm volatile("s_nop " STR(Z_GAP));
        if (i == 30 + Z_WIN) { TICK(3); }
        SCHED();
      }
      least(look);
      if (n == 0) await(PRIME);
      TICK(2);
      { const int s = n; (void)s; TICK_SUM(3); }
      BAR();
    }
    RAWBAR();
  }
  TICK_OUT();
  if (c.budget == 0u) __hip_atomic_store(c.ctrl + C_TMO / 4, 1u, RLX_AGENT);
}

#define MF1A(d, a, b) asm volatile(MF16 "%0, %1, %2, %0" : "+v"(d) : "a"(a), "v"(b))
#define MF32A(d, a, b) asm volatile("v_mfma_f32_32x32x16_bf16 %0, %1, %2, %0" : "+v"(d) : "a"(a), "v"(b))
#define MF32AZ(d, a, b) asm volatile("v_mfma_f32_32x32x16_bf16 %0, %1, %2, 0" : "=&v"(d) : "a"(a), "v"(b))
#define MF1B(d, a, b) asm volatile(MF16 "%0, %1, %2, %0" : "+v"(d) : "v"(a), "a"(b))
#define MF1BZ(d, a, b) asm volatile(MF16 "%0, %1, %2, 0" : "=&v"(d) : "v"(a), "a"(b))
#define MF1AZ(d, a, b) asm volatile(MF16 "%0, %1, %2, 0" : "=&v"(d) : "a"(a), "v"(b))
DEV void g_role(const Params& p, Ctx& c, int mat, int h, int gi) {
  constexpr int EX = 131072;
  c.budget = SPIN_CAP;
  const int w = c.w, lane = c.lane = fresh_lane(), g = lane >> 4, cc = lane & 15;
  unsigned look = claim_look(c);
  const int flip = FLIP && mat < 2 ? ((h >> 4) & 1) ^ 1 : 0;
  unsigned t[7];
  {
    auto pull = [&](const u16* q, int bytes, int v, int so) { return __builtin_amdgcn_raw_buffer_load_b32(mkrs(q, bytes), v, so, 0); };
    const int all = mat < 2 ? 8 * MB : 0, xq = mat == 0 ? 4 * MB : 0;
    t[6] = pull(p.Wv, SLOW_V * all, slow_line(p.Wv, h, (h & 7) + 8 * w, lane) ^ (mat * 128), 0);
    t[0] = pull(p.x, xq, lane < 16 ? (((h & 7) * 4 + w) * 16 + lane) * 128 : 1 << 30, 0);
#pragma unroll
    for (int i = 0; i < 3; ++i) t[1 + i] = pull(p.Wv, HELP_V * all, (256 * w + 4 * (h & 7) + g) * 8192 + (h >> 3) * 2048 + cc * 128, (2 + 2 * i + mat) * 32 * 8192);
    SCHED();
    if (mat < 2) ask(c, look);
    SCHED();
#pragma unroll
    for (int i = 0; i < 2; ++i) t[4 + i] = pull(p.Wo, HELP_O * all, (h * 128 + (lane >> 1)) * 2048 + w * 512 + 256 + (lane & 1) * 128, (2 * i + mat) * 32 * 2048);
    SCHED();
    if (mat == 0) claim(c, 0);
    hold(c, mat == 0 ? GQ_AT : mat == 1 ? GK_AT : 0);
  }
  const rsrc_t rx = mkrs(p.x, 2048 * 2048);
  const int xs = w * 16384 + ((lane * 16) ^ ((w & 1) << 7));

  v32u wa[8];
  {
    const rsrc_t rw = mkrs(mat == 0 ? p.Wq : mat == 1 ? p.Wk : p.Wv, 1024 * 4096 * 2);
    const int vo0 = opq((256 * w + 128 * flip + 8 * g) * 8192 + (h * 128 + 8 * cc) * 2), vo1 = opq(vo0 ^ (128 * 8192));
    v4u row[8][8];
    auto request = [&](int u) {
#pragma unroll
      for (int r = 0; r < 8; ++r) {
        row[u][r] = ld16w<0>(rw, (u & 4) ? vo1 : vo0, (32 * (u & 3) + r) * 8192);
        if ((r & 3) == 3) { const int n = 2 * u + (r >> 2); dma16(rx, c.L + w * 16384 + n * 1024, (xs ^ ((n >> 1) << 4)) + n * 1024); }
      }
    };
    const unsigned look0 = claim_look(c);
    bool claimed = false;
    request(0);
    SCHED();
    if (mat == 2) ask(c, look);
    SCHED();
    request(1); request(2);
    if (G_DEEP) request(3);
    SCHED();
    const int sw_ = EX + w * 8192 + g * 256;
    const int sr0 = opq(EX + (cc & 3) * 1024 + g * 256 + ((4 * w + (cc >> 2) + 4 * cc) & 15) * 16), sr1 = sr0 + 4096;
#pragma unroll
    for (int u = 0; u < 8; ++u) {
#pragma unroll
      for (int i = 0; i < 8; ++i) {
        v4u f;
#pragma unroll
        for (int r = 0; r < 4; ++r) f[r] = prm(row[u][2 * r + 1][i >> 1], row[u][2 * r][i >> 1], i & 1);
        sts16(c.L, sw_ + i * 1024 + ((cc + 4 * i) & 15) * 16, f);
      }
      SCHED();
      if (u == 0) asm volatile("" :: "v"(t[0]), "v"(t[1]), "v"(t[2]), "v"(t[3]), "v"(t[4]), "v"(t[5]), "v"(t[6]));
      if (u == 0) claimed = mat == 0 || claim_try(c, look0);
      if (u == 4 - G_DEEP) look = claim_look(c);
      if (u + 3 + G_DEEP < 8) request(u + 3 + G_DEEP);
      if (u == 7 && !claimed && !claim_try(c, look)) claim(c, 0);
      BAR();
      v8u in[4];
#pragma unroll
      for (int q = 0; q < 4; ++q) in[q] = cat(lds16(c.L, sr0 + q * 8192), lds16(c.L, sr1 + q * 8192));
      SCHED();
#pragma unroll
      for (int q = 0; q < 4; ++q) {
        v8u f = in[q];
#define M_(r) asm volatile("" : "+" r(f))
        AT_ACC(M_, 8 * q + u);
#undef M_
#pragma unroll
        for (int r = 0; r < 8; ++r) wa[2 * q + (u >> 2)][8 * (u & 3) + r] = f[r];
      }
      BAR();
    }
  }
  STAMP(0);
  const int xpar = vote_a(wait_vote(c)) ^ (mat >> 1);
  TICK_DECL;
  asm volatile(G_LOOP
      :: [xo] "v"(cc * 2048 + flip * 256 + ((g ^ cc) << 4)), [xs] "v"(xs), [so] "v"((4 * w + g) * 512 + cc * 16), [fo] "v"((w | lane) ? 1 << 30 : 0),
         [rs] "s"(c.rs), [rx] "s"(rx), [rc] "s"(mkrs((const void*)c.ctrl, CTRL_BYTES)), [fl] "s"(C_FLAG + (mat * 32 + h) * 128), [xp] "s"(xpar),
         [ob] "s"((mat == 1 ? R_KX : R_QX) + h * 16384), [lw] "s"(w * 16384), GTRACE ACC_IN(wa)
      : CL_S, CL_16_32, CL_32_100);
}

DEV void zc_role(const Params& p, Ctx& c, int h) {
  c.budget = SPIN_CAP;
  const int w = c.w, lane = c.lane = fresh_lane(), g = lane >> 4, cc = lane & 15, h2 = lane >> 5, r32 = lane & 31;
  v32u wa[8];
  const unsigned first = claim_look(c);
  {
    const rsrc_t rw = mkrs(p.Wo, 4096 * 1024 * 2);
    const int vo = (h * 128 + 8 * g) * 2048 + (256 * w + 8 * cc) * 2;
    v4u row[8][8];
    auto request = [&](int u) {
#pragma unroll
      for (int r = 0; r < 8; ++r) row[u][r] = ld16w<0>(rw, vo, (32 * (u & 3) + r) * 2048 + (u >> 2) * 256);
    };
    request(0);
    SCHED();
    ask(c, first);
    SCHED();
    request(1); request(2);
    unsigned look = 0;
    const int sw_ = w * 8192 + g * 256, sr_ = w * 8192 + (cc & 7) * 1024 + g * 256;
    v8u two[8];
#pragma unroll
    for (int u = 0; u < 8; ++u) {
#pragma unroll
      for (int i = 0; i < 8; ++i) {
        v4u f;
#pragma unroll
        for (int r = 0; r < 4; ++r) f[r] = prm(row[u][2 * r + 1][i >> 1], row[u][2 * r][i >> 1], i & 1);
        sts16(c.L, sw_ + i * 1024 + ((cc + 2 * i) & 15) * 16, f);
      }
      SCHED();
      if (u == 4) look = claim_look(c);
      if (u + 3 < 8) request(u + 3);
      if (u == 7 && !claim_try(c, look)) claim(c, 0);
      LGKM(0);
#pragma unroll
      for (int q = 0; q < 8; ++q) {
        const v4u o = lds16(c.L, sr_ + ((2 * q + (cc >> 3) + 2 * cc) & 15) * 16);
#pragma unroll
        for (int r = 0; r < 4; ++r) two[q][4 * (u & 1) + r] = o[r];
      }
      SCHED();
      if (u & 1) {
#pragma unroll
        for (int q = 0; q < 8; ++q) {
          const int n = 2 * (8 * (u >> 2) + q) + ((u >> 1) & 1);
#define M_(r) asm volatile("" : "+" r(two[q]))
          AT_ACC(M_, n);
#undef M_
#pragma unroll
          for (int r = 0; r < 8; ++r) wa[n >> 2][8 * (n & 3) + r] = two[q][r];
        }
      }
      LGKM(0);
      SCHED();
    }
  }
  STAMP(0);
  {
    unsigned t = 0;
#pragma unroll
    for (int i = 0; i < 4; ++i) t += __builtin_amdgcn_raw_buffer_load_b32(mkrs(p.x, 4 * MB), ((4 * h + w) * 64 + lane) * 128, i * MB, 0);
    asm volatile("" :: "v"(t));
  }
  BAR();
  const int para = vote_a(wait_vote(c));
  const int ao = g * 512 + cc * 16, zo = 16 * w * 65536 + lane * 16 + ((para ^ (w >> 1)) & 1) * 8192;
  TICK_DECL;
  asm volatile(ZC_LOOP
      : [bd] "+s"(c.budget)
      : [ao] "v"(ao), [a1] "v"(ao + 4096), [z1] "v"(zo + amap((h ^ z_mix(16 * w)) * 1024, 0)), [z2] "v"(zo + 65536 + amap((h ^ z_mix(16 * w + 1)) * 1024, 0)), [fo] "v"(lane ? 1 << 30 : 0),
        [rs] "s"(c.rs), [rc] "s"(mkrs((const void*)c.ctrl, CTRL_BYTES)), [fv] "s"(C_FLAG + (F_V + h) * 128), [fs] "s"(C_FLAG + (F_S + h) * 128),
        [fz] "s"(C_FLAG + (F_Z + w * 32 + h) * 128), [xp] "s"(para ^ 1), [vb] "s"(R_QX + h * 16384), [vd] "s"(C_VERDICT + c.att * 128), [z0] "n"(ZC_PRIME), [zb] "n"(R_Z), [rg] "n"(ZRING - 2), [rm] "n"(ZRING - 1), GTRACE ACC_IN(wa)
      : CL_S, CL_16_32, CL_32_100);
  if (c.budget == 0u) __hip_atomic_store(c.ctrl + C_TMO / 4, 1u, RLX_AGENT);
}

DEV void conv_role(const Params& p, Ctx& c, int cg, int far) {
  constexpr int FS = 4160, PL = 1280, UO = 4 * FS, WS = UO + 8 * PL;
  const int w = c.w, lane = c.lane = fresh_lane(), tid = w * 64 + lane, g = lane >> 4, cc = lane & 15;
  const int d0 = 8 * cg + 2 * w;
  const int pg = ((far ^ (cg >> 5)) & 1) * 8192;
  auto crow = [](int ch) { return (ch >> 1) * 16384 + (ch & 1) * 4096; };
  const int de = (cg >> 3) * 64 + (tid & 7) * 8, t0 = (cg & 7) * 256 + (tid >> 3) * 8;
  auto group_sync = [&](int phase) {
    gu32* cnt = c.ctrl + (C_CONV + (cg >> 3) * 16 + phase * 8) / 4;
    VMWAIT(0);
    BAR();
    if (w == 0) {
      if (lane == 0) __hip_atomic_fetch_add(cnt, 1u, RLX_AGENT);
#pragma unroll 1
      while (__builtin_amdgcn_readfirstlane(__hip_atomic_load(cnt, RLX_AGENT)) < 8u) { if (spend(c)) break; __builtin_amdgcn_s_sleep(2); }
    }
    BAR();
  };
  v4u fr[2][4], ur[2][4], qr[8];
  unsigned up[2][4];
  const int so = cc * 64 + g * 16;
  const unsigned dd = __builtin_amdgcn_raw_buffer_load_b32(mkrs(p.ssmD, 2048), d0 * 2, 0, CONV_AUX);
  BAR();
  {
    const rsrc_t rk = mkrs(p.bk, 2048 * 2048), rv = mkrs(p.bv, 2048 * 2048);
    v4u kr[11], vr[8], sh[4];
#pragma unroll
    for (int i = 0; i < 4; ++i) sh[i] = ld16c(mkrs(p.shift, 8192), de * 8 + i * 16, 0);
#pragma unroll
    for (int i = 0; i < 11; ++i) kr[i] = ld16c(rk, (t0 + i - 3) * 2048 + de * 2, 0);
#pragma unroll
    for (int i = 0; i < 8; ++i) vr[i] = ld16c(rv, (t0 + i) * 2048 + de * 2, 0);
#pragma unroll
    for (int i = 0; i < 8; ++i) fr[i >> 2][i & 3] = ld16c(mkrs(p.ssmk, 4 * MB), lane * 16 + (i & 3) * 1024, (d0 + (i >> 2)) * 4096);
    SCHED();
    if (lane < 16) sts16(c.L, w * WS + (lane >> 2) * FS + (lane & 3) * 16, v4u{0, 0, 0, 0});
#pragma unroll
    for (int i = 0; i < 2; ++i) sts16(c.L, w * WS + UO + (4 * i + g) * PL + cc * 16, v4u{0, 0, 0, 0});
#pragma unroll
    for (int ch = 0; ch < 8; ++ch) {
      const unsigned sa = sh[ch >> 1][(ch & 1) * 2], sb = sh[ch >> 1][(ch & 1) * 2 + 1];
      const float s0 = blo(sa), s1 = bhi(sa), s2 = blo(sb), s3 = bhi(sb);
      float kk[11], o[8];
#pragma unroll
      for (int i = 0; i < 11; ++i) kk[i] = (ch & 1) ? bhi(kr[i][ch >> 1]) : blo(kr[i][ch >> 1]);
#pragma unroll
      for (int i = 0; i < 8; ++i) o[i] = fmaf(kk[i + 3], s0, fmaf(kk[i + 2], s1, fmaf(kk[i + 1], s2, kk[i] * s3))) * ((ch & 1) ? bhi(vr[i][ch >> 1]) : blo(vr[i][ch >> 1]));
      st16s(c.rs, de * 8192 + t0 * 2, R_KVT + pg + crow(ch), v4u{pk(o[0], o[1]), pk(o[2], o[3]), pk(o[4], o[5]), pk(o[6], o[7])});
    }
    group_sync(0);
#pragma unroll
    for (int i = 0; i < 8; ++i) {
      const int b = R_KVT + pg + d0 * 8192 + (i >> 2) * 4096 + (i & 3) * 1024;
      ur[i >> 2][i & 3] = ld16s(c.rs, so, b);
      up[i >> 2][i & 3] = __builtin_amdgcn_raw_buffer_load_b32(c.rs, (i & 3) == 0 && lane == 0 ? 1 << 30 : so, b - 4, AUX_SC1);
    }
#pragma unroll
    for (int i = 0; i < 8; ++i) qr[i] = ld16c(mkrs(p.bq, 4 * MB), (t0 + i) * 2048 + de * 2, 0);
  }
  STAMP(5);
  const int fw = opq(w * WS + 48 + lane * 16), uw = opq(w * WS + UO + g * PL + cc * 16);
  const int fa = opq(w * WS + (cc & 3) * FS + (3 + (cc >> 2) - g) * 16);
  sfor<2>([&](auto CH) __attribute__((always_inline)) {
    constexpr int ch = CH.value;
    SCHED();
#pragma unroll
    for (int i = 0; i < 4; ++i) {
      const v4u f = fr[ch][i], a = ur[ch][i];
      sts16(c.L, fw + 3 * FS + i * 1024, f);
      sts8(c.L, fw + FS + i * 1024 + 8, v2u{f[0], f[1]}); sts8(c.L, fw + FS + i * 1024 + 16, v2u{f[2], f[3]});
#pragma unroll
      for (int r = 0; r < 2; ++r) {
        const int o = fw + 2 * r * FS + i * 1024 + 12 - 8 * r;
        *(LAS unsigned*)(c.L + o) = f[0]; sts8(c.L, o + 4, v2u{f[1], f[2]}); *(LAS unsigned*)(c.L + o + 12) = f[3];
      }
      v4u r0, r1;
#pragma unroll
      for (int r = 0; r < 4; ++r) {
        r0[r] = __builtin_amdgcn_alignbyte(a[3 - r], a[3 - r], 2);
        r1[r] = __builtin_amdgcn_perm(r < 3 ? a[2 - r] : up[ch][i], a[3 - r], 0x07060100u);
      }
      sts16(c.L, uw + (16 + 16 * i) * 16, r0);
      sts16(c.L, uw + 4 * PL + (16 + 16 * i) * 16, r1);
    }
    f32x4 acc[4][2];
#pragma unroll
    for (int i = 0; i < 8; ++i) acc[i >> 1][i & 1] = f32x4{0.f, 0.f, 0.f, 0.f};
    v4u A[2][4], B[2][8];
    auto frag = [&](int d, int n) {
      constexpr int order[12] = {0, 4, 5, 6, 7, 8, 9, 10, 11, 1, 2, 3};
      const int x = order[n];
      if (x < 4) A[d & 1][x] = lds16(c.L, fa + (d + 16 * x) * 64);
      else B[d & 1][x - 4] = lds16(c.L, uw + ((x - 4) & 1) * 4 * PL + (16 + 16 * ((x - 4) >> 1) - d) * 16);
    };
    asm volatile("" ::: "memory");
#pragma unroll
    for (int n = 0; n < 12; ++n) frag(0, n);
    SCHED();
    sfor<16>([&](auto D) __attribute__((always_inline)) {
      constexpr int d = D.value;
      int n = 0;
#pragma unroll
      for (int k = 0; k < 4; ++k)
#pragma unroll
        for (int m = 0; m + k < 4; ++m)
#pragma unroll
          for (int e = 0; e < 2; ++e) {
            acc[m + k][e] = mfma16(A[d & 1][k], B[d & 1][2 * m + e], acc[m + k][e]);
            if (d < 15 && n < 12) frag(d + 1, n);
            ++n;
            SCHED();
          }
    });
    const float dv = ch ? bhi(dd) : blo(dd);
#pragma unroll
    for (int q = 0; q < 4; ++q) {
      v4u z;
#pragma unroll
      for (int i = 0; i < 4; ++i) {
        const unsigned yy = pk(acc[q][1][i], acc[q][0][i]), ud = pk(blo(ur[ch][q][i]) * dv, bhi(ur[ch][q][i]) * dv);
        z[i] = pk(blo(yy) + blo(ud), bhi(yy) + bhi(ud));
      }
      st16s(c.rs, so + q * 1024, R_YT + pg + d0 * 8192 + ch * 4096, z);
    }
  });
  STAMP(6);
  {
    const rsrc_t ro = mkrs(p.out, 2048 * 2048 * 2);
    v4u yv[8];
    group_sync(1);
#pragma unroll
    for (int ch = 0; ch < 8; ++ch) yv[ch] = ld16s(c.rs, de * 8192 + t0 * 2, R_YT + pg + crow(ch));
#pragma unroll
    for (int i = 0; i < 8; ++i) {
      float o[8];
#pragma unroll
      for (int ch = 0; ch < 8; ++ch) o[ch] = ((ch & 1) ? bhi(qr[i][ch >> 1]) : blo(qr[i][ch >> 1])) * ((i & 1) ? bhi(yv[ch][i >> 1]) : blo(yv[ch][i >> 1]));
      __builtin_amdgcn_raw_buffer_store_b128(v4u{pk(o[0], o[1]), pk(o[2], o[3]), pk(o[4], o[5]), pk(o[6], o[7])}, ro, (t0 + i) * 4096 + (1024 + de) * 2, 0, OUT_AUX2);
    }
  }
  if (c.budget == 0u) __hip_atomic_store(c.ctrl + C_TMO / 4, 1u, RLX_AGENT);
}

extern "C" __global__ void __launch_bounds__(256, 1) mk_mega(Params p) {
  extern __shared__ __attribute__((aligned(16))) unsigned char lds_raw[];
  Ctx c;
  c.ctrl = (gu32*)p.ctrl;
  c.rs = mkrs(p.data, R_END);
  c.L = (lds_t)lds_raw;
  c.w = __builtin_amdgcn_readfirstlane(threadIdx.x >> 6);
  c.lane = threadIdx.x & 63;
  c.t0 = (unsigned)__builtin_amdgcn_s_memrealtime();
  c.b = (int)(blockIdx.x & 255u);
  c.att = (int)(blockIdx.x >> 8);
  c.ask = 0;
  c.slot = (c.att ? (c.b & 7) : (int)((unsigned)__builtin_amdgcn_s_getreg(20 | (3 << 11)) & 7u)) * 32 + (c.b >> 3);
  const int x = c.slot >> 5, t = c.slot & 31;
  if (c.w == 1) {
    asm volatile("s_getpc_b64 s[60:61]\n1:\n\ts_sub_u32 s60, s60, 1b-mk_mega\n\ts_subb_u32 s61, s61, 0\n\ts_mov_b32 s62, mk_tail-mk_mega\n\ts_mov_b32 s63, 0x20000\n\t"
                 "buffer_load_dword v255, %0, s[60:63], 0 offen\n\ts_waitcnt vmcnt(0)" :: "v"((c.lane * 32 + t) * 128) : "s60", "s61", "s62", "s63", "v255", "scc", "memory");
  }
  if (c.b == SERVER && c.w == 3) serve(c);
  if (x < 4) {
    if (t < 8) qk_role(p, c, x * 8 + t);
    else if (t < 16) vo_role(p, c, x * 8 + t - 8);
    else y_role(p, c, x * 16 + t - 16);
  } else {
    if (t < 24) g_role(p, c, t >> 3, (x - 4) * 8 + (t & 7), t);
    else zc_role(p, c, (x - 4) * 8 + t - 24);
    STAMP(2);
    c.budget = SPIN_CAP;
    conv_role(p, c, (x - 4) * 32 + t, vote_a(wait_verdict(c)) ^ 1);
  }
  STAMP(3);
}

}

extern "C" void kernel_launch(void* const* d_in, const int* in_sizes, int n_in, void* d_out, int out_size, void* d_ws, size_t ws_size,
                              hipStream_t stream) {
  using namespace mk;
  (void)in_sizes; (void)n_in; (void)out_size;
  Params p;
  p.x = (const u16*)d_in[0]; p.mem0 = (const u16*)d_in[1];
  p.Wq = (const u16*)d_in[2]; p.Wk = (const u16*)d_in[3]; p.Wv = (const u16*)d_in[4]; p.Wo = (const u16*)d_in[5];
  p.bq = (const u16*)d_in[7]; p.bk = (const u16*)d_in[8]; p.bv = (const u16*)d_in[9];
  p.shift = (const u16*)d_in[10]; p.ssmk = (const u16*)d_in[11]; p.ssmD = (const u16*)d_in[12];
  p.out = (u16*)d_out;
  p.ctrl = (unsigned char*)d_ws;
  p.data = data_base(d_ws, ws_size);
  (void)hipFuncSetAttribute((const void*)mk_mega, hipFuncAttributeMaxDynamicSharedMemorySize, LDS_BYTES);
  hipLaunchKernelGGL(mk_mega, dim3(256), dim3(256), LDS_BYTES, stream, p);
}
```

```cpp
#include <hip/hip_runtime.h>
#include <hip/hip_bf16.h>

namespace mk {

#define GAS __attribute__((address_space(1)))
#define LAS __attribute__((address_space(3)))
#define DEV __device__ __forceinline__
#define RLX_AGENT __ATOMIC_RELAXED, __HIP_MEMORY_SCOPE_AGENT
typedef unsigned short u16;
typedef unsigned v2u __attribute__((ext_vector_type(2)));
typedef unsigned v4u __attribute__((ext_vector_type(4)));
typedef unsigned v8u __attribute__((ext_vector_type(8)));
typedef unsigned v16u __attribute__((ext_vector_type(16)));
typedef unsigned v32u __attribute__((ext_vector_type(32)));
typedef float f32x2 __attribute__((ext_vector_type(2)));
typedef float f32x4 __attribute__((ext_vector_type(4)));
typedef float f32x16 __attribute__((ext_vector_type(16)));
typedef __bf16 bf16x2 __attribute__((ext_vector_type(2)));
typedef __bf16 bf16x8 __attribute__((ext_vector_type(8)));
typedef __bf16 bf16x16 __attribute__((ext_vector_type(16)));
typedef GAS unsigned gu32;
typedef __amdgpu_buffer_rsrc_t rsrc_t;
typedef LAS unsigned char* lds_t;

#define XCNT(r) ""
#define UCNT ""
#define CL_T
#define XT(a, b) ""
#define LMARK ""
#define GSUM ""
#define ZSUM ""
#define GTRACE
#define MIXED 1
#define NOFLAG 0
#define TOK_KICK 1
#define ZRING 16
#define Z_AT 16
#define Z_GAP 4
#define STR_(x) #x
#define STR(x) STR_(x)
#define GQ_AT 500
#define GK_AT 300
#define PULL_AT 800
#define QK_AT 0
#define SERVER 194
#define SLOW_LEAD 100
#define PULL_DEPTH 1
#define PRIME 2
#define X_AT 400
#define VO_AT 200
#define ZC_AT 0
#define CONV_AUX 2
#define W_AUX 0
#define ZC_PRIME 1
#define CODE_PULL 1
#define PROBES 2
#define Z_WIN 20
#define TOK_WAIT 0
#define Z_AUX 0
#define OUT_AUX 2
#define OUT_AUX2 2
#define URING 8
#define YH_AT 0
#define G_DEEP 1
#define AGAIN 0
#define PRE 0
#define YH 1
#define HELP_O 0
#define HELP_V 1
#define SLOW_V 1
#define K_FIRST 1
#define FLIP 1
#define YH_STEP 300
constexpr int NSEG = 64;
constexpr unsigned SENT = 0xFFFFFFFFu;
constexpr int AUX_SC1 = 16, AUX_NT = 2;
constexpr int T_LAG = 61;
constexpr int G_AHEAD = 4;
constexpr unsigned SPIN_CAP = 1u << 21;
constexpr int LDS_BYTES = 163840;

constexpr int C_ECHO = 0, C_CLAIM = 1024, C_VERDICT = 3072, C_TMO = 3328, C_CONV = 3456, C_VOTE = 3840, C_FLAG = 4096, C_GRP = C_FLAG + 256 * 128, C_ASK = C_GRP + 32 * 128;
constexpr int F_Q = 0, F_K = 32, F_V = 64, F_S = 96, F_Z = 128;
constexpr int CTRL_BYTES = C_ASK + 256 * 64;
constexpr int MB = 1 << 20;
constexpr int R_U = 136 * MB;
constexpr int R_MEM = 2 * MB, MEM_SLOT = 32768;
constexpr int R_P = R_MEM + 4 * MEM_SLOT, P_SLOT = 65536;
constexpr int R_PT = 4 * MB, PT_SLOT = 131072;
constexpr int R_QX = 8 * MB, R_KX = 40 * MB, R_Z = 72 * MB, Z_SLOT = 4 * MB, R_KVT = R_KX, R_YT = R_KX + 8 * MB, R_END = (136 + URING) * MB;

constexpr size_t R_DIAG = 0;
#define TOP 1
static inline unsigned char* data_base(void* ws, size_t size) {
  return (unsigned char*)((TOP ? (size_t)ws + size - R_END - R_DIAG : (size_t)ws + CTRL_BYTES + 2 * MB - 1) & ~(size_t)(2 * MB - 1));
}
struct Params {
  const u16 *x, *mem0, *Wq, *Wk, *Wv, *Wo, *bq, *bk, *bv, *shift, *ssmk, *ssmD;
  u16* out;
  unsigned char* ctrl;
  unsigned char* data;
};

DEV rsrc_t mkrs(const void* p, unsigned bytes) { return __builtin_amdgcn_make_buffer_rsrc((void*)p, 0, bytes, 0x00020000); }
DEV v4u ld16s(rsrc_t rs, int v, int s) { return __builtin_amdgcn_raw_buffer_load_b128(rs, v, s, AUX_SC1); }
DEV v2u ld8s(rsrc_t rs, int v, int s) { return __builtin_amdgcn_raw_buffer_load_b64(rs, v, s, AUX_SC1); }
DEV v4u ld16(rsrc_t rs, int v, int s) { return __builtin_amdgcn_raw_buffer_load_b128(rs, v, s, 0); }
DEV v4u ld16z(rsrc_t rs, int v, int s) { return __builtin_amdgcn_raw_buffer_load_b128(rs, v, s, AUX_SC1 | Z_AUX); }
DEV v4u ld16c(rsrc_t rs, int v, int s) { return __builtin_amdgcn_raw_buffer_load_b128(rs, v, s, CONV_AUX); }
template <int A> DEV v4u ld16w(rsrc_t rs, int v, int s) { return __builtin_amdgcn_raw_buffer_load_b128(rs, v, s, A); }
DEV v2u ld8(rsrc_t rs, int v, int s) { return __builtin_amdgcn_raw_buffer_load_b64(rs, v, s, 0); }
DEV void dma16(rsrc_t rs, lds_t l, int v) {
#if __HIP_DEVICE_COMPILE__
  __builtin_amdgcn_raw_ptr_buffer_load_lds(rs, (LAS void*)l, 16, v, 0, 0, 0);
#endif
}
DEV void st16s(rsrc_t rs, int v, int s, v4u d) { __builtin_amdgcn_raw_buffer_store_b128(d, rs, v, s, AUX_SC1); }
DEV void st8s(rsrc_t rs, int v, int s, v2u d) { __builtin_amdgcn_raw_buffer_store_b64(d, rs, v, s, AUX_SC1); }
DEV void st4s(rsrc_t rs, int v, int s, unsigned d) { __builtin_amdgcn_raw_buffer_store_b32(d, rs, v, s, AUX_SC1); }
DEV int amap(int o, int par) { return ((o >> 13) << 14) | (par << 13) | (o & 8191); }
DEV int xblk(int s, int h, int par) { return (s * 32 + h) * 16384 + ((par ^ (s >> 2)) & 1) * 8192; }
DEV unsigned umax(unsigned a, unsigned b) { return a > b ? a : b; }
DEV unsigned top(v4u v) { return umax(umax(v.x, v.y), umax(v.z, v.w)); }
DEV unsigned pk(float a, float b) { bf16x2 v = {(__bf16)a, (__bf16)b}; return __builtin_bit_cast(unsigned, v); }
DEV unsigned pks(float a, float b) { unsigned u = pk(a, b); return u < SENT - 1u ? u : SENT - 1u; }
DEV float blo(unsigned u) { return __builtin_bit_cast(float, u << 16); }
DEV float bhi(unsigned u) { return __builtin_bit_cast(float, u & 0xFFFF0000u); }
DEV float rb(float a) { return blo(pk(a, a)); }
DEV unsigned fu(float f) { return __builtin_bit_cast(unsigned, f); }
DEV float uf(unsigned u) { return __builtin_bit_cast(float, u); }
DEV f32x4 mfma16(v4u a, v4u b, f32x4 c) {
  return __builtin_amdgcn_mfma_f32_16x16x32_bf16(__builtin_bit_cast(bf16x8, a), __builtin_bit_cast(bf16x8, b), c, 0, 0, 0);
}
DEV f32x16 mfma32(v4u a, v4u b, f32x16 c) {
  return __builtin_amdgcn_mfma_f32_32x32x16_bf16(__builtin_bit_cast(bf16x8, a), __builtin_bit_cast(bf16x8, b), c, 0, 0, 0);
}
DEV f32x4 smf(v4u a, v8u b, f32x4 c, int idx) {
  return __builtin_amdgcn_smfmac_f32_16x16x64_bf16(__builtin_bit_cast(bf16x8, a), __builtin_bit_cast(bf16x16, b), c, idx, 0, 0);
}
DEV v8u cat(v4u a, v4u b) { return __builtin_shufflevector(a, b, 0, 1, 2, 3, 4, 5, 6, 7); }
DEV v4u lo4(v8u a) { return __builtin_shufflevector(a, a, 0, 1, 2, 3); }
DEV v4u hi4(v8u a) { return __builtin_shufflevector(a, a, 4, 5, 6, 7); }
DEV v4u lds16(lds_t L, int o) { return *(LAS v4u*)(L + o); }
DEV void sts16(lds_t L, int o, v4u v) { *(LAS v4u*)(L + o) = v; }
DEV void sts8(lds_t L, int o, v2u v) { *(LAS v2u*)(L + o) = v; }
#define SCHED() __builtin_amdgcn_sched_barrier(0)
#define PIN_A(x) asm volatile("" : "+a"(x))
#define PIN_V(x) asm volatile("" : "+v"(x))
DEV int opq(int v) { asm volatile("" : "+v"(v)); return v; }
DEV int fresh_lane() {
  int l; asm volatile("v_mbcnt_lo_u32_b32 %0, -1, 0\n\tv_mbcnt_hi_u32_b32 %0, -1, %0" : "=v"(l)); return l;
}
DEV f32x4 fresh_zero() {
  f32x2 a, b;
  asm volatile("v_mov_b64 %0, 0" : "=v"(a));
  asm volatile("v_mov_b64 %0, 0" : "=v"(b));
  return __builtin_shufflevector(a, b, 0, 1, 2, 3);
}
DEV unsigned fresh_sent() { unsigned z; asm volatile("v_mov_b32 %0, -1" : "=v"(z)); return z; }
#define SMF "v_smfmac_f32_16x16x64_bf16 "
#define MF16 "v_mfma_f32_16x16x32_bf16 "
#define SMF1(cls, d, a, b) asm volatile(SMF "%0, %1, %2, %3" : "+v"(d) : "v"(a), cls(b), "v"(idx))
#define SMF1F(cls, lo, hi, d, a, b) asm volatile(SMF "v[" #lo ":" #hi "], %1, %2, %3" : "+{v[" #lo ":" #hi "]}"(d) : "v"(a), cls(b), "v"(idx))
#define AT_HOME(M, j) do { const int j_ = (j); if (j_ == 0) M("{v[32:39]}"); if (j_ == 1) M("{v[40:47]}"); if (j_ == 2) M("{v[48:55]}"); if (j_ == 3) M("{v[56:63]}"); if (j_ == 4) M("{v[64:71]}"); if (j_ == 5) M("{v[72:79]}"); if (j_ == 6) M("{v[80:87]}"); if (j_ == 7) M("{v[88:95]}"); if (j_ == 8) M("{v[96:103]}"); if (j_ == 9) M("{v[104:111]}"); if (j_ == 10) M("{v[112:119]}"); if (j_ == 11) M("{v[120:127]}"); if (j_ == 12) M("{v[128:135]}"); if (j_ == 13) M("{v[136:143]}"); if (j_ == 14) M("{v[144:151]}"); if (j_ == 15) M("{v[152:159]}"); } while (0)
#define MF1(d, a, b) asm volatile(MF16 "%0, %1, %2, %0" : "+v"(d) : "v"(a), "v"(b))
#define MF1Z(d, a, b) asm volatile(MF16 "%0, %1, %2, 0" : "=&v"(d) : "v"(a), "v"(b))
#define LGKM(n) asm volatile("s_waitcnt lgkmcnt(" #n ")" ::: "memory")
#define RAWBAR() do { __builtin_amdgcn_s_barrier(); asm volatile("" ::: "memory"); } while (0)
DEV void settle3(f32x4& s0, f32x4& s1, f32x4& s2) { asm volatile("s_nop 7" : "+v"(s0), "+v"(s1), "+v"(s2)); }
DEV void settle4(f32x4 (&d)[4]) { asm volatile("s_nop 7" : "+v"(d[0]), "+v"(d[1]), "+v"(d[2]), "+v"(d[3])); }
#define BAR() do { asm volatile("s_waitcnt lgkmcnt(0)" ::: "memory"); __builtin_amdgcn_s_barrier(); asm volatile("" ::: "memory"); } while (0)
#define VMWAIT(n) asm volatile("s_waitcnt vmcnt(" #n ")" ::: "memory")
DEV float add1(float a, float b) {
  float o; asm("v_add_f32 %0, %1, %2" : "=v"(o) : "v"(a), "v"(b)); return o;
}
DEV float xmax(float v) {
  auto r = __builtin_amdgcn_permlane32_swap(fu(v), fu(v), false, false); v = fmaxf(uf(r[0]), uf(r[1]));
  auto q = __builtin_amdgcn_permlane16_swap(fu(v), fu(v), false, false); return fmaxf(uf(q[0]), uf(q[1]));
}
DEV float xsum(float v) {
  auto r = __builtin_amdgcn_permlane32_swap(fu(v), fu(v), false, false); v = uf(r[0]) + uf(r[1]);
  auto q = __builtin_amdgcn_permlane16_swap(fu(v), fu(v), false, false); return uf(q[0]) + uf(q[1]);
}

DEV float ror8(float v) { return uf(__builtin_amdgcn_update_dpp(0u, fu(v), 0x128, 0xf, 0xf, true)); }
DEV float fmax2(float a, float b) { return __builtin_amdgcn_fmed3f(a, b, __builtin_inff()); }
DEV float fmax3(float a, float b, float c) { float r; asm("v_max3_f32 %0, %1, %2, %3" : "=v"(r) : "v"(a), "v"(b), "v"(c)); return r; }
DEV bool allok(unsigned t) { return __all(t != SENT); }

template <int N> struct IC { static constexpr int value = N; };
template <int N, class F> DEV void sfor(F&& f) { if constexpr (N > 0) { sfor<N - 1>(f); f(IC<N - 1>{}); } }
struct Ctx {
  gu32* ctrl;
  rsrc_t rs;
  lds_t L;
  int w, lane;
  int b, slot, att;
  unsigned ask;
  unsigned t0;
  unsigned budget;
};
DEV bool spend(Ctx& c) {
  const unsigned old = c.budget;
  asm volatile("s_cmp_lg_u32 %0, 0\n\ts_subb_u32 %0, %0, 0" : "+s"(c.budget) :: "scc");
  return old == 0u;
}
DEV unsigned wait_flag(Ctx& c, int flag, unsigned need) {
  gu32* f = c.ctrl + (C_FLAG + flag * 128) / 4;
  unsigned v;
#pragma unroll 1
  for (;;) {
    v = __builtin_amdgcn_readfirstlane(__hip_atomic_load(f, RLX_AGENT));
    if (v >= need || spend(c)) break;
    __builtin_amdgcn_s_sleep(2);
  }
  __builtin_amdgcn_fence(__ATOMIC_ACQUIRE, "wavefront");
  return v;
}
DEV void post_flag(Ctx& c, int flag, unsigned v, int off = 0) {
  st4s(mkrs((const void*)c.ctrl, CTRL_BYTES), (c.w | off | fresh_lane()) ? 1 << 30 : 0, C_FLAG + flag * 128, v);
}
constexpr unsigned CL_A = 1u << 16, CL_VOTED = 1u << 24, CL_FILLED = 1u << 28;
DEV unsigned claim_look(Ctx& c) { return __hip_atomic_load(c.ctrl + C_ECHO / 4 + c.b, RLX_AGENT); }
DEV void ask(Ctx& c, unsigned first) {
  c.ask = __builtin_amdgcn_readfirstlane(first) + 1u;
  st4s(mkrs((const void*)c.ctrl, CTRL_BYTES), (c.w | c.att | fresh_lane()) ? 1 << 30 : 0, C_ASK + c.b * 64, c.ask);
}
DEV void claim(Ctx& c, unsigned votes) {
  if (c.w != 0) return;
  if (c.att == 0) {
#pragma unroll 1
    while (__builtin_amdgcn_readfirstlane(__hip_atomic_load(c.ctrl + C_ECHO / 4 + c.b, RLX_AGENT)) != c.ask) {
      if (spend(c)) break;
      __builtin_amdgcn_s_sleep(2);
    }
  }
  if (fresh_lane() == 0) __hip_atomic_fetch_add(c.ctrl + (C_CLAIM + c.att * 1024) / 4 + c.slot, 1u + votes, RLX_AGENT);
}
DEV bool claim_try(Ctx& c, unsigned look) {
  const bool ok = c.att || __builtin_amdgcn_readfirstlane(look) == c.ask;
  if (ok && c.w == 0 && fresh_lane() == 0) __hip_atomic_fetch_add(c.ctrl + (C_CLAIM + c.att * 1024) / 4 + c.slot, 1u, RLX_AGENT);
  return ok;
}
DEV unsigned wait_verdict(Ctx& c) {
  unsigned v;
#pragma unroll 1
  for (;;) {
    v = __builtin_amdgcn_readfirstlane(__hip_atomic_load(c.ctrl + (C_VERDICT + c.att * 128) / 4, RLX_AGENT));
    if ((v & 1u) || spend(c)) break;
    __builtin_amdgcn_s_sleep(2);
  }
  __builtin_amdgcn_fence(__ATOMIC_ACQUIRE, "wavefront");
  if ((v & 3u) == 3u && c.att == 0) {
    asm volatile("s_waitcnt vmcnt(0) lgkmcnt(0)\n\ts_barrier\n\ts_mov_b64 s[0:1], %0\n\ts_mov_b32 s2, %1\n\tv_mov_b32 v0, %2\n\t"
                 "s_getpc_b64 s[4:5]\n1:\n\ts_sub_u32 s4, s4, 1b-mk_mega\n\ts_subb_u32 s5, s5, 0\n\ts_setpc_b64 s[4:5]"
                 :: "s"(__builtin_amdgcn_kernarg_segment_ptr()), "s"(c.b + 256), "v"(c.w * 64 + fresh_lane()) : "s0", "s1", "s2", "s4", "s5", "v0", "scc", "memory");
    __builtin_unreachable();
  }
  return v;
}
DEV unsigned wait_vote(Ctx& c) {
  unsigned v;
#pragma unroll 1
  for (;;) {
    v = __builtin_amdgcn_readfirstlane(__hip_atomic_load(c.ctrl + C_VOTE / 4, RLX_AGENT));
    if ((v & 1u) || spend(c)) break;
    __builtin_amdgcn_s_sleep(2);
  }
  return v;
}
DEV void serve(Ctx& c) {
  c.budget = SPIN_CAP;
  const int lane = fresh_lane();
  const rsrc_t rc = mkrs((const void*)c.ctrl, CTRL_BYTES);
  if (c.att == 0) {
#pragma unroll
    for (int i = C_CLAIM / 1024; i < C_ASK / 1024; ++i) st16s(rc, lane * 16, i * 1024, v4u{0, 0, 0, 0});
    VMWAIT(0);
  }
  unsigned vote = c.att ? wait_vote(c) : 0u;
  bool echo = c.att == 0, first = true;
  v4u last = {0u, 0u, 0u, 0u};
#pragma unroll 1
  for (;;) {
    asm volatile("" ::: "memory");
    const v4u cl = ld16s(rc, lane * 16, C_CLAIM + c.att * 1024);
    if (echo) {
      v4u e;
#pragma unroll
      for (int i = 0; i < 4; ++i) e[i] = __builtin_amdgcn_raw_buffer_load_b32(rc, (lane * 4 + i) * 64, C_ASK, AUX_SC1);
      if (first || __any(e[0] != last[0] || e[1] != last[1] || e[2] != last[2] || e[3] != last[3])) st16s(rc, lane * 16, C_ECHO, e);
      last = e; first = false;
    }
    unsigned n = 0, one = 1;
    int va = 0, nv = 0, nf = 0;
#pragma unroll
    for (int i = 0; i < 4; ++i) {
      n += cl[i] & 0xFFFFu; one &= (cl[i] & 0xFFFFu) == 1u;
      va += __popcll(__ballot(cl[i] & CL_A)); nv += __popcll(__ballot(cl[i] & CL_VOTED)); nf += __popcll(__ballot(cl[i] & CL_FILLED));
    }
#pragma unroll
    for (int o = 1; o < 64; o <<= 1) n += (unsigned)__shfl_xor((int)n, o);
    const bool all = __builtin_amdgcn_readfirstlane(n) >= 256u, good = __all(one);
    echo = echo && !all;
    if (!vote && (all || max(va, nv - va) > 20)) {
      vote = 2 * va > nv ? 5u : 9u;
      if (lane == 0) __hip_atomic_store(c.ctrl + C_VOTE / 4, vote, RLX_AGENT);
    }
    if (all && (!good || nf >= 64)) {
      if (lane == 0) __hip_atomic_store(c.ctrl + (C_VERDICT + c.att * 128) / 4, vote | (good ? 0u : 2u), RLX_AGENT);
      break;
    }
    if (spend(c)) break;
  }
}
DEV int vote_a(unsigned v) { return (int)((v >> 2) & 1u); }
DEV int vote_b(unsigned v) { return (int)((v >> 3) & 1u); }

#define STAMP(slot) do {} while (0)
#define TICK_DECL
#define LOG(i, s, v) do {} while (0)
#define LOGS(i, s) do {} while (0)
#define CST(i) do {} while (0)
#define LITE(s) do {} while (0)
#define TICK(i) do {} while (0)
#define TICK_SUM(n) do {} while (0)
#define TICK_LDS(b) do {} while (0)
#define TICK_DUMP(b) do {} while (0)
#define TICK_OUT() do {} while (0)

DEV constexpr int krow(int rho8, int i) { return 32 * (rho8 >> 2) + 8 * (rho8 & 1) + 2 * ((rho8 >> 1) & 1) + i; }
DEV int krow_lane(int g) { return 16 * (g >> 1) + 4 * (g & 1); }
DEV unsigned prm(unsigned hi, unsigned lo, int odd) { return __builtin_amdgcn_perm(hi, lo, odd ? 0x07060302u : 0x05040100u); }

#define AT_ACC(M, n) do { const int n_ = (n); if (n_ == 0) M("{a[0:7]}"); if (n_ == 1) M("{a[8:15]}"); if (n_ == 2) M("{a[16:23]}"); if (n_ == 3) M("{a[24:31]}"); if (n_ == 4) M("{a[32:39]}"); if (n_ == 5) M("{a[40:47]}"); if (n_ == 6) M("{a[48:55]}"); if (n_ == 7) M("{a[56:63]}"); if (n_ == 8) M("{a[64:71]}"); if (n_ == 9) M("{a[72:79]}"); if (n_ == 10) M("{a[80:87]}"); if (n_ == 11) M("{a[88:95]}"); if (n_ == 12) M("{a[96:103]}"); if (n_ == 13) M("{a[104:111]}"); if (n_ == 14) M("{a[112:119]}"); if (n_ == 15) M("{a[120:127]}"); if (n_ == 16) M("{a[128:135]}"); if (n_ == 17) M("{a[136:143]}"); if (n_ == 18) M("{a[144:151]}"); if (n_ == 19) M("{a[152:159]}"); if (n_ == 20) M("{a[160:167]}"); if (n_ == 21) M("{a[168:175]}"); if (n_ == 22) M("{a[176:183]}"); if (n_ == 23) M("{a[184:191]}"); if (n_ == 24) M("{a[192:199]}"); if (n_ == 25) M("{a[200:207]}"); if (n_ == 26) M("{a[208:215]}"); if (n_ == 27) M("{a[216:223]}"); if (n_ == 28) M("{a[224:231]}"); if (n_ == 29) M("{a[232:239]}"); if (n_ == 30) M("{a[240:247]}"); if (n_ == 31) M("{a[248:255]}"); } while (0)
#define PIN_ACC0(x) asm volatile("" :: "{a[0:7]}"(x[0]), "{a[8:15]}"(x[1]), "{a[16:23]}"(x[2]), "{a[24:31]}"(x[3]), "{a[32:39]}"(x[4]), "{a[40:47]}"(x[5]), "{a[48:55]}"(x[6]), "{a[56:63]}"(x[7]), "{a[64:71]}"(x[8]), "{a[72:79]}"(x[9]), "{a[80:87]}"(x[10]), "{a[88:95]}"(x[11]), "{a[96:103]}"(x[12]), "{a[104:111]}"(x[13]), "{a[112:119]}"(x[14]), "{a[120:127]}"(x[15]))
#define PIN_ACC1(x) asm volatile("" :: "{a[128:135]}"(x[16]), "{a[136:143]}"(x[17]), "{a[144:151]}"(x[18]), "{a[152:159]}"(x[19]), "{a[160:167]}"(x[20]), "{a[168:175]}"(x[21]), "{a[176:183]}"(x[22]), "{a[184:191]}"(x[23]), "{a[192:199]}"(x[24]), "{a[200:207]}"(x[25]), "{a[208:215]}"(x[26]), "{a[216:223]}"(x[27]), "{a[224:231]}"(x[28]), "{a[232:239]}"(x[29]), "{a[240:247]}"(x[30]), "{a[248:255]}"(x[31]))
#define PIN_HOME(x) asm volatile("" :: "{v[32:39]}"(x[0]), "{v[40:47]}"(x[1]), "{v[48:55]}"(x[2]), "{v[56:63]}"(x[3]), "{v[64:71]}"(x[4]), "{v[72:79]}"(x[5]), "{v[80:87]}"(x[6]), "{v[88:95]}"(x[7]), "{v[96:103]}"(x[8]), "{v[104:111]}"(x[9]), "{v[112:119]}"(x[10]), "{v[120:127]}"(x[11]), "{v[128:135]}"(x[12]), "{v[136:143]}"(x[13]), "{v[144:151]}"(x[14]), "{v[152:159]}"(x[15]))
#define CL_32_100 "v32", "v33", "v34", "v35", "v36", "v37", "v38", "v39", "v40", "v41", "v42", "v43", "v44", "v45", "v46", "v47", "v48", "v49", "v50", "v51", "v52", "v53", "v54", "v55", "v56", "v57", "v58", "v59", "v60", "v61", "v62", "v63", "v64", "v65", "v66", "v67", "v68", "v69", "v70", "v71", "v72", "v73", "v74", "v75", "v76", "v77", "v78", "v79", "v80", "v81", "v82", "v83", "v84", "v85", "v86", "v87", "v88", "v89", "v90", "v91", "v92", "v93", "v94", "v95", "v96", "v97", "v98", "v99", "v100", "v101", "v102", "v103", "v104", "v105", "v106", "v107", "v108", "v109", "v110", "v111", "v112", "v113", "v114", "v115", "v116", "v117", "v118", "v119", "v120", "v121", "v122", "v123", "v124", "v125", "v126", "v127", "v128", "v129", "v130", "v131"
#define ACC_IN(x) "{a[0:31]}"(x[0]), "{a[32:63]}"(x[1]), "{a[64:95]}"(x[2]), "{a[96:127]}"(x[3]), "{a[128:159]}"(x[4]), "{a[160:191]}"(x[5]), "{a[192:223]}"(x[6]), "{a[224:255]}"(x[7])
#define CL_16_32 "v16", "v17", "v18", "v19", "v20", "v21", "v22", "v23", "v24", "v25", "v26", "v27", "v28", "v29", "v30", "v31"
#define CL_120_128 "v120", "v121", "v122", "v123", "v124", "v125", "v126", "v127", "v128", "v129", "v130", "v131", "v132", "v133", "v134", "v135", "v136", "v137", "v138", "v139"
#define CL_160_200 "v160", "v161", "v162", "v163", "v164", "v165", "v166", "v167", "v168", "v169", "v170", "v171", "v172", "v173", "v174", "v175", "v176", "v177", "v178", "v179", "v180", "v181", "v182", "v183", "v184", "v185", "v186", "v187", "v188", "v189", "v190", "v191", "v192", "v193", "v194", "v195", "v196", "v197", "v198", "v199"
#define CL_176_200 "v176", "v177", "v178", "v179", "v180", "v181", "v182", "v183", "v184", "v185", "v186", "v187", "v188", "v189", "v190", "v191", "v192", "v193", "v194", "v195", "v196", "v197", "v198", "v199"
#define CL_200_216 "v200", "v201", "v202", "v203", "v204", "v205", "v206", "v207", "v208", "v209", "v210", "v211", "v212", "v213", "v214", "v215"
#define CL_216_256 "v216", "v217", "v218", "v219", "v220", "v221", "v222", "v223", "v224", "v225", "v226", "v227", "v228", "v229", "v230", "v231", "v232", "v233", "v234", "v235", "v236", "v237", "v238", "v239", "v240", "v241", "v242", "v243", "v244", "v245", "v246", "v247", "v248", "v249", "v250", "v251", "v252", "v253", "v254", "v255"
#define CL_S "memory", "vcc", "scc", "s60", "s61", "s62", "s63", "s64", "s65", "s66", "s67", "s68" CL_T
#define TIMED_WAIT \
    "s_getpc_b64 s[62:63]\n\t" "1:\n\t" "s_memtime s[64:65]\n\t" "s_waitcnt lgkmcnt(0)\n\t" "s_sub_u32 s61, %0, s64\n\t" "s_cmp_gt_i32 s61, 124\n\t" "s_cbranch_scc1 1b\n\t" \
    "s_max_i32 s61, s61, 0\n\t" "s_and_b32 s61, s61, 124\n\t" "s_sub_u32 s61, 2f-1b, s61\n\t" "s_add_u32 s62, s62, s61\n\t" "s_addc_u32 s63, s63, 0\n\t" \
    "s_setpc_b64 s[62:63]\n\t" ".rept 31\n\t" "s_nop 0\n\t" ".endr\n\t" "2:\n\t"
#define QK_STEP0 \
    ".p2align 6\n\t" "v_xor_b32 v216, 0x400, %[mo]\n\t" "s_xor_b32 s60, %[sb], 0x800\n\t" "s_mov_b32 s66, 0x3e0293ee\n\t" "s_mov_b32 s67, 0x3e0293ee\n\t" XT(80, 81) \
    "s_add_u32 s61, %[kb], 0x1000\n\t" "buffer_load_dwordx4 v[16:19], %[kx], %[rs], %[kb] offen offset:0 sc1\n\t" \
    "buffer_load_dwordx4 v[20:23], %[kx], %[rs], %[kb] offen offset:2048 sc1\n\t" "buffer_load_dwordx4 v[24:27], %[kx], %[rs], s61 offen offset:0 sc1\n\t" \
    "buffer_load_dwordx4 v[28:31], %[kx], %[rs], s61 offen offset:2048 sc1\n\t" "buffer_load_dwordx4 v[200:203], %[kx], %[rs], %[kb] offen offset:256 sc1\n\t" \
    "buffer_load_dwordx4 v[204:207], %[kx], %[rs], %[kb] offen offset:2304 sc1\n\t" "buffer_load_dwordx4 v[208:211], %[kx], %[rs], s61 offen offset:256 sc1\n\t" \
    "buffer_load_dwordx4 v[212:215], %[kx], %[rs], s61 offen offset:2304 sc1\n\t" "s_barrier\n\t" "s_waitcnt vmcnt(7)\n\t" "ds_write_b128 %[kx], v[16:19] offset:0\n\t" \
    "s_waitcnt vmcnt(6)\n\t" "ds_write_b128 %[kx], v[20:23] offset:2048\n\t" "s_waitcnt vmcnt(5)\n\t" "ds_write_b128 %[kx], v[24:27] offset:4096\n\t" "s_waitcnt vmcnt(4)\n\t" \
    "ds_write_b128 %[kx], v[28:31] offset:6144\n\t" "s_waitcnt vmcnt(3)\n\t" "ds_write_b128 %[kx], v[200:203] offset:256\n\t" "s_waitcnt vmcnt(2)\n\t" \
    "ds_write_b128 %[kx], v[204:207] offset:2304\n\t" "s_waitcnt vmcnt(1)\n\t" "ds_write_b128 %[kx], v[208:211] offset:4352\n\t" "s_waitcnt vmcnt(0)\n\t" \
    "ds_write_b128 %[kx], v[212:215] offset:6400\n\t" "s_waitcnt lgkmcnt(0)\n\t" "v_mov_b64 v[208:209], 0\n\t" "v_mov_b64 v[210:211], 0\n\t" "v_mov_b64 v[212:213], 0\n\t" \
    "v_mov_b64 v[214:215], 0\n\t" XT(98, 99) "s_barrier\n\t" XT(82, 83) "buffer_load_dwordx4 v[240:243], %[mo], %[rs], %[sb] offen sc1\n\t" "v_mov_b64 v[224:225], 0\n\t" \
    "v_mov_b64 v[226:227], 0\n\t" "v_mov_b64 v[228:229], 0\n\t" "v_mov_b64 v[230:231], 0\n\t" "v_mov_b64 v[232:233], 0\n\t" "v_mov_b64 v[234:235], 0\n\t" \
    "v_mov_b64 v[236:237], 0\n\t" "v_mov_b64 v[238:239], 0\n\t" "buffer_load_dwordx4 v[244:247], v216, %[rs], %[sb] offen sc1\n\t" \
    "ds_read_b128 v[160:163], %[l0] offset:0\n\t" "ds_read_b128 v[164:167], %[l0] offset:1024\n\t" "ds_read_b128 v[168:171], %[l0] offset:2048\n\t" \
    "ds_read_b128 v[172:175], %[l0] offset:3072\n\t" "s_nop 3\n\t" "buffer_load_dwordx4 v[248:251], %[mo], %[rs], s60 offen sc1\n\t" \
    "ds_read_b128 v[176:179], %[l0] offset:4096\n\t" "ds_read_b128 v[180:183], %[l0] offset:5120\n\t" "ds_read_b128 v[184:187], %[l0] offset:6144\n\t" \
    "ds_read_b128 v[188:191], %[l0] offset:7168\n\t" "s_nop 3\n\t" "buffer_load_dwordx4 v[252:255], v216, %[rs], s60 offen sc1\n\t" "v_mov_b32 v219, 160512\n\t" \
    "v_mov_b64 v[220:221], -1\n\t" "v_mov_b64 v[222:223], -1\n\t" "v_mov_b32 v217, 0\n\t" "buffer_store_dwordx2 v[220:221], %[px], %[rs], %[pr] offen sc1\n\t" \
    "buffer_store_dword v220, %[pm], %[rs], %[pr] offen sc1\n\t" "global_load_dword v217, v217, %[fq] sc1\n\t" "10:\n\t" "s_waitcnt vmcnt(6)\n\t" XT(84, 85) \
    "v_smfmac_f32_16x16x64_bf16 v[224:227], v[240:243], a[0:7], %[ix]\n\t" "ds_write_b128 %[l1], v[240:243] offset:0\n\t" \
    "v_smfmac_f32_16x16x64_bf16 v[228:231], v[240:243], a[8:15], %[ix]\n\t" "v_smfmac_f32_16x16x64_bf16 v[232:235], v[240:243], v[32:39], %[ix]\n\t" \
    "s_waitcnt lgkmcnt(7)\n\t" "v_smfmac_f32_16x16x64_bf16 v[236:239], v[240:243], v[160:167], %[ix]\n\t" "ds_read_b128 v[160:163], %[l0] offset:8192\n\t" \
    "ds_read_b128 v[164:167], %[l0] offset:9216\n\t" "s_waitcnt vmcnt(5)\n\t" "v_smfmac_f32_16x16x64_bf16 v[224:227], v[244:247], a[16:23], %[ix]\n\t" \
    "ds_write_b128 %[l1], v[244:247] offset:1024\n\t" "v_smfmac_f32_16x16x64_bf16 v[228:231], v[244:247], a[24:31], %[ix]\n\t" \
    "v_smfmac_f32_16x16x64_bf16 v[232:235], v[244:247], v[40:47], %[ix]\n\t" "s_waitcnt lgkmcnt(8)\n\t" \
    "v_smfmac_f32_16x16x64_bf16 v[236:239], v[244:247], v[168:175], %[ix]\n\t" "ds_read_b128 v[168:171], %[l0] offset:10240\n\t" \
    "ds_read_b128 v[172:175], %[l0] offset:11264\n\t" "s_waitcnt vmcnt(4)\n\t" "v_smfmac_f32_16x16x64_bf16 v[224:227], v[248:251], a[32:39], %[ix]\n\t" \
    "ds_write_b128 %[l1], v[248:251] offset:2048\n\t" "v_smfmac_f32_16x16x64_bf16 v[228:231], v[248:251], a[40:47], %[ix]\n\t" "s_waitcnt lgkmcnt(3)\n\t" "s_barrier\n\t" \
    XT(86, 87) "v_smfmac_f32_16x16x64_bf16 v[232:235], v[248:251], v[48:55], %[ix]\n\t" "ds_read_b128 v[192:195], %[l1] offset:4096\n\t" \
    "ds_read_b128 v[196:199], %[l1] offset:8192\n\t" "v_smfmac_f32_16x16x64_bf16 v[236:239], v[248:251], v[176:183], %[ix]\n\t" \
    "ds_read_b128 v[176:179], %[l0] offset:12288\n\t" "ds_read_b128 v[180:183], %[l0] offset:13312\n\t" "s_waitcnt vmcnt(3)\n\t" \
    "v_smfmac_f32_16x16x64_bf16 v[224:227], v[252:255], a[48:55], %[ix]\n\t" "ds_write_b128 %[l1], v[252:255] offset:3072\n\t" \
    "v_smfmac_f32_16x16x64_bf16 v[228:231], v[252:255], a[56:63], %[ix]\n\t" "ds_read_b128 v[240:243], %[l1] offset:12288\n\t" \
    "ds_read_b128 v[244:247], %[l1] offset:5120\n\t" "v_smfmac_f32_16x16x64_bf16 v[232:235], v[252:255], v[56:63], %[ix]\n\t" "s_nop 1\n\t" \
    "v_smfmac_f32_16x16x64_bf16 v[236:239], v[252:255], v[184:191], %[ix]\n\t" "ds_read_b128 v[184:187], %[l0] offset:14336\n\t" "v_cmp_u_f32 vcc, v224, v225\n\t" \
    "v_cmp_u_f32 s[62:63], v226, v227\n\t" "s_waitcnt lgkmcnt(7)\n\t" "v_smfmac_f32_16x16x64_bf16 v[224:227], v[192:195], a[64:71], %[ix]\n\t" \
    "ds_read_b128 v[188:191], %[l0] offset:15360\n\t" "v_smfmac_f32_16x16x64_bf16 v[228:231], v[192:195], a[72:79], %[ix]\n\t" "s_or_b64 vcc, vcc, s[62:63]\n\t" \
    "s_cbranch_scc1 11f\n\t" "12:\n\t" "v_smfmac_f32_16x16x64_bf16 v[232:235], v[192:195], v[64:71], %[ix]\n\t" "s_waitcnt lgkmcnt(4)\n\t" "s_barrier\n\t" XT(88, 89) \
    "ds_read_b32 v218, v219\n\t" "v_smfmac_f32_16x16x64_bf16 v[236:239], v[192:195], v[160:167], %[ix]\n\t" "ds_read_b128 v[160:163], %[l0] offset:16384\n\t" \
    "ds_read_b128 v[164:167], %[l0] offset:17408\n\t" "v_smfmac_f32_16x16x64_bf16 v[224:227], v[196:199], a[80:87], %[ix]\n\t" \
    "ds_read_b128 v[248:251], %[l1] offset:9216\n\t" "ds_read_b128 v[252:255], %[l1] offset:13312\n\t" \
    "v_smfmac_f32_16x16x64_bf16 v[228:231], v[196:199], a[88:95], %[ix]\n\t" "ds_read_b128 v[192:195], %[l1] offset:6144\n\t" \
    "v_smfmac_f32_16x16x64_bf16 v[232:235], v[196:199], v[72:79], %[ix]\n\t" "v_smfmac_f32_16x16x64_bf16 v[236:239], v[196:199], v[168:175], %[ix]\n\t" \
    "s_waitcnt lgkmcnt(5)\n\t" "v_cmp_eq_u32 vcc, %[rt], v218\n\t" "v_smfmac_f32_16x16x64_bf16 v[224:227], v[240:243], a[96:103], %[ix]\n\t" "s_cbranch_vccnz 13f\n\t" \
    "ds_read_b128 v[168:171], %[l0] offset:18432\n\t" "v_smfmac_f32_16x16x64_bf16 v[228:231], v[240:243], a[104:111], %[ix]\n\t" \
    "ds_read_b128 v[172:175], %[l0] offset:19456\n\t" "ds_read_b128 v[196:199], %[l1] offset:10240\n\t" \
    "v_smfmac_f32_16x16x64_bf16 v[232:235], v[240:243], v[80:87], %[ix]\n\t" "v_smfmac_f32_16x16x64_bf16 v[236:239], v[240:243], v[176:183], %[ix]\n\t" \
    "ds_read_b128 v[176:179], %[l0] offset:20480\n\t" "ds_read_b128 v[180:183], %[l0] offset:21504\n\t" \
    "v_smfmac_f32_16x16x64_bf16 v[224:227], v[244:247], a[112:119], %[ix]\n\t" "ds_read_b128 v[240:243], %[l1] offset:14336\n\t" \
    "v_smfmac_f32_16x16x64_bf16 v[228:231], v[244:247], a[120:127], %[ix]\n\t" "buffer_load_dword %[pd], %[pa], %[rs], %[kn] offen sc1\n\t" \
    "v_smfmac_f32_16x16x64_bf16 v[232:235], v[244:247], v[88:95], %[ix]\n\t" "v_smfmac_f32_16x16x64_bf16 v[236:239], v[244:247], v[184:191], %[ix]\n\t" \
    "ds_read_b128 v[184:187], %[l0] offset:22528\n\t" "ds_read_b128 v[188:191], %[l0] offset:23552\n\t" "s_waitcnt lgkmcnt(10)\n\t" \
    "v_smfmac_f32_16x16x64_bf16 v[224:227], v[248:251], a[128:135], %[ix]\n\t" "ds_read_b128 v[244:247], %[l1] offset:7168\n\t" \
    "v_smfmac_f32_16x16x64_bf16 v[228:231], v[248:251], a[136:143], %[ix]\n\t" "v_smfmac_f32_16x16x64_bf16 v[232:235], v[248:251], v[96:103], %[ix]\n\t" \
    "v_smfmac_f32_16x16x64_bf16 v[236:239], v[248:251], v[160:167], %[ix]\n\t" "ds_read_b128 v[160:163], %[l0] offset:24576\n\t" \
    "ds_read_b128 v[164:167], %[l0] offset:25600\n\t" "s_waitcnt lgkmcnt(12)\n\t" "v_smfmac_f32_16x16x64_bf16 v[224:227], v[252:255], a[144:151], %[ix]\n\t" \
    "ds_read_b128 v[248:251], %[l1] offset:11264\n\t" "v_smfmac_f32_16x16x64_bf16 v[228:231], v[252:255], a[152:159], %[ix]\n\t" \
    "v_smfmac_f32_16x16x64_bf16 v[232:235], v[252:255], v[104:111], %[ix]\n\t" "s_waitcnt lgkmcnt(10)\n\t" \
    "v_smfmac_f32_16x16x64_bf16 v[236:239], v[252:255], v[168:175], %[ix]\n\t" "ds_read_b128 v[168:171], %[l0] offset:26624\n\t" \
    "ds_read_b128 v[172:175], %[l0] offset:27648\n\t" "v_smfmac_f32_16x16x64_bf16 v[224:227], v[192:195], a[160:167], %[ix]\n\t" \
    "ds_read_b128 v[252:255], %[l1] offset:15360\n\t" "v_smfmac_f32_16x16x64_bf16 v[228:231], v[192:195], a[168:175], %[ix]\n\t" "s_waitcnt lgkmcnt(12)\n\t" \
    "v_smfmac_f32_16x16x64_bf16 v[224:227], v[196:199], a[176:183], %[ix]\n\t" "v_smfmac_f32_16x16x64_bf16 v[228:231], v[196:199], a[184:191], %[ix]\n\t" \
    "s_waitcnt lgkmcnt(9)\n\t" "v_smfmac_f32_16x16x64_bf16 v[224:227], v[240:243], a[192:199], %[ix]\n\t" \
    "v_smfmac_f32_16x16x64_bf16 v[228:231], v[240:243], a[200:207], %[ix]\n\t" "s_waitcnt vmcnt(1)\n\t" "v_readfirstlane_b32 s62, v217\n\t" "s_waitcnt lgkmcnt(6)\n\t" \
    "v_smfmac_f32_16x16x64_bf16 v[224:227], v[244:247], a[208:215], %[ix]\n\t" "v_smfmac_f32_16x16x64_bf16 v[228:231], v[244:247], a[216:223], %[ix]\n\t" \
    "s_max_u32 %[sq], %[sq], s62\n\t" "s_waitcnt lgkmcnt(3)\n\t" "v_smfmac_f32_16x16x64_bf16 v[224:227], v[248:251], a[224:231], %[ix]\n\t" \
    "v_smfmac_f32_16x16x64_bf16 v[228:231], v[248:251], a[232:239], %[ix]\n\t" "s_waitcnt lgkmcnt(0)\n\t" \
    "v_smfmac_f32_16x16x64_bf16 v[224:227], v[252:255], a[240:247], %[ix]\n\t" "s_nop 1\n\t" "v_smfmac_f32_16x16x64_bf16 v[228:231], v[252:255], a[248:255], %[ix]\n\t" \
    XT(90, 91) "s_nop 1\n\t" "v_smfmac_f32_16x16x64_bf16 v[232:235], v[192:195], v[112:119], %[ix]\n\t" "s_nop 1\n\t" \
    "v_smfmac_f32_16x16x64_bf16 v[236:239], v[192:195], v[176:183], %[ix]\n\t" "ds_read_b128 v[176:179], %[l0] offset:28672\n\t" "v_add_f32 v224, v224, v225\n\t" \
    "v_add_f32 v226, v226, v227\n\t" "v_smfmac_f32_16x16x64_bf16 v[232:235], v[196:199], v[120:127], %[ix]\n\t" "ds_read_b128 v[180:183], %[l0] offset:29696\n\t" \
    "v_add_f32 v228, v228, v229\n\t" "v_add_f32 v230, v230, v231\n\t" "v_smfmac_f32_16x16x64_bf16 v[236:239], v[196:199], v[184:191], %[ix]\n\t" \
    "ds_read_b128 v[184:187], %[l0] offset:30720\n\t" "v_cvt_pk_bf16_f32 v218, v224, v228\n\t" "v_cvt_pk_bf16_f32 v219, v226, v230\n\t" \
    "v_smfmac_f32_16x16x64_bf16 v[232:235], v[240:243], v[128:135], %[ix]\n\t" "ds_write2_b32 %[qw], v218, v219 offset1:8\n\t" \
    "ds_read_b128 v[188:191], %[l0] offset:31744\n\t" "v_smfmac_f32_16x16x64_bf16 v[236:239], v[240:243], v[160:167], %[ix]\n\t" \
    "ds_read_b128 v[192:195], %[kx] offset:4352\n\t" "ds_read_b128 v[196:199], %[kx] offset:6400\n\t" \
    "v_smfmac_f32_16x16x64_bf16 v[232:235], v[244:247], v[136:143], %[ix]\n\t" "v_smfmac_f32_16x16x64_bf16 v[236:239], v[244:247], v[168:175], %[ix]\n\t" \
    "s_waitcnt lgkmcnt(3)\n\t" "s_barrier\n\t" "v_smfmac_f32_16x16x64_bf16 v[232:235], v[248:251], v[144:151], %[ix]\n\t" "ds_read_b128 v[216:219], %[l1] offset:24576\n\t" \
    "ds_read_b128 v[220:223], %[l1] offset:25600\n\t" "v_smfmac_f32_16x16x64_bf16 v[236:239], v[248:251], v[176:183], %[ix]\n\t" \
    "v_smfmac_f32_16x16x64_bf16 v[232:235], v[252:255], v[152:159], %[ix]\n\t" "s_waitcnt lgkmcnt(4)\n\t" \
    "v_smfmac_f32_16x16x64_bf16 v[236:239], v[252:255], v[184:191], %[ix]\n\t" XT(92, 93) "s_waitcnt lgkmcnt(1)\n\t" \
    "v_smfmac_f32_16x16x64_bf16 v[208:211], v[216:219], v[16:23], %[ix]\n\t" "s_nop 1\n\t" "v_smfmac_f32_16x16x64_bf16 v[212:215], v[216:219], v[200:207], %[ix]\n\t" \
    "s_waitcnt lgkmcnt(0)\n\t" "s_nop 0\n\t" "v_add_f32 v232, v232, v233\n\t" "v_add_f32 v234, v234, v235\n\t" "v_add_f32 v236, v236, v237\n\t" \
    "v_add_f32 v238, v238, v239\n\t" "v_cvt_pk_bf16_f32 v232, v232, v236\n\t" "v_cvt_pk_bf16_f32 v233, v234, v238\n\t" "ds_write2_b32 %[kw], v232, v233 offset1:68\n\t" \
    "v_smfmac_f32_16x16x64_bf16 v[208:211], v[220:223], v[24:31], %[ix]\n\t" "v_smfmac_f32_16x16x64_bf16 v[212:215], v[220:223], v[192:199], %[ix]\n\t" \
    "v_mov_b64 v[224:225], 0\n\t" "v_mov_b64 v[226:227], 0\n\t" "s_waitcnt lgkmcnt(0)\n\t" "s_barrier\n\t" XT(94, 95) "ds_read_b128 v[160:163], %[km] offset:0\n\t" \
    "ds_read_b128 v[164:167], %[km] offset:64\n\t" "ds_read_b128 v[168:171], %[km] offset:128\n\t" "ds_read_b128 v[172:175], %[km] offset:192\n\t" \
    "v_add_f32 v178, v208, v209\n\t" "v_add_f32 v179, v210, v211\n\t" "v_add_f32 v180, v212, v213\n\t" "v_add_f32 v181, v214, v215\n\t" \
    "v_pk_mul_f32 v[178:179], v[178:179], s[66:67]\n\t" "v_pk_mul_f32 v[180:181], v[180:181], s[66:67]\n\t" "v_max_f32 v182, v178, v180\n\t" "v_max_f32 v183, v179, v181\n\t" \
    "s_nop 0\n\t" "v_max_f32_dpp v182, v182, v182 row_ror:8 row_mask:0xf bank_mask:0xf\n\t" "v_max_f32_dpp v183, v183, v183 row_ror:8 row_mask:0xf bank_mask:0xf\n\t" \
    "s_nop 0\n\t" "s_waitcnt lgkmcnt(2)\n\t" "v_smfmac_f32_16x16x64_bf16 v[224:227], v[216:219], v[160:167], %[ix]\n\t" "s_waitcnt lgkmcnt(0)\n\t" \
    "v_smfmac_f32_16x16x64_bf16 v[224:227], v[220:223], v[168:175], %[ix]\n\t" "s_nop 7\n\t" "v_add_f32 v176, v224, v225\n\t" "v_add_f32 v177, v226, v227\n\t" \
    "v_mul_f32 v176, s66, v176\n\t" "v_mul_f32 v177, s66, v177\n\t" "v_max_f32 v182, v176, v182\n\t" "v_max_f32 v183, v177, v183\n\t" "s_nop 0\n\t" \
    "v_max_f32_dpp v182, v182, v182 row_ror:4 row_mask:0xf bank_mask:0xf\n\t" "v_max_f32_dpp v183, v183, v183 row_ror:4 row_mask:0xf bank_mask:0xf\n\t" "s_nop 0\n\t" \
    "v_max_f32_dpp v182, v182, v182 row_ror:2 row_mask:0xf bank_mask:0xf\n\t" "v_max_f32_dpp v183, v183, v183 row_ror:2 row_mask:0xf bank_mask:0xf\n\t" "s_nop 0\n\t" \
    "v_max_f32_dpp v182, v182, v182 row_ror:1 row_mask:0xf bank_mask:0xf\n\t" "v_max_f32_dpp v183, v183, v183 row_ror:1 row_mask:0xf bank_mask:0xf\n\t" \
    "v_pk_add_f32 v[178:179], v[178:179], v[182:183] neg_lo:[0,1] neg_hi:[0,1]\n\t" "v_pk_add_f32 v[180:181], v[180:181], v[182:183] neg_lo:[0,1] neg_hi:[0,1]\n\t" \
    "v_pk_add_f32 v[176:177], v[176:177], v[182:183] neg_lo:[0,1] neg_hi:[0,1]\n\t" "v_exp_f32 v178, v178\n\t" "v_exp_f32 v179, v179\n\t" "v_exp_f32 v180, v180\n\t" \
    "v_exp_f32 v181, v181\n\t" "v_exp_f32 v176, v176\n\t" "v_pk_add_f32 v[182:183], v[178:179], v[180:181]\n\t" "v_exp_f32 v177, v177\n\t" "s_nop 0\n\t" \
    "v_add_f32_dpp v182, v182, v182 row_ror:8 row_mask:0xf bank_mask:0xf\n\t" "v_add_f32_dpp v183, v183, v183 row_ror:8 row_mask:0xf bank_mask:0xf\n\t" \
    "v_add_f32 v182, v182, v176\n\t" "v_add_f32 v183, v183, v177\n\t" "s_nop 0\n\t" "v_add_f32_dpp v182, v182, v182 row_ror:4 row_mask:0xf bank_mask:0xf\n\t" \
    "v_add_f32_dpp v183, v183, v183 row_ror:4 row_mask:0xf bank_mask:0xf\n\t" "s_nop 0\n\t" "v_add_f32_dpp v182, v182, v182 row_ror:2 row_mask:0xf bank_mask:0xf\n\t" \
    "v_add_f32_dpp v183, v183, v183 row_ror:2 row_mask:0xf bank_mask:0xf\n\t" "s_nop 0\n\t" "v_add_f32_dpp v182, v182, v182 row_ror:1 row_mask:0xf bank_mask:0xf\n\t" \
    "v_add_f32_dpp v183, v183, v183 row_ror:1 row_mask:0xf bank_mask:0xf\n\t" "v_rcp_f32 v182, v182\n\t" "v_rcp_f32 v183, v183\n\t" "s_nop 0\n\t" \
    "v_pk_mul_f32 v[176:177], v[176:177], v[182:183]\n\t" "v_pk_mul_f32 v[178:179], v[178:179], v[182:183]\n\t" "v_pk_mul_f32 v[180:181], v[180:181], v[182:183]\n\t" \
    "v_cvt_pk_bf16_f32 v176, v176, v177\n\t" "v_cvt_pk_bf16_f32 v178, v178, v179\n\t" "v_cvt_pk_bf16_f32 v180, v180, v181\n\t" \
    "v_mov_b32_dpp v177, v176 quad_perm:[1,0,3,2] row_mask:0xf bank_mask:0xf\n\t" "v_mov_b32_dpp v179, v178 quad_perm:[1,0,3,2] row_mask:0xf bank_mask:0xf\n\t" \
    "v_mov_b32_dpp v181, v180 quad_perm:[1,0,3,2] row_mask:0xf bank_mask:0xf\n\t" "v_perm_b32 v184, v179, v178, %[ps]\n\t" "v_perm_b32 v185, v181, v180, %[ps]\n\t" \
    "buffer_store_dwordx2 v[184:185], %[px], %[rs], %[pb] offen sc1\n\t" "v_perm_b32 v186, v177, v176, %[ps]\n\t" "buffer_store_dword v186, %[pm], %[rs], %[pb] offen sc1\n\t" \
    XT(96, 97) LMARK "s_waitcnt vmcnt(2)\n\t" "s_branch 99f\n\t" "11:\n\t" "ds_read_b128 v[220:223], %[l1] offset:0\n\t" "s_waitcnt lgkmcnt(0)\n\t" \
    "v_max3_u32 v218, v220, v221, v222\n\t" "v_max_u32 v218, v218, v223\n\t" "ds_read_b128 v[220:223], %[l1] offset:1024\n\t" "s_waitcnt lgkmcnt(0)\n\t" \
    "v_max3_u32 v218, v218, v220, v221\n\t" "v_max3_u32 v218, v218, v222, v223\n\t" "ds_read_b128 v[220:223], %[l1] offset:2048\n\t" "s_waitcnt lgkmcnt(0)\n\t" \
    "v_max3_u32 v218, v218, v220, v221\n\t" "v_max3_u32 v218, v218, v222, v223\n\t" "ds_read_b128 v[220:223], %[l1] offset:3072\n\t" "s_waitcnt lgkmcnt(0)\n\t" \
    "v_max3_u32 v218, v218, v220, v221\n\t" "v_max3_u32 v218, v218, v222, v223\n\t" "v_cmp_eq_u32 vcc, -1, v218\n\t" "s_cbranch_vccz 12b\n\t" "s_cmp_eq_u32 %[rt], 1\n\t" \
    "s_cbranch_scc1 12b\n\t" "v_mov_b32 v218, %[rt]\n\t" "ds_write_b32 v219, v218\n\t" "s_waitcnt lgkmcnt(0)\n\t" "s_branch 12b\n\t" "13:\n\t" "s_sub_u32 %[rt], %[rt], 1\n\t" \
    "s_waitcnt lgkmcnt(0)\n\t" XCNT(100) "14:\n\t" "buffer_load_dwordx4 v[240:243], %[mo], %[rs], %[sb] offen sc1\n\t" \
    "buffer_load_dwordx4 v[244:247], v216, %[rs], %[sb] offen sc1\n\t" "buffer_load_dwordx4 v[248:251], %[mo], %[rs], s60 offen sc1\n\t" \
    "buffer_load_dwordx4 v[252:255], v216, %[rs], s60 offen sc1\n\t" "s_waitcnt vmcnt(0)\n\t" "v_max_u32 v218, v240, v241\n\t" "v_max3_u32 v218, v218, v242, v243\n\t" \
    "v_max3_u32 v218, v218, v244, v245\n\t" "v_max3_u32 v218, v218, v246, v247\n\t" "v_max3_u32 v218, v218, v248, v249\n\t" "v_max3_u32 v218, v218, v250, v251\n\t" \
    "v_max3_u32 v218, v218, v252, v253\n\t" "v_max3_u32 v218, v218, v254, v255\n\t" "v_cmp_eq_u32 vcc, -1, v218\n\t" "s_cbranch_vccz 15f\n\t" "s_cmp_eq_u32 %[bd], 0\n\t" \
    "s_cbranch_scc1 15f\n\t" "s_sub_u32 %[bd], %[bd], 1\n\t" XCNT(101) "s_branch 14b\n\t" "15:\n\t" "ds_read_b128 v[160:163], %[l0] offset:0\n\t" \
    "ds_read_b128 v[164:167], %[l0] offset:1024\n\t" "ds_read_b128 v[168:171], %[l0] offset:2048\n\t" "ds_read_b128 v[172:175], %[l0] offset:3072\n\t" \
    "ds_read_b128 v[176:179], %[l0] offset:4096\n\t" "ds_read_b128 v[180:183], %[l0] offset:5120\n\t" "ds_read_b128 v[184:187], %[l0] offset:6144\n\t" \
    "ds_read_b128 v[188:191], %[l0] offset:7168\n\t" "v_mov_b64 v[224:225], 0\n\t" "v_mov_b64 v[226:227], 0\n\t" "v_mov_b64 v[228:229], 0\n\t" "v_mov_b64 v[230:231], 0\n\t" \
    "v_mov_b64 v[232:233], 0\n\t" "v_mov_b64 v[234:235], 0\n\t" "v_mov_b64 v[236:237], 0\n\t" "v_mov_b64 v[238:239], 0\n\t" "s_barrier\n\t" "s_branch 10b\n\t" "99:\n\t"
#define QK_STEP1 \
    ".p2align 6\n\t" "v_xor_b32 v216, 0x400, %[mo]\n\t" "s_xor_b32 s60, %[sb], 0x800\n\t" "s_add_u32 s61, %[qb], 0x1000\n\t" "s_mov_b32 s66, 0x3e0293ee\n\t" \
    "s_mov_b32 s67, 0x3e0293ee\n\t" XT(80, 81) XT(98, 99) "s_barrier\n\t" XT(82, 83) "buffer_load_dwordx4 v[240:243], %[mo], %[rs], %[sb] offen sc1\n\t" \
    "v_mov_b64 v[224:225], 0\n\t" "v_mov_b64 v[226:227], 0\n\t" "v_mov_b64 v[228:229], 0\n\t" "v_mov_b64 v[230:231], 0\n\t" "v_mov_b64 v[232:233], 0\n\t" \
    "v_mov_b64 v[234:235], 0\n\t" "v_mov_b64 v[236:237], 0\n\t" "v_mov_b64 v[238:239], 0\n\t" "buffer_load_dwordx4 v[244:247], v216, %[rs], %[sb] offen sc1\n\t" \
    "ds_read_b128 v[160:163], %[l0] offset:0\n\t" "ds_read_b128 v[164:167], %[l0] offset:1024\n\t" "ds_read_b128 v[168:171], %[l0] offset:2048\n\t" \
    "ds_read_b128 v[172:175], %[l0] offset:3072\n\t" "s_nop 3\n\t" "buffer_load_dwordx4 v[248:251], %[mo], %[rs], s60 offen sc1\n\t" \
    "ds_read_b128 v[176:179], %[l0] offset:4096\n\t" "ds_read_b128 v[180:183], %[l0] offset:5120\n\t" "ds_read_b128 v[184:187], %[l0] offset:6144\n\t" \
    "ds_read_b128 v[188:191], %[l0] offset:7168\n\t" "s_nop 3\n\t" "buffer_load_dwordx4 v[252:255], v216, %[rs], s60 offen sc1\n\t" "v_mov_b32 v219, 160512\n\t" \
    "v_mov_b64 v[220:221], -1\n\t" "v_mov_b64 v[222:223], -1\n\t" "buffer_store_dwordx2 v[220:221], %[px], %[rs], %[tr] offen sc1\n\t" \
    "buffer_store_dwordx2 v[220:221], %[pm], %[rs], %[tr] offen sc1\n\t" "v_mov_b32 v208, 0xd028965f\n\t" "v_mov_b32 v209, 0xff800000\n\t" "s_mov_b32 s62, 0\n\t" \
    "s_mov_b32 s63, -1\n\t" "v_mov_b32 v210, 0\n\t" "v_cmp_gt_i32 vcc, 0, %[ps]\n\t" "v_cndmask_b32 v204, 0, v209, s[62:63]\n\t" "s_nop 0\n\t" \
    "v_cndmask_b32 v200, 0, v208, vcc\n\t" "v_cmp_gt_i32 vcc, 1, %[ps]\n\t" "v_cndmask_b32 v205, 0, v209, s[62:63]\n\t" "s_nop 0\n\t" "v_cndmask_b32 v201, 0, v208, vcc\n\t" \
    "v_cmp_gt_i32 vcc, 2, %[ps]\n\t" "v_cndmask_b32 v206, 0, v209, s[62:63]\n\t" "s_nop 0\n\t" "v_cndmask_b32 v202, 0, v208, vcc\n\t" "v_cmp_gt_i32 vcc, 3, %[ps]\n\t" \
    "v_cndmask_b32 v207, 0, v209, s[62:63]\n\t" "s_nop 0\n\t" "v_cndmask_b32 v203, 0, v208, vcc\n\t" "20:\n\t" "s_waitcnt vmcnt(5)\n\t" XT(84, 85) \
    "v_smfmac_f32_16x16x64_bf16 v[224:227], v[240:243], a[0:7], %[ix]\n\t" "ds_write_b128 %[l1], v[240:243] offset:4096\n\t" \
    "v_smfmac_f32_16x16x64_bf16 v[228:231], v[240:243], a[8:15], %[ix]\n\t" "v_smfmac_f32_16x16x64_bf16 v[232:235], v[240:243], v[32:39], %[ix]\n\t" \
    "s_waitcnt lgkmcnt(7)\n\t" "v_smfmac_f32_16x16x64_bf16 v[236:239], v[240:243], v[160:167], %[ix]\n\t" "ds_read_b128 v[160:163], %[l0] offset:8192\n\t" \
    "ds_read_b128 v[164:167], %[l0] offset:9216\n\t" "s_waitcnt vmcnt(4)\n\t" "v_smfmac_f32_16x16x64_bf16 v[224:227], v[244:247], a[16:23], %[ix]\n\t" \
    "ds_write_b128 %[l1], v[244:247] offset:5120\n\t" "v_smfmac_f32_16x16x64_bf16 v[228:231], v[244:247], a[24:31], %[ix]\n\t" \
    "v_smfmac_f32_16x16x64_bf16 v[232:235], v[244:247], v[40:47], %[ix]\n\t" "s_waitcnt lgkmcnt(8)\n\t" \
    "v_smfmac_f32_16x16x64_bf16 v[236:239], v[244:247], v[168:175], %[ix]\n\t" "ds_read_b128 v[168:171], %[l0] offset:10240\n\t" \
    "ds_read_b128 v[172:175], %[l0] offset:11264\n\t" "s_waitcnt vmcnt(3)\n\t" "v_smfmac_f32_16x16x64_bf16 v[224:227], v[248:251], a[32:39], %[ix]\n\t" \
    "ds_write_b128 %[l1], v[248:251] offset:6144\n\t" "v_smfmac_f32_16x16x64_bf16 v[228:231], v[248:251], a[40:47], %[ix]\n\t" "s_waitcnt lgkmcnt(3)\n\t" "s_barrier\n\t" \
    XT(86, 87) "v_smfmac_f32_16x16x64_bf16 v[232:235], v[248:251], v[48:55], %[ix]\n\t" "ds_read_b128 v[192:195], %[l1] offset:8192\n\t" \
    "ds_read_b128 v[196:199], %[l1] offset:12288\n\t" "v_smfmac_f32_16x16x64_bf16 v[236:239], v[248:251], v[176:183], %[ix]\n\t" \
    "ds_read_b128 v[176:179], %[l0] offset:12288\n\t" "ds_read_b128 v[180:183], %[l0] offset:13312\n\t" "s_waitcnt vmcnt(2)\n\t" \
    "v_smfmac_f32_16x16x64_bf16 v[224:227], v[252:255], a[48:55], %[ix]\n\t" "ds_write_b128 %[l1], v[252:255] offset:7168\n\t" \
    "v_smfmac_f32_16x16x64_bf16 v[228:231], v[252:255], a[56:63], %[ix]\n\t" "ds_read_b128 v[240:243], %[l1] offset:0\n\t" "ds_read_b128 v[244:247], %[l1] offset:9216\n\t" \
    "v_smfmac_f32_16x16x64_bf16 v[232:235], v[252:255], v[56:63], %[ix]\n\t" "s_nop 1\n\t" "v_smfmac_f32_16x16x64_bf16 v[236:239], v[252:255], v[184:191], %[ix]\n\t" \
    "ds_read_b128 v[184:187], %[l0] offset:14336\n\t" "v_cmp_u_f32 vcc, v224, v225\n\t" "v_cmp_u_f32 s[62:63], v226, v227\n\t" "s_waitcnt lgkmcnt(7)\n\t" \
    "v_smfmac_f32_16x16x64_bf16 v[224:227], v[192:195], a[64:71], %[ix]\n\t" "ds_read_b128 v[188:191], %[l0] offset:15360\n\t" \
    "v_smfmac_f32_16x16x64_bf16 v[228:231], v[192:195], a[72:79], %[ix]\n\t" "s_or_b64 vcc, vcc, s[62:63]\n\t" "s_cbranch_scc1 21f\n\t" "22:\n\t" \
    "v_smfmac_f32_16x16x64_bf16 v[232:235], v[192:195], v[64:71], %[ix]\n\t" "s_waitcnt lgkmcnt(4)\n\t" "s_barrier\n\t" XT(88, 89) "ds_read_b32 v218, v219\n\t" \
    "v_smfmac_f32_16x16x64_bf16 v[236:239], v[192:195], v[160:167], %[ix]\n\t" "ds_read_b128 v[160:163], %[l0] offset:16384\n\t" \
    "ds_read_b128 v[164:167], %[l0] offset:17408\n\t" "v_smfmac_f32_16x16x64_bf16 v[224:227], v[196:199], a[80:87], %[ix]\n\t" \
    "ds_read_b128 v[248:251], %[l1] offset:13312\n\t" "ds_read_b128 v[252:255], %[l1] offset:1024\n\t" \
    "v_smfmac_f32_16x16x64_bf16 v[228:231], v[196:199], a[88:95], %[ix]\n\t" "ds_read_b128 v[192:195], %[l1] offset:10240\n\t" \
    "v_smfmac_f32_16x16x64_bf16 v[232:235], v[196:199], v[72:79], %[ix]\n\t" "v_smfmac_f32_16x16x64_bf16 v[236:239], v[196:199], v[168:175], %[ix]\n\t" \
    "s_waitcnt lgkmcnt(5)\n\t" "v_cmp_eq_u32 vcc, %[rt], v218\n\t" "v_smfmac_f32_16x16x64_bf16 v[224:227], v[240:243], a[96:103], %[ix]\n\t" "s_cbranch_vccnz 23f\n\t" \
    "ds_read_b128 v[168:171], %[l0] offset:18432\n\t" "v_smfmac_f32_16x16x64_bf16 v[228:231], v[240:243], a[104:111], %[ix]\n\t" \
    "ds_read_b128 v[172:175], %[l0] offset:19456\n\t" "ds_read_b128 v[196:199], %[l1] offset:14336\n\t" \
    "v_smfmac_f32_16x16x64_bf16 v[232:235], v[240:243], v[80:87], %[ix]\n\t" "buffer_load_dwordx4 v[16:19], %[hq], %[rs], %[qb] offen offset:0 sc1\n\t" \
    "v_smfmac_f32_16x16x64_bf16 v[236:239], v[240:243], v[176:183], %[ix]\n\t" "ds_read_b128 v[176:179], %[l0] offset:20480\n\t" \
    "ds_read_b128 v[180:183], %[l0] offset:21504\n\t" "v_smfmac_f32_16x16x64_bf16 v[224:227], v[244:247], a[112:119], %[ix]\n\t" \
    "ds_read_b128 v[240:243], %[l1] offset:2048\n\t" "v_smfmac_f32_16x16x64_bf16 v[228:231], v[244:247], a[120:127], %[ix]\n\t" \
    "v_smfmac_f32_16x16x64_bf16 v[232:235], v[244:247], v[88:95], %[ix]\n\t" "buffer_load_dwordx4 v[20:23], %[hq], %[rs], %[qb] offen offset:2048 sc1\n\t" \
    "v_smfmac_f32_16x16x64_bf16 v[236:239], v[244:247], v[184:191], %[ix]\n\t" "ds_read_b128 v[184:187], %[l0] offset:22528\n\t" \
    "ds_read_b128 v[188:191], %[l0] offset:23552\n\t" "s_waitcnt lgkmcnt(10)\n\t" "v_smfmac_f32_16x16x64_bf16 v[224:227], v[248:251], a[128:135], %[ix]\n\t" \
    "ds_read_b128 v[244:247], %[l1] offset:11264\n\t" "v_smfmac_f32_16x16x64_bf16 v[228:231], v[248:251], a[136:143], %[ix]\n\t" \
    "v_smfmac_f32_16x16x64_bf16 v[232:235], v[248:251], v[96:103], %[ix]\n\t" "buffer_load_dwordx4 v[24:27], %[hq], %[rs], s61 offen offset:0 sc1\n\t" \
    "v_smfmac_f32_16x16x64_bf16 v[236:239], v[248:251], v[160:167], %[ix]\n\t" "ds_read_b128 v[160:163], %[l0] offset:24576\n\t" \
    "ds_read_b128 v[164:167], %[l0] offset:25600\n\t" "s_waitcnt lgkmcnt(12)\n\t" "v_smfmac_f32_16x16x64_bf16 v[224:227], v[252:255], a[144:151], %[ix]\n\t" \
    "ds_read_b128 v[248:251], %[l1] offset:15360\n\t" "v_smfmac_f32_16x16x64_bf16 v[228:231], v[252:255], a[152:159], %[ix]\n\t" \
    "v_smfmac_f32_16x16x64_bf16 v[232:235], v[252:255], v[104:111], %[ix]\n\t" "buffer_load_dwordx4 v[28:31], %[hq], %[rs], s61 offen offset:2048 sc1\n\t" \
    "s_waitcnt lgkmcnt(10)\n\t" "v_smfmac_f32_16x16x64_bf16 v[236:239], v[252:255], v[168:175], %[ix]\n\t" "ds_read_b128 v[168:171], %[l0] offset:26624\n\t" \
    "ds_read_b128 v[172:175], %[l0] offset:27648\n\t" "v_smfmac_f32_16x16x64_bf16 v[224:227], v[192:195], a[160:167], %[ix]\n\t" \
    "ds_read_b128 v[252:255], %[l1] offset:3072\n\t" "v_smfmac_f32_16x16x64_bf16 v[228:231], v[192:195], a[168:175], %[ix]\n\t" "s_waitcnt lgkmcnt(12)\n\t" \
    "v_smfmac_f32_16x16x64_bf16 v[224:227], v[196:199], a[176:183], %[ix]\n\t" "v_smfmac_f32_16x16x64_bf16 v[228:231], v[196:199], a[184:191], %[ix]\n\t" \
    "s_waitcnt lgkmcnt(9)\n\t" "v_smfmac_f32_16x16x64_bf16 v[224:227], v[240:243], a[192:199], %[ix]\n\t" \
    "v_smfmac_f32_16x16x64_bf16 v[228:231], v[240:243], a[200:207], %[ix]\n\t" "s_waitcnt lgkmcnt(6)\n\t" \
    "v_smfmac_f32_16x16x64_bf16 v[224:227], v[244:247], a[208:215], %[ix]\n\t" "v_smfmac_f32_16x16x64_bf16 v[228:231], v[244:247], a[216:223], %[ix]\n\t" \
    "s_waitcnt vmcnt(4)\n\t" "s_waitcnt lgkmcnt(3)\n\t" "v_smfmac_f32_16x16x64_bf16 v[224:227], v[248:251], a[224:231], %[ix]\n\t" \
    "v_smfmac_f32_16x16x64_bf16 v[228:231], v[248:251], a[232:239], %[ix]\n\t" "s_waitcnt lgkmcnt(0)\n\t" \
    "v_smfmac_f32_16x16x64_bf16 v[224:227], v[252:255], a[240:247], %[ix]\n\t" "s_nop 1\n\t" "v_smfmac_f32_16x16x64_bf16 v[228:231], v[252:255], a[248:255], %[ix]\n\t" \
    XT(90, 91) "s_nop 1\n\t" "v_smfmac_f32_16x16x64_bf16 v[232:235], v[192:195], v[112:119], %[ix]\n\t" "s_nop 1\n\t" \
    "v_smfmac_f32_16x16x64_bf16 v[236:239], v[192:195], v[176:183], %[ix]\n\t" "ds_read_b128 v[176:179], %[l0] offset:28672\n\t" "v_add_f32 v224, v224, v225\n\t" \
    "v_add_f32 v226, v226, v227\n\t" "v_smfmac_f32_16x16x64_bf16 v[232:235], v[196:199], v[120:127], %[ix]\n\t" "ds_read_b128 v[180:183], %[l0] offset:29696\n\t" \
    "v_add_f32 v228, v228, v229\n\t" "v_add_f32 v230, v230, v231\n\t" "v_smfmac_f32_16x16x64_bf16 v[236:239], v[196:199], v[184:191], %[ix]\n\t" \
    "ds_read_b128 v[184:187], %[l0] offset:30720\n\t" "v_cvt_pk_bf16_f32 v218, v224, v228\n\t" "v_cvt_pk_bf16_f32 v219, v226, v230\n\t" \
    "v_smfmac_f32_16x16x64_bf16 v[232:235], v[240:243], v[128:135], %[ix]\n\t" "ds_write2_b32 %[qw], v218, v219 offset1:8\n\t" \
    "ds_read_b128 v[188:191], %[l0] offset:31744\n\t" "v_smfmac_f32_16x16x64_bf16 v[236:239], v[240:243], v[160:167], %[ix]\n\t" \
    "v_smfmac_f32_16x16x64_bf16 v[232:235], v[244:247], v[136:143], %[ix]\n\t" "v_smfmac_f32_16x16x64_bf16 v[236:239], v[244:247], v[168:175], %[ix]\n\t" \
    "s_waitcnt lgkmcnt(1)\n\t" "s_barrier\n\t" "v_smfmac_f32_16x16x64_bf16 v[232:235], v[248:251], v[144:151], %[ix]\n\t" \
    "v_smfmac_f32_16x16x64_bf16 v[236:239], v[248:251], v[176:183], %[ix]\n\t" "v_smfmac_f32_16x16x64_bf16 v[232:235], v[252:255], v[152:159], %[ix]\n\t" \
    "s_waitcnt lgkmcnt(0)\n\t" "v_smfmac_f32_16x16x64_bf16 v[236:239], v[252:255], v[184:191], %[ix]\n\t" XT(92, 93) "s_nop 6\n\t" "v_add_f32 v232, v232, v233\n\t" \
    "v_add_f32 v234, v234, v235\n\t" "v_add_f32 v236, v236, v237\n\t" "v_add_f32 v238, v238, v239\n\t" "v_cvt_pk_bf16_f32 v232, v232, v236\n\t" \
    "v_cvt_pk_bf16_f32 v233, v234, v238\n\t" "ds_write2_b32 %[kw], v232, v233 offset1:68\n\t" "s_waitcnt lgkmcnt(0)\n\t" "s_barrier\n\t" XT(94, 95) \
    "s_cmp_ge_u32 %[sq], %[sn]\n\t" "s_cbranch_scc1 277f\n\t" "275:\n\t" "global_load_dword v240, v210, %[fq] sc1\n\t" "s_waitcnt vmcnt(0)\n\t" \
    "v_readfirstlane_b32 %[sq], v240\n\t" "s_nop 3\n\t" "s_cmp_ge_u32 %[sq], %[sn]\n\t" "s_cbranch_scc1 276f\n\t" "s_cmp_eq_u32 %[bd], 0\n\t" "s_cbranch_scc1 276f\n\t" \
    "s_sub_u32 %[bd], %[bd], 1\n\t" "s_sleep 2\n\t" "s_branch 275b\n\t" "276:\n\t" "buffer_load_dwordx4 v[16:19], %[hq], %[rs], %[qb] offen offset:0 sc1\n\t" \
    "buffer_load_dwordx4 v[20:23], %[hq], %[rs], %[qb] offen offset:2048 sc1\n\t" "buffer_load_dwordx4 v[24:27], %[hq], %[rs], s61 offen offset:0 sc1\n\t" \
    "buffer_load_dwordx4 v[28:31], %[hq], %[rs], s61 offen offset:2048 sc1\n\t" "277:\n\t" "s_nop 3\n\t" "ds_read_b128 v[160:163], %[kx] offset:0\n\t" \
    "ds_read_b128 v[164:167], %[kx] offset:2048\n\t" "ds_read_b128 v[168:171], %[kx] offset:4096\n\t" "ds_read_b128 v[172:175], %[kx] offset:6144\n\t" \
    "ds_read_b128 v[224:227], %[km] offset:0\n\t" "ds_read_b128 v[228:231], %[km] offset:64\n\t" "ds_read_b128 v[232:235], %[km] offset:128\n\t" \
    "ds_read_b128 v[236:239], %[km] offset:192\n\t" "s_waitcnt vmcnt(0)\n\t" "global_load_dword v240, v210, %[fq] sc1\n\t" "s_waitcnt lgkmcnt(7)\n\t" \
    "v_mfma_f32_16x16x32_bf16 v[248:251], v[160:163], v[16:19], v[200:203]\n\t" "s_waitcnt lgkmcnt(6)\n\t" \
    "v_mfma_f32_16x16x32_bf16 v[248:251], v[164:167], v[20:23], v[248:251]\n\t" "s_waitcnt lgkmcnt(5)\n\t" \
    "v_mfma_f32_16x16x32_bf16 v[248:251], v[168:171], v[24:27], v[248:251]\n\t" "s_waitcnt lgkmcnt(4)\n\t" \
    "v_mfma_f32_16x16x32_bf16 v[248:251], v[172:175], v[28:31], v[248:251]\n\t" "s_waitcnt lgkmcnt(3)\n\t" \
    "v_mfma_f32_16x16x32_bf16 v[244:247], v[224:227], v[16:19], v[204:207]\n\t" "s_waitcnt lgkmcnt(2)\n\t" \
    "v_mfma_f32_16x16x32_bf16 v[244:247], v[228:231], v[20:23], v[244:247]\n\t" "s_waitcnt lgkmcnt(1)\n\t" \
    "v_mfma_f32_16x16x32_bf16 v[244:247], v[232:235], v[24:27], v[244:247]\n\t" "s_waitcnt lgkmcnt(0)\n\t" \
    "v_mfma_f32_16x16x32_bf16 v[244:247], v[236:239], v[28:31], v[244:247]\n\t" "s_nop 3\n\t" "v_max3_f32 v242, v248, v249, v250\n\t" "v_max_f32 v242, v242, v251\n\t" \
    "s_nop 1\n\t" "v_max3_f32 v242, v242, v244, v245\n\t" "v_max3_f32 v242, v242, v246, v247\n\t" "v_mov_b32 v243, v242\n\t" "s_nop 1\n\t" \
    "v_permlane32_swap_b32 v242, v243\n\t" "v_max_f32 v242, v242, v243\n\t" "v_mov_b32 v243, v242\n\t" "s_nop 1\n\t" "v_permlane16_swap_b32 v242, v243\n\t" \
    "v_max_f32 v242, v242, v243\n\t" "v_pk_add_f32 v[244:245], v[244:245], v[242:243] op_sel_hi:[1,0] neg_lo:[0,1] neg_hi:[0,1]\n\t" \
    "v_pk_add_f32 v[246:247], v[246:247], v[242:243] op_sel_hi:[1,0] neg_lo:[0,1] neg_hi:[0,1]\n\t" \
    "v_pk_add_f32 v[248:249], v[248:249], v[242:243] op_sel_hi:[1,0] neg_lo:[0,1] neg_hi:[0,1]\n\t" \
    "v_pk_add_f32 v[250:251], v[250:251], v[242:243] op_sel_hi:[1,0] neg_lo:[0,1] neg_hi:[0,1]\n\t" "v_pk_mul_f32 v[244:245], v[244:245], s[66:67]\n\t" \
    "v_pk_mul_f32 v[246:247], v[246:247], s[66:67]\n\t" "v_pk_mul_f32 v[248:249], v[248:249], s[66:67]\n\t" "v_pk_mul_f32 v[250:251], v[250:251], s[66:67]\n\t" \
    "v_exp_f32 v244, v244\n\t" "v_exp_f32 v245, v245\n\t" "v_exp_f32 v246, v246\n\t" "v_exp_f32 v247, v247\n\t" "v_exp_f32 v248, v248\n\t" "v_exp_f32 v249, v249\n\t" \
    "v_exp_f32 v250, v250\n\t" "v_exp_f32 v251, v251\n\t" "v_pk_add_f32 v[242:243], v[244:245], v[246:247]\n\t" "v_pk_add_f32 v[242:243], v[242:243], v[248:249]\n\t" \
    "v_pk_add_f32 v[242:243], v[242:243], v[250:251]\n\t" "v_add_f32 v242, v242, v243\n\t" "v_mov_b32 v243, v242\n\t" "s_nop 1\n\t" "v_permlane32_swap_b32 v242, v243\n\t" \
    "v_add_f32 v242, v242, v243\n\t" "v_mov_b32 v243, v242\n\t" "s_nop 1\n\t" "v_permlane16_swap_b32 v242, v243\n\t" "v_add_f32 v242, v242, v243\n\t" \
    "v_rcp_f32 v242, v242\n\t" "s_nop 0\n\t" "v_pk_mul_f32 v[244:245], v[244:245], v[242:243] op_sel_hi:[1,0]\n\t" \
    "v_pk_mul_f32 v[246:247], v[246:247], v[242:243] op_sel_hi:[1,0]\n\t" "v_pk_mul_f32 v[248:249], v[248:249], v[242:243] op_sel_hi:[1,0]\n\t" \
    "v_pk_mul_f32 v[250:251], v[250:251], v[242:243] op_sel_hi:[1,0]\n\t" "v_cvt_pk_bf16_f32 v162, v244, v245\n\t" "v_cvt_pk_bf16_f32 v163, v246, v247\n\t" \
    "v_cvt_pk_bf16_f32 v164, v248, v249\n\t" "v_cvt_pk_bf16_f32 v165, v250, v251\n\t" "buffer_store_dwordx2 v[164:165], %[px], %[rs], %[tb] offen sc1\n\t" \
    "buffer_store_dwordx2 v[162:163], %[pm], %[rs], %[tb] offen sc1\n\t" "s_waitcnt vmcnt(2)\n\t" "v_readfirstlane_b32 s62, v240\n\t" "s_nop 3\n\t" \
    "s_max_u32 %[sq], %[sq], s62\n\t" "s_barrier\n\t" XT(96, 97) "s_branch 99f\n\t" "21:\n\t" "ds_read_b128 v[220:223], %[l1] offset:4096\n\t" "s_waitcnt lgkmcnt(0)\n\t" \
    "v_max3_u32 v218, v220, v221, v222\n\t" "v_max_u32 v218, v218, v223\n\t" "ds_read_b128 v[220:223], %[l1] offset:5120\n\t" "s_waitcnt lgkmcnt(0)\n\t" \
    "v_max3_u32 v218, v218, v220, v221\n\t" "v_max3_u32 v218, v218, v222, v223\n\t" "ds_read_b128 v[220:223], %[l1] offset:6144\n\t" "s_waitcnt lgkmcnt(0)\n\t" \
    "v_max3_u32 v218, v218, v220, v221\n\t" "v_max3_u32 v218, v218, v222, v223\n\t" "ds_read_b128 v[220:223], %[l1] offset:7168\n\t" "s_waitcnt lgkmcnt(0)\n\t" \
    "v_max3_u32 v218, v218, v220, v221\n\t" "v_max3_u32 v218, v218, v222, v223\n\t" "v_cmp_eq_u32 vcc, -1, v218\n\t" "s_cbranch_vccz 22b\n\t" "s_cmp_eq_u32 %[rt], 1\n\t" \
    "s_cbranch_scc1 22b\n\t" "v_mov_b32 v218, %[rt]\n\t" "ds_write_b32 v219, v218\n\t" "s_waitcnt lgkmcnt(0)\n\t" "s_branch 22b\n\t" "23:\n\t" "s_sub_u32 %[rt], %[rt], 1\n\t" \
    "s_waitcnt lgkmcnt(0)\n\t" XCNT(100) "24:\n\t" "buffer_load_dwordx4 v[240:243], %[mo], %[rs], %[sb] offen sc1\n\t" \
    "buffer_load_dwordx4 v[244:247], v216, %[rs], %[sb] offen sc1\n\t" "buffer_load_dwordx4 v[248:251], %[mo], %[rs], s60 offen sc1\n\t" \
    "buffer_load_dwordx4 v[252:255], v216, %[rs], s60 offen sc1\n\t" "s_waitcnt vmcnt(0)\n\t" "v_max_u32 v218, v240, v241\n\t" "v_max3_u32 v218, v218, v242, v243\n\t" \
    "v_max3_u32 v218, v218, v244, v245\n\t" "v_max3_u32 v218, v218, v246, v247\n\t" "v_max3_u32 v218, v218, v248, v249\n\t" "v_max3_u32 v218, v218, v250, v251\n\t" \
    "v_max3_u32 v218, v218, v252, v253\n\t" "v_max3_u32 v218, v218, v254, v255\n\t" "v_cmp_eq_u32 vcc, -1, v218\n\t" "s_cbranch_vccz 25f\n\t" "s_cmp_eq_u32 %[bd], 0\n\t" \
    "s_cbranch_scc1 25f\n\t" "s_sub_u32 %[bd], %[bd], 1\n\t" XCNT(101) "s_branch 24b\n\t" "25:\n\t" "ds_read_b128 v[160:163], %[l0] offset:0\n\t" \
    "ds_read_b128 v[164:167], %[l0] offset:1024\n\t" "ds_read_b128 v[168:171], %[l0] offset:2048\n\t" "ds_read_b128 v[172:175], %[l0] offset:3072\n\t" \
    "ds_read_b128 v[176:179], %[l0] offset:4096\n\t" "ds_read_b128 v[180:183], %[l0] offset:5120\n\t" "ds_read_b128 v[184:187], %[l0] offset:6144\n\t" \
    "ds_read_b128 v[188:191], %[l0] offset:7168\n\t" "v_mov_b64 v[224:225], 0\n\t" "v_mov_b64 v[226:227], 0\n\t" "v_mov_b64 v[228:229], 0\n\t" "v_mov_b64 v[230:231], 0\n\t" \
    "v_mov_b64 v[232:233], 0\n\t" "v_mov_b64 v[234:235], 0\n\t" "v_mov_b64 v[236:237], 0\n\t" "v_mov_b64 v[238:239], 0\n\t" "s_barrier\n\t" "s_branch 20b\n\t" "99:\n\t"
#define QK_STEP2 \
    ".p2align 6\n\t" "v_xor_b32 v216, 0x400, %[mo]\n\t" "s_xor_b32 s60, %[sb], 0x800\n\t" "s_add_u32 s61, %[qb], 0x1000\n\t" "s_mov_b32 s66, 0x3e0293ee\n\t" \
    "s_mov_b32 s67, 0x3e0293ee\n\t" XT(80, 81) XT(98, 99) "s_barrier\n\t" XT(82, 83) "buffer_load_dwordx4 v[240:243], %[mo], %[rs], %[sb] offen sc1\n\t" \
    "v_mov_b64 v[224:225], 0\n\t" "v_mov_b64 v[226:227], 0\n\t" "v_mov_b64 v[228:229], 0\n\t" "v_mov_b64 v[230:231], 0\n\t" "v_mov_b64 v[232:233], 0\n\t" \
    "v_mov_b64 v[234:235], 0\n\t" "v_mov_b64 v[236:237], 0\n\t" "v_mov_b64 v[238:239], 0\n\t" "buffer_load_dwordx4 v[244:247], v216, %[rs], %[sb] offen sc1\n\t" \
    "ds_read_b128 v[160:163], %[l0] offset:0\n\t" "ds_read_b128 v[164:167], %[l0] offset:1024\n\t" "ds_read_b128 v[168:171], %[l0] offset:2048\n\t" \
    "ds_read_b128 v[172:175], %[l0] offset:3072\n\t" "s_nop 3\n\t" "buffer_load_dwordx4 v[248:251], %[mo], %[rs], s60 offen sc1\n\t" \
    "ds_read_b128 v[176:179], %[l0] offset:4096\n\t" "ds_read_b128 v[180:183], %[l0] offset:5120\n\t" "ds_read_b128 v[184:187], %[l0] offset:6144\n\t" \
    "ds_read_b128 v[188:191], %[l0] offset:7168\n\t" "s_nop 3\n\t" "buffer_load_dwordx4 v[252:255], v216, %[rs], s60 offen sc1\n\t" "v_mov_b32 v219, 160512\n\t" \
    "v_mov_b64 v[220:221], -1\n\t" "v_mov_b64 v[222:223], -1\n\t" "buffer_store_dwordx4 v[220:223], %[px], %[rs], %[tr] offen sc1\n\t" \
    "buffer_store_dwordx2 v[220:221], %[pm], %[rs], %[tr] offen sc1\n\t" "v_mov_b32 v208, 0xd028965f\n\t" "v_mov_b32 v209, 0xff800000\n\t" "s_mov_b32 s62, 0\n\t" \
    "s_mov_b32 s63, -1\n\t" "v_mov_b32 v210, 0\n\t" "v_cmp_gt_i32 vcc, 0, %[ps]\n\t" "v_cndmask_b32 v204, 0, v209, s[62:63]\n\t" "s_nop 0\n\t" \
    "v_cndmask_b32 v200, 0, v208, vcc\n\t" "v_cmp_gt_i32 vcc, 1, %[ps]\n\t" "v_cndmask_b32 v205, 0, v209, s[62:63]\n\t" "s_nop 0\n\t" "v_cndmask_b32 v201, 0, v208, vcc\n\t" \
    "v_cmp_gt_i32 vcc, 2, %[ps]\n\t" "v_cndmask_b32 v206, 0, v209, s[62:63]\n\t" "s_nop 0\n\t" "v_cndmask_b32 v202, 0, v208, vcc\n\t" "v_cmp_gt_i32 vcc, 3, %[ps]\n\t" \
    "v_cndmask_b32 v207, 0, v209, s[62:63]\n\t" "s_nop 0\n\t" "v_cndmask_b32 v203, 0, v208, vcc\n\t" "30:\n\t" "s_waitcnt vmcnt(5)\n\t" XT(84, 85) \
    "v_smfmac_f32_16x16x64_bf16 v[224:227], v[240:243], a[0:7], %[ix]\n\t" "ds_write_b128 %[l1], v[240:243] offset:8192\n\t" \
    "v_smfmac_f32_16x16x64_bf16 v[228:231], v[240:243], a[8:15], %[ix]\n\t" "v_smfmac_f32_16x16x64_bf16 v[232:235], v[240:243], v[32:39], %[ix]\n\t" \
    "s_waitcnt lgkmcnt(7)\n\t" "v_smfmac_f32_16x16x64_bf16 v[236:239], v[240:243], v[160:167], %[ix]\n\t" "ds_read_b128 v[160:163], %[l0] offset:8192\n\t" \
    "ds_read_b128 v[164:167], %[l0] offset:9216\n\t" "s_waitcnt vmcnt(4)\n\t" "v_smfmac_f32_16x16x64_bf16 v[224:227], v[244:247], a[16:23], %[ix]\n\t" \
    "ds_write_b128 %[l1], v[244:247] offset:9216\n\t" "v_smfmac_f32_16x16x64_bf16 v[228:231], v[244:247], a[24:31], %[ix]\n\t" \
    "v_smfmac_f32_16x16x64_bf16 v[232:235], v[244:247], v[40:47], %[ix]\n\t" "s_waitcnt lgkmcnt(8)\n\t" \
    "v_smfmac_f32_16x16x64_bf16 v[236:239], v[244:247], v[168:175], %[ix]\n\t" "ds_read_b128 v[168:171], %[l0] offset:10240\n\t" \
    "ds_read_b128 v[172:175], %[l0] offset:11264\n\t" "s_waitcnt vmcnt(3)\n\t" "v_smfmac_f32_16x16x64_bf16 v[224:227], v[248:251], a[32:39], %[ix]\n\t" \
    "ds_write_b128 %[l1], v[248:251] offset:10240\n\t" "v_smfmac_f32_16x16x64_bf16 v[228:231], v[248:251], a[40:47], %[ix]\n\t" "s_waitcnt lgkmcnt(3)\n\t" "s_barrier\n\t" \
    XT(86, 87) "v_smfmac_f32_16x16x64_bf16 v[232:235], v[248:251], v[48:55], %[ix]\n\t" "ds_read_b128 v[192:195], %[l1] offset:12288\n\t" \
    "ds_read_b128 v[196:199], %[l1] offset:0\n\t" "v_smfmac_f32_16x16x64_bf16 v[236:239], v[248:251], v[176:183], %[ix]\n\t" "ds_read_b128 v[176:179], %[l0] offset:12288\n\t" \
    "ds_read_b128 v[180:183], %[l0] offset:13312\n\t" "s_waitcnt vmcnt(2)\n\t" "v_smfmac_f32_16x16x64_bf16 v[224:227], v[252:255], a[48:55], %[ix]\n\t" \
    "ds_write_b128 %[l1], v[252:255] offset:11264\n\t" "v_smfmac_f32_16x16x64_bf16 v[228:231], v[252:255], a[56:63], %[ix]\n\t" \
    "ds_read_b128 v[240:243], %[l1] offset:4096\n\t" "ds_read_b128 v[244:247], %[l1] offset:13312\n\t" \
    "v_smfmac_f32_16x16x64_bf16 v[232:235], v[252:255], v[56:63], %[ix]\n\t" "s_nop 1\n\t" "v_smfmac_f32_16x16x64_bf16 v[236:239], v[252:255], v[184:191], %[ix]\n\t" \
    "ds_read_b128 v[184:187], %[l0] offset:14336\n\t" "v_cmp_u_f32 vcc, v224, v225\n\t" "v_cmp_u_f32 s[62:63], v226, v227\n\t" "s_waitcnt lgkmcnt(7)\n\t" \
    "v_smfmac_f32_16x16x64_bf16 v[224:227], v[192:195], a[64:71], %[ix]\n\t" "ds_read_b128 v[188:191], %[l0] offset:15360\n\t" \
    "v_smfmac_f32_16x16x64_bf16 v[228:231], v[192:195], a[72:79], %[ix]\n\t" "s_or_b64 vcc, vcc, s[62:63]\n\t" "s_cbranch_scc1 31f\n\t" "32:\n\t" \
    "v_smfmac_f32_16x16x64_bf16 v[232:235], v[192:195], v[64:71], %[ix]\n\t" "s_waitcnt lgkmcnt(4)\n\t" "s_barrier\n\t" XT(88, 89) "ds_read_b32 v218, v219\n\t" \
    "v_smfmac_f32_16x16x64_bf16 v[236:239], v[192:195], v[160:167], %[ix]\n\t" "ds_read_b128 v[160:163], %[l0] offset:16384\n\t" \
    "ds_read_b128 v[164:167], %[l0] offset:17408\n\t" "v_smfmac_f32_16x16x64_bf16 v[224:227], v[196:199], a[80:87], %[ix]\n\t" \
    "ds_read_b128 v[248:251], %[l1] offset:1024\n\t" "ds_read_b128 v[252:255], %[l1] offset:5120\n\t" "v_smfmac_f32_16x16x64_bf16 v[228:231], v[196:199], a[88:95], %[ix]\n\t" \
    "ds_read_b128 v[192:195], %[l1] offset:14336\n\t" "v_smfmac_f32_16x16x64_bf16 v[232:235], v[196:199], v[72:79], %[ix]\n\t" \
    "v_smfmac_f32_16x16x64_bf16 v[236:239], v[196:199], v[168:175], %[ix]\n\t" "s_waitcnt lgkmcnt(5)\n\t" "v_cmp_eq_u32 vcc, %[rt], v218\n\t" \
    "v_smfmac_f32_16x16x64_bf16 v[224:227], v[240:243], a[96:103], %[ix]\n\t" "s_cbranch_vccnz 33f\n\t" "ds_read_b128 v[168:171], %[l0] offset:18432\n\t" \
    "v_smfmac_f32_16x16x64_bf16 v[228:231], v[240:243], a[104:111], %[ix]\n\t" "ds_read_b128 v[172:175], %[l0] offset:19456\n\t" \
    "ds_read_b128 v[196:199], %[l1] offset:2048\n\t" "v_smfmac_f32_16x16x64_bf16 v[232:235], v[240:243], v[80:87], %[ix]\n\t" \
    "buffer_load_dwordx4 v[16:19], %[hq], %[rs], %[qb] offen offset:0 sc1\n\t" "v_smfmac_f32_16x16x64_bf16 v[236:239], v[240:243], v[176:183], %[ix]\n\t" \
    "ds_read_b128 v[176:179], %[l0] offset:20480\n\t" "ds_read_b128 v[180:183], %[l0] offset:21504\n\t" \
    "v_smfmac_f32_16x16x64_bf16 v[224:227], v[244:247], a[112:119], %[ix]\n\t" "ds_read_b128 v[240:243], %[l1] offset:6144\n\t" \
    "v_smfmac_f32_16x16x64_bf16 v[228:231], v[244:247], a[120:127], %[ix]\n\t" "v_smfmac_f32_16x16x64_bf16 v[232:235], v[244:247], v[88:95], %[ix]\n\t" \
    "buffer_load_dwordx4 v[20:23], %[hq], %[rs], %[qb] offen offset:2048 sc1\n\t" "v_smfmac_f32_16x16x64_bf16 v[236:239], v[244:247], v[184:191], %[ix]\n\t" \
    "ds_read_b128 v[184:187], %[l0] offset:22528\n\t" "ds_read_b128 v[188:191], %[l0] offset:23552\n\t" "s_waitcnt lgkmcnt(10)\n\t" \
    "v_smfmac_f32_16x16x64_bf16 v[224:227], v[248:251], a[128:135], %[ix]\n\t" "ds_read_b128 v[244:247], %[l1] offset:15360\n\t" \
    "v_smfmac_f32_16x16x64_bf16 v[228:231], v[248:251], a[136:143], %[ix]\n\t" "v_smfmac_f32_16x16x64_bf16 v[232:235], v[248:251], v[96:103], %[ix]\n\t" \
    "buffer_load_dwordx4 v[24:27], %[hq], %[rs], s61 offen offset:0 sc1\n\t" "v_smfmac_f32_16x16x64_bf16 v[236:239], v[248:251], v[160:167], %[ix]\n\t" \
    "ds_read_b128 v[160:163], %[l0] offset:24576\n\t" "ds_read_b128 v[164:167], %[l0] offset:25600\n\t" "s_waitcnt lgkmcnt(12)\n\t" \
    "v_smfmac_f32_16x16x64_bf16 v[224:227], v[252:255], a[144:151], %[ix]\n\t" "ds_read_b128 v[248:251], %[l1] offset:3072\n\t" \
    "v_smfmac_f32_16x16x64_bf16 v[228:231], v[252:255], a[152:159], %[ix]\n\t" "v_smfmac_f32_16x16x64_bf16 v[232:235], v[252:255], v[104:111], %[ix]\n\t" \
    "buffer_load_dwordx4 v[28:31], %[hq], %[rs], s61 offen offset:2048 sc1\n\t" "s_waitcnt lgkmcnt(10)\n\t" \
    "v_smfmac_f32_16x16x64_bf16 v[236:239], v[252:255], v[168:175], %[ix]\n\t" "ds_read_b128 v[168:171], %[l0] offset:26624\n\t" \
    "ds_read_b128 v[172:175], %[l0] offset:27648\n\t" "v_smfmac_f32_16x16x64_bf16 v[224:227], v[192:195], a[160:167], %[ix]\n\t" \
    "ds_read_b128 v[252:255], %[l1] offset:7168\n\t" "v_smfmac_f32_16x16x64_bf16 v[228:231], v[192:195], a[168:175], %[ix]\n\t" "s_waitcnt lgkmcnt(12)\n\t" \
    "v_smfmac_f32_16x16x64_bf16 v[224:227], v[196:199], a[176:183], %[ix]\n\t" "v_smfmac_f32_16x16x64_bf16 v[228:231], v[196:199], a[184:191], %[ix]\n\t" \
    "s_waitcnt lgkmcnt(9)\n\t" "v_smfmac_f32_16x16x64_bf16 v[224:227], v[240:243], a[192:199], %[ix]\n\t" \
    "v_smfmac_f32_16x16x64_bf16 v[228:231], v[240:243], a[200:207], %[ix]\n\t" "s_waitcnt lgkmcnt(6)\n\t" \
    "v_smfmac_f32_16x16x64_bf16 v[224:227], v[244:247], a[208:215], %[ix]\n\t" "v_smfmac_f32_16x16x64_bf16 v[228:231], v[244:247], a[216:223], %[ix]\n\t" \
    "s_waitcnt vmcnt(4)\n\t" "s_waitcnt lgkmcnt(3)\n\t" "v_smfmac_f32_16x16x64_bf16 v[224:227], v[248:251], a[224:231], %[ix]\n\t" \
    "v_smfmac_f32_16x16x64_bf16 v[228:231], v[248:251], a[232:239], %[ix]\n\t" "s_waitcnt lgkmcnt(0)\n\t" \
    "v_smfmac_f32_16x16x64_bf16 v[224:227], v[252:255], a[240:247], %[ix]\n\t" "s_nop 1\n\t" "v_smfmac_f32_16x16x64_bf16 v[228:231], v[252:255], a[248:255], %[ix]\n\t" \
    XT(90, 91) "s_nop 1\n\t" "v_smfmac_f32_16x16x64_bf16 v[232:235], v[192:195], v[112:119], %[ix]\n\t" "s_nop 1\n\t" \
    "v_smfmac_f32_16x16x64_bf16 v[236:239], v[192:195], v[176:183], %[ix]\n\t" "ds_read_b128 v[176:179], %[l0] offset:28672\n\t" "v_add_f32 v224, v224, v225\n\t" \
    "v_add_f32 v226, v226, v227\n\t" "v_smfmac_f32_16x16x64_bf16 v[232:235], v[196:199], v[120:127], %[ix]\n\t" "ds_read_b128 v[180:183], %[l0] offset:29696\n\t" \
    "v_add_f32 v228, v228, v229\n\t" "v_add_f32 v230, v230, v231\n\t" "v_smfmac_f32_16x16x64_bf16 v[236:239], v[196:199], v[184:191], %[ix]\n\t" \
    "ds_read_b128 v[184:187], %[l0] offset:30720\n\t" "v_cvt_pk_bf16_f32 v218, v224, v228\n\t" "v_cvt_pk_bf16_f32 v219, v226, v230\n\t" \
    "v_smfmac_f32_16x16x64_bf16 v[232:235], v[240:243], v[128:135], %[ix]\n\t" "ds_write2_b32 %[qw], v218, v219 offset1:8\n\t" \
    "ds_read_b128 v[188:191], %[l0] offset:31744\n\t" "v_smfmac_f32_16x16x64_bf16 v[236:239], v[240:243], v[160:167], %[ix]\n\t" \
    "v_smfmac_f32_16x16x64_bf16 v[232:235], v[244:247], v[136:143], %[ix]\n\t" "v_smfmac_f32_16x16x64_bf16 v[236:239], v[244:247], v[168:175], %[ix]\n\t" \
    "s_waitcnt lgkmcnt(1)\n\t" "s_barrier\n\t" "v_smfmac_f32_16x16x64_bf16 v[232:235], v[248:251], v[144:151], %[ix]\n\t" \
    "v_smfmac_f32_16x16x64_bf16 v[236:239], v[248:251], v[176:183], %[ix]\n\t" "v_smfmac_f32_16x16x64_bf16 v[232:235], v[252:255], v[152:159], %[ix]\n\t" \
    "s_waitcnt lgkmcnt(0)\n\t" "v_smfmac_f32_16x16x64_bf16 v[236:239], v[252:255], v[184:191], %[ix]\n\t" XT(92, 93) "s_nop 6\n\t" "v_add_f32 v232, v232, v233\n\t" \
    "v_add_f32 v234, v234, v235\n\t" "v_add_f32 v236, v236, v237\n\t" "v_add_f32 v238, v238, v239\n\t" "v_cvt_pk_bf16_f32 v232, v232, v236\n\t" \
    "v_cvt_pk_bf16_f32 v233, v234, v238\n\t" "ds_write2_b32 %[kw], v232, v233 offset1:68\n\t" "s_waitcnt lgkmcnt(0)\n\t" "s_barrier\n\t" XT(94, 95) \
    "s_cmp_ge_u32 %[sq], %[sn]\n\t" "s_cbranch_scc1 377f\n\t" "375:\n\t" "global_load_dword v240, v210, %[fq] sc1\n\t" "s_waitcnt vmcnt(0)\n\t" \
    "v_readfirstlane_b32 %[sq], v240\n\t" "s_nop 3\n\t" "s_cmp_ge_u32 %[sq], %[sn]\n\t" "s_cbranch_scc1 376f\n\t" "s_cmp_eq_u32 %[bd], 0\n\t" "s_cbranch_scc1 376f\n\t" \
    "s_sub_u32 %[bd], %[bd], 1\n\t" "s_sleep 2\n\t" "s_branch 375b\n\t" "376:\n\t" "buffer_load_dwordx4 v[16:19], %[hq], %[rs], %[qb] offen offset:0 sc1\n\t" \
    "buffer_load_dwordx4 v[20:23], %[hq], %[rs], %[qb] offen offset:2048 sc1\n\t" "buffer_load_dwordx4 v[24:27], %[hq], %[rs], s61 offen offset:0 sc1\n\t" \
    "buffer_load_dwordx4 v[28:31], %[hq], %[rs], s61 offen offset:2048 sc1\n\t" "377:\n\t" "s_nop 3\n\t" "ds_read_b128 v[160:163], %[kx] offset:0\n\t" \
    "ds_read_b128 v[164:167], %[kx] offset:2048\n\t" "ds_read_b128 v[168:171], %[kx] offset:4096\n\t" "ds_read_b128 v[172:175], %[kx] offset:6144\n\t" \
    "ds_read_b128 v[176:179], %[kx] offset:256\n\t" "ds_read_b128 v[180:183], %[kx] offset:2304\n\t" "ds_read_b128 v[184:187], %[kx] offset:4352\n\t" \
    "ds_read_b128 v[188:191], %[kx] offset:6400\n\t" "ds_read_b128 v[224:227], %[km] offset:0\n\t" "ds_read_b128 v[228:231], %[km] offset:64\n\t" \
    "ds_read_b128 v[232:235], %[km] offset:128\n\t" "ds_read_b128 v[236:239], %[km] offset:192\n\t" "s_waitcnt vmcnt(0)\n\t" "global_load_dword v240, v210, %[fq] sc1\n\t" \
    "s_waitcnt lgkmcnt(11)\n\t" "v_mfma_f32_16x16x32_bf16 v[248:251], v[160:163], v[16:19], 0\n\t" "s_waitcnt lgkmcnt(10)\n\t" \
    "v_mfma_f32_16x16x32_bf16 v[248:251], v[164:167], v[20:23], v[248:251]\n\t" "s_waitcnt lgkmcnt(9)\n\t" \
    "v_mfma_f32_16x16x32_bf16 v[248:251], v[168:171], v[24:27], v[248:251]\n\t" "s_waitcnt lgkmcnt(8)\n\t" \
    "v_mfma_f32_16x16x32_bf16 v[248:251], v[172:175], v[28:31], v[248:251]\n\t" "s_waitcnt lgkmcnt(7)\n\t" \
    "v_mfma_f32_16x16x32_bf16 v[252:255], v[176:179], v[16:19], v[200:203]\n\t" "s_waitcnt lgkmcnt(6)\n\t" \
    "v_mfma_f32_16x16x32_bf16 v[252:255], v[180:183], v[20:23], v[252:255]\n\t" "s_waitcnt lgkmcnt(5)\n\t" \
    "v_mfma_f32_16x16x32_bf16 v[252:255], v[184:187], v[24:27], v[252:255]\n\t" "s_waitcnt lgkmcnt(4)\n\t" \
    "v_mfma_f32_16x16x32_bf16 v[252:255], v[188:191], v[28:31], v[252:255]\n\t" "s_waitcnt lgkmcnt(3)\n\t" \
    "v_mfma_f32_16x16x32_bf16 v[244:247], v[224:227], v[16:19], v[204:207]\n\t" "s_waitcnt lgkmcnt(2)\n\t" \
    "v_mfma_f32_16x16x32_bf16 v[244:247], v[228:231], v[20:23], v[244:247]\n\t" "s_waitcnt lgkmcnt(1)\n\t" \
    "v_mfma_f32_16x16x32_bf16 v[244:247], v[232:235], v[24:27], v[244:247]\n\t" "s_waitcnt lgkmcnt(0)\n\t" \
    "v_mfma_f32_16x16x32_bf16 v[244:247], v[236:239], v[28:31], v[244:247]\n\t" "s_nop 3\n\t" "v_max3_f32 v242, v248, v249, v250\n\t" "v_max3_f32 v242, v242, v251, v252\n\t" \
    "v_max3_f32 v242, v242, v253, v254\n\t" "v_max_f32 v242, v242, v255\n\t" "v_max3_f32 v242, v242, v244, v245\n\t" "v_max3_f32 v242, v242, v246, v247\n\t" \
    "v_mov_b32 v243, v242\n\t" "s_nop 1\n\t" "v_permlane32_swap_b32 v242, v243\n\t" "v_max_f32 v242, v242, v243\n\t" "v_mov_b32 v243, v242\n\t" "s_nop 1\n\t" \
    "v_permlane16_swap_b32 v242, v243\n\t" "v_max_f32 v242, v242, v243\n\t" "v_pk_add_f32 v[244:245], v[244:245], v[242:243] op_sel_hi:[1,0] neg_lo:[0,1] neg_hi:[0,1]\n\t" \
    "v_pk_add_f32 v[246:247], v[246:247], v[242:243] op_sel_hi:[1,0] neg_lo:[0,1] neg_hi:[0,1]\n\t" \
    "v_pk_add_f32 v[248:249], v[248:249], v[242:243] op_sel_hi:[1,0] neg_lo:[0,1] neg_hi:[0,1]\n\t" \
    "v_pk_add_f32 v[250:251], v[250:251], v[242:243] op_sel_hi:[1,0] neg_lo:[0,1] neg_hi:[0,1]\n\t" \
    "v_pk_add_f32 v[252:253], v[252:253], v[242:243] op_sel_hi:[1,0] neg_lo:[0,1] neg_hi:[0,1]\n\t" \
    "v_pk_add_f32 v[254:255], v[254:255], v[242:243] op_sel_hi:[1,0] neg_lo:[0,1] neg_hi:[0,1]\n\t" "v_pk_mul_f32 v[244:245], v[244:245], s[66:67]\n\t" \
    "v_pk_mul_f32 v[246:247], v[246:247], s[66:67]\n\t" "v_pk_mul_f32 v[248:249], v[248:249], s[66:67]\n\t" "v_pk_mul_f32 v[250:251], v[250:251], s[66:67]\n\t" \
    "v_pk_mul_f32 v[252:253], v[252:253], s[66:67]\n\t" "v_pk_mul_f32 v[254:255], v[254:255], s[66:67]\n\t" "v_exp_f32 v244, v244\n\t" "v_exp_f32 v245, v245\n\t" \
    "v_exp_f32 v246, v246\n\t" "v_exp_f32 v247, v247\n\t" "v_exp_f32 v248, v248\n\t" "v_exp_f32 v249, v249\n\t" "v_exp_f32 v250, v250\n\t" "v_exp_f32 v251, v251\n\t" \
    "v_exp_f32 v252, v252\n\t" "v_exp_f32 v253, v253\n\t" "v_exp_f32 v254, v254\n\t" "v_exp_f32 v255, v255\n\t" "v_pk_add_f32 v[242:243], v[244:245], v[246:247]\n\t" \
    "v_pk_add_f32 v[242:243], v[242:243], v[248:249]\n\t" "v_pk_add_f32 v[242:243], v[242:243], v[250:251]\n\t" "v_pk_add_f32 v[242:243], v[242:243], v[252:253]\n\t" \
    "v_pk_add_f32 v[242:243], v[242:243], v[254:255]\n\t" "v_add_f32 v242, v242, v243\n\t" "v_mov_b32 v243, v242\n\t" "s_nop 1\n\t" "v_permlane32_swap_b32 v242, v243\n\t" \
    "v_add_f32 v242, v242, v243\n\t" "v_mov_b32 v243, v242\n\t" "s_nop 1\n\t" "v_permlane16_swap_b32 v242, v243\n\t" "v_add_f32 v242, v242, v243\n\t" \
    "v_rcp_f32 v242, v242\n\t" "s_nop 0\n\t" "v_pk_mul_f32 v[244:245], v[244:245], v[242:243] op_sel_hi:[1,0]\n\t" \
    "v_pk_mul_f32 v[246:247], v[246:247], v[242:243] op_sel_hi:[1,0]\n\t" "v_pk_mul_f32 v[248:249], v[248:249], v[242:243] op_sel_hi:[1,0]\n\t" \
    "v_pk_mul_f32 v[250:251], v[250:251], v[242:243] op_sel_hi:[1,0]\n\t" "v_pk_mul_f32 v[252:253], v[252:253], v[242:243] op_sel_hi:[1,0]\n\t" \
    "v_pk_mul_f32 v[254:255], v[254:255], v[242:243] op_sel_hi:[1,0]\n\t" "v_cvt_pk_bf16_f32 v162, v244, v245\n\t" "v_cvt_pk_bf16_f32 v163, v246, v247\n\t" \
    "v_cvt_pk_bf16_f32 v164, v248, v249\n\t" "v_cvt_pk_bf16_f32 v165, v250, v251\n\t" "v_cvt_pk_bf16_f32 v166, v252, v253\n\t" "v_cvt_pk_bf16_f32 v167, v254, v255\n\t" \
    "buffer_store_dwordx4 v[164:167], %[px], %[rs], %[tb] offen sc1\n\t" "buffer_store_dwordx2 v[162:163], %[pm], %[rs], %[tb] offen sc1\n\t" "s_waitcnt vmcnt(2)\n\t" \
    "v_readfirstlane_b32 s62, v240\n\t" "s_nop 3\n\t" "s_max_u32 %[sq], %[sq], s62\n\t" "s_barrier\n\t" XT(96, 97) "s_branch 99f\n\t" "31:\n\t" \
    "ds_read_b128 v[220:223], %[l1] offset:8192\n\t" "s_waitcnt lgkmcnt(0)\n\t" "v_max3_u32 v218, v220, v221, v222\n\t" "v_max_u32 v218, v218, v223\n\t" \
    "ds_read_b128 v[220:223], %[l1] offset:9216\n\t" "s_waitcnt lgkmcnt(0)\n\t" "v_max3_u32 v218, v218, v220, v221\n\t" "v_max3_u32 v218, v218, v222, v223\n\t" \
    "ds_read_b128 v[220:223], %[l1] offset:10240\n\t" "s_waitcnt lgkmcnt(0)\n\t" "v_max3_u32 v218, v218, v220, v221\n\t" "v_max3_u32 v218, v218, v222, v223\n\t" \
    "ds_read_b128 v[220:223], %[l1] offset:11264\n\t" "s_waitcnt lgkmcnt(0)\n\t" "v_max3_u32 v218, v218, v220, v221\n\t" "v_max3_u32 v218, v218, v222, v223\n\t" \
    "v_cmp_eq_u32 vcc, -1, v218\n\t" "s_cbranch_vccz 32b\n\t" "s_cmp_eq_u32 %[rt], 1\n\t" "s_cbranch_scc1 32b\n\t" "v_mov_b32 v218, %[rt]\n\t" "ds_write_b32 v219, v218\n\t" \
    "s_waitcnt lgkmcnt(0)\n\t" "s_branch 32b\n\t" "33:\n\t" "s_sub_u32 %[rt], %[rt], 1\n\t" "s_waitcnt lgkmcnt(0)\n\t" XCNT(100) "34:\n\t" \
    "buffer_load_dwordx4 v[240:243], %[mo], %[rs], %[sb] offen sc1\n\t" "buffer_load_dwordx4 v[244:247], v216, %[rs], %[sb] offen sc1\n\t" \
    "buffer_load_dwordx4 v[248:251], %[mo], %[rs], s60 offen sc1\n\t" "buffer_load_dwordx4 v[252:255], v216, %[rs], s60 offen sc1\n\t" "s_waitcnt vmcnt(0)\n\t" \
    "v_max_u32 v218, v240, v241\n\t" "v_max3_u32 v218, v218, v242, v243\n\t" "v_max3_u32 v218, v218, v244, v245\n\t" "v_max3_u32 v218, v218, v246, v247\n\t" \
    "v_max3_u32 v218, v218, v248, v249\n\t" "v_max3_u32 v218, v218, v250, v251\n\t" "v_max3_u32 v218, v218, v252, v253\n\t" "v_max3_u32 v218, v218, v254, v255\n\t" \
    "v_cmp_eq_u32 vcc, -1, v218\n\t" "s_cbranch_vccz 35f\n\t" "s_cmp_eq_u32 %[bd], 0\n\t" "s_cbranch_scc1 35f\n\t" "s_sub_u32 %[bd], %[bd], 1\n\t" XCNT(101) \
    "s_branch 34b\n\t" "35:\n\t" "ds_read_b128 v[160:163], %[l0] offset:0\n\t" "ds_read_b128 v[164:167], %[l0] offset:1024\n\t" \
    "ds_read_b128 v[168:171], %[l0] offset:2048\n\t" "ds_read_b128 v[172:175], %[l0] offset:3072\n\t" "ds_read_b128 v[176:179], %[l0] offset:4096\n\t" \
    "ds_read_b128 v[180:183], %[l0] offset:5120\n\t" "ds_read_b128 v[184:187], %[l0] offset:6144\n\t" "ds_read_b128 v[188:191], %[l0] offset:7168\n\t" \
    "v_mov_b64 v[224:225], 0\n\t" "v_mov_b64 v[226:227], 0\n\t" "v_mov_b64 v[228:229], 0\n\t" "v_mov_b64 v[230:231], 0\n\t" "v_mov_b64 v[232:233], 0\n\t" \
    "v_mov_b64 v[234:235], 0\n\t" "v_mov_b64 v[236:237], 0\n\t" "v_mov_b64 v[238:239], 0\n\t" "s_barrier\n\t" "s_branch 30b\n\t" "99:\n\t"
#define QK_STEP3 \
    ".p2align 6\n\t" "v_xor_b32 v216, 0x400, %[mo]\n\t" "s_xor_b32 s60, %[sb], 0x800\n\t" "s_mov_b32 s67, %[pb]\n\t" XT(80, 81) "s_getpc_b64 s[62:63]\n\t" "48:\n\t" \
    "s_memtime s[64:65]\n\t" "s_waitcnt lgkmcnt(0)\n\t" "s_sub_u32 s61, %[tg], s64\n\t" "s_cmp_gt_i32 s61, 124\n\t" "s_cbranch_scc1 48b\n\t" "s_max_i32 s61, s61, 0\n\t" \
    "s_and_b32 s61, s61, 124\n\t" "s_sub_u32 s61, 49f-48b, s61\n\t" "s_add_u32 s62, s62, s61\n\t" "s_addc_u32 s63, s63, 0\n\t" "s_setpc_b64 s[62:63]\n\t" ".rept 31\n\t" \
    "s_nop 0\n\t" ".endr\n\t" "49:\n\t" XT(98, 99) "s_barrier\n\t" XT(82, 83) "buffer_load_dwordx4 v[240:243], %[mo], %[rs], %[sb] offen sc1\n\t" \
    "v_mov_b64 v[224:225], 0\n\t" "v_mov_b64 v[226:227], 0\n\t" "v_mov_b64 v[228:229], 0\n\t" "v_mov_b64 v[230:231], 0\n\t" "v_mov_b64 v[232:233], 0\n\t" \
    "v_mov_b64 v[234:235], 0\n\t" "v_mov_b64 v[236:237], 0\n\t" "v_mov_b64 v[238:239], 0\n\t" "buffer_load_dwordx4 v[244:247], v216, %[rs], %[sb] offen sc1\n\t" \
    "ds_read_b128 v[160:163], %[l0] offset:0\n\t" "ds_read_b128 v[164:167], %[l0] offset:1024\n\t" "ds_read_b128 v[168:171], %[l0] offset:2048\n\t" \
    "ds_read_b128 v[172:175], %[l0] offset:3072\n\t" "s_nop 3\n\t" "buffer_load_dwordx4 v[248:251], %[mo], %[rs], s60 offen sc1\n\t" \
    "ds_read_b128 v[176:179], %[l0] offset:4096\n\t" "ds_read_b128 v[180:183], %[l0] offset:5120\n\t" "ds_read_b128 v[184:187], %[l0] offset:6144\n\t" \
    "ds_read_b128 v[188:191], %[l0] offset:7168\n\t" "s_nop 3\n\t" "buffer_load_dwordx4 v[252:255], v216, %[rs], s60 offen sc1\n\t" "v_mov_b32 v219, 160512\n\t" \
    "v_mov_b32 v220, -1\n\t" "buffer_store_dword v220, %[hq], %[rs], %[pr] offen sc1\n\t" "40:\n\t" "s_waitcnt vmcnt(4)\n\t" XT(84, 85) \
    "v_smfmac_f32_16x16x64_bf16 v[224:227], v[240:243], a[0:7], %[ix]\n\t" "ds_write_b128 %[l1], v[240:243] offset:12288\n\t" \
    "v_smfmac_f32_16x16x64_bf16 v[228:231], v[240:243], a[8:15], %[ix]\n\t" "v_smfmac_f32_16x16x64_bf16 v[232:235], v[240:243], v[32:39], %[ix]\n\t" \
    "s_waitcnt lgkmcnt(7)\n\t" "v_smfmac_f32_16x16x64_bf16 v[236:239], v[240:243], v[160:167], %[ix]\n\t" "ds_read_b128 v[160:163], %[l0] offset:8192\n\t" \
    "ds_read_b128 v[164:167], %[l0] offset:9216\n\t" "s_waitcnt vmcnt(3)\n\t" "v_smfmac_f32_16x16x64_bf16 v[224:227], v[244:247], a[16:23], %[ix]\n\t" \
    "ds_write_b128 %[l1], v[244:247] offset:13312\n\t" "v_smfmac_f32_16x16x64_bf16 v[228:231], v[244:247], a[24:31], %[ix]\n\t" \
    "v_smfmac_f32_16x16x64_bf16 v[232:235], v[244:247], v[40:47], %[ix]\n\t" "s_waitcnt lgkmcnt(8)\n\t" \
    "v_smfmac_f32_16x16x64_bf16 v[236:239], v[244:247], v[168:175], %[ix]\n\t" "ds_read_b128 v[168:171], %[l0] offset:10240\n\t" \
    "ds_read_b128 v[172:175], %[l0] offset:11264\n\t" "s_waitcnt vmcnt(2)\n\t" "v_smfmac_f32_16x16x64_bf16 v[224:227], v[248:251], a[32:39], %[ix]\n\t" \
    "ds_write_b128 %[l1], v[248:251] offset:14336\n\t" "v_smfmac_f32_16x16x64_bf16 v[228:231], v[248:251], a[40:47], %[ix]\n\t" "s_waitcnt lgkmcnt(3)\n\t" "s_barrier\n\t" \
    XT(86, 87) "v_smfmac_f32_16x16x64_bf16 v[232:235], v[248:251], v[48:55], %[ix]\n\t" "ds_read_b128 v[192:195], %[l1] offset:0\n\t" \
    "ds_read_b128 v[196:199], %[l1] offset:4096\n\t" "v_smfmac_f32_16x16x64_bf16 v[236:239], v[248:251], v[176:183], %[ix]\n\t" \
    "ds_read_b128 v[176:179], %[l0] offset:12288\n\t" "ds_read_b128 v[180:183], %[l0] offset:13312\n\t" "s_waitcnt vmcnt(1)\n\t" \
    "v_smfmac_f32_16x16x64_bf16 v[224:227], v[252:255], a[48:55], %[ix]\n\t" "ds_write_b128 %[l1], v[252:255] offset:15360\n\t" \
    "v_smfmac_f32_16x16x64_bf16 v[228:231], v[252:255], a[56:63], %[ix]\n\t" "ds_read_b128 v[240:243], %[l1] offset:8192\n\t" "ds_read_b128 v[244:247], %[l1] offset:1024\n\t" \
    "v_smfmac_f32_16x16x64_bf16 v[232:235], v[252:255], v[56:63], %[ix]\n\t" "s_nop 1\n\t" "v_smfmac_f32_16x16x64_bf16 v[236:239], v[252:255], v[184:191], %[ix]\n\t" \
    "ds_read_b128 v[184:187], %[l0] offset:14336\n\t" "v_cmp_u_f32 vcc, v224, v225\n\t" "v_cmp_u_f32 s[62:63], v226, v227\n\t" "s_waitcnt lgkmcnt(7)\n\t" \
    "v_smfmac_f32_16x16x64_bf16 v[224:227], v[192:195], a[64:71], %[ix]\n\t" "ds_read_b128 v[188:191], %[l0] offset:15360\n\t" \
    "v_smfmac_f32_16x16x64_bf16 v[228:231], v[192:195], a[72:79], %[ix]\n\t" "s_or_b64 vcc, vcc, s[62:63]\n\t" "s_cbranch_scc1 41f\n\t" "42:\n\t" \
    "v_smfmac_f32_16x16x64_bf16 v[232:235], v[192:195], v[64:71], %[ix]\n\t" "s_waitcnt lgkmcnt(4)\n\t" "s_barrier\n\t" XT(88, 89) "ds_read_b32 v218, v219\n\t" \
    "v_smfmac_f32_16x16x64_bf16 v[236:239], v[192:195], v[160:167], %[ix]\n\t" "s_memtime s[64:65]\n\t" "ds_read_b128 v[160:163], %[l0] offset:16384\n\t" \
    "v_smfmac_f32_16x16x64_bf16 v[224:227], v[196:199], a[80:87], %[ix]\n\t" "ds_read_b128 v[164:167], %[l0] offset:17408\n\t" \
    "ds_read_b128 v[248:251], %[l1] offset:5120\n\t" "v_smfmac_f32_16x16x64_bf16 v[228:231], v[196:199], a[88:95], %[ix]\n\t" "ds_read_b128 v[252:255], %[l1] offset:9216\n\t" \
    "ds_read_b128 v[192:195], %[l1] offset:2048\n\t" "v_smfmac_f32_16x16x64_bf16 v[232:235], v[196:199], v[72:79], %[ix]\n\t" "s_sub_u32 s62, s64, %[ca]\n\t" \
    "s_mul_hi_u32 s63, s62, %[k1]\n\t" "s_mul_i32 s62, s62, %[k1]\n\t" "v_smfmac_f32_16x16x64_bf16 v[236:239], v[196:199], v[168:175], %[ix]\n\t" "s_waitcnt lgkmcnt(5)\n\t" \
    "v_cmp_eq_u32 vcc, %[rt], v218\n\t" "s_lshr_b64 s[62:63], s[62:63], 16\n\t" "s_add_u32 s62, s62, %[ga]\n\t" "s_bitset0_b32 s62, 0\n\t" \
    "v_smfmac_f32_16x16x64_bf16 v[224:227], v[240:243], a[96:103], %[ix]\n\t" "s_cbranch_vccnz 43f\n\t" "ds_read_b128 v[168:171], %[l0] offset:18432\n\t" \
    "v_smfmac_f32_16x16x64_bf16 v[228:231], v[240:243], a[104:111], %[ix]\n\t" "ds_read_b128 v[172:175], %[l0] offset:19456\n\t" "v_mov_b32 v217, s62\n\t" \
    "buffer_store_dword v217, %[hq], %[rs], s67 offen sc1\n\t" "v_smfmac_f32_16x16x64_bf16 v[232:235], v[240:243], v[80:87], %[ix]\n\t" \
    "ds_read_b128 v[196:199], %[l1] offset:6144\n\t" "v_smfmac_f32_16x16x64_bf16 v[236:239], v[240:243], v[176:183], %[ix]\n\t" \
    "ds_read_b128 v[176:179], %[l0] offset:20480\n\t" "ds_read_b128 v[180:183], %[l0] offset:21504\n\t" \
    "v_smfmac_f32_16x16x64_bf16 v[224:227], v[244:247], a[112:119], %[ix]\n\t" "ds_read_b128 v[240:243], %[l1] offset:10240\n\t" \
    "v_smfmac_f32_16x16x64_bf16 v[228:231], v[244:247], a[120:127], %[ix]\n\t" "v_smfmac_f32_16x16x64_bf16 v[232:235], v[244:247], v[88:95], %[ix]\n\t" \
    "v_smfmac_f32_16x16x64_bf16 v[236:239], v[244:247], v[184:191], %[ix]\n\t" "ds_read_b128 v[184:187], %[l0] offset:22528\n\t" \
    "ds_read_b128 v[188:191], %[l0] offset:23552\n\t" "s_waitcnt lgkmcnt(10)\n\t" "v_smfmac_f32_16x16x64_bf16 v[224:227], v[248:251], a[128:135], %[ix]\n\t" \
    "ds_read_b128 v[244:247], %[l1] offset:3072\n\t" "v_smfmac_f32_16x16x64_bf16 v[228:231], v[248:251], a[136:143], %[ix]\n\t" \
    "v_smfmac_f32_16x16x64_bf16 v[232:235], v[248:251], v[96:103], %[ix]\n\t" "v_smfmac_f32_16x16x64_bf16 v[236:239], v[248:251], v[160:167], %[ix]\n\t" \
    "ds_read_b128 v[160:163], %[l0] offset:24576\n\t" "ds_read_b128 v[164:167], %[l0] offset:25600\n\t" "s_waitcnt lgkmcnt(12)\n\t" \
    "v_smfmac_f32_16x16x64_bf16 v[224:227], v[252:255], a[144:151], %[ix]\n\t" "ds_read_b128 v[248:251], %[l1] offset:7168\n\t" \
    "v_smfmac_f32_16x16x64_bf16 v[228:231], v[252:255], a[152:159], %[ix]\n\t" "v_smfmac_f32_16x16x64_bf16 v[232:235], v[252:255], v[104:111], %[ix]\n\t" \
    "s_waitcnt lgkmcnt(10)\n\t" "v_smfmac_f32_16x16x64_bf16 v[236:239], v[252:255], v[168:175], %[ix]\n\t" "ds_read_b128 v[168:171], %[l0] offset:26624\n\t" \
    "ds_read_b128 v[172:175], %[l0] offset:27648\n\t" "v_smfmac_f32_16x16x64_bf16 v[224:227], v[192:195], a[160:167], %[ix]\n\t" \
    "ds_read_b128 v[252:255], %[l1] offset:11264\n\t" "v_smfmac_f32_16x16x64_bf16 v[228:231], v[192:195], a[168:175], %[ix]\n\t" "s_waitcnt lgkmcnt(12)\n\t" \
    "v_smfmac_f32_16x16x64_bf16 v[224:227], v[196:199], a[176:183], %[ix]\n\t" "v_smfmac_f32_16x16x64_bf16 v[228:231], v[196:199], a[184:191], %[ix]\n\t" \
    "s_waitcnt lgkmcnt(9)\n\t" "v_smfmac_f32_16x16x64_bf16 v[224:227], v[240:243], a[192:199], %[ix]\n\t" \
    "v_smfmac_f32_16x16x64_bf16 v[228:231], v[240:243], a[200:207], %[ix]\n\t" "s_waitcnt lgkmcnt(6)\n\t" \
    "v_smfmac_f32_16x16x64_bf16 v[224:227], v[244:247], a[208:215], %[ix]\n\t" "v_smfmac_f32_16x16x64_bf16 v[228:231], v[244:247], a[216:223], %[ix]\n\t" \
    "s_waitcnt lgkmcnt(3)\n\t" "v_smfmac_f32_16x16x64_bf16 v[224:227], v[248:251], a[224:231], %[ix]\n\t" \
    "v_smfmac_f32_16x16x64_bf16 v[228:231], v[248:251], a[232:239], %[ix]\n\t" "s_waitcnt lgkmcnt(0)\n\t" \
    "v_smfmac_f32_16x16x64_bf16 v[224:227], v[252:255], a[240:247], %[ix]\n\t" "s_nop 1\n\t" "v_smfmac_f32_16x16x64_bf16 v[228:231], v[252:255], a[248:255], %[ix]\n\t" \
    XT(90, 91) "s_nop 1\n\t" "v_smfmac_f32_16x16x64_bf16 v[232:235], v[192:195], v[112:119], %[ix]\n\t" "s_nop 1\n\t" \
    "v_smfmac_f32_16x16x64_bf16 v[236:239], v[192:195], v[176:183], %[ix]\n\t" "ds_read_b128 v[176:179], %[l0] offset:28672\n\t" "v_add_f32 v224, v224, v225\n\t" \
    "v_add_f32 v226, v226, v227\n\t" "v_smfmac_f32_16x16x64_bf16 v[232:235], v[196:199], v[120:127], %[ix]\n\t" "ds_read_b128 v[180:183], %[l0] offset:29696\n\t" \
    "v_add_f32 v228, v228, v229\n\t" "v_add_f32 v230, v230, v231\n\t" "v_smfmac_f32_16x16x64_bf16 v[236:239], v[196:199], v[184:191], %[ix]\n\t" \
    "ds_read_b128 v[184:187], %[l0] offset:30720\n\t" "v_cvt_pk_bf16_f32 v218, v224, v228\n\t" "v_cvt_pk_bf16_f32 v219, v226, v230\n\t" \
    "v_smfmac_f32_16x16x64_bf16 v[232:235], v[240:243], v[128:135], %[ix]\n\t" "ds_write2_b32 %[qw], v218, v219 offset1:8\n\t" \
    "ds_read_b128 v[188:191], %[l0] offset:31744\n\t" "v_smfmac_f32_16x16x64_bf16 v[236:239], v[240:243], v[160:167], %[ix]\n\t" \
    "v_smfmac_f32_16x16x64_bf16 v[232:235], v[244:247], v[136:143], %[ix]\n\t" "v_smfmac_f32_16x16x64_bf16 v[236:239], v[244:247], v[168:175], %[ix]\n\t" \
    "s_waitcnt lgkmcnt(1)\n\t" "s_barrier\n\t" "v_smfmac_f32_16x16x64_bf16 v[232:235], v[248:251], v[144:151], %[ix]\n\t" \
    "v_smfmac_f32_16x16x64_bf16 v[236:239], v[248:251], v[176:183], %[ix]\n\t" "v_smfmac_f32_16x16x64_bf16 v[232:235], v[252:255], v[152:159], %[ix]\n\t" \
    "s_waitcnt lgkmcnt(0)\n\t" "v_smfmac_f32_16x16x64_bf16 v[236:239], v[252:255], v[184:191], %[ix]\n\t" XT(92, 93) "s_nop 6\n\t" "v_add_f32 v232, v232, v233\n\t" \
    "v_add_f32 v234, v234, v235\n\t" "v_add_f32 v236, v236, v237\n\t" "v_add_f32 v238, v238, v239\n\t" "v_cvt_pk_bf16_f32 v232, v232, v236\n\t" \
    "v_cvt_pk_bf16_f32 v233, v234, v238\n\t" "ds_write2_b32 %[kw], v232, v233 offset1:68\n\t" "s_waitcnt lgkmcnt(0)\n\t" "s_barrier\n\t" XT(94, 95) XT(96, 97) \
    "s_branch 99f\n\t" "41:\n\t" "ds_read_b128 v[220:223], %[l1] offset:12288\n\t" "s_waitcnt lgkmcnt(0)\n\t" "v_max3_u32 v218, v220, v221, v222\n\t" \
    "v_max_u32 v218, v218, v223\n\t" "ds_read_b128 v[220:223], %[l1] offset:13312\n\t" "s_waitcnt lgkmcnt(0)\n\t" "v_max3_u32 v218, v218, v220, v221\n\t" \
    "v_max3_u32 v218, v218, v222, v223\n\t" "ds_read_b128 v[220:223], %[l1] offset:14336\n\t" "s_waitcnt lgkmcnt(0)\n\t" "v_max3_u32 v218, v218, v220, v221\n\t" \
    "v_max3_u32 v218, v218, v222, v223\n\t" "ds_read_b128 v[220:223], %[l1] offset:15360\n\t" "s_waitcnt lgkmcnt(0)\n\t" "v_max3_u32 v218, v218, v220, v221\n\t" \
    "v_max3_u32 v218, v218, v222, v223\n\t" "v_cmp_eq_u32 vcc, -1, v218\n\t" "s_cbranch_vccz 42b\n\t" "s_cmp_eq_u32 %[rt], 1\n\t" "s_cbranch_scc1 42b\n\t" \
    "v_mov_b32 v218, %[rt]\n\t" "ds_write_b32 v219, v218\n\t" "s_waitcnt lgkmcnt(0)\n\t" "s_branch 42b\n\t" "43:\n\t" "s_sub_u32 %[rt], %[rt], 1\n\t" \
    "s_waitcnt lgkmcnt(0)\n\t" XCNT(100) "44:\n\t" "buffer_load_dwordx4 v[240:243], %[mo], %[rs], %[sb] offen sc1\n\t" \
    "buffer_load_dwordx4 v[244:247], v216, %[rs], %[sb] offen sc1\n\t" "buffer_load_dwordx4 v[248:251], %[mo], %[rs], s60 offen sc1\n\t" \
    "buffer_load_dwordx4 v[252:255], v216, %[rs], s60 offen sc1\n\t" "s_waitcnt vmcnt(0)\n\t" "v_max_u32 v218, v240, v241\n\t" "v_max3_u32 v218, v218, v242, v243\n\t" \
    "v_max3_u32 v218, v218, v244, v245\n\t" "v_max3_u32 v218, v218, v246, v247\n\t" "v_max3_u32 v218, v218, v248, v249\n\t" "v_max3_u32 v218, v218, v250, v251\n\t" \
    "v_max3_u32 v218, v218, v252, v253\n\t" "v_max3_u32 v218, v218, v254, v255\n\t" "v_cmp_eq_u32 vcc, -1, v218\n\t" "s_cbranch_vccz 45f\n\t" "s_cmp_eq_u32 %[bd], 0\n\t" \
    "s_cbranch_scc1 45f\n\t" "s_sub_u32 %[bd], %[bd], 1\n\t" XCNT(101) "s_branch 44b\n\t" "45:\n\t" "ds_read_b128 v[160:163], %[l0] offset:0\n\t" \
    "ds_read_b128 v[164:167], %[l0] offset:1024\n\t" "ds_read_b128 v[168:171], %[l0] offset:2048\n\t" "ds_read_b128 v[172:175], %[l0] offset:3072\n\t" \
    "ds_read_b128 v[176:179], %[l0] offset:4096\n\t" "ds_read_b128 v[180:183], %[l0] offset:5120\n\t" "ds_read_b128 v[184:187], %[l0] offset:6144\n\t" \
    "ds_read_b128 v[188:191], %[l0] offset:7168\n\t" "v_mov_b64 v[224:225], 0\n\t" "v_mov_b64 v[226:227], 0\n\t" "v_mov_b64 v[228:229], 0\n\t" "v_mov_b64 v[230:231], 0\n\t" \
    "v_mov_b64 v[232:233], 0\n\t" "v_mov_b64 v[234:235], 0\n\t" "v_mov_b64 v[236:237], 0\n\t" "v_mov_b64 v[238:239], 0\n\t" "s_barrier\n\t" "s_memtime s[64:65]\n\t" \
    "s_waitcnt lgkmcnt(0)\n\t" "s_add_u32 s64, s64, %[dr]\n\t" "s_sub_u32 s62, s64, %[ca]\n\t" "s_mul_hi_u32 s63, s62, %[k1]\n\t" "s_mul_i32 s62, s62, %[k1]\n\t" \
    "s_lshr_b64 s[62:63], s[62:63], 16\n\t" "s_add_u32 s62, s62, %[ga]\n\t" "s_bitset0_b32 s62, 0\n\t" "v_mov_b32 v218, s62\n\t" \
    "buffer_store_dword v218, %[hq], %[rs], s67 offen sc1\n\t" "s_mov_b32 s67, 0x7f000000\n\t" "s_branch 40b\n\t" "99:\n\t" "mk_tail:\n\t"
#define VO_STEP \
    ".p2align 6\n\t" "v_xor_b32 v224, 0x400, %[mo]\n\t" "s_xor_b32 s60, %[sb], 0x800\n\t" "s_mov_b32 s67, %[pb]\n\t" "s_cmp_eq_u32 %[w], 1\n\t" "s_cbranch_scc1 91f\n\t" \
    "s_cmp_eq_u32 %[w], 2\n\t" "s_cbranch_scc1 92f\n\t" "s_cmp_eq_u32 %[w], 3\n\t" "s_cbranch_scc1 93f\n\t" XT(80, 81) "s_getpc_b64 s[62:63]\n\t" "18:\n\t" \
    "s_memtime s[64:65]\n\t" "s_waitcnt lgkmcnt(0)\n\t" "s_sub_u32 s61, %[tg], s64\n\t" "s_cmp_gt_i32 s61, 124\n\t" "s_cbranch_scc1 18b\n\t" "s_max_i32 s61, s61, 0\n\t" \
    "s_and_b32 s61, s61, 124\n\t" "s_sub_u32 s61, 19f-18b, s61\n\t" "s_add_u32 s62, s62, s61\n\t" "s_addc_u32 s63, s63, 0\n\t" "s_setpc_b64 s[62:63]\n\t" ".rept 31\n\t" \
    "s_nop 0\n\t" ".endr\n\t" "19:\n\t" "s_barrier\n\t" XT(82, 83) "buffer_load_dwordx4 v[240:243], %[mo], %[rs], %[sb] offen sc1\n\t" "v_mov_b64 v[232:233], 0\n\t" \
    "v_mov_b64 v[234:235], 0\n\t" "v_mov_b64 v[236:237], 0\n\t" "v_mov_b64 v[238:239], 0\n\t" "v_mov_b64 v[192:193], 0\n\t" "v_mov_b64 v[194:195], 0\n\t" \
    "v_mov_b64 v[196:197], 0\n\t" "v_mov_b64 v[198:199], 0\n\t" "buffer_load_dwordx4 v[244:247], v224, %[rs], %[sb] offen sc1\n\t" \
    "ds_read_b128 v[160:163], %[l0] offset:0\n\t" "ds_read_b128 v[164:167], %[l0] offset:1024\n\t" "ds_read_b128 v[168:171], %[l0] offset:2048\n\t" \
    "ds_read_b128 v[172:175], %[l0] offset:3072\n\t" "s_nop 3\n\t" "buffer_load_dwordx4 v[248:251], %[mo], %[rs], s60 offen sc1\n\t" \
    "ds_read_b128 v[176:179], %[l0] offset:4096\n\t" "ds_read_b128 v[180:183], %[l0] offset:5120\n\t" "ds_read_b128 v[184:187], %[l0] offset:6144\n\t" \
    "ds_read_b128 v[188:191], %[l0] offset:7168\n\t" "s_nop 3\n\t" "buffer_load_dwordx4 v[252:255], v224, %[rs], s60 offen sc1\n\t" "v_mov_b64 v[200:201], 0\n\t" \
    "v_mov_b64 v[202:203], 0\n\t" "v_mov_b64 v[204:205], 0\n\t" "v_mov_b64 v[206:207], 0\n\t" "v_mov_b64 v[208:209], 0\n\t" "v_mov_b64 v[210:211], 0\n\t" \
    "v_mov_b64 v[212:213], 0\n\t" "v_mov_b64 v[214:215], 0\n\t" "v_mov_b64 v[216:217], 0\n\t" "v_mov_b64 v[218:219], 0\n\t" "v_mov_b64 v[220:221], 0\n\t" \
    "v_mov_b64 v[222:223], 0\n\t" "v_mov_b32 v226, -1\n\t" "buffer_store_dword v226, %[hq], %[rs], %[pr] offen sc1\n\t" "v_mov_b32 v227, 160512\n\t" "10:\n\t" \
    "s_waitcnt vmcnt(4)\n\t" XT(84, 85) "v_smfmac_f32_16x16x64_bf16 v[232:235], v[240:243], a[0:7], %[ix]\n\t" "ds_write_b128 %[l1], v[240:243] offset:0\n\t" \
    "v_smfmac_f32_16x16x64_bf16 v[236:239], v[240:243], a[8:15], %[ix]\n\t" "s_waitcnt vmcnt(3)\n\t" "v_smfmac_f32_16x16x64_bf16 v[232:235], v[244:247], a[16:23], %[ix]\n\t" \
    "ds_write_b128 %[l1], v[244:247] offset:1024\n\t" "s_waitcnt lgkmcnt(8)\n\t" "v_smfmac_f32_16x16x64_bf16 v[236:239], v[244:247], v[160:167], %[ix]\n\t" \
    "s_waitcnt lgkmcnt(0)\n\t" "s_barrier\n\t" XT(86, 87) "ds_read_b128 v[16:19], %[l1] offset:4096\n\t" "ds_read_b128 v[20:23], %[l1] offset:8192\n\t" \
    "ds_read_b128 v[24:27], %[l1] offset:12288\n\t" "s_waitcnt vmcnt(2)\n\t" "v_smfmac_f32_16x16x64_bf16 v[232:235], v[248:251], a[24:31], %[ix]\n\t" \
    "ds_write_b128 %[l1], v[248:251] offset:2048\n\t" "v_smfmac_f32_16x16x64_bf16 v[236:239], v[248:251], a[32:39], %[ix]\n\t" "ds_read_b128 v[28:31], %[l1] offset:5120\n\t" \
    "ds_read_b128 v[240:243], %[l1] offset:9216\n\t" "ds_read_b128 v[244:247], %[l1] offset:13312\n\t" "s_waitcnt vmcnt(1)\n\t" \
    "v_smfmac_f32_16x16x64_bf16 v[232:235], v[252:255], a[40:47], %[ix]\n\t" "ds_write_b128 %[l1], v[252:255] offset:3072\n\t" \
    "v_smfmac_f32_16x16x64_bf16 v[236:239], v[252:255], v[168:175], %[ix]\n\t" "ds_read_b128 v[160:163], %[l0] offset:8192\n\t" \
    "ds_read_b128 v[164:167], %[l0] offset:9216\n\t" "ds_read_b128 v[168:171], %[l0] offset:10240\n\t" "ds_read_b128 v[172:175], %[l0] offset:11264\n\t" "s_nop 1\n\t" \
    "v_cmp_u_f32 vcc, v232, v233\n\t" "v_cmp_u_f32 s[62:63], v234, v235\n\t" "s_waitcnt lgkmcnt(11)\n\t" \
    "v_smfmac_f32_16x16x64_bf16 v[232:235], v[16:19], a[48:55], %[ix]\n\t" "v_smfmac_f32_16x16x64_bf16 v[236:239], v[16:19], a[56:63], %[ix]\n\t" \
    "s_or_b64 vcc, vcc, s[62:63]\n\t" "s_cbranch_scc1 11f\n\t" "12:\n\t" "s_waitcnt lgkmcnt(10)\n\t" "v_smfmac_f32_16x16x64_bf16 v[232:235], v[20:23], a[64:71], %[ix]\n\t" \
    "v_smfmac_f32_16x16x64_bf16 v[236:239], v[20:23], v[176:183], %[ix]\n\t" "s_waitcnt lgkmcnt(4)\n\t" "s_barrier\n\t" XT(88, 89) "ds_read_b32 v225, v227\n\t" \
    "v_smfmac_f32_16x16x64_bf16 v[232:235], v[24:27], a[72:79], %[ix]\n\t" "ds_read_b128 v[176:179], %[l0] offset:12288\n\t" "ds_read_b128 v[180:183], %[l0] offset:13312\n\t" \
    "v_smfmac_f32_16x16x64_bf16 v[236:239], v[24:27], a[80:87], %[ix]\n\t" "ds_read_b128 v[248:251], %[l1] offset:6144\n\t" "ds_read_b128 v[252:255], %[l1] offset:10240\n\t" \
    "v_smfmac_f32_16x16x64_bf16 v[232:235], v[28:31], a[88:95], %[ix]\n\t" "ds_read_b128 v[16:19], %[l1] offset:14336\n\t" "ds_read_b128 v[20:23], %[l1] offset:7168\n\t" \
    "v_smfmac_f32_16x16x64_bf16 v[236:239], v[28:31], v[184:191], %[ix]\n\t" "s_memtime s[64:65]\n\t" "ds_read_b128 v[24:27], %[l1] offset:11264\n\t" \
    "v_smfmac_f32_16x16x64_bf16 v[232:235], v[240:243], a[96:103], %[ix]\n\t" "ds_read_b128 v[184:187], %[l0] offset:14336\n\t" \
    "ds_read_b128 v[188:191], %[l0] offset:15360\n\t" "v_smfmac_f32_16x16x64_bf16 v[236:239], v[240:243], a[104:111], %[ix]\n\t" "s_waitcnt lgkmcnt(9)\n\t" \
    "v_cmp_eq_u32 vcc, %[rt], v225\n\t" "v_smfmac_f32_16x16x64_bf16 v[232:235], v[244:247], a[112:119], %[ix]\n\t" "s_cbranch_vccnz 13f\n\t" \
    "ds_read_b128 v[28:31], %[l1] offset:15360\n\t" "v_smfmac_f32_16x16x64_bf16 v[236:239], v[244:247], v[160:167], %[ix]\n\t" \
    "ds_read_b128 v[160:163], %[l0] offset:16384\n\t" "ds_read_b128 v[164:167], %[l0] offset:17408\n\t" "s_waitcnt lgkmcnt(9)\n\t" \
    "v_smfmac_f32_16x16x64_bf16 v[232:235], v[248:251], a[120:127], %[ix]\n\t" "s_sub_u32 s62, s64, %[ca]\n\t" "s_mul_hi_u32 s63, s62, %[k1]\n\t" \
    "v_smfmac_f32_16x16x64_bf16 v[236:239], v[248:251], a[128:135], %[ix]\n\t" "s_mul_i32 s62, s62, %[k1]\n\t" "s_lshr_b64 s[62:63], s[62:63], 16\n\t" \
    "s_waitcnt lgkmcnt(8)\n\t" "v_smfmac_f32_16x16x64_bf16 v[232:235], v[252:255], a[136:143], %[ix]\n\t" "s_add_u32 s62, s62, %[ga]\n\t" "s_bitset0_b32 s62, 0\n\t" \
    "v_smfmac_f32_16x16x64_bf16 v[236:239], v[252:255], v[168:175], %[ix]\n\t" "v_mov_b32 v226, s62\n\t" "buffer_store_dword v226, %[hq], %[rs], s67 offen sc1\n\t" \
    "s_waitcnt lgkmcnt(7)\n\t" "v_smfmac_f32_16x16x64_bf16 v[232:235], v[16:19], a[144:151], %[ix]\n\t" "ds_read_b128 v[168:171], %[l0] offset:18432\n\t" \
    "ds_read_b128 v[172:175], %[l0] offset:19456\n\t" "v_smfmac_f32_16x16x64_bf16 v[236:239], v[16:19], a[152:159], %[ix]\n\t" "s_waitcnt lgkmcnt(8)\n\t" \
    "v_smfmac_f32_16x16x64_bf16 v[232:235], v[20:23], a[160:167], %[ix]\n\t" "v_smfmac_f32_16x16x64_bf16 v[236:239], v[20:23], v[176:183], %[ix]\n\t" \
    "ds_read_b128 v[176:179], %[l0] offset:20480\n\t" "ds_read_b128 v[180:183], %[l0] offset:21504\n\t" "s_waitcnt lgkmcnt(9)\n\t" \
    "v_smfmac_f32_16x16x64_bf16 v[232:235], v[24:27], a[168:175], %[ix]\n\t" "v_smfmac_f32_16x16x64_bf16 v[236:239], v[24:27], a[176:183], %[ix]\n\t" \
    "s_waitcnt lgkmcnt(6)\n\t" "v_smfmac_f32_16x16x64_bf16 v[232:235], v[28:31], a[184:191], %[ix]\n\t" \
    "v_smfmac_f32_16x16x64_bf16 v[236:239], v[28:31], v[184:191], %[ix]\n\t" "ds_read_b128 v[184:187], %[l0] offset:22528\n\t" \
    "ds_read_b128 v[188:191], %[l0] offset:23552\n\t" XT(90, 91) "s_nop 4\n\t" "v_add_f32 v232, v232, v233\n\t" "v_add_f32 v234, v234, v235\n\t" \
    "v_add_f32 v236, v236, v237\n\t" "v_add_f32 v238, v238, v239\n\t" "v_cvt_pk_bf16_f32 v236, v232, v236\n\t" "v_cvt_pk_bf16_f32 v237, v234, v238\n\t" \
    "ds_write2_b32 %[vw], v236, v237 offset1:68\n\t" "s_waitcnt lgkmcnt(0)\n\t" "s_barrier\n\t" XT(92, 93) "ds_read_b128 v[240:243], %[vr] offset:0\n\t" \
    "ds_read_b128 v[244:247], %[vr] offset:64\n\t" "ds_read_b128 v[248:251], %[vr] offset:128\n\t" "ds_read_b128 v[252:255], %[vr] offset:192\n\t" "s_mov_b32 s61, %[ub]\n\t" \
    "s_waitcnt lgkmcnt(3)\n\t" "v_smfmac_f32_16x16x64_bf16 v[192:195], v[240:243], v[32:39], %[ix]\n\t" \
    "v_smfmac_f32_16x16x64_bf16 v[196:199], v[240:243], v[48:55], %[ix]\n\t" "s_waitcnt lgkmcnt(2)\n\t" \
    "v_smfmac_f32_16x16x64_bf16 v[192:195], v[244:247], v[40:47], %[ix]\n\t" "v_smfmac_f32_16x16x64_bf16 v[196:199], v[244:247], v[56:63], %[ix]\n\t" \
    "s_waitcnt lgkmcnt(1)\n\t" "v_smfmac_f32_16x16x64_bf16 v[192:195], v[248:251], a[192:199], %[ix]\n\t" \
    "v_smfmac_f32_16x16x64_bf16 v[196:199], v[248:251], a[200:207], %[ix]\n\t" "s_waitcnt lgkmcnt(0)\n\t" \
    "v_smfmac_f32_16x16x64_bf16 v[192:195], v[252:255], v[160:167], %[ix]\n\t" "ds_read_b128 v[160:163], %[l0] offset:24576\n\t" \
    "ds_read_b128 v[164:167], %[l0] offset:25600\n\t" "v_smfmac_f32_16x16x64_bf16 v[196:199], v[252:255], v[168:175], %[ix]\n\t" \
    "ds_read_b128 v[168:171], %[l0] offset:26624\n\t" "ds_read_b128 v[172:175], %[l0] offset:27648\n\t" \
    "v_smfmac_f32_16x16x64_bf16 v[200:203], v[240:243], v[64:71], %[ix]\n\t" "s_nop 1\n\t" "v_smfmac_f32_16x16x64_bf16 v[204:207], v[240:243], v[80:87], %[ix]\n\t" \
    "v_cvt_pk_bf16_f32 v224, v192, v194\n\t" "v_cvt_pk_bf16_f32 v225, v193, v195\n\t" "v_smfmac_f32_16x16x64_bf16 v[200:203], v[244:247], v[72:79], %[ix]\n\t" \
    "v_cvt_pk_bf16_f32 v226, v196, v198\n\t" "v_cvt_pk_bf16_f32 v227, v197, v199\n\t" "v_smfmac_f32_16x16x64_bf16 v[204:207], v[244:247], v[88:95], %[ix]\n\t" "s_nop 0\n\t" \
    "v_permlane32_swap_b32 v224, v226\n\t" "v_smfmac_f32_16x16x64_bf16 v[200:203], v[248:251], a[208:215], %[ix]\n\t" "v_permlane32_swap_b32 v225, v227\n\t" "s_nop 0\n\t" \
    "v_smfmac_f32_16x16x64_bf16 v[204:207], v[248:251], a[216:223], %[ix]\n\t" "v_permlane16_swap_b32 v224, v225\n\t" "v_permlane16_swap_b32 v226, v227\n\t" \
    "v_smfmac_f32_16x16x64_bf16 v[200:203], v[252:255], v[176:183], %[ix]\n\t" "ds_read_b128 v[176:179], %[l0] offset:28672\n\t" \
    "buffer_store_dwordx4 v[224:227], %[uo], %[rs], s61 offen sc1\n\t" "s_xor_b32 s61, s61, 0x40000\n\t" XT(94, 95) \
    "v_smfmac_f32_16x16x64_bf16 v[204:207], v[252:255], v[184:191], %[ix]\n\t" "ds_read_b128 v[180:183], %[l0] offset:29696\n\t" \
    "ds_read_b128 v[184:187], %[l0] offset:30720\n\t" "v_smfmac_f32_16x16x64_bf16 v[208:211], v[240:243], v[96:103], %[ix]\n\t" \
    "ds_read_b128 v[188:191], %[l0] offset:31744\n\t" "s_nop 0\n\t" "v_smfmac_f32_16x16x64_bf16 v[212:215], v[240:243], v[112:119], %[ix]\n\t" \
    "v_cvt_pk_bf16_f32 v228, v200, v202\n\t" "v_cvt_pk_bf16_f32 v229, v201, v203\n\t" "v_smfmac_f32_16x16x64_bf16 v[208:211], v[244:247], v[104:111], %[ix]\n\t" \
    "v_cvt_pk_bf16_f32 v230, v204, v206\n\t" "v_cvt_pk_bf16_f32 v231, v205, v207\n\t" "v_smfmac_f32_16x16x64_bf16 v[212:215], v[244:247], v[120:127], %[ix]\n\t" "s_nop 0\n\t" \
    "v_permlane32_swap_b32 v228, v230\n\t" "v_smfmac_f32_16x16x64_bf16 v[208:211], v[248:251], a[224:231], %[ix]\n\t" "v_permlane32_swap_b32 v229, v231\n\t" "s_nop 0\n\t" \
    "v_smfmac_f32_16x16x64_bf16 v[212:215], v[248:251], a[232:239], %[ix]\n\t" "v_permlane16_swap_b32 v228, v229\n\t" "v_permlane16_swap_b32 v230, v231\n\t" \
    "s_waitcnt lgkmcnt(6)\n\t" "v_smfmac_f32_16x16x64_bf16 v[208:211], v[252:255], v[160:167], %[ix]\n\t" "buffer_store_dwordx4 v[228:231], %[uo], %[rs], s61 offen sc1\n\t" \
    "s_xor_b32 s61, s61, 0xc0000\n\t" "s_waitcnt lgkmcnt(4)\n\t" "v_smfmac_f32_16x16x64_bf16 v[212:215], v[252:255], v[168:175], %[ix]\n\t" "s_nop 1\n\t" \
    "v_smfmac_f32_16x16x64_bf16 v[216:219], v[240:243], v[128:135], %[ix]\n\t" "s_nop 1\n\t" "v_smfmac_f32_16x16x64_bf16 v[220:223], v[240:243], v[144:151], %[ix]\n\t" \
    "v_cvt_pk_bf16_f32 v224, v208, v210\n\t" "v_cvt_pk_bf16_f32 v225, v209, v211\n\t" "v_smfmac_f32_16x16x64_bf16 v[216:219], v[244:247], v[136:143], %[ix]\n\t" \
    "v_cvt_pk_bf16_f32 v226, v212, v214\n\t" "v_cvt_pk_bf16_f32 v227, v213, v215\n\t" "v_smfmac_f32_16x16x64_bf16 v[220:223], v[244:247], v[152:159], %[ix]\n\t" "s_nop 0\n\t" \
    "v_permlane32_swap_b32 v224, v226\n\t" "v_smfmac_f32_16x16x64_bf16 v[216:219], v[248:251], a[240:247], %[ix]\n\t" "v_permlane32_swap_b32 v225, v227\n\t" "s_nop 0\n\t" \
    "v_smfmac_f32_16x16x64_bf16 v[220:223], v[248:251], a[248:255], %[ix]\n\t" "v_permlane16_swap_b32 v224, v225\n\t" "v_permlane16_swap_b32 v226, v227\n\t" \
    "s_waitcnt lgkmcnt(2)\n\t" "v_smfmac_f32_16x16x64_bf16 v[216:219], v[252:255], v[176:183], %[ix]\n\t" "buffer_store_dwordx4 v[224:227], %[uo], %[rs], s61 offen sc1\n\t" \
    "s_xor_b32 s61, s61, 0x40000\n\t" "s_waitcnt lgkmcnt(0)\n\t" "v_smfmac_f32_16x16x64_bf16 v[220:223], v[252:255], v[184:191], %[ix]\n\t" "s_nop 5\n\t" \
    "v_cvt_pk_bf16_f32 v228, v216, v218\n\t" "v_cvt_pk_bf16_f32 v229, v217, v219\n\t" "v_cvt_pk_bf16_f32 v230, v220, v222\n\t" "v_cvt_pk_bf16_f32 v231, v221, v223\n\t" \
    "s_nop 0\n\t" "v_permlane32_swap_b32 v228, v230\n\t" "v_permlane32_swap_b32 v229, v231\n\t" "s_nop 1\n\t" "v_permlane16_swap_b32 v228, v229\n\t" \
    "v_permlane16_swap_b32 v230, v231\n\t" "buffer_store_dwordx4 v[228:231], %[uo], %[rs], s61 offen sc1\n\t" "s_nop 1\n\t" XT(96, 97) "s_branch 99f\n\t" "11:\n\t" \
    "ds_read_b128 v[228:231], %[l1] offset:0\n\t" "s_waitcnt lgkmcnt(0)\n\t" "v_max3_u32 v225, v228, v229, v230\n\t" "v_max_u32 v225, v225, v231\n\t" \
    "ds_read_b128 v[228:231], %[l1] offset:1024\n\t" "s_waitcnt lgkmcnt(0)\n\t" "v_max3_u32 v225, v225, v228, v229\n\t" "v_max3_u32 v225, v225, v230, v231\n\t" \
    "ds_read_b128 v[228:231], %[l1] offset:2048\n\t" "s_waitcnt lgkmcnt(0)\n\t" "v_max3_u32 v225, v225, v228, v229\n\t" "v_max3_u32 v225, v225, v230, v231\n\t" \
    "ds_read_b128 v[228:231], %[l1] offset:3072\n\t" "s_waitcnt lgkmcnt(0)\n\t" "v_max3_u32 v225, v225, v228, v229\n\t" "v_max3_u32 v225, v225, v230, v231\n\t" \
    "v_cmp_eq_u32 vcc, -1, v225\n\t" "s_cbranch_vccz 12b\n\t" "s_cmp_eq_u32 %[rt], 1\n\t" "s_cbranch_scc1 12b\n\t" "v_mov_b32 v225, %[rt]\n\t" "ds_write_b32 v227, v225\n\t" \
    "s_waitcnt lgkmcnt(0)\n\t" "s_branch 12b\n\t" "13:\n\t" "s_sub_u32 %[rt], %[rt], 1\n\t" "s_waitcnt lgkmcnt(0)\n\t" XCNT(100) "14:\n\t" \
    "buffer_load_dwordx4 v[240:243], %[mo], %[rs], %[sb] offen sc1\n\t" "buffer_load_dwordx4 v[244:247], v224, %[rs], %[sb] offen sc1\n\t" \
    "buffer_load_dwordx4 v[248:251], %[mo], %[rs], s60 offen sc1\n\t" "buffer_load_dwordx4 v[252:255], v224, %[rs], s60 offen sc1\n\t" "s_waitcnt vmcnt(0)\n\t" \
    "v_max_u32 v225, v240, v241\n\t" "v_max3_u32 v225, v225, v242, v243\n\t" "v_max3_u32 v225, v225, v244, v245\n\t" "v_max3_u32 v225, v225, v246, v247\n\t" \
    "v_max3_u32 v225, v225, v248, v249\n\t" "v_max3_u32 v225, v225, v250, v251\n\t" "v_max3_u32 v225, v225, v252, v253\n\t" "v_max3_u32 v225, v225, v254, v255\n\t" \
    "v_cmp_eq_u32 vcc, -1, v225\n\t" "s_cbranch_vccz 15f\n\t" "s_cmp_eq_u32 %[bd], 0\n\t" "s_cbranch_scc1 15f\n\t" "s_sub_u32 %[bd], %[bd], 1\n\t" XCNT(101) \
    "s_branch 14b\n\t" "15:\n\t" "ds_read_b128 v[160:163], %[l0] offset:0\n\t" "ds_read_b128 v[164:167], %[l0] offset:1024\n\t" \
    "ds_read_b128 v[168:171], %[l0] offset:2048\n\t" "ds_read_b128 v[172:175], %[l0] offset:3072\n\t" "ds_read_b128 v[176:179], %[l0] offset:4096\n\t" \
    "ds_read_b128 v[180:183], %[l0] offset:5120\n\t" "ds_read_b128 v[184:187], %[l0] offset:6144\n\t" "ds_read_b128 v[188:191], %[l0] offset:7168\n\t" \
    "v_mov_b64 v[232:233], 0\n\t" "v_mov_b64 v[234:235], 0\n\t" "v_mov_b64 v[236:237], 0\n\t" "v_mov_b64 v[238:239], 0\n\t" "s_barrier\n\t" "s_memtime s[64:65]\n\t" \
    "s_waitcnt lgkmcnt(0)\n\t" "s_add_u32 s64, s64, %[dr]\n\t" "s_sub_u32 s62, s64, %[ca]\n\t" "s_mul_hi_u32 s63, s62, %[k1]\n\t" "s_mul_i32 s62, s62, %[k1]\n\t" \
    "s_lshr_b64 s[62:63], s[62:63], 16\n\t" "s_add_u32 s62, s62, %[ga]\n\t" "s_bitset0_b32 s62, 0\n\t" "v_mov_b32 v225, s62\n\t" \
    "buffer_store_dword v225, %[hq], %[rs], s67 offen sc1\n\t" "s_mov_b32 s67, 0x7f000000\n\t" "s_branch 10b\n\t" "91:\n\t" XT(80, 81) "s_barrier\n\t" XT(82, 83) \
    "buffer_load_dwordx4 v[240:243], %[mo], %[rs], %[sb] offen sc1\n\t" "v_mov_b64 v[232:233], 0\n\t" "v_mov_b64 v[234:235], 0\n\t" "v_mov_b64 v[236:237], 0\n\t" \
    "v_mov_b64 v[238:239], 0\n\t" "v_mov_b64 v[192:193], 0\n\t" "v_mov_b64 v[194:195], 0\n\t" "v_mov_b64 v[196:197], 0\n\t" "v_mov_b64 v[198:199], 0\n\t" \
    "buffer_load_dwordx4 v[244:247], v224, %[rs], %[sb] offen sc1\n\t" "ds_read_b128 v[160:163], %[l0] offset:0\n\t" "ds_read_b128 v[164:167], %[l0] offset:1024\n\t" \
    "ds_read_b128 v[168:171], %[l0] offset:2048\n\t" "ds_read_b128 v[172:175], %[l0] offset:3072\n\t" "s_nop 3\n\t" \
    "buffer_load_dwordx4 v[248:251], %[mo], %[rs], s60 offen sc1\n\t" "ds_read_b128 v[176:179], %[l0] offset:4096\n\t" "ds_read_b128 v[180:183], %[l0] offset:5120\n\t" \
    "ds_read_b128 v[184:187], %[l0] offset:6144\n\t" "ds_read_b128 v[188:191], %[l0] offset:7168\n\t" "s_nop 3\n\t" \
    "buffer_load_dwordx4 v[252:255], v224, %[rs], s60 offen sc1\n\t" "v_mov_b64 v[200:201], 0\n\t" "v_mov_b64 v[202:203], 0\n\t" "v_mov_b64 v[204:205], 0\n\t" \
    "v_mov_b64 v[206:207], 0\n\t" "v_mov_b64 v[208:209], 0\n\t" "v_mov_b64 v[210:211], 0\n\t" "v_mov_b64 v[212:213], 0\n\t" "v_mov_b64 v[214:215], 0\n\t" \
    "v_mov_b64 v[216:217], 0\n\t" "v_mov_b64 v[218:219], 0\n\t" "v_mov_b64 v[220:221], 0\n\t" "v_mov_b64 v[222:223], 0\n\t" "v_mov_b32 v227, 160512\n\t" "20:\n\t" \
    "s_waitcnt vmcnt(3)\n\t" XT(84, 85) "v_smfmac_f32_16x16x64_bf16 v[232:235], v[240:243], a[0:7], %[ix]\n\t" "ds_write_b128 %[l1], v[240:243] offset:4096\n\t" \
    "v_smfmac_f32_16x16x64_bf16 v[236:239], v[240:243], a[8:15], %[ix]\n\t" "s_waitcnt vmcnt(2)\n\t" "v_smfmac_f32_16x16x64_bf16 v[232:235], v[244:247], a[16:23], %[ix]\n\t" \
    "ds_write_b128 %[l1], v[244:247] offset:5120\n\t" "s_waitcnt lgkmcnt(8)\n\t" "v_smfmac_f32_16x16x64_bf16 v[236:239], v[244:247], v[160:167], %[ix]\n\t" \
    "s_waitcnt lgkmcnt(0)\n\t" "s_barrier\n\t" XT(86, 87) "ds_read_b128 v[16:19], %[l1] offset:8192\n\t" "ds_read_b128 v[20:23], %[l1] offset:12288\n\t" \
    "ds_read_b128 v[24:27], %[l1] offset:0\n\t" "s_waitcnt vmcnt(1)\n\t" "v_smfmac_f32_16x16x64_bf16 v[232:235], v[248:251], a[24:31], %[ix]\n\t" \
    "ds_write_b128 %[l1], v[248:251] offset:6144\n\t" "v_smfmac_f32_16x16x64_bf16 v[236:239], v[248:251], a[32:39], %[ix]\n\t" "ds_read_b128 v[28:31], %[l1] offset:9216\n\t" \
    "ds_read_b128 v[240:243], %[l1] offset:13312\n\t" "ds_read_b128 v[244:247], %[l1] offset:1024\n\t" "s_waitcnt vmcnt(0)\n\t" \
    "v_smfmac_f32_16x16x64_bf16 v[232:235], v[252:255], a[40:47], %[ix]\n\t" "ds_write_b128 %[l1], v[252:255] offset:7168\n\t" \
    "v_smfmac_f32_16x16x64_bf16 v[236:239], v[252:255], v[168:175], %[ix]\n\t" "ds_read_b128 v[160:163], %[l0] offset:8192\n\t" \
    "ds_read_b128 v[164:167], %[l0] offset:9216\n\t" "ds_read_b128 v[168:171], %[l0] offset:10240\n\t" "ds_read_b128 v[172:175], %[l0] offset:11264\n\t" "s_nop 1\n\t" \
    "v_cmp_u_f32 vcc, v232, v233\n\t" "v_cmp_u_f32 s[62:63], v234, v235\n\t" "s_waitcnt lgkmcnt(11)\n\t" \
    "v_smfmac_f32_16x16x64_bf16 v[232:235], v[16:19], a[48:55], %[ix]\n\t" "v_smfmac_f32_16x16x64_bf16 v[236:239], v[16:19], a[56:63], %[ix]\n\t" \
    "s_or_b64 vcc, vcc, s[62:63]\n\t" "s_cbranch_scc1 21f\n\t" "22:\n\t" "s_waitcnt lgkmcnt(10)\n\t" "v_smfmac_f32_16x16x64_bf16 v[232:235], v[20:23], a[64:71], %[ix]\n\t" \
    "v_smfmac_f32_16x16x64_bf16 v[236:239], v[20:23], v[176:183], %[ix]\n\t" "s_waitcnt lgkmcnt(4)\n\t" "s_barrier\n\t" XT(88, 89) "ds_read_b32 v225, v227\n\t" \
    "v_smfmac_f32_16x16x64_bf16 v[232:235], v[24:27], a[72:79], %[ix]\n\t" "ds_read_b128 v[176:179], %[l0] offset:12288\n\t" "ds_read_b128 v[180:183], %[l0] offset:13312\n\t" \
    "v_smfmac_f32_16x16x64_bf16 v[236:239], v[24:27], a[80:87], %[ix]\n\t" "ds_read_b128 v[248:251], %[l1] offset:10240\n\t" "ds_read_b128 v[252:255], %[l1] offset:14336\n\t" \
    "v_smfmac_f32_16x16x64_bf16 v[232:235], v[28:31], a[88:95], %[ix]\n\t" "ds_read_b128 v[16:19], %[l1] offset:2048\n\t" "ds_read_b128 v[20:23], %[l1] offset:11264\n\t" \
    "v_smfmac_f32_16x16x64_bf16 v[236:239], v[28:31], v[184:191], %[ix]\n\t" "ds_read_b128 v[24:27], %[l1] offset:15360\n\t" "ds_read_b128 v[184:187], %[l0] offset:14336\n\t" \
    "v_smfmac_f32_16x16x64_bf16 v[232:235], v[240:243], a[96:103], %[ix]\n\t" "ds_read_b128 v[188:191], %[l0] offset:15360\n\t" "ds_read_b128 v[28:31], %[l1] offset:3072\n\t" \
    "v_smfmac_f32_16x16x64_bf16 v[236:239], v[240:243], a[104:111], %[ix]\n\t" "s_waitcnt lgkmcnt(10)\n\t" "v_cmp_eq_u32 vcc, %[rt], v225\n\t" \
    "v_smfmac_f32_16x16x64_bf16 v[232:235], v[244:247], a[112:119], %[ix]\n\t" "s_cbranch_vccnz 23f\n\t" \
    "v_smfmac_f32_16x16x64_bf16 v[236:239], v[244:247], v[160:167], %[ix]\n\t" "ds_read_b128 v[160:163], %[l0] offset:16384\n\t" \
    "ds_read_b128 v[164:167], %[l0] offset:17408\n\t" "s_waitcnt lgkmcnt(9)\n\t" "v_smfmac_f32_16x16x64_bf16 v[232:235], v[248:251], a[120:127], %[ix]\n\t" \
    "v_smfmac_f32_16x16x64_bf16 v[236:239], v[248:251], a[128:135], %[ix]\n\t" "s_waitcnt lgkmcnt(8)\n\t" \
    "v_smfmac_f32_16x16x64_bf16 v[232:235], v[252:255], a[136:143], %[ix]\n\t" "v_smfmac_f32_16x16x64_bf16 v[236:239], v[252:255], v[168:175], %[ix]\n\t" \
    "ds_read_b128 v[168:171], %[l0] offset:18432\n\t" "ds_read_b128 v[172:175], %[l0] offset:19456\n\t" "s_waitcnt lgkmcnt(9)\n\t" \
    "v_smfmac_f32_16x16x64_bf16 v[232:235], v[16:19], a[144:151], %[ix]\n\t" "v_smfmac_f32_16x16x64_bf16 v[236:239], v[16:19], a[152:159], %[ix]\n\t" \
    "s_waitcnt lgkmcnt(8)\n\t" "v_smfmac_f32_16x16x64_bf16 v[232:235], v[20:23], a[160:167], %[ix]\n\t" \
    "v_smfmac_f32_16x16x64_bf16 v[236:239], v[20:23], v[176:183], %[ix]\n\t" "ds_read_b128 v[176:179], %[l0] offset:20480\n\t" \
    "ds_read_b128 v[180:183], %[l0] offset:21504\n\t" "s_waitcnt lgkmcnt(9)\n\t" "v_smfmac_f32_16x16x64_bf16 v[232:235], v[24:27], a[168:175], %[ix]\n\t" \
    "v_smfmac_f32_16x16x64_bf16 v[236:239], v[24:27], a[176:183], %[ix]\n\t" "s_waitcnt lgkmcnt(6)\n\t" \
    "v_smfmac_f32_16x16x64_bf16 v[232:235], v[28:31], a[184:191], %[ix]\n\t" "v_smfmac_f32_16x16x64_bf16 v[236:239], v[28:31], v[184:191], %[ix]\n\t" \
    "ds_read_b128 v[184:187], %[l0] offset:22528\n\t" "ds_read_b128 v[188:191], %[l0] offset:23552\n\t" XT(90, 91) "s_nop 4\n\t" "v_add_f32 v232, v232, v233\n\t" \
    "v_add_f32 v234, v234, v235\n\t" "v_add_f32 v236, v236, v237\n\t" "v_add_f32 v238, v238, v239\n\t" "v_cvt_pk_bf16_f32 v236, v232, v236\n\t" \
    "v_cvt_pk_bf16_f32 v237, v234, v238\n\t" "ds_write2_b32 %[vw], v236, v237 offset1:68\n\t" "s_waitcnt lgkmcnt(0)\n\t" "s_barrier\n\t" XT(92, 93) \
    "ds_read_b128 v[240:243], %[vr] offset:0\n\t" "ds_read_b128 v[244:247], %[vr] offset:64\n\t" "ds_read_b128 v[248:251], %[vr] offset:128\n\t" \
    "ds_read_b128 v[252:255], %[vr] offset:192\n\t" "s_mov_b32 s61, %[ub]\n\t" "s_waitcnt lgkmcnt(3)\n\t" \
    "v_smfmac_f32_16x16x64_bf16 v[192:195], v[240:243], v[32:39], %[ix]\n\t" "v_smfmac_f32_16x16x64_bf16 v[196:199], v[240:243], v[48:55], %[ix]\n\t" \
    "s_waitcnt lgkmcnt(2)\n\t" "v_smfmac_f32_16x16x64_bf16 v[192:195], v[244:247], v[40:47], %[ix]\n\t" \
    "v_smfmac_f32_16x16x64_bf16 v[196:199], v[244:247], v[56:63], %[ix]\n\t" "s_waitcnt lgkmcnt(1)\n\t" \
    "v_smfmac_f32_16x16x64_bf16 v[192:195], v[248:251], a[192:199], %[ix]\n\t" "v_smfmac_f32_16x16x64_bf16 v[196:199], v[248:251], a[200:207], %[ix]\n\t" \
    "s_waitcnt lgkmcnt(0)\n\t" "v_smfmac_f32_16x16x64_bf16 v[192:195], v[252:255], v[160:167], %[ix]\n\t" "ds_read_b128 v[160:163], %[l0] offset:24576\n\t" \
    "ds_read_b128 v[164:167], %[l0] offset:25600\n\t" "v_smfmac_f32_16x16x64_bf16 v[196:199], v[252:255], v[168:175], %[ix]\n\t" \
    "ds_read_b128 v[168:171], %[l0] offset:26624\n\t" "ds_read_b128 v[172:175], %[l0] offset:27648\n\t" \
    "v_smfmac_f32_16x16x64_bf16 v[200:203], v[240:243], v[64:71], %[ix]\n\t" "s_nop 1\n\t" "v_smfmac_f32_16x16x64_bf16 v[204:207], v[240:243], v[80:87], %[ix]\n\t" \
    "v_cvt_pk_bf16_f32 v224, v192, v194\n\t" "v_cvt_pk_bf16_f32 v225, v193, v195\n\t" "v_smfmac_f32_16x16x64_bf16 v[200:203], v[244:247], v[72:79], %[ix]\n\t" \
    "v_cvt_pk_bf16_f32 v226, v196, v198\n\t" "v_cvt_pk_bf16_f32 v227, v197, v199\n\t" "v_smfmac_f32_16x16x64_bf16 v[204:207], v[244:247], v[88:95], %[ix]\n\t" "s_nop 0\n\t" \
    "v_permlane32_swap_b32 v224, v226\n\t" "v_smfmac_f32_16x16x64_bf16 v[200:203], v[248:251], a[208:215], %[ix]\n\t" "v_permlane32_swap_b32 v225, v227\n\t" "s_nop 0\n\t" \
    "v_smfmac_f32_16x16x64_bf16 v[204:207], v[248:251], a[216:223], %[ix]\n\t" "v_permlane16_swap_b32 v224, v225\n\t" "v_permlane16_swap_b32 v226, v227\n\t" \
    "v_smfmac_f32_16x16x64_bf16 v[200:203], v[252:255], v[176:183], %[ix]\n\t" "ds_read_b128 v[176:179], %[l0] offset:28672\n\t" \
    "buffer_store_dwordx4 v[224:227], %[uo], %[rs], s61 offen sc1\n\t" "s_xor_b32 s61, s61, 0x40000\n\t" XT(94, 95) \
    "v_smfmac_f32_16x16x64_bf16 v[204:207], v[252:255], v[184:191], %[ix]\n\t" "ds_read_b128 v[180:183], %[l0] offset:29696\n\t" \
    "ds_read_b128 v[184:187], %[l0] offset:30720\n\t" "v_smfmac_f32_16x16x64_bf16 v[208:211], v[240:243], v[96:103], %[ix]\n\t" \
    "ds_read_b128 v[188:191], %[l0] offset:31744\n\t" "s_nop 0\n\t" "v_smfmac_f32_16x16x64_bf16 v[212:215], v[240:243], v[112:119], %[ix]\n\t" \
    "v_cvt_pk_bf16_f32 v228, v200, v202\n\t" "v_cvt_pk_bf16_f32 v229, v201, v203\n\t" "v_smfmac_f32_16x16x64_bf16 v[208:211], v[244:247], v[104:111], %[ix]\n\t" \
    "v_cvt_pk_bf16_f32 v230, v204, v206\n\t" "v_cvt_pk_bf16_f32 v231, v205, v207\n\t" "v_smfmac_f32_16x16x64_bf16 v[212:215], v[244:247], v[120:127], %[ix]\n\t" "s_nop 0\n\t" \
    "v_permlane32_swap_b32 v228, v230\n\t" "v_smfmac_f32_16x16x64_bf16 v[208:211], v[248:251], a[224:231], %[ix]\n\t" "v_permlane32_swap_b32 v229, v231\n\t" "s_nop 0\n\t" \
    "v_smfmac_f32_16x16x64_bf16 v[212:215], v[248:251], a[232:239], %[ix]\n\t" "v_permlane16_swap_b32 v228, v229\n\t" "v_permlane16_swap_b32 v230, v231\n\t" \
    "s_waitcnt lgkmcnt(6)\n\t" "v_smfmac_f32_16x16x64_bf16 v[208:211], v[252:255], v[160:167], %[ix]\n\t" "buffer_store_dwordx4 v[228:231], %[uo], %[rs], s61 offen sc1\n\t" \
    "s_xor_b32 s61, s61, 0xc0000\n\t" "s_waitcnt lgkmcnt(4)\n\t" "v_smfmac_f32_16x16x64_bf16 v[212:215], v[252:255], v[168:175], %[ix]\n\t" "s_nop 1\n\t" \
    "v_smfmac_f32_16x16x64_bf16 v[216:219], v[240:243], v[128:135], %[ix]\n\t" "s_nop 1\n\t" "v_smfmac_f32_16x16x64_bf16 v[220:223], v[240:243], v[144:151], %[ix]\n\t" \
    "v_cvt_pk_bf16_f32 v224, v208, v210\n\t" "v_cvt_pk_bf16_f32 v225, v209, v211\n\t" "v_smfmac_f32_16x16x64_bf16 v[216:219], v[244:247], v[136:143], %[ix]\n\t" \
    "v_cvt_pk_bf16_f32 v226, v212, v214\n\t" "v_cvt_pk_bf16_f32 v227, v213, v215\n\t" "v_smfmac_f32_16x16x64_bf16 v[220:223], v[244:247], v[152:159], %[ix]\n\t" "s_nop 0\n\t" \
    "v_permlane32_swap_b32 v224, v226\n\t" "v_smfmac_f32_16x16x64_bf16 v[216:219], v[248:251], a[240:247], %[ix]\n\t" "v_permlane32_swap_b32 v225, v227\n\t" "s_nop 0\n\t" \
    "v_smfmac_f32_16x16x64_bf16 v[220:223], v[248:251], a[248:255], %[ix]\n\t" "v_permlane16_swap_b32 v224, v225\n\t" "v_permlane16_swap_b32 v226, v227\n\t" \
    "s_waitcnt lgkmcnt(2)\n\t" "v_smfmac_f32_16x16x64_bf16 v[216:219], v[252:255], v[176:183], %[ix]\n\t" "buffer_store_dwordx4 v[224:227], %[uo], %[rs], s61 offen sc1\n\t" \
    "s_xor_b32 s61, s61, 0x40000\n\t" "s_waitcnt lgkmcnt(0)\n\t" "v_smfmac_f32_16x16x64_bf16 v[220:223], v[252:255], v[184:191], %[ix]\n\t" "s_nop 5\n\t" \
    "v_cvt_pk_bf16_f32 v228, v216, v218\n\t" "v_cvt_pk_bf16_f32 v229, v217, v219\n\t" "v_cvt_pk_bf16_f32 v230, v220, v222\n\t" "v_cvt_pk_bf16_f32 v231, v221, v223\n\t" \
    "s_nop 0\n\t" "v_permlane32_swap_b32 v228, v230\n\t" "v_permlane32_swap_b32 v229, v231\n\t" "s_nop 1\n\t" "v_permlane16_swap_b32 v228, v229\n\t" \
    "v_permlane16_swap_b32 v230, v231\n\t" "buffer_store_dwordx4 v[228:231], %[uo], %[rs], s61 offen sc1\n\t" "s_nop 1\n\t" XT(96, 97) "v_mov_b64 v[16:17], -1\n\t" \
    "v_mov_b64 v[18:19], -1\n\t" "s_barrier\n\t" "buffer_store_dwordx4 v[16:19], %[l1], %[rs], %[ur] offen offset:0 sc1\n\t" \
    "buffer_store_dwordx4 v[16:19], %[l1], %[rs], %[ur] offen offset:1024 sc1\n\t" "buffer_store_dwordx4 v[16:19], %[l1], %[rs], %[ur] offen offset:2048 sc1\n\t" \
    "buffer_store_dwordx4 v[16:19], %[l1], %[rs], %[ur] offen offset:3072 sc1\n\t" "s_sub_u32 s61, %[ur], 0x1000\n\t" \
    "buffer_store_dwordx4 v[16:19], %[l1], %[rs], s61 offen offset:0 sc1\n\t" "buffer_store_dwordx4 v[16:19], %[l1], %[rs], s61 offen offset:1024 sc1\n\t" "s_branch 99f\n\t" \
    "21:\n\t" "ds_read_b128 v[228:231], %[l1] offset:4096\n\t" "s_waitcnt lgkmcnt(0)\n\t" "v_max3_u32 v225, v228, v229, v230\n\t" "v_max_u32 v225, v225, v231\n\t" \
    "ds_read_b128 v[228:231], %[l1] offset:5120\n\t" "s_waitcnt lgkmcnt(0)\n\t" "v_max3_u32 v225, v225, v228, v229\n\t" "v_max3_u32 v225, v225, v230, v231\n\t" \
    "ds_read_b128 v[228:231], %[l1] offset:6144\n\t" "s_waitcnt lgkmcnt(0)\n\t" "v_max3_u32 v225, v225, v228, v229\n\t" "v_max3_u32 v225, v225, v230, v231\n\t" \
    "ds_read_b128 v[228:231], %[l1] offset:7168\n\t" "s_waitcnt lgkmcnt(0)\n\t" "v_max3_u32 v225, v225, v228, v229\n\t" "v_max3_u32 v225, v225, v230, v231\n\t" \
    "v_cmp_eq_u32 vcc, -1, v225\n\t" "s_cbranch_vccz 22b\n\t" "s_cmp_eq_u32 %[rt], 1\n\t" "s_cbranch_scc1 22b\n\t" "v_mov_b32 v225, %[rt]\n\t" "ds_write_b32 v227, v225\n\t" \
    "s_waitcnt lgkmcnt(0)\n\t" "s_branch 22b\n\t" "23:\n\t" "s_sub_u32 %[rt], %[rt], 1\n\t" "s_waitcnt lgkmcnt(0)\n\t" XCNT(100) "24:\n\t" \
    "buffer_load_dwordx4 v[240:243], %[mo], %[rs], %[sb] offen sc1\n\t" "buffer_load_dwordx4 v[244:247], v224, %[rs], %[sb] offen sc1\n\t" \
    "buffer_load_dwordx4 v[248:251], %[mo], %[rs], s60 offen sc1\n\t" "buffer_load_dwordx4 v[252:255], v224, %[rs], s60 offen sc1\n\t" "s_waitcnt vmcnt(0)\n\t" \
    "v_max_u32 v225, v240, v241\n\t" "v_max3_u32 v225, v225, v242, v243\n\t" "v_max3_u32 v225, v225, v244, v245\n\t" "v_max3_u32 v225, v225, v246, v247\n\t" \
    "v_max3_u32 v225, v225, v248, v249\n\t" "v_max3_u32 v225, v225, v250, v251\n\t" "v_max3_u32 v225, v225, v252, v253\n\t" "v_max3_u32 v225, v225, v254, v255\n\t" \
    "v_cmp_eq_u32 vcc, -1, v225\n\t" "s_cbranch_vccz 25f\n\t" "s_cmp_eq_u32 %[bd], 0\n\t" "s_cbranch_scc1 25f\n\t" "s_sub_u32 %[bd], %[bd], 1\n\t" XCNT(101) \
    "s_branch 24b\n\t" "25:\n\t" "ds_read_b128 v[160:163], %[l0] offset:0\n\t" "ds_read_b128 v[164:167], %[l0] offset:1024\n\t" \
    "ds_read_b128 v[168:171], %[l0] offset:2048\n\t" "ds_read_b128 v[172:175], %[l0] offset:3072\n\t" "ds_read_b128 v[176:179], %[l0] offset:4096\n\t" \
    "ds_read_b128 v[180:183], %[l0] offset:5120\n\t" "ds_read_b128 v[184:187], %[l0] offset:6144\n\t" "ds_read_b128 v[188:191], %[l0] offset:7168\n\t" \
    "v_mov_b64 v[232:233], 0\n\t" "v_mov_b64 v[234:235], 0\n\t" "v_mov_b64 v[236:237], 0\n\t" "v_mov_b64 v[238:239], 0\n\t" "s_barrier\n\t" "s_branch 20b\n\t" "92:\n\t" \
    XT(80, 81) "s_barrier\n\t" XT(82, 83) "buffer_load_dwordx4 v[240:243], %[mo], %[rs], %[sb] offen sc1\n\t" "v_mov_b64 v[232:233], 0\n\t" "v_mov_b64 v[234:235], 0\n\t" \
    "v_mov_b64 v[236:237], 0\n\t" "v_mov_b64 v[238:239], 0\n\t" "v_mov_b64 v[192:193], 0\n\t" "v_mov_b64 v[194:195], 0\n\t" "v_mov_b64 v[196:197], 0\n\t" \
    "v_mov_b64 v[198:199], 0\n\t" "buffer_load_dwordx4 v[244:247], v224, %[rs], %[sb] offen sc1\n\t" "ds_read_b128 v[160:163], %[l0] offset:0\n\t" \
    "ds_read_b128 v[164:167], %[l0] offset:1024\n\t" "ds_read_b128 v[168:171], %[l0] offset:2048\n\t" "ds_read_b128 v[172:175], %[l0] offset:3072\n\t" "s_nop 3\n\t" \
    "buffer_load_dwordx4 v[248:251], %[mo], %[rs], s60 offen sc1\n\t" "ds_read_b128 v[176:179], %[l0] offset:4096\n\t" "ds_read_b128 v[180:183], %[l0] offset:5120\n\t" \
    "ds_read_b128 v[184:187], %[l0] offset:6144\n\t" "ds_read_b128 v[188:191], %[l0] offset:7168\n\t" "s_nop 3\n\t" \
    "buffer_load_dwordx4 v[252:255], v224, %[rs], s60 offen sc1\n\t" "v_mov_b64 v[200:201], 0\n\t" "v_mov_b64 v[202:203], 0\n\t" "v_mov_b64 v[204:205], 0\n\t" \
    "v_mov_b64 v[206:207], 0\n\t" "v_mov_b64 v[208:209], 0\n\t" "v_mov_b64 v[210:211], 0\n\t" "v_mov_b64 v[212:213], 0\n\t" "v_mov_b64 v[214:215], 0\n\t" \
    "v_mov_b64 v[216:217], 0\n\t" "v_mov_b64 v[218:219], 0\n\t" "v_mov_b64 v[220:221], 0\n\t" "v_mov_b64 v[222:223], 0\n\t" "v_mov_b32 v227, 160512\n\t" "30:\n\t" \
    "s_waitcnt vmcnt(3)\n\t" XT(84, 85) "v_smfmac_f32_16x16x64_bf16 v[232:235], v[240:243], a[0:7], %[ix]\n\t" "ds_write_b128 %[l1], v[240:243] offset:8192\n\t" \
    "v_smfmac_f32_16x16x64_bf16 v[236:239], v[240:243], a[8:15], %[ix]\n\t" "s_waitcnt vmcnt(2)\n\t" "v_smfmac_f32_16x16x64_bf16 v[232:235], v[244:247], a[16:23], %[ix]\n\t" \
    "ds_write_b128 %[l1], v[244:247] offset:9216\n\t" "s_waitcnt lgkmcnt(8)\n\t" "v_smfmac_f32_16x16x64_bf16 v[236:239], v[244:247], v[160:167], %[ix]\n\t" \
    "s_waitcnt lgkmcnt(0)\n\t" "s_barrier\n\t" XT(86, 87) "ds_read_b128 v[16:19], %[l1] offset:12288\n\t" "ds_read_b128 v[20:23], %[l1] offset:0\n\t" \
    "ds_read_b128 v[24:27], %[l1] offset:4096\n\t" "s_waitcnt vmcnt(1)\n\t" "v_smfmac_f32_16x16x64_bf16 v[232:235], v[248:251], a[24:31], %[ix]\n\t" \
    "ds_write_b128 %[l1], v[248:251] offset:10240\n\t" "v_smfmac_f32_16x16x64_bf16 v[236:239], v[248:251], a[32:39], %[ix]\n\t" \
    "ds_read_b128 v[28:31], %[l1] offset:13312\n\t" "ds_read_b128 v[240:243], %[l1] offset:1024\n\t" "ds_read_b128 v[244:247], %[l1] offset:5120\n\t" "s_waitcnt vmcnt(0)\n\t" \
    "v_smfmac_f32_16x16x64_bf16 v[232:235], v[252:255], a[40:47], %[ix]\n\t" "ds_write_b128 %[l1], v[252:255] offset:11264\n\t" \
    "v_smfmac_f32_16x16x64_bf16 v[236:239], v[252:255], v[168:175], %[ix]\n\t" "ds_read_b128 v[160:163], %[l0] offset:8192\n\t" \
    "ds_read_b128 v[164:167], %[l0] offset:9216\n\t" "ds_read_b128 v[168:171], %[l0] offset:10240\n\t" "ds_read_b128 v[172:175], %[l0] offset:11264\n\t" "s_nop 1\n\t" \
    "v_cmp_u_f32 vcc, v232, v233\n\t" "v_cmp_u_f32 s[62:63], v234, v235\n\t" "s_waitcnt lgkmcnt(11)\n\t" \
    "v_smfmac_f32_16x16x64_bf16 v[232:235], v[16:19], a[48:55], %[ix]\n\t" "v_smfmac_f32_16x16x64_bf16 v[236:239], v[16:19], a[56:63], %[ix]\n\t" \
    "s_or_b64 vcc, vcc, s[62:63]\n\t" "s_cbranch_scc1 31f\n\t" "32:\n\t" "s_waitcnt lgkmcnt(10)\n\t" "v_smfmac_f32_16x16x64_bf16 v[232:235], v[20:23], a[64:71], %[ix]\n\t" \
    "v_smfmac_f32_16x16x64_bf16 v[236:239], v[20:23], v[176:183], %[ix]\n\t" "s_waitcnt lgkmcnt(4)\n\t" "s_barrier\n\t" XT(88, 89) "ds_read_b32 v225, v227\n\t" \
    "v_smfmac_f32_16x16x64_bf16 v[232:235], v[24:27], a[72:79], %[ix]\n\t" "ds_read_b128 v[176:179], %[l0] offset:12288\n\t" "ds_read_b128 v[180:183], %[l0] offset:13312\n\t" \
    "v_smfmac_f32_16x16x64_bf16 v[236:239], v[24:27], a[80:87], %[ix]\n\t" "ds_read_b128 v[248:251], %[l1] offset:14336\n\t" "ds_read_b128 v[252:255], %[l1] offset:2048\n\t" \
    "v_smfmac_f32_16x16x64_bf16 v[232:235], v[28:31], a[88:95], %[ix]\n\t" "ds_read_b128 v[16:19], %[l1] offset:6144\n\t" "ds_read_b128 v[20:23], %[l1] offset:15360\n\t" \
    "v_smfmac_f32_16x16x64_bf16 v[236:239], v[28:31], v[184:191], %[ix]\n\t" "ds_read_b128 v[24:27], %[l1] offset:3072\n\t" "ds_read_b128 v[184:187], %[l0] offset:14336\n\t" \
    "v_smfmac_f32_16x16x64_bf16 v[232:235], v[240:243], a[96:103], %[ix]\n\t" "ds_read_b128 v[188:191], %[l0] offset:15360\n\t" "ds_read_b128 v[28:31], %[l1] offset:7168\n\t" \
    "v_smfmac_f32_16x16x64_bf16 v[236:239], v[240:243], a[104:111], %[ix]\n\t" "s_waitcnt lgkmcnt(10)\n\t" "v_cmp_eq_u32 vcc, %[rt], v225\n\t" \
    "v_smfmac_f32_16x16x64_bf16 v[232:235], v[244:247], a[112:119], %[ix]\n\t" "s_cbranch_vccnz 33f\n\t" \
    "v_smfmac_f32_16x16x64_bf16 v[236:239], v[244:247], v[160:167], %[ix]\n\t" "ds_read_b128 v[160:163], %[l0] offset:16384\n\t" \
    "ds_read_b128 v[164:167], %[l0] offset:17408\n\t" "s_waitcnt lgkmcnt(9)\n\t" "v_smfmac_f32_16x16x64_bf16 v[232:235], v[248:251], a[120:127], %[ix]\n\t" \
    "v_smfmac_f32_16x16x64_bf16 v[236:239], v[248:251], a[128:135], %[ix]\n\t" "s_waitcnt lgkmcnt(8)\n\t" \
    "v_smfmac_f32_16x16x64_bf16 v[232:235], v[252:255], a[136:143], %[ix]\n\t" "v_smfmac_f32_16x16x64_bf16 v[236:239], v[252:255], v[168:175], %[ix]\n\t" \
    "ds_read_b128 v[168:171], %[l0] offset:18432\n\t" "ds_read_b128 v[172:175], %[l0] offset:19456\n\t" "s_waitcnt lgkmcnt(9)\n\t" \
    "v_smfmac_f32_16x16x64_bf16 v[232:235], v[16:19], a[144:151], %[ix]\n\t" "v_smfmac_f32_16x16x64_bf16 v[236:239], v[16:19], a[152:159], %[ix]\n\t" \
    "s_waitcnt lgkmcnt(8)\n\t" "v_smfmac_f32_16x16x64_bf16 v[232:235], v[20:23], a[160:167], %[ix]\n\t" \
    "v_smfmac_f32_16x16x64_bf16 v[236:239], v[20:23], v[176:183], %[ix]\n\t" "ds_read_b128 v[176:179], %[l0] offset:20480\n\t" \
    "ds_read_b128 v[180:183], %[l0] offset:21504\n\t" "s_waitcnt lgkmcnt(9)\n\t" "v_smfmac_f32_16x16x64_bf16 v[232:235], v[24:27], a[168:175], %[ix]\n\t" \
    "v_smfmac_f32_16x16x64_bf16 v[236:239], v[24:27], a[176:183], %[ix]\n\t" "s_waitcnt lgkmcnt(6)\n\t" \
    "v_smfmac_f32_16x16x64_bf16 v[232:235], v[28:31], a[184:191], %[ix]\n\t" "v_smfmac_f32_16x16x64_bf16 v[236:239], v[28:31], v[184:191], %[ix]\n\t" \
    "ds_read_b128 v[184:187], %[l0] offset:22528\n\t" "ds_read_b128 v[188:191], %[l0] offset:23552\n\t" XT(90, 91) "s_nop 4\n\t" "v_add_f32 v232, v232, v233\n\t" \
    "v_add_f32 v234, v234, v235\n\t" "v_add_f32 v236, v236, v237\n\t" "v_add_f32 v238, v238, v239\n\t" "v_cvt_pk_bf16_f32 v236, v232, v236\n\t" \
    "v_cvt_pk_bf16_f32 v237, v234, v238\n\t" "ds_write2_b32 %[vw], v236, v237 offset1:68\n\t" "s_waitcnt lgkmcnt(0)\n\t" "s_barrier\n\t" XT(92, 93) \
    "ds_read_b128 v[240:243], %[vr] offset:0\n\t" "ds_read_b128 v[244:247], %[vr] offset:64\n\t" "ds_read_b128 v[248:251], %[vr] offset:128\n\t" \
    "ds_read_b128 v[252:255], %[vr] offset:192\n\t" "s_mov_b32 s61, %[ub]\n\t" "s_waitcnt lgkmcnt(3)\n\t" \
    "v_smfmac_f32_16x16x64_bf16 v[192:195], v[240:243], v[32:39], %[ix]\n\t" "v_smfmac_f32_16x16x64_bf16 v[196:199], v[240:243], v[48:55], %[ix]\n\t" \
    "s_waitcnt lgkmcnt(2)\n\t" "v_smfmac_f32_16x16x64_bf16 v[192:195], v[244:247], v[40:47], %[ix]\n\t" \
    "v_smfmac_f32_16x16x64_bf16 v[196:199], v[244:247], v[56:63], %[ix]\n\t" "s_waitcnt lgkmcnt(1)\n\t" \
    "v_smfmac_f32_16x16x64_bf16 v[192:195], v[248:251], a[192:199], %[ix]\n\t" "v_smfmac_f32_16x16x64_bf16 v[196:199], v[248:251], a[200:207], %[ix]\n\t" \
    "s_waitcnt lgkmcnt(0)\n\t" "v_smfmac_f32_16x16x64_bf16 v[192:195], v[252:255], v[160:167], %[ix]\n\t" "ds_read_b128 v[160:163], %[l0] offset:24576\n\t" \
    "ds_read_b128 v[164:167], %[l0] offset:25600\n\t" "v_smfmac_f32_16x16x64_bf16 v[196:199], v[252:255], v[168:175], %[ix]\n\t" \
    "ds_read_b128 v[168:171], %[l0] offset:26624\n\t" "ds_read_b128 v[172:175], %[l0] offset:27648\n\t" \
    "v_smfmac_f32_16x16x64_bf16 v[200:203], v[240:243], v[64:71], %[ix]\n\t" "s_nop 1\n\t" "v_smfmac_f32_16x16x64_bf16 v[204:207], v[240:243], v[80:87], %[ix]\n\t" \
    "v_cvt_pk_bf16_f32 v224, v192, v194\n\t" "v_cvt_pk_bf16_f32 v225, v193, v195\n\t" "v_smfmac_f32_16x16x64_bf16 v[200:203], v[244:247], v[72:79], %[ix]\n\t" \
    "v_cvt_pk_bf16_f32 v226, v196, v198\n\t" "v_cvt_pk_bf16_f32 v227, v197, v199\n\t" "v_smfmac_f32_16x16x64_bf16 v[204:207], v[244:247], v[88:95], %[ix]\n\t" "s_nop 0\n\t" \
    "v_permlane32_swap_b32 v224, v226\n\t" "v_smfmac_f32_16x16x64_bf16 v[200:203], v[248:251], a[208:215], %[ix]\n\t" "v_permlane32_swap_b32 v225, v227\n\t" "s_nop 0\n\t" \
    "v_smfmac_f32_16x16x64_bf16 v[204:207], v[248:251], a[216:223], %[ix]\n\t" "v_permlane16_swap_b32 v224, v225\n\t" "v_permlane16_swap_b32 v226, v227\n\t" \
    "v_smfmac_f32_16x16x64_bf16 v[200:203], v[252:255], v[176:183], %[ix]\n\t" "ds_read_b128 v[176:179], %[l0] offset:28672\n\t" \
    "buffer_store_dwordx4 v[224:227], %[uo], %[rs], s61 offen sc1\n\t" "s_xor_b32 s61, s61, 0x40000\n\t" XT(94, 95) \
    "v_smfmac_f32_16x16x64_bf16 v[204:207], v[252:255], v[184:191], %[ix]\n\t" "ds_read_b128 v[180:183], %[l0] offset:29696\n\t" \
    "ds_read_b128 v[184:187], %[l0] offset:30720\n\t" "v_smfmac_f32_16x16x64_bf16 v[208:211], v[240:243], v[96:103], %[ix]\n\t" \
    "ds_read_b128 v[188:191], %[l0] offset:31744\n\t" "s_nop 0\n\t" "v_smfmac_f32_16x16x64_bf16 v[212:215], v[240:243], v[112:119], %[ix]\n\t" \
    "v_cvt_pk_bf16_f32 v228, v200, v202\n\t" "v_cvt_pk_bf16_f32 v229, v201, v203\n\t" "v_smfmac_f32_16x16x64_bf16 v[208:211], v[244:247], v[104:111], %[ix]\n\t" \
    "v_cvt_pk_bf16_f32 v230, v204, v206\n\t" "v_cvt_pk_bf16_f32 v231, v205, v207\n\t" "v_smfmac_f32_16x16x64_bf16 v[212:215], v[244:247], v[120:127], %[ix]\n\t" "s_nop 0\n\t" \
    "v_permlane32_swap_b32 v228, v230\n\t" "v_smfmac_f32_16x16x64_bf16 v[208:211], v[248:251], a[224:231], %[ix]\n\t" "v_permlane32_swap_b32 v229, v231\n\t" "s_nop 0\n\t" \
    "v_smfmac_f32_16x16x64_bf16 v[212:215], v[248:251], a[232:239], %[ix]\n\t" "v_permlane16_swap_b32 v228, v229\n\t" "v_permlane16_swap_b32 v230, v231\n\t" \
    "s_waitcnt lgkmcnt(6)\n\t" "v_smfmac_f32_16x16x64_bf16 v[208:211], v[252:255], v[160:167], %[ix]\n\t" "buffer_store_dwordx4 v[228:231], %[uo], %[rs], s61 offen sc1\n\t" \
    "s_xor_b32 s61, s61, 0xc0000\n\t" "s_waitcnt lgkmcnt(4)\n\t" "v_smfmac_f32_16x16x64_bf16 v[212:215], v[252:255], v[168:175], %[ix]\n\t" "s_nop 1\n\t" \
    "v_smfmac_f32_16x16x64_bf16 v[216:219], v[240:243], v[128:135], %[ix]\n\t" "s_nop 1\n\t" "v_smfmac_f32_16x16x64_bf16 v[220:223], v[240:243], v[144:151], %[ix]\n\t" \
    "v_cvt_pk_bf16_f32 v224, v208, v210\n\t" "v_cvt_pk_bf16_f32 v225, v209, v211\n\t" "v_smfmac_f32_16x16x64_bf16 v[216:219], v[244:247], v[136:143], %[ix]\n\t" \
    "v_cvt_pk_bf16_f32 v226, v212, v214\n\t" "v_cvt_pk_bf16_f32 v227, v213, v215\n\t" "v_smfmac_f32_16x16x64_bf16 v[220:223], v[244:247], v[152:159], %[ix]\n\t" "s_nop 0\n\t" \
    "v_permlane32_swap_b32 v224, v226\n\t" "v_smfmac_f32_16x16x64_bf16 v[216:219], v[248:251], a[240:247], %[ix]\n\t" "v_permlane32_swap_b32 v225, v227\n\t" "s_nop 0\n\t" \
    "v_smfmac_f32_16x16x64_bf16 v[220:223], v[248:251], a[248:255], %[ix]\n\t" "v_permlane16_swap_b32 v224, v225\n\t" "v_permlane16_swap_b32 v226, v227\n\t" \
    "s_waitcnt lgkmcnt(2)\n\t" "v_smfmac_f32_16x16x64_bf16 v[216:219], v[252:255], v[176:183], %[ix]\n\t" "buffer_store_dwordx4 v[224:227], %[uo], %[rs], s61 offen sc1\n\t" \
    "s_xor_b32 s61, s61, 0x40000\n\t" "s_waitcnt lgkmcnt(0)\n\t" "v_smfmac_f32_16x16x64_bf16 v[220:223], v[252:255], v[184:191], %[ix]\n\t" "s_nop 5\n\t" \
    "v_cvt_pk_bf16_f32 v228, v216, v218\n\t" "v_cvt_pk_bf16_f32 v229, v217, v219\n\t" "v_cvt_pk_bf16_f32 v230, v220, v222\n\t" "v_cvt_pk_bf16_f32 v231, v221, v223\n\t" \
    "s_nop 0\n\t" "v_permlane32_swap_b32 v228, v230\n\t" "v_permlane32_swap_b32 v229, v231\n\t" "s_nop 1\n\t" "v_permlane16_swap_b32 v228, v229\n\t" \
    "v_permlane16_swap_b32 v230, v231\n\t" "buffer_store_dwordx4 v[228:231], %[uo], %[rs], s61 offen sc1\n\t" "s_nop 1\n\t" XT(96, 97) "v_mov_b64 v[16:17], -1\n\t" \
    "v_mov_b64 v[18:19], -1\n\t" "s_barrier\n\t" "buffer_store_dwordx4 v[16:19], %[l1], %[rs], %[ur] offen offset:0 sc1\n\t" \
    "buffer_store_dwordx4 v[16:19], %[l1], %[rs], %[ur] offen offset:1024 sc1\n\t" "buffer_store_dwordx4 v[16:19], %[l1], %[rs], %[ur] offen offset:2048 sc1\n\t" \
    "buffer_store_dwordx4 v[16:19], %[l1], %[rs], %[ur] offen offset:3072 sc1\n\t" "s_sub_u32 s61, %[ur], 0x4000\n\t" \
    "buffer_store_dwordx4 v[16:19], %[l1], %[rs], s61 offen offset:2048 sc1\n\t" "s_branch 99f\n\t" "31:\n\t" "ds_read_b128 v[228:231], %[l1] offset:8192\n\t" \
    "s_waitcnt lgkmcnt(0)\n\t" "v_max3_u32 v225, v228, v229, v230\n\t" "v_max_u32 v225, v225, v231\n\t" "ds_read_b128 v[228:231], %[l1] offset:9216\n\t" \
    "s_waitcnt lgkmcnt(0)\n\t" "v_max3_u32 v225, v225, v228, v229\n\t" "v_max3_u32 v225, v225, v230, v231\n\t" "ds_read_b128 v[228:231], %[l1] offset:10240\n\t" \
    "s_waitcnt lgkmcnt(0)\n\t" "v_max3_u32 v225, v225, v228, v229\n\t" "v_max3_u32 v225, v225, v230, v231\n\t" "ds_read_b128 v[228:231], %[l1] offset:11264\n\t" \
    "s_waitcnt lgkmcnt(0)\n\t" "v_max3_u32 v225, v225, v228, v229\n\t" "v_max3_u32 v225, v225, v230, v231\n\t" "v_cmp_eq_u32 vcc, -1, v225\n\t" "s_cbranch_vccz 32b\n\t" \
    "s_cmp_eq_u32 %[rt], 1\n\t" "s_cbranch_scc1 32b\n\t" "v_mov_b32 v225, %[rt]\n\t" "ds_write_b32 v227, v225\n\t" "s_waitcnt lgkmcnt(0)\n\t" "s_branch 32b\n\t" "33:\n\t" \
    "s_sub_u32 %[rt], %[rt], 1\n\t" "s_waitcnt lgkmcnt(0)\n\t" XCNT(100) "34:\n\t" "buffer_load_dwordx4 v[240:243], %[mo], %[rs], %[sb] offen sc1\n\t" \
    "buffer_load_dwordx4 v[244:247], v224, %[rs], %[sb] offen sc1\n\t" "buffer_load_dwordx4 v[248:251], %[mo], %[rs], s60 offen sc1\n\t" \
    "buffer_load_dwordx4 v[252:255], v224, %[rs], s60 offen sc1\n\t" "s_waitcnt vmcnt(0)\n\t" "v_max_u32 v225, v240, v241\n\t" "v_max3_u32 v225, v225, v242, v243\n\t" \
    "v_max3_u32 v225, v225, v244, v245\n\t" "v_max3_u32 v225, v225, v246, v247\n\t" "v_max3_u32 v225, v225, v248, v249\n\t" "v_max3_u32 v225, v225, v250, v251\n\t" \
    "v_max3_u32 v225, v225, v252, v253\n\t" "v_max3_u32 v225, v225, v254, v255\n\t" "v_cmp_eq_u32 vcc, -1, v225\n\t" "s_cbranch_vccz 35f\n\t" "s_cmp_eq_u32 %[bd], 0\n\t" \
    "s_cbranch_scc1 35f\n\t" "s_sub_u32 %[bd], %[bd], 1\n\t" XCNT(101) "s_branch 34b\n\t" "35:\n\t" "ds_read_b128 v[160:163], %[l0] offset:0\n\t" \
    "ds_read_b128 v[164:167], %[l0] offset:1024\n\t" "ds_read_b128 v[168:171], %[l0] offset:2048\n\t" "ds_read_b128 v[172:175], %[l0] offset:3072\n\t" \
    "ds_read_b128 v[176:179], %[l0] offset:4096\n\t" "ds_read_b128 v[180:183], %[l0] offset:5120\n\t" "ds_read_b128 v[184:187], %[l0] offset:6144\n\t" \
    "ds_read_b128 v[188:191], %[l0] offset:7168\n\t" "v_mov_b64 v[232:233], 0\n\t" "v_mov_b64 v[234:235], 0\n\t" "v_mov_b64 v[236:237], 0\n\t" "v_mov_b64 v[238:239], 0\n\t" \
    "s_barrier\n\t" "s_branch 30b\n\t" "93:\n\t" XT(80, 81) "s_barrier\n\t" XT(82, 83) "buffer_load_dwordx4 v[240:243], %[mo], %[rs], %[sb] offen sc1\n\t" \
    "v_mov_b64 v[232:233], 0\n\t" "v_mov_b64 v[234:235], 0\n\t" "v_mov_b64 v[236:237], 0\n\t" "v_mov_b64 v[238:239], 0\n\t" "v_mov_b64 v[192:193], 0\n\t" \
    "v_mov_b64 v[194:195], 0\n\t" "v_mov_b64 v[196:197], 0\n\t" "v_mov_b64 v[198:199], 0\n\t" "buffer_load_dwordx4 v[244:247], v224, %[rs], %[sb] offen sc1\n\t" \
    "ds_read_b128 v[160:163], %[l0] offset:0\n\t" "ds_read_b128 v[164:167], %[l0] offset:1024\n\t" "ds_read_b128 v[168:171], %[l0] offset:2048\n\t" \
    "ds_read_b128 v[172:175], %[l0] offset:3072\n\t" "s_nop 3\n\t" "buffer_load_dwordx4 v[248:251], %[mo], %[rs], s60 offen sc1\n\t" \
    "ds_read_b128 v[176:179], %[l0] offset:4096\n\t" "ds_read_b128 v[180:183], %[l0] offset:5120\n\t" "ds_read_b128 v[184:187], %[l0] offset:6144\n\t" \
    "ds_read_b128 v[188:191], %[l0] offset:7168\n\t" "s_nop 3\n\t" "buffer_load_dwordx4 v[252:255], v224, %[rs], s60 offen sc1\n\t" "v_mov_b64 v[200:201], 0\n\t" \
    "v_mov_b64 v[202:203], 0\n\t" "v_mov_b64 v[204:205], 0\n\t" "v_mov_b64 v[206:207], 0\n\t" "v_mov_b64 v[208:209], 0\n\t" "v_mov_b64 v[210:211], 0\n\t" \
    "v_mov_b64 v[212:213], 0\n\t" "v_mov_b64 v[214:215], 0\n\t" "v_mov_b64 v[216:217], 0\n\t" "v_mov_b64 v[218:219], 0\n\t" "v_mov_b64 v[220:221], 0\n\t" \
    "v_mov_b64 v[222:223], 0\n\t" "v_mov_b32 v227, 160512\n\t" "40:\n\t" "s_waitcnt vmcnt(3)\n\t" XT(84, 85) \
    "v_smfmac_f32_16x16x64_bf16 v[232:235], v[240:243], a[0:7], %[ix]\n\t" "ds_write_b128 %[l1], v[240:243] offset:12288\n\t" \
    "v_smfmac_f32_16x16x64_bf16 v[236:239], v[240:243], a[8:15], %[ix]\n\t" "s_waitcnt vmcnt(2)\n\t" "v_smfmac_f32_16x16x64_bf16 v[232:235], v[244:247], a[16:23], %[ix]\n\t" \
    "ds_write_b128 %[l1], v[244:247] offset:13312\n\t" "s_waitcnt lgkmcnt(8)\n\t" "v_smfmac_f32_16x16x64_bf16 v[236:239], v[244:247], v[160:167], %[ix]\n\t" \
    "s_waitcnt lgkmcnt(0)\n\t" "s_barrier\n\t" XT(86, 87) "ds_read_b128 v[16:19], %[l1] offset:0\n\t" "ds_read_b128 v[20:23], %[l1] offset:4096\n\t" \
    "ds_read_b128 v[24:27], %[l1] offset:8192\n\t" "s_waitcnt vmcnt(1)\n\t" "v_smfmac_f32_16x16x64_bf16 v[232:235], v[248:251], a[24:31], %[ix]\n\t" \
    "ds_write_b128 %[l1], v[248:251] offset:14336\n\t" "v_smfmac_f32_16x16x64_bf16 v[236:239], v[248:251], a[32:39], %[ix]\n\t" "ds_read_b128 v[28:31], %[l1] offset:1024\n\t" \
    "ds_read_b128 v[240:243], %[l1] offset:5120\n\t" "ds_read_b128 v[244:247], %[l1] offset:9216\n\t" "s_waitcnt vmcnt(0)\n\t" \
    "v_smfmac_f32_16x16x64_bf16 v[232:235], v[252:255], a[40:47], %[ix]\n\t" "ds_write_b128 %[l1], v[252:255] offset:15360\n\t" \
    "v_smfmac_f32_16x16x64_bf16 v[236:239], v[252:255], v[168:175], %[ix]\n\t" "ds_read_b128 v[160:163], %[l0] offset:8192\n\t" \
    "ds_read_b128 v[164:167], %[l0] offset:9216\n\t" "ds_read_b128 v[168:171], %[l0] offset:10240\n\t" "ds_read_b128 v[172:175], %[l0] offset:11264\n\t" "s_nop 1\n\t" \
    "v_cmp_u_f32 vcc, v232, v233\n\t" "v_cmp_u_f32 s[62:63], v234, v235\n\t" "s_waitcnt lgkmcnt(11)\n\t" \
    "v_smfmac_f32_16x16x64_bf16 v[232:235], v[16:19], a[48:55], %[ix]\n\t" "v_smfmac_f32_16x16x64_bf16 v[236:239], v[16:19], a[56:63], %[ix]\n\t" \
    "s_or_b64 vcc, vcc, s[62:63]\n\t" "s_cbranch_scc1 41f\n\t" "42:\n\t" "s_waitcnt lgkmcnt(10)\n\t" "v_smfmac_f32_16x16x64_bf16 v[232:235], v[20:23], a[64:71], %[ix]\n\t" \
    "v_smfmac_f32_16x16x64_bf16 v[236:239], v[20:23], v[176:183], %[ix]\n\t" "s_waitcnt lgkmcnt(4)\n\t" "s_barrier\n\t" XT(88, 89) "ds_read_b32 v225, v227\n\t" \
    "v_smfmac_f32_16x16x64_bf16 v[232:235], v[24:27], a[72:79], %[ix]\n\t" "ds_read_b128 v[176:179], %[l0] offset:12288\n\t" "ds_read_b128 v[180:183], %[l0] offset:13312\n\t" \
    "v_smfmac_f32_16x16x64_bf16 v[236:239], v[24:27], a[80:87], %[ix]\n\t" "ds_read_b128 v[248:251], %[l1] offset:2048\n\t" "ds_read_b128 v[252:255], %[l1] offset:6144\n\t" \
    "v_smfmac_f32_16x16x64_bf16 v[232:235], v[28:31], a[88:95], %[ix]\n\t" "ds_read_b128 v[16:19], %[l1] offset:10240\n\t" "ds_read_b128 v[20:23], %[l1] offset:3072\n\t" \
    "v_smfmac_f32_16x16x64_bf16 v[236:239], v[28:31], v[184:191], %[ix]\n\t" "ds_read_b128 v[24:27], %[l1] offset:7168\n\t" "ds_read_b128 v[184:187], %[l0] offset:14336\n\t" \
    "v_smfmac_f32_16x16x64_bf16 v[232:235], v[240:243], a[96:103], %[ix]\n\t" "ds_read_b128 v[188:191], %[l0] offset:15360\n\t" \
    "ds_read_b128 v[28:31], %[l1] offset:11264\n\t" "v_smfmac_f32_16x16x64_bf16 v[236:239], v[240:243], a[104:111], %[ix]\n\t" "s_waitcnt lgkmcnt(10)\n\t" \
    "v_cmp_eq_u32 vcc, %[rt], v225\n\t" "v_smfmac_f32_16x16x64_bf16 v[232:235], v[244:247], a[112:119], %[ix]\n\t" "s_cbranch_vccnz 43f\n\t" \
    "v_smfmac_f32_16x16x64_bf16 v[236:239], v[244:247], v[160:167], %[ix]\n\t" "ds_read_b128 v[160:163], %[l0] offset:16384\n\t" \
    "ds_read_b128 v[164:167], %[l0] offset:17408\n\t" "s_waitcnt lgkmcnt(9)\n\t" "v_smfmac_f32_16x16x64_bf16 v[232:235], v[248:251], a[120:127], %[ix]\n\t" \
    "v_smfmac_f32_16x16x64_bf16 v[236:239], v[248:251], a[128:135], %[ix]\n\t" "s_waitcnt lgkmcnt(8)\n\t" \
    "v_smfmac_f32_16x16x64_bf16 v[232:235], v[252:255], a[136:143], %[ix]\n\t" "v_smfmac_f32_16x16x64_bf16 v[236:239], v[252:255], v[168:175], %[ix]\n\t" \
    "ds_read_b128 v[168:171], %[l0] offset:18432\n\t" "ds_read_b128 v[172:175], %[l0] offset:19456\n\t" "s_waitcnt lgkmcnt(9)\n\t" \
    "v_smfmac_f32_16x16x64_bf16 v[232:235], v[16:19], a[144:151], %[ix]\n\t" "v_smfmac_f32_16x16x64_bf16 v[236:239], v[16:19], a[152:159], %[ix]\n\t" \
    "s_waitcnt lgkmcnt(8)\n\t" "v_smfmac_f32_16x16x64_bf16 v[232:235], v[20:23], a[160:167], %[ix]\n\t" \
    "v_smfmac_f32_16x16x64_bf16 v[236:239], v[20:23], v[176:183], %[ix]\n\t" "ds_read_b128 v[176:179], %[l0] offset:20480\n\t" \
    "ds_read_b128 v[180:183], %[l0] offset:21504\n\t" "s_waitcnt lgkmcnt(9)\n\t" "v_smfmac_f32_16x16x64_bf16 v[232:235], v[24:27], a[168:175], %[ix]\n\t" \
    "v_smfmac_f32_16x16x64_bf16 v[236:239], v[24:27], a[176:183], %[ix]\n\t" "s_waitcnt lgkmcnt(6)\n\t" \
    "v_smfmac_f32_16x16x64_bf16 v[232:235], v[28:31], a[184:191], %[ix]\n\t" "v_smfmac_f32_16x16x64_bf16 v[236:239], v[28:31], v[184:191], %[ix]\n\t" \
    "ds_read_b128 v[184:187], %[l0] offset:22528\n\t" "ds_read_b128 v[188:191], %[l0] offset:23552\n\t" XT(90, 91) "s_nop 4\n\t" "v_add_f32 v232, v232, v233\n\t" \
    "v_add_f32 v234, v234, v235\n\t" "v_add_f32 v236, v236, v237\n\t" "v_add_f32 v238, v238, v239\n\t" "v_cvt_pk_bf16_f32 v236, v232, v236\n\t" \
    "v_cvt_pk_bf16_f32 v237, v234, v238\n\t" "ds_write2_b32 %[vw], v236, v237 offset1:68\n\t" "s_waitcnt lgkmcnt(0)\n\t" "s_barrier\n\t" XT(92, 93) \
    "ds_read_b128 v[240:243], %[vr] offset:0\n\t" "ds_read_b128 v[244:247], %[vr] offset:64\n\t" "ds_read_b128 v[248:251], %[vr] offset:128\n\t" \
    "ds_read_b128 v[252:255], %[vr] offset:192\n\t" "s_mov_b32 s61, %[ub]\n\t" "s_waitcnt lgkmcnt(3)\n\t" \
    "v_smfmac_f32_16x16x64_bf16 v[192:195], v[240:243], v[32:39], %[ix]\n\t" "v_smfmac_f32_16x16x64_bf16 v[196:199], v[240:243], v[48:55], %[ix]\n\t" \
    "s_waitcnt lgkmcnt(2)\n\t" "v_smfmac_f32_16x16x64_bf16 v[192:195], v[244:247], v[40:47], %[ix]\n\t" \
    "v_smfmac_f32_16x16x64_bf16 v[196:199], v[244:247], v[56:63], %[ix]\n\t" "s_waitcnt lgkmcnt(1)\n\t" \
    "v_smfmac_f32_16x16x64_bf16 v[192:195], v[248:251], a[192:199], %[ix]\n\t" "v_smfmac_f32_16x16x64_bf16 v[196:199], v[248:251], a[200:207], %[ix]\n\t" \
    "s_waitcnt lgkmcnt(0)\n\t" "v_smfmac_f32_16x16x64_bf16 v[192:195], v[252:255], v[160:167], %[ix]\n\t" "ds_read_b128 v[160:163], %[l0] offset:24576\n\t" \
    "ds_read_b128 v[164:167], %[l0] offset:25600\n\t" "v_smfmac_f32_16x16x64_bf16 v[196:199], v[252:255], v[168:175], %[ix]\n\t" \
    "ds_read_b128 v[168:171], %[l0] offset:26624\n\t" "ds_read_b128 v[172:175], %[l0] offset:27648\n\t" \
    "v_smfmac_f32_16x16x64_bf16 v[200:203], v[240:243], v[64:71], %[ix]\n\t" "s_nop 1\n\t" "v_smfmac_f32_16x16x64_bf16 v[204:207], v[240:243], v[80:87], %[ix]\n\t" \
    "v_cvt_pk_bf16_f32 v224, v192, v194\n\t" "v_cvt_pk_bf16_f32 v225, v193, v195\n\t" "v_smfmac_f32_16x16x64_bf16 v[200:203], v[244:247], v[72:79], %[ix]\n\t" \
    "v_cvt_pk_bf16_f32 v226, v196, v198\n\t" "v_cvt_pk_bf16_f32 v227, v197, v199\n\t" "v_smfmac_f32_16x16x64_bf16 v[204:207], v[244:247], v[88:95], %[ix]\n\t" "s_nop 0\n\t" \
    "v_permlane32_swap_b32 v224, v226\n\t" "v_smfmac_f32_16x16x64_bf16 v[200:203], v[248:251], a[208:215], %[ix]\n\t" "v_permlane32_swap_b32 v225, v227\n\t" "s_nop 0\n\t" \
    "v_smfmac_f32_16x16x64_bf16 v[204:207], v[248:251], a[216:223], %[ix]\n\t" "v_permlane16_swap_b32 v224, v225\n\t" "v_permlane16_swap_b32 v226, v227\n\t" \
    "v_smfmac_f32_16x16x64_bf16 v[200:203], v[252:255], v[176:183], %[ix]\n\t" "ds_read_b128 v[176:179], %[l0] offset:28672\n\t" \
    "buffer_store_dwordx4 v[224:227], %[uo], %[rs], s61 offen sc1\n\t" "s_xor_b32 s61, s61, 0x40000\n\t" XT(94, 95) \
    "v_smfmac_f32_16x16x64_bf16 v[204:207], v[252:255], v[184:191], %[ix]\n\t" "ds_read_b128 v[180:183], %[l0] offset:29696\n\t" \
    "ds_read_b128 v[184:187], %[l0] offset:30720\n\t" "v_smfmac_f32_16x16x64_bf16 v[208:211], v[240:243], v[96:103], %[ix]\n\t" \
    "ds_read_b128 v[188:191], %[l0] offset:31744\n\t" "s_nop 0\n\t" "v_smfmac_f32_16x16x64_bf16 v[212:215], v[240:243], v[112:119], %[ix]\n\t" \
    "v_cvt_pk_bf16_f32 v228, v200, v202\n\t" "v_cvt_pk_bf16_f32 v229, v201, v203\n\t" "v_smfmac_f32_16x16x64_bf16 v[208:211], v[244:247], v[104:111], %[ix]\n\t" \
    "v_cvt_pk_bf16_f32 v230, v204, v206\n\t" "v_cvt_pk_bf16_f32 v231, v205, v207\n\t" "v_smfmac_f32_16x16x64_bf16 v[212:215], v[244:247], v[120:127], %[ix]\n\t" "s_nop 0\n\t" \
    "v_permlane32_swap_b32 v228, v230\n\t" "v_smfmac_f32_16x16x64_bf16 v[208:211], v[248:251], a[224:231], %[ix]\n\t" "v_permlane32_swap_b32 v229, v231\n\t" "s_nop 0\n\t" \
    "v_smfmac_f32_16x16x64_bf16 v[212:215], v[248:251], a[232:239], %[ix]\n\t" "v_permlane16_swap_b32 v228, v229\n\t" "v_permlane16_swap_b32 v230, v231\n\t" \
    "s_waitcnt lgkmcnt(6)\n\t" "v_smfmac_f32_16x16x64_bf16 v[208:211], v[252:255], v[160:167], %[ix]\n\t" "buffer_store_dwordx4 v[228:231], %[uo], %[rs], s61 offen sc1\n\t" \
    "s_xor_b32 s61, s61, 0xc0000\n\t" "s_waitcnt lgkmcnt(4)\n\t" "v_smfmac_f32_16x16x64_bf16 v[212:215], v[252:255], v[168:175], %[ix]\n\t" "s_nop 1\n\t" \
    "v_smfmac_f32_16x16x64_bf16 v[216:219], v[240:243], v[128:135], %[ix]\n\t" "s_nop 1\n\t" "v_smfmac_f32_16x16x64_bf16 v[220:223], v[240:243], v[144:151], %[ix]\n\t" \
    "v_cvt_pk_bf16_f32 v224, v208, v210\n\t" "v_cvt_pk_bf16_f32 v225, v209, v211\n\t" "v_smfmac_f32_16x16x64_bf16 v[216:219], v[244:247], v[136:143], %[ix]\n\t" \
    "v_cvt_pk_bf16_f32 v226, v212, v214\n\t" "v_cvt_pk_bf16_f32 v227, v213, v215\n\t" "v_smfmac_f32_16x16x64_bf16 v[220:223], v[244:247], v[152:159], %[ix]\n\t" "s_nop 0\n\t" \
    "v_permlane32_swap_b32 v224, v226\n\t" "v_smfmac_f32_16x16x64_bf16 v[216:219], v[248:251], a[240:247], %[ix]\n\t" "v_permlane32_swap_b32 v225, v227\n\t" "s_nop 0\n\t" \
    "v_smfmac_f32_16x16x64_bf16 v[220:223], v[248:251], a[248:255], %[ix]\n\t" "v_permlane16_swap_b32 v224, v225\n\t" "v_permlane16_swap_b32 v226, v227\n\t" \
    "s_waitcnt lgkmcnt(2)\n\t" "v_smfmac_f32_16x16x64_bf16 v[216:219], v[252:255], v[176:183], %[ix]\n\t" "buffer_store_dwordx4 v[224:227], %[uo], %[rs], s61 offen sc1\n\t" \
    "s_xor_b32 s61, s61, 0x40000\n\t" "s_waitcnt lgkmcnt(0)\n\t" "v_smfmac_f32_16x16x64_bf16 v[220:223], v[252:255], v[184:191], %[ix]\n\t" "s_nop 5\n\t" \
    "v_cvt_pk_bf16_f32 v228, v216, v218\n\t" "v_cvt_pk_bf16_f32 v229, v217, v219\n\t" "v_cvt_pk_bf16_f32 v230, v220, v222\n\t" "v_cvt_pk_bf16_f32 v231, v221, v223\n\t" \
    "s_nop 0\n\t" "v_permlane32_swap_b32 v228, v230\n\t" "v_permlane32_swap_b32 v229, v231\n\t" "s_nop 1\n\t" "v_permlane16_swap_b32 v228, v229\n\t" \
    "v_permlane16_swap_b32 v230, v231\n\t" "buffer_store_dwordx4 v[228:231], %[uo], %[rs], s61 offen sc1\n\t" "s_nop 1\n\t" XT(96, 97) "v_mov_b64 v[16:17], -1\n\t" \
    "v_mov_b64 v[18:19], -1\n\t" "s_barrier\n\t" "buffer_store_dwordx4 v[16:19], %[l1], %[rs], %[ur] offen offset:0 sc1\n\t" \
    "buffer_store_dwordx4 v[16:19], %[l1], %[rs], %[ur] offen offset:1024 sc1\n\t" "buffer_store_dwordx4 v[16:19], %[l1], %[rs], %[ur] offen offset:2048 sc1\n\t" \
    "buffer_store_dwordx4 v[16:19], %[l1], %[rs], %[ur] offen offset:3072 sc1\n\t" "s_sub_u32 s61, %[ur], 0x5000\n\t" \
    "buffer_store_dwordx4 v[16:19], %[l1], %[rs], s61 offen offset:3072 sc1\n\t" "s_branch 99f\n\t" "41:\n\t" "ds_read_b128 v[228:231], %[l1] offset:12288\n\t" \
    "s_waitcnt lgkmcnt(0)\n\t" "v_max3_u32 v225, v228, v229, v230\n\t" "v_max_u32 v225, v225, v231\n\t" "ds_read_b128 v[228:231], %[l1] offset:13312\n\t" \
    "s_waitcnt lgkmcnt(0)\n\t" "v_max3_u32 v225, v225, v228, v229\n\t" "v_max3_u32 v225, v225, v230, v231\n\t" "ds_read_b128 v[228:231], %[l1] offset:14336\n\t" \
    "s_waitcnt lgkmcnt(0)\n\t" "v_max3_u32 v225, v225, v228, v229\n\t" "v_max3_u32 v225, v225, v230, v231\n\t" "ds_read_b128 v[228:231], %[l1] offset:15360\n\t" \
    "s_waitcnt lgkmcnt(0)\n\t" "v_max3_u32 v225, v225, v228, v229\n\t" "v_max3_u32 v225, v225, v230, v231\n\t" "v_cmp_eq_u32 vcc, -1, v225\n\t" "s_cbranch_vccz 42b\n\t" \
    "s_cmp_eq_u32 %[rt], 1\n\t" "s_cbranch_scc1 42b\n\t" "v_mov_b32 v225, %[rt]\n\t" "ds_write_b32 v227, v225\n\t" "s_waitcnt lgkmcnt(0)\n\t" "s_branch 42b\n\t" "43:\n\t" \
    "s_sub_u32 %[rt], %[rt], 1\n\t" "s_waitcnt lgkmcnt(0)\n\t" XCNT(100) "44:\n\t" "buffer_load_dwordx4 v[240:243], %[mo], %[rs], %[sb] offen sc1\n\t" \
    "buffer_load_dwordx4 v[244:247], v224, %[rs], %[sb] offen sc1\n\t" "buffer_load_dwordx4 v[248:251], %[mo], %[rs], s60 offen sc1\n\t" \
    "buffer_load_dwordx4 v[252:255], v224, %[rs], s60 offen sc1\n\t" "s_waitcnt vmcnt(0)\n\t" "v_max_u32 v225, v240, v241\n\t" "v_max3_u32 v225, v225, v242, v243\n\t" \
    "v_max3_u32 v225, v225, v244, v245\n\t" "v_max3_u32 v225, v225, v246, v247\n\t" "v_max3_u32 v225, v225, v248, v249\n\t" "v_max3_u32 v225, v225, v250, v251\n\t" \
    "v_max3_u32 v225, v225, v252, v253\n\t" "v_max3_u32 v225, v225, v254, v255\n\t" "v_cmp_eq_u32 vcc, -1, v225\n\t" "s_cbranch_vccz 45f\n\t" "s_cmp_eq_u32 %[bd], 0\n\t" \
    "s_cbranch_scc1 45f\n\t" "s_sub_u32 %[bd], %[bd], 1\n\t" XCNT(101) "s_branch 44b\n\t" "45:\n\t" "ds_read_b128 v[160:163], %[l0] offset:0\n\t" \
    "ds_read_b128 v[164:167], %[l0] offset:1024\n\t" "ds_read_b128 v[168:171], %[l0] offset:2048\n\t" "ds_read_b128 v[172:175], %[l0] offset:3072\n\t" \
    "ds_read_b128 v[176:179], %[l0] offset:4096\n\t" "ds_read_b128 v[180:183], %[l0] offset:5120\n\t" "ds_read_b128 v[184:187], %[l0] offset:6144\n\t" \
    "ds_read_b128 v[188:191], %[l0] offset:7168\n\t" "v_mov_b64 v[232:233], 0\n\t" "v_mov_b64 v[234:235], 0\n\t" "v_mov_b64 v[236:237], 0\n\t" "v_mov_b64 v[238:239], 0\n\t" \
    "s_barrier\n\t" "s_branch 40b\n\t" "99:\n\t"
#define Y_STEP \
    ".p2align 6\n\t" "v_xor_b32 v128, 0, %[u0]\n\t" "v_xor_b32 v132, 0, %[pm]\n\t" "v_xor_b32 v129, 2048, %[u0]\n\t" "v_xor_b32 v133, 1024, %[pm]\n\t" \
    "v_xor_b32 v130, 4096, %[u0]\n\t" "v_xor_b32 v134, 2048, %[pm]\n\t" "v_xor_b32 v131, 6144, %[u0]\n\t" "v_xor_b32 v135, 3072, %[pm]\n\t" "s_getpc_b64 s[62:63]\n\t" \
    "11:\n\t" "s_memtime s[64:65]\n\t" "s_waitcnt lgkmcnt(0)\n\t" "s_sub_u32 s61, %[tu], s64\n\t" "s_cmp_gt_i32 s61, 124\n\t" "s_cbranch_scc1 11b\n\t" \
    "s_max_i32 s61, s61, 0\n\t" "s_and_b32 s61, s61, 124\n\t" "s_sub_u32 s61, 12f-11b, s61\n\t" "s_add_u32 s62, s62, s61\n\t" "s_addc_u32 s63, s63, 0\n\t" \
    "s_setpc_b64 s[62:63]\n\t" ".rept 31\n\t" "s_nop 0\n\t" ".endr\n\t" "12:\n\t" XT(90, 91) "buffer_load_dwordx4 v[144:147], v128, %[rs], %[ub] offen offset:0 sc1\n\t" \
    "buffer_load_dwordx4 v[148:151], v128, %[rs], %[ub] offen offset:1024 sc1\n\t" "buffer_load_dwordx4 v[152:155], v129, %[rs], %[ub] offen offset:0 sc1\n\t" \
    "buffer_load_dwordx4 v[156:159], v129, %[rs], %[ub] offen offset:1024 sc1\n\t" "s_cmp_lg_u32 %[late], 0\n\t" "s_cbranch_scc1 14f\n\t" "s_getpc_b64 s[62:63]\n\t" "13:\n\t" \
    "s_memtime s[64:65]\n\t" "s_waitcnt lgkmcnt(0)\n\t" "s_sub_u32 s61, %[tg], s64\n\t" "s_cmp_gt_i32 s61, 124\n\t" "s_cbranch_scc1 13b\n\t" "s_max_i32 s61, s61, 0\n\t" \
    "s_and_b32 s61, s61, 124\n\t" "s_sub_u32 s61, 14f-13b, s61\n\t" "s_add_u32 s62, s62, s61\n\t" "s_addc_u32 s63, s63, 0\n\t" "s_setpc_b64 s[62:63]\n\t" ".rept 31\n\t" \
    "s_nop 0\n\t" ".endr\n\t" "14:\n\t" XT(92, 93) "20:\n\t" "buffer_load_dwordx4 v[160:163], v130, %[rs], %[ub] offen offset:0 sc1\n\t" \
    "buffer_load_dwordx4 v[164:167], v130, %[rs], %[ub] offen offset:1024 sc1\n\t" "buffer_load_dwordx4 v[192:195], %[x0], %[rs], %[pb] offen offset:0 sc1\n\t" \
    "buffer_load_dwordx4 v[196:199], %[x0], %[rs], %[pb] offen offset:1024 sc1\n\t" "buffer_load_dwordx4 v[200:203], %[x0], %[rs], %[pb] offen offset:2048 sc1\n\t" \
    "buffer_load_dwordx4 v[204:207], %[x0], %[rs], %[pb] offen offset:3072 sc1\n\t" "buffer_load_dwordx4 v[176:179], v132, %[rs], %[pb] offen sc1\n\t" \
    "v_mov_b64 v[120:121], 0\n\t" "v_mov_b64 v[122:123], 0\n\t" "v_mov_b64 v[136:137], 0\n\t" "v_mov_b64 v[138:139], 0\n\t" \
    "buffer_load_dwordx4 v[208:211], %[x1], %[rs], %[pb] offen offset:0 sc1\n\t" "buffer_load_dwordx4 v[212:215], %[x1], %[rs], %[pb] offen offset:1024 sc1\n\t" \
    "buffer_load_dwordx4 v[216:219], %[x1], %[rs], %[pb] offen offset:2048 sc1\n\t" "buffer_load_dwordx4 v[220:223], %[x1], %[rs], %[pb] offen offset:3072 sc1\n\t" \
    "buffer_load_dwordx4 v[180:183], v133, %[rs], %[pb] offen sc1\n\t" "buffer_load_dwordx4 v[224:227], %[x2], %[rs], %[pb] offen offset:0 sc1\n\t" \
    "buffer_load_dwordx4 v[228:231], %[x2], %[rs], %[pb] offen offset:1024 sc1\n\t" "buffer_load_dwordx4 v[232:235], %[x2], %[rs], %[pb] offen offset:2048 sc1\n\t" \
    "buffer_load_dwordx4 v[236:239], %[x2], %[rs], %[pb] offen offset:3072 sc1\n\t" "buffer_load_dwordx4 v[184:187], v134, %[rs], %[pb] offen sc1\n\t" \
    "buffer_load_dwordx4 v[168:171], v131, %[rs], %[ub] offen offset:0 sc1\n\t" "buffer_load_dwordx4 v[172:175], v131, %[rs], %[ub] offen offset:1024 sc1\n\t" \
    "buffer_load_dwordx4 v[240:243], %[x3], %[rs], %[pb] offen offset:0 sc1\n\t" "buffer_load_dwordx4 v[244:247], %[x3], %[rs], %[pb] offen offset:1024 sc1\n\t" \
    "buffer_load_dwordx4 v[248:251], %[x3], %[rs], %[pb] offen offset:2048 sc1\n\t" "buffer_load_dwordx4 v[252:255], %[x3], %[rs], %[pb] offen offset:3072 sc1\n\t" \
    "buffer_load_dwordx4 v[188:191], v135, %[rs], %[pb] offen sc1\n\t" "buffer_load_dword %[sv], %[so], %[rs], %[pb] offen sc1\n\t" "v_mov_b32 v127, -1\n\t" \
    "s_waitcnt vmcnt(25)\n\t" "v_permlane16_swap_b32 v144, v145\n\t" "v_permlane16_swap_b32 v146, v147\n\t" "v_permlane16_swap_b32 v148, v149\n\t" \
    "v_permlane16_swap_b32 v150, v151\n\t" "v_permlane16_swap_b32 v152, v153\n\t" "v_permlane16_swap_b32 v154, v155\n\t" "v_permlane16_swap_b32 v156, v157\n\t" \
    "v_permlane16_swap_b32 v158, v159\n\t" "s_waitcnt vmcnt(22)\n\t" XT(94, 95) "v_smfmac_f32_16x16x64_bf16 v[136:139], v[192:195], a[0:7], %[ix]\n\t" \
    "s_waitcnt vmcnt(21)\n\t" "v_smfmac_f32_16x16x64_bf16 v[120:123], v[196:199], a[8:15], %[ix]\n\t" "v_permlane16_swap_b32 v160, v161\n\t" "s_waitcnt vmcnt(20)\n\t" \
    "v_smfmac_f32_16x16x64_bf16 v[136:139], v[200:203], a[16:23], %[ix]\n\t" "s_waitcnt vmcnt(19)\n\t" \
    "v_smfmac_f32_16x16x64_bf16 v[120:123], v[204:207], a[24:31], %[ix]\n\t" "v_permlane16_swap_b32 v162, v163\n\t" "s_waitcnt vmcnt(18)\n\t" \
    "v_smfmac_f32_16x16x64_bf16 v[136:139], v[176:179], v[144:151], %[ix]\n\t" "s_waitcnt vmcnt(17)\n\t" \
    "v_smfmac_f32_16x16x64_bf16 v[120:123], v[208:211], a[32:39], %[ix]\n\t" "s_waitcnt vmcnt(16)\n\t" \
    "v_smfmac_f32_16x16x64_bf16 v[136:139], v[212:215], a[40:47], %[ix]\n\t" "v_permlane16_swap_b32 v164, v165\n\t" "s_waitcnt vmcnt(15)\n\t" \
    "v_smfmac_f32_16x16x64_bf16 v[120:123], v[216:219], a[48:55], %[ix]\n\t" "s_waitcnt vmcnt(14)\n\t" \
    "v_smfmac_f32_16x16x64_bf16 v[136:139], v[220:223], a[56:63], %[ix]\n\t" "v_permlane16_swap_b32 v166, v167\n\t" "s_waitcnt vmcnt(13)\n\t" \
    "v_smfmac_f32_16x16x64_bf16 v[120:123], v[180:183], v[152:159], %[ix]\n\t" "s_waitcnt vmcnt(12)\n\t" \
    "v_smfmac_f32_16x16x64_bf16 v[136:139], v[224:227], a[64:71], %[ix]\n\t" "s_waitcnt vmcnt(11)\n\t" \
    "v_smfmac_f32_16x16x64_bf16 v[120:123], v[228:231], a[72:79], %[ix]\n\t" "s_waitcnt vmcnt(10)\n\t" \
    "v_smfmac_f32_16x16x64_bf16 v[136:139], v[232:235], a[80:87], %[ix]\n\t" "s_waitcnt vmcnt(9)\n\t" "v_smfmac_f32_16x16x64_bf16 v[120:123], v[236:239], a[88:95], %[ix]\n\t" \
    "s_waitcnt vmcnt(8)\n\t" "v_smfmac_f32_16x16x64_bf16 v[136:139], v[184:187], v[160:167], %[ix]\n\t" "s_waitcnt vmcnt(5)\n\t" \
    "v_smfmac_f32_16x16x64_bf16 v[120:123], v[240:243], a[96:103], %[ix]\n\t" "v_permlane16_swap_b32 v168, v169\n\t" "v_permlane16_swap_b32 v170, v171\n\t" \
    "s_waitcnt vmcnt(4)\n\t" "v_smfmac_f32_16x16x64_bf16 v[136:139], v[244:247], a[104:111], %[ix]\n\t" "v_permlane16_swap_b32 v172, v173\n\t" \
    "v_permlane16_swap_b32 v174, v175\n\t" "s_waitcnt vmcnt(3)\n\t" "v_smfmac_f32_16x16x64_bf16 v[120:123], v[248:251], a[112:119], %[ix]\n\t" "s_waitcnt vmcnt(2)\n\t" \
    "v_smfmac_f32_16x16x64_bf16 v[120:123], v[252:255], a[120:127], %[ix]\n\t" "s_waitcnt vmcnt(1)\n\t" \
    "v_smfmac_f32_16x16x64_bf16 v[120:123], v[188:191], v[168:175], %[ix]\n\t" XT(82, 83) "v_add_f32 v136, v136, v137\n\t" "v_add_f32 v138, v138, v139\n\t" "s_nop 5\n\t" \
    "v_add_f32 v120, v120, v121\n\t" "v_add_f32 v122, v122, v123\n\t" "v_add_f32 v120, v120, v136\n\t" "v_add_f32 v122, v122, v138\n\t" \
    "v_cvt_pk_bf16_f32 v124, v120, v122\n\t" "v_cmp_u_f32 vcc, v120, v122\n\t" "s_cbranch_vccnz 30f\n\t" "21:\n\t" \
    "v_mov_b32_dpp v125, v124 quad_perm:[1,0,3,2] row_mask:0xf bank_mask:0xf\n\t" "v_perm_b32 v124, v125, v124, %[ps]\n\t" \
    "buffer_store_dword v124, %[mw], %[rs], %[mb] offen sc1\n\t" XT(84, 85) "buffer_store_dword v127, %[mw], %[rs], %[mr] offen sc1\n\t" "s_memtime s[64:65]\n\t" \
    "s_barrier\n\t" XT(98, 99) "v_permlane16_swap_b32 v144, v145\n\t" "v_permlane16_swap_b32 v146, v147\n\t" "v_permlane16_swap_b32 v148, v149\n\t" \
    "v_permlane16_swap_b32 v150, v151\n\t" "v_permlane16_swap_b32 v152, v153\n\t" "v_permlane16_swap_b32 v154, v155\n\t" "v_permlane16_swap_b32 v156, v157\n\t" \
    "v_permlane16_swap_b32 v158, v159\n\t" "v_permlane16_swap_b32 v160, v161\n\t" "v_permlane16_swap_b32 v162, v163\n\t" "v_permlane16_swap_b32 v164, v165\n\t" \
    "v_permlane16_swap_b32 v166, v167\n\t" "v_permlane16_swap_b32 v168, v169\n\t" "v_permlane16_swap_b32 v170, v171\n\t" "v_permlane16_swap_b32 v172, v173\n\t" \
    "v_permlane16_swap_b32 v174, v175\n\t" "s_waitcnt lgkmcnt(0)\n\t" "s_and_b32 %[bad], s64, -2\n\t" "s_branch 40f\n\t" "30:\n\t" "s_cmp_lg_u32 %[late], 0\n\t" \
    "s_cbranch_scc1 32f\n\t" XCNT(100) "s_mov_b32 %[bad], 1\n\t" "s_waitcnt vmcnt(0)\n\t" "s_branch 40f\n\t" "32:\n\t" "v_max_u32 v126, v144, v145\n\t" \
    "v_max3_u32 v126, v126, v146, v147\n\t" "v_max3_u32 v126, v126, v148, v149\n\t" "v_max3_u32 v126, v126, v150, v151\n\t" "v_max3_u32 v126, v126, v152, v153\n\t" \
    "v_max3_u32 v126, v126, v154, v155\n\t" "v_max3_u32 v126, v126, v156, v157\n\t" "v_max3_u32 v126, v126, v158, v159\n\t" "v_max3_u32 v126, v126, v160, v161\n\t" \
    "v_max3_u32 v126, v126, v162, v163\n\t" "v_max3_u32 v126, v126, v164, v165\n\t" "v_max3_u32 v126, v126, v166, v167\n\t" "v_max3_u32 v126, v126, v168, v169\n\t" \
    "v_max3_u32 v126, v126, v170, v171\n\t" "v_max3_u32 v126, v126, v172, v173\n\t" "v_max3_u32 v126, v126, v174, v175\n\t" UCNT "v_max3_u32 v126, v126, v176, v177\n\t" \
    "v_max3_u32 v126, v126, v178, v179\n\t" "v_max3_u32 v126, v126, v180, v181\n\t" "v_max3_u32 v126, v126, v182, v183\n\t" "v_max3_u32 v126, v126, v184, v185\n\t" \
    "v_max3_u32 v126, v126, v186, v187\n\t" "v_max3_u32 v126, v126, v188, v189\n\t" "v_max3_u32 v126, v126, v190, v191\n\t" "v_max3_u32 v126, v126, v192, v193\n\t" \
    "v_max3_u32 v126, v126, v194, v195\n\t" "v_max3_u32 v126, v126, v196, v197\n\t" "v_max3_u32 v126, v126, v198, v199\n\t" "v_max3_u32 v126, v126, v200, v201\n\t" \
    "v_max3_u32 v126, v126, v202, v203\n\t" "v_max3_u32 v126, v126, v204, v205\n\t" "v_max3_u32 v126, v126, v206, v207\n\t" "v_max3_u32 v126, v126, v208, v209\n\t" \
    "v_max3_u32 v126, v126, v210, v211\n\t" "v_max3_u32 v126, v126, v212, v213\n\t" "v_max3_u32 v126, v126, v214, v215\n\t" "v_max3_u32 v126, v126, v216, v217\n\t" \
    "v_max3_u32 v126, v126, v218, v219\n\t" "v_max3_u32 v126, v126, v220, v221\n\t" "v_max3_u32 v126, v126, v222, v223\n\t" "v_max3_u32 v126, v126, v224, v225\n\t" \
    "v_max3_u32 v126, v126, v226, v227\n\t" "v_max3_u32 v126, v126, v228, v229\n\t" "v_max3_u32 v126, v126, v230, v231\n\t" "v_max3_u32 v126, v126, v232, v233\n\t" \
    "v_max3_u32 v126, v126, v234, v235\n\t" "v_max3_u32 v126, v126, v236, v237\n\t" "v_max3_u32 v126, v126, v238, v239\n\t" "v_max3_u32 v126, v126, v240, v241\n\t" \
    "v_max3_u32 v126, v126, v242, v243\n\t" "v_max3_u32 v126, v126, v244, v245\n\t" "v_max3_u32 v126, v126, v246, v247\n\t" "v_max3_u32 v126, v126, v248, v249\n\t" \
    "v_max3_u32 v126, v126, v250, v251\n\t" "v_max3_u32 v126, v126, v252, v253\n\t" "v_max3_u32 v126, v126, v254, v255\n\t" "v_cmp_eq_u32 vcc, -1, v126\n\t" \
    "s_cbranch_vccz 21b\n\t" "s_cmp_eq_u32 %[bd], 0\n\t" "s_cbranch_scc1 21b\n\t" XCNT(100) "s_mov_b32 %[bad], 1\n\t" "s_waitcnt vmcnt(0)\n\t" "40:\n\t"
#define G_LOOP \
    ".p2align 6\n\t" "v_xor_b32 v72, 0, %[xo]\n\t" "v_add_u32 v80, 0x10000, v72\n\t" "v_xor_b32 v88, 0, %[xs]\n\t" "v_add_u32 v88, 0, v88\n\t" "v_xor_b32 v73, 64, %[xo]\n\t" \
    "v_add_u32 v81, 0x10000, v73\n\t" "v_xor_b32 v89, 16, %[xs]\n\t" "v_add_u32 v89, 2048, v89\n\t" "v_xor_b32 v74, 128, %[xo]\n\t" "v_add_u32 v82, 0x10000, v74\n\t" \
    "v_xor_b32 v90, 32, %[xs]\n\t" "v_add_u32 v90, 4096, v90\n\t" "v_xor_b32 v75, 192, %[xo]\n\t" "v_add_u32 v83, 0x10000, v75\n\t" "v_xor_b32 v91, 48, %[xs]\n\t" \
    "v_add_u32 v91, 6144, v91\n\t" "v_xor_b32 v76, 256, %[xo]\n\t" "v_add_u32 v84, 0x10000, v76\n\t" "v_xor_b32 v92, 64, %[xs]\n\t" "v_add_u32 v92, 8192, v92\n\t" \
    "v_xor_b32 v77, 320, %[xo]\n\t" "v_add_u32 v85, 0x10000, v77\n\t" "v_xor_b32 v93, 80, %[xs]\n\t" "v_add_u32 v93, 10240, v93\n\t" "v_xor_b32 v78, 384, %[xo]\n\t" \
    "v_add_u32 v86, 0x10000, v78\n\t" "v_xor_b32 v94, 96, %[xs]\n\t" "v_add_u32 v94, 12288, v94\n\t" "v_xor_b32 v79, 448, %[xo]\n\t" "v_add_u32 v87, 0x10000, v79\n\t" \
    "v_xor_b32 v95, 112, %[xs]\n\t" "v_add_u32 v95, 14336, v95\n\t" "v_mov_b32 v96, 2.0\n\t" "s_mov_b32 s60, 0\n\t" "s_mov_b32 s62, 0\n\t" "s_waitcnt vmcnt(0)\n\t" \
    "s_barrier\n\t" "ds_read_b128 v[100:103], v72 offset:0\n\t" "ds_read_b128 v[104:107], v72 offset:32768\n\t" "ds_read_b128 v[108:111], v73 offset:0\n\t" \
    "ds_read_b128 v[112:115], v73 offset:32768\n\t" "ds_read_b128 v[116:119], v74 offset:0\n\t" "10:\n\t" XT(80, 81) "s_waitcnt lgkmcnt(4)\n\t" \
    "v_mfma_f32_16x16x32_bf16 v[32:35], a[0:3], v[100:103], 0\n\t" "ds_read_b128 v[120:123], v74 offset:32768\n\t" "s_add_u32 s61, s60, 1\n\t" "s_and_b32 s61, s61, 63\n\t" \
    "v_mfma_f32_16x16x32_bf16 v[36:39], a[4:7], v[100:103], 0\n\t" "s_lshl_b32 s61, s61, 16\n\t" "s_add_u32 s63, %[lw], 65536\n\t" "s_waitcnt lgkmcnt(4)\n\t" \
    "v_mfma_f32_16x16x32_bf16 v[40:43], a[0:3], v[104:107], 0\n\t" "ds_read_b128 v[124:127], v75 offset:0\n\t" "v_mfma_f32_16x16x32_bf16 v[44:47], a[4:7], v[104:107], 0\n\t" \
    "s_waitcnt lgkmcnt(4)\n\t" "v_mfma_f32_16x16x32_bf16 v[32:35], a[8:11], v[108:111], v[32:35]\n\t" "ds_read_b128 v[128:131], v75 offset:32768\n\t" \
    "v_cvt_pk_bf16_f32 v64, v48, v49\n\t" "v_mfma_f32_16x16x32_bf16 v[36:39], a[12:15], v[108:111], v[36:39]\n\t" "v_cvt_pk_bf16_f32 v65, v50, v51\n\t" \
    "v_cvt_pk_bf16_f32 v66, v52, v53\n\t" "s_waitcnt lgkmcnt(4)\n\t" "v_mfma_f32_16x16x32_bf16 v[40:43], a[8:11], v[112:115], v[40:43]\n\t" \
    "ds_read_b128 v[100:103], v76 offset:0\n\t" "v_cvt_pk_bf16_f32 v67, v54, v55\n\t" "v_mfma_f32_16x16x32_bf16 v[44:47], a[12:15], v[112:115], v[44:47]\n\t" \
    "v_cvt_pk_bf16_f32 v68, v56, v57\n\t" "v_cvt_pk_bf16_f32 v69, v58, v59\n\t" "s_waitcnt lgkmcnt(4)\n\t" \
    "v_mfma_f32_16x16x32_bf16 v[32:35], a[16:19], v[116:119], v[32:35]\n\t" "ds_read_b128 v[104:107], v76 offset:32768\n\t" "v_cvt_pk_bf16_f32 v70, v60, v61\n\t" \
    "v_mfma_f32_16x16x32_bf16 v[36:39], a[20:23], v[116:119], v[36:39]\n\t" "v_cvt_pk_bf16_f32 v71, v62, v63\n\t" \
    "buffer_store_dwordx4 v[64:67], v96, %[rs], s62 offen offset:0 sc1\n\t" "s_waitcnt lgkmcnt(4)\n\t" "v_mfma_f32_16x16x32_bf16 v[40:43], a[16:19], v[120:123], v[40:43]\n\t" \
    "ds_read_b128 v[108:111], v77 offset:0\n\t" "v_mfma_f32_16x16x32_bf16 v[44:47], a[20:23], v[120:123], v[44:47]\n\t" "s_waitcnt lgkmcnt(4)\n\t" \
    "v_mfma_f32_16x16x32_bf16 v[32:35], a[24:27], v[124:127], v[32:35]\n\t" "ds_read_b128 v[112:115], v77 offset:32768\n\t" \
    "v_mfma_f32_16x16x32_bf16 v[36:39], a[28:31], v[124:127], v[36:39]\n\t" "buffer_store_dwordx4 v[68:71], v96, %[rs], s62 offen offset:256 sc1\n\t" \
    "s_waitcnt lgkmcnt(4)\n\t" "v_mfma_f32_16x16x32_bf16 v[40:43], a[24:27], v[128:131], v[40:43]\n\t" "ds_read_b128 v[116:119], v78 offset:0\n\t" "s_mov_b32 m0, s63\n\t" \
    "v_mfma_f32_16x16x32_bf16 v[44:47], a[28:31], v[128:131], v[44:47]\n\t" "s_add_u32 s63, s63, 0x800\n\t" "buffer_load_dwordx4 v88, %[rx], s61 offen lds\n\t" \
    "s_waitcnt lgkmcnt(4)\n\t" "v_mfma_f32_16x16x32_bf16 v[32:35], a[32:35], v[100:103], v[32:35]\n\t" "ds_read_b128 v[120:123], v78 offset:32768\n\t" \
    "v_mfma_f32_16x16x32_bf16 v[36:39], a[36:39], v[100:103], v[36:39]\n\t" "buffer_load_dwordx4 v88, %[rx], s61 offen offset:1024 lds\n\t" "s_waitcnt lgkmcnt(4)\n\t" \
    "v_mfma_f32_16x16x32_bf16 v[40:43], a[32:35], v[104:107], v[40:43]\n\t" "ds_read_b128 v[124:127], v79 offset:0\n\t" "s_mov_b32 m0, s63\n\t" \
    "v_mfma_f32_16x16x32_bf16 v[44:47], a[36:39], v[104:107], v[44:47]\n\t" "s_add_u32 s63, s63, 0x800\n\t" "buffer_load_dwordx4 v89, %[rx], s61 offen lds\n\t" \
    "s_waitcnt lgkmcnt(4)\n\t" "v_mfma_f32_16x16x32_bf16 v[32:35], a[40:43], v[108:111], v[32:35]\n\t" "ds_read_b128 v[128:131], v79 offset:32768\n\t" \
    "v_mfma_f32_16x16x32_bf16 v[36:39], a[44:47], v[108:111], v[36:39]\n\t" "buffer_load_dwordx4 v89, %[rx], s61 offen offset:1024 lds\n\t" "s_waitcnt lgkmcnt(4)\n\t" \
    "v_mfma_f32_16x16x32_bf16 v[40:43], a[40:43], v[112:115], v[40:43]\n\t" "ds_read_b128 v[100:103], v72 offset:512\n\t" "s_mov_b32 m0, s63\n\t" \
    "v_mfma_f32_16x16x32_bf16 v[44:47], a[44:47], v[112:115], v[44:47]\n\t" "s_add_u32 s63, s63, 0x800\n\t" "buffer_load_dwordx4 v90, %[rx], s61 offen lds\n\t" \
    "s_waitcnt lgkmcnt(4)\n\t" "v_mfma_f32_16x16x32_bf16 v[32:35], a[48:51], v[116:119], v[32:35]\n\t" "ds_read_b128 v[104:107], v72 offset:33280\n\t" \
    "v_mfma_f32_16x16x32_bf16 v[36:39], a[52:55], v[116:119], v[36:39]\n\t" "buffer_load_dwordx4 v90, %[rx], s61 offen offset:1024 lds\n\t" "s_waitcnt lgkmcnt(4)\n\t" \
    "v_mfma_f32_16x16x32_bf16 v[40:43], a[48:51], v[120:123], v[40:43]\n\t" "ds_read_b128 v[108:111], v73 offset:512\n\t" "s_mov_b32 m0, s63\n\t" \
    "v_mfma_f32_16x16x32_bf16 v[44:47], a[52:55], v[120:123], v[44:47]\n\t" "s_add_u32 s63, s63, 0x800\n\t" "buffer_load_dwordx4 v91, %[rx], s61 offen lds\n\t" \
    "s_waitcnt lgkmcnt(4)\n\t" "v_mfma_f32_16x16x32_bf16 v[32:35], a[56:59], v[124:127], v[32:35]\n\t" "ds_read_b128 v[112:115], v73 offset:33280\n\t" \
    "v_mfma_f32_16x16x32_bf16 v[36:39], a[60:63], v[124:127], v[36:39]\n\t" "buffer_load_dwordx4 v91, %[rx], s61 offen offset:1024 lds\n\t" "s_waitcnt lgkmcnt(4)\n\t" \
    "v_mfma_f32_16x16x32_bf16 v[40:43], a[56:59], v[128:131], v[40:43]\n\t" "ds_read_b128 v[116:119], v74 offset:512\n\t" "s_mov_b32 m0, s63\n\t" \
    "v_mfma_f32_16x16x32_bf16 v[44:47], a[60:63], v[128:131], v[44:47]\n\t" "s_add_u32 s63, s63, 0x800\n\t" "buffer_load_dwordx4 v92, %[rx], s61 offen lds\n\t" \
    "s_waitcnt lgkmcnt(4)\n\t" "v_mfma_f32_16x16x32_bf16 v[32:35], a[64:67], v[100:103], v[32:35]\n\t" "ds_read_b128 v[120:123], v74 offset:33280\n\t" \
    "v_mfma_f32_16x16x32_bf16 v[36:39], a[68:71], v[100:103], v[36:39]\n\t" "buffer_load_dwordx4 v92, %[rx], s61 offen offset:1024 lds\n\t" "s_waitcnt lgkmcnt(4)\n\t" \
    "v_mfma_f32_16x16x32_bf16 v[40:43], a[64:67], v[104:107], v[40:43]\n\t" "ds_read_b128 v[124:127], v75 offset:512\n\t" "s_mov_b32 m0, s63\n\t" \
    "v_mfma_f32_16x16x32_bf16 v[44:47], a[68:71], v[104:107], v[44:47]\n\t" "s_add_u32 s63, s63, 0x800\n\t" "buffer_load_dwordx4 v93, %[rx], s61 offen lds\n\t" \
    "s_waitcnt lgkmcnt(4)\n\t" "v_mfma_f32_16x16x32_bf16 v[32:35], a[72:75], v[108:111], v[32:35]\n\t" "ds_read_b128 v[128:131], v75 offset:33280\n\t" \
    "v_mfma_f32_16x16x32_bf16 v[36:39], a[76:79], v[108:111], v[36:39]\n\t" "buffer_load_dwordx4 v93, %[rx], s61 offen offset:1024 lds\n\t" "s_waitcnt lgkmcnt(4)\n\t" \
    "v_mfma_f32_16x16x32_bf16 v[40:43], a[72:75], v[112:115], v[40:43]\n\t" "ds_read_b128 v[100:103], v76 offset:512\n\t" "s_mov_b32 m0, s63\n\t" \
    "v_mfma_f32_16x16x32_bf16 v[44:47], a[76:79], v[112:115], v[44:47]\n\t" "s_add_u32 s63, s63, 0x800\n\t" "buffer_load_dwordx4 v94, %[rx], s61 offen lds\n\t" \
    "s_waitcnt lgkmcnt(4)\n\t" "v_mfma_f32_16x16x32_bf16 v[32:35], a[80:83], v[116:119], v[32:35]\n\t" "ds_read_b128 v[104:107], v76 offset:33280\n\t" \
    "v_mfma_f32_16x16x32_bf16 v[36:39], a[84:87], v[116:119], v[36:39]\n\t" "buffer_load_dwordx4 v94, %[rx], s61 offen offset:1024 lds\n\t" "s_waitcnt lgkmcnt(4)\n\t" \
    "v_mfma_f32_16x16x32_bf16 v[40:43], a[80:83], v[120:123], v[40:43]\n\t" "ds_read_b128 v[108:111], v77 offset:512\n\t" "s_mov_b32 m0, s63\n\t" \
    "v_mfma_f32_16x16x32_bf16 v[44:47], a[84:87], v[120:123], v[44:47]\n\t" "s_add_u32 s63, s63, 0x800\n\t" "buffer_load_dwordx4 v95, %[rx], s61 offen lds\n\t" \
    "s_waitcnt lgkmcnt(4)\n\t" "v_mfma_f32_16x16x32_bf16 v[32:35], a[88:91], v[124:127], v[32:35]\n\t" "ds_read_b128 v[112:115], v77 offset:33280\n\t" \
    "v_mfma_f32_16x16x32_bf16 v[36:39], a[92:95], v[124:127], v[36:39]\n\t" "buffer_load_dwordx4 v95, %[rx], s61 offen offset:1024 lds\n\t" "s_waitcnt lgkmcnt(4)\n\t" \
    "v_mfma_f32_16x16x32_bf16 v[40:43], a[88:91], v[128:131], v[40:43]\n\t" "ds_read_b128 v[116:119], v78 offset:512\n\t" \
    "v_mfma_f32_16x16x32_bf16 v[44:47], a[92:95], v[128:131], v[44:47]\n\t" "s_waitcnt lgkmcnt(4)\n\t" "v_mfma_f32_16x16x32_bf16 v[32:35], a[96:99], v[100:103], v[32:35]\n\t" \
    "ds_read_b128 v[120:123], v78 offset:33280\n\t" "s_lshr_b32 s64, s60, 2\n\t" "s_xor_b32 s64, s64, %[xp]\n\t" \
    "v_mfma_f32_16x16x32_bf16 v[36:39], a[100:103], v[100:103], v[36:39]\n\t" "s_and_b32 s64, s64, 1\n\t" "s_lshl_b32 s64, s64, 13\n\t" "s_waitcnt lgkmcnt(4)\n\t" \
    "v_mfma_f32_16x16x32_bf16 v[40:43], a[96:99], v[104:107], v[40:43]\n\t" "ds_read_b128 v[124:127], v79 offset:512\n\t" "s_lshl_b32 s62, s60, 19\n\t" \
    "v_mfma_f32_16x16x32_bf16 v[44:47], a[100:103], v[104:107], v[44:47]\n\t" "s_add_u32 s62, s62, s64\n\t" "s_add_u32 s62, s62, %[ob]\n\t" "s_waitcnt lgkmcnt(4)\n\t" \
    "v_mfma_f32_16x16x32_bf16 v[32:35], a[104:107], v[108:111], v[32:35]\n\t" "ds_read_b128 v[128:131], v79 offset:33280\n\t" \
    "v_mfma_f32_16x16x32_bf16 v[36:39], a[108:111], v[108:111], v[36:39]\n\t" "s_waitcnt lgkmcnt(4)\n\t" \
    "v_mfma_f32_16x16x32_bf16 v[40:43], a[104:107], v[112:115], v[40:43]\n\t" "ds_read_b128 v[100:103], v72 offset:1024\n\t" \
    "v_mfma_f32_16x16x32_bf16 v[44:47], a[108:111], v[112:115], v[44:47]\n\t" "s_waitcnt lgkmcnt(4)\n\t" \
    "v_mfma_f32_16x16x32_bf16 v[32:35], a[112:115], v[116:119], v[32:35]\n\t" "ds_read_b128 v[104:107], v72 offset:33792\n\t" \
    "v_mfma_f32_16x16x32_bf16 v[36:39], a[116:119], v[116:119], v[36:39]\n\t" "s_waitcnt lgkmcnt(4)\n\t" \
    "v_mfma_f32_16x16x32_bf16 v[40:43], a[112:115], v[120:123], v[40:43]\n\t" "ds_read_b128 v[108:111], v73 offset:1024\n\t" \
    "v_mfma_f32_16x16x32_bf16 v[44:47], a[116:119], v[120:123], v[44:47]\n\t" "s_waitcnt lgkmcnt(4)\n\t" \
    "v_mfma_f32_16x16x32_bf16 v[32:35], a[120:123], v[124:127], v[32:35]\n\t" "ds_read_b128 v[112:115], v73 offset:33792\n\t" \
    "v_mfma_f32_16x16x32_bf16 v[36:39], a[124:127], v[124:127], v[36:39]\n\t" "s_waitcnt lgkmcnt(4)\n\t" \
    "v_mfma_f32_16x16x32_bf16 v[40:43], a[120:123], v[128:131], v[40:43]\n\t" "ds_read_b128 v[116:119], v74 offset:1024\n\t" \
    "v_mfma_f32_16x16x32_bf16 v[44:47], a[124:127], v[128:131], v[44:47]\n\t" "s_waitcnt lgkmcnt(4)\n\t" \
    "v_mfma_f32_16x16x32_bf16 v[32:35], a[128:131], v[100:103], v[32:35]\n\t" "ds_read_b128 v[120:123], v74 offset:33792\n\t" \
    "v_mfma_f32_16x16x32_bf16 v[36:39], a[132:135], v[100:103], v[36:39]\n\t" "s_waitcnt lgkmcnt(4)\n\t" \
    "v_mfma_f32_16x16x32_bf16 v[40:43], a[128:131], v[104:107], v[40:43]\n\t" "ds_read_b128 v[124:127], v75 offset:1024\n\t" \
    "v_mfma_f32_16x16x32_bf16 v[44:47], a[132:135], v[104:107], v[44:47]\n\t" "s_waitcnt lgkmcnt(4)\n\t" \
    "v_mfma_f32_16x16x32_bf16 v[32:35], a[136:139], v[108:111], v[32:35]\n\t" "ds_read_b128 v[128:131], v75 offset:33792\n\t" "s_waitcnt vmcnt(16)\n\t" "s_barrier\n\t" \
    "v_mfma_f32_16x16x32_bf16 v[36:39], a[140:143], v[108:111], v[36:39]\n\t" "v_mov_b32 v98, s60\n\t" "buffer_store_dword v98, %[fo], %[rc], %[fl] offen sc1\n\t" \
    "s_waitcnt lgkmcnt(4)\n\t" "v_mfma_f32_16x16x32_bf16 v[40:43], a[136:139], v[112:115], v[40:43]\n\t" "ds_read_b128 v[100:103], v76 offset:1024\n\t" \
    "v_mfma_f32_16x16x32_bf16 v[44:47], a[140:143], v[112:115], v[44:47]\n\t" "s_waitcnt lgkmcnt(4)\n\t" \
    "v_mfma_f32_16x16x32_bf16 v[32:35], a[144:147], v[116:119], v[32:35]\n\t" "ds_read_b128 v[104:107], v76 offset:33792\n\t" \
    "v_mfma_f32_16x16x32_bf16 v[36:39], a[148:151], v[116:119], v[36:39]\n\t" "s_waitcnt lgkmcnt(4)\n\t" \
    "v_mfma_f32_16x16x32_bf16 v[40:43], a[144:147], v[120:123], v[40:43]\n\t" "ds_read_b128 v[108:111], v77 offset:1024\n\t" \
    "v_mfma_f32_16x16x32_bf16 v[44:47], a[148:151], v[120:123], v[44:47]\n\t" "s_waitcnt lgkmcnt(4)\n\t" \
    "v_mfma_f32_16x16x32_bf16 v[32:35], a[152:155], v[124:127], v[32:35]\n\t" "ds_read_b128 v[112:115], v77 offset:33792\n\t" \
    "v_mfma_f32_16x16x32_bf16 v[36:39], a[156:159], v[124:127], v[36:39]\n\t" "s_waitcnt lgkmcnt(4)\n\t" \
    "v_mfma_f32_16x16x32_bf16 v[40:43], a[152:155], v[128:131], v[40:43]\n\t" "ds_read_b128 v[116:119], v78 offset:1024\n\t" \
    "v_mfma_f32_16x16x32_bf16 v[44:47], a[156:159], v[128:131], v[44:47]\n\t" "s_waitcnt lgkmcnt(4)\n\t" \
    "v_mfma_f32_16x16x32_bf16 v[32:35], a[160:163], v[100:103], v[32:35]\n\t" "ds_read_b128 v[120:123], v78 offset:33792\n\t" \
    "v_mfma_f32_16x16x32_bf16 v[36:39], a[164:167], v[100:103], v[36:39]\n\t" "s_waitcnt lgkmcnt(4)\n\t" \
    "v_mfma_f32_16x16x32_bf16 v[40:43], a[160:163], v[104:107], v[40:43]\n\t" "ds_read_b128 v[124:127], v79 offset:1024\n\t" \
    "v_mfma_f32_16x16x32_bf16 v[44:47], a[164:167], v[104:107], v[44:47]\n\t" "s_waitcnt lgkmcnt(4)\n\t" \
    "v_mfma_f32_16x16x32_bf16 v[32:35], a[168:171], v[108:111], v[32:35]\n\t" "ds_read_b128 v[128:131], v79 offset:33792\n\t" \
    "v_mfma_f32_16x16x32_bf16 v[36:39], a[172:175], v[108:111], v[36:39]\n\t" "s_waitcnt lgkmcnt(4)\n\t" \
    "v_mfma_f32_16x16x32_bf16 v[40:43], a[168:171], v[112:115], v[40:43]\n\t" "ds_read_b128 v[100:103], v72 offset:1536\n\t" \
    "v_mfma_f32_16x16x32_bf16 v[44:47], a[172:175], v[112:115], v[44:47]\n\t" "s_waitcnt lgkmcnt(4)\n\t" \
    "v_mfma_f32_16x16x32_bf16 v[32:35], a[176:179], v[116:119], v[32:35]\n\t" "ds_read_b128 v[104:107], v72 offset:34304\n\t" \
    "v_mfma_f32_16x16x32_bf16 v[36:39], a[180:183], v[116:119], v[36:39]\n\t" "s_waitcnt lgkmcnt(4)\n\t" \
    "v_mfma_f32_16x16x32_bf16 v[40:43], a[176:179], v[120:123], v[40:43]\n\t" "ds_read_b128 v[108:111], v73 offset:1536\n\t" \
    "v_mfma_f32_16x16x32_bf16 v[44:47], a[180:183], v[120:123], v[44:47]\n\t" "s_waitcnt lgkmcnt(4)\n\t" \
    "v_mfma_f32_16x16x32_bf16 v[32:35], a[184:187], v[124:127], v[32:35]\n\t" "ds_read_b128 v[112:115], v73 offset:34304\n\t" \
    "v_mfma_f32_16x16x32_bf16 v[36:39], a[188:191], v[124:127], v[36:39]\n\t" "s_waitcnt lgkmcnt(4)\n\t" \
    "v_mfma_f32_16x16x32_bf16 v[40:43], a[184:187], v[128:131], v[40:43]\n\t" "ds_read_b128 v[116:119], v74 offset:1536\n\t" \
    "v_mfma_f32_16x16x32_bf16 v[44:47], a[188:191], v[128:131], v[44:47]\n\t" "s_waitcnt lgkmcnt(4)\n\t" \
    "v_mfma_f32_16x16x32_bf16 v[32:35], a[192:195], v[100:103], v[32:35]\n\t" "ds_read_b128 v[120:123], v74 offset:34304\n\t" \
    "v_mfma_f32_16x16x32_bf16 v[36:39], a[196:199], v[100:103], v[36:39]\n\t" "s_waitcnt lgkmcnt(4)\n\t" \
    "v_mfma_f32_16x16x32_bf16 v[40:43], a[192:195], v[104:107], v[40:43]\n\t" "ds_read_b128 v[124:127], v75 offset:1536\n\t" \
    "v_mfma_f32_16x16x32_bf16 v[44:47], a[196:199], v[104:107], v[44:47]\n\t" "s_waitcnt lgkmcnt(4)\n\t" \
    "v_mfma_f32_16x16x32_bf16 v[32:35], a[200:203], v[108:111], v[32:35]\n\t" "ds_read_b128 v[128:131], v75 offset:34304\n\t" \
    "v_mfma_f32_16x16x32_bf16 v[36:39], a[204:207], v[108:111], v[36:39]\n\t" "s_waitcnt lgkmcnt(4)\n\t" \
    "v_mfma_f32_16x16x32_bf16 v[40:43], a[200:203], v[112:115], v[40:43]\n\t" "ds_read_b128 v[100:103], v76 offset:1536\n\t" \
    "v_mfma_f32_16x16x32_bf16 v[44:47], a[204:207], v[112:115], v[44:47]\n\t" "s_waitcnt lgkmcnt(4)\n\t" \
    "v_mfma_f32_16x16x32_bf16 v[32:35], a[208:211], v[116:119], v[32:35]\n\t" "ds_read_b128 v[104:107], v76 offset:34304\n\t" \
    "v_mfma_f32_16x16x32_bf16 v[36:39], a[212:215], v[116:119], v[36:39]\n\t" "s_waitcnt lgkmcnt(4)\n\t" \
    "v_mfma_f32_16x16x32_bf16 v[40:43], a[208:211], v[120:123], v[40:43]\n\t" "ds_read_b128 v[108:111], v77 offset:1536\n\t" \
    "v_mfma_f32_16x16x32_bf16 v[44:47], a[212:215], v[120:123], v[44:47]\n\t" "s_waitcnt lgkmcnt(4)\n\t" \
    "v_mfma_f32_16x16x32_bf16 v[32:35], a[216:219], v[124:127], v[32:35]\n\t" "ds_read_b128 v[112:115], v77 offset:34304\n\t" \
    "v_mfma_f32_16x16x32_bf16 v[36:39], a[220:223], v[124:127], v[36:39]\n\t" "s_waitcnt lgkmcnt(4)\n\t" \
    "v_mfma_f32_16x16x32_bf16 v[40:43], a[216:219], v[128:131], v[40:43]\n\t" "ds_read_b128 v[116:119], v78 offset:1536\n\t" \
    "v_mfma_f32_16x16x32_bf16 v[44:47], a[220:223], v[128:131], v[44:47]\n\t" "s_waitcnt lgkmcnt(4)\n\t" \
    "v_mfma_f32_16x16x32_bf16 v[32:35], a[224:227], v[100:103], v[32:35]\n\t" "ds_read_b128 v[120:123], v78 offset:34304\n\t" \
    "v_mfma_f32_16x16x32_bf16 v[36:39], a[228:231], v[100:103], v[36:39]\n\t" "s_waitcnt lgkmcnt(4)\n\t" \
    "v_mfma_f32_16x16x32_bf16 v[40:43], a[224:227], v[104:107], v[40:43]\n\t" "ds_read_b128 v[124:127], v79 offset:1536\n\t" \
    "v_mfma_f32_16x16x32_bf16 v[44:47], a[228:231], v[104:107], v[44:47]\n\t" "s_waitcnt lgkmcnt(4)\n\t" \
    "v_mfma_f32_16x16x32_bf16 v[32:35], a[232:235], v[108:111], v[32:35]\n\t" "ds_read_b128 v[128:131], v79 offset:34304\n\t" \
    "v_mfma_f32_16x16x32_bf16 v[36:39], a[236:239], v[108:111], v[36:39]\n\t" "s_waitcnt lgkmcnt(4)\n\t" \
    "v_mfma_f32_16x16x32_bf16 v[40:43], a[232:235], v[112:115], v[40:43]\n\t" "v_mfma_f32_16x16x32_bf16 v[44:47], a[236:239], v[112:115], v[44:47]\n\t" \
    "s_waitcnt lgkmcnt(3)\n\t" "v_mfma_f32_16x16x32_bf16 v[32:35], a[240:243], v[116:119], v[32:35]\n\t" \
    "v_mfma_f32_16x16x32_bf16 v[36:39], a[244:247], v[116:119], v[36:39]\n\t" "s_waitcnt vmcnt(1) lgkmcnt(0)\n\t" "s_barrier\n\t" XT(82, 83) \
    "v_mfma_f32_16x16x32_bf16 v[40:43], a[240:243], v[120:123], v[40:43]\n\t" "ds_read_b128 v[100:103], v80 offset:0\n\t" \
    "v_mfma_f32_16x16x32_bf16 v[44:47], a[244:247], v[120:123], v[44:47]\n\t" "v_mfma_f32_16x16x32_bf16 v[32:35], a[248:251], v[124:127], v[32:35]\n\t" \
    "ds_read_b128 v[104:107], v80 offset:32768\n\t" "v_mfma_f32_16x16x32_bf16 v[36:39], a[252:255], v[124:127], v[36:39]\n\t" "ds_read_b128 v[108:111], v81 offset:0\n\t" \
    "v_mfma_f32_16x16x32_bf16 v[40:43], a[248:251], v[128:131], v[40:43]\n\t" "ds_read_b128 v[112:115], v81 offset:32768\n\t" "ds_read_b128 v[116:119], v82 offset:0\n\t" \
    "v_mfma_f32_16x16x32_bf16 v[44:47], a[252:255], v[128:131], v[44:47]\n\t" "s_add_u32 s60, s60, 1\n\t" GSUM "s_cmp_eq_u32 s60, 1\n\t" "s_cbranch_scc1 20f\n\t" "11:\n\t" \
    XT(80, 81) "s_waitcnt lgkmcnt(4)\n\t" "v_mfma_f32_16x16x32_bf16 v[48:51], a[0:3], v[100:103], 0\n\t" "ds_read_b128 v[120:123], v82 offset:32768\n\t" \
    "s_add_u32 s61, s60, 1\n\t" "s_and_b32 s61, s61, 63\n\t" "v_mfma_f32_16x16x32_bf16 v[52:55], a[4:7], v[100:103], 0\n\t" "s_lshl_b32 s61, s61, 16\n\t" \
    "s_add_u32 s63, %[lw], 0\n\t" "s_waitcnt lgkmcnt(4)\n\t" "v_mfma_f32_16x16x32_bf16 v[56:59], a[0:3], v[104:107], 0\n\t" "ds_read_b128 v[124:127], v83 offset:0\n\t" \
    "v_mfma_f32_16x16x32_bf16 v[60:63], a[4:7], v[104:107], 0\n\t" "s_waitcnt lgkmcnt(4)\n\t" "v_mfma_f32_16x16x32_bf16 v[48:51], a[8:11], v[108:111], v[48:51]\n\t" \
    "ds_read_b128 v[128:131], v83 offset:32768\n\t" "v_cvt_pk_bf16_f32 v64, v32, v33\n\t" "v_mfma_f32_16x16x32_bf16 v[52:55], a[12:15], v[108:111], v[52:55]\n\t" \
    "v_cvt_pk_bf16_f32 v65, v34, v35\n\t" "v_cvt_pk_bf16_f32 v66, v36, v37\n\t" "s_waitcnt lgkmcnt(4)\n\t" \
    "v_mfma_f32_16x16x32_bf16 v[56:59], a[8:11], v[112:115], v[56:59]\n\t" "ds_read_b128 v[100:103], v84 offset:0\n\t" "v_cvt_pk_bf16_f32 v67, v38, v39\n\t" \
    "v_mfma_f32_16x16x32_bf16 v[60:63], a[12:15], v[112:115], v[60:63]\n\t" "v_cvt_pk_bf16_f32 v68, v40, v41\n\t" "v_cvt_pk_bf16_f32 v69, v42, v43\n\t" \
    "s_waitcnt lgkmcnt(4)\n\t" "v_mfma_f32_16x16x32_bf16 v[48:51], a[16:19], v[116:119], v[48:51]\n\t" "ds_read_b128 v[104:107], v84 offset:32768\n\t" \
    "v_cvt_pk_bf16_f32 v70, v44, v45\n\t" "v_mfma_f32_16x16x32_bf16 v[52:55], a[20:23], v[116:119], v[52:55]\n\t" "v_cvt_pk_bf16_f32 v71, v46, v47\n\t" \
    "buffer_store_dwordx4 v[64:67], v96, %[rs], s62 offen offset:0 sc1\n\t" "s_waitcnt lgkmcnt(4)\n\t" "v_mfma_f32_16x16x32_bf16 v[56:59], a[16:19], v[120:123], v[56:59]\n\t" \
    "ds_read_b128 v[108:111], v85 offset:0\n\t" "v_mfma_f32_16x16x32_bf16 v[60:63], a[20:23], v[120:123], v[60:63]\n\t" "s_waitcnt lgkmcnt(4)\n\t" \
    "v_mfma_f32_16x16x32_bf16 v[48:51], a[24:27], v[124:127], v[48:51]\n\t" "ds_read_b128 v[112:115], v85 offset:32768\n\t" \
    "v_mfma_f32_16x16x32_bf16 v[52:55], a[28:31], v[124:127], v[52:55]\n\t" "buffer_store_dwordx4 v[68:71], v96, %[rs], s62 offen offset:256 sc1\n\t" \
    "s_waitcnt lgkmcnt(4)\n\t" "v_mfma_f32_16x16x32_bf16 v[56:59], a[24:27], v[128:131], v[56:59]\n\t" "ds_read_b128 v[116:119], v86 offset:0\n\t" "s_mov_b32 m0, s63\n\t" \
    "v_mfma_f32_16x16x32_bf16 v[60:63], a[28:31], v[128:131], v[60:63]\n\t" "s_add_u32 s63, s63, 0x800\n\t" "buffer_load_dwordx4 v88, %[rx], s61 offen lds\n\t" \
    "s_waitcnt lgkmcnt(4)\n\t" "v_mfma_f32_16x16x32_bf16 v[48:51], a[32:35], v[100:103], v[48:51]\n\t" "ds_read_b128 v[120:123], v86 offset:32768\n\t" \
    "v_mfma_f32_16x16x32_bf16 v[52:55], a[36:39], v[100:103], v[52:55]\n\t" "buffer_load_dwordx4 v88, %[rx], s61 offen offset:1024 lds\n\t" "s_waitcnt lgkmcnt(4)\n\t" \
    "v_mfma_f32_16x16x32_bf16 v[56:59], a[32:35], v[104:107], v[56:59]\n\t" "ds_read_b128 v[124:127], v87 offset:0\n\t" "s_mov_b32 m0, s63\n\t" \
    "v_mfma_f32_16x16x32_bf16 v[60:63], a[36:39], v[104:107], v[60:63]\n\t" "s_add_u32 s63, s63, 0x800\n\t" "buffer_load_dwordx4 v89, %[rx], s61 offen lds\n\t" \
    "s_waitcnt lgkmcnt(4)\n\t" "v_mfma_f32_16x16x32_bf16 v[48:51], a[40:43], v[108:111], v[48:51]\n\t" "ds_read_b128 v[128:131], v87 offset:32768\n\t" \
    "v_mfma_f32_16x16x32_bf16 v[52:55], a[44:47], v[108:111], v[52:55]\n\t" "buffer_load_dwordx4 v89, %[rx], s61 offen offset:1024 lds\n\t" "s_waitcnt lgkmcnt(4)\n\t" \
    "v_mfma_f32_16x16x32_bf16 v[56:59], a[40:43], v[112:115], v[56:59]\n\t" "ds_read_b128 v[100:103], v80 offset:512\n\t" "s_mov_b32 m0, s63\n\t" \
    "v_mfma_f32_16x16x32_bf16 v[60:63], a[44:47], v[112:115], v[60:63]\n\t" "s_add_u32 s63, s63, 0x800\n\t" "buffer_load_dwordx4 v90, %[rx], s61 offen lds\n\t" \
    "s_waitcnt lgkmcnt(4)\n\t" "v_mfma_f32_16x16x32_bf16 v[48:51], a[48:51], v[116:119], v[48:51]\n\t" "ds_read_b128 v[104:107], v80 offset:33280\n\t" \
    "v_mfma_f32_16x16x32_bf16 v[52:55], a[52:55], v[116:119], v[52:55]\n\t" "buffer_load_dwordx4 v90, %[rx], s61 offen offset:1024 lds\n\t" "s_waitcnt lgkmcnt(4)\n\t" \
    "v_mfma_f32_16x16x32_bf16 v[56:59], a[48:51], v[120:123], v[56:59]\n\t" "ds_read_b128 v[108:111], v81 offset:512\n\t" "s_mov_b32 m0, s63\n\t" \
    "v_mfma_f32_16x16x32_bf16 v[60:63], a[52:55], v[120:123], v[60:63]\n\t" "s_add_u32 s63, s63, 0x800\n\t" "buffer_load_dwordx4 v91, %[rx], s61 offen lds\n\t" \
    "s_waitcnt lgkmcnt(4)\n\t" "v_mfma_f32_16x16x32_bf16 v[48:51], a[56:59], v[124:127], v[48:51]\n\t" "ds_read_b128 v[112:115], v81 offset:33280\n\t" \
    "v_mfma_f32_16x16x32_bf16 v[52:55], a[60:63], v[124:127], v[52:55]\n\t" "buffer_load_dwordx4 v91, %[rx], s61 offen offset:1024 lds\n\t" "s_waitcnt lgkmcnt(4)\n\t" \
    "v_mfma_f32_16x16x32_bf16 v[56:59], a[56:59], v[128:131], v[56:59]\n\t" "ds_read_b128 v[116:119], v82 offset:512\n\t" "s_mov_b32 m0, s63\n\t" \
    "v_mfma_f32_16x16x32_bf16 v[60:63], a[60:63], v[128:131], v[60:63]\n\t" "s_add_u32 s63, s63, 0x800\n\t" "buffer_load_dwordx4 v92, %[rx], s61 offen lds\n\t" \
    "s_waitcnt lgkmcnt(4)\n\t" "v_mfma_f32_16x16x32_bf16 v[48:51], a[64:67], v[100:103], v[48:51]\n\t" "ds_read_b128 v[120:123], v82 offset:33280\n\t" \
    "v_mfma_f32_16x16x32_bf16 v[52:55], a[68:71], v[100:103], v[52:55]\n\t" "buffer_load_dwordx4 v92, %[rx], s61 offen offset:1024 lds\n\t" "s_waitcnt lgkmcnt(4)\n\t" \
    "v_mfma_f32_16x16x32_bf16 v[56:59], a[64:67], v[104:107], v[56:59]\n\t" "ds_read_b128 v[124:127], v83 offset:512\n\t" "s_mov_b32 m0, s63\n\t" \
    "v_mfma_f32_16x16x32_bf16 v[60:63], a[68:71], v[104:107], v[60:63]\n\t" "s_add_u32 s63, s63, 0x800\n\t" "buffer_load_dwordx4 v93, %[rx], s61 offen lds\n\t" \
    "s_waitcnt lgkmcnt(4)\n\t" "v_mfma_f32_16x16x32_bf16 v[48:51], a[72:75], v[108:111], v[48:51]\n\t" "ds_read_b128 v[128:131], v83 offset:33280\n\t" \
    "v_mfma_f32_16x16x32_bf16 v[52:55], a[76:79], v[108:111], v[52:55]\n\t" "buffer_load_dwordx4 v93, %[rx], s61 offen offset:1024 lds\n\t" "s_waitcnt lgkmcnt(4)\n\t" \
    "v_mfma_f32_16x16x32_bf16 v[56:59], a[72:75], v[112:115], v[56:59]\n\t" "ds_read_b128 v[100:103], v84 offset:512\n\t" "s_mov_b32 m0, s63\n\t" \
    "v_mfma_f32_16x16x32_bf16 v[60:63], a[76:79], v[112:115], v[60:63]\n\t" "s_add_u32 s63, s63, 0x800\n\t" "buffer_load_dwordx4 v94, %[rx], s61 offen lds\n\t" \
    "s_waitcnt lgkmcnt(4)\n\t" "v_mfma_f32_16x16x32_bf16 v[48:51], a[80:83], v[116:119], v[48:51]\n\t" "ds_read_b128 v[104:107], v84 offset:33280\n\t" \
    "v_mfma_f32_16x16x32_bf16 v[52:55], a[84:87], v[116:119], v[52:55]\n\t" "buffer_load_dwordx4 v94, %[rx], s61 offen offset:1024 lds\n\t" "s_waitcnt lgkmcnt(4)\n\t" \
    "v_mfma_f32_16x16x32_bf16 v[56:59], a[80:83], v[120:123], v[56:59]\n\t" "ds_read_b128 v[108:111], v85 offset:512\n\t" "s_mov_b32 m0, s63\n\t" \
    "v_mfma_f32_16x16x32_bf16 v[60:63], a[84:87], v[120:123], v[60:63]\n\t" "s_add_u32 s63, s63, 0x800\n\t" "buffer_load_dwordx4 v95, %[rx], s61 offen lds\n\t" \
    "s_waitcnt lgkmcnt(4)\n\t" "v_mfma_f32_16x16x32_bf16 v[48:51], a[88:91], v[124:127], v[48:51]\n\t" "ds_read_b128 v[112:115], v85 offset:33280\n\t" \
    "v_mfma_f32_16x16x32_bf16 v[52:55], a[92:95], v[124:127], v[52:55]\n\t" "buffer_load_dwordx4 v95, %[rx], s61 offen offset:1024 lds\n\t" "s_waitcnt lgkmcnt(4)\n\t" \
    "v_mfma_f32_16x16x32_bf16 v[56:59], a[88:91], v[128:131], v[56:59]\n\t" "ds_read_b128 v[116:119], v86 offset:512\n\t" \
    "v_mfma_f32_16x16x32_bf16 v[60:63], a[92:95], v[128:131], v[60:63]\n\t" "s_waitcnt lgkmcnt(4)\n\t" "v_mfma_f32_16x16x32_bf16 v[48:51], a[96:99], v[100:103], v[48:51]\n\t" \
    "ds_read_b128 v[120:123], v86 offset:33280\n\t" "s_lshr_b32 s64, s60, 2\n\t" "s_xor_b32 s64, s64, %[xp]\n\t" \
    "v_mfma_f32_16x16x32_bf16 v[52:55], a[100:103], v[100:103], v[52:55]\n\t" "s_and_b32 s64, s64, 1\n\t" "s_lshl_b32 s64, s64, 13\n\t" "s_waitcnt lgkmcnt(4)\n\t" \
    "v_mfma_f32_16x16x32_bf16 v[56:59], a[96:99], v[104:107], v[56:59]\n\t" "ds_read_b128 v[124:127], v87 offset:512\n\t" "s_lshl_b32 s62, s60, 19\n\t" \
    "v_mfma_f32_16x16x32_bf16 v[60:63], a[100:103], v[104:107], v[60:63]\n\t" "s_add_u32 s62, s62, s64\n\t" "s_add_u32 s62, s62, %[ob]\n\t" "s_waitcnt lgkmcnt(4)\n\t" \
    "v_mfma_f32_16x16x32_bf16 v[48:51], a[104:107], v[108:111], v[48:51]\n\t" "ds_read_b128 v[128:131], v87 offset:33280\n\t" \
    "v_mfma_f32_16x16x32_bf16 v[52:55], a[108:111], v[108:111], v[52:55]\n\t" "s_waitcnt lgkmcnt(4)\n\t" \
    "v_mfma_f32_16x16x32_bf16 v[56:59], a[104:107], v[112:115], v[56:59]\n\t" "ds_read_b128 v[100:103], v80 offset:1024\n\t" \
    "v_mfma_f32_16x16x32_bf16 v[60:63], a[108:111], v[112:115], v[60:63]\n\t" "s_waitcnt lgkmcnt(4)\n\t" \
    "v_mfma_f32_16x16x32_bf16 v[48:51], a[112:115], v[116:119], v[48:51]\n\t" "ds_read_b128 v[104:107], v80 offset:33792\n\t" \
    "v_mfma_f32_16x16x32_bf16 v[52:55], a[116:119], v[116:119], v[52:55]\n\t" "s_waitcnt lgkmcnt(4)\n\t" \
    "v_mfma_f32_16x16x32_bf16 v[56:59], a[112:115], v[120:123], v[56:59]\n\t" "ds_read_b128 v[108:111], v81 offset:1024\n\t" \
    "v_mfma_f32_16x16x32_bf16 v[60:63], a[116:119], v[120:123], v[60:63]\n\t" "s_waitcnt lgkmcnt(4)\n\t" \
    "v_mfma_f32_16x16x32_bf16 v[48:51], a[120:123], v[124:127], v[48:51]\n\t" "ds_read_b128 v[112:115], v81 offset:33792\n\t" \
    "v_mfma_f32_16x16x32_bf16 v[52:55], a[124:127], v[124:127], v[52:55]\n\t" "s_waitcnt lgkmcnt(4)\n\t" \
    "v_mfma_f32_16x16x32_bf16 v[56:59], a[120:123], v[128:131], v[56:59]\n\t" "ds_read_b128 v[116:119], v82 offset:1024\n\t" \
    "v_mfma_f32_16x16x32_bf16 v[60:63], a[124:127], v[128:131], v[60:63]\n\t" "s_waitcnt lgkmcnt(4)\n\t" \
    "v_mfma_f32_16x16x32_bf16 v[48:51], a[128:131], v[100:103], v[48:51]\n\t" "ds_read_b128 v[120:123], v82 offset:33792\n\t" \
    "v_mfma_f32_16x16x32_bf16 v[52:55], a[132:135], v[100:103], v[52:55]\n\t" "s_waitcnt lgkmcnt(4)\n\t" \
    "v_mfma_f32_16x16x32_bf16 v[56:59], a[128:131], v[104:107], v[56:59]\n\t" "ds_read_b128 v[124:127], v83 offset:1024\n\t" \
    "v_mfma_f32_16x16x32_bf16 v[60:63], a[132:135], v[104:107], v[60:63]\n\t" "s_waitcnt lgkmcnt(4)\n\t" \
    "v_mfma_f32_16x16x32_bf16 v[48:51], a[136:139], v[108:111], v[48:51]\n\t" "ds_read_b128 v[128:131], v83 offset:33792\n\t" "s_waitcnt vmcnt(16)\n\t" "s_barrier\n\t" \
    "v_mfma_f32_16x16x32_bf16 v[52:55], a[140:143], v[108:111], v[52:55]\n\t" "v_mov_b32 v98, s60\n\t" "buffer_store_dword v98, %[fo], %[rc], %[fl] offen sc1\n\t" \
    "s_waitcnt lgkmcnt(4)\n\t" "v_mfma_f32_16x16x32_bf16 v[56:59], a[136:139], v[112:115], v[56:59]\n\t" "ds_read_b128 v[100:103], v84 offset:1024\n\t" \
    "v_mfma_f32_16x16x32_bf16 v[60:63], a[140:143], v[112:115], v[60:63]\n\t" "s_waitcnt lgkmcnt(4)\n\t" \
    "v_mfma_f32_16x16x32_bf16 v[48:51], a[144:147], v[116:119], v[48:51]\n\t" "ds_read_b128 v[104:107], v84 offset:33792\n\t" \
    "v_mfma_f32_16x16x32_bf16 v[52:55], a[148:151], v[116:119], v[52:55]\n\t" "s_waitcnt lgkmcnt(4)\n\t" \
    "v_mfma_f32_16x16x32_bf16 v[56:59], a[144:147], v[120:123], v[56:59]\n\t" "ds_read_b128 v[108:111], v85 offset:1024\n\t" \
    "v_mfma_f32_16x16x32_bf16 v[60:63], a[148:151], v[120:123], v[60:63]\n\t" "s_waitcnt lgkmcnt(4)\n\t" \
    "v_mfma_f32_16x16x32_bf16 v[48:51], a[152:155], v[124:127], v[48:51]\n\t" "ds_read_b128 v[112:115], v85 offset:33792\n\t" \
    "v_mfma_f32_16x16x32_bf16 v[52:55], a[156:159], v[124:127], v[52:55]\n\t" "s_waitcnt lgkmcnt(4)\n\t" \
    "v_mfma_f32_16x16x32_bf16 v[56:59], a[152:155], v[128:131], v[56:59]\n\t" "ds_read_b128 v[116:119], v86 offset:1024\n\t" \
    "v_mfma_f32_16x16x32_bf16 v[60:63], a[156:159], v[128:131], v[60:63]\n\t" "s_waitcnt lgkmcnt(4)\n\t" \
    "v_mfma_f32_16x16x32_bf16 v[48:51], a[160:163], v[100:103], v[48:51]\n\t" "ds_read_b128 v[120:123], v86 offset:33792\n\t" \
    "v_mfma_f32_16x16x32_bf16 v[52:55], a[164:167], v[100:103], v[52:55]\n\t" "s_waitcnt lgkmcnt(4)\n\t" \
    "v_mfma_f32_16x16x32_bf16 v[56:59], a[160:163], v[104:107], v[56:59]\n\t" "ds_read_b128 v[124:127], v87 offset:1024\n\t" \
    "v_mfma_f32_16x16x32_bf16 v[60:63], a[164:167], v[104:107], v[60:63]\n\t" "s_waitcnt lgkmcnt(4)\n\t" \
    "v_mfma_f32_16x16x32_bf16 v[48:51], a[168:171], v[108:111], v[48:51]\n\t" "ds_read_b128 v[128:131], v87 offset:33792\n\t" \
    "v_mfma_f32_16x16x32_bf16 v[52:55], a[172:175], v[108:111], v[52:55]\n\t" "s_waitcnt lgkmcnt(4)\n\t" \
    "v_mfma_f32_16x16x32_bf16 v[56:59], a[168:171], v[112:115], v[56:59]\n\t" "ds_read_b128 v[100:103], v80 offset:1536\n\t" \
    "v_mfma_f32_16x16x32_bf16 v[60:63], a[172:175], v[112:115], v[60:63]\n\t" "s_waitcnt lgkmcnt(4)\n\t" \
    "v_mfma_f32_16x16x32_bf16 v[48:51], a[176:179], v[116:119], v[48:51]\n\t" "ds_read_b128 v[104:107], v80 offset:34304\n\t" \
    "v_mfma_f32_16x16x32_bf16 v[52:55], a[180:183], v[116:119], v[52:55]\n\t" "s_waitcnt lgkmcnt(4)\n\t" \
    "v_mfma_f32_16x16x32_bf16 v[56:59], a[176:179], v[120:123], v[56:59]\n\t" "ds_read_b128 v[108:111], v81 offset:1536\n\t" \
    "v_mfma_f32_16x16x32_bf16 v[60:63], a[180:183], v[120:123], v[60:63]\n\t" "s_waitcnt lgkmcnt(4)\n\t" \
    "v_mfma_f32_16x16x32_bf16 v[48:51], a[184:187], v[124:127], v[48:51]\n\t" "ds_read_b128 v[112:115], v81 offset:34304\n\t" \
    "v_mfma_f32_16x16x32_bf16 v[52:55], a[188:191], v[124:127], v[52:55]\n\t" "s_waitcnt lgkmcnt(4)\n\t" \
    "v_mfma_f32_16x16x32_bf16 v[56:59], a[184:187], v[128:131], v[56:59]\n\t" "ds_read_b128 v[116:119], v82 offset:1536\n\t" \
    "v_mfma_f32_16x16x32_bf16 v[60:63], a[188:191], v[128:131], v[60:63]\n\t" "s_waitcnt lgkmcnt(4)\n\t" \
    "v_mfma_f32_16x16x32_bf16 v[48:51], a[192:195], v[100:103], v[48:51]\n\t" "ds_read_b128 v[120:123], v82 offset:34304\n\t" \
    "v_mfma_f32_16x16x32_bf16 v[52:55], a[196:199], v[100:103], v[52:55]\n\t" "s_waitcnt lgkmcnt(4)\n\t" \
    "v_mfma_f32_16x16x32_bf16 v[56:59], a[192:195], v[104:107], v[56:59]\n\t" "ds_read_b128 v[124:127], v83 offset:1536\n\t" \
    "v_mfma_f32_16x16x32_bf16 v[60:63], a[196:199], v[104:107], v[60:63]\n\t" "s_waitcnt lgkmcnt(4)\n\t" \
    "v_mfma_f32_16x16x32_bf16 v[48:51], a[200:203], v[108:111], v[48:51]\n\t" "ds_read_b128 v[128:131], v83 offset:34304\n\t" \
    "v_mfma_f32_16x16x32_bf16 v[52:55], a[204:207], v[108:111], v[52:55]\n\t" "s_waitcnt lgkmcnt(4)\n\t" \
    "v_mfma_f32_16x16x32_bf16 v[56:59], a[200:203], v[112:115], v[56:59]\n\t" "ds_read_b128 v[100:103], v84 offset:1536\n\t" \
    "v_mfma_f32_16x16x32_bf16 v[60:63], a[204:207], v[112:115], v[60:63]\n\t" "s_waitcnt lgkmcnt(4)\n\t" \
    "v_mfma_f32_16x16x32_bf16 v[48:51], a[208:211], v[116:119], v[48:51]\n\t" "ds_read_b128 v[104:107], v84 offset:34304\n\t" \
    "v_mfma_f32_16x16x32_bf16 v[52:55], a[212:215], v[116:119], v[52:55]\n\t" "s_waitcnt lgkmcnt(4)\n\t" \
    "v_mfma_f32_16x16x32_bf16 v[56:59], a[208:211], v[120:123], v[56:59]\n\t" "ds_read_b128 v[108:111], v85 offset:1536\n\t" \
    "v_mfma_f32_16x16x32_bf16 v[60:63], a[212:215], v[120:123], v[60:63]\n\t" "s_waitcnt lgkmcnt(4)\n\t" \
    "v_mfma_f32_16x16x32_bf16 v[48:51], a[216:219], v[124:127], v[48:51]\n\t" "ds_read_b128 v[112:115], v85 offset:34304\n\t" \
    "v_mfma_f32_16x16x32_bf16 v[52:55], a[220:223], v[124:127], v[52:55]\n\t" "s_waitcnt lgkmcnt(4)\n\t" \
    "v_mfma_f32_16x16x32_bf16 v[56:59], a[216:219], v[128:131], v[56:59]\n\t" "ds_read_b128 v[116:119], v86 offset:1536\n\t" \
    "v_mfma_f32_16x16x32_bf16 v[60:63], a[220:223], v[128:131], v[60:63]\n\t" "s_waitcnt lgkmcnt(4)\n\t" \
    "v_mfma_f32_16x16x32_bf16 v[48:51], a[224:227], v[100:103], v[48:51]\n\t" "ds_read_b128 v[120:123], v86 offset:34304\n\t" \
    "v_mfma_f32_16x16x32_bf16 v[52:55], a[228:231], v[100:103], v[52:55]\n\t" "s_waitcnt lgkmcnt(4)\n\t" \
    "v_mfma_f32_16x16x32_bf16 v[56:59], a[224:227], v[104:107], v[56:59]\n\t" "ds_read_b128 v[124:127], v87 offset:1536\n\t" \
    "v_mfma_f32_16x16x32_bf16 v[60:63], a[228:231], v[104:107], v[60:63]\n\t" "s_waitcnt lgkmcnt(4)\n\t" \
    "v_mfma_f32_16x16x32_bf16 v[48:51], a[232:235], v[108:111], v[48:51]\n\t" "ds_read_b128 v[128:131], v87 offset:34304\n\t" \
    "v_mfma_f32_16x16x32_bf16 v[52:55], a[236:239], v[108:111], v[52:55]\n\t" "s_waitcnt lgkmcnt(4)\n\t" \
    "v_mfma_f32_16x16x32_bf16 v[56:59], a[232:235], v[112:115], v[56:59]\n\t" "v_mfma_f32_16x16x32_bf16 v[60:63], a[236:239], v[112:115], v[60:63]\n\t" \
    "s_waitcnt lgkmcnt(3)\n\t" "v_mfma_f32_16x16x32_bf16 v[48:51], a[240:243], v[116:119], v[48:51]\n\t" \
    "v_mfma_f32_16x16x32_bf16 v[52:55], a[244:247], v[116:119], v[52:55]\n\t" "s_waitcnt vmcnt(1) lgkmcnt(0)\n\t" "s_barrier\n\t" XT(82, 83) \
    "v_mfma_f32_16x16x32_bf16 v[56:59], a[240:243], v[120:123], v[56:59]\n\t" "ds_read_b128 v[100:103], v72 offset:0\n\t" \
    "v_mfma_f32_16x16x32_bf16 v[60:63], a[244:247], v[120:123], v[60:63]\n\t" "v_mfma_f32_16x16x32_bf16 v[48:51], a[248:251], v[124:127], v[48:51]\n\t" \
    "ds_read_b128 v[104:107], v72 offset:32768\n\t" "v_mfma_f32_16x16x32_bf16 v[52:55], a[252:255], v[124:127], v[52:55]\n\t" "ds_read_b128 v[108:111], v73 offset:0\n\t" \
    "v_mfma_f32_16x16x32_bf16 v[56:59], a[248:251], v[128:131], v[56:59]\n\t" "ds_read_b128 v[112:115], v73 offset:32768\n\t" "ds_read_b128 v[116:119], v74 offset:0\n\t" \
    "v_mfma_f32_16x16x32_bf16 v[60:63], a[252:255], v[128:131], v[60:63]\n\t" "s_add_u32 s60, s60, 1\n\t" GSUM "s_cmp_lt_u32 s60, 64\n\t" "s_cbranch_scc1 10b\n\t" \
    "s_nop 7\n\t" "v_cvt_pk_bf16_f32 v64, v48, v49\n\t" "v_cvt_pk_bf16_f32 v65, v50, v51\n\t" "v_cvt_pk_bf16_f32 v66, v52, v53\n\t" "v_cvt_pk_bf16_f32 v67, v54, v55\n\t" \
    "v_cvt_pk_bf16_f32 v68, v56, v57\n\t" "v_cvt_pk_bf16_f32 v69, v58, v59\n\t" "v_cvt_pk_bf16_f32 v70, v60, v61\n\t" "v_cvt_pk_bf16_f32 v71, v62, v63\n\t" \
    "buffer_store_dwordx4 v[64:67], v96, %[rs], s62 offen offset:0 sc1\n\t" "buffer_store_dwordx4 v[68:71], v96, %[rs], s62 offen offset:256 sc1\n\t" "s_waitcnt vmcnt(0)\n\t" \
    "s_barrier\n\t" "v_mov_b32 v98, s60\n\t" "buffer_store_dword v98, %[fo], %[rc], %[fl] offen sc1\n\t" "s_waitcnt lgkmcnt(0)\n\t" "s_branch 30f\n\t" "20:\n\t" \
    "v_mov_b32 v96, %[so]\n\t" "s_nop 7\n\t" "v_cvt_pk_bf16_f32 v64, v32, v33\n\t" "v_cvt_pk_bf16_f32 v65, v34, v35\n\t" "v_cvt_pk_bf16_f32 v66, v36, v37\n\t" \
    "v_cvt_pk_bf16_f32 v67, v38, v39\n\t" "v_cvt_pk_bf16_f32 v68, v40, v41\n\t" "v_cvt_pk_bf16_f32 v69, v42, v43\n\t" "v_cvt_pk_bf16_f32 v70, v44, v45\n\t" \
    "v_cvt_pk_bf16_f32 v71, v46, v47\n\t" "buffer_store_dwordx4 v[64:67], v96, %[rs], s62 offen offset:0 sc1\n\t" \
    "buffer_store_dwordx4 v[68:71], v96, %[rs], s62 offen offset:256 sc1\n\t" "s_waitcnt vmcnt(0)\n\t" "s_barrier\n\t" "v_mov_b32 v98, s60\n\t" \
    "buffer_store_dword v98, %[fo], %[rc], %[fl] offen sc1\n\t" "s_branch 11b\n\t" "30:\n\t"
#define ZC_LOOP \
    ".p2align 6\n\t" "s_mov_b32 s60, 0\n\t" "s_mov_b32 s61, 0\n\t" "s_mov_b32 s62, 0\n\t" "s_mov_b32 s63, 0x7f000000\n\t" "s_cmp_ge_u32 s61, %[z0]\n\t" \
    "s_cbranch_scc1 52f\n\t" "51:\n\t" "buffer_load_dword v120, %[fo], %[rc], %[fv] offen sc1\n\t" "buffer_load_dword v121, %[fo], %[rc], %[vd] offen sc1\n\t" \
    "s_waitcnt vmcnt(0)\n\t" "v_readfirstlane_b32 s61, v120\n\t" "v_readfirstlane_b32 s66, v121\n\t" "s_nop 3\n\t" "s_cmp_ge_u32 s61, %[z0]\n\t" "s_cbranch_scc1 52f\n\t" \
    "s_bitcmp1_b32 s66, 1\n\t" "s_cbranch_scc1 90f\n\t" "s_cmp_eq_u32 %[bd], 0\n\t" "s_cbranch_scc1 52f\n\t" "s_sub_u32 %[bd], %[bd], 1\n\t" "s_sleep 2\n\t" \
    "s_branch 51b\n\t" "52:\n\t" "s_lshr_b32 s66, s60, 2\n\t" "s_xor_b32 s66, s66, %[xp]\n\t" "s_and_b32 s66, s66, 1\n\t" "s_lshl_b32 s66, s66, 13\n\t" \
    "s_lshl_b32 s64, s60, 19\n\t" "s_add_u32 s64, s64, s66\n\t" "s_add_u32 s64, s64, %[vb]\n\t" "buffer_load_dwordx4 v[16:19], %[ao], %[rs], s64 offen offset:0 sc1\n\t" \
    "buffer_load_dwordx4 v[20:23], %[ao], %[rs], s64 offen offset:256 sc1\n\t" "buffer_load_dwordx4 v[24:27], %[ao], %[rs], s64 offen offset:2048 sc1\n\t" \
    "buffer_load_dwordx4 v[28:31], %[ao], %[rs], s64 offen offset:2304 sc1\n\t" "buffer_load_dwordx4 v[32:35], %[a1], %[rs], s64 offen offset:0 sc1\n\t" \
    "buffer_load_dwordx4 v[36:39], %[a1], %[rs], s64 offen offset:256 sc1\n\t" "buffer_load_dwordx4 v[40:43], %[a1], %[rs], s64 offen offset:2048 sc1\n\t" \
    "buffer_load_dwordx4 v[44:47], %[a1], %[rs], s64 offen offset:2304 sc1\n\t" "s_waitcnt vmcnt(0)\n\t" "10:\n\t" XT(80, 81) "s_add_u32 s66, s62, %[rg]\n\t" \
    "s_cmp_lt_u32 s66, s60\n\t" "s_cbranch_scc1 60f\n\t" "63:\n\t" "s_add_u32 s66, s60, 1\n\t" "s_cmp_gt_u32 s61, s66\n\t" "s_cbranch_scc0 40f\n\t" \
    "v_mfma_f32_16x16x32_bf16 v[80:83], v[16:19], a[0:3], 0\n\t" "v_mfma_f32_16x16x32_bf16 v[84:87], v[20:23], a[0:3], 0\n\t" \
    "v_mfma_f32_16x16x32_bf16 v[80:83], v[24:27], a[4:7], v[80:83]\n\t" "v_mfma_f32_16x16x32_bf16 v[84:87], v[28:31], a[4:7], v[84:87]\n\t" \
    "v_mfma_f32_16x16x32_bf16 v[80:83], v[32:35], a[8:11], v[80:83]\n\t" "v_cvt_pk_bf16_f32 v112, v96, v97\n\t" \
    "v_mfma_f32_16x16x32_bf16 v[84:87], v[36:39], a[8:11], v[84:87]\n\t" "v_cvt_pk_bf16_f32 v113, v98, v99\n\t" \
    "v_mfma_f32_16x16x32_bf16 v[80:83], v[40:43], a[12:15], v[80:83]\n\t" "v_cvt_pk_bf16_f32 v114, v100, v101\n\t" \
    "v_mfma_f32_16x16x32_bf16 v[84:87], v[44:47], a[12:15], v[84:87]\n\t" "v_cvt_pk_bf16_f32 v115, v102, v103\n\t" \
    "v_mfma_f32_16x16x32_bf16 v[88:91], v[16:19], a[16:19], 0\n\t" "v_cvt_pk_bf16_f32 v116, v104, v105\n\t" "v_mfma_f32_16x16x32_bf16 v[92:95], v[20:23], a[16:19], 0\n\t" \
    "v_cvt_pk_bf16_f32 v117, v106, v107\n\t" "v_mfma_f32_16x16x32_bf16 v[88:91], v[24:27], a[20:23], v[88:91]\n\t" \
    "buffer_store_dwordx4 v[112:115], %[z1], %[rs], s63 offen sc1\n\t" "v_mfma_f32_16x16x32_bf16 v[92:95], v[28:31], a[20:23], v[92:95]\n\t" \
    "v_mfma_f32_16x16x32_bf16 v[88:91], v[32:35], a[24:27], v[88:91]\n\t" "v_cvt_pk_bf16_f32 v118, v108, v109\n\t" \
    "v_mfma_f32_16x16x32_bf16 v[92:95], v[36:39], a[24:27], v[92:95]\n\t" "v_cvt_pk_bf16_f32 v119, v110, v111\n\t" \
    "v_mfma_f32_16x16x32_bf16 v[88:91], v[40:43], a[28:31], v[88:91]\n\t" "buffer_store_dwordx4 v[116:119], %[z2], %[rs], s63 offen sc1\n\t" "s_and_b32 s63, s60, %[rm]\n\t" \
    "v_mfma_f32_16x16x32_bf16 v[92:95], v[44:47], a[28:31], v[92:95]\n\t" "s_lshl_b32 s63, s63, 22\n\t" "s_add_u32 s63, s63, %[zb]\n\t" \
    "v_mfma_f32_16x16x32_bf16 v[96:99], v[16:19], a[32:35], 0\n\t" "s_add_u32 s65, s60, 1\n\t" "s_lshr_b32 s66, s65, 2\n\t" \
    "v_mfma_f32_16x16x32_bf16 v[100:103], v[20:23], a[32:35], 0\n\t" "s_xor_b32 s66, s66, %[xp]\n\t" "s_and_b32 s66, s66, 1\n\t" \
    "v_mfma_f32_16x16x32_bf16 v[96:99], v[24:27], a[36:39], v[96:99]\n\t" "s_lshl_b32 s66, s66, 13\n\t" "s_lshl_b32 s64, s65, 19\n\t" \
    "v_mfma_f32_16x16x32_bf16 v[100:103], v[28:31], a[36:39], v[100:103]\n\t" "s_add_u32 s64, s64, s66\n\t" "s_add_u32 s64, s64, %[vb]\n\t" \
    "v_mfma_f32_16x16x32_bf16 v[96:99], v[32:35], a[40:43], v[96:99]\n\t" "buffer_load_dwordx4 v[48:51], %[ao], %[rs], s64 offen offset:0 sc1\n\t" \
    "v_cvt_pk_bf16_f32 v112, v80, v81\n\t" "v_mfma_f32_16x16x32_bf16 v[100:103], v[36:39], a[40:43], v[100:103]\n\t" "v_cvt_pk_bf16_f32 v113, v82, v83\n\t" \
    "v_mfma_f32_16x16x32_bf16 v[96:99], v[40:43], a[44:47], v[96:99]\n\t" "buffer_load_dwordx4 v[52:55], %[ao], %[rs], s64 offen offset:256 sc1\n\t" \
    "v_cvt_pk_bf16_f32 v114, v84, v85\n\t" "v_mfma_f32_16x16x32_bf16 v[100:103], v[44:47], a[44:47], v[100:103]\n\t" "v_cvt_pk_bf16_f32 v115, v86, v87\n\t" \
    "v_mfma_f32_16x16x32_bf16 v[104:107], v[16:19], a[48:51], 0\n\t" "buffer_load_dwordx4 v[56:59], %[ao], %[rs], s64 offen offset:2048 sc1\n\t" \
    "v_cvt_pk_bf16_f32 v116, v88, v89\n\t" "v_mfma_f32_16x16x32_bf16 v[108:111], v[20:23], a[48:51], 0\n\t" "v_cvt_pk_bf16_f32 v117, v90, v91\n\t" \
    "v_mfma_f32_16x16x32_bf16 v[104:107], v[24:27], a[52:55], v[104:107]\n\t" "buffer_load_dwordx4 v[60:63], %[ao], %[rs], s64 offen offset:2304 sc1\n\t" \
    "v_mfma_f32_16x16x32_bf16 v[108:111], v[28:31], a[52:55], v[108:111]\n\t" "buffer_store_dwordx4 v[112:115], %[z1], %[rs], s63 offen sc1\n\t" \
    "v_mfma_f32_16x16x32_bf16 v[104:107], v[32:35], a[56:59], v[104:107]\n\t" "buffer_load_dwordx4 v[64:67], %[a1], %[rs], s64 offen offset:0 sc1\n\t" \
    "v_cvt_pk_bf16_f32 v118, v92, v93\n\t" "v_mfma_f32_16x16x32_bf16 v[108:111], v[36:39], a[56:59], v[108:111]\n\t" "v_cvt_pk_bf16_f32 v119, v94, v95\n\t" \
    "v_mfma_f32_16x16x32_bf16 v[104:107], v[40:43], a[60:63], v[104:107]\n\t" "buffer_load_dwordx4 v[68:71], %[a1], %[rs], s64 offen offset:256 sc1\n\t" \
    "buffer_store_dwordx4 v[116:119], %[z2], %[rs], s63 offen sc1\n\t" "v_mfma_f32_16x16x32_bf16 v[108:111], v[44:47], a[60:63], v[108:111]\n\t" \
    "s_add_u32 s63, s63, 0x20000\n\t" "v_mfma_f32_16x16x32_bf16 v[80:83], v[16:19], a[64:67], 0\n\t" \
    "buffer_load_dwordx4 v[72:75], %[a1], %[rs], s64 offen offset:2048 sc1\n\t" "v_mfma_f32_16x16x32_bf16 v[84:87], v[20:23], a[64:67], 0\n\t" \
    "v_mfma_f32_16x16x32_bf16 v[80:83], v[24:27], a[68:71], v[80:83]\n\t" "buffer_load_dwordx4 v[76:79], %[a1], %[rs], s64 offen offset:2304 sc1\n\t" \
    "v_mfma_f32_16x16x32_bf16 v[84:87], v[28:31], a[68:71], v[84:87]\n\t" "v_mfma_f32_16x16x32_bf16 v[80:83], v[32:35], a[72:75], v[80:83]\n\t" \
    "buffer_load_dword v120, %[fo], %[rc], %[fv] offen sc1\n\t" "v_cvt_pk_bf16_f32 v112, v96, v97\n\t" "v_mfma_f32_16x16x32_bf16 v[84:87], v[36:39], a[72:75], v[84:87]\n\t" \
    "v_cvt_pk_bf16_f32 v113, v98, v99\n\t" "v_mfma_f32_16x16x32_bf16 v[80:83], v[40:43], a[76:79], v[80:83]\n\t" "buffer_load_dword v121, %[fo], %[rc], %[fs] offen sc1\n\t" \
    "v_mfma_f32_16x16x32_bf16 v[84:87], v[44:47], a[76:79], v[84:87]\n\t" "v_cvt_pk_bf16_f32 v114, v100, v101\n\t" "v_cvt_pk_bf16_f32 v115, v102, v103\n\t" \
    "v_mfma_f32_16x16x32_bf16 v[88:91], v[16:19], a[80:83], 0\n\t" "v_cvt_pk_bf16_f32 v116, v104, v105\n\t" "v_mfma_f32_16x16x32_bf16 v[92:95], v[20:23], a[80:83], 0\n\t" \
    "v_cvt_pk_bf16_f32 v117, v106, v107\n\t" "v_mfma_f32_16x16x32_bf16 v[88:91], v[24:27], a[84:87], v[88:91]\n\t" \
    "buffer_store_dwordx4 v[112:115], %[z1], %[rs], s63 offen sc1\n\t" "v_mfma_f32_16x16x32_bf16 v[92:95], v[28:31], a[84:87], v[92:95]\n\t" \
    "v_mfma_f32_16x16x32_bf16 v[88:91], v[32:35], a[88:91], v[88:91]\n\t" "v_cvt_pk_bf16_f32 v118, v108, v109\n\t" \
    "v_mfma_f32_16x16x32_bf16 v[92:95], v[36:39], a[88:91], v[92:95]\n\t" "v_cvt_pk_bf16_f32 v119, v110, v111\n\t" \
    "v_mfma_f32_16x16x32_bf16 v[88:91], v[40:43], a[92:95], v[88:91]\n\t" "buffer_store_dwordx4 v[116:119], %[z2], %[rs], s63 offen sc1\n\t" \
    "v_mfma_f32_16x16x32_bf16 v[92:95], v[44:47], a[92:95], v[92:95]\n\t" "s_add_u32 s63, s63, 0x20000\n\t" "v_mfma_f32_16x16x32_bf16 v[96:99], v[16:19], a[96:99], 0\n\t" \
    "v_mfma_f32_16x16x32_bf16 v[100:103], v[20:23], a[96:99], 0\n\t" "v_mfma_f32_16x16x32_bf16 v[96:99], v[24:27], a[100:103], v[96:99]\n\t" \
    "v_mfma_f32_16x16x32_bf16 v[100:103], v[28:31], a[100:103], v[100:103]\n\t" "v_mfma_f32_16x16x32_bf16 v[96:99], v[32:35], a[104:107], v[96:99]\n\t" \
    "v_cvt_pk_bf16_f32 v112, v80, v81\n\t" "v_mfma_f32_16x16x32_bf16 v[100:103], v[36:39], a[104:107], v[100:103]\n\t" "v_cvt_pk_bf16_f32 v113, v82, v83\n\t" \
    "v_mfma_f32_16x16x32_bf16 v[96:99], v[40:43], a[108:111], v[96:99]\n\t" "v_cvt_pk_bf16_f32 v114, v84, v85\n\t" \
    "v_mfma_f32_16x16x32_bf16 v[100:103], v[44:47], a[108:111], v[100:103]\n\t" "v_cvt_pk_bf16_f32 v115, v86, v87\n\t" \
    "v_mfma_f32_16x16x32_bf16 v[104:107], v[16:19], a[112:115], 0\n\t" "v_cvt_pk_bf16_f32 v116, v88, v89\n\t" \
    "v_mfma_f32_16x16x32_bf16 v[108:111], v[20:23], a[112:115], 0\n\t" "v_cvt_pk_bf16_f32 v117, v90, v91\n\t" \
    "v_mfma_f32_16x16x32_bf16 v[104:107], v[24:27], a[116:119], v[104:107]\n\t" "buffer_store_dwordx4 v[112:115], %[z1], %[rs], s63 offen sc1\n\t" \
    "v_mfma_f32_16x16x32_bf16 v[108:111], v[28:31], a[116:119], v[108:111]\n\t" "v_mfma_f32_16x16x32_bf16 v[104:107], v[32:35], a[120:123], v[104:107]\n\t" \
    "v_cvt_pk_bf16_f32 v118, v92, v93\n\t" "v_mfma_f32_16x16x32_bf16 v[108:111], v[36:39], a[120:123], v[108:111]\n\t" "v_cvt_pk_bf16_f32 v119, v94, v95\n\t" \
    "v_mfma_f32_16x16x32_bf16 v[104:107], v[40:43], a[124:127], v[104:107]\n\t" "buffer_store_dwordx4 v[116:119], %[z2], %[rs], s63 offen sc1\n\t" \
    "v_mfma_f32_16x16x32_bf16 v[108:111], v[44:47], a[124:127], v[108:111]\n\t" "s_add_u32 s63, s63, 0x20000\n\t" \
    "v_mfma_f32_16x16x32_bf16 v[80:83], v[16:19], a[128:131], 0\n\t" "v_mfma_f32_16x16x32_bf16 v[84:87], v[20:23], a[128:131], 0\n\t" \
    "v_mfma_f32_16x16x32_bf16 v[80:83], v[24:27], a[132:135], v[80:83]\n\t" "v_mfma_f32_16x16x32_bf16 v[84:87], v[28:31], a[132:135], v[84:87]\n\t" \
    "v_mfma_f32_16x16x32_bf16 v[80:83], v[32:35], a[136:139], v[80:83]\n\t" "v_cvt_pk_bf16_f32 v112, v96, v97\n\t" \
    "v_mfma_f32_16x16x32_bf16 v[84:87], v[36:39], a[136:139], v[84:87]\n\t" "v_cvt_pk_bf16_f32 v113, v98, v99\n\t" \
    "v_mfma_f32_16x16x32_bf16 v[80:83], v[40:43], a[140:143], v[80:83]\n\t" "v_cvt_pk_bf16_f32 v114, v100, v101\n\t" \
    "v_mfma_f32_16x16x32_bf16 v[84:87], v[44:47], a[140:143], v[84:87]\n\t" "v_cvt_pk_bf16_f32 v115, v102, v103\n\t" \
    "v_mfma_f32_16x16x32_bf16 v[88:91], v[16:19], a[144:147], 0\n\t" "v_cvt_pk_bf16_f32 v116, v104, v105\n\t" "v_mfma_f32_16x16x32_bf16 v[92:95], v[20:23], a[144:147], 0\n\t" \
    "v_cvt_pk_bf16_f32 v117, v106, v107\n\t" "v_mfma_f32_16x16x32_bf16 v[88:91], v[24:27], a[148:151], v[88:91]\n\t" \
    "buffer_store_dwordx4 v[112:115], %[z1], %[rs], s63 offen sc1\n\t" "v_mfma_f32_16x16x32_bf16 v[92:95], v[28:31], a[148:151], v[92:95]\n\t" \
    "v_mfma_f32_16x16x32_bf16 v[88:91], v[32:35], a[152:155], v[88:91]\n\t" "v_cvt_pk_bf16_f32 v118, v108, v109\n\t" \
    "v_mfma_f32_16x16x32_bf16 v[92:95], v[36:39], a[152:155], v[92:95]\n\t" "v_cvt_pk_bf16_f32 v119, v110, v111\n\t" \
    "v_mfma_f32_16x16x32_bf16 v[88:91], v[40:43], a[156:159], v[88:91]\n\t" "buffer_store_dwordx4 v[116:119], %[z2], %[rs], s63 offen sc1\n\t" \
    "v_mfma_f32_16x16x32_bf16 v[92:95], v[44:47], a[156:159], v[92:95]\n\t" "s_add_u32 s63, s63, 0x20000\n\t" "v_mfma_f32_16x16x32_bf16 v[96:99], v[16:19], a[160:163], 0\n\t" \
    "v_mfma_f32_16x16x32_bf16 v[100:103], v[20:23], a[160:163], 0\n\t" "v_mfma_f32_16x16x32_bf16 v[96:99], v[24:27], a[164:167], v[96:99]\n\t" \
    "v_mfma_f32_16x16x32_bf16 v[100:103], v[28:31], a[164:167], v[100:103]\n\t" "v_mfma_f32_16x16x32_bf16 v[96:99], v[32:35], a[168:171], v[96:99]\n\t" \
    "v_cvt_pk_bf16_f32 v112, v80, v81\n\t" "v_mfma_f32_16x16x32_bf16 v[100:103], v[36:39], a[168:171], v[100:103]\n\t" "v_cvt_pk_bf16_f32 v113, v82, v83\n\t" \
    "v_mfma_f32_16x16x32_bf16 v[96:99], v[40:43], a[172:175], v[96:99]\n\t" "v_cvt_pk_bf16_f32 v114, v84, v85\n\t" \
    "v_mfma_f32_16x16x32_bf16 v[100:103], v[44:47], a[172:175], v[100:103]\n\t" "v_cvt_pk_bf16_f32 v115, v86, v87\n\t" \
    "v_mfma_f32_16x16x32_bf16 v[104:107], v[16:19], a[176:179], 0\n\t" "v_cvt_pk_bf16_f32 v116, v88, v89\n\t" \
    "v_mfma_f32_16x16x32_bf16 v[108:111], v[20:23], a[176:179], 0\n\t" "v_cvt_pk_bf16_f32 v117, v90, v91\n\t" \
    "v_mfma_f32_16x16x32_bf16 v[104:107], v[24:27], a[180:183], v[104:107]\n\t" "buffer_store_dwordx4 v[112:115], %[z1], %[rs], s63 offen sc1\n\t" \
    "v_mfma_f32_16x16x32_bf16 v[108:111], v[28:31], a[180:183], v[108:111]\n\t" "v_mfma_f32_16x16x32_bf16 v[104:107], v[32:35], a[184:187], v[104:107]\n\t" \
    "v_cvt_pk_bf16_f32 v118, v92, v93\n\t" "v_mfma_f32_16x16x32_bf16 v[108:111], v[36:39], a[184:187], v[108:111]\n\t" "v_cvt_pk_bf16_f32 v119, v94, v95\n\t" \
    "v_mfma_f32_16x16x32_bf16 v[104:107], v[40:43], a[188:191], v[104:107]\n\t" "buffer_store_dwordx4 v[116:119], %[z2], %[rs], s63 offen sc1\n\t" \
    "v_mfma_f32_16x16x32_bf16 v[108:111], v[44:47], a[188:191], v[108:111]\n\t" "s_add_u32 s63, s63, 0x20000\n\t" \
    "v_mfma_f32_16x16x32_bf16 v[80:83], v[16:19], a[192:195], 0\n\t" "v_mfma_f32_16x16x32_bf16 v[84:87], v[20:23], a[192:195], 0\n\t" \
    "v_mfma_f32_16x16x32_bf16 v[80:83], v[24:27], a[196:199], v[80:83]\n\t" "v_mfma_f32_16x16x32_bf16 v[84:87], v[28:31], a[196:199], v[84:87]\n\t" \
    "v_mfma_f32_16x16x32_bf16 v[80:83], v[32:35], a[200:203], v[80:83]\n\t" "v_cvt_pk_bf16_f32 v112, v96, v97\n\t" \
    "v_mfma_f32_16x16x32_bf16 v[84:87], v[36:39], a[200:203], v[84:87]\n\t" "v_cvt_pk_bf16_f32 v113, v98, v99\n\t" \
    "v_mfma_f32_16x16x32_bf16 v[80:83], v[40:43], a[204:207], v[80:83]\n\t" "v_cvt_pk_bf16_f32 v114, v100, v101\n\t" \
    "v_mfma_f32_16x16x32_bf16 v[84:87], v[44:47], a[204:207], v[84:87]\n\t" "v_cvt_pk_bf16_f32 v115, v102, v103\n\t" \
    "v_mfma_f32_16x16x32_bf16 v[88:91], v[16:19], a[208:211], 0\n\t" "v_cvt_pk_bf16_f32 v116, v104, v105\n\t" "v_mfma_f32_16x16x32_bf16 v[92:95], v[20:23], a[208:211], 0\n\t" \
    "v_cvt_pk_bf16_f32 v117, v106, v107\n\t" "v_mfma_f32_16x16x32_bf16 v[88:91], v[24:27], a[212:215], v[88:91]\n\t" \
    "buffer_store_dwordx4 v[112:115], %[z1], %[rs], s63 offen sc1\n\t" "v_mfma_f32_16x16x32_bf16 v[92:95], v[28:31], a[212:215], v[92:95]\n\t" \
    "v_mfma_f32_16x16x32_bf16 v[88:91], v[32:35], a[216:219], v[88:91]\n\t" "v_cvt_pk_bf16_f32 v118, v108, v109\n\t" \
    "v_mfma_f32_16x16x32_bf16 v[92:95], v[36:39], a[216:219], v[92:95]\n\t" "v_cvt_pk_bf16_f32 v119, v110, v111\n\t" \
    "v_mfma_f32_16x16x32_bf16 v[88:91], v[40:43], a[220:223], v[88:91]\n\t" "buffer_store_dwordx4 v[116:119], %[z2], %[rs], s63 offen sc1\n\t" \
    "v_mfma_f32_16x16x32_bf16 v[92:95], v[44:47], a[220:223], v[92:95]\n\t" "s_add_u32 s63, s63, 0x20000\n\t" "v_mfma_f32_16x16x32_bf16 v[96:99], v[16:19], a[224:227], 0\n\t" \
    "v_mfma_f32_16x16x32_bf16 v[100:103], v[20:23], a[224:227], 0\n\t" "v_mfma_f32_16x16x32_bf16 v[96:99], v[24:27], a[228:231], v[96:99]\n\t" \
    "v_mfma_f32_16x16x32_bf16 v[100:103], v[28:31], a[228:231], v[100:103]\n\t" "v_mfma_f32_16x16x32_bf16 v[96:99], v[32:35], a[232:235], v[96:99]\n\t" \
    "v_cvt_pk_bf16_f32 v112, v80, v81\n\t" "v_mfma_f32_16x16x32_bf16 v[100:103], v[36:39], a[232:235], v[100:103]\n\t" "v_cvt_pk_bf16_f32 v113, v82, v83\n\t" \
    "v_mfma_f32_16x16x32_bf16 v[96:99], v[40:43], a[236:239], v[96:99]\n\t" "v_cvt_pk_bf16_f32 v114, v84, v85\n\t" \
    "v_mfma_f32_16x16x32_bf16 v[100:103], v[44:47], a[236:239], v[100:103]\n\t" "v_cvt_pk_bf16_f32 v115, v86, v87\n\t" \
    "v_mfma_f32_16x16x32_bf16 v[104:107], v[16:19], a[240:243], 0\n\t" "v_cvt_pk_bf16_f32 v116, v88, v89\n\t" \
    "v_mfma_f32_16x16x32_bf16 v[108:111], v[20:23], a[240:243], 0\n\t" "v_cvt_pk_bf16_f32 v117, v90, v91\n\t" \
    "v_mfma_f32_16x16x32_bf16 v[104:107], v[24:27], a[244:247], v[104:107]\n\t" "buffer_store_dwordx4 v[112:115], %[z1], %[rs], s63 offen sc1\n\t" \
    "v_mfma_f32_16x16x32_bf16 v[108:111], v[28:31], a[244:247], v[108:111]\n\t" "v_mfma_f32_16x16x32_bf16 v[104:107], v[32:35], a[248:251], v[104:107]\n\t" \
    "v_cvt_pk_bf16_f32 v118, v92, v93\n\t" "v_cvt_pk_bf16_f32 v119, v94, v95\n\t" "s_waitcnt vmcnt(11)\n\t" "v_readfirstlane_b32 s66, v120\n\t" \
    "v_mfma_f32_16x16x32_bf16 v[108:111], v[36:39], a[248:251], v[108:111]\n\t" "v_readfirstlane_b32 s67, v121\n\t" "v_mov_b32 v122, s60\n\t" \
    "buffer_store_dword v122, %[fo], %[rc], %[fz] offen sc1\n\t" "v_mfma_f32_16x16x32_bf16 v[104:107], v[40:43], a[252:255], v[104:107]\n\t" \
    "buffer_store_dwordx4 v[116:119], %[z2], %[rs], s63 offen sc1\n\t" "s_add_u32 s63, s63, 0x20000\n\t" "s_max_u32 s61, s61, s66\n\t" \
    "v_mfma_f32_16x16x32_bf16 v[108:111], v[44:47], a[252:255], v[108:111]\n\t" "s_max_u32 s62, s62, s67\n\t" "s_add_u32 s60, s60, 1\n\t" ZSUM "23:\n\t" XT(80, 81) \
    "s_add_u32 s66, s62, %[rg]\n\t" "s_cmp_lt_u32 s66, s60\n\t" "s_cbranch_scc1 70f\n\t" "73:\n\t" "s_add_u32 s66, s60, 1\n\t" "s_cmp_gt_u32 s61, s66\n\t" \
    "s_cbranch_scc0 41f\n\t" "v_mfma_f32_16x16x32_bf16 v[80:83], v[48:51], a[0:3], 0\n\t" "v_mfma_f32_16x16x32_bf16 v[84:87], v[52:55], a[0:3], 0\n\t" \
    "v_mfma_f32_16x16x32_bf16 v[80:83], v[56:59], a[4:7], v[80:83]\n\t" "v_mfma_f32_16x16x32_bf16 v[84:87], v[60:63], a[4:7], v[84:87]\n\t" \
    "v_mfma_f32_16x16x32_bf16 v[80:83], v[64:67], a[8:11], v[80:83]\n\t" "v_cvt_pk_bf16_f32 v112, v96, v97\n\t" \
    "v_mfma_f32_16x16x32_bf16 v[84:87], v[68:71], a[8:11], v[84:87]\n\t" "v_cvt_pk_bf16_f32 v113, v98, v99\n\t" \
    "v_mfma_f32_16x16x32_bf16 v[80:83], v[72:75], a[12:15], v[80:83]\n\t" "v_cvt_pk_bf16_f32 v114, v100, v101\n\t" \
    "v_mfma_f32_16x16x32_bf16 v[84:87], v[76:79], a[12:15], v[84:87]\n\t" "v_cvt_pk_bf16_f32 v115, v102, v103\n\t" \
    "v_mfma_f32_16x16x32_bf16 v[88:91], v[48:51], a[16:19], 0\n\t" "v_cvt_pk_bf16_f32 v116, v104, v105\n\t" "v_mfma_f32_16x16x32_bf16 v[92:95], v[52:55], a[16:19], 0\n\t" \
    "v_cvt_pk_bf16_f32 v117, v106, v107\n\t" "v_mfma_f32_16x16x32_bf16 v[88:91], v[56:59], a[20:23], v[88:91]\n\t" \
    "buffer_store_dwordx4 v[112:115], %[z1], %[rs], s63 offen sc1\n\t" "v_mfma_f32_16x16x32_bf16 v[92:95], v[60:63], a[20:23], v[92:95]\n\t" \
    "v_mfma_f32_16x16x32_bf16 v[88:91], v[64:67], a[24:27], v[88:91]\n\t" "v_cvt_pk_bf16_f32 v118, v108, v109\n\t" \
    "v_mfma_f32_16x16x32_bf16 v[92:95], v[68:71], a[24:27], v[92:95]\n\t" "v_cvt_pk_bf16_f32 v119, v110, v111\n\t" \
    "v_mfma_f32_16x16x32_bf16 v[88:91], v[72:75], a[28:31], v[88:91]\n\t" "buffer_store_dwordx4 v[116:119], %[z2], %[rs], s63 offen sc1\n\t" "s_and_b32 s63, s60, %[rm]\n\t" \
    "v_mfma_f32_16x16x32_bf16 v[92:95], v[76:79], a[28:31], v[92:95]\n\t" "s_lshl_b32 s63, s63, 22\n\t" "s_add_u32 s63, s63, %[zb]\n\t" \
    "v_mfma_f32_16x16x32_bf16 v[96:99], v[48:51], a[32:35], 0\n\t" "s_add_u32 s65, s60, 1\n\t" "s_lshr_b32 s66, s65, 2\n\t" \
    "v_mfma_f32_16x16x32_bf16 v[100:103], v[52:55], a[32:35], 0\n\t" "s_xor_b32 s66, s66, %[xp]\n\t" "s_and_b32 s66, s66, 1\n\t" \
    "v_mfma_f32_16x16x32_bf16 v[96:99], v[56:59], a[36:39], v[96:99]\n\t" "s_lshl_b32 s66, s66, 13\n\t" "s_lshl_b32 s64, s65, 19\n\t" \
    "v_mfma_f32_16x16x32_bf16 v[100:103], v[60:63], a[36:39], v[100:103]\n\t" "s_add_u32 s64, s64, s66\n\t" "s_add_u32 s64, s64, %[vb]\n\t" \
    "v_mfma_f32_16x16x32_bf16 v[96:99], v[64:67], a[40:43], v[96:99]\n\t" "buffer_load_dwordx4 v[16:19], %[ao], %[rs], s64 offen offset:0 sc1\n\t" \
    "v_cvt_pk_bf16_f32 v112, v80, v81\n\t" "v_mfma_f32_16x16x32_bf16 v[100:103], v[68:71], a[40:43], v[100:103]\n\t" "v_cvt_pk_bf16_f32 v113, v82, v83\n\t" \
    "v_mfma_f32_16x16x32_bf16 v[96:99], v[72:75], a[44:47], v[96:99]\n\t" "buffer_load_dwordx4 v[20:23], %[ao], %[rs], s64 offen offset:256 sc1\n\t" \
    "v_cvt_pk_bf16_f32 v114, v84, v85\n\t" "v_mfma_f32_16x16x32_bf16 v[100:103], v[76:79], a[44:47], v[100:103]\n\t" "v_cvt_pk_bf16_f32 v115, v86, v87\n\t" \
    "v_mfma_f32_16x16x32_bf16 v[104:107], v[48:51], a[48:51], 0\n\t" "buffer_load_dwordx4 v[24:27], %[ao], %[rs], s64 offen offset:2048 sc1\n\t" \
    "v_cvt_pk_bf16_f32 v116, v88, v89\n\t" "v_mfma_f32_16x16x32_bf16 v[108:111], v[52:55], a[48:51], 0\n\t" "v_cvt_pk_bf16_f32 v117, v90, v91\n\t" \
    "v_mfma_f32_16x16x32_bf16 v[104:107], v[56:59], a[52:55], v[104:107]\n\t" "buffer_load_dwordx4 v[28:31], %[ao], %[rs], s64 offen offset:2304 sc1\n\t" \
    "v_mfma_f32_16x16x32_bf16 v[108:111], v[60:63], a[52:55], v[108:111]\n\t" "buffer_store_dwordx4 v[112:115], %[z1], %[rs], s63 offen sc1\n\t" \
    "v_mfma_f32_16x16x32_bf16 v[104:107], v[64:67], a[56:59], v[104:107]\n\t" "buffer_load_dwordx4 v[32:35], %[a1], %[rs], s64 offen offset:0 sc1\n\t" \
    "v_cvt_pk_bf16_f32 v118, v92, v93\n\t" "v_mfma_f32_16x16x32_bf16 v[108:111], v[68:71], a[56:59], v[108:111]\n\t" "v_cvt_pk_bf16_f32 v119, v94, v95\n\t" \
    "v_mfma_f32_16x16x32_bf16 v[104:107], v[72:75], a[60:63], v[104:107]\n\t" "buffer_load_dwordx4 v[36:39], %[a1], %[rs], s64 offen offset:256 sc1\n\t" \
    "buffer_store_dwordx4 v[116:119], %[z2], %[rs], s63 offen sc1\n\t" "v_mfma_f32_16x16x32_bf16 v[108:111], v[76:79], a[60:63], v[108:111]\n\t" \
    "s_add_u32 s63, s63, 0x20000\n\t" "v_mfma_f32_16x16x32_bf16 v[80:83], v[48:51], a[64:67], 0\n\t" \
    "buffer_load_dwordx4 v[40:43], %[a1], %[rs], s64 offen offset:2048 sc1\n\t" "v_mfma_f32_16x16x32_bf16 v[84:87], v[52:55], a[64:67], 0\n\t" \
    "v_mfma_f32_16x16x32_bf16 v[80:83], v[56:59], a[68:71], v[80:83]\n\t" "buffer_load_dwordx4 v[44:47], %[a1], %[rs], s64 offen offset:2304 sc1\n\t" \
    "v_mfma_f32_16x16x32_bf16 v[84:87], v[60:63], a[68:71], v[84:87]\n\t" "v_mfma_f32_16x16x32_bf16 v[80:83], v[64:67], a[72:75], v[80:83]\n\t" \
    "buffer_load_dword v120, %[fo], %[rc], %[fv] offen sc1\n\t" "v_cvt_pk_bf16_f32 v112, v96, v97\n\t" "v_mfma_f32_16x16x32_bf16 v[84:87], v[68:71], a[72:75], v[84:87]\n\t" \
    "v_cvt_pk_bf16_f32 v113, v98, v99\n\t" "v_mfma_f32_16x16x32_bf16 v[80:83], v[72:75], a[76:79], v[80:83]\n\t" "buffer_load_dword v121, %[fo], %[rc], %[fs] offen sc1\n\t" \
    "v_mfma_f32_16x16x32_bf16 v[84:87], v[76:79], a[76:79], v[84:87]\n\t" "v_cvt_pk_bf16_f32 v114, v100, v101\n\t" "v_cvt_pk_bf16_f32 v115, v102, v103\n\t" \
    "v_mfma_f32_16x16x32_bf16 v[88:91], v[48:51], a[80:83], 0\n\t" "v_cvt_pk_bf16_f32 v116, v104, v105\n\t" "v_mfma_f32_16x16x32_bf16 v[92:95], v[52:55], a[80:83], 0\n\t" \
    "v_cvt_pk_bf16_f32 v117, v106, v107\n\t" "v_mfma_f32_16x16x32_bf16 v[88:91], v[56:59], a[84:87], v[88:91]\n\t" \
    "buffer_store_dwordx4 v[112:115], %[z1], %[rs], s63 offen sc1\n\t" "v_mfma_f32_16x16x32_bf16 v[92:95], v[60:63], a[84:87], v[92:95]\n\t" \
    "v_mfma_f32_16x16x32_bf16 v[88:91], v[64:67], a[88:91], v[88:91]\n\t" "v_cvt_pk_bf16_f32 v118, v108, v109\n\t" \
    "v_mfma_f32_16x16x32_bf16 v[92:95], v[68:71], a[88:91], v[92:95]\n\t" "v_cvt_pk_bf16_f32 v119, v110, v111\n\t" \
    "v_mfma_f32_16x16x32_bf16 v[88:91], v[72:75], a[92:95], v[88:91]\n\t" "buffer_store_dwordx4 v[116:119], %[z2], %[rs], s63 offen sc1\n\t" \
    "v_mfma_f32_16x16x32_bf16 v[92:95], v[76:79], a[92:95], v[92:95]\n\t" "s_add_u32 s63, s63, 0x20000\n\t" "v_mfma_f32_16x16x32_bf16 v[96:99], v[48:51], a[96:99], 0\n\t" \
    "v_mfma_f32_16x16x32_bf16 v[100:103], v[52:55], a[96:99], 0\n\t" "v_mfma_f32_16x16x32_bf16 v[96:99], v[56:59], a[100:103], v[96:99]\n\t" \
    "v_mfma_f32_16x16x32_bf16 v[100:103], v[60:63], a[100:103], v[100:103]\n\t" "v_mfma_f32_16x16x32_bf16 v[96:99], v[64:67], a[104:107], v[96:99]\n\t" \
    "v_cvt_pk_bf16_f32 v112, v80, v81\n\t" "v_mfma_f32_16x16x32_bf16 v[100:103], v[68:71], a[104:107], v[100:103]\n\t" "v_cvt_pk_bf16_f32 v113, v82, v83\n\t" \
    "v_mfma_f32_16x16x32_bf16 v[96:99], v[72:75], a[108:111], v[96:99]\n\t" "v_cvt_pk_bf16_f32 v114, v84, v85\n\t" \
    "v_mfma_f32_16x16x32_bf16 v[100:103], v[76:79], a[108:111], v[100:103]\n\t" "v_cvt_pk_bf16_f32 v115, v86, v87\n\t" \
    "v_mfma_f32_16x16x32_bf16 v[104:107], v[48:51], a[112:115], 0\n\t" "v_cvt_pk_bf16_f32 v116, v88, v89\n\t" \
    "v_mfma_f32_16x16x32_bf16 v[108:111], v[52:55], a[112:115], 0\n\t" "v_cvt_pk_bf16_f32 v117, v90, v91\n\t" \
    "v_mfma_f32_16x16x32_bf16 v[104:107], v[56:59], a[116:119], v[104:107]\n\t" "buffer_store_dwordx4 v[112:115], %[z1], %[rs], s63 offen sc1\n\t" \
    "v_mfma_f32_16x16x32_bf16 v[108:111], v[60:63], a[116:119], v[108:111]\n\t" "v_mfma_f32_16x16x32_bf16 v[104:107], v[64:67], a[120:123], v[104:107]\n\t" \
    "v_cvt_pk_bf16_f32 v118, v92, v93\n\t" "v_mfma_f32_16x16x32_bf16 v[108:111], v[68:71], a[120:123], v[108:111]\n\t" "v_cvt_pk_bf16_f32 v119, v94, v95\n\t" \
    "v_mfma_f32_16x16x32_bf16 v[104:107], v[72:75], a[124:127], v[104:107]\n\t" "buffer_store_dwordx4 v[116:119], %[z2], %[rs], s63 offen sc1\n\t" \
    "v_mfma_f32_16x16x32_bf16 v[108:111], v[76:79], a[124:127], v[108:111]\n\t" "s_add_u32 s63, s63, 0x20000\n\t" \
    "v_mfma_f32_16x16x32_bf16 v[80:83], v[48:51], a[128:131], 0\n\t" "v_mfma_f32_16x16x32_bf16 v[84:87], v[52:55], a[128:131], 0\n\t" \
    "v_mfma_f32_16x16x32_bf16 v[80:83], v[56:59], a[132:135], v[80:83]\n\t" "v_mfma_f32_16x16x32_bf16 v[84:87], v[60:63], a[132:135], v[84:87]\n\t" \
    "v_mfma_f32_16x16x32_bf16 v[80:83], v[64:67], a[136:139], v[80:83]\n\t" "v_cvt_pk_bf16_f32 v112, v96, v97\n\t" \
    "v_mfma_f32_16x16x32_bf16 v[84:87], v[68:71], a[136:139], v[84:87]\n\t" "v_cvt_pk_bf16_f32 v113, v98, v99\n\t" \
    "v_mfma_f32_16x16x32_bf16 v[80:83], v[72:75], a[140:143], v[80:83]\n\t" "v_cvt_pk_bf16_f32 v114, v100, v101\n\t" \
    "v_mfma_f32_16x16x32_bf16 v[84:87], v[76:79], a[140:143], v[84:87]\n\t" "v_cvt_pk_bf16_f32 v115, v102, v103\n\t" \
    "v_mfma_f32_16x16x32_bf16 v[88:91], v[48:51], a[144:147], 0\n\t" "v_cvt_pk_bf16_f32 v116, v104, v105\n\t" "v_mfma_f32_16x16x32_bf16 v[92:95], v[52:55], a[144:147], 0\n\t" \
    "v_cvt_pk_bf16_f32 v117, v106, v107\n\t" "v_mfma_f32_16x16x32_bf16 v[88:91], v[56:59], a[148:151], v[88:91]\n\t" \
    "buffer_store_dwordx4 v[112:115], %[z1], %[rs], s63 offen sc1\n\t" "v_mfma_f32_16x16x32_bf16 v[92:95], v[60:63], a[148:151], v[92:95]\n\t" \
    "v_mfma_f32_16x16x32_bf16 v[88:91], v[64:67], a[152:155], v[88:91]\n\t" "v_cvt_pk_bf16_f32 v118, v108, v109\n\t" \
    "v_mfma_f32_16x16x32_bf16 v[92:95], v[68:71], a[152:155], v[92:95]\n\t" "v_cvt_pk_bf16_f32 v119, v110, v111\n\t" \
    "v_mfma_f32_16x16x32_bf16 v[88:91], v[72:75], a[156:159], v[88:91]\n\t" "buffer_store_dwordx4 v[116:119], %[z2], %[rs], s63 offen sc1\n\t" \
    "v_mfma_f32_16x16x32_bf16 v[92:95], v[76:79], a[156:159], v[92:95]\n\t" "s_add_u32 s63, s63, 0x20000\n\t" "v_mfma_f32_16x16x32_bf16 v[96:99], v[48:51], a[160:163], 0\n\t" \
    "v_mfma_f32_16x16x32_bf16 v[100:103], v[52:55], a[160:163], 0\n\t" "v_mfma_f32_16x16x32_bf16 v[96:99], v[56:59], a[164:167], v[96:99]\n\t" \
    "v_mfma_f32_16x16x32_bf16 v[100:103], v[60:63], a[164:167], v[100:103]\n\t" "v_mfma_f32_16x16x32_bf16 v[96:99], v[64:67], a[168:171], v[96:99]\n\t" \
    "v_cvt_pk_bf16_f32 v112, v80, v81\n\t" "v_mfma_f32_16x16x32_bf16 v[100:103], v[68:71], a[168:171], v[100:103]\n\t" "v_cvt_pk_bf16_f32 v113, v82, v83\n\t" \
    "v_mfma_f32_16x16x32_bf16 v[96:99], v[72:75], a[172:175], v[96:99]\n\t" "v_cvt_pk_bf16_f32 v114, v84, v85\n\t" \
    "v_mfma_f32_16x16x32_bf16 v[100:103], v[76:79], a[172:175], v[100:103]\n\t" "v_cvt_pk_bf16_f32 v115, v86, v87\n\t" \
    "v_mfma_f32_16x16x32_bf16 v[104:107], v[48:51], a[176:179], 0\n\t" "v_cvt_pk_bf16_f32 v116, v88, v89\n\t" \
    "v_mfma_f32_16x16x32_bf16 v[108:111], v[52:55], a[176:179], 0\n\t" "v_cvt_pk_bf16_f32 v117, v90, v91\n\t" \
    "v_mfma_f32_16x16x32_bf16 v[104:107], v[56:59], a[180:183], v[104:107]\n\t" "buffer_store_dwordx4 v[112:115], %[z1], %[rs], s63 offen sc1\n\t" \
    "v_mfma_f32_16x16x32_bf16 v[108:111], v[60:63], a[180:183], v[108:111]\n\t" "v_mfma_f32_16x16x32_bf16 v[104:107], v[64:67], a[184:187], v[104:107]\n\t" \
    "v_cvt_pk_bf16_f32 v118, v92, v93\n\t" "v_mfma_f32_16x16x32_bf16 v[108:111], v[68:71], a[184:187], v[108:111]\n\t" "v_cvt_pk_bf16_f32 v119, v94, v95\n\t" \
    "v_mfma_f32_16x16x32_bf16 v[104:107], v[72:75], a[188:191], v[104:107]\n\t" "buffer_store_dwordx4 v[116:119], %[z2], %[rs], s63 offen sc1\n\t" \
    "v_mfma_f32_16x16x32_bf16 v[108:111], v[76:79], a[188:191], v[108:111]\n\t" "s_add_u32 s63, s63, 0x20000\n\t" \
    "v_mfma_f32_16x16x32_bf16 v[80:83], v[48:51], a[192:195], 0\n\t" "v_mfma_f32_16x16x32_bf16 v[84:87], v[52:55], a[192:195], 0\n\t" \
    "v_mfma_f32_16x16x32_bf16 v[80:83], v[56:59], a[196:199], v[80:83]\n\t" "v_mfma_f32_16x16x32_bf16 v[84:87], v[60:63], a[196:199], v[84:87]\n\t" \
    "v_mfma_f32_16x16x32_bf16 v[80:83], v[64:67], a[200:203], v[80:83]\n\t" "v_cvt_pk_bf16_f32 v112, v96, v97\n\t" \
    "v_mfma_f32_16x16x32_bf16 v[84:87], v[68:71], a[200:203], v[84:87]\n\t" "v_cvt_pk_bf16_f32 v113, v98, v99\n\t" \
    "v_mfma_f32_16x16x32_bf16 v[80:83], v[72:75], a[204:207], v[80:83]\n\t" "v_cvt_pk_bf16_f32 v114, v100, v101\n\t" \
    "v_mfma_f32_16x16x32_bf16 v[84:87], v[76:79], a[204:207], v[84:87]\n\t" "v_cvt_pk_bf16_f32 v115, v102, v103\n\t" \
    "v_mfma_f32_16x16x32_bf16 v[88:91], v[48:51], a[208:211], 0\n\t" "v_cvt_pk_bf16_f32 v116, v104, v105\n\t" "v_mfma_f32_16x16x32_bf16 v[92:95], v[52:55], a[208:211], 0\n\t" \
    "v_cvt_pk_bf16_f32 v117, v106, v107\n\t" "v_mfma_f32_16x16x32_bf16 v[88:91], v[56:59], a[212:215], v[88:91]\n\t" \
    "buffer_store_dwordx4 v[112:115], %[z1], %[rs], s63 offen sc1\n\t" "v_mfma_f32_16x16x32_bf16 v[92:95], v[60:63], a[212:215], v[92:95]\n\t" \
    "v_mfma_f32_16x16x32_bf16 v[88:91], v[64:67], a[216:219], v[88:91]\n\t" "v_cvt_pk_bf16_f32 v118, v108, v109\n\t" \
    "v_mfma_f32_16x16x32_bf16 v[92:95], v[68:71], a[216:219], v[92:95]\n\t" "v_cvt_pk_bf16_f32 v119, v110, v111\n\t" \
    "v_mfma_f32_16x16x32_bf16 v[88:91], v[72:75], a[220:223], v[88:91]\n\t" "buffer_store_dwordx4 v[116:119], %[z2], %[rs], s63 offen sc1\n\t" \
    "v_mfma_f32_16x16x32_bf16 v[92:95], v[76:79], a[220:223], v[92:95]\n\t" "s_add_u32 s63, s63, 0x20000\n\t" "v_mfma_f32_16x16x32_bf16 v[96:99], v[48:51], a[224:227], 0\n\t" \
    "v_mfma_f32_16x16x32_bf16 v[100:103], v[52:55], a[224:227], 0\n\t" "v_mfma_f32_16x16x32_bf16 v[96:99], v[56:59], a[228:231], v[96:99]\n\t" \
    "v_mfma_f32_16x16x32_bf16 v[100:103], v[60:63], a[228:231], v[100:103]\n\t" "v_mfma_f32_16x16x32_bf16 v[96:99], v[64:67], a[232:235], v[96:99]\n\t" \
    "v_cvt_pk_bf16_f32 v112, v80, v81\n\t" "v_mfma_f32_16x16x32_bf16 v[100:103], v[68:71], a[232:235], v[100:103]\n\t" "v_cvt_pk_bf16_f32 v113, v82, v83\n\t" \
    "v_mfma_f32_16x16x32_bf16 v[96:99], v[72:75], a[236:239], v[96:99]\n\t" "v_cvt_pk_bf16_f32 v114, v84, v85\n\t" \
    "v_mfma_f32_16x16x32_bf16 v[100:103], v[76:79], a[236:239], v[100:103]\n\t" "v_cvt_pk_bf16_f32 v115, v86, v87\n\t" \
    "v_mfma_f32_16x16x32_bf16 v[104:107], v[48:51], a[240:243], 0\n\t" "v_cvt_pk_bf16_f32 v116, v88, v89\n\t" \
    "v_mfma_f32_16x16x32_bf16 v[108:111], v[52:55], a[240:243], 0\n\t" "v_cvt_pk_bf16_f32 v117, v90, v91\n\t" \
    "v_mfma_f32_16x16x32_bf16 v[104:107], v[56:59], a[244:247], v[104:107]\n\t" "buffer_store_dwordx4 v[112:115], %[z1], %[rs], s63 offen sc1\n\t" \
    "v_mfma_f32_16x16x32_bf16 v[108:111], v[60:63], a[244:247], v[108:111]\n\t" "v_mfma_f32_16x16x32_bf16 v[104:107], v[64:67], a[248:251], v[104:107]\n\t" \
    "v_cvt_pk_bf16_f32 v118, v92, v93\n\t" "v_cvt_pk_bf16_f32 v119, v94, v95\n\t" "s_waitcnt vmcnt(11)\n\t" "v_readfirstlane_b32 s66, v120\n\t" \
    "v_mfma_f32_16x16x32_bf16 v[108:111], v[68:71], a[248:251], v[108:111]\n\t" "v_readfirstlane_b32 s67, v121\n\t" "v_mov_b32 v122, s60\n\t" \
    "buffer_store_dword v122, %[fo], %[rc], %[fz] offen sc1\n\t" "v_mfma_f32_16x16x32_bf16 v[104:107], v[72:75], a[252:255], v[104:107]\n\t" \
    "buffer_store_dwordx4 v[116:119], %[z2], %[rs], s63 offen sc1\n\t" "s_add_u32 s63, s63, 0x20000\n\t" "s_max_u32 s61, s61, s66\n\t" \
    "v_mfma_f32_16x16x32_bf16 v[108:111], v[76:79], a[252:255], v[108:111]\n\t" "s_max_u32 s62, s62, s67\n\t" "s_add_u32 s60, s60, 1\n\t" ZSUM "33:\n\t" "s_branch 10b\n\t" \
    "40:\n\t" "v_mfma_f32_16x16x32_bf16 v[80:83], v[16:19], a[0:3], 0\n\t" "v_mfma_f32_16x16x32_bf16 v[84:87], v[20:23], a[0:3], 0\n\t" \
    "v_mfma_f32_16x16x32_bf16 v[80:83], v[24:27], a[4:7], v[80:83]\n\t" "v_mfma_f32_16x16x32_bf16 v[84:87], v[28:31], a[4:7], v[84:87]\n\t" \
    "v_mfma_f32_16x16x32_bf16 v[80:83], v[32:35], a[8:11], v[80:83]\n\t" "v_cvt_pk_bf16_f32 v112, v96, v97\n\t" \
    "v_mfma_f32_16x16x32_bf16 v[84:87], v[36:39], a[8:11], v[84:87]\n\t" "v_cvt_pk_bf16_f32 v113, v98, v99\n\t" \
    "v_mfma_f32_16x16x32_bf16 v[80:83], v[40:43], a[12:15], v[80:83]\n\t" "v_cvt_pk_bf16_f32 v114, v100, v101\n\t" \
    "v_mfma_f32_16x16x32_bf16 v[84:87], v[44:47], a[12:15], v[84:87]\n\t" "v_cvt_pk_bf16_f32 v115, v102, v103\n\t" \
    "v_mfma_f32_16x16x32_bf16 v[88:91], v[16:19], a[16:19], 0\n\t" "v_cvt_pk_bf16_f32 v116, v104, v105\n\t" "v_mfma_f32_16x16x32_bf16 v[92:95], v[20:23], a[16:19], 0\n\t" \
    "v_cvt_pk_bf16_f32 v117, v106, v107\n\t" "v_mfma_f32_16x16x32_bf16 v[88:91], v[24:27], a[20:23], v[88:91]\n\t" \
    "buffer_store_dwordx4 v[112:115], %[z1], %[rs], s63 offen sc1\n\t" "v_mfma_f32_16x16x32_bf16 v[92:95], v[28:31], a[20:23], v[92:95]\n\t" \
    "v_mfma_f32_16x16x32_bf16 v[88:91], v[32:35], a[24:27], v[88:91]\n\t" "v_cvt_pk_bf16_f32 v118, v108, v109\n\t" \
    "v_mfma_f32_16x16x32_bf16 v[92:95], v[36:39], a[24:27], v[92:95]\n\t" "v_cvt_pk_bf16_f32 v119, v110, v111\n\t" \
    "v_mfma_f32_16x16x32_bf16 v[88:91], v[40:43], a[28:31], v[88:91]\n\t" "buffer_store_dwordx4 v[116:119], %[z2], %[rs], s63 offen sc1\n\t" "s_and_b32 s63, s60, %[rm]\n\t" \
    "v_mfma_f32_16x16x32_bf16 v[92:95], v[44:47], a[28:31], v[92:95]\n\t" "s_lshl_b32 s63, s63, 22\n\t" "s_add_u32 s63, s63, %[zb]\n\t" \
    "v_mfma_f32_16x16x32_bf16 v[96:99], v[16:19], a[32:35], 0\n\t" "buffer_load_dword v120, %[fo], %[rc], %[fv] offen sc1\n\t" \
    "v_mfma_f32_16x16x32_bf16 v[100:103], v[20:23], a[32:35], 0\n\t" "v_mfma_f32_16x16x32_bf16 v[96:99], v[24:27], a[36:39], v[96:99]\n\t" \
    "buffer_load_dword v121, %[fo], %[rc], %[fs] offen sc1\n\t" "v_mfma_f32_16x16x32_bf16 v[100:103], v[28:31], a[36:39], v[100:103]\n\t" \
    "v_mfma_f32_16x16x32_bf16 v[96:99], v[32:35], a[40:43], v[96:99]\n\t" "v_cvt_pk_bf16_f32 v112, v80, v81\n\t" \
    "v_mfma_f32_16x16x32_bf16 v[100:103], v[36:39], a[40:43], v[100:103]\n\t" "v_cvt_pk_bf16_f32 v113, v82, v83\n\t" \
    "v_mfma_f32_16x16x32_bf16 v[96:99], v[40:43], a[44:47], v[96:99]\n\t" "v_cvt_pk_bf16_f32 v114, v84, v85\n\t" \
    "v_mfma_f32_16x16x32_bf16 v[100:103], v[44:47], a[44:47], v[100:103]\n\t" "v_cvt_pk_bf16_f32 v115, v86, v87\n\t" \
    "v_mfma_f32_16x16x32_bf16 v[104:107], v[16:19], a[48:51], 0\n\t" "v_cvt_pk_bf16_f32 v116, v88, v89\n\t" "v_mfma_f32_16x16x32_bf16 v[108:111], v[20:23], a[48:51], 0\n\t" \
    "v_cvt_pk_bf16_f32 v117, v90, v91\n\t" "v_mfma_f32_16x16x32_bf16 v[104:107], v[24:27], a[52:55], v[104:107]\n\t" \
    "buffer_store_dwordx4 v[112:115], %[z1], %[rs], s63 offen sc1\n\t" "v_mfma_f32_16x16x32_bf16 v[108:111], v[28:31], a[52:55], v[108:111]\n\t" \
    "v_mfma_f32_16x16x32_bf16 v[104:107], v[32:35], a[56:59], v[104:107]\n\t" "v_cvt_pk_bf16_f32 v118, v92, v93\n\t" \
    "v_mfma_f32_16x16x32_bf16 v[108:111], v[36:39], a[56:59], v[108:111]\n\t" "v_cvt_pk_bf16_f32 v119, v94, v95\n\t" \
    "v_mfma_f32_16x16x32_bf16 v[104:107], v[40:43], a[60:63], v[104:107]\n\t" "buffer_store_dwordx4 v[116:119], %[z2], %[rs], s63 offen sc1\n\t" \
    "v_mfma_f32_16x16x32_bf16 v[108:111], v[44:47], a[60:63], v[108:111]\n\t" "s_add_u32 s63, s63, 0x20000\n\t" "v_mfma_f32_16x16x32_bf16 v[80:83], v[16:19], a[64:67], 0\n\t" \
    "s_add_u32 s65, s60, 1\n\t" "s_lshr_b32 s66, s65, 2\n\t" "v_mfma_f32_16x16x32_bf16 v[84:87], v[20:23], a[64:67], 0\n\t" "s_xor_b32 s66, s66, %[xp]\n\t" \
    "s_and_b32 s66, s66, 1\n\t" "v_mfma_f32_16x16x32_bf16 v[80:83], v[24:27], a[68:71], v[80:83]\n\t" "v_mfma_f32_16x16x32_bf16 v[84:87], v[28:31], a[68:71], v[84:87]\n\t" \
    "v_mfma_f32_16x16x32_bf16 v[80:83], v[32:35], a[72:75], v[80:83]\n\t" "v_cvt_pk_bf16_f32 v112, v96, v97\n\t" \
    "v_mfma_f32_16x16x32_bf16 v[84:87], v[36:39], a[72:75], v[84:87]\n\t" "v_cvt_pk_bf16_f32 v113, v98, v99\n\t" \
    "v_mfma_f32_16x16x32_bf16 v[80:83], v[40:43], a[76:79], v[80:83]\n\t" "v_cvt_pk_bf16_f32 v114, v100, v101\n\t" \
    "v_mfma_f32_16x16x32_bf16 v[84:87], v[44:47], a[76:79], v[84:87]\n\t" "v_cvt_pk_bf16_f32 v115, v102, v103\n\t" \
    "v_mfma_f32_16x16x32_bf16 v[88:91], v[16:19], a[80:83], 0\n\t" "v_cvt_pk_bf16_f32 v116, v104, v105\n\t" "v_mfma_f32_16x16x32_bf16 v[92:95], v[20:23], a[80:83], 0\n\t" \
    "v_cvt_pk_bf16_f32 v117, v106, v107\n\t" "v_mfma_f32_16x16x32_bf16 v[88:91], v[24:27], a[84:87], v[88:91]\n\t" \
    "buffer_store_dwordx4 v[112:115], %[z1], %[rs], s63 offen sc1\n\t" "v_mfma_f32_16x16x32_bf16 v[92:95], v[28:31], a[84:87], v[92:95]\n\t" \
    "v_mfma_f32_16x16x32_bf16 v[88:91], v[32:35], a[88:91], v[88:91]\n\t" "v_cvt_pk_bf16_f32 v118, v108, v109\n\t" \
    "v_mfma_f32_16x16x32_bf16 v[92:95], v[36:39], a[88:91], v[92:95]\n\t" "v_cvt_pk_bf16_f32 v119, v110, v111\n\t" \
    "v_mfma_f32_16x16x32_bf16 v[88:91], v[40:43], a[92:95], v[88:91]\n\t" "buffer_store_dwordx4 v[116:119], %[z2], %[rs], s63 offen sc1\n\t" \
    "v_mfma_f32_16x16x32_bf16 v[92:95], v[44:47], a[92:95], v[92:95]\n\t" "s_add_u32 s63, s63, 0x20000\n\t" "v_mfma_f32_16x16x32_bf16 v[96:99], v[16:19], a[96:99], 0\n\t" \
    "s_lshl_b32 s66, s66, 13\n\t" "s_lshl_b32 s64, s65, 19\n\t" "v_mfma_f32_16x16x32_bf16 v[100:103], v[20:23], a[96:99], 0\n\t" "s_add_u32 s64, s64, s66\n\t" \
    "s_add_u32 s64, s64, %[vb]\n\t" "v_mfma_f32_16x16x32_bf16 v[96:99], v[24:27], a[100:103], v[96:99]\n\t" \
    "v_mfma_f32_16x16x32_bf16 v[100:103], v[28:31], a[100:103], v[100:103]\n\t" "v_mfma_f32_16x16x32_bf16 v[96:99], v[32:35], a[104:107], v[96:99]\n\t" \
    "v_cvt_pk_bf16_f32 v112, v80, v81\n\t" "v_mfma_f32_16x16x32_bf16 v[100:103], v[36:39], a[104:107], v[100:103]\n\t" "v_cvt_pk_bf16_f32 v113, v82, v83\n\t" \
    "v_mfma_f32_16x16x32_bf16 v[96:99], v[40:43], a[108:111], v[96:99]\n\t" "v_cvt_pk_bf16_f32 v114, v84, v85\n\t" \
    "v_mfma_f32_16x16x32_bf16 v[100:103], v[44:47], a[108:111], v[100:103]\n\t" "v_cvt_pk_bf16_f32 v115, v86, v87\n\t" \
    "v_mfma_f32_16x16x32_bf16 v[104:107], v[16:19], a[112:115], 0\n\t" "v_cvt_pk_bf16_f32 v116, v88, v89\n\t" \
    "v_mfma_f32_16x16x32_bf16 v[108:111], v[20:23], a[112:115], 0\n\t" "v_cvt_pk_bf16_f32 v117, v90, v91\n\t" \
    "v_mfma_f32_16x16x32_bf16 v[104:107], v[24:27], a[116:119], v[104:107]\n\t" "buffer_store_dwordx4 v[112:115], %[z1], %[rs], s63 offen sc1\n\t" \
    "v_mfma_f32_16x16x32_bf16 v[108:111], v[28:31], a[116:119], v[108:111]\n\t" "v_mfma_f32_16x16x32_bf16 v[104:107], v[32:35], a[120:123], v[104:107]\n\t" \
    "v_cvt_pk_bf16_f32 v118, v92, v93\n\t" "v_mfma_f32_16x16x32_bf16 v[108:111], v[36:39], a[120:123], v[108:111]\n\t" "v_cvt_pk_bf16_f32 v119, v94, v95\n\t" \
    "v_mfma_f32_16x16x32_bf16 v[104:107], v[40:43], a[124:127], v[104:107]\n\t" "buffer_store_dwordx4 v[116:119], %[z2], %[rs], s63 offen sc1\n\t" \
    "v_mfma_f32_16x16x32_bf16 v[108:111], v[44:47], a[124:127], v[108:111]\n\t" "s_add_u32 s63, s63, 0x20000\n\t" \
    "v_mfma_f32_16x16x32_bf16 v[80:83], v[16:19], a[128:131], 0\n\t" "s_waitcnt vmcnt(6)\n\t" "v_readfirstlane_b32 s66, v120\n\t" "v_readfirstlane_b32 s67, v121\n\t" \
    "v_mfma_f32_16x16x32_bf16 v[84:87], v[20:23], a[128:131], 0\n\t" "v_mov_b32 v122, s60\n\t" "buffer_store_dword v122, %[fo], %[rc], %[fz] offen sc1\n\t" \
    "v_mfma_f32_16x16x32_bf16 v[80:83], v[24:27], a[132:135], v[80:83]\n\t" "s_max_u32 s61, s61, s66\n\t" "s_max_u32 s62, s62, s67\n\t" "s_cmp_gt_u32 s61, s65\n\t" \
    "v_mfma_f32_16x16x32_bf16 v[84:87], v[28:31], a[132:135], v[84:87]\n\t" "s_cselect_b32 s65, 1, 0\n\t" "s_cselect_b32 s64, s64, 0x7f000000\n\t" \
    "v_mfma_f32_16x16x32_bf16 v[80:83], v[32:35], a[136:139], v[80:83]\n\t" "buffer_load_dwordx4 v[48:51], %[ao], %[rs], s64 offen offset:0 sc1\n\t" \
    "v_cvt_pk_bf16_f32 v112, v96, v97\n\t" "v_mfma_f32_16x16x32_bf16 v[84:87], v[36:39], a[136:139], v[84:87]\n\t" "v_cvt_pk_bf16_f32 v113, v98, v99\n\t" \
    "v_mfma_f32_16x16x32_bf16 v[80:83], v[40:43], a[140:143], v[80:83]\n\t" "buffer_load_dwordx4 v[52:55], %[ao], %[rs], s64 offen offset:256 sc1\n\t" \
    "v_cvt_pk_bf16_f32 v114, v100, v101\n\t" "v_mfma_f32_16x16x32_bf16 v[84:87], v[44:47], a[140:143], v[84:87]\n\t" "v_cvt_pk_bf16_f32 v115, v102, v103\n\t" \
    "v_mfma_f32_16x16x32_bf16 v[88:91], v[16:19], a[144:147], 0\n\t" "buffer_load_dwordx4 v[56:59], %[ao], %[rs], s64 offen offset:2048 sc1\n\t" \
    "v_cvt_pk_bf16_f32 v116, v104, v105\n\t" "v_mfma_f32_16x16x32_bf16 v[92:95], v[20:23], a[144:147], 0\n\t" "v_cvt_pk_bf16_f32 v117, v106, v107\n\t" \
    "v_mfma_f32_16x16x32_bf16 v[88:91], v[24:27], a[148:151], v[88:91]\n\t" "buffer_load_dwordx4 v[60:63], %[ao], %[rs], s64 offen offset:2304 sc1\n\t" \
    "v_mfma_f32_16x16x32_bf16 v[92:95], v[28:31], a[148:151], v[92:95]\n\t" "buffer_store_dwordx4 v[112:115], %[z1], %[rs], s63 offen sc1\n\t" \
    "v_mfma_f32_16x16x32_bf16 v[88:91], v[32:35], a[152:155], v[88:91]\n\t" "buffer_load_dwordx4 v[64:67], %[a1], %[rs], s64 offen offset:0 sc1\n\t" \
    "v_cvt_pk_bf16_f32 v118, v108, v109\n\t" "v_mfma_f32_16x16x32_bf16 v[92:95], v[36:39], a[152:155], v[92:95]\n\t" "v_cvt_pk_bf16_f32 v119, v110, v111\n\t" \
    "v_mfma_f32_16x16x32_bf16 v[88:91], v[40:43], a[156:159], v[88:91]\n\t" "buffer_load_dwordx4 v[68:71], %[a1], %[rs], s64 offen offset:256 sc1\n\t" \
    "buffer_store_dwordx4 v[116:119], %[z2], %[rs], s63 offen sc1\n\t" "v_mfma_f32_16x16x32_bf16 v[92:95], v[44:47], a[156:159], v[92:95]\n\t" \
    "s_add_u32 s63, s63, 0x20000\n\t" "v_mfma_f32_16x16x32_bf16 v[96:99], v[16:19], a[160:163], 0\n\t" \
    "buffer_load_dwordx4 v[72:75], %[a1], %[rs], s64 offen offset:2048 sc1\n\t" "v_mfma_f32_16x16x32_bf16 v[100:103], v[20:23], a[160:163], 0\n\t" \
    "v_mfma_f32_16x16x32_bf16 v[96:99], v[24:27], a[164:167], v[96:99]\n\t" "buffer_load_dwordx4 v[76:79], %[a1], %[rs], s64 offen offset:2304 sc1\n\t" \
    "v_mfma_f32_16x16x32_bf16 v[100:103], v[28:31], a[164:167], v[100:103]\n\t" "v_mfma_f32_16x16x32_bf16 v[96:99], v[32:35], a[168:171], v[96:99]\n\t" \
    "v_cvt_pk_bf16_f32 v112, v80, v81\n\t" "v_mfma_f32_16x16x32_bf16 v[100:103], v[36:39], a[168:171], v[100:103]\n\t" "v_cvt_pk_bf16_f32 v113, v82, v83\n\t" \
    "v_mfma_f32_16x16x32_bf16 v[96:99], v[40:43], a[172:175], v[96:99]\n\t" "v_cvt_pk_bf16_f32 v114, v84, v85\n\t" \
    "v_mfma_f32_16x16x32_bf16 v[100:103], v[44:47], a[172:175], v[100:103]\n\t" "v_cvt_pk_bf16_f32 v115, v86, v87\n\t" \
    "v_mfma_f32_16x16x32_bf16 v[104:107], v[16:19], a[176:179], 0\n\t" "v_cvt_pk_bf16_f32 v116, v88, v89\n\t" \
    "v_mfma_f32_16x16x32_bf16 v[108:111], v[20:23], a[176:179], 0\n\t" "v_cvt_pk_bf16_f32 v117, v90, v91\n\t" \
    "v_mfma_f32_16x16x32_bf16 v[104:107], v[24:27], a[180:183], v[104:107]\n\t" "buffer_store_dwordx4 v[112:115], %[z1], %[rs], s63 offen sc1\n\t" \
    "v_mfma_f32_16x16x32_bf16 v[108:111], v[28:31], a[180:183], v[108:111]\n\t" "v_mfma_f32_16x16x32_bf16 v[104:107], v[32:35], a[184:187], v[104:107]\n\t" \
    "v_cvt_pk_bf16_f32 v118, v92, v93\n\t" "v_mfma_f32_16x16x32_bf16 v[108:111], v[36:39], a[184:187], v[108:111]\n\t" "v_cvt_pk_bf16_f32 v119, v94, v95\n\t" \
    "v_mfma_f32_16x16x32_bf16 v[104:107], v[40:43], a[188:191], v[104:107]\n\t" "buffer_store_dwordx4 v[116:119], %[z2], %[rs], s63 offen sc1\n\t" \
    "v_mfma_f32_16x16x32_bf16 v[108:111], v[44:47], a[188:191], v[108:111]\n\t" "s_add_u32 s63, s63, 0x20000\n\t" \
    "v_mfma_f32_16x16x32_bf16 v[80:83], v[16:19], a[192:195], 0\n\t" "v_mfma_f32_16x16x32_bf16 v[84:87], v[20:23], a[192:195], 0\n\t" \
    "v_mfma_f32_16x16x32_bf16 v[80:83], v[24:27], a[196:199], v[80:83]\n\t" "v_mfma_f32_16x16x32_bf16 v[84:87], v[28:31], a[196:199], v[84:87]\n\t" \
    "v_mfma_f32_16x16x32_bf16 v[80:83], v[32:35], a[200:203], v[80:83]\n\t" "v_cvt_pk_bf16_f32 v112, v96, v97\n\t" \
    "v_mfma_f32_16x16x32_bf16 v[84:87], v[36:39], a[200:203], v[84:87]\n\t" "v_cvt_pk_bf16_f32 v113, v98, v99\n\t" \
    "v_mfma_f32_16x16x32_bf16 v[80:83], v[40:43], a[204:207], v[80:83]\n\t" "v_cvt_pk_bf16_f32 v114, v100, v101\n\t" \
    "v_mfma_f32_16x16x32_bf16 v[84:87], v[44:47], a[204:207], v[84:87]\n\t" "v_cvt_pk_bf16_f32 v115, v102, v103\n\t" \
    "v_mfma_f32_16x16x32_bf16 v[88:91], v[16:19], a[208:211], 0\n\t" "v_cvt_pk_bf16_f32 v116, v104, v105\n\t" "v_mfma_f32_16x16x32_bf16 v[92:95], v[20:23], a[208:211], 0\n\t" \
    "v_cvt_pk_bf16_f32 v117, v106, v107\n\t" "v_mfma_f32_16x16x32_bf16 v[88:91], v[24:27], a[212:215], v[88:91]\n\t" \
    "buffer_store_dwordx4 v[112:115], %[z1], %[rs], s63 offen sc1\n\t" "v_mfma_f32_16x16x32_bf16 v[92:95], v[28:31], a[212:215], v[92:95]\n\t" \
    "v_mfma_f32_16x16x32_bf16 v[88:91], v[32:35], a[216:219], v[88:91]\n\t" "v_cvt_pk_bf16_f32 v118, v108, v109\n\t" \
    "v_mfma_f32_16x16x32_bf16 v[92:95], v[36:39], a[216:219], v[92:95]\n\t" "v_cvt_pk_bf16_f32 v119, v110, v111\n\t" \
    "v_mfma_f32_16x16x32_bf16 v[88:91], v[40:43], a[220:223], v[88:91]\n\t" "buffer_store_dwordx4 v[116:119], %[z2], %[rs], s63 offen sc1\n\t" \
    "v_mfma_f32_16x16x32_bf16 v[92:95], v[44:47], a[220:223], v[92:95]\n\t" "s_add_u32 s63, s63, 0x20000\n\t" "v_mfma_f32_16x16x32_bf16 v[96:99], v[16:19], a[224:227], 0\n\t" \
    "v_mfma_f32_16x16x32_bf16 v[100:103], v[20:23], a[224:227], 0\n\t" "v_mfma_f32_16x16x32_bf16 v[96:99], v[24:27], a[228:231], v[96:99]\n\t" \
    "v_mfma_f32_16x16x32_bf16 v[100:103], v[28:31], a[228:231], v[100:103]\n\t" "v_mfma_f32_16x16x32_bf16 v[96:99], v[32:35], a[232:235], v[96:99]\n\t" \
    "v_cvt_pk_bf16_f32 v112, v80, v81\n\t" "v_mfma_f32_16x16x32_bf16 v[100:103], v[36:39], a[232:235], v[100:103]\n\t" "v_cvt_pk_bf16_f32 v113, v82, v83\n\t" \
    "v_mfma_f32_16x16x32_bf16 v[96:99], v[40:43], a[236:239], v[96:99]\n\t" "v_cvt_pk_bf16_f32 v114, v84, v85\n\t" \
    "v_mfma_f32_16x16x32_bf16 v[100:103], v[44:47], a[236:239], v[100:103]\n\t" "v_cvt_pk_bf16_f32 v115, v86, v87\n\t" \
    "v_mfma_f32_16x16x32_bf16 v[104:107], v[16:19], a[240:243], 0\n\t" "v_cvt_pk_bf16_f32 v116, v88, v89\n\t" \
    "v_mfma_f32_16x16x32_bf16 v[108:111], v[20:23], a[240:243], 0\n\t" "v_cvt_pk_bf16_f32 v117, v90, v91\n\t" \
    "v_mfma_f32_16x16x32_bf16 v[104:107], v[24:27], a[244:247], v[104:107]\n\t" "buffer_store_dwordx4 v[112:115], %[z1], %[rs], s63 offen sc1\n\t" \
    "v_mfma_f32_16x16x32_bf16 v[108:111], v[28:31], a[244:247], v[108:111]\n\t" "v_mfma_f32_16x16x32_bf16 v[104:107], v[32:35], a[248:251], v[104:107]\n\t" \
    "v_cvt_pk_bf16_f32 v118, v92, v93\n\t" "v_mfma_f32_16x16x32_bf16 v[108:111], v[36:39], a[248:251], v[108:111]\n\t" "v_cvt_pk_bf16_f32 v119, v94, v95\n\t" \
    "v_mfma_f32_16x16x32_bf16 v[104:107], v[40:43], a[252:255], v[104:107]\n\t" "buffer_store_dwordx4 v[116:119], %[z2], %[rs], s63 offen sc1\n\t" \
    "s_add_u32 s63, s63, 0x20000\n\t" "s_add_u32 s60, s60, 1\n\t" "v_mfma_f32_16x16x32_bf16 v[108:111], v[44:47], a[252:255], v[108:111]\n\t" "s_cmp_eq_u32 s65, 0\n\t" \
    "s_waitcnt vmcnt(6)\n\t" ZSUM "s_cbranch_scc0 23b\n\t" "s_branch 20f\n\t" "41:\n\t" "v_mfma_f32_16x16x32_bf16 v[80:83], v[48:51], a[0:3], 0\n\t" \
    "v_mfma_f32_16x16x32_bf16 v[84:87], v[52:55], a[0:3], 0\n\t" "v_mfma_f32_16x16x32_bf16 v[80:83], v[56:59], a[4:7], v[80:83]\n\t" \
    "v_mfma_f32_16x16x32_bf16 v[84:87], v[60:63], a[4:7], v[84:87]\n\t" "v_mfma_f32_16x16x32_bf16 v[80:83], v[64:67], a[8:11], v[80:83]\n\t" \
    "v_cvt_pk_bf16_f32 v112, v96, v97\n\t" "v_mfma_f32_16x16x32_bf16 v[84:87], v[68:71], a[8:11], v[84:87]\n\t" "v_cvt_pk_bf16_f32 v113, v98, v99\n\t" \
    "v_mfma_f32_16x16x32_bf16 v[80:83], v[72:75], a[12:15], v[80:83]\n\t" "v_cvt_pk_bf16_f32 v114, v100, v101\n\t" \
    "v_mfma_f32_16x16x32_bf16 v[84:87], v[76:79], a[12:15], v[84:87]\n\t" "v_cvt_pk_bf16_f32 v115, v102, v103\n\t" \
    "v_mfma_f32_16x16x32_bf16 v[88:91], v[48:51], a[16:19], 0\n\t" "v_cvt_pk_bf16_f32 v116, v104, v105\n\t" "v_mfma_f32_16x16x32_bf16 v[92:95], v[52:55], a[16:19], 0\n\t" \
    "v_cvt_pk_bf16_f32 v117, v106, v107\n\t" "v_mfma_f32_16x16x32_bf16 v[88:91], v[56:59], a[20:23], v[88:91]\n\t" \
    "buffer_store_dwordx4 v[112:115], %[z1], %[rs], s63 offen sc1\n\t" "v_mfma_f32_16x16x32_bf16 v[92:95], v[60:63], a[20:23], v[92:95]\n\t" \
    "v_mfma_f32_16x16x32_bf16 v[88:91], v[64:67], a[24:27], v[88:91]\n\t" "v_cvt_pk_bf16_f32 v118, v108, v109\n\t" \
    "v_mfma_f32_16x16x32_bf16 v[92:95], v[68:71], a[24:27], v[92:95]\n\t" "v_cvt_pk_bf16_f32 v119, v110, v111\n\t" \
    "v_mfma_f32_16x16x32_bf16 v[88:91], v[72:75], a[28:31], v[88:91]\n\t" "buffer_store_dwordx4 v[116:119], %[z2], %[rs], s63 offen sc1\n\t" "s_and_b32 s63, s60, %[rm]\n\t" \
    "v_mfma_f32_16x16x32_bf16 v[92:95], v[76:79], a[28:31], v[92:95]\n\t" "s_lshl_b32 s63, s63, 22\n\t" "s_add_u32 s63, s63, %[zb]\n\t" \
    "v_mfma_f32_16x16x32_bf16 v[96:99], v[48:51], a[32:35], 0\n\t" "buffer_load_dword v120, %[fo], %[rc], %[fv] offen sc1\n\t" \
    "v_mfma_f32_16x16x32_bf16 v[100:103], v[52:55], a[32:35], 0\n\t" "v_mfma_f32_16x16x32_bf16 v[96:99], v[56:59], a[36:39], v[96:99]\n\t" \
    "buffer_load_dword v121, %[fo], %[rc], %[fs] offen sc1\n\t" "v_mfma_f32_16x16x32_bf16 v[100:103], v[60:63], a[36:39], v[100:103]\n\t" \
    "v_mfma_f32_16x16x32_bf16 v[96:99], v[64:67], a[40:43], v[96:99]\n\t" "v_cvt_pk_bf16_f32 v112, v80, v81\n\t" \
    "v_mfma_f32_16x16x32_bf16 v[100:103], v[68:71], a[40:43], v[100:103]\n\t" "v_cvt_pk_bf16_f32 v113, v82, v83\n\t" \
    "v_mfma_f32_16x16x32_bf16 v[96:99], v[72:75], a[44:47], v[96:99]\n\t" "v_cvt_pk_bf16_f32 v114, v84, v85\n\t" \
    "v_mfma_f32_16x16x32_bf16 v[100:103], v[76:79], a[44:47], v[100:103]\n\t" "v_cvt_pk_bf16_f32 v115, v86, v87\n\t" \
    "v_mfma_f32_16x16x32_bf16 v[104:107], v[48:51], a[48:51], 0\n\t" "v_cvt_pk_bf16_f32 v116, v88, v89\n\t" "v_mfma_f32_16x16x32_bf16 v[108:111], v[52:55], a[48:51], 0\n\t" \
    "v_cvt_pk_bf16_f32 v117, v90, v91\n\t" "v_mfma_f32_16x16x32_bf16 v[104:107], v[56:59], a[52:55], v[104:107]\n\t" \
    "buffer_store_dwordx4 v[112:115], %[z1], %[rs], s63 offen sc1\n\t" "v_mfma_f32_16x16x32_bf16 v[108:111], v[60:63], a[52:55], v[108:111]\n\t" \
    "v_mfma_f32_16x16x32_bf16 v[104:107], v[64:67], a[56:59], v[104:107]\n\t" "v_cvt_pk_bf16_f32 v118, v92, v93\n\t" \
    "v_mfma_f32_16x16x32_bf16 v[108:111], v[68:71], a[56:59], v[108:111]\n\t" "v_cvt_pk_bf16_f32 v119, v94, v95\n\t" \
    "v_mfma_f32_16x16x32_bf16 v[104:107], v[72:75], a[60:63], v[104:107]\n\t" "buffer_store_dwordx4 v[116:119], %[z2], %[rs], s63 offen sc1\n\t" \
    "v_mfma_f32_16x16x32_bf16 v[108:111], v[76:79], a[60:63], v[108:111]\n\t" "s_add_u32 s63, s63, 0x20000\n\t" "v_mfma_f32_16x16x32_bf16 v[80:83], v[48:51], a[64:67], 0\n\t" \
    "s_add_u32 s65, s60, 1\n\t" "s_lshr_b32 s66, s65, 2\n\t" "v_mfma_f32_16x16x32_bf16 v[84:87], v[52:55], a[64:67], 0\n\t" "s_xor_b32 s66, s66, %[xp]\n\t" \
    "s_and_b32 s66, s66, 1\n\t" "v_mfma_f32_16x16x32_bf16 v[80:83], v[56:59], a[68:71], v[80:83]\n\t" "v_mfma_f32_16x16x32_bf16 v[84:87], v[60:63], a[68:71], v[84:87]\n\t" \
    "v_mfma_f32_16x16x32_bf16 v[80:83], v[64:67], a[72:75], v[80:83]\n\t" "v_cvt_pk_bf16_f32 v112, v96, v97\n\t" \
    "v_mfma_f32_16x16x32_bf16 v[84:87], v[68:71], a[72:75], v[84:87]\n\t" "v_cvt_pk_bf16_f32 v113, v98, v99\n\t" \
    "v_mfma_f32_16x16x32_bf16 v[80:83], v[72:75], a[76:79], v[80:83]\n\t" "v_cvt_pk_bf16_f32 v114, v100, v101\n\t" \
    "v_mfma_f32_16x16x32_bf16 v[84:87], v[76:79], a[76:79], v[84:87]\n\t" "v_cvt_pk_bf16_f32 v115, v102, v103\n\t" \
    "v_mfma_f32_16x16x32_bf16 v[88:91], v[48:51], a[80:83], 0\n\t" "v_cvt_pk_bf16_f32 v116, v104, v105\n\t" "v_mfma_f32_16x16x32_bf16 v[92:95], v[52:55], a[80:83], 0\n\t" \
    "v_cvt_pk_bf16_f32 v117, v106, v107\n\t" "v_mfma_f32_16x16x32_bf16 v[88:91], v[56:59], a[84:87], v[88:91]\n\t" \
    "buffer_store_dwordx4 v[112:115], %[z1], %[rs], s63 offen sc1\n\t" "v_mfma_f32_16x16x32_bf16 v[92:95], v[60:63], a[84:87], v[92:95]\n\t" \
    "v_mfma_f32_16x16x32_bf16 v[88:91], v[64:67], a[88:91], v[88:91]\n\t" "v_cvt_pk_bf16_f32 v118, v108, v109\n\t" \
    "v_mfma_f32_16x16x32_bf16 v[92:95], v[68:71], a[88:91], v[92:95]\n\t" "v_cvt_pk_bf16_f32 v119, v110, v111\n\t" \
    "v_mfma_f32_16x16x32_bf16 v[88:91], v[72:75], a[92:95], v[88:91]\n\t" "buffer_store_dwordx4 v[116:119], %[z2], %[rs], s63 offen sc1\n\t" \
    "v_mfma_f32_16x16x32_bf16 v[92:95], v[76:79], a[92:95], v[92:95]\n\t" "s_add_u32 s63, s63, 0x20000\n\t" "v_mfma_f32_16x16x32_bf16 v[96:99], v[48:51], a[96:99], 0\n\t" \
    "s_lshl_b32 s66, s66, 13\n\t" "s_lshl_b32 s64, s65, 19\n\t" "v_mfma_f32_16x16x32_bf16 v[100:103], v[52:55], a[96:99], 0\n\t" "s_add_u32 s64, s64, s66\n\t" \
    "s_add_u32 s64, s64, %[vb]\n\t" "v_mfma_f32_16x16x32_bf16 v[96:99], v[56:59], a[100:103], v[96:99]\n\t" \
    "v_mfma_f32_16x16x32_bf16 v[100:103], v[60:63], a[100:103], v[100:103]\n\t" "v_mfma_f32_16x16x32_bf16 v[96:99], v[64:67], a[104:107], v[96:99]\n\t" \
    "v_cvt_pk_bf16_f32 v112, v80, v81\n\t" "v_mfma_f32_16x16x32_bf16 v[100:103], v[68:71], a[104:107], v[100:103]\n\t" "v_cvt_pk_bf16_f32 v113, v82, v83\n\t" \
    "v_mfma_f32_16x16x32_bf16 v[96:99], v[72:75], a[108:111], v[96:99]\n\t" "v_cvt_pk_bf16_f32 v114, v84, v85\n\t" \
    "v_mfma_f32_16x16x32_bf16 v[100:103], v[76:79], a[108:111], v[100:103]\n\t" "v_cvt_pk_bf16_f32 v115, v86, v87\n\t" \
    "v_mfma_f32_16x16x32_bf16 v[104:107], v[48:51], a[112:115], 0\n\t" "v_cvt_pk_bf16_f32 v116, v88, v89\n\t" \
    "v_mfma_f32_16x16x32_bf16 v[108:111], v[52:55], a[112:115], 0\n\t" "v_cvt_pk_bf16_f32 v117, v90, v91\n\t" \
    "v_mfma_f32_16x16x32_bf16 v[104:107], v[56:59], a[116:119], v[104:107]\n\t" "buffer_store_dwordx4 v[112:115], %[z1], %[rs], s63 offen sc1\n\t" \
    "v_mfma_f32_16x16x32_bf16 v[108:111], v[60:63], a[116:119], v[108:111]\n\t" "v_mfma_f32_16x16x32_bf16 v[104:107], v[64:67], a[120:123], v[104:107]\n\t" \
    "v_cvt_pk_bf16_f32 v118, v92, v93\n\t" "v_mfma_f32_16x16x32_bf16 v[108:111], v[68:71], a[120:123], v[108:111]\n\t" "v_cvt_pk_bf16_f32 v119, v94, v95\n\t" \
    "v_mfma_f32_16x16x32_bf16 v[104:107], v[72:75], a[124:127], v[104:107]\n\t" "buffer_store_dwordx4 v[116:119], %[z2], %[rs], s63 offen sc1\n\t" \
    "v_mfma_f32_16x16x32_bf16 v[108:111], v[76:79], a[124:127], v[108:111]\n\t" "s_add_u32 s63, s63, 0x20000\n\t" \
    "v_mfma_f32_16x16x32_bf16 v[80:83], v[48:51], a[128:131], 0\n\t" "s_waitcnt vmcnt(6)\n\t" "v_readfirstlane_b32 s66, v120\n\t" "v_readfirstlane_b32 s67, v121\n\t" \
    "v_mfma_f32_16x16x32_bf16 v[84:87], v[52:55], a[128:131], 0\n\t" "v_mov_b32 v122, s60\n\t" "buffer_store_dword v122, %[fo], %[rc], %[fz] offen sc1\n\t" \
    "v_mfma_f32_16x16x32_bf16 v[80:83], v[56:59], a[132:135], v[80:83]\n\t" "s_max_u32 s61, s61, s66\n\t" "s_max_u32 s62, s62, s67\n\t" "s_cmp_gt_u32 s61, s65\n\t" \
    "v_mfma_f32_16x16x32_bf16 v[84:87], v[60:63], a[132:135], v[84:87]\n\t" "s_cselect_b32 s65, 1, 0\n\t" "s_cselect_b32 s64, s64, 0x7f000000\n\t" \
    "v_mfma_f32_16x16x32_bf16 v[80:83], v[64:67], a[136:139], v[80:83]\n\t" "buffer_load_dwordx4 v[16:19], %[ao], %[rs], s64 offen offset:0 sc1\n\t" \
    "v_cvt_pk_bf16_f32 v112, v96, v97\n\t" "v_mfma_f32_16x16x32_bf16 v[84:87], v[68:71], a[136:139], v[84:87]\n\t" "v_cvt_pk_bf16_f32 v113, v98, v99\n\t" \
    "v_mfma_f32_16x16x32_bf16 v[80:83], v[72:75], a[140:143], v[80:83]\n\t" "buffer_load_dwordx4 v[20:23], %[ao], %[rs], s64 offen offset:256 sc1\n\t" \
    "v_cvt_pk_bf16_f32 v114, v100, v101\n\t" "v_mfma_f32_16x16x32_bf16 v[84:87], v[76:79], a[140:143], v[84:87]\n\t" "v_cvt_pk_bf16_f32 v115, v102, v103\n\t" \
    "v_mfma_f32_16x16x32_bf16 v[88:91], v[48:51], a[144:147], 0\n\t" "buffer_load_dwordx4 v[24:27], %[ao], %[rs], s64 offen offset:2048 sc1\n\t" \
    "v_cvt_pk_bf16_f32 v116, v104, v105\n\t" "v_mfma_f32_16x16x32_bf16 v[92:95], v[52:55], a[144:147], 0\n\t" "v_cvt_pk_bf16_f32 v117, v106, v107\n\t" \
    "v_mfma_f32_16x16x32_bf16 v[88:91], v[56:59], a[148:151], v[88:91]\n\t" "buffer_load_dwordx4 v[28:31], %[ao], %[rs], s64 offen offset:2304 sc1\n\t" \
    "v_mfma_f32_16x16x32_bf16 v[92:95], v[60:63], a[148:151], v[92:95]\n\t" "buffer_store_dwordx4 v[112:115], %[z1], %[rs], s63 offen sc1\n\t" \
    "v_mfma_f32_16x16x32_bf16 v[88:91], v[64:67], a[152:155], v[88:91]\n\t" "buffer_load_dwordx4 v[32:35], %[a1], %[rs], s64 offen offset:0 sc1\n\t" \
    "v_cvt_pk_bf16_f32 v118, v108, v109\n\t" "v_mfma_f32_16x16x32_bf16 v[92:95], v[68:71], a[152:155], v[92:95]\n\t" "v_cvt_pk_bf16_f32 v119, v110, v111\n\t" \
    "v_mfma_f32_16x16x32_bf16 v[88:91], v[72:75], a[156:159], v[88:91]\n\t" "buffer_load_dwordx4 v[36:39], %[a1], %[rs], s64 offen offset:256 sc1\n\t" \
    "buffer_store_dwordx4 v[116:119], %[z2], %[rs], s63 offen sc1\n\t" "v_mfma_f32_16x16x32_bf16 v[92:95], v[76:79], a[156:159], v[92:95]\n\t" \
    "s_add_u32 s63, s63, 0x20000\n\t" "v_mfma_f32_16x16x32_bf16 v[96:99], v[48:51], a[160:163], 0\n\t" \
    "buffer_load_dwordx4 v[40:43], %[a1], %[rs], s64 offen offset:2048 sc1\n\t" "v_mfma_f32_16x16x32_bf16 v[100:103], v[52:55], a[160:163], 0\n\t" \
    "v_mfma_f32_16x16x32_bf16 v[96:99], v[56:59], a[164:167], v[96:99]\n\t" "buffer_load_dwordx4 v[44:47], %[a1], %[rs], s64 offen offset:2304 sc1\n\t" \
    "v_mfma_f32_16x16x32_bf16 v[100:103], v[60:63], a[164:167], v[100:103]\n\t" "v_mfma_f32_16x16x32_bf16 v[96:99], v[64:67], a[168:171], v[96:99]\n\t" \
    "v_cvt_pk_bf16_f32 v112, v80, v81\n\t" "v_mfma_f32_16x16x32_bf16 v[100:103], v[68:71], a[168:171], v[100:103]\n\t" "v_cvt_pk_bf16_f32 v113, v82, v83\n\t" \
    "v_mfma_f32_16x16x32_bf16 v[96:99], v[72:75], a[172:175], v[96:99]\n\t" "v_cvt_pk_bf16_f32 v114, v84, v85\n\t" \
    "v_mfma_f32_16x16x32_bf16 v[100:103], v[76:79], a[172:175], v[100:103]\n\t" "v_cvt_pk_bf16_f32 v115, v86, v87\n\t" \
    "v_mfma_f32_16x16x32_bf16 v[104:107], v[48:51], a[176:179], 0\n\t" "v_cvt_pk_bf16_f32 v116, v88, v89\n\t" \
    "v_mfma_f32_16x16x32_bf16 v[108:111], v[52:55], a[176:179], 0\n\t" "v_cvt_pk_bf16_f32 v117, v90, v91\n\t" \
    "v_mfma_f32_16x16x32_bf16 v[104:107], v[56:59], a[180:183], v[104:107]\n\t" "buffer_store_dwordx4 v[112:115], %[z1], %[rs], s63 offen sc1\n\t" \
    "v_mfma_f32_16x16x32_bf16 v[108:111], v[60:63], a[180:183], v[108:111]\n\t" "v_mfma_f32_16x16x32_bf16 v[104:107], v[64:67], a[184:187], v[104:107]\n\t" \
    "v_cvt_pk_bf16_f32 v118, v92, v93\n\t" "v_mfma_f32_16x16x32_bf16 v[108:111], v[68:71], a[184:187], v[108:111]\n\t" "v_cvt_pk_bf16_f32 v119, v94, v95\n\t" \
    "v_mfma_f32_16x16x32_bf16 v[104:107], v[72:75], a[188:191], v[104:107]\n\t" "buffer_store_dwordx4 v[116:119], %[z2], %[rs], s63 offen sc1\n\t" \
    "v_mfma_f32_16x16x32_bf16 v[108:111], v[76:79], a[188:191], v[108:111]\n\t" "s_add_u32 s63, s63, 0x20000\n\t" \
    "v_mfma_f32_16x16x32_bf16 v[80:83], v[48:51], a[192:195], 0\n\t" "v_mfma_f32_16x16x32_bf16 v[84:87], v[52:55], a[192:195], 0\n\t" \
    "v_mfma_f32_16x16x32_bf16 v[80:83], v[56:59], a[196:199], v[80:83]\n\t" "v_mfma_f32_16x16x32_bf16 v[84:87], v[60:63], a[196:199], v[84:87]\n\t" \
    "v_mfma_f32_16x16x32_bf16 v[80:83], v[64:67], a[200:203], v[80:83]\n\t" "v_cvt_pk_bf16_f32 v112, v96, v97\n\t" \
    "v_mfma_f32_16x16x32_bf16 v[84:87], v[68:71], a[200:203], v[84:87]\n\t" "v_cvt_pk_bf16_f32 v113, v98, v99\n\t" \
    "v_mfma_f32_16x16x32_bf16 v[80:83], v[72:75], a[204:207], v[80:83]\n\t" "v_cvt_pk_bf16_f32 v114, v100, v101\n\t" \
    "v_mfma_f32_16x16x32_bf16 v[84:87], v[76:79], a[204:207], v[84:87]\n\t" "v_cvt_pk_bf16_f32 v115, v102, v103\n\t" \
    "v_mfma_f32_16x16x32_bf16 v[88:91], v[48:51], a[208:211], 0\n\t" "v_cvt_pk_bf16_f32 v116, v104, v105\n\t" "v_mfma_f32_16x16x32_bf16 v[92:95], v[52:55], a[208:211], 0\n\t" \
    "v_cvt_pk_bf16_f32 v117, v106, v107\n\t" "v_mfma_f32_16x16x32_bf16 v[88:91], v[56:59], a[212:215], v[88:91]\n\t" \
    "buffer_store_dwordx4 v[112:115], %[z1], %[rs], s63 offen sc1\n\t" "v_mfma_f32_16x16x32_bf16 v[92:95], v[60:63], a[212:215], v[92:95]\n\t" \
    "v_mfma_f32_16x16x32_bf16 v[88:91], v[64:67], a[216:219], v[88:91]\n\t" "v_cvt_pk_bf16_f32 v118, v108, v109\n\t" \
    "v_mfma_f32_16x16x32_bf16 v[92:95], v[68:71], a[216:219], v[92:95]\n\t" "v_cvt_pk_bf16_f32 v119, v110, v111\n\t" \
    "v_mfma_f32_16x16x32_bf16 v[88:91], v[72:75], a[220:223], v[88:91]\n\t" "buffer_store_dwordx4 v[116:119], %[z2], %[rs], s63 offen sc1\n\t" \
    "v_mfma_f32_16x16x32_bf16 v[92:95], v[76:79], a[220:223], v[92:95]\n\t" "s_add_u32 s63, s63, 0x20000\n\t" "v_mfma_f32_16x16x32_bf16 v[96:99], v[48:51], a[224:227], 0\n\t" \
    "v_mfma_f32_16x16x32_bf16 v[100:103], v[52:55], a[224:227], 0\n\t" "v_mfma_f32_16x16x32_bf16 v[96:99], v[56:59], a[228:231], v[96:99]\n\t" \
    "v_mfma_f32_16x16x32_bf16 v[100:103], v[60:63], a[228:231], v[100:103]\n\t" "v_mfma_f32_16x16x32_bf16 v[96:99], v[64:67], a[232:235], v[96:99]\n\t" \
    "v_cvt_pk_bf16_f32 v112, v80, v81\n\t" "v_mfma_f32_16x16x32_bf16 v[100:103], v[68:71], a[232:235], v[100:103]\n\t" "v_cvt_pk_bf16_f32 v113, v82, v83\n\t" \
    "v_mfma_f32_16x16x32_bf16 v[96:99], v[72:75], a[236:239], v[96:99]\n\t" "v_cvt_pk_bf16_f32 v114, v84, v85\n\t" \
    "v_mfma_f32_16x16x32_bf16 v[100:103], v[76:79], a[236:239], v[100:103]\n\t" "v_cvt_pk_bf16_f32 v115, v86, v87\n\t" \
    "v_mfma_f32_16x16x32_bf16 v[104:107], v[48:51], a[240:243], 0\n\t" "v_cvt_pk_bf16_f32 v116, v88, v89\n\t" \
    "v_mfma_f32_16x16x32_bf16 v[108:111], v[52:55], a[240:243], 0\n\t" "v_cvt_pk_bf16_f32 v117, v90, v91\n\t" \
    "v_mfma_f32_16x16x32_bf16 v[104:107], v[56:59], a[244:247], v[104:107]\n\t" "buffer_store_dwordx4 v[112:115], %[z1], %[rs], s63 offen sc1\n\t" \
    "v_mfma_f32_16x16x32_bf16 v[108:111], v[60:63], a[244:247], v[108:111]\n\t" "v_mfma_f32_16x16x32_bf16 v[104:107], v[64:67], a[248:251], v[104:107]\n\t" \
    "v_cvt_pk_bf16_f32 v118, v92, v93\n\t" "v_mfma_f32_16x16x32_bf16 v[108:111], v[68:71], a[248:251], v[108:111]\n\t" "v_cvt_pk_bf16_f32 v119, v94, v95\n\t" \
    "v_mfma_f32_16x16x32_bf16 v[104:107], v[72:75], a[252:255], v[104:107]\n\t" "buffer_store_dwordx4 v[116:119], %[z2], %[rs], s63 offen sc1\n\t" \
    "s_add_u32 s63, s63, 0x20000\n\t" "s_add_u32 s60, s60, 1\n\t" "v_mfma_f32_16x16x32_bf16 v[108:111], v[76:79], a[252:255], v[108:111]\n\t" "s_cmp_eq_u32 s65, 0\n\t" \
    "s_waitcnt vmcnt(6)\n\t" ZSUM "s_cbranch_scc0 33b\n\t" "s_branch 30f\n\t" "20:\n\t" "s_nop 7\n\t" "v_cvt_pk_bf16_f32 v112, v96, v97\n\t" \
    "v_cvt_pk_bf16_f32 v113, v98, v99\n\t" "v_cvt_pk_bf16_f32 v114, v100, v101\n\t" "v_cvt_pk_bf16_f32 v115, v102, v103\n\t" "v_cvt_pk_bf16_f32 v116, v104, v105\n\t" \
    "v_cvt_pk_bf16_f32 v117, v106, v107\n\t" "v_cvt_pk_bf16_f32 v118, v108, v109\n\t" "v_cvt_pk_bf16_f32 v119, v110, v111\n\t" \
    "buffer_store_dwordx4 v[112:115], %[z1], %[rs], s63 offen sc1\n\t" "buffer_store_dwordx4 v[116:119], %[z2], %[rs], s63 offen sc1\n\t" \
    "buffer_load_dword v120, %[fo], %[rc], %[fv] offen sc1\n\t" "s_waitcnt vmcnt(0)\n\t" "v_readfirstlane_b32 s61, v120\n\t" "s_lshr_b32 s66, s60, 2\n\t" \
    "s_xor_b32 s66, s66, %[xp]\n\t" "s_and_b32 s66, s66, 1\n\t" "s_lshl_b32 s66, s66, 13\n\t" "s_lshl_b32 s64, s60, 19\n\t" "s_add_u32 s64, s64, s66\n\t" \
    "s_add_u32 s64, s64, %[vb]\n\t" "s_cmp_gt_u32 s61, s60\n\t" "s_cbranch_scc0 24f\n\t" "s_cmp_eq_u32 s60, 64\n\t" "s_cbranch_scc1 24f\n\t" \
    "buffer_load_dwordx4 v[48:51], %[ao], %[rs], s64 offen offset:0 sc1\n\t" "buffer_load_dwordx4 v[52:55], %[ao], %[rs], s64 offen offset:256 sc1\n\t" \
    "buffer_load_dwordx4 v[56:59], %[ao], %[rs], s64 offen offset:2048 sc1\n\t" "buffer_load_dwordx4 v[60:63], %[ao], %[rs], s64 offen offset:2304 sc1\n\t" \
    "buffer_load_dwordx4 v[64:67], %[a1], %[rs], s64 offen offset:0 sc1\n\t" "buffer_load_dwordx4 v[68:71], %[a1], %[rs], s64 offen offset:256 sc1\n\t" \
    "buffer_load_dwordx4 v[72:75], %[a1], %[rs], s64 offen offset:2048 sc1\n\t" "buffer_load_dwordx4 v[76:79], %[a1], %[rs], s64 offen offset:2304 sc1\n\t" \
    "v_mov_b32 v122, s60\n\t" "buffer_store_dword v122, %[fo], %[rc], %[fz] offen sc1\n\t" "s_waitcnt vmcnt(1)\n\t" "s_branch 23b\n\t" "24:\n\t" "v_mov_b32 v122, s60\n\t" \
    "buffer_store_dword v122, %[fo], %[rc], %[fz] offen sc1\n\t" "s_cmp_eq_u32 s60, 64\n\t" "s_cbranch_scc1 90f\n\t" "s_add_u32 s67, s60, 1\n\t" "s_cmp_ge_u32 s61, s67\n\t" \
    "s_cbranch_scc1 22f\n\t" "21:\n\t" "buffer_load_dword v120, %[fo], %[rc], %[fv] offen sc1\n\t" "buffer_load_dword v121, %[fo], %[rc], %[vd] offen sc1\n\t" \
    "s_waitcnt vmcnt(0)\n\t" "v_readfirstlane_b32 s61, v120\n\t" "v_readfirstlane_b32 s66, v121\n\t" "s_nop 3\n\t" "s_cmp_ge_u32 s61, s67\n\t" "s_cbranch_scc1 22f\n\t" \
    "s_bitcmp1_b32 s66, 1\n\t" "s_cbranch_scc1 90f\n\t" "s_cmp_eq_u32 %[bd], 0\n\t" "s_cbranch_scc1 22f\n\t" "s_sub_u32 %[bd], %[bd], 1\n\t" "s_sleep 2\n\t" \
    "s_branch 21b\n\t" "22:\n\t" "s_lshr_b32 s66, s60, 2\n\t" "s_xor_b32 s66, s66, %[xp]\n\t" "s_and_b32 s66, s66, 1\n\t" "s_lshl_b32 s66, s66, 13\n\t" \
    "s_lshl_b32 s64, s60, 19\n\t" "s_add_u32 s64, s64, s66\n\t" "s_add_u32 s64, s64, %[vb]\n\t" "buffer_load_dwordx4 v[48:51], %[ao], %[rs], s64 offen offset:0 sc1\n\t" \
    "buffer_load_dwordx4 v[52:55], %[ao], %[rs], s64 offen offset:256 sc1\n\t" "buffer_load_dwordx4 v[56:59], %[ao], %[rs], s64 offen offset:2048 sc1\n\t" \
    "buffer_load_dwordx4 v[60:63], %[ao], %[rs], s64 offen offset:2304 sc1\n\t" "buffer_load_dwordx4 v[64:67], %[a1], %[rs], s64 offen offset:0 sc1\n\t" \
    "buffer_load_dwordx4 v[68:71], %[a1], %[rs], s64 offen offset:256 sc1\n\t" "buffer_load_dwordx4 v[72:75], %[a1], %[rs], s64 offen offset:2048 sc1\n\t" \
    "buffer_load_dwordx4 v[76:79], %[a1], %[rs], s64 offen offset:2304 sc1\n\t" "s_waitcnt vmcnt(0)\n\t" "s_branch 23b\n\t" "60:\n\t" "s_sub_u32 s67, s60, %[rg]\n\t" \
    "s_cmp_ge_u32 s62, s67\n\t" "s_cbranch_scc1 62f\n\t" "61:\n\t" "buffer_load_dword v120, %[fo], %[rc], %[fs] offen sc1\n\t" \
    "buffer_load_dword v121, %[fo], %[rc], %[vd] offen sc1\n\t" "s_waitcnt vmcnt(0)\n\t" "v_readfirstlane_b32 s62, v120\n\t" "v_readfirstlane_b32 s66, v121\n\t" "s_nop 3\n\t" \
    "s_cmp_ge_u32 s62, s67\n\t" "s_cbranch_scc1 62f\n\t" "s_bitcmp1_b32 s66, 1\n\t" "s_cbranch_scc1 90f\n\t" "s_cmp_eq_u32 %[bd], 0\n\t" "s_cbranch_scc1 62f\n\t" \
    "s_sub_u32 %[bd], %[bd], 1\n\t" "s_sleep 2\n\t" "s_branch 61b\n\t" "62:\n\t" "s_branch 63b\n\t" "30:\n\t" "s_nop 7\n\t" "v_cvt_pk_bf16_f32 v112, v96, v97\n\t" \
    "v_cvt_pk_bf16_f32 v113, v98, v99\n\t" "v_cvt_pk_bf16_f32 v114, v100, v101\n\t" "v_cvt_pk_bf16_f32 v115, v102, v103\n\t" "v_cvt_pk_bf16_f32 v116, v104, v105\n\t" \
    "v_cvt_pk_bf16_f32 v117, v106, v107\n\t" "v_cvt_pk_bf16_f32 v118, v108, v109\n\t" "v_cvt_pk_bf16_f32 v119, v110, v111\n\t" \
    "buffer_store_dwordx4 v[112:115], %[z1], %[rs], s63 offen sc1\n\t" "buffer_store_dwordx4 v[116:119], %[z2], %[rs], s63 offen sc1\n\t" \
    "buffer_load_dword v120, %[fo], %[rc], %[fv] offen sc1\n\t" "s_waitcnt vmcnt(0)\n\t" "v_readfirstlane_b32 s61, v120\n\t" "s_lshr_b32 s66, s60, 2\n\t" \
    "s_xor_b32 s66, s66, %[xp]\n\t" "s_and_b32 s66, s66, 1\n\t" "s_lshl_b32 s66, s66, 13\n\t" "s_lshl_b32 s64, s60, 19\n\t" "s_add_u32 s64, s64, s66\n\t" \
    "s_add_u32 s64, s64, %[vb]\n\t" "s_cmp_gt_u32 s61, s60\n\t" "s_cbranch_scc0 34f\n\t" "s_cmp_eq_u32 s60, 64\n\t" "s_cbranch_scc1 34f\n\t" \
    "buffer_load_dwordx4 v[16:19], %[ao], %[rs], s64 offen offset:0 sc1\n\t" "buffer_load_dwordx4 v[20:23], %[ao], %[rs], s64 offen offset:256 sc1\n\t" \
    "buffer_load_dwordx4 v[24:27], %[ao], %[rs], s64 offen offset:2048 sc1\n\t" "buffer_load_dwordx4 v[28:31], %[ao], %[rs], s64 offen offset:2304 sc1\n\t" \
    "buffer_load_dwordx4 v[32:35], %[a1], %[rs], s64 offen offset:0 sc1\n\t" "buffer_load_dwordx4 v[36:39], %[a1], %[rs], s64 offen offset:256 sc1\n\t" \
    "buffer_load_dwordx4 v[40:43], %[a1], %[rs], s64 offen offset:2048 sc1\n\t" "buffer_load_dwordx4 v[44:47], %[a1], %[rs], s64 offen offset:2304 sc1\n\t" \
    "v_mov_b32 v122, s60\n\t" "buffer_store_dword v122, %[fo], %[rc], %[fz] offen sc1\n\t" "s_waitcnt vmcnt(1)\n\t" "s_branch 33b\n\t" "34:\n\t" "v_mov_b32 v122, s60\n\t" \
    "buffer_store_dword v122, %[fo], %[rc], %[fz] offen sc1\n\t" "s_cmp_eq_u32 s60, 64\n\t" "s_cbranch_scc1 90f\n\t" "s_add_u32 s67, s60, 1\n\t" "s_cmp_ge_u32 s61, s67\n\t" \
    "s_cbranch_scc1 32f\n\t" "31:\n\t" "buffer_load_dword v120, %[fo], %[rc], %[fv] offen sc1\n\t" "buffer_load_dword v121, %[fo], %[rc], %[vd] offen sc1\n\t" \
    "s_waitcnt vmcnt(0)\n\t" "v_readfirstlane_b32 s61, v120\n\t" "v_readfirstlane_b32 s66, v121\n\t" "s_nop 3\n\t" "s_cmp_ge_u32 s61, s67\n\t" "s_cbranch_scc1 32f\n\t" \
    "s_bitcmp1_b32 s66, 1\n\t" "s_cbranch_scc1 90f\n\t" "s_cmp_eq_u32 %[bd], 0\n\t" "s_cbranch_scc1 32f\n\t" "s_sub_u32 %[bd], %[bd], 1\n\t" "s_sleep 2\n\t" \
    "s_branch 31b\n\t" "32:\n\t" "s_lshr_b32 s66, s60, 2\n\t" "s_xor_b32 s66, s66, %[xp]\n\t" "s_and_b32 s66, s66, 1\n\t" "s_lshl_b32 s66, s66, 13\n\t" \
    "s_lshl_b32 s64, s60, 19\n\t" "s_add_u32 s64, s64, s66\n\t" "s_add_u32 s64, s64, %[vb]\n\t" "buffer_load_dwordx4 v[16:19], %[ao], %[rs], s64 offen offset:0 sc1\n\t" \
    "buffer_load_dwordx4 v[20:23], %[ao], %[rs], s64 offen offset:256 sc1\n\t" "buffer_load_dwordx4 v[24:27], %[ao], %[rs], s64 offen offset:2048 sc1\n\t" \
    "buffer_load_dwordx4 v[28:31], %[ao], %[rs], s64 offen offset:2304 sc1\n\t" "buffer_load_dwordx4 v[32:35], %[a1], %[rs], s64 offen offset:0 sc1\n\t" \
    "buffer_load_dwordx4 v[36:39], %[a1], %[rs], s64 offen offset:256 sc1\n\t" "buffer_load_dwordx4 v[40:43], %[a1], %[rs], s64 offen offset:2048 sc1\n\t" \
    "buffer_load_dwordx4 v[44:47], %[a1], %[rs], s64 offen offset:2304 sc1\n\t" "s_waitcnt vmcnt(0)\n\t" "s_branch 33b\n\t" "70:\n\t" "s_sub_u32 s67, s60, %[rg]\n\t" \
    "s_cmp_ge_u32 s62, s67\n\t" "s_cbranch_scc1 72f\n\t" "71:\n\t" "buffer_load_dword v120, %[fo], %[rc], %[fs] offen sc1\n\t" \
    "buffer_load_dword v121, %[fo], %[rc], %[vd] offen sc1\n\t" "s_waitcnt vmcnt(0)\n\t" "v_readfirstlane_b32 s62, v120\n\t" "v_readfirstlane_b32 s66, v121\n\t" "s_nop 3\n\t" \
    "s_cmp_ge_u32 s62, s67\n\t" "s_cbranch_scc1 72f\n\t" "s_bitcmp1_b32 s66, 1\n\t" "s_cbranch_scc1 90f\n\t" "s_cmp_eq_u32 %[bd], 0\n\t" "s_cbranch_scc1 72f\n\t" \
    "s_sub_u32 %[bd], %[bd], 1\n\t" "s_sleep 2\n\t" "s_branch 71b\n\t" "72:\n\t" "s_branch 73b\n\t" "90:\n\t"

#define T_DQ 1380
#define T_DV 1290
#define T_RQ 670
#define T_RV 640
#define T_M1 100
#define T_DY 570
#define T_DX 1240
#define T_MIN 35
#define T_PEN 280
#define T_SAFE 80
#define T_RETRY 700
#define T_POLL 1
#define T_KS 0
#define T_TS 0
#define T_CLR 210
#define T_VLAG 150
#define T_U0 340
constexpr int T_OVH = 118, T_NEAR = 460;
constexpr unsigned NS(int ns) { return (unsigned)(ns * 64 / 10); }
struct Clk { unsigned cyc, g, pc, pg, k1, k2; };
DEV void mark(Clk& k) {
  unsigned long long c, r[8];
  asm volatile("s_memtime %0\n\ts_memrealtime %1\n\ts_nop 2\n\ts_memrealtime %2\n\ts_nop 2\n\ts_memrealtime %3\n\ts_nop 2\n\ts_memrealtime %4\n\ts_nop 2\n\t"
               "s_memrealtime %5\n\ts_nop 2\n\ts_memrealtime %6\n\ts_nop 2\n\ts_memrealtime %7\n\ts_waitcnt lgkmcnt(0)"
               : "=s"(c), "=s"(r[0]), "=s"(r[1]), "=s"(r[2]), "=s"(r[3]), "=s"(r[4]), "=s"(r[5]), "=s"(r[6]) :: "memory");
  const unsigned a = (unsigned)r[0];
  unsigned n = 0;
#pragma unroll
  for (int i = 1; i < 7; ++i) asm("s_cmp_eq_u32 %1, %2\n\ts_addc_u32 %0, %0, 0" : "+s"(n) : "s"((unsigned)r[i]), "s"(a) : "scc");
  k.g = (a + 4u) << 6; k.cyc = (unsigned)c + 12u + 16u * n;
}
DEV void anchor(Clk& k, bool any = false) {
  mark(k);
  const unsigned dg_ = k.g - k.pg;
  const float dc = (float)(k.cyc - k.pc), dg = (float)dg_;
  unsigned k1, k2;
  asm volatile("s_nop 0\n\tv_readfirstlane_b32 %0, %2\n\tv_readfirstlane_b32 %1, %3\n\ts_nop 3\n\ts_min_u32 %0, %0, 0x80000\n\ts_min_u32 %1, %1, 0x8000"
               : "=&s"(k1), "=&s"(k2) : "v"((unsigned)(dg * __builtin_amdgcn_rcpf(dc) * 65536.f)), "v"((unsigned)(dc * __builtin_amdgcn_rcpf(dg) * 65536.f)) : "scc");
  if (any) { k.k1 = k1; k.k2 = k2; }
  else asm("s_cmp_lt_u32 %4, %5\n\ts_cselect_b32 %2, %2, %0\n\ts_cselect_b32 %3, %3, %1\n\ts_add_u32 %0, %0, %2\n\ts_lshr_b32 %0, %0, 1\n\ts_add_u32 %1, %1, %3\n\ts_lshr_b32 %1, %1, 1"
           : "+s"(k.k1), "+s"(k.k2), "+s"(k1), "+s"(k2) : "s"(dg_ - NS(1400)), "n"(NS(800)) : "scc");
  k.pc = k.cyc; k.pg = k.g;
}
DEV unsigned to_cyc(const Clk& k, unsigned g) {
  unsigned d;
  asm("s_max_i32 %0, %1, 0xffff8000\n\ts_min_i32 %0, %0, 0x8000\n\ts_mul_i32 %0, %0, %2\n\ts_ashr_i32 %0, %0, 16" : "=&s"(d) : "s"(g - k.g), "s"(k.k2) : "scc");
  return k.cyc + d - T_OVH;
}
DEV void wait_cyc(unsigned t) { asm volatile(TIMED_WAIT :: "s"(t) : CL_S); }
DEV void close_in(Clk& k, unsigned t) {
  if ((int)(t - (unsigned)__builtin_amdgcn_s_memtime()) > T_NEAR + 60) { wait_cyc(t - T_NEAR); mark(k); }
}
DEV int slow_line(const u16* q, int h, int j, int lane) { return (j * 32 + (lane >> 1)) * 8192 + (h >> 3) * 2048 + (lane & 1) * 1024 + ((3 - (int)((size_t)q >> 7)) & 7) * 128; }
DEV int turn(int h, int w) { return ((h >> 3) ^ (w & 1)) & 3; }
DEV void hold(const Ctx& c, int ticks) {
#pragma unroll 1
  while ((unsigned)__builtin_amdgcn_s_memrealtime() - c.t0 < (unsigned)ticks) __builtin_amdgcn_s_sleep(4);
}
DEV int z_mix(int y) { return (y >> 4) | ((y & 1) << 2); }
DEV int u_slot(int s, int para) { return R_U + (s & (URING == 64 ? 127 : URING - 1)) * MB + ((para ^ (s >> 1)) & 1) * 8192; }
DEV unsigned ask_stamps(Ctx& c, int par, int slot) { return __builtin_amdgcn_raw_buffer_load_b32(c.rs, amap(24576, par) + fresh_lane() * 128, slot, AUX_SC1); }
DEV unsigned latest(Ctx& c, const Clk& k, int par, int slot, unsigned v) {
  if (__builtin_expect(!allok(v), 0)) {
#pragma unroll 1
    do {
      __builtin_amdgcn_s_sleep(T_POLL);
      asm volatile("" ::: "memory");
      const unsigned t = __builtin_amdgcn_raw_buffer_load_b32(c.rs, v == SENT ? amap(24576, par) + fresh_lane() * 128 : 1 << 30, slot, AUX_SC1);
      v = v == SENT ? t : v;
    } while (!allok(v) && !spend(c));
  }
  int d = (int)(v - k.g);
  d = max(d, __builtin_amdgcn_update_dpp(d, d, 0x128, 0xf, 0xf, true));
  d = max(d, __builtin_amdgcn_update_dpp(d, d, 0x124, 0xf, 0xf, true));
  d = max(d, __builtin_amdgcn_update_dpp(d, d, 0x122, 0xf, 0xf, true));
  d = max(d, __builtin_amdgcn_update_dpp(d, d, 0x121, 0xf, 0xf, true));
  unsigned m;
  asm("s_max_i32 %0, %1, %2\n\ts_max_i32 %0, %0, %3\n\ts_max_i32 %0, %0, %4" : "=&s"(m)
      : "s"(__builtin_amdgcn_readlane(d, 0)), "s"(__builtin_amdgcn_readlane(d, 16)), "s"(__builtin_amdgcn_readlane(d, 32)), "s"(__builtin_amdgcn_readlane(d, 48)) : "scc");
  return k.g + m;
}
struct Plan { unsigned gx, dx; };
DEV unsigned smax(unsigned a, unsigned b) { return (int)(a - b) > 0 ? a : b; }
DEV unsigned pick(int d, unsigned a, unsigned b) { unsigned r; asm("s_cmp_gt_i32 %1, 0\n\ts_cselect_b32 %0, %2, %3" : "=s"(r) : "s"(d), "s"(a), "s"(b) : "scc"); return r; }
DEV unsigned when_p(const Plan& t, unsigned last, unsigned more = 0) {
  const unsigned gy = t.gx + t.dx + NS(T_M1), a = last + NS(T_PEN), b = gy + NS(T_RETRY);
  return pick((int)(last + NS(T_MIN) - gy), pick((int)(a - b), a, b) + more, gy);
}
DEV void advance(Plan& t, unsigned last) {
  const int over = (int)(last - t.gx - t.dx);
  t.gx = when_p(t, last, NS(T_SAFE)) + NS(T_DY);
  t.dx += pick(over - (int)NS(80), NS(4), pick(over, (unsigned)(over >> 1), (unsigned)(over >> 3)));
}
DEV unsigned next_go(Ctx& c, Clk& k, Plan& t, int par, int slot, unsigned v) {
  SCHED();
  anchor(k);
  SCHED();
  advance(t, latest(c, k, par, slot, v));
  return to_cyc(k, t.gx);
}

DEV constexpr int qk_r(int j) { return j < 4 ? 0 : 1 + (j - 4) % 3; }
DEV constexpr int qk_i(int j) { return j < 4 ? j : (j - 4) / 3; }
DEV constexpr int qk_upto(int u) { return K_FIRST && u >= 3 ? (u + 3 < 16 ? u + 3 : 16) : (u + 4 < 16 ? u + 4 : 16); }
DEV void qk_role(const Params& p, Ctx& c, int h) {
  constexpr int WL = 0, MEMS = 131072, KXL = 147456, QKX = 155648, QROW = 272;
  c.budget = SPIN_CAP;
  const int w = c.w;
  Clk k = {0u, 0u, 0u, 0u, 0u, 0u};
  anchor(k, true);
  v8u wa[32], wh[16];
  unsigned look = claim_look(c);
  bool claimed = false;
  hold(c, QK_AT);
  {
    const int lane = fresh_lane(), g = lane >> 4, cc = lane & 15;
    const rsrc_t rq = mkrs(p.Wq, 1024 * 4096 * 2), rk = mkrs(p.Wk, 1024 * 4096 * 2);
    int vo[4];
#pragma unroll
    for (int i = 0; i < 4; ++i) vo[i] = opq((256 * w + 64 * (i ^ turn(h, w)) + krow_lane(g)) * 8192 + (h * 128 + 8 * cc) * 2);
    v4u row[16][8];
    auto request = [&](int u) {
#pragma unroll
      for (int r = 0; r < 8; ++r) row[u][r] = ld16w<W_AUX>((u < 8) != K_FIRST ? rq : rk, vo[(u >> 1) & 3], krow(4 * (u & 1) + (r >> 1), r & 1) * 8192);
    };
#pragma unroll
    for (int u = 0; u < qk_upto(-1); ++u) { request(u); if (u == 0) { SCHED(); ask(c, look); SCHED(); } }
    SCHED();
    const int sw = MEMS + w * 8192 + g * 256, lw = opq(WL + w * 32768 + lane * 16);
    int sr[2];
#pragma unroll
    for (int t = 0; t < 2; ++t) { const int i = (2 * cc + t) & 7; sr[t] = opq(i * 1024 + g * 256 + ((4 * w + (cc >> 2) + 2 * i) & 15) * 16); }
    sfor<16>([&](auto U) __attribute__((always_inline)) {
      constexpr int u = U.value;
#pragma unroll
      for (int i = 0; i < 8; ++i) {
        v4u f;
#pragma unroll
        for (int r = 0; r < 4; ++r) f[r] = prm(row[u][2 * r + 1][i >> 1], row[u][2 * r][i >> 1], i & 1);
        sts16(c.L, sw + i * 1024 + ((cc + 2 * i) & 15) * 16, f);
      }
      SCHED();
      if ((u == 5 || u == 10) && !claimed) claimed = claim_try(c, look);
      if (u == 2 || u == 7) look = claim_look(c);
      if (qk_upto(u) > qk_upto(u - 1)) request(qk_upto(u - 1));
      if (u == 15 && !claimed) claim(c, 0);
      BAR();
      v4u in[4][2];
#pragma unroll
      for (int t = 1; t >= 0; --t)
#pragma unroll
        for (int r = 0; r < 4; ++r) in[r][t] = lds16(c.L, sr[t] + MEMS + ((w + r) & 3) * 8192);
      SCHED();
#pragma unroll
      for (int j = 0; j < 16; ++j) {
        if (qk_i(j) != ((u >> 1) & 3)) continue;
#pragma unroll
        for (int t = 1; t >= 0; --t) {
          const v4u o = in[qk_r(j)][t];
          constexpr bool isq = (u < 8) != K_FIRST;
          if (!isq && t == 1) sts16(c.L, lw + j * 2048 + (u & 1) * 1024, o);
          else {
            v8u& d = isq ? wa[2 * j + t] : wh[j];
#pragma unroll
            for (int r = 0; r < 4; ++r) d[4 * (u & 1) + r] = o[r];
#define M_(r) asm volatile("" : "+" r(d))
            if (u & 1) { if (isq) AT_ACC(M_, 2 * j + t); else AT_HOME(M_, j); }
#undef M_
          }
        }
      }
      BAR();
    });
  }
  STAMP(0);
  BAR();
  const unsigned vote = wait_verdict(c);
  const int par = vote_b(vote), para = vote_a(vote);
  const int lane = fresh_lane(), g = lane >> 4, cc = lane & 15;
  const int moff = opq(amap(w * 4096 + lane * 16, par) + (turn(h, w) & 1) * 1024), m2 = R_MEM + (turn(h, w) & 2) * 1024;
  unsigned tries = 1u << 12;
  *(volatile LAS unsigned*)(c.L + 160512) = 0u;
  LGKM(0);
  const int idx = opq((lane & 1) ? 0xEEEE : 0x4444);
  TICK_DECL;
  const int l0 = opq(WL + w * 32768 + lane * 16), l1 = opq(MEMS + lane * 16);
  const int qw = opq(QKX + (2 * w + (cc >> 3)) * 256 + g * 64 + (cc & 1) * 16 + ((cc >> 1) & 3) * 4), kw = opq(QKX + (8 + 2 * g) * QROW + (32 * w + 2 * cc) * 2);
#define QK_PINS PIN_ACC0(wa); PIN_ACC1(wa); PIN_HOME(wh)
#define QK_IN [l0] "v"(l0), [l1] "v"(l1), [mo] "v"(moff), [ix] "v"(idx), [qw] "v"(qw), [kw] "v"(kw), [rs] "s"(c.rs), [sb] "s"(m2 + (s & 3) * MEM_SLOT)
#define QK_CL CL_S, CL_16_32, CL_160_200, CL_200_216, CL_216_256
  if (w) RAWBAR();
  if (w == 3) {
    const int hq = opq(lane ? 1 << 30 : amap(24576 + h * 128, par));
    anchor(k, true);
    unsigned tg = k.cyc;
    Plan tt = {k.g - NS(100000), NS(T_DX)};
    for (int s = 0; s < NSEG; ++s) {
      const int pb = R_P + (s & 3) * P_SLOT;
      QK_PINS;
      asm volatile(QK_STEP3 : [bd] "+s"(c.budget), [rt] "+s"(tries) : QK_IN, [hq] "v"(hq), [pb] "s"(pb), [tg] "s"(tg), [ca] "s"(k.cyc - T_DQ), [k1] "s"(k.k1), [ga] "s"(k.g), [dr] "n"(T_RQ), [pr] "s"(R_P + ((s + 1) & 3) * P_SLOT) : QK_CL);
      QK_PINS;
      LOG(1, s, tg);
      const unsigned sv = ask_stamps(c, par, pb);
      SCHED();
      anchor(k);
      SCHED();
      advance(tt, latest(c, k, par, pb, sv));
      tg = to_cyc(k, tt.gx);
      RAWBAR();
      LOG(2, s, k.cyc); LOG(3, s, k.g); LOG(4, s, tt.dx);
      st4s(mkrs((const void*)c.ctrl, CTRL_BYTES), hq & (1 << 30), C_FLAG + (F_S + h) * 128, (unsigned)s + 1u);
      TICK_SUM(10);
      close_in(k, tg);
      tg = to_cyc(k, tt.gx);
    }
  } else {
    const int kx0 = opq(KXL + g * 512 + cc * 16), kmrd = opq(QKX + g * 16 + (8 + (cc & 7)) * QROW);
    unsigned seen = NOFLAG;
    if (w == 0) {
      const int prow = (2 * g + (cc & 1)) * 2 + ((cc >> 1) & 1);
      const int px = opq(amap((h >> 1) * 1024 + ((2 * (h & 1) + (cc >> 3)) * 16 + prow) * 16 + ((cc >> 2) & 1) * 8, par));
      const int pm = opq(cc < 8 ? amap(16384 + h * 128 + (2 * g + (cc & 1)) * 16 + (cc & 2) * 4 + (cc & 4), par) : 1 << 30);
      const int ps = opq((cc & 1) ? 0x03020706 : 0x05040100), pa = opq(lane * 128);
      for (int s = 0; s < NSEG; ++s) {
        if (seen < (unsigned)s + 1u) seen = wait_flag(c, F_K + h, s + 1);
        QK_PINS;
        unsigned pd;
        asm volatile(QK_STEP0 : [bd] "+s"(c.budget), [rt] "+s"(tries), [sq] "+s"(seen), [pd] "=&v"(pd)
                     : QK_IN, [kx] "v"(kx0), [km] "v"(kmrd), [px] "v"(px), [pm] "v"(pm), [ps] "v"(ps), [pb] "s"(R_P + (s & 3) * P_SLOT), [pr] "s"(R_P + ((s + 1) & 3) * P_SLOT),
                       [kb] "s"(R_KX - KXL + xblk(s, h, para)), [fq] "s"(c.ctrl + (C_FLAG + (F_K + h) * 128) / 4),
                       [kn] "s"(R_KX + xblk(min(s + 1, NSEG - 1), h, para)), [pa] "v"(pa) : QK_CL);
        QK_PINS;
        LITE(s); LOGS(0, s);
        TICK_SUM(10);
      }
      RAWBAR();
    } else {
      const int qt = w - 1;
      const int px = opq(amap(qt ? 16384 + h * 1024 + lane * 16 : h * 512 + (g >> 1) * 256 + cc * 16 + (g & 1) * 8, para));
      const int pm = opq(g < 2 ? amap(49152 + (qt * 32 + h) * 256 + cc * 16 + g * 8, para) : 1 << 30);
      const int ps = opq(cc - 4 * g), hq = opq(g * 512 + (16 * qt + cc) * 16);
#define QK_TOK(STEP) for (int s = 0; s < NSEG; ++s) { QK_PINS; \
        asm volatile(STEP : [bd] "+s"(c.budget), [rt] "+s"(tries), [sq] "+s"(seen) \
                     : QK_IN, [kx] "v"(kx0), [km] "v"(kmrd), [px] "v"(px), [pm] "v"(pm), [ps] "v"(ps), [hq] "v"(hq), [qb] "s"(R_QX + xblk(s, h, para)), [tb] "s"(R_PT + (s & 7) * PT_SLOT), \
                       [tr] "s"(R_PT + ((s + 2) & 7) * PT_SLOT), [sn] "s"(s + 1), [fq] "s"(c.ctrl + (C_FLAG + (F_Q + h) * 128) / 4) : QK_CL); \
        QK_PINS; TICK_SUM(10); }
      if (w == 1) { QK_TOK(QK_STEP1) } else { QK_TOK(QK_STEP2) }
    }
  }
  TICK_OUT();
  if (c.budget == 0u) __hip_atomic_store(c.ctrl + C_TMO / 4, 1u, RLX_AGENT);
}

DEV void shuffle_cols(Ctx& c, int scratch, const v4u (&rows)[8], v4u (&o)[8]) {
  const int g = c.lane >> 4, cc = c.lane & 15;
#pragma unroll
  for (int i = 0; i < 8; ++i) {
    v4u f;
#pragma unroll
    for (int r = 0; r < 4; ++r) f[r] = prm(rows[2 * r + 1][i >> 1], rows[2 * r][i >> 1], i & 1);
    sts16(c.L, scratch + (8 * cc + i) * 64 + ((g ^ (cc & 3)) << 4), f);
  }
  asm volatile("s_waitcnt lgkmcnt(0)" ::: "memory");
#pragma unroll
  for (int i = 0; i < 8; ++i) { const int col = 16 * i + cc; o[i] = lds16(c.L, scratch + col * 64 + ((g ^ ((col >> 3) & 3)) << 4)); }
  asm volatile("s_waitcnt lgkmcnt(0)" ::: "memory");
}

DEV void vo_role(const Params& p, Ctx& c, int h) {
  constexpr int WL = 0, MEMS = 131072, VM = 147456, VROW = 272;
  c.budget = SPIN_CAP;
  const int w = c.w, lane = c.lane = fresh_lane(), g = lane >> 4, cc = lane & 15;
  Clk k = {0u, 0u, 0u, 0u, 0u, 0u};
  anchor(k, true);
  v8u wa[32], wh[16];
  ask(c, claim_look(c));
  unsigned look = 0;
  bool claimed = false;
  hold(c, VO_AT);
  const rsrc_t rwo = mkrs(p.Wo, 4096 * 1024 * 2);
  const int voo = (h * 128 + 4 * g) * 2048 + (256 * ((cc >> 3) ^ (h >> 3)) + 64 * w + 8 * (cc & 7)) * 2;
  v4u buf[3][8];
  auto request_o = [&](int u) {
#pragma unroll
    for (int r = 0; r < 8; ++r) buf[u % 3][r] = ld16w<W_AUX>(rwo, voo ^ ((u >> 2) * 1024), (32 * (u & 3) + 16 * (r >> 2) + (r & 3)) * 2048);
  };
  {
    const rsrc_t rw = mkrs(p.Wv, 1024 * 4096 * 2);
    const int vo = (256 * w + krow_lane(g)) * 8192 + (h * 128 + 8 * cc) * 2;
    v4u vrow[8][8];
    auto request = [&](int u) {
#pragma unroll
      for (int r = 0; r < 8; ++r) vrow[u][r] = ld16w<W_AUX>(rw, vo, (64 * ((u >> 1) ^ turn(h, w)) + krow(4 * (u & 1) + (r >> 1), r & 1)) * 8192);
    };
#pragma unroll
    for (int u = 0; u < 3; ++u) request(u);
    SCHED();
    const int wr_ = MEMS + w * 8192 + g * 256;
    int sr[2];
#pragma unroll
    for (int t = 0; t < 2; ++t) { const int i = (2 * cc + t) & 7; sr[t] = opq(i * 1024 + g * 256 + ((4 * w + (cc >> 2) + 2 * i) & 15) * 16); }
#pragma unroll
    for (int u = 0; u < 8; ++u) {
#pragma unroll
      for (int i = 0; i < 8; ++i) {
        v4u f;
#pragma unroll
        for (int r = 0; r < 4; ++r) f[r] = prm(vrow[u][2 * r + 1][i >> 1], vrow[u][2 * r][i >> 1], i & 1);
        sts16(c.L, wr_ + i * 1024 + ((cc + 2 * i) & 15) * 16, f);
      }
      SCHED();
      if (u == 7) claimed = claim_try(c, look);
      if (u == 4) look = claim_look(c);
      if (u + 3 < 8) request(u + 3); else request_o(u - 5);
      BAR();
      v4u in[4][2];
#pragma unroll
      for (int t = 1; t >= 0; --t)
#pragma unroll
        for (int r = 0; r < 4; ++r) in[r][t] = lds16(c.L, sr[t] + MEMS + ((w + r) & 3) * 8192);
      SCHED();
#pragma unroll
      for (int j = 0; j < 16; ++j) {
        if (qk_i(j) != (u >> 1)) continue;
#pragma unroll
        for (int t = 1; t >= 0; --t) {
          const v4u o = in[qk_r(j)][t];
          if (t == 1 && (j & 1)) sts16(c.L, WL + (w * 16 + (j >> 1)) * 2048 + (u & 1) * 1024 + lane * 16, o);
          else {
            const int n = 3 * (j >> 1) + ((j & 1) ? 2 : t);
#pragma unroll
            for (int r = 0; r < 4; ++r) wa[n][4 * (u & 1) + r] = o[r];
#define M_(r) asm volatile("" : "+" r(wa[n]))
            if (u & 1) AT_ACC(M_, n);
#undef M_
          }
        }
      }
      BAR();
    }
  }
#pragma unroll
  for (int u = 0; u < 8; ++u) {
    v4u o[8];
    shuffle_cols(c, MEMS + w * 8192, buf[u % 3], o);
    SCHED();
    if (u + 3 < 8) request_o(u + 3);
#pragma unroll
    for (int i = 0; i < 4; ++i) {
      const int pr = 4 * (u >> 2) + i, ch = u & 3;
      const v8u f = {o[2 * i][0], o[2 * i + 1][0], o[2 * i][1], o[2 * i + 1][1], o[2 * i][2], o[2 * i + 1][2], o[2 * i][3], o[2 * i + 1][3]};
      if (ch < 2) {
        wh[2 * pr + ch] = f;
#define M_(r) asm volatile("" : "+" r(wh[2 * pr + ch]))
        AT_HOME(M_, 2 * pr + ch);
#undef M_
      } else if (ch == 2) {
        wa[24 + pr] = f;
#define M_(r) asm volatile("" : "+" r(wa[24 + pr]))
        AT_ACC(M_, 24 + pr);
#undef M_
      } else {
        sts16(c.L, WL + (w * 16 + 8 + pr) * 2048 + lane * 16, lo4(f));
        sts16(c.L, WL + (w * 16 + 8 + pr) * 2048 + 1024 + lane * 16, hi4(f));
      }
    }
    SCHED();
  }
  if (!claimed) claim(c, 0);
  BAR();
  STAMP(0);
  const unsigned vote = wait_verdict(c);
  const int par = vote_b(vote), para = vote_a(vote);
  const int moff = opq(amap(w * 4096 + lane * 16, par) + (turn(h, w) & 1) * 1024), m2 = R_MEM + (turn(h, w) & 2) * 1024;
  unsigned tries = 1u << 12;
  *(volatile LAS unsigned*)(c.L + 160512) = 0u;
  LGKM(0);
  const int idx = opq((lane & 1) ? 0xEEEE : 0x4444);
  const int u_off = opq((4 * w + g) * 16384 + (h ^ (8 * g)) * 256 + cc * 16);
  const int hq = opq((lane | w) ? 1 << 30 : amap(24576 + (32 + h) * 128, par));
  const int l0 = opq(WL + w * 32768 + lane * 16), l1 = opq(MEMS + lane * 16);
  const int vmw = opq(VM + 2 * g * VROW + (32 * w + 2 * cc) * 2);
  const int vmr = opq(VM + (cc >> 1) * VROW + g * 16);
  TICK_DECL;
  anchor(k, true);
  unsigned tg = k.cyc;
  Plan tt = {k.g - NS(100000), NS(T_DX)};
  for (int s = 0; s < NSEG; ++s) {
    const int pb = R_P + (s & 3) * P_SLOT;
    PIN_ACC0(wa); PIN_ACC1(wa); PIN_HOME(wh);
    asm volatile(VO_STEP
        : [bd] "+s"(c.budget), [rt] "+s"(tries)
        : [l0] "v"(l0), [l1] "v"(l1), [mo] "v"(moff), [ix] "v"(idx), [vw] "v"(vmw), [vr] "v"(vmr), [uo] "v"(u_off), [w] "s"(w), [rs] "s"(c.rs),
          [sb] "s"(m2 + (s & 3) * MEM_SLOT), [ub] "s"(u_slot(s, para) + (h >> 3) * 16 * 16384), [ur] "s"(u_slot(s + 2, para) + (2 * h + (w >> 1)) * 16384 + (w & 1) * 4096 - MEMS), [tg] "s"(tg + T_VLAG), [hq] "v"(hq), [pb] "s"(pb),
          [ca] "s"(k.cyc - T_DV), [k1] "s"(k.k1), [ga] "s"(k.g), [dr] "n"(T_RV), [pr] "s"(R_P + ((s + 1) & 3) * P_SLOT)
        : CL_S, CL_16_32, CL_160_200, CL_200_216, CL_216_256);
    PIN_ACC0(wa); PIN_ACC1(wa); PIN_HOME(wh);
    if (w == 0) {
      LITE(s);
      LOG(1, s, tg);
      const unsigned sv = ask_stamps(c, par, pb);
      SCHED();
      anchor(k);
      const unsigned gc = tt.gx + tt.dx + NS(T_M1 + T_CLR);
      const bool ontime = (int)(gc - k.g) > 0;
      if (ontime) { wait_cyc(to_cyc(k, gc)); RAWBAR(); }
      SCHED();
      advance(tt, latest(c, k, par, pb, sv));
      if (!ontime) { wait_cyc(to_cyc(k, tt.gx - NS(T_DY + T_SAFE - T_CLR))); RAWBAR(); }
      tg = to_cyc(k, tt.gx);
      LOG(2, s, k.cyc); LOG(3, s, k.g);
    }
    TICK_SUM(10);
    if (w == 0) { close_in(k, tg); tg = to_cyc(k, tt.gx); }
  }
  TICK_OUT();
  if (c.budget == 0u) __hip_atomic_store(c.ctrl + C_TMO / 4, 1u, RLX_AGENT);
}

DEV void y_role(const Params& p, Ctx& c, int y) {
  constexpr int ZR = 0, UL = 131072, CW = 155648;
  c.budget = SPIN_CAP;
  const int w = c.w, lane = c.lane = fresh_lane(), g = lane >> 4, cc = lane & 15;
  const v4u sent4 = {SENT, SENT, SENT, SENT};
  STAMP(2);
  Clk k = {0u, 0u, 0u, 0u, 0u, 0u};
  anchor(k, true);
  unsigned m0 = 0;
  if (w == 0) {
    unsigned best[2] = {~0u, ~0u};
    const unsigned first = claim_look(c);
    st4s(c.rs, y * 16384 + (lane >> 5) * 8192 + (lane & 31) * 4, R_U, SENT);
    ask(c, first);
#pragma unroll 1
    for (int n = 0; n < 12; ++n) {
#pragma unroll
      for (int r = 0; r < 2; ++r) {
        VMWAIT(0);
        const unsigned t0 = (unsigned)__builtin_amdgcn_s_memtime();
        LGKM(0);
        st4s(c.rs, y * 16384 + (lane & 31) * 4, R_U + r * 8192, SENT);
        VMWAIT(0);
        const unsigned t1 = (unsigned)__builtin_amdgcn_s_memtime();
        LGKM(0);
        best[r] = min(best[r], t1 - t0);
      }
      if (n + 1 >= PROBES && min(best[0], best[1]) < 600u && max(best[0], best[1]) > min(best[0], best[1]) + 128u) break;
    }
    STAMP(5);
    claim(c, CL_VOTED | (best[1] < best[0] ? CL_A : 0u));
    m0 = *(const GAS unsigned*)(p.mem0 + (lane >> 3) * 1024 + 16 * y + 2 * (lane & 7));
    m0 = m0 < SENT - 1u ? m0 : SENT - 1u;
    if (lane < 8) *(LAS unsigned*)(c.L + CW + lane * 4) = 0u;
    const unsigned v = wait_vote(c);
    STAMP(7);
    const int oa = y * 16384 + vote_a(v) * 8192 + lane * 16, ob = amap(y * 3072 + lane * 16, vote_b(v));
#pragma unroll
    for (int i = 0; i < 16; ++i) st16s(c.rs, oa + (i & 3) * 1024, R_U + (i >> 3) * MB + ((i >> 2) & 1) * 4096, sent4);
#pragma unroll
    for (int i = 0; i < 3; ++i) st16s(c.rs, amap(y * 3072 + i * 1024 + lane * 16, vote_b(v)), R_MEM, sent4);
#pragma unroll
    for (int i = 0; i < 8; ++i) st16s(c.rs, oa + (i & 3) * 1024, R_PT + (i >> 2) * 4096, sent4);
    (void)ob;
    VMWAIT(0);
    if (lane == 0) __hip_atomic_fetch_add(c.ctrl + (C_CLAIM + c.att * 1024) / 4 + c.slot, CL_FILLED, RLX_AGENT);
    STAMP(6);
  } else {
    if (YH && w < 3 && (y & 8) == 0) {
      hold(c, YH_AT);
#pragma unroll
      for (int i = 0; i < 8; ++i) {
        const u16* q = (i < 4) != K_FIRST ? p.Wq : p.Wk;
        if (i == 4) hold(c, YH_AT + YH_STEP);
        m0 += __builtin_amdgcn_raw_buffer_load_b32(mkrs(q, 8 * MB), slow_line(q, y >> 1, (y & 7) + 8 * (i & 3), lane) ^ ((w - 1) * 128), 0, 0);
      }
      asm volatile("" :: "v"(m0));
    }
  }
  BAR();
  const unsigned vote = wait_verdict(c);
  const int par = vote_b(vote), para = vote_a(vote);
  const int mo = amap(y * 256 + lane * 4, par);
  if (w == 0) st4s(c.rs, mo, R_MEM, m0);
  STAMP(0);
  const int rot = y >> 4;
  volatile LAS unsigned* const GO = (volatile LAS unsigned*)(c.L + CW);
  TICK_DECL;
  if (w == 0) {
    int xo[4];
#pragma unroll
    for (int i = 0; i < 4; ++i) xo[i] = amap((i ^ rot) * 4096 + lane * 16, par);
    const int pmo = amap(16384 + rot * 1024 + (2 * g + (cc & 1)) * 128 + (cc >> 1) * 16, par), psel = (cc & 1) ? 0x03020706 : 0x05040100;
    constexpr int lead = T_U0, late = 0;
    anchor(k, true);
    Plan tt = {k.g - NS(100000), NS(T_DX)};
    unsigned done = k.cyc;
    const int so = amap(24576, par) + lane * 128;
    const int idx = opq((lane & 1) ? 0xEEEE : 0x4444);
    const int uo = y * 16384 + lane * 16;
    const int mw = amap(y * 256 + (2 * g + (cc & 1)) * 32 + (cc >> 1) * 4, par);
    for (int s = 0; s < NSEG - 1; ++s) {
      if (s == 0) RAWBAR();
      const int za = ZR + (s & 3) * 32768 + rot * 8192 + lane * 16;
      v32u z0, z1, z2, z3;
      asm volatile(
          "ds_read_b128 a[0:3], %[z0] offset:0\n\t" "ds_read_b128 a[4:7], %[z0] offset:1024\n\t" "ds_read_b128 a[8:11], %[z0] offset:2048\n\t" "ds_read_b128 a[12:15], %[z0] offset:3072\n\t"
          "ds_read_b128 a[16:19], %[z0] offset:4096\n\t" "ds_read_b128 a[20:23], %[z0] offset:5120\n\t" "ds_read_b128 a[24:27], %[z0] offset:6144\n\t" "ds_read_b128 a[28:31], %[z0] offset:7168\n\t"
          "ds_read_b128 a[32:35], %[z1] offset:0\n\t" "ds_read_b128 a[36:39], %[z1] offset:1024\n\t" "ds_read_b128 a[40:43], %[z1] offset:2048\n\t" "ds_read_b128 a[44:47], %[z1] offset:3072\n\t"
          "ds_read_b128 a[48:51], %[z1] offset:4096\n\t" "ds_read_b128 a[52:55], %[z1] offset:5120\n\t" "ds_read_b128 a[56:59], %[z1] offset:6144\n\t" "ds_read_b128 a[60:63], %[z1] offset:7168\n\t"
          "ds_read_b128 a[64:67], %[z2] offset:0\n\t" "ds_read_b128 a[68:71], %[z2] offset:1024\n\t" "ds_read_b128 a[72:75], %[z2] offset:2048\n\t" "ds_read_b128 a[76:79], %[z2] offset:3072\n\t"
          "ds_read_b128 a[80:83], %[z2] offset:4096\n\t" "ds_read_b128 a[84:87], %[z2] offset:5120\n\t" "ds_read_b128 a[88:91], %[z2] offset:6144\n\t" "ds_read_b128 a[92:95], %[z2] offset:7168\n\t"
          "ds_read_b128 a[96:99], %[z3] offset:0\n\t" "ds_read_b128 a[100:103], %[z3] offset:1024\n\t" "ds_read_b128 a[104:107], %[z3] offset:2048\n\t" "ds_read_b128 a[108:111], %[z3] offset:3072\n\t"
          "ds_read_b128 a[112:115], %[z3] offset:4096\n\t" "ds_read_b128 a[116:119], %[z3] offset:5120\n\t" "ds_read_b128 a[120:123], %[z3] offset:6144\n\t" "ds_read_b128 a[124:127], %[z3] offset:7168\n\t"
          "s_waitcnt lgkmcnt(0)"
          : "={a[0:31]}"(z0), "={a[32:63]}"(z1), "={a[64:95]}"(z2), "={a[96:127]}"(z3) : [z0] "v"(za), [z1] "v"(za ^ 8192), [z2] "v"(za ^ 16384), [z3] "v"(za ^ 24576) : "memory");
      const int pb = R_P + (s & 3) * P_SLOT;
      v32u uu;
      const int ub = u_slot(s, para);
      TICK(0);
      anchor(k);
      TICK(8);
      unsigned gp = tt.gx + tt.dx + NS(T_M1), lt = late, sv = SENT;
      unsigned bad = (int)(done - to_cyc(k, tt.gx)) > T_OVH;
      TICK(4);
      LOG(2, s, k.cyc); LOG(3, s, k.g);
#pragma unroll 1
      for (;;) {
        if (bad) {
          const bool all = allok(sv);
          const unsigned last = latest(c, k, par, pb, sv);
          if (!all) mark(k);
          gp = when_p(tt, last);
          lt = 1;
        }
        close_in(k, to_cyc(k, gp) - (lt ? 96 : lead));
        const unsigned tg = to_cyc(k, gp);
        if (!bad) LOG(1, s, tg);
        asm volatile(Y_STEP
          : "=&{v[144:175]}"(uu), [bad] "=&s"(bad), [sv] "=&v"(sv)
          : [x0] "v"(xo[0]), [x1] "v"(xo[1]), [x2] "v"(xo[2]), [x3] "v"(xo[3]), [pm] "v"(pmo), [u0] "v"(uo + (rot ^ (y & 3)) * 2048), [mw] "v"(mw), [ps] "v"(psel), [ix] "v"(idx),
            [rs] "s"(c.rs), [pb] "s"(pb), [ub] "s"(ub), [mb] "s"(R_MEM + ((s + 1) & 3) * MEM_SLOT), [mr] "s"(R_MEM + ((s + 3) & 3) * MEM_SLOT), [tu] "s"(tg - (lt ? 96 : lead)), [tg] "s"(tg - 32), [late] "s"(lt),
            [so] "v"(so), [bd] "s"(c.budget),
            "{a[0:31]}"(z0), "{a[32:63]}"(z1), "{a[64:95]}"(z2), "{a[96:127]}"(z3)
          : CL_S, CL_120_128, CL_176_200, CL_200_216, CL_216_256);
        bad = __builtin_amdgcn_readfirstlane(bad);
        if (bad != 1u) break;
        spend(c);
      }
      done = bad;
      SCHED();
      const int ul = opq(UL + rot * 2048 + lane * 16);
#pragma unroll
      for (int i = 0; i < 8; ++i) sts16(c.L, (ul ^ ((i >> 1) * 2048)) + (i & 1) * 1024, v4u{uu[4 * i], uu[4 * i + 1], uu[4 * i + 2], uu[4 * i + 3]});
      asm volatile("" ::: "memory");
      *GO = s + 1;
      asm volatile("s_waitcnt vmcnt(2)" : "+v"(sv));
      advance(tt, latest(c, k, par, pb, sv));
      TICK(3);
      LITE(s); LOG(0, s, done);
      TICK_LDS(CW + 256);
    }
    {
      anchor(k);
      wait_cyc(to_cyc(k, tt.gx + tt.dx + NS(T_M1)));
      const int ub = u_slot(NSEG - 1, para);
      v4u u[8];
#pragma unroll 1
      for (;;) {
        asm volatile("" ::: "memory");
#pragma unroll
        for (int i = 0; i < 8; ++i) u[i] = ld16s(c.rs, uo + (i & 3) * 1024, ub + (i >> 2) * 4096);
        SCHED();
        unsigned t = 0;
#pragma unroll
        for (int i = 0; i < 8; ++i) t = umax(t, top(u[i]));
        if (allok(t) || spend(c)) break;
      }
      RAWBAR();
#pragma unroll
      for (int i = 0; i < 8; ++i) sts16(c.L, UL + (i ^ (2 * (y & 3))) * 1024 + lane * 16, u[i]);
      asm volatile("" ::: "memory");
      *GO = NSEG;
    }
    TICK_DUMP(CW + 256);
  } else if (w < 3) {
    const rsrc_t ro = mkrs(p.out, 2048 * 2048 * 2);
    auto tokens = [&](auto QT) {
      constexpr int qt = QT.value, NX = qt ? 32 : 16;
      RAWBAR();
      for (int s = 0; s < NSEG; ++s) {
        const int tb = R_PT + (s & 7) * PT_SLOT + para * 8192;
        int ua[4], za[4], xb[4], mb[4];
#pragma unroll
        for (int i = 0; i < 4; ++i) {
          ua[i] = opq(UL + (i ^ rot) * 2048 + lane * 16);
          za[i] = opq(ZR + (s & 3) * 32768 + (i ^ rot) * 8192 + (qt ? lane * 16 : (g >> 1) * 1024 + (g & 1) * 512 + cc * 16));
          xb[i] = tb + amap(qt * 16384 + (i ^ rot) * (qt ? 8192 : 4096), 0);
          mb[i] = tb + amap(49152 + qt * 8192, 0) + (i ^ rot) * 2048;
        }
        SCHED();
        RAWBAR();
        if (TOK_WAIT) __builtin_amdgcn_s_sleep(TOK_WAIT);
        TICK(0);
        f32x4 a;
#pragma unroll 1
        for (;;) {
          asm volatile("" ::: "memory");
          v4u pt[NX + 8];
#pragma unroll
          for (int i = 0; i < NX; ++i) pt[i] = ld16s(c.rs, (i & 3) * 1024 + lane * 16, qt ? xb[i >> 3] + ((i >> 2) & 1) * 4096 : xb[i >> 2]);
#pragma unroll
          for (int i = 0; i < 8; ++i) pt[NX + i] = ld16s(c.rs, (i & 1) * 1024 + lane * 16, mb[i >> 1]);
          SCHED();
          TICK(1);
#pragma unroll 1
          while (__builtin_amdgcn_readfirstlane(*GO) < (unsigned)s + 1u) { if (spend(c)) break; }
          asm volatile("" ::: "memory");
          f32x4 d[2] = {f32x4{0.f, 0.f, 0.f, 0.f}, f32x4{0.f, 0.f, 0.f, 0.f}};
          v4u fa[8];
          auto frag = [&](int i) {
            if (i >= NX) fa[i & 7] = lds16(c.L, ua[(i - NX) >> 1] + (i & 1) * 1024);
            else if (qt) fa[i & 7] = lds16(c.L, za[i >> 3] + (i & 7) * 1024);
            else {
              const v2u lo = *(LAS v2u*)(c.L + za[i >> 2] + (i & 3) * 2048), hi = *(LAS v2u*)(c.L + za[i >> 2] + (i & 3) * 2048 + 256);
              fa[i & 7] = v4u{lo.x, lo.y, hi.x, hi.y};
            }
          };
#pragma unroll
          for (int i = 0; i < 7; ++i) frag(i);
          SCHED();
#pragma unroll
          for (int i = 0; i < NX + 8; ++i) {
            if (i + 7 < NX + 8) frag(i + 7);
            d[i & 1] = mfma16(fa[i & 7], pt[i], d[i & 1]);
            SCHED();
          }
          a = d[0] + d[1];
          if (!__any(a[0] != a[0])) break;
          unsigned t = 0;
#pragma unroll
          for (int i = 0; i < NX + 8; ++i) t = umax(t, top(pt[i]));
          if (allok(t) || spend(c)) break;
        }
        __builtin_amdgcn_raw_buffer_store_b64(v2u{pk(a[0], a[1]), pk(a[2], a[3])}, ro, (16 * qt + cc) * 4096 + (16 * y + 4 * g) * 2, s * 32 * 4096, OUT_AUX);
        TICK(2);
        TICK_SUM(3);
      }
    };
    if (w == 1) tokens(IC<0>{}); else tokens(IC<1>{});
  } else {
    unsigned seenZ = NOFLAG;
    const rsrc_t rc = mkrs((const void*)c.ctrl, CTRL_BYTES);
    const int fo = lane < 32 ? lane * 128 : 1 << 30;
    __builtin_amdgcn_s_setprio(3);
    auto least = [&](unsigned v) {
      v |= lane < 32 ? 0u : ~0u;
#pragma unroll
      for (int o = 1; o < 32; o <<= 1) v = min(v, (unsigned)__shfl_xor((int)v, o));
      seenZ = umax(seenZ, __builtin_amdgcn_readfirstlane(v));
    };
    auto await = [&](unsigned need) {
#pragma unroll 1
      while (seenZ < need) {
        asm volatile("" ::: "memory");
        least(__builtin_amdgcn_raw_buffer_load_b32(rc, fo, C_FLAG + (F_Z + (y >> 4) * 32) * 128, AUX_SC1));
        if (seenZ >= need || spend(c)) break;
        __builtin_amdgcn_s_sleep(4);
      }
      __builtin_amdgcn_fence(__ATOMIC_ACQUIRE, "wavefront");
    };
    for (int n = 0; n < NSEG; ++n) {
      TICK(0);
      await(n + 1);
      TICK(1);
      v4u z[32];
      int za[8];
#pragma unroll
      for (int i = 0; i < 8; ++i) za[i] = opq(ZR + (n & 3) * 32768 + (i ^ z_mix(y)) * 1024 + lane * 16);
      unsigned look;
#pragma unroll
      for (int i = 0; i < 32 + Z_WIN; ++i) {
        if (i < 32) z[i] = ld16z(c.rs, (i & 3) * 1024 + lane * 16, R_Z + (n & (ZRING - 1)) * Z_SLOT + y * 65536 + ((para ^ (y >> 5)) & 1) * 8192 + amap((i >> 2) * 4096, 0));
        if (i == 32) { look = __builtin_amdgcn_raw_buffer_load_b32(rc, seenZ < (unsigned)n + 2u ? fo : 1 << 30, C_FLAG + (F_Z + (y >> 4) * 32) * 128, AUX_SC1); TICK(4); }
        SCHED();
        if (i >= Z_WIN) sts16(c.L, za[(i - Z_WIN) & 7] + ((i - Z_WIN) >> 3) * 8192, z[i - Z_WIN]);
        else asm volatile("s_nop " STR(Z_GAP));
        if (i == 30 + Z_WIN) { TICK(3); }
        SCHED();
      }
      least(look);
      if (n == 0) await(PRIME);
      TICK(2);
      { const int s = n; (void)s; TICK_SUM(3); }
      BAR();
    }
    RAWBAR();
  }
  TICK_OUT();
  if (c.budget == 0u) __hip_atomic_store(c.ctrl + C_TMO / 4, 1u, RLX_AGENT);
}

#define MF1A(d, a, b) asm volatile(MF16 "%0, %1, %2, %0" : "+v"(d) : "a"(a), "v"(b))
#define MF32A(d, a, b) asm volatile("v_mfma_f32_32x32x16_bf16 %0, %1, %2, %0" : "+v"(d) : "a"(a), "v"(b))
#define MF32AZ(d, a, b) asm volatile("v_mfma_f32_32x32x16_bf16 %0, %1, %2, 0" : "=&v"(d) : "a"(a), "v"(b))
#define MF1B(d, a, b) asm volatile(MF16 "%0, %1, %2, %0" : "+v"(d) : "v"(a), "a"(b))
#define MF1BZ(d, a, b) asm volatile(MF16 "%0, %1, %2, 0" : "=&v"(d) : "v"(a), "a"(b))
#define MF1AZ(d, a, b) asm volatile(MF16 "%0, %1, %2, 0" : "=&v"(d) : "a"(a), "v"(b))
DEV void g_role(const Params& p, Ctx& c, int mat, int h, int gi) {
  constexpr int EX = 131072;
  c.budget = SPIN_CAP;
  const int w = c.w, lane = c.lane = fresh_lane(), g = lane >> 4, cc = lane & 15;
  unsigned look = claim_look(c);
  const int flip = FLIP && mat < 2 ? ((h >> 4) & 1) ^ 1 : 0;
  unsigned t[7];
  {
    auto pull = [&](const u16* q, int bytes, int v, int so) { return __builtin_amdgcn_raw_buffer_load_b32(mkrs(q, bytes), v, so, 0); };
    const int all = mat < 2 ? 8 * MB : 0, xq = mat == 0 ? 4 * MB : 0;
    t[6] = pull(p.Wv, SLOW_V * all, slow_line(p.Wv, h, (h & 7) + 8 * w, lane) ^ (mat * 128), 0);
    t[0] = pull(p.x, xq, lane < 16 ? (((h & 7) * 4 + w) * 16 + lane) * 128 : 1 << 30, 0);
#pragma unroll
    for (int i = 0; i < 3; ++i) t[1 + i] = pull(p.Wv, HELP_V * all, (256 * w + 4 * (h & 7) + g) * 8192 + (h >> 3) * 2048 + cc * 128, (2 + 2 * i + mat) * 32 * 8192);
    SCHED();
    if (mat < 2) ask(c, look);
    SCHED();
#pragma unroll
    for (int i = 0; i < 2; ++i) t[4 + i] = pull(p.Wo, HELP_O * all, (h * 128 + (lane >> 1)) * 2048 + w * 512 + 256 + (lane & 1) * 128, (2 * i + mat) * 32 * 2048);
    SCHED();
    if (mat == 0) claim(c, 0);
    hold(c, mat == 0 ? GQ_AT : mat == 1 ? GK_AT : 0);
  }
  const rsrc_t rx = mkrs(p.x, 2048 * 2048);
  const int xs = w * 16384 + ((lane * 16) ^ ((w & 1) << 7));

  v32u wa[8];
  {
    const rsrc_t rw = mkrs(mat == 0 ? p.Wq : mat == 1 ? p.Wk : p.Wv, 1024 * 4096 * 2);
    const int vo0 = opq((256 * w + 128 * flip + 8 * g) * 8192 + (h * 128 + 8 * cc) * 2), vo1 = opq(vo0 ^ (128 * 8192));
    v4u row[8][8];
    auto request = [&](int u) {
#pragma unroll
      for (int r = 0; r < 8; ++r) {
        row[u][r] = ld16w<0>(rw, (u & 4) ? vo1 : vo0, (32 * (u & 3) + r) * 8192);
        if ((r & 3) == 3) { const int n = 2 * u + (r >> 2); dma16(rx, c.L + w * 16384 + n * 1024, (xs ^ ((n >> 1) << 4)) + n * 1024); }
      }
    };
    const unsigned look0 = claim_look(c);
    bool claimed = false;
    request(0);
    SCHED();
    if (mat == 2) ask(c, look);
    SCHED();
    request(1); request(2);
    if (G_DEEP) request(3);
    SCHED();
    const int sw_ = EX + w * 8192 + g * 256;
    const int sr0 = opq(EX + (cc & 3) * 1024 + g * 256 + ((4 * w + (cc >> 2) + 4 * cc) & 15) * 16), sr1 = sr0 + 4096;
#pragma unroll
    for (int u = 0; u < 8; ++u) {
#pragma unroll
      for (int i = 0; i < 8; ++i) {
        v4u f;
#pragma unroll
        for (int r = 0; r < 4; ++r) f[r] = prm(row[u][2 * r + 1][i >> 1], row[u][2 * r][i >> 1], i & 1);
        sts16(c.L, sw_ + i * 1024 + ((cc + 4 * i) & 15) * 16, f);
      }
      SCHED();
      if (u == 0) asm volatile("" :: "v"(t[0]), "v"(t[1]), "v"(t[2]), "v"(t[3]), "v"(t[4]), "v"(t[5]), "v"(t[6]));
      if (u == 0) claimed = mat == 0 || claim_try(c, look0);
      if (u == 4 - G_DEEP) look = claim_look(c);
      if (u + 3 + G_DEEP < 8) request(u + 3 + G_DEEP);
      if (u == 7 && !claimed && !claim_try(c, look)) claim(c, 0);
      BAR();
      v8u in[4];
#pragma unroll
      for (int q = 0; q < 4; ++q) in[q] = cat(lds16(c.L, sr0 + q * 8192), lds16(c.L, sr1 + q * 8192));
      SCHED();
#pragma unroll
      for (int q = 0; q < 4; ++q) {
        v8u f = in[q];
#define M_(r) asm volatile("" : "+" r(f))
        AT_ACC(M_, 8 * q + u);
#undef M_
#pragma unroll
        for (int r = 0; r < 8; ++r) wa[2 * q + (u >> 2)][8 * (u & 3) + r] = f[r];
      }
      BAR();
    }
  }
  STAMP(0);
  const int xpar = vote_a(wait_vote(c)) ^ (mat >> 1);
  TICK_DECL;
  asm volatile(G_LOOP
      :: [xo] "v"(cc * 2048 + flip * 256 + ((g ^ cc) << 4)), [xs] "v"(xs), [so] "v"((4 * w + g) * 512 + cc * 16), [fo] "v"((w | lane) ? 1 << 30 : 0),
         [rs] "s"(c.rs), [rx] "s"(rx), [rc] "s"(mkrs((const void*)c.ctrl, CTRL_BYTES)), [fl] "s"(C_FLAG + (mat * 32 + h) * 128), [xp] "s"(xpar),
         [ob] "s"((mat == 1 ? R_KX : R_QX) + h * 16384), [lw] "s"(w * 16384), GTRACE ACC_IN(wa)
      : CL_S, CL_16_32, CL_32_100);
}

DEV void zc_role(const Params& p, Ctx& c, int h) {
  c.budget = SPIN_CAP;
  const int w = c.w, lane = c.lane = fresh_lane(), g = lane >> 4, cc = lane & 15, h2 = lane >> 5, r32 = lane & 31;
  v32u wa[8];
  const unsigned first = claim_look(c);
  {
    const rsrc_t rw = mkrs(p.Wo, 4096 * 1024 * 2);
    const int vo = (h * 128 + 8 * g) * 2048 + (256 * w + 8 * cc) * 2;
    v4u row[8][8];
    auto request = [&](int u) {
#pragma unroll
      for (int r = 0; r < 8; ++r) row[u][r] = ld16w<0>(rw, vo, (32 * (u & 3) + r) * 2048 + (u >> 2) * 256);
    };
    request(0);
    SCHED();
    ask(c, first);
    SCHED();
    request(1); request(2);
    unsigned look = 0;
    const int sw_ = w * 8192 + g * 256, sr_ = w * 8192 + (cc & 7) * 1024 + g * 256;
    v8u two[8];
#pragma unroll
    for (int u = 0; u < 8; ++u) {
#pragma unroll
      for (int i = 0; i < 8; ++i) {
        v4u f;
#pragma unroll
        for (int r = 0; r < 4; ++r) f[r] = prm(row[u][2 * r + 1][i >> 1], row[u][2 * r][i >> 1], i & 1);
        sts16(c.L, sw_ + i * 1024 + ((cc + 2 * i) & 15) * 16, f);
      }
      SCHED();
      if (u == 4) look = claim_look(c);
      if (u + 3 < 8) request(u + 3);
      if (u == 7 && !claim_try(c, look)) claim(c, 0);
      LGKM(0);
#pragma unroll
      for (int q = 0; q < 8; ++q) {
        const v4u o = lds16(c.L, sr_ + ((2 * q + (cc >> 3) + 2 * cc) & 15) * 16);
#pragma unroll
        for (int r = 0; r < 4; ++r) two[q][4 * (u & 1) + r] = o[r];
      }
      SCHED();
      if (u & 1) {
#pragma unroll
        for (int q = 0; q < 8; ++q) {
          const int n = 2 * (8 * (u >> 2) + q) + ((u >> 1) & 1);
#define M_(r) asm volatile("" : "+" r(two[q]))
          AT_ACC(M_, n);
#undef M_
#pragma unroll
          for (int r = 0; r < 8; ++r) wa[n >> 2][8 * (n & 3) + r] = two[q][r];
        }
      }
      LGKM(0);
      SCHED();
    }
  }
  STAMP(0);
  {
    unsigned t = 0;
#pragma unroll
    for (int i = 0; i < 4; ++i) t += __builtin_amdgcn_raw_buffer_load_b32(mkrs(p.x, 4 * MB), ((4 * h + w) * 64 + lane) * 128, i * MB, 0);
    asm volatile("" :: "v"(t));
  }
  BAR();
  const int para = vote_a(wait_vote(c));
  const int ao = g * 512 + cc * 16, zo = 16 * w * 65536 + lane * 16 + ((para ^ (w >> 1)) & 1) * 8192;
  TICK_DECL;
  asm volatile(ZC_LOOP
      : [bd] "+s"(c.budget)
      : [ao] "v"(ao), [a1] "v"(ao + 4096), [z1] "v"(zo + amap((h ^ z_mix(16 * w)) * 1024, 0)), [z2] "v"(zo + 65536 + amap((h ^ z_mix(16 * w + 1)) * 1024, 0)), [fo] "v"(lane ? 1 << 30 : 0),
        [rs] "s"(c.rs), [rc] "s"(mkrs((const void*)c.ctrl, CTRL_BYTES)), [fv] "s"(C_FLAG + (F_V + h) * 128), [fs] "s"(C_FLAG + (F_S + h) * 128),
        [fz] "s"(C_FLAG + (F_Z + w * 32 + h) * 128), [xp] "s"(para ^ 1), [vb] "s"(R_QX + h * 16384), [vd] "s"(C_VERDICT + c.att * 128), [z0] "n"(ZC_PRIME), [zb] "n"(R_Z), [rg] "n"(ZRING - 2), [rm] "n"(ZRING - 1), GTRACE ACC_IN(wa)
      : CL_S, CL_16_32, CL_32_100);
  if (c.budget == 0u) __hip_atomic_store(c.ctrl + C_TMO / 4, 1u, RLX_AGENT);
}

DEV void conv_role(const Params& p, Ctx& c, int cg, int far) {
  constexpr int FS = 4160, PL = 1280, UO = 4 * FS, WS = UO + 8 * PL;
  const int w = c.w, lane = c.lane = fresh_lane(), tid = w * 64 + lane, g = lane >> 4, cc = lane & 15;
  const int d0 = 8 * cg + 2 * w;
  const int pg = ((far ^ (cg >> 5)) & 1) * 8192;
  auto crow = [](int ch) { return (ch >> 1) * 16384 + (ch & 1) * 4096; };
  const int grp = cg >> 3, de = grp * 64 + (tid & 7) * 8, t0 = (cg & 7) * 256 + (tid >> 3) * 8;
  auto arrive = [&](int n) {
    VMWAIT(0);
    BAR();
    if (w == 0 && lane == 0) __hip_atomic_fetch_add(c.ctrl + (C_GRP + n * 128) / 4, 1u, RLX_AGENT);
  };
  auto await = [&](int n) {
    if (w == 0) {
#pragma unroll 1
      while (__builtin_amdgcn_readfirstlane(__hip_atomic_load(c.ctrl + (C_GRP + n * 128) / 4, RLX_AGENT)) < 8u) { if (spend(c)) break; __builtin_amdgcn_s_sleep(1); }
    }
    BAR();
  };
  v4u fr[2][4], ur[2][4], qr[8];
  unsigned up[2][4];
  const int so = cc * 64 + g * 16;
  const int fw = opq(w * WS + 48 + lane * 16), uw = opq(w * WS + UO + g * PL + cc * 16);
  auto copies = [&](int ch) {
#pragma unroll
    for (int i = 0; i < 4; ++i) {
      const v4u f = fr[ch][i];
      sts16(c.L, fw + 3 * FS + i * 1024, f);
      sts8(c.L, fw + FS + i * 1024 + 8, v2u{f[0], f[1]}); sts8(c.L, fw + FS + i * 1024 + 16, v2u{f[2], f[3]});
#pragma unroll
      for (int r = 0; r < 2; ++r) {
        const int o = fw + 2 * r * FS + i * 1024 + 12 - 8 * r;
        *(LAS unsigned*)(c.L + o) = f[0]; sts8(c.L, o + 4, v2u{f[1], f[2]}); *(LAS unsigned*)(c.L + o + 12) = f[3];
      }
    }
  };
  CST(0);
  const int turns = (grp & 3) == 2 ? 2 : (grp & 3) == 3 ? 0 : 1;
#pragma unroll 1
  for (int n = turns - 1; n >= 0; --n) {
    const rsrc_t rk = mkrs(p.bk, 2048 * 2048), rv = mkrs(p.bv, 2048 * 2048);
    const int dn = de + 64 * n;
    v4u kr[11], vr[8], sh[4];
#pragma unroll
    for (int i = 0; i < 4; ++i) sh[i] = ld16c(mkrs(p.shift, 8192), dn * 8 + i * 16, 0);
#pragma unroll
    for (int i = 0; i < 11; ++i) kr[i] = ld16c(rk, (t0 + i - 3) * 2048 + dn * 2, 0);
#pragma unroll
    for (int i = 0; i < 8; ++i) vr[i] = ld16c(rv, (t0 + i) * 2048 + dn * 2, 0);
    SCHED();
    CST(1);
#pragma unroll
    for (int ch = 0; ch < 8; ++ch) {
      const unsigned sa = sh[ch >> 1][(ch & 1) * 2], sb = sh[ch >> 1][(ch & 1) * 2 + 1];
      const float s0 = blo(sa), s1 = bhi(sa), s2 = blo(sb), s3 = bhi(sb);
      float kk[11], o[8];
#pragma unroll
      for (int i = 0; i < 11; ++i) kk[i] = (ch & 1) ? bhi(kr[i][ch >> 1]) : blo(kr[i][ch >> 1]);
#pragma unroll
      for (int i = 0; i < 8; ++i) o[i] = fmaf(kk[i + 3], s0, fmaf(kk[i + 2], s1, fmaf(kk[i + 1], s2, kk[i] * s3))) * ((ch & 1) ? bhi(vr[i][ch >> 1]) : blo(vr[i][ch >> 1]));
      st16s(c.rs, dn * 8192 + t0 * 2, R_KVT + pg + crow(ch), v4u{pk(o[0], o[1]), pk(o[2], o[3]), pk(o[4], o[5]), pk(o[6], o[7])});
    }
    CST(3);
    arrive(2 * (grp + n));
  }
  const unsigned dd = __builtin_amdgcn_raw_buffer_load_b32(mkrs(p.ssmD, 2048), d0 * 2, 0, CONV_AUX);
#pragma unroll
  for (int i = 0; i < 8; ++i) fr[i >> 2][i & 3] = ld16c(mkrs(p.ssmk, 4 * MB), lane * 16 + (i & 3) * 1024, (d0 + (i >> 2)) * 4096);
  SCHED();
  CST(4);
  await(2 * grp);
  CST(5);
#pragma unroll
  for (int i = 0; i < 8; ++i) {
    const int b = R_KVT + pg + d0 * 8192 + (i >> 2) * 4096 + (i & 3) * 1024;
    ur[i >> 2][i & 3] = ld16s(c.rs, so, b);
    up[i >> 2][i & 3] = __builtin_amdgcn_raw_buffer_load_b32(c.rs, (i & 3) == 0 && lane == 0 ? 1 << 30 : so, b - 4, AUX_SC1);
  }
  SCHED();
  if (lane < 16) sts16(c.L, w * WS + (lane >> 2) * FS + (lane & 3) * 16, v4u{0, 0, 0, 0});
#pragma unroll
  for (int i = 0; i < 2; ++i) sts16(c.L, w * WS + UO + (4 * i + g) * PL + cc * 16, v4u{0, 0, 0, 0});
  STAMP(5);
  const int fa = opq(w * WS + (cc & 3) * FS + (3 + (cc >> 2) - g) * 16);
  sfor<2>([&](auto CH) __attribute__((always_inline)) {
    constexpr int ch = CH.value;
    SCHED();
    copies(ch);
    SCHED();
#pragma unroll
    for (int i = 0; i < 4; ++i) {
      const v4u a = ur[ch][i];
      v4u r0, r1;
#pragma unroll
      for (int r = 0; r < 4; ++r) {
        r0[r] = __builtin_amdgcn_alignbyte(a[3 - r], a[3 - r], 2);
        r1[r] = __builtin_amdgcn_perm(r < 3 ? a[2 - r] : up[ch][i], a[3 - r], 0x07060100u);
      }
      sts16(c.L, uw + (16 + 16 * i) * 16, r0);
      sts16(c.L, uw + 4 * PL + (16 + 16 * i) * 16, r1);
    }
    CST(6 + 2 * ch);
    f32x4 acc[4][2];
#pragma unroll
    for (int i = 0; i < 8; ++i) acc[i >> 1][i & 1] = f32x4{0.f, 0.f, 0.f, 0.f};
    v4u A[2][4], B[2][8];
    auto frag = [&](int d, int n) {
      constexpr int order[12] = {0, 4, 5, 6, 7, 8, 9, 10, 11, 1, 2, 3};
      const int x = order[n];
      if (x < 4) A[d & 1][x] = lds16(c.L, fa + (d + 16 * x) * 64);
      else B[d & 1][x - 4] = lds16(c.L, uw + ((x - 4) & 1) * 4 * PL + (16 + 16 * ((x - 4) >> 1) - d) * 16);
    };
    asm volatile("" ::: "memory");
#pragma unroll
    for (int n = 0; n < 12; ++n) frag(0, n);
    SCHED();
    sfor<16>([&](auto D) __attribute__((always_inline)) {
      constexpr int d = D.value;
      int n = 0;
#pragma unroll
      for (int k = 0; k < 4; ++k)
#pragma unroll
        for (int m = 0; m + k < 4; ++m)
#pragma unroll
          for (int e = 0; e < 2; ++e) {
            acc[m + k][e] = mfma16(A[d & 1][k], B[d & 1][2 * m + e], acc[m + k][e]);
            if (d < 15 && n < 12) frag(d + 1, n);
            if (ch == 0 && n == 14 && (d & 1)) qr[d >> 1] = ld16c(mkrs(p.bq, 4 * MB), (t0 + (d >> 1)) * 2048 + de * 2, 0);
            ++n;
            SCHED();
          }
    });
    CST(7 + 2 * ch);
    const float dv = ch ? bhi(dd) : blo(dd);
#pragma unroll
    for (int q = 0; q < 4; ++q) {
      v4u z;
#pragma unroll
      for (int i = 0; i < 4; ++i) {
        const unsigned yy = pk(acc[q][1][i], acc[q][0][i]), ud = pk(blo(ur[ch][q][i]) * dv, bhi(ur[ch][q][i]) * dv);
        z[i] = pk(blo(yy) + blo(ud), bhi(yy) + bhi(ud));
      }
      st16s(c.rs, so + q * 1024, R_YT + pg + d0 * 8192 + ch * 4096, z);
    }
  });
  STAMP(6);
  {
    const rsrc_t ro = mkrs(p.out, 2048 * 2048 * 2);
    v4u yv[8];
    arrive(2 * grp + 1);
    await(2 * grp + 1);
    CST(10);
#pragma unroll
    for (int ch = 0; ch < 8; ++ch) yv[ch] = ld16s(c.rs, de * 8192 + t0 * 2, R_YT + pg + crow(ch));
#pragma unroll
    for (int i = 0; i < 8; ++i) {
      float o[8];
#pragma unroll
      for (int ch = 0; ch < 8; ++ch) o[ch] = ((ch & 1) ? bhi(qr[i][ch >> 1]) : blo(qr[i][ch >> 1])) * ((i & 1) ? bhi(yv[ch][i >> 1]) : blo(yv[ch][i >> 1]));
      __builtin_amdgcn_raw_buffer_store_b128(v4u{pk(o[0], o[1]), pk(o[2], o[3]), pk(o[4], o[5]), pk(o[6], o[7])}, ro, (t0 + i) * 4096 + (1024 + de) * 2, 0, OUT_AUX2);
    }
  }
  if (c.budget == 0u) __hip_atomic_store(c.ctrl + C_TMO / 4, 1u, RLX_AGENT);
}

extern "C" __global__ void __launch_bounds__(256, 1) mk_mega(Params p) {
  extern __shared__ __attribute__((aligned(16))) unsigned char lds_raw[];
  Ctx c;
  c.ctrl = (gu32*)p.ctrl;
  c.rs = mkrs(p.data, R_END);
  c.L = (lds_t)lds_raw;
  c.w = __builtin_amdgcn_readfirstlane(threadIdx.x >> 6);
  c.lane = threadIdx.x & 63;
  c.t0 = (unsigned)__builtin_amdgcn_s_memrealtime();
  c.b = (int)(blockIdx.x & 255u);
  c.att = (int)(blockIdx.x >> 8);
  c.ask = 0;
  c.slot = (c.att ? (c.b & 7) : (int)((unsigned)__builtin_amdgcn_s_getreg(20 | (3 << 11)) & 7u)) * 32 + (c.b >> 3);
  const int x = c.slot >> 5, t = c.slot & 31;
  if (c.w == 1) {
    asm volatile("s_getpc_b64 s[60:61]\n1:\n\ts_sub_u32 s60, s60, 1b-mk_mega\n\ts_subb_u32 s61, s61, 0\n\ts_mov_b32 s62, mk_tail-mk_mega\n\ts_mov_b32 s63, 0x20000\n\t"
                 "buffer_load_dword v255, %0, s[60:63], 0 offen\n\ts_waitcnt vmcnt(0)" :: "v"((c.lane * 32 + t) * 128) : "s60", "s61", "s62", "s63", "v255", "scc", "memory");
  }
  if (c.b == SERVER && c.w == 3) serve(c);
  if (x < 4) {
    if (t < 8) qk_role(p, c, x * 8 + t);
    else if (t < 16) vo_role(p, c, x * 8 + t - 8);
    else y_role(p, c, x * 16 + t - 16);
  } else {
    if (t < 24) g_role(p, c, t >> 3, (x - 4) * 8 + (t & 7), t);
    else zc_role(p, c, (x - 4) * 8 + t - 24);
    STAMP(2);
    c.budget = SPIN_CAP;
    conv_role(p, c, (x - 4) * 32 + t, vote_a(wait_verdict(c)) ^ 1);
  }
  STAMP(3);
}

}

extern "C" void kernel_launch(void* const* d_in, const int* in_sizes, int n_in, void* d_out, int out_size, void* d_ws, size_t ws_size,
                              hipStream_t stream) {
  using namespace mk;
  (void)in_sizes; (void)n_in; (void)out_size;
  Params p;
  p.x = (const u16*)d_in[0]; p.mem0 = (const u16*)d_in[1];
  p.Wq = (const u16*)d_in[2]; p.Wk = (const u16*)d_in[3]; p.Wv = (const u16*)d_in[4]; p.Wo = (const u16*)d_in[5];
  p.bq = (const u16*)d_in[7]; p.bk = (const u16*)d_in[8]; p.bv = (const u16*)d_in[9];
  p.shift = (const u16*)d_in[10]; p.ssmk = (const u16*)d_in[11]; p.ssmD = (const u16*)d_in[12];
  p.out = (u16*)d_out;
  p.ctrl = (unsigned char*)d_ws;
  p.data = data_base(d_ws, ws_size);
  (void)hipFuncSetAttribute((const void*)mk_mega, hipFuncAttributeMaxDynamicSharedMemorySize, LDS_BYTES);
  hipLaunchKernelGGL(mk_mega, dim3(256), dim3(256), LDS_BYTES, stream, p);
}
```

```cpp
#include <hip/hip_runtime.h>
#include <hip/hip_bf16.h>

namespace mk {

#define GAS __attribute__((address_space(1)))
#define LAS __attribute__((address_space(3)))
#define DEV __device__ __forceinline__
#define RLX_AGENT __ATOMIC_RELAXED, __HIP_MEMORY_SCOPE_AGENT
typedef unsigned short u16;
typedef unsigned v2u __attribute__((ext_vector_type(2)));
typedef unsigned v4u __attribute__((ext_vector_type(4)));
typedef unsigned v8u __attribute__((ext_vector_type(8)));
typedef unsigned v16u __attribute__((ext_vector_type(16)));
typedef unsigned v32u __attribute__((ext_vector_type(32)));
typedef float f32x2 __attribute__((ext_vector_type(2)));
typedef float f32x4 __attribute__((ext_vector_type(4)));
typedef float f32x16 __attribute__((ext_vector_type(16)));
typedef __bf16 bf16x2 __attribute__((ext_vector_type(2)));
typedef __bf16 bf16x8 __attribute__((ext_vector_type(8)));
typedef __bf16 bf16x16 __attribute__((ext_vector_type(16)));
typedef GAS unsigned gu32;
typedef __amdgpu_buffer_rsrc_t rsrc_t;
typedef LAS unsigned char* lds_t;

#define XCNT(r) ""
#define UCNT ""
#define CL_T
#define XT(a, b) ""
#define LMARK ""
#define GSUM ""
#define ZSUM ""
#define GTRACE
#define MIXED 1
#define NOFLAG 0
#define TOK_KICK 1
#define ZRING 16
#define Z_AT 16
#define Z_GAP 4
#define STR_(x) #x
#define STR(x) STR_(x)
#define GQ_AT 500
#define GK_AT 300
#define PULL_AT 800
#define QK_AT 0
#define SERVER 194
#define SLOW_LEAD 100
#define PULL_DEPTH 1
#define PRIME 1
#define X_AT 1300
#define VO_AT 200
#define ZC_AT 0
#define CONV_AUX 2
#define W_AUX 0
#define ZC_PRIME 2
#define CODE_PULL 1
#define PROBES 2
#define Z_WIN 20
#define TOK_WAIT 0
#define Z_AUX 0
#define OUT_AUX 2
#define OUT_AUX2 2
#define URING 8
#define YH_AT 0
#define G_DEEP 1
#define AGAIN 0
#define PRE 0
#define YH 1
#define HELP_O 0
#define HELP_V 1
#define SLOW_V 1
#define K_FIRST 1
#define FLIP 1
#define YH_STEP 300
#define Z_WAIT 0
#define HELPERS 2
constexpr int NSEG = 64;
constexpr unsigned SENT = 0xFFFFFFFFu;
constexpr int AUX_SC1 = 16, AUX_NT = 2;
constexpr int T_LAG = 61;
constexpr int G_AHEAD = 4;
constexpr unsigned SPIN_CAP = 1u << 21;
constexpr int LDS_BYTES = 163840;

constexpr int C_ECHO = 0, C_CLAIM = 1024, C_VERDICT = 3072, C_TMO = 3328, C_CONV = 3456, C_VOTE = 3840, C_FLAG = 4096, C_GRP = C_FLAG + 256 * 128, C_ASK = C_GRP + 32 * 128;
constexpr int F_Q = 0, F_K = 32, F_V = 64, F_S = 96, F_Z = 128;
constexpr int CTRL_BYTES = C_ASK + 256 * 64;
constexpr int MB = 1 << 20;
constexpr int R_U = 136 * MB;
constexpr int R_MEM = 2 * MB, MEM_SLOT = 32768;
constexpr int R_P = R_MEM + 4 * MEM_SLOT, P_SLOT = 65536;
constexpr int R_PT = 4 * MB, PT_SLOT = 131072;
constexpr int R_QX = 8 * MB, R_KX = 40 * MB, R_Z = 72 * MB, Z_SLOT = 4 * MB, R_KVT = R_KX, R_YT = R_KX + 8 * MB, R_END = (136 + URING) * MB;

constexpr size_t R_DIAG = 0;
#define TOP 1
static inline unsigned char* data_base(void* ws, size_t size) {
  return (unsigned char*)((TOP ? (size_t)ws + size - R_END - R_DIAG : (size_t)ws + CTRL_BYTES + 2 * MB - 1) & ~(size_t)(2 * MB - 1));
}
struct Params {
  const u16 *x, *mem0, *Wq, *Wk, *Wv, *Wo, *bq, *bk, *bv, *shift, *ssmk, *ssmD;
  u16* out;
  unsigned char* ctrl;
  unsigned char* data;
};

DEV rsrc_t mkrs(const void* p, unsigned bytes) { return __builtin_amdgcn_make_buffer_rsrc((void*)p, 0, bytes, 0x00020000); }
DEV v4u ld16s(rsrc_t rs, int v, int s) { return __builtin_amdgcn_raw_buffer_load_b128(rs, v, s, AUX_SC1); }
DEV v2u ld8s(rsrc_t rs, int v, int s) { return __builtin_amdgcn_raw_buffer_load_b64(rs, v, s, AUX_SC1); }
DEV v4u ld16(rsrc_t rs, int v, int s) { return __builtin_amdgcn_raw_buffer_load_b128(rs, v, s, 0); }
DEV v4u ld16z(rsrc_t rs, int v, int s) { return __builtin_amdgcn_raw_buffer_load_b128(rs, v, s, AUX_SC1 | Z_AUX); }
DEV v4u ld16c(rsrc_t rs, int v, int s) { return __builtin_amdgcn_raw_buffer_load_b128(rs, v, s, CONV_AUX); }
template <int A> DEV v4u ld16w(rsrc_t rs, int v, int s) { return __builtin_amdgcn_raw_buffer_load_b128(rs, v, s, A); }
DEV v2u ld8(rsrc_t rs, int v, int s) { return __builtin_amdgcn_raw_buffer_load_b64(rs, v, s, 0); }
DEV void dma16(rsrc_t rs, lds_t l, int v) {
#if __HIP_DEVICE_COMPILE__
  __builtin_amdgcn_raw_ptr_buffer_load_lds(rs, (LAS void*)l, 16, v, 0, 0, 0);
#endif
}
DEV void st16s(rsrc_t rs, int v, int s, v4u d) { __builtin_amdgcn_raw_buffer_store_b128(d, rs, v, s, AUX_SC1); }
DEV void st8s(rsrc_t rs, int v, int s, v2u d) { __builtin_amdgcn_raw_buffer_store_b64(d, rs, v, s, AUX_SC1); }
DEV void st4s(rsrc_t rs, int v, int s, unsigned d) { __builtin_amdgcn_raw_buffer_store_b32(d, rs, v, s, AUX_SC1); }
DEV int amap(int o, int par) { return ((o >> 13) << 14) | (par << 13) | (o & 8191); }
DEV int xblk(int s, int h, int par) { return (s * 32 + h) * 16384 + ((par ^ (s >> 2)) & 1) * 8192; }
DEV unsigned umax(unsigned a, unsigned b) { return a > b ? a : b; }
DEV unsigned top(v4u v) { return umax(umax(v.x, v.y), umax(v.z, v.w)); }
DEV unsigned pk(float a, float b) { bf16x2 v = {(__bf16)a, (__bf16)b}; return __builtin_bit_cast(unsigned, v); }
DEV unsigned pks(float a, float b) { unsigned u = pk(a, b); return u < SENT - 1u ? u : SENT - 1u; }
DEV float blo(unsigned u) { return __builtin_bit_cast(float, u << 16); }
DEV float bhi(unsigned u) { return __builtin_bit_cast(float, u & 0xFFFF0000u); }
DEV float rb(float a) { return blo(pk(a, a)); }
DEV unsigned fu(float f) { return __builtin_bit_cast(unsigned, f); }
DEV float uf(unsigned u) { return __builtin_bit_cast(float, u); }
DEV f32x4 mfma16(v4u a, v4u b, f32x4 c) {
  return __builtin_amdgcn_mfma_f32_16x16x32_bf16(__builtin_bit_cast(bf16x8, a), __builtin_bit_cast(bf16x8, b), c, 0, 0, 0);
}
DEV f32x16 mfma32(v4u a, v4u b, f32x16 c) {
  return __builtin_amdgcn_mfma_f32_32x32x16_bf16(__builtin_bit_cast(bf16x8, a), __builtin_bit_cast(bf16x8, b), c, 0, 0, 0);
}
DEV f32x4 smf(v4u a, v8u b, f32x4 c, int idx) {
  return __builtin_amdgcn_smfmac_f32_16x16x64_bf16(__builtin_bit_cast(bf16x8, a), __builtin_bit_cast(bf16x16, b), c, idx, 0, 0);
}
DEV v8u cat(v4u a, v4u b) { return __builtin_shufflevector(a, b, 0, 1, 2, 3, 4, 5, 6, 7); }
DEV v4u lo4(v8u a) { return __builtin_shufflevector(a, a, 0, 1, 2, 3); }
DEV v4u hi4(v8u a) { return __builtin_shufflevector(a, a, 4, 5, 6, 7); }
DEV v4u lds16(lds_t L, int o) { return *(LAS v4u*)(L + o); }
DEV void sts16(lds_t L, int o, v4u v) { *(LAS v4u*)(L + o) = v; }
DEV void sts8(lds_t L, int o, v2u v) { *(LAS v2u*)(L + o) = v; }
#define SCHED() __builtin_amdgcn_sched_barrier(0)
#define PIN_A(x) asm volatile("" : "+a"(x))
#define PIN_V(x) asm volatile("" : "+v"(x))
DEV int opq(int v) { asm volatile("" : "+v"(v)); return v; }
DEV int fresh_lane() {
  int l; asm volatile("v_mbcnt_lo_u32_b32 %0, -1, 0\n\tv_mbcnt_hi_u32_b32 %0, -1, %0" : "=v"(l)); return l;
}
DEV f32x4 fresh_zero() {
  f32x2 a, b;
  asm volatile("v_mov_b64 %0, 0" : "=v"(a));
  asm volatile("v_mov_b64 %0, 0" : "=v"(b));
  return __builtin_shufflevector(a, b, 0, 1, 2, 3);
}
DEV unsigned fresh_sent() { unsigned z; asm volatile("v_mov_b32 %0, -1" : "=v"(z)); return z; }
#define SMF "v_smfmac_f32_16x16x64_bf16 "
#define MF16 "v_mfma_f32_16x16x32_bf16 "
#define SMF1(cls, d, a, b) asm volatile(SMF "%0, %1, %2, %3" : "+v"(d) : "v"(a), cls(b), "v"(idx))
#define SMF1F(cls, lo, hi, d, a, b) asm volatile(SMF "v[" #lo ":" #hi "], %1, %2, %3" : "+{v[" #lo ":" #hi "]}"(d) : "v"(a), cls(b), "v"(idx))
#define AT_HOME(M, j) do { const int j_ = (j); if (j_ == 0) M("{v[32:39]}"); if (j_ == 1) M("{v[40:47]}"); if (j_ == 2) M("{v[48:55]}"); if (j_ == 3) M("{v[56:63]}"); if (j_ == 4) M("{v[64:71]}"); if (j_ == 5) M("{v[72:79]}"); if (j_ == 6) M("{v[80:87]}"); if (j_ == 7) M("{v[88:95]}"); if (j_ == 8) M("{v[96:103]}"); if (j_ == 9) M("{v[104:111]}"); if (j_ == 10) M("{v[112:119]}"); if (j_ == 11) M("{v[120:127]}"); if (j_ == 12) M("{v[128:135]}"); if (j_ == 13) M("{v[136:143]}"); if (j_ == 14) M("{v[144:151]}"); if (j_ == 15) M("{v[152:159]}"); } while (0)
#define MF1(d, a, b) asm volatile(MF16 "%0, %1, %2, %0" : "+v"(d) : "v"(a), "v"(b))
#define MF1Z(d, a, b) asm volatile(MF16 "%0, %1, %2, 0" : "=&v"(d) : "v"(a), "v"(b))
#define LGKM(n) asm volatile("s_waitcnt lgkmcnt(" #n ")" ::: "memory")
#define RAWBAR() do { __builtin_amdgcn_s_barrier(); asm volatile("" ::: "memory"); } while (0)
DEV void settle3(f32x4& s0, f32x4& s1, f32x4& s2) { asm volatile("s_nop 7" : "+v"(s0), "+v"(s1), "+v"(s2)); }
DEV void settle4(f32x4 (&d)[4]) { asm volatile("s_nop 7" : "+v"(d[0]), "+v"(d[1]), "+v"(d[2]), "+v"(d[3])); }
#define BAR() do { asm volatile("s_waitcnt lgkmcnt(0)" ::: "memory"); __builtin_amdgcn_s_barrier(); asm volatile("" ::: "memory"); } while (0)
#define VMWAIT(n) asm volatile("s_waitcnt vmcnt(" #n ")" ::: "memory")
DEV float add1(float a, float b) {
  float o; asm("v_add_f32 %0, %1, %2" : "=v"(o) : "v"(a), "v"(b)); return o;
}
DEV float xmax(float v) {
  auto r = __builtin_amdgcn_permlane32_swap(fu(v), fu(v), false, false); v = fmaxf(uf(r[0]), uf(r[1]));
  auto q = __builtin_amdgcn_permlane16_swap(fu(v), fu(v), false, false); return fmaxf(uf(q[0]), uf(q[1]));
}
DEV float xsum(float v) {
  auto r = __builtin_amdgcn_permlane32_swap(fu(v), fu(v), false, false); v = uf(r[0]) + uf(r[1]);
  auto q = __builtin_amdgcn_permlane16_swap(fu(v), fu(v), false, false); return uf(q[0]) + uf(q[1]);
}

DEV float ror8(float v) { return uf(__builtin_amdgcn_update_dpp(0u, fu(v), 0x128, 0xf, 0xf, true)); }
DEV float fmax2(float a, float b) { return __builtin_amdgcn_fmed3f(a, b, __builtin_inff()); }
DEV float fmax3(float a, float b, float c) { float r; asm("v_max3_f32 %0, %1, %2, %3" : "=v"(r) : "v"(a), "v"(b), "v"(c)); return r; }
DEV bool allok(unsigned t) { return __all(t != SENT); }

template <int N> struct IC { static constexpr int value = N; };
template <int N, class F> DEV void sfor(F&& f) { if constexpr (N > 0) { sfor<N - 1>(f); f(IC<N - 1>{}); } }
struct Ctx {
  gu32* ctrl;
  rsrc_t rs;
  lds_t L;
  int w, lane;
  int b, slot, att;
  unsigned ask;
  unsigned t0;
  unsigned budget;
};
DEV bool spend(Ctx& c) {
  const unsigned old = c.budget;
  asm volatile("s_cmp_lg_u32 %0, 0\n\ts_subb_u32 %0, %0, 0" : "+s"(c.budget) :: "scc");
  return old == 0u;
}
DEV unsigned wait_flag(Ctx& c, int flag, unsigned need) {
  gu32* f = c.ctrl + (C_FLAG + flag * 128) / 4;
  unsigned v;
#pragma unroll 1
  for (;;) {
    v = __builtin_amdgcn_readfirstlane(__hip_atomic_load(f, RLX_AGENT));
    if (v >= need || spend(c)) break;
    __builtin_amdgcn_s_sleep(2);
  }
  __builtin_amdgcn_fence(__ATOMIC_ACQUIRE, "wavefront");
  return v;
}
DEV void post_flag(Ctx& c, int flag, unsigned v, int off = 0) {
  st4s(mkrs((const void*)c.ctrl, CTRL_BYTES), (c.w | off | fresh_lane()) ? 1 << 30 : 0, C_FLAG + flag * 128, v);
}
constexpr unsigned CL_A = 1u << 16, CL_VOTED = 1u << 24, CL_FILLED = 1u << 28;
DEV unsigned claim_look(Ctx& c) { return __hip_atomic_load(c.ctrl + C_ECHO / 4 + c.b, RLX_AGENT); }
DEV void ask(Ctx& c, unsigned first) {
  c.ask = __builtin_amdgcn_readfirstlane(first) + 1u;
  st4s(mkrs((const void*)c.ctrl, CTRL_BYTES), (c.w | c.att | fresh_lane()) ? 1 << 30 : 0, C_ASK + c.b * 64, c.ask);
}
DEV void claim(Ctx& c, unsigned votes) {
  if (c.w != 0) return;
  if (c.att == 0) {
#pragma unroll 1
    while (__builtin_amdgcn_readfirstlane(__hip_atomic_load(c.ctrl + C_ECHO / 4 + c.b, RLX_AGENT)) != c.ask) {
      if (spend(c)) break;
      __builtin_amdgcn_s_sleep(2);
    }
  }
  if (fresh_lane() == 0) __hip_atomic_fetch_add(c.ctrl + (C_CLAIM + c.att * 1024) / 4 + c.slot, 1u + votes, RLX_AGENT);
}
DEV bool claim_try(Ctx& c, unsigned look) {
  const bool ok = c.att || __builtin_amdgcn_readfirstlane(look) == c.ask;
  if (ok && c.w == 0 && fresh_lane() == 0) __hip_atomic_fetch_add(c.ctrl + (C_CLAIM + c.att * 1024) / 4 + c.slot, 1u, RLX_AGENT);
  return ok;
}
DEV unsigned wait_verdict(Ctx& c) {
  unsigned v;
#pragma unroll 1
  for (;;) {
    v = __builtin_amdgcn_readfirstlane(__hip_atomic_load(c.ctrl + (C_VERDICT + c.att * 128) / 4, RLX_AGENT));
    if ((v & 1u) || spend(c)) break;
    __builtin_amdgcn_s_sleep(2);
  }
  __builtin_amdgcn_fence(__ATOMIC_ACQUIRE, "wavefront");
  if ((v & 3u) == 3u && c.att == 0) {
    asm volatile("s_waitcnt vmcnt(0) lgkmcnt(0)\n\ts_barrier\n\ts_mov_b64 s[0:1], %0\n\ts_mov_b32 s2, %1\n\tv_mov_b32 v0, %2\n\t"
                 "s_getpc_b64 s[4:5]\n1:\n\ts_sub_u32 s4, s4, 1b-mk_mega\n\ts_subb_u32 s5, s5, 0\n\ts_setpc_b64 s[4:5]"
                 :: "s"(__builtin_amdgcn_kernarg_segment_ptr()), "s"(c.b + 256), "v"(c.w * 64 + fresh_lane()) : "s0", "s1", "s2", "s4", "s5", "v0", "scc", "memory");
    __builtin_unreachable();
  }
  return v;
}
DEV unsigned wait_vote(Ctx& c) {
  unsigned v;
#pragma unroll 1
  for (;;) {
    v = __builtin_amdgcn_readfirstlane(__hip_atomic_load(c.ctrl + C_VOTE / 4, RLX_AGENT));
    if ((v & 1u) || spend(c)) break;
    __builtin_amdgcn_s_sleep(2);
  }
  return v;
}
DEV void serve(Ctx& c) {
  c.budget = SPIN_CAP;
  const int lane = fresh_lane();
  const rsrc_t rc = mkrs((const void*)c.ctrl, CTRL_BYTES);
  if (c.att == 0) {
#pragma unroll
    for (int i = C_CLAIM / 1024; i < C_ASK / 1024; ++i) st16s(rc, lane * 16, i * 1024, v4u{0, 0, 0, 0});
    VMWAIT(0);
  }
  unsigned vote = c.att ? wait_vote(c) : 0u;
  bool echo = c.att == 0, first = true;
  v4u last = {0u, 0u, 0u, 0u};
#pragma unroll 1
  for (;;) {
    asm volatile("" ::: "memory");
    const v4u cl = ld16s(rc, lane * 16, C_CLAIM + c.att * 1024);
    if (echo) {
      v4u e;
#pragma unroll
      for (int i = 0; i < 4; ++i) e[i] = __builtin_amdgcn_raw_buffer_load_b32(rc, (lane * 4 + i) * 64, C_ASK, AUX_SC1);
      if (first || __any(e[0] != last[0] || e[1] != last[1] || e[2] != last[2] || e[3] != last[3])) st16s(rc, lane * 16, C_ECHO, e);
      last = e; first = false;
    }
    unsigned n = 0, one = 1;
    int va = 0, nv = 0, nf = 0;
#pragma unroll
    for (int i = 0; i < 4; ++i) {
      n += cl[i] & 0xFFFFu; one &= (cl[i] & 0xFFFFu) == 1u;
      va += __popcll(__ballot(cl[i] & CL_A)); nv += __popcll(__ballot(cl[i] & CL_VOTED)); nf += __popcll(__ballot(cl[i] & CL_FILLED));
    }
#pragma unroll
    for (int o = 1; o < 64; o <<= 1) n += (unsigned)__shfl_xor((int)n, o);
    const bool all = __builtin_amdgcn_readfirstlane(n) >= 256u, good = __all(one);
    echo = echo && !all;
    if (!vote && (all || max(va, nv - va) > 20)) {
      vote = 2 * va > nv ? 5u : 9u;
      if (lane == 0) __hip_atomic_store(c.ctrl + C_VOTE / 4, vote, RLX_AGENT);
    }
    if (all && (!good || nf >= 64)) {
      if (lane == 0) __hip_atomic_store(c.ctrl + (C_VERDICT + c.att * 128) / 4, vote | (good ? 0u : 2u), RLX_AGENT);
      break;
    }
    if (spend(c)) break;
  }
}
DEV int vote_a(unsigned v) { return (int)((v >> 2) & 1u); }
DEV int vote_b(unsigned v) { return (int)((v >> 3) & 1u); }

#define STAMP(slot) do {} while (0)
#define TICK_DECL
#define LOG(i, s, v) do {} while (0)
#define LOGS(i, s) do {} while (0)
#define CST(i) do {} while (0)
#define LITE(s) do {} while (0)
#define TICK(i) do {} while (0)
#define TICK_SUM(n) do {} while (0)
#define TICK_LDS(b) do {} while (0)
#define TICK_DUMP(b) do {} while (0)
#define TICK_OUT() do {} while (0)

DEV constexpr int krow(int rho8, int i) { return 32 * (rho8 >> 2) + 8 * (rho8 & 1) + 2 * ((rho8 >> 1) & 1) + i; }
DEV int krow_lane(int g) { return 16 * (g >> 1) + 4 * (g & 1); }
DEV unsigned prm(unsigned hi, unsigned lo, int odd) { return __builtin_amdgcn_perm(hi, lo, odd ? 0x07060302u : 0x05040100u); }

#define AT_ACC(M, n) do { const int n_ = (n); if (n_ == 0) M("{a[0:7]}"); if (n_ == 1) M("{a[8:15]}"); if (n_ == 2) M("{a[16:23]}"); if (n_ == 3) M("{a[24:31]}"); if (n_ == 4) M("{a[32:39]}"); if (n_ == 5) M("{a[40:47]}"); if (n_ == 6) M("{a[48:55]}"); if (n_ == 7) M("{a[56:63]}"); if (n_ == 8) M("{a[64:71]}"); if (n_ == 9) M("{a[72:79]}"); if (n_ == 10) M("{a[80:87]}"); if (n_ == 11) M("{a[88:95]}"); if (n_ == 12) M("{a[96:103]}"); if (n_ == 13) M("{a[104:111]}"); if (n_ == 14) M("{a[112:119]}"); if (n_ == 15) M("{a[120:127]}"); if (n_ == 16) M("{a[128:135]}"); if (n_ == 17) M("{a[136:143]}"); if (n_ == 18) M("{a[144:151]}"); if (n_ == 19) M("{a[152:159]}"); if (n_ == 20) M("{a[160:167]}"); if (n_ == 21) M("{a[168:175]}"); if (n_ == 22) M("{a[176:183]}"); if (n_ == 23) M("{a[184:191]}"); if (n_ == 24) M("{a[192:199]}"); if (n_ == 25) M("{a[200:207]}"); if (n_ == 26) M("{a[208:215]}"); if (n_ == 27) M("{a[216:223]}"); if (n_ == 28) M("{a[224:231]}"); if (n_ == 29) M("{a[232:239]}"); if (n_ == 30) M("{a[240:247]}"); if (n_ == 31) M("{a[248:255]}"); } while (0)
#define PIN_ACC0(x) asm volatile("" :: "{a[0:7]}"(x[0]), "{a[8:15]}"(x[1]), "{a[16:23]}"(x[2]), "{a[24:31]}"(x[3]), "{a[32:39]}"(x[4]), "{a[40:47]}"(x[5]), "{a[48:55]}"(x[6]), "{a[56:63]}"(x[7]), "{a[64:71]}"(x[8]), "{a[72:79]}"(x[9]), "{a[80:87]}"(x[10]), "{a[88:95]}"(x[11]), "{a[96:103]}"(x[12]), "{a[104:111]}"(x[13]), "{a[112:119]}"(x[14]), "{a[120:127]}"(x[15]))
#define PIN_ACC1(x) asm volatile("" :: "{a[128:135]}"(x[16]), "{a[136:143]}"(x[17]), "{a[144:151]}"(x[18]), "{a[152:159]}"(x[19]), "{a[160:167]}"(x[20]), "{a[168:175]}"(x[21]), "{a[176:183]}"(x[22]), "{a[184:191]}"(x[23]), "{a[192:199]}"(x[24]), "{a[200:207]}"(x[25]), "{a[208:215]}"(x[26]), "{a[216:223]}"(x[27]), "{a[224:231]}"(x[28]), "{a[232:239]}"(x[29]), "{a[240:247]}"(x[30]), "{a[248:255]}"(x[31]))
#define PIN_HOME(x) asm volatile("" :: "{v[32:39]}"(x[0]), "{v[40:47]}"(x[1]), "{v[48:55]}"(x[2]), "{v[56:63]}"(x[3]), "{v[64:71]}"(x[4]), "{v[72:79]}"(x[5]), "{v[80:87]}"(x[6]), "{v[88:95]}"(x[7]), "{v[96:103]}"(x[8]), "{v[104:111]}"(x[9]), "{v[112:119]}"(x[10]), "{v[120:127]}"(x[11]), "{v[128:135]}"(x[12]), "{v[136:143]}"(x[13]), "{v[144:151]}"(x[14]), "{v[152:159]}"(x[15]))
#define CL_32_100 "v32", "v33", "v34", "v35", "v36", "v37", "v38", "v39", "v40", "v41", "v42", "v43", "v44", "v45", "v46", "v47", "v48", "v49", "v50", "v51", "v52", "v53", "v54", "v55", "v56", "v57", "v58", "v59", "v60", "v61", "v62", "v63", "v64", "v65", "v66", "v67", "v68", "v69", "v70", "v71", "v72", "v73", "v74", "v75", "v76", "v77", "v78", "v79", "v80", "v81", "v82", "v83", "v84", "v85", "v86", "v87", "v88", "v89", "v90", "v91", "v92", "v93", "v94", "v95", "v96", "v97", "v98", "v99", "v100", "v101", "v102", "v103", "v104", "v105", "v106", "v107", "v108", "v109", "v110", "v111", "v112", "v113", "v114", "v115", "v116", "v117", "v118", "v119", "v120", "v121", "v122", "v123", "v124", "v125", "v126", "v127", "v128", "v129", "v130", "v131"
#define ACC_IN(x) "{a[0:31]}"(x[0]), "{a[32:63]}"(x[1]), "{a[64:95]}"(x[2]), "{a[96:127]}"(x[3]), "{a[128:159]}"(x[4]), "{a[160:191]}"(x[5]), "{a[192:223]}"(x[6]), "{a[224:255]}"(x[7])
#define CL_16_32 "v16", "v17", "v18", "v19", "v20", "v21", "v22", "v23", "v24", "v25", "v26", "v27", "v28", "v29", "v30", "v31"
#define CL_120_128 "v120", "v121", "v122", "v123", "v124", "v125", "v126", "v127", "v128", "v129", "v130", "v131", "v132", "v133", "v134", "v135", "v136", "v137", "v138", "v139"
#define CL_160_200 "v160", "v161", "v162", "v163", "v164", "v165", "v166", "v167", "v168", "v169", "v170", "v171", "v172", "v173", "v174", "v175", "v176", "v177", "v178", "v179", "v180", "v181", "v182", "v183", "v184", "v185", "v186", "v187", "v188", "v189", "v190", "v191", "v192", "v193", "v194", "v195", "v196", "v197", "v198", "v199"
#define CL_176_200 "v176", "v177", "v178", "v179", "v180", "v181", "v182", "v183", "v184", "v185", "v186", "v187", "v188", "v189", "v190", "v191", "v192", "v193", "v194", "v195", "v196", "v197", "v198", "v199"
#define CL_200_216 "v200", "v201", "v202", "v203", "v204", "v205", "v206", "v207", "v208", "v209", "v210", "v211", "v212", "v213", "v214", "v215"
#define CL_216_256 "v216", "v217", "v218", "v219", "v220", "v221", "v222", "v223", "v224", "v225", "v226", "v227", "v228", "v229", "v230", "v231", "v232", "v233", "v234", "v235", "v236", "v237", "v238", "v239", "v240", "v241", "v242", "v243", "v244", "v245", "v246", "v247", "v248", "v249", "v250", "v251", "v252", "v253", "v254", "v255"
#define CL_S "memory", "vcc", "scc", "s60", "s61", "s62", "s63", "s64", "s65", "s66", "s67", "s68" CL_T
#define TIMED_WAIT \
    "s_getpc_b64 s[62:63]\n\t" "1:\n\t" "s_memtime s[64:65]\n\t" "s_waitcnt lgkmcnt(0)\n\t" "s_sub_u32 s61, %0, s64\n\t" "s_cmp_gt_i32 s61, 124\n\t" "s_cbranch_scc1 1b\n\t" \
    "s_max_i32 s61, s61, 0\n\t" "s_and_b32 s61, s61, 124\n\t" "s_sub_u32 s61, 2f-1b, s61\n\t" "s_add_u32 s62, s62, s61\n\t" "s_addc_u32 s63, s63, 0\n\t" \
    "s_setpc_b64 s[62:63]\n\t" ".rept 31\n\t" "s_nop 0\n\t" ".endr\n\t" "2:\n\t"
#define QK_STEP0 \
    ".p2align 6\n\t" "v_xor_b32 v216, 0x400, %[mo]\n\t" "s_xor_b32 s60, %[sb], 0x800\n\t" "s_mov_b32 s66, 0x3e0293ee\n\t" "s_mov_b32 s67, 0x3e0293ee\n\t" XT(80, 81) \
    "s_add_u32 s61, %[kb], 0x1000\n\t" "buffer_load_dwordx4 v[16:19], %[kx], %[rs], %[kb] offen offset:0 sc1\n\t" \
    "buffer_load_dwordx4 v[20:23], %[kx], %[rs], %[kb] offen offset:2048 sc1\n\t" "buffer_load_dwordx4 v[24:27], %[kx], %[rs], s61 offen offset:0 sc1\n\t" \
    "buffer_load_dwordx4 v[28:31], %[kx], %[rs], s61 offen offset:2048 sc1\n\t" "buffer_load_dwordx4 v[200:203], %[kx], %[rs], %[kb] offen offset:256 sc1\n\t" \
    "buffer_load_dwordx4 v[204:207], %[kx], %[rs], %[kb] offen offset:2304 sc1\n\t" "buffer_load_dwordx4 v[208:211], %[kx], %[rs], s61 offen offset:256 sc1\n\t" \
    "buffer_load_dwordx4 v[212:215], %[kx], %[rs], s61 offen offset:2304 sc1\n\t" "s_barrier\n\t" "s_waitcnt vmcnt(7)\n\t" "ds_write_b128 %[kx], v[16:19] offset:0\n\t" \
    "s_waitcnt vmcnt(6)\n\t" "ds_write_b128 %[kx], v[20:23] offset:2048\n\t" "s_waitcnt vmcnt(5)\n\t" "ds_write_b128 %[kx], v[24:27] offset:4096\n\t" "s_waitcnt vmcnt(4)\n\t" \
    "ds_write_b128 %[kx], v[28:31] offset:6144\n\t" "s_waitcnt vmcnt(3)\n\t" "ds_write_b128 %[kx], v[200:203] offset:256\n\t" "s_waitcnt vmcnt(2)\n\t" \
    "ds_write_b128 %[kx], v[204:207] offset:2304\n\t" "s_waitcnt vmcnt(1)\n\t" "ds_write_b128 %[kx], v[208:211] offset:4352\n\t" "s_waitcnt vmcnt(0)\n\t" \
    "ds_write_b128 %[kx], v[212:215] offset:6400\n\t" "s_waitcnt lgkmcnt(0)\n\t" "v_mov_b64 v[208:209], 0\n\t" "v_mov_b64 v[210:211], 0\n\t" "v_mov_b64 v[212:213], 0\n\t" \
    "v_mov_b64 v[214:215], 0\n\t" XT(98, 99) "s_barrier\n\t" XT(82, 83) "buffer_load_dwordx4 v[240:243], %[mo], %[rs], %[sb] offen sc1\n\t" "v_mov_b64 v[224:225], 0\n\t" \
    "v_mov_b64 v[226:227], 0\n\t" "v_mov_b64 v[228:229], 0\n\t" "v_mov_b64 v[230:231], 0\n\t" "v_mov_b64 v[232:233], 0\n\t" "v_mov_b64 v[234:235], 0\n\t" \
    "v_mov_b64 v[236:237], 0\n\t" "v_mov_b64 v[238:239], 0\n\t" "buffer_load_dwordx4 v[244:247], v216, %[rs], %[sb] offen sc1\n\t" \
    "ds_read_b128 v[160:163], %[l0] offset:0\n\t" "ds_read_b128 v[164:167], %[l0] offset:1024\n\t" "ds_read_b128 v[168:171], %[l0] offset:2048\n\t" \
    "ds_read_b128 v[172:175], %[l0] offset:3072\n\t" "s_nop 3\n\t" "buffer_load_dwordx4 v[248:251], %[mo], %[rs], s60 offen sc1\n\t" \
    "ds_read_b128 v[176:179], %[l0] offset:4096\n\t" "ds_read_b128 v[180:183], %[l0] offset:5120\n\t" "ds_read_b128 v[184:187], %[l0] offset:6144\n\t" \
    "ds_read_b128 v[188:191], %[l0] offset:7168\n\t" "s_nop 3\n\t" "buffer_load_dwordx4 v[252:255], v216, %[rs], s60 offen sc1\n\t" "v_mov_b32 v219, 160512\n\t" \
    "v_mov_b64 v[220:221], -1\n\t" "v_mov_b64 v[222:223], -1\n\t" "v_mov_b32 v217, 0\n\t" "buffer_store_dwordx2 v[220:221], %[px], %[rs], %[pr] offen sc1\n\t" \
    "buffer_store_dword v220, %[pm], %[rs], %[pr] offen sc1\n\t" "global_load_dword v217, v217, %[fq] sc1\n\t" "10:\n\t" "s_waitcnt vmcnt(6)\n\t" XT(84, 85) \
    "v_smfmac_f32_16x16x64_bf16 v[224:227], v[240:243], a[0:7], %[ix]\n\t" "ds_write_b128 %[l1], v[240:243] offset:0\n\t" \
    "v_smfmac_f32_16x16x64_bf16 v[228:231], v[240:243], a[8:15], %[ix]\n\t" "v_smfmac_f32_16x16x64_bf16 v[232:235], v[240:243], v[32:39], %[ix]\n\t" \
    "s_waitcnt lgkmcnt(7)\n\t" "v_smfmac_f32_16x16x64_bf16 v[236:239], v[240:243], v[160:167], %[ix]\n\t" "ds_read_b128 v[160:163], %[l0] offset:8192\n\t" \
    "ds_read_b128 v[164:167], %[l0] offset:9216\n\t" "s_waitcnt vmcnt(5)\n\t" "v_smfmac_f32_16x16x64_bf16 v[224:227], v[244:247], a[16:23], %[ix]\n\t" \
    "ds_write_b128 %[l1], v[244:247] offset:1024\n\t" "v_smfmac_f32_16x16x64_bf16 v[228:231], v[244:247], a[24:31], %[ix]\n\t" \
    "v_smfmac_f32_16x16x64_bf16 v[232:235], v[244:247], v[40:47], %[ix]\n\t" "s_waitcnt lgkmcnt(8)\n\t" \
    "v_smfmac_f32_16x16x64_bf16 v[236:239], v[244:247], v[168:175], %[ix]\n\t" "ds_read_b128 v[168:171], %[l0] offset:10240\n\t" \
    "ds_read_b128 v[172:175], %[l0] offset:11264\n\t" "s_waitcnt vmcnt(4)\n\t" "v_smfmac_f32_16x16x64_bf16 v[224:227], v[248:251], a[32:39], %[ix]\n\t" \
    "ds_write_b128 %[l1], v[248:251] offset:2048\n\t" "v_smfmac_f32_16x16x64_bf16 v[228:231], v[248:251], a[40:47], %[ix]\n\t" "s_waitcnt lgkmcnt(3)\n\t" "s_barrier\n\t" \
    XT(86, 87) "v_smfmac_f32_16x16x64_bf16 v[232:235], v[248:251], v[48:55], %[ix]\n\t" "ds_read_b128 v[192:195], %[l1] offset:4096\n\t" \
    "ds_read_b128 v[196:199], %[l1] offset:8192\n\t" "v_smfmac_f32_16x16x64_bf16 v[236:239], v[248:251], v[176:183], %[ix]\n\t" \
    "ds_read_b128 v[176:179], %[l0] offset:12288\n\t" "ds_read_b128 v[180:183], %[l0] offset:13312\n\t" "s_waitcnt vmcnt(3)\n\t" \
    "v_smfmac_f32_16x16x64_bf16 v[224:227], v[252:255], a[48:55], %[ix]\n\t" "ds_write_b128 %[l1], v[252:255] offset:3072\n\t" \
    "v_smfmac_f32_16x16x64_bf16 v[228:231], v[252:255], a[56:63], %[ix]\n\t" "ds_read_b128 v[240:243], %[l1] offset:12288\n\t" \
    "ds_read_b128 v[244:247], %[l1] offset:5120\n\t" "v_smfmac_f32_16x16x64_bf16 v[232:235], v[252:255], v[56:63], %[ix]\n\t" "s_nop 1\n\t" \
    "v_smfmac_f32_16x16x64_bf16 v[236:239], v[252:255], v[184:191], %[ix]\n\t" "ds_read_b128 v[184:187], %[l0] offset:14336\n\t" "v_cmp_u_f32 vcc, v224, v225\n\t" \
    "v_cmp_u_f32 s[62:63], v226, v227\n\t" "s_waitcnt lgkmcnt(7)\n\t" "v_smfmac_f32_16x16x64_bf16 v[224:227], v[192:195], a[64:71], %[ix]\n\t" \
    "ds_read_b128 v[188:191], %[l0] offset:15360\n\t" "v_smfmac_f32_16x16x64_bf16 v[228:231], v[192:195], a[72:79], %[ix]\n\t" "s_or_b64 vcc, vcc, s[62:63]\n\t" \
    "s_cbranch_scc1 11f\n\t" "12:\n\t" "v_smfmac_f32_16x16x64_bf16 v[232:235], v[192:195], v[64:71], %[ix]\n\t" "s_waitcnt lgkmcnt(4)\n\t" "s_barrier\n\t" XT(88, 89) \
    "ds_read_b32 v218, v219\n\t" "v_smfmac_f32_16x16x64_bf16 v[236:239], v[192:195], v[160:167], %[ix]\n\t" "ds_read_b128 v[160:163], %[l0] offset:16384\n\t" \
    "ds_read_b128 v[164:167], %[l0] offset:17408\n\t" "v_smfmac_f32_16x16x64_bf16 v[224:227], v[196:199], a[80:87], %[ix]\n\t" \
    "ds_read_b128 v[248:251], %[l1] offset:9216\n\t" "ds_read_b128 v[252:255], %[l1] offset:13312\n\t" \
    "v_smfmac_f32_16x16x64_bf16 v[228:231], v[196:199], a[88:95], %[ix]\n\t" "ds_read_b128 v[192:195], %[l1] offset:6144\n\t" \
    "v_smfmac_f32_16x16x64_bf16 v[232:235], v[196:199], v[72:79], %[ix]\n\t" "v_smfmac_f32_16x16x64_bf16 v[236:239], v[196:199], v[168:175], %[ix]\n\t" \
    "s_waitcnt lgkmcnt(5)\n\t" "v_cmp_eq_u32 vcc, %[rt], v218\n\t" "v_smfmac_f32_16x16x64_bf16 v[224:227], v[240:243], a[96:103], %[ix]\n\t" "s_cbranch_vccnz 13f\n\t" \
    "ds_read_b128 v[168:171], %[l0] offset:18432\n\t" "v_smfmac_f32_16x16x64_bf16 v[228:231], v[240:243], a[104:111], %[ix]\n\t" \
    "ds_read_b128 v[172:175], %[l0] offset:19456\n\t" "ds_read_b128 v[196:199], %[l1] offset:10240\n\t" \
    "v_smfmac_f32_16x16x64_bf16 v[232:235], v[240:243], v[80:87], %[ix]\n\t" "v_smfmac_f32_16x16x64_bf16 v[236:239], v[240:243], v[176:183], %[ix]\n\t" \
    "ds_read_b128 v[176:179], %[l0] offset:20480\n\t" "ds_read_b128 v[180:183], %[l0] offset:21504\n\t" \
    "v_smfmac_f32_16x16x64_bf16 v[224:227], v[244:247], a[112:119], %[ix]\n\t" "ds_read_b128 v[240:243], %[l1] offset:14336\n\t" \
    "v_smfmac_f32_16x16x64_bf16 v[228:231], v[244:247], a[120:127], %[ix]\n\t" "buffer_load_dword %[pd], %[pa], %[rs], %[kn] offen sc1\n\t" \
    "v_smfmac_f32_16x16x64_bf16 v[232:235], v[244:247], v[88:95], %[ix]\n\t" "v_smfmac_f32_16x16x64_bf16 v[236:239], v[244:247], v[184:191], %[ix]\n\t" \
    "ds_read_b128 v[184:187], %[l0] offset:22528\n\t" "ds_read_b128 v[188:191], %[l0] offset:23552\n\t" "s_waitcnt lgkmcnt(10)\n\t" \
    "v_smfmac_f32_16x16x64_bf16 v[224:227], v[248:251], a[128:135], %[ix]\n\t" "ds_read_b128 v[244:247], %[l1] offset:7168\n\t" \
    "v_smfmac_f32_16x16x64_bf16 v[228:231], v[248:251], a[136:143], %[ix]\n\t" "v_smfmac_f32_16x16x64_bf16 v[232:235], v[248:251], v[96:103], %[ix]\n\t" \
    "v_smfmac_f32_16x16x64_bf16 v[236:239], v[248:251], v[160:167], %[ix]\n\t" "ds_read_b128 v[160:163], %[l0] offset:24576\n\t" \
    "ds_read_b128 v[164:167], %[l0] offset:25600\n\t" "s_waitcnt lgkmcnt(12)\n\t" "v_smfmac_f32_16x16x64_bf16 v[224:227], v[252:255], a[144:151], %[ix]\n\t" \
    "ds_read_b128 v[248:251], %[l1] offset:11264\n\t" "v_smfmac_f32_16x16x64_bf16 v[228:231], v[252:255], a[152:159], %[ix]\n\t" \
    "v_smfmac_f32_16x16x64_bf16 v[232:235], v[252:255], v[104:111], %[ix]\n\t" "s_waitcnt lgkmcnt(10)\n\t" \
    "v_smfmac_f32_16x16x64_bf16 v[236:239], v[252:255], v[168:175], %[ix]\n\t" "ds_read_b128 v[168:171], %[l0] offset:26624\n\t" \
    "ds_read_b128 v[172:175], %[l0] offset:27648\n\t" "v_smfmac_f32_16x16x64_bf16 v[224:227], v[192:195], a[160:167], %[ix]\n\t" \
    "ds_read_b128 v[252:255], %[l1] offset:15360\n\t" "v_smfmac_f32_16x16x64_bf16 v[228:231], v[192:195], a[168:175], %[ix]\n\t" "s_waitcnt lgkmcnt(12)\n\t" \
    "v_smfmac_f32_16x16x64_bf16 v[224:227], v[196:199], a[176:183], %[ix]\n\t" "v_smfmac_f32_16x16x64_bf16 v[228:231], v[196:199], a[184:191], %[ix]\n\t" \
    "s_waitcnt lgkmcnt(9)\n\t" "v_smfmac_f32_16x16x64_bf16 v[224:227], v[240:243], a[192:199], %[ix]\n\t" \
    "v_smfmac_f32_16x16x64_bf16 v[228:231], v[240:243], a[200:207], %[ix]\n\t" "s_waitcnt vmcnt(1)\n\t" "v_readfirstlane_b32 s62, v217\n\t" "s_waitcnt lgkmcnt(6)\n\t" \
    "v_smfmac_f32_16x16x64_bf16 v[224:227], v[244:247], a[208:215], %[ix]\n\t" "v_smfmac_f32_16x16x64_bf16 v[228:231], v[244:247], a[216:223], %[ix]\n\t" \
    "s_max_u32 %[sq], %[sq], s62\n\t" "s_waitcnt lgkmcnt(3)\n\t" "v_smfmac_f32_16x16x64_bf16 v[224:227], v[248:251], a[224:231], %[ix]\n\t" \
    "v_smfmac_f32_16x16x64_bf16 v[228:231], v[248:251], a[232:239], %[ix]\n\t" "s_waitcnt lgkmcnt(0)\n\t" \
    "v_smfmac_f32_16x16x64_bf16 v[224:227], v[252:255], a[240:247], %[ix]\n\t" "s_nop 1\n\t" "v_smfmac_f32_16x16x64_bf16 v[228:231], v[252:255], a[248:255], %[ix]\n\t" \
    XT(90, 91) "s_nop 1\n\t" "v_smfmac_f32_16x16x64_bf16 v[232:235], v[192:195], v[112:119], %[ix]\n\t" "s_nop 1\n\t" \
    "v_smfmac_f32_16x16x64_bf16 v[236:239], v[192:195], v[176:183], %[ix]\n\t" "ds_read_b128 v[176:179], %[l0] offset:28672\n\t" "v_add_f32 v224, v224, v225\n\t" \
    "v_add_f32 v226, v226, v227\n\t" "v_smfmac_f32_16x16x64_bf16 v[232:235], v[196:199], v[120:127], %[ix]\n\t" "ds_read_b128 v[180:183], %[l0] offset:29696\n\t" \
    "v_add_f32 v228, v228, v229\n\t" "v_add_f32 v230, v230, v231\n\t" "v_smfmac_f32_16x16x64_bf16 v[236:239], v[196:199], v[184:191], %[ix]\n\t" \
    "ds_read_b128 v[184:187], %[l0] offset:30720\n\t" "v_cvt_pk_bf16_f32 v218, v224, v228\n\t" "v_cvt_pk_bf16_f32 v219, v226, v230\n\t" \
    "v_smfmac_f32_16x16x64_bf16 v[232:235], v[240:243], v[128:135], %[ix]\n\t" "ds_write2_b32 %[qw], v218, v219 offset1:8\n\t" \
    "ds_read_b128 v[188:191], %[l0] offset:31744\n\t" "v_smfmac_f32_16x16x64_bf16 v[236:239], v[240:243], v[160:167], %[ix]\n\t" \
    "ds_read_b128 v[192:195], %[kx] offset:4352\n\t" "ds_read_b128 v[196:199], %[kx] offset:6400\n\t" \
    "v_smfmac_f32_16x16x64_bf16 v[232:235], v[244:247], v[136:143], %[ix]\n\t" "v_smfmac_f32_16x16x64_bf16 v[236:239], v[244:247], v[168:175], %[ix]\n\t" \
    "s_waitcnt lgkmcnt(3)\n\t" "s_barrier\n\t" "v_smfmac_f32_16x16x64_bf16 v[232:235], v[248:251], v[144:151], %[ix]\n\t" "ds_read_b128 v[216:219], %[l1] offset:24576\n\t" \
    "ds_read_b128 v[220:223], %[l1] offset:25600\n\t" "v_smfmac_f32_16x16x64_bf16 v[236:239], v[248:251], v[176:183], %[ix]\n\t" \
    "v_smfmac_f32_16x16x64_bf16 v[232:235], v[252:255], v[152:159], %[ix]\n\t" "s_waitcnt lgkmcnt(4)\n\t" \
    "v_smfmac_f32_16x16x64_bf16 v[236:239], v[252:255], v[184:191], %[ix]\n\t" XT(92, 93) "s_waitcnt lgkmcnt(1)\n\t" \
    "v_smfmac_f32_16x16x64_bf16 v[208:211], v[216:219], v[16:23], %[ix]\n\t" "s_nop 1\n\t" "v_smfmac_f32_16x16x64_bf16 v[212:215], v[216:219], v[200:207], %[ix]\n\t" \
    "s_waitcnt lgkmcnt(0)\n\t" "s_nop 0\n\t" "v_add_f32 v232, v232, v233\n\t" "v_add_f32 v234, v234, v235\n\t" "v_add_f32 v236, v236, v237\n\t" \
    "v_add_f32 v238, v238, v239\n\t" "v_cvt_pk_bf16_f32 v232, v232, v236\n\t" "v_cvt_pk_bf16_f32 v233, v234, v238\n\t" "ds_write2_b32 %[kw], v232, v233 offset1:68\n\t" \
    "v_smfmac_f32_16x16x64_bf16 v[208:211], v[220:223], v[24:31], %[ix]\n\t" "v_smfmac_f32_16x16x64_bf16 v[212:215], v[220:223], v[192:199], %[ix]\n\t" \
    "v_mov_b64 v[224:225], 0\n\t" "v_mov_b64 v[226:227], 0\n\t" "s_waitcnt lgkmcnt(0)\n\t" "s_barrier\n\t" XT(94, 95) "ds_read_b128 v[160:163], %[km] offset:0\n\t" \
    "ds_read_b128 v[164:167], %[km] offset:64\n\t" "ds_read_b128 v[168:171], %[km] offset:128\n\t" "ds_read_b128 v[172:175], %[km] offset:192\n\t" \
    "v_add_f32 v178, v208, v209\n\t" "v_add_f32 v179, v210, v211\n\t" "v_add_f32 v180, v212, v213\n\t" "v_add_f32 v181, v214, v215\n\t" \
    "v_pk_mul_f32 v[178:179], v[178:179], s[66:67]\n\t" "v_pk_mul_f32 v[180:181], v[180:181], s[66:67]\n\t" "v_max_f32 v182, v178, v180\n\t" "v_max_f32 v183, v179, v181\n\t" \
    "s_nop 0\n\t" "v_max_f32_dpp v182, v182, v182 row_ror:8 row_mask:0xf bank_mask:0xf\n\t" "v_max_f32_dpp v183, v183, v183 row_ror:8 row_mask:0xf bank_mask:0xf\n\t" \
    "s_nop 0\n\t" "s_waitcnt lgkmcnt(2)\n\t" "v_smfmac_f32_16x16x64_bf16 v[224:227], v[216:219], v[160:167], %[ix]\n\t" "s_waitcnt lgkmcnt(0)\n\t" \
    "v_smfmac_f32_16x16x64_bf16 v[224:227], v[220:223], v[168:175], %[ix]\n\t" "s_nop 7\n\t" "v_add_f32 v176, v224, v225\n\t" "v_add_f32 v177, v226, v227\n\t" \
    "v_mul_f32 v176, s66, v176\n\t" "v_mul_f32 v177, s66, v177\n\t" "v_max_f32 v182, v176, v182\n\t" "v_max_f32 v183, v177, v183\n\t" "s_nop 0\n\t" \
    "v_max_f32_dpp v182, v182, v182 row_ror:4 row_mask:0xf bank_mask:0xf\n\t" "v_max_f32_dpp v183, v183, v183 row_ror:4 row_mask:0xf bank_mask:0xf\n\t" "s_nop 0\n\t" \
    "v_max_f32_dpp v182, v182, v182 row_ror:2 row_mask:0xf bank_mask:0xf\n\t" "v_max_f32_dpp v183, v183, v183 row_ror:2 row_mask:0xf bank_mask:0xf\n\t" "s_nop 0\n\t" \
    "v_max_f32_dpp v182, v182, v182 row_ror:1 row_mask:0xf bank_mask:0xf\n\t" "v_max_f32_dpp v183, v183, v183 row_ror:1 row_mask:0xf bank_mask:0xf\n\t" \
    "v_pk_add_f32 v[178:179], v[178:179], v[182:183] neg_lo:[0,1] neg_hi:[0,1]\n\t" "v_pk_add_f32 v[180:181], v[180:181], v[182:183] neg_lo:[0,1] neg_hi:[0,1]\n\t" \
    "v_pk_add_f32 v[176:177], v[176:177], v[182:183] neg_lo:[0,1] neg_hi:[0,1]\n\t" "v_exp_f32 v178, v178\n\t" "v_exp_f32 v179, v179\n\t" "v_exp_f32 v180, v180\n\t" \
    "v_exp_f32 v181, v181\n\t" "v_exp_f32 v176, v176\n\t" "v_pk_add_f32 v[182:183], v[178:179], v[180:181]\n\t" "v_exp_f32 v177, v177\n\t" "s_nop 0\n\t" \
    "v_add_f32_dpp v182, v182, v182 row_ror:8 row_mask:0xf bank_mask:0xf\n\t" "v_add_f32_dpp v183, v183, v183 row_ror:8 row_mask:0xf bank_mask:0xf\n\t" \
    "v_add_f32 v182, v182, v176\n\t" "v_add_f32 v183, v183, v177\n\t" "s_nop 0\n\t" "v_add_f32_dpp v182, v182, v182 row_ror:4 row_mask:0xf bank_mask:0xf\n\t" \
    "v_add_f32_dpp v183, v183, v183 row_ror:4 row_mask:0xf bank_mask:0xf\n\t" "s_nop 0\n\t" "v_add_f32_dpp v182, v182, v182 row_ror:2 row_mask:0xf bank_mask:0xf\n\t" \
    "v_add_f32_dpp v183, v183, v183 row_ror:2 row_mask:0xf bank_mask:0xf\n\t" "s_nop 0\n\t" "v_add_f32_dpp v182, v182, v182 row_ror:1 row_mask:0xf bank_mask:0xf\n\t" \
    "v_add_f32_dpp v183, v183, v183 row_ror:1 row_mask:0xf bank_mask:0xf\n\t" "v_rcp_f32 v182, v182\n\t" "v_rcp_f32 v183, v183\n\t" "s_nop 0\n\t" \
    "v_pk_mul_f32 v[176:177], v[176:177], v[182:183]\n\t" "v_pk_mul_f32 v[178:179], v[178:179], v[182:183]\n\t" "v_pk_mul_f32 v[180:181], v[180:181], v[182:183]\n\t" \
    "v_cvt_pk_bf16_f32 v176, v176, v177\n\t" "v_cvt_pk_bf16_f32 v178, v178, v179\n\t" "v_cvt_pk_bf16_f32 v180, v180, v181\n\t" \
    "v_mov_b32_dpp v177, v176 quad_perm:[1,0,3,2] row_mask:0xf bank_mask:0xf\n\t" "v_mov_b32_dpp v179, v178 quad_perm:[1,0,3,2] row_mask:0xf bank_mask:0xf\n\t" \
    "v_mov_b32_dpp v181, v180 quad_perm:[1,0,3,2] row_mask:0xf bank_mask:0xf\n\t" "v_perm_b32 v184, v179, v178, %[ps]\n\t" "v_perm_b32 v185, v181, v180, %[ps]\n\t" \
    "buffer_store_dwordx2 v[184:185], %[px], %[rs], %[pb] offen sc1\n\t" "v_perm_b32 v186, v177, v176, %[ps]\n\t" "buffer_store_dword v186, %[pm], %[rs], %[pb] offen sc1\n\t" \
    XT(96, 97) LMARK "s_waitcnt vmcnt(2)\n\t" "s_branch 99f\n\t" "11:\n\t" "ds_read_b128 v[220:223], %[l1] offset:0\n\t" "s_waitcnt lgkmcnt(0)\n\t" \
    "v_max3_u32 v218, v220, v221, v222\n\t" "v_max_u32 v218, v218, v223\n\t" "ds_read_b128 v[220:223], %[l1] offset:1024\n\t" "s_waitcnt lgkmcnt(0)\n\t" \
    "v_max3_u32 v218, v218, v220, v221\n\t" "v_max3_u32 v218, v218, v222, v223\n\t" "ds_read_b128 v[220:223], %[l1] offset:2048\n\t" "s_waitcnt lgkmcnt(0)\n\t" \
    "v_max3_u32 v218, v218, v220, v221\n\t" "v_max3_u32 v218, v218, v222, v223\n\t" "ds_read_b128 v[220:223], %[l1] offset:3072\n\t" "s_waitcnt lgkmcnt(0)\n\t" \
    "v_max3_u32 v218, v218, v220, v221\n\t" "v_max3_u32 v218, v218, v222, v223\n\t" "v_cmp_eq_u32 vcc, -1, v218\n\t" "s_cbranch_vccz 12b\n\t" "s_cmp_eq_u32 %[rt], 1\n\t" \
    "s_cbranch_scc1 12b\n\t" "v_mov_b32 v218, %[rt]\n\t" "ds_write_b32 v219, v218\n\t" "s_waitcnt lgkmcnt(0)\n\t" "s_branch 12b\n\t" "13:\n\t" "s_sub_u32 %[rt], %[rt], 1\n\t" \
    "s_waitcnt lgkmcnt(0)\n\t" XCNT(100) "14:\n\t" "buffer_load_dwordx4 v[240:243], %[mo], %[rs], %[sb] offen sc1\n\t" \
    "buffer_load_dwordx4 v[244:247], v216, %[rs], %[sb] offen sc1\n\t" "buffer_load_dwordx4 v[248:251], %[mo], %[rs], s60 offen sc1\n\t" \
    "buffer_load_dwordx4 v[252:255], v216, %[rs], s60 offen sc1\n\t" "s_waitcnt vmcnt(0)\n\t" "v_max_u32 v218, v240, v241\n\t" "v_max3_u32 v218, v218, v242, v243\n\t" \
    "v_max3_u32 v218, v218, v244, v245\n\t" "v_max3_u32 v218, v218, v246, v247\n\t" "v_max3_u32 v218, v218, v248, v249\n\t" "v_max3_u32 v218, v218, v250, v251\n\t" \
    "v_max3_u32 v218, v218, v252, v253\n\t" "v_max3_u32 v218, v218, v254, v255\n\t" "v_cmp_eq_u32 vcc, -1, v218\n\t" "s_cbranch_vccz 15f\n\t" "s_cmp_eq_u32 %[bd], 0\n\t" \
    "s_cbranch_scc1 15f\n\t" "s_sub_u32 %[bd], %[bd], 1\n\t" XCNT(101) "s_branch 14b\n\t" "15:\n\t" "ds_read_b128 v[160:163], %[l0] offset:0\n\t" \
    "ds_read_b128 v[164:167], %[l0] offset:1024\n\t" "ds_read_b128 v[168:171], %[l0] offset:2048\n\t" "ds_read_b128 v[172:175], %[l0] offset:3072\n\t" \
    "ds_read_b128 v[176:179], %[l0] offset:4096\n\t" "ds_read_b128 v[180:183], %[l0] offset:5120\n\t" "ds_read_b128 v[184:187], %[l0] offset:6144\n\t" \
    "ds_read_b128 v[188:191], %[l0] offset:7168\n\t" "v_mov_b64 v[224:225], 0\n\t" "v_mov_b64 v[226:227], 0\n\t" "v_mov_b64 v[228:229], 0\n\t" "v_mov_b64 v[230:231], 0\n\t" \
    "v_mov_b64 v[232:233], 0\n\t" "v_mov_b64 v[234:235], 0\n\t" "v_mov_b64 v[236:237], 0\n\t" "v_mov_b64 v[238:239], 0\n\t" "s_barrier\n\t" "s_branch 10b\n\t" "99:\n\t"
#define QK_STEP1 \
    ".p2align 6\n\t" "v_xor_b32 v216, 0x400, %[mo]\n\t" "s_xor_b32 s60, %[sb], 0x800\n\t" "s_add_u32 s61, %[qb], 0x1000\n\t" "s_mov_b32 s66, 0x3e0293ee\n\t" \
    "s_mov_b32 s67, 0x3e0293ee\n\t" XT(80, 81) XT(98, 99) "s_barrier\n\t" XT(82, 83) "buffer_load_dwordx4 v[240:243], %[mo], %[rs], %[sb] offen sc1\n\t" \
    "v_mov_b64 v[224:225], 0\n\t" "v_mov_b64 v[226:227], 0\n\t" "v_mov_b64 v[228:229], 0\n\t" "v_mov_b64 v[230:231], 0\n\t" "v_mov_b64 v[232:233], 0\n\t" \
    "v_mov_b64 v[234:235], 0\n\t" "v_mov_b64 v[236:237], 0\n\t" "v_mov_b64 v[238:239], 0\n\t" "buffer_load_dwordx4 v[244:247], v216, %[rs], %[sb] offen sc1\n\t" \
    "ds_read_b128 v[160:163], %[l0] offset:0\n\t" "ds_read_b128 v[164:167], %[l0] offset:1024\n\t" "ds_read_b128 v[168:171], %[l0] offset:2048\n\t" \
    "ds_read_b128 v[172:175], %[l0] offset:3072\n\t" "s_nop 3\n\t" "buffer_load_dwordx4 v[248:251], %[mo], %[rs], s60 offen sc1\n\t" \
    "ds_read_b128 v[176:179], %[l0] offset:4096\n\t" "ds_read_b128 v[180:183], %[l0] offset:5120\n\t" "ds_read_b128 v[184:187], %[l0] offset:6144\n\t" \
    "ds_read_b128 v[188:191], %[l0] offset:7168\n\t" "s_nop 3\n\t" "buffer_load_dwordx4 v[252:255], v216, %[rs], s60 offen sc1\n\t" "v_mov_b32 v219, 160512\n\t" \
    "v_mov_b64 v[220:221], -1\n\t" "v_mov_b64 v[222:223], -1\n\t" "buffer_store_dwordx2 v[220:221], %[px], %[rs], %[tr] offen sc1\n\t" \
    "buffer_store_dwordx2 v[220:221], %[pm], %[rs], %[tr] offen sc1\n\t" "v_mov_b32 v208, 0xd028965f\n\t" "v_mov_b32 v209, 0xff800000\n\t" "s_mov_b32 s62, 0\n\t" \
    "s_mov_b32 s63, -1\n\t" "v_mov_b32 v210, 0\n\t" "v_cmp_gt_i32 vcc, 0, %[ps]\n\t" "v_cndmask_b32 v204, 0, v209, s[62:63]\n\t" "s_nop 0\n\t" \
    "v_cndmask_b32 v200, 0, v208, vcc\n\t" "v_cmp_gt_i32 vcc, 1, %[ps]\n\t" "v_cndmask_b32 v205, 0, v209, s[62:63]\n\t" "s_nop 0\n\t" "v_cndmask_b32 v201, 0, v208, vcc\n\t" \
    "v_cmp_gt_i32 vcc, 2, %[ps]\n\t" "v_cndmask_b32 v206, 0, v209, s[62:63]\n\t" "s_nop 0\n\t" "v_cndmask_b32 v202, 0, v208, vcc\n\t" "v_cmp_gt_i32 vcc, 3, %[ps]\n\t" \
    "v_cndmask_b32 v207, 0, v209, s[62:63]\n\t" "s_nop 0\n\t" "v_cndmask_b32 v203, 0, v208, vcc\n\t" "20:\n\t" "s_waitcnt vmcnt(5)\n\t" XT(84, 85) \
    "v_smfmac_f32_16x16x64_bf16 v[224:227], v[240:243], a[0:7], %[ix]\n\t" "ds_write_b128 %[l1], v[240:243] offset:4096\n\t" \
    "v_smfmac_f32_16x16x64_bf16 v[228:231], v[240:243], a[8:15], %[ix]\n\t" "v_smfmac_f32_16x16x64_bf16 v[232:235], v[240:243], v[32:39], %[ix]\n\t" \
    "s_waitcnt lgkmcnt(7)\n\t" "v_smfmac_f32_16x16x64_bf16 v[236:239], v[240:243], v[160:167], %[ix]\n\t" "ds_read_b128 v[160:163], %[l0] offset:8192\n\t" \
    "ds_read_b128 v[164:167], %[l0] offset:9216\n\t" "s_waitcnt vmcnt(4)\n\t" "v_smfmac_f32_16x16x64_bf16 v[224:227], v[244:247], a[16:23], %[ix]\n\t" \
    "ds_write_b128 %[l1], v[244:247] offset:5120\n\t" "v_smfmac_f32_16x16x64_bf16 v[228:231], v[244:247], a[24:31], %[ix]\n\t" \
    "v_smfmac_f32_16x16x64_bf16 v[232:235], v[244:247], v[40:47], %[ix]\n\t" "s_waitcnt lgkmcnt(8)\n\t" \
    "v_smfmac_f32_16x16x64_bf16 v[236:239], v[244:247], v[168:175], %[ix]\n\t" "ds_read_b128 v[168:171], %[l0] offset:10240\n\t" \
    "ds_read_b128 v[172:175], %[l0] offset:11264\n\t" "s_waitcnt vmcnt(3)\n\t" "v_smfmac_f32_16x16x64_bf16 v[224:227], v[248:251], a[32:39], %[ix]\n\t" \
    "ds_write_b128 %[l1], v[248:251] offset:6144\n\t" "v_smfmac_f32_16x16x64_bf16 v[228:231], v[248:251], a[40:47], %[ix]\n\t" "s_waitcnt lgkmcnt(3)\n\t" "s_barrier\n\t" \
    XT(86, 87) "v_smfmac_f32_16x16x64_bf16 v[232:235], v[248:251], v[48:55], %[ix]\n\t" "ds_read_b128 v[192:195], %[l1] offset:8192\n\t" \
    "ds_read_b128 v[196:199], %[l1] offset:12288\n\t" "v_smfmac_f32_16x16x64_bf16 v[236:239], v[248:251], v[176:183], %[ix]\n\t" \
    "ds_read_b128 v[176:179], %[l0] offset:12288\n\t" "ds_read_b128 v[180:183], %[l0] offset:13312\n\t" "s_waitcnt vmcnt(2)\n\t" \
    "v_smfmac_f32_16x16x64_bf16 v[224:227], v[252:255], a[48:55], %[ix]\n\t" "ds_write_b128 %[l1], v[252:255] offset:7168\n\t" \
    "v_smfmac_f32_16x16x64_bf16 v[228:231], v[252:255], a[56:63], %[ix]\n\t" "ds_read_b128 v[240:243], %[l1] offset:0\n\t" "ds_read_b128 v[244:247], %[l1] offset:9216\n\t" \
    "v_smfmac_f32_16x16x64_bf16 v[232:235], v[252:255], v[56:63], %[ix]\n\t" "s_nop 1\n\t" "v_smfmac_f32_16x16x64_bf16 v[236:239], v[252:255], v[184:191], %[ix]\n\t" \
    "ds_read_b128 v[184:187], %[l0] offset:14336\n\t" "v_cmp_u_f32 vcc, v224, v225\n\t" "v_cmp_u_f32 s[62:63], v226, v227\n\t" "s_waitcnt lgkmcnt(7)\n\t" \
    "v_smfmac_f32_16x16x64_bf16 v[224:227], v[192:195], a[64:71], %[ix]\n\t" "ds_read_b128 v[188:191], %[l0] offset:15360\n\t" \
    "v_smfmac_f32_16x16x64_bf16 v[228:231], v[192:195], a[72:79], %[ix]\n\t" "s_or_b64 vcc, vcc, s[62:63]\n\t" "s_cbranch_scc1 21f\n\t" "22:\n\t" \
    "v_smfmac_f32_16x16x64_bf16 v[232:235], v[192:195], v[64:71], %[ix]\n\t" "s_waitcnt lgkmcnt(4)\n\t" "s_barrier\n\t" XT(88, 89) "ds_read_b32 v218, v219\n\t" \
    "v_smfmac_f32_16x16x64_bf16 v[236:239], v[192:195], v[160:167], %[ix]\n\t" "ds_read_b128 v[160:163], %[l0] offset:16384\n\t" \
    "ds_read_b128 v[164:167], %[l0] offset:17408\n\t" "v_smfmac_f32_16x16x64_bf16 v[224:227], v[196:199], a[80:87], %[ix]\n\t" \
    "ds_read_b128 v[248:251], %[l1] offset:13312\n\t" "ds_read_b128 v[252:255], %[l1] offset:1024\n\t" \
    "v_smfmac_f32_16x16x64_bf16 v[228:231], v[196:199], a[88:95], %[ix]\n\t" "ds_read_b128 v[192:195], %[l1] offset:10240\n\t" \
    "v_smfmac_f32_16x16x64_bf16 v[232:235], v[196:199], v[72:79], %[ix]\n\t" "v_smfmac_f32_16x16x64_bf16 v[236:239], v[196:199], v[168:175], %[ix]\n\t" \
    "s_waitcnt lgkmcnt(5)\n\t" "v_cmp_eq_u32 vcc, %[rt], v218\n\t" "v_smfmac_f32_16x16x64_bf16 v[224:227], v[240:243], a[96:103], %[ix]\n\t" "s_cbranch_vccnz 23f\n\t" \
    "ds_read_b128 v[168:171], %[l0] offset:18432\n\t" "v_smfmac_f32_16x16x64_bf16 v[228:231], v[240:243], a[104:111], %[ix]\n\t" \
    "ds_read_b128 v[172:175], %[l0] offset:19456\n\t" "ds_read_b128 v[196:199], %[l1] offset:14336\n\t" \
    "v_smfmac_f32_16x16x64_bf16 v[232:235], v[240:243], v[80:87], %[ix]\n\t" "buffer_load_dwordx4 v[16:19], %[hq], %[rs], %[qb] offen offset:0 sc1\n\t" \
    "v_smfmac_f32_16x16x64_bf16 v[236:239], v[240:243], v[176:183], %[ix]\n\t" "ds_read_b128 v[176:179], %[l0] offset:20480\n\t" \
    "ds_read_b128 v[180:183], %[l0] offset:21504\n\t" "v_smfmac_f32_16x16x64_bf16 v[224:227], v[244:247], a[112:119], %[ix]\n\t" \
    "ds_read_b128 v[240:243], %[l1] offset:2048\n\t" "v_smfmac_f32_16x16x64_bf16 v[228:231], v[244:247], a[120:127], %[ix]\n\t" \
    "v_smfmac_f32_16x16x64_bf16 v[232:235], v[244:247], v[88:95], %[ix]\n\t" "buffer_load_dwordx4 v[20:23], %[hq], %[rs], %[qb] offen offset:2048 sc1\n\t" \
    "v_smfmac_f32_16x16x64_bf16 v[236:239], v[244:247], v[184:191], %[ix]\n\t" "ds_read_b128 v[184:187], %[l0] offset:22528\n\t" \
    "ds_read_b128 v[188:191], %[l0] offset:23552\n\t" "s_waitcnt lgkmcnt(10)\n\t" "v_smfmac_f32_16x16x64_bf16 v[224:227], v[248:251], a[128:135], %[ix]\n\t" \
    "ds_read_b128 v[244:247], %[l1] offset:11264\n\t" "v_smfmac_f32_16x16x64_bf16 v[228:231], v[248:251], a[136:143], %[ix]\n\t" \
    "v_smfmac_f32_16x16x64_bf16 v[232:235], v[248:251], v[96:103], %[ix]\n\t" "buffer_load_dwordx4 v[24:27], %[hq], %[rs], s61 offen offset:0 sc1\n\t" \
    "v_smfmac_f32_16x16x64_bf16 v[236:239], v[248:251], v[160:167], %[ix]\n\t" "ds_read_b128 v[160:163], %[l0] offset:24576\n\t" \
    "ds_read_b128 v[164:167], %[l0] offset:25600\n\t" "s_waitcnt lgkmcnt(12)\n\t" "v_smfmac_f32_16x16x64_bf16 v[224:227], v[252:255], a[144:151], %[ix]\n\t" \
    "ds_read_b128 v[248:251], %[l1] offset:15360\n\t" "v_smfmac_f32_16x16x64_bf16 v[228:231], v[252:255], a[152:159], %[ix]\n\t" \
    "v_smfmac_f32_16x16x64_bf16 v[232:235], v[252:255], v[104:111], %[ix]\n\t" "buffer_load_dwordx4 v[28:31], %[hq], %[rs], s61 offen offset:2048 sc1\n\t" \
    "s_waitcnt lgkmcnt(10)\n\t" "v_smfmac_f32_16x16x64_bf16 v[236:239], v[252:255], v[168:175], %[ix]\n\t" "ds_read_b128 v[168:171], %[l0] offset:26624\n\t" \
    "ds_read_b128 v[172:175], %[l0] offset:27648\n\t" "v_smfmac_f32_16x16x64_bf16 v[224:227], v[192:195], a[160:167], %[ix]\n\t" \
    "ds_read_b128 v[252:255], %[l1] offset:3072\n\t" "v_smfmac_f32_16x16x64_bf16 v[228:231], v[192:195], a[168:175], %[ix]\n\t" "s_waitcnt lgkmcnt(12)\n\t" \
    "v_smfmac_f32_16x16x64_bf16 v[224:227], v[196:199], a[176:183], %[ix]\n\t" "v_smfmac_f32_16x16x64_bf16 v[228:231], v[196:199], a[184:191], %[ix]\n\t" \
    "s_waitcnt lgkmcnt(9)\n\t" "v_smfmac_f32_16x16x64_bf16 v[224:227], v[240:243], a[192:199], %[ix]\n\t" \
    "v_smfmac_f32_16x16x64_bf16 v[228:231], v[240:243], a[200:207], %[ix]\n\t" "s_waitcnt lgkmcnt(6)\n\t" \
    "v_smfmac_f32_16x16x64_bf16 v[224:227], v[244:247], a[208:215], %[ix]\n\t" "v_smfmac_f32_16x16x64_bf16 v[228:231], v[244:247], a[216:223], %[ix]\n\t" \
    "s_waitcnt vmcnt(4)\n\t" "s_waitcnt lgkmcnt(3)\n\t" "v_smfmac_f32_16x16x64_bf16 v[224:227], v[248:251], a[224:231], %[ix]\n\t" \
    "v_smfmac_f32_16x16x64_bf16 v[228:231], v[248:251], a[232:239], %[ix]\n\t" "s_waitcnt lgkmcnt(0)\n\t" \
    "v_smfmac_f32_16x16x64_bf16 v[224:227], v[252:255], a[240:247], %[ix]\n\t" "s_nop 1\n\t" "v_smfmac_f32_16x16x64_bf16 v[228:231], v[252:255], a[248:255], %[ix]\n\t" \
    XT(90, 91) "s_nop 1\n\t" "v_smfmac_f32_16x16x64_bf16 v[232:235], v[192:195], v[112:119], %[ix]\n\t" "s_nop 1\n\t" \
    "v_smfmac_f32_16x16x64_bf16 v[236:239], v[192:195], v[176:183], %[ix]\n\t" "ds_read_b128 v[176:179], %[l0] offset:28672\n\t" "v_add_f32 v224, v224, v225\n\t" \
    "v_add_f32 v226, v226, v227\n\t" "v_smfmac_f32_16x16x64_bf16 v[232:235], v[196:199], v[120:127], %[ix]\n\t" "ds_read_b128 v[180:183], %[l0] offset:29696\n\t" \
    "v_add_f32 v228, v228, v229\n\t" "v_add_f32 v230, v230, v231\n\t" "v_smfmac_f32_16x16x64_bf16 v[236:239], v[196:199], v[184:191], %[ix]\n\t" \
    "ds_read_b128 v[184:187], %[l0] offset:30720\n\t" "v_cvt_pk_bf16_f32 v218, v224, v228\n\t" "v_cvt_pk_bf16_f32 v219, v226, v230\n\t" \
    "v_smfmac_f32_16x16x64_bf16 v[232:235], v[240:243], v[128:135], %[ix]\n\t" "ds_write2_b32 %[qw], v218, v219 offset1:8\n\t" \
    "ds_read_b128 v[188:191], %[l0] offset:31744\n\t" "v_smfmac_f32_16x16x64_bf16 v[236:239], v[240:243], v[160:167], %[ix]\n\t" \
    "v_smfmac_f32_16x16x64_bf16 v[232:235], v[244:247], v[136:143], %[ix]\n\t" "v_smfmac_f32_16x16x64_bf16 v[236:239], v[244:247], v[168:175], %[ix]\n\t" \
    "s_waitcnt lgkmcnt(1)\n\t" "s_barrier\n\t" "v_smfmac_f32_16x16x64_bf16 v[232:235], v[248:251], v[144:151], %[ix]\n\t" \
    "v_smfmac_f32_16x16x64_bf16 v[236:239], v[248:251], v[176:183], %[ix]\n\t" "v_smfmac_f32_16x16x64_bf16 v[232:235], v[252:255], v[152:159], %[ix]\n\t" \
    "s_waitcnt lgkmcnt(0)\n\t" "v_smfmac_f32_16x16x64_bf16 v[236:239], v[252:255], v[184:191], %[ix]\n\t" XT(92, 93) "s_nop 6\n\t" "v_add_f32 v232, v232, v233\n\t" \
    "v_add_f32 v234, v234, v235\n\t" "v_add_f32 v236, v236, v237\n\t" "v_add_f32 v238, v238, v239\n\t" "v_cvt_pk_bf16_f32 v232, v232, v236\n\t" \
    "v_cvt_pk_bf16_f32 v233, v234, v238\n\t" "ds_write2_b32 %[kw], v232, v233 offset1:68\n\t" "s_waitcnt lgkmcnt(0)\n\t" "s_barrier\n\t" XT(94, 95) \
    "s_cmp_ge_u32 %[sq], %[sn]\n\t" "s_cbranch_scc1 277f\n\t" "275:\n\t" "global_load_dword v240, v210, %[fq] sc1\n\t" "s_waitcnt vmcnt(0)\n\t" \
    "v_readfirstlane_b32 %[sq], v240\n\t" "s_nop 3\n\t" "s_cmp_ge_u32 %[sq], %[sn]\n\t" "s_cbranch_scc1 276f\n\t" "s_cmp_eq_u32 %[bd], 0\n\t" "s_cbranch_scc1 276f\n\t" \
    "s_sub_u32 %[bd], %[bd], 1\n\t" "s_sleep 2\n\t" "s_branch 275b\n\t" "276:\n\t" "buffer_load_dwordx4 v[16:19], %[hq], %[rs], %[qb] offen offset:0 sc1\n\t" \
    "buffer_load_dwordx4 v[20:23], %[hq], %[rs], %[qb] offen offset:2048 sc1\n\t" "buffer_load_dwordx4 v[24:27], %[hq], %[rs], s61 offen offset:0 sc1\n\t" \
    "buffer_load_dwordx4 v[28:31], %[hq], %[rs], s61 offen offset:2048 sc1\n\t" "277:\n\t" "s_nop 3\n\t" "ds_read_b128 v[160:163], %[kx] offset:0\n\t" \
    "ds_read_b128 v[164:167], %[kx] offset:2048\n\t" "ds_read_b128 v[168:171], %[kx] offset:4096\n\t" "ds_read_b128 v[172:175], %[kx] offset:6144\n\t" \
    "ds_read_b128 v[224:227], %[km] offset:0\n\t" "ds_read_b128 v[228:231], %[km] offset:64\n\t" "ds_read_b128 v[232:235], %[km] offset:128\n\t" \
    "ds_read_b128 v[236:239], %[km] offset:192\n\t" "s_waitcnt vmcnt(0)\n\t" "global_load_dword v240, v210, %[fq] sc1\n\t" "s_waitcnt lgkmcnt(7)\n\t" \
    "v_mfma_f32_16x16x32_bf16 v[248:251], v[160:163], v[16:19], v[200:203]\n\t" "s_waitcnt lgkmcnt(6)\n\t" \
    "v_mfma_f32_16x16x32_bf16 v[248:251], v[164:167], v[20:23], v[248:251]\n\t" "s_waitcnt lgkmcnt(5)\n\t" \
    "v_mfma_f32_16x16x32_bf16 v[248:251], v[168:171], v[24:27], v[248:251]\n\t" "s_waitcnt lgkmcnt(4)\n\t" \
    "v_mfma_f32_16x16x32_bf16 v[248:251], v[172:175], v[28:31], v[248:251]\n\t" "s_waitcnt lgkmcnt(3)\n\t" \
    "v_mfma_f32_16x16x32_bf16 v[244:247], v[224:227], v[16:19], v[204:207]\n\t" "s_waitcnt lgkmcnt(2)\n\t" \
    "v_mfma_f32_16x16x32_bf16 v[244:247], v[228:231], v[20:23], v[244:247]\n\t" "s_waitcnt lgkmcnt(1)\n\t" \
    "v_mfma_f32_16x16x32_bf16 v[244:247], v[232:235], v[24:27], v[244:247]\n\t" "s_waitcnt lgkmcnt(0)\n\t" \
    "v_mfma_f32_16x16x32_bf16 v[244:247], v[236:239], v[28:31], v[244:247]\n\t" "s_nop 3\n\t" "v_max3_f32 v242, v248, v249, v250\n\t" "v_max_f32 v242, v242, v251\n\t" \
    "s_nop 1\n\t" "v_max3_f32 v242, v242, v244, v245\n\t" "v_max3_f32 v242, v242, v246, v247\n\t" "v_mov_b32 v243, v242\n\t" "s_nop 1\n\t" \
    "v_permlane32_swap_b32 v242, v243\n\t" "v_max_f32 v242, v242, v243\n\t" "v_mov_b32 v243, v242\n\t" "s_nop 1\n\t" "v_permlane16_swap_b32 v242, v243\n\t" \
    "v_max_f32 v242, v242, v243\n\t" "v_pk_add_f32 v[244:245], v[244:245], v[242:243] op_sel_hi:[1,0] neg_lo:[0,1] neg_hi:[0,1]\n\t" \
    "v_pk_add_f32 v[246:247], v[246:247], v[242:243] op_sel_hi:[1,0] neg_lo:[0,1] neg_hi:[0,1]\n\t" \
    "v_pk_add_f32 v[248:249], v[248:249], v[242:243] op_sel_hi:[1,0] neg_lo:[0,1] neg_hi:[0,1]\n\t" \
    "v_pk_add_f32 v[250:251], v[250:251], v[242:243] op_sel_hi:[1,0] neg_lo:[0,1] neg_hi:[0,1]\n\t" "v_pk_mul_f32 v[244:245], v[244:245], s[66:67]\n\t" \
    "v_pk_mul_f32 v[246:247], v[246:247], s[66:67]\n\t" "v_pk_mul_f32 v[248:249], v[248:249], s[66:67]\n\t" "v_pk_mul_f32 v[250:251], v[250:251], s[66:67]\n\t" \
    "v_exp_f32 v244, v244\n\t" "v_exp_f32 v245, v245\n\t" "v_exp_f32 v246, v246\n\t" "v_exp_f32 v247, v247\n\t" "v_exp_f32 v248, v248\n\t" "v_exp_f32 v249, v249\n\t" \
    "v_exp_f32 v250, v250\n\t" "v_exp_f32 v251, v251\n\t" "v_pk_add_f32 v[242:243], v[244:245], v[246:247]\n\t" "v_pk_add_f32 v[242:243], v[242:243], v[248:249]\n\t" \
    "v_pk_add_f32 v[242:243], v[242:243], v[250:251]\n\t" "v_add_f32 v242, v242, v243\n\t" "v_mov_b32 v243, v242\n\t" "s_nop 1\n\t" "v_permlane32_swap_b32 v242, v243\n\t" \
    "v_add_f32 v242, v242, v243\n\t" "v_mov_b32 v243, v242\n\t" "s_nop 1\n\t" "v_permlane16_swap_b32 v242, v243\n\t" "v_add_f32 v242, v242, v243\n\t" \
    "v_rcp_f32 v242, v242\n\t" "s_nop 0\n\t" "v_pk_mul_f32 v[244:245], v[244:245], v[242:243] op_sel_hi:[1,0]\n\t" \
    "v_pk_mul_f32 v[246:247], v[246:247], v[242:243] op_sel_hi:[1,0]\n\t" "v_pk_mul_f32 v[248:249], v[248:249], v[242:243] op_sel_hi:[1,0]\n\t" \
    "v_pk_mul_f32 v[250:251], v[250:251], v[242:243] op_sel_hi:[1,0]\n\t" "v_cvt_pk_bf16_f32 v162, v244, v245\n\t" "v_cvt_pk_bf16_f32 v163, v246, v247\n\t" \
    "v_cvt_pk_bf16_f32 v164, v248, v249\n\t" "v_cvt_pk_bf16_f32 v165, v250, v251\n\t" "buffer_store_dwordx2 v[164:165], %[px], %[rs], %[tb] offen sc1\n\t" \
    "buffer_store_dwordx2 v[162:163], %[pm], %[rs], %[tb] offen sc1\n\t" "s_waitcnt vmcnt(2)\n\t" "v_readfirstlane_b32 s62, v240\n\t" "s_nop 3\n\t" \
    "s_max_u32 %[sq], %[sq], s62\n\t" "s_barrier\n\t" XT(96, 97) "s_branch 99f\n\t" "21:\n\t" "ds_read_b128 v[220:223], %[l1] offset:4096\n\t" "s_waitcnt lgkmcnt(0)\n\t" \
    "v_max3_u32 v218, v220, v221, v222\n\t" "v_max_u32 v218, v218, v223\n\t" "ds_read_b128 v[220:223], %[l1] offset:5120\n\t" "s_waitcnt lgkmcnt(0)\n\t" \
    "v_max3_u32 v218, v218, v220, v221\n\t" "v_max3_u32 v218, v218, v222, v223\n\t" "ds_read_b128 v[220:223], %[l1] offset:6144\n\t" "s_waitcnt lgkmcnt(0)\n\t" \
    "v_max3_u32 v218, v218, v220, v221\n\t" "v_max3_u32 v218, v218, v222, v223\n\t" "ds_read_b128 v[220:223], %[l1] offset:7168\n\t" "s_waitcnt lgkmcnt(0)\n\t" \
    "v_max3_u32 v218, v218, v220, v221\n\t" "v_max3_u32 v218, v218, v222, v223\n\t" "v_cmp_eq_u32 vcc, -1, v218\n\t" "s_cbranch_vccz 22b\n\t" "s_cmp_eq_u32 %[rt], 1\n\t" \
    "s_cbranch_scc1 22b\n\t" "v_mov_b32 v218, %[rt]\n\t" "ds_write_b32 v219, v218\n\t" "s_waitcnt lgkmcnt(0)\n\t" "s_branch 22b\n\t" "23:\n\t" "s_sub_u32 %[rt], %[rt], 1\n\t" \
    "s_waitcnt lgkmcnt(0)\n\t" XCNT(100) "24:\n\t" "buffer_load_dwordx4 v[240:243], %[mo], %[rs], %[sb] offen sc1\n\t" \
    "buffer_load_dwordx4 v[244:247], v216, %[rs], %[sb] offen sc1\n\t" "buffer_load_dwordx4 v[248:251], %[mo], %[rs], s60 offen sc1\n\t" \
    "buffer_load_dwordx4 v[252:255], v216, %[rs], s60 offen sc1\n\t" "s_waitcnt vmcnt(0)\n\t" "v_max_u32 v218, v240, v241\n\t" "v_max3_u32 v218, v218, v242, v243\n\t" \
    "v_max3_u32 v218, v218, v244, v245\n\t" "v_max3_u32 v218, v218, v246, v247\n\t" "v_max3_u32 v218, v218, v248, v249\n\t" "v_max3_u32 v218, v218, v250, v251\n\t" \
    "v_max3_u32 v218, v218, v252, v253\n\t" "v_max3_u32 v218, v218, v254, v255\n\t" "v_cmp_eq_u32 vcc, -1, v218\n\t" "s_cbranch_vccz 25f\n\t" "s_cmp_eq_u32 %[bd], 0\n\t" \
    "s_cbranch_scc1 25f\n\t" "s_sub_u32 %[bd], %[bd], 1\n\t" XCNT(101) "s_branch 24b\n\t" "25:\n\t" "ds_read_b128 v[160:163], %[l0] offset:0\n\t" \
    "ds_read_b128 v[164:167], %[l0] offset:1024\n\t" "ds_read_b128 v[168:171], %[l0] offset:2048\n\t" "ds_read_b128 v[172:175], %[l0] offset:3072\n\t" \
    "ds_read_b128 v[176:179], %[l0] offset:4096\n\t" "ds_read_b128 v[180:183], %[l0] offset:5120\n\t" "ds_read_b128 v[184:187], %[l0] offset:6144\n\t" \
    "ds_read_b128 v[188:191], %[l0] offset:7168\n\t" "v_mov_b64 v[224:225], 0\n\t" "v_mov_b64 v[226:227], 0\n\t" "v_mov_b64 v[228:229], 0\n\t" "v_mov_b64 v[230:231], 0\n\t" \
    "v_mov_b64 v[232:233], 0\n\t" "v_mov_b64 v[234:235], 0\n\t" "v_mov_b64 v[236:237], 0\n\t" "v_mov_b64 v[238:239], 0\n\t" "s_barrier\n\t" "s_branch 20b\n\t" "99:\n\t"
#define QK_STEP2 \
    ".p2align 6\n\t" "v_xor_b32 v216, 0x400, %[mo]\n\t" "s_xor_b32 s60, %[sb], 0x800\n\t" "s_add_u32 s61, %[qb], 0x1000\n\t" "s_mov_b32 s66, 0x3e0293ee\n\t" \
    "s_mov_b32 s67, 0x3e0293ee\n\t" XT(80, 81) XT(98, 99) "s_barrier\n\t" XT(82, 83) "buffer_load_dwordx4 v[240:243], %[mo], %[rs], %[sb] offen sc1\n\t" \
    "v_mov_b64 v[224:225], 0\n\t" "v_mov_b64 v[226:227], 0\n\t" "v_mov_b64 v[228:229], 0\n\t" "v_mov_b64 v[230:231], 0\n\t" "v_mov_b64 v[232:233], 0\n\t" \
    "v_mov_b64 v[234:235], 0\n\t" "v_mov_b64 v[236:237], 0\n\t" "v_mov_b64 v[238:239], 0\n\t" "buffer_load_dwordx4 v[244:247], v216, %[rs], %[sb] offen sc1\n\t" \
    "ds_read_b128 v[160:163], %[l0] offset:0\n\t" "ds_read_b128 v[164:167], %[l0] offset:1024\n\t" "ds_read_b128 v[168:171], %[l0] offset:2048\n\t" \
    "ds_read_b128 v[172:175], %[l0] offset:3072\n\t" "s_nop 3\n\t" "buffer_load_dwordx4 v[248:251], %[mo], %[rs], s60 offen sc1\n\t" \
    "ds_read_b128 v[176:179], %[l0] offset:4096\n\t" "ds_read_b128 v[180:183], %[l0] offset:5120\n\t" "ds_read_b128 v[184:187], %[l0] offset:6144\n\t" \
    "ds_read_b128 v[188:191], %[l0] offset:7168\n\t" "s_nop 3\n\t" "buffer_load_dwordx4 v[252:255], v216, %[rs], s60 offen sc1\n\t" "v_mov_b32 v219, 160512\n\t" \
    "v_mov_b64 v[220:221], -1\n\t" "v_mov_b64 v[222:223], -1\n\t" "buffer_store_dwordx4 v[220:223], %[px], %[rs], %[tr] offen sc1\n\t" \
    "buffer_store_dwordx2 v[220:221], %[pm], %[rs], %[tr] offen sc1\n\t" "v_mov_b32 v208, 0xd028965f\n\t" "v_mov_b32 v209, 0xff800000\n\t" "s_mov_b32 s62, 0\n\t" \
    "s_mov_b32 s63, -1\n\t" "v_mov_b32 v210, 0\n\t" "v_cmp_gt_i32 vcc, 0, %[ps]\n\t" "v_cndmask_b32 v204, 0, v209, s[62:63]\n\t" "s_nop 0\n\t" \
    "v_cndmask_b32 v200, 0, v208, vcc\n\t" "v_cmp_gt_i32 vcc, 1, %[ps]\n\t" "v_cndmask_b32 v205, 0, v209, s[62:63]\n\t" "s_nop 0\n\t" "v_cndmask_b32 v201, 0, v208, vcc\n\t" \
    "v_cmp_gt_i32 vcc, 2, %[ps]\n\t" "v_cndmask_b32 v206, 0, v209, s[62:63]\n\t" "s_nop 0\n\t" "v_cndmask_b32 v202, 0, v208, vcc\n\t" "v_cmp_gt_i32 vcc, 3, %[ps]\n\t" \
    "v_cndmask_b32 v207, 0, v209, s[62:63]\n\t" "s_nop 0\n\t" "v_cndmask_b32 v203, 0, v208, vcc\n\t" "30:\n\t" "s_waitcnt vmcnt(5)\n\t" XT(84, 85) \
    "v_smfmac_f32_16x16x64_bf16 v[224:227], v[240:243], a[0:7], %[ix]\n\t" "ds_write_b128 %[l1], v[240:243] offset:8192\n\t" \
    "v_smfmac_f32_16x16x64_bf16 v[228:231], v[240:243], a[8:15], %[ix]\n\t" "v_smfmac_f32_16x16x64_bf16 v[232:235], v[240:243], v[32:39], %[ix]\n\t" \
    "s_waitcnt lgkmcnt(7)\n\t" "v_smfmac_f32_16x16x64_bf16 v[236:239], v[240:243], v[160:167], %[ix]\n\t" "ds_read_b128 v[160:163], %[l0] offset:8192\n\t" \
    "ds_read_b128 v[164:167], %[l0] offset:9216\n\t" "s_waitcnt vmcnt(4)\n\t" "v_smfmac_f32_16x16x64_bf16 v[224:227], v[244:247], a[16:23], %[ix]\n\t" \
    "ds_write_b128 %[l1], v[244:247] offset:9216\n\t" "v_smfmac_f32_16x16x64_bf16 v[228:231], v[244:247], a[24:31], %[ix]\n\t" \
    "v_smfmac_f32_16x16x64_bf16 v[232:235], v[244:247], v[40:47], %[ix]\n\t" "s_waitcnt lgkmcnt(8)\n\t" \
    "v_smfmac_f32_16x16x64_bf16 v[236:239], v[244:247], v[168:175], %[ix]\n\t" "ds_read_b128 v[168:171], %[l0] offset:10240\n\t" \
    "ds_read_b128 v[172:175], %[l0] offset:11264\n\t" "s_waitcnt vmcnt(3)\n\t" "v_smfmac_f32_16x16x64_bf16 v[224:227], v[248:251], a[32:39], %[ix]\n\t" \
    "ds_write_b128 %[l1], v[248:251] offset:10240\n\t" "v_smfmac_f32_16x16x64_bf16 v[228:231], v[248:251], a[40:47], %[ix]\n\t" "s_waitcnt lgkmcnt(3)\n\t" "s_barrier\n\t" \
    XT(86, 87) "v_smfmac_f32_16x16x64_bf16 v[232:235], v[248:251], v[48:55], %[ix]\n\t" "ds_read_b128 v[192:195], %[l1] offset:12288\n\t" \
    "ds_read_b128 v[196:199], %[l1] offset:0\n\t" "v_smfmac_f32_16x16x64_bf16 v[236:239], v[248:251], v[176:183], %[ix]\n\t" "ds_read_b128 v[176:179], %[l0] offset:12288\n\t" \
    "ds_read_b128 v[180:183], %[l0] offset:13312\n\t" "s_waitcnt vmcnt(2)\n\t" "v_smfmac_f32_16x16x64_bf16 v[224:227], v[252:255], a[48:55], %[ix]\n\t" \
    "ds_write_b128 %[l1], v[252:255] offset:11264\n\t" "v_smfmac_f32_16x16x64_bf16 v[228:231], v[252:255], a[56:63], %[ix]\n\t" \
    "ds_read_b128 v[240:243], %[l1] offset:4096\n\t" "ds_read_b128 v[244:247], %[l1] offset:13312\n\t" \
    "v_smfmac_f32_16x16x64_bf16 v[232:235], v[252:255], v[56:63], %[ix]\n\t" "s_nop 1\n\t" "v_smfmac_f32_16x16x64_bf16 v[236:239], v[252:255], v[184:191], %[ix]\n\t" \
    "ds_read_b128 v[184:187], %[l0] offset:14336\n\t" "v_cmp_u_f32 vcc, v224, v225\n\t" "v_cmp_u_f32 s[62:63], v226, v227\n\t" "s_waitcnt lgkmcnt(7)\n\t" \
    "v_smfmac_f32_16x16x64_bf16 v[224:227], v[192:195], a[64:71], %[ix]\n\t" "ds_read_b128 v[188:191], %[l0] offset:15360\n\t" \
    "v_smfmac_f32_16x16x64_bf16 v[228:231], v[192:195], a[72:79], %[ix]\n\t" "s_or_b64 vcc, vcc, s[62:63]\n\t" "s_cbranch_scc1 31f\n\t" "32:\n\t" \
    "v_smfmac_f32_16x16x64_bf16 v[232:235], v[192:195], v[64:71], %[ix]\n\t" "s_waitcnt lgkmcnt(4)\n\t" "s_barrier\n\t" XT(88, 89) "ds_read_b32 v218, v219\n\t" \
    "v_smfmac_f32_16x16x64_bf16 v[236:239], v[192:195], v[160:167], %[ix]\n\t" "ds_read_b128 v[160:163], %[l0] offset:16384\n\t" \
    "ds_read_b128 v[164:167], %[l0] offset:17408\n\t" "v_smfmac_f32_16x16x64_bf16 v[224:227], v[196:199], a[80:87], %[ix]\n\t" \
    "ds_read_b128 v[248:251], %[l1] offset:1024\n\t" "ds_read_b128 v[252:255], %[l1] offset:5120\n\t" "v_smfmac_f32_16x16x64_bf16 v[228:231], v[196:199], a[88:95], %[ix]\n\t" \
    "ds_read_b128 v[192:195], %[l1] offset:14336\n\t" "v_smfmac_f32_16x16x64_bf16 v[232:235], v[196:199], v[72:79], %[ix]\n\t" \
    "v_smfmac_f32_16x16x64_bf16 v[236:239], v[196:199], v[168:175], %[ix]\n\t" "s_waitcnt lgkmcnt(5)\n\t" "v_cmp_eq_u32 vcc, %[rt], v218\n\t" \
    "v_smfmac_f32_16x16x64_bf16 v[224:227], v[240:243], a[96:103], %[ix]\n\t" "s_cbranch_vccnz 33f\n\t" "ds_read_b128 v[168:171], %[l0] offset:18432\n\t" \
    "v_smfmac_f32_16x16x64_bf16 v[228:231], v[240:243], a[104:111], %[ix]\n\t" "ds_read_b128 v[172:175], %[l0] offset:19456\n\t" \
    "ds_read_b128 v[196:199], %[l1] offset:2048\n\t" "v_smfmac_f32_16x16x64_bf16 v[232:235], v[240:243], v[80:87], %[ix]\n\t" \
    "buffer_load_dwordx4 v[16:19], %[hq], %[rs], %[qb] offen offset:0 sc1\n\t" "v_smfmac_f32_16x16x64_bf16 v[236:239], v[240:243], v[176:183], %[ix]\n\t" \
    "ds_read_b128 v[176:179], %[l0] offset:20480\n\t" "ds_read_b128 v[180:183], %[l0] offset:21504\n\t" \
    "v_smfmac_f32_16x16x64_bf16 v[224:227], v[244:247], a[112:119], %[ix]\n\t" "ds_read_b128 v[240:243], %[l1] offset:6144\n\t" \
    "v_smfmac_f32_16x16x64_bf16 v[228:231], v[244:247], a[120:127], %[ix]\n\t" "v_smfmac_f32_16x16x64_bf16 v[232:235], v[244:247], v[88:95], %[ix]\n\t" \
    "buffer_load_dwordx4 v[20:23], %[hq], %[rs], %[qb] offen offset:2048 sc1\n\t" "v_smfmac_f32_16x16x64_bf16 v[236:239], v[244:247], v[184:191], %[ix]\n\t" \
    "ds_read_b128 v[184:187], %[l0] offset:22528\n\t" "ds_read_b128 v[188:191], %[l0] offset:23552\n\t" "s_waitcnt lgkmcnt(10)\n\t" \
    "v_smfmac_f32_16x16x64_bf16 v[224:227], v[248:251], a[128:135], %[ix]\n\t" "ds_read_b128 v[244:247], %[l1] offset:15360\n\t" \
    "v_smfmac_f32_16x16x64_bf16 v[228:231], v[248:251], a[136:143], %[ix]\n\t" "v_smfmac_f32_16x16x64_bf16 v[232:235], v[248:251], v[96:103], %[ix]\n\t" \
    "buffer_load_dwordx4 v[24:27], %[hq], %[rs], s61 offen offset:0 sc1\n\t" "v_smfmac_f32_16x16x64_bf16 v[236:239], v[248:251], v[160:167], %[ix]\n\t" \
    "ds_read_b128 v[160:163], %[l0] offset:24576\n\t" "ds_read_b128 v[164:167], %[l0] offset:25600\n\t" "s_waitcnt lgkmcnt(12)\n\t" \
    "v_smfmac_f32_16x16x64_bf16 v[224:227], v[252:255], a[144:151], %[ix]\n\t" "ds_read_b128 v[248:251], %[l1] offset:3072\n\t" \
    "v_smfmac_f32_16x16x64_bf16 v[228:231], v[252:255], a[152:159], %[ix]\n\t" "v_smfmac_f32_16x16x64_bf16 v[232:235], v[252:255], v[104:111], %[ix]\n\t" \
    "buffer_load_dwordx4 v[28:31], %[hq], %[rs], s61 offen offset:2048 sc1\n\t" "s_waitcnt lgkmcnt(10)\n\t" \
    "v_smfmac_f32_16x16x64_bf16 v[236:239], v[252:255], v[168:175], %[ix]\n\t" "ds_read_b128 v[168:171], %[l0] offset:26624\n\t" \
    "ds_read_b128 v[172:175], %[l0] offset:27648\n\t" "v_smfmac_f32_16x16x64_bf16 v[224:227], v[192:195], a[160:167], %[ix]\n\t" \
    "ds_read_b128 v[252:255], %[l1] offset:7168\n\t" "v_smfmac_f32_16x16x64_bf16 v[228:231], v[192:195], a[168:175], %[ix]\n\t" "s_waitcnt lgkmcnt(12)\n\t" \
    "v_smfmac_f32_16x16x64_bf16 v[224:227], v[196:199], a[176:183], %[ix]\n\t" "v_smfmac_f32_16x16x64_bf16 v[228:231], v[196:199], a[184:191], %[ix]\n\t" \
    "s_waitcnt lgkmcnt(9)\n\t" "v_smfmac_f32_16x16x64_bf16 v[224:227], v[240:243], a[192:199], %[ix]\n\t" \
    "v_smfmac_f32_16x16x64_bf16 v[228:231], v[240:243], a[200:207], %[ix]\n\t" "s_waitcnt lgkmcnt(6)\n\t" \
    "v_smfmac_f32_16x16x64_bf16 v[224:227], v[244:247], a[208:215], %[ix]\n\t" "v_smfmac_f32_16x16x64_bf16 v[228:231], v[244:247], a[216:223], %[ix]\n\t" \
    "s_waitcnt vmcnt(4)\n\t" "s_waitcnt lgkmcnt(3)\n\t" "v_smfmac_f32_16x16x64_bf16 v[224:227], v[248:251], a[224:231], %[ix]\n\t" \
    "v_smfmac_f32_16x16x64_bf16 v[228:231], v[248:251], a[232:239], %[ix]\n\t" "s_waitcnt lgkmcnt(0)\n\t" \
    "v_smfmac_f32_16x16x64_bf16 v[224:227], v[252:255], a[240:247], %[ix]\n\t" "s_nop 1\n\t" "v_smfmac_f32_16x16x64_bf16 v[228:231], v[252:255], a[248:255], %[ix]\n\t" \
    XT(90, 91) "s_nop 1\n\t" "v_smfmac_f32_16x16x64_bf16 v[232:235], v[192:195], v[112:119], %[ix]\n\t" "s_nop 1\n\t" \
    "v_smfmac_f32_16x16x64_bf16 v[236:239], v[192:195], v[176:183], %[ix]\n\t" "ds_read_b128 v[176:179], %[l0] offset:28672\n\t" "v_add_f32 v224, v224, v225\n\t" \
    "v_add_f32 v226, v226, v227\n\t" "v_smfmac_f32_16x16x64_bf16 v[232:235], v[196:199], v[120:127], %[ix]\n\t" "ds_read_b128 v[180:183], %[l0] offset:29696\n\t" \
    "v_add_f32 v228, v228, v229\n\t" "v_add_f32 v230, v230, v231\n\t" "v_smfmac_f32_16x16x64_bf16 v[236:239], v[196:199], v[184:191], %[ix]\n\t" \
    "ds_read_b128 v[184:187], %[l0] offset:30720\n\t" "v_cvt_pk_bf16_f32 v218, v224, v228\n\t" "v_cvt_pk_bf16_f32 v219, v226, v230\n\t" \
    "v_smfmac_f32_16x16x64_bf16 v[232:235], v[240:243], v[128:135], %[ix]\n\t" "ds_write2_b32 %[qw], v218, v219 offset1:8\n\t" \
    "ds_read_b128 v[188:191], %[l0] offset:31744\n\t" "v_smfmac_f32_16x16x64_bf16 v[236:239], v[240:243], v[160:167], %[ix]\n\t" \
    "v_smfmac_f32_16x16x64_bf16 v[232:235], v[244:247], v[136:143], %[ix]\n\t" "v_smfmac_f32_16x16x64_bf16 v[236:239], v[244:247], v[168:175], %[ix]\n\t" \
    "s_waitcnt lgkmcnt(1)\n\t" "s_barrier\n\t" "v_smfmac_f32_16x16x64_bf16 v[232:235], v[248:251], v[144:151], %[ix]\n\t" \
    "v_smfmac_f32_16x16x64_bf16 v[236:239], v[248:251], v[176:183], %[ix]\n\t" "v_smfmac_f32_16x16x64_bf16 v[232:235], v[252:255], v[152:159], %[ix]\n\t" \
    "s_waitcnt lgkmcnt(0)\n\t" "v_smfmac_f32_16x16x64_bf16 v[236:239], v[252:255], v[184:191], %[ix]\n\t" XT(92, 93) "s_nop 6\n\t" "v_add_f32 v232, v232, v233\n\t" \
    "v_add_f32 v234, v234, v235\n\t" "v_add_f32 v236, v236, v237\n\t" "v_add_f32 v238, v238, v239\n\t" "v_cvt_pk_bf16_f32 v232, v232, v236\n\t" \
    "v_cvt_pk_bf16_f32 v233, v234, v238\n\t" "ds_write2_b32 %[kw], v232, v233 offset1:68\n\t" "s_waitcnt lgkmcnt(0)\n\t" "s_barrier\n\t" XT(94, 95) \
    "s_cmp_ge_u32 %[sq], %[sn]\n\t" "s_cbranch_scc1 377f\n\t" "375:\n\t" "global_load_dword v240, v210, %[fq] sc1\n\t" "s_waitcnt vmcnt(0)\n\t" \
    "v_readfirstlane_b32 %[sq], v240\n\t" "s_nop 3\n\t" "s_cmp_ge_u32 %[sq], %[sn]\n\t" "s_cbranch_scc1 376f\n\t" "s_cmp_eq_u32 %[bd], 0\n\t" "s_cbranch_scc1 376f\n\t" \
    "s_sub_u32 %[bd], %[bd], 1\n\t" "s_sleep 2\n\t" "s_branch 375b\n\t" "376:\n\t" "buffer_load_dwordx4 v[16:19], %[hq], %[rs], %[qb] offen offset:0 sc1\n\t" \
    "buffer_load_dwordx4 v[20:23], %[hq], %[rs], %[qb] offen offset:2048 sc1\n\t" "buffer_load_dwordx4 v[24:27], %[hq], %[rs], s61 offen offset:0 sc1\n\t" \
    "buffer_load_dwordx4 v[28:31], %[hq], %[rs], s61 offen offset:2048 sc1\n\t" "377:\n\t" "s_nop 3\n\t" "ds_read_b128 v[160:163], %[kx] offset:0\n\t" \
    "ds_read_b128 v[164:167], %[kx] offset:2048\n\t" "ds_read_b128 v[168:171], %[kx] offset:4096\n\t" "ds_read_b128 v[172:175], %[kx] offset:6144\n\t" \
    "ds_read_b128 v[176:179], %[kx] offset:256\n\t" "ds_read_b128 v[180:183], %[kx] offset:2304\n\t" "ds_read_b128 v[184:187], %[kx] offset:4352\n\t" \
    "ds_read_b128 v[188:191], %[kx] offset:6400\n\t" "ds_read_b128 v[224:227], %[km] offset:0\n\t" "ds_read_b128 v[228:231], %[km] offset:64\n\t" \
    "ds_read_b128 v[232:235], %[km] offset:128\n\t" "ds_read_b128 v[236:239], %[km] offset:192\n\t" "s_waitcnt vmcnt(0)\n\t" "global_load_dword v240, v210, %[fq] sc1\n\t" \
    "s_waitcnt lgkmcnt(11)\n\t" "v_mfma_f32_16x16x32_bf16 v[248:251], v[160:163], v[16:19], 0\n\t" "s_waitcnt lgkmcnt(10)\n\t" \
    "v_mfma_f32_16x16x32_bf16 v[248:251], v[164:167], v[20:23], v[248:251]\n\t" "s_waitcnt lgkmcnt(9)\n\t" \
    "v_mfma_f32_16x16x32_bf16 v[248:251], v[168:171], v[24:27], v[248:251]\n\t" "s_waitcnt lgkmcnt(8)\n\t" \
    "v_mfma_f32_16x16x32_bf16 v[248:251], v[172:175], v[28:31], v[248:251]\n\t" "s_waitcnt lgkmcnt(7)\n\t" \
    "v_mfma_f32_16x16x32_bf16 v[252:255], v[176:179], v[16:19], v[200:203]\n\t" "s_waitcnt lgkmcnt(6)\n\t" \
    "v_mfma_f32_16x16x32_bf16 v[252:255], v[180:183], v[20:23], v[252:255]\n\t" "s_waitcnt lgkmcnt(5)\n\t" \
    "v_mfma_f32_16x16x32_bf16 v[252:255], v[184:187], v[24:27], v[252:255]\n\t" "s_waitcnt lgkmcnt(4)\n\t" \
    "v_mfma_f32_16x16x32_bf16 v[252:255], v[188:191], v[28:31], v[252:255]\n\t" "s_waitcnt lgkmcnt(3)\n\t" \
    "v_mfma_f32_16x16x32_bf16 v[244:247], v[224:227], v[16:19], v[204:207]\n\t" "s_waitcnt lgkmcnt(2)\n\t" \
    "v_mfma_f32_16x16x32_bf16 v[244:247], v[228:231], v[20:23], v[244:247]\n\t" "s_waitcnt lgkmcnt(1)\n\t" \
    "v_mfma_f32_16x16x32_bf16 v[244:247], v[232:235], v[24:27], v[244:247]\n\t" "s_waitcnt lgkmcnt(0)\n\t" \
    "v_mfma_f32_16x16x32_bf16 v[244:247], v[236:239], v[28:31], v[244:247]\n\t" "s_nop 3\n\t" "v_max3_f32 v242, v248, v249, v250\n\t" "v_max3_f32 v242, v242, v251, v252\n\t" \
    "v_max3_f32 v242, v242, v253, v254\n\t" "v_max_f32 v242, v242, v255\n\t" "v_max3_f32 v242, v242, v244, v245\n\t" "v_max3_f32 v242, v242, v246, v247\n\t" \
    "v_mov_b32 v243, v242\n\t" "s_nop 1\n\t" "v_permlane32_swap_b32 v242, v243\n\t" "v_max_f32 v242, v242, v243\n\t" "v_mov_b32 v243, v242\n\t" "s_nop 1\n\t" \
    "v_permlane16_swap_b32 v242, v243\n\t" "v_max_f32 v242, v242, v243\n\t" "v_pk_add_f32 v[244:245], v[244:245], v[242:243] op_sel_hi:[1,0] neg_lo:[0,1] neg_hi:[0,1]\n\t" \
    "v_pk_add_f32 v[246:247], v[246:247], v[242:243] op_sel_hi:[1,0] neg_lo:[0,1] neg_hi:[0,1]\n\t" \
    "v_pk_add_f32 v[248:249], v[248:249], v[242:243] op_sel_hi:[1,0] neg_lo:[0,1] neg_hi:[0,1]\n\t" \
    "v_pk_add_f32 v[250:251], v[250:251], v[242:243] op_sel_hi:[1,0] neg_lo:[0,1] neg_hi:[0,1]\n\t" \
    "v_pk_add_f32 v[252:253], v[252:253], v[242:243] op_sel_hi:[1,0] neg_lo:[0,1] neg_hi:[0,1]\n\t" \
    "v_pk_add_f32 v[254:255], v[254:255], v[242:243] op_sel_hi:[1,0] neg_lo:[0,1] neg_hi:[0,1]\n\t" "v_pk_mul_f32 v[244:245], v[244:245], s[66:67]\n\t" \
    "v_pk_mul_f32 v[246:247], v[246:247], s[66:67]\n\t" "v_pk_mul_f32 v[248:249], v[248:249], s[66:67]\n\t" "v_pk_mul_f32 v[250:251], v[250:251], s[66:67]\n\t" \
    "v_pk_mul_f32 v[252:253], v[252:253], s[66:67]\n\t" "v_pk_mul_f32 v[254:255], v[254:255], s[66:67]\n\t" "v_exp_f32 v244, v244\n\t" "v_exp_f32 v245, v245\n\t" \
    "v_exp_f32 v246, v246\n\t" "v_exp_f32 v247, v247\n\t" "v_exp_f32 v248, v248\n\t" "v_exp_f32 v249, v249\n\t" "v_exp_f32 v250, v250\n\t" "v_exp_f32 v251, v251\n\t" \
    "v_exp_f32 v252, v252\n\t" "v_exp_f32 v253, v253\n\t" "v_exp_f32 v254, v254\n\t" "v_exp_f32 v255, v255\n\t" "v_pk_add_f32 v[242:243], v[244:245], v[246:247]\n\t" \
    "v_pk_add_f32 v[242:243], v[242:243], v[248:249]\n\t" "v_pk_add_f32 v[242:243], v[242:243], v[250:251]\n\t" "v_pk_add_f32 v[242:243], v[242:243], v[252:253]\n\t" \
    "v_pk_add_f32 v[242:243], v[242:243], v[254:255]\n\t" "v_add_f32 v242, v242, v243\n\t" "v_mov_b32 v243, v242\n\t" "s_nop 1\n\t" "v_permlane32_swap_b32 v242, v243\n\t" \
    "v_add_f32 v242, v242, v243\n\t" "v_mov_b32 v243, v242\n\t" "s_nop 1\n\t" "v_permlane16_swap_b32 v242, v243\n\t" "v_add_f32 v242, v242, v243\n\t" \
    "v_rcp_f32 v242, v242\n\t" "s_nop 0\n\t" "v_pk_mul_f32 v[244:245], v[244:245], v[242:243] op_sel_hi:[1,0]\n\t" \
    "v_pk_mul_f32 v[246:247], v[246:247], v[242:243] op_sel_hi:[1,0]\n\t" "v_pk_mul_f32 v[248:249], v[248:249], v[242:243] op_sel_hi:[1,0]\n\t" \
    "v_pk_mul_f32 v[250:251], v[250:251], v[242:243] op_sel_hi:[1,0]\n\t" "v_pk_mul_f32 v[252:253], v[252:253], v[242:243] op_sel_hi:[1,0]\n\t" \
    "v_pk_mul_f32 v[254:255], v[254:255], v[242:243] op_sel_hi:[1,0]\n\t" "v_cvt_pk_bf16_f32 v162, v244, v245\n\t" "v_cvt_pk_bf16_f32 v163, v246, v247\n\t" \
    "v_cvt_pk_bf16_f32 v164, v248, v249\n\t" "v_cvt_pk_bf16_f32 v165, v250, v251\n\t" "v_cvt_pk_bf16_f32 v166, v252, v253\n\t" "v_cvt_pk_bf16_f32 v167, v254, v255\n\t" \
    "buffer_store_dwordx4 v[164:167], %[px], %[rs], %[tb] offen sc1\n\t" "buffer_store_dwordx2 v[162:163], %[pm], %[rs], %[tb] offen sc1\n\t" "s_waitcnt vmcnt(2)\n\t" \
    "v_readfirstlane_b32 s62, v240\n\t" "s_nop 3\n\t" "s_max_u32 %[sq], %[sq], s62\n\t" "s_barrier\n\t" XT(96, 97) "s_branch 99f\n\t" "31:\n\t" \
    "ds_read_b128 v[220:223], %[l1] offset:8192\n\t" "s_waitcnt lgkmcnt(0)\n\t" "v_max3_u32 v218, v220, v221, v222\n\t" "v_max_u32 v218, v218, v223\n\t" \
    "ds_read_b128 v[220:223], %[l1] offset:9216\n\t" "s_waitcnt lgkmcnt(0)\n\t" "v_max3_u32 v218, v218, v220, v221\n\t" "v_max3_u32 v218, v218, v222, v223\n\t" \
    "ds_read_b128 v[220:223], %[l1] offset:10240\n\t" "s_waitcnt lgkmcnt(0)\n\t" "v_max3_u32 v218, v218, v220, v221\n\t" "v_max3_u32 v218, v218, v222, v223\n\t" \
    "ds_read_b128 v[220:223], %[l1] offset:11264\n\t" "s_waitcnt lgkmcnt(0)\n\t" "v_max3_u32 v218, v218, v220, v221\n\t" "v_max3_u32 v218, v218, v222, v223\n\t" \
    "v_cmp_eq_u32 vcc, -1, v218\n\t" "s_cbranch_vccz 32b\n\t" "s_cmp_eq_u32 %[rt], 1\n\t" "s_cbranch_scc1 32b\n\t" "v_mov_b32 v218, %[rt]\n\t" "ds_write_b32 v219, v218\n\t" \
    "s_waitcnt lgkmcnt(0)\n\t" "s_branch 32b\n\t" "33:\n\t" "s_sub_u32 %[rt], %[rt], 1\n\t" "s_waitcnt lgkmcnt(0)\n\t" XCNT(100) "34:\n\t" \
    "buffer_load_dwordx4 v[240:243], %[mo], %[rs], %[sb] offen sc1\n\t" "buffer_load_dwordx4 v[244:247], v216, %[rs], %[sb] offen sc1\n\t" \
    "buffer_load_dwordx4 v[248:251], %[mo], %[rs], s60 offen sc1\n\t" "buffer_load_dwordx4 v[252:255], v216, %[rs], s60 offen sc1\n\t" "s_waitcnt vmcnt(0)\n\t" \
    "v_max_u32 v218, v240, v241\n\t" "v_max3_u32 v218, v218, v242, v243\n\t" "v_max3_u32 v218, v218, v244, v245\n\t" "v_max3_u32 v218, v218, v246, v247\n\t" \
    "v_max3_u32 v218, v218, v248, v249\n\t" "v_max3_u32 v218, v218, v250, v251\n\t" "v_max3_u32 v218, v218, v252, v253\n\t" "v_max3_u32 v218, v218, v254, v255\n\t" \
    "v_cmp_eq_u32 vcc, -1, v218\n\t" "s_cbranch_vccz 35f\n\t" "s_cmp_eq_u32 %[bd], 0\n\t" "s_cbranch_scc1 35f\n\t" "s_sub_u32 %[bd], %[bd], 1\n\t" XCNT(101) \
    "s_branch 34b\n\t" "35:\n\t" "ds_read_b128 v[160:163], %[l0] offset:0\n\t" "ds_read_b128 v[164:167], %[l0] offset:1024\n\t" \
    "ds_read_b128 v[168:171], %[l0] offset:2048\n\t" "ds_read_b128 v[172:175], %[l0] offset:3072\n\t" "ds_read_b128 v[176:179], %[l0] offset:4096\n\t" \
    "ds_read_b128 v[180:183], %[l0] offset:5120\n\t" "ds_read_b128 v[184:187], %[l0] offset:6144\n\t" "ds_read_b128 v[188:191], %[l0] offset:7168\n\t" \
    "v_mov_b64 v[224:225], 0\n\t" "v_mov_b64 v[226:227], 0\n\t" "v_mov_b64 v[228:229], 0\n\t" "v_mov_b64 v[230:231], 0\n\t" "v_mov_b64 v[232:233], 0\n\t" \
    "v_mov_b64 v[234:235], 0\n\t" "v_mov_b64 v[236:237], 0\n\t" "v_mov_b64 v[238:239], 0\n\t" "s_barrier\n\t" "s_branch 30b\n\t" "99:\n\t"
#define QK_STEP3 \
    ".p2align 6\n\t" "v_xor_b32 v216, 0x400, %[mo]\n\t" "s_xor_b32 s60, %[sb], 0x800\n\t" "s_mov_b32 s67, %[pb]\n\t" XT(80, 81) "s_getpc_b64 s[62:63]\n\t" "48:\n\t" \
    "s_memtime s[64:65]\n\t" "s_waitcnt lgkmcnt(0)\n\t" "s_sub_u32 s61, %[tg], s64\n\t" "s_cmp_gt_i32 s61, 124\n\t" "s_cbranch_scc1 48b\n\t" "s_max_i32 s61, s61, 0\n\t" \
    "s_and_b32 s61, s61, 124\n\t" "s_sub_u32 s61, 49f-48b, s61\n\t" "s_add_u32 s62, s62, s61\n\t" "s_addc_u32 s63, s63, 0\n\t" "s_setpc_b64 s[62:63]\n\t" ".rept 31\n\t" \
    "s_nop 0\n\t" ".endr\n\t" "49:\n\t" XT(98, 99) "s_barrier\n\t" XT(82, 83) "buffer_load_dwordx4 v[240:243], %[mo], %[rs], %[sb] offen sc1\n\t" \
    "v_mov_b64 v[224:225], 0\n\t" "v_mov_b64 v[226:227], 0\n\t" "v_mov_b64 v[228:229], 0\n\t" "v_mov_b64 v[230:231], 0\n\t" "v_mov_b64 v[232:233], 0\n\t" \
    "v_mov_b64 v[234:235], 0\n\t" "v_mov_b64 v[236:237], 0\n\t" "v_mov_b64 v[238:239], 0\n\t" "buffer_load_dwordx4 v[244:247], v216, %[rs], %[sb] offen sc1\n\t" \
    "ds_read_b128 v[160:163], %[l0] offset:0\n\t" "ds_read_b128 v[164:167], %[l0] offset:1024\n\t" "ds_read_b128 v[168:171], %[l0] offset:2048\n\t" \
    "ds_read_b128 v[172:175], %[l0] offset:3072\n\t" "s_nop 3\n\t" "buffer_load_dwordx4 v[248:251], %[mo], %[rs], s60 offen sc1\n\t" \
    "ds_read_b128 v[176:179], %[l0] offset:4096\n\t" "ds_read_b128 v[180:183], %[l0] offset:5120\n\t" "ds_read_b128 v[184:187], %[l0] offset:6144\n\t" \
    "ds_read_b128 v[188:191], %[l0] offset:7168\n\t" "s_nop 3\n\t" "buffer_load_dwordx4 v[252:255], v216, %[rs], s60 offen sc1\n\t" "v_mov_b32 v219, 160512\n\t" \
    "v_mov_b32 v220, -1\n\t" "buffer_store_dword v220, %[hq], %[rs], %[pr] offen sc1\n\t" "40:\n\t" "s_waitcnt vmcnt(4)\n\t" XT(84, 85) \
    "v_smfmac_f32_16x16x64_bf16 v[224:227], v[240:243], a[0:7], %[ix]\n\t" "ds_write_b128 %[l1], v[240:243] offset:12288\n\t" \
    "v_smfmac_f32_16x16x64_bf16 v[228:231], v[240:243], a[8:15], %[ix]\n\t" "v_smfmac_f32_16x16x64_bf16 v[232:235], v[240:243], v[32:39], %[ix]\n\t" \
    "s_waitcnt lgkmcnt(7)\n\t" "v_smfmac_f32_16x16x64_bf16 v[236:239], v[240:243], v[160:167], %[ix]\n\t" "ds_read_b128 v[160:163], %[l0] offset:8192\n\t" \
    "ds_read_b128 v[164:167], %[l0] offset:9216\n\t" "s_waitcnt vmcnt(3)\n\t" "v_smfmac_f32_16x16x64_bf16 v[224:227], v[244:247], a[16:23], %[ix]\n\t" \
    "ds_write_b128 %[l1], v[244:247] offset:13312\n\t" "v_smfmac_f32_16x16x64_bf16 v[228:231], v[244:247], a[24:31], %[ix]\n\t" \
    "v_smfmac_f32_16x16x64_bf16 v[232:235], v[244:247], v[40:47], %[ix]\n\t" "s_waitcnt lgkmcnt(8)\n\t" \
    "v_smfmac_f32_16x16x64_bf16 v[236:239], v[244:247], v[168:175], %[ix]\n\t" "ds_read_b128 v[168:171], %[l0] offset:10240\n\t" \
    "ds_read_b128 v[172:175], %[l0] offset:11264\n\t" "s_waitcnt vmcnt(2)\n\t" "v_smfmac_f32_16x16x64_bf16 v[224:227], v[248:251], a[32:39], %[ix]\n\t" \
    "ds_write_b128 %[l1], v[248:251] offset:14336\n\t" "v_smfmac_f32_16x16x64_bf16 v[228:231], v[248:251], a[40:47], %[ix]\n\t" "s_waitcnt lgkmcnt(3)\n\t" "s_barrier\n\t" \
    XT(86, 87) "v_smfmac_f32_16x16x64_bf16 v[232:235], v[248:251], v[48:55], %[ix]\n\t" "ds_read_b128 v[192:195], %[l1] offset:0\n\t" \
    "ds_read_b128 v[196:199], %[l1] offset:4096\n\t" "v_smfmac_f32_16x16x64_bf16 v[236:239], v[248:251], v[176:183], %[ix]\n\t" \
    "ds_read_b128 v[176:179], %[l0] offset:12288\n\t" "ds_read_b128 v[180:183], %[l0] offset:13312\n\t" "s_waitcnt vmcnt(1)\n\t" \
    "v_smfmac_f32_16x16x64_bf16 v[224:227], v[252:255], a[48:55], %[ix]\n\t" "ds_write_b128 %[l1], v[252:255] offset:15360\n\t" \
    "v_smfmac_f32_16x16x64_bf16 v[228:231], v[252:255], a[56:63], %[ix]\n\t" "ds_read_b128 v[240:243], %[l1] offset:8192\n\t" "ds_read_b128 v[244:247], %[l1] offset:1024\n\t" \
    "v_smfmac_f32_16x16x64_bf16 v[232:235], v[252:255], v[56:63], %[ix]\n\t" "s_nop 1\n\t" "v_smfmac_f32_16x16x64_bf16 v[236:239], v[252:255], v[184:191], %[ix]\n\t" \
    "ds_read_b128 v[184:187], %[l0] offset:14336\n\t" "v_cmp_u_f32 vcc, v224, v225\n\t" "v_cmp_u_f32 s[62:63], v226, v227\n\t" "s_waitcnt lgkmcnt(7)\n\t" \
    "v_smfmac_f32_16x16x64_bf16 v[224:227], v[192:195], a[64:71], %[ix]\n\t" "ds_read_b128 v[188:191], %[l0] offset:15360\n\t" \
    "v_smfmac_f32_16x16x64_bf16 v[228:231], v[192:195], a[72:79], %[ix]\n\t" "s_or_b64 vcc, vcc, s[62:63]\n\t" "s_cbranch_scc1 41f\n\t" "42:\n\t" \
    "v_smfmac_f32_16x16x64_bf16 v[232:235], v[192:195], v[64:71], %[ix]\n\t" "s_waitcnt lgkmcnt(4)\n\t" "s_barrier\n\t" XT(88, 89) "ds_read_b32 v218, v219\n\t" \
    "v_smfmac_f32_16x16x64_bf16 v[236:239], v[192:195], v[160:167], %[ix]\n\t" "s_memtime s[64:65]\n\t" "ds_read_b128 v[160:163], %[l0] offset:16384\n\t" \
    "v_smfmac_f32_16x16x64_bf16 v[224:227], v[196:199], a[80:87], %[ix]\n\t" "ds_read_b128 v[164:167], %[l0] offset:17408\n\t" \
    "ds_read_b128 v[248:251], %[l1] offset:5120\n\t" "v_smfmac_f32_16x16x64_bf16 v[228:231], v[196:199], a[88:95], %[ix]\n\t" "ds_read_b128 v[252:255], %[l1] offset:9216\n\t" \
    "ds_read_b128 v[192:195], %[l1] offset:2048\n\t" "v_smfmac_f32_16x16x64_bf16 v[232:235], v[196:199], v[72:79], %[ix]\n\t" "s_sub_u32 s62, s64, %[ca]\n\t" \
    "s_mul_hi_u32 s63, s62, %[k1]\n\t" "s_mul_i32 s62, s62, %[k1]\n\t" "v_smfmac_f32_16x16x64_bf16 v[236:239], v[196:199], v[168:175], %[ix]\n\t" "s_waitcnt lgkmcnt(5)\n\t" \
    "v_cmp_eq_u32 vcc, %[rt], v218\n\t" "s_lshr_b64 s[62:63], s[62:63], 16\n\t" "s_add_u32 s62, s62, %[ga]\n\t" "s_bitset0_b32 s62, 0\n\t" \
    "v_smfmac_f32_16x16x64_bf16 v[224:227], v[240:243], a[96:103], %[ix]\n\t" "s_cbranch_vccnz 43f\n\t" "ds_read_b128 v[168:171], %[l0] offset:18432\n\t" \
    "v_smfmac_f32_16x16x64_bf16 v[228:231], v[240:243], a[104:111], %[ix]\n\t" "ds_read_b128 v[172:175], %[l0] offset:19456\n\t" "v_mov_b32 v217, s62\n\t" \
    "buffer_store_dword v217, %[hq], %[rs], s67 offen sc1\n\t" "v_smfmac_f32_16x16x64_bf16 v[232:235], v[240:243], v[80:87], %[ix]\n\t" \
    "ds_read_b128 v[196:199], %[l1] offset:6144\n\t" "v_smfmac_f32_16x16x64_bf16 v[236:239], v[240:243], v[176:183], %[ix]\n\t" \
    "ds_read_b128 v[176:179], %[l0] offset:20480\n\t" "ds_read_b128 v[180:183], %[l0] offset:21504\n\t" \
    "v_smfmac_f32_16x16x64_bf16 v[224:227], v[244:247], a[112:119], %[ix]\n\t" "ds_read_b128 v[240:243], %[l1] offset:10240\n\t" \
    "v_smfmac_f32_16x16x64_bf16 v[228:231], v[244:247], a[120:127], %[ix]\n\t" "v_smfmac_f32_16x16x64_bf16 v[232:235], v[244:247], v[88:95], %[ix]\n\t" \
    "v_smfmac_f32_16x16x64_bf16 v[236:239], v[244:247], v[184:191], %[ix]\n\t" "ds_read_b128 v[184:187], %[l0] offset:22528\n\t" \
    "ds_read_b128 v[188:191], %[l0] offset:23552\n\t" "s_waitcnt lgkmcnt(10)\n\t" "v_smfmac_f32_16x16x64_bf16 v[224:227], v[248:251], a[128:135], %[ix]\n\t" \
    "ds_read_b128 v[244:247], %[l1] offset:3072\n\t" "v_smfmac_f32_16x16x64_bf16 v[228:231], v[248:251], a[136:143], %[ix]\n\t" \
    "v_smfmac_f32_16x16x64_bf16 v[232:235], v[248:251], v[96:103], %[ix]\n\t" "v_smfmac_f32_16x16x64_bf16 v[236:239], v[248:251], v[160:167], %[ix]\n\t" \
    "ds_read_b128 v[160:163], %[l0] offset:24576\n\t" "ds_read_b128 v[164:167], %[l0] offset:25600\n\t" "s_waitcnt lgkmcnt(12)\n\t" \
    "v_smfmac_f32_16x16x64_bf16 v[224:227], v[252:255], a[144:151], %[ix]\n\t" "ds_read_b128 v[248:251], %[l1] offset:7168\n\t" \
    "v_smfmac_f32_16x16x64_bf16 v[228:231], v[252:255], a[152:159], %[ix]\n\t" "v_smfmac_f32_16x16x64_bf16 v[232:235], v[252:255], v[104:111], %[ix]\n\t" \
    "s_waitcnt lgkmcnt(10)\n\t" "v_smfmac_f32_16x16x64_bf16 v[236:239], v[252:255], v[168:175], %[ix]\n\t" "ds_read_b128 v[168:171], %[l0] offset:26624\n\t" \
    "ds_read_b128 v[172:175], %[l0] offset:27648\n\t" "v_smfmac_f32_16x16x64_bf16 v[224:227], v[192:195], a[160:167], %[ix]\n\t" \
    "ds_read_b128 v[252:255], %[l1] offset:11264\n\t" "v_smfmac_f32_16x16x64_bf16 v[228:231], v[192:195], a[168:175], %[ix]\n\t" "s_waitcnt lgkmcnt(12)\n\t" \
    "v_smfmac_f32_16x16x64_bf16 v[224:227], v[196:199], a[176:183], %[ix]\n\t" "v_smfmac_f32_16x16x64_bf16 v[228:231], v[196:199], a[184:191], %[ix]\n\t" \
    "s_waitcnt lgkmcnt(9)\n\t" "v_smfmac_f32_16x16x64_bf16 v[224:227], v[240:243], a[192:199], %[ix]\n\t" \
    "v_smfmac_f32_16x16x64_bf16 v[228:231], v[240:243], a[200:207], %[ix]\n\t" "s_waitcnt lgkmcnt(6)\n\t" \
    "v_smfmac_f32_16x16x64_bf16 v[224:227], v[244:247], a[208:215], %[ix]\n\t" "v_smfmac_f32_16x16x64_bf16 v[228:231], v[244:247], a[216:223], %[ix]\n\t" \
    "s_waitcnt lgkmcnt(3)\n\t" "v_smfmac_f32_16x16x64_bf16 v[224:227], v[248:251], a[224:231], %[ix]\n\t" \
    "v_smfmac_f32_16x16x64_bf16 v[228:231], v[248:251], a[232:239], %[ix]\n\t" "s_waitcnt lgkmcnt(0)\n\t" \
    "v_smfmac_f32_16x16x64_bf16 v[224:227], v[252:255], a[240:247], %[ix]\n\t" "s_nop 1\n\t" "v_smfmac_f32_16x16x64_bf16 v[228:231], v[252:255], a[248:255], %[ix]\n\t" \
    XT(90, 91) "s_nop 1\n\t" "v_smfmac_f32_16x16x64_bf16 v[232:235], v[192:195], v[112:119], %[ix]\n\t" "s_nop 1\n\t" \
    "v_smfmac_f32_16x16x64_bf16 v[236:239], v[192:195], v[176:183], %[ix]\n\t" "ds_read_b128 v[176:179], %[l0] offset:28672\n\t" "v_add_f32 v224, v224, v225\n\t" \
    "v_add_f32 v226, v226, v227\n\t" "v_smfmac_f32_16x16x64_bf16 v[232:235], v[196:199], v[120:127], %[ix]\n\t" "ds_read_b128 v[180:183], %[l0] offset:29696\n\t" \
    "v_add_f32 v228, v228, v229\n\t" "v_add_f32 v230, v230, v231\n\t" "v_smfmac_f32_16x16x64_bf16 v[236:239], v[196:199], v[184:191], %[ix]\n\t" \
    "ds_read_b128 v[184:187], %[l0] offset:30720\n\t" "v_cvt_pk_bf16_f32 v218, v224, v228\n\t" "v_cvt_pk_bf16_f32 v219, v226, v230\n\t" \
    "v_smfmac_f32_16x16x64_bf16 v[232:235], v[240:243], v[128:135], %[ix]\n\t" "ds_write2_b32 %[qw], v218, v219 offset1:8\n\t" \
    "ds_read_b128 v[188:191], %[l0] offset:31744\n\t" "v_smfmac_f32_16x16x64_bf16 v[236:239], v[240:243], v[160:167], %[ix]\n\t" \
    "v_smfmac_f32_16x16x64_bf16 v[232:235], v[244:247], v[136:143], %[ix]\n\t" "v_smfmac_f32_16x16x64_bf16 v[236:239], v[244:247], v[168:175], %[ix]\n\t" \
    "s_waitcnt lgkmcnt(1)\n\t" "s_barrier\n\t" "v_smfmac_f32_16x16x64_bf16 v[232:235], v[248:251], v[144:151], %[ix]\n\t" \
    "v_smfmac_f32_16x16x64_bf16 v[236:239], v[248:251], v[176:183], %[ix]\n\t" "v_smfmac_f32_16x16x64_bf16 v[232:235], v[252:255], v[152:159], %[ix]\n\t" \
    "s_waitcnt lgkmcnt(0)\n\t" "v_smfmac_f32_16x16x64_bf16 v[236:239], v[252:255], v[184:191], %[ix]\n\t" XT(92, 93) "s_nop 6\n\t" "v_add_f32 v232, v232, v233\n\t" \
    "v_add_f32 v234, v234, v235\n\t" "v_add_f32 v236, v236, v237\n\t" "v_add_f32 v238, v238, v239\n\t" "v_cvt_pk_bf16_f32 v232, v232, v236\n\t" \
    "v_cvt_pk_bf16_f32 v233, v234, v238\n\t" "ds_write2_b32 %[kw], v232, v233 offset1:68\n\t" "s_waitcnt lgkmcnt(0)\n\t" "s_barrier\n\t" XT(94, 95) XT(96, 97) \
    "s_branch 99f\n\t" "41:\n\t" "ds_read_b128 v[220:223], %[l1] offset:12288\n\t" "s_waitcnt lgkmcnt(0)\n\t" "v_max3_u32 v218, v220, v221, v222\n\t" \
    "v_max_u32 v218, v218, v223\n\t" "ds_read_b128 v[220:223], %[l1] offset:13312\n\t" "s_waitcnt lgkmcnt(0)\n\t" "v_max3_u32 v218, v218, v220, v221\n\t" \
    "v_max3_u32 v218, v218, v222, v223\n\t" "ds_read_b128 v[220:223], %[l1] offset:14336\n\t" "s_waitcnt lgkmcnt(0)\n\t" "v_max3_u32 v218, v218, v220, v221\n\t" \
    "v_max3_u32 v218, v218, v222, v223\n\t" "ds_read_b128 v[220:223], %[l1] offset:15360\n\t" "s_waitcnt lgkmcnt(0)\n\t" "v_max3_u32 v218, v218, v220, v221\n\t" \
    "v_max3_u32 v218, v218, v222, v223\n\t" "v_cmp_eq_u32 vcc, -1, v218\n\t" "s_cbranch_vccz 42b\n\t" "s_cmp_eq_u32 %[rt], 1\n\t" "s_cbranch_scc1 42b\n\t" \
    "v_mov_b32 v218, %[rt]\n\t" "ds_write_b32 v219, v218\n\t" "s_waitcnt lgkmcnt(0)\n\t" "s_branch 42b\n\t" "43:\n\t" "s_sub_u32 %[rt], %[rt], 1\n\t" \
    "s_waitcnt lgkmcnt(0)\n\t" XCNT(100) "44:\n\t" "buffer_load_dwordx4 v[240:243], %[mo], %[rs], %[sb] offen sc1\n\t" \
    "buffer_load_dwordx4 v[244:247], v216, %[rs], %[sb] offen sc1\n\t" "buffer_load_dwordx4 v[248:251], %[mo], %[rs], s60 offen sc1\n\t" \
    "buffer_load_dwordx4 v[252:255], v216, %[rs], s60 offen sc1\n\t" "s_waitcnt vmcnt(0)\n\t" "v_max_u32 v218, v240, v241\n\t" "v_max3_u32 v218, v218, v242, v243\n\t" \
    "v_max3_u32 v218, v218, v244, v245\n\t" "v_max3_u32 v218, v218, v246, v247\n\t" "v_max3_u32 v218, v218, v248, v249\n\t" "v_max3_u32 v218, v218, v250, v251\n\t" \
    "v_max3_u32 v218, v218, v252, v253\n\t" "v_max3_u32 v218, v218, v254, v255\n\t" "v_cmp_eq_u32 vcc, -1, v218\n\t" "s_cbranch_vccz 45f\n\t" "s_cmp_eq_u32 %[bd], 0\n\t" \
    "s_cbranch_scc1 45f\n\t" "s_sub_u32 %[bd], %[bd], 1\n\t" XCNT(101) "s_branch 44b\n\t" "45:\n\t" "ds_read_b128 v[160:163], %[l0] offset:0\n\t" \
    "ds_read_b128 v[164:167], %[l0] offset:1024\n\t" "ds_read_b128 v[168:171], %[l0] offset:2048\n\t" "ds_read_b128 v[172:175], %[l0] offset:3072\n\t" \
    "ds_read_b128 v[176:179], %[l0] offset:4096\n\t" "ds_read_b128 v[180:183], %[l0] offset:5120\n\t" "ds_read_b128 v[184:187], %[l0] offset:6144\n\t" \
    "ds_read_b128 v[188:191], %[l0] offset:7168\n\t" "v_mov_b64 v[224:225], 0\n\t" "v_mov_b64 v[226:227], 0\n\t" "v_mov_b64 v[228:229], 0\n\t" "v_mov_b64 v[230:231], 0\n\t" \
    "v_mov_b64 v[232:233], 0\n\t" "v_mov_b64 v[234:235], 0\n\t" "v_mov_b64 v[236:237], 0\n\t" "v_mov_b64 v[238:239], 0\n\t" "s_barrier\n\t" "s_memtime s[64:65]\n\t" \
    "s_waitcnt lgkmcnt(0)\n\t" "s_add_u32 s64, s64, %[dr]\n\t" "s_sub_u32 s62, s64, %[ca]\n\t" "s_mul_hi_u32 s63, s62, %[k1]\n\t" "s_mul_i32 s62, s62, %[k1]\n\t" \
    "s_lshr_b64 s[62:63], s[62:63], 16\n\t" "s_add_u32 s62, s62, %[ga]\n\t" "s_bitset0_b32 s62, 0\n\t" "v_mov_b32 v218, s62\n\t" \
    "buffer_store_dword v218, %[hq], %[rs], s67 offen sc1\n\t" "s_mov_b32 s67, 0x7f000000\n\t" "s_branch 40b\n\t" "99:\n\t" "mk_tail:\n\t"
#define VO_STEP \
    ".p2align 6\n\t" "v_xor_b32 v224, 0x400, %[mo]\n\t" "s_xor_b32 s60, %[sb], 0x800\n\t" "s_mov_b32 s67, %[pb]\n\t" "s_cmp_eq_u32 %[w], 1\n\t" "s_cbranch_scc1 91f\n\t" \
    "s_cmp_eq_u32 %[w], 2\n\t" "s_cbranch_scc1 92f\n\t" "s_cmp_eq_u32 %[w], 3\n\t" "s_cbranch_scc1 93f\n\t" XT(80, 81) "s_getpc_b64 s[62:63]\n\t" "18:\n\t" \
    "s_memtime s[64:65]\n\t" "s_waitcnt lgkmcnt(0)\n\t" "s_sub_u32 s61, %[tg], s64\n\t" "s_cmp_gt_i32 s61, 124\n\t" "s_cbranch_scc1 18b\n\t" "s_max_i32 s61, s61, 0\n\t" \
    "s_and_b32 s61, s61, 124\n\t" "s_sub_u32 s61, 19f-18b, s61\n\t" "s_add_u32 s62, s62, s61\n\t" "s_addc_u32 s63, s63, 0\n\t" "s_setpc_b64 s[62:63]\n\t" ".rept 31\n\t" \
    "s_nop 0\n\t" ".endr\n\t" "19:\n\t" "s_barrier\n\t" XT(82, 83) "buffer_load_dwordx4 v[240:243], %[mo], %[rs], %[sb] offen sc1\n\t" "v_mov_b64 v[232:233], 0\n\t" \
    "v_mov_b64 v[234:235], 0\n\t" "v_mov_b64 v[236:237], 0\n\t" "v_mov_b64 v[238:239], 0\n\t" "v_mov_b64 v[192:193], 0\n\t" "v_mov_b64 v[194:195], 0\n\t" \
    "v_mov_b64 v[196:197], 0\n\t" "v_mov_b64 v[198:199], 0\n\t" "buffer_load_dwordx4 v[244:247], v224, %[rs], %[sb] offen sc1\n\t" \
    "ds_read_b128 v[160:163], %[l0] offset:0\n\t" "ds_read_b128 v[164:167], %[l0] offset:1024\n\t" "ds_read_b128 v[168:171], %[l0] offset:2048\n\t" \
    "ds_read_b128 v[172:175], %[l0] offset:3072\n\t" "s_nop 3\n\t" "buffer_load_dwordx4 v[248:251], %[mo], %[rs], s60 offen sc1\n\t" \
    "ds_read_b128 v[176:179], %[l0] offset:4096\n\t" "ds_read_b128 v[180:183], %[l0] offset:5120\n\t" "ds_read_b128 v[184:187], %[l0] offset:6144\n\t" \
    "ds_read_b128 v[188:191], %[l0] offset:7168\n\t" "s_nop 3\n\t" "buffer_load_dwordx4 v[252:255], v224, %[rs], s60 offen sc1\n\t" "v_mov_b64 v[200:201], 0\n\t" \
    "v_mov_b64 v[202:203], 0\n\t" "v_mov_b64 v[204:205], 0\n\t" "v_mov_b64 v[206:207], 0\n\t" "v_mov_b64 v[208:209], 0\n\t" "v_mov_b64 v[210:211], 0\n\t" \
    "v_mov_b64 v[212:213], 0\n\t" "v_mov_b64 v[214:215], 0\n\t" "v_mov_b64 v[216:217], 0\n\t" "v_mov_b64 v[218:219], 0\n\t" "v_mov_b64 v[220:221], 0\n\t" \
    "v_mov_b64 v[222:223], 0\n\t" "v_mov_b32 v226, -1\n\t" "buffer_store_dword v226, %[hq], %[rs], %[pr] offen sc1\n\t" "v_mov_b32 v227, 160512\n\t" "10:\n\t" \
    "s_waitcnt vmcnt(4)\n\t" XT(84, 85) "v_smfmac_f32_16x16x64_bf16 v[232:235], v[240:243], a[0:7], %[ix]\n\t" "ds_write_b128 %[l1], v[240:243] offset:0\n\t" \
    "v_smfmac_f32_16x16x64_bf16 v[236:239], v[240:243], a[8:15], %[ix]\n\t" "s_waitcnt vmcnt(3)\n\t" "v_smfmac_f32_16x16x64_bf16 v[232:235], v[244:247], a[16:23], %[ix]\n\t" \
    "ds_write_b128 %[l1], v[244:247] offset:1024\n\t" "s_waitcnt lgkmcnt(8)\n\t" "v_smfmac_f32_16x16x64_bf16 v[236:239], v[244:247], v[160:167], %[ix]\n\t" \
    "s_waitcnt lgkmcnt(0)\n\t" "s_barrier\n\t" XT(86, 87) "ds_read_b128 v[16:19], %[l1] offset:4096\n\t" "ds_read_b128 v[20:23], %[l1] offset:8192\n\t" \
    "ds_read_b128 v[24:27], %[l1] offset:12288\n\t" "s_waitcnt vmcnt(2)\n\t" "v_smfmac_f32_16x16x64_bf16 v[232:235], v[248:251], a[24:31], %[ix]\n\t" \
    "ds_write_b128 %[l1], v[248:251] offset:2048\n\t" "v_smfmac_f32_16x16x64_bf16 v[236:239], v[248:251], a[32:39], %[ix]\n\t" "ds_read_b128 v[28:31], %[l1] offset:5120\n\t" \
    "ds_read_b128 v[240:243], %[l1] offset:9216\n\t" "ds_read_b128 v[244:247], %[l1] offset:13312\n\t" "s_waitcnt vmcnt(1)\n\t" \
    "v_smfmac_f32_16x16x64_bf16 v[232:235], v[252:255], a[40:47], %[ix]\n\t" "ds_write_b128 %[l1], v[252:255] offset:3072\n\t" \
    "v_smfmac_f32_16x16x64_bf16 v[236:239], v[252:255], v[168:175], %[ix]\n\t" "ds_read_b128 v[160:163], %[l0] offset:8192\n\t" \
    "ds_read_b128 v[164:167], %[l0] offset:9216\n\t" "ds_read_b128 v[168:171], %[l0] offset:10240\n\t" "ds_read_b128 v[172:175], %[l0] offset:11264\n\t" "s_nop 1\n\t" \
    "v_cmp_u_f32 vcc, v232, v233\n\t" "v_cmp_u_f32 s[62:63], v234, v235\n\t" "s_waitcnt lgkmcnt(11)\n\t" \
    "v_smfmac_f32_16x16x64_bf16 v[232:235], v[16:19], a[48:55], %[ix]\n\t" "v_smfmac_f32_16x16x64_bf16 v[236:239], v[16:19], a[56:63], %[ix]\n\t" \
    "s_or_b64 vcc, vcc, s[62:63]\n\t" "s_cbranch_scc1 11f\n\t" "12:\n\t" "s_waitcnt lgkmcnt(10)\n\t" "v_smfmac_f32_16x16x64_bf16 v[232:235], v[20:23], a[64:71], %[ix]\n\t" \
    "v_smfmac_f32_16x16x64_bf16 v[236:239], v[20:23], v[176:183], %[ix]\n\t" "s_waitcnt lgkmcnt(4)\n\t" "s_barrier\n\t" XT(88, 89) "ds_read_b32 v225, v227\n\t" \
    "v_smfmac_f32_16x16x64_bf16 v[232:235], v[24:27], a[72:79], %[ix]\n\t" "ds_read_b128 v[176:179], %[l0] offset:12288\n\t" "ds_read_b128 v[180:183], %[l0] offset:13312\n\t" \
    "v_smfmac_f32_16x16x64_bf16 v[236:239], v[24:27], a[80:87], %[ix]\n\t" "ds_read_b128 v[248:251], %[l1] offset:6144\n\t" "ds_read_b128 v[252:255], %[l1] offset:10240\n\t" \
    "v_smfmac_f32_16x16x64_bf16 v[232:235], v[28:31], a[88:95], %[ix]\n\t" "ds_read_b128 v[16:19], %[l1] offset:14336\n\t" "ds_read_b128 v[20:23], %[l1] offset:7168\n\t" \
    "v_smfmac_f32_16x16x64_bf16 v[236:239], v[28:31], v[184:191], %[ix]\n\t" "s_memtime s[64:65]\n\t" "ds_read_b128 v[24:27], %[l1] offset:11264\n\t" \
    "v_smfmac_f32_16x16x64_bf16 v[232:235], v[240:243], a[96:103], %[ix]\n\t" "ds_read_b128 v[184:187], %[l0] offset:14336\n\t" \
    "ds_read_b128 v[188:191], %[l0] offset:15360\n\t" "v_smfmac_f32_16x16x64_bf16 v[236:239], v[240:243], a[104:111], %[ix]\n\t" "s_waitcnt lgkmcnt(9)\n\t" \
    "v_cmp_eq_u32 vcc, %[rt], v225\n\t" "v_smfmac_f32_16x16x64_bf16 v[232:235], v[244:247], a[112:119], %[ix]\n\t" "s_cbranch_vccnz 13f\n\t" \
    "ds_read_b128 v[28:31], %[l1] offset:15360\n\t" "v_smfmac_f32_16x16x64_bf16 v[236:239], v[244:247], v[160:167], %[ix]\n\t" \
    "ds_read_b128 v[160:163], %[l0] offset:16384\n\t" "ds_read_b128 v[164:167], %[l0] offset:17408\n\t" "s_waitcnt lgkmcnt(9)\n\t" \
    "v_smfmac_f32_16x16x64_bf16 v[232:235], v[248:251], a[120:127], %[ix]\n\t" "s_sub_u32 s62, s64, %[ca]\n\t" "s_mul_hi_u32 s63, s62, %[k1]\n\t" \
    "v_smfmac_f32_16x16x64_bf16 v[236:239], v[248:251], a[128:135], %[ix]\n\t" "s_mul_i32 s62, s62, %[k1]\n\t" "s_lshr_b64 s[62:63], s[62:63], 16\n\t" \
    "s_waitcnt lgkmcnt(8)\n\t" "v_smfmac_f32_16x16x64_bf16 v[232:235], v[252:255], a[136:143], %[ix]\n\t" "s_add_u32 s62, s62, %[ga]\n\t" "s_bitset0_b32 s62, 0\n\t" \
    "v_smfmac_f32_16x16x64_bf16 v[236:239], v[252:255], v[168:175], %[ix]\n\t" "v_mov_b32 v226, s62\n\t" "buffer_store_dword v226, %[hq], %[rs], s67 offen sc1\n\t" \
    "s_waitcnt lgkmcnt(7)\n\t" "v_smfmac_f32_16x16x64_bf16 v[232:235], v[16:19], a[144:151], %[ix]\n\t" "ds_read_b128 v[168:171], %[l0] offset:18432\n\t" \
    "ds_read_b128 v[172:175], %[l0] offset:19456\n\t" "v_smfmac_f32_16x16x64_bf16 v[236:239], v[16:19], a[152:159], %[ix]\n\t" "s_waitcnt lgkmcnt(8)\n\t" \
    "v_smfmac_f32_16x16x64_bf16 v[232:235], v[20:23], a[160:167], %[ix]\n\t" "v_smfmac_f32_16x16x64_bf16 v[236:239], v[20:23], v[176:183], %[ix]\n\t" \
    "ds_read_b128 v[176:179], %[l0] offset:20480\n\t" "ds_read_b128 v[180:183], %[l0] offset:21504\n\t" "s_waitcnt lgkmcnt(9)\n\t" \
    "v_smfmac_f32_16x16x64_bf16 v[232:235], v[24:27], a[168:175], %[ix]\n\t" "v_smfmac_f32_16x16x64_bf16 v[236:239], v[24:27], a[176:183], %[ix]\n\t" \
    "s_waitcnt lgkmcnt(6)\n\t" "v_smfmac_f32_16x16x64_bf16 v[232:235], v[28:31], a[184:191], %[ix]\n\t" \
    "v_smfmac_f32_16x16x64_bf16 v[236:239], v[28:31], v[184:191], %[ix]\n\t" "ds_read_b128 v[184:187], %[l0] offset:22528\n\t" \
    "ds_read_b128 v[188:191], %[l0] offset:23552\n\t" XT(90, 91) "s_nop 4\n\t" "v_add_f32 v232, v232, v233\n\t" "v_add_f32 v234, v234, v235\n\t" \
    "v_add_f32 v236, v236, v237\n\t" "v_add_f32 v238, v238, v239\n\t" "v_cvt_pk_bf16_f32 v236, v232, v236\n\t" "v_cvt_pk_bf16_f32 v237, v234, v238\n\t" \
    "ds_write2_b32 %[vw], v236, v237 offset1:68\n\t" "s_waitcnt lgkmcnt(0)\n\t" "s_barrier\n\t" XT(92, 93) "ds_read_b128 v[240:243], %[vr] offset:0\n\t" \
    "ds_read_b128 v[244:247], %[vr] offset:64\n\t" "ds_read_b128 v[248:251], %[vr] offset:128\n\t" "ds_read_b128 v[252:255], %[vr] offset:192\n\t" "s_mov_b32 s61, %[ub]\n\t" \
    "s_waitcnt lgkmcnt(3)\n\t" "v_smfmac_f32_16x16x64_bf16 v[192:195], v[240:243], v[32:39], %[ix]\n\t" \
    "v_smfmac_f32_16x16x64_bf16 v[196:199], v[240:243], v[48:55], %[ix]\n\t" "s_waitcnt lgkmcnt(2)\n\t" \
    "v_smfmac_f32_16x16x64_bf16 v[192:195], v[244:247], v[40:47], %[ix]\n\t" "v_smfmac_f32_16x16x64_bf16 v[196:199], v[244:247], v[56:63], %[ix]\n\t" \
    "s_waitcnt lgkmcnt(1)\n\t" "v_smfmac_f32_16x16x64_bf16 v[192:195], v[248:251], a[192:199], %[ix]\n\t" \
    "v_smfmac_f32_16x16x64_bf16 v[196:199], v[248:251], a[200:207], %[ix]\n\t" "s_waitcnt lgkmcnt(0)\n\t" \
    "v_smfmac_f32_16x16x64_bf16 v[192:195], v[252:255], v[160:167], %[ix]\n\t" "ds_read_b128 v[160:163], %[l0] offset:24576\n\t" \
    "ds_read_b128 v[164:167], %[l0] offset:25600\n\t" "v_smfmac_f32_16x16x64_bf16 v[196:199], v[252:255], v[168:175], %[ix]\n\t" \
    "ds_read_b128 v[168:171], %[l0] offset:26624\n\t" "ds_read_b128 v[172:175], %[l0] offset:27648\n\t" \
    "v_smfmac_f32_16x16x64_bf16 v[200:203], v[240:243], v[64:71], %[ix]\n\t" "s_nop 1\n\t" "v_smfmac_f32_16x16x64_bf16 v[204:207], v[240:243], v[80:87], %[ix]\n\t" \
    "v_cvt_pk_bf16_f32 v224, v192, v194\n\t" "v_cvt_pk_bf16_f32 v225, v193, v195\n\t" "v_smfmac_f32_16x16x64_bf16 v[200:203], v[244:247], v[72:79], %[ix]\n\t" \
    "v_cvt_pk_bf16_f32 v226, v196, v198\n\t" "v_cvt_pk_bf16_f32 v227, v197, v199\n\t" "v_smfmac_f32_16x16x64_bf16 v[204:207], v[244:247], v[88:95], %[ix]\n\t" "s_nop 0\n\t" \
    "v_permlane32_swap_b32 v224, v226\n\t" "v_smfmac_f32_16x16x64_bf16 v[200:203], v[248:251], a[208:215], %[ix]\n\t" "v_permlane32_swap_b32 v225, v227\n\t" "s_nop 0\n\t" \
    "v_smfmac_f32_16x16x64_bf16 v[204:207], v[248:251], a[216:223], %[ix]\n\t" "v_permlane16_swap_b32 v224, v225\n\t" "v_permlane16_swap_b32 v226, v227\n\t" \
    "v_smfmac_f32_16x16x64_bf16 v[200:203], v[252:255], v[176:183], %[ix]\n\t" "ds_read_b128 v[176:179], %[l0] offset:28672\n\t" \
    "buffer_store_dwordx4 v[224:227], %[uo], %[rs], s61 offen sc1\n\t" "s_xor_b32 s61, s61, 0x40000\n\t" XT(94, 95) \
    "v_smfmac_f32_16x16x64_bf16 v[204:207], v[252:255], v[184:191], %[ix]\n\t" "ds_read_b128 v[180:183], %[l0] offset:29696\n\t" \
    "ds_read_b128 v[184:187], %[l0] offset:30720\n\t" "v_smfmac_f32_16x16x64_bf16 v[208:211], v[240:243], v[96:103], %[ix]\n\t" \
    "ds_read_b128 v[188:191], %[l0] offset:31744\n\t" "s_nop 0\n\t" "v_smfmac_f32_16x16x64_bf16 v[212:215], v[240:243], v[112:119], %[ix]\n\t" \
    "v_cvt_pk_bf16_f32 v228, v200, v202\n\t" "v_cvt_pk_bf16_f32 v229, v201, v203\n\t" "v_smfmac_f32_16x16x64_bf16 v[208:211], v[244:247], v[104:111], %[ix]\n\t" \
    "v_cvt_pk_bf16_f32 v230, v204, v206\n\t" "v_cvt_pk_bf16_f32 v231, v205, v207\n\t" "v_smfmac_f32_16x16x64_bf16 v[212:215], v[244:247], v[120:127], %[ix]\n\t" "s_nop 0\n\t" \
    "v_permlane32_swap_b32 v228, v230\n\t" "v_smfmac_f32_16x16x64_bf16 v[208:211], v[248:251], a[224:231], %[ix]\n\t" "v_permlane32_swap_b32 v229, v231\n\t" "s_nop 0\n\t" \
    "v_smfmac_f32_16x16x64_bf16 v[212:215], v[248:251], a[232:239], %[ix]\n\t" "v_permlane16_swap_b32 v228, v229\n\t" "v_permlane16_swap_b32 v230, v231\n\t" \
    "s_waitcnt lgkmcnt(6)\n\t" "v_smfmac_f32_16x16x64_bf16 v[208:211], v[252:255], v[160:167], %[ix]\n\t" "buffer_store_dwordx4 v[228:231], %[uo], %[rs], s61 offen sc1\n\t" \
    "s_xor_b32 s61, s61, 0xc0000\n\t" "s_waitcnt lgkmcnt(4)\n\t" "v_smfmac_f32_16x16x64_bf16 v[212:215], v[252:255], v[168:175], %[ix]\n\t" "s_nop 1\n\t" \
    "v_smfmac_f32_16x16x64_bf16 v[216:219], v[240:243], v[128:135], %[ix]\n\t" "s_nop 1\n\t" "v_smfmac_f32_16x16x64_bf16 v[220:223], v[240:243], v[144:151], %[ix]\n\t" \
    "v_cvt_pk_bf16_f32 v224, v208, v210\n\t" "v_cvt_pk_bf16_f32 v225, v209, v211\n\t" "v_smfmac_f32_16x16x64_bf16 v[216:219], v[244:247], v[136:143], %[ix]\n\t" \
    "v_cvt_pk_bf16_f32 v226, v212, v214\n\t" "v_cvt_pk_bf16_f32 v227, v213, v215\n\t" "v_smfmac_f32_16x16x64_bf16 v[220:223], v[244:247], v[152:159], %[ix]\n\t" "s_nop 0\n\t" \
    "v_permlane32_swap_b32 v224, v226\n\t" "v_smfmac_f32_16x16x64_bf16 v[216:219], v[248:251], a[240:247], %[ix]\n\t" "v_permlane32_swap_b32 v225, v227\n\t" "s_nop 0\n\t" \
    "v_smfmac_f32_16x16x64_bf16 v[220:223], v[248:251], a[248:255], %[ix]\n\t" "v_permlane16_swap_b32 v224, v225\n\t" "v_permlane16_swap_b32 v226, v227\n\t" \
    "s_waitcnt lgkmcnt(2)\n\t" "v_smfmac_f32_16x16x64_bf16 v[216:219], v[252:255], v[176:183], %[ix]\n\t" "buffer_store_dwordx4 v[224:227], %[uo], %[rs], s61 offen sc1\n\t" \
    "s_xor_b32 s61, s61, 0x40000\n\t" "s_waitcnt lgkmcnt(0)\n\t" "v_smfmac_f32_16x16x64_bf16 v[220:223], v[252:255], v[184:191], %[ix]\n\t" "s_nop 5\n\t" \
    "v_cvt_pk_bf16_f32 v228, v216, v218\n\t" "v_cvt_pk_bf16_f32 v229, v217, v219\n\t" "v_cvt_pk_bf16_f32 v230, v220, v222\n\t" "v_cvt_pk_bf16_f32 v231, v221, v223\n\t" \
    "s_nop 0\n\t" "v_permlane32_swap_b32 v228, v230\n\t" "v_permlane32_swap_b32 v229, v231\n\t" "s_nop 1\n\t" "v_permlane16_swap_b32 v228, v229\n\t" \
    "v_permlane16_swap_b32 v230, v231\n\t" "buffer_store_dwordx4 v[228:231], %[uo], %[rs], s61 offen sc1\n\t" "s_nop 1\n\t" XT(96, 97) "s_branch 99f\n\t" "11:\n\t" \
    "ds_read_b128 v[228:231], %[l1] offset:0\n\t" "s_waitcnt lgkmcnt(0)\n\t" "v_max3_u32 v225, v228, v229, v230\n\t" "v_max_u32 v225, v225, v231\n\t" \
    "ds_read_b128 v[228:231], %[l1] offset:1024\n\t" "s_waitcnt lgkmcnt(0)\n\t" "v_max3_u32 v225, v225, v228, v229\n\t" "v_max3_u32 v225, v225, v230, v231\n\t" \
    "ds_read_b128 v[228:231], %[l1] offset:2048\n\t" "s_waitcnt lgkmcnt(0)\n\t" "v_max3_u32 v225, v225, v228, v229\n\t" "v_max3_u32 v225, v225, v230, v231\n\t" \
    "ds_read_b128 v[228:231], %[l1] offset:3072\n\t" "s_waitcnt lgkmcnt(0)\n\t" "v_max3_u32 v225, v225, v228, v229\n\t" "v_max3_u32 v225, v225, v230, v231\n\t" \
    "v_cmp_eq_u32 vcc, -1, v225\n\t" "s_cbranch_vccz 12b\n\t" "s_cmp_eq_u32 %[rt], 1\n\t" "s_cbranch_scc1 12b\n\t" "v_mov_b32 v225, %[rt]\n\t" "ds_write_b32 v227, v225\n\t" \
    "s_waitcnt lgkmcnt(0)\n\t" "s_branch 12b\n\t" "13:\n\t" "s_sub_u32 %[rt], %[rt], 1\n\t" "s_waitcnt lgkmcnt(0)\n\t" XCNT(100) "14:\n\t" \
    "buffer_load_dwordx4 v[240:243], %[mo], %[rs], %[sb] offen sc1\n\t" "buffer_load_dwordx4 v[244:247], v224, %[rs], %[sb] offen sc1\n\t" \
    "buffer_load_dwordx4 v[248:251], %[mo], %[rs], s60 offen sc1\n\t" "buffer_load_dwordx4 v[252:255], v224, %[rs], s60 offen sc1\n\t" "s_waitcnt vmcnt(0)\n\t" \
    "v_max_u32 v225, v240, v241\n\t" "v_max3_u32 v225, v225, v242, v243\n\t" "v_max3_u32 v225, v225, v244, v245\n\t" "v_max3_u32 v225, v225, v246, v247\n\t" \
    "v_max3_u32 v225, v225, v248, v249\n\t" "v_max3_u32 v225, v225, v250, v251\n\t" "v_max3_u32 v225, v225, v252, v253\n\t" "v_max3_u32 v225, v225, v254, v255\n\t" \
    "v_cmp_eq_u32 vcc, -1, v225\n\t" "s_cbranch_vccz 15f\n\t" "s_cmp_eq_u32 %[bd], 0\n\t" "s_cbranch_scc1 15f\n\t" "s_sub_u32 %[bd], %[bd], 1\n\t" XCNT(101) \
    "s_branch 14b\n\t" "15:\n\t" "ds_read_b128 v[160:163], %[l0] offset:0\n\t" "ds_read_b128 v[164:167], %[l0] offset:1024\n\t" \
    "ds_read_b128 v[168:171], %[l0] offset:2048\n\t" "ds_read_b128 v[172:175], %[l0] offset:3072\n\t" "ds_read_b128 v[176:179], %[l0] offset:4096\n\t" \
    "ds_read_b128 v[180:183], %[l0] offset:5120\n\t" "ds_read_b128 v[184:187], %[l0] offset:6144\n\t" "ds_read_b128 v[188:191], %[l0] offset:7168\n\t" \
    "v_mov_b64 v[232:233], 0\n\t" "v_mov_b64 v[234:235], 0\n\t" "v_mov_b64 v[236:237], 0\n\t" "v_mov_b64 v[238:239], 0\n\t" "s_barrier\n\t" "s_memtime s[64:65]\n\t" \
    "s_waitcnt lgkmcnt(0)\n\t" "s_add_u32 s64, s64, %[dr]\n\t" "s_sub_u32 s62, s64, %[ca]\n\t" "s_mul_hi_u32 s63, s62, %[k1]\n\t" "s_mul_i32 s62, s62, %[k1]\n\t" \
    "s_lshr_b64 s[62:63], s[62:63], 16\n\t" "s_add_u32 s62, s62, %[ga]\n\t" "s_bitset0_b32 s62, 0\n\t" "v_mov_b32 v225, s62\n\t" \
    "buffer_store_dword v225, %[hq], %[rs], s67 offen sc1\n\t" "s_mov_b32 s67, 0x7f000000\n\t" "s_branch 10b\n\t" "91:\n\t" XT(80, 81) "s_barrier\n\t" XT(82, 83) \
    "buffer_load_dwordx4 v[240:243], %[mo], %[rs], %[sb] offen sc1\n\t" "v_mov_b64 v[232:233], 0\n\t" "v_mov_b64 v[234:235], 0\n\t" "v_mov_b64 v[236:237], 0\n\t" \
    "v_mov_b64 v[238:239], 0\n\t" "v_mov_b64 v[192:193], 0\n\t" "v_mov_b64 v[194:195], 0\n\t" "v_mov_b64 v[196:197], 0\n\t" "v_mov_b64 v[198:199], 0\n\t" \
    "buffer_load_dwordx4 v[244:247], v224, %[rs], %[sb] offen sc1\n\t" "ds_read_b128 v[160:163], %[l0] offset:0\n\t" "ds_read_b128 v[164:167], %[l0] offset:1024\n\t" \
    "ds_read_b128 v[168:171], %[l0] offset:2048\n\t" "ds_read_b128 v[172:175], %[l0] offset:3072\n\t" "s_nop 3\n\t" \
    "buffer_load_dwordx4 v[248:251], %[mo], %[rs], s60 offen sc1\n\t" "ds_read_b128 v[176:179], %[l0] offset:4096\n\t" "ds_read_b128 v[180:183], %[l0] offset:5120\n\t" \
    "ds_read_b128 v[184:187], %[l0] offset:6144\n\t" "ds_read_b128 v[188:191], %[l0] offset:7168\n\t" "s_nop 3\n\t" \
    "buffer_load_dwordx4 v[252:255], v224, %[rs], s60 offen sc1\n\t" "v_mov_b64 v[200:201], 0\n\t" "v_mov_b64 v[202:203], 0\n\t" "v_mov_b64 v[204:205], 0\n\t" \
    "v_mov_b64 v[206:207], 0\n\t" "v_mov_b64 v[208:209], 0\n\t" "v_mov_b64 v[210:211], 0\n\t" "v_mov_b64 v[212:213], 0\n\t" "v_mov_b64 v[214:215], 0\n\t" \
    "v_mov_b64 v[216:217], 0\n\t" "v_mov_b64 v[218:219], 0\n\t" "v_mov_b64 v[220:221], 0\n\t" "v_mov_b64 v[222:223], 0\n\t" "v_mov_b32 v227, 160512\n\t" "20:\n\t" \
    "s_waitcnt vmcnt(3)\n\t" XT(84, 85) "v_smfmac_f32_16x16x64_bf16 v[232:235], v[240:243], a[0:7], %[ix]\n\t" "ds_write_b128 %[l1], v[240:243] offset:4096\n\t" \
    "v_smfmac_f32_16x16x64_bf16 v[236:239], v[240:243], a[8:15], %[ix]\n\t" "s_waitcnt vmcnt(2)\n\t" "v_smfmac_f32_16x16x64_bf16 v[232:235], v[244:247], a[16:23], %[ix]\n\t" \
    "ds_write_b128 %[l1], v[244:247] offset:5120\n\t" "s_waitcnt lgkmcnt(8)\n\t" "v_smfmac_f32_16x16x64_bf16 v[236:239], v[244:247], v[160:167], %[ix]\n\t" \
    "s_waitcnt lgkmcnt(0)\n\t" "s_barrier\n\t" XT(86, 87) "ds_read_b128 v[16:19], %[l1] offset:8192\n\t" "ds_read_b128 v[20:23], %[l1] offset:12288\n\t" \
    "ds_read_b128 v[24:27], %[l1] offset:0\n\t" "s_waitcnt vmcnt(1)\n\t" "v_smfmac_f32_16x16x64_bf16 v[232:235], v[248:251], a[24:31], %[ix]\n\t" \
    "ds_write_b128 %[l1], v[248:251] offset:6144\n\t" "v_smfmac_f32_16x16x64_bf16 v[236:239], v[248:251], a[32:39], %[ix]\n\t" "ds_read_b128 v[28:31], %[l1] offset:9216\n\t" \
    "ds_read_b128 v[240:243], %[l1] offset:13312\n\t" "ds_read_b128 v[244:247], %[l1] offset:1024\n\t" "s_waitcnt vmcnt(0)\n\t" \
    "v_smfmac_f32_16x16x64_bf16 v[232:235], v[252:255], a[40:47], %[ix]\n\t" "ds_write_b128 %[l1], v[252:255] offset:7168\n\t" \
    "v_smfmac_f32_16x16x64_bf16 v[236:239], v[252:255], v[168:175], %[ix]\n\t" "ds_read_b128 v[160:163], %[l0] offset:8192\n\t" \
    "ds_read_b128 v[164:167], %[l0] offset:9216\n\t" "ds_read_b128 v[168:171], %[l0] offset:10240\n\t" "ds_read_b128 v[172:175], %[l0] offset:11264\n\t" "s_nop 1\n\t" \
    "v_cmp_u_f32 vcc, v232, v233\n\t" "v_cmp_u_f32 s[62:63], v234, v235\n\t" "s_waitcnt lgkmcnt(11)\n\t" \
    "v_smfmac_f32_16x16x64_bf16 v[232:235], v[16:19], a[48:55], %[ix]\n\t" "v_smfmac_f32_16x16x64_bf16 v[236:239], v[16:19], a[56:63], %[ix]\n\t" \
    "s_or_b64 vcc, vcc, s[62:63]\n\t" "s_cbranch_scc1 21f\n\t" "22:\n\t" "s_waitcnt lgkmcnt(10)\n\t" "v_smfmac_f32_16x16x64_bf16 v[232:235], v[20:23], a[64:71], %[ix]\n\t" \
    "v_smfmac_f32_16x16x64_bf16 v[236:239], v[20:23], v[176:183], %[ix]\n\t" "s_waitcnt lgkmcnt(4)\n\t" "s_barrier\n\t" XT(88, 89) "ds_read_b32 v225, v227\n\t" \
    "v_smfmac_f32_16x16x64_bf16 v[232:235], v[24:27], a[72:79], %[ix]\n\t" "ds_read_b128 v[176:179], %[l0] offset:12288\n\t" "ds_read_b128 v[180:183], %[l0] offset:13312\n\t" \
    "v_smfmac_f32_16x16x64_bf16 v[236:239], v[24:27], a[80:87], %[ix]\n\t" "ds_read_b128 v[248:251], %[l1] offset:10240\n\t" "ds_read_b128 v[252:255], %[l1] offset:14336\n\t" \
    "v_smfmac_f32_16x16x64_bf16 v[232:235], v[28:31], a[88:95], %[ix]\n\t" "ds_read_b128 v[16:19], %[l1] offset:2048\n\t" "ds_read_b128 v[20:23], %[l1] offset:11264\n\t" \
    "v_smfmac_f32_16x16x64_bf16 v[236:239], v[28:31], v[184:191], %[ix]\n\t" "ds_read_b128 v[24:27], %[l1] offset:15360\n\t" "ds_read_b128 v[184:187], %[l0] offset:14336\n\t" \
    "v_smfmac_f32_16x16x64_bf16 v[232:235], v[240:243], a[96:103], %[ix]\n\t" "ds_read_b128 v[188:191], %[l0] offset:15360\n\t" "ds_read_b128 v[28:31], %[l1] offset:3072\n\t" \
    "v_smfmac_f32_16x16x64_bf16 v[236:239], v[240:243], a[104:111], %[ix]\n\t" "s_waitcnt lgkmcnt(10)\n\t" "v_cmp_eq_u32 vcc, %[rt], v225\n\t" \
    "v_smfmac_f32_16x16x64_bf16 v[232:235], v[244:247], a[112:119], %[ix]\n\t" "s_cbranch_vccnz 23f\n\t" \
    "v_smfmac_f32_16x16x64_bf16 v[236:239], v[244:247], v[160:167], %[ix]\n\t" "ds_read_b128 v[160:163], %[l0] offset:16384\n\t" \
    "ds_read_b128 v[164:167], %[l0] offset:17408\n\t" "s_waitcnt lgkmcnt(9)\n\t" "v_smfmac_f32_16x16x64_bf16 v[232:235], v[248:251], a[120:127], %[ix]\n\t" \
    "v_smfmac_f32_16x16x64_bf16 v[236:239], v[248:251], a[128:135], %[ix]\n\t" "s_waitcnt lgkmcnt(8)\n\t" \
    "v_smfmac_f32_16x16x64_bf16 v[232:235], v[252:255], a[136:143], %[ix]\n\t" "v_smfmac_f32_16x16x64_bf16 v[236:239], v[252:255], v[168:175], %[ix]\n\t" \
    "ds_read_b128 v[168:171], %[l0] offset:18432\n\t" "ds_read_b128 v[172:175], %[l0] offset:19456\n\t" "s_waitcnt lgkmcnt(9)\n\t" \
    "v_smfmac_f32_16x16x64_bf16 v[232:235], v[16:19], a[144:151], %[ix]\n\t" "v_smfmac_f32_16x16x64_bf16 v[236:239], v[16:19], a[152:159], %[ix]\n\t" \
    "s_waitcnt lgkmcnt(8)\n\t" "v_smfmac_f32_16x16x64_bf16 v[232:235], v[20:23], a[160:167], %[ix]\n\t" \
    "v_smfmac_f32_16x16x64_bf16 v[236:239], v[20:23], v[176:183], %[ix]\n\t" "ds_read_b128 v[176:179], %[l0] offset:20480\n\t" \
    "ds_read_b128 v[180:183], %[l0] offset:21504\n\t" "s_waitcnt lgkmcnt(9)\n\t" "v_smfmac_f32_16x16x64_bf16 v[232:235], v[24:27], a[168:175], %[ix]\n\t" \
    "v_smfmac_f32_16x16x64_bf16 v[236:239], v[24:27], a[176:183], %[ix]\n\t" "s_waitcnt lgkmcnt(6)\n\t" \
    "v_smfmac_f32_16x16x64_bf16 v[232:235], v[28:31], a[184:191], %[ix]\n\t" "v_smfmac_f32_16x16x64_bf16 v[236:239], v[28:31], v[184:191], %[ix]\n\t" \
    "ds_read_b128 v[184:187], %[l0] offset:22528\n\t" "ds_read_b128 v[188:191], %[l0] offset:23552\n\t" XT(90, 91) "s_nop 4\n\t" "v_add_f32 v232, v232, v233\n\t" \
    "v_add_f32 v234, v234, v235\n\t" "v_add_f32 v236, v236, v237\n\t" "v_add_f32 v238, v238, v239\n\t" "v_cvt_pk_bf16_f32 v236, v232, v236\n\t" \
    "v_cvt_pk_bf16_f32 v237, v234, v238\n\t" "ds_write2_b32 %[vw], v236, v237 offset1:68\n\t" "s_waitcnt lgkmcnt(0)\n\t" "s_barrier\n\t" XT(92, 93) \
    "ds_read_b128 v[240:243], %[vr] offset:0\n\t" "ds_read_b128 v[244:247], %[vr] offset:64\n\t" "ds_read_b128 v[248:251], %[vr] offset:128\n\t" \
    "ds_read_b128 v[252:255], %[vr] offset:192\n\t" "s_mov_b32 s61, %[ub]\n\t" "s_waitcnt lgkmcnt(3)\n\t" \
    "v_smfmac_f32_16x16x64_bf16 v[192:195], v[240:243], v[32:39], %[ix]\n\t" "v_smfmac_f32_16x16x64_bf16 v[196:199], v[240:243], v[48:55], %[ix]\n\t" \
    "s_waitcnt lgkmcnt(2)\n\t" "v_smfmac_f32_16x16x64_bf16 v[192:195], v[244:247], v[40:47], %[ix]\n\t" \
    "v_smfmac_f32_16x16x64_bf16 v[196:199], v[244:247], v[56:63], %[ix]\n\t" "s_waitcnt lgkmcnt(1)\n\t" \
    "v_smfmac_f32_16x16x64_bf16 v[192:195], v[248:251], a[192:199], %[ix]\n\t" "v_smfmac_f32_16x16x64_bf16 v[196:199], v[248:251], a[200:207], %[ix]\n\t" \
    "s_waitcnt lgkmcnt(0)\n\t" "v_smfmac_f32_16x16x64_bf16 v[192:195], v[252:255], v[160:167], %[ix]\n\t" "ds_read_b128 v[160:163], %[l0] offset:24576\n\t" \
    "ds_read_b128 v[164:167], %[l0] offset:25600\n\t" "v_smfmac_f32_16x16x64_bf16 v[196:199], v[252:255], v[168:175], %[ix]\n\t" \
    "ds_read_b128 v[168:171], %[l0] offset:26624\n\t" "ds_read_b128 v[172:175], %[l0] offset:27648\n\t" \
    "v_smfmac_f32_16x16x64_bf16 v[200:203], v[240:243], v[64:71], %[ix]\n\t" "s_nop 1\n\t" "v_smfmac_f32_16x16x64_bf16 v[204:207], v[240:243], v[80:87], %[ix]\n\t" \
    "v_cvt_pk_bf16_f32 v224, v192, v194\n\t" "v_cvt_pk_bf16_f32 v225, v193, v195\n\t" "v_smfmac_f32_16x16x64_bf16 v[200:203], v[244:247], v[72:79], %[ix]\n\t" \
    "v_cvt_pk_bf16_f32 v226, v196, v198\n\t" "v_cvt_pk_bf16_f32 v227, v197, v199\n\t" "v_smfmac_f32_16x16x64_bf16 v[204:207], v[244:247], v[88:95], %[ix]\n\t" "s_nop 0\n\t" \
    "v_permlane32_swap_b32 v224, v226\n\t" "v_smfmac_f32_16x16x64_bf16 v[200:203], v[248:251], a[208:215], %[ix]\n\t" "v_permlane32_swap_b32 v225, v227\n\t" "s_nop 0\n\t" \
    "v_smfmac_f32_16x16x64_bf16 v[204:207], v[248:251], a[216:223], %[ix]\n\t" "v_permlane16_swap_b32 v224, v225\n\t" "v_permlane16_swap_b32 v226, v227\n\t" \
    "v_smfmac_f32_16x16x64_bf16 v[200:203], v[252:255], v[176:183], %[ix]\n\t" "ds_read_b128 v[176:179], %[l0] offset:28672\n\t" \
    "buffer_store_dwordx4 v[224:227], %[uo], %[rs], s61 offen sc1\n\t" "s_xor_b32 s61, s61, 0x40000\n\t" XT(94, 95) \
    "v_smfmac_f32_16x16x64_bf16 v[204:207], v[252:255], v[184:191], %[ix]\n\t" "ds_read_b128 v[180:183], %[l0] offset:29696\n\t" \
    "ds_read_b128 v[184:187], %[l0] offset:30720\n\t" "v_smfmac_f32_16x16x64_bf16 v[208:211], v[240:243], v[96:103], %[ix]\n\t" \
    "ds_read_b128 v[188:191], %[l0] offset:31744\n\t" "s_nop 0\n\t" "v_smfmac_f32_16x16x64_bf16 v[212:215], v[240:243], v[112:119], %[ix]\n\t" \
    "v_cvt_pk_bf16_f32 v228, v200, v202\n\t" "v_cvt_pk_bf16_f32 v229, v201, v203\n\t" "v_smfmac_f32_16x16x64_bf16 v[208:211], v[244:247], v[104:111], %[ix]\n\t" \
    "v_cvt_pk_bf16_f32 v230, v204, v206\n\t" "v_cvt_pk_bf16_f32 v231, v205, v207\n\t" "v_smfmac_f32_16x16x64_bf16 v[212:215], v[244:247], v[120:127], %[ix]\n\t" "s_nop 0\n\t" \
    "v_permlane32_swap_b32 v228, v230\n\t" "v_smfmac_f32_16x16x64_bf16 v[208:211], v[248:251], a[224:231], %[ix]\n\t" "v_permlane32_swap_b32 v229, v231\n\t" "s_nop 0\n\t" \
    "v_smfmac_f32_16x16x64_bf16 v[212:215], v[248:251], a[232:239], %[ix]\n\t" "v_permlane16_swap_b32 v228, v229\n\t" "v_permlane16_swap_b32 v230, v231\n\t" \
    "s_waitcnt lgkmcnt(6)\n\t" "v_smfmac_f32_16x16x64_bf16 v[208:211], v[252:255], v[160:167], %[ix]\n\t" "buffer_store_dwordx4 v[228:231], %[uo], %[rs], s61 offen sc1\n\t" \
    "s_xor_b32 s61, s61, 0xc0000\n\t" "s_waitcnt lgkmcnt(4)\n\t" "v_smfmac_f32_16x16x64_bf16 v[212:215], v[252:255], v[168:175], %[ix]\n\t" "s_nop 1\n\t" \
    "v_smfmac_f32_16x16x64_bf16 v[216:219], v[240:243], v[128:135], %[ix]\n\t" "s_nop 1\n\t" "v_smfmac_f32_16x16x64_bf16 v[220:223], v[240:243], v[144:151], %[ix]\n\t" \
    "v_cvt_pk_bf16_f32 v224, v208, v210\n\t" "v_cvt_pk_bf16_f32 v225, v209, v211\n\t" "v_smfmac_f32_16x16x64_bf16 v[216:219], v[244:247], v[136:143], %[ix]\n\t" \
    "v_cvt_pk_bf16_f32 v226, v212, v214\n\t" "v_cvt_pk_bf16_f32 v227, v213, v215\n\t" "v_smfmac_f32_16x16x64_bf16 v[220:223], v[244:247], v[152:159], %[ix]\n\t" "s_nop 0\n\t" \
    "v_permlane32_swap_b32 v224, v226\n\t" "v_smfmac_f32_16x16x64_bf16 v[216:219], v[248:251], a[240:247], %[ix]\n\t" "v_permlane32_swap_b32 v225, v227\n\t" "s_nop 0\n\t" \
    "v_smfmac_f32_16x16x64_bf16 v[220:223], v[248:251], a[248:255], %[ix]\n\t" "v_permlane16_swap_b32 v224, v225\n\t" "v_permlane16_swap_b32 v226, v227\n\t" \
    "s_waitcnt lgkmcnt(2)\n\t" "v_smfmac_f32_16x16x64_bf16 v[216:219], v[252:255], v[176:183], %[ix]\n\t" "buffer_store_dwordx4 v[224:227], %[uo], %[rs], s61 offen sc1\n\t" \
    "s_xor_b32 s61, s61, 0x40000\n\t" "s_waitcnt lgkmcnt(0)\n\t" "v_smfmac_f32_16x16x64_bf16 v[220:223], v[252:255], v[184:191], %[ix]\n\t" "s_nop 5\n\t" \
    "v_cvt_pk_bf16_f32 v228, v216, v218\n\t" "v_cvt_pk_bf16_f32 v229, v217, v219\n\t" "v_cvt_pk_bf16_f32 v230, v220, v222\n\t" "v_cvt_pk_bf16_f32 v231, v221, v223\n\t" \
    "s_nop 0\n\t" "v_permlane32_swap_b32 v228, v230\n\t" "v_permlane32_swap_b32 v229, v231\n\t" "s_nop 1\n\t" "v_permlane16_swap_b32 v228, v229\n\t" \
    "v_permlane16_swap_b32 v230, v231\n\t" "buffer_store_dwordx4 v[228:231], %[uo], %[rs], s61 offen sc1\n\t" "s_nop 1\n\t" XT(96, 97) "v_mov_b64 v[16:17], -1\n\t" \
    "v_mov_b64 v[18:19], -1\n\t" "s_barrier\n\t" "buffer_store_dwordx4 v[16:19], %[l1], %[rs], %[ur] offen offset:0 sc1\n\t" \
    "buffer_store_dwordx4 v[16:19], %[l1], %[rs], %[ur] offen offset:1024 sc1\n\t" "buffer_store_dwordx4 v[16:19], %[l1], %[rs], %[ur] offen offset:2048 sc1\n\t" \
    "buffer_store_dwordx4 v[16:19], %[l1], %[rs], %[ur] offen offset:3072 sc1\n\t" "s_sub_u32 s61, %[ur], 0x1000\n\t" \
    "buffer_store_dwordx4 v[16:19], %[l1], %[rs], s61 offen offset:0 sc1\n\t" "buffer_store_dwordx4 v[16:19], %[l1], %[rs], s61 offen offset:1024 sc1\n\t" "s_branch 99f\n\t" \
    "21:\n\t" "ds_read_b128 v[228:231], %[l1] offset:4096\n\t" "s_waitcnt lgkmcnt(0)\n\t" "v_max3_u32 v225, v228, v229, v230\n\t" "v_max_u32 v225, v225, v231\n\t" \
    "ds_read_b128 v[228:231], %[l1] offset:5120\n\t" "s_waitcnt lgkmcnt(0)\n\t" "v_max3_u32 v225, v225, v228, v229\n\t" "v_max3_u32 v225, v225, v230, v231\n\t" \
    "ds_read_b128 v[228:231], %[l1] offset:6144\n\t" "s_waitcnt lgkmcnt(0)\n\t" "v_max3_u32 v225, v225, v228, v229\n\t" "v_max3_u32 v225, v225, v230, v231\n\t" \
    "ds_read_b128 v[228:231], %[l1] offset:7168\n\t" "s_waitcnt lgkmcnt(0)\n\t" "v_max3_u32 v225, v225, v228, v229\n\t" "v_max3_u32 v225, v225, v230, v231\n\t" \
    "v_cmp_eq_u32 vcc, -1, v225\n\t" "s_cbranch_vccz 22b\n\t" "s_cmp_eq_u32 %[rt], 1\n\t" "s_cbranch_scc1 22b\n\t" "v_mov_b32 v225, %[rt]\n\t" "ds_write_b32 v227, v225\n\t" \
    "s_waitcnt lgkmcnt(0)\n\t" "s_branch 22b\n\t" "23:\n\t" "s_sub_u32 %[rt], %[rt], 1\n\t" "s_waitcnt lgkmcnt(0)\n\t" XCNT(100) "24:\n\t" \
    "buffer_load_dwordx4 v[240:243], %[mo], %[rs], %[sb] offen sc1\n\t" "buffer_load_dwordx4 v[244:247], v224, %[rs], %[sb] offen sc1\n\t" \
    "buffer_load_dwordx4 v[248:251], %[mo], %[rs], s60 offen sc1\n\t" "buffer_load_dwordx4 v[252:255], v224, %[rs], s60 offen sc1\n\t" "s_waitcnt vmcnt(0)\n\t" \
    "v_max_u32 v225, v240, v241\n\t" "v_max3_u32 v225, v225, v242, v243\n\t" "v_max3_u32 v225, v225, v244, v245\n\t" "v_max3_u32 v225, v225, v246, v247\n\t" \
    "v_max3_u32 v225, v225, v248, v249\n\t" "v_max3_u32 v225, v225, v250, v251\n\t" "v_max3_u32 v225, v225, v252, v253\n\t" "v_max3_u32 v225, v225, v254, v255\n\t" \
    "v_cmp_eq_u32 vcc, -1, v225\n\t" "s_cbranch_vccz 25f\n\t" "s_cmp_eq_u32 %[bd], 0\n\t" "s_cbranch_scc1 25f\n\t" "s_sub_u32 %[bd], %[bd], 1\n\t" XCNT(101) \
    "s_branch 24b\n\t" "25:\n\t" "ds_read_b128 v[160:163], %[l0] offset:0\n\t" "ds_read_b128 v[164:167], %[l0] offset:1024\n\t" \
    "ds_read_b128 v[168:171], %[l0] offset:2048\n\t" "ds_read_b128 v[172:175], %[l0] offset:3072\n\t" "ds_read_b128 v[176:179], %[l0] offset:4096\n\t" \
    "ds_read_b128 v[180:183], %[l0] offset:5120\n\t" "ds_read_b128 v[184:187], %[l0] offset:6144\n\t" "ds_read_b128 v[188:191], %[l0] offset:7168\n\t" \
    "v_mov_b64 v[232:233], 0\n\t" "v_mov_b64 v[234:235], 0\n\t" "v_mov_b64 v[236:237], 0\n\t" "v_mov_b64 v[238:239], 0\n\t" "s_barrier\n\t" "s_branch 20b\n\t" "92:\n\t" \
    XT(80, 81) "s_barrier\n\t" XT(82, 83) "buffer_load_dwordx4 v[240:243], %[mo], %[rs], %[sb] offen sc1\n\t" "v_mov_b64 v[232:233], 0\n\t" "v_mov_b64 v[234:235], 0\n\t" \
    "v_mov_b64 v[236:237], 0\n\t" "v_mov_b64 v[238:239], 0\n\t" "v_mov_b64 v[192:193], 0\n\t" "v_mov_b64 v[194:195], 0\n\t" "v_mov_b64 v[196:197], 0\n\t" \
    "v_mov_b64 v[198:199], 0\n\t" "buffer_load_dwordx4 v[244:247], v224, %[rs], %[sb] offen sc1\n\t" "ds_read_b128 v[160:163], %[l0] offset:0\n\t" \
    "ds_read_b128 v[164:167], %[l0] offset:1024\n\t" "ds_read_b128 v[168:171], %[l0] offset:2048\n\t" "ds_read_b128 v[172:175], %[l0] offset:3072\n\t" "s_nop 3\n\t" \
    "buffer_load_dwordx4 v[248:251], %[mo], %[rs], s60 offen sc1\n\t" "ds_read_b128 v[176:179], %[l0] offset:4096\n\t" "ds_read_b128 v[180:183], %[l0] offset:5120\n\t" \
    "ds_read_b128 v[184:187], %[l0] offset:6144\n\t" "ds_read_b128 v[188:191], %[l0] offset:7168\n\t" "s_nop 3\n\t" \
    "buffer_load_dwordx4 v[252:255], v224, %[rs], s60 offen sc1\n\t" "v_mov_b64 v[200:201], 0\n\t" "v_mov_b64 v[202:203], 0\n\t" "v_mov_b64 v[204:205], 0\n\t" \
    "v_mov_b64 v[206:207], 0\n\t" "v_mov_b64 v[208:209], 0\n\t" "v_mov_b64 v[210:211], 0\n\t" "v_mov_b64 v[212:213], 0\n\t" "v_mov_b64 v[214:215], 0\n\t" \
    "v_mov_b64 v[216:217], 0\n\t" "v_mov_b64 v[218:219], 0\n\t" "v_mov_b64 v[220:221], 0\n\t" "v_mov_b64 v[222:223], 0\n\t" "v_mov_b32 v227, 160512\n\t" "30:\n\t" \
    "s_waitcnt vmcnt(3)\n\t" XT(84, 85) "v_smfmac_f32_16x16x64_bf16 v[232:235], v[240:243], a[0:7], %[ix]\n\t" "ds_write_b128 %[l1], v[240:243] offset:8192\n\t" \
    "v_smfmac_f32_16x16x64_bf16 v[236:239], v[240:243], a[8:15], %[ix]\n\t" "s_waitcnt vmcnt(2)\n\t" "v_smfmac_f32_16x16x64_bf16 v[232:235], v[244:247], a[16:23], %[ix]\n\t" \
    "ds_write_b128 %[l1], v[244:247] offset:9216\n\t" "s_waitcnt lgkmcnt(8)\n\t" "v_smfmac_f32_16x16x64_bf16 v[236:239], v[244:247], v[160:167], %[ix]\n\t" \
    "s_waitcnt lgkmcnt(0)\n\t" "s_barrier\n\t" XT(86, 87) "ds_read_b128 v[16:19], %[l1] offset:12288\n\t" "ds_read_b128 v[20:23], %[l1] offset:0\n\t" \
    "ds_read_b128 v[24:27], %[l1] offset:4096\n\t" "s_waitcnt vmcnt(1)\n\t" "v_smfmac_f32_16x16x64_bf16 v[232:235], v[248:251], a[24:31], %[ix]\n\t" \
    "ds_write_b128 %[l1], v[248:251] offset:10240\n\t" "v_smfmac_f32_16x16x64_bf16 v[236:239], v[248:251], a[32:39], %[ix]\n\t" \
    "ds_read_b128 v[28:31], %[l1] offset:13312\n\t" "ds_read_b128 v[240:243], %[l1] offset:1024\n\t" "ds_read_b128 v[244:247], %[l1] offset:5120\n\t" "s_waitcnt vmcnt(0)\n\t" \
    "v_smfmac_f32_16x16x64_bf16 v[232:235], v[252:255], a[40:47], %[ix]\n\t" "ds_write_b128 %[l1], v[252:255] offset:11264\n\t" \
    "v_smfmac_f32_16x16x64_bf16 v[236:239], v[252:255], v[168:175], %[ix]\n\t" "ds_read_b128 v[160:163], %[l0] offset:8192\n\t" \
    "ds_read_b128 v[164:167], %[l0] offset:9216\n\t" "ds_read_b128 v[168:171], %[l0] offset:10240\n\t" "ds_read_b128 v[172:175], %[l0] offset:11264\n\t" "s_nop 1\n\t" \
    "v_cmp_u_f32 vcc, v232, v233\n\t" "v_cmp_u_f32 s[62:63], v234, v235\n\t" "s_waitcnt lgkmcnt(11)\n\t" \
    "v_smfmac_f32_16x16x64_bf16 v[232:235], v[16:19], a[48:55], %[ix]\n\t" "v_smfmac_f32_16x16x64_bf16 v[236:239], v[16:19], a[56:63], %[ix]\n\t" \
    "s_or_b64 vcc, vcc, s[62:63]\n\t" "s_cbranch_scc1 31f\n\t" "32:\n\t" "s_waitcnt lgkmcnt(10)\n\t" "v_smfmac_f32_16x16x64_bf16 v[232:235], v[20:23], a[64:71], %[ix]\n\t" \
    "v_smfmac_f32_16x16x64_bf16 v[236:239], v[20:23], v[176:183], %[ix]\n\t" "s_waitcnt lgkmcnt(4)\n\t" "s_barrier\n\t" XT(88, 89) "ds_read_b32 v225, v227\n\t" \
    "v_smfmac_f32_16x16x64_bf16 v[232:235], v[24:27], a[72:79], %[ix]\n\t" "ds_read_b128 v[176:179], %[l0] offset:12288\n\t" "ds_read_b128 v[180:183], %[l0] offset:13312\n\t" \
    "v_smfmac_f32_16x16x64_bf16 v[236:239], v[24:27], a[80:87], %[ix]\n\t" "ds_read_b128 v[248:251], %[l1] offset:14336\n\t" "ds_read_b128 v[252:255], %[l1] offset:2048\n\t" \
    "v_smfmac_f32_16x16x64_bf16 v[232:235], v[28:31], a[88:95], %[ix]\n\t" "ds_read_b128 v[16:19], %[l1] offset:6144\n\t" "ds_read_b128 v[20:23], %[l1] offset:15360\n\t" \
    "v_smfmac_f32_16x16x64_bf16 v[236:239], v[28:31], v[184:191], %[ix]\n\t" "ds_read_b128 v[24:27], %[l1] offset:3072\n\t" "ds_read_b128 v[184:187], %[l0] offset:14336\n\t" \
    "v_smfmac_f32_16x16x64_bf16 v[232:235], v[240:243], a[96:103], %[ix]\n\t" "ds_read_b128 v[188:191], %[l0] offset:15360\n\t" "ds_read_b128 v[28:31], %[l1] offset:7168\n\t" \
    "v_smfmac_f32_16x16x64_bf16 v[236:239], v[240:243], a[104:111], %[ix]\n\t" "s_waitcnt lgkmcnt(10)\n\t" "v_cmp_eq_u32 vcc, %[rt], v225\n\t" \
    "v_smfmac_f32_16x16x64_bf16 v[232:235], v[244:247], a[112:119], %[ix]\n\t" "s_cbranch_vccnz 33f\n\t" \
    "v_smfmac_f32_16x16x64_bf16 v[236:239], v[244:247], v[160:167], %[ix]\n\t" "ds_read_b128 v[160:163], %[l0] offset:16384\n\t" \
    "ds_read_b128 v[164:167], %[l0] offset:17408\n\t" "s_waitcnt lgkmcnt(9)\n\t" "v_smfmac_f32_16x16x64_bf16 v[232:235], v[248:251], a[120:127], %[ix]\n\t" \
    "v_smfmac_f32_16x16x64_bf16 v[236:239], v[248:251], a[128:135], %[ix]\n\t" "s_waitcnt lgkmcnt(8)\n\t" \
    "v_smfmac_f32_16x16x64_bf16 v[232:235], v[252:255], a[136:143], %[ix]\n\t" "v_smfmac_f32_16x16x64_bf16 v[236:239], v[252:255], v[168:175], %[ix]\n\t" \
    "ds_read_b128 v[168:171], %[l0] offset:18432\n\t" "ds_read_b128 v[172:175], %[l0] offset:19456\n\t" "s_waitcnt lgkmcnt(9)\n\t" \
    "v_smfmac_f32_16x16x64_bf16 v[232:235], v[16:19], a[144:151], %[ix]\n\t" "v_smfmac_f32_16x16x64_bf16 v[236:239], v[16:19], a[152:159], %[ix]\n\t" \
    "s_waitcnt lgkmcnt(8)\n\t" "v_smfmac_f32_16x16x64_bf16 v[232:235], v[20:23], a[160:167], %[ix]\n\t" \
    "v_smfmac_f32_16x16x64_bf16 v[236:239], v[20:23], v[176:183], %[ix]\n\t" "ds_read_b128 v[176:179], %[l0] offset:20480\n\t" \
    "ds_read_b128 v[180:183], %[l0] offset:21504\n\t" "s_waitcnt lgkmcnt(9)\n\t" "v_smfmac_f32_16x16x64_bf16 v[232:235], v[24:27], a[168:175], %[ix]\n\t" \
    "v_smfmac_f32_16x16x64_bf16 v[236:239], v[24:27], a[176:183], %[ix]\n\t" "s_waitcnt lgkmcnt(6)\n\t" \
    "v_smfmac_f32_16x16x64_bf16 v[232:235], v[28:31], a[184:191], %[ix]\n\t" "v_smfmac_f32_16x16x64_bf16 v[236:239], v[28:31], v[184:191], %[ix]\n\t" \
    "ds_read_b128 v[184:187], %[l0] offset:22528\n\t" "ds_read_b128 v[188:191], %[l0] offset:23552\n\t" XT(90, 91) "s_nop 4\n\t" "v_add_f32 v232, v232, v233\n\t" \
    "v_add_f32 v234, v234, v235\n\t" "v_add_f32 v236, v236, v237\n\t" "v_add_f32 v238, v238, v239\n\t" "v_cvt_pk_bf16_f32 v236, v232, v236\n\t" \
    "v_cvt_pk_bf16_f32 v237, v234, v238\n\t" "ds_write2_b32 %[vw], v236, v237 offset1:68\n\t" "s_waitcnt lgkmcnt(0)\n\t" "s_barrier\n\t" XT(92, 93) \
    "ds_read_b128 v[240:243], %[vr] offset:0\n\t" "ds_read_b128 v[244:247], %[vr] offset:64\n\t" "ds_read_b128 v[248:251], %[vr] offset:128\n\t" \
    "ds_read_b128 v[252:255], %[vr] offset:192\n\t" "s_mov_b32 s61, %[ub]\n\t" "s_waitcnt lgkmcnt(3)\n\t" \
    "v_smfmac_f32_16x16x64_bf16 v[192:195], v[240:243], v[32:39], %[ix]\n\t" "v_smfmac_f32_16x16x64_bf16 v[196:199], v[240:243], v[48:55], %[ix]\n\t" \
    "s_waitcnt lgkmcnt(2)\n\t" "v_smfmac_f32_16x16x64_bf16 v[192:195], v[244:247], v[40:47], %[ix]\n\t" \
    "v_smfmac_f32_16x16x64_bf16 v[196:199], v[244:247], v[56:63], %[ix]\n\t" "s_waitcnt lgkmcnt(1)\n\t" \
    "v_smfmac_f32_16x16x64_bf16 v[192:195], v[248:251], a[192:199], %[ix]\n\t" "v_smfmac_f32_16x16x64_bf16 v[196:199], v[248:251], a[200:207], %[ix]\n\t" \
    "s_waitcnt lgkmcnt(0)\n\t" "v_smfmac_f32_16x16x64_bf16 v[192:195], v[252:255], v[160:167], %[ix]\n\t" "ds_read_b128 v[160:163], %[l0] offset:24576\n\t" \
    "ds_read_b128 v[164:167], %[l0] offset:25600\n\t" "v_smfmac_f32_16x16x64_bf16 v[196:199], v[252:255], v[168:175], %[ix]\n\t" \
    "ds_read_b128 v[168:171], %[l0] offset:26624\n\t" "ds_read_b128 v[172:175], %[l0] offset:27648\n\t" \
    "v_smfmac_f32_16x16x64_bf16 v[200:203], v[240:243], v[64:71], %[ix]\n\t" "s_nop 1\n\t" "v_smfmac_f32_16x16x64_bf16 v[204:207], v[240:243], v[80:87], %[ix]\n\t" \
    "v_cvt_pk_bf16_f32 v224, v192, v194\n\t" "v_cvt_pk_bf16_f32 v225, v193, v195\n\t" "v_smfmac_f32_16x16x64_bf16 v[200:203], v[244:247], v[72:79], %[ix]\n\t" \
    "v_cvt_pk_bf16_f32 v226, v196, v198\n\t" "v_cvt_pk_bf16_f32 v227, v197, v199\n\t" "v_smfmac_f32_16x16x64_bf16 v[204:207], v[244:247], v[88:95], %[ix]\n\t" "s_nop 0\n\t" \
    "v_permlane32_swap_b32 v224, v226\n\t" "v_smfmac_f32_16x16x64_bf16 v[200:203], v[248:251], a[208:215], %[ix]\n\t" "v_permlane32_swap_b32 v225, v227\n\t" "s_nop 0\n\t" \
    "v_smfmac_f32_16x16x64_bf16 v[204:207], v[248:251], a[216:223], %[ix]\n\t" "v_permlane16_swap_b32 v224, v225\n\t" "v_permlane16_swap_b32 v226, v227\n\t" \
    "v_smfmac_f32_16x16x64_bf16 v[200:203], v[252:255], v[176:183], %[ix]\n\t" "ds_read_b128 v[176:179], %[l0] offset:28672\n\t" \
    "buffer_store_dwordx4 v[224:227], %[uo], %[rs], s61 offen sc1\n\t" "s_xor_b32 s61, s61, 0x40000\n\t" XT(94, 95) \
    "v_smfmac_f32_16x16x64_bf16 v[204:207], v[252:255], v[184:191], %[ix]\n\t" "ds_read_b128 v[180:183], %[l0] offset:29696\n\t" \
    "ds_read_b128 v[184:187], %[l0] offset:30720\n\t" "v_smfmac_f32_16x16x64_bf16 v[208:211], v[240:243], v[96:103], %[ix]\n\t" \
    "ds_read_b128 v[188:191], %[l0] offset:31744\n\t" "s_nop 0\n\t" "v_smfmac_f32_16x16x64_bf16 v[212:215], v[240:243], v[112:119], %[ix]\n\t" \
    "v_cvt_pk_bf16_f32 v228, v200, v202\n\t" "v_cvt_pk_bf16_f32 v229, v201, v203\n\t" "v_smfmac_f32_16x16x64_bf16 v[208:211], v[244:247], v[104:111], %[ix]\n\t" \
    "v_cvt_pk_bf16_f32 v230, v204, v206\n\t" "v_cvt_pk_bf16_f32 v231, v205, v207\n\t" "v_smfmac_f32_16x16x64_bf16 v[212:215], v[244:247], v[120:127], %[ix]\n\t" "s_nop 0\n\t" \
    "v_permlane32_swap_b32 v228, v230\n\t" "v_smfmac_f32_16x16x64_bf16 v[208:211], v[248:251], a[224:231], %[ix]\n\t" "v_permlane32_swap_b32 v229, v231\n\t" "s_nop 0\n\t" \
    "v_smfmac_f32_16x16x64_bf16 v[212:215], v[248:251], a[232:239], %[ix]\n\t" "v_permlane16_swap_b32 v228, v229\n\t" "v_permlane16_swap_b32 v230, v231\n\t" \
    "s_waitcnt lgkmcnt(6)\n\t" "v_smfmac_f32_16x16x64_bf16 v[208:211], v[252:255], v[160:167], %[ix]\n\t" "buffer_store_dwordx4 v[228:231], %[uo], %[rs], s61 offen sc1\n\t" \
    "s_xor_b32 s61, s61, 0xc0000\n\t" "s_waitcnt lgkmcnt(4)\n\t" "v_smfmac_f32_16x16x64_bf16 v[212:215], v[252:255], v[168:175], %[ix]\n\t" "s_nop 1\n\t" \
    "v_smfmac_f32_16x16x64_bf16 v[216:219], v[240:243], v[128:135], %[ix]\n\t" "s_nop 1\n\t" "v_smfmac_f32_16x16x64_bf16 v[220:223], v[240:243], v[144:151], %[ix]\n\t" \
    "v_cvt_pk_bf16_f32 v224, v208, v210\n\t" "v_cvt_pk_bf16_f32 v225, v209, v211\n\t" "v_smfmac_f32_16x16x64_bf16 v[216:219], v[244:247], v[136:143], %[ix]\n\t" \
    "v_cvt_pk_bf16_f32 v226, v212, v214\n\t" "v_cvt_pk_bf16_f32 v227, v213, v215\n\t" "v_smfmac_f32_16x16x64_bf16 v[220:223], v[244:247], v[152:159], %[ix]\n\t" "s_nop 0\n\t" \
    "v_permlane32_swap_b32 v224, v226\n\t" "v_smfmac_f32_16x16x64_bf16 v[216:219], v[248:251], a[240:247], %[ix]\n\t" "v_permlane32_swap_b32 v225, v227\n\t" "s_nop 0\n\t" \
    "v_smfmac_f32_16x16x64_bf16 v[220:223], v[248:251], a[248:255], %[ix]\n\t" "v_permlane16_swap_b32 v224, v225\n\t" "v_permlane16_swap_b32 v226, v227\n\t" \
    "s_waitcnt lgkmcnt(2)\n\t" "v_smfmac_f32_16x16x64_bf16 v[216:219], v[252:255], v[176:183], %[ix]\n\t" "buffer_store_dwordx4 v[224:227], %[uo], %[rs], s61 offen sc1\n\t" \
    "s_xor_b32 s61, s61, 0x40000\n\t" "s_waitcnt lgkmcnt(0)\n\t" "v_smfmac_f32_16x16x64_bf16 v[220:223], v[252:255], v[184:191], %[ix]\n\t" "s_nop 5\n\t" \
    "v_cvt_pk_bf16_f32 v228, v216, v218\n\t" "v_cvt_pk_bf16_f32 v229, v217, v219\n\t" "v_cvt_pk_bf16_f32 v230, v220, v222\n\t" "v_cvt_pk_bf16_f32 v231, v221, v223\n\t" \
    "s_nop 0\n\t" "v_permlane32_swap_b32 v228, v230\n\t" "v_permlane32_swap_b32 v229, v231\n\t" "s_nop 1\n\t" "v_permlane16_swap_b32 v228, v229\n\t" \
    "v_permlane16_swap_b32 v230, v231\n\t" "buffer_store_dwordx4 v[228:231], %[uo], %[rs], s61 offen sc1\n\t" "s_nop 1\n\t" XT(96, 97) "v_mov_b64 v[16:17], -1\n\t" \
    "v_mov_b64 v[18:19], -1\n\t" "s_barrier\n\t" "buffer_store_dwordx4 v[16:19], %[l1], %[rs], %[ur] offen offset:0 sc1\n\t" \
    "buffer_store_dwordx4 v[16:19], %[l1], %[rs], %[ur] offen offset:1024 sc1\n\t" "buffer_store_dwordx4 v[16:19], %[l1], %[rs], %[ur] offen offset:2048 sc1\n\t" \
    "buffer_store_dwordx4 v[16:19], %[l1], %[rs], %[ur] offen offset:3072 sc1\n\t" "s_sub_u32 s61, %[ur], 0x4000\n\t" \
    "buffer_store_dwordx4 v[16:19], %[l1], %[rs], s61 offen offset:2048 sc1\n\t" "s_branch 99f\n\t" "31:\n\t" "ds_read_b128 v[228:231], %[l1] offset:8192\n\t" \
    "s_waitcnt lgkmcnt(0)\n\t" "v_max3_u32 v225, v228, v229, v230\n\t" "v_max_u32 v225, v225, v231\n\t" "ds_read_b128 v[228:231], %[l1] offset:9216\n\t" \
    "s_waitcnt lgkmcnt(0)\n\t" "v_max3_u32 v225, v225, v228, v229\n\t" "v_max3_u32 v225, v225, v230, v231\n\t" "ds_read_b128 v[228:231], %[l1] offset:10240\n\t" \
    "s_waitcnt lgkmcnt(0)\n\t" "v_max3_u32 v225, v225, v228, v229\n\t" "v_max3_u32 v225, v225, v230, v231\n\t" "ds_read_b128 v[228:231], %[l1] offset:11264\n\t" \
    "s_waitcnt lgkmcnt(0)\n\t" "v_max3_u32 v225, v225, v228, v229\n\t" "v_max3_u32 v225, v225, v230, v231\n\t" "v_cmp_eq_u32 vcc, -1, v225\n\t" "s_cbranch_vccz 32b\n\t" \
    "s_cmp_eq_u32 %[rt], 1\n\t" "s_cbranch_scc1 32b\n\t" "v_mov_b32 v225, %[rt]\n\t" "ds_write_b32 v227, v225\n\t" "s_waitcnt lgkmcnt(0)\n\t" "s_branch 32b\n\t" "33:\n\t" \
    "s_sub_u32 %[rt], %[rt], 1\n\t" "s_waitcnt lgkmcnt(0)\n\t" XCNT(100) "34:\n\t" "buffer_load_dwordx4 v[240:243], %[mo], %[rs], %[sb] offen sc1\n\t" \
    "buffer_load_dwordx4 v[244:247], v224, %[rs], %[sb] offen sc1\n\t" "buffer_load_dwordx4 v[248:251], %[mo], %[rs], s60 offen sc1\n\t" \
    "buffer_load_dwordx4 v[252:255], v224, %[rs], s60 offen sc1\n\t" "s_waitcnt vmcnt(0)\n\t" "v_max_u32 v225, v240, v241\n\t" "v_max3_u32 v225, v225, v242, v243\n\t" \
    "v_max3_u32 v225, v225, v244, v245\n\t" "v_max3_u32 v225, v225, v246, v247\n\t" "v_max3_u32 v225, v225, v248, v249\n\t" "v_max3_u32 v225, v225, v250, v251\n\t" \
    "v_max3_u32 v225, v225, v252, v253\n\t" "v_max3_u32 v225, v225, v254, v255\n\t" "v_cmp_eq_u32 vcc, -1, v225\n\t" "s_cbranch_vccz 35f\n\t" "s_cmp_eq_u32 %[bd], 0\n\t" \
    "s_cbranch_scc1 35f\n\t" "s_sub_u32 %[bd], %[bd], 1\n\t" XCNT(101) "s_branch 34b\n\t" "35:\n\t" "ds_read_b128 v[160:163], %[l0] offset:0\n\t" \
    "ds_read_b128 v[164:167], %[l0] offset:1024\n\t" "ds_read_b128 v[168:171], %[l0] offset:2048\n\t" "ds_read_b128 v[172:175], %[l0] offset:3072\n\t" \
    "ds_read_b128 v[176:179], %[l0] offset:4096\n\t" "ds_read_b128 v[180:183], %[l0] offset:5120\n\t" "ds_read_b128 v[184:187], %[l0] offset:6144\n\t" \
    "ds_read_b128 v[188:191], %[l0] offset:7168\n\t" "v_mov_b64 v[232:233], 0\n\t" "v_mov_b64 v[234:235], 0\n\t" "v_mov_b64 v[236:237], 0\n\t" "v_mov_b64 v[238:239], 0\n\t" \
    "s_barrier\n\t" "s_branch 30b\n\t" "93:\n\t" XT(80, 81) "s_barrier\n\t" XT(82, 83) "buffer_load_dwordx4 v[240:243], %[mo], %[rs], %[sb] offen sc1\n\t" \
    "v_mov_b64 v[232:233], 0\n\t" "v_mov_b64 v[234:235], 0\n\t" "v_mov_b64 v[236:237], 0\n\t" "v_mov_b64 v[238:239], 0\n\t" "v_mov_b64 v[192:193], 0\n\t" \
    "v_mov_b64 v[194:195], 0\n\t" "v_mov_b64 v[196:197], 0\n\t" "v_mov_b64 v[198:199], 0\n\t" "buffer_load_dwordx4 v[244:247], v224, %[rs], %[sb] offen sc1\n\t" \
    "ds_read_b128 v[160:163], %[l0] offset:0\n\t" "ds_read_b128 v[164:167], %[l0] offset:1024\n\t" "ds_read_b128 v[168:171], %[l0] offset:2048\n\t" \
    "ds_read_b128 v[172:175], %[l0] offset:3072\n\t" "s_nop 3\n\t" "buffer_load_dwordx4 v[248:251], %[mo], %[rs], s60 offen sc1\n\t" \
    "ds_read_b128 v[176:179], %[l0] offset:4096\n\t" "ds_read_b128 v[180:183], %[l0] offset:5120\n\t" "ds_read_b128 v[184:187], %[l0] offset:6144\n\t" \
    "ds_read_b128 v[188:191], %[l0] offset:7168\n\t" "s_nop 3\n\t" "buffer_load_dwordx4 v[252:255], v224, %[rs], s60 offen sc1\n\t" "v_mov_b64 v[200:201], 0\n\t" \
    "v_mov_b64 v[202:203], 0\n\t" "v_mov_b64 v[204:205], 0\n\t" "v_mov_b64 v[206:207], 0\n\t" "v_mov_b64 v[208:209], 0\n\t" "v_mov_b64 v[210:211], 0\n\t" \
    "v_mov_b64 v[212:213], 0\n\t" "v_mov_b64 v[214:215], 0\n\t" "v_mov_b64 v[216:217], 0\n\t" "v_mov_b64 v[218:219], 0\n\t" "v_mov_b64 v[220:221], 0\n\t" \
    "v_mov_b64 v[222:223], 0\n\t" "v_mov_b32 v227, 160512\n\t" "40:\n\t" "s_waitcnt vmcnt(3)\n\t" XT(84, 85) \
    "v_smfmac_f32_16x16x64_bf16 v[232:235], v[240:243], a[0:7], %[ix]\n\t" "ds_write_b128 %[l1], v[240:243] offset:12288\n\t" \
    "v_smfmac_f32_16x16x64_bf16 v[236:239], v[240:243], a[8:15], %[ix]\n\t" "s_waitcnt vmcnt(2)\n\t" "v_smfmac_f32_16x16x64_bf16 v[232:235], v[244:247], a[16:23], %[ix]\n\t" \
    "ds_write_b128 %[l1], v[244:247] offset:13312\n\t" "s_waitcnt lgkmcnt(8)\n\t" "v_smfmac_f32_16x16x64_bf16 v[236:239], v[244:247], v[160:167], %[ix]\n\t" \
    "s_waitcnt lgkmcnt(0)\n\t" "s_barrier\n\t" XT(86, 87) "ds_read_b128 v[16:19], %[l1] offset:0\n\t" "ds_read_b128 v[20:23], %[l1] offset:4096\n\t" \
    "ds_read_b128 v[24:27], %[l1] offset:8192\n\t" "s_waitcnt vmcnt(1)\n\t" "v_smfmac_f32_16x16x64_bf16 v[232:235], v[248:251], a[24:31], %[ix]\n\t" \
    "ds_write_b128 %[l1], v[248:251] offset:14336\n\t" "v_smfmac_f32_16x16x64_bf16 v[236:239], v[248:251], a[32:39], %[ix]\n\t" "ds_read_b128 v[28:31], %[l1] offset:1024\n\t" \
    "ds_read_b128 v[240:243], %[l1] offset:5120\n\t" "ds_read_b128 v[244:247], %[l1] offset:9216\n\t" "s_waitcnt vmcnt(0)\n\t" \
    "v_smfmac_f32_16x16x64_bf16 v[232:235], v[252:255], a[40:47], %[ix]\n\t" "ds_write_b128 %[l1], v[252:255] offset:15360\n\t" \
    "v_smfmac_f32_16x16x64_bf16 v[236:239], v[252:255], v[168:175], %[ix]\n\t" "ds_read_b128 v[160:163], %[l0] offset:8192\n\t" \
    "ds_read_b128 v[164:167], %[l0] offset:9216\n\t" "ds_read_b128 v[168:171], %[l0] offset:10240\n\t" "ds_read_b128 v[172:175], %[l0] offset:11264\n\t" "s_nop 1\n\t" \
    "v_cmp_u_f32 vcc, v232, v233\n\t" "v_cmp_u_f32 s[62:63], v234, v235\n\t" "s_waitcnt lgkmcnt(11)\n\t" \
    "v_smfmac_f32_16x16x64_bf16 v[232:235], v[16:19], a[48:55], %[ix]\n\t" "v_smfmac_f32_16x16x64_bf16 v[236:239], v[16:19], a[56:63], %[ix]\n\t" \
    "s_or_b64 vcc, vcc, s[62:63]\n\t" "s_cbranch_scc1 41f\n\t" "42:\n\t" "s_waitcnt lgkmcnt(10)\n\t" "v_smfmac_f32_16x16x64_bf16 v[232:235], v[20:23], a[64:71], %[ix]\n\t" \
    "v_smfmac_f32_16x16x64_bf16 v[236:239], v[20:23], v[176:183], %[ix]\n\t" "s_waitcnt lgkmcnt(4)\n\t" "s_barrier\n\t" XT(88, 89) "ds_read_b32 v225, v227\n\t" \
    "v_smfmac_f32_16x16x64_bf16 v[232:235], v[24:27], a[72:79], %[ix]\n\t" "ds_read_b128 v[176:179], %[l0] offset:12288\n\t" "ds_read_b128 v[180:183], %[l0] offset:13312\n\t" \
    "v_smfmac_f32_16x16x64_bf16 v[236:239], v[24:27], a[80:87], %[ix]\n\t" "ds_read_b128 v[248:251], %[l1] offset:2048\n\t" "ds_read_b128 v[252:255], %[l1] offset:6144\n\t" \
    "v_smfmac_f32_16x16x64_bf16 v[232:235], v[28:31], a[88:95], %[ix]\n\t" "ds_read_b128 v[16:19], %[l1] offset:10240\n\t" "ds_read_b128 v[20:23], %[l1] offset:3072\n\t" \
    "v_smfmac_f32_16x16x64_bf16 v[236:239], v[28:31], v[184:191], %[ix]\n\t" "ds_read_b128 v[24:27], %[l1] offset:7168\n\t" "ds_read_b128 v[184:187], %[l0] offset:14336\n\t" \
    "v_smfmac_f32_16x16x64_bf16 v[232:235], v[240:243], a[96:103], %[ix]\n\t" "ds_read_b128 v[188:191], %[l0] offset:15360\n\t" \
    "ds_read_b128 v[28:31], %[l1] offset:11264\n\t" "v_smfmac_f32_16x16x64_bf16 v[236:239], v[240:243], a[104:111], %[ix]\n\t" "s_waitcnt lgkmcnt(10)\n\t" \
    "v_cmp_eq_u32 vcc, %[rt], v225\n\t" "v_smfmac_f32_16x16x64_bf16 v[232:235], v[244:247], a[112:119], %[ix]\n\t" "s_cbranch_vccnz 43f\n\t" \
    "v_smfmac_f32_16x16x64_bf16 v[236:239], v[244:247], v[160:167], %[ix]\n\t" "ds_read_b128 v[160:163], %[l0] offset:16384\n\t" \
    "ds_read_b128 v[164:167], %[l0] offset:17408\n\t" "s_waitcnt lgkmcnt(9)\n\t" "v_smfmac_f32_16x16x64_bf16 v[232:235], v[248:251], a[120:127], %[ix]\n\t" \
    "v_smfmac_f32_16x16x64_bf16 v[236:239], v[248:251], a[128:135], %[ix]\n\t" "s_waitcnt lgkmcnt(8)\n\t" \
    "v_smfmac_f32_16x16x64_bf16 v[232:235], v[252:255], a[136:143], %[ix]\n\t" "v_smfmac_f32_16x16x64_bf16 v[236:239], v[252:255], v[168:175], %[ix]\n\t" \
    "ds_read_b128 v[168:171], %[l0] offset:18432\n\t" "ds_read_b128 v[172:175], %[l0] offset:19456\n\t" "s_waitcnt lgkmcnt(9)\n\t" \
    "v_smfmac_f32_16x16x64_bf16 v[232:235], v[16:19], a[144:151], %[ix]\n\t" "v_smfmac_f32_16x16x64_bf16 v[236:239], v[16:19], a[152:159], %[ix]\n\t" \
    "s_waitcnt lgkmcnt(8)\n\t" "v_smfmac_f32_16x16x64_bf16 v[232:235], v[20:23], a[160:167], %[ix]\n\t" \
    "v_smfmac_f32_16x16x64_bf16 v[236:239], v[20:23], v[176:183], %[ix]\n\t" "ds_read_b128 v[176:179], %[l0] offset:20480\n\t" \
    "ds_read_b128 v[180:183], %[l0] offset:21504\n\t" "s_waitcnt lgkmcnt(9)\n\t" "v_smfmac_f32_16x16x64_bf16 v[232:235], v[24:27], a[168:175], %[ix]\n\t" \
    "v_smfmac_f32_16x16x64_bf16 v[236:239], v[24:27], a[176:183], %[ix]\n\t" "s_waitcnt lgkmcnt(6)\n\t" \
    "v_smfmac_f32_16x16x64_bf16 v[232:235], v[28:31], a[184:191], %[ix]\n\t" "v_smfmac_f32_16x16x64_bf16 v[236:239], v[28:31], v[184:191], %[ix]\n\t" \
    "ds_read_b128 v[184:187], %[l0] offset:22528\n\t" "ds_read_b128 v[188:191], %[l0] offset:23552\n\t" XT(90, 91) "s_nop 4\n\t" "v_add_f32 v232, v232, v233\n\t" \
    "v_add_f32 v234, v234, v235\n\t" "v_add_f32 v236, v236, v237\n\t" "v_add_f32 v238, v238, v239\n\t" "v_cvt_pk_bf16_f32 v236, v232, v236\n\t" \
    "v_cvt_pk_bf16_f32 v237, v234, v238\n\t" "ds_write2_b32 %[vw], v236, v237 offset1:68\n\t" "s_waitcnt lgkmcnt(0)\n\t" "s_barrier\n\t" XT(92, 93) \
    "ds_read_b128 v[240:243], %[vr] offset:0\n\t" "ds_read_b128 v[244:247], %[vr] offset:64\n\t" "ds_read_b128 v[248:251], %[vr] offset:128\n\t" \
    "ds_read_b128 v[252:255], %[vr] offset:192\n\t" "s_mov_b32 s61, %[ub]\n\t" "s_waitcnt lgkmcnt(3)\n\t" \
    "v_smfmac_f32_16x16x64_bf16 v[192:195], v[240:243], v[32:39], %[ix]\n\t" "v_smfmac_f32_16x16x64_bf16 v[196:199], v[240:243], v[48:55], %[ix]\n\t" \
    "s_waitcnt lgkmcnt(2)\n\t" "v_smfmac_f32_16x16x64_bf16 v[192:195], v[244:247], v[40:47], %[ix]\n\t" \
    "v_smfmac_f32_16x16x64_bf16 v[196:199], v[244:247], v[56:63], %[ix]\n\t" "s_waitcnt lgkmcnt(1)\n\t" \
    "v_smfmac_f32_16x16x64_bf16 v[192:195], v[248:251], a[192:199], %[ix]\n\t" "v_smfmac_f32_16x16x64_bf16 v[196:199], v[248:251], a[200:207], %[ix]\n\t" \
    "s_waitcnt lgkmcnt(0)\n\t" "v_smfmac_f32_16x16x64_bf16 v[192:195], v[252:255], v[160:167], %[ix]\n\t" "ds_read_b128 v[160:163], %[l0] offset:24576\n\t" \
    "ds_read_b128 v[164:167], %[l0] offset:25600\n\t" "v_smfmac_f32_16x16x64_bf16 v[196:199], v[252:255], v[168:175], %[ix]\n\t" \
    "ds_read_b128 v[168:171], %[l0] offset:26624\n\t" "ds_read_b128 v[172:175], %[l0] offset:27648\n\t" \
    "v_smfmac_f32_16x16x64_bf16 v[200:203], v[240:243], v[64:71], %[ix]\n\t" "s_nop 1\n\t" "v_smfmac_f32_16x16x64_bf16 v[204:207], v[240:243], v[80:87], %[ix]\n\t" \
    "v_cvt_pk_bf16_f32 v224, v192, v194\n\t" "v_cvt_pk_bf16_f32 v225, v193, v195\n\t" "v_smfmac_f32_16x16x64_bf16 v[200:203], v[244:247], v[72:79], %[ix]\n\t" \
    "v_cvt_pk_bf16_f32 v226, v196, v198\n\t" "v_cvt_pk_bf16_f32 v227, v197, v199\n\t" "v_smfmac_f32_16x16x64_bf16 v[204:207], v[244:247], v[88:95], %[ix]\n\t" "s_nop 0\n\t" \
    "v_permlane32_swap_b32 v224, v226\n\t" "v_smfmac_f32_16x16x64_bf16 v[200:203], v[248:251], a[208:215], %[ix]\n\t" "v_permlane32_swap_b32 v225, v227\n\t" "s_nop 0\n\t" \
    "v_smfmac_f32_16x16x64_bf16 v[204:207], v[248:251], a[216:223], %[ix]\n\t" "v_permlane16_swap_b32 v224, v225\n\t" "v_permlane16_swap_b32 v226, v227\n\t" \
    "v_smfmac_f32_16x16x64_bf16 v[200:203], v[252:255], v[176:183], %[ix]\n\t" "ds_read_b128 v[176:179], %[l0] offset:28672\n\t" \
    "buffer_store_dwordx4 v[224:227], %[uo], %[rs], s61 offen sc1\n\t" "s_xor_b32 s61, s61, 0x40000\n\t" XT(94, 95) \
    "v_smfmac_f32_16x16x64_bf16 v[204:207], v[252:255], v[184:191], %[ix]\n\t" "ds_read_b128 v[180:183], %[l0] offset:29696\n\t" \
    "ds_read_b128 v[184:187], %[l0] offset:30720\n\t" "v_smfmac_f32_16x16x64_bf16 v[208:211], v[240:243], v[96:103], %[ix]\n\t" \
    "ds_read_b128 v[188:191], %[l0] offset:31744\n\t" "s_nop 0\n\t" "v_smfmac_f32_16x16x64_bf16 v[212:215], v[240:243], v[112:119], %[ix]\n\t" \
    "v_cvt_pk_bf16_f32 v228, v200, v202\n\t" "v_cvt_pk_bf16_f32 v229, v201, v203\n\t" "v_smfmac_f32_16x16x64_bf16 v[208:211], v[244:247], v[104:111], %[ix]\n\t" \
    "v_cvt_pk_bf16_f32 v230, v204, v206\n\t" "v_cvt_pk_bf16_f32 v231, v205, v207\n\t" "v_smfmac_f32_16x16x64_bf16 v[212:215], v[244:247], v[120:127], %[ix]\n\t" "s_nop 0\n\t" \
    "v_permlane32_swap_b32 v228, v230\n\t" "v_smfmac_f32_16x16x64_bf16 v[208:211], v[248:251], a[224:231], %[ix]\n\t" "v_permlane32_swap_b32 v229, v231\n\t" "s_nop 0\n\t" \
    "v_smfmac_f32_16x16x64_bf16 v[212:215], v[248:251], a[232:239], %[ix]\n\t" "v_permlane16_swap_b32 v228, v229\n\t" "v_permlane16_swap_b32 v230, v231\n\t" \
    "s_waitcnt lgkmcnt(6)\n\t" "v_smfmac_f32_16x16x64_bf16 v[208:211], v[252:255], v[160:167], %[ix]\n\t" "buffer_store_dwordx4 v[228:231], %[uo], %[rs], s61 offen sc1\n\t" \
    "s_xor_b32 s61, s61, 0xc0000\n\t" "s_waitcnt lgkmcnt(4)\n\t" "v_smfmac_f32_16x16x64_bf16 v[212:215], v[252:255], v[168:175], %[ix]\n\t" "s_nop 1\n\t" \
    "v_smfmac_f32_16x16x64_bf16 v[216:219], v[240:243], v[128:135], %[ix]\n\t" "s_nop 1\n\t" "v_smfmac_f32_16x16x64_bf16 v[220:223], v[240:243], v[144:151], %[ix]\n\t" \
    "v_cvt_pk_bf16_f32 v224, v208, v210\n\t" "v_cvt_pk_bf16_f32 v225, v209, v211\n\t" "v_smfmac_f32_16x16x64_bf16 v[216:219], v[244:247], v[136:143], %[ix]\n\t" \
    "v_cvt_pk_bf16_f32 v226, v212, v214\n\t" "v_cvt_pk_bf16_f32 v227, v213, v215\n\t" "v_smfmac_f32_16x16x64_bf16 v[220:223], v[244:247], v[152:159], %[ix]\n\t" "s_nop 0\n\t" \
    "v_permlane32_swap_b32 v224, v226\n\t" "v_smfmac_f32_16x16x64_bf16 v[216:219], v[248:251], a[240:247], %[ix]\n\t" "v_permlane32_swap_b32 v225, v227\n\t" "s_nop 0\n\t" \
    "v_smfmac_f32_16x16x64_bf16 v[220:223], v[248:251], a[248:255], %[ix]\n\t" "v_permlane16_swap_b32 v224, v225\n\t" "v_permlane16_swap_b32 v226, v227\n\t" \
    "s_waitcnt lgkmcnt(2)\n\t" "v_smfmac_f32_16x16x64_bf16 v[216:219], v[252:255], v[176:183], %[ix]\n\t" "buffer_store_dwordx4 v[224:227], %[uo], %[rs], s61 offen sc1\n\t" \
    "s_xor_b32 s61, s61, 0x40000\n\t" "s_waitcnt lgkmcnt(0)\n\t" "v_smfmac_f32_16x16x64_bf16 v[220:223], v[252:255], v[184:191], %[ix]\n\t" "s_nop 5\n\t" \
    "v_cvt_pk_bf16_f32 v228, v216, v218\n\t" "v_cvt_pk_bf16_f32 v229, v217, v219\n\t" "v_cvt_pk_bf16_f32 v230, v220, v222\n\t" "v_cvt_pk_bf16_f32 v231, v221, v223\n\t" \
    "s_nop 0\n\t" "v_permlane32_swap_b32 v228, v230\n\t" "v_permlane32_swap_b32 v229, v231\n\t" "s_nop 1\n\t" "v_permlane16_swap_b32 v228, v229\n\t" \
    "v_permlane16_swap_b32 v230, v231\n\t" "buffer_store_dwordx4 v[228:231], %[uo], %[rs], s61 offen sc1\n\t" "s_nop 1\n\t" XT(96, 97) "v_mov_b64 v[16:17], -1\n\t" \
    "v_mov_b64 v[18:19], -1\n\t" "s_barrier\n\t" "buffer_store_dwordx4 v[16:19], %[l1], %[rs], %[ur] offen offset:0 sc1\n\t" \
    "buffer_store_dwordx4 v[16:19], %[l1], %[rs], %[ur] offen offset:1024 sc1\n\t" "buffer_store_dwordx4 v[16:19], %[l1], %[rs], %[ur] offen offset:2048 sc1\n\t" \
    "buffer_store_dwordx4 v[16:19], %[l1], %[rs], %[ur] offen offset:3072 sc1\n\t" "s_sub_u32 s61, %[ur], 0x5000\n\t" \
    "buffer_store_dwordx4 v[16:19], %[l1], %[rs], s61 offen offset:3072 sc1\n\t" "s_branch 99f\n\t" "41:\n\t" "ds_read_b128 v[228:231], %[l1] offset:12288\n\t" \
    "s_waitcnt lgkmcnt(0)\n\t" "v_max3_u32 v225, v228, v229, v230\n\t" "v_max_u32 v225, v225, v231\n\t" "ds_read_b128 v[228:231], %[l1] offset:13312\n\t" \
    "s_waitcnt lgkmcnt(0)\n\t" "v_max3_u32 v225, v225, v228, v229\n\t" "v_max3_u32 v225, v225, v230, v231\n\t" "ds_read_b128 v[228:231], %[l1] offset:14336\n\t" \
    "s_waitcnt lgkmcnt(0)\n\t" "v_max3_u32 v225, v225, v228, v229\n\t" "v_max3_u32 v225, v225, v230, v231\n\t" "ds_read_b128 v[228:231], %[l1] offset:15360\n\t" \
    "s_waitcnt lgkmcnt(0)\n\t" "v_max3_u32 v225, v225, v228, v229\n\t" "v_max3_u32 v225, v225, v230, v231\n\t" "v_cmp_eq_u32 vcc, -1, v225\n\t" "s_cbranch_vccz 42b\n\t" \
    "s_cmp_eq_u32 %[rt], 1\n\t" "s_cbranch_scc1 42b\n\t" "v_mov_b32 v225, %[rt]\n\t" "ds_write_b32 v227, v225\n\t" "s_waitcnt lgkmcnt(0)\n\t" "s_branch 42b\n\t" "43:\n\t" \
    "s_sub_u32 %[rt], %[rt], 1\n\t" "s_waitcnt lgkmcnt(0)\n\t" XCNT(100) "44:\n\t" "buffer_load_dwordx4 v[240:243], %[mo], %[rs], %[sb] offen sc1\n\t" \
    "buffer_load_dwordx4 v[244:247], v224, %[rs], %[sb] offen sc1\n\t" "buffer_load_dwordx4 v[248:251], %[mo], %[rs], s60 offen sc1\n\t" \
    "buffer_load_dwordx4 v[252:255], v224, %[rs], s60 offen sc1\n\t" "s_waitcnt vmcnt(0)\n\t" "v_max_u32 v225, v240, v241\n\t" "v_max3_u32 v225, v225, v242, v243\n\t" \
    "v_max3_u32 v225, v225, v244, v245\n\t" "v_max3_u32 v225, v225, v246, v247\n\t" "v_max3_u32 v225, v225, v248, v249\n\t" "v_max3_u32 v225, v225, v250, v251\n\t" \
    "v_max3_u32 v225, v225, v252, v253\n\t" "v_max3_u32 v225, v225, v254, v255\n\t" "v_cmp_eq_u32 vcc, -1, v225\n\t" "s_cbranch_vccz 45f\n\t" "s_cmp_eq_u32 %[bd], 0\n\t" \
    "s_cbranch_scc1 45f\n\t" "s_sub_u32 %[bd], %[bd], 1\n\t" XCNT(101) "s_branch 44b\n\t" "45:\n\t" "ds_read_b128 v[160:163], %[l0] offset:0\n\t" \
    "ds_read_b128 v[164:167], %[l0] offset:1024\n\t" "ds_read_b128 v[168:171], %[l0] offset:2048\n\t" "ds_read_b128 v[172:175], %[l0] offset:3072\n\t" \
    "ds_read_b128 v[176:179], %[l0] offset:4096\n\t" "ds_read_b128 v[180:183], %[l0] offset:5120\n\t" "ds_read_b128 v[184:187], %[l0] offset:6144\n\t" \
    "ds_read_b128 v[188:191], %[l0] offset:7168\n\t" "v_mov_b64 v[232:233], 0\n\t" "v_mov_b64 v[234:235], 0\n\t" "v_mov_b64 v[236:237], 0\n\t" "v_mov_b64 v[238:239], 0\n\t" \
    "s_barrier\n\t" "s_branch 40b\n\t" "99:\n\t"
#define Y_STEP \
    ".p2align 6\n\t" "v_xor_b32 v128, 0, %[u0]\n\t" "v_xor_b32 v132, 0, %[pm]\n\t" "v_xor_b32 v129, 2048, %[u0]\n\t" "v_xor_b32 v133, 1024, %[pm]\n\t" \
    "v_xor_b32 v130, 4096, %[u0]\n\t" "v_xor_b32 v134, 2048, %[pm]\n\t" "v_xor_b32 v131, 6144, %[u0]\n\t" "v_xor_b32 v135, 3072, %[pm]\n\t" "s_getpc_b64 s[62:63]\n\t" \
    "11:\n\t" "s_memtime s[64:65]\n\t" "s_waitcnt lgkmcnt(0)\n\t" "s_sub_u32 s61, %[tu], s64\n\t" "s_cmp_gt_i32 s61, 124\n\t" "s_cbranch_scc1 11b\n\t" \
    "s_max_i32 s61, s61, 0\n\t" "s_and_b32 s61, s61, 124\n\t" "s_sub_u32 s61, 12f-11b, s61\n\t" "s_add_u32 s62, s62, s61\n\t" "s_addc_u32 s63, s63, 0\n\t" \
    "s_setpc_b64 s[62:63]\n\t" ".rept 31\n\t" "s_nop 0\n\t" ".endr\n\t" "12:\n\t" XT(90, 91) "buffer_load_dwordx4 v[144:147], v128, %[rs], %[ub] offen offset:0 sc1\n\t" \
    "buffer_load_dwordx4 v[148:151], v128, %[rs], %[ub] offen offset:1024 sc1\n\t" "buffer_load_dwordx4 v[152:155], v129, %[rs], %[ub] offen offset:0 sc1\n\t" \
    "buffer_load_dwordx4 v[156:159], v129, %[rs], %[ub] offen offset:1024 sc1\n\t" "s_cmp_lg_u32 %[late], 0\n\t" "s_cbranch_scc1 14f\n\t" "s_getpc_b64 s[62:63]\n\t" "13:\n\t" \
    "s_memtime s[64:65]\n\t" "s_waitcnt lgkmcnt(0)\n\t" "s_sub_u32 s61, %[tg], s64\n\t" "s_cmp_gt_i32 s61, 124\n\t" "s_cbranch_scc1 13b\n\t" "s_max_i32 s61, s61, 0\n\t" \
    "s_and_b32 s61, s61, 124\n\t" "s_sub_u32 s61, 14f-13b, s61\n\t" "s_add_u32 s62, s62, s61\n\t" "s_addc_u32 s63, s63, 0\n\t" "s_setpc_b64 s[62:63]\n\t" ".rept 31\n\t" \
    "s_nop 0\n\t" ".endr\n\t" "14:\n\t" XT(92, 93) "20:\n\t" "buffer_load_dwordx4 v[160:163], v130, %[rs], %[ub] offen offset:0 sc1\n\t" \
    "buffer_load_dwordx4 v[164:167], v130, %[rs], %[ub] offen offset:1024 sc1\n\t" "buffer_load_dwordx4 v[192:195], %[x0], %[rs], %[pb] offen offset:0 sc1\n\t" \
    "buffer_load_dwordx4 v[196:199], %[x0], %[rs], %[pb] offen offset:1024 sc1\n\t" "buffer_load_dwordx4 v[200:203], %[x0], %[rs], %[pb] offen offset:2048 sc1\n\t" \
    "buffer_load_dwordx4 v[204:207], %[x0], %[rs], %[pb] offen offset:3072 sc1\n\t" "buffer_load_dwordx4 v[176:179], v132, %[rs], %[pb] offen sc1\n\t" \
    "v_mov_b64 v[120:121], 0\n\t" "v_mov_b64 v[122:123], 0\n\t" "v_mov_b64 v[136:137], 0\n\t" "v_mov_b64 v[138:139], 0\n\t" \
    "buffer_load_dwordx4 v[208:211], %[x1], %[rs], %[pb] offen offset:0 sc1\n\t" "buffer_load_dwordx4 v[212:215], %[x1], %[rs], %[pb] offen offset:1024 sc1\n\t" \
    "buffer_load_dwordx4 v[216:219], %[x1], %[rs], %[pb] offen offset:2048 sc1\n\t" "buffer_load_dwordx4 v[220:223], %[x1], %[rs], %[pb] offen offset:3072 sc1\n\t" \
    "buffer_load_dwordx4 v[180:183], v133, %[rs], %[pb] offen sc1\n\t" "buffer_load_dwordx4 v[224:227], %[x2], %[rs], %[pb] offen offset:0 sc1\n\t" \
    "buffer_load_dwordx4 v[228:231], %[x2], %[rs], %[pb] offen offset:1024 sc1\n\t" "buffer_load_dwordx4 v[232:235], %[x2], %[rs], %[pb] offen offset:2048 sc1\n\t" \
    "buffer_load_dwordx4 v[236:239], %[x2], %[rs], %[pb] offen offset:3072 sc1\n\t" "buffer_load_dwordx4 v[184:187], v134, %[rs], %[pb] offen sc1\n\t" \
    "buffer_load_dwordx4 v[168:171], v131, %[rs], %[ub] offen offset:0 sc1\n\t" "buffer_load_dwordx4 v[172:175], v131, %[rs], %[ub] offen offset:1024 sc1\n\t" \
    "buffer_load_dwordx4 v[240:243], %[x3], %[rs], %[pb] offen offset:0 sc1\n\t" "buffer_load_dwordx4 v[244:247], %[x3], %[rs], %[pb] offen offset:1024 sc1\n\t" \
    "buffer_load_dwordx4 v[248:251], %[x3], %[rs], %[pb] offen offset:2048 sc1\n\t" "buffer_load_dwordx4 v[252:255], %[x3], %[rs], %[pb] offen offset:3072 sc1\n\t" \
    "buffer_load_dwordx4 v[188:191], v135, %[rs], %[pb] offen sc1\n\t" "buffer_load_dword %[sv], %[so], %[rs], %[pb] offen sc1\n\t" "v_mov_b32 v127, -1\n\t" \
    "s_waitcnt vmcnt(25)\n\t" "v_permlane16_swap_b32 v144, v145\n\t" "v_permlane16_swap_b32 v146, v147\n\t" "v_permlane16_swap_b32 v148, v149\n\t" \
    "v_permlane16_swap_b32 v150, v151\n\t" "v_permlane16_swap_b32 v152, v153\n\t" "v_permlane16_swap_b32 v154, v155\n\t" "v_permlane16_swap_b32 v156, v157\n\t" \
    "v_permlane16_swap_b32 v158, v159\n\t" "s_waitcnt vmcnt(22)\n\t" XT(94, 95) "v_smfmac_f32_16x16x64_bf16 v[136:139], v[192:195], a[0:7], %[ix]\n\t" \
    "s_waitcnt vmcnt(21)\n\t" "v_smfmac_f32_16x16x64_bf16 v[120:123], v[196:199], a[8:15], %[ix]\n\t" "v_permlane16_swap_b32 v160, v161\n\t" "s_waitcnt vmcnt(20)\n\t" \
    "v_smfmac_f32_16x16x64_bf16 v[136:139], v[200:203], a[16:23], %[ix]\n\t" "s_waitcnt vmcnt(19)\n\t" \
    "v_smfmac_f32_16x16x64_bf16 v[120:123], v[204:207], a[24:31], %[ix]\n\t" "v_permlane16_swap_b32 v162, v163\n\t" "s_waitcnt vmcnt(18)\n\t" \
    "v_smfmac_f32_16x16x64_bf16 v[136:139], v[176:179], v[144:151], %[ix]\n\t" "s_waitcnt vmcnt(17)\n\t" \
    "v_smfmac_f32_16x16x64_bf16 v[120:123], v[208:211], a[32:39], %[ix]\n\t" "s_waitcnt vmcnt(16)\n\t" \
    "v_smfmac_f32_16x16x64_bf16 v[136:139], v[212:215], a[40:47], %[ix]\n\t" "v_permlane16_swap_b32 v164, v165\n\t" "s_waitcnt vmcnt(15)\n\t" \
    "v_smfmac_f32_16x16x64_bf16 v[120:123], v[216:219], a[48:55], %[ix]\n\t" "s_waitcnt vmcnt(14)\n\t" \
    "v_smfmac_f32_16x16x64_bf16 v[136:139], v[220:223], a[56:63], %[ix]\n\t" "v_permlane16_swap_b32 v166, v167\n\t" "s_waitcnt vmcnt(13)\n\t" \
    "v_smfmac_f32_16x16x64_bf16 v[120:123], v[180:183], v[152:159], %[ix]\n\t" "s_waitcnt vmcnt(12)\n\t" \
    "v_smfmac_f32_16x16x64_bf16 v[136:139], v[224:227], a[64:71], %[ix]\n\t" "s_waitcnt vmcnt(11)\n\t" \
    "v_smfmac_f32_16x16x64_bf16 v[120:123], v[228:231], a[72:79], %[ix]\n\t" "s_waitcnt vmcnt(10)\n\t" \
    "v_smfmac_f32_16x16x64_bf16 v[136:139], v[232:235], a[80:87], %[ix]\n\t" "s_waitcnt vmcnt(9)\n\t" "v_smfmac_f32_16x16x64_bf16 v[120:123], v[236:239], a[88:95], %[ix]\n\t" \
    "s_waitcnt vmcnt(8)\n\t" "v_smfmac_f32_16x16x64_bf16 v[136:139], v[184:187], v[160:167], %[ix]\n\t" "s_waitcnt vmcnt(5)\n\t" \
    "v_smfmac_f32_16x16x64_bf16 v[120:123], v[240:243], a[96:103], %[ix]\n\t" "v_permlane16_swap_b32 v168, v169\n\t" "v_permlane16_swap_b32 v170, v171\n\t" \
    "s_waitcnt vmcnt(4)\n\t" "v_smfmac_f32_16x16x64_bf16 v[136:139], v[244:247], a[104:111], %[ix]\n\t" "v_permlane16_swap_b32 v172, v173\n\t" \
    "v_permlane16_swap_b32 v174, v175\n\t" "s_waitcnt vmcnt(3)\n\t" "v_smfmac_f32_16x16x64_bf16 v[120:123], v[248:251], a[112:119], %[ix]\n\t" "s_waitcnt vmcnt(2)\n\t" \
    "v_smfmac_f32_16x16x64_bf16 v[120:123], v[252:255], a[120:127], %[ix]\n\t" "s_waitcnt vmcnt(1)\n\t" \
    "v_smfmac_f32_16x16x64_bf16 v[120:123], v[188:191], v[168:175], %[ix]\n\t" XT(82, 83) "v_add_f32 v136, v136, v137\n\t" "v_add_f32 v138, v138, v139\n\t" "s_nop 5\n\t" \
    "v_add_f32 v120, v120, v121\n\t" "v_add_f32 v122, v122, v123\n\t" "v_add_f32 v120, v120, v136\n\t" "v_add_f32 v122, v122, v138\n\t" \
    "v_cvt_pk_bf16_f32 v124, v120, v122\n\t" "v_cmp_u_f32 vcc, v120, v122\n\t" "s_cbranch_vccnz 30f\n\t" "21:\n\t" \
    "v_mov_b32_dpp v125, v124 quad_perm:[1,0,3,2] row_mask:0xf bank_mask:0xf\n\t" "v_perm_b32 v124, v125, v124, %[ps]\n\t" \
    "buffer_store_dword v124, %[mw], %[rs], %[mb] offen sc1\n\t" XT(84, 85) "buffer_store_dword v127, %[mw], %[rs], %[mr] offen sc1\n\t" "s_memtime s[64:65]\n\t" \
    "s_barrier\n\t" XT(98, 99) "v_permlane16_swap_b32 v144, v145\n\t" "v_permlane16_swap_b32 v146, v147\n\t" "v_permlane16_swap_b32 v148, v149\n\t" \
    "v_permlane16_swap_b32 v150, v151\n\t" "v_permlane16_swap_b32 v152, v153\n\t" "v_permlane16_swap_b32 v154, v155\n\t" "v_permlane16_swap_b32 v156, v157\n\t" \
    "v_permlane16_swap_b32 v158, v159\n\t" "v_permlane16_swap_b32 v160, v161\n\t" "v_permlane16_swap_b32 v162, v163\n\t" "v_permlane16_swap_b32 v164, v165\n\t" \
    "v_permlane16_swap_b32 v166, v167\n\t" "v_permlane16_swap_b32 v168, v169\n\t" "v_permlane16_swap_b32 v170, v171\n\t" "v_permlane16_swap_b32 v172, v173\n\t" \
    "v_permlane16_swap_b32 v174, v175\n\t" "s_waitcnt lgkmcnt(0)\n\t" "s_and_b32 %[bad], s64, -2\n\t" "s_branch 40f\n\t" "30:\n\t" "s_cmp_lg_u32 %[late], 0\n\t" \
    "s_cbranch_scc1 32f\n\t" XCNT(100) "s_mov_b32 %[bad], 1\n\t" "s_waitcnt vmcnt(0)\n\t" "s_branch 40f\n\t" "32:\n\t" "v_max_u32 v126, v144, v145\n\t" \
    "v_max3_u32 v126, v126, v146, v147\n\t" "v_max3_u32 v126, v126, v148, v149\n\t" "v_max3_u32 v126, v126, v150, v151\n\t" "v_max3_u32 v126, v126, v152, v153\n\t" \
    "v_max3_u32 v126, v126, v154, v155\n\t" "v_max3_u32 v126, v126, v156, v157\n\t" "v_max3_u32 v126, v126, v158, v159\n\t" "v_max3_u32 v126, v126, v160, v161\n\t" \
    "v_max3_u32 v126, v126, v162, v163\n\t" "v_max3_u32 v126, v126, v164, v165\n\t" "v_max3_u32 v126, v126, v166, v167\n\t" "v_max3_u32 v126, v126, v168, v169\n\t" \
    "v_max3_u32 v126, v126, v170, v171\n\t" "v_max3_u32 v126, v126, v172, v173\n\t" "v_max3_u32 v126, v126, v174, v175\n\t" UCNT "v_max3_u32 v126, v126, v176, v177\n\t" \
    "v_max3_u32 v126, v126, v178, v179\n\t" "v_max3_u32 v126, v126, v180, v181\n\t" "v_max3_u32 v126, v126, v182, v183\n\t" "v_max3_u32 v126, v126, v184, v185\n\t" \
    "v_max3_u32 v126, v126, v186, v187\n\t" "v_max3_u32 v126, v126, v188, v189\n\t" "v_max3_u32 v126, v126, v190, v191\n\t" "v_max3_u32 v126, v126, v192, v193\n\t" \
    "v_max3_u32 v126, v126, v194, v195\n\t" "v_max3_u32 v126, v126, v196, v197\n\t" "v_max3_u32 v126, v126, v198, v199\n\t" "v_max3_u32 v126, v126, v200, v201\n\t" \
    "v_max3_u32 v126, v126, v202, v203\n\t" "v_max3_u32 v126, v126, v204, v205\n\t" "v_max3_u32 v126, v126, v206, v207\n\t" "v_max3_u32 v126, v126, v208, v209\n\t" \
    "v_max3_u32 v126, v126, v210, v211\n\t" "v_max3_u32 v126, v126, v212, v213\n\t" "v_max3_u32 v126, v126, v214, v215\n\t" "v_max3_u32 v126, v126, v216, v217\n\t" \
    "v_max3_u32 v126, v126, v218, v219\n\t" "v_max3_u32 v126, v126, v220, v221\n\t" "v_max3_u32 v126, v126, v222, v223\n\t" "v_max3_u32 v126, v126, v224, v225\n\t" \
    "v_max3_u32 v126, v126, v226, v227\n\t" "v_max3_u32 v126, v126, v228, v229\n\t" "v_max3_u32 v126, v126, v230, v231\n\t" "v_max3_u32 v126, v126, v232, v233\n\t" \
    "v_max3_u32 v126, v126, v234, v235\n\t" "v_max3_u32 v126, v126, v236, v237\n\t" "v_max3_u32 v126, v126, v238, v239\n\t" "v_max3_u32 v126, v126, v240, v241\n\t" \
    "v_max3_u32 v126, v126, v242, v243\n\t" "v_max3_u32 v126, v126, v244, v245\n\t" "v_max3_u32 v126, v126, v246, v247\n\t" "v_max3_u32 v126, v126, v248, v249\n\t" \
    "v_max3_u32 v126, v126, v250, v251\n\t" "v_max3_u32 v126, v126, v252, v253\n\t" "v_max3_u32 v126, v126, v254, v255\n\t" "v_cmp_eq_u32 vcc, -1, v126\n\t" \
    "s_cbranch_vccz 21b\n\t" "s_cmp_eq_u32 %[bd], 0\n\t" "s_cbranch_scc1 21b\n\t" XCNT(100) "s_mov_b32 %[bad], 1\n\t" "s_waitcnt vmcnt(0)\n\t" "40:\n\t"
#define G_LOOP \
    ".p2align 6\n\t" "v_xor_b32 v72, 0, %[xo]\n\t" "v_add_u32 v80, 0x10000, v72\n\t" "v_xor_b32 v88, 0, %[xs]\n\t" "v_add_u32 v88, 0, v88\n\t" "v_xor_b32 v73, 64, %[xo]\n\t" \
    "v_add_u32 v81, 0x10000, v73\n\t" "v_xor_b32 v89, 16, %[xs]\n\t" "v_add_u32 v89, 2048, v89\n\t" "v_xor_b32 v74, 128, %[xo]\n\t" "v_add_u32 v82, 0x10000, v74\n\t" \
    "v_xor_b32 v90, 32, %[xs]\n\t" "v_add_u32 v90, 4096, v90\n\t" "v_xor_b32 v75, 192, %[xo]\n\t" "v_add_u32 v83, 0x10000, v75\n\t" "v_xor_b32 v91, 48, %[xs]\n\t" \
    "v_add_u32 v91, 6144, v91\n\t" "v_xor_b32 v76, 256, %[xo]\n\t" "v_add_u32 v84, 0x10000, v76\n\t" "v_xor_b32 v92, 64, %[xs]\n\t" "v_add_u32 v92, 8192, v92\n\t" \
    "v_xor_b32 v77, 320, %[xo]\n\t" "v_add_u32 v85, 0x10000, v77\n\t" "v_xor_b32 v93, 80, %[xs]\n\t" "v_add_u32 v93, 10240, v93\n\t" "v_xor_b32 v78, 384, %[xo]\n\t" \
    "v_add_u32 v86, 0x10000, v78\n\t" "v_xor_b32 v94, 96, %[xs]\n\t" "v_add_u32 v94, 12288, v94\n\t" "v_xor_b32 v79, 448, %[xo]\n\t" "v_add_u32 v87, 0x10000, v79\n\t" \
    "v_xor_b32 v95, 112, %[xs]\n\t" "v_add_u32 v95, 14336, v95\n\t" "v_mov_b32 v96, 2.0\n\t" "s_mov_b32 s60, 0\n\t" "s_mov_b32 s62, 0\n\t" "s_waitcnt vmcnt(0)\n\t" \
    "s_barrier\n\t" "ds_read_b128 v[100:103], v72 offset:0\n\t" "ds_read_b128 v[104:107], v72 offset:32768\n\t" "ds_read_b128 v[108:111], v73 offset:0\n\t" \
    "ds_read_b128 v[112:115], v73 offset:32768\n\t" "ds_read_b128 v[116:119], v74 offset:0\n\t" "10:\n\t" XT(80, 81) "s_waitcnt lgkmcnt(4)\n\t" \
    "v_mfma_f32_16x16x32_bf16 v[32:35], a[0:3], v[100:103], 0\n\t" "ds_read_b128 v[120:123], v74 offset:32768\n\t" "s_add_u32 s61, s60, 1\n\t" "s_and_b32 s61, s61, 63\n\t" \
    "v_mfma_f32_16x16x32_bf16 v[36:39], a[4:7], v[100:103], 0\n\t" "s_lshl_b32 s61, s61, 16\n\t" "s_add_u32 s63, %[lw], 65536\n\t" "s_waitcnt lgkmcnt(4)\n\t" \
    "v_mfma_f32_16x16x32_bf16 v[40:43], a[0:3], v[104:107], 0\n\t" "ds_read_b128 v[124:127], v75 offset:0\n\t" "v_mfma_f32_16x16x32_bf16 v[44:47], a[4:7], v[104:107], 0\n\t" \
    "s_waitcnt lgkmcnt(4)\n\t" "v_mfma_f32_16x16x32_bf16 v[32:35], a[8:11], v[108:111], v[32:35]\n\t" "ds_read_b128 v[128:131], v75 offset:32768\n\t" \
    "v_cvt_pk_bf16_f32 v64, v48, v49\n\t" "v_mfma_f32_16x16x32_bf16 v[36:39], a[12:15], v[108:111], v[36:39]\n\t" "v_cvt_pk_bf16_f32 v65, v50, v51\n\t" \
    "v_cvt_pk_bf16_f32 v66, v52, v53\n\t" "s_waitcnt lgkmcnt(4)\n\t" "v_mfma_f32_16x16x32_bf16 v[40:43], a[8:11], v[112:115], v[40:43]\n\t" \
    "ds_read_b128 v[100:103], v76 offset:0\n\t" "v_cvt_pk_bf16_f32 v67, v54, v55\n\t" "v_mfma_f32_16x16x32_bf16 v[44:47], a[12:15], v[112:115], v[44:47]\n\t" \
    "v_cvt_pk_bf16_f32 v68, v56, v57\n\t" "v_cvt_pk_bf16_f32 v69, v58, v59\n\t" "s_waitcnt lgkmcnt(4)\n\t" \
    "v_mfma_f32_16x16x32_bf16 v[32:35], a[16:19], v[116:119], v[32:35]\n\t" "ds_read_b128 v[104:107], v76 offset:32768\n\t" "v_cvt_pk_bf16_f32 v70, v60, v61\n\t" \
    "v_mfma_f32_16x16x32_bf16 v[36:39], a[20:23], v[116:119], v[36:39]\n\t" "v_cvt_pk_bf16_f32 v71, v62, v63\n\t" \
    "buffer_store_dwordx4 v[64:67], v96, %[rs], s62 offen offset:0 sc1\n\t" "s_waitcnt lgkmcnt(4)\n\t" "v_mfma_f32_16x16x32_bf16 v[40:43], a[16:19], v[120:123], v[40:43]\n\t" \
    "ds_read_b128 v[108:111], v77 offset:0\n\t" "v_mfma_f32_16x16x32_bf16 v[44:47], a[20:23], v[120:123], v[44:47]\n\t" "s_waitcnt lgkmcnt(4)\n\t" \
    "v_mfma_f32_16x16x32_bf16 v[32:35], a[24:27], v[124:127], v[32:35]\n\t" "ds_read_b128 v[112:115], v77 offset:32768\n\t" \
    "v_mfma_f32_16x16x32_bf16 v[36:39], a[28:31], v[124:127], v[36:39]\n\t" "buffer_store_dwordx4 v[68:71], v96, %[rs], s62 offen offset:256 sc1\n\t" \
    "s_waitcnt lgkmcnt(4)\n\t" "v_mfma_f32_16x16x32_bf16 v[40:43], a[24:27], v[128:131], v[40:43]\n\t" "ds_read_b128 v[116:119], v78 offset:0\n\t" "s_mov_b32 m0, s63\n\t" \
    "v_mfma_f32_16x16x32_bf16 v[44:47], a[28:31], v[128:131], v[44:47]\n\t" "s_add_u32 s63, s63, 0x800\n\t" "buffer_load_dwordx4 v88, %[rx], s61 offen lds\n\t" \
    "s_waitcnt lgkmcnt(4)\n\t" "v_mfma_f32_16x16x32_bf16 v[32:35], a[32:35], v[100:103], v[32:35]\n\t" "ds_read_b128 v[120:123], v78 offset:32768\n\t" \
    "v_mfma_f32_16x16x32_bf16 v[36:39], a[36:39], v[100:103], v[36:39]\n\t" "buffer_load_dwordx4 v88, %[rx], s61 offen offset:1024 lds\n\t" "s_waitcnt lgkmcnt(4)\n\t" \
    "v_mfma_f32_16x16x32_bf16 v[40:43], a[32:35], v[104:107], v[40:43]\n\t" "ds_read_b128 v[124:127], v79 offset:0\n\t" "s_mov_b32 m0, s63\n\t" \
    "v_mfma_f32_16x16x32_bf16 v[44:47], a[36:39], v[104:107], v[44:47]\n\t" "s_add_u32 s63, s63, 0x800\n\t" "buffer_load_dwordx4 v89, %[rx], s61 offen lds\n\t" \
    "s_waitcnt lgkmcnt(4)\n\t" "v_mfma_f32_16x16x32_bf16 v[32:35], a[40:43], v[108:111], v[32:35]\n\t" "ds_read_b128 v[128:131], v79 offset:32768\n\t" \
    "v_mfma_f32_16x16x32_bf16 v[36:39], a[44:47], v[108:111], v[36:39]\n\t" "buffer_load_dwordx4 v89, %[rx], s61 offen offset:1024 lds\n\t" "s_waitcnt lgkmcnt(4)\n\t" \
    "v_mfma_f32_16x16x32_bf16 v[40:43], a[40:43], v[112:115], v[40:43]\n\t" "ds_read_b128 v[100:103], v72 offset:512\n\t" "s_mov_b32 m0, s63\n\t" \
    "v_mfma_f32_16x16x32_bf16 v[44:47], a[44:47], v[112:115], v[44:47]\n\t" "s_add_u32 s63, s63, 0x800\n\t" "buffer_load_dwordx4 v90, %[rx], s61 offen lds\n\t" \
    "s_waitcnt lgkmcnt(4)\n\t" "v_mfma_f32_16x16x32_bf16 v[32:35], a[48:51], v[116:119], v[32:35]\n\t" "ds_read_b128 v[104:107], v72 offset:33280\n\t" \
    "v_mfma_f32_16x16x32_bf16 v[36:39], a[52:55], v[116:119], v[36:39]\n\t" "buffer_load_dwordx4 v90, %[rx], s61 offen offset:1024 lds\n\t" "s_waitcnt lgkmcnt(4)\n\t" \
    "v_mfma_f32_16x16x32_bf16 v[40:43], a[48:51], v[120:123], v[40:43]\n\t" "ds_read_b128 v[108:111], v73 offset:512\n\t" "s_mov_b32 m0, s63\n\t" \
    "v_mfma_f32_16x16x32_bf16 v[44:47], a[52:55], v[120:123], v[44:47]\n\t" "s_add_u32 s63, s63, 0x800\n\t" "buffer_load_dwordx4 v91, %[rx], s61 offen lds\n\t" \
    "s_waitcnt lgkmcnt(4)\n\t" "v_mfma_f32_16x16x32_bf16 v[32:35], a[56:59], v[124:127], v[32:35]\n\t" "ds_read_b128 v[112:115], v73 offset:33280\n\t" \
    "v_mfma_f32_16x16x32_bf16 v[36:39], a[60:63], v[124:127], v[36:39]\n\t" "buffer_load_dwordx4 v91, %[rx], s61 offen offset:1024 lds\n\t" "s_waitcnt lgkmcnt(4)\n\t" \
    "v_mfma_f32_16x16x32_bf16 v[40:43], a[56:59], v[128:131], v[40:43]\n\t" "ds_read_b128 v[116:119], v74 offset:512\n\t" "s_mov_b32 m0, s63\n\t" \
    "v_mfma_f32_16x16x32_bf16 v[44:47], a[60:63], v[128:131], v[44:47]\n\t" "s_add_u32 s63, s63, 0x800\n\t" "buffer_load_dwordx4 v92, %[rx], s61 offen lds\n\t" \
    "s_waitcnt lgkmcnt(4)\n\t" "v_mfma_f32_16x16x32_bf16 v[32:35], a[64:67], v[100:103], v[32:35]\n\t" "ds_read_b128 v[120:123], v74 offset:33280\n\t" \
    "v_mfma_f32_16x16x32_bf16 v[36:39], a[68:71], v[100:103], v[36:39]\n\t" "buffer_load_dwordx4 v92, %[rx], s61 offen offset:1024 lds\n\t" "s_waitcnt lgkmcnt(4)\n\t" \
    "v_mfma_f32_16x16x32_bf16 v[40:43], a[64:67], v[104:107], v[40:43]\n\t" "ds_read_b128 v[124:127], v75 offset:512\n\t" "s_mov_b32 m0, s63\n\t" \
    "v_mfma_f32_16x16x32_bf16 v[44:47], a[68:71], v[104:107], v[44:47]\n\t" "s_add_u32 s63, s63, 0x800\n\t" "buffer_load_dwordx4 v93, %[rx], s61 offen lds\n\t" \
    "s_waitcnt lgkmcnt(4)\n\t" "v_mfma_f32_16x16x32_bf16 v[32:35], a[72:75], v[108:111], v[32:35]\n\t" "ds_read_b128 v[128:131], v75 offset:33280\n\t" \
    "v_mfma_f32_16x16x32_bf16 v[36:39], a[76:79], v[108:111], v[36:39]\n\t" "buffer_load_dwordx4 v93, %[rx], s61 offen offset:1024 lds\n\t" "s_waitcnt lgkmcnt(4)\n\t" \
    "v_mfma_f32_16x16x32_bf16 v[40:43], a[72:75], v[112:115], v[40:43]\n\t" "ds_read_b128 v[100:103], v76 offset:512\n\t" "s_mov_b32 m0, s63\n\t" \
    "v_mfma_f32_16x16x32_bf16 v[44:47], a[76:79], v[112:115], v[44:47]\n\t" "s_add_u32 s63, s63, 0x800\n\t" "buffer_load_dwordx4 v94, %[rx], s61 offen lds\n\t" \
    "s_waitcnt lgkmcnt(4)\n\t" "v_mfma_f32_16x16x32_bf16 v[32:35], a[80:83], v[116:119], v[32:35]\n\t" "ds_read_b128 v[104:107], v76 offset:33280\n\t" \
    "v_mfma_f32_16x16x32_bf16 v[36:39], a[84:87], v[116:119], v[36:39]\n\t" "buffer_load_dwordx4 v94, %[rx], s61 offen offset:1024 lds\n\t" "s_waitcnt lgkmcnt(4)\n\t" \
    "v_mfma_f32_16x16x32_bf16 v[40:43], a[80:83], v[120:123], v[40:43]\n\t" "ds_read_b128 v[108:111], v77 offset:512\n\t" "s_mov_b32 m0, s63\n\t" \
    "v_mfma_f32_16x16x32_bf16 v[44:47], a[84:87], v[120:123], v[44:47]\n\t" "s_add_u32 s63, s63, 0x800\n\t" "buffer_load_dwordx4 v95, %[rx], s61 offen lds\n\t" \
    "s_waitcnt lgkmcnt(4)\n\t" "v_mfma_f32_16x16x32_bf16 v[32:35], a[88:91], v[124:127], v[32:35]\n\t" "ds_read_b128 v[112:115], v77 offset:33280\n\t" \
    "v_mfma_f32_16x16x32_bf16 v[36:39], a[92:95], v[124:127], v[36:39]\n\t" "buffer_load_dwordx4 v95, %[rx], s61 offen offset:1024 lds\n\t" "s_waitcnt lgkmcnt(4)\n\t" \
    "v_mfma_f32_16x16x32_bf16 v[40:43], a[88:91], v[128:131], v[40:43]\n\t" "ds_read_b128 v[116:119], v78 offset:512\n\t" \
    "v_mfma_f32_16x16x32_bf16 v[44:47], a[92:95], v[128:131], v[44:47]\n\t" "s_waitcnt lgkmcnt(4)\n\t" "v_mfma_f32_16x16x32_bf16 v[32:35], a[96:99], v[100:103], v[32:35]\n\t" \
    "ds_read_b128 v[120:123], v78 offset:33280\n\t" "s_lshr_b32 s64, s60, 2\n\t" "s_xor_b32 s64, s64, %[xp]\n\t" \
    "v_mfma_f32_16x16x32_bf16 v[36:39], a[100:103], v[100:103], v[36:39]\n\t" "s_and_b32 s64, s64, 1\n\t" "s_lshl_b32 s64, s64, 13\n\t" "s_waitcnt lgkmcnt(4)\n\t" \
    "v_mfma_f32_16x16x32_bf16 v[40:43], a[96:99], v[104:107], v[40:43]\n\t" "ds_read_b128 v[124:127], v79 offset:512\n\t" "s_lshl_b32 s62, s60, 19\n\t" \
    "v_mfma_f32_16x16x32_bf16 v[44:47], a[100:103], v[104:107], v[44:47]\n\t" "s_add_u32 s62, s62, s64\n\t" "s_add_u32 s62, s62, %[ob]\n\t" "s_waitcnt lgkmcnt(4)\n\t" \
    "v_mfma_f32_16x16x32_bf16 v[32:35], a[104:107], v[108:111], v[32:35]\n\t" "ds_read_b128 v[128:131], v79 offset:33280\n\t" "s_waitcnt vmcnt(16)\n\t" "s_barrier\n\t" \
    "v_mfma_f32_16x16x32_bf16 v[36:39], a[108:111], v[108:111], v[36:39]\n\t" "v_mov_b32 v98, s60\n\t" "buffer_store_dword v98, %[fo], %[rc], %[fl] offen sc1\n\t" \
    "s_waitcnt lgkmcnt(4)\n\t" "v_mfma_f32_16x16x32_bf16 v[40:43], a[104:107], v[112:115], v[40:43]\n\t" "ds_read_b128 v[100:103], v72 offset:1024\n\t" \
    "v_mfma_f32_16x16x32_bf16 v[44:47], a[108:111], v[112:115], v[44:47]\n\t" "s_waitcnt lgkmcnt(4)\n\t" \
    "v_mfma_f32_16x16x32_bf16 v[32:35], a[112:115], v[116:119], v[32:35]\n\t" "ds_read_b128 v[104:107], v72 offset:33792\n\t" \
    "v_mfma_f32_16x16x32_bf16 v[36:39], a[116:119], v[116:119], v[36:39]\n\t" "s_waitcnt lgkmcnt(4)\n\t" \
    "v_mfma_f32_16x16x32_bf16 v[40:43], a[112:115], v[120:123], v[40:43]\n\t" "ds_read_b128 v[108:111], v73 offset:1024\n\t" \
    "v_mfma_f32_16x16x32_bf16 v[44:47], a[116:119], v[120:123], v[44:47]\n\t" "s_waitcnt lgkmcnt(4)\n\t" \
    "v_mfma_f32_16x16x32_bf16 v[32:35], a[120:123], v[124:127], v[32:35]\n\t" "ds_read_b128 v[112:115], v73 offset:33792\n\t" \
    "v_mfma_f32_16x16x32_bf16 v[36:39], a[124:127], v[124:127], v[36:39]\n\t" "s_waitcnt lgkmcnt(4)\n\t" \
    "v_mfma_f32_16x16x32_bf16 v[40:43], a[120:123], v[128:131], v[40:43]\n\t" "ds_read_b128 v[116:119], v74 offset:1024\n\t" \
    "v_mfma_f32_16x16x32_bf16 v[44:47], a[124:127], v[128:131], v[44:47]\n\t" "s_waitcnt lgkmcnt(4)\n\t" \
    "v_mfma_f32_16x16x32_bf16 v[32:35], a[128:131], v[100:103], v[32:35]\n\t" "ds_read_b128 v[120:123], v74 offset:33792\n\t" \
    "v_mfma_f32_16x16x32_bf16 v[36:39], a[132:135], v[100:103], v[36:39]\n\t" "s_waitcnt lgkmcnt(4)\n\t" \
    "v_mfma_f32_16x16x32_bf16 v[40:43], a[128:131], v[104:107], v[40:43]\n\t" "ds_read_b128 v[124:127], v75 offset:1024\n\t" \
    "v_mfma_f32_16x16x32_bf16 v[44:47], a[132:135], v[104:107], v[44:47]\n\t" "s_waitcnt lgkmcnt(4)\n\t" \
    "v_mfma_f32_16x16x32_bf16 v[32:35], a[136:139], v[108:111], v[32:35]\n\t" "ds_read_b128 v[128:131], v75 offset:33792\n\t" \
    "v_mfma_f32_16x16x32_bf16 v[36:39], a[140:143], v[108:111], v[36:39]\n\t" "s_waitcnt lgkmcnt(4)\n\t" \
    "v_mfma_f32_16x16x32_bf16 v[40:43], a[136:139], v[112:115], v[40:43]\n\t" "ds_read_b128 v[100:103], v76 offset:1024\n\t" \
    "v_mfma_f32_16x16x32_bf16 v[44:47], a[140:143], v[112:115], v[44:47]\n\t" "s_waitcnt lgkmcnt(4)\n\t" \
    "v_mfma_f32_16x16x32_bf16 v[32:35], a[144:147], v[116:119], v[32:35]\n\t" "ds_read_b128 v[104:107], v76 offset:33792\n\t" \
    "v_mfma_f32_16x16x32_bf16 v[36:39], a[148:151], v[116:119], v[36:39]\n\t" "s_waitcnt lgkmcnt(4)\n\t" \
    "v_mfma_f32_16x16x32_bf16 v[40:43], a[144:147], v[120:123], v[40:43]\n\t" "ds_read_b128 v[108:111], v77 offset:1024\n\t" \
    "v_mfma_f32_16x16x32_bf16 v[44:47], a[148:151], v[120:123], v[44:47]\n\t" "s_waitcnt lgkmcnt(4)\n\t" \
    "v_mfma_f32_16x16x32_bf16 v[32:35], a[152:155], v[124:127], v[32:35]\n\t" "ds_read_b128 v[112:115], v77 offset:33792\n\t" \
    "v_mfma_f32_16x16x32_bf16 v[36:39], a[156:159], v[124:127], v[36:39]\n\t" "s_waitcnt lgkmcnt(4)\n\t" \
    "v_mfma_f32_16x16x32_bf16 v[40:43], a[152:155], v[128:131], v[40:43]\n\t" "ds_read_b128 v[116:119], v78 offset:1024\n\t" \
    "v_mfma_f32_16x16x32_bf16 v[44:47], a[156:159], v[128:131], v[44:47]\n\t" "s_waitcnt lgkmcnt(4)\n\t" \
    "v_mfma_f32_16x16x32_bf16 v[32:35], a[160:163], v[100:103], v[32:35]\n\t" "ds_read_b128 v[120:123], v78 offset:33792\n\t" \
    "v_mfma_f32_16x16x32_bf16 v[36:39], a[164:167], v[100:103], v[36:39]\n\t" "s_waitcnt lgkmcnt(4)\n\t" \
    "v_mfma_f32_16x16x32_bf16 v[40:43], a[160:163], v[104:107], v[40:43]\n\t" "ds_read_b128 v[124:127], v79 offset:1024\n\t" \
    "v_mfma_f32_16x16x32_bf16 v[44:47], a[164:167], v[104:107], v[44:47]\n\t" "s_waitcnt lgkmcnt(4)\n\t" \
    "v_mfma_f32_16x16x32_bf16 v[32:35], a[168:171], v[108:111], v[32:35]\n\t" "ds_read_b128 v[128:131], v79 offset:33792\n\t" \
    "v_mfma_f32_16x16x32_bf16 v[36:39], a[172:175], v[108:111], v[36:39]\n\t" "s_waitcnt lgkmcnt(4)\n\t" \
    "v_mfma_f32_16x16x32_bf16 v[40:43], a[168:171], v[112:115], v[40:43]\n\t" "ds_read_b128 v[100:103], v72 offset:1536\n\t" \
    "v_mfma_f32_16x16x32_bf16 v[44:47], a[172:175], v[112:115], v[44:47]\n\t" "s_waitcnt lgkmcnt(4)\n\t" \
    "v_mfma_f32_16x16x32_bf16 v[32:35], a[176:179], v[116:119], v[32:35]\n\t" "ds_read_b128 v[104:107], v72 offset:34304\n\t" \
    "v_mfma_f32_16x16x32_bf16 v[36:39], a[180:183], v[116:119], v[36:39]\n\t" "s_waitcnt lgkmcnt(4)\n\t" \
    "v_mfma_f32_16x16x32_bf16 v[40:43], a[176:179], v[120:123], v[40:43]\n\t" "ds_read_b128 v[108:111], v73 offset:1536\n\t" \
    "v_mfma_f32_16x16x32_bf16 v[44:47], a[180:183], v[120:123], v[44:47]\n\t" "s_waitcnt lgkmcnt(4)\n\t" \
    "v_mfma_f32_16x16x32_bf16 v[32:35], a[184:187], v[124:127], v[32:35]\n\t" "ds_read_b128 v[112:115], v73 offset:34304\n\t" \
    "v_mfma_f32_16x16x32_bf16 v[36:39], a[188:191], v[124:127], v[36:39]\n\t" "s_waitcnt lgkmcnt(4)\n\t" \
    "v_mfma_f32_16x16x32_bf16 v[40:43], a[184:187], v[128:131], v[40:43]\n\t" "ds_read_b128 v[116:119], v74 offset:1536\n\t" \
    "v_mfma_f32_16x16x32_bf16 v[44:47], a[188:191], v[128:131], v[44:47]\n\t" "s_waitcnt lgkmcnt(4)\n\t" \
    "v_mfma_f32_16x16x32_bf16 v[32:35], a[192:195], v[100:103], v[32:35]\n\t" "ds_read_b128 v[120:123], v74 offset:34304\n\t" \
    "v_mfma_f32_16x16x32_bf16 v[36:39], a[196:199], v[100:103], v[36:39]\n\t" "s_waitcnt lgkmcnt(4)\n\t" \
    "v_mfma_f32_16x16x32_bf16 v[40:43], a[192:195], v[104:107], v[40:43]\n\t" "ds_read_b128 v[124:127], v75 offset:1536\n\t" \
    "v_mfma_f32_16x16x32_bf16 v[44:47], a[196:199], v[104:107], v[44:47]\n\t" "s_waitcnt lgkmcnt(4)\n\t" \
    "v_mfma_f32_16x16x32_bf16 v[32:35], a[200:203], v[108:111], v[32:35]\n\t" "ds_read_b128 v[128:131], v75 offset:34304\n\t" \
    "v_mfma_f32_16x16x32_bf16 v[36:39], a[204:207], v[108:111], v[36:39]\n\t" "s_waitcnt lgkmcnt(4)\n\t" \
    "v_mfma_f32_16x16x32_bf16 v[40:43], a[200:203], v[112:115], v[40:43]\n\t" "ds_read_b128 v[100:103], v76 offset:1536\n\t" \
    "v_mfma_f32_16x16x32_bf16 v[44:47], a[204:207], v[112:115], v[44:47]\n\t" "s_waitcnt lgkmcnt(4)\n\t" \
    "v_mfma_f32_16x16x32_bf16 v[32:35], a[208:211], v[116:119], v[32:35]\n\t" "ds_read_b128 v[104:107], v76 offset:34304\n\t" \
    "v_mfma_f32_16x16x32_bf16 v[36:39], a[212:215], v[116:119], v[36:39]\n\t" "s_waitcnt lgkmcnt(4)\n\t" \
    "v_mfma_f32_16x16x32_bf16 v[40:43], a[208:211], v[120:123], v[40:43]\n\t" "ds_read_b128 v[108:111], v77 offset:1536\n\t" \
    "v_mfma_f32_16x16x32_bf16 v[44:47], a[212:215], v[120:123], v[44:47]\n\t" "s_waitcnt lgkmcnt(4)\n\t" \
    "v_mfma_f32_16x16x32_bf16 v[32:35], a[216:219], v[124:127], v[32:35]\n\t" "ds_read_b128 v[112:115], v77 offset:34304\n\t" \
    "v_mfma_f32_16x16x32_bf16 v[36:39], a[220:223], v[124:127], v[36:39]\n\t" "s_waitcnt lgkmcnt(4)\n\t" \
    "v_mfma_f32_16x16x32_bf16 v[40:43], a[216:219], v[128:131], v[40:43]\n\t" "ds_read_b128 v[116:119], v78 offset:1536\n\t" \
    "v_mfma_f32_16x16x32_bf16 v[44:47], a[220:223], v[128:131], v[44:47]\n\t" "s_waitcnt lgkmcnt(4)\n\t" \
    "v_mfma_f32_16x16x32_bf16 v[32:35], a[224:227], v[100:103], v[32:35]\n\t" "ds_read_b128 v[120:123], v78 offset:34304\n\t" \
    "v_mfma_f32_16x16x32_bf16 v[36:39], a[228:231], v[100:103], v[36:39]\n\t" "s_waitcnt lgkmcnt(4)\n\t" \
    "v_mfma_f32_16x16x32_bf16 v[40:43], a[224:227], v[104:107], v[40:43]\n\t" "ds_read_b128 v[124:127], v79 offset:1536\n\t" \
    "v_mfma_f32_16x16x32_bf16 v[44:47], a[228:231], v[104:107], v[44:47]\n\t" "s_waitcnt lgkmcnt(4)\n\t" \
    "v_mfma_f32_16x16x32_bf16 v[32:35], a[232:235], v[108:111], v[32:35]\n\t" "ds_read_b128 v[128:131], v79 offset:34304\n\t" \
    "v_mfma_f32_16x16x32_bf16 v[36:39], a[236:239], v[108:111], v[36:39]\n\t" "s_waitcnt lgkmcnt(4)\n\t" \
    "v_mfma_f32_16x16x32_bf16 v[40:43], a[232:235], v[112:115], v[40:43]\n\t" "v_mfma_f32_16x16x32_bf16 v[44:47], a[236:239], v[112:115], v[44:47]\n\t" \
    "s_waitcnt lgkmcnt(3)\n\t" "v_mfma_f32_16x16x32_bf16 v[32:35], a[240:243], v[116:119], v[32:35]\n\t" \
    "v_mfma_f32_16x16x32_bf16 v[36:39], a[244:247], v[116:119], v[36:39]\n\t" "s_waitcnt vmcnt(1) lgkmcnt(0)\n\t" "s_barrier\n\t" XT(82, 83) \
    "v_mfma_f32_16x16x32_bf16 v[40:43], a[240:243], v[120:123], v[40:43]\n\t" "ds_read_b128 v[100:103], v80 offset:0\n\t" \
    "v_mfma_f32_16x16x32_bf16 v[44:47], a[244:247], v[120:123], v[44:47]\n\t" "v_mfma_f32_16x16x32_bf16 v[32:35], a[248:251], v[124:127], v[32:35]\n\t" \
    "ds_read_b128 v[104:107], v80 offset:32768\n\t" "v_mfma_f32_16x16x32_bf16 v[36:39], a[252:255], v[124:127], v[36:39]\n\t" "ds_read_b128 v[108:111], v81 offset:0\n\t" \
    "v_mfma_f32_16x16x32_bf16 v[40:43], a[248:251], v[128:131], v[40:43]\n\t" "ds_read_b128 v[112:115], v81 offset:32768\n\t" "ds_read_b128 v[116:119], v82 offset:0\n\t" \
    "v_mfma_f32_16x16x32_bf16 v[44:47], a[252:255], v[128:131], v[44:47]\n\t" "s_add_u32 s60, s60, 1\n\t" GSUM "s_cmp_eq_u32 s60, 1\n\t" "s_cbranch_scc1 20f\n\t" "11:\n\t" \
    XT(80, 81) "s_waitcnt lgkmcnt(4)\n\t" "v_mfma_f32_16x16x32_bf16 v[48:51], a[0:3], v[100:103], 0\n\t" "ds_read_b128 v[120:123], v82 offset:32768\n\t" \
    "s_add_u32 s61, s60, 1\n\t" "s_and_b32 s61, s61, 63\n\t" "v_mfma_f32_16x16x32_bf16 v[52:55], a[4:7], v[100:103], 0\n\t" "s_lshl_b32 s61, s61, 16\n\t" \
    "s_add_u32 s63, %[lw], 0\n\t" "s_waitcnt lgkmcnt(4)\n\t" "v_mfma_f32_16x16x32_bf16 v[56:59], a[0:3], v[104:107], 0\n\t" "ds_read_b128 v[124:127], v83 offset:0\n\t" \
    "v_mfma_f32_16x16x32_bf16 v[60:63], a[4:7], v[104:107], 0\n\t" "s_waitcnt lgkmcnt(4)\n\t" "v_mfma_f32_16x16x32_bf16 v[48:51], a[8:11], v[108:111], v[48:51]\n\t" \
    "ds_read_b128 v[128:131], v83 offset:32768\n\t" "v_cvt_pk_bf16_f32 v64, v32, v33\n\t" "v_mfma_f32_16x16x32_bf16 v[52:55], a[12:15], v[108:111], v[52:55]\n\t" \
    "v_cvt_pk_bf16_f32 v65, v34, v35\n\t" "v_cvt_pk_bf16_f32 v66, v36, v37\n\t" "s_waitcnt lgkmcnt(4)\n\t" \
    "v_mfma_f32_16x16x32_bf16 v[56:59], a[8:11], v[112:115], v[56:59]\n\t" "ds_read_b128 v[100:103], v84 offset:0\n\t" "v_cvt_pk_bf16_f32 v67, v38, v39\n\t" \
    "v_mfma_f32_16x16x32_bf16 v[60:63], a[12:15], v[112:115], v[60:63]\n\t" "v_cvt_pk_bf16_f32 v68, v40, v41\n\t" "v_cvt_pk_bf16_f32 v69, v42, v43\n\t" \
    "s_waitcnt lgkmcnt(4)\n\t" "v_mfma_f32_16x16x32_bf16 v[48:51], a[16:19], v[116:119], v[48:51]\n\t" "ds_read_b128 v[104:107], v84 offset:32768\n\t" \
    "v_cvt_pk_bf16_f32 v70, v44, v45\n\t" "v_mfma_f32_16x16x32_bf16 v[52:55], a[20:23], v[116:119], v[52:55]\n\t" "v_cvt_pk_bf16_f32 v71, v46, v47\n\t" \
    "buffer_store_dwordx4 v[64:67], v96, %[rs], s62 offen offset:0 sc1\n\t" "s_waitcnt lgkmcnt(4)\n\t" "v_mfma_f32_16x16x32_bf16 v[56:59], a[16:19], v[120:123], v[56:59]\n\t" \
    "ds_read_b128 v[108:111], v85 offset:0\n\t" "v_mfma_f32_16x16x32_bf16 v[60:63], a[20:23], v[120:123], v[60:63]\n\t" "s_waitcnt lgkmcnt(4)\n\t" \
    "v_mfma_f32_16x16x32_bf16 v[48:51], a[24:27], v[124:127], v[48:51]\n\t" "ds_read_b128 v[112:115], v85 offset:32768\n\t" \
    "v_mfma_f32_16x16x32_bf16 v[52:55], a[28:31], v[124:127], v[52:55]\n\t" "buffer_store_dwordx4 v[68:71], v96, %[rs], s62 offen offset:256 sc1\n\t" \
    "s_waitcnt lgkmcnt(4)\n\t" "v_mfma_f32_16x16x32_bf16 v[56:59], a[24:27], v[128:131], v[56:59]\n\t" "ds_read_b128 v[116:119], v86 offset:0\n\t" "s_mov_b32 m0, s63\n\t" \
    "v_mfma_f32_16x16x32_bf16 v[60:63], a[28:31], v[128:131], v[60:63]\n\t" "s_add_u32 s63, s63, 0x800\n\t" "buffer_load_dwordx4 v88, %[rx], s61 offen lds\n\t" \
    "s_waitcnt lgkmcnt(4)\n\t" "v_mfma_f32_16x16x32_bf16 v[48:51], a[32:35], v[100:103], v[48:51]\n\t" "ds_read_b128 v[120:123], v86 offset:32768\n\t" \
    "v_mfma_f32_16x16x32_bf16 v[52:55], a[36:39], v[100:103], v[52:55]\n\t" "buffer_load_dwordx4 v88, %[rx], s61 offen offset:1024 lds\n\t" "s_waitcnt lgkmcnt(4)\n\t" \
    "v_mfma_f32_16x16x32_bf16 v[56:59], a[32:35], v[104:107], v[56:59]\n\t" "ds_read_b128 v[124:127], v87 offset:0\n\t" "s_mov_b32 m0, s63\n\t" \
    "v_mfma_f32_16x16x32_bf16 v[60:63], a[36:39], v[104:107], v[60:63]\n\t" "s_add_u32 s63, s63, 0x800\n\t" "buffer_load_dwordx4 v89, %[rx], s61 offen lds\n\t" \
    "s_waitcnt lgkmcnt(4)\n\t" "v_mfma_f32_16x16x32_bf16 v[48:51], a[40:43], v[108:111], v[48:51]\n\t" "ds_read_b128 v[128:131], v87 offset:32768\n\t" \
    "v_mfma_f32_16x16x32_bf16 v[52:55], a[44:47], v[108:111], v[52:55]\n\t" "buffer_load_dwordx4 v89, %[rx], s61 offen offset:1024 lds\n\t" "s_waitcnt lgkmcnt(4)\n\t" \
    "v_mfma_f32_16x16x32_bf16 v[56:59], a[40:43], v[112:115], v[56:59]\n\t" "ds_read_b128 v[100:103], v80 offset:512\n\t" "s_mov_b32 m0, s63\n\t" \
    "v_mfma_f32_16x16x32_bf16 v[60:63], a[44:47], v[112:115], v[60:63]\n\t" "s_add_u32 s63, s63, 0x800\n\t" "buffer_load_dwordx4 v90, %[rx], s61 offen lds\n\t" \
    "s_waitcnt lgkmcnt(4)\n\t" "v_mfma_f32_16x16x32_bf16 v[48:51], a[48:51], v[116:119], v[48:51]\n\t" "ds_read_b128 v[104:107], v80 offset:33280\n\t" \
    "v_mfma_f32_16x16x32_bf16 v[52:55], a[52:55], v[116:119], v[52:55]\n\t" "buffer_load_dwordx4 v90, %[rx], s61 offen offset:1024 lds\n\t" "s_waitcnt lgkmcnt(4)\n\t" \
    "v_mfma_f32_16x16x32_bf16 v[56:59], a[48:51], v[120:123], v[56:59]\n\t" "ds_read_b128 v[108:111], v81 offset:512\n\t" "s_mov_b32 m0, s63\n\t" \
    "v_mfma_f32_16x16x32_bf16 v[60:63], a[52:55], v[120:123], v[60:63]\n\t" "s_add_u32 s63, s63, 0x800\n\t" "buffer_load_dwordx4 v91, %[rx], s61 offen lds\n\t" \
    "s_waitcnt lgkmcnt(4)\n\t" "v_mfma_f32_16x16x32_bf16 v[48:51], a[56:59], v[124:127], v[48:51]\n\t" "ds_read_b128 v[112:115], v81 offset:33280\n\t" \
    "v_mfma_f32_16x16x32_bf16 v[52:55], a[60:63], v[124:127], v[52:55]\n\t" "buffer_load_dwordx4 v91, %[rx], s61 offen offset:1024 lds\n\t" "s_waitcnt lgkmcnt(4)\n\t" \
    "v_mfma_f32_16x16x32_bf16 v[56:59], a[56:59], v[128:131], v[56:59]\n\t" "ds_read_b128 v[116:119], v82 offset:512\n\t" "s_mov_b32 m0, s63\n\t" \
    "v_mfma_f32_16x16x32_bf16 v[60:63], a[60:63], v[128:131], v[60:63]\n\t" "s_add_u32 s63, s63, 0x800\n\t" "buffer_load_dwordx4 v92, %[rx], s61 offen lds\n\t" \
    "s_waitcnt lgkmcnt(4)\n\t" "v_mfma_f32_16x16x32_bf16 v[48:51], a[64:67], v[100:103], v[48:51]\n\t" "ds_read_b128 v[120:123], v82 offset:33280\n\t" \
    "v_mfma_f32_16x16x32_bf16 v[52:55], a[68:71], v[100:103], v[52:55]\n\t" "buffer_load_dwordx4 v92, %[rx], s61 offen offset:1024 lds\n\t" "s_waitcnt lgkmcnt(4)\n\t" \
    "v_mfma_f32_16x16x32_bf16 v[56:59], a[64:67], v[104:107], v[56:59]\n\t" "ds_read_b128 v[124:127], v83 offset:512\n\t" "s_mov_b32 m0, s63\n\t" \
    "v_mfma_f32_16x16x32_bf16 v[60:63], a[68:71], v[104:107], v[60:63]\n\t" "s_add_u32 s63, s63, 0x800\n\t" "buffer_load_dwordx4 v93, %[rx], s61 offen lds\n\t" \
    "s_waitcnt lgkmcnt(4)\n\t" "v_mfma_f32_16x16x32_bf16 v[48:51], a[72:75], v[108:111], v[48:51]\n\t" "ds_read_b128 v[128:131], v83 offset:33280\n\t" \
    "v_mfma_f32_16x16x32_bf16 v[52:55], a[76:79], v[108:111], v[52:55]\n\t" "buffer_load_dwordx4 v93, %[rx], s61 offen offset:1024 lds\n\t" "s_waitcnt lgkmcnt(4)\n\t" \
    "v_mfma_f32_16x16x32_bf16 v[56:59], a[72:75], v[112:115], v[56:59]\n\t" "ds_read_b128 v[100:103], v84 offset:512\n\t" "s_mov_b32 m0, s63\n\t" \
    "v_mfma_f32_16x16x32_bf16 v[60:63], a[76:79], v[112:115], v[60:63]\n\t" "s_add_u32 s63, s63, 0x800\n\t" "buffer_load_dwordx4 v94, %[rx], s61 offen lds\n\t" \
    "s_waitcnt lgkmcnt(4)\n\t" "v_mfma_f32_16x16x32_bf16 v[48:51], a[80:83], v[116:119], v[48:51]\n\t" "ds_read_b128 v[104:107], v84 offset:33280\n\t" \
    "v_mfma_f32_16x16x32_bf16 v[52:55], a[84:87], v[116:119], v[52:55]\n\t" "buffer_load_dwordx4 v94, %[rx], s61 offen offset:1024 lds\n\t" "s_waitcnt lgkmcnt(4)\n\t" \
    "v_mfma_f32_16x16x32_bf16 v[56:59], a[80:83], v[120:123], v[56:59]\n\t" "ds_read_b128 v[108:111], v85 offset:512\n\t" "s_mov_b32 m0, s63\n\t" \
    "v_mfma_f32_16x16x32_bf16 v[60:63], a[84:87], v[120:123], v[60:63]\n\t" "s_add_u32 s63, s63, 0x800\n\t" "buffer_load_dwordx4 v95, %[rx], s61 offen lds\n\t" \
    "s_waitcnt lgkmcnt(4)\n\t" "v_mfma_f32_16x16x32_bf16 v[48:51], a[88:91], v[124:127], v[48:51]\n\t" "ds_read_b128 v[112:115], v85 offset:33280\n\t" \
    "v_mfma_f32_16x16x32_bf16 v[52:55], a[92:95], v[124:127], v[52:55]\n\t" "buffer_load_dwordx4 v95, %[rx], s61 offen offset:1024 lds\n\t" "s_waitcnt lgkmcnt(4)\n\t" \
    "v_mfma_f32_16x16x32_bf16 v[56:59], a[88:91], v[128:131], v[56:59]\n\t" "ds_read_b128 v[116:119], v86 offset:512\n\t" \
    "v_mfma_f32_16x16x32_bf16 v[60:63], a[92:95], v[128:131], v[60:63]\n\t" "s_waitcnt lgkmcnt(4)\n\t" "v_mfma_f32_16x16x32_bf16 v[48:51], a[96:99], v[100:103], v[48:51]\n\t" \
    "ds_read_b128 v[120:123], v86 offset:33280\n\t" "s_lshr_b32 s64, s60, 2\n\t" "s_xor_b32 s64, s64, %[xp]\n\t" \
    "v_mfma_f32_16x16x32_bf16 v[52:55], a[100:103], v[100:103], v[52:55]\n\t" "s_and_b32 s64, s64, 1\n\t" "s_lshl_b32 s64, s64, 13\n\t" "s_waitcnt lgkmcnt(4)\n\t" \
    "v_mfma_f32_16x16x32_bf16 v[56:59], a[96:99], v[104:107], v[56:59]\n\t" "ds_read_b128 v[124:127], v87 offset:512\n\t" "s_lshl_b32 s62, s60, 19\n\t" \
    "v_mfma_f32_16x16x32_bf16 v[60:63], a[100:103], v[104:107], v[60:63]\n\t" "s_add_u32 s62, s62, s64\n\t" "s_add_u32 s62, s62, %[ob]\n\t" "s_waitcnt lgkmcnt(4)\n\t" \
    "v_mfma_f32_16x16x32_bf16 v[48:51], a[104:107], v[108:111], v[48:51]\n\t" "ds_read_b128 v[128:131], v87 offset:33280\n\t" "s_waitcnt vmcnt(16)\n\t" "s_barrier\n\t" \
    "v_mfma_f32_16x16x32_bf16 v[52:55], a[108:111], v[108:111], v[52:55]\n\t" "v_mov_b32 v98, s60\n\t" "buffer_store_dword v98, %[fo], %[rc], %[fl] offen sc1\n\t" \
    "s_waitcnt lgkmcnt(4)\n\t" "v_mfma_f32_16x16x32_bf16 v[56:59], a[104:107], v[112:115], v[56:59]\n\t" "ds_read_b128 v[100:103], v80 offset:1024\n\t" \
    "v_mfma_f32_16x16x32_bf16 v[60:63], a[108:111], v[112:115], v[60:63]\n\t" "s_waitcnt lgkmcnt(4)\n\t" \
    "v_mfma_f32_16x16x32_bf16 v[48:51], a[112:115], v[116:119], v[48:51]\n\t" "ds_read_b128 v[104:107], v80 offset:33792\n\t" \
    "v_mfma_f32_16x16x32_bf16 v[52:55], a[116:119], v[116:119], v[52:55]\n\t" "s_waitcnt lgkmcnt(4)\n\t" \
    "v_mfma_f32_16x16x32_bf16 v[56:59], a[112:115], v[120:123], v[56:59]\n\t" "ds_read_b128 v[108:111], v81 offset:1024\n\t" \
    "v_mfma_f32_16x16x32_bf16 v[60:63], a[116:119], v[120:123], v[60:63]\n\t" "s_waitcnt lgkmcnt(4)\n\t" \
    "v_mfma_f32_16x16x32_bf16 v[48:51], a[120:123], v[124:127], v[48:51]\n\t" "ds_read_b128 v[112:115], v81 offset:33792\n\t" \
    "v_mfma_f32_16x16x32_bf16 v[52:55], a[124:127], v[124:127], v[52:55]\n\t" "s_waitcnt lgkmcnt(4)\n\t" \
    "v_mfma_f32_16x16x32_bf16 v[56:59], a[120:123], v[128:131], v[56:59]\n\t" "ds_read_b128 v[116:119], v82 offset:1024\n\t" \
    "v_mfma_f32_16x16x32_bf16 v[60:63], a[124:127], v[128:131], v[60:63]\n\t" "s_waitcnt lgkmcnt(4)\n\t" \
    "v_mfma_f32_16x16x32_bf16 v[48:51], a[128:131], v[100:103], v[48:51]\n\t" "ds_read_b128 v[120:123], v82 offset:33792\n\t" \
    "v_mfma_f32_16x16x32_bf16 v[52:55], a[132:135], v[100:103], v[52:55]\n\t" "s_waitcnt lgkmcnt(4)\n\t" \
    "v_mfma_f32_16x16x32_bf16 v[56:59], a[128:131], v[104:107], v[56:59]\n\t" "ds_read_b128 v[124:127], v83 offset:1024\n\t" \
    "v_mfma_f32_16x16x32_bf16 v[60:63], a[132:135], v[104:107], v[60:63]\n\t" "s_waitcnt lgkmcnt(4)\n\t" \
    "v_mfma_f32_16x16x32_bf16 v[48:51], a[136:139], v[108:111], v[48:51]\n\t" "ds_read_b128 v[128:131], v83 offset:33792\n\t" \
    "v_mfma_f32_16x16x32_bf16 v[52:55], a[140:143], v[108:111], v[52:55]\n\t" "s_waitcnt lgkmcnt(4)\n\t" \
    "v_mfma_f32_16x16x32_bf16 v[56:59], a[136:139], v[112:115], v[56:59]\n\t" "ds_read_b128 v[100:103], v84 offset:1024\n\t" \
    "v_mfma_f32_16x16x32_bf16 v[60:63], a[140:143], v[112:115], v[60:63]\n\t" "s_waitcnt lgkmcnt(4)\n\t" \
    "v_mfma_f32_16x16x32_bf16 v[48:51], a[144:147], v[116:119], v[48:51]\n\t" "ds_read_b128 v[104:107], v84 offset:33792\n\t" \
    "v_mfma_f32_16x16x32_bf16 v[52:55], a[148:151], v[116:119], v[52:55]\n\t" "s_waitcnt lgkmcnt(4)\n\t" \
    "v_mfma_f32_16x16x32_bf16 v[56:59], a[144:147], v[120:123], v[56:59]\n\t" "ds_read_b128 v[108:111], v85 offset:1024\n\t" \
    "v_mfma_f32_16x16x32_bf16 v[60:63], a[148:151], v[120:123], v[60:63]\n\t" "s_waitcnt lgkmcnt(4)\n\t" \
    "v_mfma_f32_16x16x32_bf16 v[48:51], a[152:155], v[124:127], v[48:51]\n\t" "ds_read_b128 v[112:115], v85 offset:33792\n\t" \
    "v_mfma_f32_16x16x32_bf16 v[52:55], a[156:159], v[124:127], v[52:55]\n\t" "s_waitcnt lgkmcnt(4)\n\t" \
    "v_mfma_f32_16x16x32_bf16 v[56:59], a[152:155], v[128:131], v[56:59]\n\t" "ds_read_b128 v[116:119], v86 offset:1024\n\t" \
    "v_mfma_f32_16x16x32_bf16 v[60:63], a[156:159], v[128:131], v[60:63]\n\t" "s_waitcnt lgkmcnt(4)\n\t" \
    "v_mfma_f32_16x16x32_bf16 v[48:51], a[160:163], v[100:103], v[48:51]\n\t" "ds_read_b128 v[120:123], v86 offset:33792\n\t" \
    "v_mfma_f32_16x16x32_bf16 v[52:55], a[164:167], v[100:103], v[52:55]\n\t" "s_waitcnt lgkmcnt(4)\n\t" \
    "v_mfma_f32_16x16x32_bf16 v[56:59], a[160:163], v[104:107], v[56:59]\n\t" "ds_read_b128 v[124:127], v87 offset:1024\n\t" \
    "v_mfma_f32_16x16x32_bf16 v[60:63], a[164:167], v[104:107], v[60:63]\n\t" "s_waitcnt lgkmcnt(4)\n\t" \
    "v_mfma_f32_16x16x32_bf16 v[48:51], a[168:171], v[108:111], v[48:51]\n\t" "ds_read_b128 v[128:131], v87 offset:33792\n\t" \
    "v_mfma_f32_16x16x32_bf16 v[52:55], a[172:175], v[108:111], v[52:55]\n\t" "s_waitcnt lgkmcnt(4)\n\t" \
    "v_mfma_f32_16x16x32_bf16 v[56:59], a[168:171], v[112:115], v[56:59]\n\t" "ds_read_b128 v[100:103], v80 offset:1536\n\t" \
    "v_mfma_f32_16x16x32_bf16 v[60:63], a[172:175], v[112:115], v[60:63]\n\t" "s_waitcnt lgkmcnt(4)\n\t" \
    "v_mfma_f32_16x16x32_bf16 v[48:51], a[176:179], v[116:119], v[48:51]\n\t" "ds_read_b128 v[104:107], v80 offset:34304\n\t" \
    "v_mfma_f32_16x16x32_bf16 v[52:55], a[180:183], v[116:119], v[52:55]\n\t" "s_waitcnt lgkmcnt(4)\n\t" \
    "v_mfma_f32_16x16x32_bf16 v[56:59], a[176:179], v[120:123], v[56:59]\n\t" "ds_read_b128 v[108:111], v81 offset:1536\n\t" \
    "v_mfma_f32_16x16x32_bf16 v[60:63], a[180:183], v[120:123], v[60:63]\n\t" "s_waitcnt lgkmcnt(4)\n\t" \
    "v_mfma_f32_16x16x32_bf16 v[48:51], a[184:187], v[124:127], v[48:51]\n\t" "ds_read_b128 v[112:115], v81 offset:34304\n\t" \
    "v_mfma_f32_16x16x32_bf16 v[52:55], a[188:191], v[124:127], v[52:55]\n\t" "s_waitcnt lgkmcnt(4)\n\t" \
    "v_mfma_f32_16x16x32_bf16 v[56:59], a[184:187], v[128:131], v[56:59]\n\t" "ds_read_b128 v[116:119], v82 offset:1536\n\t" \
    "v_mfma_f32_16x16x32_bf16 v[60:63], a[188:191], v[128:131], v[60:63]\n\t" "s_waitcnt lgkmcnt(4)\n\t" \
    "v_mfma_f32_16x16x32_bf16 v[48:51], a[192:195], v[100:103], v[48:51]\n\t" "ds_read_b128 v[120:123], v82 offset:34304\n\t" \
    "v_mfma_f32_16x16x32_bf16 v[52:55], a[196:199], v[100:103], v[52:55]\n\t" "s_waitcnt lgkmcnt(4)\n\t" \
    "v_mfma_f32_16x16x32_bf16 v[56:59], a[192:195], v[104:107], v[56:59]\n\t" "ds_read_b128 v[124:127], v83 offset:1536\n\t" \
    "v_mfma_f32_16x16x32_bf16 v[60:63], a[196:199], v[104:107], v[60:63]\n\t" "s_waitcnt lgkmcnt(4)\n\t" \
    "v_mfma_f32_16x16x32_bf16 v[48:51], a[200:203], v[108:111], v[48:51]\n\t" "ds_read_b128 v[128:131], v83 offset:34304\n\t" \
    "v_mfma_f32_16x16x32_bf16 v[52:55], a[204:207], v[108:111], v[52:55]\n\t" "s_waitcnt lgkmcnt(4)\n\t" \
    "v_mfma_f32_16x16x32_bf16 v[56:59], a[200:203], v[112:115], v[56:59]\n\t" "ds_read_b128 v[100:103], v84 offset:1536\n\t" \
    "v_mfma_f32_16x16x32_bf16 v[60:63], a[204:207], v[112:115], v[60:63]\n\t" "s_waitcnt lgkmcnt(4)\n\t" \
    "v_mfma_f32_16x16x32_bf16 v[48:51], a[208:211], v[116:119], v[48:51]\n\t" "ds_read_b128 v[104:107], v84 offset:34304\n\t" \
    "v_mfma_f32_16x16x32_bf16 v[52:55], a[212:215], v[116:119], v[52:55]\n\t" "s_waitcnt lgkmcnt(4)\n\t" \
    "v_mfma_f32_16x16x32_bf16 v[56:59], a[208:211], v[120:123], v[56:59]\n\t" "ds_read_b128 v[108:111], v85 offset:1536\n\t" \
    "v_mfma_f32_16x16x32_bf16 v[60:63], a[212:215], v[120:123], v[60:63]\n\t" "s_waitcnt lgkmcnt(4)\n\t" \
    "v_mfma_f32_16x16x32_bf16 v[48:51], a[216:219], v[124:127], v[48:51]\n\t" "ds_read_b128 v[112:115], v85 offset:34304\n\t" \
    "v_mfma_f32_16x16x32_bf16 v[52:55], a[220:223], v[124:127], v[52:55]\n\t" "s_waitcnt lgkmcnt(4)\n\t" \
    "v_mfma_f32_16x16x32_bf16 v[56:59], a[216:219], v[128:131], v[56:59]\n\t" "ds_read_b128 v[116:119], v86 offset:1536\n\t" \
    "v_mfma_f32_16x16x32_bf16 v[60:63], a[220:223], v[128:131], v[60:63]\n\t" "s_waitcnt lgkmcnt(4)\n\t" \
    "v_mfma_f32_16x16x32_bf16 v[48:51], a[224:227], v[100:103], v[48:51]\n\t" "ds_read_b128 v[120:123], v86 offset:34304\n\t" \
    "v_mfma_f32_16x16x32_bf16 v[52:55], a[228:231], v[100:103], v[52:55]\n\t" "s_waitcnt lgkmcnt(4)\n\t" \
    "v_mfma_f32_16x16x32_bf16 v[56:59], a[224:227], v[104:107], v[56:59]\n\t" "ds_read_b128 v[124:127], v87 offset:1536\n\t" \
    "v_mfma_f32_16x16x32_bf16 v[60:63], a[228:231], v[104:107], v[60:63]\n\t" "s_waitcnt lgkmcnt(4)\n\t" \
    "v_mfma_f32_16x16x32_bf16 v[48:51], a[232:235], v[108:111], v[48:51]\n\t" "ds_read_b128 v[128:131], v87 offset:34304\n\t" \
    "v_mfma_f32_16x16x32_bf16 v[52:55], a[236:239], v[108:111], v[52:55]\n\t" "s_waitcnt lgkmcnt(4)\n\t" \
    "v_mfma_f32_16x16x32_bf16 v[56:59], a[232:235], v[112:115], v[56:59]\n\t" "v_mfma_f32_16x16x32_bf16 v[60:63], a[236:239], v[112:115], v[60:63]\n\t" \
    "s_waitcnt lgkmcnt(3)\n\t" "v_mfma_f32_16x16x32_bf16 v[48:51], a[240:243], v[116:119], v[48:51]\n\t" \
    "v_mfma_f32_16x16x32_bf16 v[52:55], a[244:247], v[116:119], v[52:55]\n\t" "s_waitcnt vmcnt(1) lgkmcnt(0)\n\t" "s_barrier\n\t" XT(82, 83) \
    "v_mfma_f32_16x16x32_bf16 v[56:59], a[240:243], v[120:123], v[56:59]\n\t" "ds_read_b128 v[100:103], v72 offset:0\n\t" \
    "v_mfma_f32_16x16x32_bf16 v[60:63], a[244:247], v[120:123], v[60:63]\n\t" "v_mfma_f32_16x16x32_bf16 v[48:51], a[248:251], v[124:127], v[48:51]\n\t" \
    "ds_read_b128 v[104:107], v72 offset:32768\n\t" "v_mfma_f32_16x16x32_bf16 v[52:55], a[252:255], v[124:127], v[52:55]\n\t" "ds_read_b128 v[108:111], v73 offset:0\n\t" \
    "v_mfma_f32_16x16x32_bf16 v[56:59], a[248:251], v[128:131], v[56:59]\n\t" "ds_read_b128 v[112:115], v73 offset:32768\n\t" "ds_read_b128 v[116:119], v74 offset:0\n\t" \
    "v_mfma_f32_16x16x32_bf16 v[60:63], a[252:255], v[128:131], v[60:63]\n\t" "s_add_u32 s60, s60, 1\n\t" GSUM "s_cmp_lt_u32 s60, 64\n\t" "s_cbranch_scc1 10b\n\t" \
    "s_nop 7\n\t" "v_cvt_pk_bf16_f32 v64, v48, v49\n\t" "v_cvt_pk_bf16_f32 v65, v50, v51\n\t" "v_cvt_pk_bf16_f32 v66, v52, v53\n\t" "v_cvt_pk_bf16_f32 v67, v54, v55\n\t" \
    "v_cvt_pk_bf16_f32 v68, v56, v57\n\t" "v_cvt_pk_bf16_f32 v69, v58, v59\n\t" "v_cvt_pk_bf16_f32 v70, v60, v61\n\t" "v_cvt_pk_bf16_f32 v71, v62, v63\n\t" \
    "buffer_store_dwordx4 v[64:67], v96, %[rs], s62 offen offset:0 sc1\n\t" "buffer_store_dwordx4 v[68:71], v96, %[rs], s62 offen offset:256 sc1\n\t" "s_waitcnt vmcnt(0)\n\t" \
    "s_barrier\n\t" "v_mov_b32 v98, s60\n\t" "buffer_store_dword v98, %[fo], %[rc], %[fl] offen sc1\n\t" "s_waitcnt lgkmcnt(0)\n\t" "s_branch 30f\n\t" "20:\n\t" \
    "v_mov_b32 v96, %[so]\n\t" "s_nop 7\n\t" "v_cvt_pk_bf16_f32 v64, v32, v33\n\t" "v_cvt_pk_bf16_f32 v65, v34, v35\n\t" "v_cvt_pk_bf16_f32 v66, v36, v37\n\t" \
    "v_cvt_pk_bf16_f32 v67, v38, v39\n\t" "v_cvt_pk_bf16_f32 v68, v40, v41\n\t" "v_cvt_pk_bf16_f32 v69, v42, v43\n\t" "v_cvt_pk_bf16_f32 v70, v44, v45\n\t" \
    "v_cvt_pk_bf16_f32 v71, v46, v47\n\t" "buffer_store_dwordx4 v[64:67], v96, %[rs], s62 offen offset:0 sc1\n\t" \
    "buffer_store_dwordx4 v[68:71], v96, %[rs], s62 offen offset:256 sc1\n\t" "s_waitcnt vmcnt(0)\n\t" "s_barrier\n\t" "v_mov_b32 v98, s60\n\t" \
    "buffer_store_dword v98, %[fo], %[rc], %[fl] offen sc1\n\t" "s_branch 11b\n\t" "30:\n\t"
#define ZC_LOOP \
    ".p2align 6\n\t" "s_mov_b32 s60, 0\n\t" "s_mov_b32 s61, 0\n\t" "s_mov_b32 s62, 0\n\t" "s_mov_b32 s63, 0x7f000000\n\t" "s_cmp_ge_u32 s61, %[z0]\n\t" \
    "s_cbranch_scc1 52f\n\t" "51:\n\t" "buffer_load_dword v120, %[fo], %[rc], %[fv] offen sc1\n\t" "buffer_load_dword v121, %[fo], %[rc], %[vd] offen sc1\n\t" \
    "s_waitcnt vmcnt(0)\n\t" "v_readfirstlane_b32 s61, v120\n\t" "v_readfirstlane_b32 s66, v121\n\t" "s_nop 3\n\t" "s_cmp_ge_u32 s61, %[z0]\n\t" "s_cbranch_scc1 52f\n\t" \
    "s_bitcmp1_b32 s66, 1\n\t" "s_cbranch_scc1 90f\n\t" "s_cmp_eq_u32 %[bd], 0\n\t" "s_cbranch_scc1 52f\n\t" "s_sub_u32 %[bd], %[bd], 1\n\t" "s_sleep 2\n\t" \
    "s_branch 51b\n\t" "52:\n\t" "s_lshr_b32 s66, s60, 2\n\t" "s_xor_b32 s66, s66, %[xp]\n\t" "s_and_b32 s66, s66, 1\n\t" "s_lshl_b32 s66, s66, 13\n\t" \
    "s_lshl_b32 s64, s60, 19\n\t" "s_add_u32 s64, s64, s66\n\t" "s_add_u32 s64, s64, %[vb]\n\t" "buffer_load_dwordx4 v[16:19], %[ao], %[rs], s64 offen offset:0 sc1\n\t" \
    "buffer_load_dwordx4 v[20:23], %[ao], %[rs], s64 offen offset:256 sc1\n\t" "buffer_load_dwordx4 v[24:27], %[ao], %[rs], s64 offen offset:2048 sc1\n\t" \
    "buffer_load_dwordx4 v[28:31], %[ao], %[rs], s64 offen offset:2304 sc1\n\t" "buffer_load_dwordx4 v[32:35], %[a1], %[rs], s64 offen offset:0 sc1\n\t" \
    "buffer_load_dwordx4 v[36:39], %[a1], %[rs], s64 offen offset:256 sc1\n\t" "buffer_load_dwordx4 v[40:43], %[a1], %[rs], s64 offen offset:2048 sc1\n\t" \
    "buffer_load_dwordx4 v[44:47], %[a1], %[rs], s64 offen offset:2304 sc1\n\t" "s_waitcnt vmcnt(0)\n\t" "10:\n\t" XT(80, 81) "s_add_u32 s66, s62, %[rg]\n\t" \
    "s_cmp_lt_u32 s66, s60\n\t" "s_cbranch_scc1 60f\n\t" "63:\n\t" "s_cmp_lt_u32 s60, 12\n\t" "s_cbranch_scc1 40f\n\t" "s_add_u32 s66, s60, 1\n\t" "s_cmp_gt_u32 s61, s66\n\t" \
    "s_cbranch_scc0 40f\n\t" "v_mfma_f32_16x16x32_bf16 v[80:83], v[16:19], a[0:3], 0\n\t" "v_mfma_f32_16x16x32_bf16 v[84:87], v[20:23], a[0:3], 0\n\t" \
    "v_mfma_f32_16x16x32_bf16 v[80:83], v[24:27], a[4:7], v[80:83]\n\t" "v_mfma_f32_16x16x32_bf16 v[84:87], v[28:31], a[4:7], v[84:87]\n\t" \
    "v_mfma_f32_16x16x32_bf16 v[80:83], v[32:35], a[8:11], v[80:83]\n\t" "v_cvt_pk_bf16_f32 v112, v96, v97\n\t" \
    "v_mfma_f32_16x16x32_bf16 v[84:87], v[36:39], a[8:11], v[84:87]\n\t" "v_cvt_pk_bf16_f32 v113, v98, v99\n\t" \
    "v_mfma_f32_16x16x32_bf16 v[80:83], v[40:43], a[12:15], v[80:83]\n\t" "v_cvt_pk_bf16_f32 v114, v100, v101\n\t" \
    "v_mfma_f32_16x16x32_bf16 v[84:87], v[44:47], a[12:15], v[84:87]\n\t" "v_cvt_pk_bf16_f32 v115, v102, v103\n\t" \
    "v_mfma_f32_16x16x32_bf16 v[88:91], v[16:19], a[16:19], 0\n\t" "v_cvt_pk_bf16_f32 v116, v104, v105\n\t" "v_mfma_f32_16x16x32_bf16 v[92:95], v[20:23], a[16:19], 0\n\t" \
    "v_cvt_pk_bf16_f32 v117, v106, v107\n\t" "v_mfma_f32_16x16x32_bf16 v[88:91], v[24:27], a[20:23], v[88:91]\n\t" \
    "buffer_store_dwordx4 v[112:115], %[z1], %[rs], s63 offen sc1\n\t" "v_mfma_f32_16x16x32_bf16 v[92:95], v[28:31], a[20:23], v[92:95]\n\t" \
    "v_mfma_f32_16x16x32_bf16 v[88:91], v[32:35], a[24:27], v[88:91]\n\t" "v_cvt_pk_bf16_f32 v118, v108, v109\n\t" \
    "v_mfma_f32_16x16x32_bf16 v[92:95], v[36:39], a[24:27], v[92:95]\n\t" "v_cvt_pk_bf16_f32 v119, v110, v111\n\t" \
    "v_mfma_f32_16x16x32_bf16 v[88:91], v[40:43], a[28:31], v[88:91]\n\t" "buffer_store_dwordx4 v[116:119], %[z2], %[rs], s63 offen sc1\n\t" "s_and_b32 s63, s60, %[rm]\n\t" \
    "v_mfma_f32_16x16x32_bf16 v[92:95], v[44:47], a[28:31], v[92:95]\n\t" "s_lshl_b32 s63, s63, 22\n\t" "s_add_u32 s63, s63, %[zb]\n\t" \
    "v_mfma_f32_16x16x32_bf16 v[96:99], v[16:19], a[32:35], 0\n\t" "s_add_u32 s65, s60, 1\n\t" "s_lshr_b32 s66, s65, 2\n\t" \
    "v_mfma_f32_16x16x32_bf16 v[100:103], v[20:23], a[32:35], 0\n\t" "s_xor_b32 s66, s66, %[xp]\n\t" "s_and_b32 s66, s66, 1\n\t" \
    "v_mfma_f32_16x16x32_bf16 v[96:99], v[24:27], a[36:39], v[96:99]\n\t" "s_lshl_b32 s66, s66, 13\n\t" "s_lshl_b32 s64, s65, 19\n\t" \
    "v_mfma_f32_16x16x32_bf16 v[100:103], v[28:31], a[36:39], v[100:103]\n\t" "s_add_u32 s64, s64, s66\n\t" "s_add_u32 s64, s64, %[vb]\n\t" \
    "v_mfma_f32_16x16x32_bf16 v[96:99], v[32:35], a[40:43], v[96:99]\n\t" "buffer_load_dwordx4 v[48:51], %[ao], %[rs], s64 offen offset:0 sc1\n\t" \
    "v_cvt_pk_bf16_f32 v112, v80, v81\n\t" "v_mfma_f32_16x16x32_bf16 v[100:103], v[36:39], a[40:43], v[100:103]\n\t" "v_cvt_pk_bf16_f32 v113, v82, v83\n\t" \
    "v_mfma_f32_16x16x32_bf16 v[96:99], v[40:43], a[44:47], v[96:99]\n\t" "buffer_load_dwordx4 v[52:55], %[ao], %[rs], s64 offen offset:256 sc1\n\t" \
    "v_cvt_pk_bf16_f32 v114, v84, v85\n\t" "v_mfma_f32_16x16x32_bf16 v[100:103], v[44:47], a[44:47], v[100:103]\n\t" "v_cvt_pk_bf16_f32 v115, v86, v87\n\t" \
    "v_mfma_f32_16x16x32_bf16 v[104:107], v[16:19], a[48:51], 0\n\t" "buffer_load_dwordx4 v[56:59], %[ao], %[rs], s64 offen offset:2048 sc1\n\t" \
    "v_cvt_pk_bf16_f32 v116, v88, v89\n\t" "v_mfma_f32_16x16x32_bf16 v[108:111], v[20:23], a[48:51], 0\n\t" "v_cvt_pk_bf16_f32 v117, v90, v91\n\t" \
    "v_mfma_f32_16x16x32_bf16 v[104:107], v[24:27], a[52:55], v[104:107]\n\t" "buffer_load_dwordx4 v[60:63], %[ao], %[rs], s64 offen offset:2304 sc1\n\t" \
    "v_mfma_f32_16x16x32_bf16 v[108:111], v[28:31], a[52:55], v[108:111]\n\t" "buffer_store_dwordx4 v[112:115], %[z1], %[rs], s63 offen sc1\n\t" \
    "v_mfma_f32_16x16x32_bf16 v[104:107], v[32:35], a[56:59], v[104:107]\n\t" "buffer_load_dwordx4 v[64:67], %[a1], %[rs], s64 offen offset:0 sc1\n\t" \
    "v_cvt_pk_bf16_f32 v118, v92, v93\n\t" "v_mfma_f32_16x16x32_bf16 v[108:111], v[36:39], a[56:59], v[108:111]\n\t" "v_cvt_pk_bf16_f32 v119, v94, v95\n\t" \
    "v_mfma_f32_16x16x32_bf16 v[104:107], v[40:43], a[60:63], v[104:107]\n\t" "buffer_load_dwordx4 v[68:71], %[a1], %[rs], s64 offen offset:256 sc1\n\t" \
    "buffer_store_dwordx4 v[116:119], %[z2], %[rs], s63 offen sc1\n\t" "v_mfma_f32_16x16x32_bf16 v[108:111], v[44:47], a[60:63], v[108:111]\n\t" \
    "s_add_u32 s63, s63, 0x20000\n\t" "v_mfma_f32_16x16x32_bf16 v[80:83], v[16:19], a[64:67], 0\n\t" \
    "buffer_load_dwordx4 v[72:75], %[a1], %[rs], s64 offen offset:2048 sc1\n\t" "v_mfma_f32_16x16x32_bf16 v[84:87], v[20:23], a[64:67], 0\n\t" \
    "v_mfma_f32_16x16x32_bf16 v[80:83], v[24:27], a[68:71], v[80:83]\n\t" "buffer_load_dwordx4 v[76:79], %[a1], %[rs], s64 offen offset:2304 sc1\n\t" \
    "v_mfma_f32_16x16x32_bf16 v[84:87], v[28:31], a[68:71], v[84:87]\n\t" "v_mfma_f32_16x16x32_bf16 v[80:83], v[32:35], a[72:75], v[80:83]\n\t" \
    "buffer_load_dword v120, %[fo], %[rc], %[fv] offen sc1\n\t" "v_cvt_pk_bf16_f32 v112, v96, v97\n\t" "v_mfma_f32_16x16x32_bf16 v[84:87], v[36:39], a[72:75], v[84:87]\n\t" \
    "v_cvt_pk_bf16_f32 v113, v98, v99\n\t" "v_mfma_f32_16x16x32_bf16 v[80:83], v[40:43], a[76:79], v[80:83]\n\t" "buffer_load_dword v121, %[fo], %[rc], %[fs] offen sc1\n\t" \
    "v_mfma_f32_16x16x32_bf16 v[84:87], v[44:47], a[76:79], v[84:87]\n\t" "v_cvt_pk_bf16_f32 v114, v100, v101\n\t" "v_cvt_pk_bf16_f32 v115, v102, v103\n\t" \
    "v_mfma_f32_16x16x32_bf16 v[88:91], v[16:19], a[80:83], 0\n\t" "v_cvt_pk_bf16_f32 v116, v104, v105\n\t" "v_mfma_f32_16x16x32_bf16 v[92:95], v[20:23], a[80:83], 0\n\t" \
    "v_cvt_pk_bf16_f32 v117, v106, v107\n\t" "v_mfma_f32_16x16x32_bf16 v[88:91], v[24:27], a[84:87], v[88:91]\n\t" \
    "buffer_store_dwordx4 v[112:115], %[z1], %[rs], s63 offen sc1\n\t" "v_mfma_f32_16x16x32_bf16 v[92:95], v[28:31], a[84:87], v[92:95]\n\t" \
    "v_mfma_f32_16x16x32_bf16 v[88:91], v[32:35], a[88:91], v[88:91]\n\t" "v_cvt_pk_bf16_f32 v118, v108, v109\n\t" \
    "v_mfma_f32_16x16x32_bf16 v[92:95], v[36:39], a[88:91], v[92:95]\n\t" "v_cvt_pk_bf16_f32 v119, v110, v111\n\t" \
    "v_mfma_f32_16x16x32_bf16 v[88:91], v[40:43], a[92:95], v[88:91]\n\t" "buffer_store_dwordx4 v[116:119], %[z2], %[rs], s63 offen sc1\n\t" \
    "v_mfma_f32_16x16x32_bf16 v[92:95], v[44:47], a[92:95], v[92:95]\n\t" "s_add_u32 s63, s63, 0x20000\n\t" "v_mfma_f32_16x16x32_bf16 v[96:99], v[16:19], a[96:99], 0\n\t" \
    "v_mfma_f32_16x16x32_bf16 v[100:103], v[20:23], a[96:99], 0\n\t" "v_mfma_f32_16x16x32_bf16 v[96:99], v[24:27], a[100:103], v[96:99]\n\t" \
    "v_mfma_f32_16x16x32_bf16 v[100:103], v[28:31], a[100:103], v[100:103]\n\t" "v_mfma_f32_16x16x32_bf16 v[96:99], v[32:35], a[104:107], v[96:99]\n\t" \
    "v_cvt_pk_bf16_f32 v112, v80, v81\n\t" "v_mfma_f32_16x16x32_bf16 v[100:103], v[36:39], a[104:107], v[100:103]\n\t" "v_cvt_pk_bf16_f32 v113, v82, v83\n\t" \
    "v_mfma_f32_16x16x32_bf16 v[96:99], v[40:43], a[108:111], v[96:99]\n\t" "v_cvt_pk_bf16_f32 v114, v84, v85\n\t" \
    "v_mfma_f32_16x16x32_bf16 v[100:103], v[44:47], a[108:111], v[100:103]\n\t" "v_cvt_pk_bf16_f32 v115, v86, v87\n\t" \
    "v_mfma_f32_16x16x32_bf16 v[104:107], v[16:19], a[112:115], 0\n\t" "v_cvt_pk_bf16_f32 v116, v88, v89\n\t" \
    "v_mfma_f32_16x16x32_bf16 v[108:111], v[20:23], a[112:115], 0\n\t" "v_cvt_pk_bf16_f32 v117, v90, v91\n\t" \
    "v_mfma_f32_16x16x32_bf16 v[104:107], v[24:27], a[116:119], v[104:107]\n\t" "buffer_store_dwordx4 v[112:115], %[z1], %[rs], s63 offen sc1\n\t" \
    "v_mfma_f32_16x16x32_bf16 v[108:111], v[28:31], a[116:119], v[108:111]\n\t" "v_mfma_f32_16x16x32_bf16 v[104:107], v[32:35], a[120:123], v[104:107]\n\t" \
    "v_cvt_pk_bf16_f32 v118, v92, v93\n\t" "v_mfma_f32_16x16x32_bf16 v[108:111], v[36:39], a[120:123], v[108:111]\n\t" "v_cvt_pk_bf16_f32 v119, v94, v95\n\t" \
    "v_mfma_f32_16x16x32_bf16 v[104:107], v[40:43], a[124:127], v[104:107]\n\t" "buffer_store_dwordx4 v[116:119], %[z2], %[rs], s63 offen sc1\n\t" \
    "v_mfma_f32_16x16x32_bf16 v[108:111], v[44:47], a[124:127], v[108:111]\n\t" "s_add_u32 s63, s63, 0x20000\n\t" \
    "v_mfma_f32_16x16x32_bf16 v[80:83], v[16:19], a[128:131], 0\n\t" "v_mfma_f32_16x16x32_bf16 v[84:87], v[20:23], a[128:131], 0\n\t" \
    "v_mfma_f32_16x16x32_bf16 v[80:83], v[24:27], a[132:135], v[80:83]\n\t" "v_mfma_f32_16x16x32_bf16 v[84:87], v[28:31], a[132:135], v[84:87]\n\t" \
    "v_mfma_f32_16x16x32_bf16 v[80:83], v[32:35], a[136:139], v[80:83]\n\t" "v_cvt_pk_bf16_f32 v112, v96, v97\n\t" \
    "v_mfma_f32_16x16x32_bf16 v[84:87], v[36:39], a[136:139], v[84:87]\n\t" "v_cvt_pk_bf16_f32 v113, v98, v99\n\t" \
    "v_mfma_f32_16x16x32_bf16 v[80:83], v[40:43], a[140:143], v[80:83]\n\t" "v_cvt_pk_bf16_f32 v114, v100, v101\n\t" \
    "v_mfma_f32_16x16x32_bf16 v[84:87], v[44:47], a[140:143], v[84:87]\n\t" "v_cvt_pk_bf16_f32 v115, v102, v103\n\t" \
    "v_mfma_f32_16x16x32_bf16 v[88:91], v[16:19], a[144:147], 0\n\t" "v_cvt_pk_bf16_f32 v116, v104, v105\n\t" "v_mfma_f32_16x16x32_bf16 v[92:95], v[20:23], a[144:147], 0\n\t" \
    "v_cvt_pk_bf16_f32 v117, v106, v107\n\t" "v_mfma_f32_16x16x32_bf16 v[88:91], v[24:27], a[148:151], v[88:91]\n\t" \
    "buffer_store_dwordx4 v[112:115], %[z1], %[rs], s63 offen sc1\n\t" "v_mfma_f32_16x16x32_bf16 v[92:95], v[28:31], a[148:151], v[92:95]\n\t" \
    "v_mfma_f32_16x16x32_bf16 v[88:91], v[32:35], a[152:155], v[88:91]\n\t" "v_cvt_pk_bf16_f32 v118, v108, v109\n\t" \
    "v_mfma_f32_16x16x32_bf16 v[92:95], v[36:39], a[152:155], v[92:95]\n\t" "v_cvt_pk_bf16_f32 v119, v110, v111\n\t" \
    "v_mfma_f32_16x16x32_bf16 v[88:91], v[40:43], a[156:159], v[88:91]\n\t" "buffer_store_dwordx4 v[116:119], %[z2], %[rs], s63 offen sc1\n\t" \
    "v_mfma_f32_16x16x32_bf16 v[92:95], v[44:47], a[156:159], v[92:95]\n\t" "s_add_u32 s63, s63, 0x20000\n\t" "v_mfma_f32_16x16x32_bf16 v[96:99], v[16:19], a[160:163], 0\n\t" \
    "v_mfma_f32_16x16x32_bf16 v[100:103], v[20:23], a[160:163], 0\n\t" "v_mfma_f32_16x16x32_bf16 v[96:99], v[24:27], a[164:167], v[96:99]\n\t" \
    "v_mfma_f32_16x16x32_bf16 v[100:103], v[28:31], a[164:167], v[100:103]\n\t" "v_mfma_f32_16x16x32_bf16 v[96:99], v[32:35], a[168:171], v[96:99]\n\t" \
    "v_cvt_pk_bf16_f32 v112, v80, v81\n\t" "v_mfma_f32_16x16x32_bf16 v[100:103], v[36:39], a[168:171], v[100:103]\n\t" "v_cvt_pk_bf16_f32 v113, v82, v83\n\t" \
    "v_mfma_f32_16x16x32_bf16 v[96:99], v[40:43], a[172:175], v[96:99]\n\t" "v_cvt_pk_bf16_f32 v114, v84, v85\n\t" \
    "v_mfma_f32_16x16x32_bf16 v[100:103], v[44:47], a[172:175], v[100:103]\n\t" "v_cvt_pk_bf16_f32 v115, v86, v87\n\t" \
    "v_mfma_f32_16x16x32_bf16 v[104:107], v[16:19], a[176:179], 0\n\t" "v_cvt_pk_bf16_f32 v116, v88, v89\n\t" \
    "v_mfma_f32_16x16x32_bf16 v[108:111], v[20:23], a[176:179], 0\n\t" "v_cvt_pk_bf16_f32 v117, v90, v91\n\t" \
    "v_mfma_f32_16x16x32_bf16 v[104:107], v[24:27], a[180:183], v[104:107]\n\t" "buffer_store_dwordx4 v[112:115], %[z1], %[rs], s63 offen sc1\n\t" \
    "v_mfma_f32_16x16x32_bf16 v[108:111], v[28:31], a[180:183], v[108:111]\n\t" "v_mfma_f32_16x16x32_bf16 v[104:107], v[32:35], a[184:187], v[104:107]\n\t" \
    "v_cvt_pk_bf16_f32 v118, v92, v93\n\t" "v_mfma_f32_16x16x32_bf16 v[108:111], v[36:39], a[184:187], v[108:111]\n\t" "v_cvt_pk_bf16_f32 v119, v94, v95\n\t" \
    "v_mfma_f32_16x16x32_bf16 v[104:107], v[40:43], a[188:191], v[104:107]\n\t" "buffer_store_dwordx4 v[116:119], %[z2], %[rs], s63 offen sc1\n\t" \
    "v_mfma_f32_16x16x32_bf16 v[108:111], v[44:47], a[188:191], v[108:111]\n\t" "s_add_u32 s63, s63, 0x20000\n\t" \
    "v_mfma_f32_16x16x32_bf16 v[80:83], v[16:19], a[192:195], 0\n\t" "v_mfma_f32_16x16x32_bf16 v[84:87], v[20:23], a[192:195], 0\n\t" \
    "v_mfma_f32_16x16x32_bf16 v[80:83], v[24:27], a[196:199], v[80:83]\n\t" "v_mfma_f32_16x16x32_bf16 v[84:87], v[28:31], a[196:199], v[84:87]\n\t" \
    "v_mfma_f32_16x16x32_bf16 v[80:83], v[32:35], a[200:203], v[80:83]\n\t" "v_cvt_pk_bf16_f32 v112, v96, v97\n\t" \
    "v_mfma_f32_16x16x32_bf16 v[84:87], v[36:39], a[200:203], v[84:87]\n\t" "v_cvt_pk_bf16_f32 v113, v98, v99\n\t" \
    "v_mfma_f32_16x16x32_bf16 v[80:83], v[40:43], a[204:207], v[80:83]\n\t" "v_cvt_pk_bf16_f32 v114, v100, v101\n\t" \
    "v_mfma_f32_16x16x32_bf16 v[84:87], v[44:47], a[204:207], v[84:87]\n\t" "v_cvt_pk_bf16_f32 v115, v102, v103\n\t" \
    "v_mfma_f32_16x16x32_bf16 v[88:91], v[16:19], a[208:211], 0\n\t" "v_cvt_pk_bf16_f32 v116, v104, v105\n\t" "v_mfma_f32_16x16x32_bf16 v[92:95], v[20:23], a[208:211], 0\n\t" \
    "v_cvt_pk_bf16_f32 v117, v106, v107\n\t" "v_mfma_f32_16x16x32_bf16 v[88:91], v[24:27], a[212:215], v[88:91]\n\t" \
    "buffer_store_dwordx4 v[112:115], %[z1], %[rs], s63 offen sc1\n\t" "v_mfma_f32_16x16x32_bf16 v[92:95], v[28:31], a[212:215], v[92:95]\n\t" \
    "v_mfma_f32_16x16x32_bf16 v[88:91], v[32:35], a[216:219], v[88:91]\n\t" "v_cvt_pk_bf16_f32 v118, v108, v109\n\t" \
    "v_mfma_f32_16x16x32_bf16 v[92:95], v[36:39], a[216:219], v[92:95]\n\t" "v_cvt_pk_bf16_f32 v119, v110, v111\n\t" \
    "v_mfma_f32_16x16x32_bf16 v[88:91], v[40:43], a[220:223], v[88:91]\n\t" "buffer_store_dwordx4 v[116:119], %[z2], %[rs], s63 offen sc1\n\t" \
    "v_mfma_f32_16x16x32_bf16 v[92:95], v[44:47], a[220:223], v[92:95]\n\t" "s_add_u32 s63, s63, 0x20000\n\t" "v_mfma_f32_16x16x32_bf16 v[96:99], v[16:19], a[224:227], 0\n\t" \
    "v_mfma_f32_16x16x32_bf16 v[100:103], v[20:23], a[224:227], 0\n\t" "v_mfma_f32_16x16x32_bf16 v[96:99], v[24:27], a[228:231], v[96:99]\n\t" \
    "v_mfma_f32_16x16x32_bf16 v[100:103], v[28:31], a[228:231], v[100:103]\n\t" "v_mfma_f32_16x16x32_bf16 v[96:99], v[32:35], a[232:235], v[96:99]\n\t" \
    "v_cvt_pk_bf16_f32 v112, v80, v81\n\t" "v_mfma_f32_16x16x32_bf16 v[100:103], v[36:39], a[232:235], v[100:103]\n\t" "v_cvt_pk_bf16_f32 v113, v82, v83\n\t" \
    "v_mfma_f32_16x16x32_bf16 v[96:99], v[40:43], a[236:239], v[96:99]\n\t" "v_cvt_pk_bf16_f32 v114, v84, v85\n\t" \
    "v_mfma_f32_16x16x32_bf16 v[100:103], v[44:47], a[236:239], v[100:103]\n\t" "v_cvt_pk_bf16_f32 v115, v86, v87\n\t" \
    "v_mfma_f32_16x16x32_bf16 v[104:107], v[16:19], a[240:243], 0\n\t" "v_cvt_pk_bf16_f32 v116, v88, v89\n\t" \
    "v_mfma_f32_16x16x32_bf16 v[108:111], v[20:23], a[240:243], 0\n\t" "v_cvt_pk_bf16_f32 v117, v90, v91\n\t" \
    "v_mfma_f32_16x16x32_bf16 v[104:107], v[24:27], a[244:247], v[104:107]\n\t" "buffer_store_dwordx4 v[112:115], %[z1], %[rs], s63 offen sc1\n\t" \
    "v_mfma_f32_16x16x32_bf16 v[108:111], v[28:31], a[244:247], v[108:111]\n\t" "v_mfma_f32_16x16x32_bf16 v[104:107], v[32:35], a[248:251], v[104:107]\n\t" \
    "v_cvt_pk_bf16_f32 v118, v92, v93\n\t" "v_cvt_pk_bf16_f32 v119, v94, v95\n\t" "s_waitcnt vmcnt(11)\n\t" "v_readfirstlane_b32 s66, v120\n\t" \
    "v_mfma_f32_16x16x32_bf16 v[108:111], v[36:39], a[248:251], v[108:111]\n\t" "v_readfirstlane_b32 s67, v121\n\t" "v_mov_b32 v122, s60\n\t" \
    "buffer_store_dword v122, %[fo], %[rc], %[fz] offen sc1\n\t" "v_mfma_f32_16x16x32_bf16 v[104:107], v[40:43], a[252:255], v[104:107]\n\t" \
    "buffer_store_dwordx4 v[116:119], %[z2], %[rs], s63 offen sc1\n\t" "s_add_u32 s63, s63, 0x20000\n\t" "s_max_u32 s61, s61, s66\n\t" \
    "v_mfma_f32_16x16x32_bf16 v[108:111], v[44:47], a[252:255], v[108:111]\n\t" "s_max_u32 s62, s62, s67\n\t" "s_add_u32 s60, s60, 1\n\t" ZSUM "23:\n\t" XT(80, 81) \
    "s_add_u32 s66, s62, %[rg]\n\t" "s_cmp_lt_u32 s66, s60\n\t" "s_cbranch_scc1 70f\n\t" "73:\n\t" "s_cmp_lt_u32 s60, 12\n\t" "s_cbranch_scc1 41f\n\t" \
    "s_add_u32 s66, s60, 1\n\t" "s_cmp_gt_u32 s61, s66\n\t" "s_cbranch_scc0 41f\n\t" "v_mfma_f32_16x16x32_bf16 v[80:83], v[48:51], a[0:3], 0\n\t" \
    "v_mfma_f32_16x16x32_bf16 v[84:87], v[52:55], a[0:3], 0\n\t" "v_mfma_f32_16x16x32_bf16 v[80:83], v[56:59], a[4:7], v[80:83]\n\t" \
    "v_mfma_f32_16x16x32_bf16 v[84:87], v[60:63], a[4:7], v[84:87]\n\t" "v_mfma_f32_16x16x32_bf16 v[80:83], v[64:67], a[8:11], v[80:83]\n\t" \
    "v_cvt_pk_bf16_f32 v112, v96, v97\n\t" "v_mfma_f32_16x16x32_bf16 v[84:87], v[68:71], a[8:11], v[84:87]\n\t" "v_cvt_pk_bf16_f32 v113, v98, v99\n\t" \
    "v_mfma_f32_16x16x32_bf16 v[80:83], v[72:75], a[12:15], v[80:83]\n\t" "v_cvt_pk_bf16_f32 v114, v100, v101\n\t" \
    "v_mfma_f32_16x16x32_bf16 v[84:87], v[76:79], a[12:15], v[84:87]\n\t" "v_cvt_pk_bf16_f32 v115, v102, v103\n\t" \
    "v_mfma_f32_16x16x32_bf16 v[88:91], v[48:51], a[16:19], 0\n\t" "v_cvt_pk_bf16_f32 v116, v104, v105\n\t" "v_mfma_f32_16x16x32_bf16 v[92:95], v[52:55], a[16:19], 0\n\t" \
    "v_cvt_pk_bf16_f32 v117, v106, v107\n\t" "v_mfma_f32_16x16x32_bf16 v[88:91], v[56:59], a[20:23], v[88:91]\n\t" \
    "buffer_store_dwordx4 v[112:115], %[z1], %[rs], s63 offen sc1\n\t" "v_mfma_f32_16x16x32_bf16 v[92:95], v[60:63], a[20:23], v[92:95]\n\t" \
    "v_mfma_f32_16x16x32_bf16 v[88:91], v[64:67], a[24:27], v[88:91]\n\t" "v_cvt_pk_bf16_f32 v118, v108, v109\n\t" \
    "v_mfma_f32_16x16x32_bf16 v[92:95], v[68:71], a[24:27], v[92:95]\n\t" "v_cvt_pk_bf16_f32 v119, v110, v111\n\t" \
    "v_mfma_f32_16x16x32_bf16 v[88:91], v[72:75], a[28:31], v[88:91]\n\t" "buffer_store_dwordx4 v[116:119], %[z2], %[rs], s63 offen sc1\n\t" "s_and_b32 s63, s60, %[rm]\n\t" \
    "v_mfma_f32_16x16x32_bf16 v[92:95], v[76:79], a[28:31], v[92:95]\n\t" "s_lshl_b32 s63, s63, 22\n\t" "s_add_u32 s63, s63, %[zb]\n\t" \
    "v_mfma_f32_16x16x32_bf16 v[96:99], v[48:51], a[32:35], 0\n\t" "s_add_u32 s65, s60, 1\n\t" "s_lshr_b32 s66, s65, 2\n\t" \
    "v_mfma_f32_16x16x32_bf16 v[100:103], v[52:55], a[32:35], 0\n\t" "s_xor_b32 s66, s66, %[xp]\n\t" "s_and_b32 s66, s66, 1\n\t" \
    "v_mfma_f32_16x16x32_bf16 v[96:99], v[56:59], a[36:39], v[96:99]\n\t" "s_lshl_b32 s66, s66, 13\n\t" "s_lshl_b32 s64, s65, 19\n\t" \
    "v_mfma_f32_16x16x32_bf16 v[100:103], v[60:63], a[36:39], v[100:103]\n\t" "s_add_u32 s64, s64, s66\n\t" "s_add_u32 s64, s64, %[vb]\n\t" \
    "v_mfma_f32_16x16x32_bf16 v[96:99], v[64:67], a[40:43], v[96:99]\n\t" "buffer_load_dwordx4 v[16:19], %[ao], %[rs], s64 offen offset:0 sc1\n\t" \
    "v_cvt_pk_bf16_f32 v112, v80, v81\n\t" "v_mfma_f32_16x16x32_bf16 v[100:103], v[68:71], a[40:43], v[100:103]\n\t" "v_cvt_pk_bf16_f32 v113, v82, v83\n\t" \
    "v_mfma_f32_16x16x32_bf16 v[96:99], v[72:75], a[44:47], v[96:99]\n\t" "buffer_load_dwordx4 v[20:23], %[ao], %[rs], s64 offen offset:256 sc1\n\t" \
    "v_cvt_pk_bf16_f32 v114, v84, v85\n\t" "v_mfma_f32_16x16x32_bf16 v[100:103], v[76:79], a[44:47], v[100:103]\n\t" "v_cvt_pk_bf16_f32 v115, v86, v87\n\t" \
    "v_mfma_f32_16x16x32_bf16 v[104:107], v[48:51], a[48:51], 0\n\t" "buffer_load_dwordx4 v[24:27], %[ao], %[rs], s64 offen offset:2048 sc1\n\t" \
    "v_cvt_pk_bf16_f32 v116, v88, v89\n\t" "v_mfma_f32_16x16x32_bf16 v[108:111], v[52:55], a[48:51], 0\n\t" "v_cvt_pk_bf16_f32 v117, v90, v91\n\t" \
    "v_mfma_f32_16x16x32_bf16 v[104:107], v[56:59], a[52:55], v[104:107]\n\t" "buffer_load_dwordx4 v[28:31], %[ao], %[rs], s64 offen offset:2304 sc1\n\t" \
    "v_mfma_f32_16x16x32_bf16 v[108:111], v[60:63], a[52:55], v[108:111]\n\t" "buffer_store_dwordx4 v[112:115], %[z1], %[rs], s63 offen sc1\n\t" \
    "v_mfma_f32_16x16x32_bf16 v[104:107], v[64:67], a[56:59], v[104:107]\n\t" "buffer_load_dwordx4 v[32:35], %[a1], %[rs], s64 offen offset:0 sc1\n\t" \
    "v_cvt_pk_bf16_f32 v118, v92, v93\n\t" "v_mfma_f32_16x16x32_bf16 v[108:111], v[68:71], a[56:59], v[108:111]\n\t" "v_cvt_pk_bf16_f32 v119, v94, v95\n\t" \
    "v_mfma_f32_16x16x32_bf16 v[104:107], v[72:75], a[60:63], v[104:107]\n\t" "buffer_load_dwordx4 v[36:39], %[a1], %[rs], s64 offen offset:256 sc1\n\t" \
    "buffer_store_dwordx4 v[116:119], %[z2], %[rs], s63 offen sc1\n\t" "v_mfma_f32_16x16x32_bf16 v[108:111], v[76:79], a[60:63], v[108:111]\n\t" \
    "s_add_u32 s63, s63, 0x20000\n\t" "v_mfma_f32_16x16x32_bf16 v[80:83], v[48:51], a[64:67], 0\n\t" \
    "buffer_load_dwordx4 v[40:43], %[a1], %[rs], s64 offen offset:2048 sc1\n\t" "v_mfma_f32_16x16x32_bf16 v[84:87], v[52:55], a[64:67], 0\n\t" \
    "v_mfma_f32_16x16x32_bf16 v[80:83], v[56:59], a[68:71], v[80:83]\n\t" "buffer_load_dwordx4 v[44:47], %[a1], %[rs], s64 offen offset:2304 sc1\n\t" \
    "v_mfma_f32_16x16x32_bf16 v[84:87], v[60:63], a[68:71], v[84:87]\n\t" "v_mfma_f32_16x16x32_bf16 v[80:83], v[64:67], a[72:75], v[80:83]\n\t" \
    "buffer_load_dword v120, %[fo], %[rc], %[fv] offen sc1\n\t" "v_cvt_pk_bf16_f32 v112, v96, v97\n\t" "v_mfma_f32_16x16x32_bf16 v[84:87], v[68:71], a[72:75], v[84:87]\n\t" \
    "v_cvt_pk_bf16_f32 v113, v98, v99\n\t" "v_mfma_f32_16x16x32_bf16 v[80:83], v[72:75], a[76:79], v[80:83]\n\t" "buffer_load_dword v121, %[fo], %[rc], %[fs] offen sc1\n\t" \
    "v_mfma_f32_16x16x32_bf16 v[84:87], v[76:79], a[76:79], v[84:87]\n\t" "v_cvt_pk_bf16_f32 v114, v100, v101\n\t" "v_cvt_pk_bf16_f32 v115, v102, v103\n\t" \
    "v_mfma_f32_16x16x32_bf16 v[88:91], v[48:51], a[80:83], 0\n\t" "v_cvt_pk_bf16_f32 v116, v104, v105\n\t" "v_mfma_f32_16x16x32_bf16 v[92:95], v[52:55], a[80:83], 0\n\t" \
    "v_cvt_pk_bf16_f32 v117, v106, v107\n\t" "v_mfma_f32_16x16x32_bf16 v[88:91], v[56:59], a[84:87], v[88:91]\n\t" \
    "buffer_store_dwordx4 v[112:115], %[z1], %[rs], s63 offen sc1\n\t" "v_mfma_f32_16x16x32_bf16 v[92:95], v[60:63], a[84:87], v[92:95]\n\t" \
    "v_mfma_f32_16x16x32_bf16 v[88:91], v[64:67], a[88:91], v[88:91]\n\t" "v_cvt_pk_bf16_f32 v118, v108, v109\n\t" \
    "v_mfma_f32_16x16x32_bf16 v[92:95], v[68:71], a[88:91], v[92:95]\n\t" "v_cvt_pk_bf16_f32 v119, v110, v111\n\t" \
    "v_mfma_f32_16x16x32_bf16 v[88:91], v[72:75], a[92:95], v[88:91]\n\t" "buffer_store_dwordx4 v[116:119], %[z2], %[rs], s63 offen sc1\n\t" \
    "v_mfma_f32_16x16x32_bf16 v[92:95], v[76:79], a[92:95], v[92:95]\n\t" "s_add_u32 s63, s63, 0x20000\n\t" "v_mfma_f32_16x16x32_bf16 v[96:99], v[48:51], a[96:99], 0\n\t" \
    "v_mfma_f32_16x16x32_bf16 v[100:103], v[52:55], a[96:99], 0\n\t" "v_mfma_f32_16x16x32_bf16 v[96:99], v[56:59], a[100:103], v[96:99]\n\t" \
    "v_mfma_f32_16x16x32_bf16 v[100:103], v[60:63], a[100:103], v[100:103]\n\t" "v_mfma_f32_16x16x32_bf16 v[96:99], v[64:67], a[104:107], v[96:99]\n\t" \
    "v_cvt_pk_bf16_f32 v112, v80, v81\n\t" "v_mfma_f32_16x16x32_bf16 v[100:103], v[68:71], a[104:107], v[100:103]\n\t" "v_cvt_pk_bf16_f32 v113, v82, v83\n\t" \
    "v_mfma_f32_16x16x32_bf16 v[96:99], v[72:75], a[108:111], v[96:99]\n\t" "v_cvt_pk_bf16_f32 v114, v84, v85\n\t" \
    "v_mfma_f32_16x16x32_bf16 v[100:103], v[76:79], a[108:111], v[100:103]\n\t" "v_cvt_pk_bf16_f32 v115, v86, v87\n\t" \
    "v_mfma_f32_16x16x32_bf16 v[104:107], v[48:51], a[112:115], 0\n\t" "v_cvt_pk_bf16_f32 v116, v88, v89\n\t" \
    "v_mfma_f32_16x16x32_bf16 v[108:111], v[52:55], a[112:115], 0\n\t" "v_cvt_pk_bf16_f32 v117, v90, v91\n\t" \
    "v_mfma_f32_16x16x32_bf16 v[104:107], v[56:59], a[116:119], v[104:107]\n\t" "buffer_store_dwordx4 v[112:115], %[z1], %[rs], s63 offen sc1\n\t" \
    "v_mfma_f32_16x16x32_bf16 v[108:111], v[60:63], a[116:119], v[108:111]\n\t" "v_mfma_f32_16x16x32_bf16 v[104:107], v[64:67], a[120:123], v[104:107]\n\t" \
    "v_cvt_pk_bf16_f32 v118, v92, v93\n\t" "v_mfma_f32_16x16x32_bf16 v[108:111], v[68:71], a[120:123], v[108:111]\n\t" "v_cvt_pk_bf16_f32 v119, v94, v95\n\t" \
    "v_mfma_f32_16x16x32_bf16 v[104:107], v[72:75], a[124:127], v[104:107]\n\t" "buffer_store_dwordx4 v[116:119], %[z2], %[rs], s63 offen sc1\n\t" \
    "v_mfma_f32_16x16x32_bf16 v[108:111], v[76:79], a[124:127], v[108:111]\n\t" "s_add_u32 s63, s63, 0x20000\n\t" \
    "v_mfma_f32_16x16x32_bf16 v[80:83], v[48:51], a[128:131], 0\n\t" "v_mfma_f32_16x16x32_bf16 v[84:87], v[52:55], a[128:131], 0\n\t" \
    "v_mfma_f32_16x16x32_bf16 v[80:83], v[56:59], a[132:135], v[80:83]\n\t" "v_mfma_f32_16x16x32_bf16 v[84:87], v[60:63], a[132:135], v[84:87]\n\t" \
    "v_mfma_f32_16x16x32_bf16 v[80:83], v[64:67], a[136:139], v[80:83]\n\t" "v_cvt_pk_bf16_f32 v112, v96, v97\n\t" \
    "v_mfma_f32_16x16x32_bf16 v[84:87], v[68:71], a[136:139], v[84:87]\n\t" "v_cvt_pk_bf16_f32 v113, v98, v99\n\t" \
    "v_mfma_f32_16x16x32_bf16 v[80:83], v[72:75], a[140:143], v[80:83]\n\t" "v_cvt_pk_bf16_f32 v114, v100, v101\n\t" \
    "v_mfma_f32_16x16x32_bf16 v[84:87], v[76:79], a[140:143], v[84:87]\n\t" "v_cvt_pk_bf16_f32 v115, v102, v103\n\t" \
    "v_mfma_f32_16x16x32_bf16 v[88:91], v[48:51], a[144:147], 0\n\t" "v_cvt_pk_bf16_f32 v116, v104, v105\n\t" "v_mfma_f32_16x16x32_bf16 v[92:95], v[52:55], a[144:147], 0\n\t" \
    "v_cvt_pk_bf16_f32 v117, v106, v107\n\t" "v_mfma_f32_16x16x32_bf16 v[88:91], v[56:59], a[148:151], v[88:91]\n\t" \
    "buffer_store_dwordx4 v[112:115], %[z1], %[rs], s63 offen sc1\n\t" "v_mfma_f32_16x16x32_bf16 v[92:95], v[60:63], a[148:151], v[92:95]\n\t" \
    "v_mfma_f32_16x16x32_bf16 v[88:91], v[64:67], a[152:155], v[88:91]\n\t" "v_cvt_pk_bf16_f32 v118, v108, v109\n\t" \
    "v_mfma_f32_16x16x32_bf16 v[92:95], v[68:71], a[152:155], v[92:95]\n\t" "v_cvt_pk_bf16_f32 v119, v110, v111\n\t" \
    "v_mfma_f32_16x16x32_bf16 v[88:91], v[72:75], a[156:159], v[88:91]\n\t" "buffer_store_dwordx4 v[116:119], %[z2], %[rs], s63 offen sc1\n\t" \
    "v_mfma_f32_16x16x32_bf16 v[92:95], v[76:79], a[156:159], v[92:95]\n\t" "s_add_u32 s63, s63, 0x20000\n\t" "v_mfma_f32_16x16x32_bf16 v[96:99], v[48:51], a[160:163], 0\n\t" \
    "v_mfma_f32_16x16x32_bf16 v[100:103], v[52:55], a[160:163], 0\n\t" "v_mfma_f32_16x16x32_bf16 v[96:99], v[56:59], a[164:167], v[96:99]\n\t" \
    "v_mfma_f32_16x16x32_bf16 v[100:103], v[60:63], a[164:167], v[100:103]\n\t" "v_mfma_f32_16x16x32_bf16 v[96:99], v[64:67], a[168:171], v[96:99]\n\t" \
    "v_cvt_pk_bf16_f32 v112, v80, v81\n\t" "v_mfma_f32_16x16x32_bf16 v[100:103], v[68:71], a[168:171], v[100:103]\n\t" "v_cvt_pk_bf16_f32 v113, v82, v83\n\t" \
    "v_mfma_f32_16x16x32_bf16 v[96:99], v[72:75], a[172:175], v[96:99]\n\t" "v_cvt_pk_bf16_f32 v114, v84, v85\n\t" \
    "v_mfma_f32_16x16x32_bf16 v[100:103], v[76:79], a[172:175], v[100:103]\n\t" "v_cvt_pk_bf16_f32 v115, v86, v87\n\t" \
    "v_mfma_f32_16x16x32_bf16 v[104:107], v[48:51], a[176:179], 0\n\t" "v_cvt_pk_bf16_f32 v116, v88, v89\n\t" \
    "v_mfma_f32_16x16x32_bf16 v[108:111], v[52:55], a[176:179], 0\n\t" "v_cvt_pk_bf16_f32 v117, v90, v91\n\t" \
    "v_mfma_f32_16x16x32_bf16 v[104:107], v[56:59], a[180:183], v[104:107]\n\t" "buffer_store_dwordx4 v[112:115], %[z1], %[rs], s63 offen sc1\n\t" \
    "v_mfma_f32_16x16x32_bf16 v[108:111], v[60:63], a[180:183], v[108:111]\n\t" "v_mfma_f32_16x16x32_bf16 v[104:107], v[64:67], a[184:187], v[104:107]\n\t" \
    "v_cvt_pk_bf16_f32 v118, v92, v93\n\t" "v_mfma_f32_16x16x32_bf16 v[108:111], v[68:71], a[184:187], v[108:111]\n\t" "v_cvt_pk_bf16_f32 v119, v94, v95\n\t" \
    "v_mfma_f32_16x16x32_bf16 v[104:107], v[72:75], a[188:191], v[104:107]\n\t" "buffer_store_dwordx4 v[116:119], %[z2], %[rs], s63 offen sc1\n\t" \
    "v_mfma_f32_16x16x32_bf16 v[108:111], v[76:79], a[188:191], v[108:111]\n\t" "s_add_u32 s63, s63, 0x20000\n\t" \
    "v_mfma_f32_16x16x32_bf16 v[80:83], v[48:51], a[192:195], 0\n\t" "v_mfma_f32_16x16x32_bf16 v[84:87], v[52:55], a[192:195], 0\n\t" \
    "v_mfma_f32_16x16x32_bf16 v[80:83], v[56:59], a[196:199], v[80:83]\n\t" "v_mfma_f32_16x16x32_bf16 v[84:87], v[60:63], a[196:199], v[84:87]\n\t" \
    "v_mfma_f32_16x16x32_bf16 v[80:83], v[64:67], a[200:203], v[80:83]\n\t" "v_cvt_pk_bf16_f32 v112, v96, v97\n\t" \
    "v_mfma_f32_16x16x32_bf16 v[84:87], v[68:71], a[200:203], v[84:87]\n\t" "v_cvt_pk_bf16_f32 v113, v98, v99\n\t" \
    "v_mfma_f32_16x16x32_bf16 v[80:83], v[72:75], a[204:207], v[80:83]\n\t" "v_cvt_pk_bf16_f32 v114, v100, v101\n\t" \
    "v_mfma_f32_16x16x32_bf16 v[84:87], v[76:79], a[204:207], v[84:87]\n\t" "v_cvt_pk_bf16_f32 v115, v102, v103\n\t" \
    "v_mfma_f32_16x16x32_bf16 v[88:91], v[48:51], a[208:211], 0\n\t" "v_cvt_pk_bf16_f32 v116, v104, v105\n\t" "v_mfma_f32_16x16x32_bf16 v[92:95], v[52:55], a[208:211], 0\n\t" \
    "v_cvt_pk_bf16_f32 v117, v106, v107\n\t" "v_mfma_f32_16x16x32_bf16 v[88:91], v[56:59], a[212:215], v[88:91]\n\t" \
    "buffer_store_dwordx4 v[112:115], %[z1], %[rs], s63 offen sc1\n\t" "v_mfma_f32_16x16x32_bf16 v[92:95], v[60:63], a[212:215], v[92:95]\n\t" \
    "v_mfma_f32_16x16x32_bf16 v[88:91], v[64:67], a[216:219], v[88:91]\n\t" "v_cvt_pk_bf16_f32 v118, v108, v109\n\t" \
    "v_mfma_f32_16x16x32_bf16 v[92:95], v[68:71], a[216:219], v[92:95]\n\t" "v_cvt_pk_bf16_f32 v119, v110, v111\n\t" \
    "v_mfma_f32_16x16x32_bf16 v[88:91], v[72:75], a[220:223], v[88:91]\n\t" "buffer_store_dwordx4 v[116:119], %[z2], %[rs], s63 offen sc1\n\t" \
    "v_mfma_f32_16x16x32_bf16 v[92:95], v[76:79], a[220:223], v[92:95]\n\t" "s_add_u32 s63, s63, 0x20000\n\t" "v_mfma_f32_16x16x32_bf16 v[96:99], v[48:51], a[224:227], 0\n\t" \
    "v_mfma_f32_16x16x32_bf16 v[100:103], v[52:55], a[224:227], 0\n\t" "v_mfma_f32_16x16x32_bf16 v[96:99], v[56:59], a[228:231], v[96:99]\n\t" \
    "v_mfma_f32_16x16x32_bf16 v[100:103], v[60:63], a[228:231], v[100:103]\n\t" "v_mfma_f32_16x16x32_bf16 v[96:99], v[64:67], a[232:235], v[96:99]\n\t" \
    "v_cvt_pk_bf16_f32 v112, v80, v81\n\t" "v_mfma_f32_16x16x32_bf16 v[100:103], v[68:71], a[232:235], v[100:103]\n\t" "v_cvt_pk_bf16_f32 v113, v82, v83\n\t" \
    "v_mfma_f32_16x16x32_bf16 v[96:99], v[72:75], a[236:239], v[96:99]\n\t" "v_cvt_pk_bf16_f32 v114, v84, v85\n\t" \
    "v_mfma_f32_16x16x32_bf16 v[100:103], v[76:79], a[236:239], v[100:103]\n\t" "v_cvt_pk_bf16_f32 v115, v86, v87\n\t" \
    "v_mfma_f32_16x16x32_bf16 v[104:107], v[48:51], a[240:243], 0\n\t" "v_cvt_pk_bf16_f32 v116, v88, v89\n\t" \
    "v_mfma_f32_16x16x32_bf16 v[108:111], v[52:55], a[240:243], 0\n\t" "v_cvt_pk_bf16_f32 v117, v90, v91\n\t" \
    "v_mfma_f32_16x16x32_bf16 v[104:107], v[56:59], a[244:247], v[104:107]\n\t" "buffer_store_dwordx4 v[112:115], %[z1], %[rs], s63 offen sc1\n\t" \
    "v_mfma_f32_16x16x32_bf16 v[108:111], v[60:63], a[244:247], v[108:111]\n\t" "v_mfma_f32_16x16x32_bf16 v[104:107], v[64:67], a[248:251], v[104:107]\n\t" \
    "v_cvt_pk_bf16_f32 v118, v92, v93\n\t" "v_cvt_pk_bf16_f32 v119, v94, v95\n\t" "s_waitcnt vmcnt(11)\n\t" "v_readfirstlane_b32 s66, v120\n\t" \
    "v_mfma_f32_16x16x32_bf16 v[108:111], v[68:71], a[248:251], v[108:111]\n\t" "v_readfirstlane_b32 s67, v121\n\t" "v_mov_b32 v122, s60\n\t" \
    "buffer_store_dword v122, %[fo], %[rc], %[fz] offen sc1\n\t" "v_mfma_f32_16x16x32_bf16 v[104:107], v[72:75], a[252:255], v[104:107]\n\t" \
    "buffer_store_dwordx4 v[116:119], %[z2], %[rs], s63 offen sc1\n\t" "s_add_u32 s63, s63, 0x20000\n\t" "s_max_u32 s61, s61, s66\n\t" \
    "v_mfma_f32_16x16x32_bf16 v[108:111], v[76:79], a[252:255], v[108:111]\n\t" "s_max_u32 s62, s62, s67\n\t" "s_add_u32 s60, s60, 1\n\t" ZSUM "33:\n\t" "s_branch 10b\n\t" \
    "40:\n\t" "v_mfma_f32_16x16x32_bf16 v[80:83], v[16:19], a[0:3], 0\n\t" "v_mfma_f32_16x16x32_bf16 v[84:87], v[20:23], a[0:3], 0\n\t" \
    "v_mfma_f32_16x16x32_bf16 v[80:83], v[24:27], a[4:7], v[80:83]\n\t" "v_mfma_f32_16x16x32_bf16 v[84:87], v[28:31], a[4:7], v[84:87]\n\t" \
    "v_mfma_f32_16x16x32_bf16 v[80:83], v[32:35], a[8:11], v[80:83]\n\t" "v_cvt_pk_bf16_f32 v112, v96, v97\n\t" \
    "v_mfma_f32_16x16x32_bf16 v[84:87], v[36:39], a[8:11], v[84:87]\n\t" "v_cvt_pk_bf16_f32 v113, v98, v99\n\t" \
    "v_mfma_f32_16x16x32_bf16 v[80:83], v[40:43], a[12:15], v[80:83]\n\t" "v_cvt_pk_bf16_f32 v114, v100, v101\n\t" \
    "v_mfma_f32_16x16x32_bf16 v[84:87], v[44:47], a[12:15], v[84:87]\n\t" "v_cvt_pk_bf16_f32 v115, v102, v103\n\t" \
    "v_mfma_f32_16x16x32_bf16 v[88:91], v[16:19], a[16:19], 0\n\t" "v_cvt_pk_bf16_f32 v116, v104, v105\n\t" "v_mfma_f32_16x16x32_bf16 v[92:95], v[20:23], a[16:19], 0\n\t" \
    "v_cvt_pk_bf16_f32 v117, v106, v107\n\t" "v_mfma_f32_16x16x32_bf16 v[88:91], v[24:27], a[20:23], v[88:91]\n\t" \
    "buffer_store_dwordx4 v[112:115], %[z1], %[rs], s63 offen sc1\n\t" "v_mfma_f32_16x16x32_bf16 v[92:95], v[28:31], a[20:23], v[92:95]\n\t" \
    "v_mfma_f32_16x16x32_bf16 v[88:91], v[32:35], a[24:27], v[88:91]\n\t" "v_cvt_pk_bf16_f32 v118, v108, v109\n\t" \
    "v_mfma_f32_16x16x32_bf16 v[92:95], v[36:39], a[24:27], v[92:95]\n\t" "v_cvt_pk_bf16_f32 v119, v110, v111\n\t" \
    "v_mfma_f32_16x16x32_bf16 v[88:91], v[40:43], a[28:31], v[88:91]\n\t" "buffer_store_dwordx4 v[116:119], %[z2], %[rs], s63 offen sc1\n\t" "s_and_b32 s63, s60, %[rm]\n\t" \
    "v_mfma_f32_16x16x32_bf16 v[92:95], v[44:47], a[28:31], v[92:95]\n\t" "s_lshl_b32 s63, s63, 22\n\t" "s_add_u32 s63, s63, %[zb]\n\t" \
    "v_mfma_f32_16x16x32_bf16 v[96:99], v[16:19], a[32:35], 0\n\t" "buffer_load_dword v120, %[fo], %[rc], %[fv] offen sc1\n\t" \
    "v_mfma_f32_16x16x32_bf16 v[100:103], v[20:23], a[32:35], 0\n\t" "v_mfma_f32_16x16x32_bf16 v[96:99], v[24:27], a[36:39], v[96:99]\n\t" \
    "buffer_load_dword v121, %[fo], %[rc], %[fs] offen sc1\n\t" "v_mfma_f32_16x16x32_bf16 v[100:103], v[28:31], a[36:39], v[100:103]\n\t" \
    "v_mfma_f32_16x16x32_bf16 v[96:99], v[32:35], a[40:43], v[96:99]\n\t" "v_cvt_pk_bf16_f32 v112, v80, v81\n\t" \
    "v_mfma_f32_16x16x32_bf16 v[100:103], v[36:39], a[40:43], v[100:103]\n\t" "v_cvt_pk_bf16_f32 v113, v82, v83\n\t" \
    "v_mfma_f32_16x16x32_bf16 v[96:99], v[40:43], a[44:47], v[96:99]\n\t" "v_cvt_pk_bf16_f32 v114, v84, v85\n\t" \
    "v_mfma_f32_16x16x32_bf16 v[100:103], v[44:47], a[44:47], v[100:103]\n\t" "v_cvt_pk_bf16_f32 v115, v86, v87\n\t" \
    "v_mfma_f32_16x16x32_bf16 v[104:107], v[16:19], a[48:51], 0\n\t" "v_cvt_pk_bf16_f32 v116, v88, v89\n\t" "v_mfma_f32_16x16x32_bf16 v[108:111], v[20:23], a[48:51], 0\n\t" \
    "v_cvt_pk_bf16_f32 v117, v90, v91\n\t" "v_mfma_f32_16x16x32_bf16 v[104:107], v[24:27], a[52:55], v[104:107]\n\t" \
    "buffer_store_dwordx4 v[112:115], %[z1], %[rs], s63 offen sc1\n\t" "v_mfma_f32_16x16x32_bf16 v[108:111], v[28:31], a[52:55], v[108:111]\n\t" \
    "v_mfma_f32_16x16x32_bf16 v[104:107], v[32:35], a[56:59], v[104:107]\n\t" "v_cvt_pk_bf16_f32 v118, v92, v93\n\t" \
    "v_mfma_f32_16x16x32_bf16 v[108:111], v[36:39], a[56:59], v[108:111]\n\t" "v_cvt_pk_bf16_f32 v119, v94, v95\n\t" \
    "v_mfma_f32_16x16x32_bf16 v[104:107], v[40:43], a[60:63], v[104:107]\n\t" "buffer_store_dwordx4 v[116:119], %[z2], %[rs], s63 offen sc1\n\t" \
    "v_mfma_f32_16x16x32_bf16 v[108:111], v[44:47], a[60:63], v[108:111]\n\t" "s_add_u32 s63, s63, 0x20000\n\t" "v_mfma_f32_16x16x32_bf16 v[80:83], v[16:19], a[64:67], 0\n\t" \
    "s_add_u32 s65, s60, 1\n\t" "s_lshr_b32 s66, s65, 2\n\t" "v_mfma_f32_16x16x32_bf16 v[84:87], v[20:23], a[64:67], 0\n\t" "s_xor_b32 s66, s66, %[xp]\n\t" \
    "s_and_b32 s66, s66, 1\n\t" "v_mfma_f32_16x16x32_bf16 v[80:83], v[24:27], a[68:71], v[80:83]\n\t" "s_lshl_b32 s66, s66, 13\n\t" "s_lshl_b32 s64, s65, 19\n\t" \
    "v_mfma_f32_16x16x32_bf16 v[84:87], v[28:31], a[68:71], v[84:87]\n\t" "s_add_u32 s64, s64, s66\n\t" "s_add_u32 s64, s64, %[vb]\n\t" \
    "v_mfma_f32_16x16x32_bf16 v[80:83], v[32:35], a[72:75], v[80:83]\n\t" "v_cvt_pk_bf16_f32 v112, v96, v97\n\t" \
    "v_mfma_f32_16x16x32_bf16 v[84:87], v[36:39], a[72:75], v[84:87]\n\t" "v_cvt_pk_bf16_f32 v113, v98, v99\n\t" \
    "v_mfma_f32_16x16x32_bf16 v[80:83], v[40:43], a[76:79], v[80:83]\n\t" "v_cvt_pk_bf16_f32 v114, v100, v101\n\t" \
    "v_mfma_f32_16x16x32_bf16 v[84:87], v[44:47], a[76:79], v[84:87]\n\t" "v_cvt_pk_bf16_f32 v115, v102, v103\n\t" \
    "v_mfma_f32_16x16x32_bf16 v[88:91], v[16:19], a[80:83], 0\n\t" "v_cvt_pk_bf16_f32 v116, v104, v105\n\t" "v_mfma_f32_16x16x32_bf16 v[92:95], v[20:23], a[80:83], 0\n\t" \
    "v_cvt_pk_bf16_f32 v117, v106, v107\n\t" "v_mfma_f32_16x16x32_bf16 v[88:91], v[24:27], a[84:87], v[88:91]\n\t" \
    "buffer_store_dwordx4 v[112:115], %[z1], %[rs], s63 offen sc1\n\t" "v_mfma_f32_16x16x32_bf16 v[92:95], v[28:31], a[84:87], v[92:95]\n\t" \
    "v_mfma_f32_16x16x32_bf16 v[88:91], v[32:35], a[88:91], v[88:91]\n\t" "v_cvt_pk_bf16_f32 v118, v108, v109\n\t" \
    "v_mfma_f32_16x16x32_bf16 v[92:95], v[36:39], a[88:91], v[92:95]\n\t" "v_cvt_pk_bf16_f32 v119, v110, v111\n\t" \
    "v_mfma_f32_16x16x32_bf16 v[88:91], v[40:43], a[92:95], v[88:91]\n\t" "buffer_store_dwordx4 v[116:119], %[z2], %[rs], s63 offen sc1\n\t" \
    "v_mfma_f32_16x16x32_bf16 v[92:95], v[44:47], a[92:95], v[92:95]\n\t" "s_add_u32 s63, s63, 0x20000\n\t" "v_mfma_f32_16x16x32_bf16 v[96:99], v[16:19], a[96:99], 0\n\t" \
    "v_mfma_f32_16x16x32_bf16 v[100:103], v[20:23], a[96:99], 0\n\t" "v_mfma_f32_16x16x32_bf16 v[96:99], v[24:27], a[100:103], v[96:99]\n\t" \
    "v_mfma_f32_16x16x32_bf16 v[100:103], v[28:31], a[100:103], v[100:103]\n\t" "v_mfma_f32_16x16x32_bf16 v[96:99], v[32:35], a[104:107], v[96:99]\n\t" \
    "v_cvt_pk_bf16_f32 v112, v80, v81\n\t" "v_mfma_f32_16x16x32_bf16 v[100:103], v[36:39], a[104:107], v[100:103]\n\t" "v_cvt_pk_bf16_f32 v113, v82, v83\n\t" \
    "v_mfma_f32_16x16x32_bf16 v[96:99], v[40:43], a[108:111], v[96:99]\n\t" "v_cvt_pk_bf16_f32 v114, v84, v85\n\t" \
    "v_mfma_f32_16x16x32_bf16 v[100:103], v[44:47], a[108:111], v[100:103]\n\t" "v_cvt_pk_bf16_f32 v115, v86, v87\n\t" \
    "v_mfma_f32_16x16x32_bf16 v[104:107], v[16:19], a[112:115], 0\n\t" "v_cvt_pk_bf16_f32 v116, v88, v89\n\t" \
    "v_mfma_f32_16x16x32_bf16 v[108:111], v[20:23], a[112:115], 0\n\t" "v_cvt_pk_bf16_f32 v117, v90, v91\n\t" \
    "v_mfma_f32_16x16x32_bf16 v[104:107], v[24:27], a[116:119], v[104:107]\n\t" "buffer_store_dwordx4 v[112:115], %[z1], %[rs], s63 offen sc1\n\t" \
    "v_mfma_f32_16x16x32_bf16 v[108:111], v[28:31], a[116:119], v[108:111]\n\t" "v_mfma_f32_16x16x32_bf16 v[104:107], v[32:35], a[120:123], v[104:107]\n\t" \
    "v_cvt_pk_bf16_f32 v118, v92, v93\n\t" "v_mfma_f32_16x16x32_bf16 v[108:111], v[36:39], a[120:123], v[108:111]\n\t" "v_cvt_pk_bf16_f32 v119, v94, v95\n\t" \
    "v_mfma_f32_16x16x32_bf16 v[104:107], v[40:43], a[124:127], v[104:107]\n\t" "buffer_store_dwordx4 v[116:119], %[z2], %[rs], s63 offen sc1\n\t" \
    "v_mfma_f32_16x16x32_bf16 v[108:111], v[44:47], a[124:127], v[108:111]\n\t" "s_add_u32 s63, s63, 0x20000\n\t" \
    "v_mfma_f32_16x16x32_bf16 v[80:83], v[16:19], a[128:131], 0\n\t" "s_waitcnt vmcnt(6)\n\t" "v_readfirstlane_b32 s66, v120\n\t" "v_readfirstlane_b32 s67, v121\n\t" \
    "v_mfma_f32_16x16x32_bf16 v[84:87], v[20:23], a[128:131], 0\n\t" "v_mov_b32 v122, s60\n\t" "buffer_store_dword v122, %[fo], %[rc], %[fz] offen sc1\n\t" \
    "v_mfma_f32_16x16x32_bf16 v[80:83], v[24:27], a[132:135], v[80:83]\n\t" "s_max_u32 s61, s61, s66\n\t" "s_max_u32 s62, s62, s67\n\t" "s_cmp_gt_u32 s61, s65\n\t" \
    "v_mfma_f32_16x16x32_bf16 v[84:87], v[28:31], a[132:135], v[84:87]\n\t" "s_cselect_b32 s65, 1, 0\n\t" "s_cselect_b32 s64, s64, 0x7f000000\n\t" \
    "v_mfma_f32_16x16x32_bf16 v[80:83], v[32:35], a[136:139], v[80:83]\n\t" "buffer_load_dwordx4 v[48:51], %[ao], %[rs], s64 offen offset:0 sc1\n\t" \
    "v_cvt_pk_bf16_f32 v112, v96, v97\n\t" "v_mfma_f32_16x16x32_bf16 v[84:87], v[36:39], a[136:139], v[84:87]\n\t" "v_cvt_pk_bf16_f32 v113, v98, v99\n\t" \
    "v_mfma_f32_16x16x32_bf16 v[80:83], v[40:43], a[140:143], v[80:83]\n\t" "buffer_load_dwordx4 v[52:55], %[ao], %[rs], s64 offen offset:256 sc1\n\t" \
    "v_cvt_pk_bf16_f32 v114, v100, v101\n\t" "v_mfma_f32_16x16x32_bf16 v[84:87], v[44:47], a[140:143], v[84:87]\n\t" "v_cvt_pk_bf16_f32 v115, v102, v103\n\t" \
    "v_mfma_f32_16x16x32_bf16 v[88:91], v[16:19], a[144:147], 0\n\t" "buffer_load_dwordx4 v[56:59], %[ao], %[rs], s64 offen offset:2048 sc1\n\t" \
    "v_cvt_pk_bf16_f32 v116, v104, v105\n\t" "v_mfma_f32_16x16x32_bf16 v[92:95], v[20:23], a[144:147], 0\n\t" "v_cvt_pk_bf16_f32 v117, v106, v107\n\t" \
    "v_mfma_f32_16x16x32_bf16 v[88:91], v[24:27], a[148:151], v[88:91]\n\t" "buffer_load_dwordx4 v[60:63], %[ao], %[rs], s64 offen offset:2304 sc1\n\t" \
    "v_mfma_f32_16x16x32_bf16 v[92:95], v[28:31], a[148:151], v[92:95]\n\t" "buffer_store_dwordx4 v[112:115], %[z1], %[rs], s63 offen sc1\n\t" \
    "v_mfma_f32_16x16x32_bf16 v[88:91], v[32:35], a[152:155], v[88:91]\n\t" "buffer_load_dwordx4 v[64:67], %[a1], %[rs], s64 offen offset:0 sc1\n\t" \
    "v_cvt_pk_bf16_f32 v118, v108, v109\n\t" "v_mfma_f32_16x16x32_bf16 v[92:95], v[36:39], a[152:155], v[92:95]\n\t" "v_cvt_pk_bf16_f32 v119, v110, v111\n\t" \
    "v_mfma_f32_16x16x32_bf16 v[88:91], v[40:43], a[156:159], v[88:91]\n\t" "buffer_load_dwordx4 v[68:71], %[a1], %[rs], s64 offen offset:256 sc1\n\t" \
    "buffer_store_dwordx4 v[116:119], %[z2], %[rs], s63 offen sc1\n\t" "v_mfma_f32_16x16x32_bf16 v[92:95], v[44:47], a[156:159], v[92:95]\n\t" \
    "s_add_u32 s63, s63, 0x20000\n\t" "v_mfma_f32_16x16x32_bf16 v[96:99], v[16:19], a[160:163], 0\n\t" \
    "buffer_load_dwordx4 v[72:75], %[a1], %[rs], s64 offen offset:2048 sc1\n\t" "v_mfma_f32_16x16x32_bf16 v[100:103], v[20:23], a[160:163], 0\n\t" \
    "v_mfma_f32_16x16x32_bf16 v[96:99], v[24:27], a[164:167], v[96:99]\n\t" "buffer_load_dwordx4 v[76:79], %[a1], %[rs], s64 offen offset:2304 sc1\n\t" \
    "v_mfma_f32_16x16x32_bf16 v[100:103], v[28:31], a[164:167], v[100:103]\n\t" "v_mfma_f32_16x16x32_bf16 v[96:99], v[32:35], a[168:171], v[96:99]\n\t" \
    "v_cvt_pk_bf16_f32 v112, v80, v81\n\t" "v_mfma_f32_16x16x32_bf16 v[100:103], v[36:39], a[168:171], v[100:103]\n\t" "v_cvt_pk_bf16_f32 v113, v82, v83\n\t" \
    "v_mfma_f32_16x16x32_bf16 v[96:99], v[40:43], a[172:175], v[96:99]\n\t" "v_cvt_pk_bf16_f32 v114, v84, v85\n\t" \
    "v_mfma_f32_16x16x32_bf16 v[100:103], v[44:47], a[172:175], v[100:103]\n\t" "v_cvt_pk_bf16_f32 v115, v86, v87\n\t" \
    "v_mfma_f32_16x16x32_bf16 v[104:107], v[16:19], a[176:179], 0\n\t" "v_cvt_pk_bf16_f32 v116, v88, v89\n\t" \
    "v_mfma_f32_16x16x32_bf16 v[108:111], v[20:23], a[176:179], 0\n\t" "v_cvt_pk_bf16_f32 v117, v90, v91\n\t" \
    "v_mfma_f32_16x16x32_bf16 v[104:107], v[24:27], a[180:183], v[104:107]\n\t" "buffer_store_dwordx4 v[112:115], %[z1], %[rs], s63 offen sc1\n\t" \
    "v_mfma_f32_16x16x32_bf16 v[108:111], v[28:31], a[180:183], v[108:111]\n\t" "v_mfma_f32_16x16x32_bf16 v[104:107], v[32:35], a[184:187], v[104:107]\n\t" \
    "v_cvt_pk_bf16_f32 v118, v92, v93\n\t" "v_mfma_f32_16x16x32_bf16 v[108:111], v[36:39], a[184:187], v[108:111]\n\t" "v_cvt_pk_bf16_f32 v119, v94, v95\n\t" \
    "v_mfma_f32_16x16x32_bf16 v[104:107], v[40:43], a[188:191], v[104:107]\n\t" "buffer_store_dwordx4 v[116:119], %[z2], %[rs], s63 offen sc1\n\t" \
    "v_mfma_f32_16x16x32_bf16 v[108:111], v[44:47], a[188:191], v[108:111]\n\t" "s_add_u32 s63, s63, 0x20000\n\t" \
    "v_mfma_f32_16x16x32_bf16 v[80:83], v[16:19], a[192:195], 0\n\t" "v_mfma_f32_16x16x32_bf16 v[84:87], v[20:23], a[192:195], 0\n\t" \
    "v_mfma_f32_16x16x32_bf16 v[80:83], v[24:27], a[196:199], v[80:83]\n\t" "v_mfma_f32_16x16x32_bf16 v[84:87], v[28:31], a[196:199], v[84:87]\n\t" \
    "v_mfma_f32_16x16x32_bf16 v[80:83], v[32:35], a[200:203], v[80:83]\n\t" "v_cvt_pk_bf16_f32 v112, v96, v97\n\t" \
    "v_mfma_f32_16x16x32_bf16 v[84:87], v[36:39], a[200:203], v[84:87]\n\t" "v_cvt_pk_bf16_f32 v113, v98, v99\n\t" \
    "v_mfma_f32_16x16x32_bf16 v[80:83], v[40:43], a[204:207], v[80:83]\n\t" "v_cvt_pk_bf16_f32 v114, v100, v101\n\t" \
    "v_mfma_f32_16x16x32_bf16 v[84:87], v[44:47], a[204:207], v[84:87]\n\t" "v_cvt_pk_bf16_f32 v115, v102, v103\n\t" \
    "v_mfma_f32_16x16x32_bf16 v[88:91], v[16:19], a[208:211], 0\n\t" "v_cvt_pk_bf16_f32 v116, v104, v105\n\t" "v_mfma_f32_16x16x32_bf16 v[92:95], v[20:23], a[208:211], 0\n\t" \
    "v_cvt_pk_bf16_f32 v117, v106, v107\n\t" "v_mfma_f32_16x16x32_bf16 v[88:91], v[24:27], a[212:215], v[88:91]\n\t" \
    "buffer_store_dwordx4 v[112:115], %[z1], %[rs], s63 offen sc1\n\t" "v_mfma_f32_16x16x32_bf16 v[92:95], v[28:31], a[212:215], v[92:95]\n\t" \
    "v_mfma_f32_16x16x32_bf16 v[88:91], v[32:35], a[216:219], v[88:91]\n\t" "v_cvt_pk_bf16_f32 v118, v108, v109\n\t" \
    "v_mfma_f32_16x16x32_bf16 v[92:95], v[36:39], a[216:219], v[92:95]\n\t" "v_cvt_pk_bf16_f32 v119, v110, v111\n\t" \
    "v_mfma_f32_16x16x32_bf16 v[88:91], v[40:43], a[220:223], v[88:91]\n\t" "buffer_store_dwordx4 v[116:119], %[z2], %[rs], s63 offen sc1\n\t" \
    "v_mfma_f32_16x16x32_bf16 v[92:95], v[44:47], a[220:223], v[92:95]\n\t" "s_add_u32 s63, s63, 0x20000\n\t" "v_mfma_f32_16x16x32_bf16 v[96:99], v[16:19], a[224:227], 0\n\t" \
    "v_mfma_f32_16x16x32_bf16 v[100:103], v[20:23], a[224:227], 0\n\t" "v_mfma_f32_16x16x32_bf16 v[96:99], v[24:27], a[228:231], v[96:99]\n\t" \
    "v_mfma_f32_16x16x32_bf16 v[100:103], v[28:31], a[228:231], v[100:103]\n\t" "v_mfma_f32_16x16x32_bf16 v[96:99], v[32:35], a[232:235], v[96:99]\n\t" \
    "v_cvt_pk_bf16_f32 v112, v80, v81\n\t" "v_mfma_f32_16x16x32_bf16 v[100:103], v[36:39], a[232:235], v[100:103]\n\t" "v_cvt_pk_bf16_f32 v113, v82, v83\n\t" \
    "v_mfma_f32_16x16x32_bf16 v[96:99], v[40:43], a[236:239], v[96:99]\n\t" "v_cvt_pk_bf16_f32 v114, v84, v85\n\t" \
    "v_mfma_f32_16x16x32_bf16 v[100:103], v[44:47], a[236:239], v[100:103]\n\t" "v_cvt_pk_bf16_f32 v115, v86, v87\n\t" \
    "v_mfma_f32_16x16x32_bf16 v[104:107], v[16:19], a[240:243], 0\n\t" "v_cvt_pk_bf16_f32 v116, v88, v89\n\t" \
    "v_mfma_f32_16x16x32_bf16 v[108:111], v[20:23], a[240:243], 0\n\t" "v_cvt_pk_bf16_f32 v117, v90, v91\n\t" \
    "v_mfma_f32_16x16x32_bf16 v[104:107], v[24:27], a[244:247], v[104:107]\n\t" "buffer_store_dwordx4 v[112:115], %[z1], %[rs], s63 offen sc1\n\t" \
    "v_mfma_f32_16x16x32_bf16 v[108:111], v[28:31], a[244:247], v[108:111]\n\t" "v_mfma_f32_16x16x32_bf16 v[104:107], v[32:35], a[248:251], v[104:107]\n\t" \
    "v_cvt_pk_bf16_f32 v118, v92, v93\n\t" "v_mfma_f32_16x16x32_bf16 v[108:111], v[36:39], a[248:251], v[108:111]\n\t" "v_cvt_pk_bf16_f32 v119, v94, v95\n\t" \
    "v_mfma_f32_16x16x32_bf16 v[104:107], v[40:43], a[252:255], v[104:107]\n\t" "buffer_store_dwordx4 v[116:119], %[z2], %[rs], s63 offen sc1\n\t" \
    "s_add_u32 s63, s63, 0x20000\n\t" "s_add_u32 s60, s60, 1\n\t" "v_mfma_f32_16x16x32_bf16 v[108:111], v[44:47], a[252:255], v[108:111]\n\t" "s_cmp_eq_u32 s65, 0\n\t" \
    "s_waitcnt vmcnt(6)\n\t" ZSUM "s_cbranch_scc0 23b\n\t" "s_branch 20f\n\t" "41:\n\t" "v_mfma_f32_16x16x32_bf16 v[80:83], v[48:51], a[0:3], 0\n\t" \
    "v_mfma_f32_16x16x32_bf16 v[84:87], v[52:55], a[0:3], 0\n\t" "v_mfma_f32_16x16x32_bf16 v[80:83], v[56:59], a[4:7], v[80:83]\n\t" \
    "v_mfma_f32_16x16x32_bf16 v[84:87], v[60:63], a[4:7], v[84:87]\n\t" "v_mfma_f32_16x16x32_bf16 v[80:83], v[64:67], a[8:11], v[80:83]\n\t" \
    "v_cvt_pk_bf16_f32 v112, v96, v97\n\t" "v_mfma_f32_16x16x32_bf16 v[84:87], v[68:71], a[8:11], v[84:87]\n\t" "v_cvt_pk_bf16_f32 v113, v98, v99\n\t" \
    "v_mfma_f32_16x16x32_bf16 v[80:83], v[72:75], a[12:15], v[80:83]\n\t" "v_cvt_pk_bf16_f32 v114, v100, v101\n\t" \
    "v_mfma_f32_16x16x32_bf16 v[84:87], v[76:79], a[12:15], v[84:87]\n\t" "v_cvt_pk_bf16_f32 v115, v102, v103\n\t" \
    "v_mfma_f32_16x16x32_bf16 v[88:91], v[48:51], a[16:19], 0\n\t" "v_cvt_pk_bf16_f32 v116, v104, v105\n\t" "v_mfma_f32_16x16x32_bf16 v[92:95], v[52:55], a[16:19], 0\n\t" \
    "v_cvt_pk_bf16_f32 v117, v106, v107\n\t" "v_mfma_f32_16x16x32_bf16 v[88:91], v[56:59], a[20:23], v[88:91]\n\t" \
    "buffer_store_dwordx4 v[112:115], %[z1], %[rs], s63 offen sc1\n\t" "v_mfma_f32_16x16x32_bf16 v[92:95], v[60:63], a[20:23], v[92:95]\n\t" \
    "v_mfma_f32_16x16x32_bf16 v[88:91], v[64:67], a[24:27], v[88:91]\n\t" "v_cvt_pk_bf16_f32 v118, v108, v109\n\t" \
    "v_mfma_f32_16x16x32_bf16 v[92:95], v[68:71], a[24:27], v[92:95]\n\t" "v_cvt_pk_bf16_f32 v119, v110, v111\n\t" \
    "v_mfma_f32_16x16x32_bf16 v[88:91], v[72:75], a[28:31], v[88:91]\n\t" "buffer_store_dwordx4 v[116:119], %[z2], %[rs], s63 offen sc1\n\t" "s_and_b32 s63, s60, %[rm]\n\t" \
    "v_mfma_f32_16x16x32_bf16 v[92:95], v[76:79], a[28:31], v[92:95]\n\t" "s_lshl_b32 s63, s63, 22\n\t" "s_add_u32 s63, s63, %[zb]\n\t" \
    "v_mfma_f32_16x16x32_bf16 v[96:99], v[48:51], a[32:35], 0\n\t" "buffer_load_dword v120, %[fo], %[rc], %[fv] offen sc1\n\t" \
    "v_mfma_f32_16x16x32_bf16 v[100:103], v[52:55], a[32:35], 0\n\t" "v_mfma_f32_16x16x32_bf16 v[96:99], v[56:59], a[36:39], v[96:99]\n\t" \
    "buffer_load_dword v121, %[fo], %[rc], %[fs] offen sc1\n\t" "v_mfma_f32_16x16x32_bf16 v[100:103], v[60:63], a[36:39], v[100:103]\n\t" \
    "v_mfma_f32_16x16x32_bf16 v[96:99], v[64:67], a[40:43], v[96:99]\n\t" "v_cvt_pk_bf16_f32 v112, v80, v81\n\t" \
    "v_mfma_f32_16x16x32_bf16 v[100:103], v[68:71], a[40:43], v[100:103]\n\t" "v_cvt_pk_bf16_f32 v113, v82, v83\n\t" \
    "v_mfma_f32_16x16x32_bf16 v[96:99], v[72:75], a[44:47], v[96:99]\n\t" "v_cvt_pk_bf16_f32 v114, v84, v85\n\t" \
    "v_mfma_f32_16x16x32_bf16 v[100:103], v[76:79], a[44:47], v[100:103]\n\t" "v_cvt_pk_bf16_f32 v115, v86, v87\n\t" \
    "v_mfma_f32_16x16x32_bf16 v[104:107], v[48:51], a[48:51], 0\n\t" "v_cvt_pk_bf16_f32 v116, v88, v89\n\t" "v_mfma_f32_16x16x32_bf16 v[108:111], v[52:55], a[48:51], 0\n\t" \
    "v_cvt_pk_bf16_f32 v117, v90, v91\n\t" "v_mfma_f32_16x16x32_bf16 v[104:107], v[56:59], a[52:55], v[104:107]\n\t" \
    "buffer_store_dwordx4 v[112:115], %[z1], %[rs], s63 offen sc1\n\t" "v_mfma_f32_16x16x32_bf16 v[108:111], v[60:63], a[52:55], v[108:111]\n\t" \
    "v_mfma_f32_16x16x32_bf16 v[104:107], v[64:67], a[56:59], v[104:107]\n\t" "v_cvt_pk_bf16_f32 v118, v92, v93\n\t" \
    "v_mfma_f32_16x16x32_bf16 v[108:111], v[68:71], a[56:59], v[108:111]\n\t" "v_cvt_pk_bf16_f32 v119, v94, v95\n\t" \
    "v_mfma_f32_16x16x32_bf16 v[104:107], v[72:75], a[60:63], v[104:107]\n\t" "buffer_store_dwordx4 v[116:119], %[z2], %[rs], s63 offen sc1\n\t" \
    "v_mfma_f32_16x16x32_bf16 v[108:111], v[76:79], a[60:63], v[108:111]\n\t" "s_add_u32 s63, s63, 0x20000\n\t" "v_mfma_f32_16x16x32_bf16 v[80:83], v[48:51], a[64:67], 0\n\t" \
    "s_add_u32 s65, s60, 1\n\t" "s_lshr_b32 s66, s65, 2\n\t" "v_mfma_f32_16x16x32_bf16 v[84:87], v[52:55], a[64:67], 0\n\t" "s_xor_b32 s66, s66, %[xp]\n\t" \
    "s_and_b32 s66, s66, 1\n\t" "v_mfma_f32_16x16x32_bf16 v[80:83], v[56:59], a[68:71], v[80:83]\n\t" "s_lshl_b32 s66, s66, 13\n\t" "s_lshl_b32 s64, s65, 19\n\t" \
    "v_mfma_f32_16x16x32_bf16 v[84:87], v[60:63], a[68:71], v[84:87]\n\t" "s_add_u32 s64, s64, s66\n\t" "s_add_u32 s64, s64, %[vb]\n\t" \
    "v_mfma_f32_16x16x32_bf16 v[80:83], v[64:67], a[72:75], v[80:83]\n\t" "v_cvt_pk_bf16_f32 v112, v96, v97\n\t" \
    "v_mfma_f32_16x16x32_bf16 v[84:87], v[68:71], a[72:75], v[84:87]\n\t" "v_cvt_pk_bf16_f32 v113, v98, v99\n\t" \
    "v_mfma_f32_16x16x32_bf16 v[80:83], v[72:75], a[76:79], v[80:83]\n\t" "v_cvt_pk_bf16_f32 v114, v100, v101\n\t" \
    "v_mfma_f32_16x16x32_bf16 v[84:87], v[76:79], a[76:79], v[84:87]\n\t" "v_cvt_pk_bf16_f32 v115, v102, v103\n\t" \
    "v_mfma_f32_16x16x32_bf16 v[88:91], v[48:51], a[80:83], 0\n\t" "v_cvt_pk_bf16_f32 v116, v104, v105\n\t" "v_mfma_f32_16x16x32_bf16 v[92:95], v[52:55], a[80:83], 0\n\t" \
    "v_cvt_pk_bf16_f32 v117, v106, v107\n\t" "v_mfma_f32_16x16x32_bf16 v[88:91], v[56:59], a[84:87], v[88:91]\n\t" \
    "buffer_store_dwordx4 v[112:115], %[z1], %[rs], s63 offen sc1\n\t" "v_mfma_f32_16x16x32_bf16 v[92:95], v[60:63], a[84:87], v[92:95]\n\t" \
    "v_mfma_f32_16x16x32_bf16 v[88:91], v[64:67], a[88:91], v[88:91]\n\t" "v_cvt_pk_bf16_f32 v118, v108, v109\n\t" \
    "v_mfma_f32_16x16x32_bf16 v[92:95], v[68:71], a[88:91], v[92:95]\n\t" "v_cvt_pk_bf16_f32 v119, v110, v111\n\t" \
    "v_mfma_f32_16x16x32_bf16 v[88:91], v[72:75], a[92:95], v[88:91]\n\t" "buffer_store_dwordx4 v[116:119], %[z2], %[rs], s63 offen sc1\n\t" \
    "v_mfma_f32_16x16x32_bf16 v[92:95], v[76:79], a[92:95], v[92:95]\n\t" "s_add_u32 s63, s63, 0x20000\n\t" "v_mfma_f32_16x16x32_bf16 v[96:99], v[48:51], a[96:99], 0\n\t" \
    "v_mfma_f32_16x16x32_bf16 v[100:103], v[52:55], a[96:99], 0\n\t" "v_mfma_f32_16x16x32_bf16 v[96:99], v[56:59], a[100:103], v[96:99]\n\t" \
    "v_mfma_f32_16x16x32_bf16 v[100:103], v[60:63], a[100:103], v[100:103]\n\t" "v_mfma_f32_16x16x32_bf16 v[96:99], v[64:67], a[104:107], v[96:99]\n\t" \
    "v_cvt_pk_bf16_f32 v112, v80, v81\n\t" "v_mfma_f32_16x16x32_bf16 v[100:103], v[68:71], a[104:107], v[100:103]\n\t" "v_cvt_pk_bf16_f32 v113, v82, v83\n\t" \
    "v_mfma_f32_16x16x32_bf16 v[96:99], v[72:75], a[108:111], v[96:99]\n\t" "v_cvt_pk_bf16_f32 v114, v84, v85\n\t" \
    "v_mfma_f32_16x16x32_bf16 v[100:103], v[76:79], a[108:111], v[100:103]\n\t" "v_cvt_pk_bf16_f32 v115, v86, v87\n\t" \
    "v_mfma_f32_16x16x32_bf16 v[104:107], v[48:51], a[112:115], 0\n\t" "v_cvt_pk_bf16_f32 v116, v88, v89\n\t" \
    "v_mfma_f32_16x16x32_bf16 v[108:111], v[52:55], a[112:115], 0\n\t" "v_cvt_pk_bf16_f32 v117, v90, v91\n\t" \
    "v_mfma_f32_16x16x32_bf16 v[104:107], v[56:59], a[116:119], v[104:107]\n\t" "buffer_store_dwordx4 v[112:115], %[z1], %[rs], s63 offen sc1\n\t" \
    "v_mfma_f32_16x16x32_bf16 v[108:111], v[60:63], a[116:119], v[108:111]\n\t" "v_mfma_f32_16x16x32_bf16 v[104:107], v[64:67], a[120:123], v[104:107]\n\t" \
    "v_cvt_pk_bf16_f32 v118, v92, v93\n\t" "v_mfma_f32_16x16x32_bf16 v[108:111], v[68:71], a[120:123], v[108:111]\n\t" "v_cvt_pk_bf16_f32 v119, v94, v95\n\t" \
    "v_mfma_f32_16x16x32_bf16 v[104:107], v[72:75], a[124:127], v[104:107]\n\t" "buffer_store_dwordx4 v[116:119], %[z2], %[rs], s63 offen sc1\n\t" \
    "v_mfma_f32_16x16x32_bf16 v[108:111], v[76:79], a[124:127], v[108:111]\n\t" "s_add_u32 s63, s63, 0x20000\n\t" \
    "v_mfma_f32_16x16x32_bf16 v[80:83], v[48:51], a[128:131], 0\n\t" "s_waitcnt vmcnt(6)\n\t" "v_readfirstlane_b32 s66, v120\n\t" "v_readfirstlane_b32 s67, v121\n\t" \
    "v_mfma_f32_16x16x32_bf16 v[84:87], v[52:55], a[128:131], 0\n\t" "v_mov_b32 v122, s60\n\t" "buffer_store_dword v122, %[fo], %[rc], %[fz] offen sc1\n\t" \
    "v_mfma_f32_16x16x32_bf16 v[80:83], v[56:59], a[132:135], v[80:83]\n\t" "s_max_u32 s61, s61, s66\n\t" "s_max_u32 s62, s62, s67\n\t" "s_cmp_gt_u32 s61, s65\n\t" \
    "v_mfma_f32_16x16x32_bf16 v[84:87], v[60:63], a[132:135], v[84:87]\n\t" "s_cselect_b32 s65, 1, 0\n\t" "s_cselect_b32 s64, s64, 0x7f000000\n\t" \
    "v_mfma_f32_16x16x32_bf16 v[80:83], v[64:67], a[136:139], v[80:83]\n\t" "buffer_load_dwordx4 v[16:19], %[ao], %[rs], s64 offen offset:0 sc1\n\t" \
    "v_cvt_pk_bf16_f32 v112, v96, v97\n\t" "v_mfma_f32_16x16x32_bf16 v[84:87], v[68:71], a[136:139], v[84:87]\n\t" "v_cvt_pk_bf16_f32 v113, v98, v99\n\t" \
    "v_mfma_f32_16x16x32_bf16 v[80:83], v[72:75], a[140:143], v[80:83]\n\t" "buffer_load_dwordx4 v[20:23], %[ao], %[rs], s64 offen offset:256 sc1\n\t" \
    "v_cvt_pk_bf16_f32 v114, v100, v101\n\t" "v_mfma_f32_16x16x32_bf16 v[84:87], v[76:79], a[140:143], v[84:87]\n\t" "v_cvt_pk_bf16_f32 v115, v102, v103\n\t" \
    "v_mfma_f32_16x16x32_bf16 v[88:91], v[48:51], a[144:147], 0\n\t" "buffer_load_dwordx4 v[24:27], %[ao], %[rs], s64 offen offset:2048 sc1\n\t" \
    "v_cvt_pk_bf16_f32 v116, v104, v105\n\t" "v_mfma_f32_16x16x32_bf16 v[92:95], v[52:55], a[144:147], 0\n\t" "v_cvt_pk_bf16_f32 v117, v106, v107\n\t" \
    "v_mfma_f32_16x16x32_bf16 v[88:91], v[56:59], a[148:151], v[88:91]\n\t" "buffer_load_dwordx4 v[28:31], %[ao], %[rs], s64 offen offset:2304 sc1\n\t" \
    "v_mfma_f32_16x16x32_bf16 v[92:95], v[60:63], a[148:151], v[92:95]\n\t" "buffer_store_dwordx4 v[112:115], %[z1], %[rs], s63 offen sc1\n\t" \
    "v_mfma_f32_16x16x32_bf16 v[88:91], v[64:67], a[152:155], v[88:91]\n\t" "buffer_load_dwordx4 v[32:35], %[a1], %[rs], s64 offen offset:0 sc1\n\t" \
    "v_cvt_pk_bf16_f32 v118, v108, v109\n\t" "v_mfma_f32_16x16x32_bf16 v[92:95], v[68:71], a[152:155], v[92:95]\n\t" "v_cvt_pk_bf16_f32 v119, v110, v111\n\t" \
    "v_mfma_f32_16x16x32_bf16 v[88:91], v[72:75], a[156:159], v[88:91]\n\t" "buffer_load_dwordx4 v[36:39], %[a1], %[rs], s64 offen offset:256 sc1\n\t" \
    "buffer_store_dwordx4 v[116:119], %[z2], %[rs], s63 offen sc1\n\t" "v_mfma_f32_16x16x32_bf16 v[92:95], v[76:79], a[156:159], v[92:95]\n\t" \
    "s_add_u32 s63, s63, 0x20000\n\t" "v_mfma_f32_16x16x32_bf16 v[96:99], v[48:51], a[160:163], 0\n\t" \
    "buffer_load_dwordx4 v[40:43], %[a1], %[rs], s64 offen offset:2048 sc1\n\t" "v_mfma_f32_16x16x32_bf16 v[100:103], v[52:55], a[160:163], 0\n\t" \
    "v_mfma_f32_16x16x32_bf16 v[96:99], v[56:59], a[164:167], v[96:99]\n\t" "buffer_load_dwordx4 v[44:47], %[a1], %[rs], s64 offen offset:2304 sc1\n\t" \
    "v_mfma_f32_16x16x32_bf16 v[100:103], v[60:63], a[164:167], v[100:103]\n\t" "v_mfma_f32_16x16x32_bf16 v[96:99], v[64:67], a[168:171], v[96:99]\n\t" \
    "v_cvt_pk_bf16_f32 v112, v80, v81\n\t" "v_mfma_f32_16x16x32_bf16 v[100:103], v[68:71], a[168:171], v[100:103]\n\t" "v_cvt_pk_bf16_f32 v113, v82, v83\n\t" \
    "v_mfma_f32_16x16x32_bf16 v[96:99], v[72:75], a[172:175], v[96:99]\n\t" "v_cvt_pk_bf16_f32 v114, v84, v85\n\t" \
    "v_mfma_f32_16x16x32_bf16 v[100:103], v[76:79], a[172:175], v[100:103]\n\t" "v_cvt_pk_bf16_f32 v115, v86, v87\n\t" \
    "v_mfma_f32_16x16x32_bf16 v[104:107], v[48:51], a[176:179], 0\n\t" "v_cvt_pk_bf16_f32 v116, v88, v89\n\t" \
    "v_mfma_f32_16x16x32_bf16 v[108:111], v[52:55], a[176:179], 0\n\t" "v_cvt_pk_bf16_f32 v117, v90, v91\n\t" \
    "v_mfma_f32_16x16x32_bf16 v[104:107], v[56:59], a[180:183], v[104:107]\n\t" "buffer_store_dwordx4 v[112:115], %[z1], %[rs], s63 offen sc1\n\t" \
    "v_mfma_f32_16x16x32_bf16 v[108:111], v[60:63], a[180:183], v[108:111]\n\t" "v_mfma_f32_16x16x32_bf16 v[104:107], v[64:67], a[184:187], v[104:107]\n\t" \
    "v_cvt_pk_bf16_f32 v118, v92, v93\n\t" "v_mfma_f32_16x16x32_bf16 v[108:111], v[68:71], a[184:187], v[108:111]\n\t" "v_cvt_pk_bf16_f32 v119, v94, v95\n\t" \
    "v_mfma_f32_16x16x32_bf16 v[104:107], v[72:75], a[188:191], v[104:107]\n\t" "buffer_store_dwordx4 v[116:119], %[z2], %[rs], s63 offen sc1\n\t" \
    "v_mfma_f32_16x16x32_bf16 v[108:111], v[76:79], a[188:191], v[108:111]\n\t" "s_add_u32 s63, s63, 0x20000\n\t" \
    "v_mfma_f32_16x16x32_bf16 v[80:83], v[48:51], a[192:195], 0\n\t" "v_mfma_f32_16x16x32_bf16 v[84:87], v[52:55], a[192:195], 0\n\t" \
    "v_mfma_f32_16x16x32_bf16 v[80:83], v[56:59], a[196:199], v[80:83]\n\t" "v_mfma_f32_16x16x32_bf16 v[84:87], v[60:63], a[196:199], v[84:87]\n\t" \
    "v_mfma_f32_16x16x32_bf16 v[80:83], v[64:67], a[200:203], v[80:83]\n\t" "v_cvt_pk_bf16_f32 v112, v96, v97\n\t" \
    "v_mfma_f32_16x16x32_bf16 v[84:87], v[68:71], a[200:203], v[84:87]\n\t" "v_cvt_pk_bf16_f32 v113, v98, v99\n\t" \
    "v_mfma_f32_16x16x32_bf16 v[80:83], v[72:75], a[204:207], v[80:83]\n\t" "v_cvt_pk_bf16_f32 v114, v100, v101\n\t" \
    "v_mfma_f32_16x16x32_bf16 v[84:87], v[76:79], a[204:207], v[84:87]\n\t" "v_cvt_pk_bf16_f32 v115, v102, v103\n\t" \
    "v_mfma_f32_16x16x32_bf16 v[88:91], v[48:51], a[208:211], 0\n\t" "v_cvt_pk_bf16_f32 v116, v104, v105\n\t" "v_mfma_f32_16x16x32_bf16 v[92:95], v[52:55], a[208:211], 0\n\t" \
    "v_cvt_pk_bf16_f32 v117, v106, v107\n\t" "v_mfma_f32_16x16x32_bf16 v[88:91], v[56:59], a[212:215], v[88:91]\n\t" \
    "buffer_store_dwordx4 v[112:115], %[z1], %[rs], s63 offen sc1\n\t" "v_mfma_f32_16x16x32_bf16 v[92:95], v[60:63], a[212:215], v[92:95]\n\t" \
    "v_mfma_f32_16x16x32_bf16 v[88:91], v[64:67], a[216:219], v[88:91]\n\t" "v_cvt_pk_bf16_f32 v118, v108, v109\n\t" \
    "v_mfma_f32_16x16x32_bf16 v[92:95], v[68:71], a[216:219], v[92:95]\n\t" "v_cvt_pk_bf16_f32 v119, v110, v111\n\t" \
    "v_mfma_f32_16x16x32_bf16 v[88:91], v[72:75], a[220:223], v[88:91]\n\t" "buffer_store_dwordx4 v[116:119], %[z2], %[rs], s63 offen sc1\n\t" \
    "v_mfma_f32_16x16x32_bf16 v[92:95], v[76:79], a[220:223], v[92:95]\n\t" "s_add_u32 s63, s63, 0x20000\n\t" "v_mfma_f32_16x16x32_bf16 v[96:99], v[48:51], a[224:227], 0\n\t" \
    "v_mfma_f32_16x16x32_bf16 v[100:103], v[52:55], a[224:227], 0\n\t" "v_mfma_f32_16x16x32_bf16 v[96:99], v[56:59], a[228:231], v[96:99]\n\t" \
    "v_mfma_f32_16x16x32_bf16 v[100:103], v[60:63], a[228:231], v[100:103]\n\t" "v_mfma_f32_16x16x32_bf16 v[96:99], v[64:67], a[232:235], v[96:99]\n\t" \
    "v_cvt_pk_bf16_f32 v112, v80, v81\n\t" "v_mfma_f32_16x16x32_bf16 v[100:103], v[68:71], a[232:235], v[100:103]\n\t" "v_cvt_pk_bf16_f32 v113, v82, v83\n\t" \
    "v_mfma_f32_16x16x32_bf16 v[96:99], v[72:75], a[236:239], v[96:99]\n\t" "v_cvt_pk_bf16_f32 v114, v84, v85\n\t" \
    "v_mfma_f32_16x16x32_bf16 v[100:103], v[76:79], a[236:239], v[100:103]\n\t" "v_cvt_pk_bf16_f32 v115, v86, v87\n\t" \
    "v_mfma_f32_16x16x32_bf16 v[104:107], v[48:51], a[240:243], 0\n\t" "v_cvt_pk_bf16_f32 v116, v88, v89\n\t" \
    "v_mfma_f32_16x16x32_bf16 v[108:111], v[52:55], a[240:243], 0\n\t" "v_cvt_pk_bf16_f32 v117, v90, v91\n\t" \
    "v_mfma_f32_16x16x32_bf16 v[104:107], v[56:59], a[244:247], v[104:107]\n\t" "buffer_store_dwordx4 v[112:115], %[z1], %[rs], s63 offen sc1\n\t" \
    "v_mfma_f32_16x16x32_bf16 v[108:111], v[60:63], a[244:247], v[108:111]\n\t" "v_mfma_f32_16x16x32_bf16 v[104:107], v[64:67], a[248:251], v[104:107]\n\t" \
    "v_cvt_pk_bf16_f32 v118, v92, v93\n\t" "v_mfma_f32_16x16x32_bf16 v[108:111], v[68:71], a[248:251], v[108:111]\n\t" "v_cvt_pk_bf16_f32 v119, v94, v95\n\t" \
    "v_mfma_f32_16x16x32_bf16 v[104:107], v[72:75], a[252:255], v[104:107]\n\t" "buffer_store_dwordx4 v[116:119], %[z2], %[rs], s63 offen sc1\n\t" \
    "s_add_u32 s63, s63, 0x20000\n\t" "s_add_u32 s60, s60, 1\n\t" "v_mfma_f32_16x16x32_bf16 v[108:111], v[76:79], a[252:255], v[108:111]\n\t" "s_cmp_eq_u32 s65, 0\n\t" \
    "s_waitcnt vmcnt(6)\n\t" ZSUM "s_cbranch_scc0 33b\n\t" "s_branch 30f\n\t" "20:\n\t" "s_nop 7\n\t" "v_cvt_pk_bf16_f32 v112, v96, v97\n\t" \
    "v_cvt_pk_bf16_f32 v113, v98, v99\n\t" "v_cvt_pk_bf16_f32 v114, v100, v101\n\t" "v_cvt_pk_bf16_f32 v115, v102, v103\n\t" "v_cvt_pk_bf16_f32 v116, v104, v105\n\t" \
    "v_cvt_pk_bf16_f32 v117, v106, v107\n\t" "v_cvt_pk_bf16_f32 v118, v108, v109\n\t" "v_cvt_pk_bf16_f32 v119, v110, v111\n\t" \
    "buffer_store_dwordx4 v[112:115], %[z1], %[rs], s63 offen sc1\n\t" "buffer_store_dwordx4 v[116:119], %[z2], %[rs], s63 offen sc1\n\t" \
    "buffer_load_dword v120, %[fo], %[rc], %[fv] offen sc1\n\t" "s_waitcnt vmcnt(0)\n\t" "v_readfirstlane_b32 s61, v120\n\t" "s_lshr_b32 s66, s60, 2\n\t" \
    "s_xor_b32 s66, s66, %[xp]\n\t" "s_and_b32 s66, s66, 1\n\t" "s_lshl_b32 s66, s66, 13\n\t" "s_lshl_b32 s64, s60, 19\n\t" "s_add_u32 s64, s64, s66\n\t" \
    "s_add_u32 s64, s64, %[vb]\n\t" "s_cmp_gt_u32 s61, s60\n\t" "s_cbranch_scc0 24f\n\t" "s_cmp_eq_u32 s60, 64\n\t" "s_cbranch_scc1 24f\n\t" \
    "buffer_load_dwordx4 v[48:51], %[ao], %[rs], s64 offen offset:0 sc1\n\t" "buffer_load_dwordx4 v[52:55], %[ao], %[rs], s64 offen offset:256 sc1\n\t" \
    "buffer_load_dwordx4 v[56:59], %[ao], %[rs], s64 offen offset:2048 sc1\n\t" "buffer_load_dwordx4 v[60:63], %[ao], %[rs], s64 offen offset:2304 sc1\n\t" \
    "buffer_load_dwordx4 v[64:67], %[a1], %[rs], s64 offen offset:0 sc1\n\t" "buffer_load_dwordx4 v[68:71], %[a1], %[rs], s64 offen offset:256 sc1\n\t" \
    "buffer_load_dwordx4 v[72:75], %[a1], %[rs], s64 offen offset:2048 sc1\n\t" "buffer_load_dwordx4 v[76:79], %[a1], %[rs], s64 offen offset:2304 sc1\n\t" \
    "v_mov_b32 v122, s60\n\t" "buffer_store_dword v122, %[fo], %[rc], %[fz] offen sc1\n\t" "s_waitcnt vmcnt(1)\n\t" "s_branch 23b\n\t" "24:\n\t" "v_mov_b32 v122, s60\n\t" \
    "buffer_store_dword v122, %[fo], %[rc], %[fz] offen sc1\n\t" "s_cmp_eq_u32 s60, 64\n\t" "s_cbranch_scc1 90f\n\t" "s_add_u32 s67, s60, 1\n\t" "s_cmp_ge_u32 s61, s67\n\t" \
    "s_cbranch_scc1 22f\n\t" "21:\n\t" "buffer_load_dword v120, %[fo], %[rc], %[fv] offen sc1\n\t" "buffer_load_dword v121, %[fo], %[rc], %[vd] offen sc1\n\t" \
    "s_waitcnt vmcnt(0)\n\t" "v_readfirstlane_b32 s61, v120\n\t" "v_readfirstlane_b32 s66, v121\n\t" "s_nop 3\n\t" "s_cmp_ge_u32 s61, s67\n\t" "s_cbranch_scc1 22f\n\t" \
    "s_bitcmp1_b32 s66, 1\n\t" "s_cbranch_scc1 90f\n\t" "s_cmp_eq_u32 %[bd], 0\n\t" "s_cbranch_scc1 22f\n\t" "s_sub_u32 %[bd], %[bd], 1\n\t" "s_sleep 2\n\t" \
    "s_branch 21b\n\t" "22:\n\t" "s_lshr_b32 s66, s60, 2\n\t" "s_xor_b32 s66, s66, %[xp]\n\t" "s_and_b32 s66, s66, 1\n\t" "s_lshl_b32 s66, s66, 13\n\t" \
    "s_lshl_b32 s64, s60, 19\n\t" "s_add_u32 s64, s64, s66\n\t" "s_add_u32 s64, s64, %[vb]\n\t" "buffer_load_dwordx4 v[48:51], %[ao], %[rs], s64 offen offset:0 sc1\n\t" \
    "buffer_load_dwordx4 v[52:55], %[ao], %[rs], s64 offen offset:256 sc1\n\t" "buffer_load_dwordx4 v[56:59], %[ao], %[rs], s64 offen offset:2048 sc1\n\t" \
    "buffer_load_dwordx4 v[60:63], %[ao], %[rs], s64 offen offset:2304 sc1\n\t" "buffer_load_dwordx4 v[64:67], %[a1], %[rs], s64 offen offset:0 sc1\n\t" \
    "buffer_load_dwordx4 v[68:71], %[a1], %[rs], s64 offen offset:256 sc1\n\t" "buffer_load_dwordx4 v[72:75], %[a1], %[rs], s64 offen offset:2048 sc1\n\t" \
    "buffer_load_dwordx4 v[76:79], %[a1], %[rs], s64 offen offset:2304 sc1\n\t" "s_waitcnt vmcnt(0)\n\t" "s_branch 23b\n\t" "60:\n\t" "s_sub_u32 s67, s60, %[rg]\n\t" \
    "s_cmp_ge_u32 s62, s67\n\t" "s_cbranch_scc1 62f\n\t" "61:\n\t" "buffer_load_dword v120, %[fo], %[rc], %[fs] offen sc1\n\t" \
    "buffer_load_dword v121, %[fo], %[rc], %[vd] offen sc1\n\t" "s_waitcnt vmcnt(0)\n\t" "v_readfirstlane_b32 s62, v120\n\t" "v_readfirstlane_b32 s66, v121\n\t" "s_nop 3\n\t" \
    "s_cmp_ge_u32 s62, s67\n\t" "s_cbranch_scc1 62f\n\t" "s_bitcmp1_b32 s66, 1\n\t" "s_cbranch_scc1 90f\n\t" "s_cmp_eq_u32 %[bd], 0\n\t" "s_cbranch_scc1 62f\n\t" \
    "s_sub_u32 %[bd], %[bd], 1\n\t" "s_sleep 2\n\t" "s_branch 61b\n\t" "62:\n\t" "s_branch 63b\n\t" "30:\n\t" "s_nop 7\n\t" "v_cvt_pk_bf16_f32 v112, v96, v97\n\t" \
    "v_cvt_pk_bf16_f32 v113, v98, v99\n\t" "v_cvt_pk_bf16_f32 v114, v100, v101\n\t" "v_cvt_pk_bf16_f32 v115, v102, v103\n\t" "v_cvt_pk_bf16_f32 v116, v104, v105\n\t" \
    "v_cvt_pk_bf16_f32 v117, v106, v107\n\t" "v_cvt_pk_bf16_f32 v118, v108, v109\n\t" "v_cvt_pk_bf16_f32 v119, v110, v111\n\t" \
    "buffer_store_dwordx4 v[112:115], %[z1], %[rs], s63 offen sc1\n\t" "buffer_store_dwordx4 v[116:119], %[z2], %[rs], s63 offen sc1\n\t" \
    "buffer_load_dword v120, %[fo], %[rc], %[fv] offen sc1\n\t" "s_waitcnt vmcnt(0)\n\t" "v_readfirstlane_b32 s61, v120\n\t" "s_lshr_b32 s66, s60, 2\n\t" \
    "s_xor_b32 s66, s66, %[xp]\n\t" "s_and_b32 s66, s66, 1\n\t" "s_lshl_b32 s66, s66, 13\n\t" "s_lshl_b32 s64, s60, 19\n\t" "s_add_u32 s64, s64, s66\n\t" \
    "s_add_u32 s64, s64, %[vb]\n\t" "s_cmp_gt_u32 s61, s60\n\t" "s_cbranch_scc0 34f\n\t" "s_cmp_eq_u32 s60, 64\n\t" "s_cbranch_scc1 34f\n\t" \
    "buffer_load_dwordx4 v[16:19], %[ao], %[rs], s64 offen offset:0 sc1\n\t" "buffer_load_dwordx4 v[20:23], %[ao], %[rs], s64 offen offset:256 sc1\n\t" \
    "buffer_load_dwordx4 v[24:27], %[ao], %[rs], s64 offen offset:2048 sc1\n\t" "buffer_load_dwordx4 v[28:31], %[ao], %[rs], s64 offen offset:2304 sc1\n\t" \
    "buffer_load_dwordx4 v[32:35], %[a1], %[rs], s64 offen offset:0 sc1\n\t" "buffer_load_dwordx4 v[36:39], %[a1], %[rs], s64 offen offset:256 sc1\n\t" \
    "buffer_load_dwordx4 v[40:43], %[a1], %[rs], s64 offen offset:2048 sc1\n\t" "buffer_load_dwordx4 v[44:47], %[a1], %[rs], s64 offen offset:2304 sc1\n\t" \
    "v_mov_b32 v122, s60\n\t" "buffer_store_dword v122, %[fo], %[rc], %[fz] offen sc1\n\t" "s_waitcnt vmcnt(1)\n\t" "s_branch 33b\n\t" "34:\n\t" "v_mov_b32 v122, s60\n\t" \
    "buffer_store_dword v122, %[fo], %[rc], %[fz] offen sc1\n\t" "s_cmp_eq_u32 s60, 64\n\t" "s_cbranch_scc1 90f\n\t" "s_add_u32 s67, s60, 1\n\t" "s_cmp_ge_u32 s61, s67\n\t" \
    "s_cbranch_scc1 32f\n\t" "31:\n\t" "buffer_load_dword v120, %[fo], %[rc], %[fv] offen sc1\n\t" "buffer_load_dword v121, %[fo], %[rc], %[vd] offen sc1\n\t" \
    "s_waitcnt vmcnt(0)\n\t" "v_readfirstlane_b32 s61, v120\n\t" "v_readfirstlane_b32 s66, v121\n\t" "s_nop 3\n\t" "s_cmp_ge_u32 s61, s67\n\t" "s_cbranch_scc1 32f\n\t" \
    "s_bitcmp1_b32 s66, 1\n\t" "s_cbranch_scc1 90f\n\t" "s_cmp_eq_u32 %[bd], 0\n\t" "s_cbranch_scc1 32f\n\t" "s_sub_u32 %[bd], %[bd], 1\n\t" "s_sleep 2\n\t" \
    "s_branch 31b\n\t" "32:\n\t" "s_lshr_b32 s66, s60, 2\n\t" "s_xor_b32 s66, s66, %[xp]\n\t" "s_and_b32 s66, s66, 1\n\t" "s_lshl_b32 s66, s66, 13\n\t" \
    "s_lshl_b32 s64, s60, 19\n\t" "s_add_u32 s64, s64, s66\n\t" "s_add_u32 s64, s64, %[vb]\n\t" "buffer_load_dwordx4 v[16:19], %[ao], %[rs], s64 offen offset:0 sc1\n\t" \
    "buffer_load_dwordx4 v[20:23], %[ao], %[rs], s64 offen offset:256 sc1\n\t" "buffer_load_dwordx4 v[24:27], %[ao], %[rs], s64 offen offset:2048 sc1\n\t" \
    "buffer_load_dwordx4 v[28:31], %[ao], %[rs], s64 offen offset:2304 sc1\n\t" "buffer_load_dwordx4 v[32:35], %[a1], %[rs], s64 offen offset:0 sc1\n\t" \
    "buffer_load_dwordx4 v[36:39], %[a1], %[rs], s64 offen offset:256 sc1\n\t" "buffer_load_dwordx4 v[40:43], %[a1], %[rs], s64 offen offset:2048 sc1\n\t" \
    "buffer_load_dwordx4 v[44:47], %[a1], %[rs], s64 offen offset:2304 sc1\n\t" "s_waitcnt vmcnt(0)\n\t" "s_branch 33b\n\t" "70:\n\t" "s_sub_u32 s67, s60, %[rg]\n\t" \
    "s_cmp_ge_u32 s62, s67\n\t" "s_cbranch_scc1 72f\n\t" "71:\n\t" "buffer_load_dword v120, %[fo], %[rc], %[fs] offen sc1\n\t" \
    "buffer_load_dword v121, %[fo], %[rc], %[vd] offen sc1\n\t" "s_waitcnt vmcnt(0)\n\t" "v_readfirstlane_b32 s62, v120\n\t" "v_readfirstlane_b32 s66, v121\n\t" "s_nop 3\n\t" \
    "s_cmp_ge_u32 s62, s67\n\t" "s_cbranch_scc1 72f\n\t" "s_bitcmp1_b32 s66, 1\n\t" "s_cbranch_scc1 90f\n\t" "s_cmp_eq_u32 %[bd], 0\n\t" "s_cbranch_scc1 72f\n\t" \
    "s_sub_u32 %[bd], %[bd], 1\n\t" "s_sleep 2\n\t" "s_branch 71b\n\t" "72:\n\t" "s_branch 73b\n\t" "90:\n\t"

#define T_DQ 1380
#define T_DV 1290
#define T_RQ 670
#define T_RV 640
#define T_M1 105
#define T_DY 585
#define T_DX 1220
#define T_MIN 35
#define T_PEN 280
#define T_SAFE 80
#define T_RETRY 700
#define T_POLL 1
#define T_KS 0
#define T_TS 0
#define T_CLR 210
#define T_EASE 1600
#define T_VLAG 150
#define T_U0 340
constexpr int T_OVH = 118, T_NEAR = 460;
constexpr unsigned NS(int ns) { return (unsigned)(ns * 64 / 10); }
struct Clk { unsigned cyc, g, pc, pg, k1, k2; };
DEV void mark(Clk& k) {
  unsigned long long c, r[8];
  asm volatile("s_memtime %0\n\ts_memrealtime %1\n\ts_nop 2\n\ts_memrealtime %2\n\ts_nop 2\n\ts_memrealtime %3\n\ts_nop 2\n\ts_memrealtime %4\n\ts_nop 2\n\t"
               "s_memrealtime %5\n\ts_nop 2\n\ts_memrealtime %6\n\ts_nop 2\n\ts_memrealtime %7\n\ts_waitcnt lgkmcnt(0)"
               : "=s"(c), "=s"(r[0]), "=s"(r[1]), "=s"(r[2]), "=s"(r[3]), "=s"(r[4]), "=s"(r[5]), "=s"(r[6]) :: "memory");
  const unsigned a = (unsigned)r[0];
  unsigned n = 0;
#pragma unroll
  for (int i = 1; i < 7; ++i) asm("s_cmp_eq_u32 %1, %2\n\ts_addc_u32 %0, %0, 0" : "+s"(n) : "s"((unsigned)r[i]), "s"(a) : "scc");
  k.g = (a + 4u) << 6; k.cyc = (unsigned)c + 12u + 16u * n;
}
DEV void anchor(Clk& k, bool any = false) {
  mark(k);
  const unsigned dg_ = k.g - k.pg;
  const float dc = (float)(k.cyc - k.pc), dg = (float)dg_;
  unsigned k1, k2;
  asm volatile("s_nop 0\n\tv_readfirstlane_b32 %0, %2\n\tv_readfirstlane_b32 %1, %3\n\ts_nop 3\n\ts_min_u32 %0, %0, 0x80000\n\ts_min_u32 %1, %1, 0x8000"
               : "=&s"(k1), "=&s"(k2) : "v"((unsigned)(dg * __builtin_amdgcn_rcpf(dc) * 65536.f)), "v"((unsigned)(dc * __builtin_amdgcn_rcpf(dg) * 65536.f)) : "scc");
  if (any) { k.k1 = k1; k.k2 = k2; }
  else asm("s_cmp_lt_u32 %4, %5\n\ts_cselect_b32 %2, %2, %0\n\ts_cselect_b32 %3, %3, %1\n\ts_add_u32 %0, %0, %2\n\ts_lshr_b32 %0, %0, 1\n\ts_add_u32 %1, %1, %3\n\ts_lshr_b32 %1, %1, 1"
           : "+s"(k.k1), "+s"(k.k2), "+s"(k1), "+s"(k2) : "s"(dg_ - NS(1400)), "n"(NS(800)) : "scc");
  k.pc = k.cyc; k.pg = k.g;
}
DEV unsigned to_cyc(const Clk& k, unsigned g) {
  unsigned d;
  asm("s_max_i32 %0, %1, 0xffff8000\n\ts_min_i32 %0, %0, 0x8000\n\ts_mul_i32 %0, %0, %2\n\ts_ashr_i32 %0, %0, 16" : "=&s"(d) : "s"(g - k.g), "s"(k.k2) : "scc");
  return k.cyc + d - T_OVH;
}
DEV void wait_cyc(unsigned t) { asm volatile(TIMED_WAIT :: "s"(t) : CL_S); }
DEV void close_in(Clk& k, unsigned t) {
  if ((int)(t - (unsigned)__builtin_amdgcn_s_memtime()) > T_NEAR + 60) { wait_cyc(t - T_NEAR); mark(k); }
}
DEV int slow_line(const u16* q, int h, int j, int lane) { return (j * 32 + (lane >> 1)) * 8192 + (h >> 3) * 2048 + (lane & 1) * 1024 + ((3 - (int)((size_t)q >> 7)) & 7) * 128; }
DEV int turn(int h, int w) { return ((h >> 3) ^ (w & 1)) & 3; }
DEV void hold(const Ctx& c, int ticks) {
#pragma unroll 1
  while ((unsigned)__builtin_amdgcn_s_memrealtime() - c.t0 < (unsigned)ticks) __builtin_amdgcn_s_sleep(4);
}
DEV int z_mix(int y) { return (y >> 4) | ((y & 1) << 2); }
DEV int u_slot(int s, int para) { return R_U + (s & (URING == 64 ? 127 : URING - 1)) * MB + ((para ^ (s >> 1)) & 1) * 8192; }
DEV unsigned ask_stamps(Ctx& c, int par, int slot) { return __builtin_amdgcn_raw_buffer_load_b32(c.rs, amap(24576, par) + fresh_lane() * 128, slot, AUX_SC1); }
DEV unsigned latest(Ctx& c, const Clk& k, int par, int slot, unsigned v) {
  if (__builtin_expect(!allok(v), 0)) {
#pragma unroll 1
    do {
      __builtin_amdgcn_s_sleep(T_POLL);
      asm volatile("" ::: "memory");
      const unsigned t = __builtin_amdgcn_raw_buffer_load_b32(c.rs, v == SENT ? amap(24576, par) + fresh_lane() * 128 : 1 << 30, slot, AUX_SC1);
      v = v == SENT ? t : v;
    } while (!allok(v) && !spend(c));
  }
  int d = (int)(v - k.g);
  d = max(d, __builtin_amdgcn_update_dpp(d, d, 0x128, 0xf, 0xf, true));
  d = max(d, __builtin_amdgcn_update_dpp(d, d, 0x124, 0xf, 0xf, true));
  d = max(d, __builtin_amdgcn_update_dpp(d, d, 0x122, 0xf, 0xf, true));
  d = max(d, __builtin_amdgcn_update_dpp(d, d, 0x121, 0xf, 0xf, true));
  unsigned m;
  asm("s_max_i32 %0, %1, %2\n\ts_max_i32 %0, %0, %3\n\ts_max_i32 %0, %0, %4" : "=&s"(m)
      : "s"(__builtin_amdgcn_readlane(d, 0)), "s"(__builtin_amdgcn_readlane(d, 16)), "s"(__builtin_amdgcn_readlane(d, 32)), "s"(__builtin_amdgcn_readlane(d, 48)) : "scc");
  return k.g + m;
}
struct Plan { unsigned gx, dx, ease; };
DEV unsigned smax(unsigned a, unsigned b) { return (int)(a - b) > 0 ? a : b; }
DEV unsigned pick(int d, unsigned a, unsigned b) { unsigned r; asm("s_cmp_gt_i32 %1, 0\n\ts_cselect_b32 %0, %2, %3" : "=s"(r) : "s"(d), "s"(a), "s"(b) : "scc"); return r; }
DEV unsigned when_p(const Plan& t, unsigned last, unsigned more = 0) {
  const unsigned gy = t.gx + t.dx + NS(T_M1), a = last + NS(T_PEN), b = gy + NS(T_RETRY);
  return pick((int)(last + NS(T_MIN) - gy), pick((int)(a - b), a, b) + more, gy);
}
DEV void advance(Plan& t, unsigned last) {
  const int over = (int)(last - t.gx - t.dx + t.ease);
  t.gx = when_p(t, last, NS(T_SAFE)) + NS(T_DY);
  t.dx -= t.ease - (t.ease >> 1); t.ease >>= 1;
  t.dx += pick(over - (int)NS(80), NS(4), pick(over, (unsigned)(over >> 1), (unsigned)(over >> 3)));
}
DEV unsigned next_go(Ctx& c, Clk& k, Plan& t, int par, int slot, unsigned v) {
  SCHED();
  anchor(k);
  SCHED();
  advance(t, latest(c, k, par, slot, v));
  return to_cyc(k, t.gx);
}

DEV constexpr int qk_r(int j) { return j < 4 ? 0 : 1 + (j - 4) % 3; }
DEV constexpr int qk_i(int j) { return j < 4 ? j : (j - 4) / 3; }
DEV constexpr int qk_upto(int u) { return K_FIRST && u >= 3 ? (u + 3 < 16 ? u + 3 : 16) : (u + 4 < 16 ? u + 4 : 16); }
DEV void qk_role(const Params& p, Ctx& c, int h) {
  constexpr int WL = 0, MEMS = 131072, KXL = 147456, QKX = 155648, QROW = 272;
  c.budget = SPIN_CAP;
  const int w = c.w;
  Clk k = {0u, 0u, 0u, 0u, 0u, 0u};
  anchor(k, true);
  v8u wa[32], wh[16];
  unsigned look = claim_look(c);
  bool claimed = false;
  hold(c, QK_AT);
  {
    const int lane = fresh_lane(), g = lane >> 4, cc = lane & 15;
    const rsrc_t rq = mkrs(p.Wq, 1024 * 4096 * 2), rk = mkrs(p.Wk, 1024 * 4096 * 2);
    int vo[4];
#pragma unroll
    for (int i = 0; i < 4; ++i) vo[i] = opq((256 * w + 64 * (i ^ turn(h, w)) + krow_lane(g)) * 8192 + (h * 128 + 8 * cc) * 2);
    v4u row[16][8];
    auto request = [&](int u) {
#pragma unroll
      for (int r = 0; r < 8; ++r) row[u][r] = ld16w<W_AUX>((u < 8) != K_FIRST ? rq : rk, vo[(u >> 1) & 3], krow(4 * (u & 1) + (r >> 1), r & 1) * 8192);
    };
#pragma unroll
    for (int u = 0; u < qk_upto(-1); ++u) { request(u); if (u == 0) { SCHED(); ask(c, look); SCHED(); } }
    SCHED();
    const int sw = MEMS + w * 8192 + g * 256, lw = opq(WL + w * 32768 + lane * 16);
    int sr[2];
#pragma unroll
    for (int t = 0; t < 2; ++t) { const int i = (2 * cc + t) & 7; sr[t] = opq(i * 1024 + g * 256 + ((4 * w + (cc >> 2) + 2 * i) & 15) * 16); }
    sfor<16>([&](auto U) __attribute__((always_inline)) {
      constexpr int u = U.value;
#pragma unroll
      for (int i = 0; i < 8; ++i) {
        v4u f;
#pragma unroll
        for (int r = 0; r < 4; ++r) f[r] = prm(row[u][2 * r + 1][i >> 1], row[u][2 * r][i >> 1], i & 1);
        sts16(c.L, sw + i * 1024 + ((cc + 2 * i) & 15) * 16, f);
      }
      SCHED();
      if ((u == 5 || u == 10) && !claimed) claimed = claim_try(c, look);
      if (u == 2 || u == 7) look = claim_look(c);
      if (qk_upto(u) > qk_upto(u - 1)) request(qk_upto(u - 1));
      if (u == 15 && !claimed) claim(c, 0);
      BAR();
      v4u in[4][2];
#pragma unroll
      for (int t = 1; t >= 0; --t)
#pragma unroll
        for (int r = 0; r < 4; ++r) in[r][t] = lds16(c.L, sr[t] + MEMS + ((w + r) & 3) * 8192);
      SCHED();
#pragma unroll
      for (int j = 0; j < 16; ++j) {
        if (qk_i(j) != ((u >> 1) & 3)) continue;
#pragma unroll
        for (int t = 1; t >= 0; --t) {
          const v4u o = in[qk_r(j)][t];
          constexpr bool isq = (u < 8) != K_FIRST;
          if (!isq && t == 1) sts16(c.L, lw + j * 2048 + (u & 1) * 1024, o);
          else {
            v8u& d = isq ? wa[2 * j + t] : wh[j];
#pragma unroll
            for (int r = 0; r < 4; ++r) d[4 * (u & 1) + r] = o[r];
#define M_(r) asm volatile("" : "+" r(d))
            if (u & 1) { if (isq) AT_ACC(M_, 2 * j + t); else AT_HOME(M_, j); }
#undef M_
          }
        }
      }
      BAR();
    });
  }
  STAMP(0);
  BAR();
  const unsigned vote = wait_verdict(c);
  const int par = vote_b(vote), para = vote_a(vote);
  const int lane = fresh_lane(), g = lane >> 4, cc = lane & 15;
  const int moff = opq(amap(w * 4096 + lane * 16, par) + (turn(h, w) & 1) * 1024), m2 = R_MEM + (turn(h, w) & 2) * 1024;
  unsigned tries = 1u << 12;
  *(volatile LAS unsigned*)(c.L + 160512) = 0u;
  LGKM(0);
  const int idx = opq((lane & 1) ? 0xEEEE : 0x4444);
  TICK_DECL;
  const int l0 = opq(WL + w * 32768 + lane * 16), l1 = opq(MEMS + lane * 16);
  const int qw = opq(QKX + (2 * w + (cc >> 3)) * 256 + g * 64 + (cc & 1) * 16 + ((cc >> 1) & 3) * 4), kw = opq(QKX + (8 + 2 * g) * QROW + (32 * w + 2 * cc) * 2);
#define QK_PINS PIN_ACC0(wa); PIN_ACC1(wa); PIN_HOME(wh)
#define QK_IN [l0] "v"(l0), [l1] "v"(l1), [mo] "v"(moff), [ix] "v"(idx), [qw] "v"(qw), [kw] "v"(kw), [rs] "s"(c.rs), [sb] "s"(m2 + (s & 3) * MEM_SLOT)
#define QK_CL CL_S, CL_16_32, CL_160_200, CL_200_216, CL_216_256
  if (w) RAWBAR();
  if (w == 3) {
    const int hq = opq(lane ? 1 << 30 : amap(24576 + h * 128, par));
    anchor(k, true);
    unsigned tg = k.cyc;
    Plan tt = {k.g - NS(100000), NS(T_DX + T_EASE), NS(T_EASE)};
    for (int s = 0; s < NSEG; ++s) {
      const int pb = R_P + (s & 3) * P_SLOT;
      QK_PINS;
      asm volatile(QK_STEP3 : [bd] "+s"(c.budget), [rt] "+s"(tries) : QK_IN, [hq] "v"(hq), [pb] "s"(pb), [tg] "s"(tg), [ca] "s"(k.cyc - T_DQ), [k1] "s"(k.k1), [ga] "s"(k.g), [dr] "n"(T_RQ), [pr] "s"(R_P + ((s + 1) & 3) * P_SLOT) : QK_CL);
      QK_PINS;
      LOG(1, s, tg);
      const unsigned sv = ask_stamps(c, par, pb);
      SCHED();
      anchor(k);
      SCHED();
      advance(tt, latest(c, k, par, pb, sv));
      tg = to_cyc(k, tt.gx);
      RAWBAR();
      LOG(2, s, k.cyc); LOG(3, s, k.g); LOG(4, s, tt.dx);
      st4s(mkrs((const void*)c.ctrl, CTRL_BYTES), hq & (1 << 30), C_FLAG + (F_S + h) * 128, (unsigned)s + 1u);
      TICK_SUM(10);
      close_in(k, tg);
      tg = to_cyc(k, tt.gx);
    }
  } else {
    const int kx0 = opq(KXL + g * 512 + cc * 16), kmrd = opq(QKX + g * 16 + (8 + (cc & 7)) * QROW);
    unsigned seen = NOFLAG;
    if (w == 0) {
      const int prow = (2 * g + (cc & 1)) * 2 + ((cc >> 1) & 1);
      const int px = opq(amap((h >> 1) * 1024 + ((2 * (h & 1) + (cc >> 3)) * 16 + prow) * 16 + ((cc >> 2) & 1) * 8, par));
      const int pm = opq(cc < 8 ? amap(16384 + h * 128 + (2 * g + (cc & 1)) * 16 + (cc & 2) * 4 + (cc & 4), par) : 1 << 30);
      const int ps = opq((cc & 1) ? 0x03020706 : 0x05040100), pa = opq(lane * 128);
      for (int s = 0; s < NSEG; ++s) {
        if (seen < (unsigned)s + 1u) seen = wait_flag(c, F_K + h, s + 1);
        QK_PINS;
        unsigned pd;
        asm volatile(QK_STEP0 : [bd] "+s"(c.budget), [rt] "+s"(tries), [sq] "+s"(seen), [pd] "=&v"(pd)
                     : QK_IN, [kx] "v"(kx0), [km] "v"(kmrd), [px] "v"(px), [pm] "v"(pm), [ps] "v"(ps), [pb] "s"(R_P + (s & 3) * P_SLOT), [pr] "s"(R_P + ((s + 1) & 3) * P_SLOT),
                       [kb] "s"(R_KX - KXL + xblk(s, h, para)), [fq] "s"(c.ctrl + (C_FLAG + (F_K + h) * 128) / 4),
                       [kn] "s"(R_KX + xblk(min(s + 1, NSEG - 1), h, para)), [pa] "v"(pa) : QK_CL);
        QK_PINS;
        LITE(s); LOGS(0, s);
        TICK_SUM(10);
      }
      RAWBAR();
    } else {
      const int qt = w - 1;
      const int px = opq(amap(qt ? 16384 + h * 1024 + lane * 16 : h * 512 + (g >> 1) * 256 + cc * 16 + (g & 1) * 8, para));
      const int pm = opq(g < 2 ? amap(49152 + (qt * 32 + h) * 256 + cc * 16 + g * 8, para) : 1 << 30);
      const int ps = opq(cc - 4 * g), hq = opq(g * 512 + (16 * qt + cc) * 16);
#define QK_TOK(STEP) for (int s = 0; s < NSEG; ++s) { QK_PINS; \
        asm volatile(STEP : [bd] "+s"(c.budget), [rt] "+s"(tries), [sq] "+s"(seen) \
                     : QK_IN, [kx] "v"(kx0), [km] "v"(kmrd), [px] "v"(px), [pm] "v"(pm), [ps] "v"(ps), [hq] "v"(hq), [qb] "s"(R_QX + xblk(s, h, para)), [tb] "s"(R_PT + (s & 7) * PT_SLOT), \
                       [tr] "s"(R_PT + ((s + 2) & 7) * PT_SLOT), [sn] "s"(s + 1), [fq] "s"(c.ctrl + (C_FLAG + (F_Q + h) * 128) / 4) : QK_CL); \
        QK_PINS; TICK_SUM(10); }
      if (w == 1) { QK_TOK(QK_STEP1) } else { QK_TOK(QK_STEP2) }
    }
  }
  TICK_OUT();
  if (c.budget == 0u) __hip_atomic_store(c.ctrl + C_TMO / 4, 1u, RLX_AGENT);
}

DEV void shuffle_cols(Ctx& c, int scratch, const v4u (&rows)[8], v4u (&o)[8]) {
  const int g = c.lane >> 4, cc = c.lane & 15;
#pragma unroll
  for (int i = 0; i < 8; ++i) {
    v4u f;
#pragma unroll
    for (int r = 0; r < 4; ++r) f[r] = prm(rows[2 * r + 1][i >> 1], rows[2 * r][i >> 1], i & 1);
    sts16(c.L, scratch + (8 * cc + i) * 64 + ((g ^ (cc & 3)) << 4), f);
  }
  asm volatile("s_waitcnt lgkmcnt(0)" ::: "memory");
#pragma unroll
  for (int i = 0; i < 8; ++i) { const int col = 16 * i + cc; o[i] = lds16(c.L, scratch + col * 64 + ((g ^ ((col >> 3) & 3)) << 4)); }
  asm volatile("s_waitcnt lgkmcnt(0)" ::: "memory");
}

DEV void vo_role(const Params& p, Ctx& c, int h) {
  constexpr int WL = 0, MEMS = 131072, VM = 147456, VROW = 272;
  c.budget = SPIN_CAP;
  const int w = c.w, lane = c.lane = fresh_lane(), g = lane >> 4, cc = lane & 15;
  Clk k = {0u, 0u, 0u, 0u, 0u, 0u};
  anchor(k, true);
  v8u wa[32], wh[16];
  ask(c, claim_look(c));
  unsigned look = 0;
  bool claimed = false;
  hold(c, VO_AT);
  const rsrc_t rwo = mkrs(p.Wo, 4096 * 1024 * 2);
  const int voo = (h * 128 + 4 * g) * 2048 + (256 * ((cc >> 3) ^ (h >> 3)) + 64 * w + 8 * (cc & 7)) * 2;
  v4u buf[3][8];
  auto request_o = [&](int u) {
#pragma unroll
    for (int r = 0; r < 8; ++r) buf[u % 3][r] = ld16w<W_AUX>(rwo, voo ^ ((u >> 2) * 1024), (32 * (u & 3) + 16 * (r >> 2) + (r & 3)) * 2048);
  };
  {
    const rsrc_t rw = mkrs(p.Wv, 1024 * 4096 * 2);
    const int vo = (256 * w + krow_lane(g)) * 8192 + (h * 128 + 8 * cc) * 2;
    v4u vrow[8][8];
    auto request = [&](int u) {
#pragma unroll
      for (int r = 0; r < 8; ++r) vrow[u][r] = ld16w<W_AUX>(rw, vo, (64 * ((u >> 1) ^ turn(h, w)) + krow(4 * (u & 1) + (r >> 1), r & 1)) * 8192);
    };
#pragma unroll
    for (int u = 0; u < 3; ++u) request(u);
    SCHED();
    const int wr_ = MEMS + w * 8192 + g * 256;
    int sr[2];
#pragma unroll
    for (int t = 0; t < 2; ++t) { const int i = (2 * cc + t) & 7; sr[t] = opq(i * 1024 + g * 256 + ((4 * w + (cc >> 2) + 2 * i) & 15) * 16); }
#pragma unroll
    for (int u = 0; u < 8; ++u) {
#pragma unroll
      for (int i = 0; i < 8; ++i) {
        v4u f;
#pragma unroll
        for (int r = 0; r < 4; ++r) f[r] = prm(vrow[u][2 * r + 1][i >> 1], vrow[u][2 * r][i >> 1], i & 1);
        sts16(c.L, wr_ + i * 1024 + ((cc + 2 * i) & 15) * 16, f);
      }
      SCHED();
      if (u == 7) claimed = claim_try(c, look);
      if (u == 4) look = claim_look(c);
      if (u + 3 < 8) request(u + 3); else request_o(u - 5);
      BAR();
      v4u in[4][2];
#pragma unroll
      for (int t = 1; t >= 0; --t)
#pragma unroll
        for (int r = 0; r < 4; ++r) in[r][t] = lds16(c.L, sr[t] + MEMS + ((w + r) & 3) * 8192);
      SCHED();
#pragma unroll
      for (int j = 0; j < 16; ++j) {
        if (qk_i(j) != (u >> 1)) continue;
#pragma unroll
        for (int t = 1; t >= 0; --t) {
          const v4u o = in[qk_r(j)][t];
          if (t == 1 && (j & 1)) sts16(c.L, WL + (w * 16 + (j >> 1)) * 2048 + (u & 1) * 1024 + lane * 16, o);
          else {
            const int n = 3 * (j >> 1) + ((j & 1) ? 2 : t);
#pragma unroll
            for (int r = 0; r < 4; ++r) wa[n][4 * (u & 1) + r] = o[r];
#define M_(r) asm volatile("" : "+" r(wa[n]))
            if (u & 1) AT_ACC(M_, n);
#undef M_
          }
        }
      }
      BAR();
    }
  }
#pragma unroll
  for (int u = 0; u < 8; ++u) {
    v4u o[8];
    shuffle_cols(c, MEMS + w * 8192, buf[u % 3], o);
    SCHED();
    if (u + 3 < 8) request_o(u + 3);
#pragma unroll
    for (int i = 0; i < 4; ++i) {
      const int pr = 4 * (u >> 2) + i, ch = u & 3;
      const v8u f = {o[2 * i][0], o[2 * i + 1][0], o[2 * i][1], o[2 * i + 1][1], o[2 * i][2], o[2 * i + 1][2], o[2 * i][3], o[2 * i + 1][3]};
      if (ch < 2) {
        wh[2 * pr + ch] = f;
#define M_(r) asm volatile("" : "+" r(wh[2 * pr + ch]))
        AT_HOME(M_, 2 * pr + ch);
#undef M_
      } else if (ch == 2) {
        wa[24 + pr] = f;
#define M_(r) asm volatile("" : "+" r(wa[24 + pr]))
        AT_ACC(M_, 24 + pr);
#undef M_
      } else {
        sts16(c.L, WL + (w * 16 + 8 + pr) * 2048 + lane * 16, lo4(f));
        sts16(c.L, WL + (w * 16 + 8 + pr) * 2048 + 1024 + lane * 16, hi4(f));
      }
    }
    SCHED();
  }
  if (!claimed) claim(c, 0);
  BAR();
  STAMP(0);
  const unsigned vote = wait_verdict(c);
  const int par = vote_b(vote), para = vote_a(vote);
  const int moff = opq(amap(w * 4096 + lane * 16, par) + (turn(h, w) & 1) * 1024), m2 = R_MEM + (turn(h, w) & 2) * 1024;
  unsigned tries = 1u << 12;
  *(volatile LAS unsigned*)(c.L + 160512) = 0u;
  LGKM(0);
  const int idx = opq((lane & 1) ? 0xEEEE : 0x4444);
  const int u_off = opq((4 * w + g) * 16384 + (h ^ (8 * g)) * 256 + cc * 16);
  const int hq = opq((lane | w) ? 1 << 30 : amap(24576 + (32 + h) * 128, par));
  const int l0 = opq(WL + w * 32768 + lane * 16), l1 = opq(MEMS + lane * 16);
  const int vmw = opq(VM + 2 * g * VROW + (32 * w + 2 * cc) * 2);
  const int vmr = opq(VM + (cc >> 1) * VROW + g * 16);
  TICK_DECL;
  anchor(k, true);
  unsigned tg = k.cyc;
  Plan tt = {k.g - NS(100000), NS(T_DX + T_EASE), NS(T_EASE)};
  for (int s = 0; s < NSEG; ++s) {
    const int pb = R_P + (s & 3) * P_SLOT;
    PIN_ACC0(wa); PIN_ACC1(wa); PIN_HOME(wh);
    asm volatile(VO_STEP
        : [bd] "+s"(c.budget), [rt] "+s"(tries)
        : [l0] "v"(l0), [l1] "v"(l1), [mo] "v"(moff), [ix] "v"(idx), [vw] "v"(vmw), [vr] "v"(vmr), [uo] "v"(u_off), [w] "s"(w), [rs] "s"(c.rs),
          [sb] "s"(m2 + (s & 3) * MEM_SLOT), [ub] "s"(u_slot(s, para) + (h >> 3) * 16 * 16384), [ur] "s"(u_slot(s + 2, para) + (2 * h + (w >> 1)) * 16384 + (w & 1) * 4096 - MEMS), [tg] "s"(tg + T_VLAG), [hq] "v"(hq), [pb] "s"(pb),
          [ca] "s"(k.cyc - T_DV), [k1] "s"(k.k1), [ga] "s"(k.g), [dr] "n"(T_RV), [pr] "s"(R_P + ((s + 1) & 3) * P_SLOT)
        : CL_S, CL_16_32, CL_160_200, CL_200_216, CL_216_256);
    PIN_ACC0(wa); PIN_ACC1(wa); PIN_HOME(wh);
    if (w == 0) {
      LITE(s);
      LOG(1, s, tg);
      const unsigned sv = ask_stamps(c, par, pb);
      SCHED();
      anchor(k);
      const unsigned gc = tt.gx + tt.dx + NS(T_M1 + T_CLR);
      const bool ontime = (int)(gc - k.g) > 0;
      if (ontime) { wait_cyc(to_cyc(k, gc)); RAWBAR(); }
      SCHED();
      advance(tt, latest(c, k, par, pb, sv));
      if (!ontime) { wait_cyc(to_cyc(k, tt.gx - NS(T_DY + T_SAFE - T_CLR))); RAWBAR(); }
      tg = to_cyc(k, tt.gx);
      LOG(2, s, k.cyc); LOG(3, s, k.g);
    }
    TICK_SUM(10);
    if (w == 0) { close_in(k, tg); tg = to_cyc(k, tt.gx); }
  }
  TICK_OUT();
  if (c.budget == 0u) __hip_atomic_store(c.ctrl + C_TMO / 4, 1u, RLX_AGENT);
}

DEV void y_role(const Params& p, Ctx& c, int y) {
  constexpr int ZR = 0, UL = 131072, CW = 155648;
  c.budget = SPIN_CAP;
  const int w = c.w, lane = c.lane = fresh_lane(), g = lane >> 4, cc = lane & 15;
  const v4u sent4 = {SENT, SENT, SENT, SENT};
  STAMP(2);
  Clk k = {0u, 0u, 0u, 0u, 0u, 0u};
  anchor(k, true);
  unsigned m0 = 0;
  if (w == 0) {
    unsigned best[2] = {~0u, ~0u};
    const unsigned first = claim_look(c);
    st4s(c.rs, y * 16384 + (lane >> 5) * 8192 + (lane & 31) * 4, R_U, SENT);
    ask(c, first);
#pragma unroll 1
    for (int n = 0; n < 12; ++n) {
#pragma unroll
      for (int r = 0; r < 2; ++r) {
        VMWAIT(0);
        const unsigned t0 = (unsigned)__builtin_amdgcn_s_memtime();
        LGKM(0);
        st4s(c.rs, y * 16384 + (lane & 31) * 4, R_U + r * 8192, SENT);
        VMWAIT(0);
        const unsigned t1 = (unsigned)__builtin_amdgcn_s_memtime();
        LGKM(0);
        best[r] = min(best[r], t1 - t0);
      }
      if (n + 1 >= PROBES && min(best[0], best[1]) < 600u && max(best[0], best[1]) > min(best[0], best[1]) + 128u) break;
    }
    STAMP(5);
    claim(c, CL_VOTED | (best[1] < best[0] ? CL_A : 0u));
    m0 = *(const GAS unsigned*)(p.mem0 + (lane >> 3) * 1024 + 16 * y + 2 * (lane & 7));
    m0 = m0 < SENT - 1u ? m0 : SENT - 1u;
    if (lane < 8) *(LAS unsigned*)(c.L + CW + lane * 4) = 0u;
    const unsigned v = wait_vote(c);
    STAMP(7);
    const int oa = y * 16384 + vote_a(v) * 8192 + lane * 16, ob = amap(y * 3072 + lane * 16, vote_b(v));
#pragma unroll
    for (int i = 0; i < 16; ++i) st16s(c.rs, oa + (i & 3) * 1024, R_U + (i >> 3) * MB + ((i >> 2) & 1) * 4096, sent4);
#pragma unroll
    for (int i = 0; i < 3; ++i) st16s(c.rs, amap(y * 3072 + i * 1024 + lane * 16, vote_b(v)), R_MEM, sent4);
#pragma unroll
    for (int i = 0; i < 8; ++i) st16s(c.rs, oa + (i & 3) * 1024, R_PT + (i >> 2) * 4096, sent4);
    (void)ob;
    VMWAIT(0);
    if (lane == 0) __hip_atomic_fetch_add(c.ctrl + (C_CLAIM + c.att * 1024) / 4 + c.slot, CL_FILLED, RLX_AGENT);
    STAMP(6);
  } else {
    if (YH && w < 3 && (y & 8) == 0) {
      hold(c, YH_AT);
#pragma unroll
      for (int i = 0; i < 8; ++i) {
        const u16* q = (i < 4) != K_FIRST ? p.Wq : p.Wk;
        if (i == 4) hold(c, YH_AT + YH_STEP);
        m0 += __builtin_amdgcn_raw_buffer_load_b32(mkrs(q, 8 * MB), slow_line(q, y >> 1, (y & 7) + 8 * (i & 3), lane) ^ ((w - 1) * 128), 0, 0);
      }
      asm volatile("" :: "v"(m0));
    }
  }
  BAR();
  if (w == 1 || w == 2) {
    if (c.b == SERVER) wait_verdict(c); else hold(c, X_AT);
#pragma unroll
    for (int i = 0; i < 4; ++i) m0 += __builtin_amdgcn_raw_buffer_load_b32(mkrs(p.x, 4 * MB), ((2 * y + w - 1) * 64 + lane) * 128, i * MB, 0);
    asm volatile("" :: "v"(m0));
  }
  const unsigned vote = wait_verdict(c);
  const int par = vote_b(vote), para = vote_a(vote);
  const int mo = amap(y * 256 + lane * 4, par);
  if (w == 0) st4s(c.rs, mo, R_MEM, m0);
  STAMP(0);
  const int rot = y >> 4;
  volatile LAS unsigned* const GO = (volatile LAS unsigned*)(c.L + CW);
  TICK_DECL;
  if (w == 0) {
    int xo[4];
#pragma unroll
    for (int i = 0; i < 4; ++i) xo[i] = amap((i ^ rot) * 4096 + lane * 16, par);
    const int pmo = amap(16384 + rot * 1024 + (2 * g + (cc & 1)) * 128 + (cc >> 1) * 16, par), psel = (cc & 1) ? 0x03020706 : 0x05040100;
    constexpr int lead = T_U0, late = 0;
    anchor(k, true);
    Plan tt = {k.g - NS(100000), NS(T_DX + T_EASE), NS(T_EASE)};
    unsigned done = k.cyc;
    const int so = amap(24576, par) + lane * 128;
    const int idx = opq((lane & 1) ? 0xEEEE : 0x4444);
    const int uo = y * 16384 + lane * 16;
    const int mw = amap(y * 256 + (2 * g + (cc & 1)) * 32 + (cc >> 1) * 4, par);
    for (int s = 0; s < NSEG - 1; ++s) {
      if (s == 0) RAWBAR();
      const int za = ZR + (s & 3) * 32768 + rot * 8192 + lane * 16;
      v32u z0, z1, z2, z3;
      asm volatile(
          "ds_read_b128 a[0:3], %[z0] offset:0\n\t" "ds_read_b128 a[4:7], %[z0] offset:1024\n\t" "ds_read_b128 a[8:11], %[z0] offset:2048\n\t" "ds_read_b128 a[12:15], %[z0] offset:3072\n\t"
          "ds_read_b128 a[16:19], %[z0] offset:4096\n\t" "ds_read_b128 a[20:23], %[z0] offset:5120\n\t" "ds_read_b128 a[24:27], %[z0] offset:6144\n\t" "ds_read_b128 a[28:31], %[z0] offset:7168\n\t"
          "ds_read_b128 a[32:35], %[z1] offset:0\n\t" "ds_read_b128 a[36:39], %[z1] offset:1024\n\t" "ds_read_b128 a[40:43], %[z1] offset:2048\n\t" "ds_read_b128 a[44:47], %[z1] offset:3072\n\t"
          "ds_read_b128 a[48:51], %[z1] offset:4096\n\t" "ds_read_b128 a[52:55], %[z1] offset:5120\n\t" "ds_read_b128 a[56:59], %[z1] offset:6144\n\t" "ds_read_b128 a[60:63], %[z1] offset:7168\n\t"
          "ds_read_b128 a[64:67], %[z2] offset:0\n\t" "ds_read_b128 a[68:71], %[z2] offset:1024\n\t" "ds_read_b128 a[72:75], %[z2] offset:2048\n\t" "ds_read_b128 a[76:79], %[z2] offset:3072\n\t"
          "ds_read_b128 a[80:83], %[z2] offset:4096\n\t" "ds_read_b128 a[84:87], %[z2] offset:5120\n\t" "ds_read_b128 a[88:91], %[z2] offset:6144\n\t" "ds_read_b128 a[92:95], %[z2] offset:7168\n\t"
          "ds_read_b128 a[96:99], %[z3] offset:0\n\t" "ds_read_b128 a[100:103], %[z3] offset:1024\n\t" "ds_read_b128 a[104:107], %[z3] offset:2048\n\t" "ds_read_b128 a[108:111], %[z3] offset:3072\n\t"
          "ds_read_b128 a[112:115], %[z3] offset:4096\n\t" "ds_read_b128 a[116:119], %[z3] offset:5120\n\t" "ds_read_b128 a[120:123], %[z3] offset:6144\n\t" "ds_read_b128 a[124:127], %[z3] offset:7168\n\t"
          "s_waitcnt lgkmcnt(0)"
          : "={a[0:31]}"(z0), "={a[32:63]}"(z1), "={a[64:95]}"(z2), "={a[96:127]}"(z3) : [z0] "v"(za), [z1] "v"(za ^ 8192), [z2] "v"(za ^ 16384), [z3] "v"(za ^ 24576) : "memory");
      const int pb = R_P + (s & 3) * P_SLOT;
      v32u uu;
      const int ub = u_slot(s, para);
      TICK(0);
      anchor(k);
      TICK(8);
      unsigned gp = tt.gx + tt.dx + NS(T_M1), lt = late, sv = SENT;
      unsigned bad = (int)(done - to_cyc(k, tt.gx)) > T_OVH;
      TICK(4);
      LOG(2, s, k.cyc); LOG(3, s, k.g);
#pragma unroll 1
      for (;;) {
        if (bad) {
          const bool all = allok(sv);
          const unsigned last = latest(c, k, par, pb, sv);
          if (!all) mark(k);
          gp = when_p(tt, last);
          lt = 1;
        }
        close_in(k, to_cyc(k, gp) - (lt ? 96 : lead));
        const unsigned tg = to_cyc(k, gp);
        if (!bad) LOG(1, s, tg);
        asm volatile(Y_STEP
          : "=&{v[144:175]}"(uu), [bad] "=&s"(bad), [sv] "=&v"(sv)
          : [x0] "v"(xo[0]), [x1] "v"(xo[1]), [x2] "v"(xo[2]), [x3] "v"(xo[3]), [pm] "v"(pmo), [u0] "v"(uo + (rot ^ (y & 3)) * 2048), [mw] "v"(mw), [ps] "v"(psel), [ix] "v"(idx),
            [rs] "s"(c.rs), [pb] "s"(pb), [ub] "s"(ub), [mb] "s"(R_MEM + ((s + 1) & 3) * MEM_SLOT), [mr] "s"(R_MEM + ((s + 3) & 3) * MEM_SLOT), [tu] "s"(tg - (lt ? 96 : lead)), [tg] "s"(tg - 32), [late] "s"(lt),
            [so] "v"(so), [bd] "s"(c.budget),
            "{a[0:31]}"(z0), "{a[32:63]}"(z1), "{a[64:95]}"(z2), "{a[96:127]}"(z3)
          : CL_S, CL_120_128, CL_176_200, CL_200_216, CL_216_256);
        bad = __builtin_amdgcn_readfirstlane(bad);
        if (bad != 1u) break;
        spend(c);
      }
      done = bad;
      SCHED();
      const int ul = opq(UL + rot * 2048 + lane * 16);
#pragma unroll
      for (int i = 0; i < 8; ++i) sts16(c.L, (ul ^ ((i >> 1) * 2048)) + (i & 1) * 1024, v4u{uu[4 * i], uu[4 * i + 1], uu[4 * i + 2], uu[4 * i + 3]});
      asm volatile("" ::: "memory");
      *GO = s + 1;
      asm volatile("s_waitcnt vmcnt(2)" : "+v"(sv));
      advance(tt, latest(c, k, par, pb, sv));
      TICK(3);
      LITE(s); LOG(0, s, done);
      TICK_LDS(CW + 256);
    }
    {
      anchor(k);
      wait_cyc(to_cyc(k, tt.gx + tt.dx + NS(T_M1)));
      const int ub = u_slot(NSEG - 1, para);
      v4u u[8];
#pragma unroll 1
      for (;;) {
        asm volatile("" ::: "memory");
#pragma unroll
        for (int i = 0; i < 8; ++i) u[i] = ld16s(c.rs, uo + (i & 3) * 1024, ub + (i >> 2) * 4096);
        SCHED();
        unsigned t = 0;
#pragma unroll
        for (int i = 0; i < 8; ++i) t = umax(t, top(u[i]));
        if (allok(t) || spend(c)) break;
      }
      RAWBAR();
#pragma unroll
      for (int i = 0; i < 8; ++i) sts16(c.L, UL + (i ^ (2 * (y & 3))) * 1024 + lane * 16, u[i]);
      asm volatile("" ::: "memory");
      *GO = NSEG;
    }
    TICK_DUMP(CW + 256);
  } else if (w < 3) {
    const rsrc_t ro = mkrs(p.out, 2048 * 2048 * 2);
    auto tokens = [&](auto QT) {
      constexpr int qt = QT.value, NX = qt ? 32 : 16;
      RAWBAR();
      for (int s = 0; s < NSEG; ++s) {
        const int tb = R_PT + (s & 7) * PT_SLOT + para * 8192;
        int ua[4], za[4], xb[4], mb[4];
#pragma unroll
        for (int i = 0; i < 4; ++i) {
          ua[i] = opq(UL + (i ^ rot) * 2048 + lane * 16);
          za[i] = opq(ZR + (s & 3) * 32768 + (i ^ rot) * 8192 + (qt ? lane * 16 : (g >> 1) * 1024 + (g & 1) * 512 + cc * 16));
          xb[i] = tb + amap(qt * 16384 + (i ^ rot) * (qt ? 8192 : 4096), 0);
          mb[i] = tb + amap(49152 + qt * 8192, 0) + (i ^ rot) * 2048;
        }
        SCHED();
        RAWBAR();
        if (TOK_WAIT) __builtin_amdgcn_s_sleep(TOK_WAIT);
        TICK(0);
        f32x4 a;
#pragma unroll 1
        for (;;) {
          asm volatile("" ::: "memory");
          v4u pt[NX + 8];
#pragma unroll
          for (int i = 0; i < NX; ++i) pt[i] = ld16s(c.rs, (i & 3) * 1024 + lane * 16, qt ? xb[i >> 3] + ((i >> 2) & 1) * 4096 : xb[i >> 2]);
#pragma unroll
          for (int i = 0; i < 8; ++i) pt[NX + i] = ld16s(c.rs, (i & 1) * 1024 + lane * 16, mb[i >> 1]);
          SCHED();
          TICK(1);
#pragma unroll 1
          while (__builtin_amdgcn_readfirstlane(*GO) < (unsigned)s + 1u) { if (spend(c)) break; }
          asm volatile("" ::: "memory");
          f32x4 d[2] = {f32x4{0.f, 0.f, 0.f, 0.f}, f32x4{0.f, 0.f, 0.f, 0.f}};
          v4u fa[8];
          auto frag = [&](int i) {
            if (i >= NX) fa[i & 7] = lds16(c.L, ua[(i - NX) >> 1] + (i & 1) * 1024);
            else if (qt) fa[i & 7] = lds16(c.L, za[i >> 3] + (i & 7) * 1024);
            else {
              const v2u lo = *(LAS v2u*)(c.L + za[i >> 2] + (i & 3) * 2048), hi = *(LAS v2u*)(c.L + za[i >> 2] + (i & 3) * 2048 + 256);
              fa[i & 7] = v4u{lo.x, lo.y, hi.x, hi.y};
            }
          };
#pragma unroll
          for (int i = 0; i < 7; ++i) frag(i);
          SCHED();
#pragma unroll
          for (int i = 0; i < NX + 8; ++i) {
            if (i + 7 < NX + 8) frag(i + 7);
            d[i & 1] = mfma16(fa[i & 7], pt[i], d[i & 1]);
            SCHED();
          }
          a = d[0] + d[1];
          if (!__any(a[0] != a[0])) break;
          unsigned t = 0;
#pragma unroll
          for (int i = 0; i < NX + 8; ++i) t = umax(t, top(pt[i]));
          if (allok(t) || spend(c)) break;
        }
        __builtin_amdgcn_raw_buffer_store_b64(v2u{pk(a[0], a[1]), pk(a[2], a[3])}, ro, (16 * qt + cc) * 4096 + (16 * y + 4 * g) * 2, s * 32 * 4096, OUT_AUX);
        TICK(2);
        TICK_SUM(3);
      }
    };
    if (w == 1) tokens(IC<0>{}); else tokens(IC<1>{});
  } else {
    unsigned seenZ = NOFLAG;
    const rsrc_t rc = mkrs((const void*)c.ctrl, CTRL_BYTES);
    const int fo = lane < 32 ? lane * 128 : 1 << 30;
    __builtin_amdgcn_s_setprio(3);
    auto least = [&](unsigned v) {
      v |= lane < 32 ? 0u : ~0u;
#pragma unroll
      for (int o = 1; o < 32; o <<= 1) v = min(v, (unsigned)__shfl_xor((int)v, o));
      seenZ = umax(seenZ, __builtin_amdgcn_readfirstlane(v));
    };
    auto await = [&](unsigned need) {
#pragma unroll 1
      while (seenZ < need) {
        asm volatile("" ::: "memory");
        least(__builtin_amdgcn_raw_buffer_load_b32(rc, fo, C_FLAG + (F_Z + (y >> 4) * 32) * 128, AUX_SC1));
        if (seenZ >= need || spend(c)) break;
        __builtin_amdgcn_s_sleep(1);
      }
      __builtin_amdgcn_fence(__ATOMIC_ACQUIRE, "wavefront");
    };
    for (int n = 0; n < NSEG; ++n) {
      TICK(0);
      await(n + 1);
      if (Z_WAIT) __builtin_amdgcn_s_sleep(Z_WAIT);
      TICK(1);
      v4u z[32];
      int za[8];
#pragma unroll
      for (int i = 0; i < 8; ++i) za[i] = opq(ZR + (n & 3) * 32768 + (i ^ z_mix(y)) * 1024 + lane * 16);
      unsigned look;
#pragma unroll
      for (int i = 0; i < 32 + Z_WIN; ++i) {
        if (i < 32) z[i] = ld16z(c.rs, (i & 3) * 1024 + lane * 16, R_Z + (n & (ZRING - 1)) * Z_SLOT + y * 65536 + ((para ^ (y >> 5)) & 1) * 8192 + amap((i >> 2) * 4096, 0));
        if (i == 32) { look = __builtin_amdgcn_raw_buffer_load_b32(rc, seenZ < (unsigned)n + 2u ? fo : 1 << 30, C_FLAG + (F_Z + (y >> 4) * 32) * 128, AUX_SC1); TICK(4); }
        SCHED();
        if (i >= Z_WIN) sts16(c.L, za[(i - Z_WIN) & 7] + ((i - Z_WIN) >> 3) * 8192, z[i - Z_WIN]);
        else asm volatile("s_nop " STR(Z_GAP));
        if (i == 30 + Z_WIN) { TICK(3); }
        SCHED();
      }
      least(look);
      if (n == 0) await(PRIME);
      TICK(2);
      { const int s = n; (void)s; TICK_SUM(3); }
      BAR();
    }
    RAWBAR();
  }
  TICK_OUT();
  if (c.budget == 0u) __hip_atomic_store(c.ctrl + C_TMO / 4, 1u, RLX_AGENT);
}

#define MF1A(d, a, b) asm volatile(MF16 "%0, %1, %2, %0" : "+v"(d) : "a"(a), "v"(b))
#define MF32A(d, a, b) asm volatile("v_mfma_f32_32x32x16_bf16 %0, %1, %2, %0" : "+v"(d) : "a"(a), "v"(b))
#define MF32AZ(d, a, b) asm volatile("v_mfma_f32_32x32x16_bf16 %0, %1, %2, 0" : "=&v"(d) : "a"(a), "v"(b))
#define MF1B(d, a, b) asm volatile(MF16 "%0, %1, %2, %0" : "+v"(d) : "v"(a), "a"(b))
#define MF1BZ(d, a, b) asm volatile(MF16 "%0, %1, %2, 0" : "=&v"(d) : "v"(a), "a"(b))
#define MF1AZ(d, a, b) asm volatile(MF16 "%0, %1, %2, 0" : "=&v"(d) : "a"(a), "v"(b))
DEV void g_role(const Params& p, Ctx& c, int mat, int h, int gi) {
  constexpr int EX = 131072;
  c.budget = SPIN_CAP;
  const int w = c.w, lane = c.lane = fresh_lane(), g = lane >> 4, cc = lane & 15;
  unsigned look = claim_look(c);
  const int flip = FLIP && mat < 2 ? ((h >> 4) & 1) ^ 1 : 0;
  unsigned t[7];
  {
    auto pull = [&](const u16* q, int bytes, int v, int so) { return __builtin_amdgcn_raw_buffer_load_b32(mkrs(q, bytes), v, so, 0); };
    const int all = mat < HELPERS ? 8 * MB : 0;
    t[6] = pull(p.Wv, SLOW_V * all, slow_line(p.Wv, h, (h & 7) + 8 * w, lane) ^ (mat * 128), 0);
    t[0] = pull(p.x, all / 2, g * 65536 + (((h & 7) * 4 + w) * 16 + cc) * 128, mat * 4 * 65536);
#pragma unroll
    for (int i = 0; i < 3; ++i) t[1 + i] = pull(p.Wv, HELP_V * all, (256 * w + 4 * (h & 7) + g) * 8192 + (h >> 3) * 2048 + cc * 128, (2 + 2 * i + mat) * 32 * 8192);
    SCHED();
    if (mat < 2) ask(c, look);
    SCHED();
#pragma unroll
    for (int i = 0; i < 2; ++i) t[4 + i] = pull(p.Wo, HELP_O * all, (h * 128 + (lane >> 1)) * 2048 + w * 512 + 256 + (lane & 1) * 128, (2 * i + mat) * 32 * 2048);
    SCHED();
    if (mat == 0) claim(c, 0);
    hold(c, mat == 0 ? GQ_AT : mat == 1 ? GK_AT : 0);
  }
  const rsrc_t rx = mkrs(p.x, 2048 * 2048);
  const int xs = w * 16384 + ((lane * 16) ^ ((w & 1) << 7));

  v32u wa[8];
  {
    const rsrc_t rw = mkrs(mat == 0 ? p.Wq : mat == 1 ? p.Wk : p.Wv, 1024 * 4096 * 2);
    const int vo0 = opq((256 * w + 128 * flip + 8 * g) * 8192 + (h * 128 + 8 * cc) * 2), vo1 = opq(vo0 ^ (128 * 8192));
    v4u row[8][8];
    auto request = [&](int u) {
#pragma unroll
      for (int r = 0; r < 8; ++r) {
        row[u][r] = ld16w<0>(rw, (u & 4) ? vo1 : vo0, (32 * (u & 3) + r) * 8192);
        if ((r & 3) == 3) { const int n = 2 * u + (r >> 2); dma16(rx, c.L + w * 16384 + n * 1024, (xs ^ ((n >> 1) << 4)) + n * 1024); }
      }
    };
    const unsigned look0 = claim_look(c);
    bool claimed = false;
    request(0);
    SCHED();
    if (mat == 2) ask(c, look);
    SCHED();
    request(1); request(2);
    if (G_DEEP) request(3);
    SCHED();
    const int sw_ = EX + w * 8192 + g * 256;
    const int sr0 = opq(EX + (cc & 3) * 1024 + g * 256 + ((4 * w + (cc >> 2) + 4 * cc) & 15) * 16), sr1 = sr0 + 4096;
#pragma unroll
    for (int u = 0; u < 8; ++u) {
#pragma unroll
      for (int i = 0; i < 8; ++i) {
        v4u f;
#pragma unroll
        for (int r = 0; r < 4; ++r) f[r] = prm(row[u][2 * r + 1][i >> 1], row[u][2 * r][i >> 1], i & 1);
        sts16(c.L, sw_ + i * 1024 + ((cc + 4 * i) & 15) * 16, f);
      }
      SCHED();
      if (u == 0) asm volatile("" :: "v"(t[0]), "v"(t[1]), "v"(t[2]), "v"(t[3]), "v"(t[4]), "v"(t[5]), "v"(t[6]));
      if (u == 0) claimed = mat == 0 || claim_try(c, look0);
      if (u == 4 - G_DEEP) look = claim_look(c);
      if (u + 3 + G_DEEP < 8) request(u + 3 + G_DEEP);
      if (u == 7 && !claimed && !claim_try(c, look)) claim(c, 0);
      BAR();
      v8u in[4];
#pragma unroll
      for (int q = 0; q < 4; ++q) in[q] = cat(lds16(c.L, sr0 + q * 8192), lds16(c.L, sr1 + q * 8192));
      SCHED();
#pragma unroll
      for (int q = 0; q < 4; ++q) {
        v8u f = in[q];
#define M_(r) asm volatile("" : "+" r(f))
        AT_ACC(M_, 8 * q + u);
#undef M_
#pragma unroll
        for (int r = 0; r < 8; ++r) wa[2 * q + (u >> 2)][8 * (u & 3) + r] = f[r];
      }
      BAR();
    }
  }
  STAMP(0);
  const int xpar = vote_a(wait_vote(c)) ^ (mat >> 1);
  TICK_DECL;
  asm volatile(G_LOOP
      :: [xo] "v"(cc * 2048 + flip * 256 + ((g ^ cc) << 4)), [xs] "v"(xs), [so] "v"((4 * w + g) * 512 + cc * 16), [fo] "v"((w | lane) ? 1 << 30 : 0),
         [rs] "s"(c.rs), [rx] "s"(rx), [rc] "s"(mkrs((const void*)c.ctrl, CTRL_BYTES)), [fl] "s"(C_FLAG + (mat * 32 + h) * 128), [xp] "s"(xpar),
         [ob] "s"((mat == 1 ? R_KX : R_QX) + h * 16384), [lw] "s"(w * 16384), GTRACE ACC_IN(wa)
      : CL_S, CL_16_32, CL_32_100);
}

DEV void zc_role(const Params& p, Ctx& c, int h) {
  c.budget = SPIN_CAP;
  const int w = c.w, lane = c.lane = fresh_lane(), g = lane >> 4, cc = lane & 15, h2 = lane >> 5, r32 = lane & 31;
  v32u wa[8];
  const unsigned first = claim_look(c);
  {
    const rsrc_t rw = mkrs(p.Wo, 4096 * 1024 * 2);
    const int vo = (h * 128 + 8 * g) * 2048 + (256 * w + 8 * cc) * 2;
    v4u row[8][8];
    auto request = [&](int u) {
#pragma unroll
      for (int r = 0; r < 8; ++r) row[u][r] = ld16w<0>(rw, vo, (32 * (u & 3) + r) * 2048 + (u >> 2) * 256);
    };
    request(0);
    SCHED();
    ask(c, first);
    SCHED();
    request(1); request(2);
    unsigned look = 0;
    const int sw_ = w * 8192 + g * 256, sr_ = w * 8192 + (cc & 7) * 1024 + g * 256;
    v8u two[8];
#pragma unroll
    for (int u = 0; u < 8; ++u) {
#pragma unroll
      for (int i = 0; i < 8; ++i) {
        v4u f;
#pragma unroll
        for (int r = 0; r < 4; ++r) f[r] = prm(row[u][2 * r + 1][i >> 1], row[u][2 * r][i >> 1], i & 1);
        sts16(c.L, sw_ + i * 1024 + ((cc + 2 * i) & 15) * 16, f);
      }
      SCHED();
      if (u == 4) look = claim_look(c);
      if (u + 3 < 8) request(u + 3);
      if (u == 7 && !claim_try(c, look)) claim(c, 0);
      LGKM(0);
#pragma unroll
      for (int q = 0; q < 8; ++q) {
        const v4u o = lds16(c.L, sr_ + ((2 * q + (cc >> 3) + 2 * cc) & 15) * 16);
#pragma unroll
        for (int r = 0; r < 4; ++r) two[q][4 * (u & 1) + r] = o[r];
      }
      SCHED();
      if (u & 1) {
#pragma unroll
        for (int q = 0; q < 8; ++q) {
          const int n = 2 * (8 * (u >> 2) + q) + ((u >> 1) & 1);
#define M_(r) asm volatile("" : "+" r(two[q]))
          AT_ACC(M_, n);
#undef M_
#pragma unroll
          for (int r = 0; r < 8; ++r) wa[n >> 2][8 * (n & 3) + r] = two[q][r];
        }
      }
      LGKM(0);
      SCHED();
    }
  }
  STAMP(0);
  BAR();
  const int para = vote_a(wait_vote(c));
  const int ao = g * 512 + cc * 16, zo = 16 * w * 65536 + lane * 16 + ((para ^ (w >> 1)) & 1) * 8192;
  TICK_DECL;
  asm volatile(ZC_LOOP
      : [bd] "+s"(c.budget)
      : [ao] "v"(ao), [a1] "v"(ao + 4096), [z1] "v"(zo + amap((h ^ z_mix(16 * w)) * 1024, 0)), [z2] "v"(zo + 65536 + amap((h ^ z_mix(16 * w + 1)) * 1024, 0)), [fo] "v"(lane ? 1 << 30 : 0),
        [rs] "s"(c.rs), [rc] "s"(mkrs((const void*)c.ctrl, CTRL_BYTES)), [fv] "s"(C_FLAG + (F_V + h) * 128), [fs] "s"(C_FLAG + (F_S + h) * 128),
        [fz] "s"(C_FLAG + (F_Z + w * 32 + h) * 128), [xp] "s"(para ^ 1), [vb] "s"(R_QX + h * 16384), [vd] "s"(C_VERDICT + c.att * 128), [z0] "n"(ZC_PRIME), [zb] "n"(R_Z), [rg] "n"(ZRING - 2), [rm] "n"(ZRING - 1), GTRACE ACC_IN(wa)
      : CL_S, CL_16_32, CL_32_100);
  if (c.budget == 0u) __hip_atomic_store(c.ctrl + C_TMO / 4, 1u, RLX_AGENT);
}

DEV void conv_role(const Params& p, Ctx& c, int cg, int far) {
  constexpr int FS = 4160, PL = 1280, UO = 4 * FS, WS = UO + 8 * PL;
  const int w = c.w, lane = c.lane = fresh_lane(), tid = w * 64 + lane, g = lane >> 4, cc = lane & 15;
  const int d0 = 8 * cg + 2 * w;
  const int pg = ((far ^ (cg >> 5)) & 1) * 8192;
  auto crow = [](int ch) { return (ch >> 1) * 16384 + (ch & 1) * 4096; };
  const int grp = cg >> 3, de = grp * 64 + (tid & 7) * 8, t0 = (cg & 7) * 256 + (tid >> 3) * 8;
  auto arrive = [&](int n) {
    VMWAIT(0);
    BAR();
    if (w == 0 && lane == 0) __hip_atomic_fetch_add(c.ctrl + (C_GRP + n * 128) / 4, 1u, RLX_AGENT);
  };
  auto await = [&](int n) {
    if (w == 0) {
#pragma unroll 1
      while (__builtin_amdgcn_readfirstlane(__hip_atomic_load(c.ctrl + (C_GRP + n * 128) / 4, RLX_AGENT)) < 8u) { if (spend(c)) break; __builtin_amdgcn_s_sleep(1); }
    }
    BAR();
  };
  v4u fr[2][4], ur[2][4], qr[8];
  unsigned up[2][4];
  const int so = cc * 64 + g * 16;
  const int fw = opq(w * WS + 48 + lane * 16), uw = opq(w * WS + UO + g * PL + cc * 16);
  auto copies = [&](int ch) {
#pragma unroll
    for (int i = 0; i < 4; ++i) {
      const v4u f = fr[ch][i];
      sts16(c.L, fw + 3 * FS + i * 1024, f);
      sts8(c.L, fw + FS + i * 1024 + 8, v2u{f[0], f[1]}); sts8(c.L, fw + FS + i * 1024 + 16, v2u{f[2], f[3]});
#pragma unroll
      for (int r = 0; r < 2; ++r) {
        const int o = fw + 2 * r * FS + i * 1024 + 12 - 8 * r;
        *(LAS unsigned*)(c.L + o) = f[0]; sts8(c.L, o + 4, v2u{f[1], f[2]}); *(LAS unsigned*)(c.L + o + 12) = f[3];
      }
    }
  };
  CST(0);
  const int turns = (grp & 3) == 2 ? 2 : (grp & 3) == 3 ? 0 : 1;
#pragma unroll 1
  for (int n = turns - 1; n >= 0; --n) {
    const rsrc_t rk = mkrs(p.bk, 2048 * 2048), rv = mkrs(p.bv, 2048 * 2048);
    const int dn = de + 64 * n;
    v4u kr[11], vr[8], sh[4];
#pragma unroll
    for (int i = 0; i < 4; ++i) sh[i] = ld16c(mkrs(p.shift, 8192), dn * 8 + i * 16, 0);
#pragma unroll
    for (int i = 0; i < 11; ++i) kr[i] = ld16c(rk, (t0 + i - 3) * 2048 + dn * 2, 0);
#pragma unroll
    for (int i = 0; i < 8; ++i) vr[i] = ld16c(rv, (t0 + i) * 2048 + dn * 2, 0);
    SCHED();
    CST(1);
#pragma unroll
    for (int ch = 0; ch < 8; ++ch) {
      const unsigned sa = sh[ch >> 1][(ch & 1) * 2], sb = sh[ch >> 1][(ch & 1) * 2 + 1];
      const float s0 = blo(sa), s1 = bhi(sa), s2 = blo(sb), s3 = bhi(sb);
      float kk[11], o[8];
#pragma unroll
      for (int i = 0; i < 11; ++i) kk[i] = (ch & 1) ? bhi(kr[i][ch >> 1]) : blo(kr[i][ch >> 1]);
#pragma unroll
      for (int i = 0; i < 8; ++i) o[i] = fmaf(kk[i + 3], s0, fmaf(kk[i + 2], s1, fmaf(kk[i + 1], s2, kk[i] * s3))) * ((ch & 1) ? bhi(vr[i][ch >> 1]) : blo(vr[i][ch >> 1]));
      st16s(c.rs, dn * 8192 + t0 * 2, R_KVT + pg + crow(ch), v4u{pk(o[0], o[1]), pk(o[2], o[3]), pk(o[4], o[5]), pk(o[6], o[7])});
    }
    CST(3);
    arrive(2 * (grp + n));
  }
  const unsigned dd = __builtin_amdgcn_raw_buffer_load_b32(mkrs(p.ssmD, 2048), d0 * 2, 0, CONV_AUX);
#pragma unroll
  for (int i = 0; i < 8; ++i) fr[i >> 2][i & 3] = ld16c(mkrs(p.ssmk, 4 * MB), lane * 16 + (i & 3) * 1024, (d0 + (i >> 2)) * 4096);
  SCHED();
  CST(4);
  await(2 * grp);
  CST(5);
#pragma unroll
  for (int i = 0; i < 8; ++i) {
    const int b = R_KVT + pg + d0 * 8192 + (i >> 2) * 4096 + (i & 3) * 1024;
    ur[i >> 2][i & 3] = ld16s(c.rs, so, b);
    up[i >> 2][i & 3] = __builtin_amdgcn_raw_buffer_load_b32(c.rs, (i & 3) == 0 && lane == 0 ? 1 << 30 : so, b - 4, AUX_SC1);
  }
  SCHED();
  if (lane < 16) sts16(c.L, w * WS + (lane >> 2) * FS + (lane & 3) * 16, v4u{0, 0, 0, 0});
#pragma unroll
  for (int i = 0; i < 2; ++i) sts16(c.L, w * WS + UO + (4 * i + g) * PL + cc * 16, v4u{0, 0, 0, 0});
  STAMP(5);
  const int fa = opq(w * WS + (cc & 3) * FS + (3 + (cc >> 2) - g) * 16);
  sfor<2>([&](auto CH) __attribute__((always_inline)) {
    constexpr int ch = CH.value;
    SCHED();
    copies(ch);
    SCHED();
#pragma unroll
    for (int i = 0; i < 4; ++i) {
      const v4u a = ur[ch][i];
      v4u r0, r1;
#pragma unroll
      for (int r = 0; r < 4; ++r) {
        r0[r] = __builtin_amdgcn_alignbyte(a[3 - r], a[3 - r], 2);
        r1[r] = __builtin_amdgcn_perm(r < 3 ? a[2 - r] : up[ch][i], a[3 - r], 0x07060100u);
      }
      sts16(c.L, uw + (16 + 16 * i) * 16, r0);
      sts16(c.L, uw + 4 * PL + (16 + 16 * i) * 16, r1);
    }
    CST(6 + 2 * ch);
    f32x4 acc[4][2];
#pragma unroll
    for (int i = 0; i < 8; ++i) acc[i >> 1][i & 1] = f32x4{0.f, 0.f, 0.f, 0.f};
    v4u A[2][4], B[2][8];
    auto frag = [&](int d, int n) {
      constexpr int order[12] = {0, 4, 5, 6, 7, 8, 9, 10, 11, 1, 2, 3};
      const int x = order[n];
      if (x < 4) A[d & 1][x] = lds16(c.L, fa + (d + 16 * x) * 64);
      else B[d & 1][x - 4] = lds16(c.L, uw + ((x - 4) & 1) * 4 * PL + (16 + 16 * ((x - 4) >> 1) - d) * 16);
    };
    asm volatile("" ::: "memory");
#pragma unroll
    for (int n = 0; n < 12; ++n) frag(0, n);
    SCHED();
    sfor<16>([&](auto D) __attribute__((always_inline)) {
      constexpr int d = D.value;
      int n = 0;
#pragma unroll
      for (int k = 0; k < 4; ++k)
#pragma unroll
        for (int m = 0; m + k < 4; ++m)
#pragma unroll
          for (int e = 0; e < 2; ++e) {
            acc[m + k][e] = mfma16(A[d & 1][k], B[d & 1][2 * m + e], acc[m + k][e]);
            if (d < 15 && n < 12) frag(d + 1, n);
            if (ch == 0 && n == 14 && (d & 1)) qr[d >> 1] = ld16c(mkrs(p.bq, 4 * MB), (t0 + (d >> 1)) * 2048 + de * 2, 0);
            ++n;
            SCHED();
          }
    });
    CST(7 + 2 * ch);
    const float dv = ch ? bhi(dd) : blo(dd);
#pragma unroll
    for (int q = 0; q < 4; ++q) {
      v4u z;
#pragma unroll
      for (int i = 0; i < 4; ++i) {
        const unsigned yy = pk(acc[q][1][i], acc[q][0][i]), ud = pk(blo(ur[ch][q][i]) * dv, bhi(ur[ch][q][i]) * dv);
        z[i] = pk(blo(yy) + blo(ud), bhi(yy) + bhi(ud));
      }
      st16s(c.rs, so + q * 1024, R_YT + pg + d0 * 8192 + ch * 4096, z);
    }
  });
  STAMP(6);
  {
    const rsrc_t ro = mkrs(p.out, 2048 * 2048 * 2);
    v4u yv[8];
    arrive(2 * grp + 1);
    await(2 * grp + 1);
    CST(10);
#pragma unroll
    for (int ch = 0; ch < 8; ++ch) yv[ch] = ld16s(c.rs, de * 8192 + t0 * 2, R_YT + pg + crow(ch));
#pragma unroll
    for (int i = 0; i < 8; ++i) {
      float o[8];
#pragma unroll
      for (int ch = 0; ch < 8; ++ch) o[ch] = ((ch & 1) ? bhi(qr[i][ch >> 1]) : blo(qr[i][ch >> 1])) * ((i & 1) ? bhi(yv[ch][i >> 1]) : blo(yv[ch][i >> 1]));
      __builtin_amdgcn_raw_buffer_store_b128(v4u{pk(o[0], o[1]), pk(o[2], o[3]), pk(o[4], o[5]), pk(o[6], o[7])}, ro, (t0 + i) * 4096 + (1024 + de) * 2, 0, OUT_AUX2);
    }
  }
  if (c.budget == 0u) __hip_atomic_store(c.ctrl + C_TMO / 4, 1u, RLX_AGENT);
}

extern "C" __global__ void __launch_bounds__(256, 1) mk_mega(Params p) {
  extern __shared__ __attribute__((aligned(16))) unsigned char lds_raw[];
  Ctx c;
  c.ctrl = (gu32*)p.ctrl;
  c.rs = mkrs(p.data, R_END);
  c.L = (lds_t)lds_raw;
  c.w = __builtin_amdgcn_readfirstlane(threadIdx.x >> 6);
  c.lane = threadIdx.x & 63;
  c.t0 = (unsigned)__builtin_amdgcn_s_memrealtime();
  c.b = (int)(blockIdx.x & 255u);
  c.att = (int)(blockIdx.x >> 8);
  c.ask = 0;
  c.slot = (c.att ? (c.b & 7) : (int)((unsigned)__builtin_amdgcn_s_getreg(20 | (3 << 11)) & 7u)) * 32 + (c.b >> 3);
  const int x = c.slot >> 5, t = c.slot & 31;
  if (c.w == 1) {
    asm volatile("s_getpc_b64 s[60:61]\n1:\n\ts_sub_u32 s60, s60, 1b-mk_mega\n\ts_subb_u32 s61, s61, 0\n\ts_mov_b32 s62, mk_tail-mk_mega\n\ts_mov_b32 s63, 0x20000\n\t"
                 "buffer_load_dword v255, %0, s[60:63], 0 offen\n\ts_waitcnt vmcnt(0)" :: "v"((c.lane * 32 + t) * 128) : "s60", "s61", "s62", "s63", "v255", "scc", "memory");
  }
  if (c.b == SERVER && c.w == 3) serve(c);
  if (x < 4) {
    if (t < 8) qk_role(p, c, x * 8 + t);
    else if (t < 16) vo_role(p, c, x * 8 + t - 8);
    else y_role(p, c, x * 16 + t - 16);
  } else {
    if (t < 24) g_role(p, c, t >> 3, (x - 4) * 8 + (t & 7), t);
    else zc_role(p, c, (x - 4) * 8 + t - 24);
    STAMP(2);
    c.budget = SPIN_CAP;
    conv_role(p, c, (x - 4) * 32 + t, vote_a(wait_verdict(c)) ^ 1);
  }
  STAMP(3);
}

}

extern "C" void kernel_launch(void* const* d_in, const int* in_sizes, int n_in, void* d_out, int out_size, void* d_ws, size_t ws_size,
                              hipStream_t stream) {
  using namespace mk;
  (void)in_sizes; (void)n_in; (void)out_size;
  Params p;
  p.x = (const u16*)d_in[0]; p.mem0 = (const u16*)d_in[1];
  p.Wq = (const u16*)d_in[2]; p.Wk = (const u16*)d_in[3]; p.Wv = (const u16*)d_in[4]; p.Wo = (const u16*)d_in[5];
  p.bq = (const u16*)d_in[7]; p.bk = (const u16*)d_in[8]; p.bv = (const u16*)d_in[9];
  p.shift = (const u16*)d_in[10]; p.ssmk = (const u16*)d_in[11]; p.ssmD = (const u16*)d_in[12];
  p.out = (u16*)d_out;
  p.ctrl = (unsigned char*)d_ws;
  p.data = data_base(d_ws, ws_size);
  (void)hipFuncSetAttribute((const void*)mk_mega, hipFuncAttributeMaxDynamicSharedMemorySize, LDS_BYTES);
  hipLaunchKernelGGL(mk_mega, dim3(256), dim3(256), LDS_BYTES, stream, p);
}
```

```cpp
#include <hip/hip_runtime.h>
#include <hip/hip_bf16.h>

namespace mk {

#define GAS __attribute__((address_space(1)))
#define LAS __attribute__((address_space(3)))
#define DEV __device__ __forceinline__
#define RLX_AGENT __ATOMIC_RELAXED, __HIP_MEMORY_SCOPE_AGENT
typedef unsigned short u16;
typedef unsigned v2u __attribute__((ext_vector_type(2)));
typedef unsigned v4u __attribute__((ext_vector_type(4)));
typedef unsigned v8u __attribute__((ext_vector_type(8)));
typedef unsigned v16u __attribute__((ext_vector_type(16)));
typedef unsigned v32u __attribute__((ext_vector_type(32)));
typedef float f32x2 __attribute__((ext_vector_type(2)));
typedef float f32x4 __attribute__((ext_vector_type(4)));
typedef float f32x16 __attribute__((ext_vector_type(16)));
typedef __bf16 bf16x2 __attribute__((ext_vector_type(2)));
typedef __bf16 bf16x8 __attribute__((ext_vector_type(8)));
typedef __bf16 bf16x16 __attribute__((ext_vector_type(16)));
typedef GAS unsigned gu32;
typedef __amdgpu_buffer_rsrc_t rsrc_t;
typedef LAS unsigned char* lds_t;

#define XCNT(r) ""
#define UCNT ""
#define CL_T
#define XT(a, b) ""
#define LMARK ""
#define GSUM ""
#define ZSUM ""
#define GTRACE
#define MIXED 1
#define NOFLAG 0
#define TOK_KICK 1
#define ZRING 16
#define Z_AT 16
#define Z_GAP 4
#define STR_(x) #x
#define STR(x) STR_(x)
#define GQ_AT 600
#define GK_AT 400
#define PULL_AT 800
#define QK_AT 400
#define SERVER 194
#define SLOW_LEAD 100
#define PULL_DEPTH 1
#define PRIME 1
#define X_AT 900
#define VO_AT 100
#define ZC_AT 0
#define CONV_AUX 2
#define W_AUX 0
#define ZC_PRIME 2
#define CODE_PULL 1
#define PROBES 2
#define Z_WIN 20
#define TOK_WAIT 0
#define Z_AUX 0
#define OUT_AUX 2
#define OUT_AUX2 2
#define URING 8
#define YH_AT 400
#define G_DEEP 1
#define AGAIN 0
#define PRE 0
#define YH 1
#define HELP_O 1
#define HELP_V 1
#define SLOW_V 1
#define K_FIRST 1
#define FLIP 1
#define YH_STEP 300
#define Z_WAIT 0
#define HELPERS 2
constexpr int NSEG = 64;
constexpr unsigned SENT = 0xFFFFFFFFu;
constexpr int AUX_SC1 = 16, AUX_NT = 2;
constexpr int T_LAG = 61;
constexpr int G_AHEAD = 4;
constexpr unsigned SPIN_CAP = 1u << 21;
constexpr int LDS_BYTES = 163840;

constexpr int C_ECHO = 0, C_CLAIM = 1024, C_VERDICT = 3072, C_TMO = 3328, C_CONV = 3456, C_VOTE = 3840, C_FLAG = 4096, C_GRP = C_FLAG + 256 * 128, C_ASK = C_GRP + 32 * 128;
constexpr int F_Q = 0, F_K = 32, F_V = 64, F_S = 96, F_Z = 128;
constexpr int CTRL_BYTES = C_ASK + 256 * 64;
constexpr int MB = 1 << 20;
constexpr int R_U = 136 * MB;
constexpr int R_MEM = 2 * MB, MEM_SLOT = 32768;
constexpr int R_P = R_MEM + 4 * MEM_SLOT, P_SLOT = 65536;
constexpr int R_PT = 4 * MB, PT_SLOT = 131072;
constexpr int R_QX = 8 * MB, R_KX = 40 * MB, R_Z = 72 * MB, Z_SLOT = 4 * MB, R_KVT = R_KX, R_YT = R_KX + 8 * MB, R_END = (136 + URING) * MB;

constexpr size_t R_DIAG = 0;
#define TOP 1
static inline unsigned char* data_base(void* ws, size_t size) {
  return (unsigned char*)((TOP ? (size_t)ws + size - R_END - R_DIAG : (size_t)ws + CTRL_BYTES + 2 * MB - 1) & ~(size_t)(2 * MB - 1));
}
struct Params {
  const u16 *x, *mem0, *Wq, *Wk, *Wv, *Wo, *bq, *bk, *bv, *shift, *ssmk, *ssmD;
  u16* out;
  unsigned char* ctrl;
  unsigned char* data;
};

DEV rsrc_t mkrs(const void* p, unsigned bytes) { return __builtin_amdgcn_make_buffer_rsrc((void*)p, 0, bytes, 0x00020000); }
DEV v4u ld16s(rsrc_t rs, int v, int s) { return __builtin_amdgcn_raw_buffer_load_b128(rs, v, s, AUX_SC1); }
DEV v2u ld8s(rsrc_t rs, int v, int s) { return __builtin_amdgcn_raw_buffer_load_b64(rs, v, s, AUX_SC1); }
DEV v4u ld16(rsrc_t rs, int v, int s) { return __builtin_amdgcn_raw_buffer_load_b128(rs, v, s, 0); }
DEV v4u ld16z(rsrc_t rs, int v, int s) { return __builtin_amdgcn_raw_buffer_load_b128(rs, v, s, AUX_SC1 | Z_AUX); }
DEV v4u ld16c(rsrc_t rs, int v, int s) { return __builtin_amdgcn_raw_buffer_load_b128(rs, v, s, CONV_AUX); }
template <int A> DEV v4u ld16w(rsrc_t rs, int v, int s) { return __builtin_amdgcn_raw_buffer_load_b128(rs, v, s, A); }
DEV v2u ld8(rsrc_t rs, int v, int s) { return __builtin_amdgcn_raw_buffer_load_b64(rs, v, s, 0); }
DEV void dma16(rsrc_t rs, lds_t l, int v) {
#if __HIP_DEVICE_COMPILE__
  __builtin_amdgcn_raw_ptr_buffer_load_lds(rs, (LAS void*)l, 16, v, 0, 0, 0);
#endif
}
DEV void st16s(rsrc_t rs, int v, int s, v4u d) { __builtin_amdgcn_raw_buffer_store_b128(d, rs, v, s, AUX_SC1); }
DEV void st8s(rsrc_t rs, int v, int s, v2u d) { __builtin_amdgcn_raw_buffer_store_b64(d, rs, v, s, AUX_SC1); }
DEV void st4s(rsrc_t rs, int v, int s, unsigned d) { __builtin_amdgcn_raw_buffer_store_b32(d, rs, v, s, AUX_SC1); }
DEV int amap(int o, int par) { return ((o >> 13) << 14) | (par << 13) | (o & 8191); }
DEV int xblk(int s, int h, int par) { return (s * 32 + h) * 16384 + ((par ^ (s >> 2)) & 1) * 8192; }
DEV unsigned umax(unsigned a, unsigned b) { return a > b ? a : b; }
DEV unsigned top(v4u v) { return umax(umax(v.x, v.y), umax(v.z, v.w)); }
DEV unsigned pk(float a, float b) { bf16x2 v = {(__bf16)a, (__bf16)b}; return __builtin_bit_cast(unsigned, v); }
DEV unsigned pks(float a, float b) { unsigned u = pk(a, b); return u < SENT - 1u ? u : SENT - 1u; }
DEV float blo(unsigned u) { return __builtin_bit_cast(float, u << 16); }
DEV float bhi(unsigned u) { return __builtin_bit_cast(float, u & 0xFFFF0000u); }
DEV float rb(float a) { return blo(pk(a, a)); }
DEV unsigned fu(float f) { return __builtin_bit_cast(unsigned, f); }
DEV float uf(unsigned u) { return __builtin_bit_cast(float, u); }
DEV f32x4 mfma16(v4u a, v4u b, f32x4 c) {
  return __builtin_amdgcn_mfma_f32_16x16x32_bf16(__builtin_bit_cast(bf16x8, a), __builtin_bit_cast(bf16x8, b), c, 0, 0, 0);
}
DEV f32x16 mfma32(v4u a, v4u b, f32x16 c) {
  return __builtin_amdgcn_mfma_f32_32x32x16_bf16(__builtin_bit_cast(bf16x8, a), __builtin_bit_cast(bf16x8, b), c, 0, 0, 0);
}
DEV f32x4 smf(v4u a, v8u b, f32x4 c, int idx) {
  return __builtin_amdgcn_smfmac_f32_16x16x64_bf16(__builtin_bit_cast(bf16x8, a), __builtin_bit_cast(bf16x16, b), c, idx, 0, 0);
}
DEV v8u cat(v4u a, v4u b) { return __builtin_shufflevector(a, b, 0, 1, 2, 3, 4, 5, 6, 7); }
DEV v4u lo4(v8u a) { return __builtin_shufflevector(a, a, 0, 1, 2, 3); }
DEV v4u hi4(v8u a) { return __builtin_shufflevector(a, a, 4, 5, 6, 7); }
DEV v4u lds16(lds_t L, int o) { return *(LAS v4u*)(L + o); }
DEV void sts16(lds_t L, int o, v4u v) { *(LAS v4u*)(L + o) = v; }
DEV void sts8(lds_t L, int o, v2u v) { *(LAS v2u*)(L + o) = v; }
#define SCHED() __builtin_amdgcn_sched_barrier(0)
#define PIN_A(x) asm volatile("" : "+a"(x))
#define PIN_V(x) asm volatile("" : "+v"(x))
DEV int opq(int v) { asm volatile("" : "+v"(v)); return v; }
DEV int fresh_lane() {
  int l; asm volatile("v_mbcnt_lo_u32_b32 %0, -1, 0\n\tv_mbcnt_hi_u32_b32 %0, -1, %0" : "=v"(l)); return l;
}
DEV f32x4 fresh_zero() {
  f32x2 a, b;
  asm volatile("v_mov_b64 %0, 0" : "=v"(a));
  asm volatile("v_mov_b64 %0, 0" : "=v"(b));
  return __builtin_shufflevector(a, b, 0, 1, 2, 3);
}
DEV unsigned fresh_sent() { unsigned z; asm volatile("v_mov_b32 %0, -1" : "=v"(z)); return z; }
#define SMF "v_smfmac_f32_16x16x64_bf16 "
#define MF16 "v_mfma_f32_16x16x32_bf16 "
#define SMF1(cls, d, a, b) asm volatile(SMF "%0, %1, %2, %3" : "+v"(d) : "v"(a), cls(b), "v"(idx))
#define SMF1F(cls, lo, hi, d, a, b) asm volatile(SMF "v[" #lo ":" #hi "], %1, %2, %3" : "+{v[" #lo ":" #hi "]}"(d) : "v"(a), cls(b), "v"(idx))
#define AT_HOME(M, j) do { const int j_ = (j); if (j_ == 0) M("{v[32:39]}"); if (j_ == 1) M("{v[40:47]}"); if (j_ == 2) M("{v[48:55]}"); if (j_ == 3) M("{v[56:63]}"); if (j_ == 4) M("{v[64:71]}"); if (j_ == 5) M("{v[72:79]}"); if (j_ == 6) M("{v[80:87]}"); if (j_ == 7) M("{v[88:95]}"); if (j_ == 8) M("{v[96:103]}"); if (j_ == 9) M("{v[104:111]}"); if (j_ == 10) M("{v[112:119]}"); if (j_ == 11) M("{v[120:127]}"); if (j_ == 12) M("{v[128:135]}"); if (j_ == 13) M("{v[136:143]}"); if (j_ == 14) M("{v[144:151]}"); if (j_ == 15) M("{v[152:159]}"); } while (0)
#define MF1(d, a, b) asm volatile(MF16 "%0, %1, %2, %0" : "+v"(d) : "v"(a), "v"(b))
#define MF1Z(d, a, b) asm volatile(MF16 "%0, %1, %2, 0" : "=&v"(d) : "v"(a), "v"(b))
#define LGKM(n) asm volatile("s_waitcnt lgkmcnt(" #n ")" ::: "memory")
#define RAWBAR() do { __builtin_amdgcn_s_barrier(); asm volatile("" ::: "memory"); } while (0)
DEV void settle3(f32x4& s0, f32x4& s1, f32x4& s2) { asm volatile("s_nop 7" : "+v"(s0), "+v"(s1), "+v"(s2)); }
DEV void settle4(f32x4 (&d)[4]) { asm volatile("s_nop 7" : "+v"(d[0]), "+v"(d[1]), "+v"(d[2]), "+v"(d[3])); }
#define BAR() do { asm volatile("s_waitcnt lgkmcnt(0)" ::: "memory"); __builtin_amdgcn_s_barrier(); asm volatile("" ::: "memory"); } while (0)
#define VMWAIT(n) asm volatile("s_waitcnt vmcnt(" #n ")" ::: "memory")
DEV float add1(float a, float b) {
  float o; asm("v_add_f32 %0, %1, %2" : "=v"(o) : "v"(a), "v"(b)); return o;
}
DEV float xmax(float v) {
  auto r = __builtin_amdgcn_permlane32_swap(fu(v), fu(v), false, false); v = fmaxf(uf(r[0]), uf(r[1]));
  auto q = __builtin_amdgcn_permlane16_swap(fu(v), fu(v), false, false); return fmaxf(uf(q[0]), uf(q[1]));
}
DEV float xsum(float v) {
  auto r = __builtin_amdgcn_permlane32_swap(fu(v), fu(v), false, false); v = uf(r[0]) + uf(r[1]);
  auto q = __builtin_amdgcn_permlane16_swap(fu(v), fu(v), false, false); return uf(q[0]) + uf(q[1]);
}

DEV float ror8(float v) { return uf(__builtin_amdgcn_update_dpp(0u, fu(v), 0x128, 0xf, 0xf, true)); }
DEV float fmax2(float a, float b) { return __builtin_amdgcn_fmed3f(a, b, __builtin_inff()); }
DEV float fmax3(float a, float b, float c) { float r; asm("v_max3_f32 %0, %1, %2, %3" : "=v"(r) : "v"(a), "v"(b), "v"(c)); return r; }
DEV bool allok(unsigned t) { return __all(t != SENT); }

template <int N> struct IC { static constexpr int value = N; };
template <int N, class F> DEV void sfor(F&& f) { if constexpr (N > 0) { sfor<N - 1>(f); f(IC<N - 1>{}); } }
struct Ctx {
  gu32* ctrl;
  rsrc_t rs;
  lds_t L;
  int w, lane;
  int b, slot, att;
  unsigned ask;
  unsigned t0;
  unsigned budget;
};
DEV bool spend(Ctx& c) {
  const unsigned old = c.budget;
  asm volatile("s_cmp_lg_u32 %0, 0\n\ts_subb_u32 %0, %0, 0" : "+s"(c.budget) :: "scc");
  return old == 0u;
}
DEV unsigned wait_flag(Ctx& c, int flag, unsigned need) {
  gu32* f = c.ctrl + (C_FLAG + flag * 128) / 4;
  unsigned v;
#pragma unroll 1
  for (;;) {
    v = __builtin_amdgcn_readfirstlane(__hip_atomic_load(f, RLX_AGENT));
    if (v >= need || spend(c)) break;
    __builtin_amdgcn_s_sleep(2);
  }
  __builtin_amdgcn_fence(__ATOMIC_ACQUIRE, "wavefront");
  return v;
}
DEV void post_flag(Ctx& c, int flag, unsigned v, int off = 0) {
  st4s(mkrs((const void*)c.ctrl, CTRL_BYTES), (c.w | off | fresh_lane()) ? 1 << 30 : 0, C_FLAG + flag * 128, v);
}
constexpr unsigned CL_A = 1u << 16, CL_VOTED = 1u << 24, CL_FILLED = 1u << 28;
DEV unsigned claim_look(Ctx& c) { return __hip_atomic_load(c.ctrl + C_ECHO / 4 + c.b, RLX_AGENT); }
DEV void ask(Ctx& c, unsigned first) {
  c.ask = __builtin_amdgcn_readfirstlane(first) + 1u;
  st4s(mkrs((const void*)c.ctrl, CTRL_BYTES), (c.w | c.att | fresh_lane()) ? 1 << 30 : 0, C_ASK + c.b * 64, c.ask);
}
DEV void claim(Ctx& c, unsigned votes) {
  if (c.w != 0) return;
  if (c.att == 0) {
#pragma unroll 1
    while (__builtin_amdgcn_readfirstlane(__hip_atomic_load(c.ctrl + C_ECHO / 4 + c.b, RLX_AGENT)) != c.ask) {
      if (spend(c)) break;
      __builtin_amdgcn_s_sleep(2);
    }
  }
  if (fresh_lane() == 0) __hip_atomic_fetch_add(c.ctrl + (C_CLAIM + c.att * 1024) / 4 + c.slot, 1u + votes, RLX_AGENT);
}
DEV bool claim_try(Ctx& c, unsigned look) {
  const bool ok = c.att || __builtin_amdgcn_readfirstlane(look) == c.ask;
  if (ok && c.w == 0 && fresh_lane() == 0) __hip_atomic_fetch_add(c.ctrl + (C_CLAIM + c.att * 1024) / 4 + c.slot, 1u, RLX_AGENT);
  return ok;
}
DEV unsigned wait_verdict(Ctx& c) {
  unsigned v;
#pragma unroll 1
  for (;;) {
    v = __builtin_amdgcn_readfirstlane(__hip_atomic_load(c.ctrl + (C_VERDICT + c.att * 128) / 4, RLX_AGENT));
    if ((v & 1u) || spend(c)) break;
    __builtin_amdgcn_s_sleep(2);
  }
  __builtin_amdgcn_fence(__ATOMIC_ACQUIRE, "wavefront");
  if ((v & 3u) == 3u && c.att == 0) {
    asm volatile("s_waitcnt vmcnt(0) lgkmcnt(0)\n\ts_barrier\n\ts_mov_b64 s[0:1], %0\n\ts_mov_b32 s2, %1\n\tv_mov_b32 v0, %2\n\t"
                 "s_getpc_b64 s[4:5]\n1:\n\ts_sub_u32 s4, s4, 1b-mk_mega\n\ts_subb_u32 s5, s5, 0\n\ts_setpc_b64 s[4:5]"
                 :: "s"(__builtin_amdgcn_kernarg_segment_ptr()), "s"(c.b + 256), "v"(c.w * 64 + fresh_lane()) : "s0", "s1", "s2", "s4", "s5", "v0", "scc", "memory");
    __builtin_unreachable();
  }
  return v;
}
DEV unsigned wait_vote(Ctx& c) {
  unsigned v;
#pragma unroll 1
  for (;;) {
    v = __builtin_amdgcn_readfirstlane(__hip_atomic_load(c.ctrl + C_VOTE / 4, RLX_AGENT));
    if ((v & 1u) || spend(c)) break;
    __builtin_amdgcn_s_sleep(2);
  }
  return v;
}
DEV void serve(Ctx& c) {
  c.budget = SPIN_CAP;
  const int lane = fresh_lane();
  const rsrc_t rc = mkrs((const void*)c.ctrl, CTRL_BYTES);
  if (c.att == 0) {
#pragma unroll
    for (int i = C_CLAIM / 1024; i < C_ASK / 1024; ++i) st16s(rc, lane * 16, i * 1024, v4u{0, 0, 0, 0});
    VMWAIT(0);
  }
  unsigned vote = c.att ? wait_vote(c) : 0u;
  bool echo = c.att == 0, first = true;
  v4u last = {0u, 0u, 0u, 0u};
#pragma unroll 1
  for (;;) {
    asm volatile("" ::: "memory");
    const v4u cl = ld16s(rc, lane * 16, C_CLAIM + c.att * 1024);
    if (echo) {
      v4u e;
#pragma unroll
      for (int i = 0; i < 4; ++i) e[i] = __builtin_amdgcn_raw_buffer_load_b32(rc, (lane * 4 + i) * 64, C_ASK, AUX_SC1);
      if (first || __any(e[0] != last[0] || e[1] != last[1] || e[2] != last[2] || e[3] != last[3])) st16s(rc, lane * 16, C_ECHO, e);
      last = e; first = false;
    }
    unsigned n = 0, one = 1;
    int va = 0, nv = 0, nf = 0;
#pragma unroll
    for (int i = 0; i < 4; ++i) {
      n += cl[i] & 0xFFFFu; one &= (cl[i] & 0xFFFFu) == 1u;
      va += __popcll(__ballot(cl[i] & CL_A)); nv += __popcll(__ballot(cl[i] & CL_VOTED)); nf += __popcll(__ballot(cl[i] & CL_FILLED));
    }
#pragma unroll
    for (int o = 1; o < 64; o <<= 1) n += (unsigned)__shfl_xor((int)n, o);
    const bool all = __builtin_amdgcn_readfirstlane(n) >= 256u, good = __all(one);
    echo = echo && !all;
    if (!vote && (all || max(va, nv - va) > 20)) {
      vote = 2 * va > nv ? 5u : 9u;
      if (lane == 0) __hip_atomic_store(c.ctrl + C_VOTE / 4, vote, RLX_AGENT);
    }
    if (all && (!good || nf >= 64)) {
      if (lane == 0) __hip_atomic_store(c.ctrl + (C_VERDICT + c.att * 128) / 4, vote | (good ? 0u : 2u), RLX_AGENT);
      break;
    }
    if (spend(c)) break;
  }
}
DEV int vote_a(unsigned v) { return (int)((v >> 2) & 1u); }
DEV int vote_b(unsigned v) { return (int)((v >> 3) & 1u); }

#define STAMP(slot) do {} while (0)
#define TICK_DECL
#define LOG(i, s, v) do {} while (0)
#define LOGS(i, s) do {} while (0)
#define CST(i) do {} while (0)
#define LITE(s) do {} while (0)
#define TICK(i) do {} while (0)
#define TICK_SUM(n) do {} while (0)
#define TICK_LDS(b) do {} while (0)
#define TICK_DUMP(b) do {} while (0)
#define TICK_OUT() do {} while (0)

DEV constexpr int krow(int rho8, int i) { return 32 * (rho8 >> 2) + 8 * (rho8 & 1) + 2 * ((rho8 >> 1) & 1) + i; }
DEV int krow_lane(int g) { return 16 * (g >> 1) + 4 * (g & 1); }
DEV unsigned prm(unsigned hi, unsigned lo, int odd) { return __builtin_amdgcn_perm(hi, lo, odd ? 0x07060302u : 0x05040100u); }

#define AT_ACC(M, n) do { const int n_ = (n); if (n_ == 0) M("{a[0:7]}"); if (n_ == 1) M("{a[8:15]}"); if (n_ == 2) M("{a[16:23]}"); if (n_ == 3) M("{a[24:31]}"); if (n_ == 4) M("{a[32:39]}"); if (n_ == 5) M("{a[40:47]}"); if (n_ == 6) M("{a[48:55]}"); if (n_ == 7) M("{a[56:63]}"); if (n_ == 8) M("{a[64:71]}"); if (n_ == 9) M("{a[72:79]}"); if (n_ == 10) M("{a[80:87]}"); if (n_ == 11) M("{a[88:95]}"); if (n_ == 12) M("{a[96:103]}"); if (n_ == 13) M("{a[104:111]}"); if (n_ == 14) M("{a[112:119]}"); if (n_ == 15) M("{a[120:127]}"); if (n_ == 16) M("{a[128:135]}"); if (n_ == 17) M("{a[136:143]}"); if (n_ == 18) M("{a[144:151]}"); if (n_ == 19) M("{a[152:159]}"); if (n_ == 20) M("{a[160:167]}"); if (n_ == 21) M("{a[168:175]}"); if (n_ == 22) M("{a[176:183]}"); if (n_ == 23) M("{a[184:191]}"); if (n_ == 24) M("{a[192:199]}"); if (n_ == 25) M("{a[200:207]}"); if (n_ == 26) M("{a[208:215]}"); if (n_ == 27) M("{a[216:223]}"); if (n_ == 28) M("{a[224:231]}"); if (n_ == 29) M("{a[232:239]}"); if (n_ == 30) M("{a[240:247]}"); if (n_ == 31) M("{a[248:255]}"); } while (0)
#define PIN_ACC0(x) asm volatile("" :: "{a[0:7]}"(x[0]), "{a[8:15]}"(x[1]), "{a[16:23]}"(x[2]), "{a[24:31]}"(x[3]), "{a[32:39]}"(x[4]), "{a[40:47]}"(x[5]), "{a[48:55]}"(x[6]), "{a[56:63]}"(x[7]), "{a[64:71]}"(x[8]), "{a[72:79]}"(x[9]), "{a[80:87]}"(x[10]), "{a[88:95]}"(x[11]), "{a[96:103]}"(x[12]), "{a[104:111]}"(x[13]), "{a[112:119]}"(x[14]), "{a[120:127]}"(x[15]))
#define PIN_ACC1(x) asm volatile("" :: "{a[128:135]}"(x[16]), "{a[136:143]}"(x[17]), "{a[144:151]}"(x[18]), "{a[152:159]}"(x[19]), "{a[160:167]}"(x[20]), "{a[168:175]}"(x[21]), "{a[176:183]}"(x[22]), "{a[184:191]}"(x[23]), "{a[192:199]}"(x[24]), "{a[200:207]}"(x[25]), "{a[208:215]}"(x[26]), "{a[216:223]}"(x[27]), "{a[224:231]}"(x[28]), "{a[232:239]}"(x[29]), "{a[240:247]}"(x[30]), "{a[248:255]}"(x[31]))
#define PIN_HOME(x) asm volatile("" :: "{v[32:39]}"(x[0]), "{v[40:47]}"(x[1]), "{v[48:55]}"(x[2]), "{v[56:63]}"(x[3]), "{v[64:71]}"(x[4]), "{v[72:79]}"(x[5]), "{v[80:87]}"(x[6]), "{v[88:95]}"(x[7]), "{v[96:103]}"(x[8]), "{v[104:111]}"(x[9]), "{v[112:119]}"(x[10]), "{v[120:127]}"(x[11]), "{v[128:135]}"(x[12]), "{v[136:143]}"(x[13]), "{v[144:151]}"(x[14]), "{v[152:159]}"(x[15]))
#define CL_32_100 "v32", "v33", "v34", "v35", "v36", "v37", "v38", "v39", "v40", "v41", "v42", "v43", "v44", "v45", "v46", "v47", "v48", "v49", "v50", "v51", "v52", "v53", "v54", "v55", "v56", "v57", "v58", "v59", "v60", "v61", "v62", "v63", "v64", "v65", "v66", "v67", "v68", "v69", "v70", "v71", "v72", "v73", "v74", "v75", "v76", "v77", "v78", "v79", "v80", "v81", "v82", "v83", "v84", "v85", "v86", "v87", "v88", "v89", "v90", "v91", "v92", "v93", "v94", "v95", "v96", "v97", "v98", "v99", "v100", "v101", "v102", "v103", "v104", "v105", "v106", "v107", "v108", "v109", "v110", "v111", "v112", "v113", "v114", "v115", "v116", "v117", "v118", "v119", "v120", "v121", "v122", "v123", "v124", "v125", "v126", "v127", "v128", "v129", "v130", "v131"
#define ACC_IN(x) "{a[0:31]}"(x[0]), "{a[32:63]}"(x[1]), "{a[64:95]}"(x[2]), "{a[96:127]}"(x[3]), "{a[128:159]}"(x[4]), "{a[160:191]}"(x[5]), "{a[192:223]}"(x[6]), "{a[224:255]}"(x[7])
#define CL_16_32 "v16", "v17", "v18", "v19", "v20", "v21", "v22", "v23", "v24", "v25", "v26", "v27", "v28", "v29", "v30", "v31"
#define CL_120_128 "v120", "v121", "v122", "v123", "v124", "v125", "v126", "v127", "v128", "v129", "v130", "v131", "v132", "v133", "v134", "v135", "v136", "v137", "v138", "v139"
#define CL_160_200 "v160", "v161", "v162", "v163", "v164", "v165", "v166", "v167", "v168", "v169", "v170", "v171", "v172", "v173", "v174", "v175", "v176", "v177", "v178", "v179", "v180", "v181", "v182", "v183", "v184", "v185", "v186", "v187", "v188", "v189", "v190", "v191", "v192", "v193", "v194", "v195", "v196", "v197", "v198", "v199"
#define CL_176_200 "v176", "v177", "v178", "v179", "v180", "v181", "v182", "v183", "v184", "v185", "v186", "v187", "v188", "v189", "v190", "v191", "v192", "v193", "v194", "v195", "v196", "v197", "v198", "v199"
#define CL_200_216 "v200", "v201", "v202", "v203", "v204", "v205", "v206", "v207", "v208", "v209", "v210", "v211", "v212", "v213", "v214", "v215"
#define CL_216_256 "v216", "v217", "v218", "v219", "v220", "v221", "v222", "v223", "v224", "v225", "v226", "v227", "v228", "v229", "v230", "v231", "v232", "v233", "v234", "v235", "v236", "v237", "v238", "v239", "v240", "v241", "v242", "v243", "v244", "v245", "v246", "v247", "v248", "v249", "v250", "v251", "v252", "v253", "v254", "v255"
#define CL_S "memory", "vcc", "scc", "s60", "s61", "s62", "s63", "s64", "s65", "s66", "s67", "s68" CL_T
#define TIMED_WAIT \
    "s_getpc_b64 s[62:63]\n\t" "1:\n\t" "s_memtime s[64:65]\n\t" "s_waitcnt lgkmcnt(0)\n\t" "s_sub_u32 s61, %0, s64\n\t" "s_cmp_gt_i32 s61, 124\n\t" "s_cbranch_scc1 1b\n\t" \
    "s_max_i32 s61, s61, 0\n\t" "s_and_b32 s61, s61, 124\n\t" "s_sub_u32 s61, 2f-1b, s61\n\t" "s_add_u32 s62, s62, s61\n\t" "s_addc_u32 s63, s63, 0\n\t" \
    "s_setpc_b64 s[62:63]\n\t" ".rept 31\n\t" "s_nop 0\n\t" ".endr\n\t" "2:\n\t"
#define QK_STEP0 \
    ".p2align 6\n\t" "v_xor_b32 v216, 0x400, %[mo]\n\t" "s_xor_b32 s60, %[sb], 0x800\n\t" "s_mov_b32 s66, 0x3e0293ee\n\t" "s_mov_b32 s67, 0x3e0293ee\n\t" XT(80, 81) \
    "s_add_u32 s61, %[kb], 0x1000\n\t" "buffer_load_dwordx4 v[16:19], %[kx], %[rs], %[kb] offen offset:0 sc1\n\t" \
    "buffer_load_dwordx4 v[20:23], %[kx], %[rs], %[kb] offen offset:2048 sc1\n\t" "buffer_load_dwordx4 v[24:27], %[kx], %[rs], s61 offen offset:0 sc1\n\t" \
    "buffer_load_dwordx4 v[28:31], %[kx], %[rs], s61 offen offset:2048 sc1\n\t" "buffer_load_dwordx4 v[200:203], %[kx], %[rs], %[kb] offen offset:256 sc1\n\t" \
    "buffer_load_dwordx4 v[204:207], %[kx], %[rs], %[kb] offen offset:2304 sc1\n\t" "buffer_load_dwordx4 v[208:211], %[kx], %[rs], s61 offen offset:256 sc1\n\t" \
    "buffer_load_dwordx4 v[212:215], %[kx], %[rs], s61 offen offset:2304 sc1\n\t" "s_barrier\n\t" "s_waitcnt vmcnt(7)\n\t" "ds_write_b128 %[kx], v[16:19] offset:0\n\t" \
    "s_waitcnt vmcnt(6)\n\t" "ds_write_b128 %[kx], v[20:23] offset:2048\n\t" "s_waitcnt vmcnt(5)\n\t" "ds_write_b128 %[kx], v[24:27] offset:4096\n\t" "s_waitcnt vmcnt(4)\n\t" \
    "ds_write_b128 %[kx], v[28:31] offset:6144\n\t" "s_waitcnt vmcnt(3)\n\t" "ds_write_b128 %[kx], v[200:203] offset:256\n\t" "s_waitcnt vmcnt(2)\n\t" \
    "ds_write_b128 %[kx], v[204:207] offset:2304\n\t" "s_waitcnt vmcnt(1)\n\t" "ds_write_b128 %[kx], v[208:211] offset:4352\n\t" "s_waitcnt vmcnt(0)\n\t" \
    "ds_write_b128 %[kx], v[212:215] offset:6400\n\t" "s_waitcnt lgkmcnt(0)\n\t" "v_mov_b64 v[208:209], 0\n\t" "v_mov_b64 v[210:211], 0\n\t" "v_mov_b64 v[212:213], 0\n\t" \
    "v_mov_b64 v[214:215], 0\n\t" XT(98, 99) "s_barrier\n\t" XT(82, 83) "buffer_load_dwordx4 v[240:243], %[mo], %[rs], %[sb] offen sc1\n\t" "v_mov_b64 v[224:225], 0\n\t" \
    "v_mov_b64 v[226:227], 0\n\t" "v_mov_b64 v[228:229], 0\n\t" "v_mov_b64 v[230:231], 0\n\t" "v_mov_b64 v[232:233], 0\n\t" "v_mov_b64 v[234:235], 0\n\t" \
    "v_mov_b64 v[236:237], 0\n\t" "v_mov_b64 v[238:239], 0\n\t" "buffer_load_dwordx4 v[244:247], v216, %[rs], %[sb] offen sc1\n\t" \
    "ds_read_b128 v[160:163], %[l0] offset:0\n\t" "ds_read_b128 v[164:167], %[l0] offset:1024\n\t" "ds_read_b128 v[168:171], %[l0] offset:2048\n\t" \
    "ds_read_b128 v[172:175], %[l0] offset:3072\n\t" "s_nop 3\n\t" "buffer_load_dwordx4 v[248:251], %[mo], %[rs], s60 offen sc1\n\t" \
    "ds_read_b128 v[176:179], %[l0] offset:4096\n\t" "ds_read_b128 v[180:183], %[l0] offset:5120\n\t" "ds_read_b128 v[184:187], %[l0] offset:6144\n\t" \
    "ds_read_b128 v[188:191], %[l0] offset:7168\n\t" "s_nop 3\n\t" "buffer_load_dwordx4 v[252:255], v216, %[rs], s60 offen sc1\n\t" "v_mov_b32 v219, 160512\n\t" \
    "v_mov_b64 v[220:221], -1\n\t" "v_mov_b64 v[222:223], -1\n\t" "v_mov_b32 v217, 0\n\t" "buffer_store_dwordx2 v[220:221], %[px], %[rs], %[pr] offen sc1\n\t" \
    "buffer_store_dword v220, %[pm], %[rs], %[pr] offen sc1\n\t" "global_load_dword v217, v217, %[fq] sc1\n\t" "10:\n\t" "s_waitcnt vmcnt(6)\n\t" XT(84, 85) \
    "v_smfmac_f32_16x16x64_bf16 v[224:227], v[240:243], a[0:7], %[ix]\n\t" "ds_write_b128 %[l1], v[240:243] offset:0\n\t" \
    "v_smfmac_f32_16x16x64_bf16 v[228:231], v[240:243], a[8:15], %[ix]\n\t" "v_smfmac_f32_16x16x64_bf16 v[232:235], v[240:243], v[32:39], %[ix]\n\t" \
    "s_waitcnt lgkmcnt(7)\n\t" "v_smfmac_f32_16x16x64_bf16 v[236:239], v[240:243], v[160:167], %[ix]\n\t" "ds_read_b128 v[160:163], %[l0] offset:8192\n\t" \
    "ds_read_b128 v[164:167], %[l0] offset:9216\n\t" "s_waitcnt vmcnt(5)\n\t" "v_smfmac_f32_16x16x64_bf16 v[224:227], v[244:247], a[16:23], %[ix]\n\t" \
    "ds_write_b128 %[l1], v[244:247] offset:1024\n\t" "v_smfmac_f32_16x16x64_bf16 v[228:231], v[244:247], a[24:31], %[ix]\n\t" \
    "v_smfmac_f32_16x16x64_bf16 v[232:235], v[244:247], v[40:47], %[ix]\n\t" "s_waitcnt lgkmcnt(8)\n\t" \
    "v_smfmac_f32_16x16x64_bf16 v[236:239], v[244:247], v[168:175], %[ix]\n\t" "ds_read_b128 v[168:171], %[l0] offset:10240\n\t" \
    "ds_read_b128 v[172:175], %[l0] offset:11264\n\t" "s_waitcnt vmcnt(4)\n\t" "v_smfmac_f32_16x16x64_bf16 v[224:227], v[248:251], a[32:39], %[ix]\n\t" \
    "ds_write_b128 %[l1], v[248:251] offset:2048\n\t" "v_smfmac_f32_16x16x64_bf16 v[228:231], v[248:251], a[40:47], %[ix]\n\t" "s_waitcnt lgkmcnt(3)\n\t" "s_barrier\n\t" \
    XT(86, 87) "v_smfmac_f32_16x16x64_bf16 v[232:235], v[248:251], v[48:55], %[ix]\n\t" "ds_read_b128 v[192:195], %[l1] offset:4096\n\t" \
    "ds_read_b128 v[196:199], %[l1] offset:8192\n\t" "v_smfmac_f32_16x16x64_bf16 v[236:239], v[248:251], v[176:183], %[ix]\n\t" \
    "ds_read_b128 v[176:179], %[l0] offset:12288\n\t" "ds_read_b128 v[180:183], %[l0] offset:13312\n\t" "s_waitcnt vmcnt(3)\n\t" \
    "v_smfmac_f32_16x16x64_bf16 v[224:227], v[252:255], a[48:55], %[ix]\n\t" "ds_write_b128 %[l1], v[252:255] offset:3072\n\t" \
    "v_smfmac_f32_16x16x64_bf16 v[228:231], v[252:255], a[56:63], %[ix]\n\t" "ds_read_b128 v[240:243], %[l1] offset:12288\n\t" \
    "ds_read_b128 v[244:247], %[l1] offset:5120\n\t" "v_smfmac_f32_16x16x64_bf16 v[232:235], v[252:255], v[56:63], %[ix]\n\t" "s_nop 1\n\t" \
    "v_smfmac_f32_16x16x64_bf16 v[236:239], v[252:255], v[184:191], %[ix]\n\t" "ds_read_b128 v[184:187], %[l0] offset:14336\n\t" "v_cmp_u_f32 vcc, v224, v225\n\t" \
    "v_cmp_u_f32 s[62:63], v226, v227\n\t" "s_waitcnt lgkmcnt(7)\n\t" "v_smfmac_f32_16x16x64_bf16 v[224:227], v[192:195], a[64:71], %[ix]\n\t" \
    "ds_read_b128 v[188:191], %[l0] offset:15360\n\t" "v_smfmac_f32_16x16x64_bf16 v[228:231], v[192:195], a[72:79], %[ix]\n\t" "s_or_b64 vcc, vcc, s[62:63]\n\t" \
    "s_cbranch_scc1 11f\n\t" "12:\n\t" "v_smfmac_f32_16x16x64_bf16 v[232:235], v[192:195], v[64:71], %[ix]\n\t" "s_waitcnt lgkmcnt(4)\n\t" "s_barrier\n\t" XT(88, 89) \
    "ds_read_b32 v218, v219\n\t" "v_smfmac_f32_16x16x64_bf16 v[236:239], v[192:195], v[160:167], %[ix]\n\t" "ds_read_b128 v[160:163], %[l0] offset:16384\n\t" \
    "ds_read_b128 v[164:167], %[l0] offset:17408\n\t" "v_smfmac_f32_16x16x64_bf16 v[224:227], v[196:199], a[80:87], %[ix]\n\t" \
    "ds_read_b128 v[248:251], %[l1] offset:9216\n\t" "ds_read_b128 v[252:255], %[l1] offset:13312\n\t" \
    "v_smfmac_f32_16x16x64_bf16 v[228:231], v[196:199], a[88:95], %[ix]\n\t" "ds_read_b128 v[192:195], %[l1] offset:6144\n\t" \
    "v_smfmac_f32_16x16x64_bf16 v[232:235], v[196:199], v[72:79], %[ix]\n\t" "v_smfmac_f32_16x16x64_bf16 v[236:239], v[196:199], v[168:175], %[ix]\n\t" \
    "s_waitcnt lgkmcnt(5)\n\t" "v_cmp_eq_u32 vcc, %[rt], v218\n\t" "v_smfmac_f32_16x16x64_bf16 v[224:227], v[240:243], a[96:103], %[ix]\n\t" "s_cbranch_vccnz 13f\n\t" \
    "ds_read_b128 v[168:171], %[l0] offset:18432\n\t" "v_smfmac_f32_16x16x64_bf16 v[228:231], v[240:243], a[104:111], %[ix]\n\t" \
    "ds_read_b128 v[172:175], %[l0] offset:19456\n\t" "ds_read_b128 v[196:199], %[l1] offset:10240\n\t" \
    "v_smfmac_f32_16x16x64_bf16 v[232:235], v[240:243], v[80:87], %[ix]\n\t" "v_smfmac_f32_16x16x64_bf16 v[236:239], v[240:243], v[176:183], %[ix]\n\t" \
    "ds_read_b128 v[176:179], %[l0] offset:20480\n\t" "ds_read_b128 v[180:183], %[l0] offset:21504\n\t" \
    "v_smfmac_f32_16x16x64_bf16 v[224:227], v[244:247], a[112:119], %[ix]\n\t" "ds_read_b128 v[240:243], %[l1] offset:14336\n\t" \
    "v_smfmac_f32_16x16x64_bf16 v[228:231], v[244:247], a[120:127], %[ix]\n\t" "buffer_load_dword %[pd], %[pa], %[rs], %[kn] offen sc1\n\t" \
    "v_smfmac_f32_16x16x64_bf16 v[232:235], v[244:247], v[88:95], %[ix]\n\t" "v_smfmac_f32_16x16x64_bf16 v[236:239], v[244:247], v[184:191], %[ix]\n\t" \
    "ds_read_b128 v[184:187], %[l0] offset:22528\n\t" "ds_read_b128 v[188:191], %[l0] offset:23552\n\t" "s_waitcnt lgkmcnt(10)\n\t" \
    "v_smfmac_f32_16x16x64_bf16 v[224:227], v[248:251], a[128:135], %[ix]\n\t" "ds_read_b128 v[244:247], %[l1] offset:7168\n\t" \
    "v_smfmac_f32_16x16x64_bf16 v[228:231], v[248:251], a[136:143], %[ix]\n\t" "v_smfmac_f32_16x16x64_bf16 v[232:235], v[248:251], v[96:103], %[ix]\n\t" \
    "v_smfmac_f32_16x16x64_bf16 v[236:239], v[248:251], v[160:167], %[ix]\n\t" "ds_read_b128 v[160:163], %[l0] offset:24576\n\t" \
    "ds_read_b128 v[164:167], %[l0] offset:25600\n\t" "s_waitcnt lgkmcnt(12)\n\t" "v_smfmac_f32_16x16x64_bf16 v[224:227], v[252:255], a[144:151], %[ix]\n\t" \
    "ds_read_b128 v[248:251], %[l1] offset:11264\n\t" "v_smfmac_f32_16x16x64_bf16 v[228:231], v[252:255], a[152:159], %[ix]\n\t" \
    "v_smfmac_f32_16x16x64_bf16 v[232:235], v[252:255], v[104:111], %[ix]\n\t" "s_waitcnt lgkmcnt(10)\n\t" \
    "v_smfmac_f32_16x16x64_bf16 v[236:239], v[252:255], v[168:175], %[ix]\n\t" "ds_read_b128 v[168:171], %[l0] offset:26624\n\t" \
    "ds_read_b128 v[172:175], %[l0] offset:27648\n\t" "v_smfmac_f32_16x16x64_bf16 v[224:227], v[192:195], a[160:167], %[ix]\n\t" \
    "ds_read_b128 v[252:255], %[l1] offset:15360\n\t" "v_smfmac_f32_16x16x64_bf16 v[228:231], v[192:195], a[168:175], %[ix]\n\t" "s_waitcnt lgkmcnt(12)\n\t" \
    "v_smfmac_f32_16x16x64_bf16 v[224:227], v[196:199], a[176:183], %[ix]\n\t" "v_smfmac_f32_16x16x64_bf16 v[228:231], v[196:199], a[184:191], %[ix]\n\t" \
    "s_waitcnt lgkmcnt(9)\n\t" "v_smfmac_f32_16x16x64_bf16 v[224:227], v[240:243], a[192:199], %[ix]\n\t" \
    "v_smfmac_f32_16x16x64_bf16 v[228:231], v[240:243], a[200:207], %[ix]\n\t" "s_waitcnt vmcnt(1)\n\t" "v_readfirstlane_b32 s62, v217\n\t" "s_waitcnt lgkmcnt(6)\n\t" \
    "v_smfmac_f32_16x16x64_bf16 v[224:227], v[244:247], a[208:215], %[ix]\n\t" "v_smfmac_f32_16x16x64_bf16 v[228:231], v[244:247], a[216:223], %[ix]\n\t" \
    "s_max_u32 %[sq], %[sq], s62\n\t" "s_waitcnt lgkmcnt(3)\n\t" "v_smfmac_f32_16x16x64_bf16 v[224:227], v[248:251], a[224:231], %[ix]\n\t" \
    "v_smfmac_f32_16x16x64_bf16 v[228:231], v[248:251], a[232:239], %[ix]\n\t" "s_waitcnt lgkmcnt(0)\n\t" \
    "v_smfmac_f32_16x16x64_bf16 v[224:227], v[252:255], a[240:247], %[ix]\n\t" "s_nop 1\n\t" "v_smfmac_f32_16x16x64_bf16 v[228:231], v[252:255], a[248:255], %[ix]\n\t" \
    XT(90, 91) "s_nop 1\n\t" "v_smfmac_f32_16x16x64_bf16 v[232:235], v[192:195], v[112:119], %[ix]\n\t" "s_nop 1\n\t" \
    "v_smfmac_f32_16x16x64_bf16 v[236:239], v[192:195], v[176:183], %[ix]\n\t" "ds_read_b128 v[176:179], %[l0] offset:28672\n\t" "v_add_f32 v224, v224, v225\n\t" \
    "v_add_f32 v226, v226, v227\n\t" "v_smfmac_f32_16x16x64_bf16 v[232:235], v[196:199], v[120:127], %[ix]\n\t" "ds_read_b128 v[180:183], %[l0] offset:29696\n\t" \
    "v_add_f32 v228, v228, v229\n\t" "v_add_f32 v230, v230, v231\n\t" "v_smfmac_f32_16x16x64_bf16 v[236:239], v[196:199], v[184:191], %[ix]\n\t" \
    "ds_read_b128 v[184:187], %[l0] offset:30720\n\t" "v_cvt_pk_bf16_f32 v218, v224, v228\n\t" "v_cvt_pk_bf16_f32 v219, v226, v230\n\t" \
    "v_smfmac_f32_16x16x64_bf16 v[232:235], v[240:243], v[128:135], %[ix]\n\t" "ds_write2_b32 %[qw], v218, v219 offset1:8\n\t" \
    "ds_read_b128 v[188:191], %[l0] offset:31744\n\t" "v_smfmac_f32_16x16x64_bf16 v[236:239], v[240:243], v[160:167], %[ix]\n\t" \
    "ds_read_b128 v[192:195], %[kx] offset:4352\n\t" "ds_read_b128 v[196:199], %[kx] offset:6400\n\t" \
    "v_smfmac_f32_16x16x64_bf16 v[232:235], v[244:247], v[136:143], %[ix]\n\t" "v_smfmac_f32_16x16x64_bf16 v[236:239], v[244:247], v[168:175], %[ix]\n\t" \
    "s_waitcnt lgkmcnt(3)\n\t" "s_barrier\n\t" "v_smfmac_f32_16x16x64_bf16 v[232:235], v[248:251], v[144:151], %[ix]\n\t" "ds_read_b128 v[216:219], %[l1] offset:24576\n\t" \
    "ds_read_b128 v[220:223], %[l1] offset:25600\n\t" "v_smfmac_f32_16x16x64_bf16 v[236:239], v[248:251], v[176:183], %[ix]\n\t" \
    "v_smfmac_f32_16x16x64_bf16 v[232:235], v[252:255], v[152:159], %[ix]\n\t" "s_waitcnt lgkmcnt(4)\n\t" \
    "v_smfmac_f32_16x16x64_bf16 v[236:239], v[252:255], v[184:191], %[ix]\n\t" XT(92, 93) "s_waitcnt lgkmcnt(1)\n\t" \
    "v_smfmac_f32_16x16x64_bf16 v[208:211], v[216:219], v[16:23], %[ix]\n\t" "s_nop 1\n\t" "v_smfmac_f32_16x16x64_bf16 v[212:215], v[216:219], v[200:207], %[ix]\n\t" \
    "s_waitcnt lgkmcnt(0)\n\t" "s_nop 0\n\t" "v_add_f32 v232, v232, v233\n\t" "v_add_f32 v234, v234, v235\n\t" "v_add_f32 v236, v236, v237\n\t" \
    "v_add_f32 v238, v238, v239\n\t" "v_cvt_pk_bf16_f32 v232, v232, v236\n\t" "v_cvt_pk_bf16_f32 v233, v234, v238\n\t" "ds_write2_b32 %[kw], v232, v233 offset1:68\n\t" \
    "v_smfmac_f32_16x16x64_bf16 v[208:211], v[220:223], v[24:31], %[ix]\n\t" "v_smfmac_f32_16x16x64_bf16 v[212:215], v[220:223], v[192:199], %[ix]\n\t" \
    "v_mov_b64 v[224:225], 0\n\t" "v_mov_b64 v[226:227], 0\n\t" "s_waitcnt lgkmcnt(0)\n\t" "s_barrier\n\t" XT(94, 95) "ds_read_b128 v[160:163], %[km] offset:0\n\t" \
    "ds_read_b128 v[164:167], %[km] offset:64\n\t" "ds_read_b128 v[168:171], %[km] offset:128\n\t" "ds_read_b128 v[172:175], %[km] offset:192\n\t" \
    "v_add_f32 v178, v208, v209\n\t" "v_add_f32 v179, v210, v211\n\t" "v_add_f32 v180, v212, v213\n\t" "v_add_f32 v181, v214, v215\n\t" \
    "v_pk_mul_f32 v[178:179], v[178:179], s[66:67]\n\t" "v_pk_mul_f32 v[180:181], v[180:181], s[66:67]\n\t" "v_max_f32 v182, v178, v180\n\t" "v_max_f32 v183, v179, v181\n\t" \
    "s_nop 0\n\t" "v_max_f32_dpp v182, v182, v182 row_ror:8 row_mask:0xf bank_mask:0xf\n\t" "v_max_f32_dpp v183, v183, v183 row_ror:8 row_mask:0xf bank_mask:0xf\n\t" \
    "s_nop 0\n\t" "s_waitcnt lgkmcnt(2)\n\t" "v_smfmac_f32_16x16x64_bf16 v[224:227], v[216:219], v[160:167], %[ix]\n\t" "s_waitcnt lgkmcnt(0)\n\t" \
    "v_smfmac_f32_16x16x64_bf16 v[224:227], v[220:223], v[168:175], %[ix]\n\t" "s_nop 7\n\t" "v_add_f32 v176, v224, v225\n\t" "v_add_f32 v177, v226, v227\n\t" \
    "v_mul_f32 v176, s66, v176\n\t" "v_mul_f32 v177, s66, v177\n\t" "v_max_f32 v182, v176, v182\n\t" "v_max_f32 v183, v177, v183\n\t" "s_nop 0\n\t" \
    "v_max_f32_dpp v182, v182, v182 row_ror:4 row_mask:0xf bank_mask:0xf\n\t" "v_max_f32_dpp v183, v183, v183 row_ror:4 row_mask:0xf bank_mask:0xf\n\t" "s_nop 0\n\t" \
    "v_max_f32_dpp v182, v182, v182 row_ror:2 row_mask:0xf bank_mask:0xf\n\t" "v_max_f32_dpp v183, v183, v183 row_ror:2 row_mask:0xf bank_mask:0xf\n\t" "s_nop 0\n\t" \
    "v_max_f32_dpp v182, v182, v182 row_ror:1 row_mask:0xf bank_mask:0xf\n\t" "v_max_f32_dpp v183, v183, v183 row_ror:1 row_mask:0xf bank_mask:0xf\n\t" \
    "v_pk_add_f32 v[178:179], v[178:179], v[182:183] neg_lo:[0,1] neg_hi:[0,1]\n\t" "v_pk_add_f32 v[180:181], v[180:181], v[182:183] neg_lo:[0,1] neg_hi:[0,1]\n\t" \
    "v_pk_add_f32 v[176:177], v[176:177], v[182:183] neg_lo:[0,1] neg_hi:[0,1]\n\t" "v_exp_f32 v178, v178\n\t" "v_exp_f32 v179, v179\n\t" "v_exp_f32 v180, v180\n\t" \
    "v_exp_f32 v181, v181\n\t" "v_exp_f32 v176, v176\n\t" "v_pk_add_f32 v[182:183], v[178:179], v[180:181]\n\t" "v_exp_f32 v177, v177\n\t" "s_nop 0\n\t" \
    "v_add_f32_dpp v182, v182, v182 row_ror:8 row_mask:0xf bank_mask:0xf\n\t" "v_add_f32_dpp v183, v183, v183 row_ror:8 row_mask:0xf bank_mask:0xf\n\t" \
    "v_add_f32 v182, v182, v176\n\t" "v_add_f32 v183, v183, v177\n\t" "s_nop 0\n\t" "v_add_f32_dpp v182, v182, v182 row_ror:4 row_mask:0xf bank_mask:0xf\n\t" \
    "v_add_f32_dpp v183, v183, v183 row_ror:4 row_mask:0xf bank_mask:0xf\n\t" "s_nop 0\n\t" "v_add_f32_dpp v182, v182, v182 row_ror:2 row_mask:0xf bank_mask:0xf\n\t" \
    "v_add_f32_dpp v183, v183, v183 row_ror:2 row_mask:0xf bank_mask:0xf\n\t" "s_nop 0\n\t" "v_add_f32_dpp v182, v182, v182 row_ror:1 row_mask:0xf bank_mask:0xf\n\t" \
    "v_add_f32_dpp v183, v183, v183 row_ror:1 row_mask:0xf bank_mask:0xf\n\t" "v_rcp_f32 v182, v182\n\t" "v_rcp_f32 v183, v183\n\t" "s_nop 0\n\t" \
    "v_pk_mul_f32 v[176:177], v[176:177], v[182:183]\n\t" "v_pk_mul_f32 v[178:179], v[178:179], v[182:183]\n\t" "v_pk_mul_f32 v[180:181], v[180:181], v[182:183]\n\t" \
    "v_cvt_pk_bf16_f32 v176, v176, v177\n\t" "v_cvt_pk_bf16_f32 v178, v178, v179\n\t" "v_cvt_pk_bf16_f32 v180, v180, v181\n\t" \
    "v_mov_b32_dpp v177, v176 quad_perm:[1,0,3,2] row_mask:0xf bank_mask:0xf\n\t" "v_mov_b32_dpp v179, v178 quad_perm:[1,0,3,2] row_mask:0xf bank_mask:0xf\n\t" \
    "v_mov_b32_dpp v181, v180 quad_perm:[1,0,3,2] row_mask:0xf bank_mask:0xf\n\t" "v_perm_b32 v184, v179, v178, %[ps]\n\t" "v_perm_b32 v185, v181, v180, %[ps]\n\t" \
    "buffer_store_dwordx2 v[184:185], %[px], %[rs], %[pb] offen sc1\n\t" "v_perm_b32 v186, v177, v176, %[ps]\n\t" "buffer_store_dword v186, %[pm], %[rs], %[pb] offen sc1\n\t" \
    XT(96, 97) LMARK "s_waitcnt vmcnt(2)\n\t" "s_branch 99f\n\t" "11:\n\t" "ds_read_b128 v[220:223], %[l1] offset:0\n\t" "s_waitcnt lgkmcnt(0)\n\t" \
    "v_max3_u32 v218, v220, v221, v222\n\t" "v_max_u32 v218, v218, v223\n\t" "ds_read_b128 v[220:223], %[l1] offset:1024\n\t" "s_waitcnt lgkmcnt(0)\n\t" \
    "v_max3_u32 v218, v218, v220, v221\n\t" "v_max3_u32 v218, v218, v222, v223\n\t" "ds_read_b128 v[220:223], %[l1] offset:2048\n\t" "s_waitcnt lgkmcnt(0)\n\t" \
    "v_max3_u32 v218, v218, v220, v221\n\t" "v_max3_u32 v218, v218, v222, v223\n\t" "ds_read_b128 v[220:223], %[l1] offset:3072\n\t" "s_waitcnt lgkmcnt(0)\n\t" \
    "v_max3_u32 v218, v218, v220, v221\n\t" "v_max3_u32 v218, v218, v222, v223\n\t" "v_cmp_eq_u32 vcc, -1, v218\n\t" "s_cbranch_vccz 12b\n\t" "s_cmp_eq_u32 %[rt], 1\n\t" \
    "s_cbranch_scc1 12b\n\t" "v_mov_b32 v218, %[rt]\n\t" "ds_write_b32 v219, v218\n\t" "s_waitcnt lgkmcnt(0)\n\t" "s_branch 12b\n\t" "13:\n\t" "s_sub_u32 %[rt], %[rt], 1\n\t" \
    "s_waitcnt lgkmcnt(0)\n\t" XCNT(100) "14:\n\t" "buffer_load_dwordx4 v[240:243], %[mo], %[rs], %[sb] offen sc1\n\t" \
    "buffer_load_dwordx4 v[244:247], v216, %[rs], %[sb] offen sc1\n\t" "buffer_load_dwordx4 v[248:251], %[mo], %[rs], s60 offen sc1\n\t" \
    "buffer_load_dwordx4 v[252:255], v216, %[rs], s60 offen sc1\n\t" "s_waitcnt vmcnt(0)\n\t" "v_max_u32 v218, v240, v241\n\t" "v_max3_u32 v218, v218, v242, v243\n\t" \
    "v_max3_u32 v218, v218, v244, v245\n\t" "v_max3_u32 v218, v218, v246, v247\n\t" "v_max3_u32 v218, v218, v248, v249\n\t" "v_max3_u32 v218, v218, v250, v251\n\t" \
    "v_max3_u32 v218, v218, v252, v253\n\t" "v_max3_u32 v218, v218, v254, v255\n\t" "v_cmp_eq_u32 vcc, -1, v218\n\t" "s_cbranch_vccz 15f\n\t" "s_cmp_eq_u32 %[bd], 0\n\t" \
    "s_cbranch_scc1 15f\n\t" "s_sub_u32 %[bd], %[bd], 1\n\t" XCNT(101) "s_branch 14b\n\t" "15:\n\t" "ds_read_b128 v[160:163], %[l0] offset:0\n\t" \
    "ds_read_b128 v[164:167], %[l0] offset:1024\n\t" "ds_read_b128 v[168:171], %[l0] offset:2048\n\t" "ds_read_b128 v[172:175], %[l0] offset:3072\n\t" \
    "ds_read_b128 v[176:179], %[l0] offset:4096\n\t" "ds_read_b128 v[180:183], %[l0] offset:5120\n\t" "ds_read_b128 v[184:187], %[l0] offset:6144\n\t" \
    "ds_read_b128 v[188:191], %[l0] offset:7168\n\t" "v_mov_b64 v[224:225], 0\n\t" "v_mov_b64 v[226:227], 0\n\t" "v_mov_b64 v[228:229], 0\n\t" "v_mov_b64 v[230:231], 0\n\t" \
    "v_mov_b64 v[232:233], 0\n\t" "v_mov_b64 v[234:235], 0\n\t" "v_mov_b64 v[236:237], 0\n\t" "v_mov_b64 v[238:239], 0\n\t" "s_barrier\n\t" "s_branch 10b\n\t" "99:\n\t"
#define QK_STEP1 \
    ".p2align 6\n\t" "v_xor_b32 v216, 0x400, %[mo]\n\t" "s_xor_b32 s60, %[sb], 0x800\n\t" "s_add_u32 s61, %[qb], 0x1000\n\t" "s_mov_b32 s66, 0x3e0293ee\n\t" \
    "s_mov_b32 s67, 0x3e0293ee\n\t" XT(80, 81) XT(98, 99) "s_barrier\n\t" XT(82, 83) "buffer_load_dwordx4 v[240:243], %[mo], %[rs], %[sb] offen sc1\n\t" \
    "v_mov_b64 v[224:225], 0\n\t" "v_mov_b64 v[226:227], 0\n\t" "v_mov_b64 v[228:229], 0\n\t" "v_mov_b64 v[230:231], 0\n\t" "v_mov_b64 v[232:233], 0\n\t" \
    "v_mov_b64 v[234:235], 0\n\t" "v_mov_b64 v[236:237], 0\n\t" "v_mov_b64 v[238:239], 0\n\t" "buffer_load_dwordx4 v[244:247], v216, %[rs], %[sb] offen sc1\n\t" \
    "ds_read_b128 v[160:163], %[l0] offset:0\n\t" "ds_read_b128 v[164:167], %[l0] offset:1024\n\t" "ds_read_b128 v[168:171], %[l0] offset:2048\n\t" \
    "ds_read_b128 v[172:175], %[l0] offset:3072\n\t" "s_nop 3\n\t" "buffer_load_dwordx4 v[248:251], %[mo], %[rs], s60 offen sc1\n\t" \
    "ds_read_b128 v[176:179], %[l0] offset:4096\n\t" "ds_read_b128 v[180:183], %[l0] offset:5120\n\t" "ds_read_b128 v[184:187], %[l0] offset:6144\n\t" \
    "ds_read_b128 v[188:191], %[l0] offset:7168\n\t" "s_nop 3\n\t" "buffer_load_dwordx4 v[252:255], v216, %[rs], s60 offen sc1\n\t" "v_mov_b32 v219, 160512\n\t" \
    "v_mov_b64 v[220:221], -1\n\t" "v_mov_b64 v[222:223], -1\n\t" "buffer_store_dwordx2 v[220:221], %[px], %[rs], %[tr] offen sc1\n\t" \
    "buffer_store_dwordx2 v[220:221], %[pm], %[rs], %[tr] offen sc1\n\t" "v_mov_b32 v208, 0xd028965f\n\t" "v_mov_b32 v209, 0xff800000\n\t" "s_mov_b32 s62, 0\n\t" \
    "s_mov_b32 s63, -1\n\t" "v_mov_b32 v210, 0\n\t" "v_cmp_gt_i32 vcc, 0, %[ps]\n\t" "v_cndmask_b32 v204, 0, v209, s[62:63]\n\t" "s_nop 0\n\t" \
    "v_cndmask_b32 v200, 0, v208, vcc\n\t" "v_cmp_gt_i32 vcc, 1, %[ps]\n\t" "v_cndmask_b32 v205, 0, v209, s[62:63]\n\t" "s_nop 0\n\t" "v_cndmask_b32 v201, 0, v208, vcc\n\t" \
    "v_cmp_gt_i32 vcc, 2, %[ps]\n\t" "v_cndmask_b32 v206, 0, v209, s[62:63]\n\t" "s_nop 0\n\t" "v_cndmask_b32 v202, 0, v208, vcc\n\t" "v_cmp_gt_i32 vcc, 3, %[ps]\n\t" \
    "v_cndmask_b32 v207, 0, v209, s[62:63]\n\t" "s_nop 0\n\t" "v_cndmask_b32 v203, 0, v208, vcc\n\t" "20:\n\t" "s_waitcnt vmcnt(5)\n\t" XT(84, 85) \
    "v_smfmac_f32_16x16x64_bf16 v[224:227], v[240:243], a[0:7], %[ix]\n\t" "ds_write_b128 %[l1], v[240:243] offset:4096\n\t" \
    "v_smfmac_f32_16x16x64_bf16 v[228:231], v[240:243], a[8:15], %[ix]\n\t" "v_smfmac_f32_16x16x64_bf16 v[232:235], v[240:243], v[32:39], %[ix]\n\t" \
    "s_waitcnt lgkmcnt(7)\n\t" "v_smfmac_f32_16x16x64_bf16 v[236:239], v[240:243], v[160:167], %[ix]\n\t" "ds_read_b128 v[160:163], %[l0] offset:8192\n\t" \
    "ds_read_b128 v[164:167], %[l0] offset:9216\n\t" "s_waitcnt vmcnt(4)\n\t" "v_smfmac_f32_16x16x64_bf16 v[224:227], v[244:247], a[16:23], %[ix]\n\t" \
    "ds_write_b128 %[l1], v[244:247] offset:5120\n\t" "v_smfmac_f32_16x16x64_bf16 v[228:231], v[244:247], a[24:31], %[ix]\n\t" \
    "v_smfmac_f32_16x16x64_bf16 v[232:235], v[244:247], v[40:47], %[ix]\n\t" "s_waitcnt lgkmcnt(8)\n\t" \
    "v_smfmac_f32_16x16x64_bf16 v[236:239], v[244:247], v[168:175], %[ix]\n\t" "ds_read_b128 v[168:171], %[l0] offset:10240\n\t" \
    "ds_read_b128 v[172:175], %[l0] offset:11264\n\t" "s_waitcnt vmcnt(3)\n\t" "v_smfmac_f32_16x16x64_bf16 v[224:227], v[248:251], a[32:39], %[ix]\n\t" \
    "ds_write_b128 %[l1], v[248:251] offset:6144\n\t" "v_smfmac_f32_16x16x64_bf16 v[228:231], v[248:251], a[40:47], %[ix]\n\t" "s_waitcnt lgkmcnt(3)\n\t" "s_barrier\n\t" \
    XT(86, 87) "v_smfmac_f32_16x16x64_bf16 v[232:235], v[248:251], v[48:55], %[ix]\n\t" "ds_read_b128 v[192:195], %[l1] offset:8192\n\t" \
    "ds_read_b128 v[196:199], %[l1] offset:12288\n\t" "v_smfmac_f32_16x16x64_bf16 v[236:239], v[248:251], v[176:183], %[ix]\n\t" \
    "ds_read_b128 v[176:179], %[l0] offset:12288\n\t" "ds_read_b128 v[180:183], %[l0] offset:13312\n\t" "s_waitcnt vmcnt(2)\n\t" \
    "v_smfmac_f32_16x16x64_bf16 v[224:227], v[252:255], a[48:55], %[ix]\n\t" "ds_write_b128 %[l1], v[252:255] offset:7168\n\t" \
    "v_smfmac_f32_16x16x64_bf16 v[228:231], v[252:255], a[56:63], %[ix]\n\t" "ds_read_b128 v[240:243], %[l1] offset:0\n\t" "ds_read_b128 v[244:247], %[l1] offset:9216\n\t" \
    "v_smfmac_f32_16x16x64_bf16 v[232:235], v[252:255], v[56:63], %[ix]\n\t" "s_nop 1\n\t" "v_smfmac_f32_16x16x64_bf16 v[236:239], v[252:255], v[184:191], %[ix]\n\t" \
    "ds_read_b128 v[184:187], %[l0] offset:14336\n\t" "v_cmp_u_f32 vcc, v224, v225\n\t" "v_cmp_u_f32 s[62:63], v226, v227\n\t" "s_waitcnt lgkmcnt(7)\n\t" \
    "v_smfmac_f32_16x16x64_bf16 v[224:227], v[192:195], a[64:71], %[ix]\n\t" "ds_read_b128 v[188:191], %[l0] offset:15360\n\t" \
    "v_smfmac_f32_16x16x64_bf16 v[228:231], v[192:195], a[72:79], %[ix]\n\t" "s_or_b64 vcc, vcc, s[62:63]\n\t" "s_cbranch_scc1 21f\n\t" "22:\n\t" \
    "v_smfmac_f32_16x16x64_bf16 v[232:235], v[192:195], v[64:71], %[ix]\n\t" "s_waitcnt lgkmcnt(4)\n\t" "s_barrier\n\t" XT(88, 89) "ds_read_b32 v218, v219\n\t" \
    "v_smfmac_f32_16x16x64_bf16 v[236:239], v[192:195], v[160:167], %[ix]\n\t" "ds_read_b128 v[160:163], %[l0] offset:16384\n\t" \
    "ds_read_b128 v[164:167], %[l0] offset:17408\n\t" "v_smfmac_f32_16x16x64_bf16 v[224:227], v[196:199], a[80:87], %[ix]\n\t" \
    "ds_read_b128 v[248:251], %[l1] offset:13312\n\t" "ds_read_b128 v[252:255], %[l1] offset:1024\n\t" \
    "v_smfmac_f32_16x16x64_bf16 v[228:231], v[196:199], a[88:95], %[ix]\n\t" "ds_read_b128 v[192:195], %[l1] offset:10240\n\t" \
    "v_smfmac_f32_16x16x64_bf16 v[232:235], v[196:199], v[72:79], %[ix]\n\t" "v_smfmac_f32_16x16x64_bf16 v[236:239], v[196:199], v[168:175], %[ix]\n\t" \
    "s_waitcnt lgkmcnt(5)\n\t" "v_cmp_eq_u32 vcc, %[rt], v218\n\t" "v_smfmac_f32_16x16x64_bf16 v[224:227], v[240:243], a[96:103], %[ix]\n\t" "s_cbranch_vccnz 23f\n\t" \
    "ds_read_b128 v[168:171], %[l0] offset:18432\n\t" "v_smfmac_f32_16x16x64_bf16 v[228:231], v[240:243], a[104:111], %[ix]\n\t" \
    "ds_read_b128 v[172:175], %[l0] offset:19456\n\t" "ds_read_b128 v[196:199], %[l1] offset:14336\n\t" \
    "v_smfmac_f32_16x16x64_bf16 v[232:235], v[240:243], v[80:87], %[ix]\n\t" "buffer_load_dwordx4 v[16:19], %[hq], %[rs], %[qb] offen offset:0 sc1\n\t" \
    "v_smfmac_f32_16x16x64_bf16 v[236:239], v[240:243], v[176:183], %[ix]\n\t" "ds_read_b128 v[176:179], %[l0] offset:20480\n\t" \
    "ds_read_b128 v[180:183], %[l0] offset:21504\n\t" "v_smfmac_f32_16x16x64_bf16 v[224:227], v[244:247], a[112:119], %[ix]\n\t" \
    "ds_read_b128 v[240:243], %[l1] offset:2048\n\t" "v_smfmac_f32_16x16x64_bf16 v[228:231], v[244:247], a[120:127], %[ix]\n\t" \
    "v_smfmac_f32_16x16x64_bf16 v[232:235], v[244:247], v[88:95], %[ix]\n\t" "buffer_load_dwordx4 v[20:23], %[hq], %[rs], %[qb] offen offset:2048 sc1\n\t" \
    "v_smfmac_f32_16x16x64_bf16 v[236:239], v[244:247], v[184:191], %[ix]\n\t" "ds_read_b128 v[184:187], %[l0] offset:22528\n\t" \
    "ds_read_b128 v[188:191], %[l0] offset:23552\n\t" "s_waitcnt lgkmcnt(10)\n\t" "v_smfmac_f32_16x16x64_bf16 v[224:227], v[248:251], a[128:135], %[ix]\n\t" \
    "ds_read_b128 v[244:247], %[l1] offset:11264\n\t" "v_smfmac_f32_16x16x64_bf16 v[228:231], v[248:251], a[136:143], %[ix]\n\t" \
    "v_smfmac_f32_16x16x64_bf16 v[232:235], v[248:251], v[96:103], %[ix]\n\t" "buffer_load_dwordx4 v[24:27], %[hq], %[rs], s61 offen offset:0 sc1\n\t" \
    "v_smfmac_f32_16x16x64_bf16 v[236:239], v[248:251], v[160:167], %[ix]\n\t" "ds_read_b128 v[160:163], %[l0] offset:24576\n\t" \
    "ds_read_b128 v[164:167], %[l0] offset:25600\n\t" "s_waitcnt lgkmcnt(12)\n\t" "v_smfmac_f32_16x16x64_bf16 v[224:227], v[252:255], a[144:151], %[ix]\n\t" \
    "ds_read_b128 v[248:251], %[l1] offset:15360\n\t" "v_smfmac_f32_16x16x64_bf16 v[228:231], v[252:255], a[152:159], %[ix]\n\t" \
    "v_smfmac_f32_16x16x64_bf16 v[232:235], v[252:255], v[104:111], %[ix]\n\t" "buffer_load_dwordx4 v[28:31], %[hq], %[rs], s61 offen offset:2048 sc1\n\t" \
    "s_waitcnt lgkmcnt(10)\n\t" "v_smfmac_f32_16x16x64_bf16 v[236:239], v[252:255], v[168:175], %[ix]\n\t" "ds_read_b128 v[168:171], %[l0] offset:26624\n\t" \
    "ds_read_b128 v[172:175], %[l0] offset:27648\n\t" "v_smfmac_f32_16x16x64_bf16 v[224:227], v[192:195], a[160:167], %[ix]\n\t" \
    "ds_read_b128 v[252:255], %[l1] offset:3072\n\t" "v_smfmac_f32_16x16x64_bf16 v[228:231], v[192:195], a[168:175], %[ix]\n\t" "s_waitcnt lgkmcnt(12)\n\t" \
    "v_smfmac_f32_16x16x64_bf16 v[224:227], v[196:199], a[176:183], %[ix]\n\t" "v_smfmac_f32_16x16x64_bf16 v[228:231], v[196:199], a[184:191], %[ix]\n\t" \
    "s_waitcnt lgkmcnt(9)\n\t" "v_smfmac_f32_16x16x64_bf16 v[224:227], v[240:243], a[192:199], %[ix]\n\t" \
    "v_smfmac_f32_16x16x64_bf16 v[228:231], v[240:243], a[200:207], %[ix]\n\t" "s_waitcnt lgkmcnt(6)\n\t" \
    "v_smfmac_f32_16x16x64_bf16 v[224:227], v[244:247], a[208:215], %[ix]\n\t" "v_smfmac_f32_16x16x64_bf16 v[228:231], v[244:247], a[216:223], %[ix]\n\t" \
    "s_waitcnt vmcnt(4)\n\t" "s_waitcnt lgkmcnt(3)\n\t" "v_smfmac_f32_16x16x64_bf16 v[224:227], v[248:251], a[224:231], %[ix]\n\t" \
    "v_smfmac_f32_16x16x64_bf16 v[228:231], v[248:251], a[232:239], %[ix]\n\t" "s_waitcnt lgkmcnt(0)\n\t" \
    "v_smfmac_f32_16x16x64_bf16 v[224:227], v[252:255], a[240:247], %[ix]\n\t" "s_nop 1\n\t" "v_smfmac_f32_16x16x64_bf16 v[228:231], v[252:255], a[248:255], %[ix]\n\t" \
    XT(90, 91) "s_nop 1\n\t" "v_smfmac_f32_16x16x64_bf16 v[232:235], v[192:195], v[112:119], %[ix]\n\t" "s_nop 1\n\t" \
    "v_smfmac_f32_16x16x64_bf16 v[236:239], v[192:195], v[176:183], %[ix]\n\t" "ds_read_b128 v[176:179], %[l0] offset:28672\n\t" "v_add_f32 v224, v224, v225\n\t" \
    "v_add_f32 v226, v226, v227\n\t" "v_smfmac_f32_16x16x64_bf16 v[232:235], v[196:199], v[120:127], %[ix]\n\t" "ds_read_b128 v[180:183], %[l0] offset:29696\n\t" \
    "v_add_f32 v228, v228, v229\n\t" "v_add_f32 v230, v230, v231\n\t" "v_smfmac_f32_16x16x64_bf16 v[236:239], v[196:199], v[184:191], %[ix]\n\t" \
    "ds_read_b128 v[184:187], %[l0] offset:30720\n\t" "v_cvt_pk_bf16_f32 v218, v224, v228\n\t" "v_cvt_pk_bf16_f32 v219, v226, v230\n\t" \
    "v_smfmac_f32_16x16x64_bf16 v[232:235], v[240:243], v[128:135], %[ix]\n\t" "ds_write2_b32 %[qw], v218, v219 offset1:8\n\t" \
    "ds_read_b128 v[188:191], %[l0] offset:31744\n\t" "v_smfmac_f32_16x16x64_bf16 v[236:239], v[240:243], v[160:167], %[ix]\n\t" \
    "v_smfmac_f32_16x16x64_bf16 v[232:235], v[244:247], v[136:143], %[ix]\n\t" "v_smfmac_f32_16x16x64_bf16 v[236:239], v[244:247], v[168:175], %[ix]\n\t" \
    "s_waitcnt lgkmcnt(1)\n\t" "s_barrier\n\t" "v_smfmac_f32_16x16x64_bf16 v[232:235], v[248:251], v[144:151], %[ix]\n\t" \
    "v_smfmac_f32_16x16x64_bf16 v[236:239], v[248:251], v[176:183], %[ix]\n\t" "v_smfmac_f32_16x16x64_bf16 v[232:235], v[252:255], v[152:159], %[ix]\n\t" \
    "s_waitcnt lgkmcnt(0)\n\t" "v_smfmac_f32_16x16x64_bf16 v[236:239], v[252:255], v[184:191], %[ix]\n\t" XT(92, 93) "s_nop 6\n\t" "v_add_f32 v232, v232, v233\n\t" \
    "v_add_f32 v234, v234, v235\n\t" "v_add_f32 v236, v236, v237\n\t" "v_add_f32 v238, v238, v239\n\t" "v_cvt_pk_bf16_f32 v232, v232, v236\n\t" \
    "v_cvt_pk_bf16_f32 v233, v234, v238\n\t" "ds_write2_b32 %[kw], v232, v233 offset1:68\n\t" "s_waitcnt lgkmcnt(0)\n\t" "s_barrier\n\t" XT(94, 95) \
    "s_cmp_ge_u32 %[sq], %[sn]\n\t" "s_cbranch_scc1 277f\n\t" "275:\n\t" "global_load_dword v240, v210, %[fq] sc1\n\t" "s_waitcnt vmcnt(0)\n\t" \
    "v_readfirstlane_b32 %[sq], v240\n\t" "s_nop 3\n\t" "s_cmp_ge_u32 %[sq], %[sn]\n\t" "s_cbranch_scc1 276f\n\t" "s_cmp_eq_u32 %[bd], 0\n\t" "s_cbranch_scc1 276f\n\t" \
    "s_sub_u32 %[bd], %[bd], 1\n\t" "s_sleep 2\n\t" "s_branch 275b\n\t" "276:\n\t" "buffer_load_dwordx4 v[16:19], %[hq], %[rs], %[qb] offen offset:0 sc1\n\t" \
    "buffer_load_dwordx4 v[20:23], %[hq], %[rs], %[qb] offen offset:2048 sc1\n\t" "buffer_load_dwordx4 v[24:27], %[hq], %[rs], s61 offen offset:0 sc1\n\t" \
    "buffer_load_dwordx4 v[28:31], %[hq], %[rs], s61 offen offset:2048 sc1\n\t" "277:\n\t" "s_nop 3\n\t" "ds_read_b128 v[160:163], %[kx] offset:0\n\t" \
    "ds_read_b128 v[164:167], %[kx] offset:2048\n\t" "ds_read_b128 v[168:171], %[kx] offset:4096\n\t" "ds_read_b128 v[172:175], %[kx] offset:6144\n\t" \
    "ds_read_b128 v[224:227], %[km] offset:0\n\t" "ds_read_b128 v[228:231], %[km] offset:64\n\t" "ds_read_b128 v[232:235], %[km] offset:128\n\t" \
    "ds_read_b128 v[236:239], %[km] offset:192\n\t" "s_waitcnt vmcnt(0)\n\t" "global_load_dword v240, v210, %[fq] sc1\n\t" "s_waitcnt lgkmcnt(7)\n\t" \
    "v_mfma_f32_16x16x32_bf16 v[248:251], v[160:163], v[16:19], v[200:203]\n\t" "s_waitcnt lgkmcnt(6)\n\t" \
    "v_mfma_f32_16x16x32_bf16 v[248:251], v[164:167], v[20:23], v[248:251]\n\t" "s_waitcnt lgkmcnt(5)\n\t" \
    "v_mfma_f32_16x16x32_bf16 v[248:251], v[168:171], v[24:27], v[248:251]\n\t" "s_waitcnt lgkmcnt(4)\n\t" \
    "v_mfma_f32_16x16x32_bf16 v[248:251], v[172:175], v[28:31], v[248:251]\n\t" "s_waitcnt lgkmcnt(3)\n\t" \
    "v_mfma_f32_16x16x32_bf16 v[244:247], v[224:227], v[16:19], v[204:207]\n\t" "s_waitcnt lgkmcnt(2)\n\t" \
    "v_mfma_f32_16x16x32_bf16 v[244:247], v[228:231], v[20:23], v[244:247]\n\t" "s_waitcnt lgkmcnt(1)\n\t" \
    "v_mfma_f32_16x16x32_bf16 v[244:247], v[232:235], v[24:27], v[244:247]\n\t" "s_waitcnt lgkmcnt(0)\n\t" \
    "v_mfma_f32_16x16x32_bf16 v[244:247], v[236:239], v[28:31], v[244:247]\n\t" "s_nop 3\n\t" "v_max3_f32 v242, v248, v249, v250\n\t" "v_max_f32 v242, v242, v251\n\t" \
    "s_nop 1\n\t" "v_max3_f32 v242, v242, v244, v245\n\t" "v_max3_f32 v242, v242, v246, v247\n\t" "v_mov_b32 v243, v242\n\t" "s_nop 1\n\t" \
    "v_permlane32_swap_b32 v242, v243\n\t" "v_max_f32 v242, v242, v243\n\t" "v_mov_b32 v243, v242\n\t" "s_nop 1\n\t" "v_permlane16_swap_b32 v242, v243\n\t" \
    "v_max_f32 v242, v242, v243\n\t" "v_pk_add_f32 v[244:245], v[244:245], v[242:243] op_sel_hi:[1,0] neg_lo:[0,1] neg_hi:[0,1]\n\t" \
    "v_pk_add_f32 v[246:247], v[246:247], v[242:243] op_sel_hi:[1,0] neg_lo:[0,1] neg_hi:[0,1]\n\t" \
    "v_pk_add_f32 v[248:249], v[248:249], v[242:243] op_sel_hi:[1,0] neg_lo:[0,1] neg_hi:[0,1]\n\t" \
    "v_pk_add_f32 v[250:251], v[250:251], v[242:243] op_sel_hi:[1,0] neg_lo:[0,1] neg_hi:[0,1]\n\t" "v_pk_mul_f32 v[244:245], v[244:245], s[66:67]\n\t" \
    "v_pk_mul_f32 v[246:247], v[246:247], s[66:67]\n\t" "v_pk_mul_f32 v[248:249], v[248:249], s[66:67]\n\t" "v_pk_mul_f32 v[250:251], v[250:251], s[66:67]\n\t" \
    "v_exp_f32 v244, v244\n\t" "v_exp_f32 v245, v245\n\t" "v_exp_f32 v246, v246\n\t" "v_exp_f32 v247, v247\n\t" "v_exp_f32 v248, v248\n\t" "v_exp_f32 v249, v249\n\t" \
    "v_exp_f32 v250, v250\n\t" "v_exp_f32 v251, v251\n\t" "v_pk_add_f32 v[242:243], v[244:245], v[246:247]\n\t" "v_pk_add_f32 v[242:243], v[242:243], v[248:249]\n\t" \
    "v_pk_add_f32 v[242:243], v[242:243], v[250:251]\n\t" "v_add_f32 v242, v242, v243\n\t" "v_mov_b32 v243, v242\n\t" "s_nop 1\n\t" "v_permlane32_swap_b32 v242, v243\n\t" \
    "v_add_f32 v242, v242, v243\n\t" "v_mov_b32 v243, v242\n\t" "s_nop 1\n\t" "v_permlane16_swap_b32 v242, v243\n\t" "v_add_f32 v242, v242, v243\n\t" \
    "v_rcp_f32 v242, v242\n\t" "s_nop 0\n\t" "v_pk_mul_f32 v[244:245], v[244:245], v[242:243] op_sel_hi:[1,0]\n\t" \
    "v_pk_mul_f32 v[246:247], v[246:247], v[242:243] op_sel_hi:[1,0]\n\t" "v_pk_mul_f32 v[248:249], v[248:249], v[242:243] op_sel_hi:[1,0]\n\t" \
    "v_pk_mul_f32 v[250:251], v[250:251], v[242:243] op_sel_hi:[1,0]\n\t" "v_cvt_pk_bf16_f32 v162, v244, v245\n\t" "v_cvt_pk_bf16_f32 v163, v246, v247\n\t" \
    "v_cvt_pk_bf16_f32 v164, v248, v249\n\t" "v_cvt_pk_bf16_f32 v165, v250, v251\n\t" "buffer_store_dwordx2 v[164:165], %[px], %[rs], %[tb] offen sc1\n\t" \
    "buffer_store_dwordx2 v[162:163], %[pm], %[rs], %[tb] offen sc1\n\t" "s_waitcnt vmcnt(2)\n\t" "v_readfirstlane_b32 s62, v240\n\t" "s_nop 3\n\t" \
    "s_max_u32 %[sq], %[sq], s62\n\t" "s_barrier\n\t" XT(96, 97) "s_branch 99f\n\t" "21:\n\t" "ds_read_b128 v[220:223], %[l1] offset:4096\n\t" "s_waitcnt lgkmcnt(0)\n\t" \
    "v_max3_u32 v218, v220, v221, v222\n\t" "v_max_u32 v218, v218, v223\n\t" "ds_read_b128 v[220:223], %[l1] offset:5120\n\t" "s_waitcnt lgkmcnt(0)\n\t" \
    "v_max3_u32 v218, v218, v220, v221\n\t" "v_max3_u32 v218, v218, v222, v223\n\t" "ds_read_b128 v[220:223], %[l1] offset:6144\n\t" "s_waitcnt lgkmcnt(0)\n\t" \
    "v_max3_u32 v218, v218, v220, v221\n\t" "v_max3_u32 v218, v218, v222, v223\n\t" "ds_read_b128 v[220:223], %[l1] offset:7168\n\t" "s_waitcnt lgkmcnt(0)\n\t" \
    "v_max3_u32 v218, v218, v220, v221\n\t" "v_max3_u32 v218, v218, v222, v223\n\t" "v_cmp_eq_u32 vcc, -1, v218\n\t" "s_cbranch_vccz 22b\n\t" "s_cmp_eq_u32 %[rt], 1\n\t" \
    "s_cbranch_scc1 22b\n\t" "v_mov_b32 v218, %[rt]\n\t" "ds_write_b32 v219, v218\n\t" "s_waitcnt lgkmcnt(0)\n\t" "s_branch 22b\n\t" "23:\n\t" "s_sub_u32 %[rt], %[rt], 1\n\t" \
    "s_waitcnt lgkmcnt(0)\n\t" XCNT(100) "24:\n\t" "buffer_load_dwordx4 v[240:243], %[mo], %[rs], %[sb] offen sc1\n\t" \
    "buffer_load_dwordx4 v[244:247], v216, %[rs], %[sb] offen sc1\n\t" "buffer_load_dwordx4 v[248:251], %[mo], %[rs], s60 offen sc1\n\t" \
    "buffer_load_dwordx4 v[252:255], v216, %[rs], s60 offen sc1\n\t" "s_waitcnt vmcnt(0)\n\t" "v_max_u32 v218, v240, v241\n\t" "v_max3_u32 v218, v218, v242, v243\n\t" \
    "v_max3_u32 v218, v218, v244, v245\n\t" "v_max3_u32 v218, v218, v246, v247\n\t" "v_max3_u32 v218, v218, v248, v249\n\t" "v_max3_u32 v218, v218, v250, v251\n\t" \
    "v_max3_u32 v218, v218, v252, v253\n\t" "v_max3_u32 v218, v218, v254, v255\n\t" "v_cmp_eq_u32 vcc, -1, v218\n\t" "s_cbranch_vccz 25f\n\t" "s_cmp_eq_u32 %[bd], 0\n\t" \
    "s_cbranch_scc1 25f\n\t" "s_sub_u32 %[bd], %[bd], 1\n\t" XCNT(101) "s_branch 24b\n\t" "25:\n\t" "ds_read_b128 v[160:163], %[l0] offset:0\n\t" \
    "ds_read_b128 v[164:167], %[l0] offset:1024\n\t" "ds_read_b128 v[168:171], %[l0] offset:2048\n\t" "ds_read_b128 v[172:175], %[l0] offset:3072\n\t" \
    "ds_read_b128 v[176:179], %[l0] offset:4096\n\t" "ds_read_b128 v[180:183], %[l0] offset:5120\n\t" "ds_read_b128 v[184:187], %[l0] offset:6144\n\t" \
    "ds_read_b128 v[188:191], %[l0] offset:7168\n\t" "v_mov_b64 v[224:225], 0\n\t" "v_mov_b64 v[226:227], 0\n\t" "v_mov_b64 v[228:229], 0\n\t" "v_mov_b64 v[230:231], 0\n\t" \
    "v_mov_b64 v[232:233], 0\n\t" "v_mov_b64 v[234:235], 0\n\t" "v_mov_b64 v[236:237], 0\n\t" "v_mov_b64 v[238:239], 0\n\t" "s_barrier\n\t" "s_branch 20b\n\t" "99:\n\t"
#define QK_STEP2 \
    ".p2align 6\n\t" "v_xor_b32 v216, 0x400, %[mo]\n\t" "s_xor_b32 s60, %[sb], 0x800\n\t" "s_add_u32 s61, %[qb], 0x1000\n\t" "s_mov_b32 s66, 0x3e0293ee\n\t" \
    "s_mov_b32 s67, 0x3e0293ee\n\t" XT(80, 81) XT(98, 99) "s_barrier\n\t" XT(82, 83) "buffer_load_dwordx4 v[240:243], %[mo], %[rs], %[sb] offen sc1\n\t" \
    "v_mov_b64 v[224:225], 0\n\t" "v_mov_b64 v[226:227], 0\n\t" "v_mov_b64 v[228:229], 0\n\t" "v_mov_b64 v[230:231], 0\n\t" "v_mov_b64 v[232:233], 0\n\t" \
    "v_mov_b64 v[234:235], 0\n\t" "v_mov_b64 v[236:237], 0\n\t" "v_mov_b64 v[238:239], 0\n\t" "buffer_load_dwordx4 v[244:247], v216, %[rs], %[sb] offen sc1\n\t" \
    "ds_read_b128 v[160:163], %[l0] offset:0\n\t" "ds_read_b128 v[164:167], %[l0] offset:1024\n\t" "ds_read_b128 v[168:171], %[l0] offset:2048\n\t" \
    "ds_read_b128 v[172:175], %[l0] offset:3072\n\t" "s_nop 3\n\t" "buffer_load_dwordx4 v[248:251], %[mo], %[rs], s60 offen sc1\n\t" \
    "ds_read_b128 v[176:179], %[l0] offset:4096\n\t" "ds_read_b128 v[180:183], %[l0] offset:5120\n\t" "ds_read_b128 v[184:187], %[l0] offset:6144\n\t" \
    "ds_read_b128 v[188:191], %[l0] offset:7168\n\t" "s_nop 3\n\t" "buffer_load_dwordx4 v[252:255], v216, %[rs], s60 offen sc1\n\t" "v_mov_b32 v219, 160512\n\t" \
    "v_mov_b64 v[220:221], -1\n\t" "v_mov_b64 v[222:223], -1\n\t" "buffer_store_dwordx4 v[220:223], %[px], %[rs], %[tr] offen sc1\n\t" \
    "buffer_store_dwordx2 v[220:221], %[pm], %[rs], %[tr] offen sc1\n\t" "v_mov_b32 v208, 0xd028965f\n\t" "v_mov_b32 v209, 0xff800000\n\t" "s_mov_b32 s62, 0\n\t" \
    "s_mov_b32 s63, -1\n\t" "v_mov_b32 v210, 0\n\t" "v_cmp_gt_i32 vcc, 0, %[ps]\n\t" "v_cndmask_b32 v204, 0, v209, s[62:63]\n\t" "s_nop 0\n\t" \
    "v_cndmask_b32 v200, 0, v208, vcc\n\t" "v_cmp_gt_i32 vcc, 1, %[ps]\n\t" "v_cndmask_b32 v205, 0, v209, s[62:63]\n\t" "s_nop 0\n\t" "v_cndmask_b32 v201, 0, v208, vcc\n\t" \
    "v_cmp_gt_i32 vcc, 2, %[ps]\n\t" "v_cndmask_b32 v206, 0, v209, s[62:63]\n\t" "s_nop 0\n\t" "v_cndmask_b32 v202, 0, v208, vcc\n\t" "v_cmp_gt_i32 vcc, 3, %[ps]\n\t" \
    "v_cndmask_b32 v207, 0, v209, s[62:63]\n\t" "s_nop 0\n\t" "v_cndmask_b32 v203, 0, v208, vcc\n\t" "30:\n\t" "s_waitcnt vmcnt(5)\n\t" XT(84, 85) \
    "v_smfmac_f32_16x16x64_bf16 v[224:227], v[240:243], a[0:7], %[ix]\n\t" "ds_write_b128 %[l1], v[240:243] offset:8192\n\t" \
    "v_smfmac_f32_16x16x64_bf16 v[228:231], v[240:243], a[8:15], %[ix]\n\t" "v_smfmac_f32_16x16x64_bf16 v[232:235], v[240:243], v[32:39], %[ix]\n\t" \
    "s_waitcnt lgkmcnt(7)\n\t" "v_smfmac_f32_16x16x64_bf16 v[236:239], v[240:243], v[160:167], %[ix]\n\t" "ds_read_b128 v[160:163], %[l0] offset:8192\n\t" \
    "ds_read_b128 v[164:167], %[l0] offset:9216\n\t" "s_waitcnt vmcnt(4)\n\t" "v_smfmac_f32_16x16x64_bf16 v[224:227], v[244:247], a[16:23], %[ix]\n\t" \
    "ds_write_b128 %[l1], v[244:247] offset:9216\n\t" "v_smfmac_f32_16x16x64_bf16 v[228:231], v[244:247], a[24:31], %[ix]\n\t" \
    "v_smfmac_f32_16x16x64_bf16 v[232:235], v[244:247], v[40:47], %[ix]\n\t" "s_waitcnt lgkmcnt(8)\n\t" \
    "v_smfmac_f32_16x16x64_bf16 v[236:239], v[244:247], v[168:175], %[ix]\n\t" "ds_read_b128 v[168:171], %[l0] offset:10240\n\t" \
    "ds_read_b128 v[172:175], %[l0] offset:11264\n\t" "s_waitcnt vmcnt(3)\n\t" "v_smfmac_f32_16x16x64_bf16 v[224:227], v[248:251], a[32:39], %[ix]\n\t" \
    "ds_write_b128 %[l1], v[248:251] offset:10240\n\t" "v_smfmac_f32_16x16x64_bf16 v[228:231], v[248:251], a[40:47], %[ix]\n\t" "s_waitcnt lgkmcnt(3)\n\t" "s_barrier\n\t" \
    XT(86, 87) "v_smfmac_f32_16x16x64_bf16 v[232:235], v[248:251], v[48:55], %[ix]\n\t" "ds_read_b128 v[192:195], %[l1] offset:12288\n\t" \
    "ds_read_b128 v[196:199], %[l1] offset:0\n\t" "v_smfmac_f32_16x16x64_bf16 v[236:239], v[248:251], v[176:183], %[ix]\n\t" "ds_read_b128 v[176:179], %[l0] offset:12288\n\t" \
    "ds_read_b128 v[180:183], %[l0] offset:13312\n\t" "s_waitcnt vmcnt(2)\n\t" "v_smfmac_f32_16x16x64_bf16 v[224:227], v[252:255], a[48:55], %[ix]\n\t" \
    "ds_write_b128 %[l1], v[252:255] offset:11264\n\t" "v_smfmac_f32_16x16x64_bf16 v[228:231], v[252:255], a[56:63], %[ix]\n\t" \
    "ds_read_b128 v[240:243], %[l1] offset:4096\n\t" "ds_read_b128 v[244:247], %[l1] offset:13312\n\t" \
    "v_smfmac_f32_16x16x64_bf16 v[232:235], v[252:255], v[56:63], %[ix]\n\t" "s_nop 1\n\t" "v_smfmac_f32_16x16x64_bf16 v[236:239], v[252:255], v[184:191], %[ix]\n\t" \
    "ds_read_b128 v[184:187], %[l0] offset:14336\n\t" "v_cmp_u_f32 vcc, v224, v225\n\t" "v_cmp_u_f32 s[62:63], v226, v227\n\t" "s_waitcnt lgkmcnt(7)\n\t" \
    "v_smfmac_f32_16x16x64_bf16 v[224:227], v[192:195], a[64:71], %[ix]\n\t" "ds_read_b128 v[188:191], %[l0] offset:15360\n\t" \
    "v_smfmac_f32_16x16x64_bf16 v[228:231], v[192:195], a[72:79], %[ix]\n\t" "s_or_b64 vcc, vcc, s[62:63]\n\t" "s_cbranch_scc1 31f\n\t" "32:\n\t" \
    "v_smfmac_f32_16x16x64_bf16 v[232:235], v[192:195], v[64:71], %[ix]\n\t" "s_waitcnt lgkmcnt(4)\n\t" "s_barrier\n\t" XT(88, 89) "ds_read_b32 v218, v219\n\t" \
    "v_smfmac_f32_16x16x64_bf16 v[236:239], v[192:195], v[160:167], %[ix]\n\t" "ds_read_b128 v[160:163], %[l0] offset:16384\n\t" \
    "ds_read_b128 v[164:167], %[l0] offset:17408\n\t" "v_smfmac_f32_16x16x64_bf16 v[224:227], v[196:199], a[80:87], %[ix]\n\t" \
    "ds_read_b128 v[248:251], %[l1] offset:1024\n\t" "ds_read_b128 v[252:255], %[l1] offset:5120\n\t" "v_smfmac_f32_16x16x64_bf16 v[228:231], v[196:199], a[88:95], %[ix]\n\t" \
    "ds_read_b128 v[192:195], %[l1] offset:14336\n\t" "v_smfmac_f32_16x16x64_bf16 v[232:235], v[196:199], v[72:79], %[ix]\n\t" \
    "v_smfmac_f32_16x16x64_bf16 v[236:239], v[196:199], v[168:175], %[ix]\n\t" "s_waitcnt lgkmcnt(5)\n\t" "v_cmp_eq_u32 vcc, %[rt], v218\n\t" \
    "v_smfmac_f32_16x16x64_bf16 v[224:227], v[240:243], a[96:103], %[ix]\n\t" "s_cbranch_vccnz 33f\n\t" "ds_read_b128 v[168:171], %[l0] offset:18432\n\t" \
    "v_smfmac_f32_16x16x64_bf16 v[228:231], v[240:243], a[104:111], %[ix]\n\t" "ds_read_b128 v[172:175], %[l0] offset:19456\n\t" \
    "ds_read_b128 v[196:199], %[l1] offset:2048\n\t" "v_smfmac_f32_16x16x64_bf16 v[232:235], v[240:243], v[80:87], %[ix]\n\t" \
    "buffer_load_dwordx4 v[16:19], %[hq], %[rs], %[qb] offen offset:0 sc1\n\t" "v_smfmac_f32_16x16x64_bf16 v[236:239], v[240:243], v[176:183], %[ix]\n\t" \
    "ds_read_b128 v[176:179], %[l0] offset:20480\n\t" "ds_read_b128 v[180:183], %[l0] offset:21504\n\t" \
    "v_smfmac_f32_16x16x64_bf16 v[224:227], v[244:247], a[112:119], %[ix]\n\t" "ds_read_b128 v[240:243], %[l1] offset:6144\n\t" \
    "v_smfmac_f32_16x16x64_bf16 v[228:231], v[244:247], a[120:127], %[ix]\n\t" "v_smfmac_f32_16x16x64_bf16 v[232:235], v[244:247], v[88:95], %[ix]\n\t" \
    "buffer_load_dwordx4 v[20:23], %[hq], %[rs], %[qb] offen offset:2048 sc1\n\t" "v_smfmac_f32_16x16x64_bf16 v[236:239], v[244:247], v[184:191], %[ix]\n\t" \
    "ds_read_b128 v[184:187], %[l0] offset:22528\n\t" "ds_read_b128 v[188:191], %[l0] offset:23552\n\t" "s_waitcnt lgkmcnt(10)\n\t" \
    "v_smfmac_f32_16x16x64_bf16 v[224:227], v[248:251], a[128:135], %[ix]\n\t" "ds_read_b128 v[244:247], %[l1] offset:15360\n\t" \
    "v_smfmac_f32_16x16x64_bf16 v[228:231], v[248:251], a[136:143], %[ix]\n\t" "v_smfmac_f32_16x16x64_bf16 v[232:235], v[248:251], v[96:103], %[ix]\n\t" \
    "buffer_load_dwordx4 v[24:27], %[hq], %[rs], s61 offen offset:0 sc1\n\t" "v_smfmac_f32_16x16x64_bf16 v[236:239], v[248:251], v[160:167], %[ix]\n\t" \
    "ds_read_b128 v[160:163], %[l0] offset:24576\n\t" "ds_read_b128 v[164:167], %[l0] offset:25600\n\t" "s_waitcnt lgkmcnt(12)\n\t" \
    "v_smfmac_f32_16x16x64_bf16 v[224:227], v[252:255], a[144:151], %[ix]\n\t" "ds_read_b128 v[248:251], %[l1] offset:3072\n\t" \
    "v_smfmac_f32_16x16x64_bf16 v[228:231], v[252:255], a[152:159], %[ix]\n\t" "v_smfmac_f32_16x16x64_bf16 v[232:235], v[252:255], v[104:111], %[ix]\n\t" \
    "buffer_load_dwordx4 v[28:31], %[hq], %[rs], s61 offen offset:2048 sc1\n\t" "s_waitcnt lgkmcnt(10)\n\t" \
    "v_smfmac_f32_16x16x64_bf16 v[236:239], v[252:255], v[168:175], %[ix]\n\t" "ds_read_b128 v[168:171], %[l0] offset:26624\n\t" \
    "ds_read_b128 v[172:175], %[l0] offset:27648\n\t" "v_smfmac_f32_16x16x64_bf16 v[224:227], v[192:195], a[160:167], %[ix]\n\t" \
    "ds_read_b128 v[252:255], %[l1] offset:7168\n\t" "v_smfmac_f32_16x16x64_bf16 v[228:231], v[192:195], a[168:175], %[ix]\n\t" "s_waitcnt lgkmcnt(12)\n\t" \
    "v_smfmac_f32_16x16x64_bf16 v[224:227], v[196:199], a[176:183], %[ix]\n\t" "v_smfmac_f32_16x16x64_bf16 v[228:231], v[196:199], a[184:191], %[ix]\n\t" \
    "s_waitcnt lgkmcnt(9)\n\t" "v_smfmac_f32_16x16x64_bf16 v[224:227], v[240:243], a[192:199], %[ix]\n\t" \
    "v_smfmac_f32_16x16x64_bf16 v[228:231], v[240:243], a[200:207], %[ix]\n\t" "s_waitcnt lgkmcnt(6)\n\t" \
    "v_smfmac_f32_16x16x64_bf16 v[224:227], v[244:247], a[208:215], %[ix]\n\t" "v_smfmac_f32_16x16x64_bf16 v[228:231], v[244:247], a[216:223], %[ix]\n\t" \
    "s_waitcnt vmcnt(4)\n\t" "s_waitcnt lgkmcnt(3)\n\t" "v_smfmac_f32_16x16x64_bf16 v[224:227], v[248:251], a[224:231], %[ix]\n\t" \
    "v_smfmac_f32_16x16x64_bf16 v[228:231], v[248:251], a[232:239], %[ix]\n\t" "s_waitcnt lgkmcnt(0)\n\t" \
    "v_smfmac_f32_16x16x64_bf16 v[224:227], v[252:255], a[240:247], %[ix]\n\t" "s_nop 1\n\t" "v_smfmac_f32_16x16x64_bf16 v[228:231], v[252:255], a[248:255], %[ix]\n\t" \
    XT(90, 91) "s_nop 1\n\t" "v_smfmac_f32_16x16x64_bf16 v[232:235], v[192:195], v[112:119], %[ix]\n\t" "s_nop 1\n\t" \
    "v_smfmac_f32_16x16x64_bf16 v[236:239], v[192:195], v[176:183], %[ix]\n\t" "ds_read_b128 v[176:179], %[l0] offset:28672\n\t" "v_add_f32 v224, v224, v225\n\t" \
    "v_add_f32 v226, v226, v227\n\t" "v_smfmac_f32_16x16x64_bf16 v[232:235], v[196:199], v[120:127], %[ix]\n\t" "ds_read_b128 v[180:183], %[l0] offset:29696\n\t" \
    "v_add_f32 v228, v228, v229\n\t" "v_add_f32 v230, v230, v231\n\t" "v_smfmac_f32_16x16x64_bf16 v[236:239], v[196:199], v[184:191], %[ix]\n\t" \
    "ds_read_b128 v[184:187], %[l0] offset:30720\n\t" "v_cvt_pk_bf16_f32 v218, v224, v228\n\t" "v_cvt_pk_bf16_f32 v219, v226, v230\n\t" \
    "v_smfmac_f32_16x16x64_bf16 v[232:235], v[240:243], v[128:135], %[ix]\n\t" "ds_write2_b32 %[qw], v218, v219 offset1:8\n\t" \
    "ds_read_b128 v[188:191], %[l0] offset:31744\n\t" "v_smfmac_f32_16x16x64_bf16 v[236:239], v[240:243], v[160:167], %[ix]\n\t" \
    "v_smfmac_f32_16x16x64_bf16 v[232:235], v[244:247], v[136:143], %[ix]\n\t" "v_smfmac_f32_16x16x64_bf16 v[236:239], v[244:247], v[168:175], %[ix]\n\t" \
    "s_waitcnt lgkmcnt(1)\n\t" "s_barrier\n\t" "v_smfmac_f32_16x16x64_bf16 v[232:235], v[248:251], v[144:151], %[ix]\n\t" \
    "v_smfmac_f32_16x16x64_bf16 v[236:239], v[248:251], v[176:183], %[ix]\n\t" "v_smfmac_f32_16x16x64_bf16 v[232:235], v[252:255], v[152:159], %[ix]\n\t" \
    "s_waitcnt lgkmcnt(0)\n\t" "v_smfmac_f32_16x16x64_bf16 v[236:239], v[252:255], v[184:191], %[ix]\n\t" XT(92, 93) "s_nop 6\n\t" "v_add_f32 v232, v232, v233\n\t" \
    "v_add_f32 v234, v234, v235\n\t" "v_add_f32 v236, v236, v237\n\t" "v_add_f32 v238, v238, v239\n\t" "v_cvt_pk_bf16_f32 v232, v232, v236\n\t" \
    "v_cvt_pk_bf16_f32 v233, v234, v238\n\t" "ds_write2_b32 %[kw], v232, v233 offset1:68\n\t" "s_waitcnt lgkmcnt(0)\n\t" "s_barrier\n\t" XT(94, 95) \
    "s_cmp_ge_u32 %[sq], %[sn]\n\t" "s_cbranch_scc1 377f\n\t" "375:\n\t" "global_load_dword v240, v210, %[fq] sc1\n\t" "s_waitcnt vmcnt(0)\n\t" \
    "v_readfirstlane_b32 %[sq], v240\n\t" "s_nop 3\n\t" "s_cmp_ge_u32 %[sq], %[sn]\n\t" "s_cbranch_scc1 376f\n\t" "s_cmp_eq_u32 %[bd], 0\n\t" "s_cbranch_scc1 376f\n\t" \
    "s_sub_u32 %[bd], %[bd], 1\n\t" "s_sleep 2\n\t" "s_branch 375b\n\t" "376:\n\t" "buffer_load_dwordx4 v[16:19], %[hq], %[rs], %[qb] offen offset:0 sc1\n\t" \
    "buffer_load_dwordx4 v[20:23], %[hq], %[rs], %[qb] offen offset:2048 sc1\n\t" "buffer_load_dwordx4 v[24:27], %[hq], %[rs], s61 offen offset:0 sc1\n\t" \
    "buffer_load_dwordx4 v[28:31], %[hq], %[rs], s61 offen offset:2048 sc1\n\t" "377:\n\t" "s_nop 3\n\t" "ds_read_b128 v[160:163], %[kx] offset:0\n\t" \
    "ds_read_b128 v[164:167], %[kx] offset:2048\n\t" "ds_read_b128 v[168:171], %[kx] offset:4096\n\t" "ds_read_b128 v[172:175], %[kx] offset:6144\n\t" \
    "ds_read_b128 v[176:179], %[kx] offset:256\n\t" "ds_read_b128 v[180:183], %[kx] offset:2304\n\t" "ds_read_b128 v[184:187], %[kx] offset:4352\n\t" \
    "ds_read_b128 v[188:191], %[kx] offset:6400\n\t" "ds_read_b128 v[224:227], %[km] offset:0\n\t" "ds_read_b128 v[228:231], %[km] offset:64\n\t" \
    "ds_read_b128 v[232:235], %[km] offset:128\n\t" "ds_read_b128 v[236:239], %[km] offset:192\n\t" "s_waitcnt vmcnt(0)\n\t" "global_load_dword v240, v210, %[fq] sc1\n\t" \
    "s_waitcnt lgkmcnt(11)\n\t" "v_mfma_f32_16x16x32_bf16 v[248:251], v[160:163], v[16:19], 0\n\t" "s_waitcnt lgkmcnt(10)\n\t" \
    "v_mfma_f32_16x16x32_bf16 v[248:251], v[164:167], v[20:23], v[248:251]\n\t" "s_waitcnt lgkmcnt(9)\n\t" \
    "v_mfma_f32_16x16x32_bf16 v[248:251], v[168:171], v[24:27], v[248:251]\n\t" "s_waitcnt lgkmcnt(8)\n\t" \
    "v_mfma_f32_16x16x32_bf16 v[248:251], v[172:175], v[28:31], v[248:251]\n\t" "s_waitcnt lgkmcnt(7)\n\t" \
    "v_mfma_f32_16x16x32_bf16 v[252:255], v[176:179], v[16:19], v[200:203]\n\t" "s_waitcnt lgkmcnt(6)\n\t" \
    "v_mfma_f32_16x16x32_bf16 v[252:255], v[180:183], v[20:23], v[252:255]\n\t" "s_waitcnt lgkmcnt(5)\n\t" \
    "v_mfma_f32_16x16x32_bf16 v[252:255], v[184:187], v[24:27], v[252:255]\n\t" "s_waitcnt lgkmcnt(4)\n\t" \
    "v_mfma_f32_16x16x32_bf16 v[252:255], v[188:191], v[28:31], v[252:255]\n\t" "s_waitcnt lgkmcnt(3)\n\t" \
    "v_mfma_f32_16x16x32_bf16 v[244:247], v[224:227], v[16:19], v[204:207]\n\t" "s_waitcnt lgkmcnt(2)\n\t" \
    "v_mfma_f32_16x16x32_bf16 v[244:247], v[228:231], v[20:23], v[244:247]\n\t" "s_waitcnt lgkmcnt(1)\n\t" \
    "v_mfma_f32_16x16x32_bf16 v[244:247], v[232:235], v[24:27], v[244:247]\n\t" "s_waitcnt lgkmcnt(0)\n\t" \
    "v_mfma_f32_16x16x32_bf16 v[244:247], v[236:239], v[28:31], v[244:247]\n\t" "s_nop 3\n\t" "v_max3_f32 v242, v248, v249, v250\n\t" "v_max3_f32 v242, v242, v251, v252\n\t" \
    "v_max3_f32 v242, v242, v253, v254\n\t" "v_max_f32 v242, v242, v255\n\t" "v_max3_f32 v242, v242, v244, v245\n\t" "v_max3_f32 v242, v242, v246, v247\n\t" \
    "v_mov_b32 v243, v242\n\t" "s_nop 1\n\t" "v_permlane32_swap_b32 v242, v243\n\t" "v_max_f32 v242, v242, v243\n\t" "v_mov_b32 v243, v242\n\t" "s_nop 1\n\t" \
    "v_permlane16_swap_b32 v242, v243\n\t" "v_max_f32 v242, v242, v243\n\t" "v_pk_add_f32 v[244:245], v[244:245], v[242:243] op_sel_hi:[1,0] neg_lo:[0,1] neg_hi:[0,1]\n\t" \
    "v_pk_add_f32 v[246:247], v[246:247], v[242:243] op_sel_hi:[1,0] neg_lo:[0,1] neg_hi:[0,1]\n\t" \
    "v_pk_add_f32 v[248:249], v[248:249], v[242:243] op_sel_hi:[1,0] neg_lo:[0,1] neg_hi:[0,1]\n\t" \
    "v_pk_add_f32 v[250:251], v[250:251], v[242:243] op_sel_hi:[1,0] neg_lo:[0,1] neg_hi:[0,1]\n\t" \
    "v_pk_add_f32 v[252:253], v[252:253], v[242:243] op_sel_hi:[1,0] neg_lo:[0,1] neg_hi:[0,1]\n\t" \
    "v_pk_add_f32 v[254:255], v[254:255], v[242:243] op_sel_hi:[1,0] neg_lo:[0,1] neg_hi:[0,1]\n\t" "v_pk_mul_f32 v[244:245], v[244:245], s[66:67]\n\t" \
    "v_pk_mul_f32 v[246:247], v[246:247], s[66:67]\n\t" "v_pk_mul_f32 v[248:249], v[248:249], s[66:67]\n\t" "v_pk_mul_f32 v[250:251], v[250:251], s[66:67]\n\t" \
    "v_pk_mul_f32 v[252:253], v[252:253], s[66:67]\n\t" "v_pk_mul_f32 v[254:255], v[254:255], s[66:67]\n\t" "v_exp_f32 v244, v244\n\t" "v_exp_f32 v245, v245\n\t" \
    "v_exp_f32 v246, v246\n\t" "v_exp_f32 v247, v247\n\t" "v_exp_f32 v248, v248\n\t" "v_exp_f32 v249, v249\n\t" "v_exp_f32 v250, v250\n\t" "v_exp_f32 v251, v251\n\t" \
    "v_exp_f32 v252, v252\n\t" "v_exp_f32 v253, v253\n\t" "v_exp_f32 v254, v254\n\t" "v_exp_f32 v255, v255\n\t" "v_pk_add_f32 v[242:243], v[244:245], v[246:247]\n\t" \
    "v_pk_add_f32 v[242:243], v[242:243], v[248:249]\n\t" "v_pk_add_f32 v[242:243], v[242:243], v[250:251]\n\t" "v_pk_add_f32 v[242:243], v[242:243], v[252:253]\n\t" \
    "v_pk_add_f32 v[242:243], v[242:243], v[254:255]\n\t" "v_add_f32 v242, v242, v243\n\t" "v_mov_b32 v243, v242\n\t" "s_nop 1\n\t" "v_permlane32_swap_b32 v242, v243\n\t" \
    "v_add_f32 v242, v242, v243\n\t" "v_mov_b32 v243, v242\n\t" "s_nop 1\n\t" "v_permlane16_swap_b32 v242, v243\n\t" "v_add_f32 v242, v242, v243\n\t" \
    "v_rcp_f32 v242, v242\n\t" "s_nop 0\n\t" "v_pk_mul_f32 v[244:245], v[244:245], v[242:243] op_sel_hi:[1,0]\n\t" \
    "v_pk_mul_f32 v[246:247], v[246:247], v[242:243] op_sel_hi:[1,0]\n\t" "v_pk_mul_f32 v[248:249], v[248:249], v[242:243] op_sel_hi:[1,0]\n\t" \
    "v_pk_mul_f32 v[250:251], v[250:251], v[242:243] op_sel_hi:[1,0]\n\t" "v_pk_mul_f32 v[252:253], v[252:253], v[242:243] op_sel_hi:[1,0]\n\t" \
    "v_pk_mul_f32 v[254:255], v[254:255], v[242:243] op_sel_hi:[1,0]\n\t" "v_cvt_pk_bf16_f32 v162, v244, v245\n\t" "v_cvt_pk_bf16_f32 v163, v246, v247\n\t" \
    "v_cvt_pk_bf16_f32 v164, v248, v249\n\t" "v_cvt_pk_bf16_f32 v165, v250, v251\n\t" "v_cvt_pk_bf16_f32 v166, v252, v253\n\t" "v_cvt_pk_bf16_f32 v167, v254, v255\n\t" \
    "buffer_store_dwordx4 v[164:167], %[px], %[rs], %[tb] offen sc1\n\t" "buffer_store_dwordx2 v[162:163], %[pm], %[rs], %[tb] offen sc1\n\t" "s_waitcnt vmcnt(2)\n\t" \
    "v_readfirstlane_b32 s62, v240\n\t" "s_nop 3\n\t" "s_max_u32 %[sq], %[sq], s62\n\t" "s_barrier\n\t" XT(96, 97) "s_branch 99f\n\t" "31:\n\t" \
    "ds_read_b128 v[220:223], %[l1] offset:8192\n\t" "s_waitcnt lgkmcnt(0)\n\t" "v_max3_u32 v218, v220, v221, v222\n\t" "v_max_u32 v218, v218, v223\n\t" \
    "ds_read_b128 v[220:223], %[l1] offset:9216\n\t" "s_waitcnt lgkmcnt(0)\n\t" "v_max3_u32 v218, v218, v220, v221\n\t" "v_max3_u32 v218, v218, v222, v223\n\t" \
    "ds_read_b128 v[220:223], %[l1] offset:10240\n\t" "s_waitcnt lgkmcnt(0)\n\t" "v_max3_u32 v218, v218, v220, v221\n\t" "v_max3_u32 v218, v218, v222, v223\n\t" \
    "ds_read_b128 v[220:223], %[l1] offset:11264\n\t" "s_waitcnt lgkmcnt(0)\n\t" "v_max3_u32 v218, v218, v220, v221\n\t" "v_max3_u32 v218, v218, v222, v223\n\t" \
    "v_cmp_eq_u32 vcc, -1, v218\n\t" "s_cbranch_vccz 32b\n\t" "s_cmp_eq_u32 %[rt], 1\n\t" "s_cbranch_scc1 32b\n\t" "v_mov_b32 v218, %[rt]\n\t" "ds_write_b32 v219, v218\n\t" \
    "s_waitcnt lgkmcnt(0)\n\t" "s_branch 32b\n\t" "33:\n\t" "s_sub_u32 %[rt], %[rt], 1\n\t" "s_waitcnt lgkmcnt(0)\n\t" XCNT(100) "34:\n\t" \
    "buffer_load_dwordx4 v[240:243], %[mo], %[rs], %[sb] offen sc1\n\t" "buffer_load_dwordx4 v[244:247], v216, %[rs], %[sb] offen sc1\n\t" \
    "buffer_load_dwordx4 v[248:251], %[mo], %[rs], s60 offen sc1\n\t" "buffer_load_dwordx4 v[252:255], v216, %[rs], s60 offen sc1\n\t" "s_waitcnt vmcnt(0)\n\t" \
    "v_max_u32 v218, v240, v241\n\t" "v_max3_u32 v218, v218, v242, v243\n\t" "v_max3_u32 v218, v218, v244, v245\n\t" "v_max3_u32 v218, v218, v246, v247\n\t" \
    "v_max3_u32 v218, v218, v248, v249\n\t" "v_max3_u32 v218, v218, v250, v251\n\t" "v_max3_u32 v218, v218, v252, v253\n\t" "v_max3_u32 v218, v218, v254, v255\n\t" \
    "v_cmp_eq_u32 vcc, -1, v218\n\t" "s_cbranch_vccz 35f\n\t" "s_cmp_eq_u32 %[bd], 0\n\t" "s_cbranch_scc1 35f\n\t" "s_sub_u32 %[bd], %[bd], 1\n\t" XCNT(101) \
    "s_branch 34b\n\t" "35:\n\t" "ds_read_b128 v[160:163], %[l0] offset:0\n\t" "ds_read_b128 v[164:167], %[l0] offset:1024\n\t" \
    "ds_read_b128 v[168:171], %[l0] offset:2048\n\t" "ds_read_b128 v[172:175], %[l0] offset:3072\n\t" "ds_read_b128 v[176:179], %[l0] offset:4096\n\t" \
    "ds_read_b128 v[180:183], %[l0] offset:5120\n\t" "ds_read_b128 v[184:187], %[l0] offset:6144\n\t" "ds_read_b128 v[188:191], %[l0] offset:7168\n\t" \
    "v_mov_b64 v[224:225], 0\n\t" "v_mov_b64 v[226:227], 0\n\t" "v_mov_b64 v[228:229], 0\n\t" "v_mov_b64 v[230:231], 0\n\t" "v_mov_b64 v[232:233], 0\n\t" \
    "v_mov_b64 v[234:235], 0\n\t" "v_mov_b64 v[236:237], 0\n\t" "v_mov_b64 v[238:239], 0\n\t" "s_barrier\n\t" "s_branch 30b\n\t" "99:\n\t"
#define QK_STEP3 \
    ".p2align 6\n\t" "v_xor_b32 v216, 0x400, %[mo]\n\t" "s_xor_b32 s60, %[sb], 0x800\n\t" "s_mov_b32 s67, %[pb]\n\t" XT(80, 81) "s_getpc_b64 s[62:63]\n\t" "48:\n\t" \
    "s_memtime s[64:65]\n\t" "s_waitcnt lgkmcnt(0)\n\t" "s_sub_u32 s61, %[tg], s64\n\t" "s_cmp_gt_i32 s61, 124\n\t" "s_cbranch_scc1 48b\n\t" "s_max_i32 s61, s61, 0\n\t" \
    "s_and_b32 s61, s61, 124\n\t" "s_sub_u32 s61, 49f-48b, s61\n\t" "s_add_u32 s62, s62, s61\n\t" "s_addc_u32 s63, s63, 0\n\t" "s_setpc_b64 s[62:63]\n\t" ".rept 31\n\t" \
    "s_nop 0\n\t" ".endr\n\t" "49:\n\t" XT(98, 99) "s_barrier\n\t" XT(82, 83) "buffer_load_dwordx4 v[240:243], %[mo], %[rs], %[sb] offen sc1\n\t" \
    "v_mov_b64 v[224:225], 0\n\t" "v_mov_b64 v[226:227], 0\n\t" "v_mov_b64 v[228:229], 0\n\t" "v_mov_b64 v[230:231], 0\n\t" "v_mov_b64 v[232:233], 0\n\t" \
    "v_mov_b64 v[234:235], 0\n\t" "v_mov_b64 v[236:237], 0\n\t" "v_mov_b64 v[238:239], 0\n\t" "buffer_load_dwordx4 v[244:247], v216, %[rs], %[sb] offen sc1\n\t" \
    "ds_read_b128 v[160:163], %[l0] offset:0\n\t" "ds_read_b128 v[164:167], %[l0] offset:1024\n\t" "ds_read_b128 v[168:171], %[l0] offset:2048\n\t" \
    "ds_read_b128 v[172:175], %[l0] offset:3072\n\t" "s_nop 3\n\t" "buffer_load_dwordx4 v[248:251], %[mo], %[rs], s60 offen sc1\n\t" \
    "ds_read_b128 v[176:179], %[l0] offset:4096\n\t" "ds_read_b128 v[180:183], %[l0] offset:5120\n\t" "ds_read_b128 v[184:187], %[l0] offset:6144\n\t" \
    "ds_read_b128 v[188:191], %[l0] offset:7168\n\t" "s_nop 3\n\t" "buffer_load_dwordx4 v[252:255], v216, %[rs], s60 offen sc1\n\t" "v_mov_b32 v219, 160512\n\t" \
    "v_mov_b32 v220, -1\n\t" "buffer_store_dword v220, %[hq], %[rs], %[pr] offen sc1\n\t" "40:\n\t" "s_waitcnt vmcnt(4)\n\t" XT(84, 85) \
    "v_smfmac_f32_16x16x64_bf16 v[224:227], v[240:243], a[0:7], %[ix]\n\t" "ds_write_b128 %[l1], v[240:243] offset:12288\n\t" \
    "v_smfmac_f32_16x16x64_bf16 v[228:231], v[240:243], a[8:15], %[ix]\n\t" "v_smfmac_f32_16x16x64_bf16 v[232:235], v[240:243], v[32:39], %[ix]\n\t" \
    "s_waitcnt lgkmcnt(7)\n\t" "v_smfmac_f32_16x16x64_bf16 v[236:239], v[240:243], v[160:167], %[ix]\n\t" "ds_read_b128 v[160:163], %[l0] offset:8192\n\t" \
    "ds_read_b128 v[164:167], %[l0] offset:9216\n\t" "s_waitcnt vmcnt(3)\n\t" "v_smfmac_f32_16x16x64_bf16 v[224:227], v[244:247], a[16:23], %[ix]\n\t" \
    "ds_write_b128 %[l1], v[244:247] offset:13312\n\t" "v_smfmac_f32_16x16x64_bf16 v[228:231], v[244:247], a[24:31], %[ix]\n\t" \
    "v_smfmac_f32_16x16x64_bf16 v[232:235], v[244:247], v[40:47], %[ix]\n\t" "s_waitcnt lgkmcnt(8)\n\t" \
    "v_smfmac_f32_16x16x64_bf16 v[236:239], v[244:247], v[168:175], %[ix]\n\t" "ds_read_b128 v[168:171], %[l0] offset:10240\n\t" \
    "ds_read_b128 v[172:175], %[l0] offset:11264\n\t" "s_waitcnt vmcnt(2)\n\t" "v_smfmac_f32_16x16x64_bf16 v[224:227], v[248:251], a[32:39], %[ix]\n\t" \
    "ds_write_b128 %[l1], v[248:251] offset:14336\n\t" "v_smfmac_f32_16x16x64_bf16 v[228:231], v[248:251], a[40:47], %[ix]\n\t" "s_waitcnt lgkmcnt(3)\n\t" "s_barrier\n\t" \
    XT(86, 87) "v_smfmac_f32_16x16x64_bf16 v[232:235], v[248:251], v[48:55], %[ix]\n\t" "ds_read_b128 v[192:195], %[l1] offset:0\n\t" \
    "ds_read_b128 v[196:199], %[l1] offset:4096\n\t" "v_smfmac_f32_16x16x64_bf16 v[236:239], v[248:251], v[176:183], %[ix]\n\t" \
    "ds_read_b128 v[176:179], %[l0] offset:12288\n\t" "ds_read_b128 v[180:183], %[l0] offset:13312\n\t" "s_waitcnt vmcnt(1)\n\t" \
    "v_smfmac_f32_16x16x64_bf16 v[224:227], v[252:255], a[48:55], %[ix]\n\t" "ds_write_b128 %[l1], v[252:255] offset:15360\n\t" \
    "v_smfmac_f32_16x16x64_bf16 v[228:231], v[252:255], a[56:63], %[ix]\n\t" "ds_read_b128 v[240:243], %[l1] offset:8192\n\t" "ds_read_b128 v[244:247], %[l1] offset:1024\n\t" \
    "v_smfmac_f32_16x16x64_bf16 v[232:235], v[252:255], v[56:63], %[ix]\n\t" "s_nop 1\n\t" "v_smfmac_f32_16x16x64_bf16 v[236:239], v[252:255], v[184:191], %[ix]\n\t" \
    "ds_read_b128 v[184:187], %[l0] offset:14336\n\t" "v_cmp_u_f32 vcc, v224, v225\n\t" "v_cmp_u_f32 s[62:63], v226, v227\n\t" "s_waitcnt lgkmcnt(7)\n\t" \
    "v_smfmac_f32_16x16x64_bf16 v[224:227], v[192:195], a[64:71], %[ix]\n\t" "ds_read_b128 v[188:191], %[l0] offset:15360\n\t" \
    "v_smfmac_f32_16x16x64_bf16 v[228:231], v[192:195], a[72:79], %[ix]\n\t" "s_or_b64 vcc, vcc, s[62:63]\n\t" "s_cbranch_scc1 41f\n\t" "42:\n\t" \
    "v_smfmac_f32_16x16x64_bf16 v[232:235], v[192:195], v[64:71], %[ix]\n\t" "s_waitcnt lgkmcnt(4)\n\t" "s_barrier\n\t" XT(88, 89) "ds_read_b32 v218, v219\n\t" \
    "v_smfmac_f32_16x16x64_bf16 v[236:239], v[192:195], v[160:167], %[ix]\n\t" "s_memtime s[64:65]\n\t" "ds_read_b128 v[160:163], %[l0] offset:16384\n\t" \
    "v_smfmac_f32_16x16x64_bf16 v[224:227], v[196:199], a[80:87], %[ix]\n\t" "ds_read_b128 v[164:167], %[l0] offset:17408\n\t" \
    "ds_read_b128 v[248:251], %[l1] offset:5120\n\t" "v_smfmac_f32_16x16x64_bf16 v[228:231], v[196:199], a[88:95], %[ix]\n\t" "ds_read_b128 v[252:255], %[l1] offset:9216\n\t" \
    "ds_read_b128 v[192:195], %[l1] offset:2048\n\t" "v_smfmac_f32_16x16x64_bf16 v[232:235], v[196:199], v[72:79], %[ix]\n\t" "s_sub_u32 s62, s64, %[ca]\n\t" \
    "s_mul_hi_u32 s63, s62, %[k1]\n\t" "s_mul_i32 s62, s62, %[k1]\n\t" "v_smfmac_f32_16x16x64_bf16 v[236:239], v[196:199], v[168:175], %[ix]\n\t" "s_waitcnt lgkmcnt(5)\n\t" \
    "v_cmp_eq_u32 vcc, %[rt], v218\n\t" "s_lshr_b64 s[62:63], s[62:63], 16\n\t" "s_add_u32 s62, s62, %[ga]\n\t" "s_bitset0_b32 s62, 0\n\t" \
    "v_smfmac_f32_16x16x64_bf16 v[224:227], v[240:243], a[96:103], %[ix]\n\t" "s_cbranch_vccnz 43f\n\t" "ds_read_b128 v[168:171], %[l0] offset:18432\n\t" \
    "v_smfmac_f32_16x16x64_bf16 v[228:231], v[240:243], a[104:111], %[ix]\n\t" "ds_read_b128 v[172:175], %[l0] offset:19456\n\t" "v_mov_b32 v217, s62\n\t" \
    "buffer_store_dword v217, %[hq], %[rs], s67 offen sc1\n\t" "v_smfmac_f32_16x16x64_bf16 v[232:235], v[240:243], v[80:87], %[ix]\n\t" \
    "ds_read_b128 v[196:199], %[l1] offset:6144\n\t" "v_smfmac_f32_16x16x64_bf16 v[236:239], v[240:243], v[176:183], %[ix]\n\t" \
    "ds_read_b128 v[176:179], %[l0] offset:20480\n\t" "ds_read_b128 v[180:183], %[l0] offset:21504\n\t" \
    "v_smfmac_f32_16x16x64_bf16 v[224:227], v[244:247], a[112:119], %[ix]\n\t" "ds_read_b128 v[240:243], %[l1] offset:10240\n\t" \
    "v_smfmac_f32_16x16x64_bf16 v[228:231], v[244:247], a[120:127], %[ix]\n\t" "v_smfmac_f32_16x16x64_bf16 v[232:235], v[244:247], v[88:95], %[ix]\n\t" \
    "v_smfmac_f32_16x16x64_bf16 v[236:239], v[244:247], v[184:191], %[ix]\n\t" "ds_read_b128 v[184:187], %[l0] offset:22528\n\t" \
    "ds_read_b128 v[188:191], %[l0] offset:23552\n\t" "s_waitcnt lgkmcnt(10)\n\t" "v_smfmac_f32_16x16x64_bf16 v[224:227], v[248:251], a[128:135], %[ix]\n\t" \
    "ds_read_b128 v[244:247], %[l1] offset:3072\n\t" "v_smfmac_f32_16x16x64_bf16 v[228:231], v[248:251], a[136:143], %[ix]\n\t" \
    "v_smfmac_f32_16x16x64_bf16 v[232:235], v[248:251], v[96:103], %[ix]\n\t" "v_smfmac_f32_16x16x64_bf16 v[236:239], v[248:251], v[160:167], %[ix]\n\t" \
    "ds_read_b128 v[160:163], %[l0] offset:24576\n\t" "ds_read_b128 v[164:167], %[l0] offset:25600\n\t" "s_waitcnt lgkmcnt(12)\n\t" \
    "v_smfmac_f32_16x16x64_bf16 v[224:227], v[252:255], a[144:151], %[ix]\n\t" "ds_read_b128 v[248:251], %[l1] offset:7168\n\t" \
    "v_smfmac_f32_16x16x64_bf16 v[228:231], v[252:255], a[152:159], %[ix]\n\t" "v_smfmac_f32_16x16x64_bf16 v[232:235], v[252:255], v[104:111], %[ix]\n\t" \
    "s_waitcnt lgkmcnt(10)\n\t" "v_smfmac_f32_16x16x64_bf16 v[236:239], v[252:255], v[168:175], %[ix]\n\t" "ds_read_b128 v[168:171], %[l0] offset:26624\n\t" \
    "ds_read_b128 v[172:175], %[l0] offset:27648\n\t" "v_smfmac_f32_16x16x64_bf16 v[224:227], v[192:195], a[160:167], %[ix]\n\t" \
    "ds_read_b128 v[252:255], %[l1] offset:11264\n\t" "v_smfmac_f32_16x16x64_bf16 v[228:231], v[192:195], a[168:175], %[ix]\n\t" "s_waitcnt lgkmcnt(12)\n\t" \
    "v_smfmac_f32_16x16x64_bf16 v[224:227], v[196:199], a[176:183], %[ix]\n\t" "v_smfmac_f32_16x16x64_bf16 v[228:231], v[196:199], a[184:191], %[ix]\n\t" \
    "s_waitcnt lgkmcnt(9)\n\t" "v_smfmac_f32_16x16x64_bf16 v[224:227], v[240:243], a[192:199], %[ix]\n\t" \
    "v_smfmac_f32_16x16x64_bf16 v[228:231], v[240:243], a[200:207], %[ix]\n\t" "s_waitcnt lgkmcnt(6)\n\t" \
    "v_smfmac_f32_16x16x64_bf16 v[224:227], v[244:247], a[208:215], %[ix]\n\t" "v_smfmac_f32_16x16x64_bf16 v[228:231], v[244:247], a[216:223], %[ix]\n\t" \
    "s_waitcnt lgkmcnt(3)\n\t" "v_smfmac_f32_16x16x64_bf16 v[224:227], v[248:251], a[224:231], %[ix]\n\t" \
    "v_smfmac_f32_16x16x64_bf16 v[228:231], v[248:251], a[232:239], %[ix]\n\t" "s_waitcnt lgkmcnt(0)\n\t" \
    "v_smfmac_f32_16x16x64_bf16 v[224:227], v[252:255], a[240:247], %[ix]\n\t" "s_nop 1\n\t" "v_smfmac_f32_16x16x64_bf16 v[228:231], v[252:255], a[248:255], %[ix]\n\t" \
    XT(90, 91) "s_nop 1\n\t" "v_smfmac_f32_16x16x64_bf16 v[232:235], v[192:195], v[112:119], %[ix]\n\t" "s_nop 1\n\t" \
    "v_smfmac_f32_16x16x64_bf16 v[236:239], v[192:195], v[176:183], %[ix]\n\t" "ds_read_b128 v[176:179], %[l0] offset:28672\n\t" "v_add_f32 v224, v224, v225\n\t" \
    "v_add_f32 v226, v226, v227\n\t" "v_smfmac_f32_16x16x64_bf16 v[232:235], v[196:199], v[120:127], %[ix]\n\t" "ds_read_b128 v[180:183], %[l0] offset:29696\n\t" \
    "v_add_f32 v228, v228, v229\n\t" "v_add_f32 v230, v230, v231\n\t" "v_smfmac_f32_16x16x64_bf16 v[236:239], v[196:199], v[184:191], %[ix]\n\t" \
    "ds_read_b128 v[184:187], %[l0] offset:30720\n\t" "v_cvt_pk_bf16_f32 v218, v224, v228\n\t" "v_cvt_pk_bf16_f32 v219, v226, v230\n\t" \
    "v_smfmac_f32_16x16x64_bf16 v[232:235], v[240:243], v[128:135], %[ix]\n\t" "ds_write2_b32 %[qw], v218, v219 offset1:8\n\t" \
    "ds_read_b128 v[188:191], %[l0] offset:31744\n\t" "v_smfmac_f32_16x16x64_bf16 v[236:239], v[240:243], v[160:167], %[ix]\n\t" \
    "v_smfmac_f32_16x16x64_bf16 v[232:235], v[244:247], v[136:143], %[ix]\n\t" "v_smfmac_f32_16x16x64_bf16 v[236:239], v[244:247], v[168:175], %[ix]\n\t" \
    "s_waitcnt lgkmcnt(1)\n\t" "s_barrier\n\t" "v_smfmac_f32_16x16x64_bf16 v[232:235], v[248:251], v[144:151], %[ix]\n\t" \
    "v_smfmac_f32_16x16x64_bf16 v[236:239], v[248:251], v[176:183], %[ix]\n\t" "v_smfmac_f32_16x16x64_bf16 v[232:235], v[252:255], v[152:159], %[ix]\n\t" \
    "s_waitcnt lgkmcnt(0)\n\t" "v_smfmac_f32_16x16x64_bf16 v[236:239], v[252:255], v[184:191], %[ix]\n\t" XT(92, 93) "s_nop 6\n\t" "v_add_f32 v232, v232, v233\n\t" \
    "v_add_f32 v234, v234, v235\n\t" "v_add_f32 v236, v236, v237\n\t" "v_add_f32 v238, v238, v239\n\t" "v_cvt_pk_bf16_f32 v232, v232, v236\n\t" \
    "v_cvt_pk_bf16_f32 v233, v234, v238\n\t" "ds_write2_b32 %[kw], v232, v233 offset1:68\n\t" "s_waitcnt lgkmcnt(0)\n\t" "s_barrier\n\t" XT(94, 95) XT(96, 97) \
    "s_branch 99f\n\t" "41:\n\t" "ds_read_b128 v[220:223], %[l1] offset:12288\n\t" "s_waitcnt lgkmcnt(0)\n\t" "v_max3_u32 v218, v220, v221, v222\n\t" \
    "v_max_u32 v218, v218, v223\n\t" "ds_read_b128 v[220:223], %[l1] offset:13312\n\t" "s_waitcnt lgkmcnt(0)\n\t" "v_max3_u32 v218, v218, v220, v221\n\t" \
    "v_max3_u32 v218, v218, v222, v223\n\t" "ds_read_b128 v[220:223], %[l1] offset:14336\n\t" "s_waitcnt lgkmcnt(0)\n\t" "v_max3_u32 v218, v218, v220, v221\n\t" \
    "v_max3_u32 v218, v218, v222, v223\n\t" "ds_read_b128 v[220:223], %[l1] offset:15360\n\t" "s_waitcnt lgkmcnt(0)\n\t" "v_max3_u32 v218, v218, v220, v221\n\t" \
    "v_max3_u32 v218, v218, v222, v223\n\t" "v_cmp_eq_u32 vcc, -1, v218\n\t" "s_cbranch_vccz 42b\n\t" "s_cmp_eq_u32 %[rt], 1\n\t" "s_cbranch_scc1 42b\n\t" \
    "v_mov_b32 v218, %[rt]\n\t" "ds_write_b32 v219, v218\n\t" "s_waitcnt lgkmcnt(0)\n\t" "s_branch 42b\n\t" "43:\n\t" "s_sub_u32 %[rt], %[rt], 1\n\t" \
    "s_waitcnt lgkmcnt(0)\n\t" XCNT(100) "44:\n\t" "buffer_load_dwordx4 v[240:243], %[mo], %[rs], %[sb] offen sc1\n\t" \
    "buffer_load_dwordx4 v[244:247], v216, %[rs], %[sb] offen sc1\n\t" "buffer_load_dwordx4 v[248:251], %[mo], %[rs], s60 offen sc1\n\t" \
    "buffer_load_dwordx4 v[252:255], v216, %[rs], s60 offen sc1\n\t" "s_waitcnt vmcnt(0)\n\t" "v_max_u32 v218, v240, v241\n\t" "v_max3_u32 v218, v218, v242, v243\n\t" \
    "v_max3_u32 v218, v218, v244, v245\n\t" "v_max3_u32 v218, v218, v246, v247\n\t" "v_max3_u32 v218, v218, v248, v249\n\t" "v_max3_u32 v218, v218, v250, v251\n\t" \
    "v_max3_u32 v218, v218, v252, v253\n\t" "v_max3_u32 v218, v218, v254, v255\n\t" "v_cmp_eq_u32 vcc, -1, v218\n\t" "s_cbranch_vccz 45f\n\t" "s_cmp_eq_u32 %[bd], 0\n\t" \
    "s_cbranch_scc1 45f\n\t" "s_sub_u32 %[bd], %[bd], 1\n\t" XCNT(101) "s_branch 44b\n\t" "45:\n\t" "ds_read_b128 v[160:163], %[l0] offset:0\n\t" \
    "ds_read_b128 v[164:167], %[l0] offset:1024\n\t" "ds_read_b128 v[168:171], %[l0] offset:2048\n\t" "ds_read_b128 v[172:175], %[l0] offset:3072\n\t" \
    "ds_read_b128 v[176:179], %[l0] offset:4096\n\t" "ds_read_b128 v[180:183], %[l0] offset:5120\n\t" "ds_read_b128 v[184:187], %[l0] offset:6144\n\t" \
    "ds_read_b128 v[188:191], %[l0] offset:7168\n\t" "v_mov_b64 v[224:225], 0\n\t" "v_mov_b64 v[226:227], 0\n\t" "v_mov_b64 v[228:229], 0\n\t" "v_mov_b64 v[230:231], 0\n\t" \
    "v_mov_b64 v[232:233], 0\n\t" "v_mov_b64 v[234:235], 0\n\t" "v_mov_b64 v[236:237], 0\n\t" "v_mov_b64 v[238:239], 0\n\t" "s_barrier\n\t" "s_memtime s[64:65]\n\t" \
    "s_waitcnt lgkmcnt(0)\n\t" "s_add_u32 s64, s64, %[dr]\n\t" "s_sub_u32 s62, s64, %[ca]\n\t" "s_mul_hi_u32 s63, s62, %[k1]\n\t" "s_mul_i32 s62, s62, %[k1]\n\t" \
    "s_lshr_b64 s[62:63], s[62:63], 16\n\t" "s_add_u32 s62, s62, %[ga]\n\t" "s_bitset0_b32 s62, 0\n\t" "v_mov_b32 v218, s62\n\t" \
    "buffer_store_dword v218, %[hq], %[rs], s67 offen sc1\n\t" "s_mov_b32 s67, 0x7f000000\n\t" "s_branch 40b\n\t" "99:\n\t" "mk_tail:\n\t"
#define VO_STEP \
    ".p2align 6\n\t" "v_xor_b32 v224, 0x400, %[mo]\n\t" "s_xor_b32 s60, %[sb], 0x800\n\t" "s_mov_b32 s67, %[pb]\n\t" "s_cmp_eq_u32 %[w], 1\n\t" "s_cbranch_scc1 91f\n\t" \
    "s_cmp_eq_u32 %[w], 2\n\t" "s_cbranch_scc1 92f\n\t" "s_cmp_eq_u32 %[w], 3\n\t" "s_cbranch_scc1 93f\n\t" XT(80, 81) "s_getpc_b64 s[62:63]\n\t" "18:\n\t" \
    "s_memtime s[64:65]\n\t" "s_waitcnt lgkmcnt(0)\n\t" "s_sub_u32 s61, %[tg], s64\n\t" "s_cmp_gt_i32 s61, 124\n\t" "s_cbranch_scc1 18b\n\t" "s_max_i32 s61, s61, 0\n\t" \
    "s_and_b32 s61, s61, 124\n\t" "s_sub_u32 s61, 19f-18b, s61\n\t" "s_add_u32 s62, s62, s61\n\t" "s_addc_u32 s63, s63, 0\n\t" "s_setpc_b64 s[62:63]\n\t" ".rept 31\n\t" \
    "s_nop 0\n\t" ".endr\n\t" "19:\n\t" "s_barrier\n\t" XT(82, 83) "buffer_load_dwordx4 v[240:243], %[mo], %[rs], %[sb] offen sc1\n\t" "v_mov_b64 v[232:233], 0\n\t" \
    "v_mov_b64 v[234:235], 0\n\t" "v_mov_b64 v[236:237], 0\n\t" "v_mov_b64 v[238:239], 0\n\t" "v_mov_b64 v[192:193], 0\n\t" "v_mov_b64 v[194:195], 0\n\t" \
    "v_mov_b64 v[196:197], 0\n\t" "v_mov_b64 v[198:199], 0\n\t" "buffer_load_dwordx4 v[244:247], v224, %[rs], %[sb] offen sc1\n\t" \
    "ds_read_b128 v[160:163], %[l0] offset:0\n\t" "ds_read_b128 v[164:167], %[l0] offset:1024\n\t" "ds_read_b128 v[168:171], %[l0] offset:2048\n\t" \
    "ds_read_b128 v[172:175], %[l0] offset:3072\n\t" "s_nop 3\n\t" "buffer_load_dwordx4 v[248:251], %[mo], %[rs], s60 offen sc1\n\t" \
    "ds_read_b128 v[176:179], %[l0] offset:4096\n\t" "ds_read_b128 v[180:183], %[l0] offset:5120\n\t" "ds_read_b128 v[184:187], %[l0] offset:6144\n\t" \
    "ds_read_b128 v[188:191], %[l0] offset:7168\n\t" "s_nop 3\n\t" "buffer_load_dwordx4 v[252:255], v224, %[rs], s60 offen sc1\n\t" "v_mov_b64 v[200:201], 0\n\t" \
    "v_mov_b64 v[202:203], 0\n\t" "v_mov_b64 v[204:205], 0\n\t" "v_mov_b64 v[206:207], 0\n\t" "v_mov_b64 v[208:209], 0\n\t" "v_mov_b64 v[210:211], 0\n\t" \
    "v_mov_b64 v[212:213], 0\n\t" "v_mov_b64 v[214:215], 0\n\t" "v_mov_b64 v[216:217], 0\n\t" "v_mov_b64 v[218:219], 0\n\t" "v_mov_b64 v[220:221], 0\n\t" \
    "v_mov_b64 v[222:223], 0\n\t" "v_mov_b32 v226, -1\n\t" "buffer_store_dword v226, %[hq], %[rs], %[pr] offen sc1\n\t" "v_mov_b32 v227, 160512\n\t" "10:\n\t" \
    "s_waitcnt vmcnt(4)\n\t" XT(84, 85) "v_smfmac_f32_16x16x64_bf16 v[232:235], v[240:243], a[0:7], %[ix]\n\t" "ds_write_b128 %[l1], v[240:243] offset:0\n\t" \
    "v_smfmac_f32_16x16x64_bf16 v[236:239], v[240:243], a[8:15], %[ix]\n\t" "s_waitcnt vmcnt(3)\n\t" "v_smfmac_f32_16x16x64_bf16 v[232:235], v[244:247], a[16:23], %[ix]\n\t" \
    "ds_write_b128 %[l1], v[244:247] offset:1024\n\t" "s_waitcnt lgkmcnt(8)\n\t" "v_smfmac_f32_16x16x64_bf16 v[236:239], v[244:247], v[160:167], %[ix]\n\t" \
    "s_waitcnt lgkmcnt(0)\n\t" "s_barrier\n\t" XT(86, 87) "ds_read_b128 v[16:19], %[l1] offset:4096\n\t" "ds_read_b128 v[20:23], %[l1] offset:8192\n\t" \
    "ds_read_b128 v[24:27], %[l1] offset:12288\n\t" "s_waitcnt vmcnt(2)\n\t" "v_smfmac_f32_16x16x64_bf16 v[232:235], v[248:251], a[24:31], %[ix]\n\t" \
    "ds_write_b128 %[l1], v[248:251] offset:2048\n\t" "v_smfmac_f32_16x16x64_bf16 v[236:239], v[248:251], a[32:39], %[ix]\n\t" "ds_read_b128 v[28:31], %[l1] offset:5120\n\t" \
    "ds_read_b128 v[240:243], %[l1] offset:9216\n\t" "ds_read_b128 v[244:247], %[l1] offset:13312\n\t" "s_waitcnt vmcnt(1)\n\t" \
    "v_smfmac_f32_16x16x64_bf16 v[232:235], v[252:255], a[40:47], %[ix]\n\t" "ds_write_b128 %[l1], v[252:255] offset:3072\n\t" \
    "v_smfmac_f32_16x16x64_bf16 v[236:239], v[252:255], v[168:175], %[ix]\n\t" "ds_read_b128 v[160:163], %[l0] offset:8192\n\t" \
    "ds_read_b128 v[164:167], %[l0] offset:9216\n\t" "ds_read_b128 v[168:171], %[l0] offset:10240\n\t" "ds_read_b128 v[172:175], %[l0] offset:11264\n\t" "s_nop 1\n\t" \
    "v_cmp_u_f32 vcc, v232, v233\n\t" "v_cmp_u_f32 s[62:63], v234, v235\n\t" "s_waitcnt lgkmcnt(11)\n\t" \
    "v_smfmac_f32_16x16x64_bf16 v[232:235], v[16:19], a[48:55], %[ix]\n\t" "v_smfmac_f32_16x16x64_bf16 v[236:239], v[16:19], a[56:63], %[ix]\n\t" \
    "s_or_b64 vcc, vcc, s[62:63]\n\t" "s_cbranch_scc1 11f\n\t" "12:\n\t" "s_waitcnt lgkmcnt(10)\n\t" "v_smfmac_f32_16x16x64_bf16 v[232:235], v[20:23], a[64:71], %[ix]\n\t" \
    "v_smfmac_f32_16x16x64_bf16 v[236:239], v[20:23], v[176:183], %[ix]\n\t" "s_waitcnt lgkmcnt(4)\n\t" "s_barrier\n\t" XT(88, 89) "ds_read_b32 v225, v227\n\t" \
    "v_smfmac_f32_16x16x64_bf16 v[232:235], v[24:27], a[72:79], %[ix]\n\t" "ds_read_b128 v[176:179], %[l0] offset:12288\n\t" "ds_read_b128 v[180:183], %[l0] offset:13312\n\t" \
    "v_smfmac_f32_16x16x64_bf16 v[236:239], v[24:27], a[80:87], %[ix]\n\t" "ds_read_b128 v[248:251], %[l1] offset:6144\n\t" "ds_read_b128 v[252:255], %[l1] offset:10240\n\t" \
    "v_smfmac_f32_16x16x64_bf16 v[232:235], v[28:31], a[88:95], %[ix]\n\t" "ds_read_b128 v[16:19], %[l1] offset:14336\n\t" "ds_read_b128 v[20:23], %[l1] offset:7168\n\t" \
    "v_smfmac_f32_16x16x64_bf16 v[236:239], v[28:31], v[184:191], %[ix]\n\t" "s_memtime s[64:65]\n\t" "ds_read_b128 v[24:27], %[l1] offset:11264\n\t" \
    "v_smfmac_f32_16x16x64_bf16 v[232:235], v[240:243], a[96:103], %[ix]\n\t" "ds_read_b128 v[184:187], %[l0] offset:14336\n\t" \
    "ds_read_b128 v[188:191], %[l0] offset:15360\n\t" "v_smfmac_f32_16x16x64_bf16 v[236:239], v[240:243], a[104:111], %[ix]\n\t" "s_waitcnt lgkmcnt(9)\n\t" \
    "v_cmp_eq_u32 vcc, %[rt], v225\n\t" "v_smfmac_f32_16x16x64_bf16 v[232:235], v[244:247], a[112:119], %[ix]\n\t" "s_cbranch_vccnz 13f\n\t" \
    "ds_read_b128 v[28:31], %[l1] offset:15360\n\t" "v_smfmac_f32_16x16x64_bf16 v[236:239], v[244:247], v[160:167], %[ix]\n\t" \
    "ds_read_b128 v[160:163], %[l0] offset:16384\n\t" "ds_read_b128 v[164:167], %[l0] offset:17408\n\t" "s_waitcnt lgkmcnt(9)\n\t" \
    "v_smfmac_f32_16x16x64_bf16 v[232:235], v[248:251], a[120:127], %[ix]\n\t" "s_sub_u32 s62, s64, %[ca]\n\t" "s_mul_hi_u32 s63, s62, %[k1]\n\t" \
    "v_smfmac_f32_16x16x64_bf16 v[236:239], v[248:251], a[128:135], %[ix]\n\t" "s_mul_i32 s62, s62, %[k1]\n\t" "s_lshr_b64 s[62:63], s[62:63], 16\n\t" \
    "s_waitcnt lgkmcnt(8)\n\t" "v_smfmac_f32_16x16x64_bf16 v[232:235], v[252:255], a[136:143], %[ix]\n\t" "s_add_u32 s62, s62, %[ga]\n\t" "s_bitset0_b32 s62, 0\n\t" \
    "v_smfmac_f32_16x16x64_bf16 v[236:239], v[252:255], v[168:175], %[ix]\n\t" "v_mov_b32 v226, s62\n\t" "buffer_store_dword v226, %[hq], %[rs], s67 offen sc1\n\t" \
    "s_waitcnt lgkmcnt(7)\n\t" "v_smfmac_f32_16x16x64_bf16 v[232:235], v[16:19], a[144:151], %[ix]\n\t" "ds_read_b128 v[168:171], %[l0] offset:18432\n\t" \
    "ds_read_b128 v[172:175], %[l0] offset:19456\n\t" "v_smfmac_f32_16x16x64_bf16 v[236:239], v[16:19], a[152:159], %[ix]\n\t" "s_waitcnt lgkmcnt(8)\n\t" \
    "v_smfmac_f32_16x16x64_bf16 v[232:235], v[20:23], a[160:167], %[ix]\n\t" "v_smfmac_f32_16x16x64_bf16 v[236:239], v[20:23], v[176:183], %[ix]\n\t" \
    "ds_read_b128 v[176:179], %[l0] offset:20480\n\t" "ds_read_b128 v[180:183], %[l0] offset:21504\n\t" "s_waitcnt lgkmcnt(9)\n\t" \
    "v_smfmac_f32_16x16x64_bf16 v[232:235], v[24:27], a[168:175], %[ix]\n\t" "v_smfmac_f32_16x16x64_bf16 v[236:239], v[24:27], a[176:183], %[ix]\n\t" \
    "s_waitcnt lgkmcnt(6)\n\t" "v_smfmac_f32_16x16x64_bf16 v[232:235], v[28:31], a[184:191], %[ix]\n\t" \
    "v_smfmac_f32_16x16x64_bf16 v[236:239], v[28:31], v[184:191], %[ix]\n\t" "ds_read_b128 v[184:187], %[l0] offset:22528\n\t" \
    "ds_read_b128 v[188:191], %[l0] offset:23552\n\t" XT(90, 91) "s_nop 4\n\t" "v_add_f32 v232, v232, v233\n\t" "v_add_f32 v234, v234, v235\n\t" \
    "v_add_f32 v236, v236, v237\n\t" "v_add_f32 v238, v238, v239\n\t" "v_cvt_pk_bf16_f32 v236, v232, v236\n\t" "v_cvt_pk_bf16_f32 v237, v234, v238\n\t" \
    "ds_write2_b32 %[vw], v236, v237 offset1:68\n\t" "s_waitcnt lgkmcnt(0)\n\t" "s_barrier\n\t" XT(92, 93) "ds_read_b128 v[240:243], %[vr] offset:0\n\t" \
    "ds_read_b128 v[244:247], %[vr] offset:64\n\t" "ds_read_b128 v[248:251], %[vr] offset:128\n\t" "ds_read_b128 v[252:255], %[vr] offset:192\n\t" "s_mov_b32 s61, %[ub]\n\t" \
    "s_waitcnt lgkmcnt(3)\n\t" "v_smfmac_f32_16x16x64_bf16 v[192:195], v[240:243], v[32:39], %[ix]\n\t" \
    "v_smfmac_f32_16x16x64_bf16 v[196:199], v[240:243], v[48:55], %[ix]\n\t" "s_waitcnt lgkmcnt(2)\n\t" \
    "v_smfmac_f32_16x16x64_bf16 v[192:195], v[244:247], v[40:47], %[ix]\n\t" "v_smfmac_f32_16x16x64_bf16 v[196:199], v[244:247], v[56:63], %[ix]\n\t" \
    "s_waitcnt lgkmcnt(1)\n\t" "v_smfmac_f32_16x16x64_bf16 v[192:195], v[248:251], a[192:199], %[ix]\n\t" \
    "v_smfmac_f32_16x16x64_bf16 v[196:199], v[248:251], a[200:207], %[ix]\n\t" "s_waitcnt lgkmcnt(0)\n\t" \
    "v_smfmac_f32_16x16x64_bf16 v[192:195], v[252:255], v[160:167], %[ix]\n\t" "ds_read_b128 v[160:163], %[l0] offset:24576\n\t" \
    "ds_read_b128 v[164:167], %[l0] offset:25600\n\t" "v_smfmac_f32_16x16x64_bf16 v[196:199], v[252:255], v[168:175], %[ix]\n\t" \
    "ds_read_b128 v[168:171], %[l0] offset:26624\n\t" "ds_read_b128 v[172:175], %[l0] offset:27648\n\t" \
    "v_smfmac_f32_16x16x64_bf16 v[200:203], v[240:243], v[64:71], %[ix]\n\t" "s_nop 1\n\t" "v_smfmac_f32_16x16x64_bf16 v[204:207], v[240:243], v[80:87], %[ix]\n\t" \
    "v_cvt_pk_bf16_f32 v224, v192, v194\n\t" "v_cvt_pk_bf16_f32 v225, v193, v195\n\t" "v_smfmac_f32_16x16x64_bf16 v[200:203], v[244:247], v[72:79], %[ix]\n\t" \
    "v_cvt_pk_bf16_f32 v226, v196, v198\n\t" "v_cvt_pk_bf16_f32 v227, v197, v199\n\t" "v_smfmac_f32_16x16x64_bf16 v[204:207], v[244:247], v[88:95], %[ix]\n\t" "s_nop 0\n\t" \
    "v_permlane32_swap_b32 v224, v226\n\t" "v_smfmac_f32_16x16x64_bf16 v[200:203], v[248:251], a[208:215], %[ix]\n\t" "v_permlane32_swap_b32 v225, v227\n\t" "s_nop 0\n\t" \
    "v_smfmac_f32_16x16x64_bf16 v[204:207], v[248:251], a[216:223], %[ix]\n\t" "v_permlane16_swap_b32 v224, v225\n\t" "v_permlane16_swap_b32 v226, v227\n\t" \
    "v_smfmac_f32_16x16x64_bf16 v[200:203], v[252:255], v[176:183], %[ix]\n\t" "ds_read_b128 v[176:179], %[l0] offset:28672\n\t" \
    "buffer_store_dwordx4 v[224:227], %[uo], %[rs], s61 offen sc1\n\t" "s_xor_b32 s61, s61, 0x40000\n\t" XT(94, 95) \
    "v_smfmac_f32_16x16x64_bf16 v[204:207], v[252:255], v[184:191], %[ix]\n\t" "ds_read_b128 v[180:183], %[l0] offset:29696\n\t" \
    "ds_read_b128 v[184:187], %[l0] offset:30720\n\t" "v_smfmac_f32_16x16x64_bf16 v[208:211], v[240:243], v[96:103], %[ix]\n\t" \
    "ds_read_b128 v[188:191], %[l0] offset:31744\n\t" "s_nop 0\n\t" "v_smfmac_f32_16x16x64_bf16 v[212:215], v[240:243], v[112:119], %[ix]\n\t" \
    "v_cvt_pk_bf16_f32 v228, v200, v202\n\t" "v_cvt_pk_bf16_f32 v229, v201, v203\n\t" "v_smfmac_f32_16x16x64_bf16 v[208:211], v[244:247], v[104:111], %[ix]\n\t" \
    "v_cvt_pk_bf16_f32 v230, v204, v206\n\t" "v_cvt_pk_bf16_f32 v231, v205, v207\n\t" "v_smfmac_f32_16x16x64_bf16 v[212:215], v[244:247], v[120:127], %[ix]\n\t" "s_nop 0\n\t" \
    "v_permlane32_swap_b32 v228, v230\n\t" "v_smfmac_f32_16x16x64_bf16 v[208:211], v[248:251], a[224:231], %[ix]\n\t" "v_permlane32_swap_b32 v229, v231\n\t" "s_nop 0\n\t" \
    "v_smfmac_f32_16x16x64_bf16 v[212:215], v[248:251], a[232:239], %[ix]\n\t" "v_permlane16_swap_b32 v228, v229\n\t" "v_permlane16_swap_b32 v230, v231\n\t" \
    "s_waitcnt lgkmcnt(6)\n\t" "v_smfmac_f32_16x16x64_bf16 v[208:211], v[252:255], v[160:167], %[ix]\n\t" "buffer_store_dwordx4 v[228:231], %[uo], %[rs], s61 offen sc1\n\t" \
    "s_xor_b32 s61, s61, 0xc0000\n\t" "s_waitcnt lgkmcnt(4)\n\t" "v_smfmac_f32_16x16x64_bf16 v[212:215], v[252:255], v[168:175], %[ix]\n\t" "s_nop 1\n\t" \
    "v_smfmac_f32_16x16x64_bf16 v[216:219], v[240:243], v[128:135], %[ix]\n\t" "s_nop 1\n\t" "v_smfmac_f32_16x16x64_bf16 v[220:223], v[240:243], v[144:151], %[ix]\n\t" \
    "v_cvt_pk_bf16_f32 v224, v208, v210\n\t" "v_cvt_pk_bf16_f32 v225, v209, v211\n\t" "v_smfmac_f32_16x16x64_bf16 v[216:219], v[244:247], v[136:143], %[ix]\n\t" \
    "v_cvt_pk_bf16_f32 v226, v212, v214\n\t" "v_cvt_pk_bf16_f32 v227, v213, v215\n\t" "v_smfmac_f32_16x16x64_bf16 v[220:223], v[244:247], v[152:159], %[ix]\n\t" "s_nop 0\n\t" \
    "v_permlane32_swap_b32 v224, v226\n\t" "v_smfmac_f32_16x16x64_bf16 v[216:219], v[248:251], a[240:247], %[ix]\n\t" "v_permlane32_swap_b32 v225, v227\n\t" "s_nop 0\n\t" \
    "v_smfmac_f32_16x16x64_bf16 v[220:223], v[248:251], a[248:255], %[ix]\n\t" "v_permlane16_swap_b32 v224, v225\n\t" "v_permlane16_swap_b32 v226, v227\n\t" \
    "s_waitcnt lgkmcnt(2)\n\t" "v_smfmac_f32_16x16x64_bf16 v[216:219], v[252:255], v[176:183], %[ix]\n\t" "buffer_store_dwordx4 v[224:227], %[uo], %[rs], s61 offen sc1\n\t" \
    "s_xor_b32 s61, s61, 0x40000\n\t" "s_waitcnt lgkmcnt(0)\n\t" "v_smfmac_f32_16x16x64_bf16 v[220:223], v[252:255], v[184:191], %[ix]\n\t" "s_nop 5\n\t" \
    "v_cvt_pk_bf16_f32 v228, v216, v218\n\t" "v_cvt_pk_bf16_f32 v229, v217, v219\n\t" "v_cvt_pk_bf16_f32 v230, v220, v222\n\t" "v_cvt_pk_bf16_f32 v231, v221, v223\n\t" \
    "s_nop 0\n\t" "v_permlane32_swap_b32 v228, v230\n\t" "v_permlane32_swap_b32 v229, v231\n\t" "s_nop 1\n\t" "v_permlane16_swap_b32 v228, v229\n\t" \
    "v_permlane16_swap_b32 v230, v231\n\t" "buffer_store_dwordx4 v[228:231], %[uo], %[rs], s61 offen sc1\n\t" "s_nop 1\n\t" XT(96, 97) "s_branch 99f\n\t" "11:\n\t" \
    "ds_read_b128 v[228:231], %[l1] offset:0\n\t" "s_waitcnt lgkmcnt(0)\n\t" "v_max3_u32 v225, v228, v229, v230\n\t" "v_max_u32 v225, v225, v231\n\t" \
    "ds_read_b128 v[228:231], %[l1] offset:1024\n\t" "s_waitcnt lgkmcnt(0)\n\t" "v_max3_u32 v225, v225, v228, v229\n\t" "v_max3_u32 v225, v225, v230, v231\n\t" \
    "ds_read_b128 v[228:231], %[l1] offset:2048\n\t" "s_waitcnt lgkmcnt(0)\n\t" "v_max3_u32 v225, v225, v228, v229\n\t" "v_max3_u32 v225, v225, v230, v231\n\t" \
    "ds_read_b128 v[228:231], %[l1] offset:3072\n\t" "s_waitcnt lgkmcnt(0)\n\t" "v_max3_u32 v225, v225, v228, v229\n\t" "v_max3_u32 v225, v225, v230, v231\n\t" \
    "v_cmp_eq_u32 vcc, -1, v225\n\t" "s_cbranch_vccz 12b\n\t" "s_cmp_eq_u32 %[rt], 1\n\t" "s_cbranch_scc1 12b\n\t" "v_mov_b32 v225, %[rt]\n\t" "ds_write_b32 v227, v225\n\t" \
    "s_waitcnt lgkmcnt(0)\n\t" "s_branch 12b\n\t" "13:\n\t" "s_sub_u32 %[rt], %[rt], 1\n\t" "s_waitcnt lgkmcnt(0)\n\t" XCNT(100) "14:\n\t" \
    "buffer_load_dwordx4 v[240:243], %[mo], %[rs], %[sb] offen sc1\n\t" "buffer_load_dwordx4 v[244:247], v224, %[rs], %[sb] offen sc1\n\t" \
    "buffer_load_dwordx4 v[248:251], %[mo], %[rs], s60 offen sc1\n\t" "buffer_load_dwordx4 v[252:255], v224, %[rs], s60 offen sc1\n\t" "s_waitcnt vmcnt(0)\n\t" \
    "v_max_u32 v225, v240, v241\n\t" "v_max3_u32 v225, v225, v242, v243\n\t" "v_max3_u32 v225, v225, v244, v245\n\t" "v_max3_u32 v225, v225, v246, v247\n\t" \
    "v_max3_u32 v225, v225, v248, v249\n\t" "v_max3_u32 v225, v225, v250, v251\n\t" "v_max3_u32 v225, v225, v252, v253\n\t" "v_max3_u32 v225, v225, v254, v255\n\t" \
    "v_cmp_eq_u32 vcc, -1, v225\n\t" "s_cbranch_vccz 15f\n\t" "s_cmp_eq_u32 %[bd], 0\n\t" "s_cbranch_scc1 15f\n\t" "s_sub_u32 %[bd], %[bd], 1\n\t" XCNT(101) \
    "s_branch 14b\n\t" "15:\n\t" "ds_read_b128 v[160:163], %[l0] offset:0\n\t" "ds_read_b128 v[164:167], %[l0] offset:1024\n\t" \
    "ds_read_b128 v[168:171], %[l0] offset:2048\n\t" "ds_read_b128 v[172:175], %[l0] offset:3072\n\t" "ds_read_b128 v[176:179], %[l0] offset:4096\n\t" \
    "ds_read_b128 v[180:183], %[l0] offset:5120\n\t" "ds_read_b128 v[184:187], %[l0] offset:6144\n\t" "ds_read_b128 v[188:191], %[l0] offset:7168\n\t" \
    "v_mov_b64 v[232:233], 0\n\t" "v_mov_b64 v[234:235], 0\n\t" "v_mov_b64 v[236:237], 0\n\t" "v_mov_b64 v[238:239], 0\n\t" "s_barrier\n\t" "s_memtime s[64:65]\n\t" \
    "s_waitcnt lgkmcnt(0)\n\t" "s_add_u32 s64, s64, %[dr]\n\t" "s_sub_u32 s62, s64, %[ca]\n\t" "s_mul_hi_u32 s63, s62, %[k1]\n\t" "s_mul_i32 s62, s62, %[k1]\n\t" \
    "s_lshr_b64 s[62:63], s[62:63], 16\n\t" "s_add_u32 s62, s62, %[ga]\n\t" "s_bitset0_b32 s62, 0\n\t" "v_mov_b32 v225, s62\n\t" \
    "buffer_store_dword v225, %[hq], %[rs], s67 offen sc1\n\t" "s_mov_b32 s67, 0x7f000000\n\t" "s_branch 10b\n\t" "91:\n\t" XT(80, 81) "s_barrier\n\t" XT(82, 83) \
    "buffer_load_dwordx4 v[240:243], %[mo], %[rs], %[sb] offen sc1\n\t" "v_mov_b64 v[232:233], 0\n\t" "v_mov_b64 v[234:235], 0\n\t" "v_mov_b64 v[236:237], 0\n\t" \
    "v_mov_b64 v[238:239], 0\n\t" "v_mov_b64 v[192:193], 0\n\t" "v_mov_b64 v[194:195], 0\n\t" "v_mov_b64 v[196:197], 0\n\t" "v_mov_b64 v[198:199], 0\n\t" \
    "buffer_load_dwordx4 v[244:247], v224, %[rs], %[sb] offen sc1\n\t" "ds_read_b128 v[160:163], %[l0] offset:0\n\t" "ds_read_b128 v[164:167], %[l0] offset:1024\n\t" \
    "ds_read_b128 v[168:171], %[l0] offset:2048\n\t" "ds_read_b128 v[172:175], %[l0] offset:3072\n\t" "s_nop 3\n\t" \
    "buffer_load_dwordx4 v[248:251], %[mo], %[rs], s60 offen sc1\n\t" "ds_read_b128 v[176:179], %[l0] offset:4096\n\t" "ds_read_b128 v[180:183], %[l0] offset:5120\n\t" \
    "ds_read_b128 v[184:187], %[l0] offset:6144\n\t" "ds_read_b128 v[188:191], %[l0] offset:7168\n\t" "s_nop 3\n\t" \
    "buffer_load_dwordx4 v[252:255], v224, %[rs], s60 offen sc1\n\t" "v_mov_b64 v[200:201], 0\n\t" "v_mov_b64 v[202:203], 0\n\t" "v_mov_b64 v[204:205], 0\n\t" \
    "v_mov_b64 v[206:207], 0\n\t" "v_mov_b64 v[208:209], 0\n\t" "v_mov_b64 v[210:211], 0\n\t" "v_mov_b64 v[212:213], 0\n\t" "v_mov_b64 v[214:215], 0\n\t" \
    "v_mov_b64 v[216:217], 0\n\t" "v_mov_b64 v[218:219], 0\n\t" "v_mov_b64 v[220:221], 0\n\t" "v_mov_b64 v[222:223], 0\n\t" "v_mov_b32 v227, 160512\n\t" "20:\n\t" \
    "s_waitcnt vmcnt(3)\n\t" XT(84, 85) "v_smfmac_f32_16x16x64_bf16 v[232:235], v[240:243], a[0:7], %[ix]\n\t" "ds_write_b128 %[l1], v[240:243] offset:4096\n\t" \
    "v_smfmac_f32_16x16x64_bf16 v[236:239], v[240:243], a[8:15], %[ix]\n\t" "s_waitcnt vmcnt(2)\n\t" "v_smfmac_f32_16x16x64_bf16 v[232:235], v[244:247], a[16:23], %[ix]\n\t" \
    "ds_write_b128 %[l1], v[244:247] offset:5120\n\t" "s_waitcnt lgkmcnt(8)\n\t" "v_smfmac_f32_16x16x64_bf16 v[236:239], v[244:247], v[160:167], %[ix]\n\t" \
    "s_waitcnt lgkmcnt(0)\n\t" "s_barrier\n\t" XT(86, 87) "ds_read_b128 v[16:19], %[l1] offset:8192\n\t" "ds_read_b128 v[20:23], %[l1] offset:12288\n\t" \
    "ds_read_b128 v[24:27], %[l1] offset:0\n\t" "s_waitcnt vmcnt(1)\n\t" "v_smfmac_f32_16x16x64_bf16 v[232:235], v[248:251], a[24:31], %[ix]\n\t" \
    "ds_write_b128 %[l1], v[248:251] offset:6144\n\t" "v_smfmac_f32_16x16x64_bf16 v[236:239], v[248:251], a[32:39], %[ix]\n\t" "ds_read_b128 v[28:31], %[l1] offset:9216\n\t" \
    "ds_read_b128 v[240:243], %[l1] offset:13312\n\t" "ds_read_b128 v[244:247], %[l1] offset:1024\n\t" "s_waitcnt vmcnt(0)\n\t" \
    "v_smfmac_f32_16x16x64_bf16 v[232:235], v[252:255], a[40:47], %[ix]\n\t" "ds_write_b128 %[l1], v[252:255] offset:7168\n\t" \
    "v_smfmac_f32_16x16x64_bf16 v[236:239], v[252:255], v[168:175], %[ix]\n\t" "ds_read_b128 v[160:163], %[l0] offset:8192\n\t" \
    "ds_read_b128 v[164:167], %[l0] offset:9216\n\t" "ds_read_b128 v[168:171], %[l0] offset:10240\n\t" "ds_read_b128 v[172:175], %[l0] offset:11264\n\t" "s_nop 1\n\t" \
    "v_cmp_u_f32 vcc, v232, v233\n\t" "v_cmp_u_f32 s[62:63], v234, v235\n\t" "s_waitcnt lgkmcnt(11)\n\t" \
    "v_smfmac_f32_16x16x64_bf16 v[232:235], v[16:19], a[48:55], %[ix]\n\t" "v_smfmac_f32_16x16x64_bf16 v[236:239], v[16:19], a[56:63], %[ix]\n\t" \
    "s_or_b64 vcc, vcc, s[62:63]\n\t" "s_cbranch_scc1 21f\n\t" "22:\n\t" "s_waitcnt lgkmcnt(10)\n\t" "v_smfmac_f32_16x16x64_bf16 v[232:235], v[20:23], a[64:71], %[ix]\n\t" \
    "v_smfmac_f32_16x16x64_bf16 v[236:239], v[20:23], v[176:183], %[ix]\n\t" "s_waitcnt lgkmcnt(4)\n\t" "s_barrier\n\t" XT(88, 89) "ds_read_b32 v225, v227\n\t" \
    "v_smfmac_f32_16x16x64_bf16 v[232:235], v[24:27], a[72:79], %[ix]\n\t" "ds_read_b128 v[176:179], %[l0] offset:12288\n\t" "ds_read_b128 v[180:183], %[l0] offset:13312\n\t" \
    "v_smfmac_f32_16x16x64_bf16 v[236:239], v[24:27], a[80:87], %[ix]\n\t" "ds_read_b128 v[248:251], %[l1] offset:10240\n\t" "ds_read_b128 v[252:255], %[l1] offset:14336\n\t" \
    "v_smfmac_f32_16x16x64_bf16 v[232:235], v[28:31], a[88:95], %[ix]\n\t" "ds_read_b128 v[16:19], %[l1] offset:2048\n\t" "ds_read_b128 v[20:23], %[l1] offset:11264\n\t" \
    "v_smfmac_f32_16x16x64_bf16 v[236:239], v[28:31], v[184:191], %[ix]\n\t" "ds_read_b128 v[24:27], %[l1] offset:15360\n\t" "ds_read_b128 v[184:187], %[l0] offset:14336\n\t" \
    "v_smfmac_f32_16x16x64_bf16 v[232:235], v[240:243], a[96:103], %[ix]\n\t" "ds_read_b128 v[188:191], %[l0] offset:15360\n\t" "ds_read_b128 v[28:31], %[l1] offset:3072\n\t" \
    "v_smfmac_f32_16x16x64_bf16 v[236:239], v[240:243], a[104:111], %[ix]\n\t" "s_waitcnt lgkmcnt(10)\n\t" "v_cmp_eq_u32 vcc, %[rt], v225\n\t" \
    "v_smfmac_f32_16x16x64_bf16 v[232:235], v[244:247], a[112:119], %[ix]\n\t" "s_cbranch_vccnz 23f\n\t" \
    "v_smfmac_f32_16x16x64_bf16 v[236:239], v[244:247], v[160:167], %[ix]\n\t" "ds_read_b128 v[160:163], %[l0] offset:16384\n\t" \
    "ds_read_b128 v[164:167], %[l0] offset:17408\n\t" "s_waitcnt lgkmcnt(9)\n\t" "v_smfmac_f32_16x16x64_bf16 v[232:235], v[248:251], a[120:127], %[ix]\n\t" \
    "v_smfmac_f32_16x16x64_bf16 v[236:239], v[248:251], a[128:135], %[ix]\n\t" "s_waitcnt lgkmcnt(8)\n\t" \
    "v_smfmac_f32_16x16x64_bf16 v[232:235], v[252:255], a[136:143], %[ix]\n\t" "v_smfmac_f32_16x16x64_bf16 v[236:239], v[252:255], v[168:175], %[ix]\n\t" \
    "ds_read_b128 v[168:171], %[l0] offset:18432\n\t" "ds_read_b128 v[172:175], %[l0] offset:19456\n\t" "s_waitcnt lgkmcnt(9)\n\t" \
    "v_smfmac_f32_16x16x64_bf16 v[232:235], v[16:19], a[144:151], %[ix]\n\t" "v_smfmac_f32_16x16x64_bf16 v[236:239], v[16:19], a[152:159], %[ix]\n\t" \
    "s_waitcnt lgkmcnt(8)\n\t" "v_smfmac_f32_16x16x64_bf16 v[232:235], v[20:23], a[160:167], %[ix]\n\t" \
    "v_smfmac_f32_16x16x64_bf16 v[236:239], v[20:23], v[176:183], %[ix]\n\t" "ds_read_b128 v[176:179], %[l0] offset:20480\n\t" \
    "ds_read_b128 v[180:183], %[l0] offset:21504\n\t" "s_waitcnt lgkmcnt(9)\n\t" "v_smfmac_f32_16x16x64_bf16 v[232:235], v[24:27], a[168:175], %[ix]\n\t" \
    "v_smfmac_f32_16x16x64_bf16 v[236:239], v[24:27], a[176:183], %[ix]\n\t" "s_waitcnt lgkmcnt(6)\n\t" \
    "v_smfmac_f32_16x16x64_bf16 v[232:235], v[28:31], a[184:191], %[ix]\n\t" "v_smfmac_f32_16x16x64_bf16 v[236:239], v[28:31], v[184:191], %[ix]\n\t" \
    "ds_read_b128 v[184:187], %[l0] offset:22528\n\t" "ds_read_b128 v[188:191], %[l0] offset:23552\n\t" XT(90, 91) "s_nop 4\n\t" "v_add_f32 v232, v232, v233\n\t" \
    "v_add_f32 v234, v234, v235\n\t" "v_add_f32 v236, v236, v237\n\t" "v_add_f32 v238, v238, v239\n\t" "v_cvt_pk_bf16_f32 v236, v232, v236\n\t" \
    "v_cvt_pk_bf16_f32 v237, v234, v238\n\t" "ds_write2_b32 %[vw], v236, v237 offset1:68\n\t" "s_waitcnt lgkmcnt(0)\n\t" "s_barrier\n\t" XT(92, 93) \
    "ds_read_b128 v[240:243], %[vr] offset:0\n\t" "ds_read_b128 v[244:247], %[vr] offset:64\n\t" "ds_read_b128 v[248:251], %[vr] offset:128\n\t" \
    "ds_read_b128 v[252:255], %[vr] offset:192\n\t" "s_mov_b32 s61, %[ub]\n\t" "s_waitcnt lgkmcnt(3)\n\t" \
    "v_smfmac_f32_16x16x64_bf16 v[192:195], v[240:243], v[32:39], %[ix]\n\t" "v_smfmac_f32_16x16x64_bf16 v[196:199], v[240:243], v[48:55], %[ix]\n\t" \
    "s_waitcnt lgkmcnt(2)\n\t" "v_smfmac_f32_16x16x64_bf16 v[192:195], v[244:247], v[40:47], %[ix]\n\t" \
    "v_smfmac_f32_16x16x64_bf16 v[196:199], v[244:247], v[56:63], %[ix]\n\t" "s_waitcnt lgkmcnt(1)\n\t" \
    "v_smfmac_f32_16x16x64_bf16 v[192:195], v[248:251], a[192:199], %[ix]\n\t" "v_smfmac_f32_16x16x64_bf16 v[196:199], v[248:251], a[200:207], %[ix]\n\t" \
    "s_waitcnt lgkmcnt(0)\n\t" "v_smfmac_f32_16x16x64_bf16 v[192:195], v[252:255], v[160:167], %[ix]\n\t" "ds_read_b128 v[160:163], %[l0] offset:24576\n\t" \
    "ds_read_b128 v[164:167], %[l0] offset:25600\n\t" "v_smfmac_f32_16x16x64_bf16 v[196:199], v[252:255], v[168:175], %[ix]\n\t" \
    "ds_read_b128 v[168:171], %[l0] offset:26624\n\t" "ds_read_b128 v[172:175], %[l0] offset:27648\n\t" \
    "v_smfmac_f32_16x16x64_bf16 v[200:203], v[240:243], v[64:71], %[ix]\n\t" "s_nop 1\n\t" "v_smfmac_f32_16x16x64_bf16 v[204:207], v[240:243], v[80:87], %[ix]\n\t" \
    "v_cvt_pk_bf16_f32 v224, v192, v194\n\t" "v_cvt_pk_bf16_f32 v225, v193, v195\n\t" "v_smfmac_f32_16x16x64_bf16 v[200:203], v[244:247], v[72:79], %[ix]\n\t" \
    "v_cvt_pk_bf16_f32 v226, v196, v198\n\t" "v_cvt_pk_bf16_f32 v227, v197, v199\n\t" "v_smfmac_f32_16x16x64_bf16 v[204:207], v[244:247], v[88:95], %[ix]\n\t" "s_nop 0\n\t" \
    "v_permlane32_swap_b32 v224, v226\n\t" "v_smfmac_f32_16x16x64_bf16 v[200:203], v[248:251], a[208:215], %[ix]\n\t" "v_permlane32_swap_b32 v225, v227\n\t" "s_nop 0\n\t" \
    "v_smfmac_f32_16x16x64_bf16 v[204:207], v[248:251], a[216:223], %[ix]\n\t" "v_permlane16_swap_b32 v224, v225\n\t" "v_permlane16_swap_b32 v226, v227\n\t" \
    "v_smfmac_f32_16x16x64_bf16 v[200:203], v[252:255], v[176:183], %[ix]\n\t" "ds_read_b128 v[176:179], %[l0] offset:28672\n\t" \
    "buffer_store_dwordx4 v[224:227], %[uo], %[rs], s61 offen sc1\n\t" "s_xor_b32 s61, s61, 0x40000\n\t" XT(94, 95) \
    "v_smfmac_f32_16x16x64_bf16 v[204:207], v[252:255], v[184:191], %[ix]\n\t" "ds_read_b128 v[180:183], %[l0] offset:29696\n\t" \
    "ds_read_b128 v[184:187], %[l0] offset:30720\n\t" "v_smfmac_f32_16x16x64_bf16 v[208:211], v[240:243], v[96:103], %[ix]\n\t" \
    "ds_read_b128 v[188:191], %[l0] offset:31744\n\t" "s_nop 0\n\t" "v_smfmac_f32_16x16x64_bf16 v[212:215], v[240:243], v[112:119], %[ix]\n\t" \
    "v_cvt_pk_bf16_f32 v228, v200, v202\n\t" "v_cvt_pk_bf16_f32 v229, v201, v203\n\t" "v_smfmac_f32_16x16x64_bf16 v[208:211], v[244:247], v[104:111], %[ix]\n\t" \
    "v_cvt_pk_bf16_f32 v230, v204, v206\n\t" "v_cvt_pk_bf16_f32 v231, v205, v207\n\t" "v_smfmac_f32_16x16x64_bf16 v[212:215], v[244:247], v[120:127], %[ix]\n\t" "s_nop 0\n\t" \
    "v_permlane32_swap_b32 v228, v230\n\t" "v_smfmac_f32_16x16x64_bf16 v[208:211], v[248:251], a[224:231], %[ix]\n\t" "v_permlane32_swap_b32 v229, v231\n\t" "s_nop 0\n\t" \
    "v_smfmac_f32_16x16x64_bf16 v[212:215], v[248:251], a[232:239], %[ix]\n\t" "v_permlane16_swap_b32 v228, v229\n\t" "v_permlane16_swap_b32 v230, v231\n\t" \
    "s_waitcnt lgkmcnt(6)\n\t" "v_smfmac_f32_16x16x64_bf16 v[208:211], v[252:255], v[160:167], %[ix]\n\t" "buffer_store_dwordx4 v[228:231], %[uo], %[rs], s61 offen sc1\n\t" \
    "s_xor_b32 s61, s61, 0xc0000\n\t" "s_waitcnt lgkmcnt(4)\n\t" "v_smfmac_f32_16x16x64_bf16 v[212:215], v[252:255], v[168:175], %[ix]\n\t" "s_nop 1\n\t" \
    "v_smfmac_f32_16x16x64_bf16 v[216:219], v[240:243], v[128:135], %[ix]\n\t" "s_nop 1\n\t" "v_smfmac_f32_16x16x64_bf16 v[220:223], v[240:243], v[144:151], %[ix]\n\t" \
    "v_cvt_pk_bf16_f32 v224, v208, v210\n\t" "v_cvt_pk_bf16_f32 v225, v209, v211\n\t" "v_smfmac_f32_16x16x64_bf16 v[216:219], v[244:247], v[136:143], %[ix]\n\t" \
    "v_cvt_pk_bf16_f32 v226, v212, v214\n\t" "v_cvt_pk_bf16_f32 v227, v213, v215\n\t" "v_smfmac_f32_16x16x64_bf16 v[220:223], v[244:247], v[152:159], %[ix]\n\t" "s_nop 0\n\t" \
    "v_permlane32_swap_b32 v224, v226\n\t" "v_smfmac_f32_16x16x64_bf16 v[216:219], v[248:251], a[240:247], %[ix]\n\t" "v_permlane32_swap_b32 v225, v227\n\t" "s_nop 0\n\t" \
    "v_smfmac_f32_16x16x64_bf16 v[220:223], v[248:251], a[248:255], %[ix]\n\t" "v_permlane16_swap_b32 v224, v225\n\t" "v_permlane16_swap_b32 v226, v227\n\t" \
    "s_waitcnt lgkmcnt(2)\n\t" "v_smfmac_f32_16x16x64_bf16 v[216:219], v[252:255], v[176:183], %[ix]\n\t" "buffer_store_dwordx4 v[224:227], %[uo], %[rs], s61 offen sc1\n\t" \
    "s_xor_b32 s61, s61, 0x40000\n\t" "s_waitcnt lgkmcnt(0)\n\t" "v_smfmac_f32_16x16x64_bf16 v[220:223], v[252:255], v[184:191], %[ix]\n\t" "s_nop 5\n\t" \
    "v_cvt_pk_bf16_f32 v228, v216, v218\n\t" "v_cvt_pk_bf16_f32 v229, v217, v219\n\t" "v_cvt_pk_bf16_f32 v230, v220, v222\n\t" "v_cvt_pk_bf16_f32 v231, v221, v223\n\t" \
    "s_nop 0\n\t" "v_permlane32_swap_b32 v228, v230\n\t" "v_permlane32_swap_b32 v229, v231\n\t" "s_nop 1\n\t" "v_permlane16_swap_b32 v228, v229\n\t" \
    "v_permlane16_swap_b32 v230, v231\n\t" "buffer_store_dwordx4 v[228:231], %[uo], %[rs], s61 offen sc1\n\t" "s_nop 1\n\t" XT(96, 97) "v_mov_b64 v[16:17], -1\n\t" \
    "v_mov_b64 v[18:19], -1\n\t" "s_barrier\n\t" "buffer_store_dwordx4 v[16:19], %[l1], %[rs], %[ur] offen offset:0 sc1\n\t" \
    "buffer_store_dwordx4 v[16:19], %[l1], %[rs], %[ur] offen offset:1024 sc1\n\t" "buffer_store_dwordx4 v[16:19], %[l1], %[rs], %[ur] offen offset:2048 sc1\n\t" \
    "buffer_store_dwordx4 v[16:19], %[l1], %[rs], %[ur] offen offset:3072 sc1\n\t" "s_sub_u32 s61, %[ur], 0x1000\n\t" \
    "buffer_store_dwordx4 v[16:19], %[l1], %[rs], s61 offen offset:0 sc1\n\t" "buffer_store_dwordx4 v[16:19], %[l1], %[rs], s61 offen offset:1024 sc1\n\t" "s_branch 99f\n\t" \
    "21:\n\t" "ds_read_b128 v[228:231], %[l1] offset:4096\n\t" "s_waitcnt lgkmcnt(0)\n\t" "v_max3_u32 v225, v228, v229, v230\n\t" "v_max_u32 v225, v225, v231\n\t" \
    "ds_read_b128 v[228:231], %[l1] offset:5120\n\t" "s_waitcnt lgkmcnt(0)\n\t" "v_max3_u32 v225, v225, v228, v229\n\t" "v_max3_u32 v225, v225, v230, v231\n\t" \
    "ds_read_b128 v[228:231], %[l1] offset:6144\n\t" "s_waitcnt lgkmcnt(0)\n\t" "v_max3_u32 v225, v225, v228, v229\n\t" "v_max3_u32 v225, v225, v230, v231\n\t" \
    "ds_read_b128 v[228:231], %[l1] offset:7168\n\t" "s_waitcnt lgkmcnt(0)\n\t" "v_max3_u32 v225, v225, v228, v229\n\t" "v_max3_u32 v225, v225, v230, v231\n\t" \
    "v_cmp_eq_u32 vcc, -1, v225\n\t" "s_cbranch_vccz 22b\n\t" "s_cmp_eq_u32 %[rt], 1\n\t" "s_cbranch_scc1 22b\n\t" "v_mov_b32 v225, %[rt]\n\t" "ds_write_b32 v227, v225\n\t" \
    "s_waitcnt lgkmcnt(0)\n\t" "s_branch 22b\n\t" "23:\n\t" "s_sub_u32 %[rt], %[rt], 1\n\t" "s_waitcnt lgkmcnt(0)\n\t" XCNT(100) "24:\n\t" \
    "buffer_load_dwordx4 v[240:243], %[mo], %[rs], %[sb] offen sc1\n\t" "buffer_load_dwordx4 v[244:247], v224, %[rs], %[sb] offen sc1\n\t" \
    "buffer_load_dwordx4 v[248:251], %[mo], %[rs], s60 offen sc1\n\t" "buffer_load_dwordx4 v[252:255], v224, %[rs], s60 offen sc1\n\t" "s_waitcnt vmcnt(0)\n\t" \
    "v_max_u32 v225, v240, v241\n\t" "v_max3_u32 v225, v225, v242, v243\n\t" "v_max3_u32 v225, v225, v244, v245\n\t" "v_max3_u32 v225, v225, v246, v247\n\t" \
    "v_max3_u32 v225, v225, v248, v249\n\t" "v_max3_u32 v225, v225, v250, v251\n\t" "v_max3_u32 v225, v225, v252, v253\n\t" "v_max3_u32 v225, v225, v254, v255\n\t" \
    "v_cmp_eq_u32 vcc, -1, v225\n\t" "s_cbranch_vccz 25f\n\t" "s_cmp_eq_u32 %[bd], 0\n\t" "s_cbranch_scc1 25f\n\t" "s_sub_u32 %[bd], %[bd], 1\n\t" XCNT(101) \
    "s_branch 24b\n\t" "25:\n\t" "ds_read_b128 v[160:163], %[l0] offset:0\n\t" "ds_read_b128 v[164:167], %[l0] offset:1024\n\t" \
    "ds_read_b128 v[168:171], %[l0] offset:2048\n\t" "ds_read_b128 v[172:175], %[l0] offset:3072\n\t" "ds_read_b128 v[176:179], %[l0] offset:4096\n\t" \
    "ds_read_b128 v[180:183], %[l0] offset:5120\n\t" "ds_read_b128 v[184:187], %[l0] offset:6144\n\t" "ds_read_b128 v[188:191], %[l0] offset:7168\n\t" \
    "v_mov_b64 v[232:233], 0\n\t" "v_mov_b64 v[234:235], 0\n\t" "v_mov_b64 v[236:237], 0\n\t" "v_mov_b64 v[238:239], 0\n\t" "s_barrier\n\t" "s_branch 20b\n\t" "92:\n\t" \
    XT(80, 81) "s_barrier\n\t" XT(82, 83) "buffer_load_dwordx4 v[240:243], %[mo], %[rs], %[sb] offen sc1\n\t" "v_mov_b64 v[232:233], 0\n\t" "v_mov_b64 v[234:235], 0\n\t" \
    "v_mov_b64 v[236:237], 0\n\t" "v_mov_b64 v[238:239], 0\n\t" "v_mov_b64 v[192:193], 0\n\t" "v_mov_b64 v[194:195], 0\n\t" "v_mov_b64 v[196:197], 0\n\t" \
    "v_mov_b64 v[198:199], 0\n\t" "buffer_load_dwordx4 v[244:247], v224, %[rs], %[sb] offen sc1\n\t" "ds_read_b128 v[160:163], %[l0] offset:0\n\t" \
    "ds_read_b128 v[164:167], %[l0] offset:1024\n\t" "ds_read_b128 v[168:171], %[l0] offset:2048\n\t" "ds_read_b128 v[172:175], %[l0] offset:3072\n\t" "s_nop 3\n\t" \
    "buffer_load_dwordx4 v[248:251], %[mo], %[rs], s60 offen sc1\n\t" "ds_read_b128 v[176:179], %[l0] offset:4096\n\t" "ds_read_b128 v[180:183], %[l0] offset:5120\n\t" \
    "ds_read_b128 v[184:187], %[l0] offset:6144\n\t" "ds_read_b128 v[188:191], %[l0] offset:7168\n\t" "s_nop 3\n\t" \
    "buffer_load_dwordx4 v[252:255], v224, %[rs], s60 offen sc1\n\t" "v_mov_b64 v[200:201], 0\n\t" "v_mov_b64 v[202:203], 0\n\t" "v_mov_b64 v[204:205], 0\n\t" \
    "v_mov_b64 v[206:207], 0\n\t" "v_mov_b64 v[208:209], 0\n\t" "v_mov_b64 v[210:211], 0\n\t" "v_mov_b64 v[212:213], 0\n\t" "v_mov_b64 v[214:215], 0\n\t" \
    "v_mov_b64 v[216:217], 0\n\t" "v_mov_b64 v[218:219], 0\n\t" "v_mov_b64 v[220:221], 0\n\t" "v_mov_b64 v[222:223], 0\n\t" "v_mov_b32 v227, 160512\n\t" "30:\n\t" \
    "s_waitcnt vmcnt(3)\n\t" XT(84, 85) "v_smfmac_f32_16x16x64_bf16 v[232:235], v[240:243], a[0:7], %[ix]\n\t" "ds_write_b128 %[l1], v[240:243] offset:8192\n\t" \
    "v_smfmac_f32_16x16x64_bf16 v[236:239], v[240:243], a[8:15], %[ix]\n\t" "s_waitcnt vmcnt(2)\n\t" "v_smfmac_f32_16x16x64_bf16 v[232:235], v[244:247], a[16:23], %[ix]\n\t" \
    "ds_write_b128 %[l1], v[244:247] offset:9216\n\t" "s_waitcnt lgkmcnt(8)\n\t" "v_smfmac_f32_16x16x64_bf16 v[236:239], v[244:247], v[160:167], %[ix]\n\t" \
    "s_waitcnt lgkmcnt(0)\n\t" "s_barrier\n\t" XT(86, 87) "ds_read_b128 v[16:19], %[l1] offset:12288\n\t" "ds_read_b128 v[20:23], %[l1] offset:0\n\t" \
    "ds_read_b128 v[24:27], %[l1] offset:4096\n\t" "s_waitcnt vmcnt(1)\n\t" "v_smfmac_f32_16x16x64_bf16 v[232:235], v[248:251], a[24:31], %[ix]\n\t" \
    "ds_write_b128 %[l1], v[248:251] offset:10240\n\t" "v_smfmac_f32_16x16x64_bf16 v[236:239], v[248:251], a[32:39], %[ix]\n\t" \
    "ds_read_b128 v[28:31], %[l1] offset:13312\n\t" "ds_read_b128 v[240:243], %[l1] offset:1024\n\t" "ds_read_b128 v[244:247], %[l1] offset:5120\n\t" "s_waitcnt vmcnt(0)\n\t" \
    "v_smfmac_f32_16x16x64_bf16 v[232:235], v[252:255], a[40:47], %[ix]\n\t" "ds_write_b128 %[l1], v[252:255] offset:11264\n\t" \
    "v_smfmac_f32_16x16x64_bf16 v[236:239], v[252:255], v[168:175], %[ix]\n\t" "ds_read_b128 v[160:163], %[l0] offset:8192\n\t" \
    "ds_read_b128 v[164:167], %[l0] offset:9216\n\t" "ds_read_b128 v[168:171], %[l0] offset:10240\n\t" "ds_read_b128 v[172:175], %[l0] offset:11264\n\t" "s_nop 1\n\t" \
    "v_cmp_u_f32 vcc, v232, v233\n\t" "v_cmp_u_f32 s[62:63], v234, v235\n\t" "s_waitcnt lgkmcnt(11)\n\t" \
    "v_smfmac_f32_16x16x64_bf16 v[232:235], v[16:19], a[48:55], %[ix]\n\t" "v_smfmac_f32_16x16x64_bf16 v[236:239], v[16:19], a[56:63], %[ix]\n\t" \
    "s_or_b64 vcc, vcc, s[62:63]\n\t" "s_cbranch_scc1 31f\n\t" "32:\n\t" "s_waitcnt lgkmcnt(10)\n\t" "v_smfmac_f32_16x16x64_bf16 v[232:235], v[20:23], a[64:71], %[ix]\n\t" \
    "v_smfmac_f32_16x16x64_bf16 v[236:239], v[20:23], v[176:183], %[ix]\n\t" "s_waitcnt lgkmcnt(4)\n\t" "s_barrier\n\t" XT(88, 89) "ds_read_b32 v225, v227\n\t" \
    "v_smfmac_f32_16x16x64_bf16 v[232:235], v[24:27], a[72:79], %[ix]\n\t" "ds_read_b128 v[176:179], %[l0] offset:12288\n\t" "ds_read_b128 v[180:183], %[l0] offset:13312\n\t" \
    "v_smfmac_f32_16x16x64_bf16 v[236:239], v[24:27], a[80:87], %[ix]\n\t" "ds_read_b128 v[248:251], %[l1] offset:14336\n\t" "ds_read_b128 v[252:255], %[l1] offset:2048\n\t" \
    "v_smfmac_f32_16x16x64_bf16 v[232:235], v[28:31], a[88:95], %[ix]\n\t" "ds_read_b128 v[16:19], %[l1] offset:6144\n\t" "ds_read_b128 v[20:23], %[l1] offset:15360\n\t" \
    "v_smfmac_f32_16x16x64_bf16 v[236:239], v[28:31], v[184:191], %[ix]\n\t" "ds_read_b128 v[24:27], %[l1] offset:3072\n\t" "ds_read_b128 v[184:187], %[l0] offset:14336\n\t" \
    "v_smfmac_f32_16x16x64_bf16 v[232:235], v[240:243], a[96:103], %[ix]\n\t" "ds_read_b128 v[188:191], %[l0] offset:15360\n\t" "ds_read_b128 v[28:31], %[l1] offset:7168\n\t" \
    "v_smfmac_f32_16x16x64_bf16 v[236:239], v[240:243], a[104:111], %[ix]\n\t" "s_waitcnt lgkmcnt(10)\n\t" "v_cmp_eq_u32 vcc, %[rt], v225\n\t" \
    "v_smfmac_f32_16x16x64_bf16 v[232:235], v[244:247], a[112:119], %[ix]\n\t" "s_cbranch_vccnz 33f\n\t" \
    "v_smfmac_f32_16x16x64_bf16 v[236:239], v[244:247], v[160:167], %[ix]\n\t" "ds_read_b128 v[160:163], %[l0] offset:16384\n\t" \
    "ds_read_b128 v[164:167], %[l0] offset:17408\n\t" "s_waitcnt lgkmcnt(9)\n\t" "v_smfmac_f32_16x16x64_bf16 v[232:235], v[248:251], a[120:127], %[ix]\n\t" \
    "v_smfmac_f32_16x16x64_bf16 v[236:239], v[248:251], a[128:135], %[ix]\n\t" "s_waitcnt lgkmcnt(8)\n\t" \
    "v_smfmac_f32_16x16x64_bf16 v[232:235], v[252:255], a[136:143], %[ix]\n\t" "v_smfmac_f32_16x16x64_bf16 v[236:239], v[252:255], v[168:175], %[ix]\n\t" \
    "ds_read_b128 v[168:171], %[l0] offset:18432\n\t" "ds_read_b128 v[172:175], %[l0] offset:19456\n\t" "s_waitcnt lgkmcnt(9)\n\t" \
    "v_smfmac_f32_16x16x64_bf16 v[232:235], v[16:19], a[144:151], %[ix]\n\t" "v_smfmac_f32_16x16x64_bf16 v[236:239], v[16:19], a[152:159], %[ix]\n\t" \
    "s_waitcnt lgkmcnt(8)\n\t" "v_smfmac_f32_16x16x64_bf16 v[232:235], v[20:23], a[160:167], %[ix]\n\t" \
    "v_smfmac_f32_16x16x64_bf16 v[236:239], v[20:23], v[176:183], %[ix]\n\t" "ds_read_b128 v[176:179], %[l0] offset:20480\n\t" \
    "ds_read_b128 v[180:183], %[l0] offset:21504\n\t" "s_waitcnt lgkmcnt(9)\n\t" "v_smfmac_f32_16x16x64_bf16 v[232:235], v[24:27], a[168:175], %[ix]\n\t" \
    "v_smfmac_f32_16x16x64_bf16 v[236:239], v[24:27], a[176:183], %[ix]\n\t" "s_waitcnt lgkmcnt(6)\n\t" \
    "v_smfmac_f32_16x16x64_bf16 v[232:235], v[28:31], a[184:191], %[ix]\n\t" "v_smfmac_f32_16x16x64_bf16 v[236:239], v[28:31], v[184:191], %[ix]\n\t" \
    "ds_read_b128 v[184:187], %[l0] offset:22528\n\t" "ds_read_b128 v[188:191], %[l0] offset:23552\n\t" XT(90, 91) "s_nop 4\n\t" "v_add_f32 v232, v232, v233\n\t" \
    "v_add_f32 v234, v234, v235\n\t" "v_add_f32 v236, v236, v237\n\t" "v_add_f32 v238, v238, v239\n\t" "v_cvt_pk_bf16_f32 v236, v232, v236\n\t" \
    "v_cvt_pk_bf16_f32 v237, v234, v238\n\t" "ds_write2_b32 %[vw], v236, v237 offset1:68\n\t" "s_waitcnt lgkmcnt(0)\n\t" "s_barrier\n\t" XT(92, 93) \
    "ds_read_b128 v[240:243], %[vr] offset:0\n\t" "ds_read_b128 v[244:247], %[vr] offset:64\n\t" "ds_read_b128 v[248:251], %[vr] offset:128\n\t" \
    "ds_read_b128 v[252:255], %[vr] offset:192\n\t" "s_mov_b32 s61, %[ub]\n\t" "s_waitcnt lgkmcnt(3)\n\t" \
    "v_smfmac_f32_16x16x64_bf16 v[192:195], v[240:243], v[32:39], %[ix]\n\t" "v_smfmac_f32_16x16x64_bf16 v[196:199], v[240:243], v[48:55], %[ix]\n\t" \
    "s_waitcnt lgkmcnt(2)\n\t" "v_smfmac_f32_16x16x64_bf16 v[192:195], v[244:247], v[40:47], %[ix]\n\t" \
    "v_smfmac_f32_16x16x64_bf16 v[196:199], v[244:247], v[56:63], %[ix]\n\t" "s_waitcnt lgkmcnt(1)\n\t" \
    "v_smfmac_f32_16x16x64_bf16 v[192:195], v[248:251], a[192:199], %[ix]\n\t" "v_smfmac_f32_16x16x64_bf16 v[196:199], v[248:251], a[200:207], %[ix]\n\t" \
    "s_waitcnt lgkmcnt(0)\n\t" "v_smfmac_f32_16x16x64_bf16 v[192:195], v[252:255], v[160:167], %[ix]\n\t" "ds_read_b128 v[160:163], %[l0] offset:24576\n\t" \
    "ds_read_b128 v[164:167], %[l0] offset:25600\n\t" "v_smfmac_f32_16x16x64_bf16 v[196:199], v[252:255], v[168:175], %[ix]\n\t" \
    "ds_read_b128 v[168:171], %[l0] offset:26624\n\t" "ds_read_b128 v[172:175], %[l0] offset:27648\n\t" \
    "v_smfmac_f32_16x16x64_bf16 v[200:203], v[240:243], v[64:71], %[ix]\n\t" "s_nop 1\n\t" "v_smfmac_f32_16x16x64_bf16 v[204:207], v[240:243], v[80:87], %[ix]\n\t" \
    "v_cvt_pk_bf16_f32 v224, v192, v194\n\t" "v_cvt_pk_bf16_f32 v225, v193, v195\n\t" "v_smfmac_f32_16x16x64_bf16 v[200:203], v[244:247], v[72:79], %[ix]\n\t" \
    "v_cvt_pk_bf16_f32 v226, v196, v198\n\t" "v_cvt_pk_bf16_f32 v227, v197, v199\n\t" "v_smfmac_f32_16x16x64_bf16 v[204:207], v[244:247], v[88:95], %[ix]\n\t" "s_nop 0\n\t" \
    "v_permlane32_swap_b32 v224, v226\n\t" "v_smfmac_f32_16x16x64_bf16 v[200:203], v[248:251], a[208:215], %[ix]\n\t" "v_permlane32_swap_b32 v225, v227\n\t" "s_nop 0\n\t" \
    "v_smfmac_f32_16x16x64_bf16 v[204:207], v[248:251], a[216:223], %[ix]\n\t" "v_permlane16_swap_b32 v224, v225\n\t" "v_permlane16_swap_b32 v226, v227\n\t" \
    "v_smfmac_f32_16x16x64_bf16 v[200:203], v[252:255], v[176:183], %[ix]\n\t" "ds_read_b128 v[176:179], %[l0] offset:28672\n\t" \
    "buffer_store_dwordx4 v[224:227], %[uo], %[rs], s61 offen sc1\n\t" "s_xor_b32 s61, s61, 0x40000\n\t" XT(94, 95) \
    "v_smfmac_f32_16x16x64_bf16 v[204:207], v[252:255], v[184:191], %[ix]\n\t" "ds_read_b128 v[180:183], %[l0] offset:29696\n\t" \
    "ds_read_b128 v[184:187], %[l0] offset:30720\n\t" "v_smfmac_f32_16x16x64_bf16 v[208:211], v[240:243], v[96:103], %[ix]\n\t" \
    "ds_read_b128 v[188:191], %[l0] offset:31744\n\t" "s_nop 0\n\t" "v_smfmac_f32_16x16x64_bf16 v[212:215], v[240:243], v[112:119], %[ix]\n\t" \
    "v_cvt_pk_bf16_f32 v228, v200, v202\n\t" "v_cvt_pk_bf16_f32 v229, v201, v203\n\t" "v_smfmac_f32_16x16x64_bf16 v[208:211], v[244:247], v[104:111], %[ix]\n\t" \
    "v_cvt_pk_bf16_f32 v230, v204, v206\n\t" "v_cvt_pk_bf16_f32 v231, v205, v207\n\t" "v_smfmac_f32_16x16x64_bf16 v[212:215], v[244:247], v[120:127], %[ix]\n\t" "s_nop 0\n\t" \
    "v_permlane32_swap_b32 v228, v230\n\t" "v_smfmac_f32_16x16x64_bf16 v[208:211], v[248:251], a[224:231], %[ix]\n\t" "v_permlane32_swap_b32 v229, v231\n\t" "s_nop 0\n\t" \
    "v_smfmac_f32_16x16x64_bf16 v[212:215], v[248:251], a[232:239], %[ix]\n\t" "v_permlane16_swap_b32 v228, v229\n\t" "v_permlane16_swap_b32 v230, v231\n\t" \
    "s_waitcnt lgkmcnt(6)\n\t" "v_smfmac_f32_16x16x64_bf16 v[208:211], v[252:255], v[160:167], %[ix]\n\t" "buffer_store_dwordx4 v[228:231], %[uo], %[rs], s61 offen sc1\n\t" \
    "s_xor_b32 s61, s61, 0xc0000\n\t" "s_waitcnt lgkmcnt(4)\n\t" "v_smfmac_f32_16x16x64_bf16 v[212:215], v[252:255], v[168:175], %[ix]\n\t" "s_nop 1\n\t" \
    "v_smfmac_f32_16x16x64_bf16 v[216:219], v[240:243], v[128:135], %[ix]\n\t" "s_nop 1\n\t" "v_smfmac_f32_16x16x64_bf16 v[220:223], v[240:243], v[144:151], %[ix]\n\t" \
    "v_cvt_pk_bf16_f32 v224, v208, v210\n\t" "v_cvt_pk_bf16_f32 v225, v209, v211\n\t" "v_smfmac_f32_16x16x64_bf16 v[216:219], v[244:247], v[136:143], %[ix]\n\t" \
    "v_cvt_pk_bf16_f32 v226, v212, v214\n\t" "v_cvt_pk_bf16_f32 v227, v213, v215\n\t" "v_smfmac_f32_16x16x64_bf16 v[220:223], v[244:247], v[152:159], %[ix]\n\t" "s_nop 0\n\t" \
    "v_permlane32_swap_b32 v224, v226\n\t" "v_smfmac_f32_16x16x64_bf16 v[216:219], v[248:251], a[240:247], %[ix]\n\t" "v_permlane32_swap_b32 v225, v227\n\t" "s_nop 0\n\t" \
    "v_smfmac_f32_16x16x64_bf16 v[220:223], v[248:251], a[248:255], %[ix]\n\t" "v_permlane16_swap_b32 v224, v225\n\t" "v_permlane16_swap_b32 v226, v227\n\t" \
    "s_waitcnt lgkmcnt(2)\n\t" "v_smfmac_f32_16x16x64_bf16 v[216:219], v[252:255], v[176:183], %[ix]\n\t" "buffer_store_dwordx4 v[224:227], %[uo], %[rs], s61 offen sc1\n\t" \
    "s_xor_b32 s61, s61, 0x40000\n\t" "s_waitcnt lgkmcnt(0)\n\t" "v_smfmac_f32_16x16x64_bf16 v[220:223], v[252:255], v[184:191], %[ix]\n\t" "s_nop 5\n\t" \
    "v_cvt_pk_bf16_f32 v228, v216, v218\n\t" "v_cvt_pk_bf16_f32 v229, v217, v219\n\t" "v_cvt_pk_bf16_f32 v230, v220, v222\n\t" "v_cvt_pk_bf16_f32 v231, v221, v223\n\t" \
    "s_nop 0\n\t" "v_permlane32_swap_b32 v228, v230\n\t" "v_permlane32_swap_b32 v229, v231\n\t" "s_nop 1\n\t" "v_permlane16_swap_b32 v228, v229\n\t" \
    "v_permlane16_swap_b32 v230, v231\n\t" "buffer_store_dwordx4 v[228:231], %[uo], %[rs], s61 offen sc1\n\t" "s_nop 1\n\t" XT(96, 97) "v_mov_b64 v[16:17], -1\n\t" \
    "v_mov_b64 v[18:19], -1\n\t" "s_barrier\n\t" "buffer_store_dwordx4 v[16:19], %[l1], %[rs], %[ur] offen offset:0 sc1\n\t" \
    "buffer_store_dwordx4 v[16:19], %[l1], %[rs], %[ur] offen offset:1024 sc1\n\t" "buffer_store_dwordx4 v[16:19], %[l1], %[rs], %[ur] offen offset:2048 sc1\n\t" \
    "buffer_store_dwordx4 v[16:19], %[l1], %[rs], %[ur] offen offset:3072 sc1\n\t" "s_sub_u32 s61, %[ur], 0x4000\n\t" \
    "buffer_store_dwordx4 v[16:19], %[l1], %[rs], s61 offen offset:2048 sc1\n\t" "s_branch 99f\n\t" "31:\n\t" "ds_read_b128 v[228:231], %[l1] offset:8192\n\t" \
    "s_waitcnt lgkmcnt(0)\n\t" "v_max3_u32 v225, v228, v229, v230\n\t" "v_max_u32 v225, v225, v231\n\t" "ds_read_b128 v[228:231], %[l1] offset:9216\n\t" \
    "s_waitcnt lgkmcnt(0)\n\t" "v_max3_u32 v225, v225, v228, v229\n\t" "v_max3_u32 v225, v225, v230, v231\n\t" "ds_read_b128 v[228:231], %[l1] offset:10240\n\t" \
    "s_waitcnt lgkmcnt(0)\n\t" "v_max3_u32 v225, v225, v228, v229\n\t" "v_max3_u32 v225, v225, v230, v231\n\t" "ds_read_b128 v[228:231], %[l1] offset:11264\n\t" \
    "s_waitcnt lgkmcnt(0)\n\t" "v_max3_u32 v225, v225, v228, v229\n\t" "v_max3_u32 v225, v225, v230, v231\n\t" "v_cmp_eq_u32 vcc, -1, v225\n\t" "s_cbranch_vccz 32b\n\t" \
    "s_cmp_eq_u32 %[rt], 1\n\t" "s_cbranch_scc1 32b\n\t" "v_mov_b32 v225, %[rt]\n\t" "ds_write_b32 v227, v225\n\t" "s_waitcnt lgkmcnt(0)\n\t" "s_branch 32b\n\t" "33:\n\t" \
    "s_sub_u32 %[rt], %[rt], 1\n\t" "s_waitcnt lgkmcnt(0)\n\t" XCNT(100) "34:\n\t" "buffer_load_dwordx4 v[240:243], %[mo], %[rs], %[sb] offen sc1\n\t" \
    "buffer_load_dwordx4 v[244:247], v224, %[rs], %[sb] offen sc1\n\t" "buffer_load_dwordx4 v[248:251], %[mo], %[rs], s60 offen sc1\n\t" \
    "buffer_load_dwordx4 v[252:255], v224, %[rs], s60 offen sc1\n\t" "s_waitcnt vmcnt(0)\n\t" "v_max_u32 v225, v240, v241\n\t" "v_max3_u32 v225, v225, v242, v243\n\t" \
    "v_max3_u32 v225, v225, v244, v245\n\t" "v_max3_u32 v225, v225, v246, v247\n\t" "v_max3_u32 v225, v225, v248, v249\n\t" "v_max3_u32 v225, v225, v250, v251\n\t" \
    "v_max3_u32 v225, v225, v252, v253\n\t" "v_max3_u32 v225, v225, v254, v255\n\t" "v_cmp_eq_u32 vcc, -1, v225\n\t" "s_cbranch_vccz 35f\n\t" "s_cmp_eq_u32 %[bd], 0\n\t" \
    "s_cbranch_scc1 35f\n\t" "s_sub_u32 %[bd], %[bd], 1\n\t" XCNT(101) "s_branch 34b\n\t" "35:\n\t" "ds_read_b128 v[160:163], %[l0] offset:0\n\t" \
    "ds_read_b128 v[164:167], %[l0] offset:1024\n\t" "ds_read_b128 v[168:171], %[l0] offset:2048\n\t" "ds_read_b128 v[172:175], %[l0] offset:3072\n\t" \
    "ds_read_b128 v[176:179], %[l0] offset:4096\n\t" "ds_read_b128 v[180:183], %[l0] offset:5120\n\t" "ds_read_b128 v[184:187], %[l0] offset:6144\n\t" \
    "ds_read_b128 v[188:191], %[l0] offset:7168\n\t" "v_mov_b64 v[232:233], 0\n\t" "v_mov_b64 v[234:235], 0\n\t" "v_mov_b64 v[236:237], 0\n\t" "v_mov_b64 v[238:239], 0\n\t" \
    "s_barrier\n\t" "s_branch 30b\n\t" "93:\n\t" XT(80, 81) "s_barrier\n\t" XT(82, 83) "buffer_load_dwordx4 v[240:243], %[mo], %[rs], %[sb] offen sc1\n\t" \
    "v_mov_b64 v[232:233], 0\n\t" "v_mov_b64 v[234:235], 0\n\t" "v_mov_b64 v[236:237], 0\n\t" "v_mov_b64 v[238:239], 0\n\t" "v_mov_b64 v[192:193], 0\n\t" \
    "v_mov_b64 v[194:195], 0\n\t" "v_mov_b64 v[196:197], 0\n\t" "v_mov_b64 v[198:199], 0\n\t" "buffer_load_dwordx4 v[244:247], v224, %[rs], %[sb] offen sc1\n\t" \
    "ds_read_b128 v[160:163], %[l0] offset:0\n\t" "ds_read_b128 v[164:167], %[l0] offset:1024\n\t" "ds_read_b128 v[168:171], %[l0] offset:2048\n\t" \
    "ds_read_b128 v[172:175], %[l0] offset:3072\n\t" "s_nop 3\n\t" "buffer_load_dwordx4 v[248:251], %[mo], %[rs], s60 offen sc1\n\t" \
    "ds_read_b128 v[176:179], %[l0] offset:4096\n\t" "ds_read_b128 v[180:183], %[l0] offset:5120\n\t" "ds_read_b128 v[184:187], %[l0] offset:6144\n\t" \
    "ds_read_b128 v[188:191], %[l0] offset:7168\n\t" "s_nop 3\n\t" "buffer_load_dwordx4 v[252:255], v224, %[rs], s60 offen sc1\n\t" "v_mov_b64 v[200:201], 0\n\t" \
    "v_mov_b64 v[202:203], 0\n\t" "v_mov_b64 v[204:205], 0\n\t" "v_mov_b64 v[206:207], 0\n\t" "v_mov_b64 v[208:209], 0\n\t" "v_mov_b64 v[210:211], 0\n\t" \
    "v_mov_b64 v[212:213], 0\n\t" "v_mov_b64 v[214:215], 0\n\t" "v_mov_b64 v[216:217], 0\n\t" "v_mov_b64 v[218:219], 0\n\t" "v_mov_b64 v[220:221], 0\n\t" \
    "v_mov_b64 v[222:223], 0\n\t" "v_mov_b32 v227, 160512\n\t" "40:\n\t" "s_waitcnt vmcnt(3)\n\t" XT(84, 85) \
    "v_smfmac_f32_16x16x64_bf16 v[232:235], v[240:243], a[0:7], %[ix]\n\t" "ds_write_b128 %[l1], v[240:243] offset:12288\n\t" \
    "v_smfmac_f32_16x16x64_bf16 v[236:239], v[240:243], a[8:15], %[ix]\n\t" "s_waitcnt vmcnt(2)\n\t" "v_smfmac_f32_16x16x64_bf16 v[232:235], v[244:247], a[16:23], %[ix]\n\t" \
    "ds_write_b128 %[l1], v[244:247] offset:13312\n\t" "s_waitcnt lgkmcnt(8)\n\t" "v_smfmac_f32_16x16x64_bf16 v[236:239], v[244:247], v[160:167], %[ix]\n\t" \
    "s_waitcnt lgkmcnt(0)\n\t" "s_barrier\n\t" XT(86, 87) "ds_read_b128 v[16:19], %[l1] offset:0\n\t" "ds_read_b128 v[20:23], %[l1] offset:4096\n\t" \
    "ds_read_b128 v[24:27], %[l1] offset:8192\n\t" "s_waitcnt vmcnt(1)\n\t" "v_smfmac_f32_16x16x64_bf16 v[232:235], v[248:251], a[24:31], %[ix]\n\t" \
    "ds_write_b128 %[l1], v[248:251] offset:14336\n\t" "v_smfmac_f32_16x16x64_bf16 v[236:239], v[248:251], a[32:39], %[ix]\n\t" "ds_read_b128 v[28:31], %[l1] offset:1024\n\t" \
    "ds_read_b128 v[240:243], %[l1] offset:5120\n\t" "ds_read_b128 v[244:247], %[l1] offset:9216\n\t" "s_waitcnt vmcnt(0)\n\t" \
    "v_smfmac_f32_16x16x64_bf16 v[232:235], v[252:255], a[40:47], %[ix]\n\t" "ds_write_b128 %[l1], v[252:255] offset:15360\n\t" \
    "v_smfmac_f32_16x16x64_bf16 v[236:239], v[252:255], v[168:175], %[ix]\n\t" "ds_read_b128 v[160:163], %[l0] offset:8192\n\t" \
    "ds_read_b128 v[164:167], %[l0] offset:9216\n\t" "ds_read_b128 v[168:171], %[l0] offset:10240\n\t" "ds_read_b128 v[172:175], %[l0] offset:11264\n\t" "s_nop 1\n\t" \
    "v_cmp_u_f32 vcc, v232, v233\n\t" "v_cmp_u_f32 s[62:63], v234, v235\n\t" "s_waitcnt lgkmcnt(11)\n\t" \
    "v_smfmac_f32_16x16x64_bf16 v[232:235], v[16:19], a[48:55], %[ix]\n\t" "v_smfmac_f32_16x16x64_bf16 v[236:239], v[16:19], a[56:63], %[ix]\n\t" \
    "s_or_b64 vcc, vcc, s[62:63]\n\t" "s_cbranch_scc1 41f\n\t" "42:\n\t" "s_waitcnt lgkmcnt(10)\n\t" "v_smfmac_f32_16x16x64_bf16 v[232:235], v[20:23], a[64:71], %[ix]\n\t" \
    "v_smfmac_f32_16x16x64_bf16 v[236:239], v[20:23], v[176:183], %[ix]\n\t" "s_waitcnt lgkmcnt(4)\n\t" "s_barrier\n\t" XT(88, 89) "ds_read_b32 v225, v227\n\t" \
    "v_smfmac_f32_16x16x64_bf16 v[232:235], v[24:27], a[72:79], %[ix]\n\t" "ds_read_b128 v[176:179], %[l0] offset:12288\n\t" "ds_read_b128 v[180:183], %[l0] offset:13312\n\t" \
    "v_smfmac_f32_16x16x64_bf16 v[236:239], v[24:27], a[80:87], %[ix]\n\t" "ds_read_b128 v[248:251], %[l1] offset:2048\n\t" "ds_read_b128 v[252:255], %[l1] offset:6144\n\t" \
    "v_smfmac_f32_16x16x64_bf16 v[232:235], v[28:31], a[88:95], %[ix]\n\t" "ds_read_b128 v[16:19], %[l1] offset:10240\n\t" "ds_read_b128 v[20:23], %[l1] offset:3072\n\t" \
    "v_smfmac_f32_16x16x64_bf16 v[236:239], v[28:31], v[184:191], %[ix]\n\t" "ds_read_b128 v[24:27], %[l1] offset:7168\n\t" "ds_read_b128 v[184:187], %[l0] offset:14336\n\t" \
    "v_smfmac_f32_16x16x64_bf16 v[232:235], v[240:243], a[96:103], %[ix]\n\t" "ds_read_b128 v[188:191], %[l0] offset:15360\n\t" \
    "ds_read_b128 v[28:31], %[l1] offset:11264\n\t" "v_smfmac_f32_16x16x64_bf16 v[236:239], v[240:243], a[104:111], %[ix]\n\t" "s_waitcnt lgkmcnt(10)\n\t" \
    "v_cmp_eq_u32 vcc, %[rt], v225\n\t" "v_smfmac_f32_16x16x64_bf16 v[232:235], v[244:247], a[112:119], %[ix]\n\t" "s_cbranch_vccnz 43f\n\t" \
    "v_smfmac_f32_16x16x64_bf16 v[236:239], v[244:247], v[160:167], %[ix]\n\t" "ds_read_b128 v[160:163], %[l0] offset:16384\n\t" \
    "ds_read_b128 v[164:167], %[l0] offset:17408\n\t" "s_waitcnt lgkmcnt(9)\n\t" "v_smfmac_f32_16x16x64_bf16 v[232:235], v[248:251], a[120:127], %[ix]\n\t" \
    "v_smfmac_f32_16x16x64_bf16 v[236:239], v[248:251], a[128:135], %[ix]\n\t" "s_waitcnt lgkmcnt(8)\n\t" \
    "v_smfmac_f32_16x16x64_bf16 v[232:235], v[252:255], a[136:143], %[ix]\n\t" "v_smfmac_f32_16x16x64_bf16 v[236:239], v[252:255], v[168:175], %[ix]\n\t" \
    "ds_read_b128 v[168:171], %[l0] offset:18432\n\t" "ds_read_b128 v[172:175], %[l0] offset:19456\n\t" "s_waitcnt lgkmcnt(9)\n\t" \
    "v_smfmac_f32_16x16x64_bf16 v[232:235], v[16:19], a[144:151], %[ix]\n\t" "v_smfmac_f32_16x16x64_bf16 v[236:239], v[16:19], a[152:159], %[ix]\n\t" \
    "s_waitcnt lgkmcnt(8)\n\t" "v_smfmac_f32_16x16x64_bf16 v[232:235], v[20:23], a[160:167], %[ix]\n\t" \
    "v_smfmac_f32_16x16x64_bf16 v[236:239], v[20:23], v[176:183], %[ix]\n\t" "ds_read_b128 v[176:179], %[l0] offset:20480\n\t" \
    "ds_read_b128 v[180:183], %[l0] offset:21504\n\t" "s_waitcnt lgkmcnt(9)\n\t" "v_smfmac_f32_16x16x64_bf16 v[232:235], v[24:27], a[168:175], %[ix]\n\t" \
    "v_smfmac_f32_16x16x64_bf16 v[236:239], v[24:27], a[176:183], %[ix]\n\t" "s_waitcnt lgkmcnt(6)\n\t" \
    "v_smfmac_f32_16x16x64_bf16 v[232:235], v[28:31], a[184:191], %[ix]\n\t" "v_smfmac_f32_16x16x64_bf16 v[236:239], v[28:31], v[184:191], %[ix]\n\t" \
    "ds_read_b128 v[184:187], %[l0] offset:22528\n\t" "ds_read_b128 v[188:191], %[l0] offset:23552\n\t" XT(90, 91) "s_nop 4\n\t" "v_add_f32 v232, v232, v233\n\t" \
    "v_add_f32 v234, v234, v235\n\t" "v_add_f32 v236, v236, v237\n\t" "v_add_f32 v238, v238, v239\n\t" "v_cvt_pk_bf16_f32 v236, v232, v236\n\t" \
    "v_cvt_pk_bf16_f32 v237, v234, v238\n\t" "ds_write2_b32 %[vw], v236, v237 offset1:68\n\t" "s_waitcnt lgkmcnt(0)\n\t" "s_barrier\n\t" XT(92, 93) \
    "ds_read_b128 v[240:243], %[vr] offset:0\n\t" "ds_read_b128 v[244:247], %[vr] offset:64\n\t" "ds_read_b128 v[248:251], %[vr] offset:128\n\t" \
    "ds_read_b128 v[252:255], %[vr] offset:192\n\t" "s_mov_b32 s61, %[ub]\n\t" "s_waitcnt lgkmcnt(3)\n\t" \
    "v_smfmac_f32_16x16x64_bf16 v[192:195], v[240:243], v[32:39], %[ix]\n\t" "v_smfmac_f32_16x16x64_bf16 v[196:199], v[240:243], v[48:55], %[ix]\n\t" \
    "s_waitcnt lgkmcnt(2)\n\t" "v_smfmac_f32_16x16x64_bf16 v[192:195], v[244:247], v[40:47], %[ix]\n\t" \
    "v_smfmac_f32_16x16x64_bf16 v[196:199], v[244:247], v[56:63], %[ix]\n\t" "s_waitcnt lgkmcnt(1)\n\t" \
    "v_smfmac_f32_16x16x64_bf16 v[192:195], v[248:251], a[192:199], %[ix]\n\t" "v_smfmac_f32_16x16x64_bf16 v[196:199], v[248:251], a[200:207], %[ix]\n\t" \
    "s_waitcnt lgkmcnt(0)\n\t" "v_smfmac_f32_16x16x64_bf16 v[192:195], v[252:255], v[160:167], %[ix]\n\t" "ds_read_b128 v[160:163], %[l0] offset:24576\n\t" \
    "ds_read_b128 v[164:167], %[l0] offset:25600\n\t" "v_smfmac_f32_16x16x64_bf16 v[196:199], v[252:255], v[168:175], %[ix]\n\t" \
    "ds_read_b128 v[168:171], %[l0] offset:26624\n\t" "ds_read_b128 v[172:175], %[l0] offset:27648\n\t" \
    "v_smfmac_f32_16x16x64_bf16 v[200:203], v[240:243], v[64:71], %[ix]\n\t" "s_nop 1\n\t" "v_smfmac_f32_16x16x64_bf16 v[204:207], v[240:243], v[80:87], %[ix]\n\t" \
    "v_cvt_pk_bf16_f32 v224, v192, v194\n\t" "v_cvt_pk_bf16_f32 v225, v193, v195\n\t" "v_smfmac_f32_16x16x64_bf16 v[200:203], v[244:247], v[72:79], %[ix]\n\t" \
    "v_cvt_pk_bf16_f32 v226, v196, v198\n\t" "v_cvt_pk_bf16_f32 v227, v197, v199\n\t" "v_smfmac_f32_16x16x64_bf16 v[204:207], v[244:247], v[88:95], %[ix]\n\t" "s_nop 0\n\t" \
    "v_permlane32_swap_b32 v224, v226\n\t" "v_smfmac_f32_16x16x64_bf16 v[200:203], v[248:251], a[208:215], %[ix]\n\t" "v_permlane32_swap_b32 v225, v227\n\t" "s_nop 0\n\t" \
    "v_smfmac_f32_16x16x64_bf16 v[204:207], v[248:251], a[216:223], %[ix]\n\t" "v_permlane16_swap_b32 v224, v225\n\t" "v_permlane16_swap_b32 v226, v227\n\t" \
    "v_smfmac_f32_16x16x64_bf16 v[200:203], v[252:255], v[176:183], %[ix]\n\t" "ds_read_b128 v[176:179], %[l0] offset:28672\n\t" \
    "buffer_store_dwordx4 v[224:227], %[uo], %[rs], s61 offen sc1\n\t" "s_xor_b32 s61, s61, 0x40000\n\t" XT(94, 95) \
    "v_smfmac_f32_16x16x64_bf16 v[204:207], v[252:255], v[184:191], %[ix]\n\t" "ds_read_b128 v[180:183], %[l0] offset:29696\n\t" \
    "ds_read_b128 v[184:187], %[l0] offset:30720\n\t" "v_smfmac_f32_16x16x64_bf16 v[208:211], v[240:243], v[96:103], %[ix]\n\t" \
    "ds_read_b128 v[188:191], %[l0] offset:31744\n\t" "s_nop 0\n\t" "v_smfmac_f32_16x16x64_bf16 v[212:215], v[240:243], v[112:119], %[ix]\n\t" \
    "v_cvt_pk_bf16_f32 v228, v200, v202\n\t" "v_cvt_pk_bf16_f32 v229, v201, v203\n\t" "v_smfmac_f32_16x16x64_bf16 v[208:211], v[244:247], v[104:111], %[ix]\n\t" \
    "v_cvt_pk_bf16_f32 v230, v204, v206\n\t" "v_cvt_pk_bf16_f32 v231, v205, v207\n\t" "v_smfmac_f32_16x16x64_bf16 v[212:215], v[244:247], v[120:127], %[ix]\n\t" "s_nop 0\n\t" \
    "v_permlane32_swap_b32 v228, v230\n\t" "v_smfmac_f32_16x16x64_bf16 v[208:211], v[248:251], a[224:231], %[ix]\n\t" "v_permlane32_swap_b32 v229, v231\n\t" "s_nop 0\n\t" \
    "v_smfmac_f32_16x16x64_bf16 v[212:215], v[248:251], a[232:239], %[ix]\n\t" "v_permlane16_swap_b32 v228, v229\n\t" "v_permlane16_swap_b32 v230, v231\n\t" \
    "s_waitcnt lgkmcnt(6)\n\t" "v_smfmac_f32_16x16x64_bf16 v[208:211], v[252:255], v[160:167], %[ix]\n\t" "buffer_store_dwordx4 v[228:231], %[uo], %[rs], s61 offen sc1\n\t" \
    "s_xor_b32 s61, s61, 0xc0000\n\t" "s_waitcnt lgkmcnt(4)\n\t" "v_smfmac_f32_16x16x64_bf16 v[212:215], v[252:255], v[168:175], %[ix]\n\t" "s_nop 1\n\t" \
    "v_smfmac_f32_16x16x64_bf16 v[216:219], v[240:243], v[128:135], %[ix]\n\t" "s_nop 1\n\t" "v_smfmac_f32_16x16x64_bf16 v[220:223], v[240:243], v[144:151], %[ix]\n\t" \
    "v_cvt_pk_bf16_f32 v224, v208, v210\n\t" "v_cvt_pk_bf16_f32 v225, v209, v211\n\t" "v_smfmac_f32_16x16x64_bf16 v[216:219], v[244:247], v[136:143], %[ix]\n\t" \
    "v_cvt_pk_bf16_f32 v226, v212, v214\n\t" "v_cvt_pk_bf16_f32 v227, v213, v215\n\t" "v_smfmac_f32_16x16x64_bf16 v[220:223], v[244:247], v[152:159], %[ix]\n\t" "s_nop 0\n\t" \
    "v_permlane32_swap_b32 v224, v226\n\t" "v_smfmac_f32_16x16x64_bf16 v[216:219], v[248:251], a[240:247], %[ix]\n\t" "v_permlane32_swap_b32 v225, v227\n\t" "s_nop 0\n\t" \
    "v_smfmac_f32_16x16x64_bf16 v[220:223], v[248:251], a[248:255], %[ix]\n\t" "v_permlane16_swap_b32 v224, v225\n\t" "v_permlane16_swap_b32 v226, v227\n\t" \
    "s_waitcnt lgkmcnt(2)\n\t" "v_smfmac_f32_16x16x64_bf16 v[216:219], v[252:255], v[176:183], %[ix]\n\t" "buffer_store_dwordx4 v[224:227], %[uo], %[rs], s61 offen sc1\n\t" \
    "s_xor_b32 s61, s61, 0x40000\n\t" "s_waitcnt lgkmcnt(0)\n\t" "v_smfmac_f32_16x16x64_bf16 v[220:223], v[252:255], v[184:191], %[ix]\n\t" "s_nop 5\n\t" \
    "v_cvt_pk_bf16_f32 v228, v216, v218\n\t" "v_cvt_pk_bf16_f32 v229, v217, v219\n\t" "v_cvt_pk_bf16_f32 v230, v220, v222\n\t" "v_cvt_pk_bf16_f32 v231, v221, v223\n\t" \
    "s_nop 0\n\t" "v_permlane32_swap_b32 v228, v230\n\t" "v_permlane32_swap_b32 v229, v231\n\t" "s_nop 1\n\t" "v_permlane16_swap_b32 v228, v229\n\t" \
    "v_permlane16_swap_b32 v230, v231\n\t" "buffer_store_dwordx4 v[228:231], %[uo], %[rs], s61 offen sc1\n\t" "s_nop 1\n\t" XT(96, 97) "v_mov_b64 v[16:17], -1\n\t" \
    "v_mov_b64 v[18:19], -1\n\t" "s_barrier\n\t" "buffer_store_dwordx4 v[16:19], %[l1], %[rs], %[ur] offen offset:0 sc1\n\t" \
    "buffer_store_dwordx4 v[16:19], %[l1], %[rs], %[ur] offen offset:1024 sc1\n\t" "buffer_store_dwordx4 v[16:19], %[l1], %[rs], %[ur] offen offset:2048 sc1\n\t" \
    "buffer_store_dwordx4 v[16:19], %[l1], %[rs], %[ur] offen offset:3072 sc1\n\t" "s_sub_u32 s61, %[ur], 0x5000\n\t" \
    "buffer_store_dwordx4 v[16:19], %[l1], %[rs], s61 offen offset:3072 sc1\n\t" "s_branch 99f\n\t" "41:\n\t" "ds_read_b128 v[228:231], %[l1] offset:12288\n\t" \
    "s_waitcnt lgkmcnt(0)\n\t" "v_max3_u32 v225, v228, v229, v230\n\t" "v_max_u32 v225, v225, v231\n\t" "ds_read_b128 v[228:231], %[l1] offset:13312\n\t" \
    "s_waitcnt lgkmcnt(0)\n\t" "v_max3_u32 v225, v225, v228, v229\n\t" "v_max3_u32 v225, v225, v230, v231\n\t" "ds_read_b128 v[228:231], %[l1] offset:14336\n\t" \
    "s_waitcnt lgkmcnt(0)\n\t" "v_max3_u32 v225, v225, v228, v229\n\t" "v_max3_u32 v225, v225, v230, v231\n\t" "ds_read_b128 v[228:231], %[l1] offset:15360\n\t" \
    "s_waitcnt lgkmcnt(0)\n\t" "v_max3_u32 v225, v225, v228, v229\n\t" "v_max3_u32 v225, v225, v230, v231\n\t" "v_cmp_eq_u32 vcc, -1, v225\n\t" "s_cbranch_vccz 42b\n\t" \
    "s_cmp_eq_u32 %[rt], 1\n\t" "s_cbranch_scc1 42b\n\t" "v_mov_b32 v225, %[rt]\n\t" "ds_write_b32 v227, v225\n\t" "s_waitcnt lgkmcnt(0)\n\t" "s_branch 42b\n\t" "43:\n\t" \
    "s_sub_u32 %[rt], %[rt], 1\n\t" "s_waitcnt lgkmcnt(0)\n\t" XCNT(100) "44:\n\t" "buffer_load_dwordx4 v[240:243], %[mo], %[rs], %[sb] offen sc1\n\t" \
    "buffer_load_dwordx4 v[244:247], v224, %[rs], %[sb] offen sc1\n\t" "buffer_load_dwordx4 v[248:251], %[mo], %[rs], s60 offen sc1\n\t" \
    "buffer_load_dwordx4 v[252:255], v224, %[rs], s60 offen sc1\n\t" "s_waitcnt vmcnt(0)\n\t" "v_max_u32 v225, v240, v241\n\t" "v_max3_u32 v225, v225, v242, v243\n\t" \
    "v_max3_u32 v225, v225, v244, v245\n\t" "v_max3_u32 v225, v225, v246, v247\n\t" "v_max3_u32 v225, v225, v248, v249\n\t" "v_max3_u32 v225, v225, v250, v251\n\t" \
    "v_max3_u32 v225, v225, v252, v253\n\t" "v_max3_u32 v225, v225, v254, v255\n\t" "v_cmp_eq_u32 vcc, -1, v225\n\t" "s_cbranch_vccz 45f\n\t" "s_cmp_eq_u32 %[bd], 0\n\t" \
    "s_cbranch_scc1 45f\n\t" "s_sub_u32 %[bd], %[bd], 1\n\t" XCNT(101) "s_branch 44b\n\t" "45:\n\t" "ds_read_b128 v[160:163], %[l0] offset:0\n\t" \
    "ds_read_b128 v[164:167], %[l0] offset:1024\n\t" "ds_read_b128 v[168:171], %[l0] offset:2048\n\t" "ds_read_b128 v[172:175], %[l0] offset:3072\n\t" \
    "ds_read_b128 v[176:179], %[l0] offset:4096\n\t" "ds_read_b128 v[180:183], %[l0] offset:5120\n\t" "ds_read_b128 v[184:187], %[l0] offset:6144\n\t" \
    "ds_read_b128 v[188:191], %[l0] offset:7168\n\t" "v_mov_b64 v[232:233], 0\n\t" "v_mov_b64 v[234:235], 0\n\t" "v_mov_b64 v[236:237], 0\n\t" "v_mov_b64 v[238:239], 0\n\t" \
    "s_barrier\n\t" "s_branch 40b\n\t" "99:\n\t"
#define Y_STEP \
    ".p2align 6\n\t" "v_xor_b32 v128, 0, %[u0]\n\t" "v_xor_b32 v132, 0, %[pm]\n\t" "v_xor_b32 v129, 2048, %[u0]\n\t" "v_xor_b32 v133, 1024, %[pm]\n\t" \
    "v_xor_b32 v130, 4096, %[u0]\n\t" "v_xor_b32 v134, 2048, %[pm]\n\t" "v_xor_b32 v131, 6144, %[u0]\n\t" "v_xor_b32 v135, 3072, %[pm]\n\t" "s_getpc_b64 s[62:63]\n\t" \
    "11:\n\t" "s_memtime s[64:65]\n\t" "s_waitcnt lgkmcnt(0)\n\t" "s_sub_u32 s61, %[tu], s64\n\t" "s_cmp_gt_i32 s61, 124\n\t" "s_cbranch_scc1 11b\n\t" \
    "s_max_i32 s61, s61, 0\n\t" "s_and_b32 s61, s61, 124\n\t" "s_sub_u32 s61, 12f-11b, s61\n\t" "s_add_u32 s62, s62, s61\n\t" "s_addc_u32 s63, s63, 0\n\t" \
    "s_setpc_b64 s[62:63]\n\t" ".rept 31\n\t" "s_nop 0\n\t" ".endr\n\t" "12:\n\t" XT(90, 91) "buffer_load_dwordx4 v[144:147], v128, %[rs], %[ub] offen offset:0 sc1\n\t" \
    "buffer_load_dwordx4 v[148:151], v128, %[rs], %[ub] offen offset:1024 sc1\n\t" "buffer_load_dwordx4 v[152:155], v129, %[rs], %[ub] offen offset:0 sc1\n\t" \
    "buffer_load_dwordx4 v[156:159], v129, %[rs], %[ub] offen offset:1024 sc1\n\t" "s_cmp_lg_u32 %[late], 0\n\t" "s_cbranch_scc1 14f\n\t" "s_getpc_b64 s[62:63]\n\t" "13:\n\t" \
    "s_memtime s[64:65]\n\t" "s_waitcnt lgkmcnt(0)\n\t" "s_sub_u32 s61, %[tg], s64\n\t" "s_cmp_gt_i32 s61, 124\n\t" "s_cbranch_scc1 13b\n\t" "s_max_i32 s61, s61, 0\n\t" \
    "s_and_b32 s61, s61, 124\n\t" "s_sub_u32 s61, 14f-13b, s61\n\t" "s_add_u32 s62, s62, s61\n\t" "s_addc_u32 s63, s63, 0\n\t" "s_setpc_b64 s[62:63]\n\t" ".rept 31\n\t" \
    "s_nop 0\n\t" ".endr\n\t" "14:\n\t" XT(92, 93) "20:\n\t" "buffer_load_dwordx4 v[160:163], v130, %[rs], %[ub] offen offset:0 sc1\n\t" \
    "buffer_load_dwordx4 v[164:167], v130, %[rs], %[ub] offen offset:1024 sc1\n\t" "buffer_load_dwordx4 v[192:195], %[x0], %[rs], %[pb] offen offset:0 sc1\n\t" \
    "buffer_load_dwordx4 v[196:199], %[x0], %[rs], %[pb] offen offset:1024 sc1\n\t" "buffer_load_dwordx4 v[200:203], %[x0], %[rs], %[pb] offen offset:2048 sc1\n\t" \
    "buffer_load_dwordx4 v[204:207], %[x0], %[rs], %[pb] offen offset:3072 sc1\n\t" "buffer_load_dwordx4 v[176:179], v132, %[rs], %[pb] offen sc1\n\t" \
    "v_mov_b64 v[120:121], 0\n\t" "v_mov_b64 v[122:123], 0\n\t" "v_mov_b64 v[136:137], 0\n\t" "v_mov_b64 v[138:139], 0\n\t" \
    "buffer_load_dwordx4 v[208:211], %[x1], %[rs], %[pb] offen offset:0 sc1\n\t" "buffer_load_dwordx4 v[212:215], %[x1], %[rs], %[pb] offen offset:1024 sc1\n\t" \
    "buffer_load_dwordx4 v[216:219], %[x1], %[rs], %[pb] offen offset:2048 sc1\n\t" "buffer_load_dwordx4 v[220:223], %[x1], %[rs], %[pb] offen offset:3072 sc1\n\t" \
    "buffer_load_dwordx4 v[180:183], v133, %[rs], %[pb] offen sc1\n\t" "buffer_load_dwordx4 v[224:227], %[x2], %[rs], %[pb] offen offset:0 sc1\n\t" \
    "buffer_load_dwordx4 v[228:231], %[x2], %[rs], %[pb] offen offset:1024 sc1\n\t" "buffer_load_dwordx4 v[232:235], %[x2], %[rs], %[pb] offen offset:2048 sc1\n\t" \
    "buffer_load_dwordx4 v[236:239], %[x2], %[rs], %[pb] offen offset:3072 sc1\n\t" "buffer_load_dwordx4 v[184:187], v134, %[rs], %[pb] offen sc1\n\t" \
    "buffer_load_dwordx4 v[168:171], v131, %[rs], %[ub] offen offset:0 sc1\n\t" "buffer_load_dwordx4 v[172:175], v131, %[rs], %[ub] offen offset:1024 sc1\n\t" \
    "buffer_load_dwordx4 v[240:243], %[x3], %[rs], %[pb] offen offset:0 sc1\n\t" "buffer_load_dwordx4 v[244:247], %[x3], %[rs], %[pb] offen offset:1024 sc1\n\t" \
    "buffer_load_dwordx4 v[248:251], %[x3], %[rs], %[pb] offen offset:2048 sc1\n\t" "buffer_load_dwordx4 v[252:255], %[x3], %[rs], %[pb] offen offset:3072 sc1\n\t" \
    "buffer_load_dwordx4 v[188:191], v135, %[rs], %[pb] offen sc1\n\t" "buffer_load_dword %[sv], %[so], %[rs], %[pb] offen sc1\n\t" "v_mov_b32 v127, -1\n\t" \
    "s_waitcnt vmcnt(25)\n\t" "v_permlane16_swap_b32 v144, v145\n\t" "v_permlane16_swap_b32 v146, v147\n\t" "v_permlane16_swap_b32 v148, v149\n\t" \
    "v_permlane16_swap_b32 v150, v151\n\t" "v_permlane16_swap_b32 v152, v153\n\t" "v_permlane16_swap_b32 v154, v155\n\t" "v_permlane16_swap_b32 v156, v157\n\t" \
    "v_permlane16_swap_b32 v158, v159\n\t" "s_waitcnt vmcnt(22)\n\t" XT(94, 95) "v_smfmac_f32_16x16x64_bf16 v[136:139], v[192:195], a[0:7], %[ix]\n\t" \
    "s_waitcnt vmcnt(21)\n\t" "v_smfmac_f32_16x16x64_bf16 v[120:123], v[196:199], a[8:15], %[ix]\n\t" "v_permlane16_swap_b32 v160, v161\n\t" "s_waitcnt vmcnt(20)\n\t" \
    "v_smfmac_f32_16x16x64_bf16 v[136:139], v[200:203], a[16:23], %[ix]\n\t" "s_waitcnt vmcnt(19)\n\t" \
    "v_smfmac_f32_16x16x64_bf16 v[120:123], v[204:207], a[24:31], %[ix]\n\t" "v_permlane16_swap_b32 v162, v163\n\t" "s_waitcnt vmcnt(18)\n\t" \
    "v_smfmac_f32_16x16x64_bf16 v[136:139], v[176:179], v[144:151], %[ix]\n\t" "s_waitcnt vmcnt(17)\n\t" \
    "v_smfmac_f32_16x16x64_bf16 v[120:123], v[208:211], a[32:39], %[ix]\n\t" "s_waitcnt vmcnt(16)\n\t" \
    "v_smfmac_f32_16x16x64_bf16 v[136:139], v[212:215], a[40:47], %[ix]\n\t" "v_permlane16_swap_b32 v164, v165\n\t" "s_waitcnt vmcnt(15)\n\t" \
    "v_smfmac_f32_16x16x64_bf16 v[120:123], v[216:219], a[48:55], %[ix]\n\t" "s_waitcnt vmcnt(14)\n\t" \
    "v_smfmac_f32_16x16x64_bf16 v[136:139], v[220:223], a[56:63], %[ix]\n\t" "v_permlane16_swap_b32 v166, v167\n\t" "s_waitcnt vmcnt(13)\n\t" \
    "v_smfmac_f32_16x16x64_bf16 v[120:123], v[180:183], v[152:159], %[ix]\n\t" "s_waitcnt vmcnt(12)\n\t" \
    "v_smfmac_f32_16x16x64_bf16 v[136:139], v[224:227], a[64:71], %[ix]\n\t" "s_waitcnt vmcnt(11)\n\t" \
    "v_smfmac_f32_16x16x64_bf16 v[120:123], v[228:231], a[72:79], %[ix]\n\t" "s_waitcnt vmcnt(10)\n\t" \
    "v_smfmac_f32_16x16x64_bf16 v[136:139], v[232:235], a[80:87], %[ix]\n\t" "s_waitcnt vmcnt(9)\n\t" "v_smfmac_f32_16x16x64_bf16 v[120:123], v[236:239], a[88:95], %[ix]\n\t" \
    "s_waitcnt vmcnt(8)\n\t" "v_smfmac_f32_16x16x64_bf16 v[136:139], v[184:187], v[160:167], %[ix]\n\t" "s_waitcnt vmcnt(5)\n\t" \
    "v_smfmac_f32_16x16x64_bf16 v[120:123], v[240:243], a[96:103], %[ix]\n\t" "v_permlane16_swap_b32 v168, v169\n\t" "v_permlane16_swap_b32 v170, v171\n\t" \
    "s_waitcnt vmcnt(4)\n\t" "v_smfmac_f32_16x16x64_bf16 v[136:139], v[244:247], a[104:111], %[ix]\n\t" "v_permlane16_swap_b32 v172, v173\n\t" \
    "v_permlane16_swap_b32 v174, v175\n\t" "s_waitcnt vmcnt(3)\n\t" "v_smfmac_f32_16x16x64_bf16 v[120:123], v[248:251], a[112:119], %[ix]\n\t" "s_waitcnt vmcnt(2)\n\t" \
    "v_smfmac_f32_16x16x64_bf16 v[120:123], v[252:255], a[120:127], %[ix]\n\t" "s_waitcnt vmcnt(1)\n\t" \
    "v_smfmac_f32_16x16x64_bf16 v[120:123], v[188:191], v[168:175], %[ix]\n\t" XT(82, 83) "v_add_f32 v136, v136, v137\n\t" "v_add_f32 v138, v138, v139\n\t" "s_nop 5\n\t" \
    "v_add_f32 v120, v120, v121\n\t" "v_add_f32 v122, v122, v123\n\t" "v_add_f32 v120, v120, v136\n\t" "v_add_f32 v122, v122, v138\n\t" \
    "v_cvt_pk_bf16_f32 v124, v120, v122\n\t" "v_cmp_u_f32 vcc, v120, v122\n\t" "s_cbranch_vccnz 30f\n\t" "21:\n\t" \
    "v_mov_b32_dpp v125, v124 quad_perm:[1,0,3,2] row_mask:0xf bank_mask:0xf\n\t" "v_perm_b32 v124, v125, v124, %[ps]\n\t" \
    "buffer_store_dword v124, %[mw], %[rs], %[mb] offen sc1\n\t" XT(84, 85) "buffer_store_dword v127, %[mw], %[rs], %[mr] offen sc1\n\t" "s_memtime s[64:65]\n\t" \
    "s_barrier\n\t" XT(98, 99) "v_permlane16_swap_b32 v144, v145\n\t" "v_permlane16_swap_b32 v146, v147\n\t" "v_permlane16_swap_b32 v148, v149\n\t" \
    "v_permlane16_swap_b32 v150, v151\n\t" "v_permlane16_swap_b32 v152, v153\n\t" "v_permlane16_swap_b32 v154, v155\n\t" "v_permlane16_swap_b32 v156, v157\n\t" \
    "v_permlane16_swap_b32 v158, v159\n\t" "v_permlane16_swap_b32 v160, v161\n\t" "v_permlane16_swap_b32 v162, v163\n\t" "v_permlane16_swap_b32 v164, v165\n\t" \
    "v_permlane16_swap_b32 v166, v167\n\t" "v_permlane16_swap_b32 v168, v169\n\t" "v_permlane16_swap_b32 v170, v171\n\t" "v_permlane16_swap_b32 v172, v173\n\t" \
    "v_permlane16_swap_b32 v174, v175\n\t" "s_waitcnt lgkmcnt(0)\n\t" "s_and_b32 %[bad], s64, -2\n\t" "s_branch 40f\n\t" "30:\n\t" "s_cmp_lg_u32 %[late], 0\n\t" \
    "s_cbranch_scc1 32f\n\t" XCNT(100) "s_mov_b32 %[bad], 1\n\t" "s_waitcnt vmcnt(0)\n\t" "s_branch 40f\n\t" "32:\n\t" "v_max_u32 v126, v144, v145\n\t" \
    "v_max3_u32 v126, v126, v146, v147\n\t" "v_max3_u32 v126, v126, v148, v149\n\t" "v_max3_u32 v126, v126, v150, v151\n\t" "v_max3_u32 v126, v126, v152, v153\n\t" \
    "v_max3_u32 v126, v126, v154, v155\n\t" "v_max3_u32 v126, v126, v156, v157\n\t" "v_max3_u32 v126, v126, v158, v159\n\t" "v_max3_u32 v126, v126, v160, v161\n\t" \
    "v_max3_u32 v126, v126, v162, v163\n\t" "v_max3_u32 v126, v126, v164, v165\n\t" "v_max3_u32 v126, v126, v166, v167\n\t" "v_max3_u32 v126, v126, v168, v169\n\t" \
    "v_max3_u32 v126, v126, v170, v171\n\t" "v_max3_u32 v126, v126, v172, v173\n\t" "v_max3_u32 v126, v126, v174, v175\n\t" UCNT "v_max3_u32 v126, v126, v176, v177\n\t" \
    "v_max3_u32 v126, v126, v178, v179\n\t" "v_max3_u32 v126, v126, v180, v181\n\t" "v_max3_u32 v126, v126, v182, v183\n\t" "v_max3_u32 v126, v126, v184, v185\n\t" \
    "v_max3_u32 v126, v126, v186, v187\n\t" "v_max3_u32 v126, v126, v188, v189\n\t" "v_max3_u32 v126, v126, v190, v191\n\t" "v_max3_u32 v126, v126, v192, v193\n\t" \
    "v_max3_u32 v126, v126, v194, v195\n\t" "v_max3_u32 v126, v126, v196, v197\n\t" "v_max3_u32 v126, v126, v198, v199\n\t" "v_max3_u32 v126, v126, v200, v201\n\t" \
    "v_max3_u32 v126, v126, v202, v203\n\t" "v_max3_u32 v126, v126, v204, v205\n\t" "v_max3_u32 v126, v126, v206, v207\n\t" "v_max3_u32 v126, v126, v208, v209\n\t" \
    "v_max3_u32 v126, v126, v210, v211\n\t" "v_max3_u32 v126, v126, v212, v213\n\t" "v_max3_u32 v126, v126, v214, v215\n\t" "v_max3_u32 v126, v126, v216, v217\n\t" \
    "v_max3_u32 v126, v126, v218, v219\n\t" "v_max3_u32 v126, v126, v220, v221\n\t" "v_max3_u32 v126, v126, v222, v223\n\t" "v_max3_u32 v126, v126, v224, v225\n\t" \
    "v_max3_u32 v126, v126, v226, v227\n\t" "v_max3_u32 v126, v126, v228, v229\n\t" "v_max3_u32 v126, v126, v230, v231\n\t" "v_max3_u32 v126, v126, v232, v233\n\t" \
    "v_max3_u32 v126, v126, v234, v235\n\t" "v_max3_u32 v126, v126, v236, v237\n\t" "v_max3_u32 v126, v126, v238, v239\n\t" "v_max3_u32 v126, v126, v240, v241\n\t" \
    "v_max3_u32 v126, v126, v242, v243\n\t" "v_max3_u32 v126, v126, v244, v245\n\t" "v_max3_u32 v126, v126, v246, v247\n\t" "v_max3_u32 v126, v126, v248, v249\n\t" \
    "v_max3_u32 v126, v126, v250, v251\n\t" "v_max3_u32 v126, v126, v252, v253\n\t" "v_max3_u32 v126, v126, v254, v255\n\t" "v_cmp_eq_u32 vcc, -1, v126\n\t" \
    "s_cbranch_vccz 21b\n\t" "s_cmp_eq_u32 %[bd], 0\n\t" "s_cbranch_scc1 21b\n\t" XCNT(100) "s_mov_b32 %[bad], 1\n\t" "s_waitcnt vmcnt(0)\n\t" "40:\n\t"
#define G_LOOP \
    ".p2align 6\n\t" "v_xor_b32 v72, 0, %[xo]\n\t" "v_add_u32 v80, 0x10000, v72\n\t" "v_xor_b32 v88, 0, %[xs]\n\t" "v_add_u32 v88, 0, v88\n\t" "v_xor_b32 v73, 64, %[xo]\n\t" \
    "v_add_u32 v81, 0x10000, v73\n\t" "v_xor_b32 v89, 16, %[xs]\n\t" "v_add_u32 v89, 2048, v89\n\t" "v_xor_b32 v74, 128, %[xo]\n\t" "v_add_u32 v82, 0x10000, v74\n\t" \
    "v_xor_b32 v90, 32, %[xs]\n\t" "v_add_u32 v90, 4096, v90\n\t" "v_xor_b32 v75, 192, %[xo]\n\t" "v_add_u32 v83, 0x10000, v75\n\t" "v_xor_b32 v91, 48, %[xs]\n\t" \
    "v_add_u32 v91, 6144, v91\n\t" "v_xor_b32 v76, 256, %[xo]\n\t" "v_add_u32 v84, 0x10000, v76\n\t" "v_xor_b32 v92, 64, %[xs]\n\t" "v_add_u32 v92, 8192, v92\n\t" \
    "v_xor_b32 v77, 320, %[xo]\n\t" "v_add_u32 v85, 0x10000, v77\n\t" "v_xor_b32 v93, 80, %[xs]\n\t" "v_add_u32 v93, 10240, v93\n\t" "v_xor_b32 v78, 384, %[xo]\n\t" \
    "v_add_u32 v86, 0x10000, v78\n\t" "v_xor_b32 v94, 96, %[xs]\n\t" "v_add_u32 v94, 12288, v94\n\t" "v_xor_b32 v79, 448, %[xo]\n\t" "v_add_u32 v87, 0x10000, v79\n\t" \
    "v_xor_b32 v95, 112, %[xs]\n\t" "v_add_u32 v95, 14336, v95\n\t" "v_mov_b32 v96, 2.0\n\t" "s_mov_b32 s60, 0\n\t" "s_mov_b32 s62, 0\n\t" "s_waitcnt vmcnt(0)\n\t" \
    "s_barrier\n\t" "ds_read_b128 v[100:103], v72 offset:0\n\t" "ds_read_b128 v[104:107], v72 offset:32768\n\t" "ds_read_b128 v[108:111], v73 offset:0\n\t" \
    "ds_read_b128 v[112:115], v73 offset:32768\n\t" "ds_read_b128 v[116:119], v74 offset:0\n\t" "10:\n\t" XT(80, 81) "s_waitcnt lgkmcnt(4)\n\t" \
    "v_mfma_f32_16x16x32_bf16 v[32:35], a[0:3], v[100:103], 0\n\t" "ds_read_b128 v[120:123], v74 offset:32768\n\t" "s_add_u32 s61, s60, 1\n\t" "s_and_b32 s61, s61, 63\n\t" \
    "v_mfma_f32_16x16x32_bf16 v[36:39], a[4:7], v[100:103], 0\n\t" "s_lshl_b32 s61, s61, 16\n\t" "s_add_u32 s63, %[lw], 65536\n\t" "s_waitcnt lgkmcnt(4)\n\t" \
    "v_mfma_f32_16x16x32_bf16 v[40:43], a[0:3], v[104:107], 0\n\t" "ds_read_b128 v[124:127], v75 offset:0\n\t" "v_mfma_f32_16x16x32_bf16 v[44:47], a[4:7], v[104:107], 0\n\t" \
    "s_waitcnt lgkmcnt(4)\n\t" "v_mfma_f32_16x16x32_bf16 v[32:35], a[8:11], v[108:111], v[32:35]\n\t" "ds_read_b128 v[128:131], v75 offset:32768\n\t" \
    "v_cvt_pk_bf16_f32 v64, v48, v49\n\t" "v_mfma_f32_16x16x32_bf16 v[36:39], a[12:15], v[108:111], v[36:39]\n\t" "v_cvt_pk_bf16_f32 v65, v50, v51\n\t" \
    "v_cvt_pk_bf16_f32 v66, v52, v53\n\t" "s_waitcnt lgkmcnt(4)\n\t" "v_mfma_f32_16x16x32_bf16 v[40:43], a[8:11], v[112:115], v[40:43]\n\t" \
    "ds_read_b128 v[100:103], v76 offset:0\n\t" "v_cvt_pk_bf16_f32 v67, v54, v55\n\t" "v_mfma_f32_16x16x32_bf16 v[44:47], a[12:15], v[112:115], v[44:47]\n\t" \
    "v_cvt_pk_bf16_f32 v68, v56, v57\n\t" "v_cvt_pk_bf16_f32 v69, v58, v59\n\t" "s_waitcnt lgkmcnt(4)\n\t" \
    "v_mfma_f32_16x16x32_bf16 v[32:35], a[16:19], v[116:119], v[32:35]\n\t" "ds_read_b128 v[104:107], v76 offset:32768\n\t" "v_cvt_pk_bf16_f32 v70, v60, v61\n\t" \
    "v_mfma_f32_16x16x32_bf16 v[36:39], a[20:23], v[116:119], v[36:39]\n\t" "v_cvt_pk_bf16_f32 v71, v62, v63\n\t" \
    "buffer_store_dwordx4 v[64:67], v96, %[rs], s62 offen offset:0 sc1\n\t" "s_waitcnt lgkmcnt(4)\n\t" "v_mfma_f32_16x16x32_bf16 v[40:43], a[16:19], v[120:123], v[40:43]\n\t" \
    "ds_read_b128 v[108:111], v77 offset:0\n\t" "v_mfma_f32_16x16x32_bf16 v[44:47], a[20:23], v[120:123], v[44:47]\n\t" "s_waitcnt lgkmcnt(4)\n\t" \
    "v_mfma_f32_16x16x32_bf16 v[32:35], a[24:27], v[124:127], v[32:35]\n\t" "ds_read_b128 v[112:115], v77 offset:32768\n\t" \
    "v_mfma_f32_16x16x32_bf16 v[36:39], a[28:31], v[124:127], v[36:39]\n\t" "buffer_store_dwordx4 v[68:71], v96, %[rs], s62 offen offset:256 sc1\n\t" \
    "s_waitcnt lgkmcnt(4)\n\t" "v_mfma_f32_16x16x32_bf16 v[40:43], a[24:27], v[128:131], v[40:43]\n\t" "ds_read_b128 v[116:119], v78 offset:0\n\t" "s_mov_b32 m0, s63\n\t" \
    "v_mfma_f32_16x16x32_bf16 v[44:47], a[28:31], v[128:131], v[44:47]\n\t" "s_add_u32 s63, s63, 0x800\n\t" "buffer_load_dwordx4 v88, %[rx], s61 offen lds\n\t" \
    "s_waitcnt lgkmcnt(4)\n\t" "v_mfma_f32_16x16x32_bf16 v[32:35], a[32:35], v[100:103], v[32:35]\n\t" "ds_read_b128 v[120:123], v78 offset:32768\n\t" \
    "v_mfma_f32_16x16x32_bf16 v[36:39], a[36:39], v[100:103], v[36:39]\n\t" "buffer_load_dwordx4 v88, %[rx], s61 offen offset:1024 lds\n\t" "s_waitcnt lgkmcnt(4)\n\t" \
    "v_mfma_f32_16x16x32_bf16 v[40:43], a[32:35], v[104:107], v[40:43]\n\t" "ds_read_b128 v[124:127], v79 offset:0\n\t" "s_mov_b32 m0, s63\n\t" \
    "v_mfma_f32_16x16x32_bf16 v[44:47], a[36:39], v[104:107], v[44:47]\n\t" "s_add_u32 s63, s63, 0x800\n\t" "buffer_load_dwordx4 v89, %[rx], s61 offen lds\n\t" \
    "s_waitcnt lgkmcnt(4)\n\t" "v_mfma_f32_16x16x32_bf16 v[32:35], a[40:43], v[108:111], v[32:35]\n\t" "ds_read_b128 v[128:131], v79 offset:32768\n\t" \
    "v_mfma_f32_16x16x32_bf16 v[36:39], a[44:47], v[108:111], v[36:39]\n\t" "buffer_load_dwordx4 v89, %[rx], s61 offen offset:1024 lds\n\t" "s_waitcnt lgkmcnt(4)\n\t" \
    "v_mfma_f32_16x16x32_bf16 v[40:43], a[40:43], v[112:115], v[40:43]\n\t" "ds_read_b128 v[100:103], v72 offset:512\n\t" "s_mov_b32 m0, s63\n\t" \
    "v_mfma_f32_16x16x32_bf16 v[44:47], a[44:47], v[112:115], v[44:47]\n\t" "s_add_u32 s63, s63, 0x800\n\t" "buffer_load_dwordx4 v90, %[rx], s61 offen lds\n\t" \
    "s_waitcnt lgkmcnt(4)\n\t" "v_mfma_f32_16x16x32_bf16 v[32:35], a[48:51], v[116:119], v[32:35]\n\t" "ds_read_b128 v[104:107], v72 offset:33280\n\t" \
    "v_mfma_f32_16x16x32_bf16 v[36:39], a[52:55], v[116:119], v[36:39]\n\t" "buffer_load_dwordx4 v90, %[rx], s61 offen offset:1024 lds\n\t" "s_waitcnt lgkmcnt(4)\n\t" \
    "v_mfma_f32_16x16x32_bf16 v[40:43], a[48:51], v[120:123], v[40:43]\n\t" "ds_read_b128 v[108:111], v73 offset:512\n\t" "s_mov_b32 m0, s63\n\t" \
    "v_mfma_f32_16x16x32_bf16 v[44:47], a[52:55], v[120:123], v[44:47]\n\t" "s_add_u32 s63, s63, 0x800\n\t" "buffer_load_dwordx4 v91, %[rx], s61 offen lds\n\t" \
    "s_waitcnt lgkmcnt(4)\n\t" "v_mfma_f32_16x16x32_bf16 v[32:35], a[56:59], v[124:127], v[32:35]\n\t" "ds_read_b128 v[112:115], v73 offset:33280\n\t" \
    "v_mfma_f32_16x16x32_bf16 v[36:39], a[60:63], v[124:127], v[36:39]\n\t" "buffer_load_dwordx4 v91, %[rx], s61 offen offset:1024 lds\n\t" "s_waitcnt lgkmcnt(4)\n\t" \
    "v_mfma_f32_16x16x32_bf16 v[40:43], a[56:59], v[128:131], v[40:43]\n\t" "ds_read_b128 v[116:119], v74 offset:512\n\t" "s_mov_b32 m0, s63\n\t" \
    "v_mfma_f32_16x16x32_bf16 v[44:47], a[60:63], v[128:131], v[44:47]\n\t" "s_add_u32 s63, s63, 0x800\n\t" "buffer_load_dwordx4 v92, %[rx], s61 offen lds\n\t" \
    "s_waitcnt lgkmcnt(4)\n\t" "v_mfma_f32_16x16x32_bf16 v[32:35], a[64:67], v[100:103], v[32:35]\n\t" "ds_read_b128 v[120:123], v74 offset:33280\n\t" \
    "v_mfma_f32_16x16x32_bf16 v[36:39], a[68:71], v[100:103], v[36:39]\n\t" "buffer_load_dwordx4 v92, %[rx], s61 offen offset:1024 lds\n\t" "s_waitcnt lgkmcnt(4)\n\t" \
    "v_mfma_f32_16x16x32_bf16 v[40:43], a[64:67], v[104:107], v[40:43]\n\t" "ds_read_b128 v[124:127], v75 offset:512\n\t" "s_mov_b32 m0, s63\n\t" \
    "v_mfma_f32_16x16x32_bf16 v[44:47], a[68:71], v[104:107], v[44:47]\n\t" "s_add_u32 s63, s63, 0x800\n\t" "buffer_load_dwordx4 v93, %[rx], s61 offen lds\n\t" \
    "s_waitcnt lgkmcnt(4)\n\t" "v_mfma_f32_16x16x32_bf16 v[32:35], a[72:75], v[108:111], v[32:35]\n\t" "ds_read_b128 v[128:131], v75 offset:33280\n\t" \
    "v_mfma_f32_16x16x32_bf16 v[36:39], a[76:79], v[108:111], v[36:39]\n\t" "buffer_load_dwordx4 v93, %[rx], s61 offen offset:1024 lds\n\t" "s_waitcnt lgkmcnt(4)\n\t" \
    "v_mfma_f32_16x16x32_bf16 v[40:43], a[72:75], v[112:115], v[40:43]\n\t" "ds_read_b128 v[100:103], v76 offset:512\n\t" "s_mov_b32 m0, s63\n\t" \
    "v_mfma_f32_16x16x32_bf16 v[44:47], a[76:79], v[112:115], v[44:47]\n\t" "s_add_u32 s63, s63, 0x800\n\t" "buffer_load_dwordx4 v94, %[rx], s61 offen lds\n\t" \
    "s_waitcnt lgkmcnt(4)\n\t" "v_mfma_f32_16x16x32_bf16 v[32:35], a[80:83], v[116:119], v[32:35]\n\t" "ds_read_b128 v[104:107], v76 offset:33280\n\t" \
    "v_mfma_f32_16x16x32_bf16 v[36:39], a[84:87], v[116:119], v[36:39]\n\t" "buffer_load_dwordx4 v94, %[rx], s61 offen offset:1024 lds\n\t" "s_waitcnt lgkmcnt(4)\n\t" \
    "v_mfma_f32_16x16x32_bf16 v[40:43], a[80:83], v[120:123], v[40:43]\n\t" "ds_read_b128 v[108:111], v77 offset:512\n\t" "s_mov_b32 m0, s63\n\t" \
    "v_mfma_f32_16x16x32_bf16 v[44:47], a[84:87], v[120:123], v[44:47]\n\t" "s_add_u32 s63, s63, 0x800\n\t" "buffer_load_dwordx4 v95, %[rx], s61 offen lds\n\t" \
    "s_waitcnt lgkmcnt(4)\n\t" "v_mfma_f32_16x16x32_bf16 v[32:35], a[88:91], v[124:127], v[32:35]\n\t" "ds_read_b128 v[112:115], v77 offset:33280\n\t" \
    "v_mfma_f32_16x16x32_bf16 v[36:39], a[92:95], v[124:127], v[36:39]\n\t" "buffer_load_dwordx4 v95, %[rx], s61 offen offset:1024 lds\n\t" "s_waitcnt lgkmcnt(4)\n\t" \
    "v_mfma_f32_16x16x32_bf16 v[40:43], a[88:91], v[128:131], v[40:43]\n\t" "ds_read_b128 v[116:119], v78 offset:512\n\t" \
    "v_mfma_f32_16x16x32_bf16 v[44:47], a[92:95], v[128:131], v[44:47]\n\t" "s_waitcnt lgkmcnt(4)\n\t" "v_mfma_f32_16x16x32_bf16 v[32:35], a[96:99], v[100:103], v[32:35]\n\t" \
    "ds_read_b128 v[120:123], v78 offset:33280\n\t" "s_lshr_b32 s64, s60, 2\n\t" "s_xor_b32 s64, s64, %[xp]\n\t" \
    "v_mfma_f32_16x16x32_bf16 v[36:39], a[100:103], v[100:103], v[36:39]\n\t" "s_and_b32 s64, s64, 1\n\t" "s_lshl_b32 s64, s64, 13\n\t" "s_waitcnt lgkmcnt(4)\n\t" \
    "v_mfma_f32_16x16x32_bf16 v[40:43], a[96:99], v[104:107], v[40:43]\n\t" "ds_read_b128 v[124:127], v79 offset:512\n\t" "s_lshl_b32 s62, s60, 19\n\t" \
    "v_mfma_f32_16x16x32_bf16 v[44:47], a[100:103], v[104:107], v[44:47]\n\t" "s_add_u32 s62, s62, s64\n\t" "s_add_u32 s62, s62, %[ob]\n\t" "s_waitcnt lgkmcnt(4)\n\t" \
    "v_mfma_f32_16x16x32_bf16 v[32:35], a[104:107], v[108:111], v[32:35]\n\t" "ds_read_b128 v[128:131], v79 offset:33280\n\t" "s_waitcnt vmcnt(16)\n\t" "s_barrier\n\t" \
    "v_mfma_f32_16x16x32_bf16 v[36:39], a[108:111], v[108:111], v[36:39]\n\t" "v_mov_b32 v98, s60\n\t" "buffer_store_dword v98, %[fo], %[rc], %[fl] offen sc1\n\t" \
    "s_waitcnt lgkmcnt(4)\n\t" "v_mfma_f32_16x16x32_bf16 v[40:43], a[104:107], v[112:115], v[40:43]\n\t" "ds_read_b128 v[100:103], v72 offset:1024\n\t" \
    "v_mfma_f32_16x16x32_bf16 v[44:47], a[108:111], v[112:115], v[44:47]\n\t" "s_waitcnt lgkmcnt(4)\n\t" \
    "v_mfma_f32_16x16x32_bf16 v[32:35], a[112:115], v[116:119], v[32:35]\n\t" "ds_read_b128 v[104:107], v72 offset:33792\n\t" \
    "v_mfma_f32_16x16x32_bf16 v[36:39], a[116:119], v[116:119], v[36:39]\n\t" "s_waitcnt lgkmcnt(4)\n\t" \
    "v_mfma_f32_16x16x32_bf16 v[40:43], a[112:115], v[120:123], v[40:43]\n\t" "ds_read_b128 v[108:111], v73 offset:1024\n\t" \
    "v_mfma_f32_16x16x32_bf16 v[44:47], a[116:119], v[120:123], v[44:47]\n\t" "s_waitcnt lgkmcnt(4)\n\t" \
    "v_mfma_f32_16x16x32_bf16 v[32:35], a[120:123], v[124:127], v[32:35]\n\t" "ds_read_b128 v[112:115], v73 offset:33792\n\t" \
    "v_mfma_f32_16x16x32_bf16 v[36:39], a[124:127], v[124:127], v[36:39]\n\t" "s_waitcnt lgkmcnt(4)\n\t" \
    "v_mfma_f32_16x16x32_bf16 v[40:43], a[120:123], v[128:131], v[40:43]\n\t" "ds_read_b128 v[116:119], v74 offset:1024\n\t" \
    "v_mfma_f32_16x16x32_bf16 v[44:47], a[124:127], v[128:131], v[44:47]\n\t" "s_waitcnt lgkmcnt(4)\n\t" \
    "v_mfma_f32_16x16x32_bf16 v[32:35], a[128:131], v[100:103], v[32:35]\n\t" "ds_read_b128 v[120:123], v74 offset:33792\n\t" \
    "v_mfma_f32_16x16x32_bf16 v[36:39], a[132:135], v[100:103], v[36:39]\n\t" "s_waitcnt lgkmcnt(4)\n\t" \
    "v_mfma_f32_16x16x32_bf16 v[40:43], a[128:131], v[104:107], v[40:43]\n\t" "ds_read_b128 v[124:127], v75 offset:1024\n\t" \
    "v_mfma_f32_16x16x32_bf16 v[44:47], a[132:135], v[104:107], v[44:47]\n\t" "s_waitcnt lgkmcnt(4)\n\t" \
    "v_mfma_f32_16x16x32_bf16 v[32:35], a[136:139], v[108:111], v[32:35]\n\t" "ds_read_b128 v[128:131], v75 offset:33792\n\t" \
    "v_mfma_f32_16x16x32_bf16 v[36:39], a[140:143], v[108:111], v[36:39]\n\t" "s_waitcnt lgkmcnt(4)\n\t" \
    "v_mfma_f32_16x16x32_bf16 v[40:43], a[136:139], v[112:115], v[40:43]\n\t" "ds_read_b128 v[100:103], v76 offset:1024\n\t" \
    "v_mfma_f32_16x16x32_bf16 v[44:47], a[140:143], v[112:115], v[44:47]\n\t" "s_waitcnt lgkmcnt(4)\n\t" \
    "v_mfma_f32_16x16x32_bf16 v[32:35], a[144:147], v[116:119], v[32:35]\n\t" "ds_read_b128 v[104:107], v76 offset:33792\n\t" \
    "v_mfma_f32_16x16x32_bf16 v[36:39], a[148:151], v[116:119], v[36:39]\n\t" "s_waitcnt lgkmcnt(4)\n\t" \
    "v_mfma_f32_16x16x32_bf16 v[40:43], a[144:147], v[120:123], v[40:43]\n\t" "ds_read_b128 v[108:111], v77 offset:1024\n\t" \
    "v_mfma_f32_16x16x32_bf16 v[44:47], a[148:151], v[120:123], v[44:47]\n\t" "s_waitcnt lgkmcnt(4)\n\t" \
    "v_mfma_f32_16x16x32_bf16 v[32:35], a[152:155], v[124:127], v[32:35]\n\t" "ds_read_b128 v[112:115], v77 offset:33792\n\t" \
    "v_mfma_f32_16x16x32_bf16 v[36:39], a[156:159], v[124:127], v[36:39]\n\t" "s_waitcnt lgkmcnt(4)\n\t" \
    "v_mfma_f32_16x16x32_bf16 v[40:43], a[152:155], v[128:131], v[40:43]\n\t" "ds_read_b128 v[116:119], v78 offset:1024\n\t" \
    "v_mfma_f32_16x16x32_bf16 v[44:47], a[156:159], v[128:131], v[44:47]\n\t" "s_waitcnt lgkmcnt(4)\n\t" \
    "v_mfma_f32_16x16x32_bf16 v[32:35], a[160:163], v[100:103], v[32:35]\n\t" "ds_read_b128 v[120:123], v78 offset:33792\n\t" \
    "v_mfma_f32_16x16x32_bf16 v[36:39], a[164:167], v[100:103], v[36:39]\n\t" "s_waitcnt lgkmcnt(4)\n\t" \
    "v_mfma_f32_16x16x32_bf16 v[40:43], a[160:163], v[104:107], v[40:43]\n\t" "ds_read_b128 v[124:127], v79 offset:1024\n\t" \
    "v_mfma_f32_16x16x32_bf16 v[44:47], a[164:167], v[104:107], v[44:47]\n\t" "s_waitcnt lgkmcnt(4)\n\t" \
    "v_mfma_f32_16x16x32_bf16 v[32:35], a[168:171], v[108:111], v[32:35]\n\t" "ds_read_b128 v[128:131], v79 offset:33792\n\t" \
    "v_mfma_f32_16x16x32_bf16 v[36:39], a[172:175], v[108:111], v[36:39]\n\t" "s_waitcnt lgkmcnt(4)\n\t" \
    "v_mfma_f32_16x16x32_bf16 v[40:43], a[168:171], v[112:115], v[40:43]\n\t" "ds_read_b128 v[100:103], v72 offset:1536\n\t" \
    "v_mfma_f32_16x16x32_bf16 v[44:47], a[172:175], v[112:115], v[44:47]\n\t" "s_waitcnt lgkmcnt(4)\n\t" \
    "v_mfma_f32_16x16x32_bf16 v[32:35], a[176:179], v[116:119], v[32:35]\n\t" "ds_read_b128 v[104:107], v72 offset:34304\n\t" \
    "v_mfma_f32_16x16x32_bf16 v[36:39], a[180:183], v[116:119], v[36:39]\n\t" "s_waitcnt lgkmcnt(4)\n\t" \
    "v_mfma_f32_16x16x32_bf16 v[40:43], a[176:179], v[120:123], v[40:43]\n\t" "ds_read_b128 v[108:111], v73 offset:1536\n\t" \
    "v_mfma_f32_16x16x32_bf16 v[44:47], a[180:183], v[120:123], v[44:47]\n\t" "s_waitcnt lgkmcnt(4)\n\t" \
    "v_mfma_f32_16x16x32_bf16 v[32:35], a[184:187], v[124:127], v[32:35]\n\t" "ds_read_b128 v[112:115], v73 offset:34304\n\t" \
    "v_mfma_f32_16x16x32_bf16 v[36:39], a[188:191], v[124:127], v[36:39]\n\t" "s_waitcnt lgkmcnt(4)\n\t" \
    "v_mfma_f32_16x16x32_bf16 v[40:43], a[184:187], v[128:131], v[40:43]\n\t" "ds_read_b128 v[116:119], v74 offset:1536\n\t" \
    "v_mfma_f32_16x16x32_bf16 v[44:47], a[188:191], v[128:131], v[44:47]\n\t" "s_waitcnt lgkmcnt(4)\n\t" \
    "v_mfma_f32_16x16x32_bf16 v[32:35], a[192:195], v[100:103], v[32:35]\n\t" "ds_read_b128 v[120:123], v74 offset:34304\n\t" \
    "v_mfma_f32_16x16x32_bf16 v[36:39], a[196:199], v[100:103], v[36:39]\n\t" "s_waitcnt lgkmcnt(4)\n\t" \
    "v_mfma_f32_16x16x32_bf16 v[40:43], a[192:195], v[104:107], v[40:43]\n\t" "ds_read_b128 v[124:127], v75 offset:1536\n\t" \
    "v_mfma_f32_16x16x32_bf16 v[44:47], a[196:199], v[104:107], v[44:47]\n\t" "s_waitcnt lgkmcnt(4)\n\t" \
    "v_mfma_f32_16x16x32_bf16 v[32:35], a[200:203], v[108:111], v[32:35]\n\t" "ds_read_b128 v[128:131], v75 offset:34304\n\t" \
    "v_mfma_f32_16x16x32_bf16 v[36:39], a[204:207], v[108:111], v[36:39]\n\t" "s_waitcnt lgkmcnt(4)\n\t" \
    "v_mfma_f32_16x16x32_bf16 v[40:43], a[200:203], v[112:115], v[40:43]\n\t" "ds_read_b128 v[100:103], v76 offset:1536\n\t" \
    "v_mfma_f32_16x16x32_bf16 v[44:47], a[204:207], v[112:115], v[44:47]\n\t" "s_waitcnt lgkmcnt(4)\n\t" \
    "v_mfma_f32_16x16x32_bf16 v[32:35], a[208:211], v[116:119], v[32:35]\n\t" "ds_read_b128 v[104:107], v76 offset:34304\n\t" \
    "v_mfma_f32_16x16x32_bf16 v[36:39], a[212:215], v[116:119], v[36:39]\n\t" "s_waitcnt lgkmcnt(4)\n\t" \
    "v_mfma_f32_16x16x32_bf16 v[40:43], a[208:211], v[120:123], v[40:43]\n\t" "ds_read_b128 v[108:111], v77 offset:1536\n\t" \
    "v_mfma_f32_16x16x32_bf16 v[44:47], a[212:215], v[120:123], v[44:47]\n\t" "s_waitcnt lgkmcnt(4)\n\t" \
    "v_mfma_f32_16x16x32_bf16 v[32:35], a[216:219], v[124:127], v[32:35]\n\t" "ds_read_b128 v[112:115], v77 offset:34304\n\t" \
    "v_mfma_f32_16x16x32_bf16 v[36:39], a[220:223], v[124:127], v[36:39]\n\t" "s_waitcnt lgkmcnt(4)\n\t" \
    "v_mfma_f32_16x16x32_bf16 v[40:43], a[216:219], v[128:131], v[40:43]\n\t" "ds_read_b128 v[116:119], v78 offset:1536\n\t" \
    "v_mfma_f32_16x16x32_bf16 v[44:47], a[220:223], v[128:131], v[44:47]\n\t" "s_waitcnt lgkmcnt(4)\n\t" \
    "v_mfma_f32_16x16x32_bf16 v[32:35], a[224:227], v[100:103], v[32:35]\n\t" "ds_read_b128 v[120:123], v78 offset:34304\n\t" \
    "v_mfma_f32_16x16x32_bf16 v[36:39], a[228:231], v[100:103], v[36:39]\n\t" "s_waitcnt lgkmcnt(4)\n\t" \
    "v_mfma_f32_16x16x32_bf16 v[40:43], a[224:227], v[104:107], v[40:43]\n\t" "ds_read_b128 v[124:127], v79 offset:1536\n\t" \
    "v_mfma_f32_16x16x32_bf16 v[44:47], a[228:231], v[104:107], v[44:47]\n\t" "s_waitcnt lgkmcnt(4)\n\t" \
    "v_mfma_f32_16x16x32_bf16 v[32:35], a[232:235], v[108:111], v[32:35]\n\t" "ds_read_b128 v[128:131], v79 offset:34304\n\t" \
    "v_mfma_f32_16x16x32_bf16 v[36:39], a[236:239], v[108:111], v[36:39]\n\t" "s_waitcnt lgkmcnt(4)\n\t" \
    "v_mfma_f32_16x16x32_bf16 v[40:43], a[232:235], v[112:115], v[40:43]\n\t" "v_mfma_f32_16x16x32_bf16 v[44:47], a[236:239], v[112:115], v[44:47]\n\t" \
    "s_waitcnt lgkmcnt(3)\n\t" "v_mfma_f32_16x16x32_bf16 v[32:35], a[240:243], v[116:119], v[32:35]\n\t" \
    "v_mfma_f32_16x16x32_bf16 v[36:39], a[244:247], v[116:119], v[36:39]\n\t" "s_waitcnt vmcnt(1) lgkmcnt(0)\n\t" "s_barrier\n\t" XT(82, 83) \
    "v_mfma_f32_16x16x32_bf16 v[40:43], a[240:243], v[120:123], v[40:43]\n\t" "ds_read_b128 v[100:103], v80 offset:0\n\t" \
    "v_mfma_f32_16x16x32_bf16 v[44:47], a[244:247], v[120:123], v[44:47]\n\t" "v_mfma_f32_16x16x32_bf16 v[32:35], a[248:251], v[124:127], v[32:35]\n\t" \
    "ds_read_b128 v[104:107], v80 offset:32768\n\t" "v_mfma_f32_16x16x32_bf16 v[36:39], a[252:255], v[124:127], v[36:39]\n\t" "ds_read_b128 v[108:111], v81 offset:0\n\t" \
    "v_mfma_f32_16x16x32_bf16 v[40:43], a[248:251], v[128:131], v[40:43]\n\t" "ds_read_b128 v[112:115], v81 offset:32768\n\t" "ds_read_b128 v[116:119], v82 offset:0\n\t" \
    "v_mfma_f32_16x16x32_bf16 v[44:47], a[252:255], v[128:131], v[44:47]\n\t" "s_add_u32 s60, s60, 1\n\t" GSUM "s_cmp_eq_u32 s60, 1\n\t" "s_cbranch_scc1 20f\n\t" "11:\n\t" \
    XT(80, 81) "s_waitcnt lgkmcnt(4)\n\t" "v_mfma_f32_16x16x32_bf16 v[48:51], a[0:3], v[100:103], 0\n\t" "ds_read_b128 v[120:123], v82 offset:32768\n\t" \
    "s_add_u32 s61, s60, 1\n\t" "s_and_b32 s61, s61, 63\n\t" "v_mfma_f32_16x16x32_bf16 v[52:55], a[4:7], v[100:103], 0\n\t" "s_lshl_b32 s61, s61, 16\n\t" \
    "s_add_u32 s63, %[lw], 0\n\t" "s_waitcnt lgkmcnt(4)\n\t" "v_mfma_f32_16x16x32_bf16 v[56:59], a[0:3], v[104:107], 0\n\t" "ds_read_b128 v[124:127], v83 offset:0\n\t" \
    "v_mfma_f32_16x16x32_bf16 v[60:63], a[4:7], v[104:107], 0\n\t" "s_waitcnt lgkmcnt(4)\n\t" "v_mfma_f32_16x16x32_bf16 v[48:51], a[8:11], v[108:111], v[48:51]\n\t" \
    "ds_read_b128 v[128:131], v83 offset:32768\n\t" "v_cvt_pk_bf16_f32 v64, v32, v33\n\t" "v_mfma_f32_16x16x32_bf16 v[52:55], a[12:15], v[108:111], v[52:55]\n\t" \
    "v_cvt_pk_bf16_f32 v65, v34, v35\n\t" "v_cvt_pk_bf16_f32 v66, v36, v37\n\t" "s_waitcnt lgkmcnt(4)\n\t" \
    "v_mfma_f32_16x16x32_bf16 v[56:59], a[8:11], v[112:115], v[56:59]\n\t" "ds_read_b128 v[100:103], v84 offset:0\n\t" "v_cvt_pk_bf16_f32 v67, v38, v39\n\t" \
    "v_mfma_f32_16x16x32_bf16 v[60:63], a[12:15], v[112:115], v[60:63]\n\t" "v_cvt_pk_bf16_f32 v68, v40, v41\n\t" "v_cvt_pk_bf16_f32 v69, v42, v43\n\t" \
    "s_waitcnt lgkmcnt(4)\n\t" "v_mfma_f32_16x16x32_bf16 v[48:51], a[16:19], v[116:119], v[48:51]\n\t" "ds_read_b128 v[104:107], v84 offset:32768\n\t" \
    "v_cvt_pk_bf16_f32 v70, v44, v45\n\t" "v_mfma_f32_16x16x32_bf16 v[52:55], a[20:23], v[116:119], v[52:55]\n\t" "v_cvt_pk_bf16_f32 v71, v46, v47\n\t" \
    "buffer_store_dwordx4 v[64:67], v96, %[rs], s62 offen offset:0 sc1\n\t" "s_waitcnt lgkmcnt(4)\n\t" "v_mfma_f32_16x16x32_bf16 v[56:59], a[16:19], v[120:123], v[56:59]\n\t" \
    "ds_read_b128 v[108:111], v85 offset:0\n\t" "v_mfma_f32_16x16x32_bf16 v[60:63], a[20:23], v[120:123], v[60:63]\n\t" "s_waitcnt lgkmcnt(4)\n\t" \
    "v_mfma_f32_16x16x32_bf16 v[48:51], a[24:27], v[124:127], v[48:51]\n\t" "ds_read_b128 v[112:115], v85 offset:32768\n\t" \
    "v_mfma_f32_16x16x32_bf16 v[52:55], a[28:31], v[124:127], v[52:55]\n\t" "buffer_store_dwordx4 v[68:71], v96, %[rs], s62 offen offset:256 sc1\n\t" \
    "s_waitcnt lgkmcnt(4)\n\t" "v_mfma_f32_16x16x32_bf16 v[56:59], a[24:27], v[128:131], v[56:59]\n\t" "ds_read_b128 v[116:119], v86 offset:0\n\t" "s_mov_b32 m0, s63\n\t" \
    "v_mfma_f32_16x16x32_bf16 v[60:63], a[28:31], v[128:131], v[60:63]\n\t" "s_add_u32 s63, s63, 0x800\n\t" "buffer_load_dwordx4 v88, %[rx], s61 offen lds\n\t" \
    "s_waitcnt lgkmcnt(4)\n\t" "v_mfma_f32_16x16x32_bf16 v[48:51], a[32:35], v[100:103], v[48:51]\n\t" "ds_read_b128 v[120:123], v86 offset:32768\n\t" \
    "v_mfma_f32_16x16x32_bf16 v[52:55], a[36:39], v[100:103], v[52:55]\n\t" "buffer_load_dwordx4 v88, %[rx], s61 offen offset:1024 lds\n\t" "s_waitcnt lgkmcnt(4)\n\t" \
    "v_mfma_f32_16x16x32_bf16 v[56:59], a[32:35], v[104:107], v[56:59]\n\t" "ds_read_b128 v[124:127], v87 offset:0\n\t" "s_mov_b32 m0, s63\n\t" \
    "v_mfma_f32_16x16x32_bf16 v[60:63], a[36:39], v[104:107], v[60:63]\n\t" "s_add_u32 s63, s63, 0x800\n\t" "buffer_load_dwordx4 v89, %[rx], s61 offen lds\n\t" \
    "s_waitcnt lgkmcnt(4)\n\t" "v_mfma_f32_16x16x32_bf16 v[48:51], a[40:43], v[108:111], v[48:51]\n\t" "ds_read_b128 v[128:131], v87 offset:32768\n\t" \
    "v_mfma_f32_16x16x32_bf16 v[52:55], a[44:47], v[108:111], v[52:55]\n\t" "buffer_load_dwordx4 v89, %[rx], s61 offen offset:1024 lds\n\t" "s_waitcnt lgkmcnt(4)\n\t" \
    "v_mfma_f32_16x16x32_bf16 v[56:59], a[40:43], v[112:115], v[56:59]\n\t" "ds_read_b128 v[100:103], v80 offset:512\n\t" "s_mov_b32 m0, s63\n\t" \
    "v_mfma_f32_16x16x32_bf16 v[60:63], a[44:47], v[112:115], v[60:63]\n\t" "s_add_u32 s63, s63, 0x800\n\t" "buffer_load_dwordx4 v90, %[rx], s61 offen lds\n\t" \
    "s_waitcnt lgkmcnt(4)\n\t" "v_mfma_f32_16x16x32_bf16 v[48:51], a[48:51], v[116:119], v[48:51]\n\t" "ds_read_b128 v[104:107], v80 offset:33280\n\t" \
    "v_mfma_f32_16x16x32_bf16 v[52:55], a[52:55], v[116:119], v[52:55]\n\t" "buffer_load_dwordx4 v90, %[rx], s61 offen offset:1024 lds\n\t" "s_waitcnt lgkmcnt(4)\n\t" \
    "v_mfma_f32_16x16x32_bf16 v[56:59], a[48:51], v[120:123], v[56:59]\n\t" "ds_read_b128 v[108:111], v81 offset:512\n\t" "s_mov_b32 m0, s63\n\t" \
    "v_mfma_f32_16x16x32_bf16 v[60:63], a[52:55], v[120:123], v[60:63]\n\t" "s_add_u32 s63, s63, 0x800\n\t" "buffer_load_dwordx4 v91, %[rx], s61 offen lds\n\t" \
    "s_waitcnt lgkmcnt(4)\n\t" "v_mfma_f32_16x16x32_bf16 v[48:51], a[56:59], v[124:127], v[48:51]\n\t" "ds_read_b128 v[112:115], v81 offset:33280\n\t" \
    "v_mfma_f32_16x16x32_bf16 v[52:55], a[60:63], v[124:127], v[52:55]\n\t" "buffer_load_dwordx4 v91, %[rx], s61 offen offset:1024 lds\n\t" "s_waitcnt lgkmcnt(4)\n\t" \
    "v_mfma_f32_16x16x32_bf16 v[56:59], a[56:59], v[128:131], v[56:59]\n\t" "ds_read_b128 v[116:119], v82 offset:512\n\t" "s_mov_b32 m0, s63\n\t" \
    "v_mfma_f32_16x16x32_bf16 v[60:63], a[60:63], v[128:131], v[60:63]\n\t" "s_add_u32 s63, s63, 0x800\n\t" "buffer_load_dwordx4 v92, %[rx], s61 offen lds\n\t" \
    "s_waitcnt lgkmcnt(4)\n\t" "v_mfma_f32_16x16x32_bf16 v[48:51], a[64:67], v[100:103], v[48:51]\n\t" "ds_read_b128 v[120:123], v82 offset:33280\n\t" \
    "v_mfma_f32_16x16x32_bf16 v[52:55], a[68:71], v[100:103], v[52:55]\n\t" "buffer_load_dwordx4 v92, %[rx], s61 offen offset:1024 lds\n\t" "s_waitcnt lgkmcnt(4)\n\t" \
    "v_mfma_f32_16x16x32_bf16 v[56:59], a[64:67], v[104:107], v[56:59]\n\t" "ds_read_b128 v[124:127], v83 offset:512\n\t" "s_mov_b32 m0, s63\n\t" \
    "v_mfma_f32_16x16x32_bf16 v[60:63], a[68:71], v[104:107], v[60:63]\n\t" "s_add_u32 s63, s63, 0x800\n\t" "buffer_load_dwordx4 v93, %[rx], s61 offen lds\n\t" \
    "s_waitcnt lgkmcnt(4)\n\t" "v_mfma_f32_16x16x32_bf16 v[48:51], a[72:75], v[108:111], v[48:51]\n\t" "ds_read_b128 v[128:131], v83 offset:33280\n\t" \
    "v_mfma_f32_16x16x32_bf16 v[52:55], a[76:79], v[108:111], v[52:55]\n\t" "buffer_load_dwordx4 v93, %[rx], s61 offen offset:1024 lds\n\t" "s_waitcnt lgkmcnt(4)\n\t" \
    "v_mfma_f32_16x16x32_bf16 v[56:59], a[72:75], v[112:115], v[56:59]\n\t" "ds_read_b128 v[100:103], v84 offset:512\n\t" "s_mov_b32 m0, s63\n\t" \
    "v_mfma_f32_16x16x32_bf16 v[60:63], a[76:79], v[112:115], v[60:63]\n\t" "s_add_u32 s63, s63, 0x800\n\t" "buffer_load_dwordx4 v94, %[rx], s61 offen lds\n\t" \
    "s_waitcnt lgkmcnt(4)\n\t" "v_mfma_f32_16x16x32_bf16 v[48:51], a[80:83], v[116:119], v[48:51]\n\t" "ds_read_b128 v[104:107], v84 offset:33280\n\t" \
    "v_mfma_f32_16x16x32_bf16 v[52:55], a[84:87], v[116:119], v[52:55]\n\t" "buffer_load_dwordx4 v94, %[rx], s61 offen offset:1024 lds\n\t" "s_waitcnt lgkmcnt(4)\n\t" \
    "v_mfma_f32_16x16x32_bf16 v[56:59], a[80:83], v[120:123], v[56:59]\n\t" "ds_read_b128 v[108:111], v85 offset:512\n\t" "s_mov_b32 m0, s63\n\t" \
    "v_mfma_f32_16x16x32_bf16 v[60:63], a[84:87], v[120:123], v[60:63]\n\t" "s_add_u32 s63, s63, 0x800\n\t" "buffer_load_dwordx4 v95, %[rx], s61 offen lds\n\t" \
    "s_waitcnt lgkmcnt(4)\n\t" "v_mfma_f32_16x16x32_bf16 v[48:51], a[88:91], v[124:127], v[48:51]\n\t" "ds_read_b128 v[112:115], v85 offset:33280\n\t" \
    "v_mfma_f32_16x16x32_bf16 v[52:55], a[92:95], v[124:127], v[52:55]\n\t" "buffer_load_dwordx4 v95, %[rx], s61 offen offset:1024 lds\n\t" "s_waitcnt lgkmcnt(4)\n\t" \
    "v_mfma_f32_16x16x32_bf16 v[56:59], a[88:91], v[128:131], v[56:59]\n\t" "ds_read_b128 v[116:119], v86 offset:512\n\t" \
    "v_mfma_f32_16x16x32_bf16 v[60:63], a[92:95], v[128:131], v[60:63]\n\t" "s_waitcnt lgkmcnt(4)\n\t" "v_mfma_f32_16x16x32_bf16 v[48:51], a[96:99], v[100:103], v[48:51]\n\t" \
    "ds_read_b128 v[120:123], v86 offset:33280\n\t" "s_lshr_b32 s64, s60, 2\n\t" "s_xor_b32 s64, s64, %[xp]\n\t" \
    "v_mfma_f32_16x16x32_bf16 v[52:55], a[100:103], v[100:103], v[52:55]\n\t" "s_and_b32 s64, s64, 1\n\t" "s_lshl_b32 s64, s64, 13\n\t" "s_waitcnt lgkmcnt(4)\n\t" \
    "v_mfma_f32_16x16x32_bf16 v[56:59], a[96:99], v[104:107], v[56:59]\n\t" "ds_read_b128 v[124:127], v87 offset:512\n\t" "s_lshl_b32 s62, s60, 19\n\t" \
    "v_mfma_f32_16x16x32_bf16 v[60:63], a[100:103], v[104:107], v[60:63]\n\t" "s_add_u32 s62, s62, s64\n\t" "s_add_u32 s62, s62, %[ob]\n\t" "s_waitcnt lgkmcnt(4)\n\t" \
    "v_mfma_f32_16x16x32_bf16 v[48:51], a[104:107], v[108:111], v[48:51]\n\t" "ds_read_b128 v[128:131], v87 offset:33280\n\t" "s_waitcnt vmcnt(16)\n\t" "s_barrier\n\t" \
    "v_mfma_f32_16x16x32_bf16 v[52:55], a[108:111], v[108:111], v[52:55]\n\t" "v_mov_b32 v98, s60\n\t" "buffer_store_dword v98, %[fo], %[rc], %[fl] offen sc1\n\t" \
    "s_waitcnt lgkmcnt(4)\n\t" "v_mfma_f32_16x16x32_bf16 v[56:59], a[104:107], v[112:115], v[56:59]\n\t" "ds_read_b128 v[100:103], v80 offset:1024\n\t" \
    "v_mfma_f32_16x16x32_bf16 v[60:63], a[108:111], v[112:115], v[60:63]\n\t" "s_waitcnt lgkmcnt(4)\n\t" \
    "v_mfma_f32_16x16x32_bf16 v[48:51], a[112:115], v[116:119], v[48:51]\n\t" "ds_read_b128 v[104:107], v80 offset:33792\n\t" \
    "v_mfma_f32_16x16x32_bf16 v[52:55], a[116:119], v[116:119], v[52:55]\n\t" "s_waitcnt lgkmcnt(4)\n\t" \
    "v_mfma_f32_16x16x32_bf16 v[56:59], a[112:115], v[120:123], v[56:59]\n\t" "ds_read_b128 v[108:111], v81 offset:1024\n\t" \
    "v_mfma_f32_16x16x32_bf16 v[60:63], a[116:119], v[120:123], v[60:63]\n\t" "s_waitcnt lgkmcnt(4)\n\t" \
    "v_mfma_f32_16x16x32_bf16 v[48:51], a[120:123], v[124:127], v[48:51]\n\t" "ds_read_b128 v[112:115], v81 offset:33792\n\t" \
    "v_mfma_f32_16x16x32_bf16 v[52:55], a[124:127], v[124:127], v[52:55]\n\t" "s_waitcnt lgkmcnt(4)\n\t" \
    "v_mfma_f32_16x16x32_bf16 v[56:59], a[120:123], v[128:131], v[56:59]\n\t" "ds_read_b128 v[116:119], v82 offset:1024\n\t" \
    "v_mfma_f32_16x16x32_bf16 v[60:63], a[124:127], v[128:131], v[60:63]\n\t" "s_waitcnt lgkmcnt(4)\n\t" \
    "v_mfma_f32_16x16x32_bf16 v[48:51], a[128:131], v[100:103], v[48:51]\n\t" "ds_read_b128 v[120:123], v82 offset:33792\n\t" \
    "v_mfma_f32_16x16x32_bf16 v[52:55], a[132:135], v[100:103], v[52:55]\n\t" "s_waitcnt lgkmcnt(4)\n\t" \
    "v_mfma_f32_16x16x32_bf16 v[56:59], a[128:131], v[104:107], v[56:59]\n\t" "ds_read_b128 v[124:127], v83 offset:1024\n\t" \
    "v_mfma_f32_16x16x32_bf16 v[60:63], a[132:135], v[104:107], v[60:63]\n\t" "s_waitcnt lgkmcnt(4)\n\t" \
    "v_mfma_f32_16x16x32_bf16 v[48:51], a[136:139], v[108:111], v[48:51]\n\t" "ds_read_b128 v[128:131], v83 offset:33792\n\t" \
    "v_mfma_f32_16x16x32_bf16 v[52:55], a[140:143], v[108:111], v[52:55]\n\t" "s_waitcnt lgkmcnt(4)\n\t" \
    "v_mfma_f32_16x16x32_bf16 v[56:59], a[136:139], v[112:115], v[56:59]\n\t" "ds_read_b128 v[100:103], v84 offset:1024\n\t" \
    "v_mfma_f32_16x16x32_bf16 v[60:63], a[140:143], v[112:115], v[60:63]\n\t" "s_waitcnt lgkmcnt(4)\n\t" \
    "v_mfma_f32_16x16x32_bf16 v[48:51], a[144:147], v[116:119], v[48:51]\n\t" "ds_read_b128 v[104:107], v84 offset:33792\n\t" \
    "v_mfma_f32_16x16x32_bf16 v[52:55], a[148:151], v[116:119], v[52:55]\n\t" "s_waitcnt lgkmcnt(4)\n\t" \
    "v_mfma_f32_16x16x32_bf16 v[56:59], a[144:147], v[120:123], v[56:59]\n\t" "ds_read_b128 v[108:111], v85 offset:1024\n\t" \
    "v_mfma_f32_16x16x32_bf16 v[60:63], a[148:151], v[120:123], v[60:63]\n\t" "s_waitcnt lgkmcnt(4)\n\t" \
    "v_mfma_f32_16x16x32_bf16 v[48:51], a[152:155], v[124:127], v[48:51]\n\t" "ds_read_b128 v[112:115], v85 offset:33792\n\t" \
    "v_mfma_f32_16x16x32_bf16 v[52:55], a[156:159], v[124:127], v[52:55]\n\t" "s_waitcnt lgkmcnt(4)\n\t" \
    "v_mfma_f32_16x16x32_bf16 v[56:59], a[152:155], v[128:131], v[56:59]\n\t" "ds_read_b128 v[116:119], v86 offset:1024\n\t" \
    "v_mfma_f32_16x16x32_bf16 v[60:63], a[156:159], v[128:131], v[60:63]\n\t" "s_waitcnt lgkmcnt(4)\n\t" \
    "v_mfma_f32_16x16x32_bf16 v[48:51], a[160:163], v[100:103], v[48:51]\n\t" "ds_read_b128 v[120:123], v86 offset:33792\n\t" \
    "v_mfma_f32_16x16x32_bf16 v[52:55], a[164:167], v[100:103], v[52:55]\n\t" "s_waitcnt lgkmcnt(4)\n\t" \
    "v_mfma_f32_16x16x32_bf16 v[56:59], a[160:163], v[104:107], v[56:59]\n\t" "ds_read_b128 v[124:127], v87 offset:1024\n\t" \
    "v_mfma_f32_16x16x32_bf16 v[60:63], a[164:167], v[104:107], v[60:63]\n\t" "s_waitcnt lgkmcnt(4)\n\t" \
    "v_mfma_f32_16x16x32_bf16 v[48:51], a[168:171], v[108:111], v[48:51]\n\t" "ds_read_b128 v[128:131], v87 offset:33792\n\t" \
    "v_mfma_f32_16x16x32_bf16 v[52:55], a[172:175], v[108:111], v[52:55]\n\t" "s_waitcnt lgkmcnt(4)\n\t" \
    "v_mfma_f32_16x16x32_bf16 v[56:59], a[168:171], v[112:115], v[56:59]\n\t" "ds_read_b128 v[100:103], v80 offset:1536\n\t" \
    "v_mfma_f32_16x16x32_bf16 v[60:63], a[172:175], v[112:115], v[60:63]\n\t" "s_waitcnt lgkmcnt(4)\n\t" \
    "v_mfma_f32_16x16x32_bf16 v[48:51], a[176:179], v[116:119], v[48:51]\n\t" "ds_read_b128 v[104:107], v80 offset:34304\n\t" \
    "v_mfma_f32_16x16x32_bf16 v[52:55], a[180:183], v[116:119], v[52:55]\n\t" "s_waitcnt lgkmcnt(4)\n\t" \
    "v_mfma_f32_16x16x32_bf16 v[56:59], a[176:179], v[120:123], v[56:59]\n\t" "ds_read_b128 v[108:111], v81 offset:1536\n\t" \
    "v_mfma_f32_16x16x32_bf16 v[60:63], a[180:183], v[120:123], v[60:63]\n\t" "s_waitcnt lgkmcnt(4)\n\t" \
    "v_mfma_f32_16x16x32_bf16 v[48:51], a[184:187], v[124:127], v[48:51]\n\t" "ds_read_b128 v[112:115], v81 offset:34304\n\t" \
    "v_mfma_f32_16x16x32_bf16 v[52:55], a[188:191], v[124:127], v[52:55]\n\t" "s_waitcnt lgkmcnt(4)\n\t" \
    "v_mfma_f32_16x16x32_bf16 v[56:59], a[184:187], v[128:131], v[56:59]\n\t" "ds_read_b128 v[116:119], v82 offset:1536\n\t" \
    "v_mfma_f32_16x16x32_bf16 v[60:63], a[188:191], v[128:131], v[60:63]\n\t" "s_waitcnt lgkmcnt(4)\n\t" \
    "v_mfma_f32_16x16x32_bf16 v[48:51], a[192:195], v[100:103], v[48:51]\n\t" "ds_read_b128 v[120:123], v82 offset:34304\n\t" \
    "v_mfma_f32_16x16x32_bf16 v[52:55], a[196:199], v[100:103], v[52:55]\n\t" "s_waitcnt lgkmcnt(4)\n\t" \
    "v_mfma_f32_16x16x32_bf16 v[56:59], a[192:195], v[104:107], v[56:59]\n\t" "ds_read_b128 v[124:127], v83 offset:1536\n\t" \
    "v_mfma_f32_16x16x32_bf16 v[60:63], a[196:199], v[104:107], v[60:63]\n\t" "s_waitcnt lgkmcnt(4)\n\t" \
    "v_mfma_f32_16x16x32_bf16 v[48:51], a[200:203], v[108:111], v[48:51]\n\t" "ds_read_b128 v[128:131], v83 offset:34304\n\t" \
    "v_mfma_f32_16x16x32_bf16 v[52:55], a[204:207], v[108:111], v[52:55]\n\t" "s_waitcnt lgkmcnt(4)\n\t" \
    "v_mfma_f32_16x16x32_bf16 v[56:59], a[200:203], v[112:115], v[56:59]\n\t" "ds_read_b128 v[100:103], v84 offset:1536\n\t" \
    "v_mfma_f32_16x16x32_bf16 v[60:63], a[204:207], v[112:115], v[60:63]\n\t" "s_waitcnt lgkmcnt(4)\n\t" \
    "v_mfma_f32_16x16x32_bf16 v[48:51], a[208:211], v[116:119], v[48:51]\n\t" "ds_read_b128 v[104:107], v84 offset:34304\n\t" \
    "v_mfma_f32_16x16x32_bf16 v[52:55], a[212:215], v[116:119], v[52:55]\n\t" "s_waitcnt lgkmcnt(4)\n\t" \
    "v_mfma_f32_16x16x32_bf16 v[56:59], a[208:211], v[120:123], v[56:59]\n\t" "ds_read_b128 v[108:111], v85 offset:1536\n\t" \
    "v_mfma_f32_16x16x32_bf16 v[60:63], a[212:215], v[120:123], v[60:63]\n\t" "s_waitcnt lgkmcnt(4)\n\t" \
    "v_mfma_f32_16x16x32_bf16 v[48:51], a[216:219], v[124:127], v[48:51]\n\t" "ds_read_b128 v[112:115], v85 offset:34304\n\t" \
    "v_mfma_f32_16x16x32_bf16 v[52:55], a[220:223], v[124:127], v[52:55]\n\t" "s_waitcnt lgkmcnt(4)\n\t" \
    "v_mfma_f32_16x16x32_bf16 v[56:59], a[216:219], v[128:131], v[56:59]\n\t" "ds_read_b128 v[116:119], v86 offset:1536\n\t" \
    "v_mfma_f32_16x16x32_bf16 v[60:63], a[220:223], v[128:131], v[60:63]\n\t" "s_waitcnt lgkmcnt(4)\n\t" \
    "v_mfma_f32_16x16x32_bf16 v[48:51], a[224:227], v[100:103], v[48:51]\n\t" "ds_read_b128 v[120:123], v86 offset:34304\n\t" \
    "v_mfma_f32_16x16x32_bf16 v[52:55], a[228:231], v[100:103], v[52:55]\n\t" "s_waitcnt lgkmcnt(4)\n\t" \
    "v_mfma_f32_16x16x32_bf16 v[56:59], a[224:227], v[104:107], v[56:59]\n\t" "ds_read_b128 v[124:127], v87 offset:1536\n\t" \
    "v_mfma_f32_16x16x32_bf16 v[60:63], a[228:231], v[104:107], v[60:63]\n\t" "s_waitcnt lgkmcnt(4)\n\t" \
    "v_mfma_f32_16x16x32_bf16 v[48:51], a[232:235], v[108:111], v[48:51]\n\t" "ds_read_b128 v[128:131], v87 offset:34304\n\t" \
    "v_mfma_f32_16x16x32_bf16 v[52:55], a[236:239], v[108:111], v[52:55]\n\t" "s_waitcnt lgkmcnt(4)\n\t" \
    "v_mfma_f32_16x16x32_bf16 v[56:59], a[232:235], v[112:115], v[56:59]\n\t" "v_mfma_f32_16x16x32_bf16 v[60:63], a[236:239], v[112:115], v[60:63]\n\t" \
    "s_waitcnt lgkmcnt(3)\n\t" "v_mfma_f32_16x16x32_bf16 v[48:51], a[240:243], v[116:119], v[48:51]\n\t" \
    "v_mfma_f32_16x16x32_bf16 v[52:55], a[244:247], v[116:119], v[52:55]\n\t" "s_waitcnt vmcnt(1) lgkmcnt(0)\n\t" "s_barrier\n\t" XT(82, 83) \
    "v_mfma_f32_16x16x32_bf16 v[56:59], a[240:243], v[120:123], v[56:59]\n\t" "ds_read_b128 v[100:103], v72 offset:0\n\t" \
    "v_mfma_f32_16x16x32_bf16 v[60:63], a[244:247], v[120:123], v[60:63]\n\t" "v_mfma_f32_16x16x32_bf16 v[48:51], a[248:251], v[124:127], v[48:51]\n\t" \
    "ds_read_b128 v[104:107], v72 offset:32768\n\t" "v_mfma_f32_16x16x32_bf16 v[52:55], a[252:255], v[124:127], v[52:55]\n\t" "ds_read_b128 v[108:111], v73 offset:0\n\t" \
    "v_mfma_f32_16x16x32_bf16 v[56:59], a[248:251], v[128:131], v[56:59]\n\t" "ds_read_b128 v[112:115], v73 offset:32768\n\t" "ds_read_b128 v[116:119], v74 offset:0\n\t" \
    "v_mfma_f32_16x16x32_bf16 v[60:63], a[252:255], v[128:131], v[60:63]\n\t" "s_add_u32 s60, s60, 1\n\t" GSUM "s_cmp_lt_u32 s60, 64\n\t" "s_cbranch_scc1 10b\n\t" \
    "s_nop 7\n\t" "v_cvt_pk_bf16_f32 v64, v48, v49\n\t" "v_cvt_pk_bf16_f32 v65, v50, v51\n\t" "v_cvt_pk_bf16_f32 v66, v52, v53\n\t" "v_cvt_pk_bf16_f32 v67, v54, v55\n\t" \
    "v_cvt_pk_bf16_f32 v68, v56, v57\n\t" "v_cvt_pk_bf16_f32 v69, v58, v59\n\t" "v_cvt_pk_bf16_f32 v70, v60, v61\n\t" "v_cvt_pk_bf16_f32 v71, v62, v63\n\t" \
    "buffer_store_dwordx4 v[64:67], v96, %[rs], s62 offen offset:0 sc1\n\t" "buffer_store_dwordx4 v[68:71], v96, %[rs], s62 offen offset:256 sc1\n\t" "s_waitcnt vmcnt(0)\n\t" \
    "s_barrier\n\t" "v_mov_b32 v98, s60\n\t" "buffer_store_dword v98, %[fo], %[rc], %[fl] offen sc1\n\t" "s_waitcnt lgkmcnt(0)\n\t" "s_branch 30f\n\t" "20:\n\t" \
    "v_mov_b32 v96, %[so]\n\t" "s_nop 7\n\t" "v_cvt_pk_bf16_f32 v64, v32, v33\n\t" "v_cvt_pk_bf16_f32 v65, v34, v35\n\t" "v_cvt_pk_bf16_f32 v66, v36, v37\n\t" \
    "v_cvt_pk_bf16_f32 v67, v38, v39\n\t" "v_cvt_pk_bf16_f32 v68, v40, v41\n\t" "v_cvt_pk_bf16_f32 v69, v42, v43\n\t" "v_cvt_pk_bf16_f32 v70, v44, v45\n\t" \
    "v_cvt_pk_bf16_f32 v71, v46, v47\n\t" "buffer_store_dwordx4 v[64:67], v96, %[rs], s62 offen offset:0 sc1\n\t" \
    "buffer_store_dwordx4 v[68:71], v96, %[rs], s62 offen offset:256 sc1\n\t" "s_waitcnt vmcnt(0)\n\t" "s_barrier\n\t" "v_mov_b32 v98, s60\n\t" \
    "buffer_store_dword v98, %[fo], %[rc], %[fl] offen sc1\n\t" "s_branch 11b\n\t" "30:\n\t"
#define ZC_LOOP \
    ".p2align 6\n\t" "s_mov_b32 s60, 0\n\t" "s_mov_b32 s61, 0\n\t" "s_mov_b32 s62, 0\n\t" "s_mov_b32 s63, 0x7f000000\n\t" "s_cmp_ge_u32 s61, %[z0]\n\t" \
    "s_cbranch_scc1 52f\n\t" "51:\n\t" "buffer_load_dword v120, %[fo], %[rc], %[fv] offen sc1\n\t" "buffer_load_dword v121, %[fo], %[rc], %[vd] offen sc1\n\t" \
    "s_waitcnt vmcnt(0)\n\t" "v_readfirstlane_b32 s61, v120\n\t" "v_readfirstlane_b32 s66, v121\n\t" "s_nop 3\n\t" "s_cmp_ge_u32 s61, %[z0]\n\t" "s_cbranch_scc1 52f\n\t" \
    "s_bitcmp1_b32 s66, 1\n\t" "s_cbranch_scc1 90f\n\t" "s_cmp_eq_u32 %[bd], 0\n\t" "s_cbranch_scc1 52f\n\t" "s_sub_u32 %[bd], %[bd], 1\n\t" "s_sleep 2\n\t" \
    "s_branch 51b\n\t" "52:\n\t" "s_lshr_b32 s66, s60, 2\n\t" "s_xor_b32 s66, s66, %[xp]\n\t" "s_and_b32 s66, s66, 1\n\t" "s_lshl_b32 s66, s66, 13\n\t" \
    "s_lshl_b32 s64, s60, 19\n\t" "s_add_u32 s64, s64, s66\n\t" "s_add_u32 s64, s64, %[vb]\n\t" "buffer_load_dwordx4 v[16:19], %[ao], %[rs], s64 offen offset:0 sc1\n\t" \
    "buffer_load_dwordx4 v[20:23], %[ao], %[rs], s64 offen offset:256 sc1\n\t" "buffer_load_dwordx4 v[24:27], %[ao], %[rs], s64 offen offset:2048 sc1\n\t" \
    "buffer_load_dwordx4 v[28:31], %[ao], %[rs], s64 offen offset:2304 sc1\n\t" "buffer_load_dwordx4 v[32:35], %[a1], %[rs], s64 offen offset:0 sc1\n\t" \
    "buffer_load_dwordx4 v[36:39], %[a1], %[rs], s64 offen offset:256 sc1\n\t" "buffer_load_dwordx4 v[40:43], %[a1], %[rs], s64 offen offset:2048 sc1\n\t" \
    "buffer_load_dwordx4 v[44:47], %[a1], %[rs], s64 offen offset:2304 sc1\n\t" "s_waitcnt vmcnt(0)\n\t" "10:\n\t" XT(80, 81) "s_add_u32 s66, s62, %[rg]\n\t" \
    "s_cmp_lt_u32 s66, s60\n\t" "s_cbranch_scc1 60f\n\t" "63:\n\t" "s_cmp_lt_u32 s60, 12\n\t" "s_cbranch_scc1 40f\n\t" "s_add_u32 s66, s60, 1\n\t" "s_cmp_gt_u32 s61, s66\n\t" \
    "s_cbranch_scc0 40f\n\t" "v_mfma_f32_16x16x32_bf16 v[80:83], v[16:19], a[0:3], 0\n\t" "v_mfma_f32_16x16x32_bf16 v[84:87], v[20:23], a[0:3], 0\n\t" \
    "v_mfma_f32_16x16x32_bf16 v[80:83], v[24:27], a[4:7], v[80:83]\n\t" "v_mfma_f32_16x16x32_bf16 v[84:87], v[28:31], a[4:7], v[84:87]\n\t" \
    "v_mfma_f32_16x16x32_bf16 v[80:83], v[32:35], a[8:11], v[80:83]\n\t" "v_cvt_pk_bf16_f32 v112, v96, v97\n\t" \
    "v_mfma_f32_16x16x32_bf16 v[84:87], v[36:39], a[8:11], v[84:87]\n\t" "v_cvt_pk_bf16_f32 v113, v98, v99\n\t" \
    "v_mfma_f32_16x16x32_bf16 v[80:83], v[40:43], a[12:15], v[80:83]\n\t" "v_cvt_pk_bf16_f32 v114, v100, v101\n\t" \
    "v_mfma_f32_16x16x32_bf16 v[84:87], v[44:47], a[12:15], v[84:87]\n\t" "v_cvt_pk_bf16_f32 v115, v102, v103\n\t" \
    "v_mfma_f32_16x16x32_bf16 v[88:91], v[16:19], a[16:19], 0\n\t" "v_cvt_pk_bf16_f32 v116, v104, v105\n\t" "v_mfma_f32_16x16x32_bf16 v[92:95], v[20:23], a[16:19], 0\n\t" \
    "v_cvt_pk_bf16_f32 v117, v106, v107\n\t" "v_mfma_f32_16x16x32_bf16 v[88:91], v[24:27], a[20:23], v[88:91]\n\t" \
    "buffer_store_dwordx4 v[112:115], %[z1], %[rs], s63 offen sc1\n\t" "v_mfma_f32_16x16x32_bf16 v[92:95], v[28:31], a[20:23], v[92:95]\n\t" \
    "v_mfma_f32_16x16x32_bf16 v[88:91], v[32:35], a[24:27], v[88:91]\n\t" "v_cvt_pk_bf16_f32 v118, v108, v109\n\t" \
    "v_mfma_f32_16x16x32_bf16 v[92:95], v[36:39], a[24:27], v[92:95]\n\t" "v_cvt_pk_bf16_f32 v119, v110, v111\n\t" \
    "v_mfma_f32_16x16x32_bf16 v[88:91], v[40:43], a[28:31], v[88:91]\n\t" "buffer_store_dwordx4 v[116:119], %[z2], %[rs], s63 offen sc1\n\t" "s_and_b32 s63, s60, %[rm]\n\t" \
    "v_mfma_f32_16x16x32_bf16 v[92:95], v[44:47], a[28:31], v[92:95]\n\t" "s_lshl_b32 s63, s63, 22\n\t" "s_add_u32 s63, s63, %[zb]\n\t" \
    "v_mfma_f32_16x16x32_bf16 v[96:99], v[16:19], a[32:35], 0\n\t" "s_add_u32 s65, s60, 1\n\t" "s_lshr_b32 s66, s65, 2\n\t" \
    "v_mfma_f32_16x16x32_bf16 v[100:103], v[20:23], a[32:35], 0\n\t" "s_xor_b32 s66, s66, %[xp]\n\t" "s_and_b32 s66, s66, 1\n\t" \
    "v_mfma_f32_16x16x32_bf16 v[96:99], v[24:27], a[36:39], v[96:99]\n\t" "s_lshl_b32 s66, s66, 13\n\t" "s_lshl_b32 s64, s65, 19\n\t" \
    "v_mfma_f32_16x16x32_bf16 v[100:103], v[28:31], a[36:39], v[100:103]\n\t" "s_add_u32 s64, s64, s66\n\t" "s_add_u32 s64, s64, %[vb]\n\t" \
    "v_mfma_f32_16x16x32_bf16 v[96:99], v[32:35], a[40:43], v[96:99]\n\t" "buffer_load_dwordx4 v[48:51], %[ao], %[rs], s64 offen offset:0 sc1\n\t" \
    "v_cvt_pk_bf16_f32 v112, v80, v81\n\t" "v_mfma_f32_16x16x32_bf16 v[100:103], v[36:39], a[40:43], v[100:103]\n\t" "v_cvt_pk_bf16_f32 v113, v82, v83\n\t" \
    "v_mfma_f32_16x16x32_bf16 v[96:99], v[40:43], a[44:47], v[96:99]\n\t" "buffer_load_dwordx4 v[52:55], %[ao], %[rs], s64 offen offset:256 sc1\n\t" \
    "v_cvt_pk_bf16_f32 v114, v84, v85\n\t" "v_mfma_f32_16x16x32_bf16 v[100:103], v[44:47], a[44:47], v[100:103]\n\t" "v_cvt_pk_bf16_f32 v115, v86, v87\n\t" \
    "v_mfma_f32_16x16x32_bf16 v[104:107], v[16:19], a[48:51], 0\n\t" "buffer_load_dwordx4 v[56:59], %[ao], %[rs], s64 offen offset:2048 sc1\n\t" \
    "v_cvt_pk_bf16_f32 v116, v88, v89\n\t" "v_mfma_f32_16x16x32_bf16 v[108:111], v[20:23], a[48:51], 0\n\t" "v_cvt_pk_bf16_f32 v117, v90, v91\n\t" \
    "v_mfma_f32_16x16x32_bf16 v[104:107], v[24:27], a[52:55], v[104:107]\n\t" "buffer_load_dwordx4 v[60:63], %[ao], %[rs], s64 offen offset:2304 sc1\n\t" \
    "v_mfma_f32_16x16x32_bf16 v[108:111], v[28:31], a[52:55], v[108:111]\n\t" "buffer_store_dwordx4 v[112:115], %[z1], %[rs], s63 offen sc1\n\t" \
    "v_mfma_f32_16x16x32_bf16 v[104:107], v[32:35], a[56:59], v[104:107]\n\t" "buffer_load_dwordx4 v[64:67], %[a1], %[rs], s64 offen offset:0 sc1\n\t" \
    "v_cvt_pk_bf16_f32 v118, v92, v93\n\t" "v_mfma_f32_16x16x32_bf16 v[108:111], v[36:39], a[56:59], v[108:111]\n\t" "v_cvt_pk_bf16_f32 v119, v94, v95\n\t" \
    "v_mfma_f32_16x16x32_bf16 v[104:107], v[40:43], a[60:63], v[104:107]\n\t" "buffer_load_dwordx4 v[68:71], %[a1], %[rs], s64 offen offset:256 sc1\n\t" \
    "buffer_store_dwordx4 v[116:119], %[z2], %[rs], s63 offen sc1\n\t" "v_mfma_f32_16x16x32_bf16 v[108:111], v[44:47], a[60:63], v[108:111]\n\t" \
    "s_add_u32 s63, s63, 0x20000\n\t" "v_mfma_f32_16x16x32_bf16 v[80:83], v[16:19], a[64:67], 0\n\t" \
    "buffer_load_dwordx4 v[72:75], %[a1], %[rs], s64 offen offset:2048 sc1\n\t" "v_mfma_f32_16x16x32_bf16 v[84:87], v[20:23], a[64:67], 0\n\t" \
    "v_mfma_f32_16x16x32_bf16 v[80:83], v[24:27], a[68:71], v[80:83]\n\t" "buffer_load_dwordx4 v[76:79], %[a1], %[rs], s64 offen offset:2304 sc1\n\t" \
    "v_mfma_f32_16x16x32_bf16 v[84:87], v[28:31], a[68:71], v[84:87]\n\t" "v_mfma_f32_16x16x32_bf16 v[80:83], v[32:35], a[72:75], v[80:83]\n\t" \
    "buffer_load_dword v120, %[fo], %[rc], %[fv] offen sc1\n\t" "v_cvt_pk_bf16_f32 v112, v96, v97\n\t" "v_mfma_f32_16x16x32_bf16 v[84:87], v[36:39], a[72:75], v[84:87]\n\t" \
    "v_cvt_pk_bf16_f32 v113, v98, v99\n\t" "v_mfma_f32_16x16x32_bf16 v[80:83], v[40:43], a[76:79], v[80:83]\n\t" "buffer_load_dword v121, %[fo], %[rc], %[fs] offen sc1\n\t" \
    "v_mfma_f32_16x16x32_bf16 v[84:87], v[44:47], a[76:79], v[84:87]\n\t" "v_cvt_pk_bf16_f32 v114, v100, v101\n\t" "v_cvt_pk_bf16_f32 v115, v102, v103\n\t" \
    "v_mfma_f32_16x16x32_bf16 v[88:91], v[16:19], a[80:83], 0\n\t" "v_cvt_pk_bf16_f32 v116, v104, v105\n\t" "v_mfma_f32_16x16x32_bf16 v[92:95], v[20:23], a[80:83], 0\n\t" \
    "v_cvt_pk_bf16_f32 v117, v106, v107\n\t" "v_mfma_f32_16x16x32_bf16 v[88:91], v[24:27], a[84:87], v[88:91]\n\t" \
    "buffer_store_dwordx4 v[112:115], %[z1], %[rs], s63 offen sc1\n\t" "v_mfma_f32_16x16x32_bf16 v[92:95], v[28:31], a[84:87], v[92:95]\n\t" \
    "v_mfma_f32_16x16x32_bf16 v[88:91], v[32:35], a[88:91], v[88:91]\n\t" "v_cvt_pk_bf16_f32 v118, v108, v109\n\t" \
    "v_mfma_f32_16x16x32_bf16 v[92:95], v[36:39], a[88:91], v[92:95]\n\t" "v_cvt_pk_bf16_f32 v119, v110, v111\n\t" \
    "v_mfma_f32_16x16x32_bf16 v[88:91], v[40:43], a[92:95], v[88:91]\n\t" "buffer_store_dwordx4 v[116:119], %[z2], %[rs], s63 offen sc1\n\t" \
    "v_mfma_f32_16x16x32_bf16 v[92:95], v[44:47], a[92:95], v[92:95]\n\t" "s_add_u32 s63, s63, 0x20000\n\t" "v_mfma_f32_16x16x32_bf16 v[96:99], v[16:19], a[96:99], 0\n\t" \
    "v_mfma_f32_16x16x32_bf16 v[100:103], v[20:23], a[96:99], 0\n\t" "v_mfma_f32_16x16x32_bf16 v[96:99], v[24:27], a[100:103], v[96:99]\n\t" \
    "v_mfma_f32_16x16x32_bf16 v[100:103], v[28:31], a[100:103], v[100:103]\n\t" "v_mfma_f32_16x16x32_bf16 v[96:99], v[32:35], a[104:107], v[96:99]\n\t" \
    "v_cvt_pk_bf16_f32 v112, v80, v81\n\t" "v_mfma_f32_16x16x32_bf16 v[100:103], v[36:39], a[104:107], v[100:103]\n\t" "v_cvt_pk_bf16_f32 v113, v82, v83\n\t" \
    "v_mfma_f32_16x16x32_bf16 v[96:99], v[40:43], a[108:111], v[96:99]\n\t" "v_cvt_pk_bf16_f32 v114, v84, v85\n\t" \
    "v_mfma_f32_16x16x32_bf16 v[100:103], v[44:47], a[108:111], v[100:103]\n\t" "v_cvt_pk_bf16_f32 v115, v86, v87\n\t" \
    "v_mfma_f32_16x16x32_bf16 v[104:107], v[16:19], a[112:115], 0\n\t" "v_cvt_pk_bf16_f32 v116, v88, v89\n\t" \
    "v_mfma_f32_16x16x32_bf16 v[108:111], v[20:23], a[112:115], 0\n\t" "v_cvt_pk_bf16_f32 v117, v90, v91\n\t" \
    "v_mfma_f32_16x16x32_bf16 v[104:107], v[24:27], a[116:119], v[104:107]\n\t" "buffer_store_dwordx4 v[112:115], %[z1], %[rs], s63 offen sc1\n\t" \
    "v_mfma_f32_16x16x32_bf16 v[108:111], v[28:31], a[116:119], v[108:111]\n\t" "v_mfma_f32_16x16x32_bf16 v[104:107], v[32:35], a[120:123], v[104:107]\n\t" \
    "v_cvt_pk_bf16_f32 v118, v92, v93\n\t" "v_mfma_f32_16x16x32_bf16 v[108:111], v[36:39], a[120:123], v[108:111]\n\t" "v_cvt_pk_bf16_f32 v119, v94, v95\n\t" \
    "v_mfma_f32_16x16x32_bf16 v[104:107], v[40:43], a[124:127], v[104:107]\n\t" "buffer_store_dwordx4 v[116:119], %[z2], %[rs], s63 offen sc1\n\t" \
    "v_mfma_f32_16x16x32_bf16 v[108:111], v[44:47], a[124:127], v[108:111]\n\t" "s_add_u32 s63, s63, 0x20000\n\t" \
    "v_mfma_f32_16x16x32_bf16 v[80:83], v[16:19], a[128:131], 0\n\t" "v_mfma_f32_16x16x32_bf16 v[84:87], v[20:23], a[128:131], 0\n\t" \
    "v_mfma_f32_16x16x32_bf16 v[80:83], v[24:27], a[132:135], v[80:83]\n\t" "v_mfma_f32_16x16x32_bf16 v[84:87], v[28:31], a[132:135], v[84:87]\n\t" \
    "v_mfma_f32_16x16x32_bf16 v[80:83], v[32:35], a[136:139], v[80:83]\n\t" "v_cvt_pk_bf16_f32 v112, v96, v97\n\t" \
    "v_mfma_f32_16x16x32_bf16 v[84:87], v[36:39], a[136:139], v[84:87]\n\t" "v_cvt_pk_bf16_f32 v113, v98, v99\n\t" \
    "v_mfma_f32_16x16x32_bf16 v[80:83], v[40:43], a[140:143], v[80:83]\n\t" "v_cvt_pk_bf16_f32 v114, v100, v101\n\t" \
    "v_mfma_f32_16x16x32_bf16 v[84:87], v[44:47], a[140:143], v[84:87]\n\t" "v_cvt_pk_bf16_f32 v115, v102, v103\n\t" \
    "v_mfma_f32_16x16x32_bf16 v[88:91], v[16:19], a[144:147], 0\n\t" "v_cvt_pk_bf16_f32 v116, v104, v105\n\t" "v_mfma_f32_16x16x32_bf16 v[92:95], v[20:23], a[144:147], 0\n\t" \
    "v_cvt_pk_bf16_f32 v117, v106, v107\n\t" "v_mfma_f32_16x16x32_bf16 v[88:91], v[24:27], a[148:151], v[88:91]\n\t" \
    "buffer_store_dwordx4 v[112:115], %[z1], %[rs], s63 offen sc1\n\t" "v_mfma_f32_16x16x32_bf16 v[92:95], v[28:31], a[148:151], v[92:95]\n\t" \
    "v_mfma_f32_16x16x32_bf16 v[88:91], v[32:35], a[152:155], v[88:91]\n\t" "v_cvt_pk_bf16_f32 v118, v108, v109\n\t" \
    "v_mfma_f32_16x16x32_bf16 v[92:95], v[36:39], a[152:155], v[92:95]\n\t" "v_cvt_pk_bf16_f32 v119, v110, v111\n\t" \
    "v_mfma_f32_16x16x32_bf16 v[88:91], v[40:43], a[156:159], v[88:91]\n\t" "buffer_store_dwordx4 v[116:119], %[z2], %[rs], s63 offen sc1\n\t" \
    "v_mfma_f32_16x16x32_bf16 v[92:95], v[44:47], a[156:159], v[92:95]\n\t" "s_add_u32 s63, s63, 0x20000\n\t" "v_mfma_f32_16x16x32_bf16 v[96:99], v[16:19], a[160:163], 0\n\t" \
    "v_mfma_f32_16x16x32_bf16 v[100:103], v[20:23], a[160:163], 0\n\t" "v_mfma_f32_16x16x32_bf16 v[96:99], v[24:27], a[164:167], v[96:99]\n\t" \
    "v_mfma_f32_16x16x32_bf16 v[100:103], v[28:31], a[164:167], v[100:103]\n\t" "v_mfma_f32_16x16x32_bf16 v[96:99], v[32:35], a[168:171], v[96:99]\n\t" \
    "v_cvt_pk_bf16_f32 v112, v80, v81\n\t" "v_mfma_f32_16x16x32_bf16 v[100:103], v[36:39], a[168:171], v[100:103]\n\t" "v_cvt_pk_bf16_f32 v113, v82, v83\n\t" \
    "v_mfma_f32_16x16x32_bf16 v[96:99], v[40:43], a[172:175], v[96:99]\n\t" "v_cvt_pk_bf16_f32 v114, v84, v85\n\t" \
    "v_mfma_f32_16x16x32_bf16 v[100:103], v[44:47], a[172:175], v[100:103]\n\t" "v_cvt_pk_bf16_f32 v115, v86, v87\n\t" \
    "v_mfma_f32_16x16x32_bf16 v[104:107], v[16:19], a[176:179], 0\n\t" "v_cvt_pk_bf16_f32 v116, v88, v89\n\t" \
    "v_mfma_f32_16x16x32_bf16 v[108:111], v[20:23], a[176:179], 0\n\t" "v_cvt_pk_bf16_f32 v117, v90, v91\n\t" \
    "v_mfma_f32_16x16x32_bf16 v[104:107], v[24:27], a[180:183], v[104:107]\n\t" "buffer_store_dwordx4 v[112:115], %[z1], %[rs], s63 offen sc1\n\t" \
    "v_mfma_f32_16x16x32_bf16 v[108:111], v[28:31], a[180:183], v[108:111]\n\t" "v_mfma_f32_16x16x32_bf16 v[104:107], v[32:35], a[184:187], v[104:107]\n\t" \
    "v_cvt_pk_bf16_f32 v118, v92, v93\n\t" "v_mfma_f32_16x16x32_bf16 v[108:111], v[36:39], a[184:187], v[108:111]\n\t" "v_cvt_pk_bf16_f32 v119, v94, v95\n\t" \
    "v_mfma_f32_16x16x32_bf16 v[104:107], v[40:43], a[188:191], v[104:107]\n\t" "buffer_store_dwordx4 v[116:119], %[z2], %[rs], s63 offen sc1\n\t" \
    "v_mfma_f32_16x16x32_bf16 v[108:111], v[44:47], a[188:191], v[108:111]\n\t" "s_add_u32 s63, s63, 0x20000\n\t" \
    "v_mfma_f32_16x16x32_bf16 v[80:83], v[16:19], a[192:195], 0\n\t" "v_mfma_f32_16x16x32_bf16 v[84:87], v[20:23], a[192:195], 0\n\t" \
    "v_mfma_f32_16x16x32_bf16 v[80:83], v[24:27], a[196:199], v[80:83]\n\t" "v_mfma_f32_16x16x32_bf16 v[84:87], v[28:31], a[196:199], v[84:87]\n\t" \
    "v_mfma_f32_16x16x32_bf16 v[80:83], v[32:35], a[200:203], v[80:83]\n\t" "v_cvt_pk_bf16_f32 v112, v96, v97\n\t" \
    "v_mfma_f32_16x16x32_bf16 v[84:87], v[36:39], a[200:203], v[84:87]\n\t" "v_cvt_pk_bf16_f32 v113, v98, v99\n\t" \
    "v_mfma_f32_16x16x32_bf16 v[80:83], v[40:43], a[204:207], v[80:83]\n\t" "v_cvt_pk_bf16_f32 v114, v100, v101\n\t" \
    "v_mfma_f32_16x16x32_bf16 v[84:87], v[44:47], a[204:207], v[84:87]\n\t" "v_cvt_pk_bf16_f32 v115, v102, v103\n\t" \
    "v_mfma_f32_16x16x32_bf16 v[88:91], v[16:19], a[208:211], 0\n\t" "v_cvt_pk_bf16_f32 v116, v104, v105\n\t" "v_mfma_f32_16x16x32_bf16 v[92:95], v[20:23], a[208:211], 0\n\t" \
    "v_cvt_pk_bf16_f32 v117, v106, v107\n\t" "v_mfma_f32_16x16x32_bf16 v[88:91], v[24:27], a[212:215], v[88:91]\n\t" \
    "buffer_store_dwordx4 v[112:115], %[z1], %[rs], s63 offen sc1\n\t" "v_mfma_f32_16x16x32_bf16 v[92:95], v[28:31], a[212:215], v[92:95]\n\t" \
    "v_mfma_f32_16x16x32_bf16 v[88:91], v[32:35], a[216:219], v[88:91]\n\t" "v_cvt_pk_bf16_f32 v118, v108, v109\n\t" \
    "v_mfma_f32_16x16x32_bf16 v[92:95], v[36:39], a[216:219], v[92:95]\n\t" "v_cvt_pk_bf16_f32 v119, v110, v111\n\t" \
    "v_mfma_f32_16x16x32_bf16 v[88:91], v[40:43], a[220:223], v[88:91]\n\t" "buffer_store_dwordx4 v[116:119], %[z2], %[rs], s63 offen sc1\n\t" \
    "v_mfma_f32_16x16x32_bf16 v[92:95], v[44:47], a[220:223], v[92:95]\n\t" "s_add_u32 s63, s63, 0x20000\n\t" "v_mfma_f32_16x16x32_bf16 v[96:99], v[16:19], a[224:227], 0\n\t" \
    "v_mfma_f32_16x16x32_bf16 v[100:103], v[20:23], a[224:227], 0\n\t" "v_mfma_f32_16x16x32_bf16 v[96:99], v[24:27], a[228:231], v[96:99]\n\t" \
    "v_mfma_f32_16x16x32_bf16 v[100:103], v[28:31], a[228:231], v[100:103]\n\t" "v_mfma_f32_16x16x32_bf16 v[96:99], v[32:35], a[232:235], v[96:99]\n\t" \
    "v_cvt_pk_bf16_f32 v112, v80, v81\n\t" "v_mfma_f32_16x16x32_bf16 v[100:103], v[36:39], a[232:235], v[100:103]\n\t" "v_cvt_pk_bf16_f32 v113, v82, v83\n\t" \
    "v_mfma_f32_16x16x32_bf16 v[96:99], v[40:43], a[236:239], v[96:99]\n\t" "v_cvt_pk_bf16_f32 v114, v84, v85\n\t" \
    "v_mfma_f32_16x16x32_bf16 v[100:103], v[44:47], a[236:239], v[100:103]\n\t" "v_cvt_pk_bf16_f32 v115, v86, v87\n\t" \
    "v_mfma_f32_16x16x32_bf16 v[104:107], v[16:19], a[240:243], 0\n\t" "v_cvt_pk_bf16_f32 v116, v88, v89\n\t" \
    "v_mfma_f32_16x16x32_bf16 v[108:111], v[20:23], a[240:243], 0\n\t" "v_cvt_pk_bf16_f32 v117, v90, v91\n\t" \
    "v_mfma_f32_16x16x32_bf16 v[104:107], v[24:27], a[244:247], v[104:107]\n\t" "buffer_store_dwordx4 v[112:115], %[z1], %[rs], s63 offen sc1\n\t" \
    "v_mfma_f32_16x16x32_bf16 v[108:111], v[28:31], a[244:247], v[108:111]\n\t" "v_mfma_f32_16x16x32_bf16 v[104:107], v[32:35], a[248:251], v[104:107]\n\t" \
    "v_cvt_pk_bf16_f32 v118, v92, v93\n\t" "v_cvt_pk_bf16_f32 v119, v94, v95\n\t" "s_waitcnt vmcnt(11)\n\t" "v_readfirstlane_b32 s66, v120\n\t" \
    "v_mfma_f32_16x16x32_bf16 v[108:111], v[36:39], a[248:251], v[108:111]\n\t" "v_readfirstlane_b32 s67, v121\n\t" "v_mov_b32 v122, s60\n\t" \
    "buffer_store_dword v122, %[fo], %[rc], %[fz] offen sc1\n\t" "v_mfma_f32_16x16x32_bf16 v[104:107], v[40:43], a[252:255], v[104:107]\n\t" \
    "buffer_store_dwordx4 v[116:119], %[z2], %[rs], s63 offen sc1\n\t" "s_add_u32 s63, s63, 0x20000\n\t" "s_max_u32 s61, s61, s66\n\t" \
    "v_mfma_f32_16x16x32_bf16 v[108:111], v[44:47], a[252:255], v[108:111]\n\t" "s_max_u32 s62, s62, s67\n\t" "s_add_u32 s60, s60, 1\n\t" ZSUM "23:\n\t" XT(80, 81) \
    "s_add_u32 s66, s62, %[rg]\n\t" "s_cmp_lt_u32 s66, s60\n\t" "s_cbranch_scc1 70f\n\t" "73:\n\t" "s_cmp_lt_u32 s60, 12\n\t" "s_cbranch_scc1 41f\n\t" \
    "s_add_u32 s66, s60, 1\n\t" "s_cmp_gt_u32 s61, s66\n\t" "s_cbranch_scc0 41f\n\t" "v_mfma_f32_16x16x32_bf16 v[80:83], v[48:51], a[0:3], 0\n\t" \
    "v_mfma_f32_16x16x32_bf16 v[84:87], v[52:55], a[0:3], 0\n\t" "v_mfma_f32_16x16x32_bf16 v[80:83], v[56:59], a[4:7], v[80:83]\n\t" \
    "v_mfma_f32_16x16x32_bf16 v[84:87], v[60:63], a[4:7], v[84:87]\n\t" "v_mfma_f32_16x16x32_bf16 v[80:83], v[64:67], a[8:11], v[80:83]\n\t" \
    "v_cvt_pk_bf16_f32 v112, v96, v97\n\t" "v_mfma_f32_16x16x32_bf16 v[84:87], v[68:71], a[8:11], v[84:87]\n\t" "v_cvt_pk_bf16_f32 v113, v98, v99\n\t" \
    "v_mfma_f32_16x16x32_bf16 v[80:83], v[72:75], a[12:15], v[80:83]\n\t" "v_cvt_pk_bf16_f32 v114, v100, v101\n\t" \
    "v_mfma_f32_16x16x32_bf16 v[84:87], v[76:79], a[12:15], v[84:87]\n\t" "v_cvt_pk_bf16_f32 v115, v102, v103\n\t" \
    "v_mfma_f32_16x16x32_bf16 v[88:91], v[48:51], a[16:19], 0\n\t" "v_cvt_pk_bf16_f32 v116, v104, v105\n\t" "v_mfma_f32_16x16x32_bf16 v[92:95], v[52:55], a[16:19], 0\n\t" \
    "v_cvt_pk_bf16_f32 v117, v106, v107\n\t" "v_mfma_f32_16x16x32_bf16 v[88:91], v[56:59], a[20:23], v[88:91]\n\t" \
    "buffer_store_dwordx4 v[112:115], %[z1], %[rs], s63 offen sc1\n\t" "v_mfma_f32_16x16x32_bf16 v[92:95], v[60:63], a[20:23], v[92:95]\n\t" \
    "v_mfma_f32_16x16x32_bf16 v[88:91], v[64:67], a[24:27], v[88:91]\n\t" "v_cvt_pk_bf16_f32 v118, v108, v109\n\t" \
    "v_mfma_f32_16x16x32_bf16 v[92:95], v[68:71], a[24:27], v[92:95]\n\t" "v_cvt_pk_bf16_f32 v119, v110, v111\n\t" \
    "v_mfma_f32_16x16x32_bf16 v[88:91], v[72:75], a[28:31], v[88:91]\n\t" "buffer_store_dwordx4 v[116:119], %[z2], %[rs], s63 offen sc1\n\t" "s_and_b32 s63, s60, %[rm]\n\t" \
    "v_mfma_f32_16x16x32_bf16 v[92:95], v[76:79], a[28:31], v[92:95]\n\t" "s_lshl_b32 s63, s63, 22\n\t" "s_add_u32 s63, s63, %[zb]\n\t" \
    "v_mfma_f32_16x16x32_bf16 v[96:99], v[48:51], a[32:35], 0\n\t" "s_add_u32 s65, s60, 1\n\t" "s_lshr_b32 s66, s65, 2\n\t" \
    "v_mfma_f32_16x16x32_bf16 v[100:103], v[52:55], a[32:35], 0\n\t" "s_xor_b32 s66, s66, %[xp]\n\t" "s_and_b32 s66, s66, 1\n\t" \
    "v_mfma_f32_16x16x32_bf16 v[96:99], v[56:59], a[36:39], v[96:99]\n\t" "s_lshl_b32 s66, s66, 13\n\t" "s_lshl_b32 s64, s65, 19\n\t" \
    "v_mfma_f32_16x16x32_bf16 v[100:103], v[60:63], a[36:39], v[100:103]\n\t" "s_add_u32 s64, s64, s66\n\t" "s_add_u32 s64, s64, %[vb]\n\t" \
    "v_mfma_f32_16x16x32_bf16 v[96:99], v[64:67], a[40:43], v[96:99]\n\t" "buffer_load_dwordx4 v[16:19], %[ao], %[rs], s64 offen offset:0 sc1\n\t" \
    "v_cvt_pk_bf16_f32 v112, v80, v81\n\t" "v_mfma_f32_16x16x32_bf16 v[100:103], v[68:71], a[40:43], v[100:103]\n\t" "v_cvt_pk_bf16_f32 v113, v82, v83\n\t" \
    "v_mfma_f32_16x16x32_bf16 v[96:99], v[72:75], a[44:47], v[96:99]\n\t" "buffer_load_dwordx4 v[20:23], %[ao], %[rs], s64 offen offset:256 sc1\n\t" \
    "v_cvt_pk_bf16_f32 v114, v84, v85\n\t" "v_mfma_f32_16x16x32_bf16 v[100:103], v[76:79], a[44:47], v[100:103]\n\t" "v_cvt_pk_bf16_f32 v115, v86, v87\n\t" \
    "v_mfma_f32_16x16x32_bf16 v[104:107], v[48:51], a[48:51], 0\n\t" "buffer_load_dwordx4 v[24:27], %[ao], %[rs], s64 offen offset:2048 sc1\n\t" \
    "v_cvt_pk_bf16_f32 v116, v88, v89\n\t" "v_mfma_f32_16x16x32_bf16 v[108:111], v[52:55], a[48:51], 0\n\t" "v_cvt_pk_bf16_f32 v117, v90, v91\n\t" \
    "v_mfma_f32_16x16x32_bf16 v[104:107], v[56:59], a[52:55], v[104:107]\n\t" "buffer_load_dwordx4 v[28:31], %[ao], %[rs], s64 offen offset:2304 sc1\n\t" \
    "v_mfma_f32_16x16x32_bf16 v[108:111], v[60:63], a[52:55], v[108:111]\n\t" "buffer_store_dwordx4 v[112:115], %[z1], %[rs], s63 offen sc1\n\t" \
    "v_mfma_f32_16x16x32_bf16 v[104:107], v[64:67], a[56:59], v[104:107]\n\t" "buffer_load_dwordx4 v[32:35], %[a1], %[rs], s64 offen offset:0 sc1\n\t" \
    "v_cvt_pk_bf16_f32 v118, v92, v93\n\t" "v_mfma_f32_16x16x32_bf16 v[108:111], v[68:71], a[56:59], v[108:111]\n\t" "v_cvt_pk_bf16_f32 v119, v94, v95\n\t" \
    "v_mfma_f32_16x16x32_bf16 v[104:107], v[72:75], a[60:63], v[104:107]\n\t" "buffer_load_dwordx4 v[36:39], %[a1], %[rs], s64 offen offset:256 sc1\n\t" \
    "buffer_store_dwordx4 v[116:119], %[z2], %[rs], s63 offen sc1\n\t" "v_mfma_f32_16x16x32_bf16 v[108:111], v[76:79], a[60:63], v[108:111]\n\t" \
    "s_add_u32 s63, s63, 0x20000\n\t" "v_mfma_f32_16x16x32_bf16 v[80:83], v[48:51], a[64:67], 0\n\t" \
    "buffer_load_dwordx4 v[40:43], %[a1], %[rs], s64 offen offset:2048 sc1\n\t" "v_mfma_f32_16x16x32_bf16 v[84:87], v[52:55], a[64:67], 0\n\t" \
    "v_mfma_f32_16x16x32_bf16 v[80:83], v[56:59], a[68:71], v[80:83]\n\t" "buffer_load_dwordx4 v[44:47], %[a1], %[rs], s64 offen offset:2304 sc1\n\t" \
    "v_mfma_f32_16x16x32_bf16 v[84:87], v[60:63], a[68:71], v[84:87]\n\t" "v_mfma_f32_16x16x32_bf16 v[80:83], v[64:67], a[72:75], v[80:83]\n\t" \
    "buffer_load_dword v120, %[fo], %[rc], %[fv] offen sc1\n\t" "v_cvt_pk_bf16_f32 v112, v96, v97\n\t" "v_mfma_f32_16x16x32_bf16 v[84:87], v[68:71], a[72:75], v[84:87]\n\t" \
    "v_cvt_pk_bf16_f32 v113, v98, v99\n\t" "v_mfma_f32_16x16x32_bf16 v[80:83], v[72:75], a[76:79], v[80:83]\n\t" "buffer_load_dword v121, %[fo], %[rc], %[fs] offen sc1\n\t" \
    "v_mfma_f32_16x16x32_bf16 v[84:87], v[76:79], a[76:79], v[84:87]\n\t" "v_cvt_pk_bf16_f32 v114, v100, v101\n\t" "v_cvt_pk_bf16_f32 v115, v102, v103\n\t" \
    "v_mfma_f32_16x16x32_bf16 v[88:91], v[48:51], a[80:83], 0\n\t" "v_cvt_pk_bf16_f32 v116, v104, v105\n\t" "v_mfma_f32_16x16x32_bf16 v[92:95], v[52:55], a[80:83], 0\n\t" \
    "v_cvt_pk_bf16_f32 v117, v106, v107\n\t" "v_mfma_f32_16x16x32_bf16 v[88:91], v[56:59], a[84:87], v[88:91]\n\t" \
    "buffer_store_dwordx4 v[112:115], %[z1], %[rs], s63 offen sc1\n\t" "v_mfma_f32_16x16x32_bf16 v[92:95], v[60:63], a[84:87], v[92:95]\n\t" \
    "v_mfma_f32_16x16x32_bf16 v[88:91], v[64:67], a[88:91], v[88:91]\n\t" "v_cvt_pk_bf16_f32 v118, v108, v109\n\t" \
    "v_mfma_f32_16x16x32_bf16 v[92:95], v[68:71], a[88:91], v[92:95]\n\t" "v_cvt_pk_bf16_f32 v119, v110, v111\n\t" \
    "v_mfma_f32_16x16x32_bf16 v[88:91], v[72:75], a[92:95], v[88:91]\n\t" "buffer_store_dwordx4 v[116:119], %[z2], %[rs], s63 offen sc1\n\t" \
    "v_mfma_f32_16x16x32_bf16 v[92:95], v[76:79], a[92:95], v[92:95]\n\t" "s_add_u32 s63, s63, 0x20000\n\t" "v_mfma_f32_16x16x32_bf16 v[96:99], v[48:51], a[96:99], 0\n\t" \
    "v_mfma_f32_16x16x32_bf16 v[100:103], v[52:55], a[96:99], 0\n\t" "v_mfma_f32_16x16x32_bf16 v[96:99], v[56:59], a[100:103], v[96:99]\n\t" \
    "v_mfma_f32_16x16x32_bf16 v[100:103], v[60:63], a[100:103], v[100:103]\n\t" "v_mfma_f32_16x16x32_bf16 v[96:99], v[64:67], a[104:107], v[96:99]\n\t" \
    "v_cvt_pk_bf16_f32 v112, v80, v81\n\t" "v_mfma_f32_16x16x32_bf16 v[100:103], v[68:71], a[104:107], v[100:103]\n\t" "v_cvt_pk_bf16_f32 v113, v82, v83\n\t" \
    "v_mfma_f32_16x16x32_bf16 v[96:99], v[72:75], a[108:111], v[96:99]\n\t" "v_cvt_pk_bf16_f32 v114, v84, v85\n\t" \
    "v_mfma_f32_16x16x32_bf16 v[100:103], v[76:79], a[108:111], v[100:103]\n\t" "v_cvt_pk_bf16_f32 v115, v86, v87\n\t" \
    "v_mfma_f32_16x16x32_bf16 v[104:107], v[48:51], a[112:115], 0\n\t" "v_cvt_pk_bf16_f32 v116, v88, v89\n\t" \
    "v_mfma_f32_16x16x32_bf16 v[108:111], v[52:55], a[112:115], 0\n\t" "v_cvt_pk_bf16_f32 v117, v90, v91\n\t" \
    "v_mfma_f32_16x16x32_bf16 v[104:107], v[56:59], a[116:119], v[104:107]\n\t" "buffer_store_dwordx4 v[112:115], %[z1], %[rs], s63 offen sc1\n\t" \
    "v_mfma_f32_16x16x32_bf16 v[108:111], v[60:63], a[116:119], v[108:111]\n\t" "v_mfma_f32_16x16x32_bf16 v[104:107], v[64:67], a[120:123], v[104:107]\n\t" \
    "v_cvt_pk_bf16_f32 v118, v92, v93\n\t" "v_mfma_f32_16x16x32_bf16 v[108:111], v[68:71], a[120:123], v[108:111]\n\t" "v_cvt_pk_bf16_f32 v119, v94, v95\n\t" \
    "v_mfma_f32_16x16x32_bf16 v[104:107], v[72:75], a[124:127], v[104:107]\n\t" "buffer_store_dwordx4 v[116:119], %[z2], %[rs], s63 offen sc1\n\t" \
    "v_mfma_f32_16x16x32_bf16 v[108:111], v[76:79], a[124:127], v[108:111]\n\t" "s_add_u32 s63, s63, 0x20000\n\t" \
    "v_mfma_f32_16x16x32_bf16 v[80:83], v[48:51], a[128:131], 0\n\t" "v_mfma_f32_16x16x32_bf16 v[84:87], v[52:55], a[128:131], 0\n\t" \
    "v_mfma_f32_16x16x32_bf16 v[80:83], v[56:59], a[132:135], v[80:83]\n\t" "v_mfma_f32_16x16x32_bf16 v[84:87], v[60:63], a[132:135], v[84:87]\n\t" \
    "v_mfma_f32_16x16x32_bf16 v[80:83], v[64:67], a[136:139], v[80:83]\n\t" "v_cvt_pk_bf16_f32 v112, v96, v97\n\t" \
    "v_mfma_f32_16x16x32_bf16 v[84:87], v[68:71], a[136:139], v[84:87]\n\t" "v_cvt_pk_bf16_f32 v113, v98, v99\n\t" \
    "v_mfma_f32_16x16x32_bf16 v[80:83], v[72:75], a[140:143], v[80:83]\n\t" "v_cvt_pk_bf16_f32 v114, v100, v101\n\t" \
    "v_mfma_f32_16x16x32_bf16 v[84:87], v[76:79], a[140:143], v[84:87]\n\t" "v_cvt_pk_bf16_f32 v115, v102, v103\n\t" \
    "v_mfma_f32_16x16x32_bf16 v[88:91], v[48:51], a[144:147], 0\n\t" "v_cvt_pk_bf16_f32 v116, v104, v105\n\t" "v_mfma_f32_16x16x32_bf16 v[92:95], v[52:55], a[144:147], 0\n\t" \
    "v_cvt_pk_bf16_f32 v117, v106, v107\n\t" "v_mfma_f32_16x16x32_bf16 v[88:91], v[56:59], a[148:151], v[88:91]\n\t" \
    "buffer_store_dwordx4 v[112:115], %[z1], %[rs], s63 offen sc1\n\t" "v_mfma_f32_16x16x32_bf16 v[92:95], v[60:63], a[148:151], v[92:95]\n\t" \
    "v_mfma_f32_16x16x32_bf16 v[88:91], v[64:67], a[152:155], v[88:91]\n\t" "v_cvt_pk_bf16_f32 v118, v108, v109\n\t" \
    "v_mfma_f32_16x16x32_bf16 v[92:95], v[68:71], a[152:155], v[92:95]\n\t" "v_cvt_pk_bf16_f32 v119, v110, v111\n\t" \
    "v_mfma_f32_16x16x32_bf16 v[88:91], v[72:75], a[156:159], v[88:91]\n\t" "buffer_store_dwordx4 v[116:119], %[z2], %[rs], s63 offen sc1\n\t" \
    "v_mfma_f32_16x16x32_bf16 v[92:95], v[76:79], a[156:159], v[92:95]\n\t" "s_add_u32 s63, s63, 0x20000\n\t" "v_mfma_f32_16x16x32_bf16 v[96:99], v[48:51], a[160:163], 0\n\t" \
    "v_mfma_f32_16x16x32_bf16 v[100:103], v[52:55], a[160:163], 0\n\t" "v_mfma_f32_16x16x32_bf16 v[96:99], v[56:59], a[164:167], v[96:99]\n\t" \
    "v_mfma_f32_16x16x32_bf16 v[100:103], v[60:63], a[164:167], v[100:103]\n\t" "v_mfma_f32_16x16x32_bf16 v[96:99], v[64:67], a[168:171], v[96:99]\n\t" \
    "v_cvt_pk_bf16_f32 v112, v80, v81\n\t" "v_mfma_f32_16x16x32_bf16 v[100:103], v[68:71], a[168:171], v[100:103]\n\t" "v_cvt_pk_bf16_f32 v113, v82, v83\n\t" \
    "v_mfma_f32_16x16x32_bf16 v[96:99], v[72:75], a[172:175], v[96:99]\n\t" "v_cvt_pk_bf16_f32 v114, v84, v85\n\t" \
    "v_mfma_f32_16x16x32_bf16 v[100:103], v[76:79], a[172:175], v[100:103]\n\t" "v_cvt_pk_bf16_f32 v115, v86, v87\n\t" \
    "v_mfma_f32_16x16x32_bf16 v[104:107], v[48:51], a[176:179], 0\n\t" "v_cvt_pk_bf16_f32 v116, v88, v89\n\t" \
    "v_mfma_f32_16x16x32_bf16 v[108:111], v[52:55], a[176:179], 0\n\t" "v_cvt_pk_bf16_f32 v117, v90, v91\n\t" \
    "v_mfma_f32_16x16x32_bf16 v[104:107], v[56:59], a[180:183], v[104:107]\n\t" "buffer_store_dwordx4 v[112:115], %[z1], %[rs], s63 offen sc1\n\t" \
    "v_mfma_f32_16x16x32_bf16 v[108:111], v[60:63], a[180:183], v[108:111]\n\t" "v_mfma_f32_16x16x32_bf16 v[104:107], v[64:67], a[184:187], v[104:107]\n\t" \
    "v_cvt_pk_bf16_f32 v118, v92, v93\n\t" "v_mfma_f32_16x16x32_bf16 v[108:111], v[68:71], a[184:187], v[108:111]\n\t" "v_cvt_pk_bf16_f32 v119, v94, v95\n\t" \
    "v_mfma_f32_16x16x32_bf16 v[104:107], v[72:75], a[188:191], v[104:107]\n\t" "buffer_store_dwordx4 v[116:119], %[z2], %[rs], s63 offen sc1\n\t" \
    "v_mfma_f32_16x16x32_bf16 v[108:111], v[76:79], a[188:191], v[108:111]\n\t" "s_add_u32 s63, s63, 0x20000\n\t" \
    "v_mfma_f32_16x16x32_bf16 v[80:83], v[48:51], a[192:195], 0\n\t" "v_mfma_f32_16x16x32_bf16 v[84:87], v[52:55], a[192:195], 0\n\t" \
    "v_mfma_f32_16x16x32_bf16 v[80:83], v[56:59], a[196:199], v[80:83]\n\t" "v_mfma_f32_16x16x32_bf16 v[84:87], v[60:63], a[196:199], v[84:87]\n\t" \
    "v_mfma_f32_16x16x32_bf16 v[80:83], v[64:67], a[200:203], v[80:83]\n\t" "v_cvt_pk_bf16_f32 v112, v96, v97\n\t" \
    "v_mfma_f32_16x16x32_bf16 v[84:87], v[68:71], a[200:203], v[84:87]\n\t" "v_cvt_pk_bf16_f32 v113, v98, v99\n\t" \
    "v_mfma_f32_16x16x32_bf16 v[80:83], v[72:75], a[204:207], v[80:83]\n\t" "v_cvt_pk_bf16_f32 v114, v100, v101\n\t" \
    "v_mfma_f32_16x16x32_bf16 v[84:87], v[76:79], a[204:207], v[84:87]\n\t" "v_cvt_pk_bf16_f32 v115, v102, v103\n\t" \
    "v_mfma_f32_16x16x32_bf16 v[88:91], v[48:51], a[208:211], 0\n\t" "v_cvt_pk_bf16_f32 v116, v104, v105\n\t" "v_mfma_f32_16x16x32_bf16 v[92:95], v[52:55], a[208:211], 0\n\t" \
    "v_cvt_pk_bf16_f32 v117, v106, v107\n\t" "v_mfma_f32_16x16x32_bf16 v[88:91], v[56:59], a[212:215], v[88:91]\n\t" \
    "buffer_store_dwordx4 v[112:115], %[z1], %[rs], s63 offen sc1\n\t" "v_mfma_f32_16x16x32_bf16 v[92:95], v[60:63], a[212:215], v[92:95]\n\t" \
    "v_mfma_f32_16x16x32_bf16 v[88:91], v[64:67], a[216:219], v[88:91]\n\t" "v_cvt_pk_bf16_f32 v118, v108, v109\n\t" \
    "v_mfma_f32_16x16x32_bf16 v[92:95], v[68:71], a[216:219], v[92:95]\n\t" "v_cvt_pk_bf16_f32 v119, v110, v111\n\t" \
    "v_mfma_f32_16x16x32_bf16 v[88:91], v[72:75], a[220:223], v[88:91]\n\t" "buffer_store_dwordx4 v[116:119], %[z2], %[rs], s63 offen sc1\n\t" \
    "v_mfma_f32_16x16x32_bf16 v[92:95], v[76:79], a[220:223], v[92:95]\n\t" "s_add_u32 s63, s63, 0x20000\n\t" "v_mfma_f32_16x16x32_bf16 v[96:99], v[48:51], a[224:227], 0\n\t" \
    "v_mfma_f32_16x16x32_bf16 v[100:103], v[52:55], a[224:227], 0\n\t" "v_mfma_f32_16x16x32_bf16 v[96:99], v[56:59], a[228:231], v[96:99]\n\t" \
    "v_mfma_f32_16x16x32_bf16 v[100:103], v[60:63], a[228:231], v[100:103]\n\t" "v_mfma_f32_16x16x32_bf16 v[96:99], v[64:67], a[232:235], v[96:99]\n\t" \
    "v_cvt_pk_bf16_f32 v112, v80, v81\n\t" "v_mfma_f32_16x16x32_bf16 v[100:103], v[68:71], a[232:235], v[100:103]\n\t" "v_cvt_pk_bf16_f32 v113, v82, v83\n\t" \
    "v_mfma_f32_16x16x32_bf16 v[96:99], v[72:75], a[236:239], v[96:99]\n\t" "v_cvt_pk_bf16_f32 v114, v84, v85\n\t" \
    "v_mfma_f32_16x16x32_bf16 v[100:103], v[76:79], a[236:239], v[100:103]\n\t" "v_cvt_pk_bf16_f32 v115, v86, v87\n\t" \
    "v_mfma_f32_16x16x32_bf16 v[104:107], v[48:51], a[240:243], 0\n\t" "v_cvt_pk_bf16_f32 v116, v88, v89\n\t" \
    "v_mfma_f32_16x16x32_bf16 v[108:111], v[52:55], a[240:243], 0\n\t" "v_cvt_pk_bf16_f32 v117, v90, v91\n\t" \
    "v_mfma_f32_16x16x32_bf16 v[104:107], v[56:59], a[244:247], v[104:107]\n\t" "buffer_store_dwordx4 v[112:115], %[z1], %[rs], s63 offen sc1\n\t" \
    "v_mfma_f32_16x16x32_bf16 v[108:111], v[60:63], a[244:247], v[108:111]\n\t" "v_mfma_f32_16x16x32_bf16 v[104:107], v[64:67], a[248:251], v[104:107]\n\t" \
    "v_cvt_pk_bf16_f32 v118, v92, v93\n\t" "v_cvt_pk_bf16_f32 v119, v94, v95\n\t" "s_waitcnt vmcnt(11)\n\t" "v_readfirstlane_b32 s66, v120\n\t" \
    "v_mfma_f32_16x16x32_bf16 v[108:111], v[68:71], a[248:251], v[108:111]\n\t" "v_readfirstlane_b32 s67, v121\n\t" "v_mov_b32 v122, s60\n\t" \
    "buffer_store_dword v122, %[fo], %[rc], %[fz] offen sc1\n\t" "v_mfma_f32_16x16x32_bf16 v[104:107], v[72:75], a[252:255], v[104:107]\n\t" \
    "buffer_store_dwordx4 v[116:119], %[z2], %[rs], s63 offen sc1\n\t" "s_add_u32 s63, s63, 0x20000\n\t" "s_max_u32 s61, s61, s66\n\t" \
    "v_mfma_f32_16x16x32_bf16 v[108:111], v[76:79], a[252:255], v[108:111]\n\t" "s_max_u32 s62, s62, s67\n\t" "s_add_u32 s60, s60, 1\n\t" ZSUM "33:\n\t" "s_branch 10b\n\t" \
    "40:\n\t" "v_mfma_f32_16x16x32_bf16 v[80:83], v[16:19], a[0:3], 0\n\t" "v_mfma_f32_16x16x32_bf16 v[84:87], v[20:23], a[0:3], 0\n\t" \
    "v_mfma_f32_16x16x32_bf16 v[80:83], v[24:27], a[4:7], v[80:83]\n\t" "v_mfma_f32_16x16x32_bf16 v[84:87], v[28:31], a[4:7], v[84:87]\n\t" \
    "v_mfma_f32_16x16x32_bf16 v[80:83], v[32:35], a[8:11], v[80:83]\n\t" "v_cvt_pk_bf16_f32 v112, v96, v97\n\t" \
    "v_mfma_f32_16x16x32_bf16 v[84:87], v[36:39], a[8:11], v[84:87]\n\t" "v_cvt_pk_bf16_f32 v113, v98, v99\n\t" \
    "v_mfma_f32_16x16x32_bf16 v[80:83], v[40:43], a[12:15], v[80:83]\n\t" "v_cvt_pk_bf16_f32 v114, v100, v101\n\t" \
    "v_mfma_f32_16x16x32_bf16 v[84:87], v[44:47], a[12:15], v[84:87]\n\t" "v_cvt_pk_bf16_f32 v115, v102, v103\n\t" \
    "v_mfma_f32_16x16x32_bf16 v[88:91], v[16:19], a[16:19], 0\n\t" "v_cvt_pk_bf16_f32 v116, v104, v105\n\t" "v_mfma_f32_16x16x32_bf16 v[92:95], v[20:23], a[16:19], 0\n\t" \
    "v_cvt_pk_bf16_f32 v117, v106, v107\n\t" "v_mfma_f32_16x16x32_bf16 v[88:91], v[24:27], a[20:23], v[88:91]\n\t" \
    "buffer_store_dwordx4 v[112:115], %[z1], %[rs], s63 offen sc1\n\t" "v_mfma_f32_16x16x32_bf16 v[92:95], v[28:31], a[20:23], v[92:95]\n\t" \
    "v_mfma_f32_16x16x32_bf16 v[88:91], v[32:35], a[24:27], v[88:91]\n\t" "v_cvt_pk_bf16_f32 v118, v108, v109\n\t" \
    "v_mfma_f32_16x16x32_bf16 v[92:95], v[36:39], a[24:27], v[92:95]\n\t" "v_cvt_pk_bf16_f32 v119, v110, v111\n\t" \
    "v_mfma_f32_16x16x32_bf16 v[88:91], v[40:43], a[28:31], v[88:91]\n\t" "buffer_store_dwordx4 v[116:119], %[z2], %[rs], s63 offen sc1\n\t" "s_and_b32 s63, s60, %[rm]\n\t" \
    "v_mfma_f32_16x16x32_bf16 v[92:95], v[44:47], a[28:31], v[92:95]\n\t" "s_lshl_b32 s63, s63, 22\n\t" "s_add_u32 s63, s63, %[zb]\n\t" \
    "v_mfma_f32_16x16x32_bf16 v[96:99], v[16:19], a[32:35], 0\n\t" "buffer_load_dword v120, %[fo], %[rc], %[fv] offen sc1\n\t" \
    "v_mfma_f32_16x16x32_bf16 v[100:103], v[20:23], a[32:35], 0\n\t" "v_mfma_f32_16x16x32_bf16 v[96:99], v[24:27], a[36:39], v[96:99]\n\t" \
    "buffer_load_dword v121, %[fo], %[rc], %[fs] offen sc1\n\t" "v_mfma_f32_16x16x32_bf16 v[100:103], v[28:31], a[36:39], v[100:103]\n\t" \
    "v_mfma_f32_16x16x32_bf16 v[96:99], v[32:35], a[40:43], v[96:99]\n\t" "v_cvt_pk_bf16_f32 v112, v80, v81\n\t" \
    "v_mfma_f32_16x16x32_bf16 v[100:103], v[36:39], a[40:43], v[100:103]\n\t" "v_cvt_pk_bf16_f32 v113, v82, v83\n\t" \
    "v_mfma_f32_16x16x32_bf16 v[96:99], v[40:43], a[44:47], v[96:99]\n\t" "v_cvt_pk_bf16_f32 v114, v84, v85\n\t" \
    "v_mfma_f32_16x16x32_bf16 v[100:103], v[44:47], a[44:47], v[100:103]\n\t" "v_cvt_pk_bf16_f32 v115, v86, v87\n\t" \
    "v_mfma_f32_16x16x32_bf16 v[104:107], v[16:19], a[48:51], 0\n\t" "v_cvt_pk_bf16_f32 v116, v88, v89\n\t" "v_mfma_f32_16x16x32_bf16 v[108:111], v[20:23], a[48:51], 0\n\t" \
    "v_cvt_pk_bf16_f32 v117, v90, v91\n\t" "v_mfma_f32_16x16x32_bf16 v[104:107], v[24:27], a[52:55], v[104:107]\n\t" \
    "buffer_store_dwordx4 v[112:115], %[z1], %[rs], s63 offen sc1\n\t" "v_mfma_f32_16x16x32_bf16 v[108:111], v[28:31], a[52:55], v[108:111]\n\t" \
    "v_mfma_f32_16x16x32_bf16 v[104:107], v[32:35], a[56:59], v[104:107]\n\t" "v_cvt_pk_bf16_f32 v118, v92, v93\n\t" \
    "v_mfma_f32_16x16x32_bf16 v[108:111], v[36:39], a[56:59], v[108:111]\n\t" "v_cvt_pk_bf16_f32 v119, v94, v95\n\t" \
    "v_mfma_f32_16x16x32_bf16 v[104:107], v[40:43], a[60:63], v[104:107]\n\t" "buffer_store_dwordx4 v[116:119], %[z2], %[rs], s63 offen sc1\n\t" \
    "v_mfma_f32_16x16x32_bf16 v[108:111], v[44:47], a[60:63], v[108:111]\n\t" "s_add_u32 s63, s63, 0x20000\n\t" "v_mfma_f32_16x16x32_bf16 v[80:83], v[16:19], a[64:67], 0\n\t" \
    "s_add_u32 s65, s60, 1\n\t" "s_lshr_b32 s66, s65, 2\n\t" "v_mfma_f32_16x16x32_bf16 v[84:87], v[20:23], a[64:67], 0\n\t" "s_xor_b32 s66, s66, %[xp]\n\t" \
    "s_and_b32 s66, s66, 1\n\t" "v_mfma_f32_16x16x32_bf16 v[80:83], v[24:27], a[68:71], v[80:83]\n\t" "s_lshl_b32 s66, s66, 13\n\t" "s_lshl_b32 s64, s65, 19\n\t" \
    "v_mfma_f32_16x16x32_bf16 v[84:87], v[28:31], a[68:71], v[84:87]\n\t" "s_add_u32 s64, s64, s66\n\t" "s_add_u32 s64, s64, %[vb]\n\t" \
    "v_mfma_f32_16x16x32_bf16 v[80:83], v[32:35], a[72:75], v[80:83]\n\t" "v_cvt_pk_bf16_f32 v112, v96, v97\n\t" \
    "v_mfma_f32_16x16x32_bf16 v[84:87], v[36:39], a[72:75], v[84:87]\n\t" "v_cvt_pk_bf16_f32 v113, v98, v99\n\t" \
    "v_mfma_f32_16x16x32_bf16 v[80:83], v[40:43], a[76:79], v[80:83]\n\t" "v_cvt_pk_bf16_f32 v114, v100, v101\n\t" \
    "v_mfma_f32_16x16x32_bf16 v[84:87], v[44:47], a[76:79], v[84:87]\n\t" "v_cvt_pk_bf16_f32 v115, v102, v103\n\t" \
    "v_mfma_f32_16x16x32_bf16 v[88:91], v[16:19], a[80:83], 0\n\t" "v_cvt_pk_bf16_f32 v116, v104, v105\n\t" "v_mfma_f32_16x16x32_bf16 v[92:95], v[20:23], a[80:83], 0\n\t" \
    "v_cvt_pk_bf16_f32 v117, v106, v107\n\t" "v_mfma_f32_16x16x32_bf16 v[88:91], v[24:27], a[84:87], v[88:91]\n\t" \
    "buffer_store_dwordx4 v[112:115], %[z1], %[rs], s63 offen sc1\n\t" "v_mfma_f32_16x16x32_bf16 v[92:95], v[28:31], a[84:87], v[92:95]\n\t" \
    "v_mfma_f32_16x16x32_bf16 v[88:91], v[32:35], a[88:91], v[88:91]\n\t" "v_cvt_pk_bf16_f32 v118, v108, v109\n\t" \
    "v_mfma_f32_16x16x32_bf16 v[92:95], v[36:39], a[88:91], v[92:95]\n\t" "v_cvt_pk_bf16_f32 v119, v110, v111\n\t" \
    "v_mfma_f32_16x16x32_bf16 v[88:91], v[40:43], a[92:95], v[88:91]\n\t" "buffer_store_dwordx4 v[116:119], %[z2], %[rs], s63 offen sc1\n\t" \
    "v_mfma_f32_16x16x32_bf16 v[92:95], v[44:47], a[92:95], v[92:95]\n\t" "s_add_u32 s63, s63, 0x20000\n\t" "v_mfma_f32_16x16x32_bf16 v[96:99], v[16:19], a[96:99], 0\n\t" \
    "v_mfma_f32_16x16x32_bf16 v[100:103], v[20:23], a[96:99], 0\n\t" "v_mfma_f32_16x16x32_bf16 v[96:99], v[24:27], a[100:103], v[96:99]\n\t" \
    "v_mfma_f32_16x16x32_bf16 v[100:103], v[28:31], a[100:103], v[100:103]\n\t" "v_mfma_f32_16x16x32_bf16 v[96:99], v[32:35], a[104:107], v[96:99]\n\t" \
    "v_cvt_pk_bf16_f32 v112, v80, v81\n\t" "v_mfma_f32_16x16x32_bf16 v[100:103], v[36:39], a[104:107], v[100:103]\n\t" "v_cvt_pk_bf16_f32 v113, v82, v83\n\t" \
    "v_mfma_f32_16x16x32_bf16 v[96:99], v[40:43], a[108:111], v[96:99]\n\t" "v_cvt_pk_bf16_f32 v114, v84, v85\n\t" \
    "v_mfma_f32_16x16x32_bf16 v[100:103], v[44:47], a[108:111], v[100:103]\n\t" "v_cvt_pk_bf16_f32 v115, v86, v87\n\t" \
    "v_mfma_f32_16x16x32_bf16 v[104:107], v[16:19], a[112:115], 0\n\t" "v_cvt_pk_bf16_f32 v116, v88, v89\n\t" \
    "v_mfma_f32_16x16x32_bf16 v[108:111], v[20:23], a[112:115], 0\n\t" "v_cvt_pk_bf16_f32 v117, v90, v91\n\t" \
    "v_mfma_f32_16x16x32_bf16 v[104:107], v[24:27], a[116:119], v[104:107]\n\t" "buffer_store_dwordx4 v[112:115], %[z1], %[rs], s63 offen sc1\n\t" \
    "v_mfma_f32_16x16x32_bf16 v[108:111], v[28:31], a[116:119], v[108:111]\n\t" "v_mfma_f32_16x16x32_bf16 v[104:107], v[32:35], a[120:123], v[104:107]\n\t" \
    "v_cvt_pk_bf16_f32 v118, v92, v93\n\t" "v_mfma_f32_16x16x32_bf16 v[108:111], v[36:39], a[120:123], v[108:111]\n\t" "v_cvt_pk_bf16_f32 v119, v94, v95\n\t" \
    "v_mfma_f32_16x16x32_bf16 v[104:107], v[40:43], a[124:127], v[104:107]\n\t" "buffer_store_dwordx4 v[116:119], %[z2], %[rs], s63 offen sc1\n\t" \
    "v_mfma_f32_16x16x32_bf16 v[108:111], v[44:47], a[124:127], v[108:111]\n\t" "s_add_u32 s63, s63, 0x20000\n\t" \
    "v_mfma_f32_16x16x32_bf16 v[80:83], v[16:19], a[128:131], 0\n\t" "s_waitcnt vmcnt(6)\n\t" "v_readfirstlane_b32 s66, v120\n\t" "v_readfirstlane_b32 s67, v121\n\t" \
    "v_mfma_f32_16x16x32_bf16 v[84:87], v[20:23], a[128:131], 0\n\t" "v_mov_b32 v122, s60\n\t" "buffer_store_dword v122, %[fo], %[rc], %[fz] offen sc1\n\t" \
    "v_mfma_f32_16x16x32_bf16 v[80:83], v[24:27], a[132:135], v[80:83]\n\t" "s_max_u32 s61, s61, s66\n\t" "s_max_u32 s62, s62, s67\n\t" "s_cmp_gt_u32 s61, s65\n\t" \
    "v_mfma_f32_16x16x32_bf16 v[84:87], v[28:31], a[132:135], v[84:87]\n\t" "s_cselect_b32 s65, 1, 0\n\t" "s_cselect_b32 s64, s64, 0x7f000000\n\t" \
    "v_mfma_f32_16x16x32_bf16 v[80:83], v[32:35], a[136:139], v[80:83]\n\t" "buffer_load_dwordx4 v[48:51], %[ao], %[rs], s64 offen offset:0 sc1\n\t" \
    "v_cvt_pk_bf16_f32 v112, v96, v97\n\t" "v_mfma_f32_16x16x32_bf16 v[84:87], v[36:39], a[136:139], v[84:87]\n\t" "v_cvt_pk_bf16_f32 v113, v98, v99\n\t" \
    "v_mfma_f32_16x16x32_bf16 v[80:83], v[40:43], a[140:143], v[80:83]\n\t" "buffer_load_dwordx4 v[52:55], %[ao], %[rs], s64 offen offset:256 sc1\n\t" \
    "v_cvt_pk_bf16_f32 v114, v100, v101\n\t" "v_mfma_f32_16x16x32_bf16 v[84:87], v[44:47], a[140:143], v[84:87]\n\t" "v_cvt_pk_bf16_f32 v115, v102, v103\n\t" \
    "v_mfma_f32_16x16x32_bf16 v[88:91], v[16:19], a[144:147], 0\n\t" "buffer_load_dwordx4 v[56:59], %[ao], %[rs], s64 offen offset:2048 sc1\n\t" \
    "v_cvt_pk_bf16_f32 v116, v104, v105\n\t" "v_mfma_f32_16x16x32_bf16 v[92:95], v[20:23], a[144:147], 0\n\t" "v_cvt_pk_bf16_f32 v117, v106, v107\n\t" \
    "v_mfma_f32_16x16x32_bf16 v[88:91], v[24:27], a[148:151], v[88:91]\n\t" "buffer_load_dwordx4 v[60:63], %[ao], %[rs], s64 offen offset:2304 sc1\n\t" \
    "v_mfma_f32_16x16x32_bf16 v[92:95], v[28:31], a[148:151], v[92:95]\n\t" "buffer_store_dwordx4 v[112:115], %[z1], %[rs], s63 offen sc1\n\t" \
    "v_mfma_f32_16x16x32_bf16 v[88:91], v[32:35], a[152:155], v[88:91]\n\t" "buffer_load_dwordx4 v[64:67], %[a1], %[rs], s64 offen offset:0 sc1\n\t" \
    "v_cvt_pk_bf16_f32 v118, v108, v109\n\t" "v_mfma_f32_16x16x32_bf16 v[92:95], v[36:39], a[152:155], v[92:95]\n\t" "v_cvt_pk_bf16_f32 v119, v110, v111\n\t" \
    "v_mfma_f32_16x16x32_bf16 v[88:91], v[40:43], a[156:159], v[88:91]\n\t" "buffer_load_dwordx4 v[68:71], %[a1], %[rs], s64 offen offset:256 sc1\n\t" \
    "buffer_store_dwordx4 v[116:119], %[z2], %[rs], s63 offen sc1\n\t" "v_mfma_f32_16x16x32_bf16 v[92:95], v[44:47], a[156:159], v[92:95]\n\t" \
    "s_add_u32 s63, s63, 0x20000\n\t" "v_mfma_f32_16x16x32_bf16 v[96:99], v[16:19], a[160:163], 0\n\t" \
    "buffer_load_dwordx4 v[72:75], %[a1], %[rs], s64 offen offset:2048 sc1\n\t" "v_mfma_f32_16x16x32_bf16 v[100:103], v[20:23], a[160:163], 0\n\t" \
    "v_mfma_f32_16x16x32_bf16 v[96:99], v[24:27], a[164:167], v[96:99]\n\t" "buffer_load_dwordx4 v[76:79], %[a1], %[rs], s64 offen offset:2304 sc1\n\t" \
    "v_mfma_f32_16x16x32_bf16 v[100:103], v[28:31], a[164:167], v[100:103]\n\t" "v_mfma_f32_16x16x32_bf16 v[96:99], v[32:35], a[168:171], v[96:99]\n\t" \
    "v_cvt_pk_bf16_f32 v112, v80, v81\n\t" "v_mfma_f32_16x16x32_bf16 v[100:103], v[36:39], a[168:171], v[100:103]\n\t" "v_cvt_pk_bf16_f32 v113, v82, v83\n\t" \
    "v_mfma_f32_16x16x32_bf16 v[96:99], v[40:43], a[172:175], v[96:99]\n\t" "v_cvt_pk_bf16_f32 v114, v84, v85\n\t" \
    "v_mfma_f32_16x16x32_bf16 v[100:103], v[44:47], a[172:175], v[100:103]\n\t" "v_cvt_pk_bf16_f32 v115, v86, v87\n\t" \
    "v_mfma_f32_16x16x32_bf16 v[104:107], v[16:19], a[176:179], 0\n\t" "v_cvt_pk_bf16_f32 v116, v88, v89\n\t" \
    "v_mfma_f32_16x16x32_bf16 v[108:111], v[20:23], a[176:179], 0\n\t" "v_cvt_pk_bf16_f32 v117, v90, v91\n\t" \
    "v_mfma_f32_16x16x32_bf16 v[104:107], v[24:27], a[180:183], v[104:107]\n\t" "buffer_store_dwordx4 v[112:115], %[z1], %[rs], s63 offen sc1\n\t" \
    "v_mfma_f32_16x16x32_bf16 v[108:111], v[28:31], a[180:183], v[108:111]\n\t" "v_mfma_f32_16x16x32_bf16 v[104:107], v[32:35], a[184:187], v[104:107]\n\t" \
    "v_cvt_pk_bf16_f32 v118, v92, v93\n\t" "v_mfma_f32_16x16x32_bf16 v[108:111], v[36:39], a[184:187], v[108:111]\n\t" "v_cvt_pk_bf16_f32 v119, v94, v95\n\t" \
    "v_mfma_f32_16x16x32_bf16 v[104:107], v[40:43], a[188:191], v[104:107]\n\t" "buffer_store_dwordx4 v[116:119], %[z2], %[rs], s63 offen sc1\n\t" \
    "v_mfma_f32_16x16x32_bf16 v[108:111], v[44:47], a[188:191], v[108:111]\n\t" "s_add_u32 s63, s63, 0x20000\n\t" \
    "v_mfma_f32_16x16x32_bf16 v[80:83], v[16:19], a[192:195], 0\n\t" "v_mfma_f32_16x16x32_bf16 v[84:87], v[20:23], a[192:195], 0\n\t" \
    "v_mfma_f32_16x16x32_bf16 v[80:83], v[24:27], a[196:199], v[80:83]\n\t" "v_mfma_f32_16x16x32_bf16 v[84:87], v[28:31], a[196:199], v[84:87]\n\t" \
    "v_mfma_f32_16x16x32_bf16 v[80:83], v[32:35], a[200:203], v[80:83]\n\t" "v_cvt_pk_bf16_f32 v112, v96, v97\n\t" \
    "v_mfma_f32_16x16x32_bf16 v[84:87], v[36:39], a[200:203], v[84:87]\n\t" "v_cvt_pk_bf16_f32 v113, v98, v99\n\t" \
    "v_mfma_f32_16x16x32_bf16 v[80:83], v[40:43], a[204:207], v[80:83]\n\t" "v_cvt_pk_bf16_f32 v114, v100, v101\n\t" \
    "v_mfma_f32_16x16x32_bf16 v[84:87], v[44:47], a[204:207], v[84:87]\n\t" "v_cvt_pk_bf16_f32 v115, v102, v103\n\t" \
    "v_mfma_f32_16x16x32_bf16 v[88:91], v[16:19], a[208:211], 0\n\t" "v_cvt_pk_bf16_f32 v116, v104, v105\n\t" "v_mfma_f32_16x16x32_bf16 v[92:95], v[20:23], a[208:211], 0\n\t" \
    "v_cvt_pk_bf16_f32 v117, v106, v107\n\t" "v_mfma_f32_16x16x32_bf16 v[88:91], v[24:27], a[212:215], v[88:91]\n\t" \
    "buffer_store_dwordx4 v[112:115], %[z1], %[rs], s63 offen sc1\n\t" "v_mfma_f32_16x16x32_bf16 v[92:95], v[28:31], a[212:215], v[92:95]\n\t" \
    "v_mfma_f32_16x16x32_bf16 v[88:91], v[32:35], a[216:219], v[88:91]\n\t" "v_cvt_pk_bf16_f32 v118, v108, v109\n\t" \
    "v_mfma_f32_16x16x32_bf16 v[92:95], v[36:39], a[216:219], v[92:95]\n\t" "v_cvt_pk_bf16_f32 v119, v110, v111\n\t" \
    "v_mfma_f32_16x16x32_bf16 v[88:91], v[40:43], a[220:223], v[88:91]\n\t" "buffer_store_dwordx4 v[116:119], %[z2], %[rs], s63 offen sc1\n\t" \
    "v_mfma_f32_16x16x32_bf16 v[92:95], v[44:47], a[220:223], v[92:95]\n\t" "s_add_u32 s63, s63, 0x20000\n\t" "v_mfma_f32_16x16x32_bf16 v[96:99], v[16:19], a[224:227], 0\n\t" \
    "v_mfma_f32_16x16x32_bf16 v[100:103], v[20:23], a[224:227], 0\n\t" "v_mfma_f32_16x16x32_bf16 v[96:99], v[24:27], a[228:231], v[96:99]\n\t" \
    "v_mfma_f32_16x16x32_bf16 v[100:103], v[28:31], a[228:231], v[100:103]\n\t" "v_mfma_f32_16x16x32_bf16 v[96:99], v[32:35], a[232:235], v[96:99]\n\t" \
    "v_cvt_pk_bf16_f32 v112, v80, v81\n\t" "v_mfma_f32_16x16x32_bf16 v[100:103], v[36:39], a[232:235], v[100:103]\n\t" "v_cvt_pk_bf16_f32 v113, v82, v83\n\t" \
    "v_mfma_f32_16x16x32_bf16 v[96:99], v[40:43], a[236:239], v[96:99]\n\t" "v_cvt_pk_bf16_f32 v114, v84, v85\n\t" \
    "v_mfma_f32_16x16x32_bf16 v[100:103], v[44:47], a[236:239], v[100:103]\n\t" "v_cvt_pk_bf16_f32 v115, v86, v87\n\t" \
    "v_mfma_f32_16x16x32_bf16 v[104:107], v[16:19], a[240:243], 0\n\t" "v_cvt_pk_bf16_f32 v116, v88, v89\n\t" \
    "v_mfma_f32_16x16x32_bf16 v[108:111], v[20:23], a[240:243], 0\n\t" "v_cvt_pk_bf16_f32 v117, v90, v91\n\t" \
    "v_mfma_f32_16x16x32_bf16 v[104:107], v[24:27], a[244:247], v[104:107]\n\t" "buffer_store_dwordx4 v[112:115], %[z1], %[rs], s63 offen sc1\n\t" \
    "v_mfma_f32_16x16x32_bf16 v[108:111], v[28:31], a[244:247], v[108:111]\n\t" "v_mfma_f32_16x16x32_bf16 v[104:107], v[32:35], a[248:251], v[104:107]\n\t" \
    "v_cvt_pk_bf16_f32 v118, v92, v93\n\t" "v_mfma_f32_16x16x32_bf16 v[108:111], v[36:39], a[248:251], v[108:111]\n\t" "v_cvt_pk_bf16_f32 v119, v94, v95\n\t" \
    "v_mfma_f32_16x16x32_bf16 v[104:107], v[40:43], a[252:255], v[104:107]\n\t" "buffer_store_dwordx4 v[116:119], %[z2], %[rs], s63 offen sc1\n\t" \
    "s_add_u32 s63, s63, 0x20000\n\t" "s_add_u32 s60, s60, 1\n\t" "v_mfma_f32_16x16x32_bf16 v[108:111], v[44:47], a[252:255], v[108:111]\n\t" "s_cmp_eq_u32 s65, 0\n\t" \
    "s_waitcnt vmcnt(6)\n\t" ZSUM "s_cbranch_scc0 23b\n\t" "s_branch 20f\n\t" "41:\n\t" "v_mfma_f32_16x16x32_bf16 v[80:83], v[48:51], a[0:3], 0\n\t" \
    "v_mfma_f32_16x16x32_bf16 v[84:87], v[52:55], a[0:3], 0\n\t" "v_mfma_f32_16x16x32_bf16 v[80:83], v[56:59], a[4:7], v[80:83]\n\t" \
    "v_mfma_f32_16x16x32_bf16 v[84:87], v[60:63], a[4:7], v[84:87]\n\t" "v_mfma_f32_16x16x32_bf16 v[80:83], v[64:67], a[8:11], v[80:83]\n\t" \
    "v_cvt_pk_bf16_f32 v112, v96, v97\n\t" "v_mfma_f32_16x16x32_bf16 v[84:87], v[68:71], a[8:11], v[84:87]\n\t" "v_cvt_pk_bf16_f32 v113, v98, v99\n\t" \
    "v_mfma_f32_16x16x32_bf16 v[80:83], v[72:75], a[12:15], v[80:83]\n\t" "v_cvt_pk_bf16_f32 v114, v100, v101\n\t" \
    "v_mfma_f32_16x16x32_bf16 v[84:87], v[76:79], a[12:15], v[84:87]\n\t" "v_cvt_pk_bf16_f32 v115, v102, v103\n\t" \
    "v_mfma_f32_16x16x32_bf16 v[88:91], v[48:51], a[16:19], 0\n\t" "v_cvt_pk_bf16_f32 v116, v104, v105\n\t" "v_mfma_f32_16x16x32_bf16 v[92:95], v[52:55], a[16:19], 0\n\t" \
    "v_cvt_pk_bf16_f32 v117, v106, v107\n\t" "v_mfma_f32_16x16x32_bf16 v[88:91], v[56:59], a[20:23], v[88:91]\n\t" \
    "buffer_store_dwordx4 v[112:115], %[z1], %[rs], s63 offen sc1\n\t" "v_mfma_f32_16x16x32_bf16 v[92:95], v[60:63], a[20:23], v[92:95]\n\t" \
    "v_mfma_f32_16x16x32_bf16 v[88:91], v[64:67], a[24:27], v[88:91]\n\t" "v_cvt_pk_bf16_f32 v118, v108, v109\n\t" \
    "v_mfma_f32_16x16x32_bf16 v[92:95], v[68:71], a[24:27], v[92:95]\n\t" "v_cvt_pk_bf16_f32 v119, v110, v111\n\t" \
    "v_mfma_f32_16x16x32_bf16 v[88:91], v[72:75], a[28:31], v[88:91]\n\t" "buffer_store_dwordx4 v[116:119], %[z2], %[rs], s63 offen sc1\n\t" "s_and_b32 s63, s60, %[rm]\n\t" \
    "v_mfma_f32_16x16x32_bf16 v[92:95], v[76:79], a[28:31], v[92:95]\n\t" "s_lshl_b32 s63, s63, 22\n\t" "s_add_u32 s63, s63, %[zb]\n\t" \
    "v_mfma_f32_16x16x32_bf16 v[96:99], v[48:51], a[32:35], 0\n\t" "buffer_load_dword v120, %[fo], %[rc], %[fv] offen sc1\n\t" \
    "v_mfma_f32_16x16x32_bf16 v[100:103], v[52:55], a[32:35], 0\n\t" "v_mfma_f32_16x16x32_bf16 v[96:99], v[56:59], a[36:39], v[96:99]\n\t" \
    "buffer_load_dword v121, %[fo], %[rc], %[fs] offen sc1\n\t" "v_mfma_f32_16x16x32_bf16 v[100:103], v[60:63], a[36:39], v[100:103]\n\t" \
    "v_mfma_f32_16x16x32_bf16 v[96:99], v[64:67], a[40:43], v[96:99]\n\t" "v_cvt_pk_bf16_f32 v112, v80, v81\n\t" \
    "v_mfma_f32_16x16x32_bf16 v[100:103], v[68:71], a[40:43], v[100:103]\n\t" "v_cvt_pk_bf16_f32 v113, v82, v83\n\t" \
    "v_mfma_f32_16x16x32_bf16 v[96:99], v[72:75], a[44:47], v[96:99]\n\t" "v_cvt_pk_bf16_f32 v114, v84, v85\n\t" \
    "v_mfma_f32_16x16x32_bf16 v[100:103], v[76:79], a[44:47], v[100:103]\n\t" "v_cvt_pk_bf16_f32 v115, v86, v87\n\t" \
    "v_mfma_f32_16x16x32_bf16 v[104:107], v[48:51], a[48:51], 0\n\t" "v_cvt_pk_bf16_f32 v116, v88, v89\n\t" "v_mfma_f32_16x16x32_bf16 v[108:111], v[52:55], a[48:51], 0\n\t" \
    "v_cvt_pk_bf16_f32 v117, v90, v91\n\t" "v_mfma_f32_16x16x32_bf16 v[104:107], v[56:59], a[52:55], v[104:107]\n\t" \
    "buffer_store_dwordx4 v[112:115], %[z1], %[rs], s63 offen sc1\n\t" "v_mfma_f32_16x16x32_bf16 v[108:111], v[60:63], a[52:55], v[108:111]\n\t" \
    "v_mfma_f32_16x16x32_bf16 v[104:107], v[64:67], a[56:59], v[104:107]\n\t" "v_cvt_pk_bf16_f32 v118, v92, v93\n\t" \
    "v_mfma_f32_16x16x32_bf16 v[108:111], v[68:71], a[56:59], v[108:111]\n\t" "v_cvt_pk_bf16_f32 v119, v94, v95\n\t" \
    "v_mfma_f32_16x16x32_bf16 v[104:107], v[72:75], a[60:63], v[104:107]\n\t" "buffer_store_dwordx4 v[116:119], %[z2], %[rs], s63 offen sc1\n\t" \
    "v_mfma_f32_16x16x32_bf16 v[108:111], v[76:79], a[60:63], v[108:111]\n\t" "s_add_u32 s63, s63, 0x20000\n\t" "v_mfma_f32_16x16x32_bf16 v[80:83], v[48:51], a[64:67], 0\n\t" \
    "s_add_u32 s65, s60, 1\n\t" "s_lshr_b32 s66, s65, 2\n\t" "v_mfma_f32_16x16x32_bf16 v[84:87], v[52:55], a[64:67], 0\n\t" "s_xor_b32 s66, s66, %[xp]\n\t" \
    "s_and_b32 s66, s66, 1\n\t" "v_mfma_f32_16x16x32_bf16 v[80:83], v[56:59], a[68:71], v[80:83]\n\t" "s_lshl_b32 s66, s66, 13\n\t" "s_lshl_b32 s64, s65, 19\n\t" \
    "v_mfma_f32_16x16x32_bf16 v[84:87], v[60:63], a[68:71], v[84:87]\n\t" "s_add_u32 s64, s64, s66\n\t" "s_add_u32 s64, s64, %[vb]\n\t" \
    "v_mfma_f32_16x16x32_bf16 v[80:83], v[64:67], a[72:75], v[80:83]\n\t" "v_cvt_pk_bf16_f32 v112, v96, v97\n\t" \
    "v_mfma_f32_16x16x32_bf16 v[84:87], v[68:71], a[72:75], v[84:87]\n\t" "v_cvt_pk_bf16_f32 v113, v98, v99\n\t" \
    "v_mfma_f32_16x16x32_bf16 v[80:83], v[72:75], a[76:79], v[80:83]\n\t" "v_cvt_pk_bf16_f32 v114, v100, v101\n\t" \
    "v_mfma_f32_16x16x32_bf16 v[84:87], v[76:79], a[76:79], v[84:87]\n\t" "v_cvt_pk_bf16_f32 v115, v102, v103\n\t" \
    "v_mfma_f32_16x16x32_bf16 v[88:91], v[48:51], a[80:83], 0\n\t" "v_cvt_pk_bf16_f32 v116, v104, v105\n\t" "v_mfma_f32_16x16x32_bf16 v[92:95], v[52:55], a[80:83], 0\n\t" \
    "v_cvt_pk_bf16_f32 v117, v106, v107\n\t" "v_mfma_f32_16x16x32_bf16 v[88:91], v[56:59], a[84:87], v[88:91]\n\t" \
    "buffer_store_dwordx4 v[112:115], %[z1], %[rs], s63 offen sc1\n\t" "v_mfma_f32_16x16x32_bf16 v[92:95], v[60:63], a[84:87], v[92:95]\n\t" \
    "v_mfma_f32_16x16x32_bf16 v[88:91], v[64:67], a[88:91], v[88:91]\n\t" "v_cvt_pk_bf16_f32 v118, v108, v109\n\t" \
    "v_mfma_f32_16x16x32_bf16 v[92:95], v[68:71], a[88:91], v[92:95]\n\t" "v_cvt_pk_bf16_f32 v119, v110, v111\n\t" \
    "v_mfma_f32_16x16x32_bf16 v[88:91], v[72:75], a[92:95], v[88:91]\n\t" "buffer_store_dwordx4 v[116:119], %[z2], %[rs], s63 offen sc1\n\t" \
    "v_mfma_f32_16x16x32_bf16 v[92:95], v[76:79], a[92:95], v[92:95]\n\t" "s_add_u32 s63, s63, 0x20000\n\t" "v_mfma_f32_16x16x32_bf16 v[96:99], v[48:51], a[96:99], 0\n\t" \
    "v_mfma_f32_16x16x32_bf16 v[100:103], v[52:55], a[96:99], 0\n\t" "v_mfma_f32_16x16x32_bf16 v[96:99], v[56:59], a[100:103], v[96:99]\n\t" \
    "v_mfma_f32_16x16x32_bf16 v[100:103], v[60:63], a[100:103], v[100:103]\n\t" "v_mfma_f32_16x16x32_bf16 v[96:99], v[64:67], a[104:107], v[96:99]\n\t" \
    "v_cvt_pk_bf16_f32 v112, v80, v81\n\t" "v_mfma_f32_16x16x32_bf16 v[100:103], v[68:71], a[104:107], v[100:103]\n\t" "v_cvt_pk_bf16_f32 v113, v82, v83\n\t" \
    "v_mfma_f32_16x16x32_bf16 v[96:99], v[72:75], a[108:111], v[96:99]\n\t" "v_cvt_pk_bf16_f32 v114, v84, v85\n\t" \
    "v_mfma_f32_16x16x32_bf16 v[100:103], v[76:79], a[108:111], v[100:103]\n\t" "v_cvt_pk_bf16_f32 v115, v86, v87\n\t" \
    "v_mfma_f32_16x16x32_bf16 v[104:107], v[48:51], a[112:115], 0\n\t" "v_cvt_pk_bf16_f32 v116, v88, v89\n\t" \
    "v_mfma_f32_16x16x32_bf16 v[108:111], v[52:55], a[112:115], 0\n\t" "v_cvt_pk_bf16_f32 v117, v90, v91\n\t" \
    "v_mfma_f32_16x16x32_bf16 v[104:107], v[56:59], a[116:119], v[104:107]\n\t" "buffer_store_dwordx4 v[112:115], %[z1], %[rs], s63 offen sc1\n\t" \
    "v_mfma_f32_16x16x32_bf16 v[108:111], v[60:63], a[116:119], v[108:111]\n\t" "v_mfma_f32_16x16x32_bf16 v[104:107], v[64:67], a[120:123], v[104:107]\n\t" \
    "v_cvt_pk_bf16_f32 v118, v92, v93\n\t" "v_mfma_f32_16x16x32_bf16 v[108:111], v[68:71], a[120:123], v[108:111]\n\t" "v_cvt_pk_bf16_f32 v119, v94, v95\n\t" \
    "v_mfma_f32_16x16x32_bf16 v[104:107], v[72:75], a[124:127], v[104:107]\n\t" "buffer_store_dwordx4 v[116:119], %[z2], %[rs], s63 offen sc1\n\t" \
    "v_mfma_f32_16x16x32_bf16 v[108:111], v[76:79], a[124:127], v[108:111]\n\t" "s_add_u32 s63, s63, 0x20000\n\t" \
    "v_mfma_f32_16x16x32_bf16 v[80:83], v[48:51], a[128:131], 0\n\t" "s_waitcnt vmcnt(6)\n\t" "v_readfirstlane_b32 s66, v120\n\t" "v_readfirstlane_b32 s67, v121\n\t" \
    "v_mfma_f32_16x16x32_bf16 v[84:87], v[52:55], a[128:131], 0\n\t" "v_mov_b32 v122, s60\n\t" "buffer_store_dword v122, %[fo], %[rc], %[fz] offen sc1\n\t" \
    "v_mfma_f32_16x16x32_bf16 v[80:83], v[56:59], a[132:135], v[80:83]\n\t" "s_max_u32 s61, s61, s66\n\t" "s_max_u32 s62, s62, s67\n\t" "s_cmp_gt_u32 s61, s65\n\t" \
    "v_mfma_f32_16x16x32_bf16 v[84:87], v[60:63], a[132:135], v[84:87]\n\t" "s_cselect_b32 s65, 1, 0\n\t" "s_cselect_b32 s64, s64, 0x7f000000\n\t" \
    "v_mfma_f32_16x16x32_bf16 v[80:83], v[64:67], a[136:139], v[80:83]\n\t" "buffer_load_dwordx4 v[16:19], %[ao], %[rs], s64 offen offset:0 sc1\n\t" \
    "v_cvt_pk_bf16_f32 v112, v96, v97\n\t" "v_mfma_f32_16x16x32_bf16 v[84:87], v[68:71], a[136:139], v[84:87]\n\t" "v_cvt_pk_bf16_f32 v113, v98, v99\n\t" \
    "v_mfma_f32_16x16x32_bf16 v[80:83], v[72:75], a[140:143], v[80:83]\n\t" "buffer_load_dwordx4 v[20:23], %[ao], %[rs], s64 offen offset:256 sc1\n\t" \
    "v_cvt_pk_bf16_f32 v114, v100, v101\n\t" "v_mfma_f32_16x16x32_bf16 v[84:87], v[76:79], a[140:143], v[84:87]\n\t" "v_cvt_pk_bf16_f32 v115, v102, v103\n\t" \
    "v_mfma_f32_16x16x32_bf16 v[88:91], v[48:51], a[144:147], 0\n\t" "buffer_load_dwordx4 v[24:27], %[ao], %[rs], s64 offen offset:2048 sc1\n\t" \
    "v_cvt_pk_bf16_f32 v116, v104, v105\n\t" "v_mfma_f32_16x16x32_bf16 v[92:95], v[52:55], a[144:147], 0\n\t" "v_cvt_pk_bf16_f32 v117, v106, v107\n\t" \
    "v_mfma_f32_16x16x32_bf16 v[88:91], v[56:59], a[148:151], v[88:91]\n\t" "buffer_load_dwordx4 v[28:31], %[ao], %[rs], s64 offen offset:2304 sc1\n\t" \
    "v_mfma_f32_16x16x32_bf16 v[92:95], v[60:63], a[148:151], v[92:95]\n\t" "buffer_store_dwordx4 v[112:115], %[z1], %[rs], s63 offen sc1\n\t" \
    "v_mfma_f32_16x16x32_bf16 v[88:91], v[64:67], a[152:155], v[88:91]\n\t" "buffer_load_dwordx4 v[32:35], %[a1], %[rs], s64 offen offset:0 sc1\n\t" \
    "v_cvt_pk_bf16_f32 v118, v108, v109\n\t" "v_mfma_f32_16x16x32_bf16 v[92:95], v[68:71], a[152:155], v[92:95]\n\t" "v_cvt_pk_bf16_f32 v119, v110, v111\n\t" \
    "v_mfma_f32_16x16x32_bf16 v[88:91], v[72:75], a[156:159], v[88:91]\n\t" "buffer_load_dwordx4 v[36:39], %[a1], %[rs], s64 offen offset:256 sc1\n\t" \
    "buffer_store_dwordx4 v[116:119], %[z2], %[rs], s63 offen sc1\n\t" "v_mfma_f32_16x16x32_bf16 v[92:95], v[76:79], a[156:159], v[92:95]\n\t" \
    "s_add_u32 s63, s63, 0x20000\n\t" "v_mfma_f32_16x16x32_bf16 v[96:99], v[48:51], a[160:163], 0\n\t" \
    "buffer_load_dwordx4 v[40:43], %[a1], %[rs], s64 offen offset:2048 sc1\n\t" "v_mfma_f32_16x16x32_bf16 v[100:103], v[52:55], a[160:163], 0\n\t" \
    "v_mfma_f32_16x16x32_bf16 v[96:99], v[56:59], a[164:167], v[96:99]\n\t" "buffer_load_dwordx4 v[44:47], %[a1], %[rs], s64 offen offset:2304 sc1\n\t" \
    "v_mfma_f32_16x16x32_bf16 v[100:103], v[60:63], a[164:167], v[100:103]\n\t" "v_mfma_f32_16x16x32_bf16 v[96:99], v[64:67], a[168:171], v[96:99]\n\t" \
    "v_cvt_pk_bf16_f32 v112, v80, v81\n\t" "v_mfma_f32_16x16x32_bf16 v[100:103], v[68:71], a[168:171], v[100:103]\n\t" "v_cvt_pk_bf16_f32 v113, v82, v83\n\t" \
    "v_mfma_f32_16x16x32_bf16 v[96:99], v[72:75], a[172:175], v[96:99]\n\t" "v_cvt_pk_bf16_f32 v114, v84, v85\n\t" \
    "v_mfma_f32_16x16x32_bf16 v[100:103], v[76:79], a[172:175], v[100:103]\n\t" "v_cvt_pk_bf16_f32 v115, v86, v87\n\t" \
    "v_mfma_f32_16x16x32_bf16 v[104:107], v[48:51], a[176:179], 0\n\t" "v_cvt_pk_bf16_f32 v116, v88, v89\n\t" \
    "v_mfma_f32_16x16x32_bf16 v[108:111], v[52:55], a[176:179], 0\n\t" "v_cvt_pk_bf16_f32 v117, v90, v91\n\t" \
    "v_mfma_f32_16x16x32_bf16 v[104:107], v[56:59], a[180:183], v[104:107]\n\t" "buffer_store_dwordx4 v[112:115], %[z1], %[rs], s63 offen sc1\n\t" \
    "v_mfma_f32_16x16x32_bf16 v[108:111], v[60:63], a[180:183], v[108:111]\n\t" "v_mfma_f32_16x16x32_bf16 v[104:107], v[64:67], a[184:187], v[104:107]\n\t" \
    "v_cvt_pk_bf16_f32 v118, v92, v93\n\t" "v_mfma_f32_16x16x32_bf16 v[108:111], v[68:71], a[184:187], v[108:111]\n\t" "v_cvt_pk_bf16_f32 v119, v94, v95\n\t" \
    "v_mfma_f32_16x16x32_bf16 v[104:107], v[72:75], a[188:191], v[104:107]\n\t" "buffer_store_dwordx4 v[116:119], %[z2], %[rs], s63 offen sc1\n\t" \
    "v_mfma_f32_16x16x32_bf16 v[108:111], v[76:79], a[188:191], v[108:111]\n\t" "s_add_u32 s63, s63, 0x20000\n\t" \
    "v_mfma_f32_16x16x32_bf16 v[80:83], v[48:51], a[192:195], 0\n\t" "v_mfma_f32_16x16x32_bf16 v[84:87], v[52:55], a[192:195], 0\n\t" \
    "v_mfma_f32_16x16x32_bf16 v[80:83], v[56:59], a[196:199], v[80:83]\n\t" "v_mfma_f32_16x16x32_bf16 v[84:87], v[60:63], a[196:199], v[84:87]\n\t" \
    "v_mfma_f32_16x16x32_bf16 v[80:83], v[64:67], a[200:203], v[80:83]\n\t" "v_cvt_pk_bf16_f32 v112, v96, v97\n\t" \
    "v_mfma_f32_16x16x32_bf16 v[84:87], v[68:71], a[200:203], v[84:87]\n\t" "v_cvt_pk_bf16_f32 v113, v98, v99\n\t" \
    "v_mfma_f32_16x16x32_bf16 v[80:83], v[72:75], a[204:207], v[80:83]\n\t" "v_cvt_pk_bf16_f32 v114, v100, v101\n\t" \
    "v_mfma_f32_16x16x32_bf16 v[84:87], v[76:79], a[204:207], v[84:87]\n\t" "v_cvt_pk_bf16_f32 v115, v102, v103\n\t" \
    "v_mfma_f32_16x16x32_bf16 v[88:91], v[48:51], a[208:211], 0\n\t" "v_cvt_pk_bf16_f32 v116, v104, v105\n\t" "v_mfma_f32_16x16x32_bf16 v[92:95], v[52:55], a[208:211], 0\n\t" \
    "v_cvt_pk_bf16_f32 v117, v106, v107\n\t" "v_mfma_f32_16x16x32_bf16 v[88:91], v[56:59], a[212:215], v[88:91]\n\t" \
    "buffer_store_dwordx4 v[112:115], %[z1], %[rs], s63 offen sc1\n\t" "v_mfma_f32_16x16x32_bf16 v[92:95], v[60:63], a[212:215], v[92:95]\n\t" \
    "v_mfma_f32_16x16x32_bf16 v[88:91], v[64:67], a[216:219], v[88:91]\n\t" "v_cvt_pk_bf16_f32 v118, v108, v109\n\t" \
    "v_mfma_f32_16x16x32_bf16 v[92:95], v[68:71], a[216:219], v[92:95]\n\t" "v_cvt_pk_bf16_f32 v119, v110, v111\n\t" \
    "v_mfma_f32_16x16x32_bf16 v[88:91], v[72:75], a[220:223], v[88:91]\n\t" "buffer_store_dwordx4 v[116:119], %[z2], %[rs], s63 offen sc1\n\t" \
    "v_mfma_f32_16x16x32_bf16 v[92:95], v[76:79], a[220:223], v[92:95]\n\t" "s_add_u32 s63, s63, 0x20000\n\t" "v_mfma_f32_16x16x32_bf16 v[96:99], v[48:51], a[224:227], 0\n\t" \
    "v_mfma_f32_16x16x32_bf16 v[100:103], v[52:55], a[224:227], 0\n\t" "v_mfma_f32_16x16x32_bf16 v[96:99], v[56:59], a[228:231], v[96:99]\n\t" \
    "v_mfma_f32_16x16x32_bf16 v[100:103], v[60:63], a[228:231], v[100:103]\n\t" "v_mfma_f32_16x16x32_bf16 v[96:99], v[64:67], a[232:235], v[96:99]\n\t" \
    "v_cvt_pk_bf16_f32 v112, v80, v81\n\t" "v_mfma_f32_16x16x32_bf16 v[100:103], v[68:71], a[232:235], v[100:103]\n\t" "v_cvt_pk_bf16_f32 v113, v82, v83\n\t" \
    "v_mfma_f32_16x16x32_bf16 v[96:99], v[72:75], a[236:239], v[96:99]\n\t" "v_cvt_pk_bf16_f32 v114, v84, v85\n\t" \
    "v_mfma_f32_16x16x32_bf16 v[100:103], v[76:79], a[236:239], v[100:103]\n\t" "v_cvt_pk_bf16_f32 v115, v86, v87\n\t" \
    "v_mfma_f32_16x16x32_bf16 v[104:107], v[48:51], a[240:243], 0\n\t" "v_cvt_pk_bf16_f32 v116, v88, v89\n\t" \
    "v_mfma_f32_16x16x32_bf16 v[108:111], v[52:55], a[240:243], 0\n\t" "v_cvt_pk_bf16_f32 v117, v90, v91\n\t" \
    "v_mfma_f32_16x16x32_bf16 v[104:107], v[56:59], a[244:247], v[104:107]\n\t" "buffer_store_dwordx4 v[112:115], %[z1], %[rs], s63 offen sc1\n\t" \
    "v_mfma_f32_16x16x32_bf16 v[108:111], v[60:63], a[244:247], v[108:111]\n\t" "v_mfma_f32_16x16x32_bf16 v[104:107], v[64:67], a[248:251], v[104:107]\n\t" \
    "v_cvt_pk_bf16_f32 v118, v92, v93\n\t" "v_mfma_f32_16x16x32_bf16 v[108:111], v[68:71], a[248:251], v[108:111]\n\t" "v_cvt_pk_bf16_f32 v119, v94, v95\n\t" \
    "v_mfma_f32_16x16x32_bf16 v[104:107], v[72:75], a[252:255], v[104:107]\n\t" "buffer_store_dwordx4 v[116:119], %[z2], %[rs], s63 offen sc1\n\t" \
    "s_add_u32 s63, s63, 0x20000\n\t" "s_add_u32 s60, s60, 1\n\t" "v_mfma_f32_16x16x32_bf16 v[108:111], v[76:79], a[252:255], v[108:111]\n\t" "s_cmp_eq_u32 s65, 0\n\t" \
    "s_waitcnt vmcnt(6)\n\t" ZSUM "s_cbranch_scc0 33b\n\t" "s_branch 30f\n\t" "20:\n\t" "s_nop 7\n\t" "v_cvt_pk_bf16_f32 v112, v96, v97\n\t" \
    "v_cvt_pk_bf16_f32 v113, v98, v99\n\t" "v_cvt_pk_bf16_f32 v114, v100, v101\n\t" "v_cvt_pk_bf16_f32 v115, v102, v103\n\t" "v_cvt_pk_bf16_f32 v116, v104, v105\n\t" \
    "v_cvt_pk_bf16_f32 v117, v106, v107\n\t" "v_cvt_pk_bf16_f32 v118, v108, v109\n\t" "v_cvt_pk_bf16_f32 v119, v110, v111\n\t" \
    "buffer_store_dwordx4 v[112:115], %[z1], %[rs], s63 offen sc1\n\t" "buffer_store_dwordx4 v[116:119], %[z2], %[rs], s63 offen sc1\n\t" \
    "buffer_load_dword v120, %[fo], %[rc], %[fv] offen sc1\n\t" "s_waitcnt vmcnt(0)\n\t" "v_readfirstlane_b32 s61, v120\n\t" "s_lshr_b32 s66, s60, 2\n\t" \
    "s_xor_b32 s66, s66, %[xp]\n\t" "s_and_b32 s66, s66, 1\n\t" "s_lshl_b32 s66, s66, 13\n\t" "s_lshl_b32 s64, s60, 19\n\t" "s_add_u32 s64, s64, s66\n\t" \
    "s_add_u32 s64, s64, %[vb]\n\t" "s_cmp_gt_u32 s61, s60\n\t" "s_cbranch_scc0 24f\n\t" "s_cmp_eq_u32 s60, 64\n\t" "s_cbranch_scc1 24f\n\t" \
    "buffer_load_dwordx4 v[48:51], %[ao], %[rs], s64 offen offset:0 sc1\n\t" "buffer_load_dwordx4 v[52:55], %[ao], %[rs], s64 offen offset:256 sc1\n\t" \
    "buffer_load_dwordx4 v[56:59], %[ao], %[rs], s64 offen offset:2048 sc1\n\t" "buffer_load_dwordx4 v[60:63], %[ao], %[rs], s64 offen offset:2304 sc1\n\t" \
    "buffer_load_dwordx4 v[64:67], %[a1], %[rs], s64 offen offset:0 sc1\n\t" "buffer_load_dwordx4 v[68:71], %[a1], %[rs], s64 offen offset:256 sc1\n\t" \
    "buffer_load_dwordx4 v[72:75], %[a1], %[rs], s64 offen offset:2048 sc1\n\t" "buffer_load_dwordx4 v[76:79], %[a1], %[rs], s64 offen offset:2304 sc1\n\t" \
    "v_mov_b32 v122, s60\n\t" "buffer_store_dword v122, %[fo], %[rc], %[fz] offen sc1\n\t" "s_waitcnt vmcnt(1)\n\t" "s_branch 23b\n\t" "24:\n\t" "v_mov_b32 v122, s60\n\t" \
    "buffer_store_dword v122, %[fo], %[rc], %[fz] offen sc1\n\t" "s_cmp_eq_u32 s60, 64\n\t" "s_cbranch_scc1 90f\n\t" "s_add_u32 s67, s60, 1\n\t" "s_cmp_ge_u32 s61, s67\n\t" \
    "s_cbranch_scc1 22f\n\t" "21:\n\t" "buffer_load_dword v120, %[fo], %[rc], %[fv] offen sc1\n\t" "buffer_load_dword v121, %[fo], %[rc], %[vd] offen sc1\n\t" \
    "s_waitcnt vmcnt(0)\n\t" "v_readfirstlane_b32 s61, v120\n\t" "v_readfirstlane_b32 s66, v121\n\t" "s_nop 3\n\t" "s_cmp_ge_u32 s61, s67\n\t" "s_cbranch_scc1 22f\n\t" \
    "s_bitcmp1_b32 s66, 1\n\t" "s_cbranch_scc1 90f\n\t" "s_cmp_eq_u32 %[bd], 0\n\t" "s_cbranch_scc1 22f\n\t" "s_sub_u32 %[bd], %[bd], 1\n\t" "s_sleep 2\n\t" \
    "s_branch 21b\n\t" "22:\n\t" "s_lshr_b32 s66, s60, 2\n\t" "s_xor_b32 s66, s66, %[xp]\n\t" "s_and_b32 s66, s66, 1\n\t" "s_lshl_b32 s66, s66, 13\n\t" \
    "s_lshl_b32 s64, s60, 19\n\t" "s_add_u32 s64, s64, s66\n\t" "s_add_u32 s64, s64, %[vb]\n\t" "buffer_load_dwordx4 v[48:51], %[ao], %[rs], s64 offen offset:0 sc1\n\t" \
    "buffer_load_dwordx4 v[52:55], %[ao], %[rs], s64 offen offset:256 sc1\n\t" "buffer_load_dwordx4 v[56:59], %[ao], %[rs], s64 offen offset:2048 sc1\n\t" \
    "buffer_load_dwordx4 v[60:63], %[ao], %[rs], s64 offen offset:2304 sc1\n\t" "buffer_load_dwordx4 v[64:67], %[a1], %[rs], s64 offen offset:0 sc1\n\t" \
    "buffer_load_dwordx4 v[68:71], %[a1], %[rs], s64 offen offset:256 sc1\n\t" "buffer_load_dwordx4 v[72:75], %[a1], %[rs], s64 offen offset:2048 sc1\n\t" \
    "buffer_load_dwordx4 v[76:79], %[a1], %[rs], s64 offen offset:2304 sc1\n\t" "s_waitcnt vmcnt(0)\n\t" "s_branch 23b\n\t" "60:\n\t" "s_sub_u32 s67, s60, %[rg]\n\t" \
    "s_cmp_ge_u32 s62, s67\n\t" "s_cbranch_scc1 62f\n\t" "61:\n\t" "buffer_load_dword v120, %[fo], %[rc], %[fs] offen sc1\n\t" \
    "buffer_load_dword v121, %[fo], %[rc], %[vd] offen sc1\n\t" "s_waitcnt vmcnt(0)\n\t" "v_readfirstlane_b32 s62, v120\n\t" "v_readfirstlane_b32 s66, v121\n\t" "s_nop 3\n\t" \
    "s_cmp_ge_u32 s62, s67\n\t" "s_cbranch_scc1 62f\n\t" "s_bitcmp1_b32 s66, 1\n\t" "s_cbranch_scc1 90f\n\t" "s_cmp_eq_u32 %[bd], 0\n\t" "s_cbranch_scc1 62f\n\t" \
    "s_sub_u32 %[bd], %[bd], 1\n\t" "s_sleep 2\n\t" "s_branch 61b\n\t" "62:\n\t" "s_branch 63b\n\t" "30:\n\t" "s_nop 7\n\t" "v_cvt_pk_bf16_f32 v112, v96, v97\n\t" \
    "v_cvt_pk_bf16_f32 v113, v98, v99\n\t" "v_cvt_pk_bf16_f32 v114, v100, v101\n\t" "v_cvt_pk_bf16_f32 v115, v102, v103\n\t" "v_cvt_pk_bf16_f32 v116, v104, v105\n\t" \
    "v_cvt_pk_bf16_f32 v117, v106, v107\n\t" "v_cvt_pk_bf16_f32 v118, v108, v109\n\t" "v_cvt_pk_bf16_f32 v119, v110, v111\n\t" \
    "buffer_store_dwordx4 v[112:115], %[z1], %[rs], s63 offen sc1\n\t" "buffer_store_dwordx4 v[116:119], %[z2], %[rs], s63 offen sc1\n\t" \
    "buffer_load_dword v120, %[fo], %[rc], %[fv] offen sc1\n\t" "s_waitcnt vmcnt(0)\n\t" "v_readfirstlane_b32 s61, v120\n\t" "s_lshr_b32 s66, s60, 2\n\t" \
    "s_xor_b32 s66, s66, %[xp]\n\t" "s_and_b32 s66, s66, 1\n\t" "s_lshl_b32 s66, s66, 13\n\t" "s_lshl_b32 s64, s60, 19\n\t" "s_add_u32 s64, s64, s66\n\t" \
    "s_add_u32 s64, s64, %[vb]\n\t" "s_cmp_gt_u32 s61, s60\n\t" "s_cbranch_scc0 34f\n\t" "s_cmp_eq_u32 s60, 64\n\t" "s_cbranch_scc1 34f\n\t" \
    "buffer_load_dwordx4 v[16:19], %[ao], %[rs], s64 offen offset:0 sc1\n\t" "buffer_load_dwordx4 v[20:23], %[ao], %[rs], s64 offen offset:256 sc1\n\t" \
    "buffer_load_dwordx4 v[24:27], %[ao], %[rs], s64 offen offset:2048 sc1\n\t" "buffer_load_dwordx4 v[28:31], %[ao], %[rs], s64 offen offset:2304 sc1\n\t" \
    "buffer_load_dwordx4 v[32:35], %[a1], %[rs], s64 offen offset:0 sc1\n\t" "buffer_load_dwordx4 v[36:39], %[a1], %[rs], s64 offen offset:256 sc1\n\t" \
    "buffer_load_dwordx4 v[40:43], %[a1], %[rs], s64 offen offset:2048 sc1\n\t" "buffer_load_dwordx4 v[44:47], %[a1], %[rs], s64 offen offset:2304 sc1\n\t" \
    "v_mov_b32 v122, s60\n\t" "buffer_store_dword v122, %[fo], %[rc], %[fz] offen sc1\n\t" "s_waitcnt vmcnt(1)\n\t" "s_branch 33b\n\t" "34:\n\t" "v_mov_b32 v122, s60\n\t" \
    "buffer_store_dword v122, %[fo], %[rc], %[fz] offen sc1\n\t" "s_cmp_eq_u32 s60, 64\n\t" "s_cbranch_scc1 90f\n\t" "s_add_u32 s67, s60, 1\n\t" "s_cmp_ge_u32 s61, s67\n\t" \
    "s_cbranch_scc1 32f\n\t" "31:\n\t" "buffer_load_dword v120, %[fo], %[rc], %[fv] offen sc1\n\t" "buffer_load_dword v121, %[fo], %[rc], %[vd] offen sc1\n\t" \
    "s_waitcnt vmcnt(0)\n\t" "v_readfirstlane_b32 s61, v120\n\t" "v_readfirstlane_b32 s66, v121\n\t" "s_nop 3\n\t" "s_cmp_ge_u32 s61, s67\n\t" "s_cbranch_scc1 32f\n\t" \
    "s_bitcmp1_b32 s66, 1\n\t" "s_cbranch_scc1 90f\n\t" "s_cmp_eq_u32 %[bd], 0\n\t" "s_cbranch_scc1 32f\n\t" "s_sub_u32 %[bd], %[bd], 1\n\t" "s_sleep 2\n\t" \
    "s_branch 31b\n\t" "32:\n\t" "s_lshr_b32 s66, s60, 2\n\t" "s_xor_b32 s66, s66, %[xp]\n\t" "s_and_b32 s66, s66, 1\n\t" "s_lshl_b32 s66, s66, 13\n\t" \
    "s_lshl_b32 s64, s60, 19\n\t" "s_add_u32 s64, s64, s66\n\t" "s_add_u32 s64, s64, %[vb]\n\t" "buffer_load_dwordx4 v[16:19], %[ao], %[rs], s64 offen offset:0 sc1\n\t" \
    "buffer_load_dwordx4 v[20:23], %[ao], %[rs], s64 offen offset:256 sc1\n\t" "buffer_load_dwordx4 v[24:27], %[ao], %[rs], s64 offen offset:2048 sc1\n\t" \
    "buffer_load_dwordx4 v[28:31], %[ao], %[rs], s64 offen offset:2304 sc1\n\t" "buffer_load_dwordx4 v[32:35], %[a1], %[rs], s64 offen offset:0 sc1\n\t" \
    "buffer_load_dwordx4 v[36:39], %[a1], %[rs], s64 offen offset:256 sc1\n\t" "buffer_load_dwordx4 v[40:43], %[a1], %[rs], s64 offen offset:2048 sc1\n\t" \
    "buffer_load_dwordx4 v[44:47], %[a1], %[rs], s64 offen offset:2304 sc1\n\t" "s_waitcnt vmcnt(0)\n\t" "s_branch 33b\n\t" "70:\n\t" "s_sub_u32 s67, s60, %[rg]\n\t" \
    "s_cmp_ge_u32 s62, s67\n\t" "s_cbranch_scc1 72f\n\t" "71:\n\t" "buffer_load_dword v120, %[fo], %[rc], %[fs] offen sc1\n\t" \
    "buffer_load_dword v121, %[fo], %[rc], %[vd] offen sc1\n\t" "s_waitcnt vmcnt(0)\n\t" "v_readfirstlane_b32 s62, v120\n\t" "v_readfirstlane_b32 s66, v121\n\t" "s_nop 3\n\t" \
    "s_cmp_ge_u32 s62, s67\n\t" "s_cbranch_scc1 72f\n\t" "s_bitcmp1_b32 s66, 1\n\t" "s_cbranch_scc1 90f\n\t" "s_cmp_eq_u32 %[bd], 0\n\t" "s_cbranch_scc1 72f\n\t" \
    "s_sub_u32 %[bd], %[bd], 1\n\t" "s_sleep 2\n\t" "s_branch 71b\n\t" "72:\n\t" "s_branch 73b\n\t" "90:\n\t"

#define T_DQ 1380
#define T_DV 1290
#define T_RQ 670
#define T_RV 640
#define T_M1 105
#define T_DY 580
#define T_DX 1250
#define T_MIN 35
#define T_PEN 280
#define T_SAFE 80
#define T_RETRY 700
#define T_POLL 1
#define T_KS 0
#define T_TS 0
#define T_CLR 210
#define T_EASE 1200
#define T_REF 1190
#define T_BETA 4
#define T_VLAG 150
#define T_U0 340
constexpr int T_OVH = 118, T_NEAR = 460;
constexpr unsigned NS(int ns) { return (unsigned)(ns * 64 / 10); }
struct Clk { unsigned cyc, g, pc, pg, k1, k2; };
DEV void mark(Clk& k) {
  unsigned long long c, r[8];
  asm volatile("s_memtime %0\n\ts_memrealtime %1\n\ts_nop 2\n\ts_memrealtime %2\n\ts_nop 2\n\ts_memrealtime %3\n\ts_nop 2\n\ts_memrealtime %4\n\ts_nop 2\n\t"
               "s_memrealtime %5\n\ts_nop 2\n\ts_memrealtime %6\n\ts_nop 2\n\ts_memrealtime %7\n\ts_waitcnt lgkmcnt(0)"
               : "=s"(c), "=s"(r[0]), "=s"(r[1]), "=s"(r[2]), "=s"(r[3]), "=s"(r[4]), "=s"(r[5]), "=s"(r[6]) :: "memory");
  const unsigned a = (unsigned)r[0];
  unsigned n = 0;
#pragma unroll
  for (int i = 1; i < 7; ++i) asm("s_cmp_eq_u32 %1, %2\n\ts_addc_u32 %0, %0, 0" : "+s"(n) : "s"((unsigned)r[i]), "s"(a) : "scc");
  k.g = (a + 4u) << 6; k.cyc = (unsigned)c + 12u + 16u * n;
}
DEV void anchor(Clk& k, bool any = false) {
  mark(k);
  const unsigned dg_ = k.g - k.pg;
  const float dc = (float)(k.cyc - k.pc), dg = (float)dg_;
  unsigned k1, k2;
  asm volatile("s_nop 0\n\tv_readfirstlane_b32 %0, %2\n\tv_readfirstlane_b32 %1, %3\n\ts_nop 3\n\ts_min_u32 %0, %0, 0x80000\n\ts_min_u32 %1, %1, 0x8000"
               : "=&s"(k1), "=&s"(k2) : "v"((unsigned)(dg * __builtin_amdgcn_rcpf(dc) * 65536.f)), "v"((unsigned)(dc * __builtin_amdgcn_rcpf(dg) * 65536.f)) : "scc");
  if (any) { k.k1 = k1; k.k2 = k2; }
  else asm("s_cmp_lt_u32 %4, %5\n\ts_cselect_b32 %2, %2, %0\n\ts_cselect_b32 %3, %3, %1\n\ts_add_u32 %0, %0, %2\n\ts_lshr_b32 %0, %0, 1\n\ts_add_u32 %1, %1, %3\n\ts_lshr_b32 %1, %1, 1"
           : "+s"(k.k1), "+s"(k.k2), "+s"(k1), "+s"(k2) : "s"(dg_ - NS(1400)), "n"(NS(800)) : "scc");
  k.pc = k.cyc; k.pg = k.g;
}
DEV unsigned to_cyc(const Clk& k, unsigned g) {
  unsigned d;
  asm("s_max_i32 %0, %1, 0xffff8000\n\ts_min_i32 %0, %0, 0x8000\n\ts_mul_i32 %0, %0, %2\n\ts_ashr_i32 %0, %0, 16" : "=&s"(d) : "s"(g - k.g), "s"(k.k2) : "scc");
  return k.cyc + d - T_OVH;
}
DEV void wait_cyc(unsigned t) { asm volatile(TIMED_WAIT :: "s"(t) : CL_S); }
DEV void close_in(Clk& k, unsigned t) {
  if ((int)(t - (unsigned)__builtin_amdgcn_s_memtime()) > T_NEAR + 60) { wait_cyc(t - T_NEAR); mark(k); }
}
DEV int slow_line(const u16* q, int h, int j, int lane) { return (j * 32 + (lane >> 1)) * 8192 + (h >> 3) * 2048 + (lane & 1) * 1024 + ((3 - (int)((size_t)q >> 7)) & 7) * 128; }
DEV int turn(int h, int w) { return ((h >> 3) ^ (w & 1)) & 3; }
DEV void hold(const Ctx& c, int ticks) {
#pragma unroll 1
  while ((unsigned)__builtin_amdgcn_s_memrealtime() - c.t0 < (unsigned)ticks) __builtin_amdgcn_s_sleep(4);
}
DEV int z_mix(int y) { return (y >> 4) | ((y & 1) << 2); }
DEV int u_slot(int s, int para) { return R_U + (s & (URING == 64 ? 127 : URING - 1)) * MB + ((para ^ (s >> 1)) & 1) * 8192; }
DEV unsigned ask_stamps(Ctx& c, int par, int slot) { return __builtin_amdgcn_raw_buffer_load_b32(c.rs, amap(24576, par) + fresh_lane() * 128, slot, AUX_SC1); }
DEV unsigned latest(Ctx& c, const Clk& k, int par, int slot, unsigned v) {
  if (__builtin_expect(!allok(v), 0)) {
#pragma unroll 1
    do {
      __builtin_amdgcn_s_sleep(T_POLL);
      asm volatile("" ::: "memory");
      const unsigned t = __builtin_amdgcn_raw_buffer_load_b32(c.rs, v == SENT ? amap(24576, par) + fresh_lane() * 128 : 1 << 30, slot, AUX_SC1);
      v = v == SENT ? t : v;
    } while (!allok(v) && !spend(c));
  }
  int d = (int)(v - k.g);
  d = max(d, __builtin_amdgcn_update_dpp(d, d, 0x128, 0xf, 0xf, true));
  d = max(d, __builtin_amdgcn_update_dpp(d, d, 0x124, 0xf, 0xf, true));
  d = max(d, __builtin_amdgcn_update_dpp(d, d, 0x122, 0xf, 0xf, true));
  d = max(d, __builtin_amdgcn_update_dpp(d, d, 0x121, 0xf, 0xf, true));
  unsigned m;
  asm("s_max_i32 %0, %1, %2\n\ts_max_i32 %0, %0, %3\n\ts_max_i32 %0, %0, %4" : "=&s"(m)
      : "s"(__builtin_amdgcn_readlane(d, 0)), "s"(__builtin_amdgcn_readlane(d, 16)), "s"(__builtin_amdgcn_readlane(d, 32)), "s"(__builtin_amdgcn_readlane(d, 48)) : "scc");
  return k.g + m;
}
struct Plan { unsigned gx, dx, ease; };
DEV unsigned smax(unsigned a, unsigned b) { return (int)(a - b) > 0 ? a : b; }
DEV unsigned pick(int d, unsigned a, unsigned b) { unsigned r; asm("s_cmp_gt_i32 %1, 0\n\ts_cselect_b32 %0, %2, %3" : "=s"(r) : "s"(d), "s"(a), "s"(b) : "scc"); return r; }
DEV unsigned when_p(const Plan& t, unsigned last, unsigned more = 0) {
  const unsigned gy = t.gx + t.dx + NS(T_M1), a = last + NS(T_PEN), b = gy + NS(T_RETRY);
  return pick((int)(last + NS(T_MIN) - gy), pick((int)(a - b), a, b) + more, gy);
}
DEV void advance(Plan& t, unsigned last) {
  const int over = (int)(last - t.gx - t.dx + t.ease);
  t.gx = when_p(t, last, NS(T_SAFE)) + NS(T_DY) + (unsigned)((int)(t.dx - t.ease - NS(T_REF)) * T_BETA >> 3);
  t.dx -= t.ease - (t.ease >> 1); t.ease >>= 1;
  t.dx += pick(over - (int)NS(80), NS(4), pick(over, (unsigned)(over >> 1), (unsigned)(over >> 3)));
}
DEV unsigned next_go(Ctx& c, Clk& k, Plan& t, int par, int slot, unsigned v) {
  SCHED();
  anchor(k);
  SCHED();
  advance(t, latest(c, k, par, slot, v));
  return to_cyc(k, t.gx);
}

DEV constexpr int qk_r(int j) { return j < 4 ? 0 : 1 + (j - 4) % 3; }
DEV constexpr int qk_i(int j) { return j < 4 ? j : (j - 4) / 3; }
DEV constexpr int qk_upto(int u) { return K_FIRST && u >= 3 ? (u + 3 < 16 ? u + 3 : 16) : (u + 4 < 16 ? u + 4 : 16); }
DEV void qk_role(const Params& p, Ctx& c, int h) {
  constexpr int WL = 0, MEMS = 131072, KXL = 147456, QKX = 155648, QROW = 272;
  c.budget = SPIN_CAP;
  const int w = c.w;
  Clk k = {0u, 0u, 0u, 0u, 0u, 0u};
  anchor(k, true);
  v8u wa[32], wh[16];
  unsigned look = claim_look(c);
  bool claimed = false;
  hold(c, QK_AT);
  {
    const int lane = fresh_lane(), g = lane >> 4, cc = lane & 15;
    const rsrc_t rq = mkrs(p.Wq, 1024 * 4096 * 2), rk = mkrs(p.Wk, 1024 * 4096 * 2);
    int vo[4];
#pragma unroll
    for (int i = 0; i < 4; ++i) vo[i] = opq((256 * w + 64 * (i ^ turn(h, w)) + krow_lane(g)) * 8192 + (h * 128 + 8 * cc) * 2);
    v4u row[16][8];
    auto request = [&](int u) {
#pragma unroll
      for (int r = 0; r < 8; ++r) row[u][r] = ld16w<W_AUX>((u < 8) != K_FIRST ? rq : rk, vo[(u >> 1) & 3], krow(4 * (u & 1) + (r >> 1), r & 1) * 8192);
    };
#pragma unroll
    for (int u = 0; u < qk_upto(-1); ++u) { request(u); if (u == 0) { SCHED(); ask(c, look); SCHED(); } }
    SCHED();
    const int sw = MEMS + w * 8192 + g * 256, lw = opq(WL + w * 32768 + lane * 16);
    int sr[2];
#pragma unroll
    for (int t = 0; t < 2; ++t) { const int i = (2 * cc + t) & 7; sr[t] = opq(i * 1024 + g * 256 + ((4 * w + (cc >> 2) + 2 * i) & 15) * 16); }
    sfor<16>([&](auto U) __attribute__((always_inline)) {
      constexpr int u = U.value;
#pragma unroll
      for (int i = 0; i < 8; ++i) {
        v4u f;
#pragma unroll
        for (int r = 0; r < 4; ++r) f[r] = prm(row[u][2 * r + 1][i >> 1], row[u][2 * r][i >> 1], i & 1);
        sts16(c.L, sw + i * 1024 + ((cc + 2 * i) & 15) * 16, f);
      }
      SCHED();
      if ((u == 5 || u == 10) && !claimed) claimed = claim_try(c, look);
      if (u == 2 || u == 7) look = claim_look(c);
      if (qk_upto(u) > qk_upto(u - 1)) request(qk_upto(u - 1));
      if (u == 15 && !claimed) claim(c, 0);
      BAR();
      v4u in[4][2];
#pragma unroll
      for (int t = 1; t >= 0; --t)
#pragma unroll
        for (int r = 0; r < 4; ++r) in[r][t] = lds16(c.L, sr[t] + MEMS + ((w + r) & 3) * 8192);
      SCHED();
#pragma unroll
      for (int j = 0; j < 16; ++j) {
        if (qk_i(j) != ((u >> 1) & 3)) continue;
#pragma unroll
        for (int t = 1; t >= 0; --t) {
          const v4u o = in[qk_r(j)][t];
          constexpr bool isq = (u < 8) != K_FIRST;
          if (!isq && t == 1) sts16(c.L, lw + j * 2048 + (u & 1) * 1024, o);
          else {
            v8u& d = isq ? wa[2 * j + t] : wh[j];
#pragma unroll
            for (int r = 0; r < 4; ++r) d[4 * (u & 1) + r] = o[r];
#define M_(r) asm volatile("" : "+" r(d))
            if (u & 1) { if (isq) AT_ACC(M_, 2 * j + t); else AT_HOME(M_, j); }
#undef M_
          }
        }
      }
      BAR();
    });
  }
  STAMP(0);
  BAR();
  const unsigned vote = wait_verdict(c);
  const int par = vote_b(vote), para = vote_a(vote);
  const int lane = fresh_lane(), g = lane >> 4, cc = lane & 15;
  const int moff = opq(amap(w * 4096 + lane * 16, par) + (turn(h, w) & 1) * 1024), m2 = R_MEM + (turn(h, w) & 2) * 1024;
  unsigned tries = 1u << 12;
  *(volatile LAS unsigned*)(c.L + 160512) = 0u;
  LGKM(0);
  const int idx = opq((lane & 1) ? 0xEEEE : 0x4444);
  TICK_DECL;
  const int l0 = opq(WL + w * 32768 + lane * 16), l1 = opq(MEMS + lane * 16);
  const int qw = opq(QKX + (2 * w + (cc >> 3)) * 256 + g * 64 + (cc & 1) * 16 + ((cc >> 1) & 3) * 4), kw = opq(QKX + (8 + 2 * g) * QROW + (32 * w + 2 * cc) * 2);
#define QK_PINS PIN_ACC0(wa); PIN_ACC1(wa); PIN_HOME(wh)
#define QK_IN [l0] "v"(l0), [l1] "v"(l1), [mo] "v"(moff), [ix] "v"(idx), [qw] "v"(qw), [kw] "v"(kw), [rs] "s"(c.rs), [sb] "s"(m2 + (s & 3) * MEM_SLOT)
#define QK_CL CL_S, CL_16_32, CL_160_200, CL_200_216, CL_216_256
  if (w) RAWBAR();
  if (w == 3) {
    const int hq = opq(lane ? 1 << 30 : amap(24576 + h * 128, par));
    anchor(k, true);
    unsigned tg = k.cyc;
    Plan tt = {k.g - NS(100000), NS(T_DX + T_EASE), NS(T_EASE)};
    for (int s = 0; s < NSEG; ++s) {
      const int pb = R_P + (s & 3) * P_SLOT;
      QK_PINS;
      asm volatile(QK_STEP3 : [bd] "+s"(c.budget), [rt] "+s"(tries) : QK_IN, [hq] "v"(hq), [pb] "s"(pb), [tg] "s"(tg), [ca] "s"(k.cyc - T_DQ), [k1] "s"(k.k1), [ga] "s"(k.g), [dr] "n"(T_RQ), [pr] "s"(R_P + ((s + 1) & 3) * P_SLOT) : QK_CL);
      QK_PINS;
      LOG(1, s, tg);
      const unsigned sv = ask_stamps(c, par, pb);
      SCHED();
      anchor(k);
      SCHED();
      advance(tt, latest(c, k, par, pb, sv));
      tg = to_cyc(k, tt.gx);
      RAWBAR();
      LOG(2, s, k.cyc); LOG(3, s, k.g); LOG(4, s, tt.dx);
      st4s(mkrs((const void*)c.ctrl, CTRL_BYTES), hq & (1 << 30), C_FLAG + (F_S + h) * 128, (unsigned)s + 1u);
      TICK_SUM(10);
      close_in(k, tg);
      tg = to_cyc(k, tt.gx);
    }
  } else {
    const int kx0 = opq(KXL + g * 512 + cc * 16), kmrd = opq(QKX + g * 16 + (8 + (cc & 7)) * QROW);
    unsigned seen = NOFLAG;
    if (w == 0) {
      const int prow = (2 * g + (cc & 1)) * 2 + ((cc >> 1) & 1);
      const int px = opq(amap((h >> 1) * 1024 + ((2 * (h & 1) + (cc >> 3)) * 16 + prow) * 16 + ((cc >> 2) & 1) * 8, par));
      const int pm = opq(cc < 8 ? amap(16384 + h * 128 + (2 * g + (cc & 1)) * 16 + (cc & 2) * 4 + (cc & 4), par) : 1 << 30);
      const int ps = opq((cc & 1) ? 0x03020706 : 0x05040100), pa = opq(lane * 128);
      for (int s = 0; s < NSEG; ++s) {
        if (seen < (unsigned)s + 1u) seen = wait_flag(c, F_K + h, s + 1);
        QK_PINS;
        unsigned pd;
        asm volatile(QK_STEP0 : [bd] "+s"(c.budget), [rt] "+s"(tries), [sq] "+s"(seen), [pd] "=&v"(pd)
                     : QK_IN, [kx] "v"(kx0), [km] "v"(kmrd), [px] "v"(px), [pm] "v"(pm), [ps] "v"(ps), [pb] "s"(R_P + (s & 3) * P_SLOT), [pr] "s"(R_P + ((s + 1) & 3) * P_SLOT),
                       [kb] "s"(R_KX - KXL + xblk(s, h, para)), [fq] "s"(c.ctrl + (C_FLAG + (F_K + h) * 128) / 4),
                       [kn] "s"(R_KX + xblk(min(s + 1, NSEG - 1), h, para)), [pa] "v"(pa) : QK_CL);
        QK_PINS;
        LITE(s); LOGS(0, s);
        TICK_SUM(10);
      }
      RAWBAR();
    } else {
      const int qt = w - 1;
      const int px = opq(amap(qt ? 16384 + h * 1024 + lane * 16 : h * 512 + (g >> 1) * 256 + cc * 16 + (g & 1) * 8, para));
      const int pm = opq(g < 2 ? amap(49152 + (qt * 32 + h) * 256 + cc * 16 + g * 8, para) : 1 << 30);
      const int ps = opq(cc - 4 * g), hq = opq(g * 512 + (16 * qt + cc) * 16);
#define QK_TOK(STEP) for (int s = 0; s < NSEG; ++s) { QK_PINS; \
        asm volatile(STEP : [bd] "+s"(c.budget), [rt] "+s"(tries), [sq] "+s"(seen) \
                     : QK_IN, [kx] "v"(kx0), [km] "v"(kmrd), [px] "v"(px), [pm] "v"(pm), [ps] "v"(ps), [hq] "v"(hq), [qb] "s"(R_QX + xblk(s, h, para)), [tb] "s"(R_PT + (s & 7) * PT_SLOT), \
                       [tr] "s"(R_PT + ((s + 2) & 7) * PT_SLOT), [sn] "s"(s + 1), [fq] "s"(c.ctrl + (C_FLAG + (F_Q + h) * 128) / 4) : QK_CL); \
        QK_PINS; TICK_SUM(10); }
      if (w == 1) { QK_TOK(QK_STEP1) } else { QK_TOK(QK_STEP2) }
    }
  }
  TICK_OUT();
  if (c.budget == 0u) __hip_atomic_store(c.ctrl + C_TMO / 4, 1u, RLX_AGENT);
}

DEV void shuffle_cols(Ctx& c, int scratch, const v4u (&rows)[8], v4u (&o)[8]) {
  const int g = c.lane >> 4, cc = c.lane & 15;
#pragma unroll
  for (int i = 0; i < 8; ++i) {
    v4u f;
#pragma unroll
    for (int r = 0; r < 4; ++r) f[r] = prm(rows[2 * r + 1][i >> 1], rows[2 * r][i >> 1], i & 1);
    sts16(c.L, scratch + (8 * cc + i) * 64 + ((g ^ (cc & 3)) << 4), f);
  }
  asm volatile("s_waitcnt lgkmcnt(0)" ::: "memory");
#pragma unroll
  for (int i = 0; i < 8; ++i) { const int col = 16 * i + cc; o[i] = lds16(c.L, scratch + col * 64 + ((g ^ ((col >> 3) & 3)) << 4)); }
  asm volatile("s_waitcnt lgkmcnt(0)" ::: "memory");
}

DEV void vo_role(const Params& p, Ctx& c, int h) {
  constexpr int WL = 0, MEMS = 131072, VM = 147456, VROW = 272;
  c.budget = SPIN_CAP;
  const int w = c.w, lane = c.lane = fresh_lane(), g = lane >> 4, cc = lane & 15;
  Clk k = {0u, 0u, 0u, 0u, 0u, 0u};
  anchor(k, true);
  v8u wa[32], wh[16];
  ask(c, claim_look(c));
  unsigned look = 0;
  bool claimed = false;
  hold(c, VO_AT);
  const rsrc_t rwo = mkrs(p.Wo, 4096 * 1024 * 2);
  const int voo = (h * 128 + 4 * g) * 2048 + (256 * ((cc >> 3) ^ (h >> 3)) + 64 * w + 8 * (cc & 7)) * 2;
  v4u buf[3][8];
  auto request_o = [&](int u) {
#pragma unroll
    for (int r = 0; r < 8; ++r) buf[u % 3][r] = ld16w<W_AUX>(rwo, voo ^ ((u >> 2) * 1024), (32 * (u & 3) + 16 * (r >> 2) + (r & 3)) * 2048);
  };
  {
    const rsrc_t rw = mkrs(p.Wv, 1024 * 4096 * 2);
    const int vo = (256 * w + krow_lane(g)) * 8192 + (h * 128 + 8 * cc) * 2;
    v4u vrow[8][8];
    auto request = [&](int u) {
#pragma unroll
      for (int r = 0; r < 8; ++r) vrow[u][r] = ld16w<W_AUX>(rw, vo, (64 * ((u >> 1) ^ turn(h, w)) + krow(4 * (u & 1) + (r >> 1), r & 1)) * 8192);
    };
#pragma unroll
    for (int u = 0; u < 3; ++u) request(u);
    SCHED();
    const int wr_ = MEMS + w * 8192 + g * 256;
    int sr[2];
#pragma unroll
    for (int t = 0; t < 2; ++t) { const int i = (2 * cc + t) & 7; sr[t] = opq(i * 1024 + g * 256 + ((4 * w + (cc >> 2) + 2 * i) & 15) * 16); }
#pragma unroll
    for (int u = 0; u < 8; ++u) {
#pragma unroll
      for (int i = 0; i < 8; ++i) {
        v4u f;
#pragma unroll
        for (int r = 0; r < 4; ++r) f[r] = prm(vrow[u][2 * r + 1][i >> 1], vrow[u][2 * r][i >> 1], i & 1);
        sts16(c.L, wr_ + i * 1024 + ((cc + 2 * i) & 15) * 16, f);
      }
      SCHED();
      if (u == 7) claimed = claim_try(c, look);
      if (u == 4) look = claim_look(c);
      if (u + 3 < 8) request(u + 3); else request_o(u - 5);
      BAR();
      v4u in[4][2];
#pragma unroll
      for (int t = 1; t >= 0; --t)
#pragma unroll
        for (int r = 0; r < 4; ++r) in[r][t] = lds16(c.L, sr[t] + MEMS + ((w + r) & 3) * 8192);
      SCHED();
#pragma unroll
      for (int j = 0; j < 16; ++j) {
        if (qk_i(j) != (u >> 1)) continue;
#pragma unroll
        for (int t = 1; t >= 0; --t) {
          const v4u o = in[qk_r(j)][t];
          if (t == 1 && (j & 1)) sts16(c.L, WL + (w * 16 + (j >> 1)) * 2048 + (u & 1) * 1024 + lane * 16, o);
          else {
            const int n = 3 * (j >> 1) + ((j & 1) ? 2 : t);
#pragma unroll
            for (int r = 0; r < 4; ++r) wa[n][4 * (u & 1) + r] = o[r];
#define M_(r) asm volatile("" : "+" r(wa[n]))
            if (u & 1) AT_ACC(M_, n);
#undef M_
          }
        }
      }
      BAR();
    }
  }
#pragma unroll
  for (int u = 0; u < 8; ++u) {
    v4u o[8];
    shuffle_cols(c, MEMS + w * 8192, buf[u % 3], o);
    SCHED();
    if (u + 3 < 8) request_o(u + 3);
#pragma unroll
    for (int i = 0; i < 4; ++i) {
      const int pr = 4 * (u >> 2) + i, ch = u & 3;
      const v8u f = {o[2 * i][0], o[2 * i + 1][0], o[2 * i][1], o[2 * i + 1][1], o[2 * i][2], o[2 * i + 1][2], o[2 * i][3], o[2 * i + 1][3]};
      if (ch < 2) {
        wh[2 * pr + ch] = f;
#define M_(r) asm volatile("" : "+" r(wh[2 * pr + ch]))
        AT_HOME(M_, 2 * pr + ch);
#undef M_
      } else if (ch == 2) {
        wa[24 + pr] = f;
#define M_(r) asm volatile("" : "+" r(wa[24 + pr]))
        AT_ACC(M_, 24 + pr);
#undef M_
      } else {
        sts16(c.L, WL + (w * 16 + 8 + pr) * 2048 + lane * 16, lo4(f));
        sts16(c.L, WL + (w * 16 + 8 + pr) * 2048 + 1024 + lane * 16, hi4(f));
      }
    }
    SCHED();
  }
  if (!claimed) claim(c, 0);
  BAR();
  STAMP(0);
  const unsigned vote = wait_verdict(c);
  const int par = vote_b(vote), para = vote_a(vote);
  const int moff = opq(amap(w * 4096 + lane * 16, par) + (turn(h, w) & 1) * 1024), m2 = R_MEM + (turn(h, w) & 2) * 1024;
  unsigned tries = 1u << 12;
  *(volatile LAS unsigned*)(c.L + 160512) = 0u;
  LGKM(0);
  const int idx = opq((lane & 1) ? 0xEEEE : 0x4444);
  const int u_off = opq((4 * w + g) * 16384 + (h ^ (8 * g)) * 256 + cc * 16);
  const int hq = opq((lane | w) ? 1 << 30 : amap(24576 + (32 + h) * 128, par));
  const int l0 = opq(WL + w * 32768 + lane * 16), l1 = opq(MEMS + lane * 16);
  const int vmw = opq(VM + 2 * g * VROW + (32 * w + 2 * cc) * 2);
  const int vmr = opq(VM + (cc >> 1) * VROW + g * 16);
  TICK_DECL;
  anchor(k, true);
  unsigned tg = k.cyc;
  Plan tt = {k.g - NS(100000), NS(T_DX + T_EASE), NS(T_EASE)};
  for (int s = 0; s < NSEG; ++s) {
    const int pb = R_P + (s & 3) * P_SLOT;
    PIN_ACC0(wa); PIN_ACC1(wa); PIN_HOME(wh);
    asm volatile(VO_STEP
        : [bd] "+s"(c.budget), [rt] "+s"(tries)
        : [l0] "v"(l0), [l1] "v"(l1), [mo] "v"(moff), [ix] "v"(idx), [vw] "v"(vmw), [vr] "v"(vmr), [uo] "v"(u_off), [w] "s"(w), [rs] "s"(c.rs),
          [sb] "s"(m2 + (s & 3) * MEM_SLOT), [ub] "s"(u_slot(s, para) + (h >> 3) * 16 * 16384), [ur] "s"(u_slot(s + 2, para) + (2 * h + (w >> 1)) * 16384 + (w & 1) * 4096 - MEMS), [tg] "s"(tg + T_VLAG), [hq] "v"(hq), [pb] "s"(pb),
          [ca] "s"(k.cyc - T_DV), [k1] "s"(k.k1), [ga] "s"(k.g), [dr] "n"(T_RV), [pr] "s"(R_P + ((s + 1) & 3) * P_SLOT)
        : CL_S, CL_16_32, CL_160_200, CL_200_216, CL_216_256);
    PIN_ACC0(wa); PIN_ACC1(wa); PIN_HOME(wh);
    if (w == 0) {
      LITE(s);
      LOG(1, s, tg);
      const unsigned sv = ask_stamps(c, par, pb);
      SCHED();
      anchor(k);
      const unsigned gc = tt.gx + tt.dx + NS(T_M1 + T_CLR);
      const bool ontime = (int)(gc - k.g) > 0;
      if (ontime) { wait_cyc(to_cyc(k, gc)); RAWBAR(); }
      SCHED();
      advance(tt, latest(c, k, par, pb, sv));
      if (!ontime) { wait_cyc(to_cyc(k, tt.gx - NS(T_DY + T_SAFE - T_CLR))); RAWBAR(); }
      tg = to_cyc(k, tt.gx);
      LOG(2, s, k.cyc); LOG(3, s, k.g);
    }
    TICK_SUM(10);
    if (w == 0) { close_in(k, tg); tg = to_cyc(k, tt.gx); }
  }
  TICK_OUT();
  if (c.budget == 0u) __hip_atomic_store(c.ctrl + C_TMO / 4, 1u, RLX_AGENT);
}

DEV void y_role(const Params& p, Ctx& c, int y) {
  constexpr int ZR = 0, UL = 131072, CW = 155648;
  c.budget = SPIN_CAP;
  const int w = c.w, lane = c.lane = fresh_lane(), g = lane >> 4, cc = lane & 15;
  const v4u sent4 = {SENT, SENT, SENT, SENT};
  STAMP(2);
  Clk k = {0u, 0u, 0u, 0u, 0u, 0u};
  anchor(k, true);
  unsigned m0 = 0;
  if (w == 0) {
    unsigned best[2] = {~0u, ~0u};
    const unsigned first = claim_look(c);
    st4s(c.rs, y * 16384 + (lane >> 5) * 8192 + (lane & 31) * 4, R_U, SENT);
    ask(c, first);
#pragma unroll 1
    for (int n = 0; n < 12; ++n) {
#pragma unroll
      for (int r = 0; r < 2; ++r) {
        VMWAIT(0);
        const unsigned t0 = (unsigned)__builtin_amdgcn_s_memtime();
        LGKM(0);
        st4s(c.rs, y * 16384 + (lane & 31) * 4, R_U + r * 8192, SENT);
        VMWAIT(0);
        const unsigned t1 = (unsigned)__builtin_amdgcn_s_memtime();
        LGKM(0);
        best[r] = min(best[r], t1 - t0);
      }
      if (n + 1 >= PROBES && min(best[0], best[1]) < 600u && max(best[0], best[1]) > min(best[0], best[1]) + 128u) break;
    }
    STAMP(5);
    claim(c, CL_VOTED | (best[1] < best[0] ? CL_A : 0u));
    m0 = *(const GAS unsigned*)(p.mem0 + (lane >> 3) * 1024 + 16 * y + 2 * (lane & 7));
    m0 = m0 < SENT - 1u ? m0 : SENT - 1u;
    if (lane < 8) *(LAS unsigned*)(c.L + CW + lane * 4) = 0u;
    const unsigned v = wait_vote(c);
    STAMP(7);
    const int oa = y * 16384 + vote_a(v) * 8192 + lane * 16, ob = amap(y * 3072 + lane * 16, vote_b(v));
#pragma unroll
    for (int i = 0; i < 16; ++i) st16s(c.rs, oa + (i & 3) * 1024, R_U + (i >> 3) * MB + ((i >> 2) & 1) * 4096, sent4);
#pragma unroll
    for (int i = 0; i < 3; ++i) st16s(c.rs, amap(y * 3072 + i * 1024 + lane * 16, vote_b(v)), R_MEM, sent4);
#pragma unroll
    for (int i = 0; i < 8; ++i) st16s(c.rs, oa + (i & 3) * 1024, R_PT + (i >> 2) * 4096, sent4);
    (void)ob;
    VMWAIT(0);
    if (lane == 0) __hip_atomic_fetch_add(c.ctrl + (C_CLAIM + c.att * 1024) / 4 + c.slot, CL_FILLED, RLX_AGENT);
    STAMP(6);
  } else {
    if (YH && w < 3 && (y & 8) == 0) {
      hold(c, YH_AT);
#pragma unroll
      for (int i = 0; i < 8; ++i) {
        const u16* q = (i < 4) != K_FIRST ? p.Wq : p.Wk;
        if (i == 4) hold(c, YH_AT + YH_STEP);
        m0 += __builtin_amdgcn_raw_buffer_load_b32(mkrs(q, 8 * MB), slow_line(q, y >> 1, (y & 7) + 8 * (i & 3), lane) ^ ((w - 1) * 128), 0, 0);
      }
      asm volatile("" :: "v"(m0));
    }
  }
  BAR();
  const int nb = (c.b - SERVER) >> 3;
  if (w == 3 ? (c.b == SERVER + 8 || c.b == SERVER + 16) : w && c.b != SERVER) {
    hold(c, X_AT);
#pragma unroll
    for (int i = 0; i < 4; ++i) m0 += __builtin_amdgcn_raw_buffer_load_b32(mkrs(p.x, 4 * MB), ((w == 3 ? 2 * y - nb - 1 : 2 * y + w - 1) * 64 + lane) * 128, i * MB, 0);
    asm volatile("" :: "v"(m0));
  }
  const unsigned vote = wait_verdict(c);
  const int par = vote_b(vote), para = vote_a(vote);
  const int mo = amap(y * 256 + lane * 4, par);
  if (w == 0) st4s(c.rs, mo, R_MEM, m0);
  STAMP(0);
  const int rot = y >> 4;
  volatile LAS unsigned* const GO = (volatile LAS unsigned*)(c.L + CW);
  TICK_DECL;
  if (w == 0) {
    int xo[4];
#pragma unroll
    for (int i = 0; i < 4; ++i) xo[i] = amap((i ^ rot) * 4096 + lane * 16, par);
    const int pmo = amap(16384 + rot * 1024 + (2 * g + (cc & 1)) * 128 + (cc >> 1) * 16, par), psel = (cc & 1) ? 0x03020706 : 0x05040100;
    constexpr int lead = T_U0, late = 0;
    anchor(k, true);
    Plan tt = {k.g - NS(100000), NS(T_DX + T_EASE), NS(T_EASE)};
    unsigned done = k.cyc;
    const int so = amap(24576, par) + lane * 128;
    const int idx = opq((lane & 1) ? 0xEEEE : 0x4444);
    const int uo = y * 16384 + lane * 16;
    const int mw = amap(y * 256 + (2 * g + (cc & 1)) * 32 + (cc >> 1) * 4, par);
    for (int s = 0; s < NSEG - 1; ++s) {
      if (s == 0) RAWBAR();
      const int za = ZR + (s & 3) * 32768 + rot * 8192 + lane * 16;
      v32u z0, z1, z2, z3;
      asm volatile(
          "ds_read_b128 a[0:3], %[z0] offset:0\n\t" "ds_read_b128 a[4:7], %[z0] offset:1024\n\t" "ds_read_b128 a[8:11], %[z0] offset:2048\n\t" "ds_read_b128 a[12:15], %[z0] offset:3072\n\t"
          "ds_read_b128 a[16:19], %[z0] offset:4096\n\t" "ds_read_b128 a[20:23], %[z0] offset:5120\n\t" "ds_read_b128 a[24:27], %[z0] offset:6144\n\t" "ds_read_b128 a[28:31], %[z0] offset:7168\n\t"
          "ds_read_b128 a[32:35], %[z1] offset:0\n\t" "ds_read_b128 a[36:39], %[z1] offset:1024\n\t" "ds_read_b128 a[40:43], %[z1] offset:2048\n\t" "ds_read_b128 a[44:47], %[z1] offset:3072\n\t"
          "ds_read_b128 a[48:51], %[z1] offset:4096\n\t" "ds_read_b128 a[52:55], %[z1] offset:5120\n\t" "ds_read_b128 a[56:59], %[z1] offset:6144\n\t" "ds_read_b128 a[60:63], %[z1] offset:7168\n\t"
          "ds_read_b128 a[64:67], %[z2] offset:0\n\t" "ds_read_b128 a[68:71], %[z2] offset:1024\n\t" "ds_read_b128 a[72:75], %[z2] offset:2048\n\t" "ds_read_b128 a[76:79], %[z2] offset:3072\n\t"
          "ds_read_b128 a[80:83], %[z2] offset:4096\n\t" "ds_read_b128 a[84:87], %[z2] offset:5120\n\t" "ds_read_b128 a[88:91], %[z2] offset:6144\n\t" "ds_read_b128 a[92:95], %[z2] offset:7168\n\t"
          "ds_read_b128 a[96:99], %[z3] offset:0\n\t" "ds_read_b128 a[100:103], %[z3] offset:1024\n\t" "ds_read_b128 a[104:107], %[z3] offset:2048\n\t" "ds_read_b128 a[108:111], %[z3] offset:3072\n\t"
          "ds_read_b128 a[112:115], %[z3] offset:4096\n\t" "ds_read_b128 a[116:119], %[z3] offset:5120\n\t" "ds_read_b128 a[120:123], %[z3] offset:6144\n\t" "ds_read_b128 a[124:127], %[z3] offset:7168\n\t"
          "s_waitcnt lgkmcnt(0)"
          : "={a[0:31]}"(z0), "={a[32:63]}"(z1), "={a[64:95]}"(z2), "={a[96:127]}"(z3) : [z0] "v"(za), [z1] "v"(za ^ 8192), [z2] "v"(za ^ 16384), [z3] "v"(za ^ 24576) : "memory");
      const int pb = R_P + (s & 3) * P_SLOT;
      v32u uu;
      const int ub = u_slot(s, para);
      TICK(0);
      anchor(k);
      TICK(8);
      unsigned gp = tt.gx + tt.dx + NS(T_M1), lt = late, sv = SENT;
      unsigned bad = (int)(done - to_cyc(k, tt.gx)) > T_OVH;
      TICK(4);
      LOG(2, s, k.cyc); LOG(3, s, k.g);
#pragma unroll 1
      for (;;) {
        if (bad) {
          const bool all = allok(sv);
          const unsigned last = latest(c, k, par, pb, sv);
          if (!all) mark(k);
          gp = when_p(tt, last);
          lt = 1;
        }
        close_in(k, to_cyc(k, gp) - (lt ? 96 : lead));
        const unsigned tg = to_cyc(k, gp);
        if (!bad) LOG(1, s, tg);
        asm volatile(Y_STEP
          : "=&{v[144:175]}"(uu), [bad] "=&s"(bad), [sv] "=&v"(sv)
          : [x0] "v"(xo[0]), [x1] "v"(xo[1]), [x2] "v"(xo[2]), [x3] "v"(xo[3]), [pm] "v"(pmo), [u0] "v"(uo + (rot ^ (y & 3)) * 2048), [mw] "v"(mw), [ps] "v"(psel), [ix] "v"(idx),
            [rs] "s"(c.rs), [pb] "s"(pb), [ub] "s"(ub), [mb] "s"(R_MEM + ((s + 1) & 3) * MEM_SLOT), [mr] "s"(R_MEM + ((s + 3) & 3) * MEM_SLOT), [tu] "s"(tg - (lt ? 96 : lead)), [tg] "s"(tg - 32), [late] "s"(lt),
            [so] "v"(so), [bd] "s"(c.budget),
            "{a[0:31]}"(z0), "{a[32:63]}"(z1), "{a[64:95]}"(z2), "{a[96:127]}"(z3)
          : CL_S, CL_120_128, CL_176_200, CL_200_216, CL_216_256);
        bad = __builtin_amdgcn_readfirstlane(bad);
        if (bad != 1u) break;
        spend(c);
      }
      done = bad;
      SCHED();
      const int ul = opq(UL + rot * 2048 + lane * 16);
#pragma unroll
      for (int i = 0; i < 8; ++i) sts16(c.L, (ul ^ ((i >> 1) * 2048)) + (i & 1) * 1024, v4u{uu[4 * i], uu[4 * i + 1], uu[4 * i + 2], uu[4 * i + 3]});
      asm volatile("" ::: "memory");
      *GO = s + 1;
      asm volatile("s_waitcnt vmcnt(2)" : "+v"(sv));
      advance(tt, latest(c, k, par, pb, sv));
      TICK(3);
      LITE(s); LOG(0, s, done);
      TICK_LDS(CW + 256);
    }
    {
      anchor(k);
      wait_cyc(to_cyc(k, tt.gx + tt.dx + NS(T_M1)));
      const int ub = u_slot(NSEG - 1, para);
      v4u u[8];
#pragma unroll 1
      for (;;) {
        asm volatile("" ::: "memory");
#pragma unroll
        for (int i = 0; i < 8; ++i) u[i] = ld16s(c.rs, uo + (i & 3) * 1024, ub + (i >> 2) * 4096);
        SCHED();
        unsigned t = 0;
#pragma unroll
        for (int i = 0; i < 8; ++i) t = umax(t, top(u[i]));
        if (allok(t) || spend(c)) break;
      }
      RAWBAR();
#pragma unroll
      for (int i = 0; i < 8; ++i) sts16(c.L, UL + (i ^ (2 * (y & 3))) * 1024 + lane * 16, u[i]);
      asm volatile("" ::: "memory");
      *GO = NSEG;
    }
    TICK_DUMP(CW + 256);
  } else if (w < 3) {
    const rsrc_t ro = mkrs(p.out, 2048 * 2048 * 2);
    auto tokens = [&](auto QT) {
      constexpr int qt = QT.value, NX = qt ? 32 : 16;
      RAWBAR();
      for (int s = 0; s < NSEG; ++s) {
        const int tb = R_PT + (s & 7) * PT_SLOT + para * 8192;
        int ua[4], za[4], xb[4], mb[4];
#pragma unroll
        for (int i = 0; i < 4; ++i) {
          ua[i] = opq(UL + (i ^ rot) * 2048 + lane * 16);
          za[i] = opq(ZR + (s & 3) * 32768 + (i ^ rot) * 8192 + (qt ? lane * 16 : (g >> 1) * 1024 + (g & 1) * 512 + cc * 16));
          xb[i] = tb + amap(qt * 16384 + (i ^ rot) * (qt ? 8192 : 4096), 0);
          mb[i] = tb + amap(49152 + qt * 8192, 0) + (i ^ rot) * 2048;
        }
        SCHED();
        RAWBAR();
        if (TOK_WAIT) __builtin_amdgcn_s_sleep(TOK_WAIT);
        TICK(0);
        f32x4 a;
#pragma unroll 1
        for (;;) {
          asm volatile("" ::: "memory");
          v4u pt[NX + 8];
#pragma unroll
          for (int i = 0; i < NX; ++i) pt[i] = ld16s(c.rs, (i & 3) * 1024 + lane * 16, qt ? xb[i >> 3] + ((i >> 2) & 1) * 4096 : xb[i >> 2]);
#pragma unroll
          for (int i = 0; i < 8; ++i) pt[NX + i] = ld16s(c.rs, (i & 1) * 1024 + lane * 16, mb[i >> 1]);
          SCHED();
          TICK(1);
#pragma unroll 1
          while (__builtin_amdgcn_readfirstlane(*GO) < (unsigned)s + 1u) { if (spend(c)) break; }
          asm volatile("" ::: "memory");
          f32x4 d[2] = {f32x4{0.f, 0.f, 0.f, 0.f}, f32x4{0.f, 0.f, 0.f, 0.f}};
          v4u fa[8];
          auto frag = [&](int i) {
            if (i >= NX) fa[i & 7] = lds16(c.L, ua[(i - NX) >> 1] + (i & 1) * 1024);
            else if (qt) fa[i & 7] = lds16(c.L, za[i >> 3] + (i & 7) * 1024);
            else {
              const v2u lo = *(LAS v2u*)(c.L + za[i >> 2] + (i & 3) * 2048), hi = *(LAS v2u*)(c.L + za[i >> 2] + (i & 3) * 2048 + 256);
              fa[i & 7] = v4u{lo.x, lo.y, hi.x, hi.y};
            }
          };
#pragma unroll
          for (int i = 0; i < 7; ++i) frag(i);
          SCHED();
#pragma unroll
          for (int i = 0; i < NX + 8; ++i) {
            if (i + 7 < NX + 8) frag(i + 7);
            d[i & 1] = mfma16(fa[i & 7], pt[i], d[i & 1]);
            SCHED();
          }
          a = d[0] + d[1];
          if (!__any(a[0] != a[0])) break;
          unsigned t = 0;
#pragma unroll
          for (int i = 0; i < NX + 8; ++i) t = umax(t, top(pt[i]));
          if (allok(t) || spend(c)) break;
        }
        __builtin_amdgcn_raw_buffer_store_b64(v2u{pk(a[0], a[1]), pk(a[2], a[3])}, ro, (16 * qt + cc) * 4096 + (16 * y + 4 * g) * 2, s * 32 * 4096, OUT_AUX);
        TICK(2);
        TICK_SUM(3);
      }
    };
    if (w == 1) tokens(IC<0>{}); else tokens(IC<1>{});
  } else {
    unsigned seenZ = NOFLAG;
    const rsrc_t rc = mkrs((const void*)c.ctrl, CTRL_BYTES);
    const int fo = lane < 32 ? lane * 128 : 1 << 30;
    __builtin_amdgcn_s_setprio(3);
    auto least = [&](unsigned v) {
      v |= lane < 32 ? 0u : ~0u;
#pragma unroll
      for (int o = 1; o < 32; o <<= 1) v = min(v, (unsigned)__shfl_xor((int)v, o));
      seenZ = umax(seenZ, __builtin_amdgcn_readfirstlane(v));
    };
    auto await = [&](unsigned need) {
#pragma unroll 1
      while (seenZ < need) {
        asm volatile("" ::: "memory");
        least(__builtin_amdgcn_raw_buffer_load_b32(rc, fo, C_FLAG + (F_Z + (y >> 4) * 32) * 128, AUX_SC1));
        if (seenZ >= need || spend(c)) break;
        __builtin_amdgcn_s_sleep(1);
      }
      __builtin_amdgcn_fence(__ATOMIC_ACQUIRE, "wavefront");
    };
    for (int n = 0; n < NSEG; ++n) {
      TICK(0);
      await(n + 1);
      if (Z_WAIT) __builtin_amdgcn_s_sleep(Z_WAIT);
      TICK(1);
      v4u z[32];
      int za[8];
#pragma unroll
      for (int i = 0; i < 8; ++i) za[i] = opq(ZR + (n & 3) * 32768 + (i ^ z_mix(y)) * 1024 + lane * 16);
      unsigned look;
#pragma unroll
      for (int i = 0; i < 32 + Z_WIN; ++i) {
        if (i < 32) z[i] = ld16z(c.rs, (i & 3) * 1024 + lane * 16, R_Z + (n & (ZRING - 1)) * Z_SLOT + y * 65536 + ((para ^ (y >> 5)) & 1) * 8192 + amap((i >> 2) * 4096, 0));
        if (i == 32) { look = __builtin_amdgcn_raw_buffer_load_b32(rc, seenZ < (unsigned)n + 2u ? fo : 1 << 30, C_FLAG + (F_Z + (y >> 4) * 32) * 128, AUX_SC1); TICK(4); }
        SCHED();
        if (i >= Z_WIN) sts16(c.L, za[(i - Z_WIN) & 7] + ((i - Z_WIN) >> 3) * 8192, z[i - Z_WIN]);
        else asm volatile("s_nop " STR(Z_GAP));
        if (i == 30 + Z_WIN) { TICK(3); }
        SCHED();
      }
      least(look);
      if (n == 0) await(PRIME);
      TICK(2);
      { const int s = n; (void)s; TICK_SUM(3); }
      BAR();
    }
    RAWBAR();
  }
  TICK_OUT();
  if (c.budget == 0u) __hip_atomic_store(c.ctrl + C_TMO / 4, 1u, RLX_AGENT);
}

#define MF1A(d, a, b) asm volatile(MF16 "%0, %1, %2, %0" : "+v"(d) : "a"(a), "v"(b))
#define MF32A(d, a, b) asm volatile("v_mfma_f32_32x32x16_bf16 %0, %1, %2, %0" : "+v"(d) : "a"(a), "v"(b))
#define MF32AZ(d, a, b) asm volatile("v_mfma_f32_32x32x16_bf16 %0, %1, %2, 0" : "=&v"(d) : "a"(a), "v"(b))
#define MF1B(d, a, b) asm volatile(MF16 "%0, %1, %2, %0" : "+v"(d) : "v"(a), "a"(b))
#define MF1BZ(d, a, b) asm volatile(MF16 "%0, %1, %2, 0" : "=&v"(d) : "v"(a), "a"(b))
#define MF1AZ(d, a, b) asm volatile(MF16 "%0, %1, %2, 0" : "=&v"(d) : "a"(a), "v"(b))
DEV void g_role(const Params& p, Ctx& c, int mat, int h, int gi) {
  constexpr int EX = 131072;
  c.budget = SPIN_CAP;
  const int w = c.w, lane = c.lane = fresh_lane(), g = lane >> 4, cc = lane & 15;
  unsigned look = claim_look(c);
  const int flip = FLIP && mat < 2 ? ((h >> 4) & 1) ^ 1 : 0;
  unsigned t[7];
  {
    auto pull = [&](const u16* q, int bytes, int v, int so) { return __builtin_amdgcn_raw_buffer_load_b32(mkrs(q, bytes), v, so, 0); };
    const int all = mat < HELPERS ? 8 * MB : 0;
    t[6] = pull(p.Wv, SLOW_V * all, slow_line(p.Wv, h, (h & 7) + 8 * w, lane) ^ (mat * 128), 0);
    t[0] = pull(p.x, all / 2, g * 65536 + (((h & 7) * 4 + w) * 16 + cc) * 128, mat * 4 * 65536);
#pragma unroll
    for (int i = 0; i < 3; ++i) t[1 + i] = pull(p.Wv, HELP_V * all, (256 * w + 4 * (h & 7) + g) * 8192 + (h >> 3) * 2048 + cc * 128, (2 + 2 * i + mat) * 32 * 8192);
    SCHED();
    if (mat < 2) ask(c, look);
    SCHED();
#pragma unroll
    for (int i = 0; i < 2; ++i) t[4 + i] = pull(p.Wo, HELP_O * all, (h * 128 + (lane >> 1)) * 2048 + w * 512 + 256 + (lane & 1) * 128, (2 * i + mat) * 32 * 2048);
    SCHED();
    if (mat == 0) claim(c, 0);
    hold(c, mat == 0 ? GQ_AT : mat == 1 ? GK_AT : 0);
  }
  const rsrc_t rx = mkrs(p.x, 2048 * 2048);
  const int xs = w * 16384 + ((lane * 16) ^ ((w & 1) << 7));

  v32u wa[8];
  {
    const rsrc_t rw = mkrs(mat == 0 ? p.Wq : mat == 1 ? p.Wk : p.Wv, 1024 * 4096 * 2);
    const int vo0 = opq((256 * w + 128 * flip + 8 * g) * 8192 + (h * 128 + 8 * cc) * 2), vo1 = opq(vo0 ^ (128 * 8192));
    v4u row[8][8];
    auto request = [&](int u) {
#pragma unroll
      for (int r = 0; r < 8; ++r) {
        row[u][r] = ld16w<0>(rw, (u & 4) ? vo1 : vo0, (32 * (u & 3) + r) * 8192);
        if ((r & 3) == 3) { const int n = 2 * u + (r >> 2); dma16(rx, c.L + w * 16384 + n * 1024, (xs ^ ((n >> 1) << 4)) + n * 1024); }
      }
    };
    const unsigned look0 = claim_look(c);
    bool claimed = false;
    request(0);
    SCHED();
    if (mat == 2) ask(c, look);
    SCHED();
    request(1); request(2);
    if (G_DEEP) request(3);
    SCHED();
    const int sw_ = EX + w * 8192 + g * 256;
    const int sr0 = opq(EX + (cc & 3) * 1024 + g * 256 + ((4 * w + (cc >> 2) + 4 * cc) & 15) * 16), sr1 = sr0 + 4096;
#pragma unroll
    for (int u = 0; u < 8; ++u) {
#pragma unroll
      for (int i = 0; i < 8; ++i) {
        v4u f;
#pragma unroll
        for (int r = 0; r < 4; ++r) f[r] = prm(row[u][2 * r + 1][i >> 1], row[u][2 * r][i >> 1], i & 1);
        sts16(c.L, sw_ + i * 1024 + ((cc + 4 * i) & 15) * 16, f);
      }
      SCHED();
      if (u == 0) asm volatile("" :: "v"(t[0]), "v"(t[1]), "v"(t[2]), "v"(t[3]), "v"(t[4]), "v"(t[5]), "v"(t[6]));
      if (u == 0) claimed = mat == 0 || claim_try(c, look0);
      if (u == 4 - G_DEEP) look = claim_look(c);
      if (u + 3 + G_DEEP < 8) request(u + 3 + G_DEEP);
      if (u == 7 && !claimed && !claim_try(c, look)) claim(c, 0);
      BAR();
      v8u in[4];
#pragma unroll
      for (int q = 0; q < 4; ++q) in[q] = cat(lds16(c.L, sr0 + q * 8192), lds16(c.L, sr1 + q * 8192));
      SCHED();
#pragma unroll
      for (int q = 0; q < 4; ++q) {
        v8u f = in[q];
#define M_(r) asm volatile("" : "+" r(f))
        AT_ACC(M_, 8 * q + u);
#undef M_
#pragma unroll
        for (int r = 0; r < 8; ++r) wa[2 * q + (u >> 2)][8 * (u & 3) + r] = f[r];
      }
      BAR();
    }
  }
  STAMP(0);
  const int xpar = vote_a(wait_vote(c)) ^ (mat >> 1);
  TICK_DECL;
  asm volatile(G_LOOP
      :: [xo] "v"(cc * 2048 + flip * 256 + ((g ^ cc) << 4)), [xs] "v"(xs), [so] "v"((4 * w + g) * 512 + cc * 16), [fo] "v"((w | lane) ? 1 << 30 : 0),
         [rs] "s"(c.rs), [rx] "s"(rx), [rc] "s"(mkrs((const void*)c.ctrl, CTRL_BYTES)), [fl] "s"(C_FLAG + (mat * 32 + h) * 128), [xp] "s"(xpar),
         [ob] "s"((mat == 1 ? R_KX : R_QX) + h * 16384), [lw] "s"(w * 16384), GTRACE ACC_IN(wa)
      : CL_S, CL_16_32, CL_32_100);
}

DEV void zc_role(const Params& p, Ctx& c, int h) {
  c.budget = SPIN_CAP;
  const int w = c.w, lane = c.lane = fresh_lane(), g = lane >> 4, cc = lane & 15, h2 = lane >> 5, r32 = lane & 31;
  v32u wa[8];
  const unsigned first = claim_look(c);
  {
    const rsrc_t rw = mkrs(p.Wo, 4096 * 1024 * 2);
    const int vo = (h * 128 + 8 * g) * 2048 + (256 * w + 8 * cc) * 2;
    v4u row[8][8];
    auto request = [&](int u) {
#pragma unroll
      for (int r = 0; r < 8; ++r) row[u][r] = ld16w<0>(rw, vo, (32 * (u & 3) + r) * 2048 + (u >> 2) * 256);
    };
    request(0);
    SCHED();
    ask(c, first);
    SCHED();
    request(1); request(2);
    unsigned look = 0;
    const int sw_ = w * 8192 + g * 256, sr_ = w * 8192 + (cc & 7) * 1024 + g * 256;
    v8u two[8];
#pragma unroll
    for (int u = 0; u < 8; ++u) {
#pragma unroll
      for (int i = 0; i < 8; ++i) {
        v4u f;
#pragma unroll
        for (int r = 0; r < 4; ++r) f[r] = prm(row[u][2 * r + 1][i >> 1], row[u][2 * r][i >> 1], i & 1);
        sts16(c.L, sw_ + i * 1024 + ((cc + 2 * i) & 15) * 16, f);
      }
      SCHED();
      if (u == 4) look = claim_look(c);
      if (u + 3 < 8) request(u + 3);
      if (u == 7 && !claim_try(c, look)) claim(c, 0);
      LGKM(0);
#pragma unroll
      for (int q = 0; q < 8; ++q) {
        const v4u o = lds16(c.L, sr_ + ((2 * q + (cc >> 3) + 2 * cc) & 15) * 16);
#pragma unroll
        for (int r = 0; r < 4; ++r) two[q][4 * (u & 1) + r] = o[r];
      }
      SCHED();
      if (u & 1) {
#pragma unroll
        for (int q = 0; q < 8; ++q) {
          const int n = 2 * (8 * (u >> 2) + q) + ((u >> 1) & 1);
#define M_(r) asm volatile("" : "+" r(two[q]))
          AT_ACC(M_, n);
#undef M_
#pragma unroll
          for (int r = 0; r < 8; ++r) wa[n >> 2][8 * (n & 3) + r] = two[q][r];
        }
      }
      LGKM(0);
      SCHED();
    }
  }
  STAMP(0);
  BAR();
  const int para = vote_a(wait_vote(c));
  const int ao = g * 512 + cc * 16, zo = 16 * w * 65536 + lane * 16 + ((para ^ (w >> 1)) & 1) * 8192;
  TICK_DECL;
  asm volatile(ZC_LOOP
      : [bd] "+s"(c.budget)
      : [ao] "v"(ao), [a1] "v"(ao + 4096), [z1] "v"(zo + amap((h ^ z_mix(16 * w)) * 1024, 0)), [z2] "v"(zo + 65536 + amap((h ^ z_mix(16 * w + 1)) * 1024, 0)), [fo] "v"(lane ? 1 << 30 : 0),
        [rs] "s"(c.rs), [rc] "s"(mkrs((const void*)c.ctrl, CTRL_BYTES)), [fv] "s"(C_FLAG + (F_V + h) * 128), [fs] "s"(C_FLAG + (F_S + h) * 128),
        [fz] "s"(C_FLAG + (F_Z + w * 32 + h) * 128), [xp] "s"(para ^ 1), [vb] "s"(R_QX + h * 16384), [vd] "s"(C_VERDICT + c.att * 128), [z0] "n"(ZC_PRIME), [zb] "n"(R_Z), [rg] "n"(ZRING - 2), [rm] "n"(ZRING - 1), GTRACE ACC_IN(wa)
      : CL_S, CL_16_32, CL_32_100);
  if (c.budget == 0u) __hip_atomic_store(c.ctrl + C_TMO / 4, 1u, RLX_AGENT);
}

DEV void conv_role(const Params& p, Ctx& c, int cg, int far) {
  constexpr int FS = 4160, PL = 1280, UO = 4 * FS, WS = UO + 8 * PL;
  const int w = c.w, lane = c.lane = fresh_lane(), tid = w * 64 + lane, g = lane >> 4, cc = lane & 15;
  const int d0 = 8 * cg + 2 * w;
  const int pg = ((far ^ (cg >> 5)) & 1) * 8192;
  auto crow = [](int ch) { return (ch >> 1) * 16384 + (ch & 1) * 4096; };
  const int grp = cg >> 3, de = grp * 64 + (tid & 7) * 8, t0 = (cg & 7) * 256 + (tid >> 3) * 8;
  auto arrive = [&](int n) {
    VMWAIT(0);
    BAR();
    if (w == 0 && lane == 0) __hip_atomic_fetch_add(c.ctrl + (C_GRP + n * 128) / 4, 1u, RLX_AGENT);
  };
  auto await = [&](int n) {
    if (w == 0) {
#pragma unroll 1
      while (__builtin_amdgcn_readfirstlane(__hip_atomic_load(c.ctrl + (C_GRP + n * 128) / 4, RLX_AGENT)) < 8u) { if (spend(c)) break; __builtin_amdgcn_s_sleep(1); }
    }
    BAR();
  };
  v4u fr[2][4], ur[2][4], qr[8];
  unsigned up[2][4];
  const int so = cc * 64 + g * 16;
  const int fw = opq(w * WS + 48 + lane * 16), uw = opq(w * WS + UO + g * PL + cc * 16);
  auto copies = [&](int ch) {
#pragma unroll
    for (int i = 0; i < 4; ++i) {
      const v4u f = fr[ch][i];
      sts16(c.L, fw + 3 * FS + i * 1024, f);
      sts8(c.L, fw + FS + i * 1024 + 8, v2u{f[0], f[1]}); sts8(c.L, fw + FS + i * 1024 + 16, v2u{f[2], f[3]});
#pragma unroll
      for (int r = 0; r < 2; ++r) {
        const int o = fw + 2 * r * FS + i * 1024 + 12 - 8 * r;
        *(LAS unsigned*)(c.L + o) = f[0]; sts8(c.L, o + 4, v2u{f[1], f[2]}); *(LAS unsigned*)(c.L + o + 12) = f[3];
      }
    }
  };
  CST(0);
  const int turns = (grp & 3) == 2 ? 2 : (grp & 3) == 3 ? 0 : 1;
#pragma unroll 1
  for (int n = turns - 1; n >= 0; --n) {
    const rsrc_t rk = mkrs(p.bk, 2048 * 2048), rv = mkrs(p.bv, 2048 * 2048);
    const int dn = de + 64 * n;
    v4u kr[11], vr[8], sh[4];
#pragma unroll
    for (int i = 0; i < 4; ++i) sh[i] = ld16c(mkrs(p.shift, 8192), dn * 8 + i * 16, 0);
#pragma unroll
    for (int i = 0; i < 11; ++i) kr[i] = ld16c(rk, (t0 + i - 3) * 2048 + dn * 2, 0);
#pragma unroll
    for (int i = 0; i < 8; ++i) vr[i] = ld16c(rv, (t0 + i) * 2048 + dn * 2, 0);
    SCHED();
    CST(1);
#pragma unroll
    for (int ch = 0; ch < 8; ++ch) {
      const unsigned sa = sh[ch >> 1][(ch & 1) * 2], sb = sh[ch >> 1][(ch & 1) * 2 + 1];
      const float s0 = blo(sa), s1 = bhi(sa), s2 = blo(sb), s3 = bhi(sb);
      float kk[11], o[8];
#pragma unroll
      for (int i = 0; i < 11; ++i) kk[i] = (ch & 1) ? bhi(kr[i][ch >> 1]) : blo(kr[i][ch >> 1]);
#pragma unroll
      for (int i = 0; i < 8; ++i) o[i] = fmaf(kk[i + 3], s0, fmaf(kk[i + 2], s1, fmaf(kk[i + 1], s2, kk[i] * s3))) * ((ch & 1) ? bhi(vr[i][ch >> 1]) : blo(vr[i][ch >> 1]));
      st16s(c.rs, dn * 8192 + t0 * 2, R_KVT + pg + crow(ch), v4u{pk(o[0], o[1]), pk(o[2], o[3]), pk(o[4], o[5]), pk(o[6], o[7])});
    }
    CST(3);
    arrive(2 * (grp + n));
  }
  const unsigned dd = __builtin_amdgcn_raw_buffer_load_b32(mkrs(p.ssmD, 2048), d0 * 2, 0, CONV_AUX);
#pragma unroll
  for (int i = 0; i < 8; ++i) fr[i >> 2][i & 3] = ld16c(mkrs(p.ssmk, 4 * MB), lane * 16 + (i & 3) * 1024, (d0 + (i >> 2)) * 4096);
  SCHED();
  CST(4);
  await(2 * grp);
  CST(5);
#pragma unroll
  for (int i = 0; i < 8; ++i) {
    const int b = R_KVT + pg + d0 * 8192 + (i >> 2) * 4096 + (i & 3) * 1024;
    ur[i >> 2][i & 3] = ld16s(c.rs, so, b);
    up[i >> 2][i & 3] = __builtin_amdgcn_raw_buffer_load_b32(c.rs, (i & 3) == 0 && lane == 0 ? 1 << 30 : so, b - 4, AUX_SC1);
  }
  SCHED();
  if (lane < 16) sts16(c.L, w * WS + (lane >> 2) * FS + (lane & 3) * 16, v4u{0, 0, 0, 0});
#pragma unroll
  for (int i = 0; i < 2; ++i) sts16(c.L, w * WS + UO + (4 * i + g) * PL + cc * 16, v4u{0, 0, 0, 0});
  STAMP(5);
  const int fa = opq(w * WS + (cc & 3) * FS + (3 + (cc >> 2) - g) * 16);
  sfor<2>([&](auto CH) __attribute__((always_inline)) {
    constexpr int ch = CH.value;
    SCHED();
    copies(ch);
    SCHED();
#pragma unroll
    for (int i = 0; i < 4; ++i) {
      const v4u a = ur[ch][i];
      v4u r0, r1;
#pragma unroll
      for (int r = 0; r < 4; ++r) {
        r0[r] = __builtin_amdgcn_alignbyte(a[3 - r], a[3 - r], 2);
        r1[r] = __builtin_amdgcn_perm(r < 3 ? a[2 - r] : up[ch][i], a[3 - r], 0x07060100u);
      }
      sts16(c.L, uw + (16 + 16 * i) * 16, r0);
      sts16(c.L, uw + 4 * PL + (16 + 16 * i) * 16, r1);
    }
    CST(6 + 2 * ch);
    f32x4 acc[4][2];
#pragma unroll
    for (int i = 0; i < 8; ++i) acc[i >> 1][i & 1] = f32x4{0.f, 0.f, 0.f, 0.f};
    v4u A[2][4], B[2][8];
    auto frag = [&](int d, int n) {
      constexpr int order[12] = {0, 4, 5, 6, 7, 8, 9, 10, 11, 1, 2, 3};
      const int x = order[n];
      if (x < 4) A[d & 1][x] = lds16(c.L, fa + (d + 16 * x) * 64);
      else B[d & 1][x - 4] = lds16(c.L, uw + ((x - 4) & 1) * 4 * PL + (16 + 16 * ((x - 4) >> 1) - d) * 16);
    };
    asm volatile("" ::: "memory");
#pragma unroll
    for (int n = 0; n < 12; ++n) frag(0, n);
    SCHED();
    sfor<16>([&](auto D) __attribute__((always_inline)) {
      constexpr int d = D.value;
      int n = 0;
#pragma unroll
      for (int k = 0; k < 4; ++k)
#pragma unroll
        for (int m = 0; m + k < 4; ++m)
#pragma unroll
          for (int e = 0; e < 2; ++e) {
            acc[m + k][e] = mfma16(A[d & 1][k], B[d & 1][2 * m + e], acc[m + k][e]);
            if (d < 15 && n < 12) frag(d + 1, n);
            if (ch == 0 && n == 14 && (d & 1)) qr[d >> 1] = ld16c(mkrs(p.bq, 4 * MB), (t0 + (d >> 1)) * 2048 + de * 2, 0);
            ++n;
            SCHED();
          }
    });
    CST(7 + 2 * ch);
    const float dv = ch ? bhi(dd) : blo(dd);
#pragma unroll
    for (int q = 0; q < 4; ++q) {
      v4u z;
#pragma unroll
      for (int i = 0; i < 4; ++i) {
        const unsigned yy = pk(acc[q][1][i], acc[q][0][i]), ud = pk(blo(ur[ch][q][i]) * dv, bhi(ur[ch][q][i]) * dv);
        z[i] = pk(blo(yy) + blo(ud), bhi(yy) + bhi(ud));
      }
      st16s(c.rs, so + q * 1024, R_YT + pg + d0 * 8192 + ch * 4096, z);
    }
  });
  STAMP(6);
  {
    const rsrc_t ro = mkrs(p.out, 2048 * 2048 * 2);
    v4u yv[8];
    arrive(2 * grp + 1);
    await(2 * grp + 1);
    CST(10);
#pragma unroll
    for (int ch = 0; ch < 8; ++ch) yv[ch] = ld16s(c.rs, de * 8192 + t0 * 2, R_YT + pg + crow(ch));
#pragma unroll
    for (int i = 0; i < 8; ++i) {
      float o[8];
#pragma unroll
      for (int ch = 0; ch < 8; ++ch) o[ch] = ((ch & 1) ? bhi(qr[i][ch >> 1]) : blo(qr[i][ch >> 1])) * ((i & 1) ? bhi(yv[ch][i >> 1]) : blo(yv[ch][i >> 1]));
      __builtin_amdgcn_raw_buffer_store_b128(v4u{pk(o[0], o[1]), pk(o[2], o[3]), pk(o[4], o[5]), pk(o[6], o[7])}, ro, (t0 + i) * 4096 + (1024 + de) * 2, 0, OUT_AUX2);
    }
  }
  if (c.budget == 0u) __hip_atomic_store(c.ctrl + C_TMO / 4, 1u, RLX_AGENT);
}

extern "C" __global__ void __launch_bounds__(256, 1) mk_mega(Params p) {
  extern __shared__ __attribute__((aligned(16))) unsigned char lds_raw[];
  Ctx c;
  c.ctrl = (gu32*)p.ctrl;
  c.rs = mkrs(p.data, R_END);
  c.L = (lds_t)lds_raw;
  c.w = __builtin_amdgcn_readfirstlane(threadIdx.x >> 6);
  c.lane = threadIdx.x & 63;
  c.t0 = (unsigned)__builtin_amdgcn_s_memrealtime();
  c.b = (int)(blockIdx.x & 255u);
  c.att = (int)(blockIdx.x >> 8);
  c.ask = 0;
  c.slot = (c.att ? (c.b & 7) : (int)((unsigned)__builtin_amdgcn_s_getreg(20 | (3 << 11)) & 7u)) * 32 + (c.b >> 3);
  const int x = c.slot >> 5, t = c.slot & 31;
  if (c.w == 1) {
    asm volatile("s_getpc_b64 s[60:61]\n1:\n\ts_sub_u32 s60, s60, 1b-mk_mega\n\ts_subb_u32 s61, s61, 0\n\ts_mov_b32 s62, mk_tail-mk_mega\n\ts_mov_b32 s63, 0x20000\n\t"
                 "buffer_load_dword v255, %0, s[60:63], 0 offen\n\ts_waitcnt vmcnt(0)" :: "v"((c.lane * 32 + t) * 128) : "s60", "s61", "s62", "s63", "v255", "scc", "memory");
  }
  if (c.b == SERVER && c.w == 3) serve(c);
  if (x < 4) {
    if (t < 8) qk_role(p, c, x * 8 + t);
    else if (t < 16) vo_role(p, c, x * 8 + t - 8);
    else y_role(p, c, x * 16 + t - 16);
  } else {
    if (t < 24) g_role(p, c, t >> 3, (x - 4) * 8 + (t & 7), t);
    else zc_role(p, c, (x - 4) * 8 + t - 24);
    STAMP(2);
    c.budget = SPIN_CAP;
    conv_role(p, c, (x - 4) * 32 + t, vote_a(wait_verdict(c)) ^ 1);
  }
  STAMP(3);
}

}

extern "C" void kernel_launch(void* const* d_in, const int* in_sizes, int n_in, void* d_out, int out_size, void* d_ws, size_t ws_size,
                              hipStream_t stream) {
  using namespace mk;
  (void)in_sizes; (void)n_in; (void)out_size;
  Params p;
  p.x = (const u16*)d_in[0]; p.mem0 = (const u16*)d_in[1];
  p.Wq = (const u16*)d_in[2]; p.Wk = (const u16*)d_in[3]; p.Wv = (const u16*)d_in[4]; p.Wo = (const u16*)d_in[5];
  p.bq = (const u16*)d_in[7]; p.bk = (const u16*)d_in[8]; p.bv = (const u16*)d_in[9];
  p.shift = (const u16*)d_in[10]; p.ssmk = (const u16*)d_in[11]; p.ssmD = (const u16*)d_in[12];
  p.out = (u16*)d_out;
  p.ctrl = (unsigned char*)d_ws;
  p.data = data_base(d_ws, ws_size);
  (void)hipFuncSetAttribute((const void*)mk_mega, hipFuncAttributeMaxDynamicSharedMemorySize, LDS_BYTES);
  hipLaunchKernelGGL(mk_mega, dim3(256), dim3(256), LDS_BYTES, stream, p);
}
```

```cpp
#include <hip/hip_runtime.h>
#include <hip/hip_bf16.h>

namespace mk {

#define GAS __attribute__((address_space(1)))
#define LAS __attribute__((address_space(3)))
#define DEV __device__ __forceinline__
#define RLX_AGENT __ATOMIC_RELAXED, __HIP_MEMORY_SCOPE_AGENT
typedef unsigned short u16;
typedef unsigned v2u __attribute__((ext_vector_type(2)));
typedef unsigned v4u __attribute__((ext_vector_type(4)));
typedef unsigned v8u __attribute__((ext_vector_type(8)));
typedef unsigned v16u __attribute__((ext_vector_type(16)));
typedef unsigned v32u __attribute__((ext_vector_type(32)));
typedef float f32x2 __attribute__((ext_vector_type(2)));
typedef float f32x4 __attribute__((ext_vector_type(4)));
typedef float f32x16 __attribute__((ext_vector_type(16)));
typedef __bf16 bf16x2 __attribute__((ext_vector_type(2)));
typedef __bf16 bf16x8 __attribute__((ext_vector_type(8)));
typedef __bf16 bf16x16 __attribute__((ext_vector_type(16)));
typedef GAS unsigned gu32;
typedef __amdgpu_buffer_rsrc_t rsrc_t;
typedef LAS unsigned char* lds_t;

#define XCNT(r) ""
#define UCNT ""
#define CL_T
#define XT(a, b) ""
#define LMARK ""
#define GSUM ""
#define ZSUM ""
#define GTRACE
#define MIXED 1
#define NOFLAG 0
#define TOK_KICK 1
#define ZRING 16
#define Z_AT 16
#define Z_GAP 4
#define STR_(x) #x
#define STR(x) STR_(x)
#define GQ_AT 600
#define GK_AT 400
#define PULL_AT 800
#define QK_AT 400
#define SERVER 194
#define SLOW_LEAD 100
#define PULL_DEPTH 1
#define PRIME 1
#define X_AT 900
#define VO_AT 100
#define ZC_AT 0
#define CONV_AUX 2
#define W_AUX 0
#define ZC_PRIME 2
#define CODE_PULL 1
#define PROBES 2
#define Z_WIN 20
#define TOK_WAIT 0
#define Z_AUX 0
#define OUT_AUX 2
#define OUT_AUX2 2
#define URING 8
#define YH_AT 400
#define G_DEEP 1
#define AGAIN 0
#define PRE 0
#define YH 1
#define HELP_O 1
#define HELP_V 1
#define SLOW_V 1
#define K_FIRST 1
#define FLIP 1
#define YH_STEP 300
#define Z_WAIT 0
#define HELPERS 2
#define B_AUX 0
constexpr int NSEG = 64;
constexpr unsigned SENT = 0xFFFFFFFFu;
constexpr int AUX_SC1 = 16, AUX_NT = 2;
constexpr int T_LAG = 61;
constexpr int G_AHEAD = 4;
constexpr unsigned SPIN_CAP = 1u << 21;
constexpr int LDS_BYTES = 163840;

constexpr int C_ECHO = 0, C_CLAIM = 1024, C_VERDICT = 3072, C_TMO = 3328, C_CONV = 3456, C_VOTE = 3840, C_FLAG = 4096, C_GRP = C_FLAG + 256 * 128, C_ASK = C_GRP + 32 * 128;
constexpr int F_Q = 0, F_K = 32, F_V = 64, F_S = 96, F_Z = 128;
constexpr int CTRL_BYTES = C_ASK + 256 * 64;
constexpr int MB = 1 << 20;
constexpr int R_U = 136 * MB;
constexpr int R_MEM = 2 * MB, MEM_SLOT = 32768;
constexpr int R_P = R_MEM + 4 * MEM_SLOT, P_SLOT = 65536;
constexpr int R_PT = 4 * MB, PT_SLOT = 131072;
constexpr int R_QX = 8 * MB, R_KX = 40 * MB, R_Z = 72 * MB, Z_SLOT = 4 * MB, R_KVT = R_KX, R_YT = R_KX + 8 * MB, R_END = (136 + URING) * MB;

constexpr size_t R_DIAG = 0;
#define TOP 1
static inline unsigned char* data_base(void* ws, size_t size) {
  return (unsigned char*)((TOP ? (size_t)ws + size - R_END - R_DIAG : (size_t)ws + CTRL_BYTES + 2 * MB - 1) & ~(size_t)(2 * MB - 1));
}
struct Params {
  const u16 *x, *mem0, *Wq, *Wk, *Wv, *Wo, *bq, *bk, *bv, *shift, *ssmk, *ssmD;
  u16* out;
  unsigned char* ctrl;
  unsigned char* data;
};

DEV rsrc_t mkrs(const void* p, unsigned bytes) { return __builtin_amdgcn_make_buffer_rsrc((void*)p, 0, bytes, 0x00020000); }
DEV v4u ld16s(rsrc_t rs, int v, int s) { return __builtin_amdgcn_raw_buffer_load_b128(rs, v, s, AUX_SC1); }
DEV v2u ld8s(rsrc_t rs, int v, int s) { return __builtin_amdgcn_raw_buffer_load_b64(rs, v, s, AUX_SC1); }
DEV v4u ld16(rsrc_t rs, int v, int s) { return __builtin_amdgcn_raw_buffer_load_b128(rs, v, s, 0); }
DEV v4u ld16z(rsrc_t rs, int v, int s) { return __builtin_amdgcn_raw_buffer_load_b128(rs, v, s, AUX_SC1 | Z_AUX); }
DEV v4u ld16c(rsrc_t rs, int v, int s) { return __builtin_amdgcn_raw_buffer_load_b128(rs, v, s, CONV_AUX); }
template <int A> DEV v4u ld16w(rsrc_t rs, int v, int s) { return __builtin_amdgcn_raw_buffer_load_b128(rs, v, s, A); }
DEV v2u ld8(rsrc_t rs, int v, int s) { return __builtin_amdgcn_raw_buffer_load_b64(rs, v, s, 0); }
DEV void dma16(rsrc_t rs, lds_t l, int v) {
#if __HIP_DEVICE_COMPILE__
  __builtin_amdgcn_raw_ptr_buffer_load_lds(rs, (LAS void*)l, 16, v, 0, 0, 0);
#endif
}
DEV void st16s(rsrc_t rs, int v, int s, v4u d) { __builtin_amdgcn_raw_buffer_store_b128(d, rs, v, s, AUX_SC1); }
DEV void st8s(rsrc_t rs, int v, int s, v2u d) { __builtin_amdgcn_raw_buffer_store_b64(d, rs, v, s, AUX_SC1); }
DEV void st4s(rsrc_t rs, int v, int s, unsigned d) { __builtin_amdgcn_raw_buffer_store_b32(d, rs, v, s, AUX_SC1); }
DEV int amap(int o, int par) { return ((o >> 13) << 14) | (par << 13) | (o & 8191); }
DEV int xblk(int s, int h, int par) { return (s * 32 + h) * 16384 + ((par ^ (s >> 2)) & 1) * 8192; }
DEV unsigned umax(unsigned a, unsigned b) { return a > b ? a : b; }
DEV unsigned top(v4u v) { return umax(umax(v.x, v.y), umax(v.z, v.w)); }
DEV unsigned pk(float a, float b) { bf16x2 v = {(__bf16)a, (__bf16)b}; return __builtin_bit_cast(unsigned, v); }
DEV unsigned pks(float a, float b) { unsigned u = pk(a, b); return u < SENT - 1u ? u : SENT - 1u; }
DEV float blo(unsigned u) { return __builtin_bit_cast(float, u << 16); }
DEV float bhi(unsigned u) { return __builtin_bit_cast(float, u & 0xFFFF0000u); }
DEV float rb(float a) { return blo(pk(a, a)); }
DEV unsigned fu(float f) { return __builtin_bit_cast(unsigned, f); }
DEV float uf(unsigned u) { return __builtin_bit_cast(float, u); }
DEV f32x4 mfma16(v4u a, v4u b, f32x4 c) {
  return __builtin_amdgcn_mfma_f32_16x16x32_bf16(__builtin_bit_cast(bf16x8, a), __builtin_bit_cast(bf16x8, b), c, 0, 0, 0);
}
DEV f32x16 mfma32(v4u a, v4u b, f32x16 c) {
  return __builtin_amdgcn_mfma_f32_32x32x16_bf16(__builtin_bit_cast(bf16x8, a), __builtin_bit_cast(bf16x8, b), c, 0, 0, 0);
}
DEV f32x4 smf(v4u a, v8u b, f32x4 c, int idx) {
  return __builtin_amdgcn_smfmac_f32_16x16x64_bf16(__builtin_bit_cast(bf16x8, a), __builtin_bit_cast(bf16x16, b), c, idx, 0, 0);
}
DEV v8u cat(v4u a, v4u b) { return __builtin_shufflevector(a, b, 0, 1, 2, 3, 4, 5, 6, 7); }
DEV v4u lo4(v8u a) { return __builtin_shufflevector(a, a, 0, 1, 2, 3); }
DEV v4u hi4(v8u a) { return __builtin_shufflevector(a, a, 4, 5, 6, 7); }
DEV v4u lds16(lds_t L, int o) { return *(LAS v4u*)(L + o); }
DEV void sts16(lds_t L, int o, v4u v) { *(LAS v4u*)(L + o) = v; }
DEV void sts8(lds_t L, int o, v2u v) { *(LAS v2u*)(L + o) = v; }
#define SCHED() __builtin_amdgcn_sched_barrier(0)
#define PIN_A(x) asm volatile("" : "+a"(x))
#define PIN_V(x) asm volatile("" : "+v"(x))
DEV int opq(int v) { asm volatile("" : "+v"(v)); return v; }
DEV int fresh_lane() {
  int l; asm volatile("v_mbcnt_lo_u32_b32 %0, -1, 0\n\tv_mbcnt_hi_u32_b32 %0, -1, %0" : "=v"(l)); return l;
}
DEV f32x4 fresh_zero() {
  f32x2 a, b;
  asm volatile("v_mov_b64 %0, 0" : "=v"(a));
  asm volatile("v_mov_b64 %0, 0" : "=v"(b));
  return __builtin_shufflevector(a, b, 0, 1, 2, 3);
}
DEV unsigned fresh_sent() { unsigned z; asm volatile("v_mov_b32 %0, -1" : "=v"(z)); return z; }
#define SMF "v_smfmac_f32_16x16x64_bf16 "
#define MF16 "v_mfma_f32_16x16x32_bf16 "
#define SMF1(cls, d, a, b) asm volatile(SMF "%0, %1, %2, %3" : "+v"(d) : "v"(a), cls(b), "v"(idx))
#define SMF1F(cls, lo, hi, d, a, b) asm volatile(SMF "v[" #lo ":" #hi "], %1, %2, %3" : "+{v[" #lo ":" #hi "]}"(d) : "v"(a), cls(b), "v"(idx))
#define AT_HOME(M, j) do { const int j_ = (j); if (j_ == 0) M("{v[32:39]}"); if (j_ == 1) M("{v[40:47]}"); if (j_ == 2) M("{v[48:55]}"); if (j_ == 3) M("{v[56:63]}"); if (j_ == 4) M("{v[64:71]}"); if (j_ == 5) M("{v[72:79]}"); if (j_ == 6) M("{v[80:87]}"); if (j_ == 7) M("{v[88:95]}"); if (j_ == 8) M("{v[96:103]}"); if (j_ == 9) M("{v[104:111]}"); if (j_ == 10) M("{v[112:119]}"); if (j_ == 11) M("{v[120:127]}"); if (j_ == 12) M("{v[128:135]}"); if (j_ == 13) M("{v[136:143]}"); if (j_ == 14) M("{v[144:151]}"); if (j_ == 15) M("{v[152:159]}"); } while (0)
#define MF1(d, a, b) asm volatile(MF16 "%0, %1, %2, %0" : "+v"(d) : "v"(a), "v"(b))
#define MF1Z(d, a, b) asm volatile(MF16 "%0, %1, %2, 0" : "=&v"(d) : "v"(a), "v"(b))
#define LGKM(n) asm volatile("s_waitcnt lgkmcnt(" #n ")" ::: "memory")
#define RAWBAR() do { __builtin_amdgcn_s_barrier(); asm volatile("" ::: "memory"); } while (0)
DEV void settle3(f32x4& s0, f32x4& s1, f32x4& s2) { asm volatile("s_nop 7" : "+v"(s0), "+v"(s1), "+v"(s2)); }
DEV void settle4(f32x4 (&d)[4]) { asm volatile("s_nop 7" : "+v"(d[0]), "+v"(d[1]), "+v"(d[2]), "+v"(d[3])); }
#define BAR() do { asm volatile("s_waitcnt lgkmcnt(0)" ::: "memory"); __builtin_amdgcn_s_barrier(); asm volatile("" ::: "memory"); } while (0)
#define VMWAIT(n) asm volatile("s_waitcnt vmcnt(" #n ")" ::: "memory")
DEV float add1(float a, float b) {
  float o; asm("v_add_f32 %0, %1, %2" : "=v"(o) : "v"(a), "v"(b)); return o;
}
DEV float xmax(float v) {
  auto r = __builtin_amdgcn_permlane32_swap(fu(v), fu(v), false, false); v = fmaxf(uf(r[0]), uf(r[1]));
  auto q = __builtin_amdgcn_permlane16_swap(fu(v), fu(v), false, false); return fmaxf(uf(q[0]), uf(q[1]));
}
DEV float xsum(float v) {
  auto r = __builtin_amdgcn_permlane32_swap(fu(v), fu(v), false, false); v = uf(r[0]) + uf(r[1]);
  auto q = __builtin_amdgcn_permlane16_swap(fu(v), fu(v), false, false); return uf(q[0]) + uf(q[1]);
}

DEV float ror8(float v) { return uf(__builtin_amdgcn_update_dpp(0u, fu(v), 0x128, 0xf, 0xf, true)); }
DEV float fmax2(float a, float b) { return __builtin_amdgcn_fmed3f(a, b, __builtin_inff()); }
DEV float fmax3(float a, float b, float c) { float r; asm("v_max3_f32 %0, %1, %2, %3" : "=v"(r) : "v"(a), "v"(b), "v"(c)); return r; }
DEV bool allok(unsigned t) { return __all(t != SENT); }

template <int N> struct IC { static constexpr int value = N; };
template <int N, class F> DEV void sfor(F&& f) { if constexpr (N > 0) { sfor<N - 1>(f); f(IC<N - 1>{}); } }
struct Ctx {
  gu32* ctrl;
  rsrc_t rs;
  lds_t L;
  int w, lane;
  int b, slot, att;
  unsigned ask;
  unsigned t0;
  unsigned budget;
};
DEV bool spend(Ctx& c) {
  const unsigned old = c.budget;
  asm volatile("s_cmp_lg_u32 %0, 0\n\ts_subb_u32 %0, %0, 0" : "+s"(c.budget) :: "scc");
  return old == 0u;
}
DEV unsigned wait_flag(Ctx& c, int flag, unsigned need) {
  gu32* f = c.ctrl + (C_FLAG + flag * 128) / 4;
  unsigned v;
#pragma unroll 1
  for (;;) {
    v = __builtin_amdgcn_readfirstlane(__hip_atomic_load(f, RLX_AGENT));
    if (v >= need || spend(c)) break;
    __builtin_amdgcn_s_sleep(2);
  }
  __builtin_amdgcn_fence(__ATOMIC_ACQUIRE, "wavefront");
  return v;
}
DEV void post_flag(Ctx& c, int flag, unsigned v, int off = 0) {
  st4s(mkrs((const void*)c.ctrl, CTRL_BYTES), (c.w | off | fresh_lane()) ? 1 << 30 : 0, C_FLAG + flag * 128, v);
}
constexpr unsigned CL_A = 1u << 16, CL_VOTED = 1u << 24, CL_FILLED = 1u << 28;
DEV unsigned claim_look(Ctx& c) { return __hip_atomic_load(c.ctrl + C_ECHO / 4 + c.b, RLX_AGENT); }
DEV void ask(Ctx& c, unsigned first) {
  c.ask = __builtin_amdgcn_readfirstlane(first) + 1u;
  st4s(mkrs((const void*)c.ctrl, CTRL_BYTES), (c.w | c.att | fresh_lane()) ? 1 << 30 : 0, C_ASK + c.b * 64, c.ask);
}
DEV void claim(Ctx& c, unsigned votes) {
  if (c.w != 0) return;
  if (c.att == 0) {
#pragma unroll 1
    while (__builtin_amdgcn_readfirstlane(__hip_atomic_load(c.ctrl + C_ECHO / 4 + c.b, RLX_AGENT)) != c.ask) {
      if (spend(c)) break;
      __builtin_amdgcn_s_sleep(2);
    }
  }
  if (fresh_lane() == 0) __hip_atomic_fetch_add(c.ctrl + (C_CLAIM + c.att * 1024) / 4 + c.slot, 1u + votes, RLX_AGENT);
}
DEV bool claim_try(Ctx& c, unsigned look) {
  const bool ok = c.att || __builtin_amdgcn_readfirstlane(look) == c.ask;
  if (ok && c.w == 0 && fresh_lane() == 0) __hip_atomic_fetch_add(c.ctrl + (C_CLAIM + c.att * 1024) / 4 + c.slot, 1u, RLX_AGENT);
  return ok;
}
DEV unsigned wait_verdict(Ctx& c) {
  unsigned v;
#pragma unroll 1
  for (;;) {
    v = __builtin_amdgcn_readfirstlane(__hip_atomic_load(c.ctrl + (C_VERDICT + c.att * 128) / 4, RLX_AGENT));
    if ((v & 1u) || spend(c)) break;
    __builtin_amdgcn_s_sleep(2);
  }
  __builtin_amdgcn_fence(__ATOMIC_ACQUIRE, "wavefront");
  if ((v & 3u) == 3u && c.att == 0) {
    asm volatile("s_waitcnt vmcnt(0) lgkmcnt(0)\n\ts_barrier\n\ts_mov_b64 s[0:1], %0\n\ts_mov_b32 s2, %1\n\tv_mov_b32 v0, %2\n\t"
                 "s_getpc_b64 s[4:5]\n1:\n\ts_sub_u32 s4, s4, 1b-mk_mega\n\ts_subb_u32 s5, s5, 0\n\ts_setpc_b64 s[4:5]"
                 :: "s"(__builtin_amdgcn_kernarg_segment_ptr()), "s"(c.b + 256), "v"(c.w * 64 + fresh_lane()) : "s0", "s1", "s2", "s4", "s5", "v0", "scc", "memory");
    __builtin_unreachable();
  }
  return v;
}
DEV unsigned wait_vote(Ctx& c) {
  unsigned v;
#pragma unroll 1
  for (;;) {
    v = __builtin_amdgcn_readfirstlane(__hip_atomic_load(c.ctrl + C_VOTE / 4, RLX_AGENT));
    if ((v & 1u) || spend(c)) break;
    __builtin_amdgcn_s_sleep(2);
  }
  return v;
}
DEV void serve(Ctx& c) {
  c.budget = SPIN_CAP;
  const int lane = fresh_lane();
  const rsrc_t rc = mkrs((const void*)c.ctrl, CTRL_BYTES);
  if (c.att == 0) {
#pragma unroll
    for (int i = C_CLAIM / 1024; i < C_ASK / 1024; ++i) st16s(rc, lane * 16, i * 1024, v4u{0, 0, 0, 0});
    VMWAIT(0);
  }
  unsigned vote = c.att ? wait_vote(c) : 0u;
  bool echo = c.att == 0, first = true;
  v4u last = {0u, 0u, 0u, 0u};
#pragma unroll 1
  for (;;) {
    asm volatile("" ::: "memory");
    const v4u cl = ld16s(rc, lane * 16, C_CLAIM + c.att * 1024);
    if (echo) {
      v4u e;
#pragma unroll
      for (int i = 0; i < 4; ++i) e[i] = __builtin_amdgcn_raw_buffer_load_b32(rc, (lane * 4 + i) * 64, C_ASK, AUX_SC1);
      if (first || __any(e[0] != last[0] || e[1] != last[1] || e[2] != last[2] || e[3] != last[3])) st16s(rc, lane * 16, C_ECHO, e);
      last = e; first = false;
    }
    unsigned n = 0, one = 1;
    int va = 0, nv = 0, nf = 0;
#pragma unroll
    for (int i = 0; i < 4; ++i) {
      n += cl[i] & 0xFFFFu; one &= (cl[i] & 0xFFFFu) == 1u;
      va += __popcll(__ballot(cl[i] & CL_A)); nv += __popcll(__ballot(cl[i] & CL_VOTED)); nf += __popcll(__ballot(cl[i] & CL_FILLED));
    }
#pragma unroll
    for (int o = 1; o < 64; o <<= 1) n += (unsigned)__shfl_xor((int)n, o);
    const bool all = __builtin_amdgcn_readfirstlane(n) >= 256u, good = __all(one);
    echo = echo && !all;
    if (!vote && (all || max(va, nv - va) > 20)) {
      vote = 2 * va > nv ? 5u : 9u;
      if (lane == 0) __hip_atomic_store(c.ctrl + C_VOTE / 4, vote, RLX_AGENT);
    }
    if (all && (!good || nf >= 64)) {
      if (lane == 0) __hip_atomic_store(c.ctrl + (C_VERDICT + c.att * 128) / 4, vote | (good ? 0u : 2u), RLX_AGENT);
      break;
    }
    if (spend(c)) break;
  }
}
DEV int vote_a(unsigned v) { return (int)((v >> 2) & 1u); }
DEV int vote_b(unsigned v) { return (int)((v >> 3) & 1u); }

#define STAMP(slot) do {} while (0)
#define TICK_DECL
#define LOG(i, s, v) do {} while (0)
#define LOGS(i, s) do {} while (0)
#define CST(i) do {} while (0)
#define LITE(s) do {} while (0)
#define TICK(i) do {} while (0)
#define TICK_SUM(n) do {} while (0)
#define TICK_LDS(b) do {} while (0)
#define TICK_DUMP(b) do {} while (0)
#define TICK_OUT() do {} while (0)

DEV constexpr int krow(int rho8, int i) { return 32 * (rho8 >> 2) + 8 * (rho8 & 1) + 2 * ((rho8 >> 1) & 1) + i; }
DEV int krow_lane(int g) { return 16 * (g >> 1) + 4 * (g & 1); }
DEV unsigned prm(unsigned hi, unsigned lo, int odd) { return __builtin_amdgcn_perm(hi, lo, odd ? 0x07060302u : 0x05040100u); }

#define AT_ACC(M, n) do { const int n_ = (n); if (n_ == 0) M("{a[0:7]}"); if (n_ == 1) M("{a[8:15]}"); if (n_ == 2) M("{a[16:23]}"); if (n_ == 3) M("{a[24:31]}"); if (n_ == 4) M("{a[32:39]}"); if (n_ == 5) M("{a[40:47]}"); if (n_ == 6) M("{a[48:55]}"); if (n_ == 7) M("{a[56:63]}"); if (n_ == 8) M("{a[64:71]}"); if (n_ == 9) M("{a[72:79]}"); if (n_ == 10) M("{a[80:87]}"); if (n_ == 11) M("{a[88:95]}"); if (n_ == 12) M("{a[96:103]}"); if (n_ == 13) M("{a[104:111]}"); if (n_ == 14) M("{a[112:119]}"); if (n_ == 15) M("{a[120:127]}"); if (n_ == 16) M("{a[128:135]}"); if (n_ == 17) M("{a[136:143]}"); if (n_ == 18) M("{a[144:151]}"); if (n_ == 19) M("{a[152:159]}"); if (n_ == 20) M("{a[160:167]}"); if (n_ == 21) M("{a[168:175]}"); if (n_ == 22) M("{a[176:183]}"); if (n_ == 23) M("{a[184:191]}"); if (n_ == 24) M("{a[192:199]}"); if (n_ == 25) M("{a[200:207]}"); if (n_ == 26) M("{a[208:215]}"); if (n_ == 27) M("{a[216:223]}"); if (n_ == 28) M("{a[224:231]}"); if (n_ == 29) M("{a[232:239]}"); if (n_ == 30) M("{a[240:247]}"); if (n_ == 31) M("{a[248:255]}"); } while (0)
#define PIN_ACC0(x) asm volatile("" :: "{a[0:7]}"(x[0]), "{a[8:15]}"(x[1]), "{a[16:23]}"(x[2]), "{a[24:31]}"(x[3]), "{a[32:39]}"(x[4]), "{a[40:47]}"(x[5]), "{a[48:55]}"(x[6]), "{a[56:63]}"(x[7]), "{a[64:71]}"(x[8]), "{a[72:79]}"(x[9]), "{a[80:87]}"(x[10]), "{a[88:95]}"(x[11]), "{a[96:103]}"(x[12]), "{a[104:111]}"(x[13]), "{a[112:119]}"(x[14]), "{a[120:127]}"(x[15]))
#define PIN_ACC1(x) asm volatile("" :: "{a[128:135]}"(x[16]), "{a[136:143]}"(x[17]), "{a[144:151]}"(x[18]), "{a[152:159]}"(x[19]), "{a[160:167]}"(x[20]), "{a[168:175]}"(x[21]), "{a[176:183]}"(x[22]), "{a[184:191]}"(x[23]), "{a[192:199]}"(x[24]), "{a[200:207]}"(x[25]), "{a[208:215]}"(x[26]), "{a[216:223]}"(x[27]), "{a[224:231]}"(x[28]), "{a[232:239]}"(x[29]), "{a[240:247]}"(x[30]), "{a[248:255]}"(x[31]))
#define PIN_HOME(x) asm volatile("" :: "{v[32:39]}"(x[0]), "{v[40:47]}"(x[1]), "{v[48:55]}"(x[2]), "{v[56:63]}"(x[3]), "{v[64:71]}"(x[4]), "{v[72:79]}"(x[5]), "{v[80:87]}"(x[6]), "{v[88:95]}"(x[7]), "{v[96:103]}"(x[8]), "{v[104:111]}"(x[9]), "{v[112:119]}"(x[10]), "{v[120:127]}"(x[11]), "{v[128:135]}"(x[12]), "{v[136:143]}"(x[13]), "{v[144:151]}"(x[14]), "{v[152:159]}"(x[15]))
#define CL_32_100 "v32", "v33", "v34", "v35", "v36", "v37", "v38", "v39", "v40", "v41", "v42", "v43", "v44", "v45", "v46", "v47", "v48", "v49", "v50", "v51", "v52", "v53", "v54", "v55", "v56", "v57", "v58", "v59", "v60", "v61", "v62", "v63", "v64", "v65", "v66", "v67", "v68", "v69", "v70", "v71", "v72", "v73", "v74", "v75", "v76", "v77", "v78", "v79", "v80", "v81", "v82", "v83", "v84", "v85", "v86", "v87", "v88", "v89", "v90", "v91", "v92", "v93", "v94", "v95", "v96", "v97", "v98", "v99", "v100", "v101", "v102", "v103", "v104", "v105", "v106", "v107", "v108", "v109", "v110", "v111", "v112", "v113", "v114", "v115", "v116", "v117", "v118", "v119", "v120", "v121", "v122", "v123", "v124", "v125", "v126", "v127", "v128", "v129", "v130", "v131"
#define ACC_IN(x) "{a[0:31]}"(x[0]), "{a[32:63]}"(x[1]), "{a[64:95]}"(x[2]), "{a[96:127]}"(x[3]), "{a[128:159]}"(x[4]), "{a[160:191]}"(x[5]), "{a[192:223]}"(x[6]), "{a[224:255]}"(x[7])
#define CL_16_32 "v16", "v17", "v18", "v19", "v20", "v21", "v22", "v23", "v24", "v25", "v26", "v27", "v28", "v29", "v30", "v31"
#define CL_120_128 "v120", "v121", "v122", "v123", "v124", "v125", "v126", "v127", "v128", "v129", "v130", "v131", "v132", "v133", "v134", "v135", "v136", "v137", "v138", "v139"
#define CL_160_200 "v160", "v161", "v162", "v163", "v164", "v165", "v166", "v167", "v168", "v169", "v170", "v171", "v172", "v173", "v174", "v175", "v176", "v177", "v178", "v179", "v180", "v181", "v182", "v183", "v184", "v185", "v186", "v187", "v188", "v189", "v190", "v191", "v192", "v193", "v194", "v195", "v196", "v197", "v198", "v199"
#define CL_176_200 "v176", "v177", "v178", "v179", "v180", "v181", "v182", "v183", "v184", "v185", "v186", "v187", "v188", "v189", "v190", "v191", "v192", "v193", "v194", "v195", "v196", "v197", "v198", "v199"
#define CL_200_216 "v200", "v201", "v202", "v203", "v204", "v205", "v206", "v207", "v208", "v209", "v210", "v211", "v212", "v213", "v214", "v215"
#define CL_216_256 "v216", "v217", "v218", "v219", "v220", "v221", "v222", "v223", "v224", "v225", "v226", "v227", "v228", "v229", "v230", "v231", "v232", "v233", "v234", "v235", "v236", "v237", "v238", "v239", "v240", "v241", "v242", "v243", "v244", "v245", "v246", "v247", "v248", "v249", "v250", "v251", "v252", "v253", "v254", "v255"
#define CL_S "memory", "vcc", "scc", "s60", "s61", "s62", "s63", "s64", "s65", "s66", "s67", "s68" CL_T
#define TIMED_WAIT \
    "s_getpc_b64 s[62:63]\n\t" "1:\n\t" "s_memtime s[64:65]\n\t" "s_waitcnt lgkmcnt(0)\n\t" "s_sub_u32 s61, %0, s64\n\t" "s_cmp_gt_i32 s61, 124\n\t" "s_cbranch_scc1 1b\n\t" \
    "s_max_i32 s61, s61, 0\n\t" "s_and_b32 s61, s61, 124\n\t" "s_sub_u32 s61, 2f-1b, s61\n\t" "s_add_u32 s62, s62, s61\n\t" "s_addc_u32 s63, s63, 0\n\t" \
    "s_setpc_b64 s[62:63]\n\t" ".rept 31\n\t" "s_nop 0\n\t" ".endr\n\t" "2:\n\t"
#define QK_STEP0 \
    ".p2align 6\n\t" "v_xor_b32 v216, 0x400, %[mo]\n\t" "s_xor_b32 s60, %[sb], 0x800\n\t" "s_mov_b32 s66, 0x3e0293ee\n\t" "s_mov_b32 s67, 0x3e0293ee\n\t" XT(80, 81) \
    "s_add_u32 s61, %[kb], 0x1000\n\t" "buffer_load_dwordx4 v[16:19], %[kx], %[rs], %[kb] offen offset:0 sc1\n\t" \
    "buffer_load_dwordx4 v[20:23], %[kx], %[rs], %[kb] offen offset:2048 sc1\n\t" "buffer_load_dwordx4 v[24:27], %[kx], %[rs], s61 offen offset:0 sc1\n\t" \
    "buffer_load_dwordx4 v[28:31], %[kx], %[rs], s61 offen offset:2048 sc1\n\t" "buffer_load_dwordx4 v[200:203], %[kx], %[rs], %[kb] offen offset:256 sc1\n\t" \
    "buffer_load_dwordx4 v[204:207], %[kx], %[rs], %[kb] offen offset:2304 sc1\n\t" "buffer_load_dwordx4 v[208:211], %[kx], %[rs], s61 offen offset:256 sc1\n\t" \
    "buffer_load_dwordx4 v[212:215], %[kx], %[rs], s61 offen offset:2304 sc1\n\t" "s_barrier\n\t" "s_waitcnt vmcnt(7)\n\t" "ds_write_b128 %[kx], v[16:19] offset:0\n\t" \
    "s_waitcnt vmcnt(6)\n\t" "ds_write_b128 %[kx], v[20:23] offset:2048\n\t" "s_waitcnt vmcnt(5)\n\t" "ds_write_b128 %[kx], v[24:27] offset:4096\n\t" "s_waitcnt vmcnt(4)\n\t" \
    "ds_write_b128 %[kx], v[28:31] offset:6144\n\t" "s_waitcnt vmcnt(3)\n\t" "ds_write_b128 %[kx], v[200:203] offset:256\n\t" "s_waitcnt vmcnt(2)\n\t" \
    "ds_write_b128 %[kx], v[204:207] offset:2304\n\t" "s_waitcnt vmcnt(1)\n\t" "ds_write_b128 %[kx], v[208:211] offset:4352\n\t" "s_waitcnt vmcnt(0)\n\t" \
    "ds_write_b128 %[kx], v[212:215] offset:6400\n\t" "s_waitcnt lgkmcnt(0)\n\t" "v_mov_b64 v[208:209], 0\n\t" "v_mov_b64 v[210:211], 0\n\t" "v_mov_b64 v[212:213], 0\n\t" \
    "v_mov_b64 v[214:215], 0\n\t" XT(98, 99) "s_barrier\n\t" XT(82, 83) "buffer_load_dwordx4 v[240:243], %[mo], %[rs], %[sb] offen sc1\n\t" "v_mov_b64 v[224:225], 0\n\t" \
    "v_mov_b64 v[226:227], 0\n\t" "v_mov_b64 v[228:229], 0\n\t" "v_mov_b64 v[230:231], 0\n\t" "v_mov_b64 v[232:233], 0\n\t" "v_mov_b64 v[234:235], 0\n\t" \
    "v_mov_b64 v[236:237], 0\n\t" "v_mov_b64 v[238:239], 0\n\t" "buffer_load_dwordx4 v[244:247], v216, %[rs], %[sb] offen sc1\n\t" \
    "ds_read_b128 v[160:163], %[l0] offset:0\n\t" "ds_read_b128 v[164:167], %[l0] offset:1024\n\t" "ds_read_b128 v[168:171], %[l0] offset:2048\n\t" \
    "ds_read_b128 v[172:175], %[l0] offset:3072\n\t" "s_nop 3\n\t" "buffer_load_dwordx4 v[248:251], %[mo], %[rs], s60 offen sc1\n\t" \
    "ds_read_b128 v[176:179], %[l0] offset:4096\n\t" "ds_read_b128 v[180:183], %[l0] offset:5120\n\t" "ds_read_b128 v[184:187], %[l0] offset:6144\n\t" \
    "ds_read_b128 v[188:191], %[l0] offset:7168\n\t" "s_nop 3\n\t" "buffer_load_dwordx4 v[252:255], v216, %[rs], s60 offen sc1\n\t" "v_mov_b32 v219, 160512\n\t" \
    "v_mov_b64 v[220:221], -1\n\t" "v_mov_b64 v[222:223], -1\n\t" "v_mov_b32 v217, 0\n\t" "buffer_store_dwordx2 v[220:221], %[px], %[rs], %[pr] offen sc1\n\t" \
    "buffer_store_dword v220, %[pm], %[rs], %[pr] offen sc1\n\t" "global_load_dword v217, v217, %[fq] sc1\n\t" "10:\n\t" "s_waitcnt vmcnt(6)\n\t" XT(84, 85) \
    "v_smfmac_f32_16x16x64_bf16 v[224:227], v[240:243], a[0:7], %[ix]\n\t" "ds_write_b128 %[l1], v[240:243] offset:0\n\t" \
    "v_smfmac_f32_16x16x64_bf16 v[228:231], v[240:243], a[8:15], %[ix]\n\t" "v_smfmac_f32_16x16x64_bf16 v[232:235], v[240:243], v[32:39], %[ix]\n\t" \
    "s_waitcnt lgkmcnt(7)\n\t" "v_smfmac_f32_16x16x64_bf16 v[236:239], v[240:243], v[160:167], %[ix]\n\t" "ds_read_b128 v[160:163], %[l0] offset:8192\n\t" \
    "ds_read_b128 v[164:167], %[l0] offset:9216\n\t" "s_waitcnt vmcnt(5)\n\t" "v_smfmac_f32_16x16x64_bf16 v[224:227], v[244:247], a[16:23], %[ix]\n\t" \
    "ds_write_b128 %[l1], v[244:247] offset:1024\n\t" "v_smfmac_f32_16x16x64_bf16 v[228:231], v[244:247], a[24:31], %[ix]\n\t" \
    "v_smfmac_f32_16x16x64_bf16 v[232:235], v[244:247], v[40:47], %[ix]\n\t" "s_waitcnt lgkmcnt(8)\n\t" \
    "v_smfmac_f32_16x16x64_bf16 v[236:239], v[244:247], v[168:175], %[ix]\n\t" "ds_read_b128 v[168:171], %[l0] offset:10240\n\t" \
    "ds_read_b128 v[172:175], %[l0] offset:11264\n\t" "s_waitcnt vmcnt(4)\n\t" "v_smfmac_f32_16x16x64_bf16 v[224:227], v[248:251], a[32:39], %[ix]\n\t" \
    "ds_write_b128 %[l1], v[248:251] offset:2048\n\t" "v_smfmac_f32_16x16x64_bf16 v[228:231], v[248:251], a[40:47], %[ix]\n\t" "s_waitcnt lgkmcnt(3)\n\t" "s_barrier\n\t" \
    XT(86, 87) "v_smfmac_f32_16x16x64_bf16 v[232:235], v[248:251], v[48:55], %[ix]\n\t" "ds_read_b128 v[192:195], %[l1] offset:4096\n\t" \
    "ds_read_b128 v[196:199], %[l1] offset:8192\n\t" "v_smfmac_f32_16x16x64_bf16 v[236:239], v[248:251], v[176:183], %[ix]\n\t" \
    "ds_read_b128 v[176:179], %[l0] offset:12288\n\t" "ds_read_b128 v[180:183], %[l0] offset:13312\n\t" "s_waitcnt vmcnt(3)\n\t" \
    "v_smfmac_f32_16x16x64_bf16 v[224:227], v[252:255], a[48:55], %[ix]\n\t" "ds_write_b128 %[l1], v[252:255] offset:3072\n\t" \
    "v_smfmac_f32_16x16x64_bf16 v[228:231], v[252:255], a[56:63], %[ix]\n\t" "ds_read_b128 v[240:243], %[l1] offset:12288\n\t" \
    "ds_read_b128 v[244:247], %[l1] offset:5120\n\t" "v_smfmac_f32_16x16x64_bf16 v[232:235], v[252:255], v[56:63], %[ix]\n\t" "s_nop 1\n\t" \
    "v_smfmac_f32_16x16x64_bf16 v[236:239], v[252:255], v[184:191], %[ix]\n\t" "ds_read_b128 v[184:187], %[l0] offset:14336\n\t" "v_cmp_u_f32 vcc, v224, v225\n\t" \
    "v_cmp_u_f32 s[62:63], v226, v227\n\t" "s_waitcnt lgkmcnt(7)\n\t" "v_smfmac_f32_16x16x64_bf16 v[224:227], v[192:195], a[64:71], %[ix]\n\t" \
    "ds_read_b128 v[188:191], %[l0] offset:15360\n\t" "v_smfmac_f32_16x16x64_bf16 v[228:231], v[192:195], a[72:79], %[ix]\n\t" "s_or_b64 vcc, vcc, s[62:63]\n\t" \
    "s_cbranch_scc1 11f\n\t" "12:\n\t" "v_smfmac_f32_16x16x64_bf16 v[232:235], v[192:195], v[64:71], %[ix]\n\t" "s_waitcnt lgkmcnt(4)\n\t" "s_barrier\n\t" XT(88, 89) \
    "ds_read_b32 v218, v219\n\t" "v_smfmac_f32_16x16x64_bf16 v[236:239], v[192:195], v[160:167], %[ix]\n\t" "ds_read_b128 v[160:163], %[l0] offset:16384\n\t" \
    "ds_read_b128 v[164:167], %[l0] offset:17408\n\t" "v_smfmac_f32_16x16x64_bf16 v[224:227], v[196:199], a[80:87], %[ix]\n\t" \
    "ds_read_b128 v[248:251], %[l1] offset:9216\n\t" "ds_read_b128 v[252:255], %[l1] offset:13312\n\t" \
    "v_smfmac_f32_16x16x64_bf16 v[228:231], v[196:199], a[88:95], %[ix]\n\t" "ds_read_b128 v[192:195], %[l1] offset:6144\n\t" \
    "v_smfmac_f32_16x16x64_bf16 v[232:235], v[196:199], v[72:79], %[ix]\n\t" "v_smfmac_f32_16x16x64_bf16 v[236:239], v[196:199], v[168:175], %[ix]\n\t" \
    "s_waitcnt lgkmcnt(5)\n\t" "v_cmp_eq_u32 vcc, %[rt], v218\n\t" "v_smfmac_f32_16x16x64_bf16 v[224:227], v[240:243], a[96:103], %[ix]\n\t" "s_cbranch_vccnz 13f\n\t" \
    "ds_read_b128 v[168:171], %[l0] offset:18432\n\t" "v_smfmac_f32_16x16x64_bf16 v[228:231], v[240:243], a[104:111], %[ix]\n\t" \
    "ds_read_b128 v[172:175], %[l0] offset:19456\n\t" "ds_read_b128 v[196:199], %[l1] offset:10240\n\t" \
    "v_smfmac_f32_16x16x64_bf16 v[232:235], v[240:243], v[80:87], %[ix]\n\t" "v_smfmac_f32_16x16x64_bf16 v[236:239], v[240:243], v[176:183], %[ix]\n\t" \
    "ds_read_b128 v[176:179], %[l0] offset:20480\n\t" "ds_read_b128 v[180:183], %[l0] offset:21504\n\t" \
    "v_smfmac_f32_16x16x64_bf16 v[224:227], v[244:247], a[112:119], %[ix]\n\t" "ds_read_b128 v[240:243], %[l1] offset:14336\n\t" \
    "v_smfmac_f32_16x16x64_bf16 v[228:231], v[244:247], a[120:127], %[ix]\n\t" "buffer_load_dword %[pd], %[pa], %[rs], %[kn] offen sc1\n\t" \
    "v_smfmac_f32_16x16x64_bf16 v[232:235], v[244:247], v[88:95], %[ix]\n\t" "v_smfmac_f32_16x16x64_bf16 v[236:239], v[244:247], v[184:191], %[ix]\n\t" \
    "ds_read_b128 v[184:187], %[l0] offset:22528\n\t" "ds_read_b128 v[188:191], %[l0] offset:23552\n\t" "s_waitcnt lgkmcnt(10)\n\t" \
    "v_smfmac_f32_16x16x64_bf16 v[224:227], v[248:251], a[128:135], %[ix]\n\t" "ds_read_b128 v[244:247], %[l1] offset:7168\n\t" \
    "v_smfmac_f32_16x16x64_bf16 v[228:231], v[248:251], a[136:143], %[ix]\n\t" "v_smfmac_f32_16x16x64_bf16 v[232:235], v[248:251], v[96:103], %[ix]\n\t" \
    "v_smfmac_f32_16x16x64_bf16 v[236:239], v[248:251], v[160:167], %[ix]\n\t" "ds_read_b128 v[160:163], %[l0] offset:24576\n\t" \
    "ds_read_b128 v[164:167], %[l0] offset:25600\n\t" "s_waitcnt lgkmcnt(12)\n\t" "v_smfmac_f32_16x16x64_bf16 v[224:227], v[252:255], a[144:151], %[ix]\n\t" \
    "ds_read_b128 v[248:251], %[l1] offset:11264\n\t" "v_smfmac_f32_16x16x64_bf16 v[228:231], v[252:255], a[152:159], %[ix]\n\t" \
    "v_smfmac_f32_16x16x64_bf16 v[232:235], v[252:255], v[104:111], %[ix]\n\t" "s_waitcnt lgkmcnt(10)\n\t" \
    "v_smfmac_f32_16x16x64_bf16 v[236:239], v[252:255], v[168:175], %[ix]\n\t" "ds_read_b128 v[168:171], %[l0] offset:26624\n\t" \
    "ds_read_b128 v[172:175], %[l0] offset:27648\n\t" "v_smfmac_f32_16x16x64_bf16 v[224:227], v[192:195], a[160:167], %[ix]\n\t" \
    "ds_read_b128 v[252:255], %[l1] offset:15360\n\t" "v_smfmac_f32_16x16x64_bf16 v[228:231], v[192:195], a[168:175], %[ix]\n\t" "s_waitcnt lgkmcnt(12)\n\t" \
    "v_smfmac_f32_16x16x64_bf16 v[224:227], v[196:199], a[176:183], %[ix]\n\t" "v_smfmac_f32_16x16x64_bf16 v[228:231], v[196:199], a[184:191], %[ix]\n\t" \
    "s_waitcnt lgkmcnt(9)\n\t" "v_smfmac_f32_16x16x64_bf16 v[224:227], v[240:243], a[192:199], %[ix]\n\t" \
    "v_smfmac_f32_16x16x64_bf16 v[228:231], v[240:243], a[200:207], %[ix]\n\t" "s_waitcnt vmcnt(1)\n\t" "v_readfirstlane_b32 s62, v217\n\t" "s_waitcnt lgkmcnt(6)\n\t" \
    "v_smfmac_f32_16x16x64_bf16 v[224:227], v[244:247], a[208:215], %[ix]\n\t" "v_smfmac_f32_16x16x64_bf16 v[228:231], v[244:247], a[216:223], %[ix]\n\t" \
    "s_max_u32 %[sq], %[sq], s62\n\t" "s_waitcnt lgkmcnt(3)\n\t" "v_smfmac_f32_16x16x64_bf16 v[224:227], v[248:251], a[224:231], %[ix]\n\t" \
    "v_smfmac_f32_16x16x64_bf16 v[228:231], v[248:251], a[232:239], %[ix]\n\t" "s_waitcnt lgkmcnt(0)\n\t" \
    "v_smfmac_f32_16x16x64_bf16 v[224:227], v[252:255], a[240:247], %[ix]\n\t" "s_nop 1\n\t" "v_smfmac_f32_16x16x64_bf16 v[228:231], v[252:255], a[248:255], %[ix]\n\t" \
    XT(90, 91) "s_nop 1\n\t" "v_smfmac_f32_16x16x64_bf16 v[232:235], v[192:195], v[112:119], %[ix]\n\t" "s_nop 1\n\t" \
    "v_smfmac_f32_16x16x64_bf16 v[236:239], v[192:195], v[176:183], %[ix]\n\t" "ds_read_b128 v[176:179], %[l0] offset:28672\n\t" "v_add_f32 v224, v224, v225\n\t" \
    "v_add_f32 v226, v226, v227\n\t" "v_smfmac_f32_16x16x64_bf16 v[232:235], v[196:199], v[120:127], %[ix]\n\t" "ds_read_b128 v[180:183], %[l0] offset:29696\n\t" \
    "v_add_f32 v228, v228, v229\n\t" "v_add_f32 v230, v230, v231\n\t" "v_smfmac_f32_16x16x64_bf16 v[236:239], v[196:199], v[184:191], %[ix]\n\t" \
    "ds_read_b128 v[184:187], %[l0] offset:30720\n\t" "v_cvt_pk_bf16_f32 v218, v224, v228\n\t" "v_cvt_pk_bf16_f32 v219, v226, v230\n\t" \
    "v_smfmac_f32_16x16x64_bf16 v[232:235], v[240:243], v[128:135], %[ix]\n\t" "ds_write2_b32 %[qw], v218, v219 offset1:8\n\t" \
    "ds_read_b128 v[188:191], %[l0] offset:31744\n\t" "v_smfmac_f32_16x16x64_bf16 v[236:239], v[240:243], v[160:167], %[ix]\n\t" \
    "ds_read_b128 v[192:195], %[kx] offset:4352\n\t" "ds_read_b128 v[196:199], %[kx] offset:6400\n\t" \
    "v_smfmac_f32_16x16x64_bf16 v[232:235], v[244:247], v[136:143], %[ix]\n\t" "v_smfmac_f32_16x16x64_bf16 v[236:239], v[244:247], v[168:175], %[ix]\n\t" \
    "s_waitcnt lgkmcnt(3)\n\t" "s_barrier\n\t" "v_smfmac_f32_16x16x64_bf16 v[232:235], v[248:251], v[144:151], %[ix]\n\t" "ds_read_b128 v[216:219], %[l1] offset:24576\n\t" \
    "ds_read_b128 v[220:223], %[l1] offset:25600\n\t" "v_smfmac_f32_16x16x64_bf16 v[236:239], v[248:251], v[176:183], %[ix]\n\t" \
    "v_smfmac_f32_16x16x64_bf16 v[232:235], v[252:255], v[152:159], %[ix]\n\t" "s_waitcnt lgkmcnt(4)\n\t" \
    "v_smfmac_f32_16x16x64_bf16 v[236:239], v[252:255], v[184:191], %[ix]\n\t" XT(92, 93) "s_waitcnt lgkmcnt(1)\n\t" \
    "v_smfmac_f32_16x16x64_bf16 v[208:211], v[216:219], v[16:23], %[ix]\n\t" "s_nop 1\n\t" "v_smfmac_f32_16x16x64_bf16 v[212:215], v[216:219], v[200:207], %[ix]\n\t" \
    "s_waitcnt lgkmcnt(0)\n\t" "s_nop 0\n\t" "v_add_f32 v232, v232, v233\n\t" "v_add_f32 v234, v234, v235\n\t" "v_add_f32 v236, v236, v237\n\t" \
    "v_add_f32 v238, v238, v239\n\t" "v_cvt_pk_bf16_f32 v232, v232, v236\n\t" "v_cvt_pk_bf16_f32 v233, v234, v238\n\t" "ds_write2_b32 %[kw], v232, v233 offset1:68\n\t" \
    "v_smfmac_f32_16x16x64_bf16 v[208:211], v[220:223], v[24:31], %[ix]\n\t" "v_smfmac_f32_16x16x64_bf16 v[212:215], v[220:223], v[192:199], %[ix]\n\t" \
    "v_mov_b64 v[224:225], 0\n\t" "v_mov_b64 v[226:227], 0\n\t" "s_waitcnt lgkmcnt(0)\n\t" "s_barrier\n\t" XT(94, 95) "ds_read_b128 v[160:163], %[km] offset:0\n\t" \
    "ds_read_b128 v[164:167], %[km] offset:64\n\t" "ds_read_b128 v[168:171], %[km] offset:128\n\t" "ds_read_b128 v[172:175], %[km] offset:192\n\t" \
    "v_add_f32 v178, v208, v209\n\t" "v_add_f32 v179, v210, v211\n\t" "v_add_f32 v180, v212, v213\n\t" "v_add_f32 v181, v214, v215\n\t" \
    "v_pk_mul_f32 v[178:179], v[178:179], s[66:67]\n\t" "v_pk_mul_f32 v[180:181], v[180:181], s[66:67]\n\t" "v_max_f32 v182, v178, v180\n\t" "v_max_f32 v183, v179, v181\n\t" \
    "s_nop 0\n\t" "v_max_f32_dpp v182, v182, v182 row_ror:8 row_mask:0xf bank_mask:0xf\n\t" "v_max_f32_dpp v183, v183, v183 row_ror:8 row_mask:0xf bank_mask:0xf\n\t" \
    "s_nop 0\n\t" "s_waitcnt lgkmcnt(2)\n\t" "v_smfmac_f32_16x16x64_bf16 v[224:227], v[216:219], v[160:167], %[ix]\n\t" "s_waitcnt lgkmcnt(0)\n\t" \
    "v_smfmac_f32_16x16x64_bf16 v[224:227], v[220:223], v[168:175], %[ix]\n\t" "s_nop 7\n\t" "v_add_f32 v176, v224, v225\n\t" "v_add_f32 v177, v226, v227\n\t" \
    "v_mul_f32 v176, s66, v176\n\t" "v_mul_f32 v177, s66, v177\n\t" "v_max_f32 v182, v176, v182\n\t" "v_max_f32 v183, v177, v183\n\t" "s_nop 0\n\t" \
    "v_max_f32_dpp v182, v182, v182 row_ror:4 row_mask:0xf bank_mask:0xf\n\t" "v_max_f32_dpp v183, v183, v183 row_ror:4 row_mask:0xf bank_mask:0xf\n\t" "s_nop 0\n\t" \
    "v_max_f32_dpp v182, v182, v182 row_ror:2 row_mask:0xf bank_mask:0xf\n\t" "v_max_f32_dpp v183, v183, v183 row_ror:2 row_mask:0xf bank_mask:0xf\n\t" "s_nop 0\n\t" \
    "v_max_f32_dpp v182, v182, v182 row_ror:1 row_mask:0xf bank_mask:0xf\n\t" "v_max_f32_dpp v183, v183, v183 row_ror:1 row_mask:0xf bank_mask:0xf\n\t" \
    "v_pk_add_f32 v[178:179], v[178:179], v[182:183] neg_lo:[0,1] neg_hi:[0,1]\n\t" "v_pk_add_f32 v[180:181], v[180:181], v[182:183] neg_lo:[0,1] neg_hi:[0,1]\n\t" \
    "v_pk_add_f32 v[176:177], v[176:177], v[182:183] neg_lo:[0,1] neg_hi:[0,1]\n\t" "v_exp_f32 v178, v178\n\t" "v_exp_f32 v179, v179\n\t" "v_exp_f32 v180, v180\n\t" \
    "v_exp_f32 v181, v181\n\t" "v_exp_f32 v176, v176\n\t" "v_pk_add_f32 v[182:183], v[178:179], v[180:181]\n\t" "v_exp_f32 v177, v177\n\t" "s_nop 0\n\t" \
    "v_add_f32_dpp v182, v182, v182 row_ror:8 row_mask:0xf bank_mask:0xf\n\t" "v_add_f32_dpp v183, v183, v183 row_ror:8 row_mask:0xf bank_mask:0xf\n\t" \
    "v_add_f32 v182, v182, v176\n\t" "v_add_f32 v183, v183, v177\n\t" "s_nop 0\n\t" "v_add_f32_dpp v182, v182, v182 row_ror:4 row_mask:0xf bank_mask:0xf\n\t" \
    "v_add_f32_dpp v183, v183, v183 row_ror:4 row_mask:0xf bank_mask:0xf\n\t" "s_nop 0\n\t" "v_add_f32_dpp v182, v182, v182 row_ror:2 row_mask:0xf bank_mask:0xf\n\t" \
    "v_add_f32_dpp v183, v183, v183 row_ror:2 row_mask:0xf bank_mask:0xf\n\t" "s_nop 0\n\t" "v_add_f32_dpp v182, v182, v182 row_ror:1 row_mask:0xf bank_mask:0xf\n\t" \
    "v_add_f32_dpp v183, v183, v183 row_ror:1 row_mask:0xf bank_mask:0xf\n\t" "v_rcp_f32 v182, v182\n\t" "v_rcp_f32 v183, v183\n\t" "s_nop 0\n\t" \
    "v_pk_mul_f32 v[176:177], v[176:177], v[182:183]\n\t" "v_pk_mul_f32 v[178:179], v[178:179], v[182:183]\n\t" "v_pk_mul_f32 v[180:181], v[180:181], v[182:183]\n\t" \
    "v_cvt_pk_bf16_f32 v176, v176, v177\n\t" "v_cvt_pk_bf16_f32 v178, v178, v179\n\t" "v_cvt_pk_bf16_f32 v180, v180, v181\n\t" \
    "v_mov_b32_dpp v177, v176 quad_perm:[1,0,3,2] row_mask:0xf bank_mask:0xf\n\t" "v_mov_b32_dpp v179, v178 quad_perm:[1,0,3,2] row_mask:0xf bank_mask:0xf\n\t" \
    "v_mov_b32_dpp v181, v180 quad_perm:[1,0,3,2] row_mask:0xf bank_mask:0xf\n\t" "v_perm_b32 v184, v179, v178, %[ps]\n\t" "v_perm_b32 v185, v181, v180, %[ps]\n\t" \
    "buffer_store_dwordx2 v[184:185], %[px], %[rs], %[pb] offen sc1\n\t" "v_perm_b32 v186, v177, v176, %[ps]\n\t" "buffer_store_dword v186, %[pm], %[rs], %[pb] offen sc1\n\t" \
    XT(96, 97) LMARK "s_waitcnt vmcnt(2)\n\t" "s_branch 99f\n\t" "11:\n\t" "ds_read_b128 v[220:223], %[l1] offset:0\n\t" "s_waitcnt lgkmcnt(0)\n\t" \
    "v_max3_u32 v218, v220, v221, v222\n\t" "v_max_u32 v218, v218, v223\n\t" "ds_read_b128 v[220:223], %[l1] offset:1024\n\t" "s_waitcnt lgkmcnt(0)\n\t" \
    "v_max3_u32 v218, v218, v220, v221\n\t" "v_max3_u32 v218, v218, v222, v223\n\t" "ds_read_b128 v[220:223], %[l1] offset:2048\n\t" "s_waitcnt lgkmcnt(0)\n\t" \
    "v_max3_u32 v218, v218, v220, v221\n\t" "v_max3_u32 v218, v218, v222, v223\n\t" "ds_read_b128 v[220:223], %[l1] offset:3072\n\t" "s_waitcnt lgkmcnt(0)\n\t" \
    "v_max3_u32 v218, v218, v220, v221\n\t" "v_max3_u32 v218, v218, v222, v223\n\t" "v_cmp_eq_u32 vcc, -1, v218\n\t" "s_cbranch_vccz 12b\n\t" "s_cmp_eq_u32 %[rt], 1\n\t" \
    "s_cbranch_scc1 12b\n\t" "v_mov_b32 v218, %[rt]\n\t" "ds_write_b32 v219, v218\n\t" "s_waitcnt lgkmcnt(0)\n\t" "s_branch 12b\n\t" "13:\n\t" "s_sub_u32 %[rt], %[rt], 1\n\t" \
    "s_waitcnt lgkmcnt(0)\n\t" XCNT(100) "14:\n\t" "buffer_load_dwordx4 v[240:243], %[mo], %[rs], %[sb] offen sc1\n\t" \
    "buffer_load_dwordx4 v[244:247], v216, %[rs], %[sb] offen sc1\n\t" "buffer_load_dwordx4 v[248:251], %[mo], %[rs], s60 offen sc1\n\t" \
    "buffer_load_dwordx4 v[252:255], v216, %[rs], s60 offen sc1\n\t" "s_waitcnt vmcnt(0)\n\t" "v_max_u32 v218, v240, v241\n\t" "v_max3_u32 v218, v218, v242, v243\n\t" \
    "v_max3_u32 v218, v218, v244, v245\n\t" "v_max3_u32 v218, v218, v246, v247\n\t" "v_max3_u32 v218, v218, v248, v249\n\t" "v_max3_u32 v218, v218, v250, v251\n\t" \
    "v_max3_u32 v218, v218, v252, v253\n\t" "v_max3_u32 v218, v218, v254, v255\n\t" "v_cmp_eq_u32 vcc, -1, v218\n\t" "s_cbranch_vccz 15f\n\t" "s_cmp_eq_u32 %[bd], 0\n\t" \
    "s_cbranch_scc1 15f\n\t" "s_sub_u32 %[bd], %[bd], 1\n\t" XCNT(101) "s_branch 14b\n\t" "15:\n\t" "ds_read_b128 v[160:163], %[l0] offset:0\n\t" \
    "ds_read_b128 v[164:167], %[l0] offset:1024\n\t" "ds_read_b128 v[168:171], %[l0] offset:2048\n\t" "ds_read_b128 v[172:175], %[l0] offset:3072\n\t" \
    "ds_read_b128 v[176:179], %[l0] offset:4096\n\t" "ds_read_b128 v[180:183], %[l0] offset:5120\n\t" "ds_read_b128 v[184:187], %[l0] offset:6144\n\t" \
    "ds_read_b128 v[188:191], %[l0] offset:7168\n\t" "v_mov_b64 v[224:225], 0\n\t" "v_mov_b64 v[226:227], 0\n\t" "v_mov_b64 v[228:229], 0\n\t" "v_mov_b64 v[230:231], 0\n\t" \
    "v_mov_b64 v[232:233], 0\n\t" "v_mov_b64 v[234:235], 0\n\t" "v_mov_b64 v[236:237], 0\n\t" "v_mov_b64 v[238:239], 0\n\t" "s_barrier\n\t" "s_branch 10b\n\t" "99:\n\t"
#define QK_STEP1 \
    ".p2align 6\n\t" "v_xor_b32 v216, 0x400, %[mo]\n\t" "s_xor_b32 s60, %[sb], 0x800\n\t" "s_add_u32 s61, %[qb], 0x1000\n\t" "s_mov_b32 s66, 0x3e0293ee\n\t" \
    "s_mov_b32 s67, 0x3e0293ee\n\t" XT(80, 81) XT(98, 99) "s_barrier\n\t" XT(82, 83) "buffer_load_dwordx4 v[240:243], %[mo], %[rs], %[sb] offen sc1\n\t" \
    "v_mov_b64 v[224:225], 0\n\t" "v_mov_b64 v[226:227], 0\n\t" "v_mov_b64 v[228:229], 0\n\t" "v_mov_b64 v[230:231], 0\n\t" "v_mov_b64 v[232:233], 0\n\t" \
    "v_mov_b64 v[234:235], 0\n\t" "v_mov_b64 v[236:237], 0\n\t" "v_mov_b64 v[238:239], 0\n\t" "buffer_load_dwordx4 v[244:247], v216, %[rs], %[sb] offen sc1\n\t" \
    "ds_read_b128 v[160:163], %[l0] offset:0\n\t" "ds_read_b128 v[164:167], %[l0] offset:1024\n\t" "ds_read_b128 v[168:171], %[l0] offset:2048\n\t" \
    "ds_read_b128 v[172:175], %[l0] offset:3072\n\t" "s_nop 3\n\t" "buffer_load_dwordx4 v[248:251], %[mo], %[rs], s60 offen sc1\n\t" \
    "ds_read_b128 v[176:179], %[l0] offset:4096\n\t" "ds_read_b128 v[180:183], %[l0] offset:5120\n\t" "ds_read_b128 v[184:187], %[l0] offset:6144\n\t" \
    "ds_read_b128 v[188:191], %[l0] offset:7168\n\t" "s_nop 3\n\t" "buffer_load_dwordx4 v[252:255], v216, %[rs], s60 offen sc1\n\t" "v_mov_b32 v219, 160512\n\t" \
    "v_mov_b64 v[220:221], -1\n\t" "v_mov_b64 v[222:223], -1\n\t" "buffer_store_dwordx2 v[220:221], %[px], %[rs], %[tr] offen sc1\n\t" \
    "buffer_store_dwordx2 v[220:221], %[pm], %[rs], %[tr] offen sc1\n\t" "v_mov_b32 v208, 0xd028965f\n\t" "v_mov_b32 v209, 0xff800000\n\t" "s_mov_b32 s62, 0\n\t" \
    "s_mov_b32 s63, -1\n\t" "v_mov_b32 v210, 0\n\t" "v_cmp_gt_i32 vcc, 0, %[ps]\n\t" "v_cndmask_b32 v204, 0, v209, s[62:63]\n\t" "s_nop 0\n\t" \
    "v_cndmask_b32 v200, 0, v208, vcc\n\t" "v_cmp_gt_i32 vcc, 1, %[ps]\n\t" "v_cndmask_b32 v205, 0, v209, s[62:63]\n\t" "s_nop 0\n\t" "v_cndmask_b32 v201, 0, v208, vcc\n\t" \
    "v_cmp_gt_i32 vcc, 2, %[ps]\n\t" "v_cndmask_b32 v206, 0, v209, s[62:63]\n\t" "s_nop 0\n\t" "v_cndmask_b32 v202, 0, v208, vcc\n\t" "v_cmp_gt_i32 vcc, 3, %[ps]\n\t" \
    "v_cndmask_b32 v207, 0, v209, s[62:63]\n\t" "s_nop 0\n\t" "v_cndmask_b32 v203, 0, v208, vcc\n\t" "20:\n\t" "s_waitcnt vmcnt(5)\n\t" XT(84, 85) \
    "v_smfmac_f32_16x16x64_bf16 v[224:227], v[240:243], a[0:7], %[ix]\n\t" "ds_write_b128 %[l1], v[240:243] offset:4096\n\t" \
    "v_smfmac_f32_16x16x64_bf16 v[228:231], v[240:243], a[8:15], %[ix]\n\t" "v_smfmac_f32_16x16x64_bf16 v[232:235], v[240:243], v[32:39], %[ix]\n\t" \
    "s_waitcnt lgkmcnt(7)\n\t" "v_smfmac_f32_16x16x64_bf16 v[236:239], v[240:243], v[160:167], %[ix]\n\t" "ds_read_b128 v[160:163], %[l0] offset:8192\n\t" \
    "ds_read_b128 v[164:167], %[l0] offset:9216\n\t" "s_waitcnt vmcnt(4)\n\t" "v_smfmac_f32_16x16x64_bf16 v[224:227], v[244:247], a[16:23], %[ix]\n\t" \
    "ds_write_b128 %[l1], v[244:247] offset:5120\n\t" "v_smfmac_f32_16x16x64_bf16 v[228:231], v[244:247], a[24:31], %[ix]\n\t" \
    "v_smfmac_f32_16x16x64_bf16 v[232:235], v[244:247], v[40:47], %[ix]\n\t" "s_waitcnt lgkmcnt(8)\n\t" \
    "v_smfmac_f32_16x16x64_bf16 v[236:239], v[244:247], v[168:175], %[ix]\n\t" "ds_read_b128 v[168:171], %[l0] offset:10240\n\t" \
    "ds_read_b128 v[172:175], %[l0] offset:11264\n\t" "s_waitcnt vmcnt(3)\n\t" "v_smfmac_f32_16x16x64_bf16 v[224:227], v[248:251], a[32:39], %[ix]\n\t" \
    "ds_write_b128 %[l1], v[248:251] offset:6144\n\t" "v_smfmac_f32_16x16x64_bf16 v[228:231], v[248:251], a[40:47], %[ix]\n\t" "s_waitcnt lgkmcnt(3)\n\t" "s_barrier\n\t" \
    XT(86, 87) "v_smfmac_f32_16x16x64_bf16 v[232:235], v[248:251], v[48:55], %[ix]\n\t" "ds_read_b128 v[192:195], %[l1] offset:8192\n\t" \
    "ds_read_b128 v[196:199], %[l1] offset:12288\n\t" "v_smfmac_f32_16x16x64_bf16 v[236:239], v[248:251], v[176:183], %[ix]\n\t" \
    "ds_read_b128 v[176:179], %[l0] offset:12288\n\t" "ds_read_b128 v[180:183], %[l0] offset:13312\n\t" "s_waitcnt vmcnt(2)\n\t" \
    "v_smfmac_f32_16x16x64_bf16 v[224:227], v[252:255], a[48:55], %[ix]\n\t" "ds_write_b128 %[l1], v[252:255] offset:7168\n\t" \
    "v_smfmac_f32_16x16x64_bf16 v[228:231], v[252:255], a[56:63], %[ix]\n\t" "ds_read_b128 v[240:243], %[l1] offset:0\n\t" "ds_read_b128 v[244:247], %[l1] offset:9216\n\t" \
    "v_smfmac_f32_16x16x64_bf16 v[232:235], v[252:255], v[56:63], %[ix]\n\t" "s_nop 1\n\t" "v_smfmac_f32_16x16x64_bf16 v[236:239], v[252:255], v[184:191], %[ix]\n\t" \
    "ds_read_b128 v[184:187], %[l0] offset:14336\n\t" "v_cmp_u_f32 vcc, v224, v225\n\t" "v_cmp_u_f32 s[62:63], v226, v227\n\t" "s_waitcnt lgkmcnt(7)\n\t" \
    "v_smfmac_f32_16x16x64_bf16 v[224:227], v[192:195], a[64:71], %[ix]\n\t" "ds_read_b128 v[188:191], %[l0] offset:15360\n\t" \
    "v_smfmac_f32_16x16x64_bf16 v[228:231], v[192:195], a[72:79], %[ix]\n\t" "s_or_b64 vcc, vcc, s[62:63]\n\t" "s_cbranch_scc1 21f\n\t" "22:\n\t" \
    "v_smfmac_f32_16x16x64_bf16 v[232:235], v[192:195], v[64:71], %[ix]\n\t" "s_waitcnt lgkmcnt(4)\n\t" "s_barrier\n\t" XT(88, 89) "ds_read_b32 v218, v219\n\t" \
    "v_smfmac_f32_16x16x64_bf16 v[236:239], v[192:195], v[160:167], %[ix]\n\t" "ds_read_b128 v[160:163], %[l0] offset:16384\n\t" \
    "ds_read_b128 v[164:167], %[l0] offset:17408\n\t" "v_smfmac_f32_16x16x64_bf16 v[224:227], v[196:199], a[80:87], %[ix]\n\t" \
    "ds_read_b128 v[248:251], %[l1] offset:13312\n\t" "ds_read_b128 v[252:255], %[l1] offset:1024\n\t" \
    "v_smfmac_f32_16x16x64_bf16 v[228:231], v[196:199], a[88:95], %[ix]\n\t" "ds_read_b128 v[192:195], %[l1] offset:10240\n\t" \
    "v_smfmac_f32_16x16x64_bf16 v[232:235], v[196:199], v[72:79], %[ix]\n\t" "v_smfmac_f32_16x16x64_bf16 v[236:239], v[196:199], v[168:175], %[ix]\n\t" \
    "s_waitcnt lgkmcnt(5)\n\t" "v_cmp_eq_u32 vcc, %[rt], v218\n\t" "v_smfmac_f32_16x16x64_bf16 v[224:227], v[240:243], a[96:103], %[ix]\n\t" "s_cbranch_vccnz 23f\n\t" \
    "ds_read_b128 v[168:171], %[l0] offset:18432\n\t" "v_smfmac_f32_16x16x64_bf16 v[228:231], v[240:243], a[104:111], %[ix]\n\t" \
    "ds_read_b128 v[172:175], %[l0] offset:19456\n\t" "ds_read_b128 v[196:199], %[l1] offset:14336\n\t" \
    "v_smfmac_f32_16x16x64_bf16 v[232:235], v[240:243], v[80:87], %[ix]\n\t" "buffer_load_dwordx4 v[16:19], %[hq], %[rs], %[qb] offen offset:0 sc1\n\t" \
    "v_smfmac_f32_16x16x64_bf16 v[236:239], v[240:243], v[176:183], %[ix]\n\t" "ds_read_b128 v[176:179], %[l0] offset:20480\n\t" \
    "ds_read_b128 v[180:183], %[l0] offset:21504\n\t" "v_smfmac_f32_16x16x64_bf16 v[224:227], v[244:247], a[112:119], %[ix]\n\t" \
    "ds_read_b128 v[240:243], %[l1] offset:2048\n\t" "v_smfmac_f32_16x16x64_bf16 v[228:231], v[244:247], a[120:127], %[ix]\n\t" \
    "v_smfmac_f32_16x16x64_bf16 v[232:235], v[244:247], v[88:95], %[ix]\n\t" "buffer_load_dwordx4 v[20:23], %[hq], %[rs], %[qb] offen offset:2048 sc1\n\t" \
    "v_smfmac_f32_16x16x64_bf16 v[236:239], v[244:247], v[184:191], %[ix]\n\t" "ds_read_b128 v[184:187], %[l0] offset:22528\n\t" \
    "ds_read_b128 v[188:191], %[l0] offset:23552\n\t" "s_waitcnt lgkmcnt(10)\n\t" "v_smfmac_f32_16x16x64_bf16 v[224:227], v[248:251], a[128:135], %[ix]\n\t" \
    "ds_read_b128 v[244:247], %[l1] offset:11264\n\t" "v_smfmac_f32_16x16x64_bf16 v[228:231], v[248:251], a[136:143], %[ix]\n\t" \
    "v_smfmac_f32_16x16x64_bf16 v[232:235], v[248:251], v[96:103], %[ix]\n\t" "buffer_load_dwordx4 v[24:27], %[hq], %[rs], s61 offen offset:0 sc1\n\t" \
    "v_smfmac_f32_16x16x64_bf16 v[236:239], v[248:251], v[160:167], %[ix]\n\t" "ds_read_b128 v[160:163], %[l0] offset:24576\n\t" \
    "ds_read_b128 v[164:167], %[l0] offset:25600\n\t" "s_waitcnt lgkmcnt(12)\n\t" "v_smfmac_f32_16x16x64_bf16 v[224:227], v[252:255], a[144:151], %[ix]\n\t" \
    "ds_read_b128 v[248:251], %[l1] offset:15360\n\t" "v_smfmac_f32_16x16x64_bf16 v[228:231], v[252:255], a[152:159], %[ix]\n\t" \
    "v_smfmac_f32_16x16x64_bf16 v[232:235], v[252:255], v[104:111], %[ix]\n\t" "buffer_load_dwordx4 v[28:31], %[hq], %[rs], s61 offen offset:2048 sc1\n\t" \
    "s_waitcnt lgkmcnt(10)\n\t" "v_smfmac_f32_16x16x64_bf16 v[236:239], v[252:255], v[168:175], %[ix]\n\t" "ds_read_b128 v[168:171], %[l0] offset:26624\n\t" \
    "ds_read_b128 v[172:175], %[l0] offset:27648\n\t" "v_smfmac_f32_16x16x64_bf16 v[224:227], v[192:195], a[160:167], %[ix]\n\t" \
    "ds_read_b128 v[252:255], %[l1] offset:3072\n\t" "v_smfmac_f32_16x16x64_bf16 v[228:231], v[192:195], a[168:175], %[ix]\n\t" "s_waitcnt lgkmcnt(12)\n\t" \
    "v_smfmac_f32_16x16x64_bf16 v[224:227], v[196:199], a[176:183], %[ix]\n\t" "v_smfmac_f32_16x16x64_bf16 v[228:231], v[196:199], a[184:191], %[ix]\n\t" \
    "s_waitcnt lgkmcnt(9)\n\t" "v_smfmac_f32_16x16x64_bf16 v[224:227], v[240:243], a[192:199], %[ix]\n\t" \
    "v_smfmac_f32_16x16x64_bf16 v[228:231], v[240:243], a[200:207], %[ix]\n\t" "s_waitcnt lgkmcnt(6)\n\t" \
    "v_smfmac_f32_16x16x64_bf16 v[224:227], v[244:247], a[208:215], %[ix]\n\t" "v_smfmac_f32_16x16x64_bf16 v[228:231], v[244:247], a[216:223], %[ix]\n\t" \
    "s_waitcnt vmcnt(4)\n\t" "s_waitcnt lgkmcnt(3)\n\t" "v_smfmac_f32_16x16x64_bf16 v[224:227], v[248:251], a[224:231], %[ix]\n\t" \
    "v_smfmac_f32_16x16x64_bf16 v[228:231], v[248:251], a[232:239], %[ix]\n\t" "s_waitcnt lgkmcnt(0)\n\t" \
    "v_smfmac_f32_16x16x64_bf16 v[224:227], v[252:255], a[240:247], %[ix]\n\t" "s_nop 1\n\t" "v_smfmac_f32_16x16x64_bf16 v[228:231], v[252:255], a[248:255], %[ix]\n\t" \
    XT(90, 91) "s_nop 1\n\t" "v_smfmac_f32_16x16x64_bf16 v[232:235], v[192:195], v[112:119], %[ix]\n\t" "s_nop 1\n\t" \
    "v_smfmac_f32_16x16x64_bf16 v[236:239], v[192:195], v[176:183], %[ix]\n\t" "ds_read_b128 v[176:179], %[l0] offset:28672\n\t" "v_add_f32 v224, v224, v225\n\t" \
    "v_add_f32 v226, v226, v227\n\t" "v_smfmac_f32_16x16x64_bf16 v[232:235], v[196:199], v[120:127], %[ix]\n\t" "ds_read_b128 v[180:183], %[l0] offset:29696\n\t" \
    "v_add_f32 v228, v228, v229\n\t" "v_add_f32 v230, v230, v231\n\t" "v_smfmac_f32_16x16x64_bf16 v[236:239], v[196:199], v[184:191], %[ix]\n\t" \
    "ds_read_b128 v[184:187], %[l0] offset:30720\n\t" "v_cvt_pk_bf16_f32 v218, v224, v228\n\t" "v_cvt_pk_bf16_f32 v219, v226, v230\n\t" \
    "v_smfmac_f32_16x16x64_bf16 v[232:235], v[240:243], v[128:135], %[ix]\n\t" "ds_write2_b32 %[qw], v218, v219 offset1:8\n\t" \
    "ds_read_b128 v[188:191], %[l0] offset:31744\n\t" "v_smfmac_f32_16x16x64_bf16 v[236:239], v[240:243], v[160:167], %[ix]\n\t" \
    "v_smfmac_f32_16x16x64_bf16 v[232:235], v[244:247], v[136:143], %[ix]\n\t" "v_smfmac_f32_16x16x64_bf16 v[236:239], v[244:247], v[168:175], %[ix]\n\t" \
    "s_waitcnt lgkmcnt(1)\n\t" "s_barrier\n\t" "v_smfmac_f32_16x16x64_bf16 v[232:235], v[248:251], v[144:151], %[ix]\n\t" \
    "v_smfmac_f32_16x16x64_bf16 v[236:239], v[248:251], v[176:183], %[ix]\n\t" "v_smfmac_f32_16x16x64_bf16 v[232:235], v[252:255], v[152:159], %[ix]\n\t" \
    "s_waitcnt lgkmcnt(0)\n\t" "v_smfmac_f32_16x16x64_bf16 v[236:239], v[252:255], v[184:191], %[ix]\n\t" XT(92, 93) "s_nop 6\n\t" "v_add_f32 v232, v232, v233\n\t" \
    "v_add_f32 v234, v234, v235\n\t" "v_add_f32 v236, v236, v237\n\t" "v_add_f32 v238, v238, v239\n\t" "v_cvt_pk_bf16_f32 v232, v232, v236\n\t" \
    "v_cvt_pk_bf16_f32 v233, v234, v238\n\t" "ds_write2_b32 %[kw], v232, v233 offset1:68\n\t" "s_waitcnt lgkmcnt(0)\n\t" "s_barrier\n\t" XT(94, 95) \
    "s_cmp_ge_u32 %[sq], %[sn]\n\t" "s_cbranch_scc1 277f\n\t" "275:\n\t" "global_load_dword v240, v210, %[fq] sc1\n\t" "s_waitcnt vmcnt(0)\n\t" \
    "v_readfirstlane_b32 %[sq], v240\n\t" "s_nop 3\n\t" "s_cmp_ge_u32 %[sq], %[sn]\n\t" "s_cbranch_scc1 276f\n\t" "s_cmp_eq_u32 %[bd], 0\n\t" "s_cbranch_scc1 276f\n\t" \
    "s_sub_u32 %[bd], %[bd], 1\n\t" "s_sleep 2\n\t" "s_branch 275b\n\t" "276:\n\t" "buffer_load_dwordx4 v[16:19], %[hq], %[rs], %[qb] offen offset:0 sc1\n\t" \
    "buffer_load_dwordx4 v[20:23], %[hq], %[rs], %[qb] offen offset:2048 sc1\n\t" "buffer_load_dwordx4 v[24:27], %[hq], %[rs], s61 offen offset:0 sc1\n\t" \
    "buffer_load_dwordx4 v[28:31], %[hq], %[rs], s61 offen offset:2048 sc1\n\t" "277:\n\t" "s_nop 3\n\t" "ds_read_b128 v[160:163], %[kx] offset:0\n\t" \
    "ds_read_b128 v[164:167], %[kx] offset:2048\n\t" "ds_read_b128 v[168:171], %[kx] offset:4096\n\t" "ds_read_b128 v[172:175], %[kx] offset:6144\n\t" \
    "ds_read_b128 v[224:227], %[km] offset:0\n\t" "ds_read_b128 v[228:231], %[km] offset:64\n\t" "ds_read_b128 v[232:235], %[km] offset:128\n\t" \
    "ds_read_b128 v[236:239], %[km] offset:192\n\t" "s_waitcnt vmcnt(0)\n\t" "global_load_dword v240, v210, %[fq] sc1\n\t" "s_waitcnt lgkmcnt(7)\n\t" \
    "v_mfma_f32_16x16x32_bf16 v[248:251], v[160:163], v[16:19], v[200:203]\n\t" "s_waitcnt lgkmcnt(6)\n\t" \
    "v_mfma_f32_16x16x32_bf16 v[248:251], v[164:167], v[20:23], v[248:251]\n\t" "s_waitcnt lgkmcnt(5)\n\t" \
    "v_mfma_f32_16x16x32_bf16 v[248:251], v[168:171], v[24:27], v[248:251]\n\t" "s_waitcnt lgkmcnt(4)\n\t" \
    "v_mfma_f32_16x16x32_bf16 v[248:251], v[172:175], v[28:31], v[248:251]\n\t" "s_waitcnt lgkmcnt(3)\n\t" \
    "v_mfma_f32_16x16x32_bf16 v[244:247], v[224:227], v[16:19], v[204:207]\n\t" "s_waitcnt lgkmcnt(2)\n\t" \
    "v_mfma_f32_16x16x32_bf16 v[244:247], v[228:231], v[20:23], v[244:247]\n\t" "s_waitcnt lgkmcnt(1)\n\t" \
    "v_mfma_f32_16x16x32_bf16 v[244:247], v[232:235], v[24:27], v[244:247]\n\t" "s_waitcnt lgkmcnt(0)\n\t" \
    "v_mfma_f32_16x16x32_bf16 v[244:247], v[236:239], v[28:31], v[244:247]\n\t" "s_nop 3\n\t" "v_max3_f32 v242, v248, v249, v250\n\t" "v_max_f32 v242, v242, v251\n\t" \
    "s_nop 1\n\t" "v_max3_f32 v242, v242, v244, v245\n\t" "v_max3_f32 v242, v242, v246, v247\n\t" "v_mov_b32 v243, v242\n\t" "s_nop 1\n\t" \
    "v_permlane32_swap_b32 v242, v243\n\t" "v_max_f32 v242, v242, v243\n\t" "v_mov_b32 v243, v242\n\t" "s_nop 1\n\t" "v_permlane16_swap_b32 v242, v243\n\t" \
    "v_max_f32 v242, v242, v243\n\t" "v_pk_add_f32 v[244:245], v[244:245], v[242:243] op_sel_hi:[1,0] neg_lo:[0,1] neg_hi:[0,1]\n\t" \
    "v_pk_add_f32 v[246:247], v[246:247], v[242:243] op_sel_hi:[1,0] neg_lo:[0,1] neg_hi:[0,1]\n\t" \
    "v_pk_add_f32 v[248:249], v[248:249], v[242:243] op_sel_hi:[1,0] neg_lo:[0,1] neg_hi:[0,1]\n\t" \
    "v_pk_add_f32 v[250:251], v[250:251], v[242:243] op_sel_hi:[1,0] neg_lo:[0,1] neg_hi:[0,1]\n\t" "v_pk_mul_f32 v[244:245], v[244:245], s[66:67]\n\t" \
    "v_pk_mul_f32 v[246:247], v[246:247], s[66:67]\n\t" "v_pk_mul_f32 v[248:249], v[248:249], s[66:67]\n\t" "v_pk_mul_f32 v[250:251], v[250:251], s[66:67]\n\t" \
    "v_exp_f32 v244, v244\n\t" "v_exp_f32 v245, v245\n\t" "v_exp_f32 v246, v246\n\t" "v_exp_f32 v247, v247\n\t" "v_exp_f32 v248, v248\n\t" "v_exp_f32 v249, v249\n\t" \
    "v_exp_f32 v250, v250\n\t" "v_exp_f32 v251, v251\n\t" "v_pk_add_f32 v[242:243], v[244:245], v[246:247]\n\t" "v_pk_add_f32 v[242:243], v[242:243], v[248:249]\n\t" \
    "v_pk_add_f32 v[242:243], v[242:243], v[250:251]\n\t" "v_add_f32 v242, v242, v243\n\t" "v_mov_b32 v243, v242\n\t" "s_nop 1\n\t" "v_permlane32_swap_b32 v242, v243\n\t" \
    "v_add_f32 v242, v242, v243\n\t" "v_mov_b32 v243, v242\n\t" "s_nop 1\n\t" "v_permlane16_swap_b32 v242, v243\n\t" "v_add_f32 v242, v242, v243\n\t" \
    "v_rcp_f32 v242, v242\n\t" "s_nop 0\n\t" "v_pk_mul_f32 v[244:245], v[244:245], v[242:243] op_sel_hi:[1,0]\n\t" \
    "v_pk_mul_f32 v[246:247], v[246:247], v[242:243] op_sel_hi:[1,0]\n\t" "v_pk_mul_f32 v[248:249], v[248:249], v[242:243] op_sel_hi:[1,0]\n\t" \
    "v_pk_mul_f32 v[250:251], v[250:251], v[242:243] op_sel_hi:[1,0]\n\t" "v_cvt_pk_bf16_f32 v162, v244, v245\n\t" "v_cvt_pk_bf16_f32 v163, v246, v247\n\t" \
    "v_cvt_pk_bf16_f32 v164, v248, v249\n\t" "v_cvt_pk_bf16_f32 v165, v250, v251\n\t" "buffer_store_dwordx2 v[164:165], %[px], %[rs], %[tb] offen sc1\n\t" \
    "buffer_store_dwordx2 v[162:163], %[pm], %[rs], %[tb] offen sc1\n\t" "s_waitcnt vmcnt(2)\n\t" "v_readfirstlane_b32 s62, v240\n\t" "s_nop 3\n\t" \
    "s_max_u32 %[sq], %[sq], s62\n\t" "s_barrier\n\t" XT(96, 97) "s_branch 99f\n\t" "21:\n\t" "ds_read_b128 v[220:223], %[l1] offset:4096\n\t" "s_waitcnt lgkmcnt(0)\n\t" \
    "v_max3_u32 v218, v220, v221, v222\n\t" "v_max_u32 v218, v218, v223\n\t" "ds_read_b128 v[220:223], %[l1] offset:5120\n\t" "s_waitcnt lgkmcnt(0)\n\t" \
    "v_max3_u32 v218, v218, v220, v221\n\t" "v_max3_u32 v218, v218, v222, v223\n\t" "ds_read_b128 v[220:223], %[l1] offset:6144\n\t" "s_waitcnt lgkmcnt(0)\n\t" \
    "v_max3_u32 v218, v218, v220, v221\n\t" "v_max3_u32 v218, v218, v222, v223\n\t" "ds_read_b128 v[220:223], %[l1] offset:7168\n\t" "s_waitcnt lgkmcnt(0)\n\t" \
    "v_max3_u32 v218, v218, v220, v221\n\t" "v_max3_u32 v218, v218, v222, v223\n\t" "v_cmp_eq_u32 vcc, -1, v218\n\t" "s_cbranch_vccz 22b\n\t" "s_cmp_eq_u32 %[rt], 1\n\t" \
    "s_cbranch_scc1 22b\n\t" "v_mov_b32 v218, %[rt]\n\t" "ds_write_b32 v219, v218\n\t" "s_waitcnt lgkmcnt(0)\n\t" "s_branch 22b\n\t" "23:\n\t" "s_sub_u32 %[rt], %[rt], 1\n\t" \
    "s_waitcnt lgkmcnt(0)\n\t" XCNT(100) "24:\n\t" "buffer_load_dwordx4 v[240:243], %[mo], %[rs], %[sb] offen sc1\n\t" \
    "buffer_load_dwordx4 v[244:247], v216, %[rs], %[sb] offen sc1\n\t" "buffer_load_dwordx4 v[248:251], %[mo], %[rs], s60 offen sc1\n\t" \
    "buffer_load_dwordx4 v[252:255], v216, %[rs], s60 offen sc1\n\t" "s_waitcnt vmcnt(0)\n\t" "v_max_u32 v218, v240, v241\n\t" "v_max3_u32 v218, v218, v242, v243\n\t" \
    "v_max3_u32 v218, v218, v244, v245\n\t" "v_max3_u32 v218, v218, v246, v247\n\t" "v_max3_u32 v218, v218, v248, v249\n\t" "v_max3_u32 v218, v218, v250, v251\n\t" \
    "v_max3_u32 v218, v218, v252, v253\n\t" "v_max3_u32 v218, v218, v254, v255\n\t" "v_cmp_eq_u32 vcc, -1, v218\n\t" "s_cbranch_vccz 25f\n\t" "s_cmp_eq_u32 %[bd], 0\n\t" \
    "s_cbranch_scc1 25f\n\t" "s_sub_u32 %[bd], %[bd], 1\n\t" XCNT(101) "s_branch 24b\n\t" "25:\n\t" "ds_read_b128 v[160:163], %[l0] offset:0\n\t" \
    "ds_read_b128 v[164:167], %[l0] offset:1024\n\t" "ds_read_b128 v[168:171], %[l0] offset:2048\n\t" "ds_read_b128 v[172:175], %[l0] offset:3072\n\t" \
    "ds_read_b128 v[176:179], %[l0] offset:4096\n\t" "ds_read_b128 v[180:183], %[l0] offset:5120\n\t" "ds_read_b128 v[184:187], %[l0] offset:6144\n\t" \
    "ds_read_b128 v[188:191], %[l0] offset:7168\n\t" "v_mov_b64 v[224:225], 0\n\t" "v_mov_b64 v[226:227], 0\n\t" "v_mov_b64 v[228:229], 0\n\t" "v_mov_b64 v[230:231], 0\n\t" \
    "v_mov_b64 v[232:233], 0\n\t" "v_mov_b64 v[234:235], 0\n\t" "v_mov_b64 v[236:237], 0\n\t" "v_mov_b64 v[238:239], 0\n\t" "s_barrier\n\t" "s_branch 20b\n\t" "99:\n\t"
#define QK_STEP2 \
    ".p2align 6\n\t" "v_xor_b32 v216, 0x400, %[mo]\n\t" "s_xor_b32 s60, %[sb], 0x800\n\t" "s_add_u32 s61, %[qb], 0x1000\n\t" "s_mov_b32 s66, 0x3e0293ee\n\t" \
    "s_mov_b32 s67, 0x3e0293ee\n\t" XT(80, 81) XT(98, 99) "s_barrier\n\t" XT(82, 83) "buffer_load_dwordx4 v[240:243], %[mo], %[rs], %[sb] offen sc1\n\t" \
    "v_mov_b64 v[224:225], 0\n\t" "v_mov_b64 v[226:227], 0\n\t" "v_mov_b64 v[228:229], 0\n\t" "v_mov_b64 v[230:231], 0\n\t" "v_mov_b64 v[232:233], 0\n\t" \
    "v_mov_b64 v[234:235], 0\n\t" "v_mov_b64 v[236:237], 0\n\t" "v_mov_b64 v[238:239], 0\n\t" "buffer_load_dwordx4 v[244:247], v216, %[rs], %[sb] offen sc1\n\t" \
    "ds_read_b128 v[160:163], %[l0] offset:0\n\t" "ds_read_b128 v[164:167], %[l0] offset:1024\n\t" "ds_read_b128 v[168:171], %[l0] offset:2048\n\t" \
    "ds_read_b128 v[172:175], %[l0] offset:3072\n\t" "s_nop 3\n\t" "buffer_load_dwordx4 v[248:251], %[mo], %[rs], s60 offen sc1\n\t" \
    "ds_read_b128 v[176:179], %[l0] offset:4096\n\t" "ds_read_b128 v[180:183], %[l0] offset:5120\n\t" "ds_read_b128 v[184:187], %[l0] offset:6144\n\t" \
    "ds_read_b128 v[188:191], %[l0] offset:7168\n\t" "s_nop 3\n\t" "buffer_load_dwordx4 v[252:255], v216, %[rs], s60 offen sc1\n\t" "v_mov_b32 v219, 160512\n\t" \
    "v_mov_b64 v[220:221], -1\n\t" "v_mov_b64 v[222:223], -1\n\t" "buffer_store_dwordx4 v[220:223], %[px], %[rs], %[tr] offen sc1\n\t" \
    "buffer_store_dwordx2 v[220:221], %[pm], %[rs], %[tr] offen sc1\n\t" "v_mov_b32 v208, 0xd028965f\n\t" "v_mov_b32 v209, 0xff800000\n\t" "s_mov_b32 s62, 0\n\t" \
    "s_mov_b32 s63, -1\n\t" "v_mov_b32 v210, 0\n\t" "v_cmp_gt_i32 vcc, 0, %[ps]\n\t" "v_cndmask_b32 v204, 0, v209, s[62:63]\n\t" "s_nop 0\n\t" \
    "v_cndmask_b32 v200, 0, v208, vcc\n\t" "v_cmp_gt_i32 vcc, 1, %[ps]\n\t" "v_cndmask_b32 v205, 0, v209, s[62:63]\n\t" "s_nop 0\n\t" "v_cndmask_b32 v201, 0, v208, vcc\n\t" \
    "v_cmp_gt_i32 vcc, 2, %[ps]\n\t" "v_cndmask_b32 v206, 0, v209, s[62:63]\n\t" "s_nop 0\n\t" "v_cndmask_b32 v202, 0, v208, vcc\n\t" "v_cmp_gt_i32 vcc, 3, %[ps]\n\t" \
    "v_cndmask_b32 v207, 0, v209, s[62:63]\n\t" "s_nop 0\n\t" "v_cndmask_b32 v203, 0, v208, vcc\n\t" "30:\n\t" "s_waitcnt vmcnt(5)\n\t" XT(84, 85) \
    "v_smfmac_f32_16x16x64_bf16 v[224:227], v[240:243], a[0:7], %[ix]\n\t" "ds_write_b128 %[l1], v[240:243] offset:8192\n\t" \
    "v_smfmac_f32_16x16x64_bf16 v[228:231], v[240:243], a[8:15], %[ix]\n\t" "v_smfmac_f32_16x16x64_bf16 v[232:235], v[240:243], v[32:39], %[ix]\n\t" \
    "s_waitcnt lgkmcnt(7)\n\t" "v_smfmac_f32_16x16x64_bf16 v[236:239], v[240:243], v[160:167], %[ix]\n\t" "ds_read_b128 v[160:163], %[l0] offset:8192\n\t" \
    "ds_read_b128 v[164:167], %[l0] offset:9216\n\t" "s_waitcnt vmcnt(4)\n\t" "v_smfmac_f32_16x16x64_bf16 v[224:227], v[244:247], a[16:23], %[ix]\n\t" \
    "ds_write_b128 %[l1], v[244:247] offset:9216\n\t" "v_smfmac_f32_16x16x64_bf16 v[228:231], v[244:247], a[24:31], %[ix]\n\t" \
    "v_smfmac_f32_16x16x64_bf16 v[232:235], v[244:247], v[40:47], %[ix]\n\t" "s_waitcnt lgkmcnt(8)\n\t" \
    "v_smfmac_f32_16x16x64_bf16 v[236:239], v[244:247], v[168:175], %[ix]\n\t" "ds_read_b128 v[168:171], %[l0] offset:10240\n\t" \
    "ds_read_b128 v[172:175], %[l0] offset:11264\n\t" "s_waitcnt vmcnt(3)\n\t" "v_smfmac_f32_16x16x64_bf16 v[224:227], v[248:251], a[32:39], %[ix]\n\t" \
    "ds_write_b128 %[l1], v[248:251] offset:10240\n\t" "v_smfmac_f32_16x16x64_bf16 v[228:231], v[248:251], a[40:47], %[ix]\n\t" "s_waitcnt lgkmcnt(3)\n\t" "s_barrier\n\t" \
    XT(86, 87) "v_smfmac_f32_16x16x64_bf16 v[232:235], v[248:251], v[48:55], %[ix]\n\t" "ds_read_b128 v[192:195], %[l1] offset:12288\n\t" \
    "ds_read_b128 v[196:199], %[l1] offset:0\n\t" "v_smfmac_f32_16x16x64_bf16 v[236:239], v[248:251], v[176:183], %[ix]\n\t" "ds_read_b128 v[176:179], %[l0] offset:12288\n\t" \
    "ds_read_b128 v[180:183], %[l0] offset:13312\n\t" "s_waitcnt vmcnt(2)\n\t" "v_smfmac_f32_16x16x64_bf16 v[224:227], v[252:255], a[48:55], %[ix]\n\t" \
    "ds_write_b128 %[l1], v[252:255] offset:11264\n\t" "v_smfmac_f32_16x16x64_bf16 v[228:231], v[252:255], a[56:63], %[ix]\n\t" \
    "ds_read_b128 v[240:243], %[l1] offset:4096\n\t" "ds_read_b128 v[244:247], %[l1] offset:13312\n\t" \
    "v_smfmac_f32_16x16x64_bf16 v[232:235], v[252:255], v[56:63], %[ix]\n\t" "s_nop 1\n\t" "v_smfmac_f32_16x16x64_bf16 v[236:239], v[252:255], v[184:191], %[ix]\n\t" \
    "ds_read_b128 v[184:187], %[l0] offset:14336\n\t" "v_cmp_u_f32 vcc, v224, v225\n\t" "v_cmp_u_f32 s[62:63], v226, v227\n\t" "s_waitcnt lgkmcnt(7)\n\t" \
    "v_smfmac_f32_16x16x64_bf16 v[224:227], v[192:195], a[64:71], %[ix]\n\t" "ds_read_b128 v[188:191], %[l0] offset:15360\n\t" \
    "v_smfmac_f32_16x16x64_bf16 v[228:231], v[192:195], a[72:79], %[ix]\n\t" "s_or_b64 vcc, vcc, s[62:63]\n\t" "s_cbranch_scc1 31f\n\t" "32:\n\t" \
    "v_smfmac_f32_16x16x64_bf16 v[232:235], v[192:195], v[64:71], %[ix]\n\t" "s_waitcnt lgkmcnt(4)\n\t" "s_barrier\n\t" XT(88, 89) "ds_read_b32 v218, v219\n\t" \
    "v_smfmac_f32_16x16x64_bf16 v[236:239], v[192:195], v[160:167], %[ix]\n\t" "ds_read_b128 v[160:163], %[l0] offset:16384\n\t" \
    "ds_read_b128 v[164:167], %[l0] offset:17408\n\t" "v_smfmac_f32_16x16x64_bf16 v[224:227], v[196:199], a[80:87], %[ix]\n\t" \
    "ds_read_b128 v[248:251], %[l1] offset:1024\n\t" "ds_read_b128 v[252:255], %[l1] offset:5120\n\t" "v_smfmac_f32_16x16x64_bf16 v[228:231], v[196:199], a[88:95], %[ix]\n\t" \
    "ds_read_b128 v[192:195], %[l1] offset:14336\n\t" "v_smfmac_f32_16x16x64_bf16 v[232:235], v[196:199], v[72:79], %[ix]\n\t" \
    "v_smfmac_f32_16x16x64_bf16 v[236:239], v[196:199], v[168:175], %[ix]\n\t" "s_waitcnt lgkmcnt(5)\n\t" "v_cmp_eq_u32 vcc, %[rt], v218\n\t" \
    "v_smfmac_f32_16x16x64_bf16 v[224:227], v[240:243], a[96:103], %[ix]\n\t" "s_cbranch_vccnz 33f\n\t" "ds_read_b128 v[168:171], %[l0] offset:18432\n\t" \
    "v_smfmac_f32_16x16x64_bf16 v[228:231], v[240:243], a[104:111], %[ix]\n\t" "ds_read_b128 v[172:175], %[l0] offset:19456\n\t" \
    "ds_read_b128 v[196:199], %[l1] offset:2048\n\t" "v_smfmac_f32_16x16x64_bf16 v[232:235], v[240:243], v[80:87], %[ix]\n\t" \
    "buffer_load_dwordx4 v[16:19], %[hq], %[rs], %[qb] offen offset:0 sc1\n\t" "v_smfmac_f32_16x16x64_bf16 v[236:239], v[240:243], v[176:183], %[ix]\n\t" \
    "ds_read_b128 v[176:179], %[l0] offset:20480\n\t" "ds_read_b128 v[180:183], %[l0] offset:21504\n\t" \
    "v_smfmac_f32_16x16x64_bf16 v[224:227], v[244:247], a[112:119], %[ix]\n\t" "ds_read_b128 v[240:243], %[l1] offset:6144\n\t" \
    "v_smfmac_f32_16x16x64_bf16 v[228:231], v[244:247], a[120:127], %[ix]\n\t" "v_smfmac_f32_16x16x64_bf16 v[232:235], v[244:247], v[88:95], %[ix]\n\t" \
    "buffer_load_dwordx4 v[20:23], %[hq], %[rs], %[qb] offen offset:2048 sc1\n\t" "v_smfmac_f32_16x16x64_bf16 v[236:239], v[244:247], v[184:191], %[ix]\n\t" \
    "ds_read_b128 v[184:187], %[l0] offset:22528\n\t" "ds_read_b128 v[188:191], %[l0] offset:23552\n\t" "s_waitcnt lgkmcnt(10)\n\t" \
    "v_smfmac_f32_16x16x64_bf16 v[224:227], v[248:251], a[128:135], %[ix]\n\t" "ds_read_b128 v[244:247], %[l1] offset:15360\n\t" \
    "v_smfmac_f32_16x16x64_bf16 v[228:231], v[248:251], a[136:143], %[ix]\n\t" "v_smfmac_f32_16x16x64_bf16 v[232:235], v[248:251], v[96:103], %[ix]\n\t" \
    "buffer_load_dwordx4 v[24:27], %[hq], %[rs], s61 offen offset:0 sc1\n\t" "v_smfmac_f32_16x16x64_bf16 v[236:239], v[248:251], v[160:167], %[ix]\n\t" \
    "ds_read_b128 v[160:163], %[l0] offset:24576\n\t" "ds_read_b128 v[164:167], %[l0] offset:25600\n\t" "s_waitcnt lgkmcnt(12)\n\t" \
    "v_smfmac_f32_16x16x64_bf16 v[224:227], v[252:255], a[144:151], %[ix]\n\t" "ds_read_b128 v[248:251], %[l1] offset:3072\n\t" \
    "v_smfmac_f32_16x16x64_bf16 v[228:231], v[252:255], a[152:159], %[ix]\n\t" "v_smfmac_f32_16x16x64_bf16 v[232:235], v[252:255], v[104:111], %[ix]\n\t" \
    "buffer_load_dwordx4 v[28:31], %[hq], %[rs], s61 offen offset:2048 sc1\n\t" "s_waitcnt lgkmcnt(10)\n\t" \
    "v_smfmac_f32_16x16x64_bf16 v[236:239], v[252:255], v[168:175], %[ix]\n\t" "ds_read_b128 v[168:171], %[l0] offset:26624\n\t" \
    "ds_read_b128 v[172:175], %[l0] offset:27648\n\t" "v_smfmac_f32_16x16x64_bf16 v[224:227], v[192:195], a[160:167], %[ix]\n\t" \
    "ds_read_b128 v[252:255], %[l1] offset:7168\n\t" "v_smfmac_f32_16x16x64_bf16 v[228:231], v[192:195], a[168:175], %[ix]\n\t" "s_waitcnt lgkmcnt(12)\n\t" \
    "v_smfmac_f32_16x16x64_bf16 v[224:227], v[196:199], a[176:183], %[ix]\n\t" "v_smfmac_f32_16x16x64_bf16 v[228:231], v[196:199], a[184:191], %[ix]\n\t" \
    "s_waitcnt lgkmcnt(9)\n\t" "v_smfmac_f32_16x16x64_bf16 v[224:227], v[240:243], a[192:199], %[ix]\n\t" \
    "v_smfmac_f32_16x16x64_bf16 v[228:231], v[240:243], a[200:207], %[ix]\n\t" "s_waitcnt lgkmcnt(6)\n\t" \
    "v_smfmac_f32_16x16x64_bf16 v[224:227], v[244:247], a[208:215], %[ix]\n\t" "v_smfmac_f32_16x16x64_bf16 v[228:231], v[244:247], a[216:223], %[ix]\n\t" \
    "s_waitcnt vmcnt(4)\n\t" "s_waitcnt lgkmcnt(3)\n\t" "v_smfmac_f32_16x16x64_bf16 v[224:227], v[248:251], a[224:231], %[ix]\n\t" \
    "v_smfmac_f32_16x16x64_bf16 v[228:231], v[248:251], a[232:239], %[ix]\n\t" "s_waitcnt lgkmcnt(0)\n\t" \
    "v_smfmac_f32_16x16x64_bf16 v[224:227], v[252:255], a[240:247], %[ix]\n\t" "s_nop 1\n\t" "v_smfmac_f32_16x16x64_bf16 v[228:231], v[252:255], a[248:255], %[ix]\n\t" \
    XT(90, 91) "s_nop 1\n\t" "v_smfmac_f32_16x16x64_bf16 v[232:235], v[192:195], v[112:119], %[ix]\n\t" "s_nop 1\n\t" \
    "v_smfmac_f32_16x16x64_bf16 v[236:239], v[192:195], v[176:183], %[ix]\n\t" "ds_read_b128 v[176:179], %[l0] offset:28672\n\t" "v_add_f32 v224, v224, v225\n\t" \
    "v_add_f32 v226, v226, v227\n\t" "v_smfmac_f32_16x16x64_bf16 v[232:235], v[196:199], v[120:127], %[ix]\n\t" "ds_read_b128 v[180:183], %[l0] offset:29696\n\t" \
    "v_add_f32 v228, v228, v229\n\t" "v_add_f32 v230, v230, v231\n\t" "v_smfmac_f32_16x16x64_bf16 v[236:239], v[196:199], v[184:191], %[ix]\n\t" \
    "ds_read_b128 v[184:187], %[l0] offset:30720\n\t" "v_cvt_pk_bf16_f32 v218, v224, v228\n\t" "v_cvt_pk_bf16_f32 v219, v226, v230\n\t" \
    "v_smfmac_f32_16x16x64_bf16 v[232:235], v[240:243], v[128:135], %[ix]\n\t" "ds_write2_b32 %[qw], v218, v219 offset1:8\n\t" \
    "ds_read_b128 v[188:191], %[l0] offset:31744\n\t" "v_smfmac_f32_16x16x64_bf16 v[236:239], v[240:243], v[160:167], %[ix]\n\t" \
    "v_smfmac_f32_16x16x64_bf16 v[232:235], v[244:247], v[136:143], %[ix]\n\t" "v_smfmac_f32_16x16x64_bf16 v[236:239], v[244:247], v[168:175], %[ix]\n\t" \
    "s_waitcnt lgkmcnt(1)\n\t" "s_barrier\n\t" "v_smfmac_f32_16x16x64_bf16 v[232:235], v[248:251], v[144:151], %[ix]\n\t" \
    "v_smfmac_f32_16x16x64_bf16 v[236:239], v[248:251], v[176:183], %[ix]\n\t" "v_smfmac_f32_16x16x64_bf16 v[232:235], v[252:255], v[152:159], %[ix]\n\t" \
    "s_waitcnt lgkmcnt(0)\n\t" "v_smfmac_f32_16x16x64_bf16 v[236:239], v[252:255], v[184:191], %[ix]\n\t" XT(92, 93) "s_nop 6\n\t" "v_add_f32 v232, v232, v233\n\t" \
    "v_add_f32 v234, v234, v235\n\t" "v_add_f32 v236, v236, v237\n\t" "v_add_f32 v238, v238, v239\n\t" "v_cvt_pk_bf16_f32 v232, v232, v236\n\t" \
    "v_cvt_pk_bf16_f32 v233, v234, v238\n\t" "ds_write2_b32 %[kw], v232, v233 offset1:68\n\t" "s_waitcnt lgkmcnt(0)\n\t" "s_barrier\n\t" XT(94, 95) \
    "s_cmp_ge_u32 %[sq], %[sn]\n\t" "s_cbranch_scc1 377f\n\t" "375:\n\t" "global_load_dword v240, v210, %[fq] sc1\n\t" "s_waitcnt vmcnt(0)\n\t" \
    "v_readfirstlane_b32 %[sq], v240\n\t" "s_nop 3\n\t" "s_cmp_ge_u32 %[sq], %[sn]\n\t" "s_cbranch_scc1 376f\n\t" "s_cmp_eq_u32 %[bd], 0\n\t" "s_cbranch_scc1 376f\n\t" \
    "s_sub_u32 %[bd], %[bd], 1\n\t" "s_sleep 2\n\t" "s_branch 375b\n\t" "376:\n\t" "buffer_load_dwordx4 v[16:19], %[hq], %[rs], %[qb] offen offset:0 sc1\n\t" \
    "buffer_load_dwordx4 v[20:23], %[hq], %[rs], %[qb] offen offset:2048 sc1\n\t" "buffer_load_dwordx4 v[24:27], %[hq], %[rs], s61 offen offset:0 sc1\n\t" \
    "buffer_load_dwordx4 v[28:31], %[hq], %[rs], s61 offen offset:2048 sc1\n\t" "377:\n\t" "s_nop 3\n\t" "ds_read_b128 v[160:163], %[kx] offset:0\n\t" \
    "ds_read_b128 v[164:167], %[kx] offset:2048\n\t" "ds_read_b128 v[168:171], %[kx] offset:4096\n\t" "ds_read_b128 v[172:175], %[kx] offset:6144\n\t" \
    "ds_read_b128 v[176:179], %[kx] offset:256\n\t" "ds_read_b128 v[180:183], %[kx] offset:2304\n\t" "ds_read_b128 v[184:187], %[kx] offset:4352\n\t" \
    "ds_read_b128 v[188:191], %[kx] offset:6400\n\t" "ds_read_b128 v[224:227], %[km] offset:0\n\t" "ds_read_b128 v[228:231], %[km] offset:64\n\t" \
    "ds_read_b128 v[232:235], %[km] offset:128\n\t" "ds_read_b128 v[236:239], %[km] offset:192\n\t" "s_waitcnt vmcnt(0)\n\t" "global_load_dword v240, v210, %[fq] sc1\n\t" \
    "s_waitcnt lgkmcnt(11)\n\t" "v_mfma_f32_16x16x32_bf16 v[248:251], v[160:163], v[16:19], 0\n\t" "s_waitcnt lgkmcnt(10)\n\t" \
    "v_mfma_f32_16x16x32_bf16 v[248:251], v[164:167], v[20:23], v[248:251]\n\t" "s_waitcnt lgkmcnt(9)\n\t" \
    "v_mfma_f32_16x16x32_bf16 v[248:251], v[168:171], v[24:27], v[248:251]\n\t" "s_waitcnt lgkmcnt(8)\n\t" \
    "v_mfma_f32_16x16x32_bf16 v[248:251], v[172:175], v[28:31], v[248:251]\n\t" "s_waitcnt lgkmcnt(7)\n\t" \
    "v_mfma_f32_16x16x32_bf16 v[252:255], v[176:179], v[16:19], v[200:203]\n\t" "s_waitcnt lgkmcnt(6)\n\t" \
    "v_mfma_f32_16x16x32_bf16 v[252:255], v[180:183], v[20:23], v[252:255]\n\t" "s_waitcnt lgkmcnt(5)\n\t" \
    "v_mfma_f32_16x16x32_bf16 v[252:255], v[184:187], v[24:27], v[252:255]\n\t" "s_waitcnt lgkmcnt(4)\n\t" \
    "v_mfma_f32_16x16x32_bf16 v[252:255], v[188:191], v[28:31], v[252:255]\n\t" "s_waitcnt lgkmcnt(3)\n\t" \
    "v_mfma_f32_16x16x32_bf16 v[244:247], v[224:227], v[16:19], v[204:207]\n\t" "s_waitcnt lgkmcnt(2)\n\t" \
    "v_mfma_f32_16x16x32_bf16 v[244:247], v[228:231], v[20:23], v[244:247]\n\t" "s_waitcnt lgkmcnt(1)\n\t" \
    "v_mfma_f32_16x16x32_bf16 v[244:247], v[232:235], v[24:27], v[244:247]\n\t" "s_waitcnt lgkmcnt(0)\n\t" \
    "v_mfma_f32_16x16x32_bf16 v[244:247], v[236:239], v[28:31], v[244:247]\n\t" "s_nop 3\n\t" "v_max3_f32 v242, v248, v249, v250\n\t" "v_max3_f32 v242, v242, v251, v252\n\t" \
    "v_max3_f32 v242, v242, v253, v254\n\t" "v_max_f32 v242, v242, v255\n\t" "v_max3_f32 v242, v242, v244, v245\n\t" "v_max3_f32 v242, v242, v246, v247\n\t" \
    "v_mov_b32 v243, v242\n\t" "s_nop 1\n\t" "v_permlane32_swap_b32 v242, v243\n\t" "v_max_f32 v242, v242, v243\n\t" "v_mov_b32 v243, v242\n\t" "s_nop 1\n\t" \
    "v_permlane16_swap_b32 v242, v243\n\t" "v_max_f32 v242, v242, v243\n\t" "v_pk_add_f32 v[244:245], v[244:245], v[242:243] op_sel_hi:[1,0] neg_lo:[0,1] neg_hi:[0,1]\n\t" \
    "v_pk_add_f32 v[246:247], v[246:247], v[242:243] op_sel_hi:[1,0] neg_lo:[0,1] neg_hi:[0,1]\n\t" \
    "v_pk_add_f32 v[248:249], v[248:249], v[242:243] op_sel_hi:[1,0] neg_lo:[0,1] neg_hi:[0,1]\n\t" \
    "v_pk_add_f32 v[250:251], v[250:251], v[242:243] op_sel_hi:[1,0] neg_lo:[0,1] neg_hi:[0,1]\n\t" \
    "v_pk_add_f32 v[252:253], v[252:253], v[242:243] op_sel_hi:[1,0] neg_lo:[0,1] neg_hi:[0,1]\n\t" \
    "v_pk_add_f32 v[254:255], v[254:255], v[242:243] op_sel_hi:[1,0] neg_lo:[0,1] neg_hi:[0,1]\n\t" "v_pk_mul_f32 v[244:245], v[244:245], s[66:67]\n\t" \
    "v_pk_mul_f32 v[246:247], v[246:247], s[66:67]\n\t" "v_pk_mul_f32 v[248:249], v[248:249], s[66:67]\n\t" "v_pk_mul_f32 v[250:251], v[250:251], s[66:67]\n\t" \
    "v_pk_mul_f32 v[252:253], v[252:253], s[66:67]\n\t" "v_pk_mul_f32 v[254:255], v[254:255], s[66:67]\n\t" "v_exp_f32 v244, v244\n\t" "v_exp_f32 v245, v245\n\t" \
    "v_exp_f32 v246, v246\n\t" "v_exp_f32 v247, v247\n\t" "v_exp_f32 v248, v248\n\t" "v_exp_f32 v249, v249\n\t" "v_exp_f32 v250, v250\n\t" "v_exp_f32 v251, v251\n\t" \
    "v_exp_f32 v252, v252\n\t" "v_exp_f32 v253, v253\n\t" "v_exp_f32 v254, v254\n\t" "v_exp_f32 v255, v255\n\t" "v_pk_add_f32 v[242:243], v[244:245], v[246:247]\n\t" \
    "v_pk_add_f32 v[242:243], v[242:243], v[248:249]\n\t" "v_pk_add_f32 v[242:243], v[242:243], v[250:251]\n\t" "v_pk_add_f32 v[242:243], v[242:243], v[252:253]\n\t" \
    "v_pk_add_f32 v[242:243], v[242:243], v[254:255]\n\t" "v_add_f32 v242, v242, v243\n\t" "v_mov_b32 v243, v242\n\t" "s_nop 1\n\t" "v_permlane32_swap_b32 v242, v243\n\t" \
    "v_add_f32 v242, v242, v243\n\t" "v_mov_b32 v243, v242\n\t" "s_nop 1\n\t" "v_permlane16_swap_b32 v242, v243\n\t" "v_add_f32 v242, v242, v243\n\t" \
    "v_rcp_f32 v242, v242\n\t" "s_nop 0\n\t" "v_pk_mul_f32 v[244:245], v[244:245], v[242:243] op_sel_hi:[1,0]\n\t" \
    "v_pk_mul_f32 v[246:247], v[246:247], v[242:243] op_sel_hi:[1,0]\n\t" "v_pk_mul_f32 v[248:249], v[248:249], v[242:243] op_sel_hi:[1,0]\n\t" \
    "v_pk_mul_f32 v[250:251], v[250:251], v[242:243] op_sel_hi:[1,0]\n\t" "v_pk_mul_f32 v[252:253], v[252:253], v[242:243] op_sel_hi:[1,0]\n\t" \
    "v_pk_mul_f32 v[254:255], v[254:255], v[242:243] op_sel_hi:[1,0]\n\t" "v_cvt_pk_bf16_f32 v162, v244, v245\n\t" "v_cvt_pk_bf16_f32 v163, v246, v247\n\t" \
    "v_cvt_pk_bf16_f32 v164, v248, v249\n\t" "v_cvt_pk_bf16_f32 v165, v250, v251\n\t" "v_cvt_pk_bf16_f32 v166, v252, v253\n\t" "v_cvt_pk_bf16_f32 v167, v254, v255\n\t" \
    "buffer_store_dwordx4 v[164:167], %[px], %[rs], %[tb] offen sc1\n\t" "buffer_store_dwordx2 v[162:163], %[pm], %[rs], %[tb] offen sc1\n\t" "s_waitcnt vmcnt(2)\n\t" \
    "v_readfirstlane_b32 s62, v240\n\t" "s_nop 3\n\t" "s_max_u32 %[sq], %[sq], s62\n\t" "s_barrier\n\t" XT(96, 97) "s_branch 99f\n\t" "31:\n\t" \
    "ds_read_b128 v[220:223], %[l1] offset:8192\n\t" "s_waitcnt lgkmcnt(0)\n\t" "v_max3_u32 v218, v220, v221, v222\n\t" "v_max_u32 v218, v218, v223\n\t" \
    "ds_read_b128 v[220:223], %[l1] offset:9216\n\t" "s_waitcnt lgkmcnt(0)\n\t" "v_max3_u32 v218, v218, v220, v221\n\t" "v_max3_u32 v218, v218, v222, v223\n\t" \
    "ds_read_b128 v[220:223], %[l1] offset:10240\n\t" "s_waitcnt lgkmcnt(0)\n\t" "v_max3_u32 v218, v218, v220, v221\n\t" "v_max3_u32 v218, v218, v222, v223\n\t" \
    "ds_read_b128 v[220:223], %[l1] offset:11264\n\t" "s_waitcnt lgkmcnt(0)\n\t" "v_max3_u32 v218, v218, v220, v221\n\t" "v_max3_u32 v218, v218, v222, v223\n\t" \
    "v_cmp_eq_u32 vcc, -1, v218\n\t" "s_cbranch_vccz 32b\n\t" "s_cmp_eq_u32 %[rt], 1\n\t" "s_cbranch_scc1 32b\n\t" "v_mov_b32 v218, %[rt]\n\t" "ds_write_b32 v219, v218\n\t" \
    "s_waitcnt lgkmcnt(0)\n\t" "s_branch 32b\n\t" "33:\n\t" "s_sub_u32 %[rt], %[rt], 1\n\t" "s_waitcnt lgkmcnt(0)\n\t" XCNT(100) "34:\n\t" \
    "buffer_load_dwordx4 v[240:243], %[mo], %[rs], %[sb] offen sc1\n\t" "buffer_load_dwordx4 v[244:247], v216, %[rs], %[sb] offen sc1\n\t" \
    "buffer_load_dwordx4 v[248:251], %[mo], %[rs], s60 offen sc1\n\t" "buffer_load_dwordx4 v[252:255], v216, %[rs], s60 offen sc1\n\t" "s_waitcnt vmcnt(0)\n\t" \
    "v_max_u32 v218, v240, v241\n\t" "v_max3_u32 v218, v218, v242, v243\n\t" "v_max3_u32 v218, v218, v244, v245\n\t" "v_max3_u32 v218, v218, v246, v247\n\t" \
    "v_max3_u32 v218, v218, v248, v249\n\t" "v_max3_u32 v218, v218, v250, v251\n\t" "v_max3_u32 v218, v218, v252, v253\n\t" "v_max3_u32 v218, v218, v254, v255\n\t" \
    "v_cmp_eq_u32 vcc, -1, v218\n\t" "s_cbranch_vccz 35f\n\t" "s_cmp_eq_u32 %[bd], 0\n\t" "s_cbranch_scc1 35f\n\t" "s_sub_u32 %[bd], %[bd], 1\n\t" XCNT(101) \
    "s_branch 34b\n\t" "35:\n\t" "ds_read_b128 v[160:163], %[l0] offset:0\n\t" "ds_read_b128 v[164:167], %[l0] offset:1024\n\t" \
    "ds_read_b128 v[168:171], %[l0] offset:2048\n\t" "ds_read_b128 v[172:175], %[l0] offset:3072\n\t" "ds_read_b128 v[176:179], %[l0] offset:4096\n\t" \
    "ds_read_b128 v[180:183], %[l0] offset:5120\n\t" "ds_read_b128 v[184:187], %[l0] offset:6144\n\t" "ds_read_b128 v[188:191], %[l0] offset:7168\n\t" \
    "v_mov_b64 v[224:225], 0\n\t" "v_mov_b64 v[226:227], 0\n\t" "v_mov_b64 v[228:229], 0\n\t" "v_mov_b64 v[230:231], 0\n\t" "v_mov_b64 v[232:233], 0\n\t" \
    "v_mov_b64 v[234:235], 0\n\t" "v_mov_b64 v[236:237], 0\n\t" "v_mov_b64 v[238:239], 0\n\t" "s_barrier\n\t" "s_branch 30b\n\t" "99:\n\t"
#define QK_STEP3 \
    ".p2align 6\n\t" "v_xor_b32 v216, 0x400, %[mo]\n\t" "s_xor_b32 s60, %[sb], 0x800\n\t" "s_mov_b32 s67, %[pb]\n\t" XT(80, 81) "s_getpc_b64 s[62:63]\n\t" "48:\n\t" \
    "s_memtime s[64:65]\n\t" "s_waitcnt lgkmcnt(0)\n\t" "s_sub_u32 s61, %[tg], s64\n\t" "s_cmp_gt_i32 s61, 124\n\t" "s_cbranch_scc1 48b\n\t" "s_max_i32 s61, s61, 0\n\t" \
    "s_and_b32 s61, s61, 124\n\t" "s_sub_u32 s61, 49f-48b, s61\n\t" "s_add_u32 s62, s62, s61\n\t" "s_addc_u32 s63, s63, 0\n\t" "s_setpc_b64 s[62:63]\n\t" ".rept 31\n\t" \
    "s_nop 0\n\t" ".endr\n\t" "49:\n\t" XT(98, 99) "s_barrier\n\t" XT(82, 83) "buffer_load_dwordx4 v[240:243], %[mo], %[rs], %[sb] offen sc1\n\t" \
    "v_mov_b64 v[224:225], 0\n\t" "v_mov_b64 v[226:227], 0\n\t" "v_mov_b64 v[228:229], 0\n\t" "v_mov_b64 v[230:231], 0\n\t" "v_mov_b64 v[232:233], 0\n\t" \
    "v_mov_b64 v[234:235], 0\n\t" "v_mov_b64 v[236:237], 0\n\t" "v_mov_b64 v[238:239], 0\n\t" "buffer_load_dwordx4 v[244:247], v216, %[rs], %[sb] offen sc1\n\t" \
    "ds_read_b128 v[160:163], %[l0] offset:0\n\t" "ds_read_b128 v[164:167], %[l0] offset:1024\n\t" "ds_read_b128 v[168:171], %[l0] offset:2048\n\t" \
    "ds_read_b128 v[172:175], %[l0] offset:3072\n\t" "s_nop 3\n\t" "buffer_load_dwordx4 v[248:251], %[mo], %[rs], s60 offen sc1\n\t" \
    "ds_read_b128 v[176:179], %[l0] offset:4096\n\t" "ds_read_b128 v[180:183], %[l0] offset:5120\n\t" "ds_read_b128 v[184:187], %[l0] offset:6144\n\t" \
    "ds_read_b128 v[188:191], %[l0] offset:7168\n\t" "s_nop 3\n\t" "buffer_load_dwordx4 v[252:255], v216, %[rs], s60 offen sc1\n\t" "v_mov_b32 v219, 160512\n\t" \
    "v_mov_b32 v220, -1\n\t" "buffer_store_dword v220, %[hq], %[rs], %[pr] offen sc1\n\t" "40:\n\t" "s_waitcnt vmcnt(4)\n\t" XT(84, 85) \
    "v_smfmac_f32_16x16x64_bf16 v[224:227], v[240:243], a[0:7], %[ix]\n\t" "ds_write_b128 %[l1], v[240:243] offset:12288\n\t" \
    "v_smfmac_f32_16x16x64_bf16 v[228:231], v[240:243], a[8:15], %[ix]\n\t" "v_smfmac_f32_16x16x64_bf16 v[232:235], v[240:243], v[32:39], %[ix]\n\t" \
    "s_waitcnt lgkmcnt(7)\n\t" "v_smfmac_f32_16x16x64_bf16 v[236:239], v[240:243], v[160:167], %[ix]\n\t" "ds_read_b128 v[160:163], %[l0] offset:8192\n\t" \
    "ds_read_b128 v[164:167], %[l0] offset:9216\n\t" "s_waitcnt vmcnt(3)\n\t" "v_smfmac_f32_16x16x64_bf16 v[224:227], v[244:247], a[16:23], %[ix]\n\t" \
    "ds_write_b128 %[l1], v[244:247] offset:13312\n\t" "v_smfmac_f32_16x16x64_bf16 v[228:231], v[244:247], a[24:31], %[ix]\n\t" \
    "v_smfmac_f32_16x16x64_bf16 v[232:235], v[244:247], v[40:47], %[ix]\n\t" "s_waitcnt lgkmcnt(8)\n\t" \
    "v_smfmac_f32_16x16x64_bf16 v[236:239], v[244:247], v[168:175], %[ix]\n\t" "ds_read_b128 v[168:171], %[l0] offset:10240\n\t" \
    "ds_read_b128 v[172:175], %[l0] offset:11264\n\t" "s_waitcnt vmcnt(2)\n\t" "v_smfmac_f32_16x16x64_bf16 v[224:227], v[248:251], a[32:39], %[ix]\n\t" \
    "ds_write_b128 %[l1], v[248:251] offset:14336\n\t" "v_smfmac_f32_16x16x64_bf16 v[228:231], v[248:251], a[40:47], %[ix]\n\t" "s_waitcnt lgkmcnt(3)\n\t" "s_barrier\n\t" \
    XT(86, 87) "v_smfmac_f32_16x16x64_bf16 v[232:235], v[248:251], v[48:55], %[ix]\n\t" "ds_read_b128 v[192:195], %[l1] offset:0\n\t" \
    "ds_read_b128 v[196:199], %[l1] offset:4096\n\t" "v_smfmac_f32_16x16x64_bf16 v[236:239], v[248:251], v[176:183], %[ix]\n\t" \
    "ds_read_b128 v[176:179], %[l0] offset:12288\n\t" "ds_read_b128 v[180:183], %[l0] offset:13312\n\t" "s_waitcnt vmcnt(1)\n\t" \
    "v_smfmac_f32_16x16x64_bf16 v[224:227], v[252:255], a[48:55], %[ix]\n\t" "ds_write_b128 %[l1], v[252:255] offset:15360\n\t" \
    "v_smfmac_f32_16x16x64_bf16 v[228:231], v[252:255], a[56:63], %[ix]\n\t" "ds_read_b128 v[240:243], %[l1] offset:8192\n\t" "ds_read_b128 v[244:247], %[l1] offset:1024\n\t" \
    "v_smfmac_f32_16x16x64_bf16 v[232:235], v[252:255], v[56:63], %[ix]\n\t" "s_nop 1\n\t" "v_smfmac_f32_16x16x64_bf16 v[236:239], v[252:255], v[184:191], %[ix]\n\t" \
    "ds_read_b128 v[184:187], %[l0] offset:14336\n\t" "v_cmp_u_f32 vcc, v224, v225\n\t" "v_cmp_u_f32 s[62:63], v226, v227\n\t" "s_waitcnt lgkmcnt(7)\n\t" \
    "v_smfmac_f32_16x16x64_bf16 v[224:227], v[192:195], a[64:71], %[ix]\n\t" "ds_read_b128 v[188:191], %[l0] offset:15360\n\t" \
    "v_smfmac_f32_16x16x64_bf16 v[228:231], v[192:195], a[72:79], %[ix]\n\t" "s_or_b64 vcc, vcc, s[62:63]\n\t" "s_cbranch_scc1 41f\n\t" "42:\n\t" \
    "v_smfmac_f32_16x16x64_bf16 v[232:235], v[192:195], v[64:71], %[ix]\n\t" "s_waitcnt lgkmcnt(4)\n\t" "s_barrier\n\t" XT(88, 89) "ds_read_b32 v218, v219\n\t" \
    "v_smfmac_f32_16x16x64_bf16 v[236:239], v[192:195], v[160:167], %[ix]\n\t" "s_memtime s[64:65]\n\t" "ds_read_b128 v[160:163], %[l0] offset:16384\n\t" \
    "v_smfmac_f32_16x16x64_bf16 v[224:227], v[196:199], a[80:87], %[ix]\n\t" "ds_read_b128 v[164:167], %[l0] offset:17408\n\t" \
    "ds_read_b128 v[248:251], %[l1] offset:5120\n\t" "v_smfmac_f32_16x16x64_bf16 v[228:231], v[196:199], a[88:95], %[ix]\n\t" "ds_read_b128 v[252:255], %[l1] offset:9216\n\t" \
    "ds_read_b128 v[192:195], %[l1] offset:2048\n\t" "v_smfmac_f32_16x16x64_bf16 v[232:235], v[196:199], v[72:79], %[ix]\n\t" "s_sub_u32 s62, s64, %[ca]\n\t" \
    "s_mul_hi_u32 s63, s62, %[k1]\n\t" "s_mul_i32 s62, s62, %[k1]\n\t" "v_smfmac_f32_16x16x64_bf16 v[236:239], v[196:199], v[168:175], %[ix]\n\t" "s_waitcnt lgkmcnt(5)\n\t" \
    "v_cmp_eq_u32 vcc, %[rt], v218\n\t" "s_lshr_b64 s[62:63], s[62:63], 16\n\t" "s_add_u32 s62, s62, %[ga]\n\t" "s_bitset0_b32 s62, 0\n\t" \
    "v_smfmac_f32_16x16x64_bf16 v[224:227], v[240:243], a[96:103], %[ix]\n\t" "s_cbranch_vccnz 43f\n\t" "ds_read_b128 v[168:171], %[l0] offset:18432\n\t" \
    "v_smfmac_f32_16x16x64_bf16 v[228:231], v[240:243], a[104:111], %[ix]\n\t" "ds_read_b128 v[172:175], %[l0] offset:19456\n\t" "v_mov_b32 v217, s62\n\t" \
    "buffer_store_dword v217, %[hq], %[rs], s67 offen sc1\n\t" "v_smfmac_f32_16x16x64_bf16 v[232:235], v[240:243], v[80:87], %[ix]\n\t" \
    "ds_read_b128 v[196:199], %[l1] offset:6144\n\t" "v_smfmac_f32_16x16x64_bf16 v[236:239], v[240:243], v[176:183], %[ix]\n\t" \
    "ds_read_b128 v[176:179], %[l0] offset:20480\n\t" "ds_read_b128 v[180:183], %[l0] offset:21504\n\t" \
    "v_smfmac_f32_16x16x64_bf16 v[224:227], v[244:247], a[112:119], %[ix]\n\t" "ds_read_b128 v[240:243], %[l1] offset:10240\n\t" \
    "v_smfmac_f32_16x16x64_bf16 v[228:231], v[244:247], a[120:127], %[ix]\n\t" "v_smfmac_f32_16x16x64_bf16 v[232:235], v[244:247], v[88:95], %[ix]\n\t" \
    "v_smfmac_f32_16x16x64_bf16 v[236:239], v[244:247], v[184:191], %[ix]\n\t" "ds_read_b128 v[184:187], %[l0] offset:22528\n\t" \
    "ds_read_b128 v[188:191], %[l0] offset:23552\n\t" "s_waitcnt lgkmcnt(10)\n\t" "v_smfmac_f32_16x16x64_bf16 v[224:227], v[248:251], a[128:135], %[ix]\n\t" \
    "ds_read_b128 v[244:247], %[l1] offset:3072\n\t" "v_smfmac_f32_16x16x64_bf16 v[228:231], v[248:251], a[136:143], %[ix]\n\t" \
    "v_smfmac_f32_16x16x64_bf16 v[232:235], v[248:251], v[96:103], %[ix]\n\t" "v_smfmac_f32_16x16x64_bf16 v[236:239], v[248:251], v[160:167], %[ix]\n\t" \
    "ds_read_b128 v[160:163], %[l0] offset:24576\n\t" "ds_read_b128 v[164:167], %[l0] offset:25600\n\t" "s_waitcnt lgkmcnt(12)\n\t" \
    "v_smfmac_f32_16x16x64_bf16 v[224:227], v[252:255], a[144:151], %[ix]\n\t" "ds_read_b128 v[248:251], %[l1] offset:7168\n\t" \
    "v_smfmac_f32_16x16x64_bf16 v[228:231], v[252:255], a[152:159], %[ix]\n\t" "v_smfmac_f32_16x16x64_bf16 v[232:235], v[252:255], v[104:111], %[ix]\n\t" \
    "s_waitcnt lgkmcnt(10)\n\t" "v_smfmac_f32_16x16x64_bf16 v[236:239], v[252:255], v[168:175], %[ix]\n\t" "ds_read_b128 v[168:171], %[l0] offset:26624\n\t" \
    "ds_read_b128 v[172:175], %[l0] offset:27648\n\t" "v_smfmac_f32_16x16x64_bf16 v[224:227], v[192:195], a[160:167], %[ix]\n\t" \
    "ds_read_b128 v[252:255], %[l1] offset:11264\n\t" "v_smfmac_f32_16x16x64_bf16 v[228:231], v[192:195], a[168:175], %[ix]\n\t" "s_waitcnt lgkmcnt(12)\n\t" \
    "v_smfmac_f32_16x16x64_bf16 v[224:227], v[196:199], a[176:183], %[ix]\n\t" "v_smfmac_f32_16x16x64_bf16 v[228:231], v[196:199], a[184:191], %[ix]\n\t" \
    "s_waitcnt lgkmcnt(9)\n\t" "v_smfmac_f32_16x16x64_bf16 v[224:227], v[240:243], a[192:199], %[ix]\n\t" \
    "v_smfmac_f32_16x16x64_bf16 v[228:231], v[240:243], a[200:207], %[ix]\n\t" "s_waitcnt lgkmcnt(6)\n\t" \
    "v_smfmac_f32_16x16x64_bf16 v[224:227], v[244:247], a[208:215], %[ix]\n\t" "v_smfmac_f32_16x16x64_bf16 v[228:231], v[244:247], a[216:223], %[ix]\n\t" \
    "s_waitcnt lgkmcnt(3)\n\t" "v_smfmac_f32_16x16x64_bf16 v[224:227], v[248:251], a[224:231], %[ix]\n\t" \
    "v_smfmac_f32_16x16x64_bf16 v[228:231], v[248:251], a[232:239], %[ix]\n\t" "s_waitcnt lgkmcnt(0)\n\t" \
    "v_smfmac_f32_16x16x64_bf16 v[224:227], v[252:255], a[240:247], %[ix]\n\t" "s_nop 1\n\t" "v_smfmac_f32_16x16x64_bf16 v[228:231], v[252:255], a[248:255], %[ix]\n\t" \
    XT(90, 91) "s_nop 1\n\t" "v_smfmac_f32_16x16x64_bf16 v[232:235], v[192:195], v[112:119], %[ix]\n\t" "s_nop 1\n\t" \
    "v_smfmac_f32_16x16x64_bf16 v[236:239], v[192:195], v[176:183], %[ix]\n\t" "ds_read_b128 v[176:179], %[l0] offset:28672\n\t" "v_add_f32 v224, v224, v225\n\t" \
    "v_add_f32 v226, v226, v227\n\t" "v_smfmac_f32_16x16x64_bf16 v[232:235], v[196:199], v[120:127], %[ix]\n\t" "ds_read_b128 v[180:183], %[l0] offset:29696\n\t" \
    "v_add_f32 v228, v228, v229\n\t" "v_add_f32 v230, v230, v231\n\t" "v_smfmac_f32_16x16x64_bf16 v[236:239], v[196:199], v[184:191], %[ix]\n\t" \
    "ds_read_b128 v[184:187], %[l0] offset:30720\n\t" "v_cvt_pk_bf16_f32 v218, v224, v228\n\t" "v_cvt_pk_bf16_f32 v219, v226, v230\n\t" \
    "v_smfmac_f32_16x16x64_bf16 v[232:235], v[240:243], v[128:135], %[ix]\n\t" "ds_write2_b32 %[qw], v218, v219 offset1:8\n\t" \
    "ds_read_b128 v[188:191], %[l0] offset:31744\n\t" "v_smfmac_f32_16x16x64_bf16 v[236:239], v[240:243], v[160:167], %[ix]\n\t" \
    "v_smfmac_f32_16x16x64_bf16 v[232:235], v[244:247], v[136:143], %[ix]\n\t" "v_smfmac_f32_16x16x64_bf16 v[236:239], v[244:247], v[168:175], %[ix]\n\t" \
    "s_waitcnt lgkmcnt(1)\n\t" "s_barrier\n\t" "v_smfmac_f32_16x16x64_bf16 v[232:235], v[248:251], v[144:151], %[ix]\n\t" \
    "v_smfmac_f32_16x16x64_bf16 v[236:239], v[248:251], v[176:183], %[ix]\n\t" "v_smfmac_f32_16x16x64_bf16 v[232:235], v[252:255], v[152:159], %[ix]\n\t" \
    "s_waitcnt lgkmcnt(0)\n\t" "v_smfmac_f32_16x16x64_bf16 v[236:239], v[252:255], v[184:191], %[ix]\n\t" XT(92, 93) "s_nop 6\n\t" "v_add_f32 v232, v232, v233\n\t" \
    "v_add_f32 v234, v234, v235\n\t" "v_add_f32 v236, v236, v237\n\t" "v_add_f32 v238, v238, v239\n\t" "v_cvt_pk_bf16_f32 v232, v232, v236\n\t" \
    "v_cvt_pk_bf16_f32 v233, v234, v238\n\t" "ds_write2_b32 %[kw], v232, v233 offset1:68\n\t" "s_waitcnt lgkmcnt(0)\n\t" "s_barrier\n\t" XT(94, 95) XT(96, 97) \
    "s_branch 99f\n\t" "41:\n\t" "ds_read_b128 v[220:223], %[l1] offset:12288\n\t" "s_waitcnt lgkmcnt(0)\n\t" "v_max3_u32 v218, v220, v221, v222\n\t" \
    "v_max_u32 v218, v218, v223\n\t" "ds_read_b128 v[220:223], %[l1] offset:13312\n\t" "s_waitcnt lgkmcnt(0)\n\t" "v_max3_u32 v218, v218, v220, v221\n\t" \
    "v_max3_u32 v218, v218, v222, v223\n\t" "ds_read_b128 v[220:223], %[l1] offset:14336\n\t" "s_waitcnt lgkmcnt(0)\n\t" "v_max3_u32 v218, v218, v220, v221\n\t" \
    "v_max3_u32 v218, v218, v222, v223\n\t" "ds_read_b128 v[220:223], %[l1] offset:15360\n\t" "s_waitcnt lgkmcnt(0)\n\t" "v_max3_u32 v218, v218, v220, v221\n\t" \
    "v_max3_u32 v218, v218, v222, v223\n\t" "v_cmp_eq_u32 vcc, -1, v218\n\t" "s_cbranch_vccz 42b\n\t" "s_cmp_eq_u32 %[rt], 1\n\t" "s_cbranch_scc1 42b\n\t" \
    "v_mov_b32 v218, %[rt]\n\t" "ds_write_b32 v219, v218\n\t" "s_waitcnt lgkmcnt(0)\n\t" "s_branch 42b\n\t" "43:\n\t" "s_sub_u32 %[rt], %[rt], 1\n\t" \
    "s_waitcnt lgkmcnt(0)\n\t" XCNT(100) "44:\n\t" "buffer_load_dwordx4 v[240:243], %[mo], %[rs], %[sb] offen sc1\n\t" \
    "buffer_load_dwordx4 v[244:247], v216, %[rs], %[sb] offen sc1\n\t" "buffer_load_dwordx4 v[248:251], %[mo], %[rs], s60 offen sc1\n\t" \
    "buffer_load_dwordx4 v[252:255], v216, %[rs], s60 offen sc1\n\t" "s_waitcnt vmcnt(0)\n\t" "v_max_u32 v218, v240, v241\n\t" "v_max3_u32 v218, v218, v242, v243\n\t" \
    "v_max3_u32 v218, v218, v244, v245\n\t" "v_max3_u32 v218, v218, v246, v247\n\t" "v_max3_u32 v218, v218, v248, v249\n\t" "v_max3_u32 v218, v218, v250, v251\n\t" \
    "v_max3_u32 v218, v218, v252, v253\n\t" "v_max3_u32 v218, v218, v254, v255\n\t" "v_cmp_eq_u32 vcc, -1, v218\n\t" "s_cbranch_vccz 45f\n\t" "s_cmp_eq_u32 %[bd], 0\n\t" \
    "s_cbranch_scc1 45f\n\t" "s_sub_u32 %[bd], %[bd], 1\n\t" XCNT(101) "s_branch 44b\n\t" "45:\n\t" "ds_read_b128 v[160:163], %[l0] offset:0\n\t" \
    "ds_read_b128 v[164:167], %[l0] offset:1024\n\t" "ds_read_b128 v[168:171], %[l0] offset:2048\n\t" "ds_read_b128 v[172:175], %[l0] offset:3072\n\t" \
    "ds_read_b128 v[176:179], %[l0] offset:4096\n\t" "ds_read_b128 v[180:183], %[l0] offset:5120\n\t" "ds_read_b128 v[184:187], %[l0] offset:6144\n\t" \
    "ds_read_b128 v[188:191], %[l0] offset:7168\n\t" "v_mov_b64 v[224:225], 0\n\t" "v_mov_b64 v[226:227], 0\n\t" "v_mov_b64 v[228:229], 0\n\t" "v_mov_b64 v[230:231], 0\n\t" \
    "v_mov_b64 v[232:233], 0\n\t" "v_mov_b64 v[234:235], 0\n\t" "v_mov_b64 v[236:237], 0\n\t" "v_mov_b64 v[238:239], 0\n\t" "s_barrier\n\t" "s_memtime s[64:65]\n\t" \
    "s_waitcnt lgkmcnt(0)\n\t" "s_add_u32 s64, s64, %[dr]\n\t" "s_sub_u32 s62, s64, %[ca]\n\t" "s_mul_hi_u32 s63, s62, %[k1]\n\t" "s_mul_i32 s62, s62, %[k1]\n\t" \
    "s_lshr_b64 s[62:63], s[62:63], 16\n\t" "s_add_u32 s62, s62, %[ga]\n\t" "s_bitset0_b32 s62, 0\n\t" "v_mov_b32 v218, s62\n\t" \
    "buffer_store_dword v218, %[hq], %[rs], s67 offen sc1\n\t" "s_mov_b32 s67, 0x7f000000\n\t" "s_branch 40b\n\t" "99:\n\t" "mk_tail:\n\t"
#define VO_STEP \
    ".p2align 6\n\t" "v_xor_b32 v224, 0x400, %[mo]\n\t" "s_xor_b32 s60, %[sb], 0x800\n\t" "s_mov_b32 s67, %[pb]\n\t" "s_cmp_eq_u32 %[w], 1\n\t" "s_cbranch_scc1 91f\n\t" \
    "s_cmp_eq_u32 %[w], 2\n\t" "s_cbranch_scc1 92f\n\t" "s_cmp_eq_u32 %[w], 3\n\t" "s_cbranch_scc1 93f\n\t" XT(80, 81) "s_getpc_b64 s[62:63]\n\t" "18:\n\t" \
    "s_memtime s[64:65]\n\t" "s_waitcnt lgkmcnt(0)\n\t" "s_sub_u32 s61, %[tg], s64\n\t" "s_cmp_gt_i32 s61, 124\n\t" "s_cbranch_scc1 18b\n\t" "s_max_i32 s61, s61, 0\n\t" \
    "s_and_b32 s61, s61, 124\n\t" "s_sub_u32 s61, 19f-18b, s61\n\t" "s_add_u32 s62, s62, s61\n\t" "s_addc_u32 s63, s63, 0\n\t" "s_setpc_b64 s[62:63]\n\t" ".rept 31\n\t" \
    "s_nop 0\n\t" ".endr\n\t" "19:\n\t" "s_barrier\n\t" XT(82, 83) "buffer_load_dwordx4 v[240:243], %[mo], %[rs], %[sb] offen sc1\n\t" "v_mov_b64 v[232:233], 0\n\t" \
    "v_mov_b64 v[234:235], 0\n\t" "v_mov_b64 v[236:237], 0\n\t" "v_mov_b64 v[238:239], 0\n\t" "v_mov_b64 v[192:193], 0\n\t" "v_mov_b64 v[194:195], 0\n\t" \
    "v_mov_b64 v[196:197], 0\n\t" "v_mov_b64 v[198:199], 0\n\t" "buffer_load_dwordx4 v[244:247], v224, %[rs], %[sb] offen sc1\n\t" \
    "ds_read_b128 v[160:163], %[l0] offset:0\n\t" "ds_read_b128 v[164:167], %[l0] offset:1024\n\t" "ds_read_b128 v[168:171], %[l0] offset:2048\n\t" \
    "ds_read_b128 v[172:175], %[l0] offset:3072\n\t" "s_nop 3\n\t" "buffer_load_dwordx4 v[248:251], %[mo], %[rs], s60 offen sc1\n\t" \
    "ds_read_b128 v[176:179], %[l0] offset:4096\n\t" "ds_read_b128 v[180:183], %[l0] offset:5120\n\t" "ds_read_b128 v[184:187], %[l0] offset:6144\n\t" \
    "ds_read_b128 v[188:191], %[l0] offset:7168\n\t" "s_nop 3\n\t" "buffer_load_dwordx4 v[252:255], v224, %[rs], s60 offen sc1\n\t" "v_mov_b64 v[200:201], 0\n\t" \
    "v_mov_b64 v[202:203], 0\n\t" "v_mov_b64 v[204:205], 0\n\t" "v_mov_b64 v[206:207], 0\n\t" "v_mov_b64 v[208:209], 0\n\t" "v_mov_b64 v[210:211], 0\n\t" \
    "v_mov_b64 v[212:213], 0\n\t" "v_mov_b64 v[214:215], 0\n\t" "v_mov_b64 v[216:217], 0\n\t" "v_mov_b64 v[218:219], 0\n\t" "v_mov_b64 v[220:221], 0\n\t" \
    "v_mov_b64 v[222:223], 0\n\t" "v_mov_b32 v226, -1\n\t" "buffer_store_dword v226, %[hq], %[rs], %[pr] offen sc1\n\t" "v_mov_b32 v227, 160512\n\t" "10:\n\t" \
    "s_waitcnt vmcnt(4)\n\t" XT(84, 85) "v_smfmac_f32_16x16x64_bf16 v[232:235], v[240:243], a[0:7], %[ix]\n\t" "ds_write_b128 %[l1], v[240:243] offset:0\n\t" \
    "v_smfmac_f32_16x16x64_bf16 v[236:239], v[240:243], a[8:15], %[ix]\n\t" "s_waitcnt vmcnt(3)\n\t" "v_smfmac_f32_16x16x64_bf16 v[232:235], v[244:247], a[16:23], %[ix]\n\t" \
    "ds_write_b128 %[l1], v[244:247] offset:1024\n\t" "s_waitcnt lgkmcnt(8)\n\t" "v_smfmac_f32_16x16x64_bf16 v[236:239], v[244:247], v[160:167], %[ix]\n\t" \
    "s_waitcnt lgkmcnt(0)\n\t" "s_barrier\n\t" XT(86, 87) "ds_read_b128 v[16:19], %[l1] offset:4096\n\t" "ds_read_b128 v[20:23], %[l1] offset:8192\n\t" \
    "ds_read_b128 v[24:27], %[l1] offset:12288\n\t" "s_waitcnt vmcnt(2)\n\t" "v_smfmac_f32_16x16x64_bf16 v[232:235], v[248:251], a[24:31], %[ix]\n\t" \
    "ds_write_b128 %[l1], v[248:251] offset:2048\n\t" "v_smfmac_f32_16x16x64_bf16 v[236:239], v[248:251], a[32:39], %[ix]\n\t" "ds_read_b128 v[28:31], %[l1] offset:5120\n\t" \
    "ds_read_b128 v[240:243], %[l1] offset:9216\n\t" "ds_read_b128 v[244:247], %[l1] offset:13312\n\t" "s_waitcnt vmcnt(1)\n\t" \
    "v_smfmac_f32_16x16x64_bf16 v[232:235], v[252:255], a[40:47], %[ix]\n\t" "ds_write_b128 %[l1], v[252:255] offset:3072\n\t" \
    "v_smfmac_f32_16x16x64_bf16 v[236:239], v[252:255], v[168:175], %[ix]\n\t" "ds_read_b128 v[160:163], %[l0] offset:8192\n\t" \
    "ds_read_b128 v[164:167], %[l0] offset:9216\n\t" "ds_read_b128 v[168:171], %[l0] offset:10240\n\t" "ds_read_b128 v[172:175], %[l0] offset:11264\n\t" "s_nop 1\n\t" \
    "v_cmp_u_f32 vcc, v232, v233\n\t" "v_cmp_u_f32 s[62:63], v234, v235\n\t" "s_waitcnt lgkmcnt(11)\n\t" \
    "v_smfmac_f32_16x16x64_bf16 v[232:235], v[16:19], a[48:55], %[ix]\n\t" "v_smfmac_f32_16x16x64_bf16 v[236:239], v[16:19], a[56:63], %[ix]\n\t" \
    "s_or_b64 vcc, vcc, s[62:63]\n\t" "s_cbranch_scc1 11f\n\t" "12:\n\t" "s_waitcnt lgkmcnt(10)\n\t" "v_smfmac_f32_16x16x64_bf16 v[232:235], v[20:23], a[64:71], %[ix]\n\t" \
    "v_smfmac_f32_16x16x64_bf16 v[236:239], v[20:23], v[176:183], %[ix]\n\t" "s_waitcnt lgkmcnt(4)\n\t" "s_barrier\n\t" XT(88, 89) "ds_read_b32 v225, v227\n\t" \
    "v_smfmac_f32_16x16x64_bf16 v[232:235], v[24:27], a[72:79], %[ix]\n\t" "ds_read_b128 v[176:179], %[l0] offset:12288\n\t" "ds_read_b128 v[180:183], %[l0] offset:13312\n\t" \
    "v_smfmac_f32_16x16x64_bf16 v[236:239], v[24:27], a[80:87], %[ix]\n\t" "ds_read_b128 v[248:251], %[l1] offset:6144\n\t" "ds_read_b128 v[252:255], %[l1] offset:10240\n\t" \
    "v_smfmac_f32_16x16x64_bf16 v[232:235], v[28:31], a[88:95], %[ix]\n\t" "ds_read_b128 v[16:19], %[l1] offset:14336\n\t" "ds_read_b128 v[20:23], %[l1] offset:7168\n\t" \
    "v_smfmac_f32_16x16x64_bf16 v[236:239], v[28:31], v[184:191], %[ix]\n\t" "s_memtime s[64:65]\n\t" "ds_read_b128 v[24:27], %[l1] offset:11264\n\t" \
    "v_smfmac_f32_16x16x64_bf16 v[232:235], v[240:243], a[96:103], %[ix]\n\t" "ds_read_b128 v[184:187], %[l0] offset:14336\n\t" \
    "ds_read_b128 v[188:191], %[l0] offset:15360\n\t" "v_smfmac_f32_16x16x64_bf16 v[236:239], v[240:243], a[104:111], %[ix]\n\t" "s_waitcnt lgkmcnt(9)\n\t" \
    "v_cmp_eq_u32 vcc, %[rt], v225\n\t" "v_smfmac_f32_16x16x64_bf16 v[232:235], v[244:247], a[112:119], %[ix]\n\t" "s_cbranch_vccnz 13f\n\t" \
    "ds_read_b128 v[28:31], %[l1] offset:15360\n\t" "v_smfmac_f32_16x16x64_bf16 v[236:239], v[244:247], v[160:167], %[ix]\n\t" \
    "ds_read_b128 v[160:163], %[l0] offset:16384\n\t" "ds_read_b128 v[164:167], %[l0] offset:17408\n\t" "s_waitcnt lgkmcnt(9)\n\t" \
    "v_smfmac_f32_16x16x64_bf16 v[232:235], v[248:251], a[120:127], %[ix]\n\t" "s_sub_u32 s62, s64, %[ca]\n\t" "s_mul_hi_u32 s63, s62, %[k1]\n\t" \
    "v_smfmac_f32_16x16x64_bf16 v[236:239], v[248:251], a[128:135], %[ix]\n\t" "s_mul_i32 s62, s62, %[k1]\n\t" "s_lshr_b64 s[62:63], s[62:63], 16\n\t" \
    "s_waitcnt lgkmcnt(8)\n\t" "v_smfmac_f32_16x16x64_bf16 v[232:235], v[252:255], a[136:143], %[ix]\n\t" "s_add_u32 s62, s62, %[ga]\n\t" "s_bitset0_b32 s62, 0\n\t" \
    "v_smfmac_f32_16x16x64_bf16 v[236:239], v[252:255], v[168:175], %[ix]\n\t" "v_mov_b32 v226, s62\n\t" "buffer_store_dword v226, %[hq], %[rs], s67 offen sc1\n\t" \
    "s_waitcnt lgkmcnt(7)\n\t" "v_smfmac_f32_16x16x64_bf16 v[232:235], v[16:19], a[144:151], %[ix]\n\t" "ds_read_b128 v[168:171], %[l0] offset:18432\n\t" \
    "ds_read_b128 v[172:175], %[l0] offset:19456\n\t" "v_smfmac_f32_16x16x64_bf16 v[236:239], v[16:19], a[152:159], %[ix]\n\t" "s_waitcnt lgkmcnt(8)\n\t" \
    "v_smfmac_f32_16x16x64_bf16 v[232:235], v[20:23], a[160:167], %[ix]\n\t" "v_smfmac_f32_16x16x64_bf16 v[236:239], v[20:23], v[176:183], %[ix]\n\t" \
    "ds_read_b128 v[176:179], %[l0] offset:20480\n\t" "ds_read_b128 v[180:183], %[l0] offset:21504\n\t" "s_waitcnt lgkmcnt(9)\n\t" \
    "v_smfmac_f32_16x16x64_bf16 v[232:235], v[24:27], a[168:175], %[ix]\n\t" "v_smfmac_f32_16x16x64_bf16 v[236:239], v[24:27], a[176:183], %[ix]\n\t" \
    "s_waitcnt lgkmcnt(6)\n\t" "v_smfmac_f32_16x16x64_bf16 v[232:235], v[28:31], a[184:191], %[ix]\n\t" \
    "v_smfmac_f32_16x16x64_bf16 v[236:239], v[28:31], v[184:191], %[ix]\n\t" "ds_read_b128 v[184:187], %[l0] offset:22528\n\t" \
    "ds_read_b128 v[188:191], %[l0] offset:23552\n\t" XT(90, 91) "s_nop 4\n\t" "v_add_f32 v232, v232, v233\n\t" "v_add_f32 v234, v234, v235\n\t" \
    "v_add_f32 v236, v236, v237\n\t" "v_add_f32 v238, v238, v239\n\t" "v_cvt_pk_bf16_f32 v236, v232, v236\n\t" "v_cvt_pk_bf16_f32 v237, v234, v238\n\t" \
    "ds_write2_b32 %[vw], v236, v237 offset1:68\n\t" "s_waitcnt lgkmcnt(0)\n\t" "s_barrier\n\t" XT(92, 93) "ds_read_b128 v[240:243], %[vr] offset:0\n\t" \
    "ds_read_b128 v[244:247], %[vr] offset:64\n\t" "ds_read_b128 v[248:251], %[vr] offset:128\n\t" "ds_read_b128 v[252:255], %[vr] offset:192\n\t" "s_mov_b32 s61, %[ub]\n\t" \
    "s_waitcnt lgkmcnt(3)\n\t" "v_smfmac_f32_16x16x64_bf16 v[192:195], v[240:243], v[32:39], %[ix]\n\t" \
    "v_smfmac_f32_16x16x64_bf16 v[196:199], v[240:243], v[48:55], %[ix]\n\t" "s_waitcnt lgkmcnt(2)\n\t" \
    "v_smfmac_f32_16x16x64_bf16 v[192:195], v[244:247], v[40:47], %[ix]\n\t" "v_smfmac_f32_16x16x64_bf16 v[196:199], v[244:247], v[56:63], %[ix]\n\t" \
    "s_waitcnt lgkmcnt(1)\n\t" "v_smfmac_f32_16x16x64_bf16 v[192:195], v[248:251], a[192:199], %[ix]\n\t" \
    "v_smfmac_f32_16x16x64_bf16 v[196:199], v[248:251], a[200:207], %[ix]\n\t" "s_waitcnt lgkmcnt(0)\n\t" \
    "v_smfmac_f32_16x16x64_bf16 v[192:195], v[252:255], v[160:167], %[ix]\n\t" "ds_read_b128 v[160:163], %[l0] offset:24576\n\t" \
    "ds_read_b128 v[164:167], %[l0] offset:25600\n\t" "v_smfmac_f32_16x16x64_bf16 v[196:199], v[252:255], v[168:175], %[ix]\n\t" \
    "ds_read_b128 v[168:171], %[l0] offset:26624\n\t" "ds_read_b128 v[172:175], %[l0] offset:27648\n\t" \
    "v_smfmac_f32_16x16x64_bf16 v[200:203], v[240:243], v[64:71], %[ix]\n\t" "s_nop 1\n\t" "v_smfmac_f32_16x16x64_bf16 v[204:207], v[240:243], v[80:87], %[ix]\n\t" \
    "v_cvt_pk_bf16_f32 v224, v192, v194\n\t" "v_cvt_pk_bf16_f32 v225, v193, v195\n\t" "v_smfmac_f32_16x16x64_bf16 v[200:203], v[244:247], v[72:79], %[ix]\n\t" \
    "v_cvt_pk_bf16_f32 v226, v196, v198\n\t" "v_cvt_pk_bf16_f32 v227, v197, v199\n\t" "v_smfmac_f32_16x16x64_bf16 v[204:207], v[244:247], v[88:95], %[ix]\n\t" "s_nop 0\n\t" \
    "v_permlane32_swap_b32 v224, v226\n\t" "v_smfmac_f32_16x16x64_bf16 v[200:203], v[248:251], a[208:215], %[ix]\n\t" "v_permlane32_swap_b32 v225, v227\n\t" "s_nop 0\n\t" \
    "v_smfmac_f32_16x16x64_bf16 v[204:207], v[248:251], a[216:223], %[ix]\n\t" "v_permlane16_swap_b32 v224, v225\n\t" "v_permlane16_swap_b32 v226, v227\n\t" \
    "v_smfmac_f32_16x16x64_bf16 v[200:203], v[252:255], v[176:183], %[ix]\n\t" "ds_read_b128 v[176:179], %[l0] offset:28672\n\t" \
    "buffer_store_dwordx4 v[224:227], %[uo], %[rs], s61 offen sc1\n\t" "s_xor_b32 s61, s61, 0x40000\n\t" XT(94, 95) \
    "v_smfmac_f32_16x16x64_bf16 v[204:207], v[252:255], v[184:191], %[ix]\n\t" "ds_read_b128 v[180:183], %[l0] offset:29696\n\t" \
    "ds_read_b128 v[184:187], %[l0] offset:30720\n\t" "v_smfmac_f32_16x16x64_bf16 v[208:211], v[240:243], v[96:103], %[ix]\n\t" \
    "ds_read_b128 v[188:191], %[l0] offset:31744\n\t" "s_nop 0\n\t" "v_smfmac_f32_16x16x64_bf16 v[212:215], v[240:243], v[112:119], %[ix]\n\t" \
    "v_cvt_pk_bf16_f32 v228, v200, v202\n\t" "v_cvt_pk_bf16_f32 v229, v201, v203\n\t" "v_smfmac_f32_16x16x64_bf16 v[208:211], v[244:247], v[104:111], %[ix]\n\t" \
    "v_cvt_pk_bf16_f32 v230, v204, v206\n\t" "v_cvt_pk_bf16_f32 v231, v205, v207\n\t" "v_smfmac_f32_16x16x64_bf16 v[212:215], v[244:247], v[120:127], %[ix]\n\t" "s_nop 0\n\t" \
    "v_permlane32_swap_b32 v228, v230\n\t" "v_smfmac_f32_16x16x64_bf16 v[208:211], v[248:251], a[224:231], %[ix]\n\t" "v_permlane32_swap_b32 v229, v231\n\t" "s_nop 0\n\t" \
    "v_smfmac_f32_16x16x64_bf16 v[212:215], v[248:251], a[232:239], %[ix]\n\t" "v_permlane16_swap_b32 v228, v229\n\t" "v_permlane16_swap_b32 v230, v231\n\t" \
    "s_waitcnt lgkmcnt(6)\n\t" "v_smfmac_f32_16x16x64_bf16 v[208:211], v[252:255], v[160:167], %[ix]\n\t" "buffer_store_dwordx4 v[228:231], %[uo], %[rs], s61 offen sc1\n\t" \
    "s_xor_b32 s61, s61, 0xc0000\n\t" "s_waitcnt lgkmcnt(4)\n\t" "v_smfmac_f32_16x16x64_bf16 v[212:215], v[252:255], v[168:175], %[ix]\n\t" "s_nop 1\n\t" \
    "v_smfmac_f32_16x16x64_bf16 v[216:219], v[240:243], v[128:135], %[ix]\n\t" "s_nop 1\n\t" "v_smfmac_f32_16x16x64_bf16 v[220:223], v[240:243], v[144:151], %[ix]\n\t" \
    "v_cvt_pk_bf16_f32 v224, v208, v210\n\t" "v_cvt_pk_bf16_f32 v225, v209, v211\n\t" "v_smfmac_f32_16x16x64_bf16 v[216:219], v[244:247], v[136:143], %[ix]\n\t" \
    "v_cvt_pk_bf16_f32 v226, v212, v214\n\t" "v_cvt_pk_bf16_f32 v227, v213, v215\n\t" "v_smfmac_f32_16x16x64_bf16 v[220:223], v[244:247], v[152:159], %[ix]\n\t" "s_nop 0\n\t" \
    "v_permlane32_swap_b32 v224, v226\n\t" "v_smfmac_f32_16x16x64_bf16 v[216:219], v[248:251], a[240:247], %[ix]\n\t" "v_permlane32_swap_b32 v225, v227\n\t" "s_nop 0\n\t" \
    "v_smfmac_f32_16x16x64_bf16 v[220:223], v[248:251], a[248:255], %[ix]\n\t" "v_permlane16_swap_b32 v224, v225\n\t" "v_permlane16_swap_b32 v226, v227\n\t" \
    "s_waitcnt lgkmcnt(2)\n\t" "v_smfmac_f32_16x16x64_bf16 v[216:219], v[252:255], v[176:183], %[ix]\n\t" "buffer_store_dwordx4 v[224:227], %[uo], %[rs], s61 offen sc1\n\t" \
    "s_xor_b32 s61, s61, 0x40000\n\t" "s_waitcnt lgkmcnt(0)\n\t" "v_smfmac_f32_16x16x64_bf16 v[220:223], v[252:255], v[184:191], %[ix]\n\t" "s_nop 5\n\t" \
    "v_cvt_pk_bf16_f32 v228, v216, v218\n\t" "v_cvt_pk_bf16_f32 v229, v217, v219\n\t" "v_cvt_pk_bf16_f32 v230, v220, v222\n\t" "v_cvt_pk_bf16_f32 v231, v221, v223\n\t" \
    "s_nop 0\n\t" "v_permlane32_swap_b32 v228, v230\n\t" "v_permlane32_swap_b32 v229, v231\n\t" "s_nop 1\n\t" "v_permlane16_swap_b32 v228, v229\n\t" \
    "v_permlane16_swap_b32 v230, v231\n\t" "buffer_store_dwordx4 v[228:231], %[uo], %[rs], s61 offen sc1\n\t" "s_nop 1\n\t" XT(96, 97) "s_branch 99f\n\t" "11:\n\t" \
    "ds_read_b128 v[228:231], %[l1] offset:0\n\t" "s_waitcnt lgkmcnt(0)\n\t" "v_max3_u32 v225, v228, v229, v230\n\t" "v_max_u32 v225, v225, v231\n\t" \
    "ds_read_b128 v[228:231], %[l1] offset:1024\n\t" "s_waitcnt lgkmcnt(0)\n\t" "v_max3_u32 v225, v225, v228, v229\n\t" "v_max3_u32 v225, v225, v230, v231\n\t" \
    "ds_read_b128 v[228:231], %[l1] offset:2048\n\t" "s_waitcnt lgkmcnt(0)\n\t" "v_max3_u32 v225, v225, v228, v229\n\t" "v_max3_u32 v225, v225, v230, v231\n\t" \
    "ds_read_b128 v[228:231], %[l1] offset:3072\n\t" "s_waitcnt lgkmcnt(0)\n\t" "v_max3_u32 v225, v225, v228, v229\n\t" "v_max3_u32 v225, v225, v230, v231\n\t" \
    "v_cmp_eq_u32 vcc, -1, v225\n\t" "s_cbranch_vccz 12b\n\t" "s_cmp_eq_u32 %[rt], 1\n\t" "s_cbranch_scc1 12b\n\t" "v_mov_b32 v225, %[rt]\n\t" "ds_write_b32 v227, v225\n\t" \
    "s_waitcnt lgkmcnt(0)\n\t" "s_branch 12b\n\t" "13:\n\t" "s_sub_u32 %[rt], %[rt], 1\n\t" "s_waitcnt lgkmcnt(0)\n\t" XCNT(100) "14:\n\t" \
    "buffer_load_dwordx4 v[240:243], %[mo], %[rs], %[sb] offen sc1\n\t" "buffer_load_dwordx4 v[244:247], v224, %[rs], %[sb] offen sc1\n\t" \
    "buffer_load_dwordx4 v[248:251], %[mo], %[rs], s60 offen sc1\n\t" "buffer_load_dwordx4 v[252:255], v224, %[rs], s60 offen sc1\n\t" "s_waitcnt vmcnt(0)\n\t" \
    "v_max_u32 v225, v240, v241\n\t" "v_max3_u32 v225, v225, v242, v243\n\t" "v_max3_u32 v225, v225, v244, v245\n\t" "v_max3_u32 v225, v225, v246, v247\n\t" \
    "v_max3_u32 v225, v225, v248, v249\n\t" "v_max3_u32 v225, v225, v250, v251\n\t" "v_max3_u32 v225, v225, v252, v253\n\t" "v_max3_u32 v225, v225, v254, v255\n\t" \
    "v_cmp_eq_u32 vcc, -1, v225\n\t" "s_cbranch_vccz 15f\n\t" "s_cmp_eq_u32 %[bd], 0\n\t" "s_cbranch_scc1 15f\n\t" "s_sub_u32 %[bd], %[bd], 1\n\t" XCNT(101) \
    "s_branch 14b\n\t" "15:\n\t" "ds_read_b128 v[160:163], %[l0] offset:0\n\t" "ds_read_b128 v[164:167], %[l0] offset:1024\n\t" \
    "ds_read_b128 v[168:171], %[l0] offset:2048\n\t" "ds_read_b128 v[172:175], %[l0] offset:3072\n\t" "ds_read_b128 v[176:179], %[l0] offset:4096\n\t" \
    "ds_read_b128 v[180:183], %[l0] offset:5120\n\t" "ds_read_b128 v[184:187], %[l0] offset:6144\n\t" "ds_read_b128 v[188:191], %[l0] offset:7168\n\t" \
    "v_mov_b64 v[232:233], 0\n\t" "v_mov_b64 v[234:235], 0\n\t" "v_mov_b64 v[236:237], 0\n\t" "v_mov_b64 v[238:239], 0\n\t" "s_barrier\n\t" "s_memtime s[64:65]\n\t" \
    "s_waitcnt lgkmcnt(0)\n\t" "s_add_u32 s64, s64, %[dr]\n\t" "s_sub_u32 s62, s64, %[ca]\n\t" "s_mul_hi_u32 s63, s62, %[k1]\n\t" "s_mul_i32 s62, s62, %[k1]\n\t" \
    "s_lshr_b64 s[62:63], s[62:63], 16\n\t" "s_add_u32 s62, s62, %[ga]\n\t" "s_bitset0_b32 s62, 0\n\t" "v_mov_b32 v225, s62\n\t" \
    "buffer_store_dword v225, %[hq], %[rs], s67 offen sc1\n\t" "s_mov_b32 s67, 0x7f000000\n\t" "s_branch 10b\n\t" "91:\n\t" XT(80, 81) "s_barrier\n\t" XT(82, 83) \
    "buffer_load_dwordx4 v[240:243], %[mo], %[rs], %[sb] offen sc1\n\t" "v_mov_b64 v[232:233], 0\n\t" "v_mov_b64 v[234:235], 0\n\t" "v_mov_b64 v[236:237], 0\n\t" \
    "v_mov_b64 v[238:239], 0\n\t" "v_mov_b64 v[192:193], 0\n\t" "v_mov_b64 v[194:195], 0\n\t" "v_mov_b64 v[196:197], 0\n\t" "v_mov_b64 v[198:199], 0\n\t" \
    "buffer_load_dwordx4 v[244:247], v224, %[rs], %[sb] offen sc1\n\t" "ds_read_b128 v[160:163], %[l0] offset:0\n\t" "ds_read_b128 v[164:167], %[l0] offset:1024\n\t" \
    "ds_read_b128 v[168:171], %[l0] offset:2048\n\t" "ds_read_b128 v[172:175], %[l0] offset:3072\n\t" "s_nop 3\n\t" \
    "buffer_load_dwordx4 v[248:251], %[mo], %[rs], s60 offen sc1\n\t" "ds_read_b128 v[176:179], %[l0] offset:4096\n\t" "ds_read_b128 v[180:183], %[l0] offset:5120\n\t" \
    "ds_read_b128 v[184:187], %[l0] offset:6144\n\t" "ds_read_b128 v[188:191], %[l0] offset:7168\n\t" "s_nop 3\n\t" \
    "buffer_load_dwordx4 v[252:255], v224, %[rs], s60 offen sc1\n\t" "v_mov_b64 v[200:201], 0\n\t" "v_mov_b64 v[202:203], 0\n\t" "v_mov_b64 v[204:205], 0\n\t" \
    "v_mov_b64 v[206:207], 0\n\t" "v_mov_b64 v[208:209], 0\n\t" "v_mov_b64 v[210:211], 0\n\t" "v_mov_b64 v[212:213], 0\n\t" "v_mov_b64 v[214:215], 0\n\t" \
    "v_mov_b64 v[216:217], 0\n\t" "v_mov_b64 v[218:219], 0\n\t" "v_mov_b64 v[220:221], 0\n\t" "v_mov_b64 v[222:223], 0\n\t" "v_mov_b32 v227, 160512\n\t" "20:\n\t" \
    "s_waitcnt vmcnt(3)\n\t" XT(84, 85) "v_smfmac_f32_16x16x64_bf16 v[232:235], v[240:243], a[0:7], %[ix]\n\t" "ds_write_b128 %[l1], v[240:243] offset:4096\n\t" \
    "v_smfmac_f32_16x16x64_bf16 v[236:239], v[240:243], a[8:15], %[ix]\n\t" "s_waitcnt vmcnt(2)\n\t" "v_smfmac_f32_16x16x64_bf16 v[232:235], v[244:247], a[16:23], %[ix]\n\t" \
    "ds_write_b128 %[l1], v[244:247] offset:5120\n\t" "s_waitcnt lgkmcnt(8)\n\t" "v_smfmac_f32_16x16x64_bf16 v[236:239], v[244:247], v[160:167], %[ix]\n\t" \
    "s_waitcnt lgkmcnt(0)\n\t" "s_barrier\n\t" XT(86, 87) "ds_read_b128 v[16:19], %[l1] offset:8192\n\t" "ds_read_b128 v[20:23], %[l1] offset:12288\n\t" \
    "ds_read_b128 v[24:27], %[l1] offset:0\n\t" "s_waitcnt vmcnt(1)\n\t" "v_smfmac_f32_16x16x64_bf16 v[232:235], v[248:251], a[24:31], %[ix]\n\t" \
    "ds_write_b128 %[l1], v[248:251] offset:6144\n\t" "v_smfmac_f32_16x16x64_bf16 v[236:239], v[248:251], a[32:39], %[ix]\n\t" "ds_read_b128 v[28:31], %[l1] offset:9216\n\t" \
    "ds_read_b128 v[240:243], %[l1] offset:13312\n\t" "ds_read_b128 v[244:247], %[l1] offset:1024\n\t" "s_waitcnt vmcnt(0)\n\t" \
    "v_smfmac_f32_16x16x64_bf16 v[232:235], v[252:255], a[40:47], %[ix]\n\t" "ds_write_b128 %[l1], v[252:255] offset:7168\n\t" \
    "v_smfmac_f32_16x16x64_bf16 v[236:239], v[252:255], v[168:175], %[ix]\n\t" "ds_read_b128 v[160:163], %[l0] offset:8192\n\t" \
    "ds_read_b128 v[164:167], %[l0] offset:9216\n\t" "ds_read_b128 v[168:171], %[l0] offset:10240\n\t" "ds_read_b128 v[172:175], %[l0] offset:11264\n\t" "s_nop 1\n\t" \
    "v_cmp_u_f32 vcc, v232, v233\n\t" "v_cmp_u_f32 s[62:63], v234, v235\n\t" "s_waitcnt lgkmcnt(11)\n\t" \
    "v_smfmac_f32_16x16x64_bf16 v[232:235], v[16:19], a[48:55], %[ix]\n\t" "v_smfmac_f32_16x16x64_bf16 v[236:239], v[16:19], a[56:63], %[ix]\n\t" \
    "s_or_b64 vcc, vcc, s[62:63]\n\t" "s_cbranch_scc1 21f\n\t" "22:\n\t" "s_waitcnt lgkmcnt(10)\n\t" "v_smfmac_f32_16x16x64_bf16 v[232:235], v[20:23], a[64:71], %[ix]\n\t" \
    "v_smfmac_f32_16x16x64_bf16 v[236:239], v[20:23], v[176:183], %[ix]\n\t" "s_waitcnt lgkmcnt(4)\n\t" "s_barrier\n\t" XT(88, 89) "ds_read_b32 v225, v227\n\t" \
    "v_smfmac_f32_16x16x64_bf16 v[232:235], v[24:27], a[72:79], %[ix]\n\t" "ds_read_b128 v[176:179], %[l0] offset:12288\n\t" "ds_read_b128 v[180:183], %[l0] offset:13312\n\t" \
    "v_smfmac_f32_16x16x64_bf16 v[236:239], v[24:27], a[80:87], %[ix]\n\t" "ds_read_b128 v[248:251], %[l1] offset:10240\n\t" "ds_read_b128 v[252:255], %[l1] offset:14336\n\t" \
    "v_smfmac_f32_16x16x64_bf16 v[232:235], v[28:31], a[88:95], %[ix]\n\t" "ds_read_b128 v[16:19], %[l1] offset:2048\n\t" "ds_read_b128 v[20:23], %[l1] offset:11264\n\t" \
    "v_smfmac_f32_16x16x64_bf16 v[236:239], v[28:31], v[184:191], %[ix]\n\t" "ds_read_b128 v[24:27], %[l1] offset:15360\n\t" "ds_read_b128 v[184:187], %[l0] offset:14336\n\t" \
    "v_smfmac_f32_16x16x64_bf16 v[232:235], v[240:243], a[96:103], %[ix]\n\t" "ds_read_b128 v[188:191], %[l0] offset:15360\n\t" "ds_read_b128 v[28:31], %[l1] offset:3072\n\t" \
    "v_smfmac_f32_16x16x64_bf16 v[236:239], v[240:243], a[104:111], %[ix]\n\t" "s_waitcnt lgkmcnt(10)\n\t" "v_cmp_eq_u32 vcc, %[rt], v225\n\t" \
    "v_smfmac_f32_16x16x64_bf16 v[232:235], v[244:247], a[112:119], %[ix]\n\t" "s_cbranch_vccnz 23f\n\t" \
    "v_smfmac_f32_16x16x64_bf16 v[236:239], v[244:247], v[160:167], %[ix]\n\t" "ds_read_b128 v[160:163], %[l0] offset:16384\n\t" \
    "ds_read_b128 v[164:167], %[l0] offset:17408\n\t" "s_waitcnt lgkmcnt(9)\n\t" "v_smfmac_f32_16x16x64_bf16 v[232:235], v[248:251], a[120:127], %[ix]\n\t" \
    "v_smfmac_f32_16x16x64_bf16 v[236:239], v[248:251], a[128:135], %[ix]\n\t" "s_waitcnt lgkmcnt(8)\n\t" \
    "v_smfmac_f32_16x16x64_bf16 v[232:235], v[252:255], a[136:143], %[ix]\n\t" "v_smfmac_f32_16x16x64_bf16 v[236:239], v[252:255], v[168:175], %[ix]\n\t" \
    "ds_read_b128 v[168:171], %[l0] offset:18432\n\t" "ds_read_b128 v[172:175], %[l0] offset:19456\n\t" "s_waitcnt lgkmcnt(9)\n\t" \
    "v_smfmac_f32_16x16x64_bf16 v[232:235], v[16:19], a[144:151], %[ix]\n\t" "v_smfmac_f32_16x16x64_bf16 v[236:239], v[16:19], a[152:159], %[ix]\n\t" \
    "s_waitcnt lgkmcnt(8)\n\t" "v_smfmac_f32_16x16x64_bf16 v[232:235], v[20:23], a[160:167], %[ix]\n\t" \
    "v_smfmac_f32_16x16x64_bf16 v[236:239], v[20:23], v[176:183], %[ix]\n\t" "ds_read_b128 v[176:179], %[l0] offset:20480\n\t" \
    "ds_read_b128 v[180:183], %[l0] offset:21504\n\t" "s_waitcnt lgkmcnt(9)\n\t" "v_smfmac_f32_16x16x64_bf16 v[232:235], v[24:27], a[168:175], %[ix]\n\t" \
    "v_smfmac_f32_16x16x64_bf16 v[236:239], v[24:27], a[176:183], %[ix]\n\t" "s_waitcnt lgkmcnt(6)\n\t" \
    "v_smfmac_f32_16x16x64_bf16 v[232:235], v[28:31], a[184:191], %[ix]\n\t" "v_smfmac_f32_16x16x64_bf16 v[236:239], v[28:31], v[184:191], %[ix]\n\t" \
    "ds_read_b128 v[184:187], %[l0] offset:22528\n\t" "ds_read_b128 v[188:191], %[l0] offset:23552\n\t" XT(90, 91) "s_nop 4\n\t" "v_add_f32 v232, v232, v233\n\t" \
    "v_add_f32 v234, v234, v235\n\t" "v_add_f32 v236, v236, v237\n\t" "v_add_f32 v238, v238, v239\n\t" "v_cvt_pk_bf16_f32 v236, v232, v236\n\t" \
    "v_cvt_pk_bf16_f32 v237, v234, v238\n\t" "ds_write2_b32 %[vw], v236, v237 offset1:68\n\t" "s_waitcnt lgkmcnt(0)\n\t" "s_barrier\n\t" XT(92, 93) \
    "ds_read_b128 v[240:243], %[vr] offset:0\n\t" "ds_read_b128 v[244:247], %[vr] offset:64\n\t" "ds_read_b128 v[248:251], %[vr] offset:128\n\t" \
    "ds_read_b128 v[252:255], %[vr] offset:192\n\t" "s_mov_b32 s61, %[ub]\n\t" "s_waitcnt lgkmcnt(3)\n\t" \
    "v_smfmac_f32_16x16x64_bf16 v[192:195], v[240:243], v[32:39], %[ix]\n\t" "v_smfmac_f32_16x16x64_bf16 v[196:199], v[240:243], v[48:55], %[ix]\n\t" \
    "s_waitcnt lgkmcnt(2)\n\t" "v_smfmac_f32_16x16x64_bf16 v[192:195], v[244:247], v[40:47], %[ix]\n\t" \
    "v_smfmac_f32_16x16x64_bf16 v[196:199], v[244:247], v[56:63], %[ix]\n\t" "s_waitcnt lgkmcnt(1)\n\t" \
    "v_smfmac_f32_16x16x64_bf16 v[192:195], v[248:251], a[192:199], %[ix]\n\t" "v_smfmac_f32_16x16x64_bf16 v[196:199], v[248:251], a[200:207], %[ix]\n\t" \
    "s_waitcnt lgkmcnt(0)\n\t" "v_smfmac_f32_16x16x64_bf16 v[192:195], v[252:255], v[160:167], %[ix]\n\t" "ds_read_b128 v[160:163], %[l0] offset:24576\n\t" \
    "ds_read_b128 v[164:167], %[l0] offset:25600\n\t" "v_smfmac_f32_16x16x64_bf16 v[196:199], v[252:255], v[168:175], %[ix]\n\t" \
    "ds_read_b128 v[168:171], %[l0] offset:26624\n\t" "ds_read_b128 v[172:175], %[l0] offset:27648\n\t" \
    "v_smfmac_f32_16x16x64_bf16 v[200:203], v[240:243], v[64:71], %[ix]\n\t" "s_nop 1\n\t" "v_smfmac_f32_16x16x64_bf16 v[204:207], v[240:243], v[80:87], %[ix]\n\t" \
    "v_cvt_pk_bf16_f32 v224, v192, v194\n\t" "v_cvt_pk_bf16_f32 v225, v193, v195\n\t" "v_smfmac_f32_16x16x64_bf16 v[200:203], v[244:247], v[72:79], %[ix]\n\t" \
    "v_cvt_pk_bf16_f32 v226, v196, v198\n\t" "v_cvt_pk_bf16_f32 v227, v197, v199\n\t" "v_smfmac_f32_16x16x64_bf16 v[204:207], v[244:247], v[88:95], %[ix]\n\t" "s_nop 0\n\t" \
    "v_permlane32_swap_b32 v224, v226\n\t" "v_smfmac_f32_16x16x64_bf16 v[200:203], v[248:251], a[208:215], %[ix]\n\t" "v_permlane32_swap_b32 v225, v227\n\t" "s_nop 0\n\t" \
    "v_smfmac_f32_16x16x64_bf16 v[204:207], v[248:251], a[216:223], %[ix]\n\t" "v_permlane16_swap_b32 v224, v225\n\t" "v_permlane16_swap_b32 v226, v227\n\t" \
    "v_smfmac_f32_16x16x64_bf16 v[200:203], v[252:255], v[176:183], %[ix]\n\t" "ds_read_b128 v[176:179], %[l0] offset:28672\n\t" \
    "buffer_store_dwordx4 v[224:227], %[uo], %[rs], s61 offen sc1\n\t" "s_xor_b32 s61, s61, 0x40000\n\t" XT(94, 95) \
    "v_smfmac_f32_16x16x64_bf16 v[204:207], v[252:255], v[184:191], %[ix]\n\t" "ds_read_b128 v[180:183], %[l0] offset:29696\n\t" \
    "ds_read_b128 v[184:187], %[l0] offset:30720\n\t" "v_smfmac_f32_16x16x64_bf16 v[208:211], v[240:243], v[96:103], %[ix]\n\t" \
    "ds_read_b128 v[188:191], %[l0] offset:31744\n\t" "s_nop 0\n\t" "v_smfmac_f32_16x16x64_bf16 v[212:215], v[240:243], v[112:119], %[ix]\n\t" \
    "v_cvt_pk_bf16_f32 v228, v200, v202\n\t" "v_cvt_pk_bf16_f32 v229, v201, v203\n\t" "v_smfmac_f32_16x16x64_bf16 v[208:211], v[244:247], v[104:111], %[ix]\n\t" \
    "v_cvt_pk_bf16_f32 v230, v204, v206\n\t" "v_cvt_pk_bf16_f32 v231, v205, v207\n\t" "v_smfmac_f32_16x16x64_bf16 v[212:215], v[244:247], v[120:127], %[ix]\n\t" "s_nop 0\n\t" \
    "v_permlane32_swap_b32 v228, v230\n\t" "v_smfmac_f32_16x16x64_bf16 v[208:211], v[248:251], a[224:231], %[ix]\n\t" "v_permlane32_swap_b32 v229, v231\n\t" "s_nop 0\n\t" \
    "v_smfmac_f32_16x16x64_bf16 v[212:215], v[248:251], a[232:239], %[ix]\n\t" "v_permlane16_swap_b32 v228, v229\n\t" "v_permlane16_swap_b32 v230, v231\n\t" \
    "s_waitcnt lgkmcnt(6)\n\t" "v_smfmac_f32_16x16x64_bf16 v[208:211], v[252:255], v[160:167], %[ix]\n\t" "buffer_store_dwordx4 v[228:231], %[uo], %[rs], s61 offen sc1\n\t" \
    "s_xor_b32 s61, s61, 0xc0000\n\t" "s_waitcnt lgkmcnt(4)\n\t" "v_smfmac_f32_16x16x64_bf16 v[212:215], v[252:255], v[168:175], %[ix]\n\t" "s_nop 1\n\t" \
    "v_smfmac_f32_16x16x64_bf16 v[216:219], v[240:243], v[128:135], %[ix]\n\t" "s_nop 1\n\t" "v_smfmac_f32_16x16x64_bf16 v[220:223], v[240:243], v[144:151], %[ix]\n\t" \
    "v_cvt_pk_bf16_f32 v224, v208, v210\n\t" "v_cvt_pk_bf16_f32 v225, v209, v211\n\t" "v_smfmac_f32_16x16x64_bf16 v[216:219], v[244:247], v[136:143], %[ix]\n\t" \
    "v_cvt_pk_bf16_f32 v226, v212, v214\n\t" "v_cvt_pk_bf16_f32 v227, v213, v215\n\t" "v_smfmac_f32_16x16x64_bf16 v[220:223], v[244:247], v[152:159], %[ix]\n\t" "s_nop 0\n\t" \
    "v_permlane32_swap_b32 v224, v226\n\t" "v_smfmac_f32_16x16x64_bf16 v[216:219], v[248:251], a[240:247], %[ix]\n\t" "v_permlane32_swap_b32 v225, v227\n\t" "s_nop 0\n\t" \
    "v_smfmac_f32_16x16x64_bf16 v[220:223], v[248:251], a[248:255], %[ix]\n\t" "v_permlane16_swap_b32 v224, v225\n\t" "v_permlane16_swap_b32 v226, v227\n\t" \
    "s_waitcnt lgkmcnt(2)\n\t" "v_smfmac_f32_16x16x64_bf16 v[216:219], v[252:255], v[176:183], %[ix]\n\t" "buffer_store_dwordx4 v[224:227], %[uo], %[rs], s61 offen sc1\n\t" \
    "s_xor_b32 s61, s61, 0x40000\n\t" "s_waitcnt lgkmcnt(0)\n\t" "v_smfmac_f32_16x16x64_bf16 v[220:223], v[252:255], v[184:191], %[ix]\n\t" "s_nop 5\n\t" \
    "v_cvt_pk_bf16_f32 v228, v216, v218\n\t" "v_cvt_pk_bf16_f32 v229, v217, v219\n\t" "v_cvt_pk_bf16_f32 v230, v220, v222\n\t" "v_cvt_pk_bf16_f32 v231, v221, v223\n\t" \
    "s_nop 0\n\t" "v_permlane32_swap_b32 v228, v230\n\t" "v_permlane32_swap_b32 v229, v231\n\t" "s_nop 1\n\t" "v_permlane16_swap_b32 v228, v229\n\t" \
    "v_permlane16_swap_b32 v230, v231\n\t" "buffer_store_dwordx4 v[228:231], %[uo], %[rs], s61 offen sc1\n\t" "s_nop 1\n\t" XT(96, 97) "v_mov_b64 v[16:17], -1\n\t" \
    "v_mov_b64 v[18:19], -1\n\t" "s_barrier\n\t" "buffer_store_dwordx4 v[16:19], %[l1], %[rs], %[ur] offen offset:0 sc1\n\t" \
    "buffer_store_dwordx4 v[16:19], %[l1], %[rs], %[ur] offen offset:1024 sc1\n\t" "buffer_store_dwordx4 v[16:19], %[l1], %[rs], %[ur] offen offset:2048 sc1\n\t" \
    "buffer_store_dwordx4 v[16:19], %[l1], %[rs], %[ur] offen offset:3072 sc1\n\t" "s_sub_u32 s61, %[ur], 0x1000\n\t" \
    "buffer_store_dwordx4 v[16:19], %[l1], %[rs], s61 offen offset:0 sc1\n\t" "buffer_store_dwordx4 v[16:19], %[l1], %[rs], s61 offen offset:1024 sc1\n\t" "s_branch 99f\n\t" \
    "21:\n\t" "ds_read_b128 v[228:231], %[l1] offset:4096\n\t" "s_waitcnt lgkmcnt(0)\n\t" "v_max3_u32 v225, v228, v229, v230\n\t" "v_max_u32 v225, v225, v231\n\t" \
    "ds_read_b128 v[228:231], %[l1] offset:5120\n\t" "s_waitcnt lgkmcnt(0)\n\t" "v_max3_u32 v225, v225, v228, v229\n\t" "v_max3_u32 v225, v225, v230, v231\n\t" \
    "ds_read_b128 v[228:231], %[l1] offset:6144\n\t" "s_waitcnt lgkmcnt(0)\n\t" "v_max3_u32 v225, v225, v228, v229\n\t" "v_max3_u32 v225, v225, v230, v231\n\t" \
    "ds_read_b128 v[228:231], %[l1] offset:7168\n\t" "s_waitcnt lgkmcnt(0)\n\t" "v_max3_u32 v225, v225, v228, v229\n\t" "v_max3_u32 v225, v225, v230, v231\n\t" \
    "v_cmp_eq_u32 vcc, -1, v225\n\t" "s_cbranch_vccz 22b\n\t" "s_cmp_eq_u32 %[rt], 1\n\t" "s_cbranch_scc1 22b\n\t" "v_mov_b32 v225, %[rt]\n\t" "ds_write_b32 v227, v225\n\t" \
    "s_waitcnt lgkmcnt(0)\n\t" "s_branch 22b\n\t" "23:\n\t" "s_sub_u32 %[rt], %[rt], 1\n\t" "s_waitcnt lgkmcnt(0)\n\t" XCNT(100) "24:\n\t" \
    "buffer_load_dwordx4 v[240:243], %[mo], %[rs], %[sb] offen sc1\n\t" "buffer_load_dwordx4 v[244:247], v224, %[rs], %[sb] offen sc1\n\t" \
    "buffer_load_dwordx4 v[248:251], %[mo], %[rs], s60 offen sc1\n\t" "buffer_load_dwordx4 v[252:255], v224, %[rs], s60 offen sc1\n\t" "s_waitcnt vmcnt(0)\n\t" \
    "v_max_u32 v225, v240, v241\n\t" "v_max3_u32 v225, v225, v242, v243\n\t" "v_max3_u32 v225, v225, v244, v245\n\t" "v_max3_u32 v225, v225, v246, v247\n\t" \
    "v_max3_u32 v225, v225, v248, v249\n\t" "v_max3_u32 v225, v225, v250, v251\n\t" "v_max3_u32 v225, v225, v252, v253\n\t" "v_max3_u32 v225, v225, v254, v255\n\t" \
    "v_cmp_eq_u32 vcc, -1, v225\n\t" "s_cbranch_vccz 25f\n\t" "s_cmp_eq_u32 %[bd], 0\n\t" "s_cbranch_scc1 25f\n\t" "s_sub_u32 %[bd], %[bd], 1\n\t" XCNT(101) \
    "s_branch 24b\n\t" "25:\n\t" "ds_read_b128 v[160:163], %[l0] offset:0\n\t" "ds_read_b128 v[164:167], %[l0] offset:1024\n\t" \
    "ds_read_b128 v[168:171], %[l0] offset:2048\n\t" "ds_read_b128 v[172:175], %[l0] offset:3072\n\t" "ds_read_b128 v[176:179], %[l0] offset:4096\n\t" \
    "ds_read_b128 v[180:183], %[l0] offset:5120\n\t" "ds_read_b128 v[184:187], %[l0] offset:6144\n\t" "ds_read_b128 v[188:191], %[l0] offset:7168\n\t" \
    "v_mov_b64 v[232:233], 0\n\t" "v_mov_b64 v[234:235], 0\n\t" "v_mov_b64 v[236:237], 0\n\t" "v_mov_b64 v[238:239], 0\n\t" "s_barrier\n\t" "s_branch 20b\n\t" "92:\n\t" \
    XT(80, 81) "s_barrier\n\t" XT(82, 83) "buffer_load_dwordx4 v[240:243], %[mo], %[rs], %[sb] offen sc1\n\t" "v_mov_b64 v[232:233], 0\n\t" "v_mov_b64 v[234:235], 0\n\t" \
    "v_mov_b64 v[236:237], 0\n\t" "v_mov_b64 v[238:239], 0\n\t" "v_mov_b64 v[192:193], 0\n\t" "v_mov_b64 v[194:195], 0\n\t" "v_mov_b64 v[196:197], 0\n\t" \
    "v_mov_b64 v[198:199], 0\n\t" "buffer_load_dwordx4 v[244:247], v224, %[rs], %[sb] offen sc1\n\t" "ds_read_b128 v[160:163], %[l0] offset:0\n\t" \
    "ds_read_b128 v[164:167], %[l0] offset:1024\n\t" "ds_read_b128 v[168:171], %[l0] offset:2048\n\t" "ds_read_b128 v[172:175], %[l0] offset:3072\n\t" "s_nop 3\n\t" \
    "buffer_load_dwordx4 v[248:251], %[mo], %[rs], s60 offen sc1\n\t" "ds_read_b128 v[176:179], %[l0] offset:4096\n\t" "ds_read_b128 v[180:183], %[l0] offset:5120\n\t" \
    "ds_read_b128 v[184:187], %[l0] offset:6144\n\t" "ds_read_b128 v[188:191], %[l0] offset:7168\n\t" "s_nop 3\n\t" \
    "buffer_load_dwordx4 v[252:255], v224, %[rs], s60 offen sc1\n\t" "v_mov_b64 v[200:201], 0\n\t" "v_mov_b64 v[202:203], 0\n\t" "v_mov_b64 v[204:205], 0\n\t" \
    "v_mov_b64 v[206:207], 0\n\t" "v_mov_b64 v[208:209], 0\n\t" "v_mov_b64 v[210:211], 0\n\t" "v_mov_b64 v[212:213], 0\n\t" "v_mov_b64 v[214:215], 0\n\t" \
    "v_mov_b64 v[216:217], 0\n\t" "v_mov_b64 v[218:219], 0\n\t" "v_mov_b64 v[220:221], 0\n\t" "v_mov_b64 v[222:223], 0\n\t" "v_mov_b32 v227, 160512\n\t" "30:\n\t" \
    "s_waitcnt vmcnt(3)\n\t" XT(84, 85) "v_smfmac_f32_16x16x64_bf16 v[232:235], v[240:243], a[0:7], %[ix]\n\t" "ds_write_b128 %[l1], v[240:243] offset:8192\n\t" \
    "v_smfmac_f32_16x16x64_bf16 v[236:239], v[240:243], a[8:15], %[ix]\n\t" "s_waitcnt vmcnt(2)\n\t" "v_smfmac_f32_16x16x64_bf16 v[232:235], v[244:247], a[16:23], %[ix]\n\t" \
    "ds_write_b128 %[l1], v[244:247] offset:9216\n\t" "s_waitcnt lgkmcnt(8)\n\t" "v_smfmac_f32_16x16x64_bf16 v[236:239], v[244:247], v[160:167], %[ix]\n\t" \
    "s_waitcnt lgkmcnt(0)\n\t" "s_barrier\n\t" XT(86, 87) "ds_read_b128 v[16:19], %[l1] offset:12288\n\t" "ds_read_b128 v[20:23], %[l1] offset:0\n\t" \
    "ds_read_b128 v[24:27], %[l1] offset:4096\n\t" "s_waitcnt vmcnt(1)\n\t" "v_smfmac_f32_16x16x64_bf16 v[232:235], v[248:251], a[24:31], %[ix]\n\t" \
    "ds_write_b128 %[l1], v[248:251] offset:10240\n\t" "v_smfmac_f32_16x16x64_bf16 v[236:239], v[248:251], a[32:39], %[ix]\n\t" \
    "ds_read_b128 v[28:31], %[l1] offset:13312\n\t" "ds_read_b128 v[240:243], %[l1] offset:1024\n\t" "ds_read_b128 v[244:247], %[l1] offset:5120\n\t" "s_waitcnt vmcnt(0)\n\t" \
    "v_smfmac_f32_16x16x64_bf16 v[232:235], v[252:255], a[40:47], %[ix]\n\t" "ds_write_b128 %[l1], v[252:255] offset:11264\n\t" \
    "v_smfmac_f32_16x16x64_bf16 v[236:239], v[252:255], v[168:175], %[ix]\n\t" "ds_read_b128 v[160:163], %[l0] offset:8192\n\t" \
    "ds_read_b128 v[164:167], %[l0] offset:9216\n\t" "ds_read_b128 v[168:171], %[l0] offset:10240\n\t" "ds_read_b128 v[172:175], %[l0] offset:11264\n\t" "s_nop 1\n\t" \
    "v_cmp_u_f32 vcc, v232, v233\n\t" "v_cmp_u_f32 s[62:63], v234, v235\n\t" "s_waitcnt lgkmcnt(11)\n\t" \
    "v_smfmac_f32_16x16x64_bf16 v[232:235], v[16:19], a[48:55], %[ix]\n\t" "v_smfmac_f32_16x16x64_bf16 v[236:239], v[16:19], a[56:63], %[ix]\n\t" \
    "s_or_b64 vcc, vcc, s[62:63]\n\t" "s_cbranch_scc1 31f\n\t" "32:\n\t" "s_waitcnt lgkmcnt(10)\n\t" "v_smfmac_f32_16x16x64_bf16 v[232:235], v[20:23], a[64:71], %[ix]\n\t" \
    "v_smfmac_f32_16x16x64_bf16 v[236:239], v[20:23], v[176:183], %[ix]\n\t" "s_waitcnt lgkmcnt(4)\n\t" "s_barrier\n\t" XT(88, 89) "ds_read_b32 v225, v227\n\t" \
    "v_smfmac_f32_16x16x64_bf16 v[232:235], v[24:27], a[72:79], %[ix]\n\t" "ds_read_b128 v[176:179], %[l0] offset:12288\n\t" "ds_read_b128 v[180:183], %[l0] offset:13312\n\t" \
    "v_smfmac_f32_16x16x64_bf16 v[236:239], v[24:27], a[80:87], %[ix]\n\t" "ds_read_b128 v[248:251], %[l1] offset:14336\n\t" "ds_read_b128 v[252:255], %[l1] offset:2048\n\t" \
    "v_smfmac_f32_16x16x64_bf16 v[232:235], v[28:31], a[88:95], %[ix]\n\t" "ds_read_b128 v[16:19], %[l1] offset:6144\n\t" "ds_read_b128 v[20:23], %[l1] offset:15360\n\t" \
    "v_smfmac_f32_16x16x64_bf16 v[236:239], v[28:31], v[184:191], %[ix]\n\t" "ds_read_b128 v[24:27], %[l1] offset:3072\n\t" "ds_read_b128 v[184:187], %[l0] offset:14336\n\t" \
    "v_smfmac_f32_16x16x64_bf16 v[232:235], v[240:243], a[96:103], %[ix]\n\t" "ds_read_b128 v[188:191], %[l0] offset:15360\n\t" "ds_read_b128 v[28:31], %[l1] offset:7168\n\t" \
    "v_smfmac_f32_16x16x64_bf16 v[236:239], v[240:243], a[104:111], %[ix]\n\t" "s_waitcnt lgkmcnt(10)\n\t" "v_cmp_eq_u32 vcc, %[rt], v225\n\t" \
    "v_smfmac_f32_16x16x64_bf16 v[232:235], v[244:247], a[112:119], %[ix]\n\t" "s_cbranch_vccnz 33f\n\t" \
    "v_smfmac_f32_16x16x64_bf16 v[236:239], v[244:247], v[160:167], %[ix]\n\t" "ds_read_b128 v[160:163], %[l0] offset:16384\n\t" \
    "ds_read_b128 v[164:167], %[l0] offset:17408\n\t" "s_waitcnt lgkmcnt(9)\n\t" "v_smfmac_f32_16x16x64_bf16 v[232:235], v[248:251], a[120:127], %[ix]\n\t" \
    "v_smfmac_f32_16x16x64_bf16 v[236:239], v[248:251], a[128:135], %[ix]\n\t" "s_waitcnt lgkmcnt(8)\n\t" \
    "v_smfmac_f32_16x16x64_bf16 v[232:235], v[252:255], a[136:143], %[ix]\n\t" "v_smfmac_f32_16x16x64_bf16 v[236:239], v[252:255], v[168:175], %[ix]\n\t" \
    "ds_read_b128 v[168:171], %[l0] offset:18432\n\t" "ds_read_b128 v[172:175], %[l0] offset:19456\n\t" "s_waitcnt lgkmcnt(9)\n\t" \
    "v_smfmac_f32_16x16x64_bf16 v[232:235], v[16:19], a[144:151], %[ix]\n\t" "v_smfmac_f32_16x16x64_bf16 v[236:239], v[16:19], a[152:159], %[ix]\n\t" \
    "s_waitcnt lgkmcnt(8)\n\t" "v_smfmac_f32_16x16x64_bf16 v[232:235], v[20:23], a[160:167], %[ix]\n\t" \
    "v_smfmac_f32_16x16x64_bf16 v[236:239], v[20:23], v[176:183], %[ix]\n\t" "ds_read_b128 v[176:179], %[l0] offset:20480\n\t" \
    "ds_read_b128 v[180:183], %[l0] offset:21504\n\t" "s_waitcnt lgkmcnt(9)\n\t" "v_smfmac_f32_16x16x64_bf16 v[232:235], v[24:27], a[168:175], %[ix]\n\t" \
    "v_smfmac_f32_16x16x64_bf16 v[236:239], v[24:27], a[176:183], %[ix]\n\t" "s_waitcnt lgkmcnt(6)\n\t" \
    "v_smfmac_f32_16x16x64_bf16 v[232:235], v[28:31], a[184:191], %[ix]\n\t" "v_smfmac_f32_16x16x64_bf16 v[236:239], v[28:31], v[184:191], %[ix]\n\t" \
    "ds_read_b128 v[184:187], %[l0] offset:22528\n\t" "ds_read_b128 v[188:191], %[l0] offset:23552\n\t" XT(90, 91) "s_nop 4\n\t" "v_add_f32 v232, v232, v233\n\t" \
    "v_add_f32 v234, v234, v235\n\t" "v_add_f32 v236, v236, v237\n\t" "v_add_f32 v238, v238, v239\n\t" "v_cvt_pk_bf16_f32 v236, v232, v236\n\t" \
    "v_cvt_pk_bf16_f32 v237, v234, v238\n\t" "ds_write2_b32 %[vw], v236, v237 offset1:68\n\t" "s_waitcnt lgkmcnt(0)\n\t" "s_barrier\n\t" XT(92, 93) \
    "ds_read_b128 v[240:243], %[vr] offset:0\n\t" "ds_read_b128 v[244:247], %[vr] offset:64\n\t" "ds_read_b128 v[248:251], %[vr] offset:128\n\t" \
    "ds_read_b128 v[252:255], %[vr] offset:192\n\t" "s_mov_b32 s61, %[ub]\n\t" "s_waitcnt lgkmcnt(3)\n\t" \
    "v_smfmac_f32_16x16x64_bf16 v[192:195], v[240:243], v[32:39], %[ix]\n\t" "v_smfmac_f32_16x16x64_bf16 v[196:199], v[240:243], v[48:55], %[ix]\n\t" \
    "s_waitcnt lgkmcnt(2)\n\t" "v_smfmac_f32_16x16x64_bf16 v[192:195], v[244:247], v[40:47], %[ix]\n\t" \
    "v_smfmac_f32_16x16x64_bf16 v[196:199], v[244:247], v[56:63], %[ix]\n\t" "s_waitcnt lgkmcnt(1)\n\t" \
    "v_smfmac_f32_16x16x64_bf16 v[192:195], v[248:251], a[192:199], %[ix]\n\t" "v_smfmac_f32_16x16x64_bf16 v[196:199], v[248:251], a[200:207], %[ix]\n\t" \
    "s_waitcnt lgkmcnt(0)\n\t" "v_smfmac_f32_16x16x64_bf16 v[192:195], v[252:255], v[160:167], %[ix]\n\t" "ds_read_b128 v[160:163], %[l0] offset:24576\n\t" \
    "ds_read_b128 v[164:167], %[l0] offset:25600\n\t" "v_smfmac_f32_16x16x64_bf16 v[196:199], v[252:255], v[168:175], %[ix]\n\t" \
    "ds_read_b128 v[168:171], %[l0] offset:26624\n\t" "ds_read_b128 v[172:175], %[l0] offset:27648\n\t" \
    "v_smfmac_f32_16x16x64_bf16 v[200:203], v[240:243], v[64:71], %[ix]\n\t" "s_nop 1\n\t" "v_smfmac_f32_16x16x64_bf16 v[204:207], v[240:243], v[80:87], %[ix]\n\t" \
    "v_cvt_pk_bf16_f32 v224, v192, v194\n\t" "v_cvt_pk_bf16_f32 v225, v193, v195\n\t" "v_smfmac_f32_16x16x64_bf16 v[200:203], v[244:247], v[72:79], %[ix]\n\t" \
    "v_cvt_pk_bf16_f32 v226, v196, v198\n\t" "v_cvt_pk_bf16_f32 v227, v197, v199\n\t" "v_smfmac_f32_16x16x64_bf16 v[204:207], v[244:247], v[88:95], %[ix]\n\t" "s_nop 0\n\t" \
    "v_permlane32_swap_b32 v224, v226\n\t" "v_smfmac_f32_16x16x64_bf16 v[200:203], v[248:251], a[208:215], %[ix]\n\t" "v_permlane32_swap_b32 v225, v227\n\t" "s_nop 0\n\t" \
    "v_smfmac_f32_16x16x64_bf16 v[204:207], v[248:251], a[216:223], %[ix]\n\t" "v_permlane16_swap_b32 v224, v225\n\t" "v_permlane16_swap_b32 v226, v227\n\t" \
    "v_smfmac_f32_16x16x64_bf16 v[200:203], v[252:255], v[176:183], %[ix]\n\t" "ds_read_b128 v[176:179], %[l0] offset:28672\n\t" \
    "buffer_store_dwordx4 v[224:227], %[uo], %[rs], s61 offen sc1\n\t" "s_xor_b32 s61, s61, 0x40000\n\t" XT(94, 95) \
    "v_smfmac_f32_16x16x64_bf16 v[204:207], v[252:255], v[184:191], %[ix]\n\t" "ds_read_b128 v[180:183], %[l0] offset:29696\n\t" \
    "ds_read_b128 v[184:187], %[l0] offset:30720\n\t" "v_smfmac_f32_16x16x64_bf16 v[208:211], v[240:243], v[96:103], %[ix]\n\t" \
    "ds_read_b128 v[188:191], %[l0] offset:31744\n\t" "s_nop 0\n\t" "v_smfmac_f32_16x16x64_bf16 v[212:215], v[240:243], v[112:119], %[ix]\n\t" \
    "v_cvt_pk_bf16_f32 v228, v200, v202\n\t" "v_cvt_pk_bf16_f32 v229, v201, v203\n\t" "v_smfmac_f32_16x16x64_bf16 v[208:211], v[244:247], v[104:111], %[ix]\n\t" \
    "v_cvt_pk_bf16_f32 v230, v204, v206\n\t" "v_cvt_pk_bf16_f32 v231, v205, v207\n\t" "v_smfmac_f32_16x16x64_bf16 v[212:215], v[244:247], v[120:127], %[ix]\n\t" "s_nop 0\n\t" \
    "v_permlane32_swap_b32 v228, v230\n\t" "v_smfmac_f32_16x16x64_bf16 v[208:211], v[248:251], a[224:231], %[ix]\n\t" "v_permlane32_swap_b32 v229, v231\n\t" "s_nop 0\n\t" \
    "v_smfmac_f32_16x16x64_bf16 v[212:215], v[248:251], a[232:239], %[ix]\n\t" "v_permlane16_swap_b32 v228, v229\n\t" "v_permlane16_swap_b32 v230, v231\n\t" \
    "s_waitcnt lgkmcnt(6)\n\t" "v_smfmac_f32_16x16x64_bf16 v[208:211], v[252:255], v[160:167], %[ix]\n\t" "buffer_store_dwordx4 v[228:231], %[uo], %[rs], s61 offen sc1\n\t" \
    "s_xor_b32 s61, s61, 0xc0000\n\t" "s_waitcnt lgkmcnt(4)\n\t" "v_smfmac_f32_16x16x64_bf16 v[212:215], v[252:255], v[168:175], %[ix]\n\t" "s_nop 1\n\t" \
    "v_smfmac_f32_16x16x64_bf16 v[216:219], v[240:243], v[128:135], %[ix]\n\t" "s_nop 1\n\t" "v_smfmac_f32_16x16x64_bf16 v[220:223], v[240:243], v[144:151], %[ix]\n\t" \
    "v_cvt_pk_bf16_f32 v224, v208, v210\n\t" "v_cvt_pk_bf16_f32 v225, v209, v211\n\t" "v_smfmac_f32_16x16x64_bf16 v[216:219], v[244:247], v[136:143], %[ix]\n\t" \
    "v_cvt_pk_bf16_f32 v226, v212, v214\n\t" "v_cvt_pk_bf16_f32 v227, v213, v215\n\t" "v_smfmac_f32_16x16x64_bf16 v[220:223], v[244:247], v[152:159], %[ix]\n\t" "s_nop 0\n\t" \
    "v_permlane32_swap_b32 v224, v226\n\t" "v_smfmac_f32_16x16x64_bf16 v[216:219], v[248:251], a[240:247], %[ix]\n\t" "v_permlane32_swap_b32 v225, v227\n\t" "s_nop 0\n\t" \
    "v_smfmac_f32_16x16x64_bf16 v[220:223], v[248:251], a[248:255], %[ix]\n\t" "v_permlane16_swap_b32 v224, v225\n\t" "v_permlane16_swap_b32 v226, v227\n\t" \
    "s_waitcnt lgkmcnt(2)\n\t" "v_smfmac_f32_16x16x64_bf16 v[216:219], v[252:255], v[176:183], %[ix]\n\t" "buffer_store_dwordx4 v[224:227], %[uo], %[rs], s61 offen sc1\n\t" \
    "s_xor_b32 s61, s61, 0x40000\n\t" "s_waitcnt lgkmcnt(0)\n\t" "v_smfmac_f32_16x16x64_bf16 v[220:223], v[252:255], v[184:191], %[ix]\n\t" "s_nop 5\n\t" \
    "v_cvt_pk_bf16_f32 v228, v216, v218\n\t" "v_cvt_pk_bf16_f32 v229, v217, v219\n\t" "v_cvt_pk_bf16_f32 v230, v220, v222\n\t" "v_cvt_pk_bf16_f32 v231, v221, v223\n\t" \
    "s_nop 0\n\t" "v_permlane32_swap_b32 v228, v230\n\t" "v_permlane32_swap_b32 v229, v231\n\t" "s_nop 1\n\t" "v_permlane16_swap_b32 v228, v229\n\t" \
    "v_permlane16_swap_b32 v230, v231\n\t" "buffer_store_dwordx4 v[228:231], %[uo], %[rs], s61 offen sc1\n\t" "s_nop 1\n\t" XT(96, 97) "v_mov_b64 v[16:17], -1\n\t" \
    "v_mov_b64 v[18:19], -1\n\t" "s_barrier\n\t" "buffer_store_dwordx4 v[16:19], %[l1], %[rs], %[ur] offen offset:0 sc1\n\t" \
    "buffer_store_dwordx4 v[16:19], %[l1], %[rs], %[ur] offen offset:1024 sc1\n\t" "buffer_store_dwordx4 v[16:19], %[l1], %[rs], %[ur] offen offset:2048 sc1\n\t" \
    "buffer_store_dwordx4 v[16:19], %[l1], %[rs], %[ur] offen offset:3072 sc1\n\t" "s_sub_u32 s61, %[ur], 0x4000\n\t" \
    "buffer_store_dwordx4 v[16:19], %[l1], %[rs], s61 offen offset:2048 sc1\n\t" "s_branch 99f\n\t" "31:\n\t" "ds_read_b128 v[228:231], %[l1] offset:8192\n\t" \
    "s_waitcnt lgkmcnt(0)\n\t" "v_max3_u32 v225, v228, v229, v230\n\t" "v_max_u32 v225, v225, v231\n\t" "ds_read_b128 v[228:231], %[l1] offset:9216\n\t" \
    "s_waitcnt lgkmcnt(0)\n\t" "v_max3_u32 v225, v225, v228, v229\n\t" "v_max3_u32 v225, v225, v230, v231\n\t" "ds_read_b128 v[228:231], %[l1] offset:10240\n\t" \
    "s_waitcnt lgkmcnt(0)\n\t" "v_max3_u32 v225, v225, v228, v229\n\t" "v_max3_u32 v225, v225, v230, v231\n\t" "ds_read_b128 v[228:231], %[l1] offset:11264\n\t" \
    "s_waitcnt lgkmcnt(0)\n\t" "v_max3_u32 v225, v225, v228, v229\n\t" "v_max3_u32 v225, v225, v230, v231\n\t" "v_cmp_eq_u32 vcc, -1, v225\n\t" "s_cbranch_vccz 32b\n\t" \
    "s_cmp_eq_u32 %[rt], 1\n\t" "s_cbranch_scc1 32b\n\t" "v_mov_b32 v225, %[rt]\n\t" "ds_write_b32 v227, v225\n\t" "s_waitcnt lgkmcnt(0)\n\t" "s_branch 32b\n\t" "33:\n\t" \
    "s_sub_u32 %[rt], %[rt], 1\n\t" "s_waitcnt lgkmcnt(0)\n\t" XCNT(100) "34:\n\t" "buffer_load_dwordx4 v[240:243], %[mo], %[rs], %[sb] offen sc1\n\t" \
    "buffer_load_dwordx4 v[244:247], v224, %[rs], %[sb] offen sc1\n\t" "buffer_load_dwordx4 v[248:251], %[mo], %[rs], s60 offen sc1\n\t" \
    "buffer_load_dwordx4 v[252:255], v224, %[rs], s60 offen sc1\n\t" "s_waitcnt vmcnt(0)\n\t" "v_max_u32 v225, v240, v241\n\t" "v_max3_u32 v225, v225, v242, v243\n\t" \
    "v_max3_u32 v225, v225, v244, v245\n\t" "v_max3_u32 v225, v225, v246, v247\n\t" "v_max3_u32 v225, v225, v248, v249\n\t" "v_max3_u32 v225, v225, v250, v251\n\t" \
    "v_max3_u32 v225, v225, v252, v253\n\t" "v_max3_u32 v225, v225, v254, v255\n\t" "v_cmp_eq_u32 vcc, -1, v225\n\t" "s_cbranch_vccz 35f\n\t" "s_cmp_eq_u32 %[bd], 0\n\t" \
    "s_cbranch_scc1 35f\n\t" "s_sub_u32 %[bd], %[bd], 1\n\t" XCNT(101) "s_branch 34b\n\t" "35:\n\t" "ds_read_b128 v[160:163], %[l0] offset:0\n\t" \
    "ds_read_b128 v[164:167], %[l0] offset:1024\n\t" "ds_read_b128 v[168:171], %[l0] offset:2048\n\t" "ds_read_b128 v[172:175], %[l0] offset:3072\n\t" \
    "ds_read_b128 v[176:179], %[l0] offset:4096\n\t" "ds_read_b128 v[180:183], %[l0] offset:5120\n\t" "ds_read_b128 v[184:187], %[l0] offset:6144\n\t" \
    "ds_read_b128 v[188:191], %[l0] offset:7168\n\t" "v_mov_b64 v[232:233], 0\n\t" "v_mov_b64 v[234:235], 0\n\t" "v_mov_b64 v[236:237], 0\n\t" "v_mov_b64 v[238:239], 0\n\t" \
    "s_barrier\n\t" "s_branch 30b\n\t" "93:\n\t" XT(80, 81) "s_barrier\n\t" XT(82, 83) "buffer_load_dwordx4 v[240:243], %[mo], %[rs], %[sb] offen sc1\n\t" \
    "v_mov_b64 v[232:233], 0\n\t" "v_mov_b64 v[234:235], 0\n\t" "v_mov_b64 v[236:237], 0\n\t" "v_mov_b64 v[238:239], 0\n\t" "v_mov_b64 v[192:193], 0\n\t" \
    "v_mov_b64 v[194:195], 0\n\t" "v_mov_b64 v[196:197], 0\n\t" "v_mov_b64 v[198:199], 0\n\t" "buffer_load_dwordx4 v[244:247], v224, %[rs], %[sb] offen sc1\n\t" \
    "ds_read_b128 v[160:163], %[l0] offset:0\n\t" "ds_read_b128 v[164:167], %[l0] offset:1024\n\t" "ds_read_b128 v[168:171], %[l0] offset:2048\n\t" \
    "ds_read_b128 v[172:175], %[l0] offset:3072\n\t" "s_nop 3\n\t" "buffer_load_dwordx4 v[248:251], %[mo], %[rs], s60 offen sc1\n\t" \
    "ds_read_b128 v[176:179], %[l0] offset:4096\n\t" "ds_read_b128 v[180:183], %[l0] offset:5120\n\t" "ds_read_b128 v[184:187], %[l0] offset:6144\n\t" \
    "ds_read_b128 v[188:191], %[l0] offset:7168\n\t" "s_nop 3\n\t" "buffer_load_dwordx4 v[252:255], v224, %[rs], s60 offen sc1\n\t" "v_mov_b64 v[200:201], 0\n\t" \
    "v_mov_b64 v[202:203], 0\n\t" "v_mov_b64 v[204:205], 0\n\t" "v_mov_b64 v[206:207], 0\n\t" "v_mov_b64 v[208:209], 0\n\t" "v_mov_b64 v[210:211], 0\n\t" \
    "v_mov_b64 v[212:213], 0\n\t" "v_mov_b64 v[214:215], 0\n\t" "v_mov_b64 v[216:217], 0\n\t" "v_mov_b64 v[218:219], 0\n\t" "v_mov_b64 v[220:221], 0\n\t" \
    "v_mov_b64 v[222:223], 0\n\t" "v_mov_b32 v227, 160512\n\t" "40:\n\t" "s_waitcnt vmcnt(3)\n\t" XT(84, 85) \
    "v_smfmac_f32_16x16x64_bf16 v[232:235], v[240:243], a[0:7], %[ix]\n\t" "ds_write_b128 %[l1], v[240:243] offset:12288\n\t" \
    "v_smfmac_f32_16x16x64_bf16 v[236:239], v[240:243], a[8:15], %[ix]\n\t" "s_waitcnt vmcnt(2)\n\t" "v_smfmac_f32_16x16x64_bf16 v[232:235], v[244:247], a[16:23], %[ix]\n\t" \
    "ds_write_b128 %[l1], v[244:247] offset:13312\n\t" "s_waitcnt lgkmcnt(8)\n\t" "v_smfmac_f32_16x16x64_bf16 v[236:239], v[244:247], v[160:167], %[ix]\n\t" \
    "s_waitcnt lgkmcnt(0)\n\t" "s_barrier\n\t" XT(86, 87) "ds_read_b128 v[16:19], %[l1] offset:0\n\t" "ds_read_b128 v[20:23], %[l1] offset:4096\n\t" \
    "ds_read_b128 v[24:27], %[l1] offset:8192\n\t" "s_waitcnt vmcnt(1)\n\t" "v_smfmac_f32_16x16x64_bf16 v[232:235], v[248:251], a[24:31], %[ix]\n\t" \
    "ds_write_b128 %[l1], v[248:251] offset:14336\n\t" "v_smfmac_f32_16x16x64_bf16 v[236:239], v[248:251], a[32:39], %[ix]\n\t" "ds_read_b128 v[28:31], %[l1] offset:1024\n\t" \
    "ds_read_b128 v[240:243], %[l1] offset:5120\n\t" "ds_read_b128 v[244:247], %[l1] offset:9216\n\t" "s_waitcnt vmcnt(0)\n\t" \
    "v_smfmac_f32_16x16x64_bf16 v[232:235], v[252:255], a[40:47], %[ix]\n\t" "ds_write_b128 %[l1], v[252:255] offset:15360\n\t" \
    "v_smfmac_f32_16x16x64_bf16 v[236:239], v[252:255], v[168:175], %[ix]\n\t" "ds_read_b128 v[160:163], %[l0] offset:8192\n\t" \
    "ds_read_b128 v[164:167], %[l0] offset:9216\n\t" "ds_read_b128 v[168:171], %[l0] offset:10240\n\t" "ds_read_b128 v[172:175], %[l0] offset:11264\n\t" "s_nop 1\n\t" \
    "v_cmp_u_f32 vcc, v232, v233\n\t" "v_cmp_u_f32 s[62:63], v234, v235\n\t" "s_waitcnt lgkmcnt(11)\n\t" \
    "v_smfmac_f32_16x16x64_bf16 v[232:235], v[16:19], a[48:55], %[ix]\n\t" "v_smfmac_f32_16x16x64_bf16 v[236:239], v[16:19], a[56:63], %[ix]\n\t" \
    "s_or_b64 vcc, vcc, s[62:63]\n\t" "s_cbranch_scc1 41f\n\t" "42:\n\t" "s_waitcnt lgkmcnt(10)\n\t" "v_smfmac_f32_16x16x64_bf16 v[232:235], v[20:23], a[64:71], %[ix]\n\t" \
    "v_smfmac_f32_16x16x64_bf16 v[236:239], v[20:23], v[176:183], %[ix]\n\t" "s_waitcnt lgkmcnt(4)\n\t" "s_barrier\n\t" XT(88, 89) "ds_read_b32 v225, v227\n\t" \
    "v_smfmac_f32_16x16x64_bf16 v[232:235], v[24:27], a[72:79], %[ix]\n\t" "ds_read_b128 v[176:179], %[l0] offset:12288\n\t" "ds_read_b128 v[180:183], %[l0] offset:13312\n\t" \
    "v_smfmac_f32_16x16x64_bf16 v[236:239], v[24:27], a[80:87], %[ix]\n\t" "ds_read_b128 v[248:251], %[l1] offset:2048\n\t" "ds_read_b128 v[252:255], %[l1] offset:6144\n\t" \
    "v_smfmac_f32_16x16x64_bf16 v[232:235], v[28:31], a[88:95], %[ix]\n\t" "ds_read_b128 v[16:19], %[l1] offset:10240\n\t" "ds_read_b128 v[20:23], %[l1] offset:3072\n\t" \
    "v_smfmac_f32_16x16x64_bf16 v[236:239], v[28:31], v[184:191], %[ix]\n\t" "ds_read_b128 v[24:27], %[l1] offset:7168\n\t" "ds_read_b128 v[184:187], %[l0] offset:14336\n\t" \
    "v_smfmac_f32_16x16x64_bf16 v[232:235], v[240:243], a[96:103], %[ix]\n\t" "ds_read_b128 v[188:191], %[l0] offset:15360\n\t" \
    "ds_read_b128 v[28:31], %[l1] offset:11264\n\t" "v_smfmac_f32_16x16x64_bf16 v[236:239], v[240:243], a[104:111], %[ix]\n\t" "s_waitcnt lgkmcnt(10)\n\t" \
    "v_cmp_eq_u32 vcc, %[rt], v225\n\t" "v_smfmac_f32_16x16x64_bf16 v[232:235], v[244:247], a[112:119], %[ix]\n\t" "s_cbranch_vccnz 43f\n\t" \
    "v_smfmac_f32_16x16x64_bf16 v[236:239], v[244:247], v[160:167], %[ix]\n\t" "ds_read_b128 v[160:163], %[l0] offset:16384\n\t" \
    "ds_read_b128 v[164:167], %[l0] offset:17408\n\t" "s_waitcnt lgkmcnt(9)\n\t" "v_smfmac_f32_16x16x64_bf16 v[232:235], v[248:251], a[120:127], %[ix]\n\t" \
    "v_smfmac_f32_16x16x64_bf16 v[236:239], v[248:251], a[128:135], %[ix]\n\t" "s_waitcnt lgkmcnt(8)\n\t" \
    "v_smfmac_f32_16x16x64_bf16 v[232:235], v[252:255], a[136:143], %[ix]\n\t" "v_smfmac_f32_16x16x64_bf16 v[236:239], v[252:255], v[168:175], %[ix]\n\t" \
    "ds_read_b128 v[168:171], %[l0] offset:18432\n\t" "ds_read_b128 v[172:175], %[l0] offset:19456\n\t" "s_waitcnt lgkmcnt(9)\n\t" \
    "v_smfmac_f32_16x16x64_bf16 v[232:235], v[16:19], a[144:151], %[ix]\n\t" "v_smfmac_f32_16x16x64_bf16 v[236:239], v[16:19], a[152:159], %[ix]\n\t" \
    "s_waitcnt lgkmcnt(8)\n\t" "v_smfmac_f32_16x16x64_bf16 v[232:235], v[20:23], a[160:167], %[ix]\n\t" \
    "v_smfmac_f32_16x16x64_bf16 v[236:239], v[20:23], v[176:183], %[ix]\n\t" "ds_read_b128 v[176:179], %[l0] offset:20480\n\t" \
    "ds_read_b128 v[180:183], %[l0] offset:21504\n\t" "s_waitcnt lgkmcnt(9)\n\t" "v_smfmac_f32_16x16x64_bf16 v[232:235], v[24:27], a[168:175], %[ix]\n\t" \
    "v_smfmac_f32_16x16x64_bf16 v[236:239], v[24:27], a[176:183], %[ix]\n\t" "s_waitcnt lgkmcnt(6)\n\t" \
    "v_smfmac_f32_16x16x64_bf16 v[232:235], v[28:31], a[184:191], %[ix]\n\t" "v_smfmac_f32_16x16x64_bf16 v[236:239], v[28:31], v[184:191], %[ix]\n\t" \
    "ds_read_b128 v[184:187], %[l0] offset:22528\n\t" "ds_read_b128 v[188:191], %[l0] offset:23552\n\t" XT(90, 91) "s_nop 4\n\t" "v_add_f32 v232, v232, v233\n\t" \
    "v_add_f32 v234, v234, v235\n\t" "v_add_f32 v236, v236, v237\n\t" "v_add_f32 v238, v238, v239\n\t" "v_cvt_pk_bf16_f32 v236, v232, v236\n\t" \
    "v_cvt_pk_bf16_f32 v237, v234, v238\n\t" "ds_write2_b32 %[vw], v236, v237 offset1:68\n\t" "s_waitcnt lgkmcnt(0)\n\t" "s_barrier\n\t" XT(92, 93) \
    "ds_read_b128 v[240:243], %[vr] offset:0\n\t" "ds_read_b128 v[244:247], %[vr] offset:64\n\t" "ds_read_b128 v[248:251], %[vr] offset:128\n\t" \
    "ds_read_b128 v[252:255], %[vr] offset:192\n\t" "s_mov_b32 s61, %[ub]\n\t" "s_waitcnt lgkmcnt(3)\n\t" \
    "v_smfmac_f32_16x16x64_bf16 v[192:195], v[240:243], v[32:39], %[ix]\n\t" "v_smfmac_f32_16x16x64_bf16 v[196:199], v[240:243], v[48:55], %[ix]\n\t" \
    "s_waitcnt lgkmcnt(2)\n\t" "v_smfmac_f32_16x16x64_bf16 v[192:195], v[244:247], v[40:47], %[ix]\n\t" \
    "v_smfmac_f32_16x16x64_bf16 v[196:199], v[244:247], v[56:63], %[ix]\n\t" "s_waitcnt lgkmcnt(1)\n\t" \
    "v_smfmac_f32_16x16x64_bf16 v[192:195], v[248:251], a[192:199], %[ix]\n\t" "v_smfmac_f32_16x16x64_bf16 v[196:199], v[248:251], a[200:207], %[ix]\n\t" \
    "s_waitcnt lgkmcnt(0)\n\t" "v_smfmac_f32_16x16x64_bf16 v[192:195], v[252:255], v[160:167], %[ix]\n\t" "ds_read_b128 v[160:163], %[l0] offset:24576\n\t" \
    "ds_read_b128 v[164:167], %[l0] offset:25600\n\t" "v_smfmac_f32_16x16x64_bf16 v[196:199], v[252:255], v[168:175], %[ix]\n\t" \
    "ds_read_b128 v[168:171], %[l0] offset:26624\n\t" "ds_read_b128 v[172:175], %[l0] offset:27648\n\t" \
    "v_smfmac_f32_16x16x64_bf16 v[200:203], v[240:243], v[64:71], %[ix]\n\t" "s_nop 1\n\t" "v_smfmac_f32_16x16x64_bf16 v[204:207], v[240:243], v[80:87], %[ix]\n\t" \
    "v_cvt_pk_bf16_f32 v224, v192, v194\n\t" "v_cvt_pk_bf16_f32 v225, v193, v195\n\t" "v_smfmac_f32_16x16x64_bf16 v[200:203], v[244:247], v[72:79], %[ix]\n\t" \
    "v_cvt_pk_bf16_f32 v226, v196, v198\n\t" "v_cvt_pk_bf16_f32 v227, v197, v199\n\t" "v_smfmac_f32_16x16x64_bf16 v[204:207], v[244:247], v[88:95], %[ix]\n\t" "s_nop 0\n\t" \
    "v_permlane32_swap_b32 v224, v226\n\t" "v_smfmac_f32_16x16x64_bf16 v[200:203], v[248:251], a[208:215], %[ix]\n\t" "v_permlane32_swap_b32 v225, v227\n\t" "s_nop 0\n\t" \
    "v_smfmac_f32_16x16x64_bf16 v[204:207], v[248:251], a[216:223], %[ix]\n\t" "v_permlane16_swap_b32 v224, v225\n\t" "v_permlane16_swap_b32 v226, v227\n\t" \
    "v_smfmac_f32_16x16x64_bf16 v[200:203], v[252:255], v[176:183], %[ix]\n\t" "ds_read_b128 v[176:179], %[l0] offset:28672\n\t" \
    "buffer_store_dwordx4 v[224:227], %[uo], %[rs], s61 offen sc1\n\t" "s_xor_b32 s61, s61, 0x40000\n\t" XT(94, 95) \
    "v_smfmac_f32_16x16x64_bf16 v[204:207], v[252:255], v[184:191], %[ix]\n\t" "ds_read_b128 v[180:183], %[l0] offset:29696\n\t" \
    "ds_read_b128 v[184:187], %[l0] offset:30720\n\t" "v_smfmac_f32_16x16x64_bf16 v[208:211], v[240:243], v[96:103], %[ix]\n\t" \
    "ds_read_b128 v[188:191], %[l0] offset:31744\n\t" "s_nop 0\n\t" "v_smfmac_f32_16x16x64_bf16 v[212:215], v[240:243], v[112:119], %[ix]\n\t" \
    "v_cvt_pk_bf16_f32 v228, v200, v202\n\t" "v_cvt_pk_bf16_f32 v229, v201, v203\n\t" "v_smfmac_f32_16x16x64_bf16 v[208:211], v[244:247], v[104:111], %[ix]\n\t" \
    "v_cvt_pk_bf16_f32 v230, v204, v206\n\t" "v_cvt_pk_bf16_f32 v231, v205, v207\n\t" "v_smfmac_f32_16x16x64_bf16 v[212:215], v[244:247], v[120:127], %[ix]\n\t" "s_nop 0\n\t" \
    "v_permlane32_swap_b32 v228, v230\n\t" "v_smfmac_f32_16x16x64_bf16 v[208:211], v[248:251], a[224:231], %[ix]\n\t" "v_permlane32_swap_b32 v229, v231\n\t" "s_nop 0\n\t" \
    "v_smfmac_f32_16x16x64_bf16 v[212:215], v[248:251], a[232:239], %[ix]\n\t" "v_permlane16_swap_b32 v228, v229\n\t" "v_permlane16_swap_b32 v230, v231\n\t" \
    "s_waitcnt lgkmcnt(6)\n\t" "v_smfmac_f32_16x16x64_bf16 v[208:211], v[252:255], v[160:167], %[ix]\n\t" "buffer_store_dwordx4 v[228:231], %[uo], %[rs], s61 offen sc1\n\t" \
    "s_xor_b32 s61, s61, 0xc0000\n\t" "s_waitcnt lgkmcnt(4)\n\t" "v_smfmac_f32_16x16x64_bf16 v[212:215], v[252:255], v[168:175], %[ix]\n\t" "s_nop 1\n\t" \
    "v_smfmac_f32_16x16x64_bf16 v[216:219], v[240:243], v[128:135], %[ix]\n\t" "s_nop 1\n\t" "v_smfmac_f32_16x16x64_bf16 v[220:223], v[240:243], v[144:151], %[ix]\n\t" \
    "v_cvt_pk_bf16_f32 v224, v208, v210\n\t" "v_cvt_pk_bf16_f32 v225, v209, v211\n\t" "v_smfmac_f32_16x16x64_bf16 v[216:219], v[244:247], v[136:143], %[ix]\n\t" \
    "v_cvt_pk_bf16_f32 v226, v212, v214\n\t" "v_cvt_pk_bf16_f32 v227, v213, v215\n\t" "v_smfmac_f32_16x16x64_bf16 v[220:223], v[244:247], v[152:159], %[ix]\n\t" "s_nop 0\n\t" \
    "v_permlane32_swap_b32 v224, v226\n\t" "v_smfmac_f32_16x16x64_bf16 v[216:219], v[248:251], a[240:247], %[ix]\n\t" "v_permlane32_swap_b32 v225, v227\n\t" "s_nop 0\n\t" \
    "v_smfmac_f32_16x16x64_bf16 v[220:223], v[248:251], a[248:255], %[ix]\n\t" "v_permlane16_swap_b32 v224, v225\n\t" "v_permlane16_swap_b32 v226, v227\n\t" \
    "s_waitcnt lgkmcnt(2)\n\t" "v_smfmac_f32_16x16x64_bf16 v[216:219], v[252:255], v[176:183], %[ix]\n\t" "buffer_store_dwordx4 v[224:227], %[uo], %[rs], s61 offen sc1\n\t" \
    "s_xor_b32 s61, s61, 0x40000\n\t" "s_waitcnt lgkmcnt(0)\n\t" "v_smfmac_f32_16x16x64_bf16 v[220:223], v[252:255], v[184:191], %[ix]\n\t" "s_nop 5\n\t" \
    "v_cvt_pk_bf16_f32 v228, v216, v218\n\t" "v_cvt_pk_bf16_f32 v229, v217, v219\n\t" "v_cvt_pk_bf16_f32 v230, v220, v222\n\t" "v_cvt_pk_bf16_f32 v231, v221, v223\n\t" \
    "s_nop 0\n\t" "v_permlane32_swap_b32 v228, v230\n\t" "v_permlane32_swap_b32 v229, v231\n\t" "s_nop 1\n\t" "v_permlane16_swap_b32 v228, v229\n\t" \
    "v_permlane16_swap_b32 v230, v231\n\t" "buffer_store_dwordx4 v[228:231], %[uo], %[rs], s61 offen sc1\n\t" "s_nop 1\n\t" XT(96, 97) "v_mov_b64 v[16:17], -1\n\t" \
    "v_mov_b64 v[18:19], -1\n\t" "s_barrier\n\t" "buffer_store_dwordx4 v[16:19], %[l1], %[rs], %[ur] offen offset:0 sc1\n\t" \
    "buffer_store_dwordx4 v[16:19], %[l1], %[rs], %[ur] offen offset:1024 sc1\n\t" "buffer_store_dwordx4 v[16:19], %[l1], %[rs], %[ur] offen offset:2048 sc1\n\t" \
    "buffer_store_dwordx4 v[16:19], %[l1], %[rs], %[ur] offen offset:3072 sc1\n\t" "s_sub_u32 s61, %[ur], 0x5000\n\t" \
    "buffer_store_dwordx4 v[16:19], %[l1], %[rs], s61 offen offset:3072 sc1\n\t" "s_branch 99f\n\t" "41:\n\t" "ds_read_b128 v[228:231], %[l1] offset:12288\n\t" \
    "s_waitcnt lgkmcnt(0)\n\t" "v_max3_u32 v225, v228, v229, v230\n\t" "v_max_u32 v225, v225, v231\n\t" "ds_read_b128 v[228:231], %[l1] offset:13312\n\t" \
    "s_waitcnt lgkmcnt(0)\n\t" "v_max3_u32 v225, v225, v228, v229\n\t" "v_max3_u32 v225, v225, v230, v231\n\t" "ds_read_b128 v[228:231], %[l1] offset:14336\n\t" \
    "s_waitcnt lgkmcnt(0)\n\t" "v_max3_u32 v225, v225, v228, v229\n\t" "v_max3_u32 v225, v225, v230, v231\n\t" "ds_read_b128 v[228:231], %[l1] offset:15360\n\t" \
    "s_waitcnt lgkmcnt(0)\n\t" "v_max3_u32 v225, v225, v228, v229\n\t" "v_max3_u32 v225, v225, v230, v231\n\t" "v_cmp_eq_u32 vcc, -1, v225\n\t" "s_cbranch_vccz 42b\n\t" \
    "s_cmp_eq_u32 %[rt], 1\n\t" "s_cbranch_scc1 42b\n\t" "v_mov_b32 v225, %[rt]\n\t" "ds_write_b32 v227, v225\n\t" "s_waitcnt lgkmcnt(0)\n\t" "s_branch 42b\n\t" "43:\n\t" \
    "s_sub_u32 %[rt], %[rt], 1\n\t" "s_waitcnt lgkmcnt(0)\n\t" XCNT(100) "44:\n\t" "buffer_load_dwordx4 v[240:243], %[mo], %[rs], %[sb] offen sc1\n\t" \
    "buffer_load_dwordx4 v[244:247], v224, %[rs], %[sb] offen sc1\n\t" "buffer_load_dwordx4 v[248:251], %[mo], %[rs], s60 offen sc1\n\t" \
    "buffer_load_dwordx4 v[252:255], v224, %[rs], s60 offen sc1\n\t" "s_waitcnt vmcnt(0)\n\t" "v_max_u32 v225, v240, v241\n\t" "v_max3_u32 v225, v225, v242, v243\n\t" \
    "v_max3_u32 v225, v225, v244, v245\n\t" "v_max3_u32 v225, v225, v246, v247\n\t" "v_max3_u32 v225, v225, v248, v249\n\t" "v_max3_u32 v225, v225, v250, v251\n\t" \
    "v_max3_u32 v225, v225, v252, v253\n\t" "v_max3_u32 v225, v225, v254, v255\n\t" "v_cmp_eq_u32 vcc, -1, v225\n\t" "s_cbranch_vccz 45f\n\t" "s_cmp_eq_u32 %[bd], 0\n\t" \
    "s_cbranch_scc1 45f\n\t" "s_sub_u32 %[bd], %[bd], 1\n\t" XCNT(101) "s_branch 44b\n\t" "45:\n\t" "ds_read_b128 v[160:163], %[l0] offset:0\n\t" \
    "ds_read_b128 v[164:167], %[l0] offset:1024\n\t" "ds_read_b128 v[168:171], %[l0] offset:2048\n\t" "ds_read_b128 v[172:175], %[l0] offset:3072\n\t" \
    "ds_read_b128 v[176:179], %[l0] offset:4096\n\t" "ds_read_b128 v[180:183], %[l0] offset:5120\n\t" "ds_read_b128 v[184:187], %[l0] offset:6144\n\t" \
    "ds_read_b128 v[188:191], %[l0] offset:7168\n\t" "v_mov_b64 v[232:233], 0\n\t" "v_mov_b64 v[234:235], 0\n\t" "v_mov_b64 v[236:237], 0\n\t" "v_mov_b64 v[238:239], 0\n\t" \
    "s_barrier\n\t" "s_branch 40b\n\t" "99:\n\t"
#define Y_STEP \
    ".p2align 6\n\t" "v_xor_b32 v128, 0, %[u0]\n\t" "v_xor_b32 v132, 0, %[pm]\n\t" "v_xor_b32 v129, 2048, %[u0]\n\t" "v_xor_b32 v133, 1024, %[pm]\n\t" \
    "v_xor_b32 v130, 4096, %[u0]\n\t" "v_xor_b32 v134, 2048, %[pm]\n\t" "v_xor_b32 v131, 6144, %[u0]\n\t" "v_xor_b32 v135, 3072, %[pm]\n\t" "s_getpc_b64 s[62:63]\n\t" \
    "11:\n\t" "s_memtime s[64:65]\n\t" "s_waitcnt lgkmcnt(0)\n\t" "s_sub_u32 s61, %[tu], s64\n\t" "s_cmp_gt_i32 s61, 124\n\t" "s_cbranch_scc1 11b\n\t" \
    "s_max_i32 s61, s61, 0\n\t" "s_and_b32 s61, s61, 124\n\t" "s_sub_u32 s61, 12f-11b, s61\n\t" "s_add_u32 s62, s62, s61\n\t" "s_addc_u32 s63, s63, 0\n\t" \
    "s_setpc_b64 s[62:63]\n\t" ".rept 31\n\t" "s_nop 0\n\t" ".endr\n\t" "12:\n\t" XT(90, 91) "buffer_load_dwordx4 v[144:147], v128, %[rs], %[ub] offen offset:0 sc1\n\t" \
    "buffer_load_dwordx4 v[148:151], v128, %[rs], %[ub] offen offset:1024 sc1\n\t" "buffer_load_dwordx4 v[152:155], v129, %[rs], %[ub] offen offset:0 sc1\n\t" \
    "buffer_load_dwordx4 v[156:159], v129, %[rs], %[ub] offen offset:1024 sc1\n\t" "s_cmp_lg_u32 %[late], 0\n\t" "s_cbranch_scc1 14f\n\t" "s_getpc_b64 s[62:63]\n\t" "13:\n\t" \
    "s_memtime s[64:65]\n\t" "s_waitcnt lgkmcnt(0)\n\t" "s_sub_u32 s61, %[tg], s64\n\t" "s_cmp_gt_i32 s61, 124\n\t" "s_cbranch_scc1 13b\n\t" "s_max_i32 s61, s61, 0\n\t" \
    "s_and_b32 s61, s61, 124\n\t" "s_sub_u32 s61, 14f-13b, s61\n\t" "s_add_u32 s62, s62, s61\n\t" "s_addc_u32 s63, s63, 0\n\t" "s_setpc_b64 s[62:63]\n\t" ".rept 31\n\t" \
    "s_nop 0\n\t" ".endr\n\t" "14:\n\t" XT(92, 93) "20:\n\t" "buffer_load_dwordx4 v[160:163], v130, %[rs], %[ub] offen offset:0 sc1\n\t" \
    "buffer_load_dwordx4 v[164:167], v130, %[rs], %[ub] offen offset:1024 sc1\n\t" "buffer_load_dwordx4 v[192:195], %[x0], %[rs], %[pb] offen offset:0 sc1\n\t" \
    "buffer_load_dwordx4 v[196:199], %[x0], %[rs], %[pb] offen offset:1024 sc1\n\t" "buffer_load_dwordx4 v[200:203], %[x0], %[rs], %[pb] offen offset:2048 sc1\n\t" \
    "buffer_load_dwordx4 v[204:207], %[x0], %[rs], %[pb] offen offset:3072 sc1\n\t" "buffer_load_dwordx4 v[176:179], v132, %[rs], %[pb] offen sc1\n\t" \
    "v_mov_b64 v[120:121], 0\n\t" "v_mov_b64 v[122:123], 0\n\t" "v_mov_b64 v[136:137], 0\n\t" "v_mov_b64 v[138:139], 0\n\t" \
    "buffer_load_dwordx4 v[208:211], %[x1], %[rs], %[pb] offen offset:0 sc1\n\t" "buffer_load_dwordx4 v[212:215], %[x1], %[rs], %[pb] offen offset:1024 sc1\n\t" \
    "buffer_load_dwordx4 v[216:219], %[x1], %[rs], %[pb] offen offset:2048 sc1\n\t" "buffer_load_dwordx4 v[220:223], %[x1], %[rs], %[pb] offen offset:3072 sc1\n\t" \
    "buffer_load_dwordx4 v[180:183], v133, %[rs], %[pb] offen sc1\n\t" "buffer_load_dwordx4 v[224:227], %[x2], %[rs], %[pb] offen offset:0 sc1\n\t" \
    "buffer_load_dwordx4 v[228:231], %[x2], %[rs], %[pb] offen offset:1024 sc1\n\t" "buffer_load_dwordx4 v[232:235], %[x2], %[rs], %[pb] offen offset:2048 sc1\n\t" \
    "buffer_load_dwordx4 v[236:239], %[x2], %[rs], %[pb] offen offset:3072 sc1\n\t" "buffer_load_dwordx4 v[184:187], v134, %[rs], %[pb] offen sc1\n\t" \
    "buffer_load_dwordx4 v[168:171], v131, %[rs], %[ub] offen offset:0 sc1\n\t" "buffer_load_dwordx4 v[172:175], v131, %[rs], %[ub] offen offset:1024 sc1\n\t" \
    "buffer_load_dwordx4 v[240:243], %[x3], %[rs], %[pb] offen offset:0 sc1\n\t" "buffer_load_dwordx4 v[244:247], %[x3], %[rs], %[pb] offen offset:1024 sc1\n\t" \
    "buffer_load_dwordx4 v[248:251], %[x3], %[rs], %[pb] offen offset:2048 sc1\n\t" "buffer_load_dwordx4 v[252:255], %[x3], %[rs], %[pb] offen offset:3072 sc1\n\t" \
    "buffer_load_dwordx4 v[188:191], v135, %[rs], %[pb] offen sc1\n\t" "buffer_load_dword %[sv], %[so], %[rs], %[pb] offen sc1\n\t" "v_mov_b32 v127, -1\n\t" \
    "s_waitcnt vmcnt(25)\n\t" "v_permlane16_swap_b32 v144, v145\n\t" "v_permlane16_swap_b32 v146, v147\n\t" "v_permlane16_swap_b32 v148, v149\n\t" \
    "v_permlane16_swap_b32 v150, v151\n\t" "v_permlane16_swap_b32 v152, v153\n\t" "v_permlane16_swap_b32 v154, v155\n\t" "v_permlane16_swap_b32 v156, v157\n\t" \
    "v_permlane16_swap_b32 v158, v159\n\t" "s_waitcnt vmcnt(22)\n\t" XT(94, 95) "v_smfmac_f32_16x16x64_bf16 v[136:139], v[192:195], a[0:7], %[ix]\n\t" \
    "s_waitcnt vmcnt(21)\n\t" "v_smfmac_f32_16x16x64_bf16 v[120:123], v[196:199], a[8:15], %[ix]\n\t" "v_permlane16_swap_b32 v160, v161\n\t" "s_waitcnt vmcnt(20)\n\t" \
    "v_smfmac_f32_16x16x64_bf16 v[136:139], v[200:203], a[16:23], %[ix]\n\t" "s_waitcnt vmcnt(19)\n\t" \
    "v_smfmac_f32_16x16x64_bf16 v[120:123], v[204:207], a[24:31], %[ix]\n\t" "v_permlane16_swap_b32 v162, v163\n\t" "s_waitcnt vmcnt(18)\n\t" \
    "v_smfmac_f32_16x16x64_bf16 v[136:139], v[176:179], v[144:151], %[ix]\n\t" "s_waitcnt vmcnt(17)\n\t" \
    "v_smfmac_f32_16x16x64_bf16 v[120:123], v[208:211], a[32:39], %[ix]\n\t" "s_waitcnt vmcnt(16)\n\t" \
    "v_smfmac_f32_16x16x64_bf16 v[136:139], v[212:215], a[40:47], %[ix]\n\t" "v_permlane16_swap_b32 v164, v165\n\t" "s_waitcnt vmcnt(15)\n\t" \
    "v_smfmac_f32_16x16x64_bf16 v[120:123], v[216:219], a[48:55], %[ix]\n\t" "s_waitcnt vmcnt(14)\n\t" \
    "v_smfmac_f32_16x16x64_bf16 v[136:139], v[220:223], a[56:63], %[ix]\n\t" "v_permlane16_swap_b32 v166, v167\n\t" "s_waitcnt vmcnt(13)\n\t" \
    "v_smfmac_f32_16x16x64_bf16 v[120:123], v[180:183], v[152:159], %[ix]\n\t" "s_waitcnt vmcnt(12)\n\t" \
    "v_smfmac_f32_16x16x64_bf16 v[136:139], v[224:227], a[64:71], %[ix]\n\t" "s_waitcnt vmcnt(11)\n\t" \
    "v_smfmac_f32_16x16x64_bf16 v[120:123], v[228:231], a[72:79], %[ix]\n\t" "s_waitcnt vmcnt(10)\n\t" \
    "v_smfmac_f32_16x16x64_bf16 v[136:139], v[232:235], a[80:87], %[ix]\n\t" "s_waitcnt vmcnt(9)\n\t" "v_smfmac_f32_16x16x64_bf16 v[120:123], v[236:239], a[88:95], %[ix]\n\t" \
    "s_waitcnt vmcnt(8)\n\t" "v_smfmac_f32_16x16x64_bf16 v[136:139], v[184:187], v[160:167], %[ix]\n\t" "s_waitcnt vmcnt(5)\n\t" \
    "v_smfmac_f32_16x16x64_bf16 v[120:123], v[240:243], a[96:103], %[ix]\n\t" "v_permlane16_swap_b32 v168, v169\n\t" "v_permlane16_swap_b32 v170, v171\n\t" \
    "s_waitcnt vmcnt(4)\n\t" "v_smfmac_f32_16x16x64_bf16 v[136:139], v[244:247], a[104:111], %[ix]\n\t" "v_permlane16_swap_b32 v172, v173\n\t" \
    "v_permlane16_swap_b32 v174, v175\n\t" "s_waitcnt vmcnt(3)\n\t" "v_smfmac_f32_16x16x64_bf16 v[120:123], v[248:251], a[112:119], %[ix]\n\t" "s_waitcnt vmcnt(2)\n\t" \
    "v_smfmac_f32_16x16x64_bf16 v[120:123], v[252:255], a[120:127], %[ix]\n\t" "s_waitcnt vmcnt(1)\n\t" \
    "v_smfmac_f32_16x16x64_bf16 v[120:123], v[188:191], v[168:175], %[ix]\n\t" XT(82, 83) "v_add_f32 v136, v136, v137\n\t" "v_add_f32 v138, v138, v139\n\t" "s_nop 5\n\t" \
    "v_add_f32 v120, v120, v121\n\t" "v_add_f32 v122, v122, v123\n\t" "v_add_f32 v120, v120, v136\n\t" "v_add_f32 v122, v122, v138\n\t" \
    "v_cvt_pk_bf16_f32 v124, v120, v122\n\t" "v_cmp_u_f32 vcc, v120, v122\n\t" "s_cbranch_vccnz 30f\n\t" "21:\n\t" \
    "v_mov_b32_dpp v125, v124 quad_perm:[1,0,3,2] row_mask:0xf bank_mask:0xf\n\t" "v_perm_b32 v124, v125, v124, %[ps]\n\t" \
    "buffer_store_dword v124, %[mw], %[rs], %[mb] offen sc1\n\t" XT(84, 85) "buffer_store_dword v127, %[mw], %[rs], %[mr] offen sc1\n\t" "s_memtime s[64:65]\n\t" \
    "s_barrier\n\t" XT(98, 99) "v_permlane16_swap_b32 v144, v145\n\t" "v_permlane16_swap_b32 v146, v147\n\t" "v_permlane16_swap_b32 v148, v149\n\t" \
    "v_permlane16_swap_b32 v150, v151\n\t" "v_permlane16_swap_b32 v152, v153\n\t" "v_permlane16_swap_b32 v154, v155\n\t" "v_permlane16_swap_b32 v156, v157\n\t" \
    "v_permlane16_swap_b32 v158, v159\n\t" "v_permlane16_swap_b32 v160, v161\n\t" "v_permlane16_swap_b32 v162, v163\n\t" "v_permlane16_swap_b32 v164, v165\n\t" \
    "v_permlane16_swap_b32 v166, v167\n\t" "v_permlane16_swap_b32 v168, v169\n\t" "v_permlane16_swap_b32 v170, v171\n\t" "v_permlane16_swap_b32 v172, v173\n\t" \
    "v_permlane16_swap_b32 v174, v175\n\t" "s_waitcnt lgkmcnt(0)\n\t" "s_and_b32 %[bad], s64, -2\n\t" "s_branch 40f\n\t" "30:\n\t" "s_cmp_lg_u32 %[late], 0\n\t" \
    "s_cbranch_scc1 32f\n\t" XCNT(100) "s_mov_b32 %[bad], 1\n\t" "s_waitcnt vmcnt(0)\n\t" "s_branch 40f\n\t" "32:\n\t" "v_max_u32 v126, v144, v145\n\t" \
    "v_max3_u32 v126, v126, v146, v147\n\t" "v_max3_u32 v126, v126, v148, v149\n\t" "v_max3_u32 v126, v126, v150, v151\n\t" "v_max3_u32 v126, v126, v152, v153\n\t" \
    "v_max3_u32 v126, v126, v154, v155\n\t" "v_max3_u32 v126, v126, v156, v157\n\t" "v_max3_u32 v126, v126, v158, v159\n\t" "v_max3_u32 v126, v126, v160, v161\n\t" \
    "v_max3_u32 v126, v126, v162, v163\n\t" "v_max3_u32 v126, v126, v164, v165\n\t" "v_max3_u32 v126, v126, v166, v167\n\t" "v_max3_u32 v126, v126, v168, v169\n\t" \
    "v_max3_u32 v126, v126, v170, v171\n\t" "v_max3_u32 v126, v126, v172, v173\n\t" "v_max3_u32 v126, v126, v174, v175\n\t" UCNT "v_max3_u32 v126, v126, v176, v177\n\t" \
    "v_max3_u32 v126, v126, v178, v179\n\t" "v_max3_u32 v126, v126, v180, v181\n\t" "v_max3_u32 v126, v126, v182, v183\n\t" "v_max3_u32 v126, v126, v184, v185\n\t" \
    "v_max3_u32 v126, v126, v186, v187\n\t" "v_max3_u32 v126, v126, v188, v189\n\t" "v_max3_u32 v126, v126, v190, v191\n\t" "v_max3_u32 v126, v126, v192, v193\n\t" \
    "v_max3_u32 v126, v126, v194, v195\n\t" "v_max3_u32 v126, v126, v196, v197\n\t" "v_max3_u32 v126, v126, v198, v199\n\t" "v_max3_u32 v126, v126, v200, v201\n\t" \
    "v_max3_u32 v126, v126, v202, v203\n\t" "v_max3_u32 v126, v126, v204, v205\n\t" "v_max3_u32 v126, v126, v206, v207\n\t" "v_max3_u32 v126, v126, v208, v209\n\t" \
    "v_max3_u32 v126, v126, v210, v211\n\t" "v_max3_u32 v126, v126, v212, v213\n\t" "v_max3_u32 v126, v126, v214, v215\n\t" "v_max3_u32 v126, v126, v216, v217\n\t" \
    "v_max3_u32 v126, v126, v218, v219\n\t" "v_max3_u32 v126, v126, v220, v221\n\t" "v_max3_u32 v126, v126, v222, v223\n\t" "v_max3_u32 v126, v126, v224, v225\n\t" \
    "v_max3_u32 v126, v126, v226, v227\n\t" "v_max3_u32 v126, v126, v228, v229\n\t" "v_max3_u32 v126, v126, v230, v231\n\t" "v_max3_u32 v126, v126, v232, v233\n\t" \
    "v_max3_u32 v126, v126, v234, v235\n\t" "v_max3_u32 v126, v126, v236, v237\n\t" "v_max3_u32 v126, v126, v238, v239\n\t" "v_max3_u32 v126, v126, v240, v241\n\t" \
    "v_max3_u32 v126, v126, v242, v243\n\t" "v_max3_u32 v126, v126, v244, v245\n\t" "v_max3_u32 v126, v126, v246, v247\n\t" "v_max3_u32 v126, v126, v248, v249\n\t" \
    "v_max3_u32 v126, v126, v250, v251\n\t" "v_max3_u32 v126, v126, v252, v253\n\t" "v_max3_u32 v126, v126, v254, v255\n\t" "v_cmp_eq_u32 vcc, -1, v126\n\t" \
    "s_cbranch_vccz 21b\n\t" "s_cmp_eq_u32 %[bd], 0\n\t" "s_cbranch_scc1 21b\n\t" XCNT(100) "s_mov_b32 %[bad], 1\n\t" "s_waitcnt vmcnt(0)\n\t" "40:\n\t"
#define G_LOOP \
    ".p2align 6\n\t" "v_xor_b32 v72, 0, %[xo]\n\t" "v_add_u32 v80, 0x10000, v72\n\t" "v_xor_b32 v88, 0, %[xs]\n\t" "v_add_u32 v88, 0, v88\n\t" "v_xor_b32 v73, 64, %[xo]\n\t" \
    "v_add_u32 v81, 0x10000, v73\n\t" "v_xor_b32 v89, 16, %[xs]\n\t" "v_add_u32 v89, 2048, v89\n\t" "v_xor_b32 v74, 128, %[xo]\n\t" "v_add_u32 v82, 0x10000, v74\n\t" \
    "v_xor_b32 v90, 32, %[xs]\n\t" "v_add_u32 v90, 4096, v90\n\t" "v_xor_b32 v75, 192, %[xo]\n\t" "v_add_u32 v83, 0x10000, v75\n\t" "v_xor_b32 v91, 48, %[xs]\n\t" \
    "v_add_u32 v91, 6144, v91\n\t" "v_xor_b32 v76, 256, %[xo]\n\t" "v_add_u32 v84, 0x10000, v76\n\t" "v_xor_b32 v92, 64, %[xs]\n\t" "v_add_u32 v92, 8192, v92\n\t" \
    "v_xor_b32 v77, 320, %[xo]\n\t" "v_add_u32 v85, 0x10000, v77\n\t" "v_xor_b32 v93, 80, %[xs]\n\t" "v_add_u32 v93, 10240, v93\n\t" "v_xor_b32 v78, 384, %[xo]\n\t" \
    "v_add_u32 v86, 0x10000, v78\n\t" "v_xor_b32 v94, 96, %[xs]\n\t" "v_add_u32 v94, 12288, v94\n\t" "v_xor_b32 v79, 448, %[xo]\n\t" "v_add_u32 v87, 0x10000, v79\n\t" \
    "v_xor_b32 v95, 112, %[xs]\n\t" "v_add_u32 v95, 14336, v95\n\t" "v_mov_b32 v96, 2.0\n\t" "s_mov_b32 s60, 0\n\t" "s_mov_b32 s62, 0\n\t" "s_waitcnt vmcnt(0)\n\t" \
    "s_barrier\n\t" "ds_read_b128 v[100:103], v72 offset:0\n\t" "ds_read_b128 v[104:107], v72 offset:32768\n\t" "ds_read_b128 v[108:111], v73 offset:0\n\t" \
    "ds_read_b128 v[112:115], v73 offset:32768\n\t" "ds_read_b128 v[116:119], v74 offset:0\n\t" "10:\n\t" XT(80, 81) "s_waitcnt lgkmcnt(4)\n\t" \
    "v_mfma_f32_16x16x32_bf16 v[32:35], a[0:3], v[100:103], 0\n\t" "ds_read_b128 v[120:123], v74 offset:32768\n\t" "s_add_u32 s61, s60, 1\n\t" "s_and_b32 s61, s61, 63\n\t" \
    "v_mfma_f32_16x16x32_bf16 v[36:39], a[4:7], v[100:103], 0\n\t" "s_lshl_b32 s61, s61, 16\n\t" "s_add_u32 s63, %[lw], 65536\n\t" "s_waitcnt lgkmcnt(4)\n\t" \
    "v_mfma_f32_16x16x32_bf16 v[40:43], a[0:3], v[104:107], 0\n\t" "ds_read_b128 v[124:127], v75 offset:0\n\t" "v_mfma_f32_16x16x32_bf16 v[44:47], a[4:7], v[104:107], 0\n\t" \
    "s_waitcnt lgkmcnt(4)\n\t" "v_mfma_f32_16x16x32_bf16 v[32:35], a[8:11], v[108:111], v[32:35]\n\t" "ds_read_b128 v[128:131], v75 offset:32768\n\t" \
    "v_cvt_pk_bf16_f32 v64, v48, v49\n\t" "v_mfma_f32_16x16x32_bf16 v[36:39], a[12:15], v[108:111], v[36:39]\n\t" "v_cvt_pk_bf16_f32 v65, v50, v51\n\t" \
    "v_cvt_pk_bf16_f32 v66, v52, v53\n\t" "s_waitcnt lgkmcnt(4)\n\t" "v_mfma_f32_16x16x32_bf16 v[40:43], a[8:11], v[112:115], v[40:43]\n\t" \
    "ds_read_b128 v[100:103], v76 offset:0\n\t" "v_cvt_pk_bf16_f32 v67, v54, v55\n\t" "v_mfma_f32_16x16x32_bf16 v[44:47], a[12:15], v[112:115], v[44:47]\n\t" \
    "v_cvt_pk_bf16_f32 v68, v56, v57\n\t" "v_cvt_pk_bf16_f32 v69, v58, v59\n\t" "s_waitcnt lgkmcnt(4)\n\t" \
    "v_mfma_f32_16x16x32_bf16 v[32:35], a[16:19], v[116:119], v[32:35]\n\t" "ds_read_b128 v[104:107], v76 offset:32768\n\t" "v_cvt_pk_bf16_f32 v70, v60, v61\n\t" \
    "v_mfma_f32_16x16x32_bf16 v[36:39], a[20:23], v[116:119], v[36:39]\n\t" "v_cvt_pk_bf16_f32 v71, v62, v63\n\t" \
    "buffer_store_dwordx4 v[64:67], v96, %[rs], s62 offen offset:0 sc1\n\t" "s_waitcnt lgkmcnt(4)\n\t" "v_mfma_f32_16x16x32_bf16 v[40:43], a[16:19], v[120:123], v[40:43]\n\t" \
    "ds_read_b128 v[108:111], v77 offset:0\n\t" "v_mfma_f32_16x16x32_bf16 v[44:47], a[20:23], v[120:123], v[44:47]\n\t" "s_waitcnt lgkmcnt(4)\n\t" \
    "v_mfma_f32_16x16x32_bf16 v[32:35], a[24:27], v[124:127], v[32:35]\n\t" "ds_read_b128 v[112:115], v77 offset:32768\n\t" \
    "v_mfma_f32_16x16x32_bf16 v[36:39], a[28:31], v[124:127], v[36:39]\n\t" "buffer_store_dwordx4 v[68:71], v96, %[rs], s62 offen offset:256 sc1\n\t" \
    "s_waitcnt lgkmcnt(4)\n\t" "v_mfma_f32_16x16x32_bf16 v[40:43], a[24:27], v[128:131], v[40:43]\n\t" "ds_read_b128 v[116:119], v78 offset:0\n\t" "s_mov_b32 m0, s63\n\t" \
    "v_mfma_f32_16x16x32_bf16 v[44:47], a[28:31], v[128:131], v[44:47]\n\t" "s_add_u32 s63, s63, 0x800\n\t" "buffer_load_dwordx4 v88, %[rx], s61 offen lds\n\t" \
    "s_waitcnt lgkmcnt(4)\n\t" "v_mfma_f32_16x16x32_bf16 v[32:35], a[32:35], v[100:103], v[32:35]\n\t" "ds_read_b128 v[120:123], v78 offset:32768\n\t" \
    "v_mfma_f32_16x16x32_bf16 v[36:39], a[36:39], v[100:103], v[36:39]\n\t" "buffer_load_dwordx4 v88, %[rx], s61 offen offset:1024 lds\n\t" "s_waitcnt lgkmcnt(4)\n\t" \
    "v_mfma_f32_16x16x32_bf16 v[40:43], a[32:35], v[104:107], v[40:43]\n\t" "ds_read_b128 v[124:127], v79 offset:0\n\t" "s_mov_b32 m0, s63\n\t" \
    "v_mfma_f32_16x16x32_bf16 v[44:47], a[36:39], v[104:107], v[44:47]\n\t" "s_add_u32 s63, s63, 0x800\n\t" "buffer_load_dwordx4 v89, %[rx], s61 offen lds\n\t" \
    "s_waitcnt lgkmcnt(4)\n\t" "v_mfma_f32_16x16x32_bf16 v[32:35], a[40:43], v[108:111], v[32:35]\n\t" "ds_read_b128 v[128:131], v79 offset:32768\n\t" \
    "v_mfma_f32_16x16x32_bf16 v[36:39], a[44:47], v[108:111], v[36:39]\n\t" "buffer_load_dwordx4 v89, %[rx], s61 offen offset:1024 lds\n\t" "s_waitcnt lgkmcnt(4)\n\t" \
    "v_mfma_f32_16x16x32_bf16 v[40:43], a[40:43], v[112:115], v[40:43]\n\t" "ds_read_b128 v[100:103], v72 offset:512\n\t" "s_mov_b32 m0, s63\n\t" \
    "v_mfma_f32_16x16x32_bf16 v[44:47], a[44:47], v[112:115], v[44:47]\n\t" "s_add_u32 s63, s63, 0x800\n\t" "buffer_load_dwordx4 v90, %[rx], s61 offen lds\n\t" \
    "s_waitcnt lgkmcnt(4)\n\t" "v_mfma_f32_16x16x32_bf16 v[32:35], a[48:51], v[116:119], v[32:35]\n\t" "ds_read_b128 v[104:107], v72 offset:33280\n\t" \
    "v_mfma_f32_16x16x32_bf16 v[36:39], a[52:55], v[116:119], v[36:39]\n\t" "buffer_load_dwordx4 v90, %[rx], s61 offen offset:1024 lds\n\t" "s_waitcnt lgkmcnt(4)\n\t" \
    "v_mfma_f32_16x16x32_bf16 v[40:43], a[48:51], v[120:123], v[40:43]\n\t" "ds_read_b128 v[108:111], v73 offset:512\n\t" "s_mov_b32 m0, s63\n\t" \
    "v_mfma_f32_16x16x32_bf16 v[44:47], a[52:55], v[120:123], v[44:47]\n\t" "s_add_u32 s63, s63, 0x800\n\t" "buffer_load_dwordx4 v91, %[rx], s61 offen lds\n\t" \
    "s_waitcnt lgkmcnt(4)\n\t" "v_mfma_f32_16x16x32_bf16 v[32:35], a[56:59], v[124:127], v[32:35]\n\t" "ds_read_b128 v[112:115], v73 offset:33280\n\t" \
    "v_mfma_f32_16x16x32_bf16 v[36:39], a[60:63], v[124:127], v[36:39]\n\t" "buffer_load_dwordx4 v91, %[rx], s61 offen offset:1024 lds\n\t" "s_waitcnt lgkmcnt(4)\n\t" \
    "v_mfma_f32_16x16x32_bf16 v[40:43], a[56:59], v[128:131], v[40:43]\n\t" "ds_read_b128 v[116:119], v74 offset:512\n\t" "s_mov_b32 m0, s63\n\t" \
    "v_mfma_f32_16x16x32_bf16 v[44:47], a[60:63], v[128:131], v[44:47]\n\t" "s_add_u32 s63, s63, 0x800\n\t" "buffer_load_dwordx4 v92, %[rx], s61 offen lds\n\t" \
    "s_waitcnt lgkmcnt(4)\n\t" "v_mfma_f32_16x16x32_bf16 v[32:35], a[64:67], v[100:103], v[32:35]\n\t" "ds_read_b128 v[120:123], v74 offset:33280\n\t" \
    "v_mfma_f32_16x16x32_bf16 v[36:39], a[68:71], v[100:103], v[36:39]\n\t" "buffer_load_dwordx4 v92, %[rx], s61 offen offset:1024 lds\n\t" "s_waitcnt lgkmcnt(4)\n\t" \
    "v_mfma_f32_16x16x32_bf16 v[40:43], a[64:67], v[104:107], v[40:43]\n\t" "ds_read_b128 v[124:127], v75 offset:512\n\t" "s_mov_b32 m0, s63\n\t" \
    "v_mfma_f32_16x16x32_bf16 v[44:47], a[68:71], v[104:107], v[44:47]\n\t" "s_add_u32 s63, s63, 0x800\n\t" "buffer_load_dwordx4 v93, %[rx], s61 offen lds\n\t" \
    "s_waitcnt lgkmcnt(4)\n\t" "v_mfma_f32_16x16x32_bf16 v[32:35], a[72:75], v[108:111], v[32:35]\n\t" "ds_read_b128 v[128:131], v75 offset:33280\n\t" \
    "v_mfma_f32_16x16x32_bf16 v[36:39], a[76:79], v[108:111], v[36:39]\n\t" "buffer_load_dwordx4 v93, %[rx], s61 offen offset:1024 lds\n\t" "s_waitcnt lgkmcnt(4)\n\t" \
    "v_mfma_f32_16x16x32_bf16 v[40:43], a[72:75], v[112:115], v[40:43]\n\t" "ds_read_b128 v[100:103], v76 offset:512\n\t" "s_mov_b32 m0, s63\n\t" \
    "v_mfma_f32_16x16x32_bf16 v[44:47], a[76:79], v[112:115], v[44:47]\n\t" "s_add_u32 s63, s63, 0x800\n\t" "buffer_load_dwordx4 v94, %[rx], s61 offen lds\n\t" \
    "s_waitcnt lgkmcnt(4)\n\t" "v_mfma_f32_16x16x32_bf16 v[32:35], a[80:83], v[116:119], v[32:35]\n\t" "ds_read_b128 v[104:107], v76 offset:33280\n\t" \
    "v_mfma_f32_16x16x32_bf16 v[36:39], a[84:87], v[116:119], v[36:39]\n\t" "buffer_load_dwordx4 v94, %[rx], s61 offen offset:1024 lds\n\t" "s_waitcnt lgkmcnt(4)\n\t" \
    "v_mfma_f32_16x16x32_bf16 v[40:43], a[80:83], v[120:123], v[40:43]\n\t" "ds_read_b128 v[108:111], v77 offset:512\n\t" "s_mov_b32 m0, s63\n\t" \
    "v_mfma_f32_16x16x32_bf16 v[44:47], a[84:87], v[120:123], v[44:47]\n\t" "s_add_u32 s63, s63, 0x800\n\t" "buffer_load_dwordx4 v95, %[rx], s61 offen lds\n\t" \
    "s_waitcnt lgkmcnt(4)\n\t" "v_mfma_f32_16x16x32_bf16 v[32:35], a[88:91], v[124:127], v[32:35]\n\t" "ds_read_b128 v[112:115], v77 offset:33280\n\t" \
    "v_mfma_f32_16x16x32_bf16 v[36:39], a[92:95], v[124:127], v[36:39]\n\t" "buffer_load_dwordx4 v95, %[rx], s61 offen offset:1024 lds\n\t" "s_waitcnt lgkmcnt(4)\n\t" \
    "v_mfma_f32_16x16x32_bf16 v[40:43], a[88:91], v[128:131], v[40:43]\n\t" "ds_read_b128 v[116:119], v78 offset:512\n\t" \
    "v_mfma_f32_16x16x32_bf16 v[44:47], a[92:95], v[128:131], v[44:47]\n\t" "s_waitcnt lgkmcnt(4)\n\t" "v_mfma_f32_16x16x32_bf16 v[32:35], a[96:99], v[100:103], v[32:35]\n\t" \
    "ds_read_b128 v[120:123], v78 offset:33280\n\t" "s_lshr_b32 s64, s60, 2\n\t" "s_xor_b32 s64, s64, %[xp]\n\t" \
    "v_mfma_f32_16x16x32_bf16 v[36:39], a[100:103], v[100:103], v[36:39]\n\t" "s_and_b32 s64, s64, 1\n\t" "s_lshl_b32 s64, s64, 13\n\t" "s_waitcnt lgkmcnt(4)\n\t" \
    "v_mfma_f32_16x16x32_bf16 v[40:43], a[96:99], v[104:107], v[40:43]\n\t" "ds_read_b128 v[124:127], v79 offset:512\n\t" "s_lshl_b32 s62, s60, 19\n\t" \
    "v_mfma_f32_16x16x32_bf16 v[44:47], a[100:103], v[104:107], v[44:47]\n\t" "s_add_u32 s62, s62, s64\n\t" "s_add_u32 s62, s62, %[ob]\n\t" "s_waitcnt lgkmcnt(4)\n\t" \
    "v_mfma_f32_16x16x32_bf16 v[32:35], a[104:107], v[108:111], v[32:35]\n\t" "ds_read_b128 v[128:131], v79 offset:33280\n\t" "s_waitcnt vmcnt(16)\n\t" "s_barrier\n\t" \
    "v_mfma_f32_16x16x32_bf16 v[36:39], a[108:111], v[108:111], v[36:39]\n\t" "v_mov_b32 v98, s60\n\t" "buffer_store_dword v98, %[fo], %[rc], %[fl] offen sc1\n\t" \
    "s_waitcnt lgkmcnt(4)\n\t" "v_mfma_f32_16x16x32_bf16 v[40:43], a[104:107], v[112:115], v[40:43]\n\t" "ds_read_b128 v[100:103], v72 offset:1024\n\t" \
    "v_mfma_f32_16x16x32_bf16 v[44:47], a[108:111], v[112:115], v[44:47]\n\t" "s_waitcnt lgkmcnt(4)\n\t" \
    "v_mfma_f32_16x16x32_bf16 v[32:35], a[112:115], v[116:119], v[32:35]\n\t" "ds_read_b128 v[104:107], v72 offset:33792\n\t" \
    "v_mfma_f32_16x16x32_bf16 v[36:39], a[116:119], v[116:119], v[36:39]\n\t" "s_waitcnt lgkmcnt(4)\n\t" \
    "v_mfma_f32_16x16x32_bf16 v[40:43], a[112:115], v[120:123], v[40:43]\n\t" "ds_read_b128 v[108:111], v73 offset:1024\n\t" \
    "v_mfma_f32_16x16x32_bf16 v[44:47], a[116:119], v[120:123], v[44:47]\n\t" "s_waitcnt lgkmcnt(4)\n\t" \
    "v_mfma_f32_16x16x32_bf16 v[32:35], a[120:123], v[124:127], v[32:35]\n\t" "ds_read_b128 v[112:115], v73 offset:33792\n\t" \
    "v_mfma_f32_16x16x32_bf16 v[36:39], a[124:127], v[124:127], v[36:39]\n\t" "s_waitcnt lgkmcnt(4)\n\t" \
    "v_mfma_f32_16x16x32_bf16 v[40:43], a[120:123], v[128:131], v[40:43]\n\t" "ds_read_b128 v[116:119], v74 offset:1024\n\t" \
    "v_mfma_f32_16x16x32_bf16 v[44:47], a[124:127], v[128:131], v[44:47]\n\t" "s_waitcnt lgkmcnt(4)\n\t" \
    "v_mfma_f32_16x16x32_bf16 v[32:35], a[128:131], v[100:103], v[32:35]\n\t" "ds_read_b128 v[120:123], v74 offset:33792\n\t" \
    "v_mfma_f32_16x16x32_bf16 v[36:39], a[132:135], v[100:103], v[36:39]\n\t" "s_waitcnt lgkmcnt(4)\n\t" \
    "v_mfma_f32_16x16x32_bf16 v[40:43], a[128:131], v[104:107], v[40:43]\n\t" "ds_read_b128 v[124:127], v75 offset:1024\n\t" \
    "v_mfma_f32_16x16x32_bf16 v[44:47], a[132:135], v[104:107], v[44:47]\n\t" "s_waitcnt lgkmcnt(4)\n\t" \
    "v_mfma_f32_16x16x32_bf16 v[32:35], a[136:139], v[108:111], v[32:35]\n\t" "ds_read_b128 v[128:131], v75 offset:33792\n\t" \
    "v_mfma_f32_16x16x32_bf16 v[36:39], a[140:143], v[108:111], v[36:39]\n\t" "s_waitcnt lgkmcnt(4)\n\t" \
    "v_mfma_f32_16x16x32_bf16 v[40:43], a[136:139], v[112:115], v[40:43]\n\t" "ds_read_b128 v[100:103], v76 offset:1024\n\t" \
    "v_mfma_f32_16x16x32_bf16 v[44:47], a[140:143], v[112:115], v[44:47]\n\t" "s_waitcnt lgkmcnt(4)\n\t" \
    "v_mfma_f32_16x16x32_bf16 v[32:35], a[144:147], v[116:119], v[32:35]\n\t" "ds_read_b128 v[104:107], v76 offset:33792\n\t" \
    "v_mfma_f32_16x16x32_bf16 v[36:39], a[148:151], v[116:119], v[36:39]\n\t" "s_waitcnt lgkmcnt(4)\n\t" \
    "v_mfma_f32_16x16x32_bf16 v[40:43], a[144:147], v[120:123], v[40:43]\n\t" "ds_read_b128 v[108:111], v77 offset:1024\n\t" \
    "v_mfma_f32_16x16x32_bf16 v[44:47], a[148:151], v[120:123], v[44:47]\n\t" "s_waitcnt lgkmcnt(4)\n\t" \
    "v_mfma_f32_16x16x32_bf16 v[32:35], a[152:155], v[124:127], v[32:35]\n\t" "ds_read_b128 v[112:115], v77 offset:33792\n\t" \
    "v_mfma_f32_16x16x32_bf16 v[36:39], a[156:159], v[124:127], v[36:39]\n\t" "s_waitcnt lgkmcnt(4)\n\t" \
    "v_mfma_f32_16x16x32_bf16 v[40:43], a[152:155], v[128:131], v[40:43]\n\t" "ds_read_b128 v[116:119], v78 offset:1024\n\t" \
    "v_mfma_f32_16x16x32_bf16 v[44:47], a[156:159], v[128:131], v[44:47]\n\t" "s_waitcnt lgkmcnt(4)\n\t" \
    "v_mfma_f32_16x16x32_bf16 v[32:35], a[160:163], v[100:103], v[32:35]\n\t" "ds_read_b128 v[120:123], v78 offset:33792\n\t" \
    "v_mfma_f32_16x16x32_bf16 v[36:39], a[164:167], v[100:103], v[36:39]\n\t" "s_waitcnt lgkmcnt(4)\n\t" \
    "v_mfma_f32_16x16x32_bf16 v[40:43], a[160:163], v[104:107], v[40:43]\n\t" "ds_read_b128 v[124:127], v79 offset:1024\n\t" \
    "v_mfma_f32_16x16x32_bf16 v[44:47], a[164:167], v[104:107], v[44:47]\n\t" "s_waitcnt lgkmcnt(4)\n\t" \
    "v_mfma_f32_16x16x32_bf16 v[32:35], a[168:171], v[108:111], v[32:35]\n\t" "ds_read_b128 v[128:131], v79 offset:33792\n\t" \
    "v_mfma_f32_16x16x32_bf16 v[36:39], a[172:175], v[108:111], v[36:39]\n\t" "s_waitcnt lgkmcnt(4)\n\t" \
    "v_mfma_f32_16x16x32_bf16 v[40:43], a[168:171], v[112:115], v[40:43]\n\t" "ds_read_b128 v[100:103], v72 offset:1536\n\t" \
    "v_mfma_f32_16x16x32_bf16 v[44:47], a[172:175], v[112:115], v[44:47]\n\t" "s_waitcnt lgkmcnt(4)\n\t" \
    "v_mfma_f32_16x16x32_bf16 v[32:35], a[176:179], v[116:119], v[32:35]\n\t" "ds_read_b128 v[104:107], v72 offset:34304\n\t" \
    "v_mfma_f32_16x16x32_bf16 v[36:39], a[180:183], v[116:119], v[36:39]\n\t" "s_waitcnt lgkmcnt(4)\n\t" \
    "v_mfma_f32_16x16x32_bf16 v[40:43], a[176:179], v[120:123], v[40:43]\n\t" "ds_read_b128 v[108:111], v73 offset:1536\n\t" \
    "v_mfma_f32_16x16x32_bf16 v[44:47], a[180:183], v[120:123], v[44:47]\n\t" "s_waitcnt lgkmcnt(4)\n\t" \
    "v_mfma_f32_16x16x32_bf16 v[32:35], a[184:187], v[124:127], v[32:35]\n\t" "ds_read_b128 v[112:115], v73 offset:34304\n\t" \
    "v_mfma_f32_16x16x32_bf16 v[36:39], a[188:191], v[124:127], v[36:39]\n\t" "s_waitcnt lgkmcnt(4)\n\t" \
    "v_mfma_f32_16x16x32_bf16 v[40:43], a[184:187], v[128:131], v[40:43]\n\t" "ds_read_b128 v[116:119], v74 offset:1536\n\t" \
    "v_mfma_f32_16x16x32_bf16 v[44:47], a[188:191], v[128:131], v[44:47]\n\t" "s_waitcnt lgkmcnt(4)\n\t" \
    "v_mfma_f32_16x16x32_bf16 v[32:35], a[192:195], v[100:103], v[32:35]\n\t" "ds_read_b128 v[120:123], v74 offset:34304\n\t" \
    "v_mfma_f32_16x16x32_bf16 v[36:39], a[196:199], v[100:103], v[36:39]\n\t" "s_waitcnt lgkmcnt(4)\n\t" \
    "v_mfma_f32_16x16x32_bf16 v[40:43], a[192:195], v[104:107], v[40:43]\n\t" "ds_read_b128 v[124:127], v75 offset:1536\n\t" \
    "v_mfma_f32_16x16x32_bf16 v[44:47], a[196:199], v[104:107], v[44:47]\n\t" "s_waitcnt lgkmcnt(4)\n\t" \
    "v_mfma_f32_16x16x32_bf16 v[32:35], a[200:203], v[108:111], v[32:35]\n\t" "ds_read_b128 v[128:131], v75 offset:34304\n\t" \
    "v_mfma_f32_16x16x32_bf16 v[36:39], a[204:207], v[108:111], v[36:39]\n\t" "s_waitcnt lgkmcnt(4)\n\t" \
    "v_mfma_f32_16x16x32_bf16 v[40:43], a[200:203], v[112:115], v[40:43]\n\t" "ds_read_b128 v[100:103], v76 offset:1536\n\t" \
    "v_mfma_f32_16x16x32_bf16 v[44:47], a[204:207], v[112:115], v[44:47]\n\t" "s_waitcnt lgkmcnt(4)\n\t" \
    "v_mfma_f32_16x16x32_bf16 v[32:35], a[208:211], v[116:119], v[32:35]\n\t" "ds_read_b128 v[104:107], v76 offset:34304\n\t" \
    "v_mfma_f32_16x16x32_bf16 v[36:39], a[212:215], v[116:119], v[36:39]\n\t" "s_waitcnt lgkmcnt(4)\n\t" \
    "v_mfma_f32_16x16x32_bf16 v[40:43], a[208:211], v[120:123], v[40:43]\n\t" "ds_read_b128 v[108:111], v77 offset:1536\n\t" \
    "v_mfma_f32_16x16x32_bf16 v[44:47], a[212:215], v[120:123], v[44:47]\n\t" "s_waitcnt lgkmcnt(4)\n\t" \
    "v_mfma_f32_16x16x32_bf16 v[32:35], a[216:219], v[124:127], v[32:35]\n\t" "ds_read_b128 v[112:115], v77 offset:34304\n\t" \
    "v_mfma_f32_16x16x32_bf16 v[36:39], a[220:223], v[124:127], v[36:39]\n\t" "s_waitcnt lgkmcnt(4)\n\t" \
    "v_mfma_f32_16x16x32_bf16 v[40:43], a[216:219], v[128:131], v[40:43]\n\t" "ds_read_b128 v[116:119], v78 offset:1536\n\t" \
    "v_mfma_f32_16x16x32_bf16 v[44:47], a[220:223], v[128:131], v[44:47]\n\t" "s_waitcnt lgkmcnt(4)\n\t" \
    "v_mfma_f32_16x16x32_bf16 v[32:35], a[224:227], v[100:103], v[32:35]\n\t" "ds_read_b128 v[120:123], v78 offset:34304\n\t" \
    "v_mfma_f32_16x16x32_bf16 v[36:39], a[228:231], v[100:103], v[36:39]\n\t" "s_waitcnt lgkmcnt(4)\n\t" \
    "v_mfma_f32_16x16x32_bf16 v[40:43], a[224:227], v[104:107], v[40:43]\n\t" "ds_read_b128 v[124:127], v79 offset:1536\n\t" \
    "v_mfma_f32_16x16x32_bf16 v[44:47], a[228:231], v[104:107], v[44:47]\n\t" "s_waitcnt lgkmcnt(4)\n\t" \
    "v_mfma_f32_16x16x32_bf16 v[32:35], a[232:235], v[108:111], v[32:35]\n\t" "ds_read_b128 v[128:131], v79 offset:34304\n\t" \
    "v_mfma_f32_16x16x32_bf16 v[36:39], a[236:239], v[108:111], v[36:39]\n\t" "s_waitcnt lgkmcnt(4)\n\t" \
    "v_mfma_f32_16x16x32_bf16 v[40:43], a[232:235], v[112:115], v[40:43]\n\t" "v_mfma_f32_16x16x32_bf16 v[44:47], a[236:239], v[112:115], v[44:47]\n\t" \
    "s_waitcnt lgkmcnt(3)\n\t" "v_mfma_f32_16x16x32_bf16 v[32:35], a[240:243], v[116:119], v[32:35]\n\t" \
    "v_mfma_f32_16x16x32_bf16 v[36:39], a[244:247], v[116:119], v[36:39]\n\t" "s_waitcnt vmcnt(1) lgkmcnt(0)\n\t" "s_barrier\n\t" XT(82, 83) \
    "v_mfma_f32_16x16x32_bf16 v[40:43], a[240:243], v[120:123], v[40:43]\n\t" "ds_read_b128 v[100:103], v80 offset:0\n\t" \
    "v_mfma_f32_16x16x32_bf16 v[44:47], a[244:247], v[120:123], v[44:47]\n\t" "v_mfma_f32_16x16x32_bf16 v[32:35], a[248:251], v[124:127], v[32:35]\n\t" \
    "ds_read_b128 v[104:107], v80 offset:32768\n\t" "v_mfma_f32_16x16x32_bf16 v[36:39], a[252:255], v[124:127], v[36:39]\n\t" "ds_read_b128 v[108:111], v81 offset:0\n\t" \
    "v_mfma_f32_16x16x32_bf16 v[40:43], a[248:251], v[128:131], v[40:43]\n\t" "ds_read_b128 v[112:115], v81 offset:32768\n\t" "ds_read_b128 v[116:119], v82 offset:0\n\t" \
    "v_mfma_f32_16x16x32_bf16 v[44:47], a[252:255], v[128:131], v[44:47]\n\t" "s_add_u32 s60, s60, 1\n\t" GSUM "s_cmp_eq_u32 s60, 1\n\t" "s_cbranch_scc1 20f\n\t" "11:\n\t" \
    XT(80, 81) "s_waitcnt lgkmcnt(4)\n\t" "v_mfma_f32_16x16x32_bf16 v[48:51], a[0:3], v[100:103], 0\n\t" "ds_read_b128 v[120:123], v82 offset:32768\n\t" \
    "s_add_u32 s61, s60, 1\n\t" "s_and_b32 s61, s61, 63\n\t" "v_mfma_f32_16x16x32_bf16 v[52:55], a[4:7], v[100:103], 0\n\t" "s_lshl_b32 s61, s61, 16\n\t" \
    "s_add_u32 s63, %[lw], 0\n\t" "s_waitcnt lgkmcnt(4)\n\t" "v_mfma_f32_16x16x32_bf16 v[56:59], a[0:3], v[104:107], 0\n\t" "ds_read_b128 v[124:127], v83 offset:0\n\t" \
    "v_mfma_f32_16x16x32_bf16 v[60:63], a[4:7], v[104:107], 0\n\t" "s_waitcnt lgkmcnt(4)\n\t" "v_mfma_f32_16x16x32_bf16 v[48:51], a[8:11], v[108:111], v[48:51]\n\t" \
    "ds_read_b128 v[128:131], v83 offset:32768\n\t" "v_cvt_pk_bf16_f32 v64, v32, v33\n\t" "v_mfma_f32_16x16x32_bf16 v[52:55], a[12:15], v[108:111], v[52:55]\n\t" \
    "v_cvt_pk_bf16_f32 v65, v34, v35\n\t" "v_cvt_pk_bf16_f32 v66, v36, v37\n\t" "s_waitcnt lgkmcnt(4)\n\t" \
    "v_mfma_f32_16x16x32_bf16 v[56:59], a[8:11], v[112:115], v[56:59]\n\t" "ds_read_b128 v[100:103], v84 offset:0\n\t" "v_cvt_pk_bf16_f32 v67, v38, v39\n\t" \
    "v_mfma_f32_16x16x32_bf16 v[60:63], a[12:15], v[112:115], v[60:63]\n\t" "v_cvt_pk_bf16_f32 v68, v40, v41\n\t" "v_cvt_pk_bf16_f32 v69, v42, v43\n\t" \
    "s_waitcnt lgkmcnt(4)\n\t" "v_mfma_f32_16x16x32_bf16 v[48:51], a[16:19], v[116:119], v[48:51]\n\t" "ds_read_b128 v[104:107], v84 offset:32768\n\t" \
    "v_cvt_pk_bf16_f32 v70, v44, v45\n\t" "v_mfma_f32_16x16x32_bf16 v[52:55], a[20:23], v[116:119], v[52:55]\n\t" "v_cvt_pk_bf16_f32 v71, v46, v47\n\t" \
    "buffer_store_dwordx4 v[64:67], v96, %[rs], s62 offen offset:0 sc1\n\t" "s_waitcnt lgkmcnt(4)\n\t" "v_mfma_f32_16x16x32_bf16 v[56:59], a[16:19], v[120:123], v[56:59]\n\t" \
    "ds_read_b128 v[108:111], v85 offset:0\n\t" "v_mfma_f32_16x16x32_bf16 v[60:63], a[20:23], v[120:123], v[60:63]\n\t" "s_waitcnt lgkmcnt(4)\n\t" \
    "v_mfma_f32_16x16x32_bf16 v[48:51], a[24:27], v[124:127], v[48:51]\n\t" "ds_read_b128 v[112:115], v85 offset:32768\n\t" \
    "v_mfma_f32_16x16x32_bf16 v[52:55], a[28:31], v[124:127], v[52:55]\n\t" "buffer_store_dwordx4 v[68:71], v96, %[rs], s62 offen offset:256 sc1\n\t" \
    "s_waitcnt lgkmcnt(4)\n\t" "v_mfma_f32_16x16x32_bf16 v[56:59], a[24:27], v[128:131], v[56:59]\n\t" "ds_read_b128 v[116:119], v86 offset:0\n\t" "s_mov_b32 m0, s63\n\t" \
    "v_mfma_f32_16x16x32_bf16 v[60:63], a[28:31], v[128:131], v[60:63]\n\t" "s_add_u32 s63, s63, 0x800\n\t" "buffer_load_dwordx4 v88, %[rx], s61 offen lds\n\t" \
    "s_waitcnt lgkmcnt(4)\n\t" "v_mfma_f32_16x16x32_bf16 v[48:51], a[32:35], v[100:103], v[48:51]\n\t" "ds_read_b128 v[120:123], v86 offset:32768\n\t" \
    "v_mfma_f32_16x16x32_bf16 v[52:55], a[36:39], v[100:103], v[52:55]\n\t" "buffer_load_dwordx4 v88, %[rx], s61 offen offset:1024 lds\n\t" "s_waitcnt lgkmcnt(4)\n\t" \
    "v_mfma_f32_16x16x32_bf16 v[56:59], a[32:35], v[104:107], v[56:59]\n\t" "ds_read_b128 v[124:127], v87 offset:0\n\t" "s_mov_b32 m0, s63\n\t" \
    "v_mfma_f32_16x16x32_bf16 v[60:63], a[36:39], v[104:107], v[60:63]\n\t" "s_add_u32 s63, s63, 0x800\n\t" "buffer_load_dwordx4 v89, %[rx], s61 offen lds\n\t" \
    "s_waitcnt lgkmcnt(4)\n\t" "v_mfma_f32_16x16x32_bf16 v[48:51], a[40:43], v[108:111], v[48:51]\n\t" "ds_read_b128 v[128:131], v87 offset:32768\n\t" \
    "v_mfma_f32_16x16x32_bf16 v[52:55], a[44:47], v[108:111], v[52:55]\n\t" "buffer_load_dwordx4 v89, %[rx], s61 offen offset:1024 lds\n\t" "s_waitcnt lgkmcnt(4)\n\t" \
    "v_mfma_f32_16x16x32_bf16 v[56:59], a[40:43], v[112:115], v[56:59]\n\t" "ds_read_b128 v[100:103], v80 offset:512\n\t" "s_mov_b32 m0, s63\n\t" \
    "v_mfma_f32_16x16x32_bf16 v[60:63], a[44:47], v[112:115], v[60:63]\n\t" "s_add_u32 s63, s63, 0x800\n\t" "buffer_load_dwordx4 v90, %[rx], s61 offen lds\n\t" \
    "s_waitcnt lgkmcnt(4)\n\t" "v_mfma_f32_16x16x32_bf16 v[48:51], a[48:51], v[116:119], v[48:51]\n\t" "ds_read_b128 v[104:107], v80 offset:33280\n\t" \
    "v_mfma_f32_16x16x32_bf16 v[52:55], a[52:55], v[116:119], v[52:55]\n\t" "buffer_load_dwordx4 v90, %[rx], s61 offen offset:1024 lds\n\t" "s_waitcnt lgkmcnt(4)\n\t" \
    "v_mfma_f32_16x16x32_bf16 v[56:59], a[48:51], v[120:123], v[56:59]\n\t" "ds_read_b128 v[108:111], v81 offset:512\n\t" "s_mov_b32 m0, s63\n\t" \
    "v_mfma_f32_16x16x32_bf16 v[60:63], a[52:55], v[120:123], v[60:63]\n\t" "s_add_u32 s63, s63, 0x800\n\t" "buffer_load_dwordx4 v91, %[rx], s61 offen lds\n\t" \
    "s_waitcnt lgkmcnt(4)\n\t" "v_mfma_f32_16x16x32_bf16 v[48:51], a[56:59], v[124:127], v[48:51]\n\t" "ds_read_b128 v[112:115], v81 offset:33280\n\t" \
    "v_mfma_f32_16x16x32_bf16 v[52:55], a[60:63], v[124:127], v[52:55]\n\t" "buffer_load_dwordx4 v91, %[rx], s61 offen offset:1024 lds\n\t" "s_waitcnt lgkmcnt(4)\n\t" \
    "v_mfma_f32_16x16x32_bf16 v[56:59], a[56:59], v[128:131], v[56:59]\n\t" "ds_read_b128 v[116:119], v82 offset:512\n\t" "s_mov_b32 m0, s63\n\t" \
    "v_mfma_f32_16x16x32_bf16 v[60:63], a[60:63], v[128:131], v[60:63]\n\t" "s_add_u32 s63, s63, 0x800\n\t" "buffer_load_dwordx4 v92, %[rx], s61 offen lds\n\t" \
    "s_waitcnt lgkmcnt(4)\n\t" "v_mfma_f32_16x16x32_bf16 v[48:51], a[64:67], v[100:103], v[48:51]\n\t" "ds_read_b128 v[120:123], v82 offset:33280\n\t" \
    "v_mfma_f32_16x16x32_bf16 v[52:55], a[68:71], v[100:103], v[52:55]\n\t" "buffer_load_dwordx4 v92, %[rx], s61 offen offset:1024 lds\n\t" "s_waitcnt lgkmcnt(4)\n\t" \
    "v_mfma_f32_16x16x32_bf16 v[56:59], a[64:67], v[104:107], v[56:59]\n\t" "ds_read_b128 v[124:127], v83 offset:512\n\t" "s_mov_b32 m0, s63\n\t" \
    "v_mfma_f32_16x16x32_bf16 v[60:63], a[68:71], v[104:107], v[60:63]\n\t" "s_add_u32 s63, s63, 0x800\n\t" "buffer_load_dwordx4 v93, %[rx], s61 offen lds\n\t" \
    "s_waitcnt lgkmcnt(4)\n\t" "v_mfma_f32_16x16x32_bf16 v[48:51], a[72:75], v[108:111], v[48:51]\n\t" "ds_read_b128 v[128:131], v83 offset:33280\n\t" \
    "v_mfma_f32_16x16x32_bf16 v[52:55], a[76:79], v[108:111], v[52:55]\n\t" "buffer_load_dwordx4 v93, %[rx], s61 offen offset:1024 lds\n\t" "s_waitcnt lgkmcnt(4)\n\t" \
    "v_mfma_f32_16x16x32_bf16 v[56:59], a[72:75], v[112:115], v[56:59]\n\t" "ds_read_b128 v[100:103], v84 offset:512\n\t" "s_mov_b32 m0, s63\n\t" \
    "v_mfma_f32_16x16x32_bf16 v[60:63], a[76:79], v[112:115], v[60:63]\n\t" "s_add_u32 s63, s63, 0x800\n\t" "buffer_load_dwordx4 v94, %[rx], s61 offen lds\n\t" \
    "s_waitcnt lgkmcnt(4)\n\t" "v_mfma_f32_16x16x32_bf16 v[48:51], a[80:83], v[116:119], v[48:51]\n\t" "ds_read_b128 v[104:107], v84 offset:33280\n\t" \
    "v_mfma_f32_16x16x32_bf16 v[52:55], a[84:87], v[116:119], v[52:55]\n\t" "buffer_load_dwordx4 v94, %[rx], s61 offen offset:1024 lds\n\t" "s_waitcnt lgkmcnt(4)\n\t" \
    "v_mfma_f32_16x16x32_bf16 v[56:59], a[80:83], v[120:123], v[56:59]\n\t" "ds_read_b128 v[108:111], v85 offset:512\n\t" "s_mov_b32 m0, s63\n\t" \
    "v_mfma_f32_16x16x32_bf16 v[60:63], a[84:87], v[120:123], v[60:63]\n\t" "s_add_u32 s63, s63, 0x800\n\t" "buffer_load_dwordx4 v95, %[rx], s61 offen lds\n\t" \
    "s_waitcnt lgkmcnt(4)\n\t" "v_mfma_f32_16x16x32_bf16 v[48:51], a[88:91], v[124:127], v[48:51]\n\t" "ds_read_b128 v[112:115], v85 offset:33280\n\t" \
    "v_mfma_f32_16x16x32_bf16 v[52:55], a[92:95], v[124:127], v[52:55]\n\t" "buffer_load_dwordx4 v95, %[rx], s61 offen offset:1024 lds\n\t" "s_waitcnt lgkmcnt(4)\n\t" \
    "v_mfma_f32_16x16x32_bf16 v[56:59], a[88:91], v[128:131], v[56:59]\n\t" "ds_read_b128 v[116:119], v86 offset:512\n\t" \
    "v_mfma_f32_16x16x32_bf16 v[60:63], a[92:95], v[128:131], v[60:63]\n\t" "s_waitcnt lgkmcnt(4)\n\t" "v_mfma_f32_16x16x32_bf16 v[48:51], a[96:99], v[100:103], v[48:51]\n\t" \
    "ds_read_b128 v[120:123], v86 offset:33280\n\t" "s_lshr_b32 s64, s60, 2\n\t" "s_xor_b32 s64, s64, %[xp]\n\t" \
    "v_mfma_f32_16x16x32_bf16 v[52:55], a[100:103], v[100:103], v[52:55]\n\t" "s_and_b32 s64, s64, 1\n\t" "s_lshl_b32 s64, s64, 13\n\t" "s_waitcnt lgkmcnt(4)\n\t" \
    "v_mfma_f32_16x16x32_bf16 v[56:59], a[96:99], v[104:107], v[56:59]\n\t" "ds_read_b128 v[124:127], v87 offset:512\n\t" "s_lshl_b32 s62, s60, 19\n\t" \
    "v_mfma_f32_16x16x32_bf16 v[60:63], a[100:103], v[104:107], v[60:63]\n\t" "s_add_u32 s62, s62, s64\n\t" "s_add_u32 s62, s62, %[ob]\n\t" "s_waitcnt lgkmcnt(4)\n\t" \
    "v_mfma_f32_16x16x32_bf16 v[48:51], a[104:107], v[108:111], v[48:51]\n\t" "ds_read_b128 v[128:131], v87 offset:33280\n\t" "s_waitcnt vmcnt(16)\n\t" "s_barrier\n\t" \
    "v_mfma_f32_16x16x32_bf16 v[52:55], a[108:111], v[108:111], v[52:55]\n\t" "v_mov_b32 v98, s60\n\t" "buffer_store_dword v98, %[fo], %[rc], %[fl] offen sc1\n\t" \
    "s_waitcnt lgkmcnt(4)\n\t" "v_mfma_f32_16x16x32_bf16 v[56:59], a[104:107], v[112:115], v[56:59]\n\t" "ds_read_b128 v[100:103], v80 offset:1024\n\t" \
    "v_mfma_f32_16x16x32_bf16 v[60:63], a[108:111], v[112:115], v[60:63]\n\t" "s_waitcnt lgkmcnt(4)\n\t" \
    "v_mfma_f32_16x16x32_bf16 v[48:51], a[112:115], v[116:119], v[48:51]\n\t" "ds_read_b128 v[104:107], v80 offset:33792\n\t" \
    "v_mfma_f32_16x16x32_bf16 v[52:55], a[116:119], v[116:119], v[52:55]\n\t" "s_waitcnt lgkmcnt(4)\n\t" \
    "v_mfma_f32_16x16x32_bf16 v[56:59], a[112:115], v[120:123], v[56:59]\n\t" "ds_read_b128 v[108:111], v81 offset:1024\n\t" \
    "v_mfma_f32_16x16x32_bf16 v[60:63], a[116:119], v[120:123], v[60:63]\n\t" "s_waitcnt lgkmcnt(4)\n\t" \
    "v_mfma_f32_16x16x32_bf16 v[48:51], a[120:123], v[124:127], v[48:51]\n\t" "ds_read_b128 v[112:115], v81 offset:33792\n\t" \
    "v_mfma_f32_16x16x32_bf16 v[52:55], a[124:127], v[124:127], v[52:55]\n\t" "s_waitcnt lgkmcnt(4)\n\t" \
    "v_mfma_f32_16x16x32_bf16 v[56:59], a[120:123], v[128:131], v[56:59]\n\t" "ds_read_b128 v[116:119], v82 offset:1024\n\t" \
    "v_mfma_f32_16x16x32_bf16 v[60:63], a[124:127], v[128:131], v[60:63]\n\t" "s_waitcnt lgkmcnt(4)\n\t" \
    "v_mfma_f32_16x16x32_bf16 v[48:51], a[128:131], v[100:103], v[48:51]\n\t" "ds_read_b128 v[120:123], v82 offset:33792\n\t" \
    "v_mfma_f32_16x16x32_bf16 v[52:55], a[132:135], v[100:103], v[52:55]\n\t" "s_waitcnt lgkmcnt(4)\n\t" \
    "v_mfma_f32_16x16x32_bf16 v[56:59], a[128:131], v[104:107], v[56:59]\n\t" "ds_read_b128 v[124:127], v83 offset:1024\n\t" \
    "v_mfma_f32_16x16x32_bf16 v[60:63], a[132:135], v[104:107], v[60:63]\n\t" "s_waitcnt lgkmcnt(4)\n\t" \
    "v_mfma_f32_16x16x32_bf16 v[48:51], a[136:139], v[108:111], v[48:51]\n\t" "ds_read_b128 v[128:131], v83 offset:33792\n\t" \
    "v_mfma_f32_16x16x32_bf16 v[52:55], a[140:143], v[108:111], v[52:55]\n\t" "s_waitcnt lgkmcnt(4)\n\t" \
    "v_mfma_f32_16x16x32_bf16 v[56:59], a[136:139], v[112:115], v[56:59]\n\t" "ds_read_b128 v[100:103], v84 offset:1024\n\t" \
    "v_mfma_f32_16x16x32_bf16 v[60:63], a[140:143], v[112:115], v[60:63]\n\t" "s_waitcnt lgkmcnt(4)\n\t" \
    "v_mfma_f32_16x16x32_bf16 v[48:51], a[144:147], v[116:119], v[48:51]\n\t" "ds_read_b128 v[104:107], v84 offset:33792\n\t" \
    "v_mfma_f32_16x16x32_bf16 v[52:55], a[148:151], v[116:119], v[52:55]\n\t" "s_waitcnt lgkmcnt(4)\n\t" \
    "v_mfma_f32_16x16x32_bf16 v[56:59], a[144:147], v[120:123], v[56:59]\n\t" "ds_read_b128 v[108:111], v85 offset:1024\n\t" \
    "v_mfma_f32_16x16x32_bf16 v[60:63], a[148:151], v[120:123], v[60:63]\n\t" "s_waitcnt lgkmcnt(4)\n\t" \
    "v_mfma_f32_16x16x32_bf16 v[48:51], a[152:155], v[124:127], v[48:51]\n\t" "ds_read_b128 v[112:115], v85 offset:33792\n\t" \
    "v_mfma_f32_16x16x32_bf16 v[52:55], a[156:159], v[124:127], v[52:55]\n\t" "s_waitcnt lgkmcnt(4)\n\t" \
    "v_mfma_f32_16x16x32_bf16 v[56:59], a[152:155], v[128:131], v[56:59]\n\t" "ds_read_b128 v[116:119], v86 offset:1024\n\t" \
    "v_mfma_f32_16x16x32_bf16 v[60:63], a[156:159], v[128:131], v[60:63]\n\t" "s_waitcnt lgkmcnt(4)\n\t" \
    "v_mfma_f32_16x16x32_bf16 v[48:51], a[160:163], v[100:103], v[48:51]\n\t" "ds_read_b128 v[120:123], v86 offset:33792\n\t" \
    "v_mfma_f32_16x16x32_bf16 v[52:55], a[164:167], v[100:103], v[52:55]\n\t" "s_waitcnt lgkmcnt(4)\n\t" \
    "v_mfma_f32_16x16x32_bf16 v[56:59], a[160:163], v[104:107], v[56:59]\n\t" "ds_read_b128 v[124:127], v87 offset:1024\n\t" \
    "v_mfma_f32_16x16x32_bf16 v[60:63], a[164:167], v[104:107], v[60:63]\n\t" "s_waitcnt lgkmcnt(4)\n\t" \
    "v_mfma_f32_16x16x32_bf16 v[48:51], a[168:171], v[108:111], v[48:51]\n\t" "ds_read_b128 v[128:131], v87 offset:33792\n\t" \
    "v_mfma_f32_16x16x32_bf16 v[52:55], a[172:175], v[108:111], v[52:55]\n\t" "s_waitcnt lgkmcnt(4)\n\t" \
    "v_mfma_f32_16x16x32_bf16 v[56:59], a[168:171], v[112:115], v[56:59]\n\t" "ds_read_b128 v[100:103], v80 offset:1536\n\t" \
    "v_mfma_f32_16x16x32_bf16 v[60:63], a[172:175], v[112:115], v[60:63]\n\t" "s_waitcnt lgkmcnt(4)\n\t" \
    "v_mfma_f32_16x16x32_bf16 v[48:51], a[176:179], v[116:119], v[48:51]\n\t" "ds_read_b128 v[104:107], v80 offset:34304\n\t" \
    "v_mfma_f32_16x16x32_bf16 v[52:55], a[180:183], v[116:119], v[52:55]\n\t" "s_waitcnt lgkmcnt(4)\n\t" \
    "v_mfma_f32_16x16x32_bf16 v[56:59], a[176:179], v[120:123], v[56:59]\n\t" "ds_read_b128 v[108:111], v81 offset:1536\n\t" \
    "v_mfma_f32_16x16x32_bf16 v[60:63], a[180:183], v[120:123], v[60:63]\n\t" "s_waitcnt lgkmcnt(4)\n\t" \
    "v_mfma_f32_16x16x32_bf16 v[48:51], a[184:187], v[124:127], v[48:51]\n\t" "ds_read_b128 v[112:115], v81 offset:34304\n\t" \
    "v_mfma_f32_16x16x32_bf16 v[52:55], a[188:191], v[124:127], v[52:55]\n\t" "s_waitcnt lgkmcnt(4)\n\t" \
    "v_mfma_f32_16x16x32_bf16 v[56:59], a[184:187], v[128:131], v[56:59]\n\t" "ds_read_b128 v[116:119], v82 offset:1536\n\t" \
    "v_mfma_f32_16x16x32_bf16 v[60:63], a[188:191], v[128:131], v[60:63]\n\t" "s_waitcnt lgkmcnt(4)\n\t" \
    "v_mfma_f32_16x16x32_bf16 v[48:51], a[192:195], v[100:103], v[48:51]\n\t" "ds_read_b128 v[120:123], v82 offset:34304\n\t" \
    "v_mfma_f32_16x16x32_bf16 v[52:55], a[196:199], v[100:103], v[52:55]\n\t" "s_waitcnt lgkmcnt(4)\n\t" \
    "v_mfma_f32_16x16x32_bf16 v[56:59], a[192:195], v[104:107], v[56:59]\n\t" "ds_read_b128 v[124:127], v83 offset:1536\n\t" \
    "v_mfma_f32_16x16x32_bf16 v[60:63], a[196:199], v[104:107], v[60:63]\n\t" "s_waitcnt lgkmcnt(4)\n\t" \
    "v_mfma_f32_16x16x32_bf16 v[48:51], a[200:203], v[108:111], v[48:51]\n\t" "ds_read_b128 v[128:131], v83 offset:34304\n\t" \
    "v_mfma_f32_16x16x32_bf16 v[52:55], a[204:207], v[108:111], v[52:55]\n\t" "s_waitcnt lgkmcnt(4)\n\t" \
    "v_mfma_f32_16x16x32_bf16 v[56:59], a[200:203], v[112:115], v[56:59]\n\t" "ds_read_b128 v[100:103], v84 offset:1536\n\t" \
    "v_mfma_f32_16x16x32_bf16 v[60:63], a[204:207], v[112:115], v[60:63]\n\t" "s_waitcnt lgkmcnt(4)\n\t" \
    "v_mfma_f32_16x16x32_bf16 v[48:51], a[208:211], v[116:119], v[48:51]\n\t" "ds_read_b128 v[104:107], v84 offset:34304\n\t" \
    "v_mfma_f32_16x16x32_bf16 v[52:55], a[212:215], v[116:119], v[52:55]\n\t" "s_waitcnt lgkmcnt(4)\n\t" \
    "v_mfma_f32_16x16x32_bf16 v[56:59], a[208:211], v[120:123], v[56:59]\n\t" "ds_read_b128 v[108:111], v85 offset:1536\n\t" \
    "v_mfma_f32_16x16x32_bf16 v[60:63], a[212:215], v[120:123], v[60:63]\n\t" "s_waitcnt lgkmcnt(4)\n\t" \
    "v_mfma_f32_16x16x32_bf16 v[48:51], a[216:219], v[124:127], v[48:51]\n\t" "ds_read_b128 v[112:115], v85 offset:34304\n\t" \
    "v_mfma_f32_16x16x32_bf16 v[52:55], a[220:223], v[124:127], v[52:55]\n\t" "s_waitcnt lgkmcnt(4)\n\t" \
    "v_mfma_f32_16x16x32_bf16 v[56:59], a[216:219], v[128:131], v[56:59]\n\t" "ds_read_b128 v[116:119], v86 offset:1536\n\t" \
    "v_mfma_f32_16x16x32_bf16 v[60:63], a[220:223], v[128:131], v[60:63]\n\t" "s_waitcnt lgkmcnt(4)\n\t" \
    "v_mfma_f32_16x16x32_bf16 v[48:51], a[224:227], v[100:103], v[48:51]\n\t" "ds_read_b128 v[120:123], v86 offset:34304\n\t" \
    "v_mfma_f32_16x16x32_bf16 v[52:55], a[228:231], v[100:103], v[52:55]\n\t" "s_waitcnt lgkmcnt(4)\n\t" \
    "v_mfma_f32_16x16x32_bf16 v[56:59], a[224:227], v[104:107], v[56:59]\n\t" "ds_read_b128 v[124:127], v87 offset:1536\n\t" \
    "v_mfma_f32_16x16x32_bf16 v[60:63], a[228:231], v[104:107], v[60:63]\n\t" "s_waitcnt lgkmcnt(4)\n\t" \
    "v_mfma_f32_16x16x32_bf16 v[48:51], a[232:235], v[108:111], v[48:51]\n\t" "ds_read_b128 v[128:131], v87 offset:34304\n\t" \
    "v_mfma_f32_16x16x32_bf16 v[52:55], a[236:239], v[108:111], v[52:55]\n\t" "s_waitcnt lgkmcnt(4)\n\t" \
    "v_mfma_f32_16x16x32_bf16 v[56:59], a[232:235], v[112:115], v[56:59]\n\t" "v_mfma_f32_16x16x32_bf16 v[60:63], a[236:239], v[112:115], v[60:63]\n\t" \
    "s_waitcnt lgkmcnt(3)\n\t" "v_mfma_f32_16x16x32_bf16 v[48:51], a[240:243], v[116:119], v[48:51]\n\t" \
    "v_mfma_f32_16x16x32_bf16 v[52:55], a[244:247], v[116:119], v[52:55]\n\t" "s_waitcnt vmcnt(1) lgkmcnt(0)\n\t" "s_barrier\n\t" XT(82, 83) \
    "v_mfma_f32_16x16x32_bf16 v[56:59], a[240:243], v[120:123], v[56:59]\n\t" "ds_read_b128 v[100:103], v72 offset:0\n\t" \
    "v_mfma_f32_16x16x32_bf16 v[60:63], a[244:247], v[120:123], v[60:63]\n\t" "v_mfma_f32_16x16x32_bf16 v[48:51], a[248:251], v[124:127], v[48:51]\n\t" \
    "ds_read_b128 v[104:107], v72 offset:32768\n\t" "v_mfma_f32_16x16x32_bf16 v[52:55], a[252:255], v[124:127], v[52:55]\n\t" "ds_read_b128 v[108:111], v73 offset:0\n\t" \
    "v_mfma_f32_16x16x32_bf16 v[56:59], a[248:251], v[128:131], v[56:59]\n\t" "ds_read_b128 v[112:115], v73 offset:32768\n\t" "ds_read_b128 v[116:119], v74 offset:0\n\t" \
    "v_mfma_f32_16x16x32_bf16 v[60:63], a[252:255], v[128:131], v[60:63]\n\t" "s_add_u32 s60, s60, 1\n\t" GSUM "s_cmp_lt_u32 s60, 64\n\t" "s_cbranch_scc1 10b\n\t" \
    "s_nop 7\n\t" "v_cvt_pk_bf16_f32 v64, v48, v49\n\t" "v_cvt_pk_bf16_f32 v65, v50, v51\n\t" "v_cvt_pk_bf16_f32 v66, v52, v53\n\t" "v_cvt_pk_bf16_f32 v67, v54, v55\n\t" \
    "v_cvt_pk_bf16_f32 v68, v56, v57\n\t" "v_cvt_pk_bf16_f32 v69, v58, v59\n\t" "v_cvt_pk_bf16_f32 v70, v60, v61\n\t" "v_cvt_pk_bf16_f32 v71, v62, v63\n\t" \
    "buffer_store_dwordx4 v[64:67], v96, %[rs], s62 offen offset:0 sc1\n\t" "buffer_store_dwordx4 v[68:71], v96, %[rs], s62 offen offset:256 sc1\n\t" "s_waitcnt vmcnt(0)\n\t" \
    "s_barrier\n\t" "v_mov_b32 v98, s60\n\t" "buffer_store_dword v98, %[fo], %[rc], %[fl] offen sc1\n\t" "s_waitcnt lgkmcnt(0)\n\t" "s_branch 30f\n\t" "20:\n\t" \
    "v_mov_b32 v96, %[so]\n\t" "s_nop 7\n\t" "v_cvt_pk_bf16_f32 v64, v32, v33\n\t" "v_cvt_pk_bf16_f32 v65, v34, v35\n\t" "v_cvt_pk_bf16_f32 v66, v36, v37\n\t" \
    "v_cvt_pk_bf16_f32 v67, v38, v39\n\t" "v_cvt_pk_bf16_f32 v68, v40, v41\n\t" "v_cvt_pk_bf16_f32 v69, v42, v43\n\t" "v_cvt_pk_bf16_f32 v70, v44, v45\n\t" \
    "v_cvt_pk_bf16_f32 v71, v46, v47\n\t" "buffer_store_dwordx4 v[64:67], v96, %[rs], s62 offen offset:0 sc1\n\t" \
    "buffer_store_dwordx4 v[68:71], v96, %[rs], s62 offen offset:256 sc1\n\t" "s_waitcnt vmcnt(0)\n\t" "s_barrier\n\t" "v_mov_b32 v98, s60\n\t" \
    "buffer_store_dword v98, %[fo], %[rc], %[fl] offen sc1\n\t" "s_branch 11b\n\t" "30:\n\t"
#define ZC_LOOP \
    ".p2align 6\n\t" "s_mov_b32 s60, 0\n\t" "s_mov_b32 s61, 0\n\t" "s_mov_b32 s62, 0\n\t" "s_mov_b32 s63, 0x7f000000\n\t" "s_cmp_ge_u32 s61, %[z0]\n\t" \
    "s_cbranch_scc1 52f\n\t" "51:\n\t" "buffer_load_dword v120, %[fo], %[rc], %[fv] offen sc1\n\t" "buffer_load_dword v121, %[fo], %[rc], %[vd] offen sc1\n\t" \
    "s_waitcnt vmcnt(0)\n\t" "v_readfirstlane_b32 s61, v120\n\t" "v_readfirstlane_b32 s66, v121\n\t" "s_nop 3\n\t" "s_cmp_ge_u32 s61, %[z0]\n\t" "s_cbranch_scc1 52f\n\t" \
    "s_bitcmp1_b32 s66, 1\n\t" "s_cbranch_scc1 90f\n\t" "s_cmp_eq_u32 %[bd], 0\n\t" "s_cbranch_scc1 52f\n\t" "s_sub_u32 %[bd], %[bd], 1\n\t" "s_sleep 2\n\t" \
    "s_branch 51b\n\t" "52:\n\t" "s_lshr_b32 s66, s60, 2\n\t" "s_xor_b32 s66, s66, %[xp]\n\t" "s_and_b32 s66, s66, 1\n\t" "s_lshl_b32 s66, s66, 13\n\t" \
    "s_lshl_b32 s64, s60, 19\n\t" "s_add_u32 s64, s64, s66\n\t" "s_add_u32 s64, s64, %[vb]\n\t" "buffer_load_dwordx4 v[16:19], %[ao], %[rs], s64 offen offset:0 sc1\n\t" \
    "buffer_load_dwordx4 v[20:23], %[ao], %[rs], s64 offen offset:256 sc1\n\t" "buffer_load_dwordx4 v[24:27], %[ao], %[rs], s64 offen offset:2048 sc1\n\t" \
    "buffer_load_dwordx4 v[28:31], %[ao], %[rs], s64 offen offset:2304 sc1\n\t" "buffer_load_dwordx4 v[32:35], %[a1], %[rs], s64 offen offset:0 sc1\n\t" \
    "buffer_load_dwordx4 v[36:39], %[a1], %[rs], s64 offen offset:256 sc1\n\t" "buffer_load_dwordx4 v[40:43], %[a1], %[rs], s64 offen offset:2048 sc1\n\t" \
    "buffer_load_dwordx4 v[44:47], %[a1], %[rs], s64 offen offset:2304 sc1\n\t" "s_waitcnt vmcnt(0)\n\t" "10:\n\t" XT(80, 81) "s_add_u32 s66, s62, %[rg]\n\t" \
    "s_cmp_lt_u32 s66, s60\n\t" "s_cbranch_scc1 60f\n\t" "63:\n\t" "s_cmp_lt_u32 s60, 12\n\t" "s_cbranch_scc1 40f\n\t" "s_add_u32 s66, s60, 1\n\t" "s_cmp_gt_u32 s61, s66\n\t" \
    "s_cbranch_scc0 40f\n\t" "v_mfma_f32_16x16x32_bf16 v[80:83], v[16:19], a[0:3], 0\n\t" "v_mfma_f32_16x16x32_bf16 v[84:87], v[20:23], a[0:3], 0\n\t" \
    "v_mfma_f32_16x16x32_bf16 v[80:83], v[24:27], a[4:7], v[80:83]\n\t" "v_mfma_f32_16x16x32_bf16 v[84:87], v[28:31], a[4:7], v[84:87]\n\t" \
    "v_mfma_f32_16x16x32_bf16 v[80:83], v[32:35], a[8:11], v[80:83]\n\t" "v_cvt_pk_bf16_f32 v112, v96, v97\n\t" \
    "v_mfma_f32_16x16x32_bf16 v[84:87], v[36:39], a[8:11], v[84:87]\n\t" "v_cvt_pk_bf16_f32 v113, v98, v99\n\t" \
    "v_mfma_f32_16x16x32_bf16 v[80:83], v[40:43], a[12:15], v[80:83]\n\t" "v_cvt_pk_bf16_f32 v114, v100, v101\n\t" \
    "v_mfma_f32_16x16x32_bf16 v[84:87], v[44:47], a[12:15], v[84:87]\n\t" "v_cvt_pk_bf16_f32 v115, v102, v103\n\t" \
    "v_mfma_f32_16x16x32_bf16 v[88:91], v[16:19], a[16:19], 0\n\t" "v_cvt_pk_bf16_f32 v116, v104, v105\n\t" "v_mfma_f32_16x16x32_bf16 v[92:95], v[20:23], a[16:19], 0\n\t" \
    "v_cvt_pk_bf16_f32 v117, v106, v107\n\t" "v_mfma_f32_16x16x32_bf16 v[88:91], v[24:27], a[20:23], v[88:91]\n\t" \
    "buffer_store_dwordx4 v[112:115], %[z1], %[rs], s63 offen sc1\n\t" "v_mfma_f32_16x16x32_bf16 v[92:95], v[28:31], a[20:23], v[92:95]\n\t" \
    "v_mfma_f32_16x16x32_bf16 v[88:91], v[32:35], a[24:27], v[88:91]\n\t" "v_cvt_pk_bf16_f32 v118, v108, v109\n\t" \
    "v_mfma_f32_16x16x32_bf16 v[92:95], v[36:39], a[24:27], v[92:95]\n\t" "v_cvt_pk_bf16_f32 v119, v110, v111\n\t" \
    "v_mfma_f32_16x16x32_bf16 v[88:91], v[40:43], a[28:31], v[88:91]\n\t" "buffer_store_dwordx4 v[116:119], %[z2], %[rs], s63 offen sc1\n\t" "s_and_b32 s63, s60, %[rm]\n\t" \
    "v_mfma_f32_16x16x32_bf16 v[92:95], v[44:47], a[28:31], v[92:95]\n\t" "s_lshl_b32 s63, s63, 22\n\t" "s_add_u32 s63, s63, %[zb]\n\t" \
    "v_mfma_f32_16x16x32_bf16 v[96:99], v[16:19], a[32:35], 0\n\t" "s_add_u32 s65, s60, 1\n\t" "s_lshr_b32 s66, s65, 2\n\t" \
    "v_mfma_f32_16x16x32_bf16 v[100:103], v[20:23], a[32:35], 0\n\t" "s_xor_b32 s66, s66, %[xp]\n\t" "s_and_b32 s66, s66, 1\n\t" \
    "v_mfma_f32_16x16x32_bf16 v[96:99], v[24:27], a[36:39], v[96:99]\n\t" "s_lshl_b32 s66, s66, 13\n\t" "s_lshl_b32 s64, s65, 19\n\t" \
    "v_mfma_f32_16x16x32_bf16 v[100:103], v[28:31], a[36:39], v[100:103]\n\t" "s_add_u32 s64, s64, s66\n\t" "s_add_u32 s64, s64, %[vb]\n\t" \
    "v_mfma_f32_16x16x32_bf16 v[96:99], v[32:35], a[40:43], v[96:99]\n\t" "buffer_load_dwordx4 v[48:51], %[ao], %[rs], s64 offen offset:0 sc1\n\t" \
    "v_cvt_pk_bf16_f32 v112, v80, v81\n\t" "v_mfma_f32_16x16x32_bf16 v[100:103], v[36:39], a[40:43], v[100:103]\n\t" "v_cvt_pk_bf16_f32 v113, v82, v83\n\t" \
    "v_mfma_f32_16x16x32_bf16 v[96:99], v[40:43], a[44:47], v[96:99]\n\t" "buffer_load_dwordx4 v[52:55], %[ao], %[rs], s64 offen offset:256 sc1\n\t" \
    "v_cvt_pk_bf16_f32 v114, v84, v85\n\t" "v_mfma_f32_16x16x32_bf16 v[100:103], v[44:47], a[44:47], v[100:103]\n\t" "v_cvt_pk_bf16_f32 v115, v86, v87\n\t" \
    "v_mfma_f32_16x16x32_bf16 v[104:107], v[16:19], a[48:51], 0\n\t" "buffer_load_dwordx4 v[56:59], %[ao], %[rs], s64 offen offset:2048 sc1\n\t" \
    "v_cvt_pk_bf16_f32 v116, v88, v89\n\t" "v_mfma_f32_16x16x32_bf16 v[108:111], v[20:23], a[48:51], 0\n\t" "v_cvt_pk_bf16_f32 v117, v90, v91\n\t" \
    "v_mfma_f32_16x16x32_bf16 v[104:107], v[24:27], a[52:55], v[104:107]\n\t" "buffer_load_dwordx4 v[60:63], %[ao], %[rs], s64 offen offset:2304 sc1\n\t" \
    "v_mfma_f32_16x16x32_bf16 v[108:111], v[28:31], a[52:55], v[108:111]\n\t" "buffer_store_dwordx4 v[112:115], %[z1], %[rs], s63 offen sc1\n\t" \
    "v_mfma_f32_16x16x32_bf16 v[104:107], v[32:35], a[56:59], v[104:107]\n\t" "buffer_load_dwordx4 v[64:67], %[a1], %[rs], s64 offen offset:0 sc1\n\t" \
    "v_cvt_pk_bf16_f32 v118, v92, v93\n\t" "v_mfma_f32_16x16x32_bf16 v[108:111], v[36:39], a[56:59], v[108:111]\n\t" "v_cvt_pk_bf16_f32 v119, v94, v95\n\t" \
    "v_mfma_f32_16x16x32_bf16 v[104:107], v[40:43], a[60:63], v[104:107]\n\t" "buffer_load_dwordx4 v[68:71], %[a1], %[rs], s64 offen offset:256 sc1\n\t" \
    "buffer_store_dwordx4 v[116:119], %[z2], %[rs], s63 offen sc1\n\t" "v_mfma_f32_16x16x32_bf16 v[108:111], v[44:47], a[60:63], v[108:111]\n\t" \
    "s_add_u32 s63, s63, 0x20000\n\t" "v_mfma_f32_16x16x32_bf16 v[80:83], v[16:19], a[64:67], 0\n\t" \
    "buffer_load_dwordx4 v[72:75], %[a1], %[rs], s64 offen offset:2048 sc1\n\t" "v_mfma_f32_16x16x32_bf16 v[84:87], v[20:23], a[64:67], 0\n\t" \
    "v_mfma_f32_16x16x32_bf16 v[80:83], v[24:27], a[68:71], v[80:83]\n\t" "buffer_load_dwordx4 v[76:79], %[a1], %[rs], s64 offen offset:2304 sc1\n\t" \
    "v_mfma_f32_16x16x32_bf16 v[84:87], v[28:31], a[68:71], v[84:87]\n\t" "v_mfma_f32_16x16x32_bf16 v[80:83], v[32:35], a[72:75], v[80:83]\n\t" \
    "buffer_load_dword v120, %[fo], %[rc], %[fv] offen sc1\n\t" "v_cvt_pk_bf16_f32 v112, v96, v97\n\t" "v_mfma_f32_16x16x32_bf16 v[84:87], v[36:39], a[72:75], v[84:87]\n\t" \
    "v_cvt_pk_bf16_f32 v113, v98, v99\n\t" "v_mfma_f32_16x16x32_bf16 v[80:83], v[40:43], a[76:79], v[80:83]\n\t" "buffer_load_dword v121, %[fo], %[rc], %[fs] offen sc1\n\t" \
    "v_mfma_f32_16x16x32_bf16 v[84:87], v[44:47], a[76:79], v[84:87]\n\t" "v_cvt_pk_bf16_f32 v114, v100, v101\n\t" "v_cvt_pk_bf16_f32 v115, v102, v103\n\t" \
    "v_mfma_f32_16x16x32_bf16 v[88:91], v[16:19], a[80:83], 0\n\t" "v_cvt_pk_bf16_f32 v116, v104, v105\n\t" "v_mfma_f32_16x16x32_bf16 v[92:95], v[20:23], a[80:83], 0\n\t" \
    "v_cvt_pk_bf16_f32 v117, v106, v107\n\t" "v_mfma_f32_16x16x32_bf16 v[88:91], v[24:27], a[84:87], v[88:91]\n\t" \
    "buffer_store_dwordx4 v[112:115], %[z1], %[rs], s63 offen sc1\n\t" "v_mfma_f32_16x16x32_bf16 v[92:95], v[28:31], a[84:87], v[92:95]\n\t" \
    "v_mfma_f32_16x16x32_bf16 v[88:91], v[32:35], a[88:91], v[88:91]\n\t" "v_cvt_pk_bf16_f32 v118, v108, v109\n\t" \
    "v_mfma_f32_16x16x32_bf16 v[92:95], v[36:39], a[88:91], v[92:95]\n\t" "v_cvt_pk_bf16_f32 v119, v110, v111\n\t" \
    "v_mfma_f32_16x16x32_bf16 v[88:91], v[40:43], a[92:95], v[88:91]\n\t" "buffer_store_dwordx4 v[116:119], %[z2], %[rs], s63 offen sc1\n\t" \
    "v_mfma_f32_16x16x32_bf16 v[92:95], v[44:47], a[92:95], v[92:95]\n\t" "s_add_u32 s63, s63, 0x20000\n\t" "v_mfma_f32_16x16x32_bf16 v[96:99], v[16:19], a[96:99], 0\n\t" \
    "v_mfma_f32_16x16x32_bf16 v[100:103], v[20:23], a[96:99], 0\n\t" "v_mfma_f32_16x16x32_bf16 v[96:99], v[24:27], a[100:103], v[96:99]\n\t" \
    "v_mfma_f32_16x16x32_bf16 v[100:103], v[28:31], a[100:103], v[100:103]\n\t" "v_mfma_f32_16x16x32_bf16 v[96:99], v[32:35], a[104:107], v[96:99]\n\t" \
    "v_cvt_pk_bf16_f32 v112, v80, v81\n\t" "v_mfma_f32_16x16x32_bf16 v[100:103], v[36:39], a[104:107], v[100:103]\n\t" "v_cvt_pk_bf16_f32 v113, v82, v83\n\t" \
    "v_mfma_f32_16x16x32_bf16 v[96:99], v[40:43], a[108:111], v[96:99]\n\t" "v_cvt_pk_bf16_f32 v114, v84, v85\n\t" \
    "v_mfma_f32_16x16x32_bf16 v[100:103], v[44:47], a[108:111], v[100:103]\n\t" "v_cvt_pk_bf16_f32 v115, v86, v87\n\t" \
    "v_mfma_f32_16x16x32_bf16 v[104:107], v[16:19], a[112:115], 0\n\t" "v_cvt_pk_bf16_f32 v116, v88, v89\n\t" \
    "v_mfma_f32_16x16x32_bf16 v[108:111], v[20:23], a[112:115], 0\n\t" "v_cvt_pk_bf16_f32 v117, v90, v91\n\t" \
    "v_mfma_f32_16x16x32_bf16 v[104:107], v[24:27], a[116:119], v[104:107]\n\t" "buffer_store_dwordx4 v[112:115], %[z1], %[rs], s63 offen sc1\n\t" \
    "v_mfma_f32_16x16x32_bf16 v[108:111], v[28:31], a[116:119], v[108:111]\n\t" "v_mfma_f32_16x16x32_bf16 v[104:107], v[32:35], a[120:123], v[104:107]\n\t" \
    "v_cvt_pk_bf16_f32 v118, v92, v93\n\t" "v_mfma_f32_16x16x32_bf16 v[108:111], v[36:39], a[120:123], v[108:111]\n\t" "v_cvt_pk_bf16_f32 v119, v94, v95\n\t" \
    "v_mfma_f32_16x16x32_bf16 v[104:107], v[40:43], a[124:127], v[104:107]\n\t" "buffer_store_dwordx4 v[116:119], %[z2], %[rs], s63 offen sc1\n\t" \
    "v_mfma_f32_16x16x32_bf16 v[108:111], v[44:47], a[124:127], v[108:111]\n\t" "s_add_u32 s63, s63, 0x20000\n\t" \
    "v_mfma_f32_16x16x32_bf16 v[80:83], v[16:19], a[128:131], 0\n\t" "v_mfma_f32_16x16x32_bf16 v[84:87], v[20:23], a[128:131], 0\n\t" \
    "v_mfma_f32_16x16x32_bf16 v[80:83], v[24:27], a[132:135], v[80:83]\n\t" "v_mfma_f32_16x16x32_bf16 v[84:87], v[28:31], a[132:135], v[84:87]\n\t" \
    "v_mfma_f32_16x16x32_bf16 v[80:83], v[32:35], a[136:139], v[80:83]\n\t" "v_cvt_pk_bf16_f32 v112, v96, v97\n\t" \
    "v_mfma_f32_16x16x32_bf16 v[84:87], v[36:39], a[136:139], v[84:87]\n\t" "v_cvt_pk_bf16_f32 v113, v98, v99\n\t" \
    "v_mfma_f32_16x16x32_bf16 v[80:83], v[40:43], a[140:143], v[80:83]\n\t" "v_cvt_pk_bf16_f32 v114, v100, v101\n\t" \
    "v_mfma_f32_16x16x32_bf16 v[84:87], v[44:47], a[140:143], v[84:87]\n\t" "v_cvt_pk_bf16_f32 v115, v102, v103\n\t" \
    "v_mfma_f32_16x16x32_bf16 v[88:91], v[16:19], a[144:147], 0\n\t" "v_cvt_pk_bf16_f32 v116, v104, v105\n\t" "v_mfma_f32_16x16x32_bf16 v[92:95], v[20:23], a[144:147], 0\n\t" \
    "v_cvt_pk_bf16_f32 v117, v106, v107\n\t" "v_mfma_f32_16x16x32_bf16 v[88:91], v[24:27], a[148:151], v[88:91]\n\t" \
    "buffer_store_dwordx4 v[112:115], %[z1], %[rs], s63 offen sc1\n\t" "v_mfma_f32_16x16x32_bf16 v[92:95], v[28:31], a[148:151], v[92:95]\n\t" \
    "v_mfma_f32_16x16x32_bf16 v[88:91], v[32:35], a[152:155], v[88:91]\n\t" "v_cvt_pk_bf16_f32 v118, v108, v109\n\t" \
    "v_mfma_f32_16x16x32_bf16 v[92:95], v[36:39], a[152:155], v[92:95]\n\t" "v_cvt_pk_bf16_f32 v119, v110, v111\n\t" \
    "v_mfma_f32_16x16x32_bf16 v[88:91], v[40:43], a[156:159], v[88:91]\n\t" "buffer_store_dwordx4 v[116:119], %[z2], %[rs], s63 offen sc1\n\t" \
    "v_mfma_f32_16x16x32_bf16 v[92:95], v[44:47], a[156:159], v[92:95]\n\t" "s_add_u32 s63, s63, 0x20000\n\t" "v_mfma_f32_16x16x32_bf16 v[96:99], v[16:19], a[160:163], 0\n\t" \
    "v_mfma_f32_16x16x32_bf16 v[100:103], v[20:23], a[160:163], 0\n\t" "v_mfma_f32_16x16x32_bf16 v[96:99], v[24:27], a[164:167], v[96:99]\n\t" \
    "v_mfma_f32_16x16x32_bf16 v[100:103], v[28:31], a[164:167], v[100:103]\n\t" "v_mfma_f32_16x16x32_bf16 v[96:99], v[32:35], a[168:171], v[96:99]\n\t" \
    "v_cvt_pk_bf16_f32 v112, v80, v81\n\t" "v_mfma_f32_16x16x32_bf16 v[100:103], v[36:39], a[168:171], v[100:103]\n\t" "v_cvt_pk_bf16_f32 v113, v82, v83\n\t" \
    "v_mfma_f32_16x16x32_bf16 v[96:99], v[40:43], a[172:175], v[96:99]\n\t" "v_cvt_pk_bf16_f32 v114, v84, v85\n\t" \
    "v_mfma_f32_16x16x32_bf16 v[100:103], v[44:47], a[172:175], v[100:103]\n\t" "v_cvt_pk_bf16_f32 v115, v86, v87\n\t" \
    "v_mfma_f32_16x16x32_bf16 v[104:107], v[16:19], a[176:179], 0\n\t" "v_cvt_pk_bf16_f32 v116, v88, v89\n\t" \
    "v_mfma_f32_16x16x32_bf16 v[108:111], v[20:23], a[176:179], 0\n\t" "v_cvt_pk_bf16_f32 v117, v90, v91\n\t" \
    "v_mfma_f32_16x16x32_bf16 v[104:107], v[24:27], a[180:183], v[104:107]\n\t" "buffer_store_dwordx4 v[112:115], %[z1], %[rs], s63 offen sc1\n\t" \
    "v_mfma_f32_16x16x32_bf16 v[108:111], v[28:31], a[180:183], v[108:111]\n\t" "v_mfma_f32_16x16x32_bf16 v[104:107], v[32:35], a[184:187], v[104:107]\n\t" \
    "v_cvt_pk_bf16_f32 v118, v92, v93\n\t" "v_mfma_f32_16x16x32_bf16 v[108:111], v[36:39], a[184:187], v[108:111]\n\t" "v_cvt_pk_bf16_f32 v119, v94, v95\n\t" \
    "v_mfma_f32_16x16x32_bf16 v[104:107], v[40:43], a[188:191], v[104:107]\n\t" "buffer_store_dwordx4 v[116:119], %[z2], %[rs], s63 offen sc1\n\t" \
    "v_mfma_f32_16x16x32_bf16 v[108:111], v[44:47], a[188:191], v[108:111]\n\t" "s_add_u32 s63, s63, 0x20000\n\t" \
    "v_mfma_f32_16x16x32_bf16 v[80:83], v[16:19], a[192:195], 0\n\t" "v_mfma_f32_16x16x32_bf16 v[84:87], v[20:23], a[192:195], 0\n\t" \
    "v_mfma_f32_16x16x32_bf16 v[80:83], v[24:27], a[196:199], v[80:83]\n\t" "v_mfma_f32_16x16x32_bf16 v[84:87], v[28:31], a[196:199], v[84:87]\n\t" \
    "v_mfma_f32_16x16x32_bf16 v[80:83], v[32:35], a[200:203], v[80:83]\n\t" "v_cvt_pk_bf16_f32 v112, v96, v97\n\t" \
    "v_mfma_f32_16x16x32_bf16 v[84:87], v[36:39], a[200:203], v[84:87]\n\t" "v_cvt_pk_bf16_f32 v113, v98, v99\n\t" \
    "v_mfma_f32_16x16x32_bf16 v[80:83], v[40:43], a[204:207], v[80:83]\n\t" "v_cvt_pk_bf16_f32 v114, v100, v101\n\t" \
    "v_mfma_f32_16x16x32_bf16 v[84:87], v[44:47], a[204:207], v[84:87]\n\t" "v_cvt_pk_bf16_f32 v115, v102, v103\n\t" \
    "v_mfma_f32_16x16x32_bf16 v[88:91], v[16:19], a[208:211], 0\n\t" "v_cvt_pk_bf16_f32 v116, v104, v105\n\t" "v_mfma_f32_16x16x32_bf16 v[92:95], v[20:23], a[208:211], 0\n\t" \
    "v_cvt_pk_bf16_f32 v117, v106, v107\n\t" "v_mfma_f32_16x16x32_bf16 v[88:91], v[24:27], a[212:215], v[88:91]\n\t" \
    "buffer_store_dwordx4 v[112:115], %[z1], %[rs], s63 offen sc1\n\t" "v_mfma_f32_16x16x32_bf16 v[92:95], v[28:31], a[212:215], v[92:95]\n\t" \
    "v_mfma_f32_16x16x32_bf16 v[88:91], v[32:35], a[216:219], v[88:91]\n\t" "v_cvt_pk_bf16_f32 v118, v108, v109\n\t" \
    "v_mfma_f32_16x16x32_bf16 v[92:95], v[36:39], a[216:219], v[92:95]\n\t" "v_cvt_pk_bf16_f32 v119, v110, v111\n\t" \
    "v_mfma_f32_16x16x32_bf16 v[88:91], v[40:43], a[220:223], v[88:91]\n\t" "buffer_store_dwordx4 v[116:119], %[z2], %[rs], s63 offen sc1\n\t" \
    "v_mfma_f32_16x16x32_bf16 v[92:95], v[44:47], a[220:223], v[92:95]\n\t" "s_add_u32 s63, s63, 0x20000\n\t" "v_mfma_f32_16x16x32_bf16 v[96:99], v[16:19], a[224:227], 0\n\t" \
    "v_mfma_f32_16x16x32_bf16 v[100:103], v[20:23], a[224:227], 0\n\t" "v_mfma_f32_16x16x32_bf16 v[96:99], v[24:27], a[228:231], v[96:99]\n\t" \
    "v_mfma_f32_16x16x32_bf16 v[100:103], v[28:31], a[228:231], v[100:103]\n\t" "v_mfma_f32_16x16x32_bf16 v[96:99], v[32:35], a[232:235], v[96:99]\n\t" \
    "v_cvt_pk_bf16_f32 v112, v80, v81\n\t" "v_mfma_f32_16x16x32_bf16 v[100:103], v[36:39], a[232:235], v[100:103]\n\t" "v_cvt_pk_bf16_f32 v113, v82, v83\n\t" \
    "v_mfma_f32_16x16x32_bf16 v[96:99], v[40:43], a[236:239], v[96:99]\n\t" "v_cvt_pk_bf16_f32 v114, v84, v85\n\t" \
    "v_mfma_f32_16x16x32_bf16 v[100:103], v[44:47], a[236:239], v[100:103]\n\t" "v_cvt_pk_bf16_f32 v115, v86, v87\n\t" \
    "v_mfma_f32_16x16x32_bf16 v[104:107], v[16:19], a[240:243], 0\n\t" "v_cvt_pk_bf16_f32 v116, v88, v89\n\t" \
    "v_mfma_f32_16x16x32_bf16 v[108:111], v[20:23], a[240:243], 0\n\t" "v_cvt_pk_bf16_f32 v117, v90, v91\n\t" \
    "v_mfma_f32_16x16x32_bf16 v[104:107], v[24:27], a[244:247], v[104:107]\n\t" "buffer_store_dwordx4 v[112:115], %[z1], %[rs], s63 offen sc1\n\t" \
    "v_mfma_f32_16x16x32_bf16 v[108:111], v[28:31], a[244:247], v[108:111]\n\t" "v_mfma_f32_16x16x32_bf16 v[104:107], v[32:35], a[248:251], v[104:107]\n\t" \
    "v_cvt_pk_bf16_f32 v118, v92, v93\n\t" "v_cvt_pk_bf16_f32 v119, v94, v95\n\t" "s_waitcnt vmcnt(11)\n\t" "v_readfirstlane_b32 s66, v120\n\t" \
    "v_mfma_f32_16x16x32_bf16 v[108:111], v[36:39], a[248:251], v[108:111]\n\t" "v_readfirstlane_b32 s67, v121\n\t" "v_mov_b32 v122, s60\n\t" \
    "buffer_store_dword v122, %[fo], %[rc], %[fz] offen sc1\n\t" "v_mfma_f32_16x16x32_bf16 v[104:107], v[40:43], a[252:255], v[104:107]\n\t" \
    "buffer_store_dwordx4 v[116:119], %[z2], %[rs], s63 offen sc1\n\t" "s_add_u32 s63, s63, 0x20000\n\t" "s_max_u32 s61, s61, s66\n\t" \
    "v_mfma_f32_16x16x32_bf16 v[108:111], v[44:47], a[252:255], v[108:111]\n\t" "s_max_u32 s62, s62, s67\n\t" "s_add_u32 s60, s60, 1\n\t" ZSUM "23:\n\t" XT(80, 81) \
    "s_add_u32 s66, s62, %[rg]\n\t" "s_cmp_lt_u32 s66, s60\n\t" "s_cbranch_scc1 70f\n\t" "73:\n\t" "s_cmp_lt_u32 s60, 12\n\t" "s_cbranch_scc1 41f\n\t" \
    "s_add_u32 s66, s60, 1\n\t" "s_cmp_gt_u32 s61, s66\n\t" "s_cbranch_scc0 41f\n\t" "v_mfma_f32_16x16x32_bf16 v[80:83], v[48:51], a[0:3], 0\n\t" \
    "v_mfma_f32_16x16x32_bf16 v[84:87], v[52:55], a[0:3], 0\n\t" "v_mfma_f32_16x16x32_bf16 v[80:83], v[56:59], a[4:7], v[80:83]\n\t" \
    "v_mfma_f32_16x16x32_bf16 v[84:87], v[60:63], a[4:7], v[84:87]\n\t" "v_mfma_f32_16x16x32_bf16 v[80:83], v[64:67], a[8:11], v[80:83]\n\t" \
    "v_cvt_pk_bf16_f32 v112, v96, v97\n\t" "v_mfma_f32_16x16x32_bf16 v[84:87], v[68:71], a[8:11], v[84:87]\n\t" "v_cvt_pk_bf16_f32 v113, v98, v99\n\t" \
    "v_mfma_f32_16x16x32_bf16 v[80:83], v[72:75], a[12:15], v[80:83]\n\t" "v_cvt_pk_bf16_f32 v114, v100, v101\n\t" \
    "v_mfma_f32_16x16x32_bf16 v[84:87], v[76:79], a[12:15], v[84:87]\n\t" "v_cvt_pk_bf16_f32 v115, v102, v103\n\t" \
    "v_mfma_f32_16x16x32_bf16 v[88:91], v[48:51], a[16:19], 0\n\t" "v_cvt_pk_bf16_f32 v116, v104, v105\n\t" "v_mfma_f32_16x16x32_bf16 v[92:95], v[52:55], a[16:19], 0\n\t" \
    "v_cvt_pk_bf16_f32 v117, v106, v107\n\t" "v_mfma_f32_16x16x32_bf16 v[88:91], v[56:59], a[20:23], v[88:91]\n\t" \
    "buffer_store_dwordx4 v[112:115], %[z1], %[rs], s63 offen sc1\n\t" "v_mfma_f32_16x16x32_bf16 v[92:95], v[60:63], a[20:23], v[92:95]\n\t" \
    "v_mfma_f32_16x16x32_bf16 v[88:91], v[64:67], a[24:27], v[88:91]\n\t" "v_cvt_pk_bf16_f32 v118, v108, v109\n\t" \
    "v_mfma_f32_16x16x32_bf16 v[92:95], v[68:71], a[24:27], v[92:95]\n\t" "v_cvt_pk_bf16_f32 v119, v110, v111\n\t" \
    "v_mfma_f32_16x16x32_bf16 v[88:91], v[72:75], a[28:31], v[88:91]\n\t" "buffer_store_dwordx4 v[116:119], %[z2], %[rs], s63 offen sc1\n\t" "s_and_b32 s63, s60, %[rm]\n\t" \
    "v_mfma_f32_16x16x32_bf16 v[92:95], v[76:79], a[28:31], v[92:95]\n\t" "s_lshl_b32 s63, s63, 22\n\t" "s_add_u32 s63, s63, %[zb]\n\t" \
    "v_mfma_f32_16x16x32_bf16 v[96:99], v[48:51], a[32:35], 0\n\t" "s_add_u32 s65, s60, 1\n\t" "s_lshr_b32 s66, s65, 2\n\t" \
    "v_mfma_f32_16x16x32_bf16 v[100:103], v[52:55], a[32:35], 0\n\t" "s_xor_b32 s66, s66, %[xp]\n\t" "s_and_b32 s66, s66, 1\n\t" \
    "v_mfma_f32_16x16x32_bf16 v[96:99], v[56:59], a[36:39], v[96:99]\n\t" "s_lshl_b32 s66, s66, 13\n\t" "s_lshl_b32 s64, s65, 19\n\t" \
    "v_mfma_f32_16x16x32_bf16 v[100:103], v[60:63], a[36:39], v[100:103]\n\t" "s_add_u32 s64, s64, s66\n\t" "s_add_u32 s64, s64, %[vb]\n\t" \
    "v_mfma_f32_16x16x32_bf16 v[96:99], v[64:67], a[40:43], v[96:99]\n\t" "buffer_load_dwordx4 v[16:19], %[ao], %[rs], s64 offen offset:0 sc1\n\t" \
    "v_cvt_pk_bf16_f32 v112, v80, v81\n\t" "v_mfma_f32_16x16x32_bf16 v[100:103], v[68:71], a[40:43], v[100:103]\n\t" "v_cvt_pk_bf16_f32 v113, v82, v83\n\t" \
    "v_mfma_f32_16x16x32_bf16 v[96:99], v[72:75], a[44:47], v[96:99]\n\t" "buffer_load_dwordx4 v[20:23], %[ao], %[rs], s64 offen offset:256 sc1\n\t" \
    "v_cvt_pk_bf16_f32 v114, v84, v85\n\t" "v_mfma_f32_16x16x32_bf16 v[100:103], v[76:79], a[44:47], v[100:103]\n\t" "v_cvt_pk_bf16_f32 v115, v86, v87\n\t" \
    "v_mfma_f32_16x16x32_bf16 v[104:107], v[48:51], a[48:51], 0\n\t" "buffer_load_dwordx4 v[24:27], %[ao], %[rs], s64 offen offset:2048 sc1\n\t" \
    "v_cvt_pk_bf16_f32 v116, v88, v89\n\t" "v_mfma_f32_16x16x32_bf16 v[108:111], v[52:55], a[48:51], 0\n\t" "v_cvt_pk_bf16_f32 v117, v90, v91\n\t" \
    "v_mfma_f32_16x16x32_bf16 v[104:107], v[56:59], a[52:55], v[104:107]\n\t" "buffer_load_dwordx4 v[28:31], %[ao], %[rs], s64 offen offset:2304 sc1\n\t" \
    "v_mfma_f32_16x16x32_bf16 v[108:111], v[60:63], a[52:55], v[108:111]\n\t" "buffer_store_dwordx4 v[112:115], %[z1], %[rs], s63 offen sc1\n\t" \
    "v_mfma_f32_16x16x32_bf16 v[104:107], v[64:67], a[56:59], v[104:107]\n\t" "buffer_load_dwordx4 v[32:35], %[a1], %[rs], s64 offen offset:0 sc1\n\t" \
    "v_cvt_pk_bf16_f32 v118, v92, v93\n\t" "v_mfma_f32_16x16x32_bf16 v[108:111], v[68:71], a[56:59], v[108:111]\n\t" "v_cvt_pk_bf16_f32 v119, v94, v95\n\t" \
    "v_mfma_f32_16x16x32_bf16 v[104:107], v[72:75], a[60:63], v[104:107]\n\t" "buffer_load_dwordx4 v[36:39], %[a1], %[rs], s64 offen offset:256 sc1\n\t" \
    "buffer_store_dwordx4 v[116:119], %[z2], %[rs], s63 offen sc1\n\t" "v_mfma_f32_16x16x32_bf16 v[108:111], v[76:79], a[60:63], v[108:111]\n\t" \
    "s_add_u32 s63, s63, 0x20000\n\t" "v_mfma_f32_16x16x32_bf16 v[80:83], v[48:51], a[64:67], 0\n\t" \
    "buffer_load_dwordx4 v[40:43], %[a1], %[rs], s64 offen offset:2048 sc1\n\t" "v_mfma_f32_16x16x32_bf16 v[84:87], v[52:55], a[64:67], 0\n\t" \
    "v_mfma_f32_16x16x32_bf16 v[80:83], v[56:59], a[68:71], v[80:83]\n\t" "buffer_load_dwordx4 v[44:47], %[a1], %[rs], s64 offen offset:2304 sc1\n\t" \
    "v_mfma_f32_16x16x32_bf16 v[84:87], v[60:63], a[68:71], v[84:87]\n\t" "v_mfma_f32_16x16x32_bf16 v[80:83], v[64:67], a[72:75], v[80:83]\n\t" \
    "buffer_load_dword v120, %[fo], %[rc], %[fv] offen sc1\n\t" "v_cvt_pk_bf16_f32 v112, v96, v97\n\t" "v_mfma_f32_16x16x32_bf16 v[84:87], v[68:71], a[72:75], v[84:87]\n\t" \
    "v_cvt_pk_bf16_f32 v113, v98, v99\n\t" "v_mfma_f32_16x16x32_bf16 v[80:83], v[72:75], a[76:79], v[80:83]\n\t" "buffer_load_dword v121, %[fo], %[rc], %[fs] offen sc1\n\t" \
    "v_mfma_f32_16x16x32_bf16 v[84:87], v[76:79], a[76:79], v[84:87]\n\t" "v_cvt_pk_bf16_f32 v114, v100, v101\n\t" "v_cvt_pk_bf16_f32 v115, v102, v103\n\t" \
    "v_mfma_f32_16x16x32_bf16 v[88:91], v[48:51], a[80:83], 0\n\t" "v_cvt_pk_bf16_f32 v116, v104, v105\n\t" "v_mfma_f32_16x16x32_bf16 v[92:95], v[52:55], a[80:83], 0\n\t" \
    "v_cvt_pk_bf16_f32 v117, v106, v107\n\t" "v_mfma_f32_16x16x32_bf16 v[88:91], v[56:59], a[84:87], v[88:91]\n\t" \
    "buffer_store_dwordx4 v[112:115], %[z1], %[rs], s63 offen sc1\n\t" "v_mfma_f32_16x16x32_bf16 v[92:95], v[60:63], a[84:87], v[92:95]\n\t" \
    "v_mfma_f32_16x16x32_bf16 v[88:91], v[64:67], a[88:91], v[88:91]\n\t" "v_cvt_pk_bf16_f32 v118, v108, v109\n\t" \
    "v_mfma_f32_16x16x32_bf16 v[92:95], v[68:71], a[88:91], v[92:95]\n\t" "v_cvt_pk_bf16_f32 v119, v110, v111\n\t" \
    "v_mfma_f32_16x16x32_bf16 v[88:91], v[72:75], a[92:95], v[88:91]\n\t" "buffer_store_dwordx4 v[116:119], %[z2], %[rs], s63 offen sc1\n\t" \
    "v_mfma_f32_16x16x32_bf16 v[92:95], v[76:79], a[92:95], v[92:95]\n\t" "s_add_u32 s63, s63, 0x20000\n\t" "v_mfma_f32_16x16x32_bf16 v[96:99], v[48:51], a[96:99], 0\n\t" \
    "v_mfma_f32_16x16x32_bf16 v[100:103], v[52:55], a[96:99], 0\n\t" "v_mfma_f32_16x16x32_bf16 v[96:99], v[56:59], a[100:103], v[96:99]\n\t" \
    "v_mfma_f32_16x16x32_bf16 v[100:103], v[60:63], a[100:103], v[100:103]\n\t" "v_mfma_f32_16x16x32_bf16 v[96:99], v[64:67], a[104:107], v[96:99]\n\t" \
    "v_cvt_pk_bf16_f32 v112, v80, v81\n\t" "v_mfma_f32_16x16x32_bf16 v[100:103], v[68:71], a[104:107], v[100:103]\n\t" "v_cvt_pk_bf16_f32 v113, v82, v83\n\t" \
    "v_mfma_f32_16x16x32_bf16 v[96:99], v[72:75], a[108:111], v[96:99]\n\t" "v_cvt_pk_bf16_f32 v114, v84, v85\n\t" \
    "v_mfma_f32_16x16x32_bf16 v[100:103], v[76:79], a[108:111], v[100:103]\n\t" "v_cvt_pk_bf16_f32 v115, v86, v87\n\t" \
    "v_mfma_f32_16x16x32_bf16 v[104:107], v[48:51], a[112:115], 0\n\t" "v_cvt_pk_bf16_f32 v116, v88, v89\n\t" \
    "v_mfma_f32_16x16x32_bf16 v[108:111], v[52:55], a[112:115], 0\n\t" "v_cvt_pk_bf16_f32 v117, v90, v91\n\t" \
    "v_mfma_f32_16x16x32_bf16 v[104:107], v[56:59], a[116:119], v[104:107]\n\t" "buffer_store_dwordx4 v[112:115], %[z1], %[rs], s63 offen sc1\n\t" \
    "v_mfma_f32_16x16x32_bf16 v[108:111], v[60:63], a[116:119], v[108:111]\n\t" "v_mfma_f32_16x16x32_bf16 v[104:107], v[64:67], a[120:123], v[104:107]\n\t" \
    "v_cvt_pk_bf16_f32 v118, v92, v93\n\t" "v_mfma_f32_16x16x32_bf16 v[108:111], v[68:71], a[120:123], v[108:111]\n\t" "v_cvt_pk_bf16_f32 v119, v94, v95\n\t" \
    "v_mfma_f32_16x16x32_bf16 v[104:107], v[72:75], a[124:127], v[104:107]\n\t" "buffer_store_dwordx4 v[116:119], %[z2], %[rs], s63 offen sc1\n\t" \
    "v_mfma_f32_16x16x32_bf16 v[108:111], v[76:79], a[124:127], v[108:111]\n\t" "s_add_u32 s63, s63, 0x20000\n\t" \
    "v_mfma_f32_16x16x32_bf16 v[80:83], v[48:51], a[128:131], 0\n\t" "v_mfma_f32_16x16x32_bf16 v[84:87], v[52:55], a[128:131], 0\n\t" \
    "v_mfma_f32_16x16x32_bf16 v[80:83], v[56:59], a[132:135], v[80:83]\n\t" "v_mfma_f32_16x16x32_bf16 v[84:87], v[60:63], a[132:135], v[84:87]\n\t" \
    "v_mfma_f32_16x16x32_bf16 v[80:83], v[64:67], a[136:139], v[80:83]\n\t" "v_cvt_pk_bf16_f32 v112, v96, v97\n\t" \
    "v_mfma_f32_16x16x32_bf16 v[84:87], v[68:71], a[136:139], v[84:87]\n\t" "v_cvt_pk_bf16_f32 v113, v98, v99\n\t" \
    "v_mfma_f32_16x16x32_bf16 v[80:83], v[72:75], a[140:143], v[80:83]\n\t" "v_cvt_pk_bf16_f32 v114, v100, v101\n\t" \
    "v_mfma_f32_16x16x32_bf16 v[84:87], v[76:79], a[140:143], v[84:87]\n\t" "v_cvt_pk_bf16_f32 v115, v102, v103\n\t" \
    "v_mfma_f32_16x16x32_bf16 v[88:91], v[48:51], a[144:147], 0\n\t" "v_cvt_pk_bf16_f32 v116, v104, v105\n\t" "v_mfma_f32_16x16x32_bf16 v[92:95], v[52:55], a[144:147], 0\n\t" \
    "v_cvt_pk_bf16_f32 v117, v106, v107\n\t" "v_mfma_f32_16x16x32_bf16 v[88:91], v[56:59], a[148:151], v[88:91]\n\t" \
    "buffer_store_dwordx4 v[112:115], %[z1], %[rs], s63 offen sc1\n\t" "v_mfma_f32_16x16x32_bf16 v[92:95], v[60:63], a[148:151], v[92:95]\n\t" \
    "v_mfma_f32_16x16x32_bf16 v[88:91], v[64:67], a[152:155], v[88:91]\n\t" "v_cvt_pk_bf16_f32 v118, v108, v109\n\t" \
    "v_mfma_f32_16x16x32_bf16 v[92:95], v[68:71], a[152:155], v[92:95]\n\t" "v_cvt_pk_bf16_f32 v119, v110, v111\n\t" \
    "v_mfma_f32_16x16x32_bf16 v[88:91], v[72:75], a[156:159], v[88:91]\n\t" "buffer_store_dwordx4 v[116:119], %[z2], %[rs], s63 offen sc1\n\t" \
    "v_mfma_f32_16x16x32_bf16 v[92:95], v[76:79], a[156:159], v[92:95]\n\t" "s_add_u32 s63, s63, 0x20000\n\t" "v_mfma_f32_16x16x32_bf16 v[96:99], v[48:51], a[160:163], 0\n\t" \
    "v_mfma_f32_16x16x32_bf16 v[100:103], v[52:55], a[160:163], 0\n\t" "v_mfma_f32_16x16x32_bf16 v[96:99], v[56:59], a[164:167], v[96:99]\n\t" \
    "v_mfma_f32_16x16x32_bf16 v[100:103], v[60:63], a[164:167], v[100:103]\n\t" "v_mfma_f32_16x16x32_bf16 v[96:99], v[64:67], a[168:171], v[96:99]\n\t" \
    "v_cvt_pk_bf16_f32 v112, v80, v81\n\t" "v_mfma_f32_16x16x32_bf16 v[100:103], v[68:71], a[168:171], v[100:103]\n\t" "v_cvt_pk_bf16_f32 v113, v82, v83\n\t" \
    "v_mfma_f32_16x16x32_bf16 v[96:99], v[72:75], a[172:175], v[96:99]\n\t" "v_cvt_pk_bf16_f32 v114, v84, v85\n\t" \
    "v_mfma_f32_16x16x32_bf16 v[100:103], v[76:79], a[172:175], v[100:103]\n\t" "v_cvt_pk_bf16_f32 v115, v86, v87\n\t" \
    "v_mfma_f32_16x16x32_bf16 v[104:107], v[48:51], a[176:179], 0\n\t" "v_cvt_pk_bf16_f32 v116, v88, v89\n\t" \
    "v_mfma_f32_16x16x32_bf16 v[108:111], v[52:55], a[176:179], 0\n\t" "v_cvt_pk_bf16_f32 v117, v90, v91\n\t" \
    "v_mfma_f32_16x16x32_bf16 v[104:107], v[56:59], a[180:183], v[104:107]\n\t" "buffer_store_dwordx4 v[112:115], %[z1], %[rs], s63 offen sc1\n\t" \
    "v_mfma_f32_16x16x32_bf16 v[108:111], v[60:63], a[180:183], v[108:111]\n\t" "v_mfma_f32_16x16x32_bf16 v[104:107], v[64:67], a[184:187], v[104:107]\n\t" \
    "v_cvt_pk_bf16_f32 v118, v92, v93\n\t" "v_mfma_f32_16x16x32_bf16 v[108:111], v[68:71], a[184:187], v[108:111]\n\t" "v_cvt_pk_bf16_f32 v119, v94, v95\n\t" \
    "v_mfma_f32_16x16x32_bf16 v[104:107], v[72:75], a[188:191], v[104:107]\n\t" "buffer_store_dwordx4 v[116:119], %[z2], %[rs], s63 offen sc1\n\t" \
    "v_mfma_f32_16x16x32_bf16 v[108:111], v[76:79], a[188:191], v[108:111]\n\t" "s_add_u32 s63, s63, 0x20000\n\t" \
    "v_mfma_f32_16x16x32_bf16 v[80:83], v[48:51], a[192:195], 0\n\t" "v_mfma_f32_16x16x32_bf16 v[84:87], v[52:55], a[192:195], 0\n\t" \
    "v_mfma_f32_16x16x32_bf16 v[80:83], v[56:59], a[196:199], v[80:83]\n\t" "v_mfma_f32_16x16x32_bf16 v[84:87], v[60:63], a[196:199], v[84:87]\n\t" \
    "v_mfma_f32_16x16x32_bf16 v[80:83], v[64:67], a[200:203], v[80:83]\n\t" "v_cvt_pk_bf16_f32 v112, v96, v97\n\t" \
    "v_mfma_f32_16x16x32_bf16 v[84:87], v[68:71], a[200:203], v[84:87]\n\t" "v_cvt_pk_bf16_f32 v113, v98, v99\n\t" \
    "v_mfma_f32_16x16x32_bf16 v[80:83], v[72:75], a[204:207], v[80:83]\n\t" "v_cvt_pk_bf16_f32 v114, v100, v101\n\t" \
    "v_mfma_f32_16x16x32_bf16 v[84:87], v[76:79], a[204:207], v[84:87]\n\t" "v_cvt_pk_bf16_f32 v115, v102, v103\n\t" \
    "v_mfma_f32_16x16x32_bf16 v[88:91], v[48:51], a[208:211], 0\n\t" "v_cvt_pk_bf16_f32 v116, v104, v105\n\t" "v_mfma_f32_16x16x32_bf16 v[92:95], v[52:55], a[208:211], 0\n\t" \
    "v_cvt_pk_bf16_f32 v117, v106, v107\n\t" "v_mfma_f32_16x16x32_bf16 v[88:91], v[56:59], a[212:215], v[88:91]\n\t" \
    "buffer_store_dwordx4 v[112:115], %[z1], %[rs], s63 offen sc1\n\t" "v_mfma_f32_16x16x32_bf16 v[92:95], v[60:63], a[212:215], v[92:95]\n\t" \
    "v_mfma_f32_16x16x32_bf16 v[88:91], v[64:67], a[216:219], v[88:91]\n\t" "v_cvt_pk_bf16_f32 v118, v108, v109\n\t" \
    "v_mfma_f32_16x16x32_bf16 v[92:95], v[68:71], a[216:219], v[92:95]\n\t" "v_cvt_pk_bf16_f32 v119, v110, v111\n\t" \
    "v_mfma_f32_16x16x32_bf16 v[88:91], v[72:75], a[220:223], v[88:91]\n\t" "buffer_store_dwordx4 v[116:119], %[z2], %[rs], s63 offen sc1\n\t" \
    "v_mfma_f32_16x16x32_bf16 v[92:95], v[76:79], a[220:223], v[92:95]\n\t" "s_add_u32 s63, s63, 0x20000\n\t" "v_mfma_f32_16x16x32_bf16 v[96:99], v[48:51], a[224:227], 0\n\t" \
    "v_mfma_f32_16x16x32_bf16 v[100:103], v[52:55], a[224:227], 0\n\t" "v_mfma_f32_16x16x32_bf16 v[96:99], v[56:59], a[228:231], v[96:99]\n\t" \
    "v_mfma_f32_16x16x32_bf16 v[100:103], v[60:63], a[228:231], v[100:103]\n\t" "v_mfma_f32_16x16x32_bf16 v[96:99], v[64:67], a[232:235], v[96:99]\n\t" \
    "v_cvt_pk_bf16_f32 v112, v80, v81\n\t" "v_mfma_f32_16x16x32_bf16 v[100:103], v[68:71], a[232:235], v[100:103]\n\t" "v_cvt_pk_bf16_f32 v113, v82, v83\n\t" \
    "v_mfma_f32_16x16x32_bf16 v[96:99], v[72:75], a[236:239], v[96:99]\n\t" "v_cvt_pk_bf16_f32 v114, v84, v85\n\t" \
    "v_mfma_f32_16x16x32_bf16 v[100:103], v[76:79], a[236:239], v[100:103]\n\t" "v_cvt_pk_bf16_f32 v115, v86, v87\n\t" \
    "v_mfma_f32_16x16x32_bf16 v[104:107], v[48:51], a[240:243], 0\n\t" "v_cvt_pk_bf16_f32 v116, v88, v89\n\t" \
    "v_mfma_f32_16x16x32_bf16 v[108:111], v[52:55], a[240:243], 0\n\t" "v_cvt_pk_bf16_f32 v117, v90, v91\n\t" \
    "v_mfma_f32_16x16x32_bf16 v[104:107], v[56:59], a[244:247], v[104:107]\n\t" "buffer_store_dwordx4 v[112:115], %[z1], %[rs], s63 offen sc1\n\t" \
    "v_mfma_f32_16x16x32_bf16 v[108:111], v[60:63], a[244:247], v[108:111]\n\t" "v_mfma_f32_16x16x32_bf16 v[104:107], v[64:67], a[248:251], v[104:107]\n\t" \
    "v_cvt_pk_bf16_f32 v118, v92, v93\n\t" "v_cvt_pk_bf16_f32 v119, v94, v95\n\t" "s_waitcnt vmcnt(11)\n\t" "v_readfirstlane_b32 s66, v120\n\t" \
    "v_mfma_f32_16x16x32_bf16 v[108:111], v[68:71], a[248:251], v[108:111]\n\t" "v_readfirstlane_b32 s67, v121\n\t" "v_mov_b32 v122, s60\n\t" \
    "buffer_store_dword v122, %[fo], %[rc], %[fz] offen sc1\n\t" "v_mfma_f32_16x16x32_bf16 v[104:107], v[72:75], a[252:255], v[104:107]\n\t" \
    "buffer_store_dwordx4 v[116:119], %[z2], %[rs], s63 offen sc1\n\t" "s_add_u32 s63, s63, 0x20000\n\t" "s_max_u32 s61, s61, s66\n\t" \
    "v_mfma_f32_16x16x32_bf16 v[108:111], v[76:79], a[252:255], v[108:111]\n\t" "s_max_u32 s62, s62, s67\n\t" "s_add_u32 s60, s60, 1\n\t" ZSUM "33:\n\t" "s_branch 10b\n\t" \
    "40:\n\t" "v_mfma_f32_16x16x32_bf16 v[80:83], v[16:19], a[0:3], 0\n\t" "v_mfma_f32_16x16x32_bf16 v[84:87], v[20:23], a[0:3], 0\n\t" \
    "v_mfma_f32_16x16x32_bf16 v[80:83], v[24:27], a[4:7], v[80:83]\n\t" "v_mfma_f32_16x16x32_bf16 v[84:87], v[28:31], a[4:7], v[84:87]\n\t" \
    "v_mfma_f32_16x16x32_bf16 v[80:83], v[32:35], a[8:11], v[80:83]\n\t" "v_cvt_pk_bf16_f32 v112, v96, v97\n\t" \
    "v_mfma_f32_16x16x32_bf16 v[84:87], v[36:39], a[8:11], v[84:87]\n\t" "v_cvt_pk_bf16_f32 v113, v98, v99\n\t" \
    "v_mfma_f32_16x16x32_bf16 v[80:83], v[40:43], a[12:15], v[80:83]\n\t" "v_cvt_pk_bf16_f32 v114, v100, v101\n\t" \
    "v_mfma_f32_16x16x32_bf16 v[84:87], v[44:47], a[12:15], v[84:87]\n\t" "v_cvt_pk_bf16_f32 v115, v102, v103\n\t" \
    "v_mfma_f32_16x16x32_bf16 v[88:91], v[16:19], a[16:19], 0\n\t" "v_cvt_pk_bf16_f32 v116, v104, v105\n\t" "v_mfma_f32_16x16x32_bf16 v[92:95], v[20:23], a[16:19], 0\n\t" \
    "v_cvt_pk_bf16_f32 v117, v106, v107\n\t" "v_mfma_f32_16x16x32_bf16 v[88:91], v[24:27], a[20:23], v[88:91]\n\t" \
    "buffer_store_dwordx4 v[112:115], %[z1], %[rs], s63 offen sc1\n\t" "v_mfma_f32_16x16x32_bf16 v[92:95], v[28:31], a[20:23], v[92:95]\n\t" \
    "v_mfma_f32_16x16x32_bf16 v[88:91], v[32:35], a[24:27], v[88:91]\n\t" "v_cvt_pk_bf16_f32 v118, v108, v109\n\t" \
    "v_mfma_f32_16x16x32_bf16 v[92:95], v[36:39], a[24:27], v[92:95]\n\t" "v_cvt_pk_bf16_f32 v119, v110, v111\n\t" \
    "v_mfma_f32_16x16x32_bf16 v[88:91], v[40:43], a[28:31], v[88:91]\n\t" "buffer_store_dwordx4 v[116:119], %[z2], %[rs], s63 offen sc1\n\t" "s_and_b32 s63, s60, %[rm]\n\t" \
    "v_mfma_f32_16x16x32_bf16 v[92:95], v[44:47], a[28:31], v[92:95]\n\t" "s_lshl_b32 s63, s63, 22\n\t" "s_add_u32 s63, s63, %[zb]\n\t" \
    "v_mfma_f32_16x16x32_bf16 v[96:99], v[16:19], a[32:35], 0\n\t" "buffer_load_dword v120, %[fo], %[rc], %[fv] offen sc1\n\t" \
    "v_mfma_f32_16x16x32_bf16 v[100:103], v[20:23], a[32:35], 0\n\t" "v_mfma_f32_16x16x32_bf16 v[96:99], v[24:27], a[36:39], v[96:99]\n\t" \
    "buffer_load_dword v121, %[fo], %[rc], %[fs] offen sc1\n\t" "v_mfma_f32_16x16x32_bf16 v[100:103], v[28:31], a[36:39], v[100:103]\n\t" \
    "v_mfma_f32_16x16x32_bf16 v[96:99], v[32:35], a[40:43], v[96:99]\n\t" "v_cvt_pk_bf16_f32 v112, v80, v81\n\t" \
    "v_mfma_f32_16x16x32_bf16 v[100:103], v[36:39], a[40:43], v[100:103]\n\t" "v_cvt_pk_bf16_f32 v113, v82, v83\n\t" \
    "v_mfma_f32_16x16x32_bf16 v[96:99], v[40:43], a[44:47], v[96:99]\n\t" "v_cvt_pk_bf16_f32 v114, v84, v85\n\t" \
    "v_mfma_f32_16x16x32_bf16 v[100:103], v[44:47], a[44:47], v[100:103]\n\t" "v_cvt_pk_bf16_f32 v115, v86, v87\n\t" \
    "v_mfma_f32_16x16x32_bf16 v[104:107], v[16:19], a[48:51], 0\n\t" "v_cvt_pk_bf16_f32 v116, v88, v89\n\t" "v_mfma_f32_16x16x32_bf16 v[108:111], v[20:23], a[48:51], 0\n\t" \
    "v_cvt_pk_bf16_f32 v117, v90, v91\n\t" "v_mfma_f32_16x16x32_bf16 v[104:107], v[24:27], a[52:55], v[104:107]\n\t" \
    "buffer_store_dwordx4 v[112:115], %[z1], %[rs], s63 offen sc1\n\t" "v_mfma_f32_16x16x32_bf16 v[108:111], v[28:31], a[52:55], v[108:111]\n\t" \
    "v_mfma_f32_16x16x32_bf16 v[104:107], v[32:35], a[56:59], v[104:107]\n\t" "v_cvt_pk_bf16_f32 v118, v92, v93\n\t" \
    "v_mfma_f32_16x16x32_bf16 v[108:111], v[36:39], a[56:59], v[108:111]\n\t" "v_cvt_pk_bf16_f32 v119, v94, v95\n\t" \
    "v_mfma_f32_16x16x32_bf16 v[104:107], v[40:43], a[60:63], v[104:107]\n\t" "buffer_store_dwordx4 v[116:119], %[z2], %[rs], s63 offen sc1\n\t" \
    "v_mfma_f32_16x16x32_bf16 v[108:111], v[44:47], a[60:63], v[108:111]\n\t" "s_add_u32 s63, s63, 0x20000\n\t" "v_mfma_f32_16x16x32_bf16 v[80:83], v[16:19], a[64:67], 0\n\t" \
    "s_add_u32 s65, s60, 1\n\t" "s_lshr_b32 s66, s65, 2\n\t" "v_mfma_f32_16x16x32_bf16 v[84:87], v[20:23], a[64:67], 0\n\t" "s_xor_b32 s66, s66, %[xp]\n\t" \
    "s_and_b32 s66, s66, 1\n\t" "v_mfma_f32_16x16x32_bf16 v[80:83], v[24:27], a[68:71], v[80:83]\n\t" "s_lshl_b32 s66, s66, 13\n\t" "s_lshl_b32 s64, s65, 19\n\t" \
    "v_mfma_f32_16x16x32_bf16 v[84:87], v[28:31], a[68:71], v[84:87]\n\t" "s_add_u32 s64, s64, s66\n\t" "s_add_u32 s64, s64, %[vb]\n\t" \
    "v_mfma_f32_16x16x32_bf16 v[80:83], v[32:35], a[72:75], v[80:83]\n\t" "v_cvt_pk_bf16_f32 v112, v96, v97\n\t" \
    "v_mfma_f32_16x16x32_bf16 v[84:87], v[36:39], a[72:75], v[84:87]\n\t" "v_cvt_pk_bf16_f32 v113, v98, v99\n\t" \
    "v_mfma_f32_16x16x32_bf16 v[80:83], v[40:43], a[76:79], v[80:83]\n\t" "v_cvt_pk_bf16_f32 v114, v100, v101\n\t" \
    "v_mfma_f32_16x16x32_bf16 v[84:87], v[44:47], a[76:79], v[84:87]\n\t" "v_cvt_pk_bf16_f32 v115, v102, v103\n\t" \
    "v_mfma_f32_16x16x32_bf16 v[88:91], v[16:19], a[80:83], 0\n\t" "v_cvt_pk_bf16_f32 v116, v104, v105\n\t" "v_mfma_f32_16x16x32_bf16 v[92:95], v[20:23], a[80:83], 0\n\t" \
    "v_cvt_pk_bf16_f32 v117, v106, v107\n\t" "v_mfma_f32_16x16x32_bf16 v[88:91], v[24:27], a[84:87], v[88:91]\n\t" \
    "buffer_store_dwordx4 v[112:115], %[z1], %[rs], s63 offen sc1\n\t" "v_mfma_f32_16x16x32_bf16 v[92:95], v[28:31], a[84:87], v[92:95]\n\t" \
    "v_mfma_f32_16x16x32_bf16 v[88:91], v[32:35], a[88:91], v[88:91]\n\t" "v_cvt_pk_bf16_f32 v118, v108, v109\n\t" \
    "v_mfma_f32_16x16x32_bf16 v[92:95], v[36:39], a[88:91], v[92:95]\n\t" "v_cvt_pk_bf16_f32 v119, v110, v111\n\t" \
    "v_mfma_f32_16x16x32_bf16 v[88:91], v[40:43], a[92:95], v[88:91]\n\t" "buffer_store_dwordx4 v[116:119], %[z2], %[rs], s63 offen sc1\n\t" \
    "v_mfma_f32_16x16x32_bf16 v[92:95], v[44:47], a[92:95], v[92:95]\n\t" "s_add_u32 s63, s63, 0x20000\n\t" "v_mfma_f32_16x16x32_bf16 v[96:99], v[16:19], a[96:99], 0\n\t" \
    "v_mfma_f32_16x16x32_bf16 v[100:103], v[20:23], a[96:99], 0\n\t" "v_mfma_f32_16x16x32_bf16 v[96:99], v[24:27], a[100:103], v[96:99]\n\t" \
    "v_mfma_f32_16x16x32_bf16 v[100:103], v[28:31], a[100:103], v[100:103]\n\t" "v_mfma_f32_16x16x32_bf16 v[96:99], v[32:35], a[104:107], v[96:99]\n\t" \
    "v_cvt_pk_bf16_f32 v112, v80, v81\n\t" "v_mfma_f32_16x16x32_bf16 v[100:103], v[36:39], a[104:107], v[100:103]\n\t" "v_cvt_pk_bf16_f32 v113, v82, v83\n\t" \
    "v_mfma_f32_16x16x32_bf16 v[96:99], v[40:43], a[108:111], v[96:99]\n\t" "v_cvt_pk_bf16_f32 v114, v84, v85\n\t" \
    "v_mfma_f32_16x16x32_bf16 v[100:103], v[44:47], a[108:111], v[100:103]\n\t" "v_cvt_pk_bf16_f32 v115, v86, v87\n\t" \
    "v_mfma_f32_16x16x32_bf16 v[104:107], v[16:19], a[112:115], 0\n\t" "v_cvt_pk_bf16_f32 v116, v88, v89\n\t" \
    "v_mfma_f32_16x16x32_bf16 v[108:111], v[20:23], a[112:115], 0\n\t" "v_cvt_pk_bf16_f32 v117, v90, v91\n\t" \
    "v_mfma_f32_16x16x32_bf16 v[104:107], v[24:27], a[116:119], v[104:107]\n\t" "buffer_store_dwordx4 v[112:115], %[z1], %[rs], s63 offen sc1\n\t" \
    "v_mfma_f32_16x16x32_bf16 v[108:111], v[28:31], a[116:119], v[108:111]\n\t" "v_mfma_f32_16x16x32_bf16 v[104:107], v[32:35], a[120:123], v[104:107]\n\t" \
    "v_cvt_pk_bf16_f32 v118, v92, v93\n\t" "v_mfma_f32_16x16x32_bf16 v[108:111], v[36:39], a[120:123], v[108:111]\n\t" "v_cvt_pk_bf16_f32 v119, v94, v95\n\t" \
    "v_mfma_f32_16x16x32_bf16 v[104:107], v[40:43], a[124:127], v[104:107]\n\t" "buffer_store_dwordx4 v[116:119], %[z2], %[rs], s63 offen sc1\n\t" \
    "v_mfma_f32_16x16x32_bf16 v[108:111], v[44:47], a[124:127], v[108:111]\n\t" "s_add_u32 s63, s63, 0x20000\n\t" \
    "v_mfma_f32_16x16x32_bf16 v[80:83], v[16:19], a[128:131], 0\n\t" "s_waitcnt vmcnt(6)\n\t" "v_readfirstlane_b32 s66, v120\n\t" "v_readfirstlane_b32 s67, v121\n\t" \
    "v_mfma_f32_16x16x32_bf16 v[84:87], v[20:23], a[128:131], 0\n\t" "v_mov_b32 v122, s60\n\t" "buffer_store_dword v122, %[fo], %[rc], %[fz] offen sc1\n\t" \
    "v_mfma_f32_16x16x32_bf16 v[80:83], v[24:27], a[132:135], v[80:83]\n\t" "s_max_u32 s61, s61, s66\n\t" "s_max_u32 s62, s62, s67\n\t" "s_cmp_gt_u32 s61, s65\n\t" \
    "v_mfma_f32_16x16x32_bf16 v[84:87], v[28:31], a[132:135], v[84:87]\n\t" "s_cselect_b32 s65, 1, 0\n\t" "s_cselect_b32 s64, s64, 0x7f000000\n\t" \
    "v_mfma_f32_16x16x32_bf16 v[80:83], v[32:35], a[136:139], v[80:83]\n\t" "buffer_load_dwordx4 v[48:51], %[ao], %[rs], s64 offen offset:0 sc1\n\t" \
    "v_cvt_pk_bf16_f32 v112, v96, v97\n\t" "v_mfma_f32_16x16x32_bf16 v[84:87], v[36:39], a[136:139], v[84:87]\n\t" "v_cvt_pk_bf16_f32 v113, v98, v99\n\t" \
    "v_mfma_f32_16x16x32_bf16 v[80:83], v[40:43], a[140:143], v[80:83]\n\t" "buffer_load_dwordx4 v[52:55], %[ao], %[rs], s64 offen offset:256 sc1\n\t" \
    "v_cvt_pk_bf16_f32 v114, v100, v101\n\t" "v_mfma_f32_16x16x32_bf16 v[84:87], v[44:47], a[140:143], v[84:87]\n\t" "v_cvt_pk_bf16_f32 v115, v102, v103\n\t" \
    "v_mfma_f32_16x16x32_bf16 v[88:91], v[16:19], a[144:147], 0\n\t" "buffer_load_dwordx4 v[56:59], %[ao], %[rs], s64 offen offset:2048 sc1\n\t" \
    "v_cvt_pk_bf16_f32 v116, v104, v105\n\t" "v_mfma_f32_16x16x32_bf16 v[92:95], v[20:23], a[144:147], 0\n\t" "v_cvt_pk_bf16_f32 v117, v106, v107\n\t" \
    "v_mfma_f32_16x16x32_bf16 v[88:91], v[24:27], a[148:151], v[88:91]\n\t" "buffer_load_dwordx4 v[60:63], %[ao], %[rs], s64 offen offset:2304 sc1\n\t" \
    "v_mfma_f32_16x16x32_bf16 v[92:95], v[28:31], a[148:151], v[92:95]\n\t" "buffer_store_dwordx4 v[112:115], %[z1], %[rs], s63 offen sc1\n\t" \
    "v_mfma_f32_16x16x32_bf16 v[88:91], v[32:35], a[152:155], v[88:91]\n\t" "buffer_load_dwordx4 v[64:67], %[a1], %[rs], s64 offen offset:0 sc1\n\t" \
    "v_cvt_pk_bf16_f32 v118, v108, v109\n\t" "v_mfma_f32_16x16x32_bf16 v[92:95], v[36:39], a[152:155], v[92:95]\n\t" "v_cvt_pk_bf16_f32 v119, v110, v111\n\t" \
    "v_mfma_f32_16x16x32_bf16 v[88:91], v[40:43], a[156:159], v[88:91]\n\t" "buffer_load_dwordx4 v[68:71], %[a1], %[rs], s64 offen offset:256 sc1\n\t" \
    "buffer_store_dwordx4 v[116:119], %[z2], %[rs], s63 offen sc1\n\t" "v_mfma_f32_16x16x32_bf16 v[92:95], v[44:47], a[156:159], v[92:95]\n\t" \
    "s_add_u32 s63, s63, 0x20000\n\t" "v_mfma_f32_16x16x32_bf16 v[96:99], v[16:19], a[160:163], 0\n\t" \
    "buffer_load_dwordx4 v[72:75], %[a1], %[rs], s64 offen offset:2048 sc1\n\t" "v_mfma_f32_16x16x32_bf16 v[100:103], v[20:23], a[160:163], 0\n\t" \
    "v_mfma_f32_16x16x32_bf16 v[96:99], v[24:27], a[164:167], v[96:99]\n\t" "buffer_load_dwordx4 v[76:79], %[a1], %[rs], s64 offen offset:2304 sc1\n\t" \
    "v_mfma_f32_16x16x32_bf16 v[100:103], v[28:31], a[164:167], v[100:103]\n\t" "v_mfma_f32_16x16x32_bf16 v[96:99], v[32:35], a[168:171], v[96:99]\n\t" \
    "v_cvt_pk_bf16_f32 v112, v80, v81\n\t" "v_mfma_f32_16x16x32_bf16 v[100:103], v[36:39], a[168:171], v[100:103]\n\t" "v_cvt_pk_bf16_f32 v113, v82, v83\n\t" \
    "v_mfma_f32_16x16x32_bf16 v[96:99], v[40:43], a[172:175], v[96:99]\n\t" "v_cvt_pk_bf16_f32 v114, v84, v85\n\t" \
    "v_mfma_f32_16x16x32_bf16 v[100:103], v[44:47], a[172:175], v[100:103]\n\t" "v_cvt_pk_bf16_f32 v115, v86, v87\n\t" \
    "v_mfma_f32_16x16x32_bf16 v[104:107], v[16:19], a[176:179], 0\n\t" "v_cvt_pk_bf16_f32 v116, v88, v89\n\t" \
    "v_mfma_f32_16x16x32_bf16 v[108:111], v[20:23], a[176:179], 0\n\t" "v_cvt_pk_bf16_f32 v117, v90, v91\n\t" \
    "v_mfma_f32_16x16x32_bf16 v[104:107], v[24:27], a[180:183], v[104:107]\n\t" "buffer_store_dwordx4 v[112:115], %[z1], %[rs], s63 offen sc1\n\t" \
    "v_mfma_f32_16x16x32_bf16 v[108:111], v[28:31], a[180:183], v[108:111]\n\t" "v_mfma_f32_16x16x32_bf16 v[104:107], v[32:35], a[184:187], v[104:107]\n\t" \
    "v_cvt_pk_bf16_f32 v118, v92, v93\n\t" "v_mfma_f32_16x16x32_bf16 v[108:111], v[36:39], a[184:187], v[108:111]\n\t" "v_cvt_pk_bf16_f32 v119, v94, v95\n\t" \
    "v_mfma_f32_16x16x32_bf16 v[104:107], v[40:43], a[188:191], v[104:107]\n\t" "buffer_store_dwordx4 v[116:119], %[z2], %[rs], s63 offen sc1\n\t" \
    "v_mfma_f32_16x16x32_bf16 v[108:111], v[44:47], a[188:191], v[108:111]\n\t" "s_add_u32 s63, s63, 0x20000\n\t" \
    "v_mfma_f32_16x16x32_bf16 v[80:83], v[16:19], a[192:195], 0\n\t" "v_mfma_f32_16x16x32_bf16 v[84:87], v[20:23], a[192:195], 0\n\t" \
    "v_mfma_f32_16x16x32_bf16 v[80:83], v[24:27], a[196:199], v[80:83]\n\t" "v_mfma_f32_16x16x32_bf16 v[84:87], v[28:31], a[196:199], v[84:87]\n\t" \
    "v_mfma_f32_16x16x32_bf16 v[80:83], v[32:35], a[200:203], v[80:83]\n\t" "v_cvt_pk_bf16_f32 v112, v96, v97\n\t" \
    "v_mfma_f32_16x16x32_bf16 v[84:87], v[36:39], a[200:203], v[84:87]\n\t" "v_cvt_pk_bf16_f32 v113, v98, v99\n\t" \
    "v_mfma_f32_16x16x32_bf16 v[80:83], v[40:43], a[204:207], v[80:83]\n\t" "v_cvt_pk_bf16_f32 v114, v100, v101\n\t" \
    "v_mfma_f32_16x16x32_bf16 v[84:87], v[44:47], a[204:207], v[84:87]\n\t" "v_cvt_pk_bf16_f32 v115, v102, v103\n\t" \
    "v_mfma_f32_16x16x32_bf16 v[88:91], v[16:19], a[208:211], 0\n\t" "v_cvt_pk_bf16_f32 v116, v104, v105\n\t" "v_mfma_f32_16x16x32_bf16 v[92:95], v[20:23], a[208:211], 0\n\t" \
    "v_cvt_pk_bf16_f32 v117, v106, v107\n\t" "v_mfma_f32_16x16x32_bf16 v[88:91], v[24:27], a[212:215], v[88:91]\n\t" \
    "buffer_store_dwordx4 v[112:115], %[z1], %[rs], s63 offen sc1\n\t" "v_mfma_f32_16x16x32_bf16 v[92:95], v[28:31], a[212:215], v[92:95]\n\t" \
    "v_mfma_f32_16x16x32_bf16 v[88:91], v[32:35], a[216:219], v[88:91]\n\t" "v_cvt_pk_bf16_f32 v118, v108, v109\n\t" \
    "v_mfma_f32_16x16x32_bf16 v[92:95], v[36:39], a[216:219], v[92:95]\n\t" "v_cvt_pk_bf16_f32 v119, v110, v111\n\t" \
    "v_mfma_f32_16x16x32_bf16 v[88:91], v[40:43], a[220:223], v[88:91]\n\t" "buffer_store_dwordx4 v[116:119], %[z2], %[rs], s63 offen sc1\n\t" \
    "v_mfma_f32_16x16x32_bf16 v[92:95], v[44:47], a[220:223], v[92:95]\n\t" "s_add_u32 s63, s63, 0x20000\n\t" "v_mfma_f32_16x16x32_bf16 v[96:99], v[16:19], a[224:227], 0\n\t" \
    "v_mfma_f32_16x16x32_bf16 v[100:103], v[20:23], a[224:227], 0\n\t" "v_mfma_f32_16x16x32_bf16 v[96:99], v[24:27], a[228:231], v[96:99]\n\t" \
    "v_mfma_f32_16x16x32_bf16 v[100:103], v[28:31], a[228:231], v[100:103]\n\t" "v_mfma_f32_16x16x32_bf16 v[96:99], v[32:35], a[232:235], v[96:99]\n\t" \
    "v_cvt_pk_bf16_f32 v112, v80, v81\n\t" "v_mfma_f32_16x16x32_bf16 v[100:103], v[36:39], a[232:235], v[100:103]\n\t" "v_cvt_pk_bf16_f32 v113, v82, v83\n\t" \
    "v_mfma_f32_16x16x32_bf16 v[96:99], v[40:43], a[236:239], v[96:99]\n\t" "v_cvt_pk_bf16_f32 v114, v84, v85\n\t" \
    "v_mfma_f32_16x16x32_bf16 v[100:103], v[44:47], a[236:239], v[100:103]\n\t" "v_cvt_pk_bf16_f32 v115, v86, v87\n\t" \
    "v_mfma_f32_16x16x32_bf16 v[104:107], v[16:19], a[240:243], 0\n\t" "v_cvt_pk_bf16_f32 v116, v88, v89\n\t" \
    "v_mfma_f32_16x16x32_bf16 v[108:111], v[20:23], a[240:243], 0\n\t" "v_cvt_pk_bf16_f32 v117, v90, v91\n\t" \
    "v_mfma_f32_16x16x32_bf16 v[104:107], v[24:27], a[244:247], v[104:107]\n\t" "buffer_store_dwordx4 v[112:115], %[z1], %[rs], s63 offen sc1\n\t" \
    "v_mfma_f32_16x16x32_bf16 v[108:111], v[28:31], a[244:247], v[108:111]\n\t" "v_mfma_f32_16x16x32_bf16 v[104:107], v[32:35], a[248:251], v[104:107]\n\t" \
    "v_cvt_pk_bf16_f32 v118, v92, v93\n\t" "v_mfma_f32_16x16x32_bf16 v[108:111], v[36:39], a[248:251], v[108:111]\n\t" "v_cvt_pk_bf16_f32 v119, v94, v95\n\t" \
    "v_mfma_f32_16x16x32_bf16 v[104:107], v[40:43], a[252:255], v[104:107]\n\t" "buffer_store_dwordx4 v[116:119], %[z2], %[rs], s63 offen sc1\n\t" \
    "s_add_u32 s63, s63, 0x20000\n\t" "s_add_u32 s60, s60, 1\n\t" "v_mfma_f32_16x16x32_bf16 v[108:111], v[44:47], a[252:255], v[108:111]\n\t" "s_cmp_eq_u32 s65, 0\n\t" \
    "s_waitcnt vmcnt(6)\n\t" ZSUM "s_cbranch_scc0 23b\n\t" "s_branch 20f\n\t" "41:\n\t" "v_mfma_f32_16x16x32_bf16 v[80:83], v[48:51], a[0:3], 0\n\t" \
    "v_mfma_f32_16x16x32_bf16 v[84:87], v[52:55], a[0:3], 0\n\t" "v_mfma_f32_16x16x32_bf16 v[80:83], v[56:59], a[4:7], v[80:83]\n\t" \
    "v_mfma_f32_16x16x32_bf16 v[84:87], v[60:63], a[4:7], v[84:87]\n\t" "v_mfma_f32_16x16x32_bf16 v[80:83], v[64:67], a[8:11], v[80:83]\n\t" \
    "v_cvt_pk_bf16_f32 v112, v96, v97\n\t" "v_mfma_f32_16x16x32_bf16 v[84:87], v[68:71], a[8:11], v[84:87]\n\t" "v_cvt_pk_bf16_f32 v113, v98, v99\n\t" \
    "v_mfma_f32_16x16x32_bf16 v[80:83], v[72:75], a[12:15], v[80:83]\n\t" "v_cvt_pk_bf16_f32 v114, v100, v101\n\t" \
    "v_mfma_f32_16x16x32_bf16 v[84:87], v[76:79], a[12:15], v[84:87]\n\t" "v_cvt_pk_bf16_f32 v115, v102, v103\n\t" \
    "v_mfma_f32_16x16x32_bf16 v[88:91], v[48:51], a[16:19], 0\n\t" "v_cvt_pk_bf16_f32 v116, v104, v105\n\t" "v_mfma_f32_16x16x32_bf16 v[92:95], v[52:55], a[16:19], 0\n\t" \
    "v_cvt_pk_bf16_f32 v117, v106, v107\n\t" "v_mfma_f32_16x16x32_bf16 v[88:91], v[56:59], a[20:23], v[88:91]\n\t" \
    "buffer_store_dwordx4 v[112:115], %[z1], %[rs], s63 offen sc1\n\t" "v_mfma_f32_16x16x32_bf16 v[92:95], v[60:63], a[20:23], v[92:95]\n\t" \
    "v_mfma_f32_16x16x32_bf16 v[88:91], v[64:67], a[24:27], v[88:91]\n\t" "v_cvt_pk_bf16_f32 v118, v108, v109\n\t" \
    "v_mfma_f32_16x16x32_bf16 v[92:95], v[68:71], a[24:27], v[92:95]\n\t" "v_cvt_pk_bf16_f32 v119, v110, v111\n\t" \
    "v_mfma_f32_16x16x32_bf16 v[88:91], v[72:75], a[28:31], v[88:91]\n\t" "buffer_store_dwordx4 v[116:119], %[z2], %[rs], s63 offen sc1\n\t" "s_and_b32 s63, s60, %[rm]\n\t" \
    "v_mfma_f32_16x16x32_bf16 v[92:95], v[76:79], a[28:31], v[92:95]\n\t" "s_lshl_b32 s63, s63, 22\n\t" "s_add_u32 s63, s63, %[zb]\n\t" \
    "v_mfma_f32_16x16x32_bf16 v[96:99], v[48:51], a[32:35], 0\n\t" "buffer_load_dword v120, %[fo], %[rc], %[fv] offen sc1\n\t" \
    "v_mfma_f32_16x16x32_bf16 v[100:103], v[52:55], a[32:35], 0\n\t" "v_mfma_f32_16x16x32_bf16 v[96:99], v[56:59], a[36:39], v[96:99]\n\t" \
    "buffer_load_dword v121, %[fo], %[rc], %[fs] offen sc1\n\t" "v_mfma_f32_16x16x32_bf16 v[100:103], v[60:63], a[36:39], v[100:103]\n\t" \
    "v_mfma_f32_16x16x32_bf16 v[96:99], v[64:67], a[40:43], v[96:99]\n\t" "v_cvt_pk_bf16_f32 v112, v80, v81\n\t" \
    "v_mfma_f32_16x16x32_bf16 v[100:103], v[68:71], a[40:43], v[100:103]\n\t" "v_cvt_pk_bf16_f32 v113, v82, v83\n\t" \
    "v_mfma_f32_16x16x32_bf16 v[96:99], v[72:75], a[44:47], v[96:99]\n\t" "v_cvt_pk_bf16_f32 v114, v84, v85\n\t" \
    "v_mfma_f32_16x16x32_bf16 v[100:103], v[76:79], a[44:47], v[100:103]\n\t" "v_cvt_pk_bf16_f32 v115, v86, v87\n\t" \
    "v_mfma_f32_16x16x32_bf16 v[104:107], v[48:51], a[48:51], 0\n\t" "v_cvt_pk_bf16_f32 v116, v88, v89\n\t" "v_mfma_f32_16x16x32_bf16 v[108:111], v[52:55], a[48:51], 0\n\t" \
    "v_cvt_pk_bf16_f32 v117, v90, v91\n\t" "v_mfma_f32_16x16x32_bf16 v[104:107], v[56:59], a[52:55], v[104:107]\n\t" \
    "buffer_store_dwordx4 v[112:115], %[z1], %[rs], s63 offen sc1\n\t" "v_mfma_f32_16x16x32_bf16 v[108:111], v[60:63], a[52:55], v[108:111]\n\t" \
    "v_mfma_f32_16x16x32_bf16 v[104:107], v[64:67], a[56:59], v[104:107]\n\t" "v_cvt_pk_bf16_f32 v118, v92, v93\n\t" \
    "v_mfma_f32_16x16x32_bf16 v[108:111], v[68:71], a[56:59], v[108:111]\n\t" "v_cvt_pk_bf16_f32 v119, v94, v95\n\t" \
    "v_mfma_f32_16x16x32_bf16 v[104:107], v[72:75], a[60:63], v[104:107]\n\t" "buffer_store_dwordx4 v[116:119], %[z2], %[rs], s63 offen sc1\n\t" \
    "v_mfma_f32_16x16x32_bf16 v[108:111], v[76:79], a[60:63], v[108:111]\n\t" "s_add_u32 s63, s63, 0x20000\n\t" "v_mfma_f32_16x16x32_bf16 v[80:83], v[48:51], a[64:67], 0\n\t" \
    "s_add_u32 s65, s60, 1\n\t" "s_lshr_b32 s66, s65, 2\n\t" "v_mfma_f32_16x16x32_bf16 v[84:87], v[52:55], a[64:67], 0\n\t" "s_xor_b32 s66, s66, %[xp]\n\t" \
    "s_and_b32 s66, s66, 1\n\t" "v_mfma_f32_16x16x32_bf16 v[80:83], v[56:59], a[68:71], v[80:83]\n\t" "s_lshl_b32 s66, s66, 13\n\t" "s_lshl_b32 s64, s65, 19\n\t" \
    "v_mfma_f32_16x16x32_bf16 v[84:87], v[60:63], a[68:71], v[84:87]\n\t" "s_add_u32 s64, s64, s66\n\t" "s_add_u32 s64, s64, %[vb]\n\t" \
    "v_mfma_f32_16x16x32_bf16 v[80:83], v[64:67], a[72:75], v[80:83]\n\t" "v_cvt_pk_bf16_f32 v112, v96, v97\n\t" \
    "v_mfma_f32_16x16x32_bf16 v[84:87], v[68:71], a[72:75], v[84:87]\n\t" "v_cvt_pk_bf16_f32 v113, v98, v99\n\t" \
    "v_mfma_f32_16x16x32_bf16 v[80:83], v[72:75], a[76:79], v[80:83]\n\t" "v_cvt_pk_bf16_f32 v114, v100, v101\n\t" \
    "v_mfma_f32_16x16x32_bf16 v[84:87], v[76:79], a[76:79], v[84:87]\n\t" "v_cvt_pk_bf16_f32 v115, v102, v103\n\t" \
    "v_mfma_f32_16x16x32_bf16 v[88:91], v[48:51], a[80:83], 0\n\t" "v_cvt_pk_bf16_f32 v116, v104, v105\n\t" "v_mfma_f32_16x16x32_bf16 v[92:95], v[52:55], a[80:83], 0\n\t" \
    "v_cvt_pk_bf16_f32 v117, v106, v107\n\t" "v_mfma_f32_16x16x32_bf16 v[88:91], v[56:59], a[84:87], v[88:91]\n\t" \
    "buffer_store_dwordx4 v[112:115], %[z1], %[rs], s63 offen sc1\n\t" "v_mfma_f32_16x16x32_bf16 v[92:95], v[60:63], a[84:87], v[92:95]\n\t" \
    "v_mfma_f32_16x16x32_bf16 v[88:91], v[64:67], a[88:91], v[88:91]\n\t" "v_cvt_pk_bf16_f32 v118, v108, v109\n\t" \
    "v_mfma_f32_16x16x32_bf16 v[92:95], v[68:71], a[88:91], v[92:95]\n\t" "v_cvt_pk_bf16_f32 v119, v110, v111\n\t" \
    "v_mfma_f32_16x16x32_bf16 v[88:91], v[72:75], a[92:95], v[88:91]\n\t" "buffer_store_dwordx4 v[116:119], %[z2], %[rs], s63 offen sc1\n\t" \
    "v_mfma_f32_16x16x32_bf16 v[92:95], v[76:79], a[92:95], v[92:95]\n\t" "s_add_u32 s63, s63, 0x20000\n\t" "v_mfma_f32_16x16x32_bf16 v[96:99], v[48:51], a[96:99], 0\n\t" \
    "v_mfma_f32_16x16x32_bf16 v[100:103], v[52:55], a[96:99], 0\n\t" "v_mfma_f32_16x16x32_bf16 v[96:99], v[56:59], a[100:103], v[96:99]\n\t" \
    "v_mfma_f32_16x16x32_bf16 v[100:103], v[60:63], a[100:103], v[100:103]\n\t" "v_mfma_f32_16x16x32_bf16 v[96:99], v[64:67], a[104:107], v[96:99]\n\t" \
    "v_cvt_pk_bf16_f32 v112, v80, v81\n\t" "v_mfma_f32_16x16x32_bf16 v[100:103], v[68:71], a[104:107], v[100:103]\n\t" "v_cvt_pk_bf16_f32 v113, v82, v83\n\t" \
    "v_mfma_f32_16x16x32_bf16 v[96:99], v[72:75], a[108:111], v[96:99]\n\t" "v_cvt_pk_bf16_f32 v114, v84, v85\n\t" \
    "v_mfma_f32_16x16x32_bf16 v[100:103], v[76:79], a[108:111], v[100:103]\n\t" "v_cvt_pk_bf16_f32 v115, v86, v87\n\t" \
    "v_mfma_f32_16x16x32_bf16 v[104:107], v[48:51], a[112:115], 0\n\t" "v_cvt_pk_bf16_f32 v116, v88, v89\n\t" \
    "v_mfma_f32_16x16x32_bf16 v[108:111], v[52:55], a[112:115], 0\n\t" "v_cvt_pk_bf16_f32 v117, v90, v91\n\t" \
    "v_mfma_f32_16x16x32_bf16 v[104:107], v[56:59], a[116:119], v[104:107]\n\t" "buffer_store_dwordx4 v[112:115], %[z1], %[rs], s63 offen sc1\n\t" \
    "v_mfma_f32_16x16x32_bf16 v[108:111], v[60:63], a[116:119], v[108:111]\n\t" "v_mfma_f32_16x16x32_bf16 v[104:107], v[64:67], a[120:123], v[104:107]\n\t" \
    "v_cvt_pk_bf16_f32 v118, v92, v93\n\t" "v_mfma_f32_16x16x32_bf16 v[108:111], v[68:71], a[120:123], v[108:111]\n\t" "v_cvt_pk_bf16_f32 v119, v94, v95\n\t" \
    "v_mfma_f32_16x16x32_bf16 v[104:107], v[72:75], a[124:127], v[104:107]\n\t" "buffer_store_dwordx4 v[116:119], %[z2], %[rs], s63 offen sc1\n\t" \
    "v_mfma_f32_16x16x32_bf16 v[108:111], v[76:79], a[124:127], v[108:111]\n\t" "s_add_u32 s63, s63, 0x20000\n\t" \
    "v_mfma_f32_16x16x32_bf16 v[80:83], v[48:51], a[128:131], 0\n\t" "s_waitcnt vmcnt(6)\n\t" "v_readfirstlane_b32 s66, v120\n\t" "v_readfirstlane_b32 s67, v121\n\t" \
    "v_mfma_f32_16x16x32_bf16 v[84:87], v[52:55], a[128:131], 0\n\t" "v_mov_b32 v122, s60\n\t" "buffer_store_dword v122, %[fo], %[rc], %[fz] offen sc1\n\t" \
    "v_mfma_f32_16x16x32_bf16 v[80:83], v[56:59], a[132:135], v[80:83]\n\t" "s_max_u32 s61, s61, s66\n\t" "s_max_u32 s62, s62, s67\n\t" "s_cmp_gt_u32 s61, s65\n\t" \
    "v_mfma_f32_16x16x32_bf16 v[84:87], v[60:63], a[132:135], v[84:87]\n\t" "s_cselect_b32 s65, 1, 0\n\t" "s_cselect_b32 s64, s64, 0x7f000000\n\t" \
    "v_mfma_f32_16x16x32_bf16 v[80:83], v[64:67], a[136:139], v[80:83]\n\t" "buffer_load_dwordx4 v[16:19], %[ao], %[rs], s64 offen offset:0 sc1\n\t" \
    "v_cvt_pk_bf16_f32 v112, v96, v97\n\t" "v_mfma_f32_16x16x32_bf16 v[84:87], v[68:71], a[136:139], v[84:87]\n\t" "v_cvt_pk_bf16_f32 v113, v98, v99\n\t" \
    "v_mfma_f32_16x16x32_bf16 v[80:83], v[72:75], a[140:143], v[80:83]\n\t" "buffer_load_dwordx4 v[20:23], %[ao], %[rs], s64 offen offset:256 sc1\n\t" \
    "v_cvt_pk_bf16_f32 v114, v100, v101\n\t" "v_mfma_f32_16x16x32_bf16 v[84:87], v[76:79], a[140:143], v[84:87]\n\t" "v_cvt_pk_bf16_f32 v115, v102, v103\n\t" \
    "v_mfma_f32_16x16x32_bf16 v[88:91], v[48:51], a[144:147], 0\n\t" "buffer_load_dwordx4 v[24:27], %[ao], %[rs], s64 offen offset:2048 sc1\n\t" \
    "v_cvt_pk_bf16_f32 v116, v104, v105\n\t" "v_mfma_f32_16x16x32_bf16 v[92:95], v[52:55], a[144:147], 0\n\t" "v_cvt_pk_bf16_f32 v117, v106, v107\n\t" \
    "v_mfma_f32_16x16x32_bf16 v[88:91], v[56:59], a[148:151], v[88:91]\n\t" "buffer_load_dwordx4 v[28:31], %[ao], %[rs], s64 offen offset:2304 sc1\n\t" \
    "v_mfma_f32_16x16x32_bf16 v[92:95], v[60:63], a[148:151], v[92:95]\n\t" "buffer_store_dwordx4 v[112:115], %[z1], %[rs], s63 offen sc1\n\t" \
    "v_mfma_f32_16x16x32_bf16 v[88:91], v[64:67], a[152:155], v[88:91]\n\t" "buffer_load_dwordx4 v[32:35], %[a1], %[rs], s64 offen offset:0 sc1\n\t" \
    "v_cvt_pk_bf16_f32 v118, v108, v109\n\t" "v_mfma_f32_16x16x32_bf16 v[92:95], v[68:71], a[152:155], v[92:95]\n\t" "v_cvt_pk_bf16_f32 v119, v110, v111\n\t" \
    "v_mfma_f32_16x16x32_bf16 v[88:91], v[72:75], a[156:159], v[88:91]\n\t" "buffer_load_dwordx4 v[36:39], %[a1], %[rs], s64 offen offset:256 sc1\n\t" \
    "buffer_store_dwordx4 v[116:119], %[z2], %[rs], s63 offen sc1\n\t" "v_mfma_f32_16x16x32_bf16 v[92:95], v[76:79], a[156:159], v[92:95]\n\t" \
    "s_add_u32 s63, s63, 0x20000\n\t" "v_mfma_f32_16x16x32_bf16 v[96:99], v[48:51], a[160:163], 0\n\t" \
    "buffer_load_dwordx4 v[40:43], %[a1], %[rs], s64 offen offset:2048 sc1\n\t" "v_mfma_f32_16x16x32_bf16 v[100:103], v[52:55], a[160:163], 0\n\t" \
    "v_mfma_f32_16x16x32_bf16 v[96:99], v[56:59], a[164:167], v[96:99]\n\t" "buffer_load_dwordx4 v[44:47], %[a1], %[rs], s64 offen offset:2304 sc1\n\t" \
    "v_mfma_f32_16x16x32_bf16 v[100:103], v[60:63], a[164:167], v[100:103]\n\t" "v_mfma_f32_16x16x32_bf16 v[96:99], v[64:67], a[168:171], v[96:99]\n\t" \
    "v_cvt_pk_bf16_f32 v112, v80, v81\n\t" "v_mfma_f32_16x16x32_bf16 v[100:103], v[68:71], a[168:171], v[100:103]\n\t" "v_cvt_pk_bf16_f32 v113, v82, v83\n\t" \
    "v_mfma_f32_16x16x32_bf16 v[96:99], v[72:75], a[172:175], v[96:99]\n\t" "v_cvt_pk_bf16_f32 v114, v84, v85\n\t" \
    "v_mfma_f32_16x16x32_bf16 v[100:103], v[76:79], a[172:175], v[100:103]\n\t" "v_cvt_pk_bf16_f32 v115, v86, v87\n\t" \
    "v_mfma_f32_16x16x32_bf16 v[104:107], v[48:51], a[176:179], 0\n\t" "v_cvt_pk_bf16_f32 v116, v88, v89\n\t" \
    "v_mfma_f32_16x16x32_bf16 v[108:111], v[52:55], a[176:179], 0\n\t" "v_cvt_pk_bf16_f32 v117, v90, v91\n\t" \
    "v_mfma_f32_16x16x32_bf16 v[104:107], v[56:59], a[180:183], v[104:107]\n\t" "buffer_store_dwordx4 v[112:115], %[z1], %[rs], s63 offen sc1\n\t" \
    "v_mfma_f32_16x16x32_bf16 v[108:111], v[60:63], a[180:183], v[108:111]\n\t" "v_mfma_f32_16x16x32_bf16 v[104:107], v[64:67], a[184:187], v[104:107]\n\t" \
    "v_cvt_pk_bf16_f32 v118, v92, v93\n\t" "v_mfma_f32_16x16x32_bf16 v[108:111], v[68:71], a[184:187], v[108:111]\n\t" "v_cvt_pk_bf16_f32 v119, v94, v95\n\t" \
    "v_mfma_f32_16x16x32_bf16 v[104:107], v[72:75], a[188:191], v[104:107]\n\t" "buffer_store_dwordx4 v[116:119], %[z2], %[rs], s63 offen sc1\n\t" \
    "v_mfma_f32_16x16x32_bf16 v[108:111], v[76:79], a[188:191], v[108:111]\n\t" "s_add_u32 s63, s63, 0x20000\n\t" \
    "v_mfma_f32_16x16x32_bf16 v[80:83], v[48:51], a[192:195], 0\n\t" "v_mfma_f32_16x16x32_bf16 v[84:87], v[52:55], a[192:195], 0\n\t" \
    "v_mfma_f32_16x16x32_bf16 v[80:83], v[56:59], a[196:199], v[80:83]\n\t" "v_mfma_f32_16x16x32_bf16 v[84:87], v[60:63], a[196:199], v[84:87]\n\t" \
    "v_mfma_f32_16x16x32_bf16 v[80:83], v[64:67], a[200:203], v[80:83]\n\t" "v_cvt_pk_bf16_f32 v112, v96, v97\n\t" \
    "v_mfma_f32_16x16x32_bf16 v[84:87], v[68:71], a[200:203], v[84:87]\n\t" "v_cvt_pk_bf16_f32 v113, v98, v99\n\t" \
    "v_mfma_f32_16x16x32_bf16 v[80:83], v[72:75], a[204:207], v[80:83]\n\t" "v_cvt_pk_bf16_f32 v114, v100, v101\n\t" \
    "v_mfma_f32_16x16x32_bf16 v[84:87], v[76:79], a[204:207], v[84:87]\n\t" "v_cvt_pk_bf16_f32 v115, v102, v103\n\t" \
    "v_mfma_f32_16x16x32_bf16 v[88:91], v[48:51], a[208:211], 0\n\t" "v_cvt_pk_bf16_f32 v116, v104, v105\n\t" "v_mfma_f32_16x16x32_bf16 v[92:95], v[52:55], a[208:211], 0\n\t" \
    "v_cvt_pk_bf16_f32 v117, v106, v107\n\t" "v_mfma_f32_16x16x32_bf16 v[88:91], v[56:59], a[212:215], v[88:91]\n\t" \
    "buffer_store_dwordx4 v[112:115], %[z1], %[rs], s63 offen sc1\n\t" "v_mfma_f32_16x16x32_bf16 v[92:95], v[60:63], a[212:215], v[92:95]\n\t" \
    "v_mfma_f32_16x16x32_bf16 v[88:91], v[64:67], a[216:219], v[88:91]\n\t" "v_cvt_pk_bf16_f32 v118, v108, v109\n\t" \
    "v_mfma_f32_16x16x32_bf16 v[92:95], v[68:71], a[216:219], v[92:95]\n\t" "v_cvt_pk_bf16_f32 v119, v110, v111\n\t" \
    "v_mfma_f32_16x16x32_bf16 v[88:91], v[72:75], a[220:223], v[88:91]\n\t" "buffer_store_dwordx4 v[116:119], %[z2], %[rs], s63 offen sc1\n\t" \
    "v_mfma_f32_16x16x32_bf16 v[92:95], v[76:79], a[220:223], v[92:95]\n\t" "s_add_u32 s63, s63, 0x20000\n\t" "v_mfma_f32_16x16x32_bf16 v[96:99], v[48:51], a[224:227], 0\n\t" \
    "v_mfma_f32_16x16x32_bf16 v[100:103], v[52:55], a[224:227], 0\n\t" "v_mfma_f32_16x16x32_bf16 v[96:99], v[56:59], a[228:231], v[96:99]\n\t" \
    "v_mfma_f32_16x16x32_bf16 v[100:103], v[60:63], a[228:231], v[100:103]\n\t" "v_mfma_f32_16x16x32_bf16 v[96:99], v[64:67], a[232:235], v[96:99]\n\t" \
    "v_cvt_pk_bf16_f32 v112, v80, v81\n\t" "v_mfma_f32_16x16x32_bf16 v[100:103], v[68:71], a[232:235], v[100:103]\n\t" "v_cvt_pk_bf16_f32 v113, v82, v83\n\t" \
    "v_mfma_f32_16x16x32_bf16 v[96:99], v[72:75], a[236:239], v[96:99]\n\t" "v_cvt_pk_bf16_f32 v114, v84, v85\n\t" \
    "v_mfma_f32_16x16x32_bf16 v[100:103], v[76:79], a[236:239], v[100:103]\n\t" "v_cvt_pk_bf16_f32 v115, v86, v87\n\t" \
    "v_mfma_f32_16x16x32_bf16 v[104:107], v[48:51], a[240:243], 0\n\t" "v_cvt_pk_bf16_f32 v116, v88, v89\n\t" \
    "v_mfma_f32_16x16x32_bf16 v[108:111], v[52:55], a[240:243], 0\n\t" "v_cvt_pk_bf16_f32 v117, v90, v91\n\t" \
    "v_mfma_f32_16x16x32_bf16 v[104:107], v[56:59], a[244:247], v[104:107]\n\t" "buffer_store_dwordx4 v[112:115], %[z1], %[rs], s63 offen sc1\n\t" \
    "v_mfma_f32_16x16x32_bf16 v[108:111], v[60:63], a[244:247], v[108:111]\n\t" "v_mfma_f32_16x16x32_bf16 v[104:107], v[64:67], a[248:251], v[104:107]\n\t" \
    "v_cvt_pk_bf16_f32 v118, v92, v93\n\t" "v_mfma_f32_16x16x32_bf16 v[108:111], v[68:71], a[248:251], v[108:111]\n\t" "v_cvt_pk_bf16_f32 v119, v94, v95\n\t" \
    "v_mfma_f32_16x16x32_bf16 v[104:107], v[72:75], a[252:255], v[104:107]\n\t" "buffer_store_dwordx4 v[116:119], %[z2], %[rs], s63 offen sc1\n\t" \
    "s_add_u32 s63, s63, 0x20000\n\t" "s_add_u32 s60, s60, 1\n\t" "v_mfma_f32_16x16x32_bf16 v[108:111], v[76:79], a[252:255], v[108:111]\n\t" "s_cmp_eq_u32 s65, 0\n\t" \
    "s_waitcnt vmcnt(6)\n\t" ZSUM "s_cbranch_scc0 33b\n\t" "s_branch 30f\n\t" "20:\n\t" "s_nop 7\n\t" "v_cvt_pk_bf16_f32 v112, v96, v97\n\t" \
    "v_cvt_pk_bf16_f32 v113, v98, v99\n\t" "v_cvt_pk_bf16_f32 v114, v100, v101\n\t" "v_cvt_pk_bf16_f32 v115, v102, v103\n\t" "v_cvt_pk_bf16_f32 v116, v104, v105\n\t" \
    "v_cvt_pk_bf16_f32 v117, v106, v107\n\t" "v_cvt_pk_bf16_f32 v118, v108, v109\n\t" "v_cvt_pk_bf16_f32 v119, v110, v111\n\t" \
    "buffer_store_dwordx4 v[112:115], %[z1], %[rs], s63 offen sc1\n\t" "buffer_store_dwordx4 v[116:119], %[z2], %[rs], s63 offen sc1\n\t" \
    "buffer_load_dword v120, %[fo], %[rc], %[fv] offen sc1\n\t" "s_waitcnt vmcnt(0)\n\t" "v_readfirstlane_b32 s61, v120\n\t" "s_lshr_b32 s66, s60, 2\n\t" \
    "s_xor_b32 s66, s66, %[xp]\n\t" "s_and_b32 s66, s66, 1\n\t" "s_lshl_b32 s66, s66, 13\n\t" "s_lshl_b32 s64, s60, 19\n\t" "s_add_u32 s64, s64, s66\n\t" \
    "s_add_u32 s64, s64, %[vb]\n\t" "s_cmp_gt_u32 s61, s60\n\t" "s_cbranch_scc0 24f\n\t" "s_cmp_eq_u32 s60, 64\n\t" "s_cbranch_scc1 24f\n\t" \
    "buffer_load_dwordx4 v[48:51], %[ao], %[rs], s64 offen offset:0 sc1\n\t" "buffer_load_dwordx4 v[52:55], %[ao], %[rs], s64 offen offset:256 sc1\n\t" \
    "buffer_load_dwordx4 v[56:59], %[ao], %[rs], s64 offen offset:2048 sc1\n\t" "buffer_load_dwordx4 v[60:63], %[ao], %[rs], s64 offen offset:2304 sc1\n\t" \
    "buffer_load_dwordx4 v[64:67], %[a1], %[rs], s64 offen offset:0 sc1\n\t" "buffer_load_dwordx4 v[68:71], %[a1], %[rs], s64 offen offset:256 sc1\n\t" \
    "buffer_load_dwordx4 v[72:75], %[a1], %[rs], s64 offen offset:2048 sc1\n\t" "buffer_load_dwordx4 v[76:79], %[a1], %[rs], s64 offen offset:2304 sc1\n\t" \
    "v_mov_b32 v122, s60\n\t" "buffer_store_dword v122, %[fo], %[rc], %[fz] offen sc1\n\t" "s_waitcnt vmcnt(1)\n\t" "s_branch 23b\n\t" "24:\n\t" "v_mov_b32 v122, s60\n\t" \
    "buffer_store_dword v122, %[fo], %[rc], %[fz] offen sc1\n\t" "s_cmp_eq_u32 s60, 64\n\t" "s_cbranch_scc1 90f\n\t" "s_add_u32 s67, s60, 1\n\t" "s_cmp_ge_u32 s61, s67\n\t" \
    "s_cbranch_scc1 22f\n\t" "21:\n\t" "buffer_load_dword v120, %[fo], %[rc], %[fv] offen sc1\n\t" "buffer_load_dword v121, %[fo], %[rc], %[vd] offen sc1\n\t" \
    "s_waitcnt vmcnt(0)\n\t" "v_readfirstlane_b32 s61, v120\n\t" "v_readfirstlane_b32 s66, v121\n\t" "s_nop 3\n\t" "s_cmp_ge_u32 s61, s67\n\t" "s_cbranch_scc1 22f\n\t" \
    "s_bitcmp1_b32 s66, 1\n\t" "s_cbranch_scc1 90f\n\t" "s_cmp_eq_u32 %[bd], 0\n\t" "s_cbranch_scc1 22f\n\t" "s_sub_u32 %[bd], %[bd], 1\n\t" "s_sleep 2\n\t" \
    "s_branch 21b\n\t" "22:\n\t" "s_lshr_b32 s66, s60, 2\n\t" "s_xor_b32 s66, s66, %[xp]\n\t" "s_and_b32 s66, s66, 1\n\t" "s_lshl_b32 s66, s66, 13\n\t" \
    "s_lshl_b32 s64, s60, 19\n\t" "s_add_u32 s64, s64, s66\n\t" "s_add_u32 s64, s64, %[vb]\n\t" "buffer_load_dwordx4 v[48:51], %[ao], %[rs], s64 offen offset:0 sc1\n\t" \
    "buffer_load_dwordx4 v[52:55], %[ao], %[rs], s64 offen offset:256 sc1\n\t" "buffer_load_dwordx4 v[56:59], %[ao], %[rs], s64 offen offset:2048 sc1\n\t" \
    "buffer_load_dwordx4 v[60:63], %[ao], %[rs], s64 offen offset:2304 sc1\n\t" "buffer_load_dwordx4 v[64:67], %[a1], %[rs], s64 offen offset:0 sc1\n\t" \
    "buffer_load_dwordx4 v[68:71], %[a1], %[rs], s64 offen offset:256 sc1\n\t" "buffer_load_dwordx4 v[72:75], %[a1], %[rs], s64 offen offset:2048 sc1\n\t" \
    "buffer_load_dwordx4 v[76:79], %[a1], %[rs], s64 offen offset:2304 sc1\n\t" "s_waitcnt vmcnt(0)\n\t" "s_branch 23b\n\t" "60:\n\t" "s_sub_u32 s67, s60, %[rg]\n\t" \
    "s_cmp_ge_u32 s62, s67\n\t" "s_cbranch_scc1 62f\n\t" "61:\n\t" "buffer_load_dword v120, %[fo], %[rc], %[fs] offen sc1\n\t" \
    "buffer_load_dword v121, %[fo], %[rc], %[vd] offen sc1\n\t" "s_waitcnt vmcnt(0)\n\t" "v_readfirstlane_b32 s62, v120\n\t" "v_readfirstlane_b32 s66, v121\n\t" "s_nop 3\n\t" \
    "s_cmp_ge_u32 s62, s67\n\t" "s_cbranch_scc1 62f\n\t" "s_bitcmp1_b32 s66, 1\n\t" "s_cbranch_scc1 90f\n\t" "s_cmp_eq_u32 %[bd], 0\n\t" "s_cbranch_scc1 62f\n\t" \
    "s_sub_u32 %[bd], %[bd], 1\n\t" "s_sleep 2\n\t" "s_branch 61b\n\t" "62:\n\t" "s_branch 63b\n\t" "30:\n\t" "s_nop 7\n\t" "v_cvt_pk_bf16_f32 v112, v96, v97\n\t" \
    "v_cvt_pk_bf16_f32 v113, v98, v99\n\t" "v_cvt_pk_bf16_f32 v114, v100, v101\n\t" "v_cvt_pk_bf16_f32 v115, v102, v103\n\t" "v_cvt_pk_bf16_f32 v116, v104, v105\n\t" \
    "v_cvt_pk_bf16_f32 v117, v106, v107\n\t" "v_cvt_pk_bf16_f32 v118, v108, v109\n\t" "v_cvt_pk_bf16_f32 v119, v110, v111\n\t" \
    "buffer_store_dwordx4 v[112:115], %[z1], %[rs], s63 offen sc1\n\t" "buffer_store_dwordx4 v[116:119], %[z2], %[rs], s63 offen sc1\n\t" \
    "buffer_load_dword v120, %[fo], %[rc], %[fv] offen sc1\n\t" "s_waitcnt vmcnt(0)\n\t" "v_readfirstlane_b32 s61, v120\n\t" "s_lshr_b32 s66, s60, 2\n\t" \
    "s_xor_b32 s66, s66, %[xp]\n\t" "s_and_b32 s66, s66, 1\n\t" "s_lshl_b32 s66, s66, 13\n\t" "s_lshl_b32 s64, s60, 19\n\t" "s_add_u32 s64, s64, s66\n\t" \
    "s_add_u32 s64, s64, %[vb]\n\t" "s_cmp_gt_u32 s61, s60\n\t" "s_cbranch_scc0 34f\n\t" "s_cmp_eq_u32 s60, 64\n\t" "s_cbranch_scc1 34f\n\t" \
    "buffer_load_dwordx4 v[16:19], %[ao], %[rs], s64 offen offset:0 sc1\n\t" "buffer_load_dwordx4 v[20:23], %[ao], %[rs], s64 offen offset:256 sc1\n\t" \
    "buffer_load_dwordx4 v[24:27], %[ao], %[rs], s64 offen offset:2048 sc1\n\t" "buffer_load_dwordx4 v[28:31], %[ao], %[rs], s64 offen offset:2304 sc1\n\t" \
    "buffer_load_dwordx4 v[32:35], %[a1], %[rs], s64 offen offset:0 sc1\n\t" "buffer_load_dwordx4 v[36:39], %[a1], %[rs], s64 offen offset:256 sc1\n\t" \
    "buffer_load_dwordx4 v[40:43], %[a1], %[rs], s64 offen offset:2048 sc1\n\t" "buffer_load_dwordx4 v[44:47], %[a1], %[rs], s64 offen offset:2304 sc1\n\t" \
    "v_mov_b32 v122, s60\n\t" "buffer_store_dword v122, %[fo], %[rc], %[fz] offen sc1\n\t" "s_waitcnt vmcnt(1)\n\t" "s_branch 33b\n\t" "34:\n\t" "v_mov_b32 v122, s60\n\t" \
    "buffer_store_dword v122, %[fo], %[rc], %[fz] offen sc1\n\t" "s_cmp_eq_u32 s60, 64\n\t" "s_cbranch_scc1 90f\n\t" "s_add_u32 s67, s60, 1\n\t" "s_cmp_ge_u32 s61, s67\n\t" \
    "s_cbranch_scc1 32f\n\t" "31:\n\t" "buffer_load_dword v120, %[fo], %[rc], %[fv] offen sc1\n\t" "buffer_load_dword v121, %[fo], %[rc], %[vd] offen sc1\n\t" \
    "s_waitcnt vmcnt(0)\n\t" "v_readfirstlane_b32 s61, v120\n\t" "v_readfirstlane_b32 s66, v121\n\t" "s_nop 3\n\t" "s_cmp_ge_u32 s61, s67\n\t" "s_cbranch_scc1 32f\n\t" \
    "s_bitcmp1_b32 s66, 1\n\t" "s_cbranch_scc1 90f\n\t" "s_cmp_eq_u32 %[bd], 0\n\t" "s_cbranch_scc1 32f\n\t" "s_sub_u32 %[bd], %[bd], 1\n\t" "s_sleep 2\n\t" \
    "s_branch 31b\n\t" "32:\n\t" "s_lshr_b32 s66, s60, 2\n\t" "s_xor_b32 s66, s66, %[xp]\n\t" "s_and_b32 s66, s66, 1\n\t" "s_lshl_b32 s66, s66, 13\n\t" \
    "s_lshl_b32 s64, s60, 19\n\t" "s_add_u32 s64, s64, s66\n\t" "s_add_u32 s64, s64, %[vb]\n\t" "buffer_load_dwordx4 v[16:19], %[ao], %[rs], s64 offen offset:0 sc1\n\t" \
    "buffer_load_dwordx4 v[20:23], %[ao], %[rs], s64 offen offset:256 sc1\n\t" "buffer_load_dwordx4 v[24:27], %[ao], %[rs], s64 offen offset:2048 sc1\n\t" \
    "buffer_load_dwordx4 v[28:31], %[ao], %[rs], s64 offen offset:2304 sc1\n\t" "buffer_load_dwordx4 v[32:35], %[a1], %[rs], s64 offen offset:0 sc1\n\t" \
    "buffer_load_dwordx4 v[36:39], %[a1], %[rs], s64 offen offset:256 sc1\n\t" "buffer_load_dwordx4 v[40:43], %[a1], %[rs], s64 offen offset:2048 sc1\n\t" \
    "buffer_load_dwordx4 v[44:47], %[a1], %[rs], s64 offen offset:2304 sc1\n\t" "s_waitcnt vmcnt(0)\n\t" "s_branch 33b\n\t" "70:\n\t" "s_sub_u32 s67, s60, %[rg]\n\t" \
    "s_cmp_ge_u32 s62, s67\n\t" "s_cbranch_scc1 72f\n\t" "71:\n\t" "buffer_load_dword v120, %[fo], %[rc], %[fs] offen sc1\n\t" \
    "buffer_load_dword v121, %[fo], %[rc], %[vd] offen sc1\n\t" "s_waitcnt vmcnt(0)\n\t" "v_readfirstlane_b32 s62, v120\n\t" "v_readfirstlane_b32 s66, v121\n\t" "s_nop 3\n\t" \
    "s_cmp_ge_u32 s62, s67\n\t" "s_cbranch_scc1 72f\n\t" "s_bitcmp1_b32 s66, 1\n\t" "s_cbranch_scc1 90f\n\t" "s_cmp_eq_u32 %[bd], 0\n\t" "s_cbranch_scc1 72f\n\t" \
    "s_sub_u32 %[bd], %[bd], 1\n\t" "s_sleep 2\n\t" "s_branch 71b\n\t" "72:\n\t" "s_branch 73b\n\t" "90:\n\t"

#define T_DQ 1380
#define T_DV 1290
#define T_RQ 670
#define T_RV 640
#define T_M1 105
#define T_DY 582
#define T_DX 1250
#define T_MIN 35
#define T_PEN 280
#define T_SAFE 80
#define T_RETRY 700
#define T_POLL 1
#define T_KS 0
#define T_TS 0
#define T_CLR 210
#define T_EASE 1200
#define T_REF 1190
#define T_BETA 4
#define T_VLAG 110
#define T_U0 310
constexpr int T_OVH = 118, T_NEAR = 460;
constexpr unsigned NS(int ns) { return (unsigned)(ns * 64 / 10); }
struct Clk { unsigned cyc, g, pc, pg, k1, k2; };
DEV void mark(Clk& k) {
  unsigned long long c, r[8];
  asm volatile("s_memtime %0\n\ts_memrealtime %1\n\ts_nop 2\n\ts_memrealtime %2\n\ts_nop 2\n\ts_memrealtime %3\n\ts_nop 2\n\ts_memrealtime %4\n\ts_nop 2\n\t"
               "s_memrealtime %5\n\ts_nop 2\n\ts_memrealtime %6\n\ts_nop 2\n\ts_memrealtime %7\n\ts_waitcnt lgkmcnt(0)"
               : "=s"(c), "=s"(r[0]), "=s"(r[1]), "=s"(r[2]), "=s"(r[3]), "=s"(r[4]), "=s"(r[5]), "=s"(r[6]) :: "memory");
  const unsigned a = (unsigned)r[0];
  unsigned n = 0;
#pragma unroll
  for (int i = 1; i < 7; ++i) asm("s_cmp_eq_u32 %1, %2\n\ts_addc_u32 %0, %0, 0" : "+s"(n) : "s"((unsigned)r[i]), "s"(a) : "scc");
  k.g = (a + 4u) << 6; k.cyc = (unsigned)c + 12u + 16u * n;
}
DEV void anchor(Clk& k, bool any = false) {
  mark(k);
  const unsigned dg_ = k.g - k.pg;
  const float dc = (float)(k.cyc - k.pc), dg = (float)dg_;
  unsigned k1, k2;
  asm volatile("s_nop 0\n\tv_readfirstlane_b32 %0, %2\n\tv_readfirstlane_b32 %1, %3\n\ts_nop 3\n\ts_min_u32 %0, %0, 0x80000\n\ts_min_u32 %1, %1, 0x8000"
               : "=&s"(k1), "=&s"(k2) : "v"((unsigned)(dg * __builtin_amdgcn_rcpf(dc) * 65536.f)), "v"((unsigned)(dc * __builtin_amdgcn_rcpf(dg) * 65536.f)) : "scc");
  if (any) { k.k1 = k1; k.k2 = k2; }
  else asm("s_cmp_lt_u32 %4, %5\n\ts_cselect_b32 %2, %2, %0\n\ts_cselect_b32 %3, %3, %1\n\ts_add_u32 %0, %0, %2\n\ts_lshr_b32 %0, %0, 1\n\ts_add_u32 %1, %1, %3\n\ts_lshr_b32 %1, %1, 1"
           : "+s"(k.k1), "+s"(k.k2), "+s"(k1), "+s"(k2) : "s"(dg_ - NS(1400)), "n"(NS(800)) : "scc");
  k.pc = k.cyc; k.pg = k.g;
}
DEV unsigned to_cyc(const Clk& k, unsigned g) {
  unsigned d;
  asm("s_max_i32 %0, %1, 0xffff8000\n\ts_min_i32 %0, %0, 0x8000\n\ts_mul_i32 %0, %0, %2\n\ts_ashr_i32 %0, %0, 16" : "=&s"(d) : "s"(g - k.g), "s"(k.k2) : "scc");
  return k.cyc + d - T_OVH;
}
DEV void wait_cyc(unsigned t) { asm volatile(TIMED_WAIT :: "s"(t) : CL_S); }
DEV void close_in(Clk& k, unsigned t) {
  if ((int)(t - (unsigned)__builtin_amdgcn_s_memtime()) > T_NEAR + 60) { wait_cyc(t - T_NEAR); mark(k); }
}
DEV int slow_line(const u16* q, int h, int j, int lane) { return (j * 32 + (lane >> 1)) * 8192 + (h >> 3) * 2048 + (lane & 1) * 1024 + ((3 - (int)((size_t)q >> 7)) & 7) * 128; }
DEV int turn(int h, int w) { return ((h >> 3) ^ (w & 1)) & 3; }
DEV void hold(const Ctx& c, int ticks) {
#pragma unroll 1
  while ((unsigned)__builtin_amdgcn_s_memrealtime() - c.t0 < (unsigned)ticks) __builtin_amdgcn_s_sleep(4);
}
DEV int z_mix(int y) { return (y >> 4) | ((y & 1) << 2); }
DEV int u_slot(int s, int para) { return R_U + (s & (URING == 64 ? 127 : URING - 1)) * MB + ((para ^ (s >> 1)) & 1) * 8192; }
DEV unsigned ask_stamps(Ctx& c, int par, int slot) { return __builtin_amdgcn_raw_buffer_load_b32(c.rs, amap(24576, par) + fresh_lane() * 128, slot, AUX_SC1); }
DEV unsigned latest(Ctx& c, const Clk& k, int par, int slot, unsigned v) {
  if (__builtin_expect(!allok(v), 0)) {
#pragma unroll 1
    do {
      __builtin_amdgcn_s_sleep(T_POLL);
      asm volatile("" ::: "memory");
      const unsigned t = __builtin_amdgcn_raw_buffer_load_b32(c.rs, v == SENT ? amap(24576, par) + fresh_lane() * 128 : 1 << 30, slot, AUX_SC1);
      v = v == SENT ? t : v;
    } while (!allok(v) && !spend(c));
  }
  int d = (int)(v - k.g);
  d = max(d, __builtin_amdgcn_update_dpp(d, d, 0x128, 0xf, 0xf, true));
  d = max(d, __builtin_amdgcn_update_dpp(d, d, 0x124, 0xf, 0xf, true));
  d = max(d, __builtin_amdgcn_update_dpp(d, d, 0x122, 0xf, 0xf, true));
  d = max(d, __builtin_amdgcn_update_dpp(d, d, 0x121, 0xf, 0xf, true));
  unsigned m;
  asm("s_max_i32 %0, %1, %2\n\ts_max_i32 %0, %0, %3\n\ts_max_i32 %0, %0, %4" : "=&s"(m)
      : "s"(__builtin_amdgcn_readlane(d, 0)), "s"(__builtin_amdgcn_readlane(d, 16)), "s"(__builtin_amdgcn_readlane(d, 32)), "s"(__builtin_amdgcn_readlane(d, 48)) : "scc");
  return k.g + m;
}
struct Plan { unsigned gx, dx, ease; };
DEV unsigned smax(unsigned a, unsigned b) { return (int)(a - b) > 0 ? a : b; }
DEV unsigned pick(int d, unsigned a, unsigned b) { unsigned r; asm("s_cmp_gt_i32 %1, 0\n\ts_cselect_b32 %0, %2, %3" : "=s"(r) : "s"(d), "s"(a), "s"(b) : "scc"); return r; }
DEV unsigned when_p(const Plan& t, unsigned last, unsigned more = 0) {
  const unsigned gy = t.gx + t.dx + NS(T_M1), a = last + NS(T_PEN), b = gy + NS(T_RETRY);
  return pick((int)(last + NS(T_MIN) - gy), pick((int)(a - b), a, b) + more, gy);
}
DEV void advance(Plan& t, unsigned last) {
  const int over = (int)(last - t.gx - t.dx + t.ease), slow = (int)(t.dx - t.ease - NS(T_REF));
  t.gx = when_p(t, last, NS(T_SAFE)) + NS(T_DY) + pick(slow, (unsigned)(slow * T_BETA >> 3), 0u);
  t.dx -= t.ease - (t.ease >> 1); t.ease >>= 1;
  t.dx += pick(over - (int)NS(80), NS(4), pick(over, (unsigned)(over >> 1), (unsigned)(over >> 3)));
}
DEV unsigned next_go(Ctx& c, Clk& k, Plan& t, int par, int slot, unsigned v) {
  SCHED();
  anchor(k);
  SCHED();
  advance(t, latest(c, k, par, slot, v));
  return to_cyc(k, t.gx);
}

DEV constexpr int qk_r(int j) { return j < 4 ? 0 : 1 + (j - 4) % 3; }
DEV constexpr int qk_i(int j) { return j < 4 ? j : (j - 4) / 3; }
DEV constexpr int qk_upto(int u) { return K_FIRST && u >= 3 ? (u + 3 < 16 ? u + 3 : 16) : (u + 4 < 16 ? u + 4 : 16); }
DEV void qk_role(const Params& p, Ctx& c, int h) {
  constexpr int WL = 0, MEMS = 131072, KXL = 147456, QKX = 155648, QROW = 272;
  c.budget = SPIN_CAP;
  const int w = c.w;
  Clk k = {0u, 0u, 0u, 0u, 0u, 0u};
  anchor(k, true);
  v8u wa[32], wh[16];
  unsigned look = claim_look(c);
  bool claimed = false;
  hold(c, QK_AT);
  {
    const int lane = fresh_lane(), g = lane >> 4, cc = lane & 15;
    const rsrc_t rq = mkrs(p.Wq, 1024 * 4096 * 2), rk = mkrs(p.Wk, 1024 * 4096 * 2);
    int vo[4];
#pragma unroll
    for (int i = 0; i < 4; ++i) vo[i] = opq((256 * w + 64 * (i ^ turn(h, w)) + krow_lane(g)) * 8192 + (h * 128 + 8 * cc) * 2);
    v4u row[16][8];
    auto request = [&](int u) {
#pragma unroll
      for (int r = 0; r < 8; ++r) row[u][r] = ld16w<W_AUX>((u < 8) != K_FIRST ? rq : rk, vo[(u >> 1) & 3], krow(4 * (u & 1) + (r >> 1), r & 1) * 8192);
    };
#pragma unroll
    for (int u = 0; u < qk_upto(-1); ++u) { request(u); if (u == 0) { SCHED(); ask(c, look); SCHED(); } }
    SCHED();
    const int sw = MEMS + w * 8192 + g * 256, lw = opq(WL + w * 32768 + lane * 16);
    int sr[2];
#pragma unroll
    for (int t = 0; t < 2; ++t) { const int i = (2 * cc + t) & 7; sr[t] = opq(i * 1024 + g * 256 + ((4 * w + (cc >> 2) + 2 * i) & 15) * 16); }
    sfor<16>([&](auto U) __attribute__((always_inline)) {
      constexpr int u = U.value;
#pragma unroll
      for (int i = 0; i < 8; ++i) {
        v4u f;
#pragma unroll
        for (int r = 0; r < 4; ++r) f[r] = prm(row[u][2 * r + 1][i >> 1], row[u][2 * r][i >> 1], i & 1);
        sts16(c.L, sw + i * 1024 + ((cc + 2 * i) & 15) * 16, f);
      }
      SCHED();
      if ((u == 5 || u == 10) && !claimed) claimed = claim_try(c, look);
      if (u == 2 || u == 7) look = claim_look(c);
      if (qk_upto(u) > qk_upto(u - 1)) request(qk_upto(u - 1));
      if (u == 15 && !claimed) claim(c, 0);
      BAR();
      v4u in[4][2];
#pragma unroll
      for (int t = 1; t >= 0; --t)
#pragma unroll
        for (int r = 0; r < 4; ++r) in[r][t] = lds16(c.L, sr[t] + MEMS + ((w + r) & 3) * 8192);
      SCHED();
#pragma unroll
      for (int j = 0; j < 16; ++j) {
        if (qk_i(j) != ((u >> 1) & 3)) continue;
#pragma unroll
        for (int t = 1; t >= 0; --t) {
          const v4u o = in[qk_r(j)][t];
          constexpr bool isq = (u < 8) != K_FIRST;
          if (!isq && t == 1) sts16(c.L, lw + j * 2048 + (u & 1) * 1024, o);
          else {
            v8u& d = isq ? wa[2 * j + t] : wh[j];
#pragma unroll
            for (int r = 0; r < 4; ++r) d[4 * (u & 1) + r] = o[r];
#define M_(r) asm volatile("" : "+" r(d))
            if (u & 1) { if (isq) AT_ACC(M_, 2 * j + t); else AT_HOME(M_, j); }
#undef M_
          }
        }
      }
      BAR();
    });
  }
  STAMP(0);
  BAR();
  const unsigned vote = wait_verdict(c);
  const int par = vote_b(vote), para = vote_a(vote);
  const int lane = fresh_lane(), g = lane >> 4, cc = lane & 15;
  const int moff = opq(amap(w * 4096 + lane * 16, par) + (turn(h, w) & 1) * 1024), m2 = R_MEM + (turn(h, w) & 2) * 1024;
  unsigned tries = 1u << 12;
  *(volatile LAS unsigned*)(c.L + 160512) = 0u;
  LGKM(0);
  const int idx = opq((lane & 1) ? 0xEEEE : 0x4444);
  TICK_DECL;
  const int l0 = opq(WL + w * 32768 + lane * 16), l1 = opq(MEMS + lane * 16);
  const int qw = opq(QKX + (2 * w + (cc >> 3)) * 256 + g * 64 + (cc & 1) * 16 + ((cc >> 1) & 3) * 4), kw = opq(QKX + (8 + 2 * g) * QROW + (32 * w + 2 * cc) * 2);
#define QK_PINS PIN_ACC0(wa); PIN_ACC1(wa); PIN_HOME(wh)
#define QK_IN [l0] "v"(l0), [l1] "v"(l1), [mo] "v"(moff), [ix] "v"(idx), [qw] "v"(qw), [kw] "v"(kw), [rs] "s"(c.rs), [sb] "s"(m2 + (s & 3) * MEM_SLOT)
#define QK_CL CL_S, CL_16_32, CL_160_200, CL_200_216, CL_216_256
  if (w) RAWBAR();
  if (w == 3) {
    const int hq = opq(lane ? 1 << 30 : amap(24576 + h * 128, par));
    anchor(k, true);
    unsigned tg = k.cyc;
    Plan tt = {k.g - NS(100000), NS(T_DX + T_EASE), NS(T_EASE)};
    for (int s = 0; s < NSEG; ++s) {
      const int pb = R_P + (s & 3) * P_SLOT;
      QK_PINS;
      asm volatile(QK_STEP3 : [bd] "+s"(c.budget), [rt] "+s"(tries) : QK_IN, [hq] "v"(hq), [pb] "s"(pb), [tg] "s"(tg), [ca] "s"(k.cyc - T_DQ), [k1] "s"(k.k1), [ga] "s"(k.g), [dr] "n"(T_RQ), [pr] "s"(R_P + ((s + 1) & 3) * P_SLOT) : QK_CL);
      QK_PINS;
      LOG(1, s, tg);
      const unsigned sv = ask_stamps(c, par, pb);
      SCHED();
      anchor(k);
      SCHED();
      advance(tt, latest(c, k, par, pb, sv));
      tg = to_cyc(k, tt.gx);
      RAWBAR();
      LOG(2, s, k.cyc); LOG(3, s, k.g); LOG(4, s, tt.dx);
      st4s(mkrs((const void*)c.ctrl, CTRL_BYTES), hq & (1 << 30), C_FLAG + (F_S + h) * 128, (unsigned)s + 1u);
      TICK_SUM(10);
      close_in(k, tg);
      tg = to_cyc(k, tt.gx);
    }
  } else {
    const int kx0 = opq(KXL + g * 512 + cc * 16), kmrd = opq(QKX + g * 16 + (8 + (cc & 7)) * QROW);
    unsigned seen = NOFLAG;
    if (w == 0) {
      const int prow = (2 * g + (cc & 1)) * 2 + ((cc >> 1) & 1);
      const int px = opq(amap((h >> 1) * 1024 + ((2 * (h & 1) + (cc >> 3)) * 16 + prow) * 16 + ((cc >> 2) & 1) * 8, par));
      const int pm = opq(cc < 8 ? amap(16384 + h * 128 + (2 * g + (cc & 1)) * 16 + (cc & 2) * 4 + (cc & 4), par) : 1 << 30);
      const int ps = opq((cc & 1) ? 0x03020706 : 0x05040100), pa = opq(lane * 128);
      for (int s = 0; s < NSEG; ++s) {
        if (seen < (unsigned)s + 1u) seen = wait_flag(c, F_K + h, s + 1);
        QK_PINS;
        unsigned pd;
        asm volatile(QK_STEP0 : [bd] "+s"(c.budget), [rt] "+s"(tries), [sq] "+s"(seen), [pd] "=&v"(pd)
                     : QK_IN, [kx] "v"(kx0), [km] "v"(kmrd), [px] "v"(px), [pm] "v"(pm), [ps] "v"(ps), [pb] "s"(R_P + (s & 3) * P_SLOT), [pr] "s"(R_P + ((s + 1) & 3) * P_SLOT),
                       [kb] "s"(R_KX - KXL + xblk(s, h, para)), [fq] "s"(c.ctrl + (C_FLAG + (F_K + h) * 128) / 4),
                       [kn] "s"(R_KX + xblk(min(s + 1, NSEG - 1), h, para)), [pa] "v"(pa) : QK_CL);
        QK_PINS;
        LITE(s); LOGS(0, s);
        TICK_SUM(10);
      }
      RAWBAR();
    } else {
      const int qt = w - 1;
      const int px = opq(amap(qt ? 16384 + h * 1024 + lane * 16 : h * 512 + (g >> 1) * 256 + cc * 16 + (g & 1) * 8, para));
      const int pm = opq(g < 2 ? amap(49152 + (qt * 32 + h) * 256 + cc * 16 + g * 8, para) : 1 << 30);
      const int ps = opq(cc - 4 * g), hq = opq(g * 512 + (16 * qt + cc) * 16);
#define QK_TOK(STEP) for (int s = 0; s < NSEG; ++s) { QK_PINS; \
        asm volatile(STEP : [bd] "+s"(c.budget), [rt] "+s"(tries), [sq] "+s"(seen) \
                     : QK_IN, [kx] "v"(kx0), [km] "v"(kmrd), [px] "v"(px), [pm] "v"(pm), [ps] "v"(ps), [hq] "v"(hq), [qb] "s"(R_QX + xblk(s, h, para)), [tb] "s"(R_PT + (s & 7) * PT_SLOT), \
                       [tr] "s"(R_PT + ((s + 2) & 7) * PT_SLOT), [sn] "s"(s + 1), [fq] "s"(c.ctrl + (C_FLAG + (F_Q + h) * 128) / 4) : QK_CL); \
        QK_PINS; TICK_SUM(10); }
      if (w == 1) { QK_TOK(QK_STEP1) } else { QK_TOK(QK_STEP2) }
    }
  }
  TICK_OUT();
  if (c.budget == 0u) __hip_atomic_store(c.ctrl + C_TMO / 4, 1u, RLX_AGENT);
}

DEV void shuffle_cols(Ctx& c, int scratch, const v4u (&rows)[8], v4u (&o)[8]) {
  const int g = c.lane >> 4, cc = c.lane & 15;
#pragma unroll
  for (int i = 0; i < 8; ++i) {
    v4u f;
#pragma unroll
    for (int r = 0; r < 4; ++r) f[r] = prm(rows[2 * r + 1][i >> 1], rows[2 * r][i >> 1], i & 1);
    sts16(c.L, scratch + (8 * cc + i) * 64 + ((g ^ (cc & 3)) << 4), f);
  }
  asm volatile("s_waitcnt lgkmcnt(0)" ::: "memory");
#pragma unroll
  for (int i = 0; i < 8; ++i) { const int col = 16 * i + cc; o[i] = lds16(c.L, scratch + col * 64 + ((g ^ ((col >> 3) & 3)) << 4)); }
  asm volatile("s_waitcnt lgkmcnt(0)" ::: "memory");
}

DEV void vo_role(const Params& p, Ctx& c, int h) {
  constexpr int WL = 0, MEMS = 131072, VM = 147456, VROW = 272;
  c.budget = SPIN_CAP;
  const int w = c.w, lane = c.lane = fresh_lane(), g = lane >> 4, cc = lane & 15;
  Clk k = {0u, 0u, 0u, 0u, 0u, 0u};
  anchor(k, true);
  v8u wa[32], wh[16];
  ask(c, claim_look(c));
  unsigned look = 0;
  bool claimed = false;
  hold(c, VO_AT);
  const rsrc_t rwo = mkrs(p.Wo, 4096 * 1024 * 2);
  const int voo = (h * 128 + 4 * g) * 2048 + (256 * ((cc >> 3) ^ (h >> 3)) + 64 * w + 8 * (cc & 7)) * 2;
  v4u buf[3][8];
  auto request_o = [&](int u) {
#pragma unroll
    for (int r = 0; r < 8; ++r) buf[u % 3][r] = ld16w<W_AUX>(rwo, voo ^ ((u >> 2) * 1024), (32 * (u & 3) + 16 * (r >> 2) + (r & 3)) * 2048);
  };
  {
    const rsrc_t rw = mkrs(p.Wv, 1024 * 4096 * 2);
    const int vo = (256 * w + krow_lane(g)) * 8192 + (h * 128 + 8 * cc) * 2;
    v4u vrow[8][8];
    auto request = [&](int u) {
#pragma unroll
      for (int r = 0; r < 8; ++r) vrow[u][r] = ld16w<W_AUX>(rw, vo, (64 * ((u >> 1) ^ turn(h, w)) + krow(4 * (u & 1) + (r >> 1), r & 1)) * 8192);
    };
#pragma unroll
    for (int u = 0; u < 3; ++u) request(u);
    SCHED();
    const int wr_ = MEMS + w * 8192 + g * 256;
    int sr[2];
#pragma unroll
    for (int t = 0; t < 2; ++t) { const int i = (2 * cc + t) & 7; sr[t] = opq(i * 1024 + g * 256 + ((4 * w + (cc >> 2) + 2 * i) & 15) * 16); }
#pragma unroll
    for (int u = 0; u < 8; ++u) {
#pragma unroll
      for (int i = 0; i < 8; ++i) {
        v4u f;
#pragma unroll
        for (int r = 0; r < 4; ++r) f[r] = prm(vrow[u][2 * r + 1][i >> 1], vrow[u][2 * r][i >> 1], i & 1);
        sts16(c.L, wr_ + i * 1024 + ((cc + 2 * i) & 15) * 16, f);
      }
      SCHED();
      if (u == 7) claimed = claim_try(c, look);
      if (u == 4) look = claim_look(c);
      if (u + 3 < 8) request(u + 3); else request_o(u - 5);
      BAR();
      v4u in[4][2];
#pragma unroll
      for (int t = 1; t >= 0; --t)
#pragma unroll
        for (int r = 0; r < 4; ++r) in[r][t] = lds16(c.L, sr[t] + MEMS + ((w + r) & 3) * 8192);
      SCHED();
#pragma unroll
      for (int j = 0; j < 16; ++j) {
        if (qk_i(j) != (u >> 1)) continue;
#pragma unroll
        for (int t = 1; t >= 0; --t) {
          const v4u o = in[qk_r(j)][t];
          if (t == 1 && (j & 1)) sts16(c.L, WL + (w * 16 + (j >> 1)) * 2048 + (u & 1) * 1024 + lane * 16, o);
          else {
            const int n = 3 * (j >> 1) + ((j & 1) ? 2 : t);
#pragma unroll
            for (int r = 0; r < 4; ++r) wa[n][4 * (u & 1) + r] = o[r];
#define M_(r) asm volatile("" : "+" r(wa[n]))
            if (u & 1) AT_ACC(M_, n);
#undef M_
          }
        }
      }
      BAR();
    }
  }
#pragma unroll
  for (int u = 0; u < 8; ++u) {
    v4u o[8];
    shuffle_cols(c, MEMS + w * 8192, buf[u % 3], o);
    SCHED();
    if (u + 3 < 8) request_o(u + 3);
#pragma unroll
    for (int i = 0; i < 4; ++i) {
      const int pr = 4 * (u >> 2) + i, ch = u & 3;
      const v8u f = {o[2 * i][0], o[2 * i + 1][0], o[2 * i][1], o[2 * i + 1][1], o[2 * i][2], o[2 * i + 1][2], o[2 * i][3], o[2 * i + 1][3]};
      if (ch < 2) {
        wh[2 * pr + ch] = f;
#define M_(r) asm volatile("" : "+" r(wh[2 * pr + ch]))
        AT_HOME(M_, 2 * pr + ch);
#undef M_
      } else if (ch == 2) {
        wa[24 + pr] = f;
#define M_(r) asm volatile("" : "+" r(wa[24 + pr]))
        AT_ACC(M_, 24 + pr);
#undef M_
      } else {
        sts16(c.L, WL + (w * 16 + 8 + pr) * 2048 + lane * 16, lo4(f));
        sts16(c.L, WL + (w * 16 + 8 + pr) * 2048 + 1024 + lane * 16, hi4(f));
      }
    }
    SCHED();
  }
  if (!claimed) claim(c, 0);
  BAR();
  STAMP(0);
  const unsigned vote = wait_verdict(c);
  const int par = vote_b(vote), para = vote_a(vote);
  const int moff = opq(amap(w * 4096 + lane * 16, par) + (turn(h, w) & 1) * 1024), m2 = R_MEM + (turn(h, w) & 2) * 1024;
  unsigned tries = 1u << 12;
  *(volatile LAS unsigned*)(c.L + 160512) = 0u;
  LGKM(0);
  const int idx = opq((lane & 1) ? 0xEEEE : 0x4444);
  const int u_off = opq((4 * w + g) * 16384 + (h ^ (8 * g)) * 256 + cc * 16);
  const int hq = opq((lane | w) ? 1 << 30 : amap(24576 + (32 + h) * 128, par));
  const int l0 = opq(WL + w * 32768 + lane * 16), l1 = opq(MEMS + lane * 16);
  const int vmw = opq(VM + 2 * g * VROW + (32 * w + 2 * cc) * 2);
  const int vmr = opq(VM + (cc >> 1) * VROW + g * 16);
  TICK_DECL;
  anchor(k, true);
  unsigned tg = k.cyc;
  Plan tt = {k.g - NS(100000), NS(T_DX + T_EASE), NS(T_EASE)};
  for (int s = 0; s < NSEG; ++s) {
    const int pb = R_P + (s & 3) * P_SLOT;
    PIN_ACC0(wa); PIN_ACC1(wa); PIN_HOME(wh);
    asm volatile(VO_STEP
        : [bd] "+s"(c.budget), [rt] "+s"(tries)
        : [l0] "v"(l0), [l1] "v"(l1), [mo] "v"(moff), [ix] "v"(idx), [vw] "v"(vmw), [vr] "v"(vmr), [uo] "v"(u_off), [w] "s"(w), [rs] "s"(c.rs),
          [sb] "s"(m2 + (s & 3) * MEM_SLOT), [ub] "s"(u_slot(s, para) + (h >> 3) * 16 * 16384), [ur] "s"(u_slot(s + 2, para) + (2 * h + (w >> 1)) * 16384 + (w & 1) * 4096 - MEMS), [tg] "s"(tg + T_VLAG), [hq] "v"(hq), [pb] "s"(pb),
          [ca] "s"(k.cyc - T_DV), [k1] "s"(k.k1), [ga] "s"(k.g), [dr] "n"(T_RV), [pr] "s"(R_P + ((s + 1) & 3) * P_SLOT)
        : CL_S, CL_16_32, CL_160_200, CL_200_216, CL_216_256);
    PIN_ACC0(wa); PIN_ACC1(wa); PIN_HOME(wh);
    if (w == 0) {
      LITE(s);
      LOG(1, s, tg);
      const unsigned sv = ask_stamps(c, par, pb);
      SCHED();
      anchor(k);
      const unsigned gc = tt.gx + tt.dx + NS(T_M1 + T_CLR);
      const bool ontime = (int)(gc - k.g) > 0;
      if (ontime) { wait_cyc(to_cyc(k, gc)); RAWBAR(); }
      SCHED();
      advance(tt, latest(c, k, par, pb, sv));
      if (!ontime) { wait_cyc(to_cyc(k, tt.gx - NS(T_DY + T_SAFE - T_CLR))); RAWBAR(); }
      tg = to_cyc(k, tt.gx);
      LOG(2, s, k.cyc); LOG(3, s, k.g);
    }
    TICK_SUM(10);
    if (w == 0) { close_in(k, tg); tg = to_cyc(k, tt.gx); }
  }
  TICK_OUT();
  if (c.budget == 0u) __hip_atomic_store(c.ctrl + C_TMO / 4, 1u, RLX_AGENT);
}

DEV void y_role(const Params& p, Ctx& c, int y) {
  constexpr int ZR = 0, UL = 131072, CW = 155648;
  c.budget = SPIN_CAP;
  const int w = c.w, lane = c.lane = fresh_lane(), g = lane >> 4, cc = lane & 15;
  const v4u sent4 = {SENT, SENT, SENT, SENT};
  STAMP(2);
  Clk k = {0u, 0u, 0u, 0u, 0u, 0u};
  anchor(k, true);
  unsigned m0 = 0;
  if (w == 0) {
    unsigned best[2] = {~0u, ~0u};
    const unsigned first = claim_look(c);
    st4s(c.rs, y * 16384 + (lane >> 5) * 8192 + (lane & 31) * 4, R_U, SENT);
    ask(c, first);
#pragma unroll 1
    for (int n = 0; n < 12; ++n) {
#pragma unroll
      for (int r = 0; r < 2; ++r) {
        VMWAIT(0);
        const unsigned t0 = (unsigned)__builtin_amdgcn_s_memtime();
        LGKM(0);
        st4s(c.rs, y * 16384 + (lane & 31) * 4, R_U + r * 8192, SENT);
        VMWAIT(0);
        const unsigned t1 = (unsigned)__builtin_amdgcn_s_memtime();
        LGKM(0);
        best[r] = min(best[r], t1 - t0);
      }
      if (n + 1 >= PROBES && min(best[0], best[1]) < 600u && max(best[0], best[1]) > min(best[0], best[1]) + 128u) break;
    }
    STAMP(5);
    claim(c, CL_VOTED | (best[1] < best[0] ? CL_A : 0u));
    m0 = *(const GAS unsigned*)(p.mem0 + (lane >> 3) * 1024 + 16 * y + 2 * (lane & 7));
    m0 = m0 < SENT - 1u ? m0 : SENT - 1u;
    if (lane < 8) *(LAS unsigned*)(c.L + CW + lane * 4) = 0u;
    const unsigned v = wait_vote(c);
    STAMP(7);
    const int oa = y * 16384 + vote_a(v) * 8192 + lane * 16, ob = amap(y * 3072 + lane * 16, vote_b(v));
#pragma unroll
    for (int i = 0; i < 16; ++i) st16s(c.rs, oa + (i & 3) * 1024, R_U + (i >> 3) * MB + ((i >> 2) & 1) * 4096, sent4);
#pragma unroll
    for (int i = 0; i < 3; ++i) st16s(c.rs, amap(y * 3072 + i * 1024 + lane * 16, vote_b(v)), R_MEM, sent4);
#pragma unroll
    for (int i = 0; i < 8; ++i) st16s(c.rs, oa + (i & 3) * 1024, R_PT + (i >> 2) * 4096, sent4);
    (void)ob;
    VMWAIT(0);
    if (lane == 0) __hip_atomic_fetch_add(c.ctrl + (C_CLAIM + c.att * 1024) / 4 + c.slot, CL_FILLED, RLX_AGENT);
    STAMP(6);
  } else {
    if (YH && w < 3 && (y & 8) == 0) {
      hold(c, YH_AT);
#pragma unroll
      for (int i = 0; i < 8; ++i) {
        const u16* q = (i < 4) != K_FIRST ? p.Wq : p.Wk;
        if (i == 4) hold(c, YH_AT + YH_STEP);
        m0 += __builtin_amdgcn_raw_buffer_load_b32(mkrs(q, 8 * MB), slow_line(q, y >> 1, (y & 7) + 8 * (i & 3), lane) ^ ((w - 1) * 128), 0, W_AUX);
      }
      asm volatile("" :: "v"(m0));
    }
  }
  BAR();
  const int nb = (c.b - SERVER) >> 3;
  if (w == 3 ? (c.b == SERVER + 8 || c.b == SERVER + 16) : w && c.b != SERVER) {
    hold(c, X_AT);
#pragma unroll
    for (int i = 0; i < 4; ++i) m0 += __builtin_amdgcn_raw_buffer_load_b32(mkrs(p.x, 4 * MB), ((w == 3 ? 2 * y - nb - 1 : 2 * y + w - 1) * 64 + lane) * 128, i * MB, 0);
    asm volatile("" :: "v"(m0));
  }
  const unsigned vote = wait_verdict(c);
  const int par = vote_b(vote), para = vote_a(vote);
  const int mo = amap(y * 256 + lane * 4, par);
  if (w == 0) st4s(c.rs, mo, R_MEM, m0);
  STAMP(0);
  const int rot = y >> 4;
  volatile LAS unsigned* const GO = (volatile LAS unsigned*)(c.L + CW);
  TICK_DECL;
  if (w == 0) {
    int xo[4];
#pragma unroll
    for (int i = 0; i < 4; ++i) xo[i] = amap((i ^ rot) * 4096 + lane * 16, par);
    const int pmo = amap(16384 + rot * 1024 + (2 * g + (cc & 1)) * 128 + (cc >> 1) * 16, par), psel = (cc & 1) ? 0x03020706 : 0x05040100;
    constexpr int lead = T_U0, late = 0;
    anchor(k, true);
    Plan tt = {k.g - NS(100000), NS(T_DX + T_EASE), NS(T_EASE)};
    unsigned done = k.cyc;
    const int so = amap(24576, par) + lane * 128;
    const int idx = opq((lane & 1) ? 0xEEEE : 0x4444);
    const int uo = y * 16384 + lane * 16;
    const int mw = amap(y * 256 + (2 * g + (cc & 1)) * 32 + (cc >> 1) * 4, par);
    for (int s = 0; s < NSEG - 1; ++s) {
      if (s == 0) RAWBAR();
      const int za = ZR + (s & 3) * 32768 + rot * 8192 + lane * 16;
      v32u z0, z1, z2, z3;
      asm volatile(
          "ds_read_b128 a[0:3], %[z0] offset:0\n\t" "ds_read_b128 a[4:7], %[z0] offset:1024\n\t" "ds_read_b128 a[8:11], %[z0] offset:2048\n\t" "ds_read_b128 a[12:15], %[z0] offset:3072\n\t"
          "ds_read_b128 a[16:19], %[z0] offset:4096\n\t" "ds_read_b128 a[20:23], %[z0] offset:5120\n\t" "ds_read_b128 a[24:27], %[z0] offset:6144\n\t" "ds_read_b128 a[28:31], %[z0] offset:7168\n\t"
          "ds_read_b128 a[32:35], %[z1] offset:0\n\t" "ds_read_b128 a[36:39], %[z1] offset:1024\n\t" "ds_read_b128 a[40:43], %[z1] offset:2048\n\t" "ds_read_b128 a[44:47], %[z1] offset:3072\n\t"
          "ds_read_b128 a[48:51], %[z1] offset:4096\n\t" "ds_read_b128 a[52:55], %[z1] offset:5120\n\t" "ds_read_b128 a[56:59], %[z1] offset:6144\n\t" "ds_read_b128 a[60:63], %[z1] offset:7168\n\t"
          "ds_read_b128 a[64:67], %[z2] offset:0\n\t" "ds_read_b128 a[68:71], %[z2] offset:1024\n\t" "ds_read_b128 a[72:75], %[z2] offset:2048\n\t" "ds_read_b128 a[76:79], %[z2] offset:3072\n\t"
          "ds_read_b128 a[80:83], %[z2] offset:4096\n\t" "ds_read_b128 a[84:87], %[z2] offset:5120\n\t" "ds_read_b128 a[88:91], %[z2] offset:6144\n\t" "ds_read_b128 a[92:95], %[z2] offset:7168\n\t"
          "ds_read_b128 a[96:99], %[z3] offset:0\n\t" "ds_read_b128 a[100:103], %[z3] offset:1024\n\t" "ds_read_b128 a[104:107], %[z3] offset:2048\n\t" "ds_read_b128 a[108:111], %[z3] offset:3072\n\t"
          "ds_read_b128 a[112:115], %[z3] offset:4096\n\t" "ds_read_b128 a[116:119], %[z3] offset:5120\n\t" "ds_read_b128 a[120:123], %[z3] offset:6144\n\t" "ds_read_b128 a[124:127], %[z3] offset:7168\n\t"
          "s_waitcnt lgkmcnt(0)"
          : "={a[0:31]}"(z0), "={a[32:63]}"(z1), "={a[64:95]}"(z2), "={a[96:127]}"(z3) : [z0] "v"(za), [z1] "v"(za ^ 8192), [z2] "v"(za ^ 16384), [z3] "v"(za ^ 24576) : "memory");
      const int pb = R_P + (s & 3) * P_SLOT;
      v32u uu;
      const int ub = u_slot(s, para);
      TICK(0);
      anchor(k);
      TICK(8);
      unsigned gp = tt.gx + tt.dx + NS(T_M1), lt = late, sv = SENT;
      unsigned bad = (int)(done - to_cyc(k, tt.gx)) > T_OVH;
      TICK(4);
      LOG(2, s, k.cyc); LOG(3, s, k.g);
#pragma unroll 1
      for (;;) {
        if (bad) {
          const bool all = allok(sv);
          const unsigned last = latest(c, k, par, pb, sv);
          if (!all) mark(k);
          gp = when_p(tt, last);
          lt = 1;
        }
        close_in(k, to_cyc(k, gp) - (lt ? 96 : lead));
        const unsigned tg = to_cyc(k, gp);
        if (!bad) LOG(1, s, tg);
        asm volatile(Y_STEP
          : "=&{v[144:175]}"(uu), [bad] "=&s"(bad), [sv] "=&v"(sv)
          : [x0] "v"(xo[0]), [x1] "v"(xo[1]), [x2] "v"(xo[2]), [x3] "v"(xo[3]), [pm] "v"(pmo), [u0] "v"(uo + (rot ^ (y & 3)) * 2048), [mw] "v"(mw), [ps] "v"(psel), [ix] "v"(idx),
            [rs] "s"(c.rs), [pb] "s"(pb), [ub] "s"(ub), [mb] "s"(R_MEM + ((s + 1) & 3) * MEM_SLOT), [mr] "s"(R_MEM + ((s + 3) & 3) * MEM_SLOT), [tu] "s"(tg - (lt ? 96 : lead)), [tg] "s"(tg - 32), [late] "s"(lt),
            [so] "v"(so), [bd] "s"(c.budget),
            "{a[0:31]}"(z0), "{a[32:63]}"(z1), "{a[64:95]}"(z2), "{a[96:127]}"(z3)
          : CL_S, CL_120_128, CL_176_200, CL_200_216, CL_216_256);
        bad = __builtin_amdgcn_readfirstlane(bad);
        if (bad != 1u) break;
        spend(c);
      }
      done = bad;
      SCHED();
      const int ul = opq(UL + rot * 2048 + lane * 16);
#pragma unroll
      for (int i = 0; i < 8; ++i) sts16(c.L, (ul ^ ((i >> 1) * 2048)) + (i & 1) * 1024, v4u{uu[4 * i], uu[4 * i + 1], uu[4 * i + 2], uu[4 * i + 3]});
      asm volatile("" ::: "memory");
      *GO = s + 1;
      asm volatile("s_waitcnt vmcnt(2)" : "+v"(sv));
      advance(tt, latest(c, k, par, pb, sv));
      TICK(3);
      LITE(s); LOG(0, s, done);
      TICK_LDS(CW + 256);
    }
    {
      anchor(k);
      wait_cyc(to_cyc(k, tt.gx + tt.dx + NS(T_M1)));
      const int ub = u_slot(NSEG - 1, para);
      v4u u[8];
#pragma unroll 1
      for (;;) {
        asm volatile("" ::: "memory");
#pragma unroll
        for (int i = 0; i < 8; ++i) u[i] = ld16s(c.rs, uo + (i & 3) * 1024, ub + (i >> 2) * 4096);
        SCHED();
        unsigned t = 0;
#pragma unroll
        for (int i = 0; i < 8; ++i) t = umax(t, top(u[i]));
        if (allok(t) || spend(c)) break;
      }
      RAWBAR();
#pragma unroll
      for (int i = 0; i < 8; ++i) sts16(c.L, UL + (i ^ (2 * (y & 3))) * 1024 + lane * 16, u[i]);
      asm volatile("" ::: "memory");
      *GO = NSEG;
    }
    TICK_DUMP(CW + 256);
  } else if (w < 3) {
    const rsrc_t ro = mkrs(p.out, 2048 * 2048 * 2);
    auto tokens = [&](auto QT) {
      constexpr int qt = QT.value, NX = qt ? 32 : 16;
      RAWBAR();
      for (int s = 0; s < NSEG; ++s) {
        const int tb = R_PT + (s & 7) * PT_SLOT + para * 8192;
        int ua[4], za[4], xb[4], mb[4];
#pragma unroll
        for (int i = 0; i < 4; ++i) {
          ua[i] = opq(UL + (i ^ rot) * 2048 + lane * 16);
          za[i] = opq(ZR + (s & 3) * 32768 + (i ^ rot) * 8192 + (qt ? lane * 16 : (g >> 1) * 1024 + (g & 1) * 512 + cc * 16));
          xb[i] = tb + amap(qt * 16384 + (i ^ rot) * (qt ? 8192 : 4096), 0);
          mb[i] = tb + amap(49152 + qt * 8192, 0) + (i ^ rot) * 2048;
        }
        SCHED();
        RAWBAR();
        if (TOK_WAIT) __builtin_amdgcn_s_sleep(TOK_WAIT);
        TICK(0);
        f32x4 a;
#pragma unroll 1
        for (;;) {
          asm volatile("" ::: "memory");
          v4u pt[NX + 8];
#pragma unroll
          for (int i = 0; i < NX; ++i) pt[i] = ld16s(c.rs, (i & 3) * 1024 + lane * 16, qt ? xb[i >> 3] + ((i >> 2) & 1) * 4096 : xb[i >> 2]);
#pragma unroll
          for (int i = 0; i < 8; ++i) pt[NX + i] = ld16s(c.rs, (i & 1) * 1024 + lane * 16, mb[i >> 1]);
          SCHED();
          TICK(1);
#pragma unroll 1
          while (__builtin_amdgcn_readfirstlane(*GO) < (unsigned)s + 1u) { if (spend(c)) break; }
          asm volatile("" ::: "memory");
          f32x4 d[2] = {f32x4{0.f, 0.f, 0.f, 0.f}, f32x4{0.f, 0.f, 0.f, 0.f}};
          v4u fa[8];
          auto frag = [&](int i) {
            if (i >= NX) fa[i & 7] = lds16(c.L, ua[(i - NX) >> 1] + (i & 1) * 1024);
            else if (qt) fa[i & 7] = lds16(c.L, za[i >> 3] + (i & 7) * 1024);
            else {
              const v2u lo = *(LAS v2u*)(c.L + za[i >> 2] + (i & 3) * 2048), hi = *(LAS v2u*)(c.L + za[i >> 2] + (i & 3) * 2048 + 256);
              fa[i & 7] = v4u{lo.x, lo.y, hi.x, hi.y};
            }
          };
#pragma unroll
          for (int i = 0; i < 7; ++i) frag(i);
          SCHED();
#pragma unroll
          for (int i = 0; i < NX + 8; ++i) {
            if (i + 7 < NX + 8) frag(i + 7);
            d[i & 1] = mfma16(fa[i & 7], pt[i], d[i & 1]);
            SCHED();
          }
          a = d[0] + d[1];
          if (!__any(a[0] != a[0])) break;
          unsigned t = 0;
#pragma unroll
          for (int i = 0; i < NX + 8; ++i) t = umax(t, top(pt[i]));
          if (allok(t) || spend(c)) break;
        }
        __builtin_amdgcn_raw_buffer_store_b64(v2u{pk(a[0], a[1]), pk(a[2], a[3])}, ro, (16 * qt + cc) * 4096 + (16 * y + 4 * g) * 2, s * 32 * 4096, OUT_AUX);
        TICK(2);
        TICK_SUM(3);
      }
    };
    if (w == 1) tokens(IC<0>{}); else tokens(IC<1>{});
  } else {
    unsigned seenZ = NOFLAG;
    const rsrc_t rc = mkrs((const void*)c.ctrl, CTRL_BYTES);
    const int fo = lane < 32 ? lane * 128 : 1 << 30;
    __builtin_amdgcn_s_setprio(3);
    auto least = [&](unsigned v) {
      v |= lane < 32 ? 0u : ~0u;
#pragma unroll
      for (int o = 1; o < 32; o <<= 1) v = min(v, (unsigned)__shfl_xor((int)v, o));
      seenZ = umax(seenZ, __builtin_amdgcn_readfirstlane(v));
    };
    auto await = [&](unsigned need) {
#pragma unroll 1
      while (seenZ < need) {
        asm volatile("" ::: "memory");
        least(__builtin_amdgcn_raw_buffer_load_b32(rc, fo, C_FLAG + (F_Z + (y >> 4) * 32) * 128, AUX_SC1));
        if (seenZ >= need || spend(c)) break;
        __builtin_amdgcn_s_sleep(1);
      }
      __builtin_amdgcn_fence(__ATOMIC_ACQUIRE, "wavefront");
    };
    for (int n = 0; n < NSEG; ++n) {
      TICK(0);
      await(n + 1);
      if (Z_WAIT) __builtin_amdgcn_s_sleep(Z_WAIT);
      TICK(1);
      v4u z[32];
      int za[8];
#pragma unroll
      for (int i = 0; i < 8; ++i) za[i] = opq(ZR + (n & 3) * 32768 + (i ^ z_mix(y)) * 1024 + lane * 16);
      unsigned look;
#pragma unroll
      for (int i = 0; i < 32 + Z_WIN; ++i) {
        if (i < 32) z[i] = ld16z(c.rs, (i & 3) * 1024 + lane * 16, R_Z + (n & (ZRING - 1)) * Z_SLOT + y * 65536 + ((para ^ (y >> 5)) & 1) * 8192 + amap((i >> 2) * 4096, 0));
        if (i == 32) { look = __builtin_amdgcn_raw_buffer_load_b32(rc, seenZ < (unsigned)n + 2u ? fo : 1 << 30, C_FLAG + (F_Z + (y >> 4) * 32) * 128, AUX_SC1); TICK(4); }
        SCHED();
        if (i >= Z_WIN) sts16(c.L, za[(i - Z_WIN) & 7] + ((i - Z_WIN) >> 3) * 8192, z[i - Z_WIN]);
        else asm volatile("s_nop " STR(Z_GAP));
        if (i == 30 + Z_WIN) { TICK(3); }
        SCHED();
      }
      least(look);
      if (n == 0) await(PRIME);
      TICK(2);
      { const int s = n; (void)s; TICK_SUM(3); }
      BAR();
    }
    RAWBAR();
  }
  TICK_OUT();
  if (c.budget == 0u) __hip_atomic_store(c.ctrl + C_TMO / 4, 1u, RLX_AGENT);
}

#define MF1A(d, a, b) asm volatile(MF16 "%0, %1, %2, %0" : "+v"(d) : "a"(a), "v"(b))
#define MF32A(d, a, b) asm volatile("v_mfma_f32_32x32x16_bf16 %0, %1, %2, %0" : "+v"(d) : "a"(a), "v"(b))
#define MF32AZ(d, a, b) asm volatile("v_mfma_f32_32x32x16_bf16 %0, %1, %2, 0" : "=&v"(d) : "a"(a), "v"(b))
#define MF1B(d, a, b) asm volatile(MF16 "%0, %1, %2, %0" : "+v"(d) : "v"(a), "a"(b))
#define MF1BZ(d, a, b) asm volatile(MF16 "%0, %1, %2, 0" : "=&v"(d) : "v"(a), "a"(b))
#define MF1AZ(d, a, b) asm volatile(MF16 "%0, %1, %2, 0" : "=&v"(d) : "a"(a), "v"(b))
DEV void g_role(const Params& p, Ctx& c, int mat, int h, int gi) {
  constexpr int EX = 131072;
  c.budget = SPIN_CAP;
  const int w = c.w, lane = c.lane = fresh_lane(), g = lane >> 4, cc = lane & 15;
  unsigned look = claim_look(c);
  const int flip = FLIP && mat < 2 ? ((h >> 4) & 1) ^ 1 : 0;
  unsigned t[7];
  {
    auto pull = [&](const u16* q, int bytes, int v, int so) { return __builtin_amdgcn_raw_buffer_load_b32(mkrs(q, bytes), v, so, B_AUX); };
    const int all = mat < HELPERS ? 8 * MB : 0;
    t[6] = pull(p.Wv, SLOW_V * all, slow_line(p.Wv, h, (h & 7) + 8 * w, lane) ^ (mat * 128), 0);
    t[0] = __builtin_amdgcn_raw_buffer_load_b32(mkrs(p.x, all / 2), g * 65536 + (((h & 7) * 4 + w) * 16 + cc) * 128, mat * 4 * 65536, 0);
#pragma unroll
    for (int i = 0; i < 3; ++i) t[1 + i] = pull(p.Wv, HELP_V * all, (256 * w + 4 * (h & 7) + g) * 8192 + (h >> 3) * 2048 + cc * 128, (2 + 2 * i + mat) * 32 * 8192);
    SCHED();
    if (mat < 2) ask(c, look);
    SCHED();
#pragma unroll
    for (int i = 0; i < 2; ++i) t[4 + i] = pull(p.Wo, HELP_O * all, (h * 128 + (lane >> 1)) * 2048 + w * 512 + 256 + (lane & 1) * 128, (2 * i + mat) * 32 * 2048);
    SCHED();
    if (mat == 0) claim(c, 0);
    hold(c, mat == 0 ? GQ_AT : mat == 1 ? GK_AT : 0);
  }
  const rsrc_t rx = mkrs(p.x, 2048 * 2048);
  const int xs = w * 16384 + ((lane * 16) ^ ((w & 1) << 7));

  v32u wa[8];
  {
    const rsrc_t rw = mkrs(mat == 0 ? p.Wq : mat == 1 ? p.Wk : p.Wv, 1024 * 4096 * 2);
    const int vo0 = opq((256 * w + 128 * flip + 8 * g) * 8192 + (h * 128 + 8 * cc) * 2), vo1 = opq(vo0 ^ (128 * 8192));
    v4u row[8][8];
    auto request = [&](int u) {
#pragma unroll
      for (int r = 0; r < 8; ++r) {
        row[u][r] = ld16w<B_AUX>(rw, (u & 4) ? vo1 : vo0, (32 * (u & 3) + r) * 8192);
        if ((r & 3) == 3) { const int n = 2 * u + (r >> 2); dma16(rx, c.L + w * 16384 + n * 1024, (xs ^ ((n >> 1) << 4)) + n * 1024); }
      }
    };
    const unsigned look0 = claim_look(c);
    bool claimed = false;
    request(0);
    SCHED();
    if (mat == 2) ask(c, look);
    SCHED();
    request(1); request(2);
    if (G_DEEP) request(3);
    SCHED();
    const int sw_ = EX + w * 8192 + g * 256;
    const int sr0 = opq(EX + (cc & 3) * 1024 + g * 256 + ((4 * w + (cc >> 2) + 4 * cc) & 15) * 16), sr1 = sr0 + 4096;
#pragma unroll
    for (int u = 0; u < 8; ++u) {
#pragma unroll
      for (int i = 0; i < 8; ++i) {
        v4u f;
#pragma unroll
        for (int r = 0; r < 4; ++r) f[r] = prm(row[u][2 * r + 1][i >> 1], row[u][2 * r][i >> 1], i & 1);
        sts16(c.L, sw_ + i * 1024 + ((cc + 4 * i) & 15) * 16, f);
      }
      SCHED();
      if (u == 0) asm volatile("" :: "v"(t[0]), "v"(t[1]), "v"(t[2]), "v"(t[3]), "v"(t[4]), "v"(t[5]), "v"(t[6]));
      if (u == 0) claimed = mat == 0 || claim_try(c, look0);
      if (u == 4 - G_DEEP) look = claim_look(c);
      if (u + 3 + G_DEEP < 8) request(u + 3 + G_DEEP);
      if (u == 7 && !claimed && !claim_try(c, look)) claim(c, 0);
      BAR();
      v8u in[4];
#pragma unroll
      for (int q = 0; q < 4; ++q) in[q] = cat(lds16(c.L, sr0 + q * 8192), lds16(c.L, sr1 + q * 8192));
      SCHED();
#pragma unroll
      for (int q = 0; q < 4; ++q) {
        v8u f = in[q];
#define M_(r) asm volatile("" : "+" r(f))
        AT_ACC(M_, 8 * q + u);
#undef M_
#pragma unroll
        for (int r = 0; r < 8; ++r) wa[2 * q + (u >> 2)][8 * (u & 3) + r] = f[r];
      }
      BAR();
    }
  }
  STAMP(0);
  const int xpar = vote_a(wait_vote(c)) ^ (mat >> 1);
  TICK_DECL;
  asm volatile(G_LOOP
      :: [xo] "v"(cc * 2048 + flip * 256 + ((g ^ cc) << 4)), [xs] "v"(xs), [so] "v"((4 * w + g) * 512 + cc * 16), [fo] "v"((w | lane) ? 1 << 30 : 0),
         [rs] "s"(c.rs), [rx] "s"(rx), [rc] "s"(mkrs((const void*)c.ctrl, CTRL_BYTES)), [fl] "s"(C_FLAG + (mat * 32 + h) * 128), [xp] "s"(xpar),
         [ob] "s"((mat == 1 ? R_KX : R_QX) + h * 16384), [lw] "s"(w * 16384), GTRACE ACC_IN(wa)
      : CL_S, CL_16_32, CL_32_100);
}

DEV void zc_role(const Params& p, Ctx& c, int h) {
  c.budget = SPIN_CAP;
  const int w = c.w, lane = c.lane = fresh_lane(), g = lane >> 4, cc = lane & 15, h2 = lane >> 5, r32 = lane & 31;
  v32u wa[8];
  const unsigned first = claim_look(c);
  {
    const rsrc_t rw = mkrs(p.Wo, 4096 * 1024 * 2);
    const int vo = (h * 128 + 8 * g) * 2048 + (256 * w + 8 * cc) * 2;
    v4u row[8][8];
    auto request = [&](int u) {
#pragma unroll
      for (int r = 0; r < 8; ++r) row[u][r] = ld16w<B_AUX>(rw, vo, (32 * (u & 3) + r) * 2048 + (u >> 2) * 256);
    };
    request(0);
    SCHED();
    ask(c, first);
    SCHED();
    request(1); request(2);
    unsigned look = 0;
    const int sw_ = w * 8192 + g * 256, sr_ = w * 8192 + (cc & 7) * 1024 + g * 256;
    v8u two[8];
#pragma unroll
    for (int u = 0; u < 8; ++u) {
#pragma unroll
      for (int i = 0; i < 8; ++i) {
        v4u f;
#pragma unroll
        for (int r = 0; r < 4; ++r) f[r] = prm(row[u][2 * r + 1][i >> 1], row[u][2 * r][i >> 1], i & 1);
        sts16(c.L, sw_ + i * 1024 + ((cc + 2 * i) & 15) * 16, f);
      }
      SCHED();
      if (u == 4) look = claim_look(c);
      if (u + 3 < 8) request(u + 3);
      if (u == 7 && !claim_try(c, look)) claim(c, 0);
      LGKM(0);
#pragma unroll
      for (int q = 0; q < 8; ++q) {
        const v4u o = lds16(c.L, sr_ + ((2 * q + (cc >> 3) + 2 * cc) & 15) * 16);
#pragma unroll
        for (int r = 0; r < 4; ++r) two[q][4 * (u & 1) + r] = o[r];
      }
      SCHED();
      if (u & 1) {
#pragma unroll
        for (int q = 0; q < 8; ++q) {
          const int n = 2 * (8 * (u >> 2) + q) + ((u >> 1) & 1);
#define M_(r) asm volatile("" : "+" r(two[q]))
          AT_ACC(M_, n);
#undef M_
#pragma unroll
          for (int r = 0; r < 8; ++r) wa[n >> 2][8 * (n & 3) + r] = two[q][r];
        }
      }
      LGKM(0);
      SCHED();
    }
  }
  STAMP(0);
  BAR();
  const int para = vote_a(wait_vote(c));
  const int ao = g * 512 + cc * 16, zo = 16 * w * 65536 + lane * 16 + ((para ^ (w >> 1)) & 1) * 8192;
  TICK_DECL;
  asm volatile(ZC_LOOP
      : [bd] "+s"(c.budget)
      : [ao] "v"(ao), [a1] "v"(ao + 4096), [z1] "v"(zo + amap((h ^ z_mix(16 * w)) * 1024, 0)), [z2] "v"(zo + 65536 + amap((h ^ z_mix(16 * w + 1)) * 1024, 0)), [fo] "v"(lane ? 1 << 30 : 0),
        [rs] "s"(c.rs), [rc] "s"(mkrs((const void*)c.ctrl, CTRL_BYTES)), [fv] "s"(C_FLAG + (F_V + h) * 128), [fs] "s"(C_FLAG + (F_S + h) * 128),
        [fz] "s"(C_FLAG + (F_Z + w * 32 + h) * 128), [xp] "s"(para ^ 1), [vb] "s"(R_QX + h * 16384), [vd] "s"(C_VERDICT + c.att * 128), [z0] "n"(ZC_PRIME), [zb] "n"(R_Z), [rg] "n"(ZRING - 2), [rm] "n"(ZRING - 1), GTRACE ACC_IN(wa)
      : CL_S, CL_16_32, CL_32_100);
  if (c.budget == 0u) __hip_atomic_store(c.ctrl + C_TMO / 4, 1u, RLX_AGENT);
}

DEV void conv_role(const Params& p, Ctx& c, int cg, int far) {
  constexpr int FS = 4160, PL = 1280, UO = 4 * FS, WS = UO + 8 * PL;
  const int w = c.w, lane = c.lane = fresh_lane(), tid = w * 64 + lane, g = lane >> 4, cc = lane & 15;
  const int d0 = 8 * cg + 2 * w;
  const int pg = ((far ^ (cg >> 5)) & 1) * 8192;
  auto crow = [](int ch) { return (ch >> 1) * 16384 + (ch & 1) * 4096; };
  const int grp = cg >> 3, de = grp * 64 + (tid & 7) * 8, t0 = (cg & 7) * 256 + (tid >> 3) * 8;
  auto arrive = [&](int n) {
    VMWAIT(0);
    BAR();
    if (w == 0 && lane == 0) __hip_atomic_fetch_add(c.ctrl + (C_GRP + n * 128) / 4, 1u, RLX_AGENT);
  };
  auto await = [&](int n) {
    if (w == 0) {
#pragma unroll 1
      while (__builtin_amdgcn_readfirstlane(__hip_atomic_load(c.ctrl + (C_GRP + n * 128) / 4, RLX_AGENT)) < 8u) { if (spend(c)) break; __builtin_amdgcn_s_sleep(1); }
    }
    BAR();
  };
  v4u fr[2][4], ur[2][4], qr[8];
  unsigned up[2][4];
  const int so = cc * 64 + g * 16;
  const int fw = opq(w * WS + 48 + lane * 16), uw = opq(w * WS + UO + g * PL + cc * 16);
  auto copies = [&](int ch) {
#pragma unroll
    for (int i = 0; i < 4; ++i) {
      const v4u f = fr[ch][i];
      sts16(c.L, fw + 3 * FS + i * 1024, f);
      sts8(c.L, fw + FS + i * 1024 + 8, v2u{f[0], f[1]}); sts8(c.L, fw + FS + i * 1024 + 16, v2u{f[2], f[3]});
#pragma unroll
      for (int r = 0; r < 2; ++r) {
        const int o = fw + 2 * r * FS + i * 1024 + 12 - 8 * r;
        *(LAS unsigned*)(c.L + o) = f[0]; sts8(c.L, o + 4, v2u{f[1], f[2]}); *(LAS unsigned*)(c.L + o + 12) = f[3];
      }
    }
  };
  CST(0);
  const int turns = (grp & 3) == 2 ? 2 : (grp & 3) == 3 ? 0 : 1;
#pragma unroll 1
  for (int n = turns - 1; n >= 0; --n) {
    const rsrc_t rk = mkrs(p.bk, 2048 * 2048), rv = mkrs(p.bv, 2048 * 2048);
    const int dn = de + 64 * n;
    v4u kr[11], vr[8], sh[4];
#pragma unroll
    for (int i = 0; i < 4; ++i) sh[i] = ld16c(mkrs(p.shift, 8192), dn * 8 + i * 16, 0);
#pragma unroll
    for (int i = 0; i < 11; ++i) kr[i] = ld16c(rk, (t0 + i - 3) * 2048 + dn * 2, 0);
#pragma unroll
    for (int i = 0; i < 8; ++i) vr[i] = ld16c(rv, (t0 + i) * 2048 + dn * 2, 0);
    SCHED();
    CST(1);
#pragma unroll
    for (int ch = 0; ch < 8; ++ch) {
      const unsigned sa = sh[ch >> 1][(ch & 1) * 2], sb = sh[ch >> 1][(ch & 1) * 2 + 1];
      const float s0 = blo(sa), s1 = bhi(sa), s2 = blo(sb), s3 = bhi(sb);
      float kk[11], o[8];
#pragma unroll
      for (int i = 0; i < 11; ++i) kk[i] = (ch & 1) ? bhi(kr[i][ch >> 1]) : blo(kr[i][ch >> 1]);
#pragma unroll
      for (int i = 0; i < 8; ++i) o[i] = fmaf(kk[i + 3], s0, fmaf(kk[i + 2], s1, fmaf(kk[i + 1], s2, kk[i] * s3))) * ((ch & 1) ? bhi(vr[i][ch >> 1]) : blo(vr[i][ch >> 1]));
      st16s(c.rs, dn * 8192 + t0 * 2, R_KVT + pg + crow(ch), v4u{pk(o[0], o[1]), pk(o[2], o[3]), pk(o[4], o[5]), pk(o[6], o[7])});
    }
    CST(3);
    arrive(2 * (grp + n));
  }
  const unsigned dd = __builtin_amdgcn_raw_buffer_load_b32(mkrs(p.ssmD, 2048), d0 * 2, 0, CONV_AUX);
#pragma unroll
  for (int i = 0; i < 8; ++i) fr[i >> 2][i & 3] = ld16c(mkrs(p.ssmk, 4 * MB), lane * 16 + (i & 3) * 1024, (d0 + (i >> 2)) * 4096);
  SCHED();
  CST(4);
  await(2 * grp);
  CST(5);
#pragma unroll
  for (int i = 0; i < 8; ++i) {
    const int b = R_KVT + pg + d0 * 8192 + (i >> 2) * 4096 + (i & 3) * 1024;
    ur[i >> 2][i & 3] = ld16s(c.rs, so, b);
    up[i >> 2][i & 3] = __builtin_amdgcn_raw_buffer_load_b32(c.rs, (i & 3) == 0 && lane == 0 ? 1 << 30 : so, b - 4, AUX_SC1);
  }
  SCHED();
  if (lane < 16) sts16(c.L, w * WS + (lane >> 2) * FS + (lane & 3) * 16, v4u{0, 0, 0, 0});
#pragma unroll
  for (int i = 0; i < 2; ++i) sts16(c.L, w * WS + UO + (4 * i + g) * PL + cc * 16, v4u{0, 0, 0, 0});
  STAMP(5);
  const int fa = opq(w * WS + (cc & 3) * FS + (3 + (cc >> 2) - g) * 16);
  sfor<2>([&](auto CH) __attribute__((always_inline)) {
    constexpr int ch = CH.value;
    SCHED();
    copies(ch);
    SCHED();
#pragma unroll
    for (int i = 0; i < 4; ++i) {
      const v4u a = ur[ch][i];
      v4u r0, r1;
#pragma unroll
      for (int r = 0; r < 4; ++r) {
        r0[r] = __builtin_amdgcn_alignbyte(a[3 - r], a[3 - r], 2);
        r1[r] = __builtin_amdgcn_perm(r < 3 ? a[2 - r] : up[ch][i], a[3 - r], 0x07060100u);
      }
      sts16(c.L, uw + (16 + 16 * i) * 16, r0);
      sts16(c.L, uw + 4 * PL + (16 + 16 * i) * 16, r1);
    }
    CST(6 + 2 * ch);
    f32x4 acc[4][2];
#pragma unroll
    for (int i = 0; i < 8; ++i) acc[i >> 1][i & 1] = f32x4{0.f, 0.f, 0.f, 0.f};
    v4u A[2][4], B[2][8];
    auto frag = [&](int d, int n) {
      constexpr int order[12] = {0, 4, 5, 6, 7, 8, 9, 10, 11, 1, 2, 3};
      const int x = order[n];
      if (x < 4) A[d & 1][x] = lds16(c.L, fa + (d + 16 * x) * 64);
      else B[d & 1][x - 4] = lds16(c.L, uw + ((x - 4) & 1) * 4 * PL + (16 + 16 * ((x - 4) >> 1) - d) * 16);
    };
    asm volatile("" ::: "memory");
#pragma unroll
    for (int n = 0; n < 12; ++n) frag(0, n);
    SCHED();
    sfor<16>([&](auto D) __attribute__((always_inline)) {
      constexpr int d = D.value;
      int n = 0;
#pragma unroll
      for (int k = 0; k < 4; ++k)
#pragma unroll
        for (int m = 0; m + k < 4; ++m)
#pragma unroll
          for (int e = 0; e < 2; ++e) {
            acc[m + k][e] = mfma16(A[d & 1][k], B[d & 1][2 * m + e], acc[m + k][e]);
            if (d < 15 && n < 12) frag(d + 1, n);
            if (ch == 0 && n == 14 && (d & 1)) qr[d >> 1] = ld16c(mkrs(p.bq, 4 * MB), (t0 + (d >> 1)) * 2048 + de * 2, 0);
            ++n;
            SCHED();
          }
    });
    CST(7 + 2 * ch);
    const float dv = ch ? bhi(dd) : blo(dd);
#pragma unroll
    for (int q = 0; q < 4; ++q) {
      v4u z;
#pragma unroll
      for (int i = 0; i < 4; ++i) {
        const unsigned yy = pk(acc[q][1][i], acc[q][0][i]), ud = pk(blo(ur[ch][q][i]) * dv, bhi(ur[ch][q][i]) * dv);
        z[i] = pk(blo(yy) + blo(ud), bhi(yy) + bhi(ud));
      }
      st16s(c.rs, so + q * 1024, R_YT + pg + d0 * 8192 + ch * 4096, z);
    }
  });
  STAMP(6);
  {
    const rsrc_t ro = mkrs(p.out, 2048 * 2048 * 2);
    v4u yv[8];
    arrive(2 * grp + 1);
    await(2 * grp + 1);
    CST(10);
#pragma unroll
    for (int ch = 0; ch < 8; ++ch) yv[ch] = ld16s(c.rs, de * 8192 + t0 * 2, R_YT + pg + crow(ch));
#pragma unroll
    for (int i = 0; i < 8; ++i) {
      float o[8];
#pragma unroll
      for (int ch = 0; ch < 8; ++ch) o[ch] = ((ch & 1) ? bhi(qr[i][ch >> 1]) : blo(qr[i][ch >> 1])) * ((i & 1) ? bhi(yv[ch][i >> 1]) : blo(yv[ch][i >> 1]));
      __builtin_amdgcn_raw_buffer_store_b128(v4u{pk(o[0], o[1]), pk(o[2], o[3]), pk(o[4], o[5]), pk(o[6], o[7])}, ro, (t0 + i) * 4096 + (1024 + de) * 2, 0, OUT_AUX2);
    }
  }
  if (c.budget == 0u) __hip_atomic_store(c.ctrl + C_TMO / 4, 1u, RLX_AGENT);
}

extern "C" __global__ void __launch_bounds__(256, 1) mk_mega(Params p) {
  extern __shared__ __attribute__((aligned(16))) unsigned char lds_raw[];
  Ctx c;
  c.ctrl = (gu32*)p.ctrl;
  c.rs = mkrs(p.data, R_END);
  c.L = (lds_t)lds_raw;
  c.w = __builtin_amdgcn_readfirstlane(threadIdx.x >> 6);
  c.lane = threadIdx.x & 63;
  c.t0 = (unsigned)__builtin_amdgcn_s_memrealtime();
  c.b = (int)(blockIdx.x & 255u);
  c.att = (int)(blockIdx.x >> 8);
  c.ask = 0;
  c.slot = (c.att ? (c.b & 7) : (int)((unsigned)__builtin_amdgcn_s_getreg(20 | (3 << 11)) & 7u)) * 32 + (c.b >> 3);
  const int x = c.slot >> 5, t = c.slot & 31;
  if (c.w == 1) {
    asm volatile("s_getpc_b64 s[60:61]\n1:\n\ts_sub_u32 s60, s60, 1b-mk_mega\n\ts_subb_u32 s61, s61, 0\n\ts_mov_b32 s62, mk_tail-mk_mega\n\ts_mov_b32 s63, 0x20000\n\t"
                 "buffer_load_dword v255, %0, s[60:63], 0 offen\n\ts_waitcnt vmcnt(0)" :: "v"((c.lane * 32 + t) * 128) : "s60", "s61", "s62", "s63", "v255", "scc", "memory");
  }
  if (c.b == SERVER && c.w == 3) serve(c);
  if (x < 4) {
    if (t < 8) qk_role(p, c, x * 8 + t);
    else if (t < 16) vo_role(p, c, x * 8 + t - 8);
    else y_role(p, c, x * 16 + t - 16);
  } else {
    if (t < 24) g_role(p, c, t >> 3, (x - 4) * 8 + (t & 7), t);
    else zc_role(p, c, (x - 4) * 8 + t - 24);
    STAMP(2);
    c.budget = SPIN_CAP;
    conv_role(p, c, (x - 4) * 32 + t, vote_a(wait_verdict(c)) ^ 1);
  }
  STAMP(3);
}

}

extern "C" void kernel_launch(void* const* d_in, const int* in_sizes, int n_in, void* d_out, int out_size, void* d_ws, size_t ws_size,
                              hipStream_t stream) {
  using namespace mk;
  (void)in_sizes; (void)n_in; (void)out_size;
  Params p;
  p.x = (const u16*)d_in[0]; p.mem0 = (const u16*)d_in[1];
  p.Wq = (const u16*)d_in[2]; p.Wk = (const u16*)d_in[3]; p.Wv = (const u16*)d_in[4]; p.Wo = (const u16*)d_in[5];
  p.bq = (const u16*)d_in[7]; p.bk = (const u16*)d_in[8]; p.bv = (const u16*)d_in[9];
  p.shift = (const u16*)d_in[10]; p.ssmk = (const u16*)d_in[11]; p.ssmD = (const u16*)d_in[12];
  p.out = (u16*)d_out;
  p.ctrl = (unsigned char*)d_ws;
  p.data = data_base(d_ws, ws_size);
  (void)hipFuncSetAttribute((const void*)mk_mega, hipFuncAttributeMaxDynamicSharedMemorySize, LDS_BYTES);
  hipLaunchKernelGGL(mk_mega, dim3(256), dim3(256), LDS_BYTES, stream, p);
}
```

```cpp
#include <hip/hip_runtime.h>
#include <hip/hip_bf16.h>

namespace mk {

#define GAS __attribute__((address_space(1)))
#define LAS __attribute__((address_space(3)))
#define DEV __device__ __forceinline__
#define RLX_AGENT __ATOMIC_RELAXED, __HIP_MEMORY_SCOPE_AGENT
typedef unsigned short u16;
typedef unsigned v2u __attribute__((ext_vector_type(2)));
typedef unsigned v4u __attribute__((ext_vector_type(4)));
typedef unsigned v8u __attribute__((ext_vector_type(8)));
typedef unsigned v16u __attribute__((ext_vector_type(16)));
typedef unsigned v32u __attribute__((ext_vector_type(32)));
typedef float f32x2 __attribute__((ext_vector_type(2)));
typedef float f32x4 __attribute__((ext_vector_type(4)));
typedef float f32x16 __attribute__((ext_vector_type(16)));
typedef __bf16 bf16x2 __attribute__((ext_vector_type(2)));
typedef __bf16 bf16x8 __attribute__((ext_vector_type(8)));
typedef __bf16 bf16x16 __attribute__((ext_vector_type(16)));
typedef GAS unsigned gu32;
typedef __amdgpu_buffer_rsrc_t rsrc_t;
typedef LAS unsigned char* lds_t;

#define XCNT(r) ""
#define UCNT ""
#define CL_T
#define XT(a, b) ""
#define LMARK ""
#define GSUM ""
#define ZSUM ""
#define GTRACE
#define MIXED 1
#define NOFLAG 0
#define TOK_KICK 1
#define ZRING 16
#define Z_AT 16
#define Z_GAP 4
#define STR_(x) #x
#define STR(x) STR_(x)
#define GQ_AT 300
#define GK_AT 0
#define PULL_AT 800
#define QK_AT 0
#define SERVER 194
#define SLOW_LEAD 100
#define PULL_DEPTH 1
#define PRIME 1
#define X_AT 1400
#define VO_AT 0
#define ZC_AT 0
#define CONV_AUX 2
#define W_AUX 2
#define ZC_PRIME 2
#define CODE_PULL 1
#define PROBES 2
#define Z_WIN 20
#define TOK_WAIT 0
#define Z_AUX 0
#define OUT_AUX 2
#define OUT_AUX2 2
#define URING 8
#define YH_AT 0
#define G_DEEP 1
#define AGAIN 0
#define PRE 0
#define YH 1
#define HELP_O 0
#define HELP_V 1
#define SLOW_V 1
#define K_FIRST 1
#define FLIP 1
#define YH_STEP 300
#define Z_WAIT 0
#define HELPERS 1
#define B_AUX 2
#define YH_AUX 0
constexpr int NSEG = 64;
constexpr unsigned SENT = 0xFFFFFFFFu;
constexpr int AUX_SC1 = 16, AUX_NT = 2;
constexpr int T_LAG = 61;
constexpr int G_AHEAD = 4;
constexpr unsigned SPIN_CAP = 1u << 21;
constexpr int LDS_BYTES = 163840;

constexpr int C_ECHO = 0, C_CLAIM = 1024, C_VERDICT = 3072, C_TMO = 3328, C_CONV = 3456, C_VOTE = 3840, C_FLAG = 4096, C_GRP = C_FLAG + 256 * 128, C_ASK = C_GRP + 32 * 128;
constexpr int F_Q = 0, F_K = 32, F_V = 64, F_S = 96, F_Z = 128;
constexpr int CTRL_BYTES = C_ASK + 256 * 64;
constexpr int MB = 1 << 20;
constexpr int R_U = 136 * MB;
constexpr int R_MEM = 2 * MB, MEM_SLOT = 32768;
constexpr int R_P = R_MEM + 4 * MEM_SLOT, P_SLOT = 65536;
constexpr int R_PT = 4 * MB, PT_SLOT = 131072;
constexpr int R_QX = 8 * MB, R_KX = 40 * MB, R_Z = 72 * MB, Z_SLOT = 4 * MB, R_KVT = R_KX, R_YT = R_KX + 8 * MB, R_END = (136 + URING) * MB;

constexpr size_t R_DIAG = 0;
#define TOP 1
static inline unsigned char* data_base(void* ws, size_t size) {
  return (unsigned char*)((TOP ? (size_t)ws + size - R_END - R_DIAG : (size_t)ws + CTRL_BYTES + 2 * MB - 1) & ~(size_t)(2 * MB - 1));
}
struct Params {
  const u16 *x, *mem0, *Wq, *Wk, *Wv, *Wo, *bq, *bk, *bv, *shift, *ssmk, *ssmD;
  u16* out;
  unsigned char* ctrl;
  unsigned char* data;
};

DEV rsrc_t mkrs(const void* p, unsigned bytes) { return __builtin_amdgcn_make_buffer_rsrc((void*)p, 0, bytes, 0x00020000); }
DEV v4u ld16s(rsrc_t rs, int v, int s) { return __builtin_amdgcn_raw_buffer_load_b128(rs, v, s, AUX_SC1); }
DEV v2u ld8s(rsrc_t rs, int v, int s) { return __builtin_amdgcn_raw_buffer_load_b64(rs, v, s, AUX_SC1); }
DEV v4u ld16(rsrc_t rs, int v, int s) { return __builtin_amdgcn_raw_buffer_load_b128(rs, v, s, 0); }
DEV v4u ld16z(rsrc_t rs, int v, int s) { return __builtin_amdgcn_raw_buffer_load_b128(rs, v, s, AUX_SC1 | Z_AUX); }
DEV v4u ld16c(rsrc_t rs, int v, int s) { return __builtin_amdgcn_raw_buffer_load_b128(rs, v, s, CONV_AUX); }
template <int A> DEV v4u ld16w(rsrc_t rs, int v, int s) { return __builtin_amdgcn_raw_buffer_load_b128(rs, v, s, A); }
DEV v2u ld8(rsrc_t rs, int v, int s) { return __builtin_amdgcn_raw_buffer_load_b64(rs, v, s, 0); }
DEV void dma16(rsrc_t rs, lds_t l, int v) {
#if __HIP_DEVICE_COMPILE__
  __builtin_amdgcn_raw_ptr_buffer_load_lds(rs, (LAS void*)l, 16, v, 0, 0, 0);
#endif
}
DEV void st16s(rsrc_t rs, int v, int s, v4u d) { __builtin_amdgcn_raw_buffer_store_b128(d, rs, v, s, AUX_SC1); }
DEV void st8s(rsrc_t rs, int v, int s, v2u d) { __builtin_amdgcn_raw_buffer_store_b64(d, rs, v, s, AUX_SC1); }
DEV void st4s(rsrc_t rs, int v, int s, unsigned d) { __builtin_amdgcn_raw_buffer_store_b32(d, rs, v, s, AUX_SC1); }
DEV int amap(int o, int par) { return ((o >> 13) << 14) | (par << 13) | (o & 8191); }
DEV int xblk(int s, int h, int par) { return (s * 32 + h) * 16384 + ((par ^ (s >> 2)) & 1) * 8192; }
DEV unsigned umax(unsigned a, unsigned b) { return a > b ? a : b; }
DEV unsigned top(v4u v) { return umax(umax(v.x, v.y), umax(v.z, v.w)); }
DEV unsigned pk(float a, float b) { bf16x2 v = {(__bf16)a, (__bf16)b}; return __builtin_bit_cast(unsigned, v); }
DEV unsigned pks(float a, float b) { unsigned u = pk(a, b); return u < SENT - 1u ? u : SENT - 1u; }
DEV float blo(unsigned u) { return __builtin_bit_cast(float, u << 16); }
DEV float bhi(unsigned u) { return __builtin_bit_cast(float, u & 0xFFFF0000u); }
DEV float rb(float a) { return blo(pk(a, a)); }
DEV unsigned fu(float f) { return __builtin_bit_cast(unsigned, f); }
DEV float uf(unsigned u) { return __builtin_bit_cast(float, u); }
DEV f32x4 mfma16(v4u a, v4u b, f32x4 c) {
  return __builtin_amdgcn_mfma_f32_16x16x32_bf16(__builtin_bit_cast(bf16x8, a), __builtin_bit_cast(bf16x8, b), c, 0, 0, 0);
}
DEV f32x16 mfma32(v4u a, v4u b, f32x16 c) {
  return __builtin_amdgcn_mfma_f32_32x32x16_bf16(__builtin_bit_cast(bf16x8, a), __builtin_bit_cast(bf16x8, b), c, 0, 0, 0);
}
DEV f32x4 smf(v4u a, v8u b, f32x4 c, int idx) {
  return __builtin_amdgcn_smfmac_f32_16x16x64_bf16(__builtin_bit_cast(bf16x8, a), __builtin_bit_cast(bf16x16, b), c, idx, 0, 0);
}
DEV v8u cat(v4u a, v4u b) { return __builtin_shufflevector(a, b, 0, 1, 2, 3, 4, 5, 6, 7); }
DEV v4u lo4(v8u a) { return __builtin_shufflevector(a, a, 0, 1, 2, 3); }
DEV v4u hi4(v8u a) { return __builtin_shufflevector(a, a, 4, 5, 6, 7); }
DEV v4u lds16(lds_t L, int o) { return *(LAS v4u*)(L + o); }
DEV void sts16(lds_t L, int o, v4u v) { *(LAS v4u*)(L + o) = v; }
DEV void sts8(lds_t L, int o, v2u v) { *(LAS v2u*)(L + o) = v; }
#define SCHED() __builtin_amdgcn_sched_barrier(0)
#define PIN_A(x) asm volatile("" : "+a"(x))
#define PIN_V(x) asm volatile("" : "+v"(x))
DEV int opq(int v) { asm volatile("" : "+v"(v)); return v; }
DEV int fresh_lane() {
  int l; asm volatile("v_mbcnt_lo_u32_b32 %0, -1, 0\n\tv_mbcnt_hi_u32_b32 %0, -1, %0" : "=v"(l)); return l;
}
DEV f32x4 fresh_zero() {
  f32x2 a, b;
  asm volatile("v_mov_b64 %0, 0" : "=v"(a));
  asm volatile("v_mov_b64 %0, 0" : "=v"(b));
  return __builtin_shufflevector(a, b, 0, 1, 2, 3);
}
DEV unsigned fresh_sent() { unsigned z; asm volatile("v_mov_b32 %0, -1" : "=v"(z)); return z; }
#define SMF "v_smfmac_f32_16x16x64_bf16 "
#define MF16 "v_mfma_f32_16x16x32_bf16 "
#define SMF1(cls, d, a, b) asm volatile(SMF "%0, %1, %2, %3" : "+v"(d) : "v"(a), cls(b), "v"(idx))
#define SMF1F(cls, lo, hi, d, a, b) asm volatile(SMF "v[" #lo ":" #hi "], %1, %2, %3" : "+{v[" #lo ":" #hi "]}"(d) : "v"(a), cls(b), "v"(idx))
#define AT_HOME(M, j) do { const int j_ = (j); if (j_ == 0) M("{v[32:39]}"); if (j_ == 1) M("{v[40:47]}"); if (j_ == 2) M("{v[48:55]}"); if (j_ == 3) M("{v[56:63]}"); if (j_ == 4) M("{v[64:71]}"); if (j_ == 5) M("{v[72:79]}"); if (j_ == 6) M("{v[80:87]}"); if (j_ == 7) M("{v[88:95]}"); if (j_ == 8) M("{v[96:103]}"); if (j_ == 9) M("{v[104:111]}"); if (j_ == 10) M("{v[112:119]}"); if (j_ == 11) M("{v[120:127]}"); if (j_ == 12) M("{v[128:135]}"); if (j_ == 13) M("{v[136:143]}"); if (j_ == 14) M("{v[144:151]}"); if (j_ == 15) M("{v[152:159]}"); } while (0)
#define MF1(d, a, b) asm volatile(MF16 "%0, %1, %2, %0" : "+v"(d) : "v"(a), "v"(b))
#define MF1Z(d, a, b) asm volatile(MF16 "%0, %1, %2, 0" : "=&v"(d) : "v"(a), "v"(b))
#define LGKM(n) asm volatile("s_waitcnt lgkmcnt(" #n ")" ::: "memory")
#define RAWBAR() do { __builtin_amdgcn_s_barrier(); asm volatile("" ::: "memory"); } while (0)
DEV void settle3(f32x4& s0, f32x4& s1, f32x4& s2) { asm volatile("s_nop 7" : "+v"(s0), "+v"(s1), "+v"(s2)); }
DEV void settle4(f32x4 (&d)[4]) { asm volatile("s_nop 7" : "+v"(d[0]), "+v"(d[1]), "+v"(d[2]), "+v"(d[3])); }
#define BAR() do { asm volatile("s_waitcnt lgkmcnt(0)" ::: "memory"); __builtin_amdgcn_s_barrier(); asm volatile("" ::: "memory"); } while (0)
#define VMWAIT(n) asm volatile("s_waitcnt vmcnt(" #n ")" ::: "memory")
DEV float add1(float a, float b) {
  float o; asm("v_add_f32 %0, %1, %2" : "=v"(o) : "v"(a), "v"(b)); return o;
}
DEV float xmax(float v) {
  auto r = __builtin_amdgcn_permlane32_swap(fu(v), fu(v), false, false); v = fmaxf(uf(r[0]), uf(r[1]));
  auto q = __builtin_amdgcn_permlane16_swap(fu(v), fu(v), false, false); return fmaxf(uf(q[0]), uf(q[1]));
}
DEV float xsum(float v) {
  auto r = __builtin_amdgcn_permlane32_swap(fu(v), fu(v), false, false); v = uf(r[0]) + uf(r[1]);
  auto q = __builtin_amdgcn_permlane16_swap(fu(v), fu(v), false, false); return uf(q[0]) + uf(q[1]);
}

DEV float ror8(float v) { return uf(__builtin_amdgcn_update_dpp(0u, fu(v), 0x128, 0xf, 0xf, true)); }
DEV float fmax2(float a, float b) { return __builtin_amdgcn_fmed3f(a, b, __builtin_inff()); }
DEV float fmax3(float a, float b, float c) { float r; asm("v_max3_f32 %0, %1, %2, %3" : "=v"(r) : "v"(a), "v"(b), "v"(c)); return r; }
DEV bool allok(unsigned t) { return __all(t != SENT); }

template <int N> struct IC { static constexpr int value = N; };
template <int N, class F> DEV void sfor(F&& f) { if constexpr (N > 0) { sfor<N - 1>(f); f(IC<N - 1>{}); } }
struct Ctx {
  gu32* ctrl;
  rsrc_t rs;
  lds_t L;
  int w, lane;
  int b, slot, att;
  unsigned ask;
  unsigned t0;
  unsigned budget;
};
DEV bool spend(Ctx& c) {
  const unsigned old = c.budget;
  asm volatile("s_cmp_lg_u32 %0, 0\n\ts_subb_u32 %0, %0, 0" : "+s"(c.budget) :: "scc");
  return old == 0u;
}
DEV unsigned wait_flag(Ctx& c, int flag, unsigned need) {
  gu32* f = c.ctrl + (C_FLAG + flag * 128) / 4;
  unsigned v;
#pragma unroll 1
  for (;;) {
    v = __builtin_amdgcn_readfirstlane(__hip_atomic_load(f, RLX_AGENT));
    if (v >= need || spend(c)) break;
    __builtin_amdgcn_s_sleep(2);
  }
  __builtin_amdgcn_fence(__ATOMIC_ACQUIRE, "wavefront");
  return v;
}
DEV void post_flag(Ctx& c, int flag, unsigned v, int off = 0) {
  st4s(mkrs((const void*)c.ctrl, CTRL_BYTES), (c.w | off | fresh_lane()) ? 1 << 30 : 0, C_FLAG + flag * 128, v);
}
constexpr unsigned CL_A = 1u << 16, CL_VOTED = 1u << 24, CL_FILLED = 1u << 28;
DEV unsigned claim_look(Ctx& c) { return __hip_atomic_load(c.ctrl + C_ECHO / 4 + c.b, RLX_AGENT); }
DEV void ask(Ctx& c, unsigned first) {
  c.ask = __builtin_amdgcn_readfirstlane(first) + 1u;
  st4s(mkrs((const void*)c.ctrl, CTRL_BYTES), (c.w | c.att | fresh_lane()) ? 1 << 30 : 0, C_ASK + c.b * 64, c.ask);
}
DEV void claim(Ctx& c, unsigned votes) {
  if (c.w != 0) return;
  if (c.att == 0) {
#pragma unroll 1
    while (__builtin_amdgcn_readfirstlane(__hip_atomic_load(c.ctrl + C_ECHO / 4 + c.b, RLX_AGENT)) != c.ask) {
      if (spend(c)) break;
      __builtin_amdgcn_s_sleep(2);
    }
  }
  if (fresh_lane() == 0) __hip_atomic_fetch_add(c.ctrl + (C_CLAIM + c.att * 1024) / 4 + c.slot, 1u + votes, RLX_AGENT);
}
DEV bool claim_try(Ctx& c, unsigned look) {
  const bool ok = c.att || __builtin_amdgcn_readfirstlane(look) == c.ask;
  if (ok && c.w == 0 && fresh_lane() == 0) __hip_atomic_fetch_add(c.ctrl + (C_CLAIM + c.att * 1024) / 4 + c.slot, 1u, RLX_AGENT);
  return ok;
}
DEV unsigned wait_verdict(Ctx& c) {
  unsigned v;
#pragma unroll 1
  for (;;) {
    v = __builtin_amdgcn_readfirstlane(__hip_atomic_load(c.ctrl + (C_VERDICT + c.att * 128) / 4, RLX_AGENT));
    if ((v & 1u) || spend(c)) break;
    __builtin_amdgcn_s_sleep(2);
  }
  __builtin_amdgcn_fence(__ATOMIC_ACQUIRE, "wavefront");
  if ((v & 3u) == 3u && c.att == 0) {
    asm volatile("s_waitcnt vmcnt(0) lgkmcnt(0)\n\ts_barrier\n\ts_mov_b64 s[0:1], %0\n\ts_mov_b32 s2, %1\n\tv_mov_b32 v0, %2\n\t"
                 "s_getpc_b64 s[4:5]\n1:\n\ts_sub_u32 s4, s4, 1b-mk_mega\n\ts_subb_u32 s5, s5, 0\n\ts_setpc_b64 s[4:5]"
                 :: "s"(__builtin_amdgcn_kernarg_segment_ptr()), "s"(c.b + 256), "v"(c.w * 64 + fresh_lane()) : "s0", "s1", "s2", "s4", "s5", "v0", "scc", "memory");
    __builtin_unreachable();
  }
  return v;
}
DEV unsigned wait_vote(Ctx& c) {
  unsigned v;
#pragma unroll 1
  for (;;) {
    v = __builtin_amdgcn_readfirstlane(__hip_atomic_load(c.ctrl + C_VOTE / 4, RLX_AGENT));
    if ((v & 1u) || spend(c)) break;
    __builtin_amdgcn_s_sleep(2);
  }
  return v;
}
DEV void serve(Ctx& c) {
  c.budget = SPIN_CAP;
  const int lane = fresh_lane();
  const rsrc_t rc = mkrs((const void*)c.ctrl, CTRL_BYTES);
  if (c.att == 0) {
#pragma unroll
    for (int i = C_CLAIM / 1024; i < C_ASK / 1024; ++i) st16s(rc, lane * 16, i * 1024, v4u{0, 0, 0, 0});
    VMWAIT(0);
  }
  unsigned vote = c.att ? wait_vote(c) : 0u;
  bool echo = c.att == 0, first = true;
  v4u last = {0u, 0u, 0u, 0u};
#pragma unroll 1
  for (;;) {
    asm volatile("" ::: "memory");
    const v4u cl = ld16s(rc, lane * 16, C_CLAIM + c.att * 1024);
    if (echo) {
      v4u e;
#pragma unroll
      for (int i = 0; i < 4; ++i) e[i] = __builtin_amdgcn_raw_buffer_load_b32(rc, (lane * 4 + i) * 64, C_ASK, AUX_SC1);
      if (first || __any(e[0] != last[0] || e[1] != last[1] || e[2] != last[2] || e[3] != last[3])) st16s(rc, lane * 16, C_ECHO, e);
      last = e; first = false;
    }
    unsigned n = 0, one = 1;
    int va = 0, nv = 0, nf = 0;
#pragma unroll
    for (int i = 0; i < 4; ++i) {
      n += cl[i] & 0xFFFFu; one &= (cl[i] & 0xFFFFu) == 1u;
      va += __popcll(__ballot(cl[i] & CL_A)); nv += __popcll(__ballot(cl[i] & CL_VOTED)); nf += __popcll(__ballot(cl[i] & CL_FILLED));
    }
#pragma unroll
    for (int o = 1; o < 64; o <<= 1) n += (unsigned)__shfl_xor((int)n, o);
    const bool all = __builtin_amdgcn_readfirstlane(n) >= 256u, good = __all(one);
    echo = echo && !all;
    if (!vote && (all || max(va, nv - va) > 20)) {
      vote = 2 * va > nv ? 5u : 9u;
      if (lane == 0) __hip_atomic_store(c.ctrl + C_VOTE / 4, vote, RLX_AGENT);
    }
    if (all && (!good || nf >= 64)) {
      if (lane == 0) __hip_atomic_store(c.ctrl + (C_VERDICT + c.att * 128) / 4, vote | (good ? 0u : 2u), RLX_AGENT);
      break;
    }
    if (spend(c)) break;
  }
}
DEV int vote_a(unsigned v) { return (int)((v >> 2) & 1u); }
DEV int vote_b(unsigned v) { return (int)((v >> 3) & 1u); }

#define STAMP(slot) do {} while (0)
#define TICK_DECL
#define LOG(i, s, v) do {} while (0)
#define LOGS(i, s) do {} while (0)
#define CST(i) do {} while (0)
#define LITE(s) do {} while (0)
#define TICK(i) do {} while (0)
#define TICK_SUM(n) do {} while (0)
#define TICK_LDS(b) do {} while (0)
#define TICK_DUMP(b) do {} while (0)
#define TICK_OUT() do {} while (0)

DEV constexpr int krow(int rho8, int i) { return 32 * (rho8 >> 2) + 8 * (rho8 & 1) + 2 * ((rho8 >> 1) & 1) + i; }
DEV int krow_lane(int g) { return 16 * (g >> 1) + 4 * (g & 1); }
DEV unsigned prm(unsigned hi, unsigned lo, int odd) { return __builtin_amdgcn_perm(hi, lo, odd ? 0x07060302u : 0x05040100u); }

#define AT_ACC(M, n) do { const int n_ = (n); if (n_ == 0) M("{a[0:7]}"); if (n_ == 1) M("{a[8:15]}"); if (n_ == 2) M("{a[16:23]}"); if (n_ == 3) M("{a[24:31]}"); if (n_ == 4) M("{a[32:39]}"); if (n_ == 5) M("{a[40:47]}"); if (n_ == 6) M("{a[48:55]}"); if (n_ == 7) M("{a[56:63]}"); if (n_ == 8) M("{a[64:71]}"); if (n_ == 9) M("{a[72:79]}"); if (n_ == 10) M("{a[80:87]}"); if (n_ == 11) M("{a[88:95]}"); if (n_ == 12) M("{a[96:103]}"); if (n_ == 13) M("{a[104:111]}"); if (n_ == 14) M("{a[112:119]}"); if (n_ == 15) M("{a[120:127]}"); if (n_ == 16) M("{a[128:135]}"); if (n_ == 17) M("{a[136:143]}"); if (n_ == 18) M("{a[144:151]}"); if (n_ == 19) M("{a[152:159]}"); if (n_ == 20) M("{a[160:167]}"); if (n_ == 21) M("{a[168:175]}"); if (n_ == 22) M("{a[176:183]}"); if (n_ == 23) M("{a[184:191]}"); if (n_ == 24) M("{a[192:199]}"); if (n_ == 25) M("{a[200:207]}"); if (n_ == 26) M("{a[208:215]}"); if (n_ == 27) M("{a[216:223]}"); if (n_ == 28) M("{a[224:231]}"); if (n_ == 29) M("{a[232:239]}"); if (n_ == 30) M("{a[240:247]}"); if (n_ == 31) M("{a[248:255]}"); } while (0)
#define PIN_ACC0(x) asm volatile("" :: "{a[0:7]}"(x[0]), "{a[8:15]}"(x[1]), "{a[16:23]}"(x[2]), "{a[24:31]}"(x[3]), "{a[32:39]}"(x[4]), "{a[40:47]}"(x[5]), "{a[48:55]}"(x[6]), "{a[56:63]}"(x[7]), "{a[64:71]}"(x[8]), "{a[72:79]}"(x[9]), "{a[80:87]}"(x[10]), "{a[88:95]}"(x[11]), "{a[96:103]}"(x[12]), "{a[104:111]}"(x[13]), "{a[112:119]}"(x[14]), "{a[120:127]}"(x[15]))
#define PIN_ACC1(x) asm volatile("" :: "{a[128:135]}"(x[16]), "{a[136:143]}"(x[17]), "{a[144:151]}"(x[18]), "{a[152:159]}"(x[19]), "{a[160:167]}"(x[20]), "{a[168:175]}"(x[21]), "{a[176:183]}"(x[22]), "{a[184:191]}"(x[23]), "{a[192:199]}"(x[24]), "{a[200:207]}"(x[25]), "{a[208:215]}"(x[26]), "{a[216:223]}"(x[27]), "{a[224:231]}"(x[28]), "{a[232:239]}"(x[29]), "{a[240:247]}"(x[30]), "{a[248:255]}"(x[31]))
#define PIN_HOME(x) asm volatile("" :: "{v[32:39]}"(x[0]), "{v[40:47]}"(x[1]), "{v[48:55]}"(x[2]), "{v[56:63]}"(x[3]), "{v[64:71]}"(x[4]), "{v[72:79]}"(x[5]), "{v[80:87]}"(x[6]), "{v[88:95]}"(x[7]), "{v[96:103]}"(x[8]), "{v[104:111]}"(x[9]), "{v[112:119]}"(x[10]), "{v[120:127]}"(x[11]), "{v[128:135]}"(x[12]), "{v[136:143]}"(x[13]), "{v[144:151]}"(x[14]), "{v[152:159]}"(x[15]))
#define CL_32_100 "v32", "v33", "v34", "v35", "v36", "v37", "v38", "v39", "v40", "v41", "v42", "v43", "v44", "v45", "v46", "v47", "v48", "v49", "v50", "v51", "v52", "v53", "v54", "v55", "v56", "v57", "v58", "v59", "v60", "v61", "v62", "v63", "v64", "v65", "v66", "v67", "v68", "v69", "v70", "v71", "v72", "v73", "v74", "v75", "v76", "v77", "v78", "v79", "v80", "v81", "v82", "v83", "v84", "v85", "v86", "v87", "v88", "v89", "v90", "v91", "v92", "v93", "v94", "v95", "v96", "v97", "v98", "v99", "v100", "v101", "v102", "v103", "v104", "v105", "v106", "v107", "v108", "v109", "v110", "v111", "v112", "v113", "v114", "v115", "v116", "v117", "v118", "v119", "v120", "v121", "v122", "v123", "v124", "v125", "v126", "v127", "v128", "v129", "v130", "v131"
#define ACC_IN(x) "{a[0:31]}"(x[0]), "{a[32:63]}"(x[1]), "{a[64:95]}"(x[2]), "{a[96:127]}"(x[3]), "{a[128:159]}"(x[4]), "{a[160:191]}"(x[5]), "{a[192:223]}"(x[6]), "{a[224:255]}"(x[7])
#define CL_16_32 "v16", "v17", "v18", "v19", "v20", "v21", "v22", "v23", "v24", "v25", "v26", "v27", "v28", "v29", "v30", "v31"
#define CL_120_128 "v120", "v121", "v122", "v123", "v124", "v125", "v126", "v127", "v128", "v129", "v130", "v131", "v132", "v133", "v134", "v135", "v136", "v137", "v138", "v139"
#define CL_160_200 "v160", "v161", "v162", "v163", "v164", "v165", "v166", "v167", "v168", "v169", "v170", "v171", "v172", "v173", "v174", "v175", "v176", "v177", "v178", "v179", "v180", "v181", "v182", "v183", "v184", "v185", "v186", "v187", "v188", "v189", "v190", "v191", "v192", "v193", "v194", "v195", "v196", "v197", "v198", "v199"
#define CL_176_200 "v176", "v177", "v178", "v179", "v180", "v181", "v182", "v183", "v184", "v185", "v186", "v187", "v188", "v189", "v190", "v191", "v192", "v193", "v194", "v195", "v196", "v197", "v198", "v199"
#define CL_200_216 "v200", "v201", "v202", "v203", "v204", "v205", "v206", "v207", "v208", "v209", "v210", "v211", "v212", "v213", "v214", "v215"
#define CL_216_256 "v216", "v217", "v218", "v219", "v220", "v221", "v222", "v223", "v224", "v225", "v226", "v227", "v228", "v229", "v230", "v231", "v232", "v233", "v234", "v235", "v236", "v237", "v238", "v239", "v240", "v241", "v242", "v243", "v244", "v245", "v246", "v247", "v248", "v249", "v250", "v251", "v252", "v253", "v254", "v255"
#define CL_S "memory", "vcc", "scc", "s60", "s61", "s62", "s63", "s64", "s65", "s66", "s67", "s68" CL_T
#define TIMED_WAIT \
    "s_getpc_b64 s[62:63]\n\t" "1:\n\t" "s_memtime s[64:65]\n\t" "s_waitcnt lgkmcnt(0)\n\t" "s_sub_u32 s61, %0, s64\n\t" "s_cmp_gt_i32 s61, 124\n\t" "s_cbranch_scc1 1b\n\t" \
    "s_max_i32 s61, s61, 0\n\t" "s_and_b32 s61, s61, 124\n\t" "s_sub_u32 s61, 2f-1b, s61\n\t" "s_add_u32 s62, s62, s61\n\t" "s_addc_u32 s63, s63, 0\n\t" \
    "s_setpc_b64 s[62:63]\n\t" ".rept 31\n\t" "s_nop 0\n\t" ".endr\n\t" "2:\n\t"
#define QK_STEP0 \
    ".p2align 6\n\t" "v_xor_b32 v216, 0x400, %[mo]\n\t" "s_xor_b32 s60, %[sb], 0x800\n\t" "s_mov_b32 s66, 0x3e0293ee\n\t" "s_mov_b32 s67, 0x3e0293ee\n\t" XT(80, 81) \
    "s_add_u32 s61, %[kb], 0x1000\n\t" "buffer_load_dwordx4 v[16:19], %[kx], %[rs], %[kb] offen offset:0 sc1\n\t" \
    "buffer_load_dwordx4 v[20:23], %[kx], %[rs], %[kb] offen offset:2048 sc1\n\t" "buffer_load_dwordx4 v[24:27], %[kx], %[rs], s61 offen offset:0 sc1\n\t" \
    "buffer_load_dwordx4 v[28:31], %[kx], %[rs], s61 offen offset:2048 sc1\n\t" "buffer_load_dwordx4 v[200:203], %[kx], %[rs], %[kb] offen offset:256 sc1\n\t" \
    "buffer_load_dwordx4 v[204:207], %[kx], %[rs], %[kb] offen offset:2304 sc1\n\t" "buffer_load_dwordx4 v[208:211], %[kx], %[rs], s61 offen offset:256 sc1\n\t" \
    "buffer_load_dwordx4 v[212:215], %[kx], %[rs], s61 offen offset:2304 sc1\n\t" "s_barrier\n\t" "s_waitcnt vmcnt(7)\n\t" "ds_write_b128 %[kx], v[16:19] offset:0\n\t" \
    "s_waitcnt vmcnt(6)\n\t" "ds_write_b128 %[kx], v[20:23] offset:2048\n\t" "s_waitcnt vmcnt(5)\n\t" "ds_write_b128 %[kx], v[24:27] offset:4096\n\t" "s_waitcnt vmcnt(4)\n\t" \
    "ds_write_b128 %[kx], v[28:31] offset:6144\n\t" "s_waitcnt vmcnt(3)\n\t" "ds_write_b128 %[kx], v[200:203] offset:256\n\t" "s_waitcnt vmcnt(2)\n\t" \
    "ds_write_b128 %[kx], v[204:207] offset:2304\n\t" "s_waitcnt vmcnt(1)\n\t" "ds_write_b128 %[kx], v[208:211] offset:4352\n\t" "s_waitcnt vmcnt(0)\n\t" \
    "ds_write_b128 %[kx], v[212:215] offset:6400\n\t" "s_waitcnt lgkmcnt(0)\n\t" "v_mov_b64 v[208:209], 0\n\t" "v_mov_b64 v[210:211], 0\n\t" "v_mov_b64 v[212:213], 0\n\t" \
    "v_mov_b64 v[214:215], 0\n\t" XT(98, 99) "s_barrier\n\t" XT(82, 83) "buffer_load_dwordx4 v[240:243], %[mo], %[rs], %[sb] offen sc1\n\t" "v_mov_b64 v[224:225], 0\n\t" \
    "v_mov_b64 v[226:227], 0\n\t" "v_mov_b64 v[228:229], 0\n\t" "v_mov_b64 v[230:231], 0\n\t" "v_mov_b64 v[232:233], 0\n\t" "v_mov_b64 v[234:235], 0\n\t" \
    "v_mov_b64 v[236:237], 0\n\t" "v_mov_b64 v[238:239], 0\n\t" "buffer_load_dwordx4 v[244:247], v216, %[rs], %[sb] offen sc1\n\t" \
    "ds_read_b128 v[160:163], %[l0] offset:0\n\t" "ds_read_b128 v[164:167], %[l0] offset:1024\n\t" "ds_read_b128 v[168:171], %[l0] offset:2048\n\t" \
    "ds_read_b128 v[172:175], %[l0] offset:3072\n\t" "s_nop 3\n\t" "buffer_load_dwordx4 v[248:251], %[mo], %[rs], s60 offen sc1\n\t" \
    "ds_read_b128 v[176:179], %[l0] offset:4096\n\t" "ds_read_b128 v[180:183], %[l0] offset:5120\n\t" "ds_read_b128 v[184:187], %[l0] offset:6144\n\t" \
    "ds_read_b128 v[188:191], %[l0] offset:7168\n\t" "s_nop 3\n\t" "buffer_load_dwordx4 v[252:255], v216, %[rs], s60 offen sc1\n\t" "v_mov_b32 v219, 160512\n\t" \
    "v_mov_b64 v[220:221], -1\n\t" "v_mov_b64 v[222:223], -1\n\t" "v_mov_b32 v217, 0\n\t" "buffer_store_dwordx2 v[220:221], %[px], %[rs], %[pr] offen sc1\n\t" \
    "buffer_store_dword v220, %[pm], %[rs], %[pr] offen sc1\n\t" "global_load_dword v217, v217, %[fq] sc1\n\t" "10:\n\t" "s_waitcnt vmcnt(6)\n\t" XT(84, 85) \
    "v_smfmac_f32_16x16x64_bf16 v[224:227], v[240:243], a[0:7], %[ix]\n\t" "ds_write_b128 %[l1], v[240:243] offset:0\n\t" \
    "v_smfmac_f32_16x16x64_bf16 v[228:231], v[240:243], a[8:15], %[ix]\n\t" "v_smfmac_f32_16x16x64_bf16 v[232:235], v[240:243], v[32:39], %[ix]\n\t" \
    "s_waitcnt lgkmcnt(7)\n\t" "v_smfmac_f32_16x16x64_bf16 v[236:239], v[240:243], v[160:167], %[ix]\n\t" "ds_read_b128 v[160:163], %[l0] offset:8192\n\t" \
    "ds_read_b128 v[164:167], %[l0] offset:9216\n\t" "s_waitcnt vmcnt(5)\n\t" "v_smfmac_f32_16x16x64_bf16 v[224:227], v[244:247], a[16:23], %[ix]\n\t" \
    "ds_write_b128 %[l1], v[244:247] offset:1024\n\t" "v_smfmac_f32_16x16x64_bf16 v[228:231], v[244:247], a[24:31], %[ix]\n\t" \
    "v_smfmac_f32_16x16x64_bf16 v[232:235], v[244:247], v[40:47], %[ix]\n\t" "s_waitcnt lgkmcnt(8)\n\t" \
    "v_smfmac_f32_16x16x64_bf16 v[236:239], v[244:247], v[168:175], %[ix]\n\t" "ds_read_b128 v[168:171], %[l0] offset:10240\n\t" \
    "ds_read_b128 v[172:175], %[l0] offset:11264\n\t" "s_waitcnt vmcnt(4)\n\t" "v_smfmac_f32_16x16x64_bf16 v[224:227], v[248:251], a[32:39], %[ix]\n\t" \
    "ds_write_b128 %[l1], v[248:251] offset:2048\n\t" "v_smfmac_f32_16x16x64_bf16 v[228:231], v[248:251], a[40:47], %[ix]\n\t" "s_waitcnt lgkmcnt(3)\n\t" "s_barrier\n\t" \
    XT(86, 87) "v_smfmac_f32_16x16x64_bf16 v[232:235], v[248:251], v[48:55], %[ix]\n\t" "ds_read_b128 v[192:195], %[l1] offset:4096\n\t" \
    "ds_read_b128 v[196:199], %[l1] offset:8192\n\t" "v_smfmac_f32_16x16x64_bf16 v[236:239], v[248:251], v[176:183], %[ix]\n\t" \
    "ds_read_b128 v[176:179], %[l0] offset:12288\n\t" "ds_read_b128 v[180:183], %[l0] offset:13312\n\t" "s_waitcnt vmcnt(3)\n\t" \
    "v_smfmac_f32_16x16x64_bf16 v[224:227], v[252:255], a[48:55], %[ix]\n\t" "ds_write_b128 %[l1], v[252:255] offset:3072\n\t" \
    "v_smfmac_f32_16x16x64_bf16 v[228:231], v[252:255], a[56:63], %[ix]\n\t" "ds_read_b128 v[240:243], %[l1] offset:12288\n\t" \
    "ds_read_b128 v[244:247], %[l1] offset:5120\n\t" "v_smfmac_f32_16x16x64_bf16 v[232:235], v[252:255], v[56:63], %[ix]\n\t" "s_nop 1\n\t" \
    "v_smfmac_f32_16x16x64_bf16 v[236:239], v[252:255], v[184:191], %[ix]\n\t" "ds_read_b128 v[184:187], %[l0] offset:14336\n\t" "v_cmp_u_f32 vcc, v224, v225\n\t" \
    "v_cmp_u_f32 s[62:63], v226, v227\n\t" "s_waitcnt lgkmcnt(7)\n\t" "v_smfmac_f32_16x16x64_bf16 v[224:227], v[192:195], a[64:71], %[ix]\n\t" \
    "ds_read_b128 v[188:191], %[l0] offset:15360\n\t" "v_smfmac_f32_16x16x64_bf16 v[228:231], v[192:195], a[72:79], %[ix]\n\t" "s_or_b64 vcc, vcc, s[62:63]\n\t" \
    "s_cbranch_scc1 11f\n\t" "12:\n\t" "v_smfmac_f32_16x16x64_bf16 v[232:235], v[192:195], v[64:71], %[ix]\n\t" "s_waitcnt lgkmcnt(4)\n\t" "s_barrier\n\t" XT(88, 89) \
    "ds_read_b32 v218, v219\n\t" "v_smfmac_f32_16x16x64_bf16 v[236:239], v[192:195], v[160:167], %[ix]\n\t" "ds_read_b128 v[160:163], %[l0] offset:16384\n\t" \
    "ds_read_b128 v[164:167], %[l0] offset:17408\n\t" "v_smfmac_f32_16x16x64_bf16 v[224:227], v[196:199], a[80:87], %[ix]\n\t" \
    "ds_read_b128 v[248:251], %[l1] offset:9216\n\t" "ds_read_b128 v[252:255], %[l1] offset:13312\n\t" \
    "v_smfmac_f32_16x16x64_bf16 v[228:231], v[196:199], a[88:95], %[ix]\n\t" "ds_read_b128 v[192:195], %[l1] offset:6144\n\t" \
    "v_smfmac_f32_16x16x64_bf16 v[232:235], v[196:199], v[72:79], %[ix]\n\t" "v_smfmac_f32_16x16x64_bf16 v[236:239], v[196:199], v[168:175], %[ix]\n\t" \
    "s_waitcnt lgkmcnt(5)\n\t" "v_cmp_eq_u32 vcc, %[rt], v218\n\t" "v_smfmac_f32_16x16x64_bf16 v[224:227], v[240:243], a[96:103], %[ix]\n\t" "s_cbranch_vccnz 13f\n\t" \
    "ds_read_b128 v[168:171], %[l0] offset:18432\n\t" "v_smfmac_f32_16x16x64_bf16 v[228:231], v[240:243], a[104:111], %[ix]\n\t" \
    "ds_read_b128 v[172:175], %[l0] offset:19456\n\t" "ds_read_b128 v[196:199], %[l1] offset:10240\n\t" \
    "v_smfmac_f32_16x16x64_bf16 v[232:235], v[240:243], v[80:87], %[ix]\n\t" "v_smfmac_f32_16x16x64_bf16 v[236:239], v[240:243], v[176:183], %[ix]\n\t" \
    "ds_read_b128 v[176:179], %[l0] offset:20480\n\t" "ds_read_b128 v[180:183], %[l0] offset:21504\n\t" \
    "v_smfmac_f32_16x16x64_bf16 v[224:227], v[244:247], a[112:119], %[ix]\n\t" "ds_read_b128 v[240:243], %[l1] offset:14336\n\t" \
    "v_smfmac_f32_16x16x64_bf16 v[228:231], v[244:247], a[120:127], %[ix]\n\t" "buffer_load_dword %[pd], %[pa], %[rs], %[kn] offen sc1\n\t" \
    "v_smfmac_f32_16x16x64_bf16 v[232:235], v[244:247], v[88:95], %[ix]\n\t" "v_smfmac_f32_16x16x64_bf16 v[236:239], v[244:247], v[184:191], %[ix]\n\t" \
    "ds_read_b128 v[184:187], %[l0] offset:22528\n\t" "ds_read_b128 v[188:191], %[l0] offset:23552\n\t" "s_waitcnt lgkmcnt(10)\n\t" \
    "v_smfmac_f32_16x16x64_bf16 v[224:227], v[248:251], a[128:135], %[ix]\n\t" "ds_read_b128 v[244:247], %[l1] offset:7168\n\t" \
    "v_smfmac_f32_16x16x64_bf16 v[228:231], v[248:251], a[136:143], %[ix]\n\t" "v_smfmac_f32_16x16x64_bf16 v[232:235], v[248:251], v[96:103], %[ix]\n\t" \
    "v_smfmac_f32_16x16x64_bf16 v[236:239], v[248:251], v[160:167], %[ix]\n\t" "ds_read_b128 v[160:163], %[l0] offset:24576\n\t" \
    "ds_read_b128 v[164:167], %[l0] offset:25600\n\t" "s_waitcnt lgkmcnt(12)\n\t" "v_smfmac_f32_16x16x64_bf16 v[224:227], v[252:255], a[144:151], %[ix]\n\t" \
    "ds_read_b128 v[248:251], %[l1] offset:11264\n\t" "v_smfmac_f32_16x16x64_bf16 v[228:231], v[252:255], a[152:159], %[ix]\n\t" \
    "v_smfmac_f32_16x16x64_bf16 v[232:235], v[252:255], v[104:111], %[ix]\n\t" "s_waitcnt lgkmcnt(10)\n\t" \
    "v_smfmac_f32_16x16x64_bf16 v[236:239], v[252:255], v[168:175], %[ix]\n\t" "ds_read_b128 v[168:171], %[l0] offset:26624\n\t" \
    "ds_read_b128 v[172:175], %[l0] offset:27648\n\t" "v_smfmac_f32_16x16x64_bf16 v[224:227], v[192:195], a[160:167], %[ix]\n\t" \
    "ds_read_b128 v[252:255], %[l1] offset:15360\n\t" "v_smfmac_f32_16x16x64_bf16 v[228:231], v[192:195], a[168:175], %[ix]\n\t" "s_waitcnt lgkmcnt(12)\n\t" \
    "v_smfmac_f32_16x16x64_bf16 v[224:227], v[196:199], a[176:183], %[ix]\n\t" "v_smfmac_f32_16x16x64_bf16 v[228:231], v[196:199], a[184:191], %[ix]\n\t" \
    "s_waitcnt lgkmcnt(9)\n\t" "v_smfmac_f32_16x16x64_bf16 v[224:227], v[240:243], a[192:199], %[ix]\n\t" \
    "v_smfmac_f32_16x16x64_bf16 v[228:231], v[240:243], a[200:207], %[ix]\n\t" "s_waitcnt vmcnt(1)\n\t" "v_readfirstlane_b32 s62, v217\n\t" "s_waitcnt lgkmcnt(6)\n\t" \
    "v_smfmac_f32_16x16x64_bf16 v[224:227], v[244:247], a[208:215], %[ix]\n\t" "v_smfmac_f32_16x16x64_bf16 v[228:231], v[244:247], a[216:223], %[ix]\n\t" \
    "s_max_u32 %[sq], %[sq], s62\n\t" "s_waitcnt lgkmcnt(3)\n\t" "v_smfmac_f32_16x16x64_bf16 v[224:227], v[248:251], a[224:231], %[ix]\n\t" \
    "v_smfmac_f32_16x16x64_bf16 v[228:231], v[248:251], a[232:239], %[ix]\n\t" "s_waitcnt lgkmcnt(0)\n\t" \
    "v_smfmac_f32_16x16x64_bf16 v[224:227], v[252:255], a[240:247], %[ix]\n\t" "s_nop 1\n\t" "v_smfmac_f32_16x16x64_bf16 v[228:231], v[252:255], a[248:255], %[ix]\n\t" \
    XT(90, 91) "s_nop 1\n\t" "v_smfmac_f32_16x16x64_bf16 v[232:235], v[192:195], v[112:119], %[ix]\n\t" "s_nop 1\n\t" \
    "v_smfmac_f32_16x16x64_bf16 v[236:239], v[192:195], v[176:183], %[ix]\n\t" "ds_read_b128 v[176:179], %[l0] offset:28672\n\t" "v_add_f32 v224, v224, v225\n\t" \
    "v_add_f32 v226, v226, v227\n\t" "v_smfmac_f32_16x16x64_bf16 v[232:235], v[196:199], v[120:127], %[ix]\n\t" "ds_read_b128 v[180:183], %[l0] offset:29696\n\t" \
    "v_add_f32 v228, v228, v229\n\t" "v_add_f32 v230, v230, v231\n\t" "v_smfmac_f32_16x16x64_bf16 v[236:239], v[196:199], v[184:191], %[ix]\n\t" \
    "ds_read_b128 v[184:187], %[l0] offset:30720\n\t" "v_cvt_pk_bf16_f32 v218, v224, v228\n\t" "v_cvt_pk_bf16_f32 v219, v226, v230\n\t" \
    "v_smfmac_f32_16x16x64_bf16 v[232:235], v[240:243], v[128:135], %[ix]\n\t" "ds_write2_b32 %[qw], v218, v219 offset1:8\n\t" \
    "ds_read_b128 v[188:191], %[l0] offset:31744\n\t" "v_smfmac_f32_16x16x64_bf16 v[236:239], v[240:243], v[160:167], %[ix]\n\t" \
    "ds_read_b128 v[192:195], %[kx] offset:4352\n\t" "ds_read_b128 v[196:199], %[kx] offset:6400\n\t" \
    "v_smfmac_f32_16x16x64_bf16 v[232:235], v[244:247], v[136:143], %[ix]\n\t" "v_smfmac_f32_16x16x64_bf16 v[236:239], v[244:247], v[168:175], %[ix]\n\t" \
    "s_waitcnt lgkmcnt(3)\n\t" "s_barrier\n\t" "v_smfmac_f32_16x16x64_bf16 v[232:235], v[248:251], v[144:151], %[ix]\n\t" "ds_read_b128 v[216:219], %[l1] offset:24576\n\t" \
    "ds_read_b128 v[220:223], %[l1] offset:25600\n\t" "v_smfmac_f32_16x16x64_bf16 v[236:239], v[248:251], v[176:183], %[ix]\n\t" \
    "v_smfmac_f32_16x16x64_bf16 v[232:235], v[252:255], v[152:159], %[ix]\n\t" "s_waitcnt lgkmcnt(4)\n\t" \
    "v_smfmac_f32_16x16x64_bf16 v[236:239], v[252:255], v[184:191], %[ix]\n\t" XT(92, 93) "s_waitcnt lgkmcnt(1)\n\t" \
    "v_smfmac_f32_16x16x64_bf16 v[208:211], v[216:219], v[16:23], %[ix]\n\t" "s_nop 1\n\t" "v_smfmac_f32_16x16x64_bf16 v[212:215], v[216:219], v[200:207], %[ix]\n\t" \
    "s_waitcnt lgkmcnt(0)\n\t" "s_nop 0\n\t" "v_add_f32 v232, v232, v233\n\t" "v_add_f32 v234, v234, v235\n\t" "v_add_f32 v236, v236, v237\n\t" \
    "v_add_f32 v238, v238, v239\n\t" "v_cvt_pk_bf16_f32 v232, v232, v236\n\t" "v_cvt_pk_bf16_f32 v233, v234, v238\n\t" "ds_write2_b32 %[kw], v232, v233 offset1:68\n\t" \
    "v_smfmac_f32_16x16x64_bf16 v[208:211], v[220:223], v[24:31], %[ix]\n\t" "v_smfmac_f32_16x16x64_bf16 v[212:215], v[220:223], v[192:199], %[ix]\n\t" \
    "v_mov_b64 v[224:225], 0\n\t" "v_mov_b64 v[226:227], 0\n\t" "s_waitcnt lgkmcnt(0)\n\t" "s_barrier\n\t" XT(94, 95) "ds_read_b128 v[160:163], %[km] offset:0\n\t" \
    "ds_read_b128 v[164:167], %[km] offset:64\n\t" "ds_read_b128 v[168:171], %[km] offset:128\n\t" "ds_read_b128 v[172:175], %[km] offset:192\n\t" \
    "v_add_f32 v178, v208, v209\n\t" "v_add_f32 v179, v210, v211\n\t" "v_add_f32 v180, v212, v213\n\t" "v_add_f32 v181, v214, v215\n\t" \
    "v_pk_mul_f32 v[178:179], v[178:179], s[66:67]\n\t" "v_pk_mul_f32 v[180:181], v[180:181], s[66:67]\n\t" "v_max_f32 v182, v178, v180\n\t" "v_max_f32 v183, v179, v181\n\t" \
    "s_nop 0\n\t" "v_max_f32_dpp v182, v182, v182 row_ror:8 row_mask:0xf bank_mask:0xf\n\t" "v_max_f32_dpp v183, v183, v183 row_ror:8 row_mask:0xf bank_mask:0xf\n\t" \
    "s_nop 0\n\t" "s_waitcnt lgkmcnt(2)\n\t" "v_smfmac_f32_16x16x64_bf16 v[224:227], v[216:219], v[160:167], %[ix]\n\t" "s_waitcnt lgkmcnt(0)\n\t" \
    "v_smfmac_f32_16x16x64_bf16 v[224:227], v[220:223], v[168:175], %[ix]\n\t" "s_nop 7\n\t" "v_add_f32 v176, v224, v225\n\t" "v_add_f32 v177, v226, v227\n\t" \
    "v_mul_f32 v176, s66, v176\n\t" "v_mul_f32 v177, s66, v177\n\t" "v_max_f32 v182, v176, v182\n\t" "v_max_f32 v183, v177, v183\n\t" "s_nop 0\n\t" \
    "v_max_f32_dpp v182, v182, v182 row_ror:4 row_mask:0xf bank_mask:0xf\n\t" "v_max_f32_dpp v183, v183, v183 row_ror:4 row_mask:0xf bank_mask:0xf\n\t" "s_nop 0\n\t" \
    "v_max_f32_dpp v182, v182, v182 row_ror:2 row_mask:0xf bank_mask:0xf\n\t" "v_max_f32_dpp v183, v183, v183 row_ror:2 row_mask:0xf bank_mask:0xf\n\t" "s_nop 0\n\t" \
    "v_max_f32_dpp v182, v182, v182 row_ror:1 row_mask:0xf bank_mask:0xf\n\t" "v_max_f32_dpp v183, v183, v183 row_ror:1 row_mask:0xf bank_mask:0xf\n\t" \
    "v_pk_add_f32 v[178:179], v[178:179], v[182:183] neg_lo:[0,1] neg_hi:[0,1]\n\t" "v_pk_add_f32 v[180:181], v[180:181], v[182:183] neg_lo:[0,1] neg_hi:[0,1]\n\t" \
    "v_pk_add_f32 v[176:177], v[176:177], v[182:183] neg_lo:[0,1] neg_hi:[0,1]\n\t" "v_exp_f32 v178, v178\n\t" "v_exp_f32 v179, v179\n\t" "v_exp_f32 v180, v180\n\t" \
    "v_exp_f32 v181, v181\n\t" "v_exp_f32 v176, v176\n\t" "v_pk_add_f32 v[182:183], v[178:179], v[180:181]\n\t" "v_exp_f32 v177, v177\n\t" "s_nop 0\n\t" \
    "v_add_f32_dpp v182, v182, v182 row_ror:8 row_mask:0xf bank_mask:0xf\n\t" "v_add_f32_dpp v183, v183, v183 row_ror:8 row_mask:0xf bank_mask:0xf\n\t" \
    "v_add_f32 v182, v182, v176\n\t" "v_add_f32 v183, v183, v177\n\t" "s_nop 0\n\t" "v_add_f32_dpp v182, v182, v182 row_ror:4 row_mask:0xf bank_mask:0xf\n\t" \
    "v_add_f32_dpp v183, v183, v183 row_ror:4 row_mask:0xf bank_mask:0xf\n\t" "s_nop 0\n\t" "v_add_f32_dpp v182, v182, v182 row_ror:2 row_mask:0xf bank_mask:0xf\n\t" \
    "v_add_f32_dpp v183, v183, v183 row_ror:2 row_mask:0xf bank_mask:0xf\n\t" "s_nop 0\n\t" "v_add_f32_dpp v182, v182, v182 row_ror:1 row_mask:0xf bank_mask:0xf\n\t" \
    "v_add_f32_dpp v183, v183, v183 row_ror:1 row_mask:0xf bank_mask:0xf\n\t" "v_rcp_f32 v182, v182\n\t" "v_rcp_f32 v183, v183\n\t" "s_nop 0\n\t" \
    "v_pk_mul_f32 v[176:177], v[176:177], v[182:183]\n\t" "v_pk_mul_f32 v[178:179], v[178:179], v[182:183]\n\t" "v_pk_mul_f32 v[180:181], v[180:181], v[182:183]\n\t" \
    "v_cvt_pk_bf16_f32 v176, v176, v177\n\t" "v_cvt_pk_bf16_f32 v178, v178, v179\n\t" "v_cvt_pk_bf16_f32 v180, v180, v181\n\t" \
    "v_mov_b32_dpp v177, v176 quad_perm:[1,0,3,2] row_mask:0xf bank_mask:0xf\n\t" "v_mov_b32_dpp v179, v178 quad_perm:[1,0,3,2] row_mask:0xf bank_mask:0xf\n\t" \
    "v_mov_b32_dpp v181, v180 quad_perm:[1,0,3,2] row_mask:0xf bank_mask:0xf\n\t" "v_perm_b32 v184, v179, v178, %[ps]\n\t" "v_perm_b32 v185, v181, v180, %[ps]\n\t" \
    "buffer_store_dwordx2 v[184:185], %[px], %[rs], %[pb] offen sc1\n\t" "v_perm_b32 v186, v177, v176, %[ps]\n\t" "buffer_store_dword v186, %[pm], %[rs], %[pb] offen sc1\n\t" \
    XT(96, 97) LMARK "s_waitcnt vmcnt(2)\n\t" "s_branch 99f\n\t" "11:\n\t" "ds_read_b128 v[220:223], %[l1] offset:0\n\t" "s_waitcnt lgkmcnt(0)\n\t" \
    "v_max3_u32 v218, v220, v221, v222\n\t" "v_max_u32 v218, v218, v223\n\t" "ds_read_b128 v[220:223], %[l1] offset:1024\n\t" "s_waitcnt lgkmcnt(0)\n\t" \
    "v_max3_u32 v218, v218, v220, v221\n\t" "v_max3_u32 v218, v218, v222, v223\n\t" "ds_read_b128 v[220:223], %[l1] offset:2048\n\t" "s_waitcnt lgkmcnt(0)\n\t" \
    "v_max3_u32 v218, v218, v220, v221\n\t" "v_max3_u32 v218, v218, v222, v223\n\t" "ds_read_b128 v[220:223], %[l1] offset:3072\n\t" "s_waitcnt lgkmcnt(0)\n\t" \
    "v_max3_u32 v218, v218, v220, v221\n\t" "v_max3_u32 v218, v218, v222, v223\n\t" "v_cmp_eq_u32 vcc, -1, v218\n\t" "s_cbranch_vccz 12b\n\t" "s_cmp_eq_u32 %[rt], 1\n\t" \
    "s_cbranch_scc1 12b\n\t" "v_mov_b32 v218, %[rt]\n\t" "ds_write_b32 v219, v218\n\t" "s_waitcnt lgkmcnt(0)\n\t" "s_branch 12b\n\t" "13:\n\t" "s_sub_u32 %[rt], %[rt], 1\n\t" \
    "s_waitcnt lgkmcnt(0)\n\t" XCNT(100) "14:\n\t" "buffer_load_dwordx4 v[240:243], %[mo], %[rs], %[sb] offen sc1\n\t" \
    "buffer_load_dwordx4 v[244:247], v216, %[rs], %[sb] offen sc1\n\t" "buffer_load_dwordx4 v[248:251], %[mo], %[rs], s60 offen sc1\n\t" \
    "buffer_load_dwordx4 v[252:255], v216, %[rs], s60 offen sc1\n\t" "s_waitcnt vmcnt(0)\n\t" "v_max_u32 v218, v240, v241\n\t" "v_max3_u32 v218, v218, v242, v243\n\t" \
    "v_max3_u32 v218, v218, v244, v245\n\t" "v_max3_u32 v218, v218, v246, v247\n\t" "v_max3_u32 v218, v218, v248, v249\n\t" "v_max3_u32 v218, v218, v250, v251\n\t" \
    "v_max3_u32 v218, v218, v252, v253\n\t" "v_max3_u32 v218, v218, v254, v255\n\t" "v_cmp_eq_u32 vcc, -1, v218\n\t" "s_cbranch_vccz 15f\n\t" "s_cmp_eq_u32 %[bd], 0\n\t" \
    "s_cbranch_scc1 15f\n\t" "s_sub_u32 %[bd], %[bd], 1\n\t" XCNT(101) "s_branch 14b\n\t" "15:\n\t" "ds_read_b128 v[160:163], %[l0] offset:0\n\t" \
    "ds_read_b128 v[164:167], %[l0] offset:1024\n\t" "ds_read_b128 v[168:171], %[l0] offset:2048\n\t" "ds_read_b128 v[172:175], %[l0] offset:3072\n\t" \
    "ds_read_b128 v[176:179], %[l0] offset:4096\n\t" "ds_read_b128 v[180:183], %[l0] offset:5120\n\t" "ds_read_b128 v[184:187], %[l0] offset:6144\n\t" \
    "ds_read_b128 v[188:191], %[l0] offset:7168\n\t" "v_mov_b64 v[224:225], 0\n\t" "v_mov_b64 v[226:227], 0\n\t" "v_mov_b64 v[228:229], 0\n\t" "v_mov_b64 v[230:231], 0\n\t" \
    "v_mov_b64 v[232:233], 0\n\t" "v_mov_b64 v[234:235], 0\n\t" "v_mov_b64 v[236:237], 0\n\t" "v_mov_b64 v[238:239], 0\n\t" "s_barrier\n\t" "s_branch 10b\n\t" "99:\n\t"
#define QK_STEP1 \
    ".p2align 6\n\t" "v_xor_b32 v216, 0x400, %[mo]\n\t" "s_xor_b32 s60, %[sb], 0x800\n\t" "s_add_u32 s61, %[qb], 0x1000\n\t" "s_mov_b32 s66, 0x3e0293ee\n\t" \
    "s_mov_b32 s67, 0x3e0293ee\n\t" XT(80, 81) XT(98, 99) "s_barrier\n\t" XT(82, 83) "buffer_load_dwordx4 v[240:243], %[mo], %[rs], %[sb] offen sc1\n\t" \
    "v_mov_b64 v[224:225], 0\n\t" "v_mov_b64 v[226:227], 0\n\t" "v_mov_b64 v[228:229], 0\n\t" "v_mov_b64 v[230:231], 0\n\t" "v_mov_b64 v[232:233], 0\n\t" \
    "v_mov_b64 v[234:235], 0\n\t" "v_mov_b64 v[236:237], 0\n\t" "v_mov_b64 v[238:239], 0\n\t" "buffer_load_dwordx4 v[244:247], v216, %[rs], %[sb] offen sc1\n\t" \
    "ds_read_b128 v[160:163], %[l0] offset:0\n\t" "ds_read_b128 v[164:167], %[l0] offset:1024\n\t" "ds_read_b128 v[168:171], %[l0] offset:2048\n\t" \
    "ds_read_b128 v[172:175], %[l0] offset:3072\n\t" "s_nop 3\n\t" "buffer_load_dwordx4 v[248:251], %[mo], %[rs], s60 offen sc1\n\t" \
    "ds_read_b128 v[176:179], %[l0] offset:4096\n\t" "ds_read_b128 v[180:183], %[l0] offset:5120\n\t" "ds_read_b128 v[184:187], %[l0] offset:6144\n\t" \
    "ds_read_b128 v[188:191], %[l0] offset:7168\n\t" "s_nop 3\n\t" "buffer_load_dwordx4 v[252:255], v216, %[rs], s60 offen sc1\n\t" "v_mov_b32 v219, 160512\n\t" \
    "v_mov_b64 v[220:221], -1\n\t" "v_mov_b64 v[222:223], -1\n\t" "buffer_store_dwordx2 v[220:221], %[px], %[rs], %[tr] offen sc1\n\t" \
    "buffer_store_dwordx2 v[220:221], %[pm], %[rs], %[tr] offen sc1\n\t" "v_mov_b32 v208, 0xd028965f\n\t" "v_mov_b32 v209, 0xff800000\n\t" "s_mov_b32 s62, 0\n\t" \
    "s_mov_b32 s63, -1\n\t" "v_mov_b32 v210, 0\n\t" "v_cmp_gt_i32 vcc, 0, %[ps]\n\t" "v_cndmask_b32 v204, 0, v209, s[62:63]\n\t" "s_nop 0\n\t" \
    "v_cndmask_b32 v200, 0, v208, vcc\n\t" "v_cmp_gt_i32 vcc, 1, %[ps]\n\t" "v_cndmask_b32 v205, 0, v209, s[62:63]\n\t" "s_nop 0\n\t" "v_cndmask_b32 v201, 0, v208, vcc\n\t" \
    "v_cmp_gt_i32 vcc, 2, %[ps]\n\t" "v_cndmask_b32 v206, 0, v209, s[62:63]\n\t" "s_nop 0\n\t" "v_cndmask_b32 v202, 0, v208, vcc\n\t" "v_cmp_gt_i32 vcc, 3, %[ps]\n\t" \
    "v_cndmask_b32 v207, 0, v209, s[62:63]\n\t" "s_nop 0\n\t" "v_cndmask_b32 v203, 0, v208, vcc\n\t" "20:\n\t" "s_waitcnt vmcnt(5)\n\t" XT(84, 85) \
    "v_smfmac_f32_16x16x64_bf16 v[224:227], v[240:243], a[0:7], %[ix]\n\t" "ds_write_b128 %[l1], v[240:243] offset:4096\n\t" \
    "v_smfmac_f32_16x16x64_bf16 v[228:231], v[240:243], a[8:15], %[ix]\n\t" "v_smfmac_f32_16x16x64_bf16 v[232:235], v[240:243], v[32:39], %[ix]\n\t" \
    "s_waitcnt lgkmcnt(7)\n\t" "v_smfmac_f32_16x16x64_bf16 v[236:239], v[240:243], v[160:167], %[ix]\n\t" "ds_read_b128 v[160:163], %[l0] offset:8192\n\t" \
    "ds_read_b128 v[164:167], %[l0] offset:9216\n\t" "s_waitcnt vmcnt(4)\n\t" "v_smfmac_f32_16x16x64_bf16 v[224:227], v[244:247], a[16:23], %[ix]\n\t" \
    "ds_write_b128 %[l1], v[244:247] offset:5120\n\t" "v_smfmac_f32_16x16x64_bf16 v[228:231], v[244:247], a[24:31], %[ix]\n\t" \
    "v_smfmac_f32_16x16x64_bf16 v[232:235], v[244:247], v[40:47], %[ix]\n\t" "s_waitcnt lgkmcnt(8)\n\t" \
    "v_smfmac_f32_16x16x64_bf16 v[236:239], v[244:247], v[168:175], %[ix]\n\t" "ds_read_b128 v[168:171], %[l0] offset:10240\n\t" \
    "ds_read_b128 v[172:175], %[l0] offset:11264\n\t" "s_waitcnt vmcnt(3)\n\t" "v_smfmac_f32_16x16x64_bf16 v[224:227], v[248:251], a[32:39], %[ix]\n\t" \
    "ds_write_b128 %[l1], v[248:251] offset:6144\n\t" "v_smfmac_f32_16x16x64_bf16 v[228:231], v[248:251], a[40:47], %[ix]\n\t" "s_waitcnt lgkmcnt(3)\n\t" "s_barrier\n\t" \
    XT(86, 87) "v_smfmac_f32_16x16x64_bf16 v[232:235], v[248:251], v[48:55], %[ix]\n\t" "ds_read_b128 v[192:195], %[l1] offset:8192\n\t" \
    "ds_read_b128 v[196:199], %[l1] offset:12288\n\t" "v_smfmac_f32_16x16x64_bf16 v[236:239], v[248:251], v[176:183], %[ix]\n\t" \
    "ds_read_b128 v[176:179], %[l0] offset:12288\n\t" "ds_read_b128 v[180:183], %[l0] offset:13312\n\t" "s_waitcnt vmcnt(2)\n\t" \
    "v_smfmac_f32_16x16x64_bf16 v[224:227], v[252:255], a[48:55], %[ix]\n\t" "ds_write_b128 %[l1], v[252:255] offset:7168\n\t" \
    "v_smfmac_f32_16x16x64_bf16 v[228:231], v[252:255], a[56:63], %[ix]\n\t" "ds_read_b128 v[240:243], %[l1] offset:0\n\t" "ds_read_b128 v[244:247], %[l1] offset:9216\n\t" \
    "v_smfmac_f32_16x16x64_bf16 v[232:235], v[252:255], v[56:63], %[ix]\n\t" "s_nop 1\n\t" "v_smfmac_f32_16x16x64_bf16 v[236:239], v[252:255], v[184:191], %[ix]\n\t" \
    "ds_read_b128 v[184:187], %[l0] offset:14336\n\t" "v_cmp_u_f32 vcc, v224, v225\n\t" "v_cmp_u_f32 s[62:63], v226, v227\n\t" "s_waitcnt lgkmcnt(7)\n\t" \
    "v_smfmac_f32_16x16x64_bf16 v[224:227], v[192:195], a[64:71], %[ix]\n\t" "ds_read_b128 v[188:191], %[l0] offset:15360\n\t" \
    "v_smfmac_f32_16x16x64_bf16 v[228:231], v[192:195], a[72:79], %[ix]\n\t" "s_or_b64 vcc, vcc, s[62:63]\n\t" "s_cbranch_scc1 21f\n\t" "22:\n\t" \
    "v_smfmac_f32_16x16x64_bf16 v[232:235], v[192:195], v[64:71], %[ix]\n\t" "s_waitcnt lgkmcnt(4)\n\t" "s_barrier\n\t" XT(88, 89) "ds_read_b32 v218, v219\n\t" \
    "v_smfmac_f32_16x16x64_bf16 v[236:239], v[192:195], v[160:167], %[ix]\n\t" "ds_read_b128 v[160:163], %[l0] offset:16384\n\t" \
    "ds_read_b128 v[164:167], %[l0] offset:17408\n\t" "v_smfmac_f32_16x16x64_bf16 v[224:227], v[196:199], a[80:87], %[ix]\n\t" \
    "ds_read_b128 v[248:251], %[l1] offset:13312\n\t" "ds_read_b128 v[252:255], %[l1] offset:1024\n\t" \
    "v_smfmac_f32_16x16x64_bf16 v[228:231], v[196:199], a[88:95], %[ix]\n\t" "ds_read_b128 v[192:195], %[l1] offset:10240\n\t" \
    "v_smfmac_f32_16x16x64_bf16 v[232:235], v[196:199], v[72:79], %[ix]\n\t" "v_smfmac_f32_16x16x64_bf16 v[236:239], v[196:199], v[168:175], %[ix]\n\t" \
    "s_waitcnt lgkmcnt(5)\n\t" "v_cmp_eq_u32 vcc, %[rt], v218\n\t" "v_smfmac_f32_16x16x64_bf16 v[224:227], v[240:243], a[96:103], %[ix]\n\t" "s_cbranch_vccnz 23f\n\t" \
    "ds_read_b128 v[168:171], %[l0] offset:18432\n\t" "v_smfmac_f32_16x16x64_bf16 v[228:231], v[240:243], a[104:111], %[ix]\n\t" \
    "ds_read_b128 v[172:175], %[l0] offset:19456\n\t" "ds_read_b128 v[196:199], %[l1] offset:14336\n\t" \
    "v_smfmac_f32_16x16x64_bf16 v[232:235], v[240:243], v[80:87], %[ix]\n\t" "buffer_load_dwordx4 v[16:19], %[hq], %[rs], %[qb] offen offset:0 sc1\n\t" \
    "v_smfmac_f32_16x16x64_bf16 v[236:239], v[240:243], v[176:183], %[ix]\n\t" "ds_read_b128 v[176:179], %[l0] offset:20480\n\t" \
    "ds_read_b128 v[180:183], %[l0] offset:21504\n\t" "v_smfmac_f32_16x16x64_bf16 v[224:227], v[244:247], a[112:119], %[ix]\n\t" \
    "ds_read_b128 v[240:243], %[l1] offset:2048\n\t" "v_smfmac_f32_16x16x64_bf16 v[228:231], v[244:247], a[120:127], %[ix]\n\t" \
    "v_smfmac_f32_16x16x64_bf16 v[232:235], v[244:247], v[88:95], %[ix]\n\t" "buffer_load_dwordx4 v[20:23], %[hq], %[rs], %[qb] offen offset:2048 sc1\n\t" \
    "v_smfmac_f32_16x16x64_bf16 v[236:239], v[244:247], v[184:191], %[ix]\n\t" "ds_read_b128 v[184:187], %[l0] offset:22528\n\t" \
    "ds_read_b128 v[188:191], %[l0] offset:23552\n\t" "s_waitcnt lgkmcnt(10)\n\t" "v_smfmac_f32_16x16x64_bf16 v[224:227], v[248:251], a[128:135], %[ix]\n\t" \
    "ds_read_b128 v[244:247], %[l1] offset:11264\n\t" "v_smfmac_f32_16x16x64_bf16 v[228:231], v[248:251], a[136:143], %[ix]\n\t" \
    "v_smfmac_f32_16x16x64_bf16 v[232:235], v[248:251], v[96:103], %[ix]\n\t" "buffer_load_dwordx4 v[24:27], %[hq], %[rs], s61 offen offset:0 sc1\n\t" \
    "v_smfmac_f32_16x16x64_bf16 v[236:239], v[248:251], v[160:167], %[ix]\n\t" "ds_read_b128 v[160:163], %[l0] offset:24576\n\t" \
    "ds_read_b128 v[164:167], %[l0] offset:25600\n\t" "s_waitcnt lgkmcnt(12)\n\t" "v_smfmac_f32_16x16x64_bf16 v[224:227], v[252:255], a[144:151], %[ix]\n\t" \
    "ds_read_b128 v[248:251], %[l1] offset:15360\n\t" "v_smfmac_f32_16x16x64_bf16 v[228:231], v[252:255], a[152:159], %[ix]\n\t" \
    "v_smfmac_f32_16x16x64_bf16 v[232:235], v[252:255], v[104:111], %[ix]\n\t" "buffer_load_dwordx4 v[28:31], %[hq], %[rs], s61 offen offset:2048 sc1\n\t" \
    "s_waitcnt lgkmcnt(10)\n\t" "v_smfmac_f32_16x16x64_bf16 v[236:239], v[252:255], v[168:175], %[ix]\n\t" "ds_read_b128 v[168:171], %[l0] offset:26624\n\t" \
    "ds_read_b128 v[172:175], %[l0] offset:27648\n\t" "v_smfmac_f32_16x16x64_bf16 v[224:227], v[192:195], a[160:167], %[ix]\n\t" \
    "ds_read_b128 v[252:255], %[l1] offset:3072\n\t" "v_smfmac_f32_16x16x64_bf16 v[228:231], v[192:195], a[168:175], %[ix]\n\t" "s_waitcnt lgkmcnt(12)\n\t" \
    "v_smfmac_f32_16x16x64_bf16 v[224:227], v[196:199], a[176:183], %[ix]\n\t" "v_smfmac_f32_16x16x64_bf16 v[228:231], v[196:199], a[184:191], %[ix]\n\t" \
    "s_waitcnt lgkmcnt(9)\n\t" "v_smfmac_f32_16x16x64_bf16 v[224:227], v[240:243], a[192:199], %[ix]\n\t" \
    "v_smfmac_f32_16x16x64_bf16 v[228:231], v[240:243], a[200:207], %[ix]\n\t" "s_waitcnt lgkmcnt(6)\n\t" \
    "v_smfmac_f32_16x16x64_bf16 v[224:227], v[244:247], a[208:215], %[ix]\n\t" "v_smfmac_f32_16x16x64_bf16 v[228:231], v[244:247], a[216:223], %[ix]\n\t" \
    "s_waitcnt vmcnt(4)\n\t" "s_waitcnt lgkmcnt(3)\n\t" "v_smfmac_f32_16x16x64_bf16 v[224:227], v[248:251], a[224:231], %[ix]\n\t" \
    "v_smfmac_f32_16x16x64_bf16 v[228:231], v[248:251], a[232:239], %[ix]\n\t" "s_waitcnt lgkmcnt(0)\n\t" \
    "v_smfmac_f32_16x16x64_bf16 v[224:227], v[252:255], a[240:247], %[ix]\n\t" "s_nop 1\n\t" "v_smfmac_f32_16x16x64_bf16 v[228:231], v[252:255], a[248:255], %[ix]\n\t" \
    XT(90, 91) "s_nop 1\n\t" "v_smfmac_f32_16x16x64_bf16 v[232:235], v[192:195], v[112:119], %[ix]\n\t" "s_nop 1\n\t" \
    "v_smfmac_f32_16x16x64_bf16 v[236:239], v[192:195], v[176:183], %[ix]\n\t" "ds_read_b128 v[176:179], %[l0] offset:28672\n\t" "v_add_f32 v224, v224, v225\n\t" \
    "v_add_f32 v226, v226, v227\n\t" "v_smfmac_f32_16x16x64_bf16 v[232:235], v[196:199], v[120:127], %[ix]\n\t" "ds_read_b128 v[180:183], %[l0] offset:29696\n\t" \
    "v_add_f32 v228, v228, v229\n\t" "v_add_f32 v230, v230, v231\n\t" "v_smfmac_f32_16x16x64_bf16 v[236:239], v[196:199], v[184:191], %[ix]\n\t" \
    "ds_read_b128 v[184:187], %[l0] offset:30720\n\t" "v_cvt_pk_bf16_f32 v218, v224, v228\n\t" "v_cvt_pk_bf16_f32 v219, v226, v230\n\t" \
    "v_smfmac_f32_16x16x64_bf16 v[232:235], v[240:243], v[128:135], %[ix]\n\t" "ds_write2_b32 %[qw], v218, v219 offset1:8\n\t" \
    "ds_read_b128 v[188:191], %[l0] offset:31744\n\t" "v_smfmac_f32_16x16x64_bf16 v[236:239], v[240:243], v[160:167], %[ix]\n\t" \
    "v_smfmac_f32_16x16x64_bf16 v[232:235], v[244:247], v[136:143], %[ix]\n\t" "v_smfmac_f32_16x16x64_bf16 v[236:239], v[244:247], v[168:175], %[ix]\n\t" \
    "s_waitcnt lgkmcnt(1)\n\t" "s_barrier\n\t" "v_smfmac_f32_16x16x64_bf16 v[232:235], v[248:251], v[144:151], %[ix]\n\t" \
    "v_smfmac_f32_16x16x64_bf16 v[236:239], v[248:251], v[176:183], %[ix]\n\t" "v_smfmac_f32_16x16x64_bf16 v[232:235], v[252:255], v[152:159], %[ix]\n\t" \
    "s_waitcnt lgkmcnt(0)\n\t" "v_smfmac_f32_16x16x64_bf16 v[236:239], v[252:255], v[184:191], %[ix]\n\t" XT(92, 93) "s_nop 6\n\t" "v_add_f32 v232, v232, v233\n\t" \
    "v_add_f32 v234, v234, v235\n\t" "v_add_f32 v236, v236, v237\n\t" "v_add_f32 v238, v238, v239\n\t" "v_cvt_pk_bf16_f32 v232, v232, v236\n\t" \
    "v_cvt_pk_bf16_f32 v233, v234, v238\n\t" "ds_write2_b32 %[kw], v232, v233 offset1:68\n\t" "s_waitcnt lgkmcnt(0)\n\t" "s_barrier\n\t" XT(94, 95) \
    "s_cmp_ge_u32 %[sq], %[sn]\n\t" "s_cbranch_scc1 277f\n\t" "275:\n\t" "global_load_dword v240, v210, %[fq] sc1\n\t" "s_waitcnt vmcnt(0)\n\t" \
    "v_readfirstlane_b32 %[sq], v240\n\t" "s_nop 3\n\t" "s_cmp_ge_u32 %[sq], %[sn]\n\t" "s_cbranch_scc1 276f\n\t" "s_cmp_eq_u32 %[bd], 0\n\t" "s_cbranch_scc1 276f\n\t" \
    "s_sub_u32 %[bd], %[bd], 1\n\t" "s_sleep 2\n\t" "s_branch 275b\n\t" "276:\n\t" "buffer_load_dwordx4 v[16:19], %[hq], %[rs], %[qb] offen offset:0 sc1\n\t" \
    "buffer_load_dwordx4 v[20:23], %[hq], %[rs], %[qb] offen offset:2048 sc1\n\t" "buffer_load_dwordx4 v[24:27], %[hq], %[rs], s61 offen offset:0 sc1\n\t" \
    "buffer_load_dwordx4 v[28:31], %[hq], %[rs], s61 offen offset:2048 sc1\n\t" "277:\n\t" "s_nop 3\n\t" "ds_read_b128 v[160:163], %[kx] offset:0\n\t" \
    "ds_read_b128 v[164:167], %[kx] offset:2048\n\t" "ds_read_b128 v[168:171], %[kx] offset:4096\n\t" "ds_read_b128 v[172:175], %[kx] offset:6144\n\t" \
    "ds_read_b128 v[224:227], %[km] offset:0\n\t" "ds_read_b128 v[228:231], %[km] offset:64\n\t" "ds_read_b128 v[232:235], %[km] offset:128\n\t" \
    "ds_read_b128 v[236:239], %[km] offset:192\n\t" "s_waitcnt vmcnt(0)\n\t" "global_load_dword v240, v210, %[fq] sc1\n\t" "s_waitcnt lgkmcnt(7)\n\t" \
    "v_mfma_f32_16x16x32_bf16 v[248:251], v[160:163], v[16:19], v[200:203]\n\t" "s_waitcnt lgkmcnt(6)\n\t" \
    "v_mfma_f32_16x16x32_bf16 v[248:251], v[164:167], v[20:23], v[248:251]\n\t" "s_waitcnt lgkmcnt(5)\n\t" \
    "v_mfma_f32_16x16x32_bf16 v[248:251], v[168:171], v[24:27], v[248:251]\n\t" "s_waitcnt lgkmcnt(4)\n\t" \
    "v_mfma_f32_16x16x32_bf16 v[248:251], v[172:175], v[28:31], v[248:251]\n\t" "s_waitcnt lgkmcnt(3)\n\t" \
    "v_mfma_f32_16x16x32_bf16 v[244:247], v[224:227], v[16:19], v[204:207]\n\t" "s_waitcnt lgkmcnt(2)\n\t" \
    "v_mfma_f32_16x16x32_bf16 v[244:247], v[228:231], v[20:23], v[244:247]\n\t" "s_waitcnt lgkmcnt(1)\n\t" \
    "v_mfma_f32_16x16x32_bf16 v[244:247], v[232:235], v[24:27], v[244:247]\n\t" "s_waitcnt lgkmcnt(0)\n\t" \
    "v_mfma_f32_16x16x32_bf16 v[244:247], v[236:239], v[28:31], v[244:247]\n\t" "s_nop 3\n\t" "v_max3_f32 v242, v248, v249, v250\n\t" "v_max_f32 v242, v242, v251\n\t" \
    "s_nop 1\n\t" "v_max3_f32 v242, v242, v244, v245\n\t" "v_max3_f32 v242, v242, v246, v247\n\t" "v_mov_b32 v243, v242\n\t" "s_nop 1\n\t" \
    "v_permlane32_swap_b32 v242, v243\n\t" "v_max_f32 v242, v242, v243\n\t" "v_mov_b32 v243, v242\n\t" "s_nop 1\n\t" "v_permlane16_swap_b32 v242, v243\n\t" \
    "v_max_f32 v242, v242, v243\n\t" "v_pk_add_f32 v[244:245], v[244:245], v[242:243] op_sel_hi:[1,0] neg_lo:[0,1] neg_hi:[0,1]\n\t" \
    "v_pk_add_f32 v[246:247], v[246:247], v[242:243] op_sel_hi:[1,0] neg_lo:[0,1] neg_hi:[0,1]\n\t" \
    "v_pk_add_f32 v[248:249], v[248:249], v[242:243] op_sel_hi:[1,0] neg_lo:[0,1] neg_hi:[0,1]\n\t" \
    "v_pk_add_f32 v[250:251], v[250:251], v[242:243] op_sel_hi:[1,0] neg_lo:[0,1] neg_hi:[0,1]\n\t" "v_pk_mul_f32 v[244:245], v[244:245], s[66:67]\n\t" \
    "v_pk_mul_f32 v[246:247], v[246:247], s[66:67]\n\t" "v_pk_mul_f32 v[248:249], v[248:249], s[66:67]\n\t" "v_pk_mul_f32 v[250:251], v[250:251], s[66:67]\n\t" \
    "v_exp_f32 v244, v244\n\t" "v_exp_f32 v245, v245\n\t" "v_exp_f32 v246, v246\n\t" "v_exp_f32 v247, v247\n\t" "v_exp_f32 v248, v248\n\t" "v_exp_f32 v249, v249\n\t" \
    "v_exp_f32 v250, v250\n\t" "v_exp_f32 v251, v251\n\t" "v_pk_add_f32 v[242:243], v[244:245], v[246:247]\n\t" "v_pk_add_f32 v[242:243], v[242:243], v[248:249]\n\t" \
    "v_pk_add_f32 v[242:243], v[242:243], v[250:251]\n\t" "v_add_f32 v242, v242, v243\n\t" "v_mov_b32 v243, v242\n\t" "s_nop 1\n\t" "v_permlane32_swap_b32 v242, v243\n\t" \
    "v_add_f32 v242, v242, v243\n\t" "v_mov_b32 v243, v242\n\t" "s_nop 1\n\t" "v_permlane16_swap_b32 v242, v243\n\t" "v_add_f32 v242, v242, v243\n\t" \
    "v_rcp_f32 v242, v242\n\t" "s_nop 0\n\t" "v_pk_mul_f32 v[244:245], v[244:245], v[242:243] op_sel_hi:[1,0]\n\t" \
    "v_pk_mul_f32 v[246:247], v[246:247], v[242:243] op_sel_hi:[1,0]\n\t" "v_pk_mul_f32 v[248:249], v[248:249], v[242:243] op_sel_hi:[1,0]\n\t" \
    "v_pk_mul_f32 v[250:251], v[250:251], v[242:243] op_sel_hi:[1,0]\n\t" "v_cvt_pk_bf16_f32 v162, v244, v245\n\t" "v_cvt_pk_bf16_f32 v163, v246, v247\n\t" \
    "v_cvt_pk_bf16_f32 v164, v248, v249\n\t" "v_cvt_pk_bf16_f32 v165, v250, v251\n\t" "buffer_store_dwordx2 v[164:165], %[px], %[rs], %[tb] offen sc1\n\t" \
    "buffer_store_dwordx2 v[162:163], %[pm], %[rs], %[tb] offen sc1\n\t" "s_waitcnt vmcnt(2)\n\t" "v_readfirstlane_b32 s62, v240\n\t" "s_nop 3\n\t" \
    "s_max_u32 %[sq], %[sq], s62\n\t" "s_barrier\n\t" XT(96, 97) "s_branch 99f\n\t" "21:\n\t" "ds_read_b128 v[220:223], %[l1] offset:4096\n\t" "s_waitcnt lgkmcnt(0)\n\t" \
    "v_max3_u32 v218, v220, v221, v222\n\t" "v_max_u32 v218, v218, v223\n\t" "ds_read_b128 v[220:223], %[l1] offset:5120\n\t" "s_waitcnt lgkmcnt(0)\n\t" \
    "v_max3_u32 v218, v218, v220, v221\n\t" "v_max3_u32 v218, v218, v222, v223\n\t" "ds_read_b128 v[220:223], %[l1] offset:6144\n\t" "s_waitcnt lgkmcnt(0)\n\t" \
    "v_max3_u32 v218, v218, v220, v221\n\t" "v_max3_u32 v218, v218, v222, v223\n\t" "ds_read_b128 v[220:223], %[l1] offset:7168\n\t" "s_waitcnt lgkmcnt(0)\n\t" \
    "v_max3_u32 v218, v218, v220, v221\n\t" "v_max3_u32 v218, v218, v222, v223\n\t" "v_cmp_eq_u32 vcc, -1, v218\n\t" "s_cbranch_vccz 22b\n\t" "s_cmp_eq_u32 %[rt], 1\n\t" \
    "s_cbranch_scc1 22b\n\t" "v_mov_b32 v218, %[rt]\n\t" "ds_write_b32 v219, v218\n\t" "s_waitcnt lgkmcnt(0)\n\t" "s_branch 22b\n\t" "23:\n\t" "s_sub_u32 %[rt], %[rt], 1\n\t" \
    "s_waitcnt lgkmcnt(0)\n\t" XCNT(100) "24:\n\t" "buffer_load_dwordx4 v[240:243], %[mo], %[rs], %[sb] offen sc1\n\t" \
    "buffer_load_dwordx4 v[244:247], v216, %[rs], %[sb] offen sc1\n\t" "buffer_load_dwordx4 v[248:251], %[mo], %[rs], s60 offen sc1\n\t" \
    "buffer_load_dwordx4 v[252:255], v216, %[rs], s60 offen sc1\n\t" "s_waitcnt vmcnt(0)\n\t" "v_max_u32 v218, v240, v241\n\t" "v_max3_u32 v218, v218, v242, v243\n\t" \
    "v_max3_u32 v218, v218, v244, v245\n\t" "v_max3_u32 v218, v218, v246, v247\n\t" "v_max3_u32 v218, v218, v248, v249\n\t" "v_max3_u32 v218, v218, v250, v251\n\t" \
    "v_max3_u32 v218, v218, v252, v253\n\t" "v_max3_u32 v218, v218, v254, v255\n\t" "v_cmp_eq_u32 vcc, -1, v218\n\t" "s_cbranch_vccz 25f\n\t" "s_cmp_eq_u32 %[bd], 0\n\t" \
    "s_cbranch_scc1 25f\n\t" "s_sub_u32 %[bd], %[bd], 1\n\t" XCNT(101) "s_branch 24b\n\t" "25:\n\t" "ds_read_b128 v[160:163], %[l0] offset:0\n\t" \
    "ds_read_b128 v[164:167], %[l0] offset:1024\n\t" "ds_read_b128 v[168:171], %[l0] offset:2048\n\t" "ds_read_b128 v[172:175], %[l0] offset:3072\n\t" \
    "ds_read_b128 v[176:179], %[l0] offset:4096\n\t" "ds_read_b128 v[180:183], %[l0] offset:5120\n\t" "ds_read_b128 v[184:187], %[l0] offset:6144\n\t" \
    "ds_read_b128 v[188:191], %[l0] offset:7168\n\t" "v_mov_b64 v[224:225], 0\n\t" "v_mov_b64 v[226:227], 0\n\t" "v_mov_b64 v[228:229], 0\n\t" "v_mov_b64 v[230:231], 0\n\t" \
    "v_mov_b64 v[232:233], 0\n\t" "v_mov_b64 v[234:235], 0\n\t" "v_mov_b64 v[236:237], 0\n\t" "v_mov_b64 v[238:239], 0\n\t" "s_barrier\n\t" "s_branch 20b\n\t" "99:\n\t"
#define QK_STEP2 \
    ".p2align 6\n\t" "v_xor_b32 v216, 0x400, %[mo]\n\t" "s_xor_b32 s60, %[sb], 0x800\n\t" "s_add_u32 s61, %[qb], 0x1000\n\t" "s_mov_b32 s66, 0x3e0293ee\n\t" \
    "s_mov_b32 s67, 0x3e0293ee\n\t" XT(80, 81) XT(98, 99) "s_barrier\n\t" XT(82, 83) "buffer_load_dwordx4 v[240:243], %[mo], %[rs], %[sb] offen sc1\n\t" \
    "v_mov_b64 v[224:225], 0\n\t" "v_mov_b64 v[226:227], 0\n\t" "v_mov_b64 v[228:229], 0\n\t" "v_mov_b64 v[230:231], 0\n\t" "v_mov_b64 v[232:233], 0\n\t" \
    "v_mov_b64 v[234:235], 0\n\t" "v_mov_b64 v[236:237], 0\n\t" "v_mov_b64 v[238:239], 0\n\t" "buffer_load_dwordx4 v[244:247], v216, %[rs], %[sb] offen sc1\n\t" \
    "ds_read_b128 v[160:163], %[l0] offset:0\n\t" "ds_read_b128 v[164:167], %[l0] offset:1024\n\t" "ds_read_b128 v[168:171], %[l0] offset:2048\n\t" \
    "ds_read_b128 v[172:175], %[l0] offset:3072\n\t" "s_nop 3\n\t" "buffer_load_dwordx4 v[248:251], %[mo], %[rs], s60 offen sc1\n\t" \
    "ds_read_b128 v[176:179], %[l0] offset:4096\n\t" "ds_read_b128 v[180:183], %[l0] offset:5120\n\t" "ds_read_b128 v[184:187], %[l0] offset:6144\n\t" \
    "ds_read_b128 v[188:191], %[l0] offset:7168\n\t" "s_nop 3\n\t" "buffer_load_dwordx4 v[252:255], v216, %[rs], s60 offen sc1\n\t" "v_mov_b32 v219, 160512\n\t" \
    "v_mov_b64 v[220:221], -1\n\t" "v_mov_b64 v[222:223], -1\n\t" "buffer_store_dwordx4 v[220:223], %[px], %[rs], %[tr] offen sc1\n\t" \
    "buffer_store_dwordx2 v[220:221], %[pm], %[rs], %[tr] offen sc1\n\t" "v_mov_b32 v208, 0xd028965f\n\t" "v_mov_b32 v209, 0xff800000\n\t" "s_mov_b32 s62, 0\n\t" \
    "s_mov_b32 s63, -1\n\t" "v_mov_b32 v210, 0\n\t" "v_cmp_gt_i32 vcc, 0, %[ps]\n\t" "v_cndmask_b32 v204, 0, v209, s[62:63]\n\t" "s_nop 0\n\t" \
    "v_cndmask_b32 v200, 0, v208, vcc\n\t" "v_cmp_gt_i32 vcc, 1, %[ps]\n\t" "v_cndmask_b32 v205, 0, v209, s[62:63]\n\t" "s_nop 0\n\t" "v_cndmask_b32 v201, 0, v208, vcc\n\t" \
    "v_cmp_gt_i32 vcc, 2, %[ps]\n\t" "v_cndmask_b32 v206, 0, v209, s[62:63]\n\t" "s_nop 0\n\t" "v_cndmask_b32 v202, 0, v208, vcc\n\t" "v_cmp_gt_i32 vcc, 3, %[ps]\n\t" \
    "v_cndmask_b32 v207, 0, v209, s[62:63]\n\t" "s_nop 0\n\t" "v_cndmask_b32 v203, 0, v208, vcc\n\t" "30:\n\t" "s_waitcnt vmcnt(5)\n\t" XT(84, 85) \
    "v_smfmac_f32_16x16x64_bf16 v[224:227], v[240:243], a[0:7], %[ix]\n\t" "ds_write_b128 %[l1], v[240:243] offset:8192\n\t" \
    "v_smfmac_f32_16x16x64_bf16 v[228:231], v[240:243], a[8:15], %[ix]\n\t" "v_smfmac_f32_16x16x64_bf16 v[232:235], v[240:243], v[32:39], %[ix]\n\t" \
    "s_waitcnt lgkmcnt(7)\n\t" "v_smfmac_f32_16x16x64_bf16 v[236:239], v[240:243], v[160:167], %[ix]\n\t" "ds_read_b128 v[160:163], %[l0] offset:8192\n\t" \
    "ds_read_b128 v[164:167], %[l0] offset:9216\n\t" "s_waitcnt vmcnt(4)\n\t" "v_smfmac_f32_16x16x64_bf16 v[224:227], v[244:247], a[16:23], %[ix]\n\t" \
    "ds_write_b128 %[l1], v[244:247] offset:9216\n\t" "v_smfmac_f32_16x16x64_bf16 v[228:231], v[244:247], a[24:31], %[ix]\n\t" \
    "v_smfmac_f32_16x16x64_bf16 v[232:235], v[244:247], v[40:47], %[ix]\n\t" "s_waitcnt lgkmcnt(8)\n\t" \
    "v_smfmac_f32_16x16x64_bf16 v[236:239], v[244:247], v[168:175], %[ix]\n\t" "ds_read_b128 v[168:171], %[l0] offset:10240\n\t" \
    "ds_read_b128 v[172:175], %[l0] offset:11264\n\t" "s_waitcnt vmcnt(3)\n\t" "v_smfmac_f32_16x16x64_bf16 v[224:227], v[248:251], a[32:39], %[ix]\n\t" \
    "ds_write_b128 %[l1], v[248:251] offset:10240\n\t" "v_smfmac_f32_16x16x64_bf16 v[228:231], v[248:251], a[40:47], %[ix]\n\t" "s_waitcnt lgkmcnt(3)\n\t" "s_barrier\n\t" \
    XT(86, 87) "v_smfmac_f32_16x16x64_bf16 v[232:235], v[248:251], v[48:55], %[ix]\n\t" "ds_read_b128 v[192:195], %[l1] offset:12288\n\t" \
    "ds_read_b128 v[196:199], %[l1] offset:0\n\t" "v_smfmac_f32_16x16x64_bf16 v[236:239], v[248:251], v[176:183], %[ix]\n\t" "ds_read_b128 v[176:179], %[l0] offset:12288\n\t" \
    "ds_read_b128 v[180:183], %[l0] offset:13312\n\t" "s_waitcnt vmcnt(2)\n\t" "v_smfmac_f32_16x16x64_bf16 v[224:227], v[252:255], a[48:55], %[ix]\n\t" \
    "ds_write_b128 %[l1], v[252:255] offset:11264\n\t" "v_smfmac_f32_16x16x64_bf16 v[228:231], v[252:255], a[56:63], %[ix]\n\t" \
    "ds_read_b128 v[240:243], %[l1] offset:4096\n\t" "ds_read_b128 v[244:247], %[l1] offset:13312\n\t" \
    "v_smfmac_f32_16x16x64_bf16 v[232:235], v[252:255], v[56:63], %[ix]\n\t" "s_nop 1\n\t" "v_smfmac_f32_16x16x64_bf16 v[236:239], v[252:255], v[184:191], %[ix]\n\t" \
    "ds_read_b128 v[184:187], %[l0] offset:14336\n\t" "v_cmp_u_f32 vcc, v224, v225\n\t" "v_cmp_u_f32 s[62:63], v226, v227\n\t" "s_waitcnt lgkmcnt(7)\n\t" \
    "v_smfmac_f32_16x16x64_bf16 v[224:227], v[192:195], a[64:71], %[ix]\n\t" "ds_read_b128 v[188:191], %[l0] offset:15360\n\t" \
    "v_smfmac_f32_16x16x64_bf16 v[228:231], v[192:195], a[72:79], %[ix]\n\t" "s_or_b64 vcc, vcc, s[62:63]\n\t" "s_cbranch_scc1 31f\n\t" "32:\n\t" \
    "v_smfmac_f32_16x16x64_bf16 v[232:235], v[192:195], v[64:71], %[ix]\n\t" "s_waitcnt lgkmcnt(4)\n\t" "s_barrier\n\t" XT(88, 89) "ds_read_b32 v218, v219\n\t" \
    "v_smfmac_f32_16x16x64_bf16 v[236:239], v[192:195], v[160:167], %[ix]\n\t" "ds_read_b128 v[160:163], %[l0] offset:16384\n\t" \
    "ds_read_b128 v[164:167], %[l0] offset:17408\n\t" "v_smfmac_f32_16x16x64_bf16 v[224:227], v[196:199], a[80:87], %[ix]\n\t" \
    "ds_read_b128 v[248:251], %[l1] offset:1024\n\t" "ds_read_b128 v[252:255], %[l1] offset:5120\n\t" "v_smfmac_f32_16x16x64_bf16 v[228:231], v[196:199], a[88:95], %[ix]\n\t" \
    "ds_read_b128 v[192:195], %[l1] offset:14336\n\t" "v_smfmac_f32_16x16x64_bf16 v[232:235], v[196:199], v[72:79], %[ix]\n\t" \
    "v_smfmac_f32_16x16x64_bf16 v[236:239], v[196:199], v[168:175], %[ix]\n\t" "s_waitcnt lgkmcnt(5)\n\t" "v_cmp_eq_u32 vcc, %[rt], v218\n\t" \
    "v_smfmac_f32_16x16x64_bf16 v[224:227], v[240:243], a[96:103], %[ix]\n\t" "s_cbranch_vccnz 33f\n\t" "ds_read_b128 v[168:171], %[l0] offset:18432\n\t" \
    "v_smfmac_f32_16x16x64_bf16 v[228:231], v[240:243], a[104:111], %[ix]\n\t" "ds_read_b128 v[172:175], %[l0] offset:19456\n\t" \
    "ds_read_b128 v[196:199], %[l1] offset:2048\n\t" "v_smfmac_f32_16x16x64_bf16 v[232:235], v[240:243], v[80:87], %[ix]\n\t" \
    "buffer_load_dwordx4 v[16:19], %[hq], %[rs], %[qb] offen offset:0 sc1\n\t" "v_smfmac_f32_16x16x64_bf16 v[236:239], v[240:243], v[176:183], %[ix]\n\t" \
    "ds_read_b128 v[176:179], %[l0] offset:20480\n\t" "ds_read_b128 v[180:183], %[l0] offset:21504\n\t" \
    "v_smfmac_f32_16x16x64_bf16 v[224:227], v[244:247], a[112:119], %[ix]\n\t" "ds_read_b128 v[240:243], %[l1] offset:6144\n\t" \
    "v_smfmac_f32_16x16x64_bf16 v[228:231], v[244:247], a[120:127], %[ix]\n\t" "v_smfmac_f32_16x16x64_bf16 v[232:235], v[244:247], v[88:95], %[ix]\n\t" \
    "buffer_load_dwordx4 v[20:23], %[hq], %[rs], %[qb] offen offset:2048 sc1\n\t" "v_smfmac_f32_16x16x64_bf16 v[236:239], v[244:247], v[184:191], %[ix]\n\t" \
    "ds_read_b128 v[184:187], %[l0] offset:22528\n\t" "ds_read_b128 v[188:191], %[l0] offset:23552\n\t" "s_waitcnt lgkmcnt(10)\n\t" \
    "v_smfmac_f32_16x16x64_bf16 v[224:227], v[248:251], a[128:135], %[ix]\n\t" "ds_read_b128 v[244:247], %[l1] offset:15360\n\t" \
    "v_smfmac_f32_16x16x64_bf16 v[228:231], v[248:251], a[136:143], %[ix]\n\t" "v_smfmac_f32_16x16x64_bf16 v[232:235], v[248:251], v[96:103], %[ix]\n\t" \
    "buffer_load_dwordx4 v[24:27], %[hq], %[rs], s61 offen offset:0 sc1\n\t" "v_smfmac_f32_16x16x64_bf16 v[236:239], v[248:251], v[160:167], %[ix]\n\t" \
    "ds_read_b128 v[160:163], %[l0] offset:24576\n\t" "ds_read_b128 v[164:167], %[l0] offset:25600\n\t" "s_waitcnt lgkmcnt(12)\n\t" \
    "v_smfmac_f32_16x16x64_bf16 v[224:227], v[252:255], a[144:151], %[ix]\n\t" "ds_read_b128 v[248:251], %[l1] offset:3072\n\t" \
    "v_smfmac_f32_16x16x64_bf16 v[228:231], v[252:255], a[152:159], %[ix]\n\t" "v_smfmac_f32_16x16x64_bf16 v[232:235], v[252:255], v[104:111], %[ix]\n\t" \
    "buffer_load_dwordx4 v[28:31], %[hq], %[rs], s61 offen offset:2048 sc1\n\t" "s_waitcnt lgkmcnt(10)\n\t" \
    "v_smfmac_f32_16x16x64_bf16 v[236:239], v[252:255], v[168:175], %[ix]\n\t" "ds_read_b128 v[168:171], %[l0] offset:26624\n\t" \
    "ds_read_b128 v[172:175], %[l0] offset:27648\n\t" "v_smfmac_f32_16x16x64_bf16 v[224:227], v[192:195], a[160:167], %[ix]\n\t" \
    "ds_read_b128 v[252:255], %[l1] offset:7168\n\t" "v_smfmac_f32_16x16x64_bf16 v[228:231], v[192:195], a[168:175], %[ix]\n\t" "s_waitcnt lgkmcnt(12)\n\t" \
    "v_smfmac_f32_16x16x64_bf16 v[224:227], v[196:199], a[176:183], %[ix]\n\t" "v_smfmac_f32_16x16x64_bf16 v[228:231], v[196:199], a[184:191], %[ix]\n\t" \
    "s_waitcnt lgkmcnt(9)\n\t" "v_smfmac_f32_16x16x64_bf16 v[224:227], v[240:243], a[192:199], %[ix]\n\t" \
    "v_smfmac_f32_16x16x64_bf16 v[228:231], v[240:243], a[200:207], %[ix]\n\t" "s_waitcnt lgkmcnt(6)\n\t" \
    "v_smfmac_f32_16x16x64_bf16 v[224:227], v[244:247], a[208:215], %[ix]\n\t" "v_smfmac_f32_16x16x64_bf16 v[228:231], v[244:247], a[216:223], %[ix]\n\t" \
    "s_waitcnt vmcnt(4)\n\t" "s_waitcnt lgkmcnt(3)\n\t" "v_smfmac_f32_16x16x64_bf16 v[224:227], v[248:251], a[224:231], %[ix]\n\t" \
    "v_smfmac_f32_16x16x64_bf16 v[228:231], v[248:251], a[232:239], %[ix]\n\t" "s_waitcnt lgkmcnt(0)\n\t" \
    "v_smfmac_f32_16x16x64_bf16 v[224:227], v[252:255], a[240:247], %[ix]\n\t" "s_nop 1\n\t" "v_smfmac_f32_16x16x64_bf16 v[228:231], v[252:255], a[248:255], %[ix]\n\t" \
    XT(90, 91) "s_nop 1\n\t" "v_smfmac_f32_16x16x64_bf16 v[232:235], v[192:195], v[112:119], %[ix]\n\t" "s_nop 1\n\t" \
    "v_smfmac_f32_16x16x64_bf16 v[236:239], v[192:195], v[176:183], %[ix]\n\t" "ds_read_b128 v[176:179], %[l0] offset:28672\n\t" "v_add_f32 v224, v224, v225\n\t" \
    "v_add_f32 v226, v226, v227\n\t" "v_smfmac_f32_16x16x64_bf16 v[232:235], v[196:199], v[120:127], %[ix]\n\t" "ds_read_b128 v[180:183], %[l0] offset:29696\n\t" \
    "v_add_f32 v228, v228, v229\n\t" "v_add_f32 v230, v230, v231\n\t" "v_smfmac_f32_16x16x64_bf16 v[236:239], v[196:199], v[184:191], %[ix]\n\t" \
    "ds_read_b128 v[184:187], %[l0] offset:30720\n\t" "v_cvt_pk_bf16_f32 v218, v224, v228\n\t" "v_cvt_pk_bf16_f32 v219, v226, v230\n\t" \
    "v_smfmac_f32_16x16x64_bf16 v[232:235], v[240:243], v[128:135], %[ix]\n\t" "ds_write2_b32 %[qw], v218, v219 offset1:8\n\t" \
    "ds_read_b128 v[188:191], %[l0] offset:31744\n\t" "v_smfmac_f32_16x16x64_bf16 v[236:239], v[240:243], v[160:167], %[ix]\n\t" \
    "v_smfmac_f32_16x16x64_bf16 v[232:235], v[244:247], v[136:143], %[ix]\n\t" "v_smfmac_f32_16x16x64_bf16 v[236:239], v[244:247], v[168:175], %[ix]\n\t" \
    "s_waitcnt lgkmcnt(1)\n\t" "s_barrier\n\t" "v_smfmac_f32_16x16x64_bf16 v[232:235], v[248:251], v[144:151], %[ix]\n\t" \
    "v_smfmac_f32_16x16x64_bf16 v[236:239], v[248:251], v[176:183], %[ix]\n\t" "v_smfmac_f32_16x16x64_bf16 v[232:235], v[252:255], v[152:159], %[ix]\n\t" \
    "s_waitcnt lgkmcnt(0)\n\t" "v_smfmac_f32_16x16x64_bf16 v[236:239], v[252:255], v[184:191], %[ix]\n\t" XT(92, 93) "s_nop 6\n\t" "v_add_f32 v232, v232, v233\n\t" \
    "v_add_f32 v234, v234, v235\n\t" "v_add_f32 v236, v236, v237\n\t" "v_add_f32 v238, v238, v239\n\t" "v_cvt_pk_bf16_f32 v232, v232, v236\n\t" \
    "v_cvt_pk_bf16_f32 v233, v234, v238\n\t" "ds_write2_b32 %[kw], v232, v233 offset1:68\n\t" "s_waitcnt lgkmcnt(0)\n\t" "s_barrier\n\t" XT(94, 95) \
    "s_cmp_ge_u32 %[sq], %[sn]\n\t" "s_cbranch_scc1 377f\n\t" "375:\n\t" "global_load_dword v240, v210, %[fq] sc1\n\t" "s_waitcnt vmcnt(0)\n\t" \
    "v_readfirstlane_b32 %[sq], v240\n\t" "s_nop 3\n\t" "s_cmp_ge_u32 %[sq], %[sn]\n\t" "s_cbranch_scc1 376f\n\t" "s_cmp_eq_u32 %[bd], 0\n\t" "s_cbranch_scc1 376f\n\t" \
    "s_sub_u32 %[bd], %[bd], 1\n\t" "s_sleep 2\n\t" "s_branch 375b\n\t" "376:\n\t" "buffer_load_dwordx4 v[16:19], %[hq], %[rs], %[qb] offen offset:0 sc1\n\t" \
    "buffer_load_dwordx4 v[20:23], %[hq], %[rs], %[qb] offen offset:2048 sc1\n\t" "buffer_load_dwordx4 v[24:27], %[hq], %[rs], s61 offen offset:0 sc1\n\t" \
    "buffer_load_dwordx4 v[28:31], %[hq], %[rs], s61 offen offset:2048 sc1\n\t" "377:\n\t" "s_nop 3\n\t" "ds_read_b128 v[160:163], %[kx] offset:0\n\t" \
    "ds_read_b128 v[164:167], %[kx] offset:2048\n\t" "ds_read_b128 v[168:171], %[kx] offset:4096\n\t" "ds_read_b128 v[172:175], %[kx] offset:6144\n\t" \
    "ds_read_b128 v[176:179], %[kx] offset:256\n\t" "ds_read_b128 v[180:183], %[kx] offset:2304\n\t" "ds_read_b128 v[184:187], %[kx] offset:4352\n\t" \
    "ds_read_b128 v[188:191], %[kx] offset:6400\n\t" "ds_read_b128 v[224:227], %[km] offset:0\n\t" "ds_read_b128 v[228:231], %[km] offset:64\n\t" \
    "ds_read_b128 v[232:235], %[km] offset:128\n\t" "ds_read_b128 v[236:239], %[km] offset:192\n\t" "s_waitcnt vmcnt(0)\n\t" "global_load_dword v240, v210, %[fq] sc1\n\t" \
    "s_waitcnt lgkmcnt(11)\n\t" "v_mfma_f32_16x16x32_bf16 v[248:251], v[160:163], v[16:19], 0\n\t" "s_waitcnt lgkmcnt(10)\n\t" \
    "v_mfma_f32_16x16x32_bf16 v[248:251], v[164:167], v[20:23], v[248:251]\n\t" "s_waitcnt lgkmcnt(9)\n\t" \
    "v_mfma_f32_16x16x32_bf16 v[248:251], v[168:171], v[24:27], v[248:251]\n\t" "s_waitcnt lgkmcnt(8)\n\t" \
    "v_mfma_f32_16x16x32_bf16 v[248:251], v[172:175], v[28:31], v[248:251]\n\t" "s_waitcnt lgkmcnt(7)\n\t" \
    "v_mfma_f32_16x16x32_bf16 v[252:255], v[176:179], v[16:19], v[200:203]\n\t" "s_waitcnt lgkmcnt(6)\n\t" \
    "v_mfma_f32_16x16x32_bf16 v[252:255], v[180:183], v[20:23], v[252:255]\n\t" "s_waitcnt lgkmcnt(5)\n\t" \
    "v_mfma_f32_16x16x32_bf16 v[252:255], v[184:187], v[24:27], v[252:255]\n\t" "s_waitcnt lgkmcnt(4)\n\t" \
    "v_mfma_f32_16x16x32_bf16 v[252:255], v[188:191], v[28:31], v[252:255]\n\t" "s_waitcnt lgkmcnt(3)\n\t" \
    "v_mfma_f32_16x16x32_bf16 v[244:247], v[224:227], v[16:19], v[204:207]\n\t" "s_waitcnt lgkmcnt(2)\n\t" \
    "v_mfma_f32_16x16x32_bf16 v[244:247], v[228:231], v[20:23], v[244:247]\n\t" "s_waitcnt lgkmcnt(1)\n\t" \
    "v_mfma_f32_16x16x32_bf16 v[244:247], v[232:235], v[24:27], v[244:247]\n\t" "s_waitcnt lgkmcnt(0)\n\t" \
    "v_mfma_f32_16x16x32_bf16 v[244:247], v[236:239], v[28:31], v[244:247]\n\t" "s_nop 3\n\t" "v_max3_f32 v242, v248, v249, v250\n\t" "v_max3_f32 v242, v242, v251, v252\n\t" \
    "v_max3_f32 v242, v242, v253, v254\n\t" "v_max_f32 v242, v242, v255\n\t" "v_max3_f32 v242, v242, v244, v245\n\t" "v_max3_f32 v242, v242, v246, v247\n\t" \
    "v_mov_b32 v243, v242\n\t" "s_nop 1\n\t" "v_permlane32_swap_b32 v242, v243\n\t" "v_max_f32 v242, v242, v243\n\t" "v_mov_b32 v243, v242\n\t" "s_nop 1\n\t" \
    "v_permlane16_swap_b32 v242, v243\n\t" "v_max_f32 v242, v242, v243\n\t" "v_pk_add_f32 v[244:245], v[244:245], v[242:243] op_sel_hi:[1,0] neg_lo:[0,1] neg_hi:[0,1]\n\t" \
    "v_pk_add_f32 v[246:247], v[246:247], v[242:243] op_sel_hi:[1,0] neg_lo:[0,1] neg_hi:[0,1]\n\t" \
    "v_pk_add_f32 v[248:249], v[248:249], v[242:243] op_sel_hi:[1,0] neg_lo:[0,1] neg_hi:[0,1]\n\t" \
    "v_pk_add_f32 v[250:251], v[250:251], v[242:243] op_sel_hi:[1,0] neg_lo:[0,1] neg_hi:[0,1]\n\t" \
    "v_pk_add_f32 v[252:253], v[252:253], v[242:243] op_sel_hi:[1,0] neg_lo:[0,1] neg_hi:[0,1]\n\t" \
    "v_pk_add_f32 v[254:255], v[254:255], v[242:243] op_sel_hi:[1,0] neg_lo:[0,1] neg_hi:[0,1]\n\t" "v_pk_mul_f32 v[244:245], v[244:245], s[66:67]\n\t" \
    "v_pk_mul_f32 v[246:247], v[246:247], s[66:67]\n\t" "v_pk_mul_f32 v[248:249], v[248:249], s[66:67]\n\t" "v_pk_mul_f32 v[250:251], v[250:251], s[66:67]\n\t" \
    "v_pk_mul_f32 v[252:253], v[252:253], s[66:67]\n\t" "v_pk_mul_f32 v[254:255], v[254:255], s[66:67]\n\t" "v_exp_f32 v244, v244\n\t" "v_exp_f32 v245, v245\n\t" \
    "v_exp_f32 v246, v246\n\t" "v_exp_f32 v247, v247\n\t" "v_exp_f32 v248, v248\n\t" "v_exp_f32 v249, v249\n\t" "v_exp_f32 v250, v250\n\t" "v_exp_f32 v251, v251\n\t" \
    "v_exp_f32 v252, v252\n\t" "v_exp_f32 v253, v253\n\t" "v_exp_f32 v254, v254\n\t" "v_exp_f32 v255, v255\n\t" "v_pk_add_f32 v[242:243], v[244:245], v[246:247]\n\t" \
    "v_pk_add_f32 v[242:243], v[242:243], v[248:249]\n\t" "v_pk_add_f32 v[242:243], v[242:243], v[250:251]\n\t" "v_pk_add_f32 v[242:243], v[242:243], v[252:253]\n\t" \
    "v_pk_add_f32 v[242:243], v[242:243], v[254:255]\n\t" "v_add_f32 v242, v242, v243\n\t" "v_mov_b32 v243, v242\n\t" "s_nop 1\n\t" "v_permlane32_swap_b32 v242, v243\n\t" \
    "v_add_f32 v242, v242, v243\n\t" "v_mov_b32 v243, v242\n\t" "s_nop 1\n\t" "v_permlane16_swap_b32 v242, v243\n\t" "v_add_f32 v242, v242, v243\n\t" \
    "v_rcp_f32 v242, v242\n\t" "s_nop 0\n\t" "v_pk_mul_f32 v[244:245], v[244:245], v[242:243] op_sel_hi:[1,0]\n\t" \
    "v_pk_mul_f32 v[246:247], v[246:247], v[242:243] op_sel_hi:[1,0]\n\t" "v_pk_mul_f32 v[248:249], v[248:249], v[242:243] op_sel_hi:[1,0]\n\t" \
    "v_pk_mul_f32 v[250:251], v[250:251], v[242:243] op_sel_hi:[1,0]\n\t" "v_pk_mul_f32 v[252:253], v[252:253], v[242:243] op_sel_hi:[1,0]\n\t" \
    "v_pk_mul_f32 v[254:255], v[254:255], v[242:243] op_sel_hi:[1,0]\n\t" "v_cvt_pk_bf16_f32 v162, v244, v245\n\t" "v_cvt_pk_bf16_f32 v163, v246, v247\n\t" \
    "v_cvt_pk_bf16_f32 v164, v248, v249\n\t" "v_cvt_pk_bf16_f32 v165, v250, v251\n\t" "v_cvt_pk_bf16_f32 v166, v252, v253\n\t" "v_cvt_pk_bf16_f32 v167, v254, v255\n\t" \
    "buffer_store_dwordx4 v[164:167], %[px], %[rs], %[tb] offen sc1\n\t" "buffer_store_dwordx2 v[162:163], %[pm], %[rs], %[tb] offen sc1\n\t" "s_waitcnt vmcnt(2)\n\t" \
    "v_readfirstlane_b32 s62, v240\n\t" "s_nop 3\n\t" "s_max_u32 %[sq], %[sq], s62\n\t" "s_barrier\n\t" XT(96, 97) "s_branch 99f\n\t" "31:\n\t" \
    "ds_read_b128 v[220:223], %[l1] offset:8192\n\t" "s_waitcnt lgkmcnt(0)\n\t" "v_max3_u32 v218, v220, v221, v222\n\t" "v_max_u32 v218, v218, v223\n\t" \
    "ds_read_b128 v[220:223], %[l1] offset:9216\n\t" "s_waitcnt lgkmcnt(0)\n\t" "v_max3_u32 v218, v218, v220, v221\n\t" "v_max3_u32 v218, v218, v222, v223\n\t" \
    "ds_read_b128 v[220:223], %[l1] offset:10240\n\t" "s_waitcnt lgkmcnt(0)\n\t" "v_max3_u32 v218, v218, v220, v221\n\t" "v_max3_u32 v218, v218, v222, v223\n\t" \
    "ds_read_b128 v[220:223], %[l1] offset:11264\n\t" "s_waitcnt lgkmcnt(0)\n\t" "v_max3_u32 v218, v218, v220, v221\n\t" "v_max3_u32 v218, v218, v222, v223\n\t" \
    "v_cmp_eq_u32 vcc, -1, v218\n\t" "s_cbranch_vccz 32b\n\t" "s_cmp_eq_u32 %[rt], 1\n\t" "s_cbranch_scc1 32b\n\t" "v_mov_b32 v218, %[rt]\n\t" "ds_write_b32 v219, v218\n\t" \
    "s_waitcnt lgkmcnt(0)\n\t" "s_branch 32b\n\t" "33:\n\t" "s_sub_u32 %[rt], %[rt], 1\n\t" "s_waitcnt lgkmcnt(0)\n\t" XCNT(100) "34:\n\t" \
    "buffer_load_dwordx4 v[240:243], %[mo], %[rs], %[sb] offen sc1\n\t" "buffer_load_dwordx4 v[244:247], v216, %[rs], %[sb] offen sc1\n\t" \
    "buffer_load_dwordx4 v[248:251], %[mo], %[rs], s60 offen sc1\n\t" "buffer_load_dwordx4 v[252:255], v216, %[rs], s60 offen sc1\n\t" "s_waitcnt vmcnt(0)\n\t" \
    "v_max_u32 v218, v240, v241\n\t" "v_max3_u32 v218, v218, v242, v243\n\t" "v_max3_u32 v218, v218, v244, v245\n\t" "v_max3_u32 v218, v218, v246, v247\n\t" \
    "v_max3_u32 v218, v218, v248, v249\n\t" "v_max3_u32 v218, v218, v250, v251\n\t" "v_max3_u32 v218, v218, v252, v253\n\t" "v_max3_u32 v218, v218, v254, v255\n\t" \
    "v_cmp_eq_u32 vcc, -1, v218\n\t" "s_cbranch_vccz 35f\n\t" "s_cmp_eq_u32 %[bd], 0\n\t" "s_cbranch_scc1 35f\n\t" "s_sub_u32 %[bd], %[bd], 1\n\t" XCNT(101) \
    "s_branch 34b\n\t" "35:\n\t" "ds_read_b128 v[160:163], %[l0] offset:0\n\t" "ds_read_b128 v[164:167], %[l0] offset:1024\n\t" \
    "ds_read_b128 v[168:171], %[l0] offset:2048\n\t" "ds_read_b128 v[172:175], %[l0] offset:3072\n\t" "ds_read_b128 v[176:179], %[l0] offset:4096\n\t" \
    "ds_read_b128 v[180:183], %[l0] offset:5120\n\t" "ds_read_b128 v[184:187], %[l0] offset:6144\n\t" "ds_read_b128 v[188:191], %[l0] offset:7168\n\t" \
    "v_mov_b64 v[224:225], 0\n\t" "v_mov_b64 v[226:227], 0\n\t" "v_mov_b64 v[228:229], 0\n\t" "v_mov_b64 v[230:231], 0\n\t" "v_mov_b64 v[232:233], 0\n\t" \
    "v_mov_b64 v[234:235], 0\n\t" "v_mov_b64 v[236:237], 0\n\t" "v_mov_b64 v[238:239], 0\n\t" "s_barrier\n\t" "s_branch 30b\n\t" "99:\n\t"
#define QK_STEP3 \
    ".p2align 6\n\t" "v_xor_b32 v216, 0x400, %[mo]\n\t" "s_xor_b32 s60, %[sb], 0x800\n\t" "s_mov_b32 s67, %[pb]\n\t" XT(80, 81) "s_getpc_b64 s[62:63]\n\t" "48:\n\t" \
    "s_memtime s[64:65]\n\t" "s_waitcnt lgkmcnt(0)\n\t" "s_sub_u32 s61, %[tg], s64\n\t" "s_cmp_gt_i32 s61, 124\n\t" "s_cbranch_scc1 48b\n\t" "s_max_i32 s61, s61, 0\n\t" \
    "s_and_b32 s61, s61, 124\n\t" "s_sub_u32 s61, 49f-48b, s61\n\t" "s_add_u32 s62, s62, s61\n\t" "s_addc_u32 s63, s63, 0\n\t" "s_setpc_b64 s[62:63]\n\t" ".rept 31\n\t" \
    "s_nop 0\n\t" ".endr\n\t" "49:\n\t" XT(98, 99) "s_barrier\n\t" XT(82, 83) "buffer_load_dwordx4 v[240:243], %[mo], %[rs], %[sb] offen sc1\n\t" \
    "v_mov_b64 v[224:225], 0\n\t" "v_mov_b64 v[226:227], 0\n\t" "v_mov_b64 v[228:229], 0\n\t" "v_mov_b64 v[230:231], 0\n\t" "v_mov_b64 v[232:233], 0\n\t" \
    "v_mov_b64 v[234:235], 0\n\t" "v_mov_b64 v[236:237], 0\n\t" "v_mov_b64 v[238:239], 0\n\t" "buffer_load_dwordx4 v[244:247], v216, %[rs], %[sb] offen sc1\n\t" \
    "ds_read_b128 v[160:163], %[l0] offset:0\n\t" "ds_read_b128 v[164:167], %[l0] offset:1024\n\t" "ds_read_b128 v[168:171], %[l0] offset:2048\n\t" \
    "ds_read_b128 v[172:175], %[l0] offset:3072\n\t" "s_nop 3\n\t" "buffer_load_dwordx4 v[248:251], %[mo], %[rs], s60 offen sc1\n\t" \
    "ds_read_b128 v[176:179], %[l0] offset:4096\n\t" "ds_read_b128 v[180:183], %[l0] offset:5120\n\t" "ds_read_b128 v[184:187], %[l0] offset:6144\n\t" \
    "ds_read_b128 v[188:191], %[l0] offset:7168\n\t" "s_nop 3\n\t" "buffer_load_dwordx4 v[252:255], v216, %[rs], s60 offen sc1\n\t" "v_mov_b32 v219, 160512\n\t" \
    "v_mov_b32 v220, -1\n\t" "buffer_store_dword v220, %[hq], %[rs], %[pr] offen sc1\n\t" "40:\n\t" "s_waitcnt vmcnt(4)\n\t" XT(84, 85) \
    "v_smfmac_f32_16x16x64_bf16 v[224:227], v[240:243], a[0:7], %[ix]\n\t" "ds_write_b128 %[l1], v[240:243] offset:12288\n\t" \
    "v_smfmac_f32_16x16x64_bf16 v[228:231], v[240:243], a[8:15], %[ix]\n\t" "v_smfmac_f32_16x16x64_bf16 v[232:235], v[240:243], v[32:39], %[ix]\n\t" \
    "s_waitcnt lgkmcnt(7)\n\t" "v_smfmac_f32_16x16x64_bf16 v[236:239], v[240:243], v[160:167], %[ix]\n\t" "ds_read_b128 v[160:163], %[l0] offset:8192\n\t" \
    "ds_read_b128 v[164:167], %[l0] offset:9216\n\t" "s_waitcnt vmcnt(3)\n\t" "v_smfmac_f32_16x16x64_bf16 v[224:227], v[244:247], a[16:23], %[ix]\n\t" \
    "ds_write_b128 %[l1], v[244:247] offset:13312\n\t" "v_smfmac_f32_16x16x64_bf16 v[228:231], v[244:247], a[24:31], %[ix]\n\t" \
    "v_smfmac_f32_16x16x64_bf16 v[232:235], v[244:247], v[40:47], %[ix]\n\t" "s_waitcnt lgkmcnt(8)\n\t" \
    "v_smfmac_f32_16x16x64_bf16 v[236:239], v[244:247], v[168:175], %[ix]\n\t" "ds_read_b128 v[168:171], %[l0] offset:10240\n\t" \
    "ds_read_b128 v[172:175], %[l0] offset:11264\n\t" "s_waitcnt vmcnt(2)\n\t" "v_smfmac_f32_16x16x64_bf16 v[224:227], v[248:251], a[32:39], %[ix]\n\t" \
    "ds_write_b128 %[l1], v[248:251] offset:14336\n\t" "v_smfmac_f32_16x16x64_bf16 v[228:231], v[248:251], a[40:47], %[ix]\n\t" "s_waitcnt lgkmcnt(3)\n\t" "s_barrier\n\t" \
    XT(86, 87) "v_smfmac_f32_16x16x64_bf16 v[232:235], v[248:251], v[48:55], %[ix]\n\t" "ds_read_b128 v[192:195], %[l1] offset:0\n\t" \
    "ds_read_b128 v[196:199], %[l1] offset:4096\n\t" "v_smfmac_f32_16x16x64_bf16 v[236:239], v[248:251], v[176:183], %[ix]\n\t" \
    "ds_read_b128 v[176:179], %[l0] offset:12288\n\t" "ds_read_b128 v[180:183], %[l0] offset:13312\n\t" "s_waitcnt vmcnt(1)\n\t" \
    "v_smfmac_f32_16x16x64_bf16 v[224:227], v[252:255], a[48:55], %[ix]\n\t" "ds_write_b128 %[l1], v[252:255] offset:15360\n\t" \
    "v_smfmac_f32_16x16x64_bf16 v[228:231], v[252:255], a[56:63], %[ix]\n\t" "ds_read_b128 v[240:243], %[l1] offset:8192\n\t" "ds_read_b128 v[244:247], %[l1] offset:1024\n\t" \
    "v_smfmac_f32_16x16x64_bf16 v[232:235], v[252:255], v[56:63], %[ix]\n\t" "s_nop 1\n\t" "v_smfmac_f32_16x16x64_bf16 v[236:239], v[252:255], v[184:191], %[ix]\n\t" \
    "ds_read_b128 v[184:187], %[l0] offset:14336\n\t" "v_cmp_u_f32 vcc, v224, v225\n\t" "v_cmp_u_f32 s[62:63], v226, v227\n\t" "s_waitcnt lgkmcnt(7)\n\t" \
    "v_smfmac_f32_16x16x64_bf16 v[224:227], v[192:195], a[64:71], %[ix]\n\t" "ds_read_b128 v[188:191], %[l0] offset:15360\n\t" \
    "v_smfmac_f32_16x16x64_bf16 v[228:231], v[192:195], a[72:79], %[ix]\n\t" "s_or_b64 vcc, vcc, s[62:63]\n\t" "s_cbranch_scc1 41f\n\t" "42:\n\t" \
    "v_smfmac_f32_16x16x64_bf16 v[232:235], v[192:195], v[64:71], %[ix]\n\t" "s_waitcnt lgkmcnt(4)\n\t" "s_barrier\n\t" XT(88, 89) "ds_read_b32 v218, v219\n\t" \
    "v_smfmac_f32_16x16x64_bf16 v[236:239], v[192:195], v[160:167], %[ix]\n\t" "s_memtime s[64:65]\n\t" "ds_read_b128 v[160:163], %[l0] offset:16384\n\t" \
    "v_smfmac_f32_16x16x64_bf16 v[224:227], v[196:199], a[80:87], %[ix]\n\t" "ds_read_b128 v[164:167], %[l0] offset:17408\n\t" \
    "ds_read_b128 v[248:251], %[l1] offset:5120\n\t" "v_smfmac_f32_16x16x64_bf16 v[228:231], v[196:199], a[88:95], %[ix]\n\t" "ds_read_b128 v[252:255], %[l1] offset:9216\n\t" \
    "ds_read_b128 v[192:195], %[l1] offset:2048\n\t" "v_smfmac_f32_16x16x64_bf16 v[232:235], v[196:199], v[72:79], %[ix]\n\t" "s_sub_u32 s62, s64, %[ca]\n\t" \
    "s_mul_hi_u32 s63, s62, %[k1]\n\t" "s_mul_i32 s62, s62, %[k1]\n\t" "v_smfmac_f32_16x16x64_bf16 v[236:239], v[196:199], v[168:175], %[ix]\n\t" "s_waitcnt lgkmcnt(5)\n\t" \
    "v_cmp_eq_u32 vcc, %[rt], v218\n\t" "s_lshr_b64 s[62:63], s[62:63], 16\n\t" "s_add_u32 s62, s62, %[ga]\n\t" "s_bitset0_b32 s62, 0\n\t" \
    "v_smfmac_f32_16x16x64_bf16 v[224:227], v[240:243], a[96:103], %[ix]\n\t" "s_cbranch_vccnz 43f\n\t" "ds_read_b128 v[168:171], %[l0] offset:18432\n\t" \
    "v_smfmac_f32_16x16x64_bf16 v[228:231], v[240:243], a[104:111], %[ix]\n\t" "ds_read_b128 v[172:175], %[l0] offset:19456\n\t" "v_mov_b32 v217, s62\n\t" \
    "buffer_store_dword v217, %[hq], %[rs], s67 offen sc1\n\t" "v_smfmac_f32_16x16x64_bf16 v[232:235], v[240:243], v[80:87], %[ix]\n\t" \
    "ds_read_b128 v[196:199], %[l1] offset:6144\n\t" "v_smfmac_f32_16x16x64_bf16 v[236:239], v[240:243], v[176:183], %[ix]\n\t" \
    "ds_read_b128 v[176:179], %[l0] offset:20480\n\t" "ds_read_b128 v[180:183], %[l0] offset:21504\n\t" \
    "v_smfmac_f32_16x16x64_bf16 v[224:227], v[244:247], a[112:119], %[ix]\n\t" "ds_read_b128 v[240:243], %[l1] offset:10240\n\t" \
    "v_smfmac_f32_16x16x64_bf16 v[228:231], v[244:247], a[120:127], %[ix]\n\t" "v_smfmac_f32_16x16x64_bf16 v[232:235], v[244:247], v[88:95], %[ix]\n\t" \
    "v_smfmac_f32_16x16x64_bf16 v[236:239], v[244:247], v[184:191], %[ix]\n\t" "ds_read_b128 v[184:187], %[l0] offset:22528\n\t" \
    "ds_read_b128 v[188:191], %[l0] offset:23552\n\t" "s_waitcnt lgkmcnt(10)\n\t" "v_smfmac_f32_16x16x64_bf16 v[224:227], v[248:251], a[128:135], %[ix]\n\t" \
    "ds_read_b128 v[244:247], %[l1] offset:3072\n\t" "v_smfmac_f32_16x16x64_bf16 v[228:231], v[248:251], a[136:143], %[ix]\n\t" \
    "v_smfmac_f32_16x16x64_bf16 v[232:235], v[248:251], v[96:103], %[ix]\n\t" "v_smfmac_f32_16x16x64_bf16 v[236:239], v[248:251], v[160:167], %[ix]\n\t" \
    "ds_read_b128 v[160:163], %[l0] offset:24576\n\t" "ds_read_b128 v[164:167], %[l0] offset:25600\n\t" "s_waitcnt lgkmcnt(12)\n\t" \
    "v_smfmac_f32_16x16x64_bf16 v[224:227], v[252:255], a[144:151], %[ix]\n\t" "ds_read_b128 v[248:251], %[l1] offset:7168\n\t" \
    "v_smfmac_f32_16x16x64_bf16 v[228:231], v[252:255], a[152:159], %[ix]\n\t" "v_smfmac_f32_16x16x64_bf16 v[232:235], v[252:255], v[104:111], %[ix]\n\t" \
    "s_waitcnt lgkmcnt(10)\n\t" "v_smfmac_f32_16x16x64_bf16 v[236:239], v[252:255], v[168:175], %[ix]\n\t" "ds_read_b128 v[168:171], %[l0] offset:26624\n\t" \
    "ds_read_b128 v[172:175], %[l0] offset:27648\n\t" "v_smfmac_f32_16x16x64_bf16 v[224:227], v[192:195], a[160:167], %[ix]\n\t" \
    "ds_read_b128 v[252:255], %[l1] offset:11264\n\t" "v_smfmac_f32_16x16x64_bf16 v[228:231], v[192:195], a[168:175], %[ix]\n\t" "s_waitcnt lgkmcnt(12)\n\t" \
    "v_smfmac_f32_16x16x64_bf16 v[224:227], v[196:199], a[176:183], %[ix]\n\t" "v_smfmac_f32_16x16x64_bf16 v[228:231], v[196:199], a[184:191], %[ix]\n\t" \
    "s_waitcnt lgkmcnt(9)\n\t" "v_smfmac_f32_16x16x64_bf16 v[224:227], v[240:243], a[192:199], %[ix]\n\t" \
    "v_smfmac_f32_16x16x64_bf16 v[228:231], v[240:243], a[200:207], %[ix]\n\t" "s_waitcnt lgkmcnt(6)\n\t" \
    "v_smfmac_f32_16x16x64_bf16 v[224:227], v[244:247], a[208:215], %[ix]\n\t" "v_smfmac_f32_16x16x64_bf16 v[228:231], v[244:247], a[216:223], %[ix]\n\t" \
    "s_waitcnt lgkmcnt(3)\n\t" "v_smfmac_f32_16x16x64_bf16 v[224:227], v[248:251], a[224:231], %[ix]\n\t" \
    "v_smfmac_f32_16x16x64_bf16 v[228:231], v[248:251], a[232:239], %[ix]\n\t" "s_waitcnt lgkmcnt(0)\n\t" \
    "v_smfmac_f32_16x16x64_bf16 v[224:227], v[252:255], a[240:247], %[ix]\n\t" "s_nop 1\n\t" "v_smfmac_f32_16x16x64_bf16 v[228:231], v[252:255], a[248:255], %[ix]\n\t" \
    XT(90, 91) "s_nop 1\n\t" "v_smfmac_f32_16x16x64_bf16 v[232:235], v[192:195], v[112:119], %[ix]\n\t" "s_nop 1\n\t" \
    "v_smfmac_f32_16x16x64_bf16 v[236:239], v[192:195], v[176:183], %[ix]\n\t" "ds_read_b128 v[176:179], %[l0] offset:28672\n\t" "v_add_f32 v224, v224, v225\n\t" \
    "v_add_f32 v226, v226, v227\n\t" "v_smfmac_f32_16x16x64_bf16 v[232:235], v[196:199], v[120:127], %[ix]\n\t" "ds_read_b128 v[180:183], %[l0] offset:29696\n\t" \
    "v_add_f32 v228, v228, v229\n\t" "v_add_f32 v230, v230, v231\n\t" "v_smfmac_f32_16x16x64_bf16 v[236:239], v[196:199], v[184:191], %[ix]\n\t" \
    "ds_read_b128 v[184:187], %[l0] offset:30720\n\t" "v_cvt_pk_bf16_f32 v218, v224, v228\n\t" "v_cvt_pk_bf16_f32 v219, v226, v230\n\t" \
    "v_smfmac_f32_16x16x64_bf16 v[232:235], v[240:243], v[128:135], %[ix]\n\t" "ds_write2_b32 %[qw], v218, v219 offset1:8\n\t" \
    "ds_read_b128 v[188:191], %[l0] offset:31744\n\t" "v_smfmac_f32_16x16x64_bf16 v[236:239], v[240:243], v[160:167], %[ix]\n\t" \
    "v_smfmac_f32_16x16x64_bf16 v[232:235], v[244:247], v[136:143], %[ix]\n\t" "v_smfmac_f32_16x16x64_bf16 v[236:239], v[244:247], v[168:175], %[ix]\n\t" \
    "s_waitcnt lgkmcnt(1)\n\t" "s_barrier\n\t" "v_smfmac_f32_16x16x64_bf16 v[232:235], v[248:251], v[144:151], %[ix]\n\t" \
    "v_smfmac_f32_16x16x64_bf16 v[236:239], v[248:251], v[176:183], %[ix]\n\t" "v_smfmac_f32_16x16x64_bf16 v[232:235], v[252:255], v[152:159], %[ix]\n\t" \
    "s_waitcnt lgkmcnt(0)\n\t" "v_smfmac_f32_16x16x64_bf16 v[236:239], v[252:255], v[184:191], %[ix]\n\t" XT(92, 93) "s_nop 6\n\t" "v_add_f32 v232, v232, v233\n\t" \
    "v_add_f32 v234, v234, v235\n\t" "v_add_f32 v236, v236, v237\n\t" "v_add_f32 v238, v238, v239\n\t" "v_cvt_pk_bf16_f32 v232, v232, v236\n\t" \
    "v_cvt_pk_bf16_f32 v233, v234, v238\n\t" "ds_write2_b32 %[kw], v232, v233 offset1:68\n\t" "s_waitcnt lgkmcnt(0)\n\t" "s_barrier\n\t" XT(94, 95) XT(96, 97) \
    "s_branch 99f\n\t" "41:\n\t" "ds_read_b128 v[220:223], %[l1] offset:12288\n\t" "s_waitcnt lgkmcnt(0)\n\t" "v_max3_u32 v218, v220, v221, v222\n\t" \
    "v_max_u32 v218, v218, v223\n\t" "ds_read_b128 v[220:223], %[l1] offset:13312\n\t" "s_waitcnt lgkmcnt(0)\n\t" "v_max3_u32 v218, v218, v220, v221\n\t" \
    "v_max3_u32 v218, v218, v222, v223\n\t" "ds_read_b128 v[220:223], %[l1] offset:14336\n\t" "s_waitcnt lgkmcnt(0)\n\t" "v_max3_u32 v218, v218, v220, v221\n\t" \
    "v_max3_u32 v218, v218, v222, v223\n\t" "ds_read_b128 v[220:223], %[l1] offset:15360\n\t" "s_waitcnt lgkmcnt(0)\n\t" "v_max3_u32 v218, v218, v220, v221\n\t" \
    "v_max3_u32 v218, v218, v222, v223\n\t" "v_cmp_eq_u32 vcc, -1, v218\n\t" "s_cbranch_vccz 42b\n\t" "s_cmp_eq_u32 %[rt], 1\n\t" "s_cbranch_scc1 42b\n\t" \
    "v_mov_b32 v218, %[rt]\n\t" "ds_write_b32 v219, v218\n\t" "s_waitcnt lgkmcnt(0)\n\t" "s_branch 42b\n\t" "43:\n\t" "s_sub_u32 %[rt], %[rt], 1\n\t" \
    "s_waitcnt lgkmcnt(0)\n\t" XCNT(100) "44:\n\t" "buffer_load_dwordx4 v[240:243], %[mo], %[rs], %[sb] offen sc1\n\t" \
    "buffer_load_dwordx4 v[244:247], v216, %[rs], %[sb] offen sc1\n\t" "buffer_load_dwordx4 v[248:251], %[mo], %[rs], s60 offen sc1\n\t" \
    "buffer_load_dwordx4 v[252:255], v216, %[rs], s60 offen sc1\n\t" "s_waitcnt vmcnt(0)\n\t" "v_max_u32 v218, v240, v241\n\t" "v_max3_u32 v218, v218, v242, v243\n\t" \
    "v_max3_u32 v218, v218, v244, v245\n\t" "v_max3_u32 v218, v218, v246, v247\n\t" "v_max3_u32 v218, v218, v248, v249\n\t" "v_max3_u32 v218, v218, v250, v251\n\t" \
    "v_max3_u32 v218, v218, v252, v253\n\t" "v_max3_u32 v218, v218, v254, v255\n\t" "v_cmp_eq_u32 vcc, -1, v218\n\t" "s_cbranch_vccz 45f\n\t" "s_cmp_eq_u32 %[bd], 0\n\t" \
    "s_cbranch_scc1 45f\n\t" "s_sub_u32 %[bd], %[bd], 1\n\t" XCNT(101) "s_branch 44b\n\t" "45:\n\t" "ds_read_b128 v[160:163], %[l0] offset:0\n\t" \
    "ds_read_b128 v[164:167], %[l0] offset:1024\n\t" "ds_read_b128 v[168:171], %[l0] offset:2048\n\t" "ds_read_b128 v[172:175], %[l0] offset:3072\n\t" \
    "ds_read_b128 v[176:179], %[l0] offset:4096\n\t" "ds_read_b128 v[180:183], %[l0] offset:5120\n\t" "ds_read_b128 v[184:187], %[l0] offset:6144\n\t" \
    "ds_read_b128 v[188:191], %[l0] offset:7168\n\t" "v_mov_b64 v[224:225], 0\n\t" "v_mov_b64 v[226:227], 0\n\t" "v_mov_b64 v[228:229], 0\n\t" "v_mov_b64 v[230:231], 0\n\t" \
    "v_mov_b64 v[232:233], 0\n\t" "v_mov_b64 v[234:235], 0\n\t" "v_mov_b64 v[236:237], 0\n\t" "v_mov_b64 v[238:239], 0\n\t" "s_barrier\n\t" "s_memtime s[64:65]\n\t" \
    "s_waitcnt lgkmcnt(0)\n\t" "s_add_u32 s64, s64, %[dr]\n\t" "s_sub_u32 s62, s64, %[ca]\n\t" "s_mul_hi_u32 s63, s62, %[k1]\n\t" "s_mul_i32 s62, s62, %[k1]\n\t" \
    "s_lshr_b64 s[62:63], s[62:63], 16\n\t" "s_add_u32 s62, s62, %[ga]\n\t" "s_bitset0_b32 s62, 0\n\t" "v_mov_b32 v218, s62\n\t" \
    "buffer_store_dword v218, %[hq], %[rs], s67 offen sc1\n\t" "s_mov_b32 s67, 0x7f000000\n\t" "s_branch 40b\n\t" "99:\n\t" "mk_tail:\n\t"
#define VO_STEP \
    ".p2align 6\n\t" "v_xor_b32 v224, 0x400, %[mo]\n\t" "s_xor_b32 s60, %[sb], 0x800\n\t" "s_mov_b32 s67, %[pb]\n\t" "s_cmp_eq_u32 %[w], 1\n\t" "s_cbranch_scc1 91f\n\t" \
    "s_cmp_eq_u32 %[w], 2\n\t" "s_cbranch_scc1 92f\n\t" "s_cmp_eq_u32 %[w], 3\n\t" "s_cbranch_scc1 93f\n\t" XT(80, 81) "s_getpc_b64 s[62:63]\n\t" "18:\n\t" \
    "s_memtime s[64:65]\n\t" "s_waitcnt lgkmcnt(0)\n\t" "s_sub_u32 s61, %[tg], s64\n\t" "s_cmp_gt_i32 s61, 124\n\t" "s_cbranch_scc1 18b\n\t" "s_max_i32 s61, s61, 0\n\t" \
    "s_and_b32 s61, s61, 124\n\t" "s_sub_u32 s61, 19f-18b, s61\n\t" "s_add_u32 s62, s62, s61\n\t" "s_addc_u32 s63, s63, 0\n\t" "s_setpc_b64 s[62:63]\n\t" ".rept 31\n\t" \
    "s_nop 0\n\t" ".endr\n\t" "19:\n\t" "s_barrier\n\t" XT(82, 83) "buffer_load_dwordx4 v[240:243], %[mo], %[rs], %[sb] offen sc1\n\t" "v_mov_b64 v[232:233], 0\n\t" \
    "v_mov_b64 v[234:235], 0\n\t" "v_mov_b64 v[236:237], 0\n\t" "v_mov_b64 v[238:239], 0\n\t" "v_mov_b64 v[192:193], 0\n\t" "v_mov_b64 v[194:195], 0\n\t" \
    "v_mov_b64 v[196:197], 0\n\t" "v_mov_b64 v[198:199], 0\n\t" "buffer_load_dwordx4 v[244:247], v224, %[rs], %[sb] offen sc1\n\t" \
    "ds_read_b128 v[160:163], %[l0] offset:0\n\t" "ds_read_b128 v[164:167], %[l0] offset:1024\n\t" "ds_read_b128 v[168:171], %[l0] offset:2048\n\t" \
    "ds_read_b128 v[172:175], %[l0] offset:3072\n\t" "s_nop 3\n\t" "buffer_load_dwordx4 v[248:251], %[mo], %[rs], s60 offen sc1\n\t" \
    "ds_read_b128 v[176:179], %[l0] offset:4096\n\t" "ds_read_b128 v[180:183], %[l0] offset:5120\n\t" "ds_read_b128 v[184:187], %[l0] offset:6144\n\t" \
    "ds_read_b128 v[188:191], %[l0] offset:7168\n\t" "s_nop 3\n\t" "buffer_load_dwordx4 v[252:255], v224, %[rs], s60 offen sc1\n\t" "v_mov_b64 v[200:201], 0\n\t" \
    "v_mov_b64 v[202:203], 0\n\t" "v_mov_b64 v[204:205], 0\n\t" "v_mov_b64 v[206:207], 0\n\t" "v_mov_b64 v[208:209], 0\n\t" "v_mov_b64 v[210:211], 0\n\t" \
    "v_mov_b64 v[212:213], 0\n\t" "v_mov_b64 v[214:215], 0\n\t" "v_mov_b64 v[216:217], 0\n\t" "v_mov_b64 v[218:219], 0\n\t" "v_mov_b64 v[220:221], 0\n\t" \
    "v_mov_b64 v[222:223], 0\n\t" "v_mov_b32 v226, -1\n\t" "buffer_store_dword v226, %[hq], %[rs], %[pr] offen sc1\n\t" "v_mov_b32 v227, 160512\n\t" "10:\n\t" \
    "s_waitcnt vmcnt(4)\n\t" XT(84, 85) "v_smfmac_f32_16x16x64_bf16 v[232:235], v[240:243], a[0:7], %[ix]\n\t" "ds_write_b128 %[l1], v[240:243] offset:0\n\t" \
    "v_smfmac_f32_16x16x64_bf16 v[236:239], v[240:243], a[8:15], %[ix]\n\t" "s_waitcnt vmcnt(3)\n\t" "v_smfmac_f32_16x16x64_bf16 v[232:235], v[244:247], a[16:23], %[ix]\n\t" \
    "ds_write_b128 %[l1], v[244:247] offset:1024\n\t" "s_waitcnt lgkmcnt(8)\n\t" "v_smfmac_f32_16x16x64_bf16 v[236:239], v[244:247], v[160:167], %[ix]\n\t" \
    "s_waitcnt lgkmcnt(0)\n\t" "s_barrier\n\t" XT(86, 87) "ds_read_b128 v[16:19], %[l1] offset:4096\n\t" "ds_read_b128 v[20:23], %[l1] offset:8192\n\t" \
    "ds_read_b128 v[24:27], %[l1] offset:12288\n\t" "s_waitcnt vmcnt(2)\n\t" "v_smfmac_f32_16x16x64_bf16 v[232:235], v[248:251], a[24:31], %[ix]\n\t" \
    "ds_write_b128 %[l1], v[248:251] offset:2048\n\t" "v_smfmac_f32_16x16x64_bf16 v[236:239], v[248:251], a[32:39], %[ix]\n\t" "ds_read_b128 v[28:31], %[l1] offset:5120\n\t" \
    "ds_read_b128 v[240:243], %[l1] offset:9216\n\t" "ds_read_b128 v[244:247], %[l1] offset:13312\n\t" "s_waitcnt vmcnt(1)\n\t" \
    "v_smfmac_f32_16x16x64_bf16 v[232:235], v[252:255], a[40:47], %[ix]\n\t" "ds_write_b128 %[l1], v[252:255] offset:3072\n\t" \
    "v_smfmac_f32_16x16x64_bf16 v[236:239], v[252:255], v[168:175], %[ix]\n\t" "ds_read_b128 v[160:163], %[l0] offset:8192\n\t" \
    "ds_read_b128 v[164:167], %[l0] offset:9216\n\t" "ds_read_b128 v[168:171], %[l0] offset:10240\n\t" "ds_read_b128 v[172:175], %[l0] offset:11264\n\t" "s_nop 1\n\t" \
    "v_cmp_u_f32 vcc, v232, v233\n\t" "v_cmp_u_f32 s[62:63], v234, v235\n\t" "s_waitcnt lgkmcnt(11)\n\t" \
    "v_smfmac_f32_16x16x64_bf16 v[232:235], v[16:19], a[48:55], %[ix]\n\t" "v_smfmac_f32_16x16x64_bf16 v[236:239], v[16:19], a[56:63], %[ix]\n\t" \
    "s_or_b64 vcc, vcc, s[62:63]\n\t" "s_cbranch_scc1 11f\n\t" "12:\n\t" "s_waitcnt lgkmcnt(10)\n\t" "v_smfmac_f32_16x16x64_bf16 v[232:235], v[20:23], a[64:71], %[ix]\n\t" \
    "v_smfmac_f32_16x16x64_bf16 v[236:239], v[20:23], v[176:183], %[ix]\n\t" "s_waitcnt lgkmcnt(4)\n\t" "s_barrier\n\t" XT(88, 89) "ds_read_b32 v225, v227\n\t" \
    "v_smfmac_f32_16x16x64_bf16 v[232:235], v[24:27], a[72:79], %[ix]\n\t" "ds_read_b128 v[176:179], %[l0] offset:12288\n\t" "ds_read_b128 v[180:183], %[l0] offset:13312\n\t" \
    "v_smfmac_f32_16x16x64_bf16 v[236:239], v[24:27], a[80:87], %[ix]\n\t" "ds_read_b128 v[248:251], %[l1] offset:6144\n\t" "ds_read_b128 v[252:255], %[l1] offset:10240\n\t" \
    "v_smfmac_f32_16x16x64_bf16 v[232:235], v[28:31], a[88:95], %[ix]\n\t" "ds_read_b128 v[16:19], %[l1] offset:14336\n\t" "ds_read_b128 v[20:23], %[l1] offset:7168\n\t" \
    "v_smfmac_f32_16x16x64_bf16 v[236:239], v[28:31], v[184:191], %[ix]\n\t" "s_memtime s[64:65]\n\t" "ds_read_b128 v[24:27], %[l1] offset:11264\n\t" \
    "v_smfmac_f32_16x16x64_bf16 v[232:235], v[240:243], a[96:103], %[ix]\n\t" "ds_read_b128 v[184:187], %[l0] offset:14336\n\t" \
    "ds_read_b128 v[188:191], %[l0] offset:15360\n\t" "v_smfmac_f32_16x16x64_bf16 v[236:239], v[240:243], a[104:111], %[ix]\n\t" "s_waitcnt lgkmcnt(9)\n\t" \
    "v_cmp_eq_u32 vcc, %[rt], v225\n\t" "v_smfmac_f32_16x16x64_bf16 v[232:235], v[244:247], a[112:119], %[ix]\n\t" "s_cbranch_vccnz 13f\n\t" \
    "ds_read_b128 v[28:31], %[l1] offset:15360\n\t" "v_smfmac_f32_16x16x64_bf16 v[236:239], v[244:247], v[160:167], %[ix]\n\t" \
    "ds_read_b128 v[160:163], %[l0] offset:16384\n\t" "ds_read_b128 v[164:167], %[l0] offset:17408\n\t" "s_waitcnt lgkmcnt(9)\n\t" \
    "v_smfmac_f32_16x16x64_bf16 v[232:235], v[248:251], a[120:127], %[ix]\n\t" "s_sub_u32 s62, s64, %[ca]\n\t" "s_mul_hi_u32 s63, s62, %[k1]\n\t" \
    "v_smfmac_f32_16x16x64_bf16 v[236:239], v[248:251], a[128:135], %[ix]\n\t" "s_mul_i32 s62, s62, %[k1]\n\t" "s_lshr_b64 s[62:63], s[62:63], 16\n\t" \
    "s_waitcnt lgkmcnt(8)\n\t" "v_smfmac_f32_16x16x64_bf16 v[232:235], v[252:255], a[136:143], %[ix]\n\t" "s_add_u32 s62, s62, %[ga]\n\t" "s_bitset0_b32 s62, 0\n\t" \
    "v_smfmac_f32_16x16x64_bf16 v[236:239], v[252:255], v[168:175], %[ix]\n\t" "v_mov_b32 v226, s62\n\t" "buffer_store_dword v226, %[hq], %[rs], s67 offen sc1\n\t" \
    "s_waitcnt lgkmcnt(7)\n\t" "v_smfmac_f32_16x16x64_bf16 v[232:235], v[16:19], a[144:151], %[ix]\n\t" "ds_read_b128 v[168:171], %[l0] offset:18432\n\t" \
    "ds_read_b128 v[172:175], %[l0] offset:19456\n\t" "v_smfmac_f32_16x16x64_bf16 v[236:239], v[16:19], a[152:159], %[ix]\n\t" "s_waitcnt lgkmcnt(8)\n\t" \
    "v_smfmac_f32_16x16x64_bf16 v[232:235], v[20:23], a[160:167], %[ix]\n\t" "v_smfmac_f32_16x16x64_bf16 v[236:239], v[20:23], v[176:183], %[ix]\n\t" \
    "ds_read_b128 v[176:179], %[l0] offset:20480\n\t" "ds_read_b128 v[180:183], %[l0] offset:21504\n\t" "s_waitcnt lgkmcnt(9)\n\t" \
    "v_smfmac_f32_16x16x64_bf16 v[232:235], v[24:27], a[168:175], %[ix]\n\t" "v_smfmac_f32_16x16x64_bf16 v[236:239], v[24:27], a[176:183], %[ix]\n\t" \
    "s_waitcnt lgkmcnt(6)\n\t" "v_smfmac_f32_16x16x64_bf16 v[232:235], v[28:31], a[184:191], %[ix]\n\t" \
    "v_smfmac_f32_16x16x64_bf16 v[236:239], v[28:31], v[184:191], %[ix]\n\t" "ds_read_b128 v[184:187], %[l0] offset:22528\n\t" \
    "ds_read_b128 v[188:191], %[l0] offset:23552\n\t" XT(90, 91) "s_nop 4\n\t" "v_add_f32 v232, v232, v233\n\t" "v_add_f32 v234, v234, v235\n\t" \
    "v_add_f32 v236, v236, v237\n\t" "v_add_f32 v238, v238, v239\n\t" "v_cvt_pk_bf16_f32 v236, v232, v236\n\t" "v_cvt_pk_bf16_f32 v237, v234, v238\n\t" \
    "ds_write2_b32 %[vw], v236, v237 offset1:68\n\t" "s_waitcnt lgkmcnt(0)\n\t" "s_barrier\n\t" XT(92, 93) "ds_read_b128 v[240:243], %[vr] offset:0\n\t" \
    "ds_read_b128 v[244:247], %[vr] offset:64\n\t" "ds_read_b128 v[248:251], %[vr] offset:128\n\t" "ds_read_b128 v[252:255], %[vr] offset:192\n\t" "s_mov_b32 s61, %[ub]\n\t" \
    "s_waitcnt lgkmcnt(3)\n\t" "v_smfmac_f32_16x16x64_bf16 v[192:195], v[240:243], v[32:39], %[ix]\n\t" \
    "v_smfmac_f32_16x16x64_bf16 v[196:199], v[240:243], v[48:55], %[ix]\n\t" "s_waitcnt lgkmcnt(2)\n\t" \
    "v_smfmac_f32_16x16x64_bf16 v[192:195], v[244:247], v[40:47], %[ix]\n\t" "v_smfmac_f32_16x16x64_bf16 v[196:199], v[244:247], v[56:63], %[ix]\n\t" \
    "s_waitcnt lgkmcnt(1)\n\t" "v_smfmac_f32_16x16x64_bf16 v[192:195], v[248:251], a[192:199], %[ix]\n\t" \
    "v_smfmac_f32_16x16x64_bf16 v[196:199], v[248:251], a[200:207], %[ix]\n\t" "s_waitcnt lgkmcnt(0)\n\t" \
    "v_smfmac_f32_16x16x64_bf16 v[192:195], v[252:255], v[160:167], %[ix]\n\t" "ds_read_b128 v[160:163], %[l0] offset:24576\n\t" \
    "ds_read_b128 v[164:167], %[l0] offset:25600\n\t" "v_smfmac_f32_16x16x64_bf16 v[196:199], v[252:255], v[168:175], %[ix]\n\t" \
    "ds_read_b128 v[168:171], %[l0] offset:26624\n\t" "ds_read_b128 v[172:175], %[l0] offset:27648\n\t" \
    "v_smfmac_f32_16x16x64_bf16 v[200:203], v[240:243], v[64:71], %[ix]\n\t" "s_nop 1\n\t" "v_smfmac_f32_16x16x64_bf16 v[204:207], v[240:243], v[80:87], %[ix]\n\t" \
    "v_cvt_pk_bf16_f32 v224, v192, v194\n\t" "v_cvt_pk_bf16_f32 v225, v193, v195\n\t" "v_smfmac_f32_16x16x64_bf16 v[200:203], v[244:247], v[72:79], %[ix]\n\t" \
    "v_cvt_pk_bf16_f32 v226, v196, v198\n\t" "v_cvt_pk_bf16_f32 v227, v197, v199\n\t" "v_smfmac_f32_16x16x64_bf16 v[204:207], v[244:247], v[88:95], %[ix]\n\t" "s_nop 0\n\t" \
    "v_permlane32_swap_b32 v224, v226\n\t" "v_smfmac_f32_16x16x64_bf16 v[200:203], v[248:251], a[208:215], %[ix]\n\t" "v_permlane32_swap_b32 v225, v227\n\t" "s_nop 0\n\t" \
    "v_smfmac_f32_16x16x64_bf16 v[204:207], v[248:251], a[216:223], %[ix]\n\t" "v_permlane16_swap_b32 v224, v225\n\t" "v_permlane16_swap_b32 v226, v227\n\t" \
    "v_smfmac_f32_16x16x64_bf16 v[200:203], v[252:255], v[176:183], %[ix]\n\t" "ds_read_b128 v[176:179], %[l0] offset:28672\n\t" \
    "buffer_store_dwordx4 v[224:227], %[uo], %[rs], s61 offen sc1\n\t" "s_xor_b32 s61, s61, 0x40000\n\t" XT(94, 95) \
    "v_smfmac_f32_16x16x64_bf16 v[204:207], v[252:255], v[184:191], %[ix]\n\t" "ds_read_b128 v[180:183], %[l0] offset:29696\n\t" \
    "ds_read_b128 v[184:187], %[l0] offset:30720\n\t" "v_smfmac_f32_16x16x64_bf16 v[208:211], v[240:243], v[96:103], %[ix]\n\t" \
    "ds_read_b128 v[188:191], %[l0] offset:31744\n\t" "s_nop 0\n\t" "v_smfmac_f32_16x16x64_bf16 v[212:215], v[240:243], v[112:119], %[ix]\n\t" \
    "v_cvt_pk_bf16_f32 v228, v200, v202\n\t" "v_cvt_pk_bf16_f32 v229, v201, v203\n\t" "v_smfmac_f32_16x16x64_bf16 v[208:211], v[244:247], v[104:111], %[ix]\n\t" \
    "v_cvt_pk_bf16_f32 v230, v204, v206\n\t" "v_cvt_pk_bf16_f32 v231, v205, v207\n\t" "v_smfmac_f32_16x16x64_bf16 v[212:215], v[244:247], v[120:127], %[ix]\n\t" "s_nop 0\n\t" \
    "v_permlane32_swap_b32 v228, v230\n\t" "v_smfmac_f32_16x16x64_bf16 v[208:211], v[248:251], a[224:231], %[ix]\n\t" "v_permlane32_swap_b32 v229, v231\n\t" "s_nop 0\n\t" \
    "v_smfmac_f32_16x16x64_bf16 v[212:215], v[248:251], a[232:239], %[ix]\n\t" "v_permlane16_swap_b32 v228, v229\n\t" "v_permlane16_swap_b32 v230, v231\n\t" \
    "s_waitcnt lgkmcnt(6)\n\t" "v_smfmac_f32_16x16x64_bf16 v[208:211], v[252:255], v[160:167], %[ix]\n\t" "buffer_store_dwordx4 v[228:231], %[uo], %[rs], s61 offen sc1\n\t" \
    "s_xor_b32 s61, s61, 0xc0000\n\t" "s_waitcnt lgkmcnt(4)\n\t" "v_smfmac_f32_16x16x64_bf16 v[212:215], v[252:255], v[168:175], %[ix]\n\t" "s_nop 1\n\t" \
    "v_smfmac_f32_16x16x64_bf16 v[216:219], v[240:243], v[128:135], %[ix]\n\t" "s_nop 1\n\t" "v_smfmac_f32_16x16x64_bf16 v[220:223], v[240:243], v[144:151], %[ix]\n\t" \
    "v_cvt_pk_bf16_f32 v224, v208, v210\n\t" "v_cvt_pk_bf16_f32 v225, v209, v211\n\t" "v_smfmac_f32_16x16x64_bf16 v[216:219], v[244:247], v[136:143], %[ix]\n\t" \
    "v_cvt_pk_bf16_f32 v226, v212, v214\n\t" "v_cvt_pk_bf16_f32 v227, v213, v215\n\t" "v_smfmac_f32_16x16x64_bf16 v[220:223], v[244:247], v[152:159], %[ix]\n\t" "s_nop 0\n\t" \
    "v_permlane32_swap_b32 v224, v226\n\t" "v_smfmac_f32_16x16x64_bf16 v[216:219], v[248:251], a[240:247], %[ix]\n\t" "v_permlane32_swap_b32 v225, v227\n\t" "s_nop 0\n\t" \
    "v_smfmac_f32_16x16x64_bf16 v[220:223], v[248:251], a[248:255], %[ix]\n\t" "v_permlane16_swap_b32 v224, v225\n\t" "v_permlane16_swap_b32 v226, v227\n\t" \
    "s_waitcnt lgkmcnt(2)\n\t" "v_smfmac_f32_16x16x64_bf16 v[216:219], v[252:255], v[176:183], %[ix]\n\t" "buffer_store_dwordx4 v[224:227], %[uo], %[rs], s61 offen sc1\n\t" \
    "s_xor_b32 s61, s61, 0x40000\n\t" "s_waitcnt lgkmcnt(0)\n\t" "v_smfmac_f32_16x16x64_bf16 v[220:223], v[252:255], v[184:191], %[ix]\n\t" "s_nop 5\n\t" \
    "v_cvt_pk_bf16_f32 v228, v216, v218\n\t" "v_cvt_pk_bf16_f32 v229, v217, v219\n\t" "v_cvt_pk_bf16_f32 v230, v220, v222\n\t" "v_cvt_pk_bf16_f32 v231, v221, v223\n\t" \
    "s_nop 0\n\t" "v_permlane32_swap_b32 v228, v230\n\t" "v_permlane32_swap_b32 v229, v231\n\t" "s_nop 1\n\t" "v_permlane16_swap_b32 v228, v229\n\t" \
    "v_permlane16_swap_b32 v230, v231\n\t" "buffer_store_dwordx4 v[228:231], %[uo], %[rs], s61 offen sc1\n\t" "s_nop 1\n\t" XT(96, 97) "s_branch 99f\n\t" "11:\n\t" \
    "ds_read_b128 v[228:231], %[l1] offset:0\n\t" "s_waitcnt lgkmcnt(0)\n\t" "v_max3_u32 v225, v228, v229, v230\n\t" "v_max_u32 v225, v225, v231\n\t" \
    "ds_read_b128 v[228:231], %[l1] offset:1024\n\t" "s_waitcnt lgkmcnt(0)\n\t" "v_max3_u32 v225, v225, v228, v229\n\t" "v_max3_u32 v225, v225, v230, v231\n\t" \
    "ds_read_b128 v[228:231], %[l1] offset:2048\n\t" "s_waitcnt lgkmcnt(0)\n\t" "v_max3_u32 v225, v225, v228, v229\n\t" "v_max3_u32 v225, v225, v230, v231\n\t" \
    "ds_read_b128 v[228:231], %[l1] offset:3072\n\t" "s_waitcnt lgkmcnt(0)\n\t" "v_max3_u32 v225, v225, v228, v229\n\t" "v_max3_u32 v225, v225, v230, v231\n\t" \
    "v_cmp_eq_u32 vcc, -1, v225\n\t" "s_cbranch_vccz 12b\n\t" "s_cmp_eq_u32 %[rt], 1\n\t" "s_cbranch_scc1 12b\n\t" "v_mov_b32 v225, %[rt]\n\t" "ds_write_b32 v227, v225\n\t" \
    "s_waitcnt lgkmcnt(0)\n\t" "s_branch 12b\n\t" "13:\n\t" "s_sub_u32 %[rt], %[rt], 1\n\t" "s_waitcnt lgkmcnt(0)\n\t" XCNT(100) "14:\n\t" \
    "buffer_load_dwordx4 v[240:243], %[mo], %[rs], %[sb] offen sc1\n\t" "buffer_load_dwordx4 v[244:247], v224, %[rs], %[sb] offen sc1\n\t" \
    "buffer_load_dwordx4 v[248:251], %[mo], %[rs], s60 offen sc1\n\t" "buffer_load_dwordx4 v[252:255], v224, %[rs], s60 offen sc1\n\t" "s_waitcnt vmcnt(0)\n\t" \
    "v_max_u32 v225, v240, v241\n\t" "v_max3_u32 v225, v225, v242, v243\n\t" "v_max3_u32 v225, v225, v244, v245\n\t" "v_max3_u32 v225, v225, v246, v247\n\t" \
    "v_max3_u32 v225, v225, v248, v249\n\t" "v_max3_u32 v225, v225, v250, v251\n\t" "v_max3_u32 v225, v225, v252, v253\n\t" "v_max3_u32 v225, v225, v254, v255\n\t" \
    "v_cmp_eq_u32 vcc, -1, v225\n\t" "s_cbranch_vccz 15f\n\t" "s_cmp_eq_u32 %[bd], 0\n\t" "s_cbranch_scc1 15f\n\t" "s_sub_u32 %[bd], %[bd], 1\n\t" XCNT(101) \
    "s_branch 14b\n\t" "15:\n\t" "ds_read_b128 v[160:163], %[l0] offset:0\n\t" "ds_read_b128 v[164:167], %[l0] offset:1024\n\t" \
    "ds_read_b128 v[168:171], %[l0] offset:2048\n\t" "ds_read_b128 v[172:175], %[l0] offset:3072\n\t" "ds_read_b128 v[176:179], %[l0] offset:4096\n\t" \
    "ds_read_b128 v[180:183], %[l0] offset:5120\n\t" "ds_read_b128 v[184:187], %[l0] offset:6144\n\t" "ds_read_b128 v[188:191], %[l0] offset:7168\n\t" \
    "v_mov_b64 v[232:233], 0\n\t" "v_mov_b64 v[234:235], 0\n\t" "v_mov_b64 v[236:237], 0\n\t" "v_mov_b64 v[238:239], 0\n\t" "s_barrier\n\t" "s_memtime s[64:65]\n\t" \
    "s_waitcnt lgkmcnt(0)\n\t" "s_add_u32 s64, s64, %[dr]\n\t" "s_sub_u32 s62, s64, %[ca]\n\t" "s_mul_hi_u32 s63, s62, %[k1]\n\t" "s_mul_i32 s62, s62, %[k1]\n\t" \
    "s_lshr_b64 s[62:63], s[62:63], 16\n\t" "s_add_u32 s62, s62, %[ga]\n\t" "s_bitset0_b32 s62, 0\n\t" "v_mov_b32 v225, s62\n\t" \
    "buffer_store_dword v225, %[hq], %[rs], s67 offen sc1\n\t" "s_mov_b32 s67, 0x7f000000\n\t" "s_branch 10b\n\t" "91:\n\t" XT(80, 81) "s_barrier\n\t" XT(82, 83) \
    "buffer_load_dwordx4 v[240:243], %[mo], %[rs], %[sb] offen sc1\n\t" "v_mov_b64 v[232:233], 0\n\t" "v_mov_b64 v[234:235], 0\n\t" "v_mov_b64 v[236:237], 0\n\t" \
    "v_mov_b64 v[238:239], 0\n\t" "v_mov_b64 v[192:193], 0\n\t" "v_mov_b64 v[194:195], 0\n\t" "v_mov_b64 v[196:197], 0\n\t" "v_mov_b64 v[198:199], 0\n\t" \
    "buffer_load_dwordx4 v[244:247], v224, %[rs], %[sb] offen sc1\n\t" "ds_read_b128 v[160:163], %[l0] offset:0\n\t" "ds_read_b128 v[164:167], %[l0] offset:1024\n\t" \
    "ds_read_b128 v[168:171], %[l0] offset:2048\n\t" "ds_read_b128 v[172:175], %[l0] offset:3072\n\t" "s_nop 3\n\t" \
    "buffer_load_dwordx4 v[248:251], %[mo], %[rs], s60 offen sc1\n\t" "ds_read_b128 v[176:179], %[l0] offset:4096\n\t" "ds_read_b128 v[180:183], %[l0] offset:5120\n\t" \
    "ds_read_b128 v[184:187], %[l0] offset:6144\n\t" "ds_read_b128 v[188:191], %[l0] offset:7168\n\t" "s_nop 3\n\t" \
    "buffer_load_dwordx4 v[252:255], v224, %[rs], s60 offen sc1\n\t" "v_mov_b64 v[200:201], 0\n\t" "v_mov_b64 v[202:203], 0\n\t" "v_mov_b64 v[204:205], 0\n\t" \
    "v_mov_b64 v[206:207], 0\n\t" "v_mov_b64 v[208:209], 0\n\t" "v_mov_b64 v[210:211], 0\n\t" "v_mov_b64 v[212:213], 0\n\t" "v_mov_b64 v[214:215], 0\n\t" \
    "v_mov_b64 v[216:217], 0\n\t" "v_mov_b64 v[218:219], 0\n\t" "v_mov_b64 v[220:221], 0\n\t" "v_mov_b64 v[222:223], 0\n\t" "v_mov_b32 v227, 160512\n\t" "20:\n\t" \
    "s_waitcnt vmcnt(3)\n\t" XT(84, 85) "v_smfmac_f32_16x16x64_bf16 v[232:235], v[240:243], a[0:7], %[ix]\n\t" "ds_write_b128 %[l1], v[240:243] offset:4096\n\t" \
    "v_smfmac_f32_16x16x64_bf16 v[236:239], v[240:243], a[8:15], %[ix]\n\t" "s_waitcnt vmcnt(2)\n\t" "v_smfmac_f32_16x16x64_bf16 v[232:235], v[244:247], a[16:23], %[ix]\n\t" \
    "ds_write_b128 %[l1], v[244:247] offset:5120\n\t" "s_waitcnt lgkmcnt(8)\n\t" "v_smfmac_f32_16x16x64_bf16 v[236:239], v[244:247], v[160:167], %[ix]\n\t" \
    "s_waitcnt lgkmcnt(0)\n\t" "s_barrier\n\t" XT(86, 87) "ds_read_b128 v[16:19], %[l1] offset:8192\n\t" "ds_read_b128 v[20:23], %[l1] offset:12288\n\t" \
    "ds_read_b128 v[24:27], %[l1] offset:0\n\t" "s_waitcnt vmcnt(1)\n\t" "v_smfmac_f32_16x16x64_bf16 v[232:235], v[248:251], a[24:31], %[ix]\n\t" \
    "ds_write_b128 %[l1], v[248:251] offset:6144\n\t" "v_smfmac_f32_16x16x64_bf16 v[236:239], v[248:251], a[32:39], %[ix]\n\t" "ds_read_b128 v[28:31], %[l1] offset:9216\n\t" \
    "ds_read_b128 v[240:243], %[l1] offset:13312\n\t" "ds_read_b128 v[244:247], %[l1] offset:1024\n\t" "s_waitcnt vmcnt(0)\n\t" \
    "v_smfmac_f32_16x16x64_bf16 v[232:235], v[252:255], a[40:47], %[ix]\n\t" "ds_write_b128 %[l1], v[252:255] offset:7168\n\t" \
    "v_smfmac_f32_16x16x64_bf16 v[236:239], v[252:255], v[168:175], %[ix]\n\t" "ds_read_b128 v[160:163], %[l0] offset:8192\n\t" \
    "ds_read_b128 v[164:167], %[l0] offset:9216\n\t" "ds_read_b128 v[168:171], %[l0] offset:10240\n\t" "ds_read_b128 v[172:175], %[l0] offset:11264\n\t" "s_nop 1\n\t" \
    "v_cmp_u_f32 vcc, v232, v233\n\t" "v_cmp_u_f32 s[62:63], v234, v235\n\t" "s_waitcnt lgkmcnt(11)\n\t" \
    "v_smfmac_f32_16x16x64_bf16 v[232:235], v[16:19], a[48:55], %[ix]\n\t" "v_smfmac_f32_16x16x64_bf16 v[236:239], v[16:19], a[56:63], %[ix]\n\t" \
    "s_or_b64 vcc, vcc, s[62:63]\n\t" "s_cbranch_scc1 21f\n\t" "22:\n\t" "s_waitcnt lgkmcnt(10)\n\t" "v_smfmac_f32_16x16x64_bf16 v[232:235], v[20:23], a[64:71], %[ix]\n\t" \
    "v_smfmac_f32_16x16x64_bf16 v[236:239], v[20:23], v[176:183], %[ix]\n\t" "s_waitcnt lgkmcnt(4)\n\t" "s_barrier\n\t" XT(88, 89) "ds_read_b32 v225, v227\n\t" \
    "v_smfmac_f32_16x16x64_bf16 v[232:235], v[24:27], a[72:79], %[ix]\n\t" "ds_read_b128 v[176:179], %[l0] offset:12288\n\t" "ds_read_b128 v[180:183], %[l0] offset:13312\n\t" \
    "v_smfmac_f32_16x16x64_bf16 v[236:239], v[24:27], a[80:87], %[ix]\n\t" "ds_read_b128 v[248:251], %[l1] offset:10240\n\t" "ds_read_b128 v[252:255], %[l1] offset:14336\n\t" \
    "v_smfmac_f32_16x16x64_bf16 v[232:235], v[28:31], a[88:95], %[ix]\n\t" "ds_read_b128 v[16:19], %[l1] offset:2048\n\t" "ds_read_b128 v[20:23], %[l1] offset:11264\n\t" \
    "v_smfmac_f32_16x16x64_bf16 v[236:239], v[28:31], v[184:191], %[ix]\n\t" "ds_read_b128 v[24:27], %[l1] offset:15360\n\t" "ds_read_b128 v[184:187], %[l0] offset:14336\n\t" \
    "v_smfmac_f32_16x16x64_bf16 v[232:235], v[240:243], a[96:103], %[ix]\n\t" "ds_read_b128 v[188:191], %[l0] offset:15360\n\t" "ds_read_b128 v[28:31], %[l1] offset:3072\n\t" \
    "v_smfmac_f32_16x16x64_bf16 v[236:239], v[240:243], a[104:111], %[ix]\n\t" "s_waitcnt lgkmcnt(10)\n\t" "v_cmp_eq_u32 vcc, %[rt], v225\n\t" \
    "v_smfmac_f32_16x16x64_bf16 v[232:235], v[244:247], a[112:119], %[ix]\n\t" "s_cbranch_vccnz 23f\n\t" \
    "v_smfmac_f32_16x16x64_bf16 v[236:239], v[244:247], v[160:167], %[ix]\n\t" "ds_read_b128 v[160:163], %[l0] offset:16384\n\t" \
    "ds_read_b128 v[164:167], %[l0] offset:17408\n\t" "s_waitcnt lgkmcnt(9)\n\t" "v_smfmac_f32_16x16x64_bf16 v[232:235], v[248:251], a[120:127], %[ix]\n\t" \
    "v_smfmac_f32_16x16x64_bf16 v[236:239], v[248:251], a[128:135], %[ix]\n\t" "s_waitcnt lgkmcnt(8)\n\t" \
    "v_smfmac_f32_16x16x64_bf16 v[232:235], v[252:255], a[136:143], %[ix]\n\t" "v_smfmac_f32_16x16x64_bf16 v[236:239], v[252:255], v[168:175], %[ix]\n\t" \
    "ds_read_b128 v[168:171], %[l0] offset:18432\n\t" "ds_read_b128 v[172:175], %[l0] offset:19456\n\t" "s_waitcnt lgkmcnt(9)\n\t" \
    "v_smfmac_f32_16x16x64_bf16 v[232:235], v[16:19], a[144:151], %[ix]\n\t" "v_smfmac_f32_16x16x64_bf16 v[236:239], v[16:19], a[152:159], %[ix]\n\t" \
    "s_waitcnt lgkmcnt(8)\n\t" "v_smfmac_f32_16x16x64_bf16 v[232:235], v[20:23], a[160:167], %[ix]\n\t" \
    "v_smfmac_f32_16x16x64_bf16 v[236:239], v[20:23], v[176:183], %[ix]\n\t" "ds_read_b128 v[176:179], %[l0] offset:20480\n\t" \
    "ds_read_b128 v[180:183], %[l0] offset:21504\n\t" "s_waitcnt lgkmcnt(9)\n\t" "v_smfmac_f32_16x16x64_bf16 v[232:235], v[24:27], a[168:175], %[ix]\n\t" \
    "v_smfmac_f32_16x16x64_bf16 v[236:239], v[24:27], a[176:183], %[ix]\n\t" "s_waitcnt lgkmcnt(6)\n\t" \
    "v_smfmac_f32_16x16x64_bf16 v[232:235], v[28:31], a[184:191], %[ix]\n\t" "v_smfmac_f32_16x16x64_bf16 v[236:239], v[28:31], v[184:191], %[ix]\n\t" \
    "ds_read_b128 v[184:187], %[l0] offset:22528\n\t" "ds_read_b128 v[188:191], %[l0] offset:23552\n\t" XT(90, 91) "s_nop 4\n\t" "v_add_f32 v232, v232, v233\n\t" \
    "v_add_f32 v234, v234, v235\n\t" "v_add_f32 v236, v236, v237\n\t" "v_add_f32 v238, v238, v239\n\t" "v_cvt_pk_bf16_f32 v236, v232, v236\n\t" \
    "v_cvt_pk_bf16_f32 v237, v234, v238\n\t" "ds_write2_b32 %[vw], v236, v237 offset1:68\n\t" "s_waitcnt lgkmcnt(0)\n\t" "s_barrier\n\t" XT(92, 93) \
    "ds_read_b128 v[240:243], %[vr] offset:0\n\t" "ds_read_b128 v[244:247], %[vr] offset:64\n\t" "ds_read_b128 v[248:251], %[vr] offset:128\n\t" \
    "ds_read_b128 v[252:255], %[vr] offset:192\n\t" "s_mov_b32 s61, %[ub]\n\t" "s_waitcnt lgkmcnt(3)\n\t" \
    "v_smfmac_f32_16x16x64_bf16 v[192:195], v[240:243], v[32:39], %[ix]\n\t" "v_smfmac_f32_16x16x64_bf16 v[196:199], v[240:243], v[48:55], %[ix]\n\t" \
    "s_waitcnt lgkmcnt(2)\n\t" "v_smfmac_f32_16x16x64_bf16 v[192:195], v[244:247], v[40:47], %[ix]\n\t" \
    "v_smfmac_f32_16x16x64_bf16 v[196:199], v[244:247], v[56:63], %[ix]\n\t" "s_waitcnt lgkmcnt(1)\n\t" \
    "v_smfmac_f32_16x16x64_bf16 v[192:195], v[248:251], a[192:199], %[ix]\n\t" "v_smfmac_f32_16x16x64_bf16 v[196:199], v[248:251], a[200:207], %[ix]\n\t" \
    "s_waitcnt lgkmcnt(0)\n\t" "v_smfmac_f32_16x16x64_bf16 v[192:195], v[252:255], v[160:167], %[ix]\n\t" "ds_read_b128 v[160:163], %[l0] offset:24576\n\t" \
    "ds_read_b128 v[164:167], %[l0] offset:25600\n\t" "v_smfmac_f32_16x16x64_bf16 v[196:199], v[252:255], v[168:175], %[ix]\n\t" \
    "ds_read_b128 v[168:171], %[l0] offset:26624\n\t" "ds_read_b128 v[172:175], %[l0] offset:27648\n\t" \
    "v_smfmac_f32_16x16x64_bf16 v[200:203], v[240:243], v[64:71], %[ix]\n\t" "s_nop 1\n\t" "v_smfmac_f32_16x16x64_bf16 v[204:207], v[240:243], v[80:87], %[ix]\n\t" \
    "v_cvt_pk_bf16_f32 v224, v192, v194\n\t" "v_cvt_pk_bf16_f32 v225, v193, v195\n\t" "v_smfmac_f32_16x16x64_bf16 v[200:203], v[244:247], v[72:79], %[ix]\n\t" \
    "v_cvt_pk_bf16_f32 v226, v196, v198\n\t" "v_cvt_pk_bf16_f32 v227, v197, v199\n\t" "v_smfmac_f32_16x16x64_bf16 v[204:207], v[244:247], v[88:95], %[ix]\n\t" "s_nop 0\n\t" \
    "v_permlane32_swap_b32 v224, v226\n\t" "v_smfmac_f32_16x16x64_bf16 v[200:203], v[248:251], a[208:215], %[ix]\n\t" "v_permlane32_swap_b32 v225, v227\n\t" "s_nop 0\n\t" \
    "v_smfmac_f32_16x16x64_bf16 v[204:207], v[248:251], a[216:223], %[ix]\n\t" "v_permlane16_swap_b32 v224, v225\n\t" "v_permlane16_swap_b32 v226, v227\n\t" \
    "v_smfmac_f32_16x16x64_bf16 v[200:203], v[252:255], v[176:183], %[ix]\n\t" "ds_read_b128 v[176:179], %[l0] offset:28672\n\t" \
    "buffer_store_dwordx4 v[224:227], %[uo], %[rs], s61 offen sc1\n\t" "s_xor_b32 s61, s61, 0x40000\n\t" XT(94, 95) \
    "v_smfmac_f32_16x16x64_bf16 v[204:207], v[252:255], v[184:191], %[ix]\n\t" "ds_read_b128 v[180:183], %[l0] offset:29696\n\t" \
    "ds_read_b128 v[184:187], %[l0] offset:30720\n\t" "v_smfmac_f32_16x16x64_bf16 v[208:211], v[240:243], v[96:103], %[ix]\n\t" \
    "ds_read_b128 v[188:191], %[l0] offset:31744\n\t" "s_nop 0\n\t" "v_smfmac_f32_16x16x64_bf16 v[212:215], v[240:243], v[112:119], %[ix]\n\t" \
    "v_cvt_pk_bf16_f32 v228, v200, v202\n\t" "v_cvt_pk_bf16_f32 v229, v201, v203\n\t" "v_smfmac_f32_16x16x64_bf16 v[208:211], v[244:247], v[104:111], %[ix]\n\t" \
    "v_cvt_pk_bf16_f32 v230, v204, v206\n\t" "v_cvt_pk_bf16_f32 v231, v205, v207\n\t" "v_smfmac_f32_16x16x64_bf16 v[212:215], v[244:247], v[120:127], %[ix]\n\t" "s_nop 0\n\t" \
    "v_permlane32_swap_b32 v228, v230\n\t" "v_smfmac_f32_16x16x64_bf16 v[208:211], v[248:251], a[224:231], %[ix]\n\t" "v_permlane32_swap_b32 v229, v231\n\t" "s_nop 0\n\t" \
    "v_smfmac_f32_16x16x64_bf16 v[212:215], v[248:251], a[232:239], %[ix]\n\t" "v_permlane16_swap_b32 v228, v229\n\t" "v_permlane16_swap_b32 v230, v231\n\t" \
    "s_waitcnt lgkmcnt(6)\n\t" "v_smfmac_f32_16x16x64_bf16 v[208:211], v[252:255], v[160:167], %[ix]\n\t" "buffer_store_dwordx4 v[228:231], %[uo], %[rs], s61 offen sc1\n\t" \
    "s_xor_b32 s61, s61, 0xc0000\n\t" "s_waitcnt lgkmcnt(4)\n\t" "v_smfmac_f32_16x16x64_bf16 v[212:215], v[252:255], v[168:175], %[ix]\n\t" "s_nop 1\n\t" \
    "v_smfmac_f32_16x16x64_bf16 v[216:219], v[240:243], v[128:135], %[ix]\n\t" "s_nop 1\n\t" "v_smfmac_f32_16x16x64_bf16 v[220:223], v[240:243], v[144:151], %[ix]\n\t" \
    "v_cvt_pk_bf16_f32 v224, v208, v210\n\t" "v_cvt_pk_bf16_f32 v225, v209, v211\n\t" "v_smfmac_f32_16x16x64_bf16 v[216:219], v[244:247], v[136:143], %[ix]\n\t" \
    "v_cvt_pk_bf16_f32 v226, v212, v214\n\t" "v_cvt_pk_bf16_f32 v227, v213, v215\n\t" "v_smfmac_f32_16x16x64_bf16 v[220:223], v[244:247], v[152:159], %[ix]\n\t" "s_nop 0\n\t" \
    "v_permlane32_swap_b32 v224, v226\n\t" "v_smfmac_f32_16x16x64_bf16 v[216:219], v[248:251], a[240:247], %[ix]\n\t" "v_permlane32_swap_b32 v225, v227\n\t" "s_nop 0\n\t" \
    "v_smfmac_f32_16x16x64_bf16 v[220:223], v[248:251], a[248:255], %[ix]\n\t" "v_permlane16_swap_b32 v224, v225\n\t" "v_permlane16_swap_b32 v226, v227\n\t" \
    "s_waitcnt lgkmcnt(2)\n\t" "v_smfmac_f32_16x16x64_bf16 v[216:219], v[252:255], v[176:183], %[ix]\n\t" "buffer_store_dwordx4 v[224:227], %[uo], %[rs], s61 offen sc1\n\t" \
    "s_xor_b32 s61, s61, 0x40000\n\t" "s_waitcnt lgkmcnt(0)\n\t" "v_smfmac_f32_16x16x64_bf16 v[220:223], v[252:255], v[184:191], %[ix]\n\t" "s_nop 5\n\t" \
    "v_cvt_pk_bf16_f32 v228, v216, v218\n\t" "v_cvt_pk_bf16_f32 v229, v217, v219\n\t" "v_cvt_pk_bf16_f32 v230, v220, v222\n\t" "v_cvt_pk_bf16_f32 v231, v221, v223\n\t" \
    "s_nop 0\n\t" "v_permlane32_swap_b32 v228, v230\n\t" "v_permlane32_swap_b32 v229, v231\n\t" "s_nop 1\n\t" "v_permlane16_swap_b32 v228, v229\n\t" \
    "v_permlane16_swap_b32 v230, v231\n\t" "buffer_store_dwordx4 v[228:231], %[uo], %[rs], s61 offen sc1\n\t" "s_nop 1\n\t" XT(96, 97) "v_mov_b64 v[16:17], -1\n\t" \
    "v_mov_b64 v[18:19], -1\n\t" "s_barrier\n\t" "buffer_store_dwordx4 v[16:19], %[l1], %[rs], %[ur] offen offset:0 sc1\n\t" \
    "buffer_store_dwordx4 v[16:19], %[l1], %[rs], %[ur] offen offset:1024 sc1\n\t" "buffer_store_dwordx4 v[16:19], %[l1], %[rs], %[ur] offen offset:2048 sc1\n\t" \
    "buffer_store_dwordx4 v[16:19], %[l1], %[rs], %[ur] offen offset:3072 sc1\n\t" "s_sub_u32 s61, %[ur], 0x1000\n\t" \
    "buffer_store_dwordx4 v[16:19], %[l1], %[rs], s61 offen offset:0 sc1\n\t" "buffer_store_dwordx4 v[16:19], %[l1], %[rs], s61 offen offset:1024 sc1\n\t" "s_branch 99f\n\t" \
    "21:\n\t" "ds_read_b128 v[228:231], %[l1] offset:4096\n\t" "s_waitcnt lgkmcnt(0)\n\t" "v_max3_u32 v225, v228, v229, v230\n\t" "v_max_u32 v225, v225, v231\n\t" \
    "ds_read_b128 v[228:231], %[l1] offset:5120\n\t" "s_waitcnt lgkmcnt(0)\n\t" "v_max3_u32 v225, v225, v228, v229\n\t" "v_max3_u32 v225, v225, v230, v231\n\t" \
    "ds_read_b128 v[228:231], %[l1] offset:6144\n\t" "s_waitcnt lgkmcnt(0)\n\t" "v_max3_u32 v225, v225, v228, v229\n\t" "v_max3_u32 v225, v225, v230, v231\n\t" \
    "ds_read_b128 v[228:231], %[l1] offset:7168\n\t" "s_waitcnt lgkmcnt(0)\n\t" "v_max3_u32 v225, v225, v228, v229\n\t" "v_max3_u32 v225, v225, v230, v231\n\t" \
    "v_cmp_eq_u32 vcc, -1, v225\n\t" "s_cbranch_vccz 22b\n\t" "s_cmp_eq_u32 %[rt], 1\n\t" "s_cbranch_scc1 22b\n\t" "v_mov_b32 v225, %[rt]\n\t" "ds_write_b32 v227, v225\n\t" \
    "s_waitcnt lgkmcnt(0)\n\t" "s_branch 22b\n\t" "23:\n\t" "s_sub_u32 %[rt], %[rt], 1\n\t" "s_waitcnt lgkmcnt(0)\n\t" XCNT(100) "24:\n\t" \
    "buffer_load_dwordx4 v[240:243], %[mo], %[rs], %[sb] offen sc1\n\t" "buffer_load_dwordx4 v[244:247], v224, %[rs], %[sb] offen sc1\n\t" \
    "buffer_load_dwordx4 v[248:251], %[mo], %[rs], s60 offen sc1\n\t" "buffer_load_dwordx4 v[252:255], v224, %[rs], s60 offen sc1\n\t" "s_waitcnt vmcnt(0)\n\t" \
    "v_max_u32 v225, v240, v241\n\t" "v_max3_u32 v225, v225, v242, v243\n\t" "v_max3_u32 v225, v225, v244, v245\n\t" "v_max3_u32 v225, v225, v246, v247\n\t" \
    "v_max3_u32 v225, v225, v248, v249\n\t" "v_max3_u32 v225, v225, v250, v251\n\t" "v_max3_u32 v225, v225, v252, v253\n\t" "v_max3_u32 v225, v225, v254, v255\n\t" \
    "v_cmp_eq_u32 vcc, -1, v225\n\t" "s_cbranch_vccz 25f\n\t" "s_cmp_eq_u32 %[bd], 0\n\t" "s_cbranch_scc1 25f\n\t" "s_sub_u32 %[bd], %[bd], 1\n\t" XCNT(101) \
    "s_branch 24b\n\t" "25:\n\t" "ds_read_b128 v[160:163], %[l0] offset:0\n\t" "ds_read_b128 v[164:167], %[l0] offset:1024\n\t" \
    "ds_read_b128 v[168:171], %[l0] offset:2048\n\t" "ds_read_b128 v[172:175], %[l0] offset:3072\n\t" "ds_read_b128 v[176:179], %[l0] offset:4096\n\t" \
    "ds_read_b128 v[180:183], %[l0] offset:5120\n\t" "ds_read_b128 v[184:187], %[l0] offset:6144\n\t" "ds_read_b128 v[188:191], %[l0] offset:7168\n\t" \
    "v_mov_b64 v[232:233], 0\n\t" "v_mov_b64 v[234:235], 0\n\t" "v_mov_b64 v[236:237], 0\n\t" "v_mov_b64 v[238:239], 0\n\t" "s_barrier\n\t" "s_branch 20b\n\t" "92:\n\t" \
    XT(80, 81) "s_barrier\n\t" XT(82, 83) "buffer_load_dwordx4 v[240:243], %[mo], %[rs], %[sb] offen sc1\n\t" "v_mov_b64 v[232:233], 0\n\t" "v_mov_b64 v[234:235], 0\n\t" \
    "v_mov_b64 v[236:237], 0\n\t" "v_mov_b64 v[238:239], 0\n\t" "v_mov_b64 v[192:193], 0\n\t" "v_mov_b64 v[194:195], 0\n\t" "v_mov_b64 v[196:197], 0\n\t" \
    "v_mov_b64 v[198:199], 0\n\t" "buffer_load_dwordx4 v[244:247], v224, %[rs], %[sb] offen sc1\n\t" "ds_read_b128 v[160:163], %[l0] offset:0\n\t" \
    "ds_read_b128 v[164:167], %[l0] offset:1024\n\t" "ds_read_b128 v[168:171], %[l0] offset:2048\n\t" "ds_read_b128 v[172:175], %[l0] offset:3072\n\t" "s_nop 3\n\t" \
    "buffer_load_dwordx4 v[248:251], %[mo], %[rs], s60 offen sc1\n\t" "ds_read_b128 v[176:179], %[l0] offset:4096\n\t" "ds_read_b128 v[180:183], %[l0] offset:5120\n\t" \
    "ds_read_b128 v[184:187], %[l0] offset:6144\n\t" "ds_read_b128 v[188:191], %[l0] offset:7168\n\t" "s_nop 3\n\t" \
    "buffer_load_dwordx4 v[252:255], v224, %[rs], s60 offen sc1\n\t" "v_mov_b64 v[200:201], 0\n\t" "v_mov_b64 v[202:203], 0\n\t" "v_mov_b64 v[204:205], 0\n\t" \
    "v_mov_b64 v[206:207], 0\n\t" "v_mov_b64 v[208:209], 0\n\t" "v_mov_b64 v[210:211], 0\n\t" "v_mov_b64 v[212:213], 0\n\t" "v_mov_b64 v[214:215], 0\n\t" \
    "v_mov_b64 v[216:217], 0\n\t" "v_mov_b64 v[218:219], 0\n\t" "v_mov_b64 v[220:221], 0\n\t" "v_mov_b64 v[222:223], 0\n\t" "v_mov_b32 v227, 160512\n\t" "30:\n\t" \
    "s_waitcnt vmcnt(3)\n\t" XT(84, 85) "v_smfmac_f32_16x16x64_bf16 v[232:235], v[240:243], a[0:7], %[ix]\n\t" "ds_write_b128 %[l1], v[240:243] offset:8192\n\t" \
    "v_smfmac_f32_16x16x64_bf16 v[236:239], v[240:243], a[8:15], %[ix]\n\t" "s_waitcnt vmcnt(2)\n\t" "v_smfmac_f32_16x16x64_bf16 v[232:235], v[244:247], a[16:23], %[ix]\n\t" \
    "ds_write_b128 %[l1], v[244:247] offset:9216\n\t" "s_waitcnt lgkmcnt(8)\n\t" "v_smfmac_f32_16x16x64_bf16 v[236:239], v[244:247], v[160:167], %[ix]\n\t" \
    "s_waitcnt lgkmcnt(0)\n\t" "s_barrier\n\t" XT(86, 87) "ds_read_b128 v[16:19], %[l1] offset:12288\n\t" "ds_read_b128 v[20:23], %[l1] offset:0\n\t" \
    "ds_read_b128 v[24:27], %[l1] offset:4096\n\t" "s_waitcnt vmcnt(1)\n\t" "v_smfmac_f32_16x16x64_bf16 v[232:235], v[248:251], a[24:31], %[ix]\n\t" \
    "ds_write_b128 %[l1], v[248:251] offset:10240\n\t" "v_smfmac_f32_16x16x64_bf16 v[236:239], v[248:251], a[32:39], %[ix]\n\t" \
    "ds_read_b128 v[28:31], %[l1] offset:13312\n\t" "ds_read_b128 v[240:243], %[l1] offset:1024\n\t" "ds_read_b128 v[244:247], %[l1] offset:5120\n\t" "s_waitcnt vmcnt(0)\n\t" \
    "v_smfmac_f32_16x16x64_bf16 v[232:235], v[252:255], a[40:47], %[ix]\n\t" "ds_write_b128 %[l1], v[252:255] offset:11264\n\t" \
    "v_smfmac_f32_16x16x64_bf16 v[236:239], v[252:255], v[168:175], %[ix]\n\t" "ds_read_b128 v[160:163], %[l0] offset:8192\n\t" \
    "ds_read_b128 v[164:167], %[l0] offset:9216\n\t" "ds_read_b128 v[168:171], %[l0] offset:10240\n\t" "ds_read_b128 v[172:175], %[l0] offset:11264\n\t" "s_nop 1\n\t" \
    "v_cmp_u_f32 vcc, v232, v233\n\t" "v_cmp_u_f32 s[62:63], v234, v235\n\t" "s_waitcnt lgkmcnt(11)\n\t" \
    "v_smfmac_f32_16x16x64_bf16 v[232:235], v[16:19], a[48:55], %[ix]\n\t" "v_smfmac_f32_16x16x64_bf16 v[236:239], v[16:19], a[56:63], %[ix]\n\t" \
    "s_or_b64 vcc, vcc, s[62:63]\n\t" "s_cbranch_scc1 31f\n\t" "32:\n\t" "s_waitcnt lgkmcnt(10)\n\t" "v_smfmac_f32_16x16x64_bf16 v[232:235], v[20:23], a[64:71], %[ix]\n\t" \
    "v_smfmac_f32_16x16x64_bf16 v[236:239], v[20:23], v[176:183], %[ix]\n\t" "s_waitcnt lgkmcnt(4)\n\t" "s_barrier\n\t" XT(88, 89) "ds_read_b32 v225, v227\n\t" \
    "v_smfmac_f32_16x16x64_bf16 v[232:235], v[24:27], a[72:79], %[ix]\n\t" "ds_read_b128 v[176:179], %[l0] offset:12288\n\t" "ds_read_b128 v[180:183], %[l0] offset:13312\n\t" \
    "v_smfmac_f32_16x16x64_bf16 v[236:239], v[24:27], a[80:87], %[ix]\n\t" "ds_read_b128 v[248:251], %[l1] offset:14336\n\t" "ds_read_b128 v[252:255], %[l1] offset:2048\n\t" \
    "v_smfmac_f32_16x16x64_bf16 v[232:235], v[28:31], a[88:95], %[ix]\n\t" "ds_read_b128 v[16:19], %[l1] offset:6144\n\t" "ds_read_b128 v[20:23], %[l1] offset:15360\n\t" \
    "v_smfmac_f32_16x16x64_bf16 v[236:239], v[28:31], v[184:191], %[ix]\n\t" "ds_read_b128 v[24:27], %[l1] offset:3072\n\t" "ds_read_b128 v[184:187], %[l0] offset:14336\n\t" \
    "v_smfmac_f32_16x16x64_bf16 v[232:235], v[240:243], a[96:103], %[ix]\n\t" "ds_read_b128 v[188:191], %[l0] offset:15360\n\t" "ds_read_b128 v[28:31], %[l1] offset:7168\n\t" \
    "v_smfmac_f32_16x16x64_bf16 v[236:239], v[240:243], a[104:111], %[ix]\n\t" "s_waitcnt lgkmcnt(10)\n\t" "v_cmp_eq_u32 vcc, %[rt], v225\n\t" \
    "v_smfmac_f32_16x16x64_bf16 v[232:235], v[244:247], a[112:119], %[ix]\n\t" "s_cbranch_vccnz 33f\n\t" \
    "v_smfmac_f32_16x16x64_bf16 v[236:239], v[244:247], v[160:167], %[ix]\n\t" "ds_read_b128 v[160:163], %[l0] offset:16384\n\t" \
    "ds_read_b128 v[164:167], %[l0] offset:17408\n\t" "s_waitcnt lgkmcnt(9)\n\t" "v_smfmac_f32_16x16x64_bf16 v[232:235], v[248:251], a[120:127], %[ix]\n\t" \
    "v_smfmac_f32_16x16x64_bf16 v[236:239], v[248:251], a[128:135], %[ix]\n\t" "s_waitcnt lgkmcnt(8)\n\t" \
    "v_smfmac_f32_16x16x64_bf16 v[232:235], v[252:255], a[136:143], %[ix]\n\t" "v_smfmac_f32_16x16x64_bf16 v[236:239], v[252:255], v[168:175], %[ix]\n\t" \
    "ds_read_b128 v[168:171], %[l0] offset:18432\n\t" "ds_read_b128 v[172:175], %[l0] offset:19456\n\t" "s_waitcnt lgkmcnt(9)\n\t" \
    "v_smfmac_f32_16x16x64_bf16 v[232:235], v[16:19], a[144:151], %[ix]\n\t" "v_smfmac_f32_16x16x64_bf16 v[236:239], v[16:19], a[152:159], %[ix]\n\t" \
    "s_waitcnt lgkmcnt(8)\n\t" "v_smfmac_f32_16x16x64_bf16 v[232:235], v[20:23], a[160:167], %[ix]\n\t" \
    "v_smfmac_f32_16x16x64_bf16 v[236:239], v[20:23], v[176:183], %[ix]\n\t" "ds_read_b128 v[176:179], %[l0] offset:20480\n\t" \
    "ds_read_b128 v[180:183], %[l0] offset:21504\n\t" "s_waitcnt lgkmcnt(9)\n\t" "v_smfmac_f32_16x16x64_bf16 v[232:235], v[24:27], a[168:175], %[ix]\n\t" \
    "v_smfmac_f32_16x16x64_bf16 v[236:239], v[24:27], a[176:183], %[ix]\n\t" "s_waitcnt lgkmcnt(6)\n\t" \
    "v_smfmac_f32_16x16x64_bf16 v[232:235], v[28:31], a[184:191], %[ix]\n\t" "v_smfmac_f32_16x16x64_bf16 v[236:239], v[28:31], v[184:191], %[ix]\n\t" \
    "ds_read_b128 v[184:187], %[l0] offset:22528\n\t" "ds_read_b128 v[188:191], %[l0] offset:23552\n\t" XT(90, 91) "s_nop 4\n\t" "v_add_f32 v232, v232, v233\n\t" \
    "v_add_f32 v234, v234, v235\n\t" "v_add_f32 v236, v236, v237\n\t" "v_add_f32 v238, v238, v239\n\t" "v_cvt_pk_bf16_f32 v236, v232, v236\n\t" \
    "v_cvt_pk_bf16_f32 v237, v234, v238\n\t" "ds_write2_b32 %[vw], v236, v237 offset1:68\n\t" "s_waitcnt lgkmcnt(0)\n\t" "s_barrier\n\t" XT(92, 93) \
    "ds_read_b128 v[240:243], %[vr] offset:0\n\t" "ds_read_b128 v[244:247], %[vr] offset:64\n\t" "ds_read_b128 v[248:251], %[vr] offset:128\n\t" \
    "ds_read_b128 v[252:255], %[vr] offset:192\n\t" "s_mov_b32 s61, %[ub]\n\t" "s_waitcnt lgkmcnt(3)\n\t" \
    "v_smfmac_f32_16x16x64_bf16 v[192:195], v[240:243], v[32:39], %[ix]\n\t" "v_smfmac_f32_16x16x64_bf16 v[196:199], v[240:243], v[48:55], %[ix]\n\t" \
    "s_waitcnt lgkmcnt(2)\n\t" "v_smfmac_f32_16x16x64_bf16 v[192:195], v[244:247], v[40:47], %[ix]\n\t" \
    "v_smfmac_f32_16x16x64_bf16 v[196:199], v[244:247], v[56:63], %[ix]\n\t" "s_waitcnt lgkmcnt(1)\n\t" \
    "v_smfmac_f32_16x16x64_bf16 v[192:195], v[248:251], a[192:199], %[ix]\n\t" "v_smfmac_f32_16x16x64_bf16 v[196:199], v[248:251], a[200:207], %[ix]\n\t" \
    "s_waitcnt lgkmcnt(0)\n\t" "v_smfmac_f32_16x16x64_bf16 v[192:195], v[252:255], v[160:167], %[ix]\n\t" "ds_read_b128 v[160:163], %[l0] offset:24576\n\t" \
    "ds_read_b128 v[164:167], %[l0] offset:25600\n\t" "v_smfmac_f32_16x16x64_bf16 v[196:199], v[252:255], v[168:175], %[ix]\n\t" \
    "ds_read_b128 v[168:171], %[l0] offset:26624\n\t" "ds_read_b128 v[172:175], %[l0] offset:27648\n\t" \
    "v_smfmac_f32_16x16x64_bf16 v[200:203], v[240:243], v[64:71], %[ix]\n\t" "s_nop 1\n\t" "v_smfmac_f32_16x16x64_bf16 v[204:207], v[240:243], v[80:87], %[ix]\n\t" \
    "v_cvt_pk_bf16_f32 v224, v192, v194\n\t" "v_cvt_pk_bf16_f32 v225, v193, v195\n\t" "v_smfmac_f32_16x16x64_bf16 v[200:203], v[244:247], v[72:79], %[ix]\n\t" \
    "v_cvt_pk_bf16_f32 v226, v196, v198\n\t" "v_cvt_pk_bf16_f32 v227, v197, v199\n\t" "v_smfmac_f32_16x16x64_bf16 v[204:207], v[244:247], v[88:95], %[ix]\n\t" "s_nop 0\n\t" \
    "v_permlane32_swap_b32 v224, v226\n\t" "v_smfmac_f32_16x16x64_bf16 v[200:203], v[248:251], a[208:215], %[ix]\n\t" "v_permlane32_swap_b32 v225, v227\n\t" "s_nop 0\n\t" \
    "v_smfmac_f32_16x16x64_bf16 v[204:207], v[248:251], a[216:223], %[ix]\n\t" "v_permlane16_swap_b32 v224, v225\n\t" "v_permlane16_swap_b32 v226, v227\n\t" \
    "v_smfmac_f32_16x16x64_bf16 v[200:203], v[252:255], v[176:183], %[ix]\n\t" "ds_read_b128 v[176:179], %[l0] offset:28672\n\t" \
    "buffer_store_dwordx4 v[224:227], %[uo], %[rs], s61 offen sc1\n\t" "s_xor_b32 s61, s61, 0x40000\n\t" XT(94, 95) \
    "v_smfmac_f32_16x16x64_bf16 v[204:207], v[252:255], v[184:191], %[ix]\n\t" "ds_read_b128 v[180:183], %[l0] offset:29696\n\t" \
    "ds_read_b128 v[184:187], %[l0] offset:30720\n\t" "v_smfmac_f32_16x16x64_bf16 v[208:211], v[240:243], v[96:103], %[ix]\n\t" \
    "ds_read_b128 v[188:191], %[l0] offset:31744\n\t" "s_nop 0\n\t" "v_smfmac_f32_16x16x64_bf16 v[212:215], v[240:243], v[112:119], %[ix]\n\t" \
    "v_cvt_pk_bf16_f32 v228, v200, v202\n\t" "v_cvt_pk_bf16_f32 v229, v201, v203\n\t" "v_smfmac_f32_16x16x64_bf16 v[208:211], v[244:247], v[104:111], %[ix]\n\t" \
    "v_cvt_pk_bf16_f32 v230, v204, v206\n\t" "v_cvt_pk_bf16_f32 v231, v205, v207\n\t" "v_smfmac_f32_16x16x64_bf16 v[212:215], v[244:247], v[120:127], %[ix]\n\t" "s_nop 0\n\t" \
    "v_permlane32_swap_b32 v228, v230\n\t" "v_smfmac_f32_16x16x64_bf16 v[208:211], v[248:251], a[224:231], %[ix]\n\t" "v_permlane32_swap_b32 v229, v231\n\t" "s_nop 0\n\t" \
    "v_smfmac_f32_16x16x64_bf16 v[212:215], v[248:251], a[232:239], %[ix]\n\t" "v_permlane16_swap_b32 v228, v229\n\t" "v_permlane16_swap_b32 v230, v231\n\t" \
    "s_waitcnt lgkmcnt(6)\n\t" "v_smfmac_f32_16x16x64_bf16 v[208:211], v[252:255], v[160:167], %[ix]\n\t" "buffer_store_dwordx4 v[228:231], %[uo], %[rs], s61 offen sc1\n\t" \
    "s_xor_b32 s61, s61, 0xc0000\n\t" "s_waitcnt lgkmcnt(4)\n\t" "v_smfmac_f32_16x16x64_bf16 v[212:215], v[252:255], v[168:175], %[ix]\n\t" "s_nop 1\n\t" \
    "v_smfmac_f32_16x16x64_bf16 v[216:219], v[240:243], v[128:135], %[ix]\n\t" "s_nop 1\n\t" "v_smfmac_f32_16x16x64_bf16 v[220:223], v[240:243], v[144:151], %[ix]\n\t" \
    "v_cvt_pk_bf16_f32 v224, v208, v210\n\t" "v_cvt_pk_bf16_f32 v225, v209, v211\n\t" "v_smfmac_f32_16x16x64_bf16 v[216:219], v[244:247], v[136:143], %[ix]\n\t" \
    "v_cvt_pk_bf16_f32 v226, v212, v214\n\t" "v_cvt_pk_bf16_f32 v227, v213, v215\n\t" "v_smfmac_f32_16x16x64_bf16 v[220:223], v[244:247], v[152:159], %[ix]\n\t" "s_nop 0\n\t" \
    "v_permlane32_swap_b32 v224, v226\n\t" "v_smfmac_f32_16x16x64_bf16 v[216:219], v[248:251], a[240:247], %[ix]\n\t" "v_permlane32_swap_b32 v225, v227\n\t" "s_nop 0\n\t" \
    "v_smfmac_f32_16x16x64_bf16 v[220:223], v[248:251], a[248:255], %[ix]\n\t" "v_permlane16_swap_b32 v224, v225\n\t" "v_permlane16_swap_b32 v226, v227\n\t" \
    "s_waitcnt lgkmcnt(2)\n\t" "v_smfmac_f32_16x16x64_bf16 v[216:219], v[252:255], v[176:183], %[ix]\n\t" "buffer_store_dwordx4 v[224:227], %[uo], %[rs], s61 offen sc1\n\t" \
    "s_xor_b32 s61, s61, 0x40000\n\t" "s_waitcnt lgkmcnt(0)\n\t" "v_smfmac_f32_16x16x64_bf16 v[220:223], v[252:255], v[184:191], %[ix]\n\t" "s_nop 5\n\t" \
    "v_cvt_pk_bf16_f32 v228, v216, v218\n\t" "v_cvt_pk_bf16_f32 v229, v217, v219\n\t" "v_cvt_pk_bf16_f32 v230, v220, v222\n\t" "v_cvt_pk_bf16_f32 v231, v221, v223\n\t" \
    "s_nop 0\n\t" "v_permlane32_swap_b32 v228, v230\n\t" "v_permlane32_swap_b32 v229, v231\n\t" "s_nop 1\n\t" "v_permlane16_swap_b32 v228, v229\n\t" \
    "v_permlane16_swap_b32 v230, v231\n\t" "buffer_store_dwordx4 v[228:231], %[uo], %[rs], s61 offen sc1\n\t" "s_nop 1\n\t" XT(96, 97) "v_mov_b64 v[16:17], -1\n\t" \
    "v_mov_b64 v[18:19], -1\n\t" "s_barrier\n\t" "buffer_store_dwordx4 v[16:19], %[l1], %[rs], %[ur] offen offset:0 sc1\n\t" \
    "buffer_store_dwordx4 v[16:19], %[l1], %[rs], %[ur] offen offset:1024 sc1\n\t" "buffer_store_dwordx4 v[16:19], %[l1], %[rs], %[ur] offen offset:2048 sc1\n\t" \
    "buffer_store_dwordx4 v[16:19], %[l1], %[rs], %[ur] offen offset:3072 sc1\n\t" "s_sub_u32 s61, %[ur], 0x4000\n\t" \
    "buffer_store_dwordx4 v[16:19], %[l1], %[rs], s61 offen offset:2048 sc1\n\t" "s_branch 99f\n\t" "31:\n\t" "ds_read_b128 v[228:231], %[l1] offset:8192\n\t" \
    "s_waitcnt lgkmcnt(0)\n\t" "v_max3_u32 v225, v228, v229, v230\n\t" "v_max_u32 v225, v225, v231\n\t" "ds_read_b128 v[228:231], %[l1] offset:9216\n\t" \
    "s_waitcnt lgkmcnt(0)\n\t" "v_max3_u32 v225, v225, v228, v229\n\t" "v_max3_u32 v225, v225, v230, v231\n\t" "ds_read_b128 v[228:231], %[l1] offset:10240\n\t" \
    "s_waitcnt lgkmcnt(0)\n\t" "v_max3_u32 v225, v225, v228, v229\n\t" "v_max3_u32 v225, v225, v230, v231\n\t" "ds_read_b128 v[228:231], %[l1] offset:11264\n\t" \
    "s_waitcnt lgkmcnt(0)\n\t" "v_max3_u32 v225, v225, v228, v229\n\t" "v_max3_u32 v225, v225, v230, v231\n\t" "v_cmp_eq_u32 vcc, -1, v225\n\t" "s_cbranch_vccz 32b\n\t" \
    "s_cmp_eq_u32 %[rt], 1\n\t" "s_cbranch_scc1 32b\n\t" "v_mov_b32 v225, %[rt]\n\t" "ds_write_b32 v227, v225\n\t" "s_waitcnt lgkmcnt(0)\n\t" "s_branch 32b\n\t" "33:\n\t" \
    "s_sub_u32 %[rt], %[rt], 1\n\t" "s_waitcnt lgkmcnt(0)\n\t" XCNT(100) "34:\n\t" "buffer_load_dwordx4 v[240:243], %[mo], %[rs], %[sb] offen sc1\n\t" \
    "buffer_load_dwordx4 v[244:247], v224, %[rs], %[sb] offen sc1\n\t" "buffer_load_dwordx4 v[248:251], %[mo], %[rs], s60 offen sc1\n\t" \
    "buffer_load_dwordx4 v[252:255], v224, %[rs], s60 offen sc1\n\t" "s_waitcnt vmcnt(0)\n\t" "v_max_u32 v225, v240, v241\n\t" "v_max3_u32 v225, v225, v242, v243\n\t" \
    "v_max3_u32 v225, v225, v244, v245\n\t" "v_max3_u32 v225, v225, v246, v247\n\t" "v_max3_u32 v225, v225, v248, v249\n\t" "v_max3_u32 v225, v225, v250, v251\n\t" \
    "v_max3_u32 v225, v225, v252, v253\n\t" "v_max3_u32 v225, v225, v254, v255\n\t" "v_cmp_eq_u32 vcc, -1, v225\n\t" "s_cbranch_vccz 35f\n\t" "s_cmp_eq_u32 %[bd], 0\n\t" \
    "s_cbranch_scc1 35f\n\t" "s_sub_u32 %[bd], %[bd], 1\n\t" XCNT(101) "s_branch 34b\n\t" "35:\n\t" "ds_read_b128 v[160:163], %[l0] offset:0\n\t" \
    "ds_read_b128 v[164:167], %[l0] offset:1024\n\t" "ds_read_b128 v[168:171], %[l0] offset:2048\n\t" "ds_read_b128 v[172:175], %[l0] offset:3072\n\t" \
    "ds_read_b128 v[176:179], %[l0] offset:4096\n\t" "ds_read_b128 v[180:183], %[l0] offset:5120\n\t" "ds_read_b128 v[184:187], %[l0] offset:6144\n\t" \
    "ds_read_b128 v[188:191], %[l0] offset:7168\n\t" "v_mov_b64 v[232:233], 0\n\t" "v_mov_b64 v[234:235], 0\n\t" "v_mov_b64 v[236:237], 0\n\t" "v_mov_b64 v[238:239], 0\n\t" \
    "s_barrier\n\t" "s_branch 30b\n\t" "93:\n\t" XT(80, 81) "s_barrier\n\t" XT(82, 83) "buffer_load_dwordx4 v[240:243], %[mo], %[rs], %[sb] offen sc1\n\t" \
    "v_mov_b64 v[232:233], 0\n\t" "v_mov_b64 v[234:235], 0\n\t" "v_mov_b64 v[236:237], 0\n\t" "v_mov_b64 v[238:239], 0\n\t" "v_mov_b64 v[192:193], 0\n\t" \
    "v_mov_b64 v[194:195], 0\n\t" "v_mov_b64 v[196:197], 0\n\t" "v_mov_b64 v[198:199], 0\n\t" "buffer_load_dwordx4 v[244:247], v224, %[rs], %[sb] offen sc1\n\t" \
    "ds_read_b128 v[160:163], %[l0] offset:0\n\t" "ds_read_b128 v[164:167], %[l0] offset:1024\n\t" "ds_read_b128 v[168:171], %[l0] offset:2048\n\t" \
    "ds_read_b128 v[172:175], %[l0] offset:3072\n\t" "s_nop 3\n\t" "buffer_load_dwordx4 v[248:251], %[mo], %[rs], s60 offen sc1\n\t" \
    "ds_read_b128 v[176:179], %[l0] offset:4096\n\t" "ds_read_b128 v[180:183], %[l0] offset:5120\n\t" "ds_read_b128 v[184:187], %[l0] offset:6144\n\t" \
    "ds_read_b128 v[188:191], %[l0] offset:7168\n\t" "s_nop 3\n\t" "buffer_load_dwordx4 v[252:255], v224, %[rs], s60 offen sc1\n\t" "v_mov_b64 v[200:201], 0\n\t" \
    "v_mov_b64 v[202:203], 0\n\t" "v_mov_b64 v[204:205], 0\n\t" "v_mov_b64 v[206:207], 0\n\t" "v_mov_b64 v[208:209], 0\n\t" "v_mov_b64 v[210:211], 0\n\t" \
    "v_mov_b64 v[212:213], 0\n\t" "v_mov_b64 v[214:215], 0\n\t" "v_mov_b64 v[216:217], 0\n\t" "v_mov_b64 v[218:219], 0\n\t" "v_mov_b64 v[220:221], 0\n\t" \
    "v_mov_b64 v[222:223], 0\n\t" "v_mov_b32 v227, 160512\n\t" "40:\n\t" "s_waitcnt vmcnt(3)\n\t" XT(84, 85) \
    "v_smfmac_f32_16x16x64_bf16 v[232:235], v[240:243], a[0:7], %[ix]\n\t" "ds_write_b128 %[l1], v[240:243] offset:12288\n\t" \
    "v_smfmac_f32_16x16x64_bf16 v[236:239], v[240:243], a[8:15], %[ix]\n\t" "s_waitcnt vmcnt(2)\n\t" "v_smfmac_f32_16x16x64_bf16 v[232:235], v[244:247], a[16:23], %[ix]\n\t" \
    "ds_write_b128 %[l1], v[244:247] offset:13312\n\t" "s_waitcnt lgkmcnt(8)\n\t" "v_smfmac_f32_16x16x64_bf16 v[236:239], v[244:247], v[160:167], %[ix]\n\t" \
    "s_waitcnt lgkmcnt(0)\n\t" "s_barrier\n\t" XT(86, 87) "ds_read_b128 v[16:19], %[l1] offset:0\n\t" "ds_read_b128 v[20:23], %[l1] offset:4096\n\t" \
    "ds_read_b128 v[24:27], %[l1] offset:8192\n\t" "s_waitcnt vmcnt(1)\n\t" "v_smfmac_f32_16x16x64_bf16 v[232:235], v[248:251], a[24:31], %[ix]\n\t" \
    "ds_write_b128 %[l1], v[248:251] offset:14336\n\t" "v_smfmac_f32_16x16x64_bf16 v[236:239], v[248:251], a[32:39], %[ix]\n\t" "ds_read_b128 v[28:31], %[l1] offset:1024\n\t" \
    "ds_read_b128 v[240:243], %[l1] offset:5120\n\t" "ds_read_b128 v[244:247], %[l1] offset:9216\n\t" "s_waitcnt vmcnt(0)\n\t" \
    "v_smfmac_f32_16x16x64_bf16 v[232:235], v[252:255], a[40:47], %[ix]\n\t" "ds_write_b128 %[l1], v[252:255] offset:15360\n\t" \
    "v_smfmac_f32_16x16x64_bf16 v[236:239], v[252:255], v[168:175], %[ix]\n\t" "ds_read_b128 v[160:163], %[l0] offset:8192\n\t" \
    "ds_read_b128 v[164:167], %[l0] offset:9216\n\t" "ds_read_b128 v[168:171], %[l0] offset:10240\n\t" "ds_read_b128 v[172:175], %[l0] offset:11264\n\t" "s_nop 1\n\t" \
    "v_cmp_u_f32 vcc, v232, v233\n\t" "v_cmp_u_f32 s[62:63], v234, v235\n\t" "s_waitcnt lgkmcnt(11)\n\t" \
    "v_smfmac_f32_16x16x64_bf16 v[232:235], v[16:19], a[48:55], %[ix]\n\t" "v_smfmac_f32_16x16x64_bf16 v[236:239], v[16:19], a[56:63], %[ix]\n\t" \
    "s_or_b64 vcc, vcc, s[62:63]\n\t" "s_cbranch_scc1 41f\n\t" "42:\n\t" "s_waitcnt lgkmcnt(10)\n\t" "v_smfmac_f32_16x16x64_bf16 v[232:235], v[20:23], a[64:71], %[ix]\n\t" \
    "v_smfmac_f32_16x16x64_bf16 v[236:239], v[20:23], v[176:183], %[ix]\n\t" "s_waitcnt lgkmcnt(4)\n\t" "s_barrier\n\t" XT(88, 89) "ds_read_b32 v225, v227\n\t" \
    "v_smfmac_f32_16x16x64_bf16 v[232:235], v[24:27], a[72:79], %[ix]\n\t" "ds_read_b128 v[176:179], %[l0] offset:12288\n\t" "ds_read_b128 v[180:183], %[l0] offset:13312\n\t" \
    "v_smfmac_f32_16x16x64_bf16 v[236:239], v[24:27], a[80:87], %[ix]\n\t" "ds_read_b128 v[248:251], %[l1] offset:2048\n\t" "ds_read_b128 v[252:255], %[l1] offset:6144\n\t" \
    "v_smfmac_f32_16x16x64_bf16 v[232:235], v[28:31], a[88:95], %[ix]\n\t" "ds_read_b128 v[16:19], %[l1] offset:10240\n\t" "ds_read_b128 v[20:23], %[l1] offset:3072\n\t" \
    "v_smfmac_f32_16x16x64_bf16 v[236:239], v[28:31], v[184:191], %[ix]\n\t" "ds_read_b128 v[24:27], %[l1] offset:7168\n\t" "ds_read_b128 v[184:187], %[l0] offset:14336\n\t" \
    "v_smfmac_f32_16x16x64_bf16 v[232:235], v[240:243], a[96:103], %[ix]\n\t" "ds_read_b128 v[188:191], %[l0] offset:15360\n\t" \
    "ds_read_b128 v[28:31], %[l1] offset:11264\n\t" "v_smfmac_f32_16x16x64_bf16 v[236:239], v[240:243], a[104:111], %[ix]\n\t" "s_waitcnt lgkmcnt(10)\n\t" \
    "v_cmp_eq_u32 vcc, %[rt], v225\n\t" "v_smfmac_f32_16x16x64_bf16 v[232:235], v[244:247], a[112:119], %[ix]\n\t" "s_cbranch_vccnz 43f\n\t" \
    "v_smfmac_f32_16x16x64_bf16 v[236:239], v[244:247], v[160:167], %[ix]\n\t" "ds_read_b128 v[160:163], %[l0] offset:16384\n\t" \
    "ds_read_b128 v[164:167], %[l0] offset:17408\n\t" "s_waitcnt lgkmcnt(9)\n\t" "v_smfmac_f32_16x16x64_bf16 v[232:235], v[248:251], a[120:127], %[ix]\n\t" \
    "v_smfmac_f32_16x16x64_bf16 v[236:239], v[248:251], a[128:135], %[ix]\n\t" "s_waitcnt lgkmcnt(8)\n\t" \
    "v_smfmac_f32_16x16x64_bf16 v[232:235], v[252:255], a[136:143], %[ix]\n\t" "v_smfmac_f32_16x16x64_bf16 v[236:239], v[252:255], v[168:175], %[ix]\n\t" \
    "ds_read_b128 v[168:171], %[l0] offset:18432\n\t" "ds_read_b128 v[172:175], %[l0] offset:19456\n\t" "s_waitcnt lgkmcnt(9)\n\t" \
    "v_smfmac_f32_16x16x64_bf16 v[232:235], v[16:19], a[144:151], %[ix]\n\t" "v_smfmac_f32_16x16x64_bf16 v[236:239], v[16:19], a[152:159], %[ix]\n\t" \
    "s_waitcnt lgkmcnt(8)\n\t" "v_smfmac_f32_16x16x64_bf16 v[232:235], v[20:23], a[160:167], %[ix]\n\t" \
    "v_smfmac_f32_16x16x64_bf16 v[236:239], v[20:23], v[176:183], %[ix]\n\t" "ds_read_b128 v[176:179], %[l0] offset:20480\n\t" \
    "ds_read_b128 v[180:183], %[l0] offset:21504\n\t" "s_waitcnt lgkmcnt(9)\n\t" "v_smfmac_f32_16x16x64_bf16 v[232:235], v[24:27], a[168:175], %[ix]\n\t" \
    "v_smfmac_f32_16x16x64_bf16 v[236:239], v[24:27], a[176:183], %[ix]\n\t" "s_waitcnt lgkmcnt(6)\n\t" \
    "v_smfmac_f32_16x16x64_bf16 v[232:235], v[28:31], a[184:191], %[ix]\n\t" "v_smfmac_f32_16x16x64_bf16 v[236:239], v[28:31], v[184:191], %[ix]\n\t" \
    "ds_read_b128 v[184:187], %[l0] offset:22528\n\t" "ds_read_b128 v[188:191], %[l0] offset:23552\n\t" XT(90, 91) "s_nop 4\n\t" "v_add_f32 v232, v232, v233\n\t" \
    "v_add_f32 v234, v234, v235\n\t" "v_add_f32 v236, v236, v237\n\t" "v_add_f32 v238, v238, v239\n\t" "v_cvt_pk_bf16_f32 v236, v232, v236\n\t" \
    "v_cvt_pk_bf16_f32 v237, v234, v238\n\t" "ds_write2_b32 %[vw], v236, v237 offset1:68\n\t" "s_waitcnt lgkmcnt(0)\n\t" "s_barrier\n\t" XT(92, 93) \
    "ds_read_b128 v[240:243], %[vr] offset:0\n\t" "ds_read_b128 v[244:247], %[vr] offset:64\n\t" "ds_read_b128 v[248:251], %[vr] offset:128\n\t" \
    "ds_read_b128 v[252:255], %[vr] offset:192\n\t" "s_mov_b32 s61, %[ub]\n\t" "s_waitcnt lgkmcnt(3)\n\t" \
    "v_smfmac_f32_16x16x64_bf16 v[192:195], v[240:243], v[32:39], %[ix]\n\t" "v_smfmac_f32_16x16x64_bf16 v[196:199], v[240:243], v[48:55], %[ix]\n\t" \
    "s_waitcnt lgkmcnt(2)\n\t" "v_smfmac_f32_16x16x64_bf16 v[192:195], v[244:247], v[40:47], %[ix]\n\t" \
    "v_smfmac_f32_16x16x64_bf16 v[196:199], v[244:247], v[56:63], %[ix]\n\t" "s_waitcnt lgkmcnt(1)\n\t" \
    "v_smfmac_f32_16x16x64_bf16 v[192:195], v[248:251], a[192:199], %[ix]\n\t" "v_smfmac_f32_16x16x64_bf16 v[196:199], v[248:251], a[200:207], %[ix]\n\t" \
    "s_waitcnt lgkmcnt(0)\n\t" "v_smfmac_f32_16x16x64_bf16 v[192:195], v[252:255], v[160:167], %[ix]\n\t" "ds_read_b128 v[160:163], %[l0] offset:24576\n\t" \
    "ds_read_b128 v[164:167], %[l0] offset:25600\n\t" "v_smfmac_f32_16x16x64_bf16 v[196:199], v[252:255], v[168:175], %[ix]\n\t" \
    "ds_read_b128 v[168:171], %[l0] offset:26624\n\t" "ds_read_b128 v[172:175], %[l0] offset:27648\n\t" \
    "v_smfmac_f32_16x16x64_bf16 v[200:203], v[240:243], v[64:71], %[ix]\n\t" "s_nop 1\n\t" "v_smfmac_f32_16x16x64_bf16 v[204:207], v[240:243], v[80:87], %[ix]\n\t" \
    "v_cvt_pk_bf16_f32 v224, v192, v194\n\t" "v_cvt_pk_bf16_f32 v225, v193, v195\n\t" "v_smfmac_f32_16x16x64_bf16 v[200:203], v[244:247], v[72:79], %[ix]\n\t" \
    "v_cvt_pk_bf16_f32 v226, v196, v198\n\t" "v_cvt_pk_bf16_f32 v227, v197, v199\n\t" "v_smfmac_f32_16x16x64_bf16 v[204:207], v[244:247], v[88:95], %[ix]\n\t" "s_nop 0\n\t" \
    "v_permlane32_swap_b32 v224, v226\n\t" "v_smfmac_f32_16x16x64_bf16 v[200:203], v[248:251], a[208:215], %[ix]\n\t" "v_permlane32_swap_b32 v225, v227\n\t" "s_nop 0\n\t" \
    "v_smfmac_f32_16x16x64_bf16 v[204:207], v[248:251], a[216:223], %[ix]\n\t" "v_permlane16_swap_b32 v224, v225\n\t" "v_permlane16_swap_b32 v226, v227\n\t" \
    "v_smfmac_f32_16x16x64_bf16 v[200:203], v[252:255], v[176:183], %[ix]\n\t" "ds_read_b128 v[176:179], %[l0] offset:28672\n\t" \
    "buffer_store_dwordx4 v[224:227], %[uo], %[rs], s61 offen sc1\n\t" "s_xor_b32 s61, s61, 0x40000\n\t" XT(94, 95) \
    "v_smfmac_f32_16x16x64_bf16 v[204:207], v[252:255], v[184:191], %[ix]\n\t" "ds_read_b128 v[180:183], %[l0] offset:29696\n\t" \
    "ds_read_b128 v[184:187], %[l0] offset:30720\n\t" "v_smfmac_f32_16x16x64_bf16 v[208:211], v[240:243], v[96:103], %[ix]\n\t" \
    "ds_read_b128 v[188:191], %[l0] offset:31744\n\t" "s_nop 0\n\t" "v_smfmac_f32_16x16x64_bf16 v[212:215], v[240:243], v[112:119], %[ix]\n\t" \
    "v_cvt_pk_bf16_f32 v228, v200, v202\n\t" "v_cvt_pk_bf16_f32 v229, v201, v203\n\t" "v_smfmac_f32_16x16x64_bf16 v[208:211], v[244:247], v[104:111], %[ix]\n\t" \
    "v_cvt_pk_bf16_f32 v230, v204, v206\n\t" "v_cvt_pk_bf16_f32 v231, v205, v207\n\t" "v_smfmac_f32_16x16x64_bf16 v[212:215], v[244:247], v[120:127], %[ix]\n\t" "s_nop 0\n\t" \
    "v_permlane32_swap_b32 v228, v230\n\t" "v_smfmac_f32_16x16x64_bf16 v[208:211], v[248:251], a[224:231], %[ix]\n\t" "v_permlane32_swap_b32 v229, v231\n\t" "s_nop 0\n\t" \
    "v_smfmac_f32_16x16x64_bf16 v[212:215], v[248:251], a[232:239], %[ix]\n\t" "v_permlane16_swap_b32 v228, v229\n\t" "v_permlane16_swap_b32 v230, v231\n\t" \
    "s_waitcnt lgkmcnt(6)\n\t" "v_smfmac_f32_16x16x64_bf16 v[208:211], v[252:255], v[160:167], %[ix]\n\t" "buffer_store_dwordx4 v[228:231], %[uo], %[rs], s61 offen sc1\n\t" \
    "s_xor_b32 s61, s61, 0xc0000\n\t" "s_waitcnt lgkmcnt(4)\n\t" "v_smfmac_f32_16x16x64_bf16 v[212:215], v[252:255], v[168:175], %[ix]\n\t" "s_nop 1\n\t" \
    "v_smfmac_f32_16x16x64_bf16 v[216:219], v[240:243], v[128:135], %[ix]\n\t" "s_nop 1\n\t" "v_smfmac_f32_16x16x64_bf16 v[220:223], v[240:243], v[144:151], %[ix]\n\t" \
    "v_cvt_pk_bf16_f32 v224, v208, v210\n\t" "v_cvt_pk_bf16_f32 v225, v209, v211\n\t" "v_smfmac_f32_16x16x64_bf16 v[216:219], v[244:247], v[136:143], %[ix]\n\t" \
    "v_cvt_pk_bf16_f32 v226, v212, v214\n\t" "v_cvt_pk_bf16_f32 v227, v213, v215\n\t" "v_smfmac_f32_16x16x64_bf16 v[220:223], v[244:247], v[152:159], %[ix]\n\t" "s_nop 0\n\t" \
    "v_permlane32_swap_b32 v224, v226\n\t" "v_smfmac_f32_16x16x64_bf16 v[216:219], v[248:251], a[240:247], %[ix]\n\t" "v_permlane32_swap_b32 v225, v227\n\t" "s_nop 0\n\t" \
    "v_smfmac_f32_16x16x64_bf16 v[220:223], v[248:251], a[248:255], %[ix]\n\t" "v_permlane16_swap_b32 v224, v225\n\t" "v_permlane16_swap_b32 v226, v227\n\t" \
    "s_waitcnt lgkmcnt(2)\n\t" "v_smfmac_f32_16x16x64_bf16 v[216:219], v[252:255], v[176:183], %[ix]\n\t" "buffer_store_dwordx4 v[224:227], %[uo], %[rs], s61 offen sc1\n\t" \
    "s_xor_b32 s61, s61, 0x40000\n\t" "s_waitcnt lgkmcnt(0)\n\t" "v_smfmac_f32_16x16x64_bf16 v[220:223], v[252:255], v[184:191], %[ix]\n\t" "s_nop 5\n\t" \
    "v_cvt_pk_bf16_f32 v228, v216, v218\n\t" "v_cvt_pk_bf16_f32 v229, v217, v219\n\t" "v_cvt_pk_bf16_f32 v230, v220, v222\n\t" "v_cvt_pk_bf16_f32 v231, v221, v223\n\t" \
    "s_nop 0\n\t" "v_permlane32_swap_b32 v228, v230\n\t" "v_permlane32_swap_b32 v229, v231\n\t" "s_nop 1\n\t" "v_permlane16_swap_b32 v228, v229\n\t" \
    "v_permlane16_swap_b32 v230, v231\n\t" "buffer_store_dwordx4 v[228:231], %[uo], %[rs], s61 offen sc1\n\t" "s_nop 1\n\t" XT(96, 97) "v_mov_b64 v[16:17], -1\n\t" \
    "v_mov_b64 v[18:19], -1\n\t" "s_barrier\n\t" "buffer_store_dwordx4 v[16:19], %[l1], %[rs], %[ur] offen offset:0 sc1\n\t" \
    "buffer_store_dwordx4 v[16:19], %[l1], %[rs], %[ur] offen offset:1024 sc1\n\t" "buffer_store_dwordx4 v[16:19], %[l1], %[rs], %[ur] offen offset:2048 sc1\n\t" \
    "buffer_store_dwordx4 v[16:19], %[l1], %[rs], %[ur] offen offset:3072 sc1\n\t" "s_sub_u32 s61, %[ur], 0x5000\n\t" \
    "buffer_store_dwordx4 v[16:19], %[l1], %[rs], s61 offen offset:3072 sc1\n\t" "s_branch 99f\n\t" "41:\n\t" "ds_read_b128 v[228:231], %[l1] offset:12288\n\t" \
    "s_waitcnt lgkmcnt(0)\n\t" "v_max3_u32 v225, v228, v229, v230\n\t" "v_max_u32 v225, v225, v231\n\t" "ds_read_b128 v[228:231], %[l1] offset:13312\n\t" \
    "s_waitcnt lgkmcnt(0)\n\t" "v_max3_u32 v225, v225, v228, v229\n\t" "v_max3_u32 v225, v225, v230, v231\n\t" "ds_read_b128 v[228:231], %[l1] offset:14336\n\t" \
    "s_waitcnt lgkmcnt(0)\n\t" "v_max3_u32 v225, v225, v228, v229\n\t" "v_max3_u32 v225, v225, v230, v231\n\t" "ds_read_b128 v[228:231], %[l1] offset:15360\n\t" \
    "s_waitcnt lgkmcnt(0)\n\t" "v_max3_u32 v225, v225, v228, v229\n\t" "v_max3_u32 v225, v225, v230, v231\n\t" "v_cmp_eq_u32 vcc, -1, v225\n\t" "s_cbranch_vccz 42b\n\t" \
    "s_cmp_eq_u32 %[rt], 1\n\t" "s_cbranch_scc1 42b\n\t" "v_mov_b32 v225, %[rt]\n\t" "ds_write_b32 v227, v225\n\t" "s_waitcnt lgkmcnt(0)\n\t" "s_branch 42b\n\t" "43:\n\t" \
    "s_sub_u32 %[rt], %[rt], 1\n\t" "s_waitcnt lgkmcnt(0)\n\t" XCNT(100) "44:\n\t" "buffer_load_dwordx4 v[240:243], %[mo], %[rs], %[sb] offen sc1\n\t" \
    "buffer_load_dwordx4 v[244:247], v224, %[rs], %[sb] offen sc1\n\t" "buffer_load_dwordx4 v[248:251], %[mo], %[rs], s60 offen sc1\n\t" \
    "buffer_load_dwordx4 v[252:255], v224, %[rs], s60 offen sc1\n\t" "s_waitcnt vmcnt(0)\n\t" "v_max_u32 v225, v240, v241\n\t" "v_max3_u32 v225, v225, v242, v243\n\t" \
    "v_max3_u32 v225, v225, v244, v245\n\t" "v_max3_u32 v225, v225, v246, v247\n\t" "v_max3_u32 v225, v225, v248, v249\n\t" "v_max3_u32 v225, v225, v250, v251\n\t" \
    "v_max3_u32 v225, v225, v252, v253\n\t" "v_max3_u32 v225, v225, v254, v255\n\t" "v_cmp_eq_u32 vcc, -1, v225\n\t" "s_cbranch_vccz 45f\n\t" "s_cmp_eq_u32 %[bd], 0\n\t" \
    "s_cbranch_scc1 45f\n\t" "s_sub_u32 %[bd], %[bd], 1\n\t" XCNT(101) "s_branch 44b\n\t" "45:\n\t" "ds_read_b128 v[160:163], %[l0] offset:0\n\t" \
    "ds_read_b128 v[164:167], %[l0] offset:1024\n\t" "ds_read_b128 v[168:171], %[l0] offset:2048\n\t" "ds_read_b128 v[172:175], %[l0] offset:3072\n\t" \
    "ds_read_b128 v[176:179], %[l0] offset:4096\n\t" "ds_read_b128 v[180:183], %[l0] offset:5120\n\t" "ds_read_b128 v[184:187], %[l0] offset:6144\n\t" \
    "ds_read_b128 v[188:191], %[l0] offset:7168\n\t" "v_mov_b64 v[232:233], 0\n\t" "v_mov_b64 v[234:235], 0\n\t" "v_mov_b64 v[236:237], 0\n\t" "v_mov_b64 v[238:239], 0\n\t" \
    "s_barrier\n\t" "s_branch 40b\n\t" "99:\n\t"
#define Y_STEP \
    ".p2align 6\n\t" "v_xor_b32 v128, 0, %[u0]\n\t" "v_xor_b32 v132, 0, %[pm]\n\t" "v_xor_b32 v129, 2048, %[u0]\n\t" "v_xor_b32 v133, 1024, %[pm]\n\t" \
    "v_xor_b32 v130, 4096, %[u0]\n\t" "v_xor_b32 v134, 2048, %[pm]\n\t" "v_xor_b32 v131, 6144, %[u0]\n\t" "v_xor_b32 v135, 3072, %[pm]\n\t" "s_getpc_b64 s[62:63]\n\t" \
    "11:\n\t" "s_memtime s[64:65]\n\t" "s_waitcnt lgkmcnt(0)\n\t" "s_sub_u32 s61, %[tu], s64\n\t" "s_cmp_gt_i32 s61, 124\n\t" "s_cbranch_scc1 11b\n\t" \
    "s_max_i32 s61, s61, 0\n\t" "s_and_b32 s61, s61, 124\n\t" "s_sub_u32 s61, 12f-11b, s61\n\t" "s_add_u32 s62, s62, s61\n\t" "s_addc_u32 s63, s63, 0\n\t" \
    "s_setpc_b64 s[62:63]\n\t" ".rept 31\n\t" "s_nop 0\n\t" ".endr\n\t" "12:\n\t" XT(90, 91) "buffer_load_dwordx4 v[144:147], v128, %[rs], %[ub] offen offset:0 sc1\n\t" \
    "buffer_load_dwordx4 v[148:151], v128, %[rs], %[ub] offen offset:1024 sc1\n\t" "buffer_load_dwordx4 v[152:155], v129, %[rs], %[ub] offen offset:0 sc1\n\t" \
    "buffer_load_dwordx4 v[156:159], v129, %[rs], %[ub] offen offset:1024 sc1\n\t" "s_cmp_lg_u32 %[late], 0\n\t" "s_cbranch_scc1 14f\n\t" "s_getpc_b64 s[62:63]\n\t" "13:\n\t" \
    "s_memtime s[64:65]\n\t" "s_waitcnt lgkmcnt(0)\n\t" "s_sub_u32 s61, %[tg], s64\n\t" "s_cmp_gt_i32 s61, 124\n\t" "s_cbranch_scc1 13b\n\t" "s_max_i32 s61, s61, 0\n\t" \
    "s_and_b32 s61, s61, 124\n\t" "s_sub_u32 s61, 14f-13b, s61\n\t" "s_add_u32 s62, s62, s61\n\t" "s_addc_u32 s63, s63, 0\n\t" "s_setpc_b64 s[62:63]\n\t" ".rept 31\n\t" \
    "s_nop 0\n\t" ".endr\n\t" "14:\n\t" XT(92, 93) "20:\n\t" "buffer_load_dwordx4 v[160:163], v130, %[rs], %[ub] offen offset:0 sc1\n\t" \
    "buffer_load_dwordx4 v[164:167], v130, %[rs], %[ub] offen offset:1024 sc1\n\t" "buffer_load_dwordx4 v[192:195], %[x0], %[rs], %[pb] offen offset:0 sc1\n\t" \
    "buffer_load_dwordx4 v[196:199], %[x0], %[rs], %[pb] offen offset:1024 sc1\n\t" "buffer_load_dwordx4 v[200:203], %[x0], %[rs], %[pb] offen offset:2048 sc1\n\t" \
    "buffer_load_dwordx4 v[204:207], %[x0], %[rs], %[pb] offen offset:3072 sc1\n\t" "buffer_load_dwordx4 v[176:179], v132, %[rs], %[pb] offen sc1\n\t" \
    "v_mov_b64 v[120:121], 0\n\t" "v_mov_b64 v[122:123], 0\n\t" "v_mov_b64 v[136:137], 0\n\t" "v_mov_b64 v[138:139], 0\n\t" \
    "buffer_load_dwordx4 v[208:211], %[x1], %[rs], %[pb] offen offset:0 sc1\n\t" "buffer_load_dwordx4 v[212:215], %[x1], %[rs], %[pb] offen offset:1024 sc1\n\t" \
    "buffer_load_dwordx4 v[216:219], %[x1], %[rs], %[pb] offen offset:2048 sc1\n\t" "buffer_load_dwordx4 v[220:223], %[x1], %[rs], %[pb] offen offset:3072 sc1\n\t" \
    "buffer_load_dwordx4 v[180:183], v133, %[rs], %[pb] offen sc1\n\t" "buffer_load_dwordx4 v[224:227], %[x2], %[rs], %[pb] offen offset:0 sc1\n\t" \
    "buffer_load_dwordx4 v[228:231], %[x2], %[rs], %[pb] offen offset:1024 sc1\n\t" "buffer_load_dwordx4 v[232:235], %[x2], %[rs], %[pb] offen offset:2048 sc1\n\t" \
    "buffer_load_dwordx4 v[236:239], %[x2], %[rs], %[pb] offen offset:3072 sc1\n\t" "buffer_load_dwordx4 v[184:187], v134, %[rs], %[pb] offen sc1\n\t" \
    "buffer_load_dwordx4 v[168:171], v131, %[rs], %[ub] offen offset:0 sc1\n\t" "buffer_load_dwordx4 v[172:175], v131, %[rs], %[ub] offen offset:1024 sc1\n\t" \
    "buffer_load_dwordx4 v[240:243], %[x3], %[rs], %[pb] offen offset:0 sc1\n\t" "buffer_load_dwordx4 v[244:247], %[x3], %[rs], %[pb] offen offset:1024 sc1\n\t" \
    "buffer_load_dwordx4 v[248:251], %[x3], %[rs], %[pb] offen offset:2048 sc1\n\t" "buffer_load_dwordx4 v[252:255], %[x3], %[rs], %[pb] offen offset:3072 sc1\n\t" \
    "buffer_load_dwordx4 v[188:191], v135, %[rs], %[pb] offen sc1\n\t" "buffer_load_dword %[sv], %[so], %[rs], %[pb] offen sc1\n\t" "v_mov_b32 v127, -1\n\t" \
    "s_waitcnt vmcnt(25)\n\t" "v_permlane16_swap_b32 v144, v145\n\t" "v_permlane16_swap_b32 v146, v147\n\t" "v_permlane16_swap_b32 v148, v149\n\t" \
    "v_permlane16_swap_b32 v150, v151\n\t" "v_permlane16_swap_b32 v152, v153\n\t" "v_permlane16_swap_b32 v154, v155\n\t" "v_permlane16_swap_b32 v156, v157\n\t" \
    "v_permlane16_swap_b32 v158, v159\n\t" "s_waitcnt vmcnt(22)\n\t" XT(94, 95) "v_smfmac_f32_16x16x64_bf16 v[136:139], v[192:195], a[0:7], %[ix]\n\t" \
    "s_waitcnt vmcnt(21)\n\t" "v_smfmac_f32_16x16x64_bf16 v[120:123], v[196:199], a[8:15], %[ix]\n\t" "v_permlane16_swap_b32 v160, v161\n\t" "s_waitcnt vmcnt(20)\n\t" \
    "v_smfmac_f32_16x16x64_bf16 v[136:139], v[200:203], a[16:23], %[ix]\n\t" "s_waitcnt vmcnt(19)\n\t" \
    "v_smfmac_f32_16x16x64_bf16 v[120:123], v[204:207], a[24:31], %[ix]\n\t" "v_permlane16_swap_b32 v162, v163\n\t" "s_waitcnt vmcnt(18)\n\t" \
    "v_smfmac_f32_16x16x64_bf16 v[136:139], v[176:179], v[144:151], %[ix]\n\t" "s_waitcnt vmcnt(17)\n\t" \
    "v_smfmac_f32_16x16x64_bf16 v[120:123], v[208:211], a[32:39], %[ix]\n\t" "s_waitcnt vmcnt(16)\n\t" \
    "v_smfmac_f32_16x16x64_bf16 v[136:139], v[212:215], a[40:47], %[ix]\n\t" "v_permlane16_swap_b32 v164, v165\n\t" "s_waitcnt vmcnt(15)\n\t" \
    "v_smfmac_f32_16x16x64_bf16 v[120:123], v[216:219], a[48:55], %[ix]\n\t" "s_waitcnt vmcnt(14)\n\t" \
    "v_smfmac_f32_16x16x64_bf16 v[136:139], v[220:223], a[56:63], %[ix]\n\t" "v_permlane16_swap_b32 v166, v167\n\t" "s_waitcnt vmcnt(13)\n\t" \
    "v_smfmac_f32_16x16x64_bf16 v[120:123], v[180:183], v[152:159], %[ix]\n\t" "s_waitcnt vmcnt(12)\n\t" \
    "v_smfmac_f32_16x16x64_bf16 v[136:139], v[224:227], a[64:71], %[ix]\n\t" "s_waitcnt vmcnt(11)\n\t" \
    "v_smfmac_f32_16x16x64_bf16 v[120:123], v[228:231], a[72:79], %[ix]\n\t" "s_waitcnt vmcnt(10)\n\t" \
    "v_smfmac_f32_16x16x64_bf16 v[136:139], v[232:235], a[80:87], %[ix]\n\t" "s_waitcnt vmcnt(9)\n\t" "v_smfmac_f32_16x16x64_bf16 v[120:123], v[236:239], a[88:95], %[ix]\n\t" \
    "s_waitcnt vmcnt(8)\n\t" "v_smfmac_f32_16x16x64_bf16 v[136:139], v[184:187], v[160:167], %[ix]\n\t" "s_waitcnt vmcnt(5)\n\t" \
    "v_smfmac_f32_16x16x64_bf16 v[120:123], v[240:243], a[96:103], %[ix]\n\t" "v_permlane16_swap_b32 v168, v169\n\t" "v_permlane16_swap_b32 v170, v171\n\t" \
    "s_waitcnt vmcnt(4)\n\t" "v_smfmac_f32_16x16x64_bf16 v[136:139], v[244:247], a[104:111], %[ix]\n\t" "v_permlane16_swap_b32 v172, v173\n\t" \
    "v_permlane16_swap_b32 v174, v175\n\t" "s_waitcnt vmcnt(3)\n\t" "v_smfmac_f32_16x16x64_bf16 v[120:123], v[248:251], a[112:119], %[ix]\n\t" "s_waitcnt vmcnt(2)\n\t" \
    "v_smfmac_f32_16x16x64_bf16 v[120:123], v[252:255], a[120:127], %[ix]\n\t" "s_waitcnt vmcnt(1)\n\t" \
    "v_smfmac_f32_16x16x64_bf16 v[120:123], v[188:191], v[168:175], %[ix]\n\t" XT(82, 83) "v_add_f32 v136, v136, v137\n\t" "v_add_f32 v138, v138, v139\n\t" "s_nop 5\n\t" \
    "v_add_f32 v120, v120, v121\n\t" "v_add_f32 v122, v122, v123\n\t" "v_add_f32 v120, v120, v136\n\t" "v_add_f32 v122, v122, v138\n\t" \
    "v_cvt_pk_bf16_f32 v124, v120, v122\n\t" "v_cmp_u_f32 vcc, v120, v122\n\t" "s_cbranch_vccnz 30f\n\t" "21:\n\t" \
    "v_mov_b32_dpp v125, v124 quad_perm:[1,0,3,2] row_mask:0xf bank_mask:0xf\n\t" "v_perm_b32 v124, v125, v124, %[ps]\n\t" \
    "buffer_store_dword v124, %[mw], %[rs], %[mb] offen sc1\n\t" XT(84, 85) "buffer_store_dword v127, %[mw], %[rs], %[mr] offen sc1\n\t" "s_memtime s[64:65]\n\t" \
    "s_barrier\n\t" XT(98, 99) "v_permlane16_swap_b32 v144, v145\n\t" "v_permlane16_swap_b32 v146, v147\n\t" "v_permlane16_swap_b32 v148, v149\n\t" \
    "v_permlane16_swap_b32 v150, v151\n\t" "v_permlane16_swap_b32 v152, v153\n\t" "v_permlane16_swap_b32 v154, v155\n\t" "v_permlane16_swap_b32 v156, v157\n\t" \
    "v_permlane16_swap_b32 v158, v159\n\t" "v_permlane16_swap_b32 v160, v161\n\t" "v_permlane16_swap_b32 v162, v163\n\t" "v_permlane16_swap_b32 v164, v165\n\t" \
    "v_permlane16_swap_b32 v166, v167\n\t" "v_permlane16_swap_b32 v168, v169\n\t" "v_permlane16_swap_b32 v170, v171\n\t" "v_permlane16_swap_b32 v172, v173\n\t" \
    "v_permlane16_swap_b32 v174, v175\n\t" "s_waitcnt lgkmcnt(0)\n\t" "s_and_b32 %[bad], s64, -2\n\t" "s_branch 40f\n\t" "30:\n\t" "s_cmp_lg_u32 %[late], 0\n\t" \
    "s_cbranch_scc1 32f\n\t" XCNT(100) "s_mov_b32 %[bad], 1\n\t" "s_waitcnt vmcnt(0)\n\t" "s_branch 40f\n\t" "32:\n\t" "v_max_u32 v126, v144, v145\n\t" \
    "v_max3_u32 v126, v126, v146, v147\n\t" "v_max3_u32 v126, v126, v148, v149\n\t" "v_max3_u32 v126, v126, v150, v151\n\t" "v_max3_u32 v126, v126, v152, v153\n\t" \
    "v_max3_u32 v126, v126, v154, v155\n\t" "v_max3_u32 v126, v126, v156, v157\n\t" "v_max3_u32 v126, v126, v158, v159\n\t" "v_max3_u32 v126, v126, v160, v161\n\t" \
    "v_max3_u32 v126, v126, v162, v163\n\t" "v_max3_u32 v126, v126, v164, v165\n\t" "v_max3_u32 v126, v126, v166, v167\n\t" "v_max3_u32 v126, v126, v168, v169\n\t" \
    "v_max3_u32 v126, v126, v170, v171\n\t" "v_max3_u32 v126, v126, v172, v173\n\t" "v_max3_u32 v126, v126, v174, v175\n\t" UCNT "v_max3_u32 v126, v126, v176, v177\n\t" \
    "v_max3_u32 v126, v126, v178, v179\n\t" "v_max3_u32 v126, v126, v180, v181\n\t" "v_max3_u32 v126, v126, v182, v183\n\t" "v_max3_u32 v126, v126, v184, v185\n\t" \
    "v_max3_u32 v126, v126, v186, v187\n\t" "v_max3_u32 v126, v126, v188, v189\n\t" "v_max3_u32 v126, v126, v190, v191\n\t" "v_max3_u32 v126, v126, v192, v193\n\t" \
    "v_max3_u32 v126, v126, v194, v195\n\t" "v_max3_u32 v126, v126, v196, v197\n\t" "v_max3_u32 v126, v126, v198, v199\n\t" "v_max3_u32 v126, v126, v200, v201\n\t" \
    "v_max3_u32 v126, v126, v202, v203\n\t" "v_max3_u32 v126, v126, v204, v205\n\t" "v_max3_u32 v126, v126, v206, v207\n\t" "v_max3_u32 v126, v126, v208, v209\n\t" \
    "v_max3_u32 v126, v126, v210, v211\n\t" "v_max3_u32 v126, v126, v212, v213\n\t" "v_max3_u32 v126, v126, v214, v215\n\t" "v_max3_u32 v126, v126, v216, v217\n\t" \
    "v_max3_u32 v126, v126, v218, v219\n\t" "v_max3_u32 v126, v126, v220, v221\n\t" "v_max3_u32 v126, v126, v222, v223\n\t" "v_max3_u32 v126, v126, v224, v225\n\t" \
    "v_max3_u32 v126, v126, v226, v227\n\t" "v_max3_u32 v126, v126, v228, v229\n\t" "v_max3_u32 v126, v126, v230, v231\n\t" "v_max3_u32 v126, v126, v232, v233\n\t" \
    "v_max3_u32 v126, v126, v234, v235\n\t" "v_max3_u32 v126, v126, v236, v237\n\t" "v_max3_u32 v126, v126, v238, v239\n\t" "v_max3_u32 v126, v126, v240, v241\n\t" \
    "v_max3_u32 v126, v126, v242, v243\n\t" "v_max3_u32 v126, v126, v244, v245\n\t" "v_max3_u32 v126, v126, v246, v247\n\t" "v_max3_u32 v126, v126, v248, v249\n\t" \
    "v_max3_u32 v126, v126, v250, v251\n\t" "v_max3_u32 v126, v126, v252, v253\n\t" "v_max3_u32 v126, v126, v254, v255\n\t" "v_cmp_eq_u32 vcc, -1, v126\n\t" \
    "s_cbranch_vccz 21b\n\t" "s_cmp_eq_u32 %[bd], 0\n\t" "s_cbranch_scc1 21b\n\t" XCNT(100) "s_mov_b32 %[bad], 1\n\t" "s_waitcnt vmcnt(0)\n\t" "40:\n\t"
#define G_LOOP \
    ".p2align 6\n\t" "v_xor_b32 v72, 0, %[xo]\n\t" "v_add_u32 v80, 0x10000, v72\n\t" "v_xor_b32 v88, 0, %[xs]\n\t" "v_add_u32 v88, 0, v88\n\t" "v_xor_b32 v73, 64, %[xo]\n\t" \
    "v_add_u32 v81, 0x10000, v73\n\t" "v_xor_b32 v89, 16, %[xs]\n\t" "v_add_u32 v89, 2048, v89\n\t" "v_xor_b32 v74, 128, %[xo]\n\t" "v_add_u32 v82, 0x10000, v74\n\t" \
    "v_xor_b32 v90, 32, %[xs]\n\t" "v_add_u32 v90, 4096, v90\n\t" "v_xor_b32 v75, 192, %[xo]\n\t" "v_add_u32 v83, 0x10000, v75\n\t" "v_xor_b32 v91, 48, %[xs]\n\t" \
    "v_add_u32 v91, 6144, v91\n\t" "v_xor_b32 v76, 256, %[xo]\n\t" "v_add_u32 v84, 0x10000, v76\n\t" "v_xor_b32 v92, 64, %[xs]\n\t" "v_add_u32 v92, 8192, v92\n\t" \
    "v_xor_b32 v77, 320, %[xo]\n\t" "v_add_u32 v85, 0x10000, v77\n\t" "v_xor_b32 v93, 80, %[xs]\n\t" "v_add_u32 v93, 10240, v93\n\t" "v_xor_b32 v78, 384, %[xo]\n\t" \
    "v_add_u32 v86, 0x10000, v78\n\t" "v_xor_b32 v94, 96, %[xs]\n\t" "v_add_u32 v94, 12288, v94\n\t" "v_xor_b32 v79, 448, %[xo]\n\t" "v_add_u32 v87, 0x10000, v79\n\t" \
    "v_xor_b32 v95, 112, %[xs]\n\t" "v_add_u32 v95, 14336, v95\n\t" "v_mov_b32 v96, 2.0\n\t" "s_mov_b32 s60, 0\n\t" "s_mov_b32 s62, 0\n\t" "s_waitcnt vmcnt(0)\n\t" \
    "s_barrier\n\t" "ds_read_b128 v[100:103], v72 offset:0\n\t" "ds_read_b128 v[104:107], v72 offset:32768\n\t" "ds_read_b128 v[108:111], v73 offset:0\n\t" \
    "ds_read_b128 v[112:115], v73 offset:32768\n\t" "ds_read_b128 v[116:119], v74 offset:0\n\t" "10:\n\t" XT(80, 81) "s_waitcnt lgkmcnt(4)\n\t" \
    "v_mfma_f32_16x16x32_bf16 v[32:35], a[0:3], v[100:103], 0\n\t" "ds_read_b128 v[120:123], v74 offset:32768\n\t" "s_add_u32 s61, s60, 1\n\t" "s_and_b32 s61, s61, 63\n\t" \
    "v_mfma_f32_16x16x32_bf16 v[36:39], a[4:7], v[100:103], 0\n\t" "s_lshl_b32 s61, s61, 16\n\t" "s_add_u32 s63, %[lw], 65536\n\t" "s_waitcnt lgkmcnt(4)\n\t" \
    "v_mfma_f32_16x16x32_bf16 v[40:43], a[0:3], v[104:107], 0\n\t" "ds_read_b128 v[124:127], v75 offset:0\n\t" "v_mfma_f32_16x16x32_bf16 v[44:47], a[4:7], v[104:107], 0\n\t" \
    "s_waitcnt lgkmcnt(4)\n\t" "v_mfma_f32_16x16x32_bf16 v[32:35], a[8:11], v[108:111], v[32:35]\n\t" "ds_read_b128 v[128:131], v75 offset:32768\n\t" \
    "v_cvt_pk_bf16_f32 v64, v48, v49\n\t" "v_mfma_f32_16x16x32_bf16 v[36:39], a[12:15], v[108:111], v[36:39]\n\t" "v_cvt_pk_bf16_f32 v65, v50, v51\n\t" \
    "v_cvt_pk_bf16_f32 v66, v52, v53\n\t" "s_waitcnt lgkmcnt(4)\n\t" "v_mfma_f32_16x16x32_bf16 v[40:43], a[8:11], v[112:115], v[40:43]\n\t" \
    "ds_read_b128 v[100:103], v76 offset:0\n\t" "v_cvt_pk_bf16_f32 v67, v54, v55\n\t" "v_mfma_f32_16x16x32_bf16 v[44:47], a[12:15], v[112:115], v[44:47]\n\t" \
    "v_cvt_pk_bf16_f32 v68, v56, v57\n\t" "v_cvt_pk_bf16_f32 v69, v58, v59\n\t" "s_waitcnt lgkmcnt(4)\n\t" \
    "v_mfma_f32_16x16x32_bf16 v[32:35], a[16:19], v[116:119], v[32:35]\n\t" "ds_read_b128 v[104:107], v76 offset:32768\n\t" "v_cvt_pk_bf16_f32 v70, v60, v61\n\t" \
    "v_mfma_f32_16x16x32_bf16 v[36:39], a[20:23], v[116:119], v[36:39]\n\t" "v_cvt_pk_bf16_f32 v71, v62, v63\n\t" \
    "buffer_store_dwordx4 v[64:67], v96, %[rs], s62 offen offset:0 sc1\n\t" "s_waitcnt lgkmcnt(4)\n\t" "v_mfma_f32_16x16x32_bf16 v[40:43], a[16:19], v[120:123], v[40:43]\n\t" \
    "ds_read_b128 v[108:111], v77 offset:0\n\t" "v_mfma_f32_16x16x32_bf16 v[44:47], a[20:23], v[120:123], v[44:47]\n\t" "s_waitcnt lgkmcnt(4)\n\t" \
    "v_mfma_f32_16x16x32_bf16 v[32:35], a[24:27], v[124:127], v[32:35]\n\t" "ds_read_b128 v[112:115], v77 offset:32768\n\t" \
    "v_mfma_f32_16x16x32_bf16 v[36:39], a[28:31], v[124:127], v[36:39]\n\t" "buffer_store_dwordx4 v[68:71], v96, %[rs], s62 offen offset:256 sc1\n\t" \
    "s_waitcnt lgkmcnt(4)\n\t" "v_mfma_f32_16x16x32_bf16 v[40:43], a[24:27], v[128:131], v[40:43]\n\t" "ds_read_b128 v[116:119], v78 offset:0\n\t" "s_mov_b32 m0, s63\n\t" \
    "v_mfma_f32_16x16x32_bf16 v[44:47], a[28:31], v[128:131], v[44:47]\n\t" "s_add_u32 s63, s63, 0x800\n\t" "buffer_load_dwordx4 v88, %[rx], s61 offen lds\n\t" \
    "s_waitcnt lgkmcnt(4)\n\t" "v_mfma_f32_16x16x32_bf16 v[32:35], a[32:35], v[100:103], v[32:35]\n\t" "ds_read_b128 v[120:123], v78 offset:32768\n\t" \
    "v_mfma_f32_16x16x32_bf16 v[36:39], a[36:39], v[100:103], v[36:39]\n\t" "buffer_load_dwordx4 v88, %[rx], s61 offen offset:1024 lds\n\t" "s_waitcnt lgkmcnt(4)\n\t" \
    "v_mfma_f32_16x16x32_bf16 v[40:43], a[32:35], v[104:107], v[40:43]\n\t" "ds_read_b128 v[124:127], v79 offset:0\n\t" "s_mov_b32 m0, s63\n\t" \
    "v_mfma_f32_16x16x32_bf16 v[44:47], a[36:39], v[104:107], v[44:47]\n\t" "s_add_u32 s63, s63, 0x800\n\t" "buffer_load_dwordx4 v89, %[rx], s61 offen lds\n\t" \
    "s_waitcnt lgkmcnt(4)\n\t" "v_mfma_f32_16x16x32_bf16 v[32:35], a[40:43], v[108:111], v[32:35]\n\t" "ds_read_b128 v[128:131], v79 offset:32768\n\t" \
    "v_mfma_f32_16x16x32_bf16 v[36:39], a[44:47], v[108:111], v[36:39]\n\t" "buffer_load_dwordx4 v89, %[rx], s61 offen offset:1024 lds\n\t" "s_waitcnt lgkmcnt(4)\n\t" \
    "v_mfma_f32_16x16x32_bf16 v[40:43], a[40:43], v[112:115], v[40:43]\n\t" "ds_read_b128 v[100:103], v72 offset:512\n\t" "s_mov_b32 m0, s63\n\t" \
    "v_mfma_f32_16x16x32_bf16 v[44:47], a[44:47], v[112:115], v[44:47]\n\t" "s_add_u32 s63, s63, 0x800\n\t" "buffer_load_dwordx4 v90, %[rx], s61 offen lds\n\t" \
    "s_waitcnt lgkmcnt(4)\n\t" "v_mfma_f32_16x16x32_bf16 v[32:35], a[48:51], v[116:119], v[32:35]\n\t" "ds_read_b128 v[104:107], v72 offset:33280\n\t" \
    "v_mfma_f32_16x16x32_bf16 v[36:39], a[52:55], v[116:119], v[36:39]\n\t" "buffer_load_dwordx4 v90, %[rx], s61 offen offset:1024 lds\n\t" "s_waitcnt lgkmcnt(4)\n\t" \
    "v_mfma_f32_16x16x32_bf16 v[40:43], a[48:51], v[120:123], v[40:43]\n\t" "ds_read_b128 v[108:111], v73 offset:512\n\t" "s_mov_b32 m0, s63\n\t" \
    "v_mfma_f32_16x16x32_bf16 v[44:47], a[52:55], v[120:123], v[44:47]\n\t" "s_add_u32 s63, s63, 0x800\n\t" "buffer_load_dwordx4 v91, %[rx], s61 offen lds\n\t" \
    "s_waitcnt lgkmcnt(4)\n\t" "v_mfma_f32_16x16x32_bf16 v[32:35], a[56:59], v[124:127], v[32:35]\n\t" "ds_read_b128 v[112:115], v73 offset:33280\n\t" \
    "v_mfma_f32_16x16x32_bf16 v[36:39], a[60:63], v[124:127], v[36:39]\n\t" "buffer_load_dwordx4 v91, %[rx], s61 offen offset:1024 lds\n\t" "s_waitcnt lgkmcnt(4)\n\t" \
    "v_mfma_f32_16x16x32_bf16 v[40:43], a[56:59], v[128:131], v[40:43]\n\t" "ds_read_b128 v[116:119], v74 offset:512\n\t" "s_mov_b32 m0, s63\n\t" \
    "v_mfma_f32_16x16x32_bf16 v[44:47], a[60:63], v[128:131], v[44:47]\n\t" "s_add_u32 s63, s63, 0x800\n\t" "buffer_load_dwordx4 v92, %[rx], s61 offen lds\n\t" \
    "s_waitcnt lgkmcnt(4)\n\t" "v_mfma_f32_16x16x32_bf16 v[32:35], a[64:67], v[100:103], v[32:35]\n\t" "ds_read_b128 v[120:123], v74 offset:33280\n\t" \
    "v_mfma_f32_16x16x32_bf16 v[36:39], a[68:71], v[100:103], v[36:39]\n\t" "buffer_load_dwordx4 v92, %[rx], s61 offen offset:1024 lds\n\t" "s_waitcnt lgkmcnt(4)\n\t" \
    "v_mfma_f32_16x16x32_bf16 v[40:43], a[64:67], v[104:107], v[40:43]\n\t" "ds_read_b128 v[124:127], v75 offset:512\n\t" "s_mov_b32 m0, s63\n\t" \
    "v_mfma_f32_16x16x32_bf16 v[44:47], a[68:71], v[104:107], v[44:47]\n\t" "s_add_u32 s63, s63, 0x800\n\t" "buffer_load_dwordx4 v93, %[rx], s61 offen lds\n\t" \
    "s_waitcnt lgkmcnt(4)\n\t" "v_mfma_f32_16x16x32_bf16 v[32:35], a[72:75], v[108:111], v[32:35]\n\t" "ds_read_b128 v[128:131], v75 offset:33280\n\t" \
    "v_mfma_f32_16x16x32_bf16 v[36:39], a[76:79], v[108:111], v[36:39]\n\t" "buffer_load_dwordx4 v93, %[rx], s61 offen offset:1024 lds\n\t" "s_waitcnt lgkmcnt(4)\n\t" \
    "v_mfma_f32_16x16x32_bf16 v[40:43], a[72:75], v[112:115], v[40:43]\n\t" "ds_read_b128 v[100:103], v76 offset:512\n\t" "s_mov_b32 m0, s63\n\t" \
    "v_mfma_f32_16x16x32_bf16 v[44:47], a[76:79], v[112:115], v[44:47]\n\t" "s_add_u32 s63, s63, 0x800\n\t" "buffer_load_dwordx4 v94, %[rx], s61 offen lds\n\t" \
    "s_waitcnt lgkmcnt(4)\n\t" "v_mfma_f32_16x16x32_bf16 v[32:35], a[80:83], v[116:119], v[32:35]\n\t" "ds_read_b128 v[104:107], v76 offset:33280\n\t" \
    "v_mfma_f32_16x16x32_bf16 v[36:39], a[84:87], v[116:119], v[36:39]\n\t" "buffer_load_dwordx4 v94, %[rx], s61 offen offset:1024 lds\n\t" "s_waitcnt lgkmcnt(4)\n\t" \
    "v_mfma_f32_16x16x32_bf16 v[40:43], a[80:83], v[120:123], v[40:43]\n\t" "ds_read_b128 v[108:111], v77 offset:512\n\t" "s_mov_b32 m0, s63\n\t" \
    "v_mfma_f32_16x16x32_bf16 v[44:47], a[84:87], v[120:123], v[44:47]\n\t" "s_add_u32 s63, s63, 0x800\n\t" "buffer_load_dwordx4 v95, %[rx], s61 offen lds\n\t" \
    "s_waitcnt lgkmcnt(4)\n\t" "v_mfma_f32_16x16x32_bf16 v[32:35], a[88:91], v[124:127], v[32:35]\n\t" "ds_read_b128 v[112:115], v77 offset:33280\n\t" \
    "v_mfma_f32_16x16x32_bf16 v[36:39], a[92:95], v[124:127], v[36:39]\n\t" "buffer_load_dwordx4 v95, %[rx], s61 offen offset:1024 lds\n\t" "s_waitcnt lgkmcnt(4)\n\t" \
    "v_mfma_f32_16x16x32_bf16 v[40:43], a[88:91], v[128:131], v[40:43]\n\t" "ds_read_b128 v[116:119], v78 offset:512\n\t" \
    "v_mfma_f32_16x16x32_bf16 v[44:47], a[92:95], v[128:131], v[44:47]\n\t" "s_waitcnt lgkmcnt(4)\n\t" "v_mfma_f32_16x16x32_bf16 v[32:35], a[96:99], v[100:103], v[32:35]\n\t" \
    "ds_read_b128 v[120:123], v78 offset:33280\n\t" "s_lshr_b32 s64, s60, 2\n\t" "s_xor_b32 s64, s64, %[xp]\n\t" \
    "v_mfma_f32_16x16x32_bf16 v[36:39], a[100:103], v[100:103], v[36:39]\n\t" "s_and_b32 s64, s64, 1\n\t" "s_lshl_b32 s64, s64, 13\n\t" "s_waitcnt lgkmcnt(4)\n\t" \
    "v_mfma_f32_16x16x32_bf16 v[40:43], a[96:99], v[104:107], v[40:43]\n\t" "ds_read_b128 v[124:127], v79 offset:512\n\t" "s_lshl_b32 s62, s60, 19\n\t" \
    "v_mfma_f32_16x16x32_bf16 v[44:47], a[100:103], v[104:107], v[44:47]\n\t" "s_add_u32 s62, s62, s64\n\t" "s_add_u32 s62, s62, %[ob]\n\t" "s_waitcnt lgkmcnt(4)\n\t" \
    "v_mfma_f32_16x16x32_bf16 v[32:35], a[104:107], v[108:111], v[32:35]\n\t" "ds_read_b128 v[128:131], v79 offset:33280\n\t" "s_waitcnt vmcnt(16)\n\t" "s_barrier\n\t" \
    "v_mfma_f32_16x16x32_bf16 v[36:39], a[108:111], v[108:111], v[36:39]\n\t" "v_mov_b32 v98, s60\n\t" "buffer_store_dword v98, %[fo], %[rc], %[fl] offen sc1\n\t" \
    "s_waitcnt lgkmcnt(4)\n\t" "v_mfma_f32_16x16x32_bf16 v[40:43], a[104:107], v[112:115], v[40:43]\n\t" "ds_read_b128 v[100:103], v72 offset:1024\n\t" \
    "v_mfma_f32_16x16x32_bf16 v[44:47], a[108:111], v[112:115], v[44:47]\n\t" "s_waitcnt lgkmcnt(4)\n\t" \
    "v_mfma_f32_16x16x32_bf16 v[32:35], a[112:115], v[116:119], v[32:35]\n\t" "ds_read_b128 v[104:107], v72 offset:33792\n\t" \
    "v_mfma_f32_16x16x32_bf16 v[36:39], a[116:119], v[116:119], v[36:39]\n\t" "s_waitcnt lgkmcnt(4)\n\t" \
    "v_mfma_f32_16x16x32_bf16 v[40:43], a[112:115], v[120:123], v[40:43]\n\t" "ds_read_b128 v[108:111], v73 offset:1024\n\t" \
    "v_mfma_f32_16x16x32_bf16 v[44:47], a[116:119], v[120:123], v[44:47]\n\t" "s_waitcnt lgkmcnt(4)\n\t" \
    "v_mfma_f32_16x16x32_bf16 v[32:35], a[120:123], v[124:127], v[32:35]\n\t" "ds_read_b128 v[112:115], v73 offset:33792\n\t" \
    "v_mfma_f32_16x16x32_bf16 v[36:39], a[124:127], v[124:127], v[36:39]\n\t" "s_waitcnt lgkmcnt(4)\n\t" \
    "v_mfma_f32_16x16x32_bf16 v[40:43], a[120:123], v[128:131], v[40:43]\n\t" "ds_read_b128 v[116:119], v74 offset:1024\n\t" \
    "v_mfma_f32_16x16x32_bf16 v[44:47], a[124:127], v[128:131], v[44:47]\n\t" "s_waitcnt lgkmcnt(4)\n\t" \
    "v_mfma_f32_16x16x32_bf16 v[32:35], a[128:131], v[100:103], v[32:35]\n\t" "ds_read_b128 v[120:123], v74 offset:33792\n\t" \
    "v_mfma_f32_16x16x32_bf16 v[36:39], a[132:135], v[100:103], v[36:39]\n\t" "s_waitcnt lgkmcnt(4)\n\t" \
    "v_mfma_f32_16x16x32_bf16 v[40:43], a[128:131], v[104:107], v[40:43]\n\t" "ds_read_b128 v[124:127], v75 offset:1024\n\t" \
    "v_mfma_f32_16x16x32_bf16 v[44:47], a[132:135], v[104:107], v[44:47]\n\t" "s_waitcnt lgkmcnt(4)\n\t" \
    "v_mfma_f32_16x16x32_bf16 v[32:35], a[136:139], v[108:111], v[32:35]\n\t" "ds_read_b128 v[128:131], v75 offset:33792\n\t" \
    "v_mfma_f32_16x16x32_bf16 v[36:39], a[140:143], v[108:111], v[36:39]\n\t" "s_waitcnt lgkmcnt(4)\n\t" \
    "v_mfma_f32_16x16x32_bf16 v[40:43], a[136:139], v[112:115], v[40:43]\n\t" "ds_read_b128 v[100:103], v76 offset:1024\n\t" \
    "v_mfma_f32_16x16x32_bf16 v[44:47], a[140:143], v[112:115], v[44:47]\n\t" "s_waitcnt lgkmcnt(4)\n\t" \
    "v_mfma_f32_16x16x32_bf16 v[32:35], a[144:147], v[116:119], v[32:35]\n\t" "ds_read_b128 v[104:107], v76 offset:33792\n\t" \
    "v_mfma_f32_16x16x32_bf16 v[36:39], a[148:151], v[116:119], v[36:39]\n\t" "s_waitcnt lgkmcnt(4)\n\t" \
    "v_mfma_f32_16x16x32_bf16 v[40:43], a[144:147], v[120:123], v[40:43]\n\t" "ds_read_b128 v[108:111], v77 offset:1024\n\t" \
    "v_mfma_f32_16x16x32_bf16 v[44:47], a[148:151], v[120:123], v[44:47]\n\t" "s_waitcnt lgkmcnt(4)\n\t" \
    "v_mfma_f32_16x16x32_bf16 v[32:35], a[152:155], v[124:127], v[32:35]\n\t" "ds_read_b128 v[112:115], v77 offset:33792\n\t" \
    "v_mfma_f32_16x16x32_bf16 v[36:39], a[156:159], v[124:127], v[36:39]\n\t" "s_waitcnt lgkmcnt(4)\n\t" \
    "v_mfma_f32_16x16x32_bf16 v[40:43], a[152:155], v[128:131], v[40:43]\n\t" "ds_read_b128 v[116:119], v78 offset:1024\n\t" \
    "v_mfma_f32_16x16x32_bf16 v[44:47], a[156:159], v[128:131], v[44:47]\n\t" "s_waitcnt lgkmcnt(4)\n\t" \
    "v_mfma_f32_16x16x32_bf16 v[32:35], a[160:163], v[100:103], v[32:35]\n\t" "ds_read_b128 v[120:123], v78 offset:33792\n\t" \
    "v_mfma_f32_16x16x32_bf16 v[36:39], a[164:167], v[100:103], v[36:39]\n\t" "s_waitcnt lgkmcnt(4)\n\t" \
    "v_mfma_f32_16x16x32_bf16 v[40:43], a[160:163], v[104:107], v[40:43]\n\t" "ds_read_b128 v[124:127], v79 offset:1024\n\t" \
    "v_mfma_f32_16x16x32_bf16 v[44:47], a[164:167], v[104:107], v[44:47]\n\t" "s_waitcnt lgkmcnt(4)\n\t" \
    "v_mfma_f32_16x16x32_bf16 v[32:35], a[168:171], v[108:111], v[32:35]\n\t" "ds_read_b128 v[128:131], v79 offset:33792\n\t" \
    "v_mfma_f32_16x16x32_bf16 v[36:39], a[172:175], v[108:111], v[36:39]\n\t" "s_waitcnt lgkmcnt(4)\n\t" \
    "v_mfma_f32_16x16x32_bf16 v[40:43], a[168:171], v[112:115], v[40:43]\n\t" "ds_read_b128 v[100:103], v72 offset:1536\n\t" \
    "v_mfma_f32_16x16x32_bf16 v[44:47], a[172:175], v[112:115], v[44:47]\n\t" "s_waitcnt lgkmcnt(4)\n\t" \
    "v_mfma_f32_16x16x32_bf16 v[32:35], a[176:179], v[116:119], v[32:35]\n\t" "ds_read_b128 v[104:107], v72 offset:34304\n\t" \
    "v_mfma_f32_16x16x32_bf16 v[36:39], a[180:183], v[116:119], v[36:39]\n\t" "s_waitcnt lgkmcnt(4)\n\t" \
    "v_mfma_f32_16x16x32_bf16 v[40:43], a[176:179], v[120:123], v[40:43]\n\t" "ds_read_b128 v[108:111], v73 offset:1536\n\t" \
    "v_mfma_f32_16x16x32_bf16 v[44:47], a[180:183], v[120:123], v[44:47]\n\t" "s_waitcnt lgkmcnt(4)\n\t" \
    "v_mfma_f32_16x16x32_bf16 v[32:35], a[184:187], v[124:127], v[32:35]\n\t" "ds_read_b128 v[112:115], v73 offset:34304\n\t" \
    "v_mfma_f32_16x16x32_bf16 v[36:39], a[188:191], v[124:127], v[36:39]\n\t" "s_waitcnt lgkmcnt(4)\n\t" \
    "v_mfma_f32_16x16x32_bf16 v[40:43], a[184:187], v[128:131], v[40:43]\n\t" "ds_read_b128 v[116:119], v74 offset:1536\n\t" \
    "v_mfma_f32_16x16x32_bf16 v[44:47], a[188:191], v[128:131], v[44:47]\n\t" "s_waitcnt lgkmcnt(4)\n\t" \
    "v_mfma_f32_16x16x32_bf16 v[32:35], a[192:195], v[100:103], v[32:35]\n\t" "ds_read_b128 v[120:123], v74 offset:34304\n\t" \
    "v_mfma_f32_16x16x32_bf16 v[36:39], a[196:199], v[100:103], v[36:39]\n\t" "s_waitcnt lgkmcnt(4)\n\t" \
    "v_mfma_f32_16x16x32_bf16 v[40:43], a[192:195], v[104:107], v[40:43]\n\t" "ds_read_b128 v[124:127], v75 offset:1536\n\t" \
    "v_mfma_f32_16x16x32_bf16 v[44:47], a[196:199], v[104:107], v[44:47]\n\t" "s_waitcnt lgkmcnt(4)\n\t" \
    "v_mfma_f32_16x16x32_bf16 v[32:35], a[200:203], v[108:111], v[32:35]\n\t" "ds_read_b128 v[128:131], v75 offset:34304\n\t" \
    "v_mfma_f32_16x16x32_bf16 v[36:39], a[204:207], v[108:111], v[36:39]\n\t" "s_waitcnt lgkmcnt(4)\n\t" \
    "v_mfma_f32_16x16x32_bf16 v[40:43], a[200:203], v[112:115], v[40:43]\n\t" "ds_read_b128 v[100:103], v76 offset:1536\n\t" \
    "v_mfma_f32_16x16x32_bf16 v[44:47], a[204:207], v[112:115], v[44:47]\n\t" "s_waitcnt lgkmcnt(4)\n\t" \
    "v_mfma_f32_16x16x32_bf16 v[32:35], a[208:211], v[116:119], v[32:35]\n\t" "ds_read_b128 v[104:107], v76 offset:34304\n\t" \
    "v_mfma_f32_16x16x32_bf16 v[36:39], a[212:215], v[116:119], v[36:39]\n\t" "s_waitcnt lgkmcnt(4)\n\t" \
    "v_mfma_f32_16x16x32_bf16 v[40:43], a[208:211], v[120:123], v[40:43]\n\t" "ds_read_b128 v[108:111], v77 offset:1536\n\t" \
    "v_mfma_f32_16x16x32_bf16 v[44:47], a[212:215], v[120:123], v[44:47]\n\t" "s_waitcnt lgkmcnt(4)\n\t" \
    "v_mfma_f32_16x16x32_bf16 v[32:35], a[216:219], v[124:127], v[32:35]\n\t" "ds_read_b128 v[112:115], v77 offset:34304\n\t" \
    "v_mfma_f32_16x16x32_bf16 v[36:39], a[220:223], v[124:127], v[36:39]\n\t" "s_waitcnt lgkmcnt(4)\n\t" \
    "v_mfma_f32_16x16x32_bf16 v[40:43], a[216:219], v[128:131], v[40:43]\n\t" "ds_read_b128 v[116:119], v78 offset:1536\n\t" \
    "v_mfma_f32_16x16x32_bf16 v[44:47], a[220:223], v[128:131], v[44:47]\n\t" "s_waitcnt lgkmcnt(4)\n\t" \
    "v_mfma_f32_16x16x32_bf16 v[32:35], a[224:227], v[100:103], v[32:35]\n\t" "ds_read_b128 v[120:123], v78 offset:34304\n\t" \
    "v_mfma_f32_16x16x32_bf16 v[36:39], a[228:231], v[100:103], v[36:39]\n\t" "s_waitcnt lgkmcnt(4)\n\t" \
    "v_mfma_f32_16x16x32_bf16 v[40:43], a[224:227], v[104:107], v[40:43]\n\t" "ds_read_b128 v[124:127], v79 offset:1536\n\t" \
    "v_mfma_f32_16x16x32_bf16 v[44:47], a[228:231], v[104:107], v[44:47]\n\t" "s_waitcnt lgkmcnt(4)\n\t" \
    "v_mfma_f32_16x16x32_bf16 v[32:35], a[232:235], v[108:111], v[32:35]\n\t" "ds_read_b128 v[128:131], v79 offset:34304\n\t" \
    "v_mfma_f32_16x16x32_bf16 v[36:39], a[236:239], v[108:111], v[36:39]\n\t" "s_waitcnt lgkmcnt(4)\n\t" \
    "v_mfma_f32_16x16x32_bf16 v[40:43], a[232:235], v[112:115], v[40:43]\n\t" "v_mfma_f32_16x16x32_bf16 v[44:47], a[236:239], v[112:115], v[44:47]\n\t" \
    "s_waitcnt lgkmcnt(3)\n\t" "v_mfma_f32_16x16x32_bf16 v[32:35], a[240:243], v[116:119], v[32:35]\n\t" \
    "v_mfma_f32_16x16x32_bf16 v[36:39], a[244:247], v[116:119], v[36:39]\n\t" "s_waitcnt vmcnt(1) lgkmcnt(0)\n\t" "s_barrier\n\t" XT(82, 83) \
    "v_mfma_f32_16x16x32_bf16 v[40:43], a[240:243], v[120:123], v[40:43]\n\t" "ds_read_b128 v[100:103], v80 offset:0\n\t" \
    "v_mfma_f32_16x16x32_bf16 v[44:47], a[244:247], v[120:123], v[44:47]\n\t" "v_mfma_f32_16x16x32_bf16 v[32:35], a[248:251], v[124:127], v[32:35]\n\t" \
    "ds_read_b128 v[104:107], v80 offset:32768\n\t" "v_mfma_f32_16x16x32_bf16 v[36:39], a[252:255], v[124:127], v[36:39]\n\t" "ds_read_b128 v[108:111], v81 offset:0\n\t" \
    "v_mfma_f32_16x16x32_bf16 v[40:43], a[248:251], v[128:131], v[40:43]\n\t" "ds_read_b128 v[112:115], v81 offset:32768\n\t" "ds_read_b128 v[116:119], v82 offset:0\n\t" \
    "v_mfma_f32_16x16x32_bf16 v[44:47], a[252:255], v[128:131], v[44:47]\n\t" "s_add_u32 s60, s60, 1\n\t" GSUM "s_cmp_eq_u32 s60, 1\n\t" "s_cbranch_scc1 20f\n\t" "11:\n\t" \
    XT(80, 81) "s_waitcnt lgkmcnt(4)\n\t" "v_mfma_f32_16x16x32_bf16 v[48:51], a[0:3], v[100:103], 0\n\t" "ds_read_b128 v[120:123], v82 offset:32768\n\t" \
    "s_add_u32 s61, s60, 1\n\t" "s_and_b32 s61, s61, 63\n\t" "v_mfma_f32_16x16x32_bf16 v[52:55], a[4:7], v[100:103], 0\n\t" "s_lshl_b32 s61, s61, 16\n\t" \
    "s_add_u32 s63, %[lw], 0\n\t" "s_waitcnt lgkmcnt(4)\n\t" "v_mfma_f32_16x16x32_bf16 v[56:59], a[0:3], v[104:107], 0\n\t" "ds_read_b128 v[124:127], v83 offset:0\n\t" \
    "v_mfma_f32_16x16x32_bf16 v[60:63], a[4:7], v[104:107], 0\n\t" "s_waitcnt lgkmcnt(4)\n\t" "v_mfma_f32_16x16x32_bf16 v[48:51], a[8:11], v[108:111], v[48:51]\n\t" \
    "ds_read_b128 v[128:131], v83 offset:32768\n\t" "v_cvt_pk_bf16_f32 v64, v32, v33\n\t" "v_mfma_f32_16x16x32_bf16 v[52:55], a[12:15], v[108:111], v[52:55]\n\t" \
    "v_cvt_pk_bf16_f32 v65, v34, v35\n\t" "v_cvt_pk_bf16_f32 v66, v36, v37\n\t" "s_waitcnt lgkmcnt(4)\n\t" \
    "v_mfma_f32_16x16x32_bf16 v[56:59], a[8:11], v[112:115], v[56:59]\n\t" "ds_read_b128 v[100:103], v84 offset:0\n\t" "v_cvt_pk_bf16_f32 v67, v38, v39\n\t" \
    "v_mfma_f32_16x16x32_bf16 v[60:63], a[12:15], v[112:115], v[60:63]\n\t" "v_cvt_pk_bf16_f32 v68, v40, v41\n\t" "v_cvt_pk_bf16_f32 v69, v42, v43\n\t" \
    "s_waitcnt lgkmcnt(4)\n\t" "v_mfma_f32_16x16x32_bf16 v[48:51], a[16:19], v[116:119], v[48:51]\n\t" "ds_read_b128 v[104:107], v84 offset:32768\n\t" \
    "v_cvt_pk_bf16_f32 v70, v44, v45\n\t" "v_mfma_f32_16x16x32_bf16 v[52:55], a[20:23], v[116:119], v[52:55]\n\t" "v_cvt_pk_bf16_f32 v71, v46, v47\n\t" \
    "buffer_store_dwordx4 v[64:67], v96, %[rs], s62 offen offset:0 sc1\n\t" "s_waitcnt lgkmcnt(4)\n\t" "v_mfma_f32_16x16x32_bf16 v[56:59], a[16:19], v[120:123], v[56:59]\n\t" \
    "ds_read_b128 v[108:111], v85 offset:0\n\t" "v_mfma_f32_16x16x32_bf16 v[60:63], a[20:23], v[120:123], v[60:63]\n\t" "s_waitcnt lgkmcnt(4)\n\t" \
    "v_mfma_f32_16x16x32_bf16 v[48:51], a[24:27], v[124:127], v[48:51]\n\t" "ds_read_b128 v[112:115], v85 offset:32768\n\t" \
    "v_mfma_f32_16x16x32_bf16 v[52:55], a[28:31], v[124:127], v[52:55]\n\t" "buffer_store_dwordx4 v[68:71], v96, %[rs], s62 offen offset:256 sc1\n\t" \
    "s_waitcnt lgkmcnt(4)\n\t" "v_mfma_f32_16x16x32_bf16 v[56:59], a[24:27], v[128:131], v[56:59]\n\t" "ds_read_b128 v[116:119], v86 offset:0\n\t" "s_mov_b32 m0, s63\n\t" \
    "v_mfma_f32_16x16x32_bf16 v[60:63], a[28:31], v[128:131], v[60:63]\n\t" "s_add_u32 s63, s63, 0x800\n\t" "buffer_load_dwordx4 v88, %[rx], s61 offen lds\n\t" \
    "s_waitcnt lgkmcnt(4)\n\t" "v_mfma_f32_16x16x32_bf16 v[48:51], a[32:35], v[100:103], v[48:51]\n\t" "ds_read_b128 v[120:123], v86 offset:32768\n\t" \
    "v_mfma_f32_16x16x32_bf16 v[52:55], a[36:39], v[100:103], v[52:55]\n\t" "buffer_load_dwordx4 v88, %[rx], s61 offen offset:1024 lds\n\t" "s_waitcnt lgkmcnt(4)\n\t" \
    "v_mfma_f32_16x16x32_bf16 v[56:59], a[32:35], v[104:107], v[56:59]\n\t" "ds_read_b128 v[124:127], v87 offset:0\n\t" "s_mov_b32 m0, s63\n\t" \
    "v_mfma_f32_16x16x32_bf16 v[60:63], a[36:39], v[104:107], v[60:63]\n\t" "s_add_u32 s63, s63, 0x800\n\t" "buffer_load_dwordx4 v89, %[rx], s61 offen lds\n\t" \
    "s_waitcnt lgkmcnt(4)\n\t" "v_mfma_f32_16x16x32_bf16 v[48:51], a[40:43], v[108:111], v[48:51]\n\t" "ds_read_b128 v[128:131], v87 offset:32768\n\t" \
    "v_mfma_f32_16x16x32_bf16 v[52:55], a[44:47], v[108:111], v[52:55]\n\t" "buffer_load_dwordx4 v89, %[rx], s61 offen offset:1024 lds\n\t" "s_waitcnt lgkmcnt(4)\n\t" \
    "v_mfma_f32_16x16x32_bf16 v[56:59], a[40:43], v[112:115], v[56:59]\n\t" "ds_read_b128 v[100:103], v80 offset:512\n\t" "s_mov_b32 m0, s63\n\t" \
    "v_mfma_f32_16x16x32_bf16 v[60:63], a[44:47], v[112:115], v[60:63]\n\t" "s_add_u32 s63, s63, 0x800\n\t" "buffer_load_dwordx4 v90, %[rx], s61 offen lds\n\t" \
    "s_waitcnt lgkmcnt(4)\n\t" "v_mfma_f32_16x16x32_bf16 v[48:51], a[48:51], v[116:119], v[48:51]\n\t" "ds_read_b128 v[104:107], v80 offset:33280\n\t" \
    "v_mfma_f32_16x16x32_bf16 v[52:55], a[52:55], v[116:119], v[52:55]\n\t" "buffer_load_dwordx4 v90, %[rx], s61 offen offset:1024 lds\n\t" "s_waitcnt lgkmcnt(4)\n\t" \
    "v_mfma_f32_16x16x32_bf16 v[56:59], a[48:51], v[120:123], v[56:59]\n\t" "ds_read_b128 v[108:111], v81 offset:512\n\t" "s_mov_b32 m0, s63\n\t" \
    "v_mfma_f32_16x16x32_bf16 v[60:63], a[52:55], v[120:123], v[60:63]\n\t" "s_add_u32 s63, s63, 0x800\n\t" "buffer_load_dwordx4 v91, %[rx], s61 offen lds\n\t" \
    "s_waitcnt lgkmcnt(4)\n\t" "v_mfma_f32_16x16x32_bf16 v[48:51], a[56:59], v[124:127], v[48:51]\n\t" "ds_read_b128 v[112:115], v81 offset:33280\n\t" \
    "v_mfma_f32_16x16x32_bf16 v[52:55], a[60:63], v[124:127], v[52:55]\n\t" "buffer_load_dwordx4 v91, %[rx], s61 offen offset:1024 lds\n\t" "s_waitcnt lgkmcnt(4)\n\t" \
    "v_mfma_f32_16x16x32_bf16 v[56:59], a[56:59], v[128:131], v[56:59]\n\t" "ds_read_b128 v[116:119], v82 offset:512\n\t" "s_mov_b32 m0, s63\n\t" \
    "v_mfma_f32_16x16x32_bf16 v[60:63], a[60:63], v[128:131], v[60:63]\n\t" "s_add_u32 s63, s63, 0x800\n\t" "buffer_load_dwordx4 v92, %[rx], s61 offen lds\n\t" \
    "s_waitcnt lgkmcnt(4)\n\t" "v_mfma_f32_16x16x32_bf16 v[48:51], a[64:67], v[100:103], v[48:51]\n\t" "ds_read_b128 v[120:123], v82 offset:33280\n\t" \
    "v_mfma_f32_16x16x32_bf16 v[52:55], a[68:71], v[100:103], v[52:55]\n\t" "buffer_load_dwordx4 v92, %[rx], s61 offen offset:1024 lds\n\t" "s_waitcnt lgkmcnt(4)\n\t" \
    "v_mfma_f32_16x16x32_bf16 v[56:59], a[64:67], v[104:107], v[56:59]\n\t" "ds_read_b128 v[124:127], v83 offset:512\n\t" "s_mov_b32 m0, s63\n\t" \
    "v_mfma_f32_16x16x32_bf16 v[60:63], a[68:71], v[104:107], v[60:63]\n\t" "s_add_u32 s63, s63, 0x800\n\t" "buffer_load_dwordx4 v93, %[rx], s61 offen lds\n\t" \
    "s_waitcnt lgkmcnt(4)\n\t" "v_mfma_f32_16x16x32_bf16 v[48:51], a[72:75], v[108:111], v[48:51]\n\t" "ds_read_b128 v[128:131], v83 offset:33280\n\t" \
    "v_mfma_f32_16x16x32_bf16 v[52:55], a[76:79], v[108:111], v[52:55]\n\t" "buffer_load_dwordx4 v93, %[rx], s61 offen offset:1024 lds\n\t" "s_waitcnt lgkmcnt(4)\n\t" \
    "v_mfma_f32_16x16x32_bf16 v[56:59], a[72:75], v[112:115], v[56:59]\n\t" "ds_read_b128 v[100:103], v84 offset:512\n\t" "s_mov_b32 m0, s63\n\t" \
    "v_mfma_f32_16x16x32_bf16 v[60:63], a[76:79], v[112:115], v[60:63]\n\t" "s_add_u32 s63, s63, 0x800\n\t" "buffer_load_dwordx4 v94, %[rx], s61 offen lds\n\t" \
    "s_waitcnt lgkmcnt(4)\n\t" "v_mfma_f32_16x16x32_bf16 v[48:51], a[80:83], v[116:119], v[48:51]\n\t" "ds_read_b128 v[104:107], v84 offset:33280\n\t" \
    "v_mfma_f32_16x16x32_bf16 v[52:55], a[84:87], v[116:119], v[52:55]\n\t" "buffer_load_dwordx4 v94, %[rx], s61 offen offset:1024 lds\n\t" "s_waitcnt lgkmcnt(4)\n\t" \
    "v_mfma_f32_16x16x32_bf16 v[56:59], a[80:83], v[120:123], v[56:59]\n\t" "ds_read_b128 v[108:111], v85 offset:512\n\t" "s_mov_b32 m0, s63\n\t" \
    "v_mfma_f32_16x16x32_bf16 v[60:63], a[84:87], v[120:123], v[60:63]\n\t" "s_add_u32 s63, s63, 0x800\n\t" "buffer_load_dwordx4 v95, %[rx], s61 offen lds\n\t" \
    "s_waitcnt lgkmcnt(4)\n\t" "v_mfma_f32_16x16x32_bf16 v[48:51], a[88:91], v[124:127], v[48:51]\n\t" "ds_read_b128 v[112:115], v85 offset:33280\n\t" \
    "v_mfma_f32_16x16x32_bf16 v[52:55], a[92:95], v[124:127], v[52:55]\n\t" "buffer_load_dwordx4 v95, %[rx], s61 offen offset:1024 lds\n\t" "s_waitcnt lgkmcnt(4)\n\t" \
    "v_mfma_f32_16x16x32_bf16 v[56:59], a[88:91], v[128:131], v[56:59]\n\t" "ds_read_b128 v[116:119], v86 offset:512\n\t" \
    "v_mfma_f32_16x16x32_bf16 v[60:63], a[92:95], v[128:131], v[60:63]\n\t" "s_waitcnt lgkmcnt(4)\n\t" "v_mfma_f32_16x16x32_bf16 v[48:51], a[96:99], v[100:103], v[48:51]\n\t" \
    "ds_read_b128 v[120:123], v86 offset:33280\n\t" "s_lshr_b32 s64, s60, 2\n\t" "s_xor_b32 s64, s64, %[xp]\n\t" \
    "v_mfma_f32_16x16x32_bf16 v[52:55], a[100:103], v[100:103], v[52:55]\n\t" "s_and_b32 s64, s64, 1\n\t" "s_lshl_b32 s64, s64, 13\n\t" "s_waitcnt lgkmcnt(4)\n\t" \
    "v_mfma_f32_16x16x32_bf16 v[56:59], a[96:99], v[104:107], v[56:59]\n\t" "ds_read_b128 v[124:127], v87 offset:512\n\t" "s_lshl_b32 s62, s60, 19\n\t" \
    "v_mfma_f32_16x16x32_bf16 v[60:63], a[100:103], v[104:107], v[60:63]\n\t" "s_add_u32 s62, s62, s64\n\t" "s_add_u32 s62, s62, %[ob]\n\t" "s_waitcnt lgkmcnt(4)\n\t" \
    "v_mfma_f32_16x16x32_bf16 v[48:51], a[104:107], v[108:111], v[48:51]\n\t" "ds_read_b128 v[128:131], v87 offset:33280\n\t" "s_waitcnt vmcnt(16)\n\t" "s_barrier\n\t" \
    "v_mfma_f32_16x16x32_bf16 v[52:55], a[108:111], v[108:111], v[52:55]\n\t" "v_mov_b32 v98, s60\n\t" "buffer_store_dword v98, %[fo], %[rc], %[fl] offen sc1\n\t" \
    "s_waitcnt lgkmcnt(4)\n\t" "v_mfma_f32_16x16x32_bf16 v[56:59], a[104:107], v[112:115], v[56:59]\n\t" "ds_read_b128 v[100:103], v80 offset:1024\n\t" \
    "v_mfma_f32_16x16x32_bf16 v[60:63], a[108:111], v[112:115], v[60:63]\n\t" "s_waitcnt lgkmcnt(4)\n\t" \
    "v_mfma_f32_16x16x32_bf16 v[48:51], a[112:115], v[116:119], v[48:51]\n\t" "ds_read_b128 v[104:107], v80 offset:33792\n\t" \
    "v_mfma_f32_16x16x32_bf16 v[52:55], a[116:119], v[116:119], v[52:55]\n\t" "s_waitcnt lgkmcnt(4)\n\t" \
    "v_mfma_f32_16x16x32_bf16 v[56:59], a[112:115], v[120:123], v[56:59]\n\t" "ds_read_b128 v[108:111], v81 offset:1024\n\t" \
    "v_mfma_f32_16x16x32_bf16 v[60:63], a[116:119], v[120:123], v[60:63]\n\t" "s_waitcnt lgkmcnt(4)\n\t" \
    "v_mfma_f32_16x16x32_bf16 v[48:51], a[120:123], v[124:127], v[48:51]\n\t" "ds_read_b128 v[112:115], v81 offset:33792\n\t" \
    "v_mfma_f32_16x16x32_bf16 v[52:55], a[124:127], v[124:127], v[52:55]\n\t" "s_waitcnt lgkmcnt(4)\n\t" \
    "v_mfma_f32_16x16x32_bf16 v[56:59], a[120:123], v[128:131], v[56:59]\n\t" "ds_read_b128 v[116:119], v82 offset:1024\n\t" \
    "v_mfma_f32_16x16x32_bf16 v[60:63], a[124:127], v[128:131], v[60:63]\n\t" "s_waitcnt lgkmcnt(4)\n\t" \
    "v_mfma_f32_16x16x32_bf16 v[48:51], a[128:131], v[100:103], v[48:51]\n\t" "ds_read_b128 v[120:123], v82 offset:33792\n\t" \
    "v_mfma_f32_16x16x32_bf16 v[52:55], a[132:135], v[100:103], v[52:55]\n\t" "s_waitcnt lgkmcnt(4)\n\t" \
    "v_mfma_f32_16x16x32_bf16 v[56:59], a[128:131], v[104:107], v[56:59]\n\t" "ds_read_b128 v[124:127], v83 offset:1024\n\t" \
    "v_mfma_f32_16x16x32_bf16 v[60:63], a[132:135], v[104:107], v[60:63]\n\t" "s_waitcnt lgkmcnt(4)\n\t" \
    "v_mfma_f32_16x16x32_bf16 v[48:51], a[136:139], v[108:111], v[48:51]\n\t" "ds_read_b128 v[128:131], v83 offset:33792\n\t" \
    "v_mfma_f32_16x16x32_bf16 v[52:55], a[140:143], v[108:111], v[52:55]\n\t" "s_waitcnt lgkmcnt(4)\n\t" \
    "v_mfma_f32_16x16x32_bf16 v[56:59], a[136:139], v[112:115], v[56:59]\n\t" "ds_read_b128 v[100:103], v84 offset:1024\n\t" \
    "v_mfma_f32_16x16x32_bf16 v[60:63], a[140:143], v[112:115], v[60:63]\n\t" "s_waitcnt lgkmcnt(4)\n\t" \
    "v_mfma_f32_16x16x32_bf16 v[48:51], a[144:147], v[116:119], v[48:51]\n\t" "ds_read_b128 v[104:107], v84 offset:33792\n\t" \
    "v_mfma_f32_16x16x32_bf16 v[52:55], a[148:151], v[116:119], v[52:55]\n\t" "s_waitcnt lgkmcnt(4)\n\t" \
    "v_mfma_f32_16x16x32_bf16 v[56:59], a[144:147], v[120:123], v[56:59]\n\t" "ds_read_b128 v[108:111], v85 offset:1024\n\t" \
    "v_mfma_f32_16x16x32_bf16 v[60:63], a[148:151], v[120:123], v[60:63]\n\t" "s_waitcnt lgkmcnt(4)\n\t" \
    "v_mfma_f32_16x16x32_bf16 v[48:51], a[152:155], v[124:127], v[48:51]\n\t" "ds_read_b128 v[112:115], v85 offset:33792\n\t" \
    "v_mfma_f32_16x16x32_bf16 v[52:55], a[156:159], v[124:127], v[52:55]\n\t" "s_waitcnt lgkmcnt(4)\n\t" \
    "v_mfma_f32_16x16x32_bf16 v[56:59], a[152:155], v[128:131], v[56:59]\n\t" "ds_read_b128 v[116:119], v86 offset:1024\n\t" \
    "v_mfma_f32_16x16x32_bf16 v[60:63], a[156:159], v[128:131], v[60:63]\n\t" "s_waitcnt lgkmcnt(4)\n\t" \
    "v_mfma_f32_16x16x32_bf16 v[48:51], a[160:163], v[100:103], v[48:51]\n\t" "ds_read_b128 v[120:123], v86 offset:33792\n\t" \
    "v_mfma_f32_16x16x32_bf16 v[52:55], a[164:167], v[100:103], v[52:55]\n\t" "s_waitcnt lgkmcnt(4)\n\t" \
    "v_mfma_f32_16x16x32_bf16 v[56:59], a[160:163], v[104:107], v[56:59]\n\t" "ds_read_b128 v[124:127], v87 offset:1024\n\t" \
    "v_mfma_f32_16x16x32_bf16 v[60:63], a[164:167], v[104:107], v[60:63]\n\t" "s_waitcnt lgkmcnt(4)\n\t" \
    "v_mfma_f32_16x16x32_bf16 v[48:51], a[168:171], v[108:111], v[48:51]\n\t" "ds_read_b128 v[128:131], v87 offset:33792\n\t" \
    "v_mfma_f32_16x16x32_bf16 v[52:55], a[172:175], v[108:111], v[52:55]\n\t" "s_waitcnt lgkmcnt(4)\n\t" \
    "v_mfma_f32_16x16x32_bf16 v[56:59], a[168:171], v[112:115], v[56:59]\n\t" "ds_read_b128 v[100:103], v80 offset:1536\n\t" \
    "v_mfma_f32_16x16x32_bf16 v[60:63], a[172:175], v[112:115], v[60:63]\n\t" "s_waitcnt lgkmcnt(4)\n\t" \
    "v_mfma_f32_16x16x32_bf16 v[48:51], a[176:179], v[116:119], v[48:51]\n\t" "ds_read_b128 v[104:107], v80 offset:34304\n\t" \
    "v_mfma_f32_16x16x32_bf16 v[52:55], a[180:183], v[116:119], v[52:55]\n\t" "s_waitcnt lgkmcnt(4)\n\t" \
    "v_mfma_f32_16x16x32_bf16 v[56:59], a[176:179], v[120:123], v[56:59]\n\t" "ds_read_b128 v[108:111], v81 offset:1536\n\t" \
    "v_mfma_f32_16x16x32_bf16 v[60:63], a[180:183], v[120:123], v[60:63]\n\t" "s_waitcnt lgkmcnt(4)\n\t" \
    "v_mfma_f32_16x16x32_bf16 v[48:51], a[184:187], v[124:127], v[48:51]\n\t" "ds_read_b128 v[112:115], v81 offset:34304\n\t" \
    "v_mfma_f32_16x16x32_bf16 v[52:55], a[188:191], v[124:127], v[52:55]\n\t" "s_waitcnt lgkmcnt(4)\n\t" \
    "v_mfma_f32_16x16x32_bf16 v[56:59], a[184:187], v[128:131], v[56:59]\n\t" "ds_read_b128 v[116:119], v82 offset:1536\n\t" \
    "v_mfma_f32_16x16x32_bf16 v[60:63], a[188:191], v[128:131], v[60:63]\n\t" "s_waitcnt lgkmcnt(4)\n\t" \
    "v_mfma_f32_16x16x32_bf16 v[48:51], a[192:195], v[100:103], v[48:51]\n\t" "ds_read_b128 v[120:123], v82 offset:34304\n\t" \
    "v_mfma_f32_16x16x32_bf16 v[52:55], a[196:199], v[100:103], v[52:55]\n\t" "s_waitcnt lgkmcnt(4)\n\t" \
    "v_mfma_f32_16x16x32_bf16 v[56:59], a[192:195], v[104:107], v[56:59]\n\t" "ds_read_b128 v[124:127], v83 offset:1536\n\t" \
    "v_mfma_f32_16x16x32_bf16 v[60:63], a[196:199], v[104:107], v[60:63]\n\t" "s_waitcnt lgkmcnt(4)\n\t" \
    "v_mfma_f32_16x16x32_bf16 v[48:51], a[200:203], v[108:111], v[48:51]\n\t" "ds_read_b128 v[128:131], v83 offset:34304\n\t" \
    "v_mfma_f32_16x16x32_bf16 v[52:55], a[204:207], v[108:111], v[52:55]\n\t" "s_waitcnt lgkmcnt(4)\n\t" \
    "v_mfma_f32_16x16x32_bf16 v[56:59], a[200:203], v[112:115], v[56:59]\n\t" "ds_read_b128 v[100:103], v84 offset:1536\n\t" \
    "v_mfma_f32_16x16x32_bf16 v[60:63], a[204:207], v[112:115], v[60:63]\n\t" "s_waitcnt lgkmcnt(4)\n\t" \
    "v_mfma_f32_16x16x32_bf16 v[48:51], a[208:211], v[116:119], v[48:51]\n\t" "ds_read_b128 v[104:107], v84 offset:34304\n\t" \
    "v_mfma_f32_16x16x32_bf16 v[52:55], a[212:215], v[116:119], v[52:55]\n\t" "s_waitcnt lgkmcnt(4)\n\t" \
    "v_mfma_f32_16x16x32_bf16 v[56:59], a[208:211], v[120:123], v[56:59]\n\t" "ds_read_b128 v[108:111], v85 offset:1536\n\t" \
    "v_mfma_f32_16x16x32_bf16 v[60:63], a[212:215], v[120:123], v[60:63]\n\t" "s_waitcnt lgkmcnt(4)\n\t" \
    "v_mfma_f32_16x16x32_bf16 v[48:51], a[216:219], v[124:127], v[48:51]\n\t" "ds_read_b128 v[112:115], v85 offset:34304\n\t" \
    "v_mfma_f32_16x16x32_bf16 v[52:55], a[220:223], v[124:127], v[52:55]\n\t" "s_waitcnt lgkmcnt(4)\n\t" \
    "v_mfma_f32_16x16x32_bf16 v[56:59], a[216:219], v[128:131], v[56:59]\n\t" "ds_read_b128 v[116:119], v86 offset:1536\n\t" \
    "v_mfma_f32_16x16x32_bf16 v[60:63], a[220:223], v[128:131], v[60:63]\n\t" "s_waitcnt lgkmcnt(4)\n\t" \
    "v_mfma_f32_16x16x32_bf16 v[48:51], a[224:227], v[100:103], v[48:51]\n\t" "ds_read_b128 v[120:123], v86 offset:34304\n\t" \
    "v_mfma_f32_16x16x32_bf16 v[52:55], a[228:231], v[100:103], v[52:55]\n\t" "s_waitcnt lgkmcnt(4)\n\t" \
    "v_mfma_f32_16x16x32_bf16 v[56:59], a[224:227], v[104:107], v[56:59]\n\t" "ds_read_b128 v[124:127], v87 offset:1536\n\t" \
    "v_mfma_f32_16x16x32_bf16 v[60:63], a[228:231], v[104:107], v[60:63]\n\t" "s_waitcnt lgkmcnt(4)\n\t" \
    "v_mfma_f32_16x16x32_bf16 v[48:51], a[232:235], v[108:111], v[48:51]\n\t" "ds_read_b128 v[128:131], v87 offset:34304\n\t" \
    "v_mfma_f32_16x16x32_bf16 v[52:55], a[236:239], v[108:111], v[52:55]\n\t" "s_waitcnt lgkmcnt(4)\n\t" \
    "v_mfma_f32_16x16x32_bf16 v[56:59], a[232:235], v[112:115], v[56:59]\n\t" "v_mfma_f32_16x16x32_bf16 v[60:63], a[236:239], v[112:115], v[60:63]\n\t" \
    "s_waitcnt lgkmcnt(3)\n\t" "v_mfma_f32_16x16x32_bf16 v[48:51], a[240:243], v[116:119], v[48:51]\n\t" \
    "v_mfma_f32_16x16x32_bf16 v[52:55], a[244:247], v[116:119], v[52:55]\n\t" "s_waitcnt vmcnt(1) lgkmcnt(0)\n\t" "s_barrier\n\t" XT(82, 83) \
    "v_mfma_f32_16x16x32_bf16 v[56:59], a[240:243], v[120:123], v[56:59]\n\t" "ds_read_b128 v[100:103], v72 offset:0\n\t" \
    "v_mfma_f32_16x16x32_bf16 v[60:63], a[244:247], v[120:123], v[60:63]\n\t" "v_mfma_f32_16x16x32_bf16 v[48:51], a[248:251], v[124:127], v[48:51]\n\t" \
    "ds_read_b128 v[104:107], v72 offset:32768\n\t" "v_mfma_f32_16x16x32_bf16 v[52:55], a[252:255], v[124:127], v[52:55]\n\t" "ds_read_b128 v[108:111], v73 offset:0\n\t" \
    "v_mfma_f32_16x16x32_bf16 v[56:59], a[248:251], v[128:131], v[56:59]\n\t" "ds_read_b128 v[112:115], v73 offset:32768\n\t" "ds_read_b128 v[116:119], v74 offset:0\n\t" \
    "v_mfma_f32_16x16x32_bf16 v[60:63], a[252:255], v[128:131], v[60:63]\n\t" "s_add_u32 s60, s60, 1\n\t" GSUM "s_cmp_lt_u32 s60, 64\n\t" "s_cbranch_scc1 10b\n\t" \
    "s_nop 7\n\t" "v_cvt_pk_bf16_f32 v64, v48, v49\n\t" "v_cvt_pk_bf16_f32 v65, v50, v51\n\t" "v_cvt_pk_bf16_f32 v66, v52, v53\n\t" "v_cvt_pk_bf16_f32 v67, v54, v55\n\t" \
    "v_cvt_pk_bf16_f32 v68, v56, v57\n\t" "v_cvt_pk_bf16_f32 v69, v58, v59\n\t" "v_cvt_pk_bf16_f32 v70, v60, v61\n\t" "v_cvt_pk_bf16_f32 v71, v62, v63\n\t" \
    "buffer_store_dwordx4 v[64:67], v96, %[rs], s62 offen offset:0 sc1\n\t" "buffer_store_dwordx4 v[68:71], v96, %[rs], s62 offen offset:256 sc1\n\t" "s_waitcnt vmcnt(0)\n\t" \
    "s_barrier\n\t" "v_mov_b32 v98, s60\n\t" "buffer_store_dword v98, %[fo], %[rc], %[fl] offen sc1\n\t" "s_waitcnt lgkmcnt(0)\n\t" "s_branch 30f\n\t" "20:\n\t" \
    "v_mov_b32 v96, %[so]\n\t" "s_nop 7\n\t" "v_cvt_pk_bf16_f32 v64, v32, v33\n\t" "v_cvt_pk_bf16_f32 v65, v34, v35\n\t" "v_cvt_pk_bf16_f32 v66, v36, v37\n\t" \
    "v_cvt_pk_bf16_f32 v67, v38, v39\n\t" "v_cvt_pk_bf16_f32 v68, v40, v41\n\t" "v_cvt_pk_bf16_f32 v69, v42, v43\n\t" "v_cvt_pk_bf16_f32 v70, v44, v45\n\t" \
    "v_cvt_pk_bf16_f32 v71, v46, v47\n\t" "buffer_store_dwordx4 v[64:67], v96, %[rs], s62 offen offset:0 sc1\n\t" \
    "buffer_store_dwordx4 v[68:71], v96, %[rs], s62 offen offset:256 sc1\n\t" "s_waitcnt vmcnt(0)\n\t" "s_barrier\n\t" "v_mov_b32 v98, s60\n\t" \
    "buffer_store_dword v98, %[fo], %[rc], %[fl] offen sc1\n\t" "s_branch 11b\n\t" "30:\n\t"
#define ZC_LOOP \
    ".p2align 6\n\t" "s_mov_b32 s60, 0\n\t" "s_mov_b32 s61, 0\n\t" "s_mov_b32 s62, 0\n\t" "s_mov_b32 s63, 0x7f000000\n\t" "s_cmp_ge_u32 s61, %[z0]\n\t" \
    "s_cbranch_scc1 52f\n\t" "51:\n\t" "buffer_load_dword v120, %[fo], %[rc], %[fv] offen sc1\n\t" "buffer_load_dword v121, %[fo], %[rc], %[vd] offen sc1\n\t" \
    "s_waitcnt vmcnt(0)\n\t" "v_readfirstlane_b32 s61, v120\n\t" "v_readfirstlane_b32 s66, v121\n\t" "s_nop 3\n\t" "s_cmp_ge_u32 s61, %[z0]\n\t" "s_cbranch_scc1 52f\n\t" \
    "s_bitcmp1_b32 s66, 1\n\t" "s_cbranch_scc1 90f\n\t" "s_cmp_eq_u32 %[bd], 0\n\t" "s_cbranch_scc1 52f\n\t" "s_sub_u32 %[bd], %[bd], 1\n\t" "s_sleep 2\n\t" \
    "s_branch 51b\n\t" "52:\n\t" "s_lshr_b32 s66, s60, 2\n\t" "s_xor_b32 s66, s66, %[xp]\n\t" "s_and_b32 s66, s66, 1\n\t" "s_lshl_b32 s66, s66, 13\n\t" \
    "s_lshl_b32 s64, s60, 19\n\t" "s_add_u32 s64, s64, s66\n\t" "s_add_u32 s64, s64, %[vb]\n\t" "buffer_load_dwordx4 v[16:19], %[ao], %[rs], s64 offen offset:0 sc1\n\t" \
    "buffer_load_dwordx4 v[20:23], %[ao], %[rs], s64 offen offset:256 sc1\n\t" "buffer_load_dwordx4 v[24:27], %[ao], %[rs], s64 offen offset:2048 sc1\n\t" \
    "buffer_load_dwordx4 v[28:31], %[ao], %[rs], s64 offen offset:2304 sc1\n\t" "buffer_load_dwordx4 v[32:35], %[a1], %[rs], s64 offen offset:0 sc1\n\t" \
    "buffer_load_dwordx4 v[36:39], %[a1], %[rs], s64 offen offset:256 sc1\n\t" "buffer_load_dwordx4 v[40:43], %[a1], %[rs], s64 offen offset:2048 sc1\n\t" \
    "buffer_load_dwordx4 v[44:47], %[a1], %[rs], s64 offen offset:2304 sc1\n\t" "s_waitcnt vmcnt(0)\n\t" "10:\n\t" XT(80, 81) "s_add_u32 s66, s62, %[rg]\n\t" \
    "s_cmp_lt_u32 s66, s60\n\t" "s_cbranch_scc1 60f\n\t" "63:\n\t" "s_cmp_lt_u32 s60, 12\n\t" "s_cbranch_scc1 40f\n\t" "s_add_u32 s66, s60, 1\n\t" "s_cmp_gt_u32 s61, s66\n\t" \
    "s_cbranch_scc0 40f\n\t" "v_mfma_f32_16x16x32_bf16 v[80:83], v[16:19], a[0:3], 0\n\t" "v_mfma_f32_16x16x32_bf16 v[84:87], v[20:23], a[0:3], 0\n\t" \
    "v_mfma_f32_16x16x32_bf16 v[80:83], v[24:27], a[4:7], v[80:83]\n\t" "v_mfma_f32_16x16x32_bf16 v[84:87], v[28:31], a[4:7], v[84:87]\n\t" \
    "v_mfma_f32_16x16x32_bf16 v[80:83], v[32:35], a[8:11], v[80:83]\n\t" "v_cvt_pk_bf16_f32 v112, v96, v97\n\t" \
    "v_mfma_f32_16x16x32_bf16 v[84:87], v[36:39], a[8:11], v[84:87]\n\t" "v_cvt_pk_bf16_f32 v113, v98, v99\n\t" \
    "v_mfma_f32_16x16x32_bf16 v[80:83], v[40:43], a[12:15], v[80:83]\n\t" "v_cvt_pk_bf16_f32 v114, v100, v101\n\t" \
    "v_mfma_f32_16x16x32_bf16 v[84:87], v[44:47], a[12:15], v[84:87]\n\t" "v_cvt_pk_bf16_f32 v115, v102, v103\n\t" \
    "v_mfma_f32_16x16x32_bf16 v[88:91], v[16:19], a[16:19], 0\n\t" "v_cvt_pk_bf16_f32 v116, v104, v105\n\t" "v_mfma_f32_16x16x32_bf16 v[92:95], v[20:23], a[16:19], 0\n\t" \
    "v_cvt_pk_bf16_f32 v117, v106, v107\n\t" "v_mfma_f32_16x16x32_bf16 v[88:91], v[24:27], a[20:23], v[88:91]\n\t" \
    "buffer_store_dwordx4 v[112:115], %[z1], %[rs], s63 offen sc1\n\t" "v_mfma_f32_16x16x32_bf16 v[92:95], v[28:31], a[20:23], v[92:95]\n\t" \
    "v_mfma_f32_16x16x32_bf16 v[88:91], v[32:35], a[24:27], v[88:91]\n\t" "v_cvt_pk_bf16_f32 v118, v108, v109\n\t" \
    "v_mfma_f32_16x16x32_bf16 v[92:95], v[36:39], a[24:27], v[92:95]\n\t" "v_cvt_pk_bf16_f32 v119, v110, v111\n\t" \
    "v_mfma_f32_16x16x32_bf16 v[88:91], v[40:43], a[28:31], v[88:91]\n\t" "buffer_store_dwordx4 v[116:119], %[z2], %[rs], s63 offen sc1\n\t" "s_and_b32 s63, s60, %[rm]\n\t" \
    "v_mfma_f32_16x16x32_bf16 v[92:95], v[44:47], a[28:31], v[92:95]\n\t" "s_lshl_b32 s63, s63, 22\n\t" "s_add_u32 s63, s63, %[zb]\n\t" \
    "v_mfma_f32_16x16x32_bf16 v[96:99], v[16:19], a[32:35], 0\n\t" "s_add_u32 s65, s60, 1\n\t" "s_lshr_b32 s66, s65, 2\n\t" \
    "v_mfma_f32_16x16x32_bf16 v[100:103], v[20:23], a[32:35], 0\n\t" "s_xor_b32 s66, s66, %[xp]\n\t" "s_and_b32 s66, s66, 1\n\t" \
    "v_mfma_f32_16x16x32_bf16 v[96:99], v[24:27], a[36:39], v[96:99]\n\t" "s_lshl_b32 s66, s66, 13\n\t" "s_lshl_b32 s64, s65, 19\n\t" \
    "v_mfma_f32_16x16x32_bf16 v[100:103], v[28:31], a[36:39], v[100:103]\n\t" "s_add_u32 s64, s64, s66\n\t" "s_add_u32 s64, s64, %[vb]\n\t" \
    "v_mfma_f32_16x16x32_bf16 v[96:99], v[32:35], a[40:43], v[96:99]\n\t" "buffer_load_dwordx4 v[48:51], %[ao], %[rs], s64 offen offset:0 sc1\n\t" \
    "v_cvt_pk_bf16_f32 v112, v80, v81\n\t" "v_mfma_f32_16x16x32_bf16 v[100:103], v[36:39], a[40:43], v[100:103]\n\t" "v_cvt_pk_bf16_f32 v113, v82, v83\n\t" \
    "v_mfma_f32_16x16x32_bf16 v[96:99], v[40:43], a[44:47], v[96:99]\n\t" "buffer_load_dwordx4 v[52:55], %[ao], %[rs], s64 offen offset:256 sc1\n\t" \
    "v_cvt_pk_bf16_f32 v114, v84, v85\n\t" "v_mfma_f32_16x16x32_bf16 v[100:103], v[44:47], a[44:47], v[100:103]\n\t" "v_cvt_pk_bf16_f32 v115, v86, v87\n\t" \
    "v_mfma_f32_16x16x32_bf16 v[104:107], v[16:19], a[48:51], 0\n\t" "buffer_load_dwordx4 v[56:59], %[ao], %[rs], s64 offen offset:2048 sc1\n\t" \
    "v_cvt_pk_bf16_f32 v116, v88, v89\n\t" "v_mfma_f32_16x16x32_bf16 v[108:111], v[20:23], a[48:51], 0\n\t" "v_cvt_pk_bf16_f32 v117, v90, v91\n\t" \
    "v_mfma_f32_16x16x32_bf16 v[104:107], v[24:27], a[52:55], v[104:107]\n\t" "buffer_load_dwordx4 v[60:63], %[ao], %[rs], s64 offen offset:2304 sc1\n\t" \
    "v_mfma_f32_16x16x32_bf16 v[108:111], v[28:31], a[52:55], v[108:111]\n\t" "buffer_store_dwordx4 v[112:115], %[z1], %[rs], s63 offen sc1\n\t" \
    "v_mfma_f32_16x16x32_bf16 v[104:107], v[32:35], a[56:59], v[104:107]\n\t" "buffer_load_dwordx4 v[64:67], %[a1], %[rs], s64 offen offset:0 sc1\n\t" \
    "v_cvt_pk_bf16_f32 v118, v92, v93\n\t" "v_mfma_f32_16x16x32_bf16 v[108:111], v[36:39], a[56:59], v[108:111]\n\t" "v_cvt_pk_bf16_f32 v119, v94, v95\n\t" \
    "v_mfma_f32_16x16x32_bf16 v[104:107], v[40:43], a[60:63], v[104:107]\n\t" "buffer_load_dwordx4 v[68:71], %[a1], %[rs], s64 offen offset:256 sc1\n\t" \
    "buffer_store_dwordx4 v[116:119], %[z2], %[rs], s63 offen sc1\n\t" "v_mfma_f32_16x16x32_bf16 v[108:111], v[44:47], a[60:63], v[108:111]\n\t" \
    "s_add_u32 s63, s63, 0x20000\n\t" "v_mfma_f32_16x16x32_bf16 v[80:83], v[16:19], a[64:67], 0\n\t" \
    "buffer_load_dwordx4 v[72:75], %[a1], %[rs], s64 offen offset:2048 sc1\n\t" "v_mfma_f32_16x16x32_bf16 v[84:87], v[20:23], a[64:67], 0\n\t" \
    "v_mfma_f32_16x16x32_bf16 v[80:83], v[24:27], a[68:71], v[80:83]\n\t" "buffer_load_dwordx4 v[76:79], %[a1], %[rs], s64 offen offset:2304 sc1\n\t" \
    "v_mfma_f32_16x16x32_bf16 v[84:87], v[28:31], a[68:71], v[84:87]\n\t" "v_mfma_f32_16x16x32_bf16 v[80:83], v[32:35], a[72:75], v[80:83]\n\t" \
    "buffer_load_dword v120, %[fo], %[rc], %[fv] offen sc1\n\t" "v_cvt_pk_bf16_f32 v112, v96, v97\n\t" "v_mfma_f32_16x16x32_bf16 v[84:87], v[36:39], a[72:75], v[84:87]\n\t" \
    "v_cvt_pk_bf16_f32 v113, v98, v99\n\t" "v_mfma_f32_16x16x32_bf16 v[80:83], v[40:43], a[76:79], v[80:83]\n\t" "buffer_load_dword v121, %[fo], %[rc], %[fs] offen sc1\n\t" \
    "v_mfma_f32_16x16x32_bf16 v[84:87], v[44:47], a[76:79], v[84:87]\n\t" "v_cvt_pk_bf16_f32 v114, v100, v101\n\t" "v_cvt_pk_bf16_f32 v115, v102, v103\n\t" \
    "v_mfma_f32_16x16x32_bf16 v[88:91], v[16:19], a[80:83], 0\n\t" "v_cvt_pk_bf16_f32 v116, v104, v105\n\t" "v_mfma_f32_16x16x32_bf16 v[92:95], v[20:23], a[80:83], 0\n\t" \
    "v_cvt_pk_bf16_f32 v117, v106, v107\n\t" "v_mfma_f32_16x16x32_bf16 v[88:91], v[24:27], a[84:87], v[88:91]\n\t" \
    "buffer_store_dwordx4 v[112:115], %[z1], %[rs], s63 offen sc1\n\t" "v_mfma_f32_16x16x32_bf16 v[92:95], v[28:31], a[84:87], v[92:95]\n\t" \
    "v_mfma_f32_16x16x32_bf16 v[88:91], v[32:35], a[88:91], v[88:91]\n\t" "v_cvt_pk_bf16_f32 v118, v108, v109\n\t" \
    "v_mfma_f32_16x16x32_bf16 v[92:95], v[36:39], a[88:91], v[92:95]\n\t" "v_cvt_pk_bf16_f32 v119, v110, v111\n\t" \
    "v_mfma_f32_16x16x32_bf16 v[88:91], v[40:43], a[92:95], v[88:91]\n\t" "buffer_store_dwordx4 v[116:119], %[z2], %[rs], s63 offen sc1\n\t" \
    "v_mfma_f32_16x16x32_bf16 v[92:95], v[44:47], a[92:95], v[92:95]\n\t" "s_add_u32 s63, s63, 0x20000\n\t" "v_mfma_f32_16x16x32_bf16 v[96:99], v[16:19], a[96:99], 0\n\t" \
    "v_mfma_f32_16x16x32_bf16 v[100:103], v[20:23], a[96:99], 0\n\t" "v_mfma_f32_16x16x32_bf16 v[96:99], v[24:27], a[100:103], v[96:99]\n\t" \
    "v_mfma_f32_16x16x32_bf16 v[100:103], v[28:31], a[100:103], v[100:103]\n\t" "v_mfma_f32_16x16x32_bf16 v[96:99], v[32:35], a[104:107], v[96:99]\n\t" \
    "v_cvt_pk_bf16_f32 v112, v80, v81\n\t" "v_mfma_f32_16x16x32_bf16 v[100:103], v[36:39], a[104:107], v[100:103]\n\t" "v_cvt_pk_bf16_f32 v113, v82, v83\n\t" \
    "v_mfma_f32_16x16x32_bf16 v[96:99], v[40:43], a[108:111], v[96:99]\n\t" "v_cvt_pk_bf16_f32 v114, v84, v85\n\t" \
    "v_mfma_f32_16x16x32_bf16 v[100:103], v[44:47], a[108:111], v[100:103]\n\t" "v_cvt_pk_bf16_f32 v115, v86, v87\n\t" \
    "v_mfma_f32_16x16x32_bf16 v[104:107], v[16:19], a[112:115], 0\n\t" "v_cvt_pk_bf16_f32 v116, v88, v89\n\t" \
    "v_mfma_f32_16x16x32_bf16 v[108:111], v[20:23], a[112:115], 0\n\t" "v_cvt_pk_bf16_f32 v117, v90, v91\n\t" \
    "v_mfma_f32_16x16x32_bf16 v[104:107], v[24:27], a[116:119], v[104:107]\n\t" "buffer_store_dwordx4 v[112:115], %[z1], %[rs], s63 offen sc1\n\t" \
    "v_mfma_f32_16x16x32_bf16 v[108:111], v[28:31], a[116:119], v[108:111]\n\t" "v_mfma_f32_16x16x32_bf16 v[104:107], v[32:35], a[120:123], v[104:107]\n\t" \
    "v_cvt_pk_bf16_f32 v118, v92, v93\n\t" "v_mfma_f32_16x16x32_bf16 v[108:111], v[36:39], a[120:123], v[108:111]\n\t" "v_cvt_pk_bf16_f32 v119, v94, v95\n\t" \
    "v_mfma_f32_16x16x32_bf16 v[104:107], v[40:43], a[124:127], v[104:107]\n\t" "buffer_store_dwordx4 v[116:119], %[z2], %[rs], s63 offen sc1\n\t" \
    "v_mfma_f32_16x16x32_bf16 v[108:111], v[44:47], a[124:127], v[108:111]\n\t" "s_add_u32 s63, s63, 0x20000\n\t" \
    "v_mfma_f32_16x16x32_bf16 v[80:83], v[16:19], a[128:131], 0\n\t" "v_mfma_f32_16x16x32_bf16 v[84:87], v[20:23], a[128:131], 0\n\t" \
    "v_mfma_f32_16x16x32_bf16 v[80:83], v[24:27], a[132:135], v[80:83]\n\t" "v_mfma_f32_16x16x32_bf16 v[84:87], v[28:31], a[132:135], v[84:87]\n\t" \
    "v_mfma_f32_16x16x32_bf16 v[80:83], v[32:35], a[136:139], v[80:83]\n\t" "v_cvt_pk_bf16_f32 v112, v96, v97\n\t" \
    "v_mfma_f32_16x16x32_bf16 v[84:87], v[36:39], a[136:139], v[84:87]\n\t" "v_cvt_pk_bf16_f32 v113, v98, v99\n\t" \
    "v_mfma_f32_16x16x32_bf16 v[80:83], v[40:43], a[140:143], v[80:83]\n\t" "v_cvt_pk_bf16_f32 v114, v100, v101\n\t" \
    "v_mfma_f32_16x16x32_bf16 v[84:87], v[44:47], a[140:143], v[84:87]\n\t" "v_cvt_pk_bf16_f32 v115, v102, v103\n\t" \
    "v_mfma_f32_16x16x32_bf16 v[88:91], v[16:19], a[144:147], 0\n\t" "v_cvt_pk_bf16_f32 v116, v104, v105\n\t" "v_mfma_f32_16x16x32_bf16 v[92:95], v[20:23], a[144:147], 0\n\t" \
    "v_cvt_pk_bf16_f32 v117, v106, v107\n\t" "v_mfma_f32_16x16x32_bf16 v[88:91], v[24:27], a[148:151], v[88:91]\n\t" \
    "buffer_store_dwordx4 v[112:115], %[z1], %[rs], s63 offen sc1\n\t" "v_mfma_f32_16x16x32_bf16 v[92:95], v[28:31], a[148:151], v[92:95]\n\t" \
    "v_mfma_f32_16x16x32_bf16 v[88:91], v[32:35], a[152:155], v[88:91]\n\t" "v_cvt_pk_bf16_f32 v118, v108, v109\n\t" \
    "v_mfma_f32_16x16x32_bf16 v[92:95], v[36:39], a[152:155], v[92:95]\n\t" "v_cvt_pk_bf16_f32 v119, v110, v111\n\t" \
    "v_mfma_f32_16x16x32_bf16 v[88:91], v[40:43], a[156:159], v[88:91]\n\t" "buffer_store_dwordx4 v[116:119], %[z2], %[rs], s63 offen sc1\n\t" \
    "v_mfma_f32_16x16x32_bf16 v[92:95], v[44:47], a[156:159], v[92:95]\n\t" "s_add_u32 s63, s63, 0x20000\n\t" "v_mfma_f32_16x16x32_bf16 v[96:99], v[16:19], a[160:163], 0\n\t" \
    "v_mfma_f32_16x16x32_bf16 v[100:103], v[20:23], a[160:163], 0\n\t" "v_mfma_f32_16x16x32_bf16 v[96:99], v[24:27], a[164:167], v[96:99]\n\t" \
    "v_mfma_f32_16x16x32_bf16 v[100:103], v[28:31], a[164:167], v[100:103]\n\t" "v_mfma_f32_16x16x32_bf16 v[96:99], v[32:35], a[168:171], v[96:99]\n\t" \
    "v_cvt_pk_bf16_f32 v112, v80, v81\n\t" "v_mfma_f32_16x16x32_bf16 v[100:103], v[36:39], a[168:171], v[100:103]\n\t" "v_cvt_pk_bf16_f32 v113, v82, v83\n\t" \
    "v_mfma_f32_16x16x32_bf16 v[96:99], v[40:43], a[172:175], v[96:99]\n\t" "v_cvt_pk_bf16_f32 v114, v84, v85\n\t" \
    "v_mfma_f32_16x16x32_bf16 v[100:103], v[44:47], a[172:175], v[100:103]\n\t" "v_cvt_pk_bf16_f32 v115, v86, v87\n\t" \
    "v_mfma_f32_16x16x32_bf16 v[104:107], v[16:19], a[176:179], 0\n\t" "v_cvt_pk_bf16_f32 v116, v88, v89\n\t" \
    "v_mfma_f32_16x16x32_bf16 v[108:111], v[20:23], a[176:179], 0\n\t" "v_cvt_pk_bf16_f32 v117, v90, v91\n\t" \
    "v_mfma_f32_16x16x32_bf16 v[104:107], v[24:27], a[180:183], v[104:107]\n\t" "buffer_store_dwordx4 v[112:115], %[z1], %[rs], s63 offen sc1\n\t" \
    "v_mfma_f32_16x16x32_bf16 v[108:111], v[28:31], a[180:183], v[108:111]\n\t" "v_mfma_f32_16x16x32_bf16 v[104:107], v[32:35], a[184:187], v[104:107]\n\t" \
    "v_cvt_pk_bf16_f32 v118, v92, v93\n\t" "v_mfma_f32_16x16x32_bf16 v[108:111], v[36:39], a[184:187], v[108:111]\n\t" "v_cvt_pk_bf16_f32 v119, v94, v95\n\t" \
    "v_mfma_f32_16x16x32_bf16 v[104:107], v[40:43], a[188:191], v[104:107]\n\t" "buffer_store_dwordx4 v[116:119], %[z2], %[rs], s63 offen sc1\n\t" \
    "v_mfma_f32_16x16x32_bf16 v[108:111], v[44:47], a[188:191], v[108:111]\n\t" "s_add_u32 s63, s63, 0x20000\n\t" \
    "v_mfma_f32_16x16x32_bf16 v[80:83], v[16:19], a[192:195], 0\n\t" "v_mfma_f32_16x16x32_bf16 v[84:87], v[20:23], a[192:195], 0\n\t" \
    "v_mfma_f32_16x16x32_bf16 v[80:83], v[24:27], a[196:199], v[80:83]\n\t" "v_mfma_f32_16x16x32_bf16 v[84:87], v[28:31], a[196:199], v[84:87]\n\t" \
    "v_mfma_f32_16x16x32_bf16 v[80:83], v[32:35], a[200:203], v[80:83]\n\t" "v_cvt_pk_bf16_f32 v112, v96, v97\n\t" \
    "v_mfma_f32_16x16x32_bf16 v[84:87], v[36:39], a[200:203], v[84:87]\n\t" "v_cvt_pk_bf16_f32 v113, v98, v99\n\t" \
    "v_mfma_f32_16x16x32_bf16 v[80:83], v[40:43], a[204:207], v[80:83]\n\t" "v_cvt_pk_bf16_f32 v114, v100, v101\n\t" \
    "v_mfma_f32_16x16x32_bf16 v[84:87], v[44:47], a[204:207], v[84:87]\n\t" "v_cvt_pk_bf16_f32 v115, v102, v103\n\t" \
    "v_mfma_f32_16x16x32_bf16 v[88:91], v[16:19], a[208:211], 0\n\t" "v_cvt_pk_bf16_f32 v116, v104, v105\n\t" "v_mfma_f32_16x16x32_bf16 v[92:95], v[20:23], a[208:211], 0\n\t" \
    "v_cvt_pk_bf16_f32 v117, v106, v107\n\t" "v_mfma_f32_16x16x32_bf16 v[88:91], v[24:27], a[212:215], v[88:91]\n\t" \
    "buffer_store_dwordx4 v[112:115], %[z1], %[rs], s63 offen sc1\n\t" "v_mfma_f32_16x16x32_bf16 v[92:95], v[28:31], a[212:215], v[92:95]\n\t" \
    "v_mfma_f32_16x16x32_bf16 v[88:91], v[32:35], a[216:219], v[88:91]\n\t" "v_cvt_pk_bf16_f32 v118, v108, v109\n\t" \
    "v_mfma_f32_16x16x32_bf16 v[92:95], v[36:39], a[216:219], v[92:95]\n\t" "v_cvt_pk_bf16_f32 v119, v110, v111\n\t" \
    "v_mfma_f32_16x16x32_bf16 v[88:91], v[40:43], a[220:223], v[88:91]\n\t" "buffer_store_dwordx4 v[116:119], %[z2], %[rs], s63 offen sc1\n\t" \
    "v_mfma_f32_16x16x32_bf16 v[92:95], v[44:47], a[220:223], v[92:95]\n\t" "s_add_u32 s63, s63, 0x20000\n\t" "v_mfma_f32_16x16x32_bf16 v[96:99], v[16:19], a[224:227], 0\n\t" \
    "v_mfma_f32_16x16x32_bf16 v[100:103], v[20:23], a[224:227], 0\n\t" "v_mfma_f32_16x16x32_bf16 v[96:99], v[24:27], a[228:231], v[96:99]\n\t" \
    "v_mfma_f32_16x16x32_bf16 v[100:103], v[28:31], a[228:231], v[100:103]\n\t" "v_mfma_f32_16x16x32_bf16 v[96:99], v[32:35], a[232:235], v[96:99]\n\t" \
    "v_cvt_pk_bf16_f32 v112, v80, v81\n\t" "v_mfma_f32_16x16x32_bf16 v[100:103], v[36:39], a[232:235], v[100:103]\n\t" "v_cvt_pk_bf16_f32 v113, v82, v83\n\t" \
    "v_mfma_f32_16x16x32_bf16 v[96:99], v[40:43], a[236:239], v[96:99]\n\t" "v_cvt_pk_bf16_f32 v114, v84, v85\n\t" \
    "v_mfma_f32_16x16x32_bf16 v[100:103], v[44:47], a[236:239], v[100:103]\n\t" "v_cvt_pk_bf16_f32 v115, v86, v87\n\t" \
    "v_mfma_f32_16x16x32_bf16 v[104:107], v[16:19], a[240:243], 0\n\t" "v_cvt_pk_bf16_f32 v116, v88, v89\n\t" \
    "v_mfma_f32_16x16x32_bf16 v[108:111], v[20:23], a[240:243], 0\n\t" "v_cvt_pk_bf16_f32 v117, v90, v91\n\t" \
    "v_mfma_f32_16x16x32_bf16 v[104:107], v[24:27], a[244:247], v[104:107]\n\t" "buffer_store_dwordx4 v[112:115], %[z1], %[rs], s63 offen sc1\n\t" \
    "v_mfma_f32_16x16x32_bf16 v[108:111], v[28:31], a[244:247], v[108:111]\n\t" "v_mfma_f32_16x16x32_bf16 v[104:107], v[32:35], a[248:251], v[104:107]\n\t" \
    "v_cvt_pk_bf16_f32 v118, v92, v93\n\t" "v_cvt_pk_bf16_f32 v119, v94, v95\n\t" "s_waitcnt vmcnt(11)\n\t" "v_readfirstlane_b32 s66, v120\n\t" \
    "v_mfma_f32_16x16x32_bf16 v[108:111], v[36:39], a[248:251], v[108:111]\n\t" "v_readfirstlane_b32 s67, v121\n\t" "v_mov_b32 v122, s60\n\t" \
    "buffer_store_dword v122, %[fo], %[rc], %[fz] offen sc1\n\t" "v_mfma_f32_16x16x32_bf16 v[104:107], v[40:43], a[252:255], v[104:107]\n\t" \
    "buffer_store_dwordx4 v[116:119], %[z2], %[rs], s63 offen sc1\n\t" "s_add_u32 s63, s63, 0x20000\n\t" "s_max_u32 s61, s61, s66\n\t" \
    "v_mfma_f32_16x16x32_bf16 v[108:111], v[44:47], a[252:255], v[108:111]\n\t" "s_max_u32 s62, s62, s67\n\t" "s_add_u32 s60, s60, 1\n\t" ZSUM "23:\n\t" XT(80, 81) \
    "s_add_u32 s66, s62, %[rg]\n\t" "s_cmp_lt_u32 s66, s60\n\t" "s_cbranch_scc1 70f\n\t" "73:\n\t" "s_cmp_lt_u32 s60, 12\n\t" "s_cbranch_scc1 41f\n\t" \
    "s_add_u32 s66, s60, 1\n\t" "s_cmp_gt_u32 s61, s66\n\t" "s_cbranch_scc0 41f\n\t" "v_mfma_f32_16x16x32_bf16 v[80:83], v[48:51], a[0:3], 0\n\t" \
    "v_mfma_f32_16x16x32_bf16 v[84:87], v[52:55], a[0:3], 0\n\t" "v_mfma_f32_16x16x32_bf16 v[80:83], v[56:59], a[4:7], v[80:83]\n\t" \
    "v_mfma_f32_16x16x32_bf16 v[84:87], v[60:63], a[4:7], v[84:87]\n\t" "v_mfma_f32_16x16x32_bf16 v[80:83], v[64:67], a[8:11], v[80:83]\n\t" \
    "v_cvt_pk_bf16_f32 v112, v96, v97\n\t" "v_mfma_f32_16x16x32_bf16 v[84:87], v[68:71], a[8:11], v[84:87]\n\t" "v_cvt_pk_bf16_f32 v113, v98, v99\n\t" \
    "v_mfma_f32_16x16x32_bf16 v[80:83], v[72:75], a[12:15], v[80:83]\n\t" "v_cvt_pk_bf16_f32 v114, v100, v101\n\t" \
    "v_mfma_f32_16x16x32_bf16 v[84:87], v[76:79], a[12:15], v[84:87]\n\t" "v_cvt_pk_bf16_f32 v115, v102, v103\n\t" \
    "v_mfma_f32_16x16x32_bf16 v[88:91], v[48:51], a[16:19], 0\n\t" "v_cvt_pk_bf16_f32 v116, v104, v105\n\t" "v_mfma_f32_16x16x32_bf16 v[92:95], v[52:55], a[16:19], 0\n\t" \
    "v_cvt_pk_bf16_f32 v117, v106, v107\n\t" "v_mfma_f32_16x16x32_bf16 v[88:91], v[56:59], a[20:23], v[88:91]\n\t" \
    "buffer_store_dwordx4 v[112:115], %[z1], %[rs], s63 offen sc1\n\t" "v_mfma_f32_16x16x32_bf16 v[92:95], v[60:63], a[20:23], v[92:95]\n\t" \
    "v_mfma_f32_16x16x32_bf16 v[88:91], v[64:67], a[24:27], v[88:91]\n\t" "v_cvt_pk_bf16_f32 v118, v108, v109\n\t" \
    "v_mfma_f32_16x16x32_bf16 v[92:95], v[68:71], a[24:27], v[92:95]\n\t" "v_cvt_pk_bf16_f32 v119, v110, v111\n\t" \
    "v_mfma_f32_16x16x32_bf16 v[88:91], v[72:75], a[28:31], v[88:91]\n\t" "buffer_store_dwordx4 v[116:119], %[z2], %[rs], s63 offen sc1\n\t" "s_and_b32 s63, s60, %[rm]\n\t" \
    "v_mfma_f32_16x16x32_bf16 v[92:95], v[76:79], a[28:31], v[92:95]\n\t" "s_lshl_b32 s63, s63, 22\n\t" "s_add_u32 s63, s63, %[zb]\n\t" \
    "v_mfma_f32_16x16x32_bf16 v[96:99], v[48:51], a[32:35], 0\n\t" "s_add_u32 s65, s60, 1\n\t" "s_lshr_b32 s66, s65, 2\n\t" \
    "v_mfma_f32_16x16x32_bf16 v[100:103], v[52:55], a[32:35], 0\n\t" "s_xor_b32 s66, s66, %[xp]\n\t" "s_and_b32 s66, s66, 1\n\t" \
    "v_mfma_f32_16x16x32_bf16 v[96:99], v[56:59], a[36:39], v[96:99]\n\t" "s_lshl_b32 s66, s66, 13\n\t" "s_lshl_b32 s64, s65, 19\n\t" \
    "v_mfma_f32_16x16x32_bf16 v[100:103], v[60:63], a[36:39], v[100:103]\n\t" "s_add_u32 s64, s64, s66\n\t" "s_add_u32 s64, s64, %[vb]\n\t" \
    "v_mfma_f32_16x16x32_bf16 v[96:99], v[64:67], a[40:43], v[96:99]\n\t" "buffer_load_dwordx4 v[16:19], %[ao], %[rs], s64 offen offset:0 sc1\n\t" \
    "v_cvt_pk_bf16_f32 v112, v80, v81\n\t" "v_mfma_f32_16x16x32_bf16 v[100:103], v[68:71], a[40:43], v[100:103]\n\t" "v_cvt_pk_bf16_f32 v113, v82, v83\n\t" \
    "v_mfma_f32_16x16x32_bf16 v[96:99], v[72:75], a[44:47], v[96:99]\n\t" "buffer_load_dwordx4 v[20:23], %[ao], %[rs], s64 offen offset:256 sc1\n\t" \
    "v_cvt_pk_bf16_f32 v114, v84, v85\n\t" "v_mfma_f32_16x16x32_bf16 v[100:103], v[76:79], a[44:47], v[100:103]\n\t" "v_cvt_pk_bf16_f32 v115, v86, v87\n\t" \
    "v_mfma_f32_16x16x32_bf16 v[104:107], v[48:51], a[48:51], 0\n\t" "buffer_load_dwordx4 v[24:27], %[ao], %[rs], s64 offen offset:2048 sc1\n\t" \
    "v_cvt_pk_bf16_f32 v116, v88, v89\n\t" "v_mfma_f32_16x16x32_bf16 v[108:111], v[52:55], a[48:51], 0\n\t" "v_cvt_pk_bf16_f32 v117, v90, v91\n\t" \
    "v_mfma_f32_16x16x32_bf16 v[104:107], v[56:59], a[52:55], v[104:107]\n\t" "buffer_load_dwordx4 v[28:31], %[ao], %[rs], s64 offen offset:2304 sc1\n\t" \
    "v_mfma_f32_16x16x32_bf16 v[108:111], v[60:63], a[52:55], v[108:111]\n\t" "buffer_store_dwordx4 v[112:115], %[z1], %[rs], s63 offen sc1\n\t" \
    "v_mfma_f32_16x16x32_bf16 v[104:107], v[64:67], a[56:59], v[104:107]\n\t" "buffer_load_dwordx4 v[32:35], %[a1], %[rs], s64 offen offset:0 sc1\n\t" \
    "v_cvt_pk_bf16_f32 v118, v92, v93\n\t" "v_mfma_f32_16x16x32_bf16 v[108:111], v[68:71], a[56:59], v[108:111]\n\t" "v_cvt_pk_bf16_f32 v119, v94, v95\n\t" \
    "v_mfma_f32_16x16x32_bf16 v[104:107], v[72:75], a[60:63], v[104:107]\n\t" "buffer_load_dwordx4 v[36:39], %[a1], %[rs], s64 offen offset:256 sc1\n\t" \
    "buffer_store_dwordx4 v[116:119], %[z2], %[rs], s63 offen sc1\n\t" "v_mfma_f32_16x16x32_bf16 v[108:111], v[76:79], a[60:63], v[108:111]\n\t" \
    "s_add_u32 s63, s63, 0x20000\n\t" "v_mfma_f32_16x16x32_bf16 v[80:83], v[48:51], a[64:67], 0\n\t" \
    "buffer_load_dwordx4 v[40:43], %[a1], %[rs], s64 offen offset:2048 sc1\n\t" "v_mfma_f32_16x16x32_bf16 v[84:87], v[52:55], a[64:67], 0\n\t" \
    "v_mfma_f32_16x16x32_bf16 v[80:83], v[56:59], a[68:71], v[80:83]\n\t" "buffer_load_dwordx4 v[44:47], %[a1], %[rs], s64 offen offset:2304 sc1\n\t" \
    "v_mfma_f32_16x16x32_bf16 v[84:87], v[60:63], a[68:71], v[84:87]\n\t" "v_mfma_f32_16x16x32_bf16 v[80:83], v[64:67], a[72:75], v[80:83]\n\t" \
    "buffer_load_dword v120, %[fo], %[rc], %[fv] offen sc1\n\t" "v_cvt_pk_bf16_f32 v112, v96, v97\n\t" "v_mfma_f32_16x16x32_bf16 v[84:87], v[68:71], a[72:75], v[84:87]\n\t" \
    "v_cvt_pk_bf16_f32 v113, v98, v99\n\t" "v_mfma_f32_16x16x32_bf16 v[80:83], v[72:75], a[76:79], v[80:83]\n\t" "buffer_load_dword v121, %[fo], %[rc], %[fs] offen sc1\n\t" \
    "v_mfma_f32_16x16x32_bf16 v[84:87], v[76:79], a[76:79], v[84:87]\n\t" "v_cvt_pk_bf16_f32 v114, v100, v101\n\t" "v_cvt_pk_bf16_f32 v115, v102, v103\n\t" \
    "v_mfma_f32_16x16x32_bf16 v[88:91], v[48:51], a[80:83], 0\n\t" "v_cvt_pk_bf16_f32 v116, v104, v105\n\t" "v_mfma_f32_16x16x32_bf16 v[92:95], v[52:55], a[80:83], 0\n\t" \
    "v_cvt_pk_bf16_f32 v117, v106, v107\n\t" "v_mfma_f32_16x16x32_bf16 v[88:91], v[56:59], a[84:87], v[88:91]\n\t" \
    "buffer_store_dwordx4 v[112:115], %[z1], %[rs], s63 offen sc1\n\t" "v_mfma_f32_16x16x32_bf16 v[92:95], v[60:63], a[84:87], v[92:95]\n\t" \
    "v_mfma_f32_16x16x32_bf16 v[88:91], v[64:67], a[88:91], v[88:91]\n\t" "v_cvt_pk_bf16_f32 v118, v108, v109\n\t" \
    "v_mfma_f32_16x16x32_bf16 v[92:95], v[68:71], a[88:91], v[92:95]\n\t" "v_cvt_pk_bf16_f32 v119, v110, v111\n\t" \
    "v_mfma_f32_16x16x32_bf16 v[88:91], v[72:75], a[92:95], v[88:91]\n\t" "buffer_store_dwordx4 v[116:119], %[z2], %[rs], s63 offen sc1\n\t" \
    "v_mfma_f32_16x16x32_bf16 v[92:95], v[76:79], a[92:95], v[92:95]\n\t" "s_add_u32 s63, s63, 0x20000\n\t" "v_mfma_f32_16x16x32_bf16 v[96:99], v[48:51], a[96:99], 0\n\t" \
    "v_mfma_f32_16x16x32_bf16 v[100:103], v[52:55], a[96:99], 0\n\t" "v_mfma_f32_16x16x32_bf16 v[96:99], v[56:59], a[100:103], v[96:99]\n\t" \
    "v_mfma_f32_16x16x32_bf16 v[100:103], v[60:63], a[100:103], v[100:103]\n\t" "v_mfma_f32_16x16x32_bf16 v[96:99], v[64:67], a[104:107], v[96:99]\n\t" \
    "v_cvt_pk_bf16_f32 v112, v80, v81\n\t" "v_mfma_f32_16x16x32_bf16 v[100:103], v[68:71], a[104:107], v[100:103]\n\t" "v_cvt_pk_bf16_f32 v113, v82, v83\n\t" \
    "v_mfma_f32_16x16x32_bf16 v[96:99], v[72:75], a[108:111], v[96:99]\n\t" "v_cvt_pk_bf16_f32 v114, v84, v85\n\t" \
    "v_mfma_f32_16x16x32_bf16 v[100:103], v[76:79], a[108:111], v[100:103]\n\t" "v_cvt_pk_bf16_f32 v115, v86, v87\n\t" \
    "v_mfma_f32_16x16x32_bf16 v[104:107], v[48:51], a[112:115], 0\n\t" "v_cvt_pk_bf16_f32 v116, v88, v89\n\t" \
    "v_mfma_f32_16x16x32_bf16 v[108:111], v[52:55], a[112:115], 0\n\t" "v_cvt_pk_bf16_f32 v117, v90, v91\n\t" \
    "v_mfma_f32_16x16x32_bf16 v[104:107], v[56:59], a[116:119], v[104:107]\n\t" "buffer_store_dwordx4 v[112:115], %[z1], %[rs], s63 offen sc1\n\t" \
    "v_mfma_f32_16x16x32_bf16 v[108:111], v[60:63], a[116:119], v[108:111]\n\t" "v_mfma_f32_16x16x32_bf16 v[104:107], v[64:67], a[120:123], v[104:107]\n\t" \
    "v_cvt_pk_bf16_f32 v118, v92, v93\n\t" "v_mfma_f32_16x16x32_bf16 v[108:111], v[68:71], a[120:123], v[108:111]\n\t" "v_cvt_pk_bf16_f32 v119, v94, v95\n\t" \
    "v_mfma_f32_16x16x32_bf16 v[104:107], v[72:75], a[124:127], v[104:107]\n\t" "buffer_store_dwordx4 v[116:119], %[z2], %[rs], s63 offen sc1\n\t" \
    "v_mfma_f32_16x16x32_bf16 v[108:111], v[76:79], a[124:127], v[108:111]\n\t" "s_add_u32 s63, s63, 0x20000\n\t" \
    "v_mfma_f32_16x16x32_bf16 v[80:83], v[48:51], a[128:131], 0\n\t" "v_mfma_f32_16x16x32_bf16 v[84:87], v[52:55], a[128:131], 0\n\t" \
    "v_mfma_f32_16x16x32_bf16 v[80:83], v[56:59], a[132:135], v[80:83]\n\t" "v_mfma_f32_16x16x32_bf16 v[84:87], v[60:63], a[132:135], v[84:87]\n\t" \
    "v_mfma_f32_16x16x32_bf16 v[80:83], v[64:67], a[136:139], v[80:83]\n\t" "v_cvt_pk_bf16_f32 v112, v96, v97\n\t" \
    "v_mfma_f32_16x16x32_bf16 v[84:87], v[68:71], a[136:139], v[84:87]\n\t" "v_cvt_pk_bf16_f32 v113, v98, v99\n\t" \
    "v_mfma_f32_16x16x32_bf16 v[80:83], v[72:75], a[140:143], v[80:83]\n\t" "v_cvt_pk_bf16_f32 v114, v100, v101\n\t" \
    "v_mfma_f32_16x16x32_bf16 v[84:87], v[76:79], a[140:143], v[84:87]\n\t" "v_cvt_pk_bf16_f32 v115, v102, v103\n\t" \
    "v_mfma_f32_16x16x32_bf16 v[88:91], v[48:51], a[144:147], 0\n\t" "v_cvt_pk_bf16_f32 v116, v104, v105\n\t" "v_mfma_f32_16x16x32_bf16 v[92:95], v[52:55], a[144:147], 0\n\t" \
    "v_cvt_pk_bf16_f32 v117, v106, v107\n\t" "v_mfma_f32_16x16x32_bf16 v[88:91], v[56:59], a[148:151], v[88:91]\n\t" \
    "buffer_store_dwordx4 v[112:115], %[z1], %[rs], s63 offen sc1\n\t" "v_mfma_f32_16x16x32_bf16 v[92:95], v[60:63], a[148:151], v[92:95]\n\t" \
    "v_mfma_f32_16x16x32_bf16 v[88:91], v[64:67], a[152:155], v[88:91]\n\t" "v_cvt_pk_bf16_f32 v118, v108, v109\n\t" \
    "v_mfma_f32_16x16x32_bf16 v[92:95], v[68:71], a[152:155], v[92:95]\n\t" "v_cvt_pk_bf16_f32 v119, v110, v111\n\t" \
    "v_mfma_f32_16x16x32_bf16 v[88:91], v[72:75], a[156:159], v[88:91]\n\t" "buffer_store_dwordx4 v[116:119], %[z2], %[rs], s63 offen sc1\n\t" \
    "v_mfma_f32_16x16x32_bf16 v[92:95], v[76:79], a[156:159], v[92:95]\n\t" "s_add_u32 s63, s63, 0x20000\n\t" "v_mfma_f32_16x16x32_bf16 v[96:99], v[48:51], a[160:163], 0\n\t" \
    "v_mfma_f32_16x16x32_bf16 v[100:103], v[52:55], a[160:163], 0\n\t" "v_mfma_f32_16x16x32_bf16 v[96:99], v[56:59], a[164:167], v[96:99]\n\t" \
    "v_mfma_f32_16x16x32_bf16 v[100:103], v[60:63], a[164:167], v[100:103]\n\t" "v_mfma_f32_16x16x32_bf16 v[96:99], v[64:67], a[168:171], v[96:99]\n\t" \
    "v_cvt_pk_bf16_f32 v112, v80, v81\n\t" "v_mfma_f32_16x16x32_bf16 v[100:103], v[68:71], a[168:171], v[100:103]\n\t" "v_cvt_pk_bf16_f32 v113, v82, v83\n\t" \
    "v_mfma_f32_16x16x32_bf16 v[96:99], v[72:75], a[172:175], v[96:99]\n\t" "v_cvt_pk_bf16_f32 v114, v84, v85\n\t" \
    "v_mfma_f32_16x16x32_bf16 v[100:103], v[76:79], a[172:175], v[100:103]\n\t" "v_cvt_pk_bf16_f32 v115, v86, v87\n\t" \
    "v_mfma_f32_16x16x32_bf16 v[104:107], v[48:51], a[176:179], 0\n\t" "v_cvt_pk_bf16_f32 v116, v88, v89\n\t" \
    "v_mfma_f32_16x16x32_bf16 v[108:111], v[52:55], a[176:179], 0\n\t" "v_cvt_pk_bf16_f32 v117, v90, v91\n\t" \
    "v_mfma_f32_16x16x32_bf16 v[104:107], v[56:59], a[180:183], v[104:107]\n\t" "buffer_store_dwordx4 v[112:115], %[z1], %[rs], s63 offen sc1\n\t" \
    "v_mfma_f32_16x16x32_bf16 v[108:111], v[60:63], a[180:183], v[108:111]\n\t" "v_mfma_f32_16x16x32_bf16 v[104:107], v[64:67], a[184:187], v[104:107]\n\t" \
    "v_cvt_pk_bf16_f32 v118, v92, v93\n\t" "v_mfma_f32_16x16x32_bf16 v[108:111], v[68:71], a[184:187], v[108:111]\n\t" "v_cvt_pk_bf16_f32 v119, v94, v95\n\t" \
    "v_mfma_f32_16x16x32_bf16 v[104:107], v[72:75], a[188:191], v[104:107]\n\t" "buffer_store_dwordx4 v[116:119], %[z2], %[rs], s63 offen sc1\n\t" \
    "v_mfma_f32_16x16x32_bf16 v[108:111], v[76:79], a[188:191], v[108:111]\n\t" "s_add_u32 s63, s63, 0x20000\n\t" \
    "v_mfma_f32_16x16x32_bf16 v[80:83], v[48:51], a[192:195], 0\n\t" "v_mfma_f32_16x16x32_bf16 v[84:87], v[52:55], a[192:195], 0\n\t" \
    "v_mfma_f32_16x16x32_bf16 v[80:83], v[56:59], a[196:199], v[80:83]\n\t" "v_mfma_f32_16x16x32_bf16 v[84:87], v[60:63], a[196:199], v[84:87]\n\t" \
    "v_mfma_f32_16x16x32_bf16 v[80:83], v[64:67], a[200:203], v[80:83]\n\t" "v_cvt_pk_bf16_f32 v112, v96, v97\n\t" \
    "v_mfma_f32_16x16x32_bf16 v[84:87], v[68:71], a[200:203], v[84:87]\n\t" "v_cvt_pk_bf16_f32 v113, v98, v99\n\t" \
    "v_mfma_f32_16x16x32_bf16 v[80:83], v[72:75], a[204:207], v[80:83]\n\t" "v_cvt_pk_bf16_f32 v114, v100, v101\n\t" \
    "v_mfma_f32_16x16x32_bf16 v[84:87], v[76:79], a[204:207], v[84:87]\n\t" "v_cvt_pk_bf16_f32 v115, v102, v103\n\t" \
    "v_mfma_f32_16x16x32_bf16 v[88:91], v[48:51], a[208:211], 0\n\t" "v_cvt_pk_bf16_f32 v116, v104, v105\n\t" "v_mfma_f32_16x16x32_bf16 v[92:95], v[52:55], a[208:211], 0\n\t" \
    "v_cvt_pk_bf16_f32 v117, v106, v107\n\t" "v_mfma_f32_16x16x32_bf16 v[88:91], v[56:59], a[212:215], v[88:91]\n\t" \
    "buffer_store_dwordx4 v[112:115], %[z1], %[rs], s63 offen sc1\n\t" "v_mfma_f32_16x16x32_bf16 v[92:95], v[60:63], a[212:215], v[92:95]\n\t" \
    "v_mfma_f32_16x16x32_bf16 v[88:91], v[64:67], a[216:219], v[88:91]\n\t" "v_cvt_pk_bf16_f32 v118, v108, v109\n\t" \
    "v_mfma_f32_16x16x32_bf16 v[92:95], v[68:71], a[216:219], v[92:95]\n\t" "v_cvt_pk_bf16_f32 v119, v110, v111\n\t" \
    "v_mfma_f32_16x16x32_bf16 v[88:91], v[72:75], a[220:223], v[88:91]\n\t" "buffer_store_dwordx4 v[116:119], %[z2], %[rs], s63 offen sc1\n\t" \
    "v_mfma_f32_16x16x32_bf16 v[92:95], v[76:79], a[220:223], v[92:95]\n\t" "s_add_u32 s63, s63, 0x20000\n\t" "v_mfma_f32_16x16x32_bf16 v[96:99], v[48:51], a[224:227], 0\n\t" \
    "v_mfma_f32_16x16x32_bf16 v[100:103], v[52:55], a[224:227], 0\n\t" "v_mfma_f32_16x16x32_bf16 v[96:99], v[56:59], a[228:231], v[96:99]\n\t" \
    "v_mfma_f32_16x16x32_bf16 v[100:103], v[60:63], a[228:231], v[100:103]\n\t" "v_mfma_f32_16x16x32_bf16 v[96:99], v[64:67], a[232:235], v[96:99]\n\t" \
    "v_cvt_pk_bf16_f32 v112, v80, v81\n\t" "v_mfma_f32_16x16x32_bf16 v[100:103], v[68:71], a[232:235], v[100:103]\n\t" "v_cvt_pk_bf16_f32 v113, v82, v83\n\t" \
    "v_mfma_f32_16x16x32_bf16 v[96:99], v[72:75], a[236:239], v[96:99]\n\t" "v_cvt_pk_bf16_f32 v114, v84, v85\n\t" \
    "v_mfma_f32_16x16x32_bf16 v[100:103], v[76:79], a[236:239], v[100:103]\n\t" "v_cvt_pk_bf16_f32 v115, v86, v87\n\t" \
    "v_mfma_f32_16x16x32_bf16 v[104:107], v[48:51], a[240:243], 0\n\t" "v_cvt_pk_bf16_f32 v116, v88, v89\n\t" \
    "v_mfma_f32_16x16x32_bf16 v[108:111], v[52:55], a[240:243], 0\n\t" "v_cvt_pk_bf16_f32 v117, v90, v91\n\t" \
    "v_mfma_f32_16x16x32_bf16 v[104:107], v[56:59], a[244:247], v[104:107]\n\t" "buffer_store_dwordx4 v[112:115], %[z1], %[rs], s63 offen sc1\n\t" \
    "v_mfma_f32_16x16x32_bf16 v[108:111], v[60:63], a[244:247], v[108:111]\n\t" "v_mfma_f32_16x16x32_bf16 v[104:107], v[64:67], a[248:251], v[104:107]\n\t" \
    "v_cvt_pk_bf16_f32 v118, v92, v93\n\t" "v_cvt_pk_bf16_f32 v119, v94, v95\n\t" "s_waitcnt vmcnt(11)\n\t" "v_readfirstlane_b32 s66, v120\n\t" \
    "v_mfma_f32_16x16x32_bf16 v[108:111], v[68:71], a[248:251], v[108:111]\n\t" "v_readfirstlane_b32 s67, v121\n\t" "v_mov_b32 v122, s60\n\t" \
    "buffer_store_dword v122, %[fo], %[rc], %[fz] offen sc1\n\t" "v_mfma_f32_16x16x32_bf16 v[104:107], v[72:75], a[252:255], v[104:107]\n\t" \
    "buffer_store_dwordx4 v[116:119], %[z2], %[rs], s63 offen sc1\n\t" "s_add_u32 s63, s63, 0x20000\n\t" "s_max_u32 s61, s61, s66\n\t" \
    "v_mfma_f32_16x16x32_bf16 v[108:111], v[76:79], a[252:255], v[108:111]\n\t" "s_max_u32 s62, s62, s67\n\t" "s_add_u32 s60, s60, 1\n\t" ZSUM "33:\n\t" "s_branch 10b\n\t" \
    "40:\n\t" "v_mfma_f32_16x16x32_bf16 v[80:83], v[16:19], a[0:3], 0\n\t" "v_mfma_f32_16x16x32_bf16 v[84:87], v[20:23], a[0:3], 0\n\t" \
    "v_mfma_f32_16x16x32_bf16 v[80:83], v[24:27], a[4:7], v[80:83]\n\t" "v_mfma_f32_16x16x32_bf16 v[84:87], v[28:31], a[4:7], v[84:87]\n\t" \
    "v_mfma_f32_16x16x32_bf16 v[80:83], v[32:35], a[8:11], v[80:83]\n\t" "v_cvt_pk_bf16_f32 v112, v96, v97\n\t" \
    "v_mfma_f32_16x16x32_bf16 v[84:87], v[36:39], a[8:11], v[84:87]\n\t" "v_cvt_pk_bf16_f32 v113, v98, v99\n\t" \
    "v_mfma_f32_16x16x32_bf16 v[80:83], v[40:43], a[12:15], v[80:83]\n\t" "v_cvt_pk_bf16_f32 v114, v100, v101\n\t" \
    "v_mfma_f32_16x16x32_bf16 v[84:87], v[44:47], a[12:15], v[84:87]\n\t" "v_cvt_pk_bf16_f32 v115, v102, v103\n\t" \
    "v_mfma_f32_16x16x32_bf16 v[88:91], v[16:19], a[16:19], 0\n\t" "v_cvt_pk_bf16_f32 v116, v104, v105\n\t" "v_mfma_f32_16x16x32_bf16 v[92:95], v[20:23], a[16:19], 0\n\t" \
    "v_cvt_pk_bf16_f32 v117, v106, v107\n\t" "v_mfma_f32_16x16x32_bf16 v[88:91], v[24:27], a[20:23], v[88:91]\n\t" \
    "buffer_store_dwordx4 v[112:115], %[z1], %[rs], s63 offen sc1\n\t" "v_mfma_f32_16x16x32_bf16 v[92:95], v[28:31], a[20:23], v[92:95]\n\t" \
    "v_mfma_f32_16x16x32_bf16 v[88:91], v[32:35], a[24:27], v[88:91]\n\t" "v_cvt_pk_bf16_f32 v118, v108, v109\n\t" \
    "v_mfma_f32_16x16x32_bf16 v[92:95], v[36:39], a[24:27], v[92:95]\n\t" "v_cvt_pk_bf16_f32 v119, v110, v111\n\t" \
    "v_mfma_f32_16x16x32_bf16 v[88:91], v[40:43], a[28:31], v[88:91]\n\t" "buffer_store_dwordx4 v[116:119], %[z2], %[rs], s63 offen sc1\n\t" "s_and_b32 s63, s60, %[rm]\n\t" \
    "v_mfma_f32_16x16x32_bf16 v[92:95], v[44:47], a[28:31], v[92:95]\n\t" "s_lshl_b32 s63, s63, 22\n\t" "s_add_u32 s63, s63, %[zb]\n\t" \
    "v_mfma_f32_16x16x32_bf16 v[96:99], v[16:19], a[32:35], 0\n\t" "buffer_load_dword v120, %[fo], %[rc], %[fv] offen sc1\n\t" \
    "v_mfma_f32_16x16x32_bf16 v[100:103], v[20:23], a[32:35], 0\n\t" "v_mfma_f32_16x16x32_bf16 v[96:99], v[24:27], a[36:39], v[96:99]\n\t" \
    "buffer_load_dword v121, %[fo], %[rc], %[fs] offen sc1\n\t" "v_mfma_f32_16x16x32_bf16 v[100:103], v[28:31], a[36:39], v[100:103]\n\t" \
    "v_mfma_f32_16x16x32_bf16 v[96:99], v[32:35], a[40:43], v[96:99]\n\t" "v_cvt_pk_bf16_f32 v112, v80, v81\n\t" \
    "v_mfma_f32_16x16x32_bf16 v[100:103], v[36:39], a[40:43], v[100:103]\n\t" "v_cvt_pk_bf16_f32 v113, v82, v83\n\t" \
    "v_mfma_f32_16x16x32_bf16 v[96:99], v[40:43], a[44:47], v[96:99]\n\t" "v_cvt_pk_bf16_f32 v114, v84, v85\n\t" \
    "v_mfma_f32_16x16x32_bf16 v[100:103], v[44:47], a[44:47], v[100:103]\n\t" "v_cvt_pk_bf16_f32 v115, v86, v87\n\t" \
    "v_mfma_f32_16x16x32_bf16 v[104:107], v[16:19], a[48:51], 0\n\t" "v_cvt_pk_bf16_f32 v116, v88, v89\n\t" "v_mfma_f32_16x16x32_bf16 v[108:111], v[20:23], a[48:51], 0\n\t" \
    "v_cvt_pk_bf16_f32 v117, v90, v91\n\t" "v_mfma_f32_16x16x32_bf16 v[104:107], v[24:27], a[52:55], v[104:107]\n\t" \
    "buffer_store_dwordx4 v[112:115], %[z1], %[rs], s63 offen sc1\n\t" "v_mfma_f32_16x16x32_bf16 v[108:111], v[28:31], a[52:55], v[108:111]\n\t" \
    "v_mfma_f32_16x16x32_bf16 v[104:107], v[32:35], a[56:59], v[104:107]\n\t" "v_cvt_pk_bf16_f32 v118, v92, v93\n\t" \
    "v_mfma_f32_16x16x32_bf16 v[108:111], v[36:39], a[56:59], v[108:111]\n\t" "v_cvt_pk_bf16_f32 v119, v94, v95\n\t" \
    "v_mfma_f32_16x16x32_bf16 v[104:107], v[40:43], a[60:63], v[104:107]\n\t" "buffer_store_dwordx4 v[116:119], %[z2], %[rs], s63 offen sc1\n\t" \
    "v_mfma_f32_16x16x32_bf16 v[108:111], v[44:47], a[60:63], v[108:111]\n\t" "s_add_u32 s63, s63, 0x20000\n\t" "v_mfma_f32_16x16x32_bf16 v[80:83], v[16:19], a[64:67], 0\n\t" \
    "s_add_u32 s65, s60, 1\n\t" "s_lshr_b32 s66, s65, 2\n\t" "v_mfma_f32_16x16x32_bf16 v[84:87], v[20:23], a[64:67], 0\n\t" "s_xor_b32 s66, s66, %[xp]\n\t" \
    "s_and_b32 s66, s66, 1\n\t" "v_mfma_f32_16x16x32_bf16 v[80:83], v[24:27], a[68:71], v[80:83]\n\t" "s_lshl_b32 s66, s66, 13\n\t" "s_lshl_b32 s64, s65, 19\n\t" \
    "v_mfma_f32_16x16x32_bf16 v[84:87], v[28:31], a[68:71], v[84:87]\n\t" "s_add_u32 s64, s64, s66\n\t" "s_add_u32 s64, s64, %[vb]\n\t" \
    "v_mfma_f32_16x16x32_bf16 v[80:83], v[32:35], a[72:75], v[80:83]\n\t" "v_cvt_pk_bf16_f32 v112, v96, v97\n\t" \
    "v_mfma_f32_16x16x32_bf16 v[84:87], v[36:39], a[72:75], v[84:87]\n\t" "v_cvt_pk_bf16_f32 v113, v98, v99\n\t" \
    "v_mfma_f32_16x16x32_bf16 v[80:83], v[40:43], a[76:79], v[80:83]\n\t" "v_cvt_pk_bf16_f32 v114, v100, v101\n\t" \
    "v_mfma_f32_16x16x32_bf16 v[84:87], v[44:47], a[76:79], v[84:87]\n\t" "v_cvt_pk_bf16_f32 v115, v102, v103\n\t" \
    "v_mfma_f32_16x16x32_bf16 v[88:91], v[16:19], a[80:83], 0\n\t" "v_cvt_pk_bf16_f32 v116, v104, v105\n\t" "v_mfma_f32_16x16x32_bf16 v[92:95], v[20:23], a[80:83], 0\n\t" \
    "v_cvt_pk_bf16_f32 v117, v106, v107\n\t" "v_mfma_f32_16x16x32_bf16 v[88:91], v[24:27], a[84:87], v[88:91]\n\t" \
    "buffer_store_dwordx4 v[112:115], %[z1], %[rs], s63 offen sc1\n\t" "v_mfma_f32_16x16x32_bf16 v[92:95], v[28:31], a[84:87], v[92:95]\n\t" \
    "v_mfma_f32_16x16x32_bf16 v[88:91], v[32:35], a[88:91], v[88:91]\n\t" "v_cvt_pk_bf16_f32 v118, v108, v109\n\t" \
    "v_mfma_f32_16x16x32_bf16 v[92:95], v[36:39], a[88:91], v[92:95]\n\t" "v_cvt_pk_bf16_f32 v119, v110, v111\n\t" \
    "v_mfma_f32_16x16x32_bf16 v[88:91], v[40:43], a[92:95], v[88:91]\n\t" "buffer_store_dwordx4 v[116:119], %[z2], %[rs], s63 offen sc1\n\t" \
    "v_mfma_f32_16x16x32_bf16 v[92:95], v[44:47], a[92:95], v[92:95]\n\t" "s_add_u32 s63, s63, 0x20000\n\t" "v_mfma_f32_16x16x32_bf16 v[96:99], v[16:19], a[96:99], 0\n\t" \
    "v_mfma_f32_16x16x32_bf16 v[100:103], v[20:23], a[96:99], 0\n\t" "v_mfma_f32_16x16x32_bf16 v[96:99], v[24:27], a[100:103], v[96:99]\n\t" \
    "v_mfma_f32_16x16x32_bf16 v[100:103], v[28:31], a[100:103], v[100:103]\n\t" "v_mfma_f32_16x16x32_bf16 v[96:99], v[32:35], a[104:107], v[96:99]\n\t" \
    "v_cvt_pk_bf16_f32 v112, v80, v81\n\t" "v_mfma_f32_16x16x32_bf16 v[100:103], v[36:39], a[104:107], v[100:103]\n\t" "v_cvt_pk_bf16_f32 v113, v82, v83\n\t" \
    "v_mfma_f32_16x16x32_bf16 v[96:99], v[40:43], a[108:111], v[96:99]\n\t" "v_cvt_pk_bf16_f32 v114, v84, v85\n\t" \
    "v_mfma_f32_16x16x32_bf16 v[100:103], v[44:47], a[108:111], v[100:103]\n\t" "v_cvt_pk_bf16_f32 v115, v86, v87\n\t" \
    "v_mfma_f32_16x16x32_bf16 v[104:107], v[16:19], a[112:115], 0\n\t" "v_cvt_pk_bf16_f32 v116, v88, v89\n\t" \
    "v_mfma_f32_16x16x32_bf16 v[108:111], v[20:23], a[112:115], 0\n\t" "v_cvt_pk_bf16_f32 v117, v90, v91\n\t" \
    "v_mfma_f32_16x16x32_bf16 v[104:107], v[24:27], a[116:119], v[104:107]\n\t" "buffer_store_dwordx4 v[112:115], %[z1], %[rs], s63 offen sc1\n\t" \
    "v_mfma_f32_16x16x32_bf16 v[108:111], v[28:31], a[116:119], v[108:111]\n\t" "v_mfma_f32_16x16x32_bf16 v[104:107], v[32:35], a[120:123], v[104:107]\n\t" \
    "v_cvt_pk_bf16_f32 v118, v92, v93\n\t" "v_mfma_f32_16x16x32_bf16 v[108:111], v[36:39], a[120:123], v[108:111]\n\t" "v_cvt_pk_bf16_f32 v119, v94, v95\n\t" \
    "v_mfma_f32_16x16x32_bf16 v[104:107], v[40:43], a[124:127], v[104:107]\n\t" "buffer_store_dwordx4 v[116:119], %[z2], %[rs], s63 offen sc1\n\t" \
    "v_mfma_f32_16x16x32_bf16 v[108:111], v[44:47], a[124:127], v[108:111]\n\t" "s_add_u32 s63, s63, 0x20000\n\t" \
    "v_mfma_f32_16x16x32_bf16 v[80:83], v[16:19], a[128:131], 0\n\t" "s_waitcnt vmcnt(6)\n\t" "v_readfirstlane_b32 s66, v120\n\t" "v_readfirstlane_b32 s67, v121\n\t" \
    "v_mfma_f32_16x16x32_bf16 v[84:87], v[20:23], a[128:131], 0\n\t" "v_mov_b32 v122, s60\n\t" "buffer_store_dword v122, %[fo], %[rc], %[fz] offen sc1\n\t" \
    "v_mfma_f32_16x16x32_bf16 v[80:83], v[24:27], a[132:135], v[80:83]\n\t" "s_max_u32 s61, s61, s66\n\t" "s_max_u32 s62, s62, s67\n\t" "s_cmp_gt_u32 s61, s65\n\t" \
    "v_mfma_f32_16x16x32_bf16 v[84:87], v[28:31], a[132:135], v[84:87]\n\t" "s_cselect_b32 s65, 1, 0\n\t" "s_cselect_b32 s64, s64, 0x7f000000\n\t" \
    "v_mfma_f32_16x16x32_bf16 v[80:83], v[32:35], a[136:139], v[80:83]\n\t" "buffer_load_dwordx4 v[48:51], %[ao], %[rs], s64 offen offset:0 sc1\n\t" \
    "v_cvt_pk_bf16_f32 v112, v96, v97\n\t" "v_mfma_f32_16x16x32_bf16 v[84:87], v[36:39], a[136:139], v[84:87]\n\t" "v_cvt_pk_bf16_f32 v113, v98, v99\n\t" \
    "v_mfma_f32_16x16x32_bf16 v[80:83], v[40:43], a[140:143], v[80:83]\n\t" "buffer_load_dwordx4 v[52:55], %[ao], %[rs], s64 offen offset:256 sc1\n\t" \
    "v_cvt_pk_bf16_f32 v114, v100, v101\n\t" "v_mfma_f32_16x16x32_bf16 v[84:87], v[44:47], a[140:143], v[84:87]\n\t" "v_cvt_pk_bf16_f32 v115, v102, v103\n\t" \
    "v_mfma_f32_16x16x32_bf16 v[88:91], v[16:19], a[144:147], 0\n\t" "buffer_load_dwordx4 v[56:59], %[ao], %[rs], s64 offen offset:2048 sc1\n\t" \
    "v_cvt_pk_bf16_f32 v116, v104, v105\n\t" "v_mfma_f32_16x16x32_bf16 v[92:95], v[20:23], a[144:147], 0\n\t" "v_cvt_pk_bf16_f32 v117, v106, v107\n\t" \
    "v_mfma_f32_16x16x32_bf16 v[88:91], v[24:27], a[148:151], v[88:91]\n\t" "buffer_load_dwordx4 v[60:63], %[ao], %[rs], s64 offen offset:2304 sc1\n\t" \
    "v_mfma_f32_16x16x32_bf16 v[92:95], v[28:31], a[148:151], v[92:95]\n\t" "buffer_store_dwordx4 v[112:115], %[z1], %[rs], s63 offen sc1\n\t" \
    "v_mfma_f32_16x16x32_bf16 v[88:91], v[32:35], a[152:155], v[88:91]\n\t" "buffer_load_dwordx4 v[64:67], %[a1], %[rs], s64 offen offset:0 sc1\n\t" \
    "v_cvt_pk_bf16_f32 v118, v108, v109\n\t" "v_mfma_f32_16x16x32_bf16 v[92:95], v[36:39], a[152:155], v[92:95]\n\t" "v_cvt_pk_bf16_f32 v119, v110, v111\n\t" \
    "v_mfma_f32_16x16x32_bf16 v[88:91], v[40:43], a[156:159], v[88:91]\n\t" "buffer_load_dwordx4 v[68:71], %[a1], %[rs], s64 offen offset:256 sc1\n\t" \
    "buffer_store_dwordx4 v[116:119], %[z2], %[rs], s63 offen sc1\n\t" "v_mfma_f32_16x16x32_bf16 v[92:95], v[44:47], a[156:159], v[92:95]\n\t" \
    "s_add_u32 s63, s63, 0x20000\n\t" "v_mfma_f32_16x16x32_bf16 v[96:99], v[16:19], a[160:163], 0\n\t" \
    "buffer_load_dwordx4 v[72:75], %[a1], %[rs], s64 offen offset:2048 sc1\n\t" "v_mfma_f32_16x16x32_bf16 v[100:103], v[20:23], a[160:163], 0\n\t" \
    "v_mfma_f32_16x16x32_bf16 v[96:99], v[24:27], a[164:167], v[96:99]\n\t" "buffer_load_dwordx4 v[76:79], %[a1], %[rs], s64 offen offset:2304 sc1\n\t" \
    "v_mfma_f32_16x16x32_bf16 v[100:103], v[28:31], a[164:167], v[100:103]\n\t" "v_mfma_f32_16x16x32_bf16 v[96:99], v[32:35], a[168:171], v[96:99]\n\t" \
    "v_cvt_pk_bf16_f32 v112, v80, v81\n\t" "v_mfma_f32_16x16x32_bf16 v[100:103], v[36:39], a[168:171], v[100:103]\n\t" "v_cvt_pk_bf16_f32 v113, v82, v83\n\t" \
    "v_mfma_f32_16x16x32_bf16 v[96:99], v[40:43], a[172:175], v[96:99]\n\t" "v_cvt_pk_bf16_f32 v114, v84, v85\n\t" \
    "v_mfma_f32_16x16x32_bf16 v[100:103], v[44:47], a[172:175], v[100:103]\n\t" "v_cvt_pk_bf16_f32 v115, v86, v87\n\t" \
    "v_mfma_f32_16x16x32_bf16 v[104:107], v[16:19], a[176:179], 0\n\t" "v_cvt_pk_bf16_f32 v116, v88, v89\n\t" \
    "v_mfma_f32_16x16x32_bf16 v[108:111], v[20:23], a[176:179], 0\n\t" "v_cvt_pk_bf16_f32 v117, v90, v91\n\t" \
    "v_mfma_f32_16x16x32_bf16 v[104:107], v[24:27], a[180:183], v[104:107]\n\t" "buffer_store_dwordx4 v[112:115], %[z1], %[rs], s63 offen sc1\n\t" \
    "v_mfma_f32_16x16x32_bf16 v[108:111], v[28:31], a[180:183], v[108:111]\n\t" "v_mfma_f32_16x16x32_bf16 v[104:107], v[32:35], a[184:187], v[104:107]\n\t" \
    "v_cvt_pk_bf16_f32 v118, v92, v93\n\t" "v_mfma_f32_16x16x32_bf16 v[108:111], v[36:39], a[184:187], v[108:111]\n\t" "v_cvt_pk_bf16_f32 v119, v94, v95\n\t" \
    "v_mfma_f32_16x16x32_bf16 v[104:107], v[40:43], a[188:191], v[104:107]\n\t" "buffer_store_dwordx4 v[116:119], %[z2], %[rs], s63 offen sc1\n\t" \
    "v_mfma_f32_16x16x32_bf16 v[108:111], v[44:47], a[188:191], v[108:111]\n\t" "s_add_u32 s63, s63, 0x20000\n\t" \
    "v_mfma_f32_16x16x32_bf16 v[80:83], v[16:19], a[192:195], 0\n\t" "v_mfma_f32_16x16x32_bf16 v[84:87], v[20:23], a[192:195], 0\n\t" \
    "v_mfma_f32_16x16x32_bf16 v[80:83], v[24:27], a[196:199], v[80:83]\n\t" "v_mfma_f32_16x16x32_bf16 v[84:87], v[28:31], a[196:199], v[84:87]\n\t" \
    "v_mfma_f32_16x16x32_bf16 v[80:83], v[32:35], a[200:203], v[80:83]\n\t" "v_cvt_pk_bf16_f32 v112, v96, v97\n\t" \
    "v_mfma_f32_16x16x32_bf16 v[84:87], v[36:39], a[200:203], v[84:87]\n\t" "v_cvt_pk_bf16_f32 v113, v98, v99\n\t" \
    "v_mfma_f32_16x16x32_bf16 v[80:83], v[40:43], a[204:207], v[80:83]\n\t" "v_cvt_pk_bf16_f32 v114, v100, v101\n\t" \
    "v_mfma_f32_16x16x32_bf16 v[84:87], v[44:47], a[204:207], v[84:87]\n\t" "v_cvt_pk_bf16_f32 v115, v102, v103\n\t" \
    "v_mfma_f32_16x16x32_bf16 v[88:91], v[16:19], a[208:211], 0\n\t" "v_cvt_pk_bf16_f32 v116, v104, v105\n\t" "v_mfma_f32_16x16x32_bf16 v[92:95], v[20:23], a[208:211], 0\n\t" \
    "v_cvt_pk_bf16_f32 v117, v106, v107\n\t" "v_mfma_f32_16x16x32_bf16 v[88:91], v[24:27], a[212:215], v[88:91]\n\t" \
    "buffer_store_dwordx4 v[112:115], %[z1], %[rs], s63 offen sc1\n\t" "v_mfma_f32_16x16x32_bf16 v[92:95], v[28:31], a[212:215], v[92:95]\n\t" \
    "v_mfma_f32_16x16x32_bf16 v[88:91], v[32:35], a[216:219], v[88:91]\n\t" "v_cvt_pk_bf16_f32 v118, v108, v109\n\t" \
    "v_mfma_f32_16x16x32_bf16 v[92:95], v[36:39], a[216:219], v[92:95]\n\t" "v_cvt_pk_bf16_f32 v119, v110, v111\n\t" \
    "v_mfma_f32_16x16x32_bf16 v[88:91], v[40:43], a[220:223], v[88:91]\n\t" "buffer_store_dwordx4 v[116:119], %[z2], %[rs], s63 offen sc1\n\t" \
    "v_mfma_f32_16x16x32_bf16 v[92:95], v[44:47], a[220:223], v[92:95]\n\t" "s_add_u32 s63, s63, 0x20000\n\t" "v_mfma_f32_16x16x32_bf16 v[96:99], v[16:19], a[224:227], 0\n\t" \
    "v_mfma_f32_16x16x32_bf16 v[100:103], v[20:23], a[224:227], 0\n\t" "v_mfma_f32_16x16x32_bf16 v[96:99], v[24:27], a[228:231], v[96:99]\n\t" \
    "v_mfma_f32_16x16x32_bf16 v[100:103], v[28:31], a[228:231], v[100:103]\n\t" "v_mfma_f32_16x16x32_bf16 v[96:99], v[32:35], a[232:235], v[96:99]\n\t" \
    "v_cvt_pk_bf16_f32 v112, v80, v81\n\t" "v_mfma_f32_16x16x32_bf16 v[100:103], v[36:39], a[232:235], v[100:103]\n\t" "v_cvt_pk_bf16_f32 v113, v82, v83\n\t" \
    "v_mfma_f32_16x16x32_bf16 v[96:99], v[40:43], a[236:239], v[96:99]\n\t" "v_cvt_pk_bf16_f32 v114, v84, v85\n\t" \
    "v_mfma_f32_16x16x32_bf16 v[100:103], v[44:47], a[236:239], v[100:103]\n\t" "v_cvt_pk_bf16_f32 v115, v86, v87\n\t" \
    "v_mfma_f32_16x16x32_bf16 v[104:107], v[16:19], a[240:243], 0\n\t" "v_cvt_pk_bf16_f32 v116, v88, v89\n\t" \
    "v_mfma_f32_16x16x32_bf16 v[108:111], v[20:23], a[240:243], 0\n\t" "v_cvt_pk_bf16_f32 v117, v90, v91\n\t" \
    "v_mfma_f32_16x16x32_bf16 v[104:107], v[24:27], a[244:247], v[104:107]\n\t" "buffer_store_dwordx4 v[112:115], %[z1], %[rs], s63 offen sc1\n\t" \
    "v_mfma_f32_16x16x32_bf16 v[108:111], v[28:31], a[244:247], v[108:111]\n\t" "v_mfma_f32_16x16x32_bf16 v[104:107], v[32:35], a[248:251], v[104:107]\n\t" \
    "v_cvt_pk_bf16_f32 v118, v92, v93\n\t" "v_mfma_f32_16x16x32_bf16 v[108:111], v[36:39], a[248:251], v[108:111]\n\t" "v_cvt_pk_bf16_f32 v119, v94, v95\n\t" \
    "v_mfma_f32_16x16x32_bf16 v[104:107], v[40:43], a[252:255], v[104:107]\n\t" "buffer_store_dwordx4 v[116:119], %[z2], %[rs], s63 offen sc1\n\t" \
    "s_add_u32 s63, s63, 0x20000\n\t" "s_add_u32 s60, s60, 1\n\t" "v_mfma_f32_16x16x32_bf16 v[108:111], v[44:47], a[252:255], v[108:111]\n\t" "s_cmp_eq_u32 s65, 0\n\t" \
    "s_waitcnt vmcnt(6)\n\t" ZSUM "s_cbranch_scc0 23b\n\t" "s_branch 20f\n\t" "41:\n\t" "v_mfma_f32_16x16x32_bf16 v[80:83], v[48:51], a[0:3], 0\n\t" \
    "v_mfma_f32_16x16x32_bf16 v[84:87], v[52:55], a[0:3], 0\n\t" "v_mfma_f32_16x16x32_bf16 v[80:83], v[56:59], a[4:7], v[80:83]\n\t" \
    "v_mfma_f32_16x16x32_bf16 v[84:87], v[60:63], a[4:7], v[84:87]\n\t" "v_mfma_f32_16x16x32_bf16 v[80:83], v[64:67], a[8:11], v[80:83]\n\t" \
    "v_cvt_pk_bf16_f32 v112, v96, v97\n\t" "v_mfma_f32_16x16x32_bf16 v[84:87], v[68:71], a[8:11], v[84:87]\n\t" "v_cvt_pk_bf16_f32 v113, v98, v99\n\t" \
    "v_mfma_f32_16x16x32_bf16 v[80:83], v[72:75], a[12:15], v[80:83]\n\t" "v_cvt_pk_bf16_f32 v114, v100, v101\n\t" \
    "v_mfma_f32_16x16x32_bf16 v[84:87], v[76:79], a[12:15], v[84:87]\n\t" "v_cvt_pk_bf16_f32 v115, v102, v103\n\t" \
    "v_mfma_f32_16x16x32_bf16 v[88:91], v[48:51], a[16:19], 0\n\t" "v_cvt_pk_bf16_f32 v116, v104, v105\n\t" "v_mfma_f32_16x16x32_bf16 v[92:95], v[52:55], a[16:19], 0\n\t" \
    "v_cvt_pk_bf16_f32 v117, v106, v107\n\t" "v_mfma_f32_16x16x32_bf16 v[88:91], v[56:59], a[20:23], v[88:91]\n\t" \
    "buffer_store_dwordx4 v[112:115], %[z1], %[rs], s63 offen sc1\n\t" "v_mfma_f32_16x16x32_bf16 v[92:95], v[60:63], a[20:23], v[92:95]\n\t" \
    "v_mfma_f32_16x16x32_bf16 v[88:91], v[64:67], a[24:27], v[88:91]\n\t" "v_cvt_pk_bf16_f32 v118, v108, v109\n\t" \
    "v_mfma_f32_16x16x32_bf16 v[92:95], v[68:71], a[24:27], v[92:95]\n\t" "v_cvt_pk_bf16_f32 v119, v110, v111\n\t" \
    "v_mfma_f32_16x16x32_bf16 v[88:91], v[72:75], a[28:31], v[88:91]\n\t" "buffer_store_dwordx4 v[116:119], %[z2], %[rs], s63 offen sc1\n\t" "s_and_b32 s63, s60, %[rm]\n\t" \
    "v_mfma_f32_16x16x32_bf16 v[92:95], v[76:79], a[28:31], v[92:95]\n\t" "s_lshl_b32 s63, s63, 22\n\t" "s_add_u32 s63, s63, %[zb]\n\t" \
    "v_mfma_f32_16x16x32_bf16 v[96:99], v[48:51], a[32:35], 0\n\t" "buffer_load_dword v120, %[fo], %[rc], %[fv] offen sc1\n\t" \
    "v_mfma_f32_16x16x32_bf16 v[100:103], v[52:55], a[32:35], 0\n\t" "v_mfma_f32_16x16x32_bf16 v[96:99], v[56:59], a[36:39], v[96:99]\n\t" \
    "buffer_load_dword v121, %[fo], %[rc], %[fs] offen sc1\n\t" "v_mfma_f32_16x16x32_bf16 v[100:103], v[60:63], a[36:39], v[100:103]\n\t" \
    "v_mfma_f32_16x16x32_bf16 v[96:99], v[64:67], a[40:43], v[96:99]\n\t" "v_cvt_pk_bf16_f32 v112, v80, v81\n\t" \
    "v_mfma_f32_16x16x32_bf16 v[100:103], v[68:71], a[40:43], v[100:103]\n\t" "v_cvt_pk_bf16_f32 v113, v82, v83\n\t" \
    "v_mfma_f32_16x16x32_bf16 v[96:99], v[72:75], a[44:47], v[96:99]\n\t" "v_cvt_pk_bf16_f32 v114, v84, v85\n\t" \
    "v_mfma_f32_16x16x32_bf16 v[100:103], v[76:79], a[44:47], v[100:103]\n\t" "v_cvt_pk_bf16_f32 v115, v86, v87\n\t" \
    "v_mfma_f32_16x16x32_bf16 v[104:107], v[48:51], a[48:51], 0\n\t" "v_cvt_pk_bf16_f32 v116, v88, v89\n\t" "v_mfma_f32_16x16x32_bf16 v[108:111], v[52:55], a[48:51], 0\n\t" \
    "v_cvt_pk_bf16_f32 v117, v90, v91\n\t" "v_mfma_f32_16x16x32_bf16 v[104:107], v[56:59], a[52:55], v[104:107]\n\t" \
    "buffer_store_dwordx4 v[112:115], %[z1], %[rs], s63 offen sc1\n\t" "v_mfma_f32_16x16x32_bf16 v[108:111], v[60:63], a[52:55], v[108:111]\n\t" \
    "v_mfma_f32_16x16x32_bf16 v[104:107], v[64:67], a[56:59], v[104:107]\n\t" "v_cvt_pk_bf16_f32 v118, v92, v93\n\t" \
    "v_mfma_f32_16x16x32_bf16 v[108:111], v[68:71], a[56:59], v[108:111]\n\t" "v_cvt_pk_bf16_f32 v119, v94, v95\n\t" \
    "v_mfma_f32_16x16x32_bf16 v[104:107], v[72:75], a[60:63], v[104:107]\n\t" "buffer_store_dwordx4 v[116:119], %[z2], %[rs], s63 offen sc1\n\t" \
    "v_mfma_f32_16x16x32_bf16 v[108:111], v[76:79], a[60:63], v[108:111]\n\t" "s_add_u32 s63, s63, 0x20000\n\t" "v_mfma_f32_16x16x32_bf16 v[80:83], v[48:51], a[64:67], 0\n\t" \
    "s_add_u32 s65, s60, 1\n\t" "s_lshr_b32 s66, s65, 2\n\t" "v_mfma_f32_16x16x32_bf16 v[84:87], v[52:55], a[64:67], 0\n\t" "s_xor_b32 s66, s66, %[xp]\n\t" \
    "s_and_b32 s66, s66, 1\n\t" "v_mfma_f32_16x16x32_bf16 v[80:83], v[56:59], a[68:71], v[80:83]\n\t" "s_lshl_b32 s66, s66, 13\n\t" "s_lshl_b32 s64, s65, 19\n\t" \
    "v_mfma_f32_16x16x32_bf16 v[84:87], v[60:63], a[68:71], v[84:87]\n\t" "s_add_u32 s64, s64, s66\n\t" "s_add_u32 s64, s64, %[vb]\n\t" \
    "v_mfma_f32_16x16x32_bf16 v[80:83], v[64:67], a[72:75], v[80:83]\n\t" "v_cvt_pk_bf16_f32 v112, v96, v97\n\t" \
    "v_mfma_f32_16x16x32_bf16 v[84:87], v[68:71], a[72:75], v[84:87]\n\t" "v_cvt_pk_bf16_f32 v113, v98, v99\n\t" \
    "v_mfma_f32_16x16x32_bf16 v[80:83], v[72:75], a[76:79], v[80:83]\n\t" "v_cvt_pk_bf16_f32 v114, v100, v101\n\t" \
    "v_mfma_f32_16x16x32_bf16 v[84:87], v[76:79], a[76:79], v[84:87]\n\t" "v_cvt_pk_bf16_f32 v115, v102, v103\n\t" \
    "v_mfma_f32_16x16x32_bf16 v[88:91], v[48:51], a[80:83], 0\n\t" "v_cvt_pk_bf16_f32 v116, v104, v105\n\t" "v_mfma_f32_16x16x32_bf16 v[92:95], v[52:55], a[80:83], 0\n\t" \
    "v_cvt_pk_bf16_f32 v117, v106, v107\n\t" "v_mfma_f32_16x16x32_bf16 v[88:91], v[56:59], a[84:87], v[88:91]\n\t" \
    "buffer_store_dwordx4 v[112:115], %[z1], %[rs], s63 offen sc1\n\t" "v_mfma_f32_16x16x32_bf16 v[92:95], v[60:63], a[84:87], v[92:95]\n\t" \
    "v_mfma_f32_16x16x32_bf16 v[88:91], v[64:67], a[88:91], v[88:91]\n\t" "v_cvt_pk_bf16_f32 v118, v108, v109\n\t" \
    "v_mfma_f32_16x16x32_bf16 v[92:95], v[68:71], a[88:91], v[92:95]\n\t" "v_cvt_pk_bf16_f32 v119, v110, v111\n\t" \
    "v_mfma_f32_16x16x32_bf16 v[88:91], v[72:75], a[92:95], v[88:91]\n\t" "buffer_store_dwordx4 v[116:119], %[z2], %[rs], s63 offen sc1\n\t" \
    "v_mfma_f32_16x16x32_bf16 v[92:95], v[76:79], a[92:95], v[92:95]\n\t" "s_add_u32 s63, s63, 0x20000\n\t" "v_mfma_f32_16x16x32_bf16 v[96:99], v[48:51], a[96:99], 0\n\t" \
    "v_mfma_f32_16x16x32_bf16 v[100:103], v[52:55], a[96:99], 0\n\t" "v_mfma_f32_16x16x32_bf16 v[96:99], v[56:59], a[100:103], v[96:99]\n\t" \
    "v_mfma_f32_16x16x32_bf16 v[100:103], v[60:63], a[100:103], v[100:103]\n\t" "v_mfma_f32_16x16x32_bf16 v[96:99], v[64:67], a[104:107], v[96:99]\n\t" \
    "v_cvt_pk_bf16_f32 v112, v80, v81\n\t" "v_mfma_f32_16x16x32_bf16 v[100:103], v[68:71], a[104:107], v[100:103]\n\t" "v_cvt_pk_bf16_f32 v113, v82, v83\n\t" \
    "v_mfma_f32_16x16x32_bf16 v[96:99], v[72:75], a[108:111], v[96:99]\n\t" "v_cvt_pk_bf16_f32 v114, v84, v85\n\t" \
    "v_mfma_f32_16x16x32_bf16 v[100:103], v[76:79], a[108:111], v[100:103]\n\t" "v_cvt_pk_bf16_f32 v115, v86, v87\n\t" \
    "v_mfma_f32_16x16x32_bf16 v[104:107], v[48:51], a[112:115], 0\n\t" "v_cvt_pk_bf16_f32 v116, v88, v89\n\t" \
    "v_mfma_f32_16x16x32_bf16 v[108:111], v[52:55], a[112:115], 0\n\t" "v_cvt_pk_bf16_f32 v117, v90, v91\n\t" \
    "v_mfma_f32_16x16x32_bf16 v[104:107], v[56:59], a[116:119], v[104:107]\n\t" "buffer_store_dwordx4 v[112:115], %[z1], %[rs], s63 offen sc1\n\t" \
    "v_mfma_f32_16x16x32_bf16 v[108:111], v[60:63], a[116:119], v[108:111]\n\t" "v_mfma_f32_16x16x32_bf16 v[104:107], v[64:67], a[120:123], v[104:107]\n\t" \
    "v_cvt_pk_bf16_f32 v118, v92, v93\n\t" "v_mfma_f32_16x16x32_bf16 v[108:111], v[68:71], a[120:123], v[108:111]\n\t" "v_cvt_pk_bf16_f32 v119, v94, v95\n\t" \
    "v_mfma_f32_16x16x32_bf16 v[104:107], v[72:75], a[124:127], v[104:107]\n\t" "buffer_store_dwordx4 v[116:119], %[z2], %[rs], s63 offen sc1\n\t" \
    "v_mfma_f32_16x16x32_bf16 v[108:111], v[76:79], a[124:127], v[108:111]\n\t" "s_add_u32 s63, s63, 0x20000\n\t" \
    "v_mfma_f32_16x16x32_bf16 v[80:83], v[48:51], a[128:131], 0\n\t" "s_waitcnt vmcnt(6)\n\t" "v_readfirstlane_b32 s66, v120\n\t" "v_readfirstlane_b32 s67, v121\n\t" \
    "v_mfma_f32_16x16x32_bf16 v[84:87], v[52:55], a[128:131], 0\n\t" "v_mov_b32 v122, s60\n\t" "buffer_store_dword v122, %[fo], %[rc], %[fz] offen sc1\n\t" \
    "v_mfma_f32_16x16x32_bf16 v[80:83], v[56:59], a[132:135], v[80:83]\n\t" "s_max_u32 s61, s61, s66\n\t" "s_max_u32 s62, s62, s67\n\t" "s_cmp_gt_u32 s61, s65\n\t" \
    "v_mfma_f32_16x16x32_bf16 v[84:87], v[60:63], a[132:135], v[84:87]\n\t" "s_cselect_b32 s65, 1, 0\n\t" "s_cselect_b32 s64, s64, 0x7f000000\n\t" \
    "v_mfma_f32_16x16x32_bf16 v[80:83], v[64:67], a[136:139], v[80:83]\n\t" "buffer_load_dwordx4 v[16:19], %[ao], %[rs], s64 offen offset:0 sc1\n\t" \
    "v_cvt_pk_bf16_f32 v112, v96, v97\n\t" "v_mfma_f32_16x16x32_bf16 v[84:87], v[68:71], a[136:139], v[84:87]\n\t" "v_cvt_pk_bf16_f32 v113, v98, v99\n\t" \
    "v_mfma_f32_16x16x32_bf16 v[80:83], v[72:75], a[140:143], v[80:83]\n\t" "buffer_load_dwordx4 v[20:23], %[ao], %[rs], s64 offen offset:256 sc1\n\t" \
    "v_cvt_pk_bf16_f32 v114, v100, v101\n\t" "v_mfma_f32_16x16x32_bf16 v[84:87], v[76:79], a[140:143], v[84:87]\n\t" "v_cvt_pk_bf16_f32 v115, v102, v103\n\t" \
    "v_mfma_f32_16x16x32_bf16 v[88:91], v[48:51], a[144:147], 0\n\t" "buffer_load_dwordx4 v[24:27], %[ao], %[rs], s64 offen offset:2048 sc1\n\t" \
    "v_cvt_pk_bf16_f32 v116, v104, v105\n\t" "v_mfma_f32_16x16x32_bf16 v[92:95], v[52:55], a[144:147], 0\n\t" "v_cvt_pk_bf16_f32 v117, v106, v107\n\t" \
    "v_mfma_f32_16x16x32_bf16 v[88:91], v[56:59], a[148:151], v[88:91]\n\t" "buffer_load_dwordx4 v[28:31], %[ao], %[rs], s64 offen offset:2304 sc1\n\t" \
    "v_mfma_f32_16x16x32_bf16 v[92:95], v[60:63], a[148:151], v[92:95]\n\t" "buffer_store_dwordx4 v[112:115], %[z1], %[rs], s63 offen sc1\n\t" \
    "v_mfma_f32_16x16x32_bf16 v[88:91], v[64:67], a[152:155], v[88:91]\n\t" "buffer_load_dwordx4 v[32:35], %[a1], %[rs], s64 offen offset:0 sc1\n\t" \
    "v_cvt_pk_bf16_f32 v118, v108, v109\n\t" "v_mfma_f32_16x16x32_bf16 v[92:95], v[68:71], a[152:155], v[92:95]\n\t" "v_cvt_pk_bf16_f32 v119, v110, v111\n\t" \
    "v_mfma_f32_16x16x32_bf16 v[88:91], v[72:75], a[156:159], v[88:91]\n\t" "buffer_load_dwordx4 v[36:39], %[a1], %[rs], s64 offen offset:256 sc1\n\t" \
    "buffer_store_dwordx4 v[116:119], %[z2], %[rs], s63 offen sc1\n\t" "v_mfma_f32_16x16x32_bf16 v[92:95], v[76:79], a[156:159], v[92:95]\n\t" \
    "s_add_u32 s63, s63, 0x20000\n\t" "v_mfma_f32_16x16x32_bf16 v[96:99], v[48:51], a[160:163], 0\n\t" \
    "buffer_load_dwordx4 v[40:43], %[a1], %[rs], s64 offen offset:2048 sc1\n\t" "v_mfma_f32_16x16x32_bf16 v[100:103], v[52:55], a[160:163], 0\n\t" \
    "v_mfma_f32_16x16x32_bf16 v[96:99], v[56:59], a[164:167], v[96:99]\n\t" "buffer_load_dwordx4 v[44:47], %[a1], %[rs], s64 offen offset:2304 sc1\n\t" \
    "v_mfma_f32_16x16x32_bf16 v[100:103], v[60:63], a[164:167], v[100:103]\n\t" "v_mfma_f32_16x16x32_bf16 v[96:99], v[64:67], a[168:171], v[96:99]\n\t" \
    "v_cvt_pk_bf16_f32 v112, v80, v81\n\t" "v_mfma_f32_16x16x32_bf16 v[100:103], v[68:71], a[168:171], v[100:103]\n\t" "v_cvt_pk_bf16_f32 v113, v82, v83\n\t" \
    "v_mfma_f32_16x16x32_bf16 v[96:99], v[72:75], a[172:175], v[96:99]\n\t" "v_cvt_pk_bf16_f32 v114, v84, v85\n\t" \
    "v_mfma_f32_16x16x32_bf16 v[100:103], v[76:79], a[172:175], v[100:103]\n\t" "v_cvt_pk_bf16_f32 v115, v86, v87\n\t" \
    "v_mfma_f32_16x16x32_bf16 v[104:107], v[48:51], a[176:179], 0\n\t" "v_cvt_pk_bf16_f32 v116, v88, v89\n\t" \
    "v_mfma_f32_16x16x32_bf16 v[108:111], v[52:55], a[176:179], 0\n\t" "v_cvt_pk_bf16_f32 v117, v90, v91\n\t" \
    "v_mfma_f32_16x16x32_bf16 v[104:107], v[56:59], a[180:183], v[104:107]\n\t" "buffer_store_dwordx4 v[112:115], %[z1], %[rs], s63 offen sc1\n\t" \
    "v_mfma_f32_16x16x32_bf16 v[108:111], v[60:63], a[180:183], v[108:111]\n\t" "v_mfma_f32_16x16x32_bf16 v[104:107], v[64:67], a[184:187], v[104:107]\n\t" \
    "v_cvt_pk_bf16_f32 v118, v92, v93\n\t" "v_mfma_f32_16x16x32_bf16 v[108:111], v[68:71], a[184:187], v[108:111]\n\t" "v_cvt_pk_bf16_f32 v119, v94, v95\n\t" \
    "v_mfma_f32_16x16x32_bf16 v[104:107], v[72:75], a[188:191], v[104:107]\n\t" "buffer_store_dwordx4 v[116:119], %[z2], %[rs], s63 offen sc1\n\t" \
    "v_mfma_f32_16x16x32_bf16 v[108:111], v[76:79], a[188:191], v[108:111]\n\t" "s_add_u32 s63, s63, 0x20000\n\t" \
    "v_mfma_f32_16x16x32_bf16 v[80:83], v[48:51], a[192:195], 0\n\t" "v_mfma_f32_16x16x32_bf16 v[84:87], v[52:55], a[192:195], 0\n\t" \
    "v_mfma_f32_16x16x32_bf16 v[80:83], v[56:59], a[196:199], v[80:83]\n\t" "v_mfma_f32_16x16x32_bf16 v[84:87], v[60:63], a[196:199], v[84:87]\n\t" \
    "v_mfma_f32_16x16x32_bf16 v[80:83], v[64:67], a[200:203], v[80:83]\n\t" "v_cvt_pk_bf16_f32 v112, v96, v97\n\t" \
    "v_mfma_f32_16x16x32_bf16 v[84:87], v[68:71], a[200:203], v[84:87]\n\t" "v_cvt_pk_bf16_f32 v113, v98, v99\n\t" \
    "v_mfma_f32_16x16x32_bf16 v[80:83], v[72:75], a[204:207], v[80:83]\n\t" "v_cvt_pk_bf16_f32 v114, v100, v101\n\t" \
    "v_mfma_f32_16x16x32_bf16 v[84:87], v[76:79], a[204:207], v[84:87]\n\t" "v_cvt_pk_bf16_f32 v115, v102, v103\n\t" \
    "v_mfma_f32_16x16x32_bf16 v[88:91], v[48:51], a[208:211], 0\n\t" "v_cvt_pk_bf16_f32 v116, v104, v105\n\t" "v_mfma_f32_16x16x32_bf16 v[92:95], v[52:55], a[208:211], 0\n\t" \
    "v_cvt_pk_bf16_f32 v117, v106, v107\n\t" "v_mfma_f32_16x16x32_bf16 v[88:91], v[56:59], a[212:215], v[88:91]\n\t" \
    "buffer_store_dwordx4 v[112:115], %[z1], %[rs], s63 offen sc1\n\t" "v_mfma_f32_16x16x32_bf16 v[92:95], v[60:63], a[212:215], v[92:95]\n\t" \
    "v_mfma_f32_16x16x32_bf16 v[88:91], v[64:67], a[216:219], v[88:91]\n\t" "v_cvt_pk_bf16_f32 v118, v108, v109\n\t" \
    "v_mfma_f32_16x16x32_bf16 v[92:95], v[68:71], a[216:219], v[92:95]\n\t" "v_cvt_pk_bf16_f32 v119, v110, v111\n\t" \
    "v_mfma_f32_16x16x32_bf16 v[88:91], v[72:75], a[220:223], v[88:91]\n\t" "buffer_store_dwordx4 v[116:119], %[z2], %[rs], s63 offen sc1\n\t" \
    "v_mfma_f32_16x16x32_bf16 v[92:95], v[76:79], a[220:223], v[92:95]\n\t" "s_add_u32 s63, s63, 0x20000\n\t" "v_mfma_f32_16x16x32_bf16 v[96:99], v[48:51], a[224:227], 0\n\t" \
    "v_mfma_f32_16x16x32_bf16 v[100:103], v[52:55], a[224:227], 0\n\t" "v_mfma_f32_16x16x32_bf16 v[96:99], v[56:59], a[228:231], v[96:99]\n\t" \
    "v_mfma_f32_16x16x32_bf16 v[100:103], v[60:63], a[228:231], v[100:103]\n\t" "v_mfma_f32_16x16x32_bf16 v[96:99], v[64:67], a[232:235], v[96:99]\n\t" \
    "v_cvt_pk_bf16_f32 v112, v80, v81\n\t" "v_mfma_f32_16x16x32_bf16 v[100:103], v[68:71], a[232:235], v[100:103]\n\t" "v_cvt_pk_bf16_f32 v113, v82, v83\n\t" \
    "v_mfma_f32_16x16x32_bf16 v[96:99], v[72:75], a[236:239], v[96:99]\n\t" "v_cvt_pk_bf16_f32 v114, v84, v85\n\t" \
    "v_mfma_f32_16x16x32_bf16 v[100:103], v[76:79], a[236:239], v[100:103]\n\t" "v_cvt_pk_bf16_f32 v115, v86, v87\n\t" \
    "v_mfma_f32_16x16x32_bf16 v[104:107], v[48:51], a[240:243], 0\n\t" "v_cvt_pk_bf16_f32 v116, v88, v89\n\t" \
    "v_mfma_f32_16x16x32_bf16 v[108:111], v[52:55], a[240:243], 0\n\t" "v_cvt_pk_bf16_f32 v117, v90, v91\n\t" \
    "v_mfma_f32_16x16x32_bf16 v[104:107], v[56:59], a[244:247], v[104:107]\n\t" "buffer_store_dwordx4 v[112:115], %[z1], %[rs], s63 offen sc1\n\t" \
    "v_mfma_f32_16x16x32_bf16 v[108:111], v[60:63], a[244:247], v[108:111]\n\t" "v_mfma_f32_16x16x32_bf16 v[104:107], v[64:67], a[248:251], v[104:107]\n\t" \
    "v_cvt_pk_bf16_f32 v118, v92, v93\n\t" "v_mfma_f32_16x16x32_bf16 v[108:111], v[68:71], a[248:251], v[108:111]\n\t" "v_cvt_pk_bf16_f32 v119, v94, v95\n\t" \
    "v_mfma_f32_16x16x32_bf16 v[104:107], v[72:75], a[252:255], v[104:107]\n\t" "buffer_store_dwordx4 v[116:119], %[z2], %[rs], s63 offen sc1\n\t" \
    "s_add_u32 s63, s63, 0x20000\n\t" "s_add_u32 s60, s60, 1\n\t" "v_mfma_f32_16x16x32_bf16 v[108:111], v[76:79], a[252:255], v[108:111]\n\t" "s_cmp_eq_u32 s65, 0\n\t" \
    "s_waitcnt vmcnt(6)\n\t" ZSUM "s_cbranch_scc0 33b\n\t" "s_branch 30f\n\t" "20:\n\t" "s_nop 7\n\t" "v_cvt_pk_bf16_f32 v112, v96, v97\n\t" \
    "v_cvt_pk_bf16_f32 v113, v98, v99\n\t" "v_cvt_pk_bf16_f32 v114, v100, v101\n\t" "v_cvt_pk_bf16_f32 v115, v102, v103\n\t" "v_cvt_pk_bf16_f32 v116, v104, v105\n\t" \
    "v_cvt_pk_bf16_f32 v117, v106, v107\n\t" "v_cvt_pk_bf16_f32 v118, v108, v109\n\t" "v_cvt_pk_bf16_f32 v119, v110, v111\n\t" \
    "buffer_store_dwordx4 v[112:115], %[z1], %[rs], s63 offen sc1\n\t" "buffer_store_dwordx4 v[116:119], %[z2], %[rs], s63 offen sc1\n\t" \
    "buffer_load_dword v120, %[fo], %[rc], %[fv] offen sc1\n\t" "s_waitcnt vmcnt(0)\n\t" "v_readfirstlane_b32 s61, v120\n\t" "s_lshr_b32 s66, s60, 2\n\t" \
    "s_xor_b32 s66, s66, %[xp]\n\t" "s_and_b32 s66, s66, 1\n\t" "s_lshl_b32 s66, s66, 13\n\t" "s_lshl_b32 s64, s60, 19\n\t" "s_add_u32 s64, s64, s66\n\t" \
    "s_add_u32 s64, s64, %[vb]\n\t" "s_cmp_gt_u32 s61, s60\n\t" "s_cbranch_scc0 24f\n\t" "s_cmp_eq_u32 s60, 64\n\t" "s_cbranch_scc1 24f\n\t" \
    "buffer_load_dwordx4 v[48:51], %[ao], %[rs], s64 offen offset:0 sc1\n\t" "buffer_load_dwordx4 v[52:55], %[ao], %[rs], s64 offen offset:256 sc1\n\t" \
    "buffer_load_dwordx4 v[56:59], %[ao], %[rs], s64 offen offset:2048 sc1\n\t" "buffer_load_dwordx4 v[60:63], %[ao], %[rs], s64 offen offset:2304 sc1\n\t" \
    "buffer_load_dwordx4 v[64:67], %[a1], %[rs], s64 offen offset:0 sc1\n\t" "buffer_load_dwordx4 v[68:71], %[a1], %[rs], s64 offen offset:256 sc1\n\t" \
    "buffer_load_dwordx4 v[72:75], %[a1], %[rs], s64 offen offset:2048 sc1\n\t" "buffer_load_dwordx4 v[76:79], %[a1], %[rs], s64 offen offset:2304 sc1\n\t" \
    "v_mov_b32 v122, s60\n\t" "buffer_store_dword v122, %[fo], %[rc], %[fz] offen sc1\n\t" "s_waitcnt vmcnt(1)\n\t" "s_branch 23b\n\t" "24:\n\t" "v_mov_b32 v122, s60\n\t" \
    "buffer_store_dword v122, %[fo], %[rc], %[fz] offen sc1\n\t" "s_cmp_eq_u32 s60, 64\n\t" "s_cbranch_scc1 90f\n\t" "s_add_u32 s67, s60, 1\n\t" "s_cmp_ge_u32 s61, s67\n\t" \
    "s_cbranch_scc1 22f\n\t" "21:\n\t" "buffer_load_dword v120, %[fo], %[rc], %[fv] offen sc1\n\t" "buffer_load_dword v121, %[fo], %[rc], %[vd] offen sc1\n\t" \
    "s_waitcnt vmcnt(0)\n\t" "v_readfirstlane_b32 s61, v120\n\t" "v_readfirstlane_b32 s66, v121\n\t" "s_nop 3\n\t" "s_cmp_ge_u32 s61, s67\n\t" "s_cbranch_scc1 22f\n\t" \
    "s_bitcmp1_b32 s66, 1\n\t" "s_cbranch_scc1 90f\n\t" "s_cmp_eq_u32 %[bd], 0\n\t" "s_cbranch_scc1 22f\n\t" "s_sub_u32 %[bd], %[bd], 1\n\t" "s_sleep 2\n\t" \
    "s_branch 21b\n\t" "22:\n\t" "s_lshr_b32 s66, s60, 2\n\t" "s_xor_b32 s66, s66, %[xp]\n\t" "s_and_b32 s66, s66, 1\n\t" "s_lshl_b32 s66, s66, 13\n\t" \
    "s_lshl_b32 s64, s60, 19\n\t" "s_add_u32 s64, s64, s66\n\t" "s_add_u32 s64, s64, %[vb]\n\t" "buffer_load_dwordx4 v[48:51], %[ao], %[rs], s64 offen offset:0 sc1\n\t" \
    "buffer_load_dwordx4 v[52:55], %[ao], %[rs], s64 offen offset:256 sc1\n\t" "buffer_load_dwordx4 v[56:59], %[ao], %[rs], s64 offen offset:2048 sc1\n\t" \
    "buffer_load_dwordx4 v[60:63], %[ao], %[rs], s64 offen offset:2304 sc1\n\t" "buffer_load_dwordx4 v[64:67], %[a1], %[rs], s64 offen offset:0 sc1\n\t" \
    "buffer_load_dwordx4 v[68:71], %[a1], %[rs], s64 offen offset:256 sc1\n\t" "buffer_load_dwordx4 v[72:75], %[a1], %[rs], s64 offen offset:2048 sc1\n\t" \
    "buffer_load_dwordx4 v[76:79], %[a1], %[rs], s64 offen offset:2304 sc1\n\t" "s_waitcnt vmcnt(0)\n\t" "s_branch 23b\n\t" "60:\n\t" "s_sub_u32 s67, s60, %[rg]\n\t" \
    "s_cmp_ge_u32 s62, s67\n\t" "s_cbranch_scc1 62f\n\t" "61:\n\t" "buffer_load_dword v120, %[fo], %[rc], %[fs] offen sc1\n\t" \
    "buffer_load_dword v121, %[fo], %[rc], %[vd] offen sc1\n\t" "s_waitcnt vmcnt(0)\n\t" "v_readfirstlane_b32 s62, v120\n\t" "v_readfirstlane_b32 s66, v121\n\t" "s_nop 3\n\t" \
    "s_cmp_ge_u32 s62, s67\n\t" "s_cbranch_scc1 62f\n\t" "s_bitcmp1_b32 s66, 1\n\t" "s_cbranch_scc1 90f\n\t" "s_cmp_eq_u32 %[bd], 0\n\t" "s_cbranch_scc1 62f\n\t" \
    "s_sub_u32 %[bd], %[bd], 1\n\t" "s_sleep 2\n\t" "s_branch 61b\n\t" "62:\n\t" "s_branch 63b\n\t" "30:\n\t" "s_nop 7\n\t" "v_cvt_pk_bf16_f32 v112, v96, v97\n\t" \
    "v_cvt_pk_bf16_f32 v113, v98, v99\n\t" "v_cvt_pk_bf16_f32 v114, v100, v101\n\t" "v_cvt_pk_bf16_f32 v115, v102, v103\n\t" "v_cvt_pk_bf16_f32 v116, v104, v105\n\t" \
    "v_cvt_pk_bf16_f32 v117, v106, v107\n\t" "v_cvt_pk_bf16_f32 v118, v108, v109\n\t" "v_cvt_pk_bf16_f32 v119, v110, v111\n\t" \
    "buffer_store_dwordx4 v[112:115], %[z1], %[rs], s63 offen sc1\n\t" "buffer_store_dwordx4 v[116:119], %[z2], %[rs], s63 offen sc1\n\t" \
    "buffer_load_dword v120, %[fo], %[rc], %[fv] offen sc1\n\t" "s_waitcnt vmcnt(0)\n\t" "v_readfirstlane_b32 s61, v120\n\t" "s_lshr_b32 s66, s60, 2\n\t" \
    "s_xor_b32 s66, s66, %[xp]\n\t" "s_and_b32 s66, s66, 1\n\t" "s_lshl_b32 s66, s66, 13\n\t" "s_lshl_b32 s64, s60, 19\n\t" "s_add_u32 s64, s64, s66\n\t" \
    "s_add_u32 s64, s64, %[vb]\n\t" "s_cmp_gt_u32 s61, s60\n\t" "s_cbranch_scc0 34f\n\t" "s_cmp_eq_u32 s60, 64\n\t" "s_cbranch_scc1 34f\n\t" \
    "buffer_load_dwordx4 v[16:19], %[ao], %[rs], s64 offen offset:0 sc1\n\t" "buffer_load_dwordx4 v[20:23], %[ao], %[rs], s64 offen offset:256 sc1\n\t" \
    "buffer_load_dwordx4 v[24:27], %[ao], %[rs], s64 offen offset:2048 sc1\n\t" "buffer_load_dwordx4 v[28:31], %[ao], %[rs], s64 offen offset:2304 sc1\n\t" \
    "buffer_load_dwordx4 v[32:35], %[a1], %[rs], s64 offen offset:0 sc1\n\t" "buffer_load_dwordx4 v[36:39], %[a1], %[rs], s64 offen offset:256 sc1\n\t" \
    "buffer_load_dwordx4 v[40:43], %[a1], %[rs], s64 offen offset:2048 sc1\n\t" "buffer_load_dwordx4 v[44:47], %[a1], %[rs], s64 offen offset:2304 sc1\n\t" \
    "v_mov_b32 v122, s60\n\t" "buffer_store_dword v122, %[fo], %[rc], %[fz] offen sc1\n\t" "s_waitcnt vmcnt(1)\n\t" "s_branch 33b\n\t" "34:\n\t" "v_mov_b32 v122, s60\n\t" \
    "buffer_store_dword v122, %[fo], %[rc], %[fz] offen sc1\n\t" "s_cmp_eq_u32 s60, 64\n\t" "s_cbranch_scc1 90f\n\t" "s_add_u32 s67, s60, 1\n\t" "s_cmp_ge_u32 s61, s67\n\t" \
    "s_cbranch_scc1 32f\n\t" "31:\n\t" "buffer_load_dword v120, %[fo], %[rc], %[fv] offen sc1\n\t" "buffer_load_dword v121, %[fo], %[rc], %[vd] offen sc1\n\t" \
    "s_waitcnt vmcnt(0)\n\t" "v_readfirstlane_b32 s61, v120\n\t" "v_readfirstlane_b32 s66, v121\n\t" "s_nop 3\n\t" "s_cmp_ge_u32 s61, s67\n\t" "s_cbranch_scc1 32f\n\t" \
    "s_bitcmp1_b32 s66, 1\n\t" "s_cbranch_scc1 90f\n\t" "s_cmp_eq_u32 %[bd], 0\n\t" "s_cbranch_scc1 32f\n\t" "s_sub_u32 %[bd], %[bd], 1\n\t" "s_sleep 2\n\t" \
    "s_branch 31b\n\t" "32:\n\t" "s_lshr_b32 s66, s60, 2\n\t" "s_xor_b32 s66, s66, %[xp]\n\t" "s_and_b32 s66, s66, 1\n\t" "s_lshl_b32 s66, s66, 13\n\t" \
    "s_lshl_b32 s64, s60, 19\n\t" "s_add_u32 s64, s64, s66\n\t" "s_add_u32 s64, s64, %[vb]\n\t" "buffer_load_dwordx4 v[16:19], %[ao], %[rs], s64 offen offset:0 sc1\n\t" \
    "buffer_load_dwordx4 v[20:23], %[ao], %[rs], s64 offen offset:256 sc1\n\t" "buffer_load_dwordx4 v[24:27], %[ao], %[rs], s64 offen offset:2048 sc1\n\t" \
    "buffer_load_dwordx4 v[28:31], %[ao], %[rs], s64 offen offset:2304 sc1\n\t" "buffer_load_dwordx4 v[32:35], %[a1], %[rs], s64 offen offset:0 sc1\n\t" \
    "buffer_load_dwordx4 v[36:39], %[a1], %[rs], s64 offen offset:256 sc1\n\t" "buffer_load_dwordx4 v[40:43], %[a1], %[rs], s64 offen offset:2048 sc1\n\t" \
    "buffer_load_dwordx4 v[44:47], %[a1], %[rs], s64 offen offset:2304 sc1\n\t" "s_waitcnt vmcnt(0)\n\t" "s_branch 33b\n\t" "70:\n\t" "s_sub_u32 s67, s60, %[rg]\n\t" \
    "s_cmp_ge_u32 s62, s67\n\t" "s_cbranch_scc1 72f\n\t" "71:\n\t" "buffer_load_dword v120, %[fo], %[rc], %[fs] offen sc1\n\t" \
    "buffer_load_dword v121, %[fo], %[rc], %[vd] offen sc1\n\t" "s_waitcnt vmcnt(0)\n\t" "v_readfirstlane_b32 s62, v120\n\t" "v_readfirstlane_b32 s66, v121\n\t" "s_nop 3\n\t" \
    "s_cmp_ge_u32 s62, s67\n\t" "s_cbranch_scc1 72f\n\t" "s_bitcmp1_b32 s66, 1\n\t" "s_cbranch_scc1 90f\n\t" "s_cmp_eq_u32 %[bd], 0\n\t" "s_cbranch_scc1 72f\n\t" \
    "s_sub_u32 %[bd], %[bd], 1\n\t" "s_sleep 2\n\t" "s_branch 71b\n\t" "72:\n\t" "s_branch 73b\n\t" "90:\n\t"

#define T_DQ 1380
#define T_DV 1290
#define T_RQ 670
#define T_RV 640
#define T_M1 105
#define T_DY 582
#define T_DX 1250
#define T_MIN 35
#define T_PEN 280
#define T_SAFE 80
#define T_RETRY 700
#define T_POLL 1
#define T_KS 0
#define T_TS 0
#define T_CLR 210
#define T_EASE 1200
#define T_REF 1190
#define T_BETA 4
#define T_VLAG 110
#define T_U0 310
constexpr int T_OVH = 118, T_NEAR = 460;
constexpr unsigned NS(int ns) { return (unsigned)(ns * 64 / 10); }
struct Clk { unsigned cyc, g, pc, pg, k1, k2; };
DEV void mark(Clk& k) {
  unsigned long long c, r[8];
  asm volatile("s_memtime %0\n\ts_memrealtime %1\n\ts_nop 2\n\ts_memrealtime %2\n\ts_nop 2\n\ts_memrealtime %3\n\ts_nop 2\n\ts_memrealtime %4\n\ts_nop 2\n\t"
               "s_memrealtime %5\n\ts_nop 2\n\ts_memrealtime %6\n\ts_nop 2\n\ts_memrealtime %7\n\ts_waitcnt lgkmcnt(0)"
               : "=s"(c), "=s"(r[0]), "=s"(r[1]), "=s"(r[2]), "=s"(r[3]), "=s"(r[4]), "=s"(r[5]), "=s"(r[6]) :: "memory");
  const unsigned a = (unsigned)r[0];
  unsigned n = 0;
#pragma unroll
  for (int i = 1; i < 7; ++i) asm("s_cmp_eq_u32 %1, %2\n\ts_addc_u32 %0, %0, 0" : "+s"(n) : "s"((unsigned)r[i]), "s"(a) : "scc");
  k.g = (a + 4u) << 6; k.cyc = (unsigned)c + 12u + 16u * n;
}
DEV void anchor(Clk& k, bool any = false) {
  mark(k);
  const unsigned dg_ = k.g - k.pg;
  const float dc = (float)(k.cyc - k.pc), dg = (float)dg_;
  unsigned k1, k2;
  asm volatile("s_nop 0\n\tv_readfirstlane_b32 %0, %2\n\tv_readfirstlane_b32 %1, %3\n\ts_nop 3\n\ts_min_u32 %0, %0, 0x80000\n\ts_min_u32 %1, %1, 0x8000"
               : "=&s"(k1), "=&s"(k2) : "v"((unsigned)(dg * __builtin_amdgcn_rcpf(dc) * 65536.f)), "v"((unsigned)(dc * __builtin_amdgcn_rcpf(dg) * 65536.f)) : "scc");
  if (any) { k.k1 = k1; k.k2 = k2; }
  else asm("s_cmp_lt_u32 %4, %5\n\ts_cselect_b32 %2, %2, %0\n\ts_cselect_b32 %3, %3, %1\n\ts_add_u32 %0, %0, %2\n\ts_lshr_b32 %0, %0, 1\n\ts_add_u32 %1, %1, %3\n\ts_lshr_b32 %1, %1, 1"
           : "+s"(k.k1), "+s"(k.k2), "+s"(k1), "+s"(k2) : "s"(dg_ - NS(1400)), "n"(NS(800)) : "scc");
  k.pc = k.cyc; k.pg = k.g;
}
DEV unsigned to_cyc(const Clk& k, unsigned g) {
  unsigned d;
  asm("s_max_i32 %0, %1, 0xffff8000\n\ts_min_i32 %0, %0, 0x8000\n\ts_mul_i32 %0, %0, %2\n\ts_ashr_i32 %0, %0, 16" : "=&s"(d) : "s"(g - k.g), "s"(k.k2) : "scc");
  return k.cyc + d - T_OVH;
}
DEV void wait_cyc(unsigned t) { asm volatile(TIMED_WAIT :: "s"(t) : CL_S); }
DEV void close_in(Clk& k, unsigned t) {
  if ((int)(t - (unsigned)__builtin_amdgcn_s_memtime()) > T_NEAR + 60) { wait_cyc(t - T_NEAR); mark(k); }
}
DEV int slow_line(const u16* q, int h, int j, int lane) { return (j * 32 + (lane >> 1)) * 8192 + (h >> 3) * 2048 + (lane & 1) * 1024 + ((3 - (int)((size_t)q >> 7)) & 7) * 128; }
DEV int turn(int h, int w) { return ((h >> 3) ^ (w & 1)) & 3; }
DEV void hold(const Ctx& c, int ticks) {
#pragma unroll 1
  while ((unsigned)__builtin_amdgcn_s_memrealtime() - c.t0 < (unsigned)ticks) __builtin_amdgcn_s_sleep(4);
}
DEV int z_mix(int y) { return (y >> 4) | ((y & 1) << 2); }
DEV int u_slot(int s, int para) { return R_U + (s & (URING == 64 ? 127 : URING - 1)) * MB + ((para ^ (s >> 1)) & 1) * 8192; }
DEV unsigned ask_stamps(Ctx& c, int par, int slot) { return __builtin_amdgcn_raw_buffer_load_b32(c.rs, amap(24576, par) + fresh_lane() * 128, slot, AUX_SC1); }
DEV unsigned latest(Ctx& c, const Clk& k, int par, int slot, unsigned v) {
  if (__builtin_expect(!allok(v), 0)) {
#pragma unroll 1
    do {
      __builtin_amdgcn_s_sleep(T_POLL);
      asm volatile("" ::: "memory");
      const unsigned t = __builtin_amdgcn_raw_buffer_load_b32(c.rs, v == SENT ? amap(24576, par) + fresh_lane() * 128 : 1 << 30, slot, AUX_SC1);
      v = v == SENT ? t : v;
    } while (!allok(v) && !spend(c));
  }
  int d = (int)(v - k.g);
  d = max(d, __builtin_amdgcn_update_dpp(d, d, 0x128, 0xf, 0xf, true));
  d = max(d, __builtin_amdgcn_update_dpp(d, d, 0x124, 0xf, 0xf, true));
  d = max(d, __builtin_amdgcn_update_dpp(d, d, 0x122, 0xf, 0xf, true));
  d = max(d, __builtin_amdgcn_update_dpp(d, d, 0x121, 0xf, 0xf, true));
  unsigned m;
  asm("s_max_i32 %0, %1, %2\n\ts_max_i32 %0, %0, %3\n\ts_max_i32 %0, %0, %4" : "=&s"(m)
      : "s"(__builtin_amdgcn_readlane(d, 0)), "s"(__builtin_amdgcn_readlane(d, 16)), "s"(__builtin_amdgcn_readlane(d, 32)), "s"(__builtin_amdgcn_readlane(d, 48)) : "scc");
  return k.g + m;
}
struct Plan { unsigned gx, dx, ease; };
DEV unsigned smax(unsigned a, unsigned b) { return (int)(a - b) > 0 ? a : b; }
DEV unsigned pick(int d, unsigned a, unsigned b) { unsigned r; asm("s_cmp_gt_i32 %1, 0\n\ts_cselect_b32 %0, %2, %3" : "=s"(r) : "s"(d), "s"(a), "s"(b) : "scc"); return r; }
DEV unsigned when_p(const Plan& t, unsigned last, unsigned more = 0) {
  const unsigned gy = t.gx + t.dx + NS(T_M1), a = last + NS(T_PEN), b = gy + NS(T_RETRY);
  return pick((int)(last + NS(T_MIN) - gy), pick((int)(a - b), a, b) + more, gy);
}
DEV void advance(Plan& t, unsigned last) {
  const int over = (int)(last - t.gx - t.dx + t.ease), slow = (int)(t.dx - t.ease - NS(T_REF));
  t.gx = when_p(t, last, NS(T_SAFE)) + NS(T_DY) + pick(slow, (unsigned)(slow * T_BETA >> 3), 0u);
  t.dx -= t.ease - (t.ease >> 1); t.ease >>= 1;
  t.dx += pick(over - (int)NS(80), NS(4), pick(over, (unsigned)(over >> 1), (unsigned)(over >> 3)));
}
DEV unsigned next_go(Ctx& c, Clk& k, Plan& t, int par, int slot, unsigned v) {
  SCHED();
  anchor(k);
  SCHED();
  advance(t, latest(c, k, par, slot, v));
  return to_cyc(k, t.gx);
}

DEV constexpr int qk_r(int j) { return j < 4 ? 0 : 1 + (j - 4) % 3; }
DEV constexpr int qk_i(int j) { return j < 4 ? j : (j - 4) / 3; }
DEV constexpr int qk_upto(int u) { return K_FIRST && u >= 3 ? (u + 3 < 16 ? u + 3 : 16) : (u + 4 < 16 ? u + 4 : 16); }
DEV void qk_role(const Params& p, Ctx& c, int h) {
  constexpr int WL = 0, MEMS = 131072, KXL = 147456, QKX = 155648, QROW = 272;
  c.budget = SPIN_CAP;
  const int w = c.w;
  Clk k = {0u, 0u, 0u, 0u, 0u, 0u};
  anchor(k, true);
  v8u wa[32], wh[16];
  unsigned look = claim_look(c);
  bool claimed = false;
  hold(c, QK_AT);
  {
    const int lane = fresh_lane(), g = lane >> 4, cc = lane & 15;
    const rsrc_t rq = mkrs(p.Wq, 1024 * 4096 * 2), rk = mkrs(p.Wk, 1024 * 4096 * 2);
    int vo[4];
#pragma unroll
    for (int i = 0; i < 4; ++i) vo[i] = opq((256 * w + 64 * (i ^ turn(h, w)) + krow_lane(g)) * 8192 + (h * 128 + 8 * cc) * 2);
    v4u row[16][8];
    auto request = [&](int u) {
#pragma unroll
      for (int r = 0; r < 8; ++r) row[u][r] = ld16w<W_AUX>((u < 8) != K_FIRST ? rq : rk, vo[(u >> 1) & 3], krow(4 * (u & 1) + (r >> 1), r & 1) * 8192);
    };
#pragma unroll
    for (int u = 0; u < qk_upto(-1); ++u) { request(u); if (u == 0) { SCHED(); ask(c, look); SCHED(); } }
    SCHED();
    const int sw = MEMS + w * 8192 + g * 256, lw = opq(WL + w * 32768 + lane * 16);
    int sr[2];
#pragma unroll
    for (int t = 0; t < 2; ++t) { const int i = (2 * cc + t) & 7; sr[t] = opq(i * 1024 + g * 256 + ((4 * w + (cc >> 2) + 2 * i) & 15) * 16); }
    sfor<16>([&](auto U) __attribute__((always_inline)) {
      constexpr int u = U.value;
#pragma unroll
      for (int i = 0; i < 8; ++i) {
        v4u f;
#pragma unroll
        for (int r = 0; r < 4; ++r) f[r] = prm(row[u][2 * r + 1][i >> 1], row[u][2 * r][i >> 1], i & 1);
        sts16(c.L, sw + i * 1024 + ((cc + 2 * i) & 15) * 16, f);
      }
      SCHED();
      if ((u == 5 || u == 10) && !claimed) claimed = claim_try(c, look);
      if (u == 2 || u == 7) look = claim_look(c);
      if (qk_upto(u) > qk_upto(u - 1)) request(qk_upto(u - 1));
      if (u == 15 && !claimed) claim(c, 0);
      BAR();
      v4u in[4][2];
#pragma unroll
      for (int t = 1; t >= 0; --t)
#pragma unroll
        for (int r = 0; r < 4; ++r) in[r][t] = lds16(c.L, sr[t] + MEMS + ((w + r) & 3) * 8192);
      SCHED();
#pragma unroll
      for (int j = 0; j < 16; ++j) {
        if (qk_i(j) != ((u >> 1) & 3)) continue;
#pragma unroll
        for (int t = 1; t >= 0; --t) {
          const v4u o = in[qk_r(j)][t];
          constexpr bool isq = (u < 8) != K_FIRST;
          if (!isq && t == 1) sts16(c.L, lw + j * 2048 + (u & 1) * 1024, o);
          else {
            v8u& d = isq ? wa[2 * j + t] : wh[j];
#pragma unroll
            for (int r = 0; r < 4; ++r) d[4 * (u & 1) + r] = o[r];
#define M_(r) asm volatile("" : "+" r(d))
            if (u & 1) { if (isq) AT_ACC(M_, 2 * j + t); else AT_HOME(M_, j); }
#undef M_
          }
        }
      }
      BAR();
    });
  }
  STAMP(0);
  BAR();
  const unsigned vote = wait_verdict(c);
  const int par = vote_b(vote), para = vote_a(vote);
  const int lane = fresh_lane(), g = lane >> 4, cc = lane & 15;
  const int moff = opq(amap(w * 4096 + lane * 16, par) + (turn(h, w) & 1) * 1024), m2 = R_MEM + (turn(h, w) & 2) * 1024;
  unsigned tries = 1u << 12;
  *(volatile LAS unsigned*)(c.L + 160512) = 0u;
  LGKM(0);
  const int idx = opq((lane & 1) ? 0xEEEE : 0x4444);
  TICK_DECL;
  const int l0 = opq(WL + w * 32768 + lane * 16), l1 = opq(MEMS + lane * 16);
  const int qw = opq(QKX + (2 * w + (cc >> 3)) * 256 + g * 64 + (cc & 1) * 16 + ((cc >> 1) & 3) * 4), kw = opq(QKX + (8 + 2 * g) * QROW + (32 * w + 2 * cc) * 2);
#define QK_PINS PIN_ACC0(wa); PIN_ACC1(wa); PIN_HOME(wh)
#define QK_IN [l0] "v"(l0), [l1] "v"(l1), [mo] "v"(moff), [ix] "v"(idx), [qw] "v"(qw), [kw] "v"(kw), [rs] "s"(c.rs), [sb] "s"(m2 + (s & 3) * MEM_SLOT)
#define QK_CL CL_S, CL_16_32, CL_160_200, CL_200_216, CL_216_256
  if (w) RAWBAR();
  if (w == 3) {
    const int hq = opq(lane ? 1 << 30 : amap(24576 + h * 128, par));
    anchor(k, true);
    unsigned tg = k.cyc;
    Plan tt = {k.g - NS(100000), NS(T_DX + T_EASE), NS(T_EASE)};
    for (int s = 0; s < NSEG; ++s) {
      const int pb = R_P + (s & 3) * P_SLOT;
      QK_PINS;
      asm volatile(QK_STEP3 : [bd] "+s"(c.budget), [rt] "+s"(tries) : QK_IN, [hq] "v"(hq), [pb] "s"(pb), [tg] "s"(tg), [ca] "s"(k.cyc - T_DQ), [k1] "s"(k.k1), [ga] "s"(k.g), [dr] "n"(T_RQ), [pr] "s"(R_P + ((s + 1) & 3) * P_SLOT) : QK_CL);
      QK_PINS;
      LOG(1, s, tg);
      const unsigned sv = ask_stamps(c, par, pb);
      SCHED();
      anchor(k);
      SCHED();
      advance(tt, latest(c, k, par, pb, sv));
      tg = to_cyc(k, tt.gx);
      RAWBAR();
      LOG(2, s, k.cyc); LOG(3, s, k.g); LOG(4, s, tt.dx);
      st4s(mkrs((const void*)c.ctrl, CTRL_BYTES), hq & (1 << 30), C_FLAG + (F_S + h) * 128, (unsigned)s + 1u);
      TICK_SUM(10);
      close_in(k, tg);
      tg = to_cyc(k, tt.gx);
    }
  } else {
    const int kx0 = opq(KXL + g * 512 + cc * 16), kmrd = opq(QKX + g * 16 + (8 + (cc & 7)) * QROW);
    unsigned seen = NOFLAG;
    if (w == 0) {
      const int prow = (2 * g + (cc & 1)) * 2 + ((cc >> 1) & 1);
      const int px = opq(amap((h >> 1) * 1024 + ((2 * (h & 1) + (cc >> 3)) * 16 + prow) * 16 + ((cc >> 2) & 1) * 8, par));
      const int pm = opq(cc < 8 ? amap(16384 + h * 128 + (2 * g + (cc & 1)) * 16 + (cc & 2) * 4 + (cc & 4), par) : 1 << 30);
      const int ps = opq((cc & 1) ? 0x03020706 : 0x05040100), pa = opq(lane * 128);
      for (int s = 0; s < NSEG; ++s) {
        if (seen < (unsigned)s + 1u) seen = wait_flag(c, F_K + h, s + 1);
        QK_PINS;
        unsigned pd;
        asm volatile(QK_STEP0 : [bd] "+s"(c.budget), [rt] "+s"(tries), [sq] "+s"(seen), [pd] "=&v"(pd)
                     : QK_IN, [kx] "v"(kx0), [km] "v"(kmrd), [px] "v"(px), [pm] "v"(pm), [ps] "v"(ps), [pb] "s"(R_P + (s & 3) * P_SLOT), [pr] "s"(R_P + ((s + 1) & 3) * P_SLOT),
                       [kb] "s"(R_KX - KXL + xblk(s, h, para)), [fq] "s"(c.ctrl + (C_FLAG + (F_K + h) * 128) / 4),
                       [kn] "s"(R_KX + xblk(min(s + 1, NSEG - 1), h, para)), [pa] "v"(pa) : QK_CL);
        QK_PINS;
        LITE(s); LOGS(0, s);
        TICK_SUM(10);
      }
      RAWBAR();
    } else {
      const int qt = w - 1;
      const int px = opq(amap(qt ? 16384 + h * 1024 + lane * 16 : h * 512 + (g >> 1) * 256 + cc * 16 + (g & 1) * 8, para));
      const int pm = opq(g < 2 ? amap(49152 + (qt * 32 + h) * 256 + cc * 16 + g * 8, para) : 1 << 30);
      const int ps = opq(cc - 4 * g), hq = opq(g * 512 + (16 * qt + cc) * 16);
#define QK_TOK(STEP) for (int s = 0; s < NSEG; ++s) { QK_PINS; \
        asm volatile(STEP : [bd] "+s"(c.budget), [rt] "+s"(tries), [sq] "+s"(seen) \
                     : QK_IN, [kx] "v"(kx0), [km] "v"(kmrd), [px] "v"(px), [pm] "v"(pm), [ps] "v"(ps), [hq] "v"(hq), [qb] "s"(R_QX + xblk(s, h, para)), [tb] "s"(R_PT + (s & 7) * PT_SLOT), \
                       [tr] "s"(R_PT + ((s + 2) & 7) * PT_SLOT), [sn] "s"(s + 1), [fq] "s"(c.ctrl + (C_FLAG + (F_Q + h) * 128) / 4) : QK_CL); \
        QK_PINS; TICK_SUM(10); }
      if (w == 1) { QK_TOK(QK_STEP1) } else { QK_TOK(QK_STEP2) }
    }
  }
  TICK_OUT();
  if (c.budget == 0u) __hip_atomic_store(c.ctrl + C_TMO / 4, 1u, RLX_AGENT);
}

DEV void shuffle_cols(Ctx& c, int scratch, const v4u (&rows)[8], v4u (&o)[8]) {
  const int g = c.lane >> 4, cc = c.lane & 15;
#pragma unroll
  for (int i = 0; i < 8; ++i) {
    v4u f;
#pragma unroll
    for (int r = 0; r < 4; ++r) f[r] = prm(rows[2 * r + 1][i >> 1], rows[2 * r][i >> 1], i & 1);
    sts16(c.L, scratch + (8 * cc + i) * 64 + ((g ^ (cc & 3)) << 4), f);
  }
  asm volatile("s_waitcnt lgkmcnt(0)" ::: "memory");
#pragma unroll
  for (int i = 0; i < 8; ++i) { const int col = 16 * i + cc; o[i] = lds16(c.L, scratch + col * 64 + ((g ^ ((col >> 3) & 3)) << 4)); }
  asm volatile("s_waitcnt lgkmcnt(0)" ::: "memory");
}

DEV void vo_role(const Params& p, Ctx& c, int h) {
  constexpr int WL = 0, MEMS = 131072, VM = 147456, VROW = 272;
  c.budget = SPIN_CAP;
  const int w = c.w, lane = c.lane = fresh_lane(), g = lane >> 4, cc = lane & 15;
  Clk k = {0u, 0u, 0u, 0u, 0u, 0u};
  anchor(k, true);
  v8u wa[32], wh[16];
  ask(c, claim_look(c));
  unsigned look = 0;
  bool claimed = false;
  hold(c, VO_AT);
  const rsrc_t rwo = mkrs(p.Wo, 4096 * 1024 * 2);
  const int voo = (h * 128 + 4 * g) * 2048 + (256 * ((cc >> 3) ^ (h >> 3)) + 64 * w + 8 * (cc & 7)) * 2;
  v4u buf[3][8];
  auto request_o = [&](int u) {
#pragma unroll
    for (int r = 0; r < 8; ++r) buf[u % 3][r] = ld16w<W_AUX>(rwo, voo ^ ((u >> 2) * 1024), (32 * (u & 3) + 16 * (r >> 2) + (r & 3)) * 2048);
  };
  {
    const rsrc_t rw = mkrs(p.Wv, 1024 * 4096 * 2);
    const int vo = (256 * w + krow_lane(g)) * 8192 + (h * 128 + 8 * cc) * 2;
    v4u vrow[8][8];
    auto request = [&](int u) {
#pragma unroll
      for (int r = 0; r < 8; ++r) vrow[u][r] = ld16w<W_AUX>(rw, vo, (64 * ((u >> 1) ^ turn(h, w)) + krow(4 * (u & 1) + (r >> 1), r & 1)) * 8192);
    };
#pragma unroll
    for (int u = 0; u < 3; ++u) request(u);
    SCHED();
    const int wr_ = MEMS + w * 8192 + g * 256;
    int sr[2];
#pragma unroll
    for (int t = 0; t < 2; ++t) { const int i = (2 * cc + t) & 7; sr[t] = opq(i * 1024 + g * 256 + ((4 * w + (cc >> 2) + 2 * i) & 15) * 16); }
#pragma unroll
    for (int u = 0; u < 8; ++u) {
#pragma unroll
      for (int i = 0; i < 8; ++i) {
        v4u f;
#pragma unroll
        for (int r = 0; r < 4; ++r) f[r] = prm(vrow[u][2 * r + 1][i >> 1], vrow[u][2 * r][i >> 1], i & 1);
        sts16(c.L, wr_ + i * 1024 + ((cc + 2 * i) & 15) * 16, f);
      }
      SCHED();
      if (u == 7) claimed = claim_try(c, look);
      if (u == 4) look = claim_look(c);
      if (u + 3 < 8) request(u + 3); else request_o(u - 5);
      BAR();
      v4u in[4][2];
#pragma unroll
      for (int t = 1; t >= 0; --t)
#pragma unroll
        for (int r = 0; r < 4; ++r) in[r][t] = lds16(c.L, sr[t] + MEMS + ((w + r) & 3) * 8192);
      SCHED();
#pragma unroll
      for (int j = 0; j < 16; ++j) {
        if (qk_i(j) != (u >> 1)) continue;
#pragma unroll
        for (int t = 1; t >= 0; --t) {
          const v4u o = in[qk_r(j)][t];
          if (t == 1 && (j & 1)) sts16(c.L, WL + (w * 16 + (j >> 1)) * 2048 + (u & 1) * 1024 + lane * 16, o);
          else {
            const int n = 3 * (j >> 1) + ((j & 1) ? 2 : t);
#pragma unroll
            for (int r = 0; r < 4; ++r) wa[n][4 * (u & 1) + r] = o[r];
#define M_(r) asm volatile("" : "+" r(wa[n]))
            if (u & 1) AT_ACC(M_, n);
#undef M_
          }
        }
      }
      BAR();
    }
  }
#pragma unroll
  for (int u = 0; u < 8; ++u) {
    v4u o[8];
    shuffle_cols(c, MEMS + w * 8192, buf[u % 3], o);
    SCHED();
    if (u + 3 < 8) request_o(u + 3);
#pragma unroll
    for (int i = 0; i < 4; ++i) {
      const int pr = 4 * (u >> 2) + i, ch = u & 3;
      const v8u f = {o[2 * i][0], o[2 * i + 1][0], o[2 * i][1], o[2 * i + 1][1], o[2 * i][2], o[2 * i + 1][2], o[2 * i][3], o[2 * i + 1][3]};
      if (ch < 2) {
        wh[2 * pr + ch] = f;
#define M_(r) asm volatile("" : "+" r(wh[2 * pr + ch]))
        AT_HOME(M_, 2 * pr + ch);
#undef M_
      } else if (ch == 2) {
        wa[24 + pr] = f;
#define M_(r) asm volatile("" : "+" r(wa[24 + pr]))
        AT_ACC(M_, 24 + pr);
#undef M_
      } else {
        sts16(c.L, WL + (w * 16 + 8 + pr) * 2048 + lane * 16, lo4(f));
        sts16(c.L, WL + (w * 16 + 8 + pr) * 2048 + 1024 + lane * 16, hi4(f));
      }
    }
    SCHED();
  }
  if (!claimed) claim(c, 0);
  BAR();
  STAMP(0);
  const unsigned vote = wait_verdict(c);
  const int par = vote_b(vote), para = vote_a(vote);
  const int moff = opq(amap(w * 4096 + lane * 16, par) + (turn(h, w) & 1) * 1024), m2 = R_MEM + (turn(h, w) & 2) * 1024;
  unsigned tries = 1u << 12;
  *(volatile LAS unsigned*)(c.L + 160512) = 0u;
  LGKM(0);
  const int idx = opq((lane & 1) ? 0xEEEE : 0x4444);
  const int u_off = opq((4 * w + g) * 16384 + (h ^ (8 * g)) * 256 + cc * 16);
  const int hq = opq((lane | w) ? 1 << 30 : amap(24576 + (32 + h) * 128, par));
  const int l0 = opq(WL + w * 32768 + lane * 16), l1 = opq(MEMS + lane * 16);
  const int vmw = opq(VM + 2 * g * VROW + (32 * w + 2 * cc) * 2);
  const int vmr = opq(VM + (cc >> 1) * VROW + g * 16);
  TICK_DECL;
  anchor(k, true);
  unsigned tg = k.cyc;
  Plan tt = {k.g - NS(100000), NS(T_DX + T_EASE), NS(T_EASE)};
  for (int s = 0; s < NSEG; ++s) {
    const int pb = R_P + (s & 3) * P_SLOT;
    PIN_ACC0(wa); PIN_ACC1(wa); PIN_HOME(wh);
    asm volatile(VO_STEP
        : [bd] "+s"(c.budget), [rt] "+s"(tries)
        : [l0] "v"(l0), [l1] "v"(l1), [mo] "v"(moff), [ix] "v"(idx), [vw] "v"(vmw), [vr] "v"(vmr), [uo] "v"(u_off), [w] "s"(w), [rs] "s"(c.rs),
          [sb] "s"(m2 + (s & 3) * MEM_SLOT), [ub] "s"(u_slot(s, para) + (h >> 3) * 16 * 16384), [ur] "s"(u_slot(s + 2, para) + (2 * h + (w >> 1)) * 16384 + (w & 1) * 4096 - MEMS), [tg] "s"(tg + T_VLAG), [hq] "v"(hq), [pb] "s"(pb),
          [ca] "s"(k.cyc - T_DV), [k1] "s"(k.k1), [ga] "s"(k.g), [dr] "n"(T_RV), [pr] "s"(R_P + ((s + 1) & 3) * P_SLOT)
        : CL_S, CL_16_32, CL_160_200, CL_200_216, CL_216_256);
    PIN_ACC0(wa); PIN_ACC1(wa); PIN_HOME(wh);
    if (w == 0) {
      LITE(s);
      LOG(1, s, tg);
      const unsigned sv = ask_stamps(c, par, pb);
      SCHED();
      anchor(k);
      const unsigned gc = tt.gx + tt.dx + NS(T_M1 + T_CLR);
      const bool ontime = (int)(gc - k.g) > 0;
      if (ontime) { wait_cyc(to_cyc(k, gc)); RAWBAR(); }
      SCHED();
      advance(tt, latest(c, k, par, pb, sv));
      if (!ontime) { wait_cyc(to_cyc(k, tt.gx - NS(T_DY + T_SAFE - T_CLR))); RAWBAR(); }
      tg = to_cyc(k, tt.gx);
      LOG(2, s, k.cyc); LOG(3, s, k.g);
    }
    TICK_SUM(10);
    if (w == 0) { close_in(k, tg); tg = to_cyc(k, tt.gx); }
  }
  TICK_OUT();
  if (c.budget == 0u) __hip_atomic_store(c.ctrl + C_TMO / 4, 1u, RLX_AGENT);
}

DEV void y_role(const Params& p, Ctx& c, int y) {
  constexpr int ZR = 0, UL = 131072, CW = 155648;
  c.budget = SPIN_CAP;
  const int w = c.w, lane = c.lane = fresh_lane(), g = lane >> 4, cc = lane & 15;
  const v4u sent4 = {SENT, SENT, SENT, SENT};
  STAMP(2);
  Clk k = {0u, 0u, 0u, 0u, 0u, 0u};
  anchor(k, true);
  unsigned m0 = 0;
  if (w == 0) {
    unsigned best[2] = {~0u, ~0u};
    const unsigned first = claim_look(c);
    st4s(c.rs, y * 16384 + (lane >> 5) * 8192 + (lane & 31) * 4, R_U, SENT);
    ask(c, first);
#pragma unroll 1
    for (int n = 0; n < 12; ++n) {
#pragma unroll
      for (int r = 0; r < 2; ++r) {
        VMWAIT(0);
        const unsigned t0 = (unsigned)__builtin_amdgcn_s_memtime();
        LGKM(0);
        st4s(c.rs, y * 16384 + (lane & 31) * 4, R_U + r * 8192, SENT);
        VMWAIT(0);
        const unsigned t1 = (unsigned)__builtin_amdgcn_s_memtime();
        LGKM(0);
        best[r] = min(best[r], t1 - t0);
      }
      if (n + 1 >= PROBES && min(best[0], best[1]) < 600u && max(best[0], best[1]) > min(best[0], best[1]) + 128u) break;
    }
    STAMP(5);
    claim(c, CL_VOTED | (best[1] < best[0] ? CL_A : 0u));
    m0 = *(const GAS unsigned*)(p.mem0 + (lane >> 3) * 1024 + 16 * y + 2 * (lane & 7));
    m0 = m0 < SENT - 1u ? m0 : SENT - 1u;
    if (lane < 8) *(LAS unsigned*)(c.L + CW + lane * 4) = 0u;
    const unsigned v = wait_vote(c);
    STAMP(7);
    const int oa = y * 16384 + vote_a(v) * 8192 + lane * 16, ob = amap(y * 3072 + lane * 16, vote_b(v));
#pragma unroll
    for (int i = 0; i < 16; ++i) st16s(c.rs, oa + (i & 3) * 1024, R_U + (i >> 3) * MB + ((i >> 2) & 1) * 4096, sent4);
#pragma unroll
    for (int i = 0; i < 3; ++i) st16s(c.rs, amap(y * 3072 + i * 1024 + lane * 16, vote_b(v)), R_MEM, sent4);
#pragma unroll
    for (int i = 0; i < 8; ++i) st16s(c.rs, oa + (i & 3) * 1024, R_PT + (i >> 2) * 4096, sent4);
    (void)ob;
    VMWAIT(0);
    if (lane == 0) __hip_atomic_fetch_add(c.ctrl + (C_CLAIM + c.att * 1024) / 4 + c.slot, CL_FILLED, RLX_AGENT);
    STAMP(6);
  } else {
    if (YH && w < 3 && (y & 8) == 0) {
      hold(c, YH_AT);
#pragma unroll
      for (int i = 0; i < 12; ++i) {
        const u16* q = i < 4 ? p.Wv : (i < 8) != K_FIRST ? p.Wq : p.Wk;
        if (i == 8) hold(c, YH_AT + YH_STEP);
        m0 += __builtin_amdgcn_raw_buffer_load_b32(mkrs(q, 8 * MB), slow_line(q, y >> 1, (y & 7) + 8 * (i & 3), lane) ^ ((w - 1) * 128), 0, YH_AUX);
      }
      asm volatile("" :: "v"(m0));
    }
  }
  BAR();
  const int nb = (c.b - SERVER) >> 3;
  if (w == 3 ? (c.b == SERVER + 8 || c.b == SERVER + 16) : w && c.b != SERVER) {
    hold(c, X_AT);
#pragma unroll
    for (int i = 0; i < 4; ++i) m0 += __builtin_amdgcn_raw_buffer_load_b32(mkrs(p.x, 4 * MB), ((w == 3 ? 2 * y - nb - 1 : 2 * y + w - 1) * 64 + lane) * 128, i * MB, 0);
    asm volatile("" :: "v"(m0));
  }
  const unsigned vote = wait_verdict(c);
  const int par = vote_b(vote), para = vote_a(vote);
  const int mo = amap(y * 256 + lane * 4, par);
  if (w == 0) st4s(c.rs, mo, R_MEM, m0);
  STAMP(0);
  const int rot = y >> 4;
  volatile LAS unsigned* const GO = (volatile LAS unsigned*)(c.L + CW);
  TICK_DECL;
  if (w == 0) {
    int xo[4];
#pragma unroll
    for (int i = 0; i < 4; ++i) xo[i] = amap((i ^ rot) * 4096 + lane * 16, par);
    const int pmo = amap(16384 + rot * 1024 + (2 * g + (cc & 1)) * 128 + (cc >> 1) * 16, par), psel = (cc & 1) ? 0x03020706 : 0x05040100;
    constexpr int lead = T_U0, late = 0;
    anchor(k, true);
    Plan tt = {k.g - NS(100000), NS(T_DX + T_EASE), NS(T_EASE)};
    unsigned done = k.cyc;
    const int so = amap(24576, par) + lane * 128;
    const int idx = opq((lane & 1) ? 0xEEEE : 0x4444);
    const int uo = y * 16384 + lane * 16;
    const int mw = amap(y * 256 + (2 * g + (cc & 1)) * 32 + (cc >> 1) * 4, par);
    for (int s = 0; s < NSEG - 1; ++s) {
      if (s == 0) RAWBAR();
      const int za = ZR + (s & 3) * 32768 + rot * 8192 + lane * 16;
      v32u z0, z1, z2, z3;
      asm volatile(
          "ds_read_b128 a[0:3], %[z0] offset:0\n\t" "ds_read_b128 a[4:7], %[z0] offset:1024\n\t" "ds_read_b128 a[8:11], %[z0] offset:2048\n\t" "ds_read_b128 a[12:15], %[z0] offset:3072\n\t"
          "ds_read_b128 a[16:19], %[z0] offset:4096\n\t" "ds_read_b128 a[20:23], %[z0] offset:5120\n\t" "ds_read_b128 a[24:27], %[z0] offset:6144\n\t" "ds_read_b128 a[28:31], %[z0] offset:7168\n\t"
          "ds_read_b128 a[32:35], %[z1] offset:0\n\t" "ds_read_b128 a[36:39], %[z1] offset:1024\n\t" "ds_read_b128 a[40:43], %[z1] offset:2048\n\t" "ds_read_b128 a[44:47], %[z1] offset:3072\n\t"
          "ds_read_b128 a[48:51], %[z1] offset:4096\n\t" "ds_read_b128 a[52:55], %[z1] offset:5120\n\t" "ds_read_b128 a[56:59], %[z1] offset:6144\n\t" "ds_read_b128 a[60:63], %[z1] offset:7168\n\t"
          "ds_read_b128 a[64:67], %[z2] offset:0\n\t" "ds_read_b128 a[68:71], %[z2] offset:1024\n\t" "ds_read_b128 a[72:75], %[z2] offset:2048\n\t" "ds_read_b128 a[76:79], %[z2] offset:3072\n\t"
          "ds_read_b128 a[80:83], %[z2] offset:4096\n\t" "ds_read_b128 a[84:87], %[z2] offset:5120\n\t" "ds_read_b128 a[88:91], %[z2] offset:6144\n\t" "ds_read_b128 a[92:95], %[z2] offset:7168\n\t"
          "ds_read_b128 a[96:99], %[z3] offset:0\n\t" "ds_read_b128 a[100:103], %[z3] offset:1024\n\t" "ds_read_b128 a[104:107], %[z3] offset:2048\n\t" "ds_read_b128 a[108:111], %[z3] offset:3072\n\t"
          "ds_read_b128 a[112:115], %[z3] offset:4096\n\t" "ds_read_b128 a[116:119], %[z3] offset:5120\n\t" "ds_read_b128 a[120:123], %[z3] offset:6144\n\t" "ds_read_b128 a[124:127], %[z3] offset:7168\n\t"
          "s_waitcnt lgkmcnt(0)"
          : "={a[0:31]}"(z0), "={a[32:63]}"(z1), "={a[64:95]}"(z2), "={a[96:127]}"(z3) : [z0] "v"(za), [z1] "v"(za ^ 8192), [z2] "v"(za ^ 16384), [z3] "v"(za ^ 24576) : "memory");
      const int pb = R_P + (s & 3) * P_SLOT;
      v32u uu;
      const int ub = u_slot(s, para);
      TICK(0);
      anchor(k);
      TICK(8);
      unsigned gp = tt.gx + tt.dx + NS(T_M1), lt = late, sv = SENT;
      unsigned bad = (int)(done - to_cyc(k, tt.gx)) > T_OVH;
      TICK(4);
      LOG(2, s, k.cyc); LOG(3, s, k.g);
#pragma unroll 1
      for (;;) {
        if (bad) {
          const bool all = allok(sv);
          const unsigned last = latest(c, k, par, pb, sv);
          if (!all) mark(k);
          gp = when_p(tt, last);
          lt = 1;
        }
        close_in(k, to_cyc(k, gp) - (lt ? 96 : lead));
        const unsigned tg = to_cyc(k, gp);
        if (!bad) LOG(1, s, tg);
        asm volatile(Y_STEP
          : "=&{v[144:175]}"(uu), [bad] "=&s"(bad), [sv] "=&v"(sv)
          : [x0] "v"(xo[0]), [x1] "v"(xo[1]), [x2] "v"(xo[2]), [x3] "v"(xo[3]), [pm] "v"(pmo), [u0] "v"(uo + (rot ^ (y & 3)) * 2048), [mw] "v"(mw), [ps] "v"(psel), [ix] "v"(idx),
            [rs] "s"(c.rs), [pb] "s"(pb), [ub] "s"(ub), [mb] "s"(R_MEM + ((s + 1) & 3) * MEM_SLOT), [mr] "s"(R_MEM + ((s + 3) & 3) * MEM_SLOT), [tu] "s"(tg - (lt ? 96 : lead)), [tg] "s"(tg - 32), [late] "s"(lt),
            [so] "v"(so), [bd] "s"(c.budget),
            "{a[0:31]}"(z0), "{a[32:63]}"(z1), "{a[64:95]}"(z2), "{a[96:127]}"(z3)
          : CL_S, CL_120_128, CL_176_200, CL_200_216, CL_216_256);
        bad = __builtin_amdgcn_readfirstlane(bad);
        if (bad != 1u) break;
        spend(c);
      }
      done = bad;
      SCHED();
      const int ul = opq(UL + rot * 2048 + lane * 16);
#pragma unroll
      for (int i = 0; i < 8; ++i) sts16(c.L, (ul ^ ((i >> 1) * 2048)) + (i & 1) * 1024, v4u{uu[4 * i], uu[4 * i + 1], uu[4 * i + 2], uu[4 * i + 3]});
      asm volatile("" ::: "memory");
      *GO = s + 1;
      asm volatile("s_waitcnt vmcnt(2)" : "+v"(sv));
      advance(tt, latest(c, k, par, pb, sv));
      TICK(3);
      LITE(s); LOG(0, s, done);
      TICK_LDS(CW + 256);
    }
    {
      anchor(k);
      wait_cyc(to_cyc(k, tt.gx + tt.dx + NS(T_M1)));
      const int ub = u_slot(NSEG - 1, para);
      v4u u[8];
#pragma unroll 1
      for (;;) {
        asm volatile("" ::: "memory");
#pragma unroll
        for (int i = 0; i < 8; ++i) u[i] = ld16s(c.rs, uo + (i & 3) * 1024, ub + (i >> 2) * 4096);
        SCHED();
        unsigned t = 0;
#pragma unroll
        for (int i = 0; i < 8; ++i) t = umax(t, top(u[i]));
        if (allok(t) || spend(c)) break;
      }
      RAWBAR();
#pragma unroll
      for (int i = 0; i < 8; ++i) sts16(c.L, UL + (i ^ (2 * (y & 3))) * 1024 + lane * 16, u[i]);
      asm volatile("" ::: "memory");
      *GO = NSEG;
    }
    TICK_DUMP(CW + 256);
  } else if (w < 3) {
    const rsrc_t ro = mkrs(p.out, 2048 * 2048 * 2);
    auto tokens = [&](auto QT) {
      constexpr int qt = QT.value, NX = qt ? 32 : 16;
      RAWBAR();
      for (int s = 0; s < NSEG; ++s) {
        const int tb = R_PT + (s & 7) * PT_SLOT + para * 8192;
        int ua[4], za[4], xb[4], mb[4];
#pragma unroll
        for (int i = 0; i < 4; ++i) {
          ua[i] = opq(UL + (i ^ rot) * 2048 + lane * 16);
          za[i] = opq(ZR + (s & 3) * 32768 + (i ^ rot) * 8192 + (qt ? lane * 16 : (g >> 1) * 1024 + (g & 1) * 512 + cc * 16));
          xb[i] = tb + amap(qt * 16384 + (i ^ rot) * (qt ? 8192 : 4096), 0);
          mb[i] = tb + amap(49152 + qt * 8192, 0) + (i ^ rot) * 2048;
        }
        SCHED();
        RAWBAR();
        if (TOK_WAIT) __builtin_amdgcn_s_sleep(TOK_WAIT);
        TICK(0);
        f32x4 a;
#pragma unroll 1
        for (;;) {
          asm volatile("" ::: "memory");
          v4u pt[NX + 8];
#pragma unroll
          for (int i = 0; i < NX; ++i) pt[i] = ld16s(c.rs, (i & 3) * 1024 + lane * 16, qt ? xb[i >> 3] + ((i >> 2) & 1) * 4096 : xb[i >> 2]);
#pragma unroll
          for (int i = 0; i < 8; ++i) pt[NX + i] = ld16s(c.rs, (i & 1) * 1024 + lane * 16, mb[i >> 1]);
          SCHED();
          TICK(1);
#pragma unroll 1
          while (__builtin_amdgcn_readfirstlane(*GO) < (unsigned)s + 1u) { if (spend(c)) break; }
          asm volatile("" ::: "memory");
          f32x4 d[2] = {f32x4{0.f, 0.f, 0.f, 0.f}, f32x4{0.f, 0.f, 0.f, 0.f}};
          v4u fa[8];
          auto frag = [&](int i) {
            if (i >= NX) fa[i & 7] = lds16(c.L, ua[(i - NX) >> 1] + (i & 1) * 1024);
            else if (qt) fa[i & 7] = lds16(c.L, za[i >> 3] + (i & 7) * 1024);
            else {
              const v2u lo = *(LAS v2u*)(c.L + za[i >> 2] + (i & 3) * 2048), hi = *(LAS v2u*)(c.L + za[i >> 2] + (i & 3) * 2048 + 256);
              fa[i & 7] = v4u{lo.x, lo.y, hi.x, hi.y};
            }
          };
#pragma unroll
          for (int i = 0; i < 7; ++i) frag(i);
          SCHED();
#pragma unroll
          for (int i = 0; i < NX + 8; ++i) {
            if (i + 7 < NX + 8) frag(i + 7);
            d[i & 1] = mfma16(fa[i & 7], pt[i], d[i & 1]);
            SCHED();
          }
          a = d[0] + d[1];
          if (!__any(a[0] != a[0])) break;
          unsigned t = 0;
#pragma unroll
          for (int i = 0; i < NX + 8; ++i) t = umax(t, top(pt[i]));
          if (allok(t) || spend(c)) break;
        }
        __builtin_amdgcn_raw_buffer_store_b64(v2u{pk(a[0], a[1]), pk(a[2], a[3])}, ro, (16 * qt + cc) * 4096 + (16 * y + 4 * g) * 2, s * 32 * 4096, OUT_AUX);
        TICK(2);
        TICK_SUM(3);
      }
    };
    if (w == 1) tokens(IC<0>{}); else tokens(IC<1>{});
  } else {
    unsigned seenZ = NOFLAG;
    const rsrc_t rc = mkrs((const void*)c.ctrl, CTRL_BYTES);
    const int fo = lane < 32 ? lane * 128 : 1 << 30;
    __builtin_amdgcn_s_setprio(3);
    auto least = [&](unsigned v) {
      v |= lane < 32 ? 0u : ~0u;
#pragma unroll
      for (int o = 1; o < 32; o <<= 1) v = min(v, (unsigned)__shfl_xor((int)v, o));
      seenZ = umax(seenZ, __builtin_amdgcn_readfirstlane(v));
    };
    auto await = [&](unsigned need) {
#pragma unroll 1
      while (seenZ < need) {
        asm volatile("" ::: "memory");
        least(__builtin_amdgcn_raw_buffer_load_b32(rc, fo, C_FLAG + (F_Z + (y >> 4) * 32) * 128, AUX_SC1));
        if (seenZ >= need || spend(c)) break;
        __builtin_amdgcn_s_sleep(1);
      }
      __builtin_amdgcn_fence(__ATOMIC_ACQUIRE, "wavefront");
    };
    for (int n = 0; n < NSEG; ++n) {
      TICK(0);
      await(n + 1);
      if (Z_WAIT) __builtin_amdgcn_s_sleep(Z_WAIT);
      TICK(1);
      v4u z[32];
      int za[8];
#pragma unroll
      for (int i = 0; i < 8; ++i) za[i] = opq(ZR + (n & 3) * 32768 + (i ^ z_mix(y)) * 1024 + lane * 16);
      unsigned look;
#pragma unroll
      for (int i = 0; i < 32 + Z_WIN; ++i) {
        if (i < 32) z[i] = ld16z(c.rs, (i & 3) * 1024 + lane * 16, R_Z + (n & (ZRING - 1)) * Z_SLOT + y * 65536 + ((para ^ (y >> 5)) & 1) * 8192 + amap((i >> 2) * 4096, 0));
        if (i == 32) { look = __builtin_amdgcn_raw_buffer_load_b32(rc, seenZ < (unsigned)n + 2u ? fo : 1 << 30, C_FLAG + (F_Z + (y >> 4) * 32) * 128, AUX_SC1); TICK(4); }
        SCHED();
        if (i >= Z_WIN) sts16(c.L, za[(i - Z_WIN) & 7] + ((i - Z_WIN) >> 3) * 8192, z[i - Z_WIN]);
        else asm volatile("s_nop " STR(Z_GAP));
        if (i == 30 + Z_WIN) { TICK(3); }
        SCHED();
      }
      least(look);
      if (n == 0) await(PRIME);
      TICK(2);
      { const int s = n; (void)s; TICK_SUM(3); }
      BAR();
    }
    RAWBAR();
  }
  TICK_OUT();
  if (c.budget == 0u) __hip_atomic_store(c.ctrl + C_TMO / 4, 1u, RLX_AGENT);
}

#define MF1A(d, a, b) asm volatile(MF16 "%0, %1, %2, %0" : "+v"(d) : "a"(a), "v"(b))
#define MF32A(d, a, b) asm volatile("v_mfma_f32_32x32x16_bf16 %0, %1, %2, %0" : "+v"(d) : "a"(a), "v"(b))
#define MF32AZ(d, a, b) asm volatile("v_mfma_f32_32x32x16_bf16 %0, %1, %2, 0" : "=&v"(d) : "a"(a), "v"(b))
#define MF1B(d, a, b) asm volatile(MF16 "%0, %1, %2, %0" : "+v"(d) : "v"(a), "a"(b))
#define MF1BZ(d, a, b) asm volatile(MF16 "%0, %1, %2, 0" : "=&v"(d) : "v"(a), "a"(b))
#define MF1AZ(d, a, b) asm volatile(MF16 "%0, %1, %2, 0" : "=&v"(d) : "a"(a), "v"(b))
DEV void g_role(const Params& p, Ctx& c, int mat, int h, int gi) {
  constexpr int EX = 131072;
  c.budget = SPIN_CAP;
  const int w = c.w, lane = c.lane = fresh_lane(), g = lane >> 4, cc = lane & 15;
  unsigned look = claim_look(c);
  const int flip = FLIP && mat < 2 ? ((h >> 4) & 1) ^ 1 : 0;
  unsigned t[8];
  {
    auto pull = [&](const u16* q, int bytes, int v, int so) { return __builtin_amdgcn_raw_buffer_load_b32(mkrs(q, bytes), v, so, B_AUX); };
    const int all = mat < HELPERS ? 8 * MB : 0;
    t[6] = pull(p.Wv, SLOW_V * all, slow_line(p.Wv, h, (h & 7) + 8 * w, lane) ^ (mat * 128), 0);
    t[7] = pull(p.Wv, HELPERS < 2 ? all : 0, slow_line(p.Wv, h, (h & 7) + 8 * w, lane) ^ 128, 0);
    t[0] = __builtin_amdgcn_raw_buffer_load_b32(mkrs(p.x, all / 2), g * 65536 + (((h & 7) * 4 + w) * 16 + cc) * 128, mat * 4 * 65536, 0);
#pragma unroll
    for (int i = 0; i < 3; ++i) t[1 + i] = pull(p.Wv, HELP_V * all, (256 * w + 4 * (h & 7) + g) * 8192 + (h >> 3) * 2048 + cc * 128, (HELPERS < 2 ? 5 + i : 2 + 2 * i + mat) * 32 * 8192);
    SCHED();
    if (mat < 2) ask(c, look);
    SCHED();
#pragma unroll
    for (int i = 0; i < 2; ++i) t[4 + i] = pull(p.Wo, HELP_O * all, (h * 128 + (lane >> 1)) * 2048 + w * 512 + 256 + (lane & 1) * 128, (2 * i + mat) * 32 * 2048);
    SCHED();
    if (mat == 0) claim(c, 0);
    hold(c, mat == 0 ? GQ_AT : mat == 1 ? GK_AT : 0);
  }
  const rsrc_t rx = mkrs(p.x, 2048 * 2048);
  const int xs = w * 16384 + ((lane * 16) ^ ((w & 1) << 7));

  v32u wa[8];
  {
    const rsrc_t rw = mkrs(mat == 0 ? p.Wq : mat == 1 ? p.Wk : p.Wv, 1024 * 4096 * 2);
    const int vo0 = opq((256 * w + 128 * flip + 8 * g) * 8192 + (h * 128 + 8 * cc) * 2), vo1 = opq(vo0 ^ (128 * 8192));
    v4u row[8][8];
    auto request = [&](int u) {
#pragma unroll
      for (int r = 0; r < 8; ++r) {
        row[u][r] = ld16w<B_AUX>(rw, (u & 4) ? vo1 : vo0, (32 * (u & 3) + r) * 8192);
        if ((r & 3) == 3) { const int n = 2 * u + (r >> 2); dma16(rx, c.L + w * 16384 + n * 1024, (xs ^ ((n >> 1) << 4)) + n * 1024); }
      }
    };
    const unsigned look0 = claim_look(c);
    bool claimed = false;
    request(0);
    SCHED();
    if (mat == 2) ask(c, look);
    SCHED();
    request(1); request(2);
    if (G_DEEP) request(3);
    SCHED();
    const int sw_ = EX + w * 8192 + g * 256;
    const int sr0 = opq(EX + (cc & 3) * 1024 + g * 256 + ((4 * w + (cc >> 2) + 4 * cc) & 15) * 16), sr1 = sr0 + 4096;
#pragma unroll
    for (int u = 0; u < 8; ++u) {
#pragma unroll
      for (int i = 0; i < 8; ++i) {
        v4u f;
#pragma unroll
        for (int r = 0; r < 4; ++r) f[r] = prm(row[u][2 * r + 1][i >> 1], row[u][2 * r][i >> 1], i & 1);
        sts16(c.L, sw_ + i * 1024 + ((cc + 4 * i) & 15) * 16, f);
      }
      SCHED();
      if (u == 0) asm volatile("" :: "v"(t[0]), "v"(t[1]), "v"(t[2]), "v"(t[3]), "v"(t[4]), "v"(t[5]), "v"(t[6]), "v"(t[7]));
      if (u == 0) claimed = mat == 0 || claim_try(c, look0);
      if (u == 4 - G_DEEP) look = claim_look(c);
      if (u + 3 + G_DEEP < 8) request(u + 3 + G_DEEP);
      if (u == 7 && !claimed && !claim_try(c, look)) claim(c, 0);
      BAR();
      v8u in[4];
#pragma unroll
      for (int q = 0; q < 4; ++q) in[q] = cat(lds16(c.L, sr0 + q * 8192), lds16(c.L, sr1 + q * 8192));
      SCHED();
#pragma unroll
      for (int q = 0; q < 4; ++q) {
        v8u f = in[q];
#define M_(r) asm volatile("" : "+" r(f))
        AT_ACC(M_, 8 * q + u);
#undef M_
#pragma unroll
        for (int r = 0; r < 8; ++r) wa[2 * q + (u >> 2)][8 * (u & 3) + r] = f[r];
      }
      BAR();
    }
  }
  STAMP(0);
  const int xpar = vote_a(wait_vote(c)) ^ (mat >> 1);
  TICK_DECL;
  asm volatile(G_LOOP
      :: [xo] "v"(cc * 2048 + flip * 256 + ((g ^ cc) << 4)), [xs] "v"(xs), [so] "v"((4 * w + g) * 512 + cc * 16), [fo] "v"((w | lane) ? 1 << 30 : 0),
         [rs] "s"(c.rs), [rx] "s"(rx), [rc] "s"(mkrs((const void*)c.ctrl, CTRL_BYTES)), [fl] "s"(C_FLAG + (mat * 32 + h) * 128), [xp] "s"(xpar),
         [ob] "s"((mat == 1 ? R_KX : R_QX) + h * 16384), [lw] "s"(w * 16384), GTRACE ACC_IN(wa)
      : CL_S, CL_16_32, CL_32_100);
}

DEV void zc_role(const Params& p, Ctx& c, int h) {
  c.budget = SPIN_CAP;
  const int w = c.w, lane = c.lane = fresh_lane(), g = lane >> 4, cc = lane & 15, h2 = lane >> 5, r32 = lane & 31;
  v32u wa[8];
  const unsigned first = claim_look(c);
  {
    const rsrc_t rw = mkrs(p.Wo, 4096 * 1024 * 2);
    const int vo = (h * 128 + 8 * g) * 2048 + (256 * w + 8 * cc) * 2;
    v4u row[8][8];
    auto request = [&](int u) {
#pragma unroll
      for (int r = 0; r < 8; ++r) row[u][r] = ld16w<B_AUX>(rw, vo, (32 * (u & 3) + r) * 2048 + (u >> 2) * 256);
    };
    request(0);
    SCHED();
    ask(c, first);
    SCHED();
    request(1); request(2);
    unsigned look = 0;
    const int sw_ = w * 8192 + g * 256, sr_ = w * 8192 + (cc & 7) * 1024 + g * 256;
    v8u two[8];
#pragma unroll
    for (int u = 0; u < 8; ++u) {
#pragma unroll
      for (int i = 0; i < 8; ++i) {
        v4u f;
#pragma unroll
        for (int r = 0; r < 4; ++r) f[r] = prm(row[u][2 * r + 1][i >> 1], row[u][2 * r][i >> 1], i & 1);
        sts16(c.L, sw_ + i * 1024 + ((cc + 2 * i) & 15) * 16, f);
      }
      SCHED();
      if (u == 4) look = claim_look(c);
      if (u + 3 < 8) request(u + 3);
      if (u == 7 && !claim_try(c, look)) claim(c, 0);
      LGKM(0);
#pragma unroll
      for (int q = 0; q < 8; ++q) {
        const v4u o = lds16(c.L, sr_ + ((2 * q + (cc >> 3) + 2 * cc) & 15) * 16);
#pragma unroll
        for (int r = 0; r < 4; ++r) two[q][4 * (u & 1) + r] = o[r];
      }
      SCHED();
      if (u & 1) {
#pragma unroll
        for (int q = 0; q < 8; ++q) {
          const int n = 2 * (8 * (u >> 2) + q) + ((u >> 1) & 1);
#define M_(r) asm volatile("" : "+" r(two[q]))
          AT_ACC(M_, n);
#undef M_
#pragma unroll
          for (int r = 0; r < 8; ++r) wa[n >> 2][8 * (n & 3) + r] = two[q][r];
        }
      }
      LGKM(0);
      SCHED();
    }
  }
  STAMP(0);
  BAR();
  const int para = vote_a(wait_vote(c));
  const int ao = g * 512 + cc * 16, zo = 16 * w * 65536 + lane * 16 + ((para ^ (w >> 1)) & 1) * 8192;
  TICK_DECL;
  asm volatile(ZC_LOOP
      : [bd] "+s"(c.budget)
      : [ao] "v"(ao), [a1] "v"(ao + 4096), [z1] "v"(zo + amap((h ^ z_mix(16 * w)) * 1024, 0)), [z2] "v"(zo + 65536 + amap((h ^ z_mix(16 * w + 1)) * 1024, 0)), [fo] "v"(lane ? 1 << 30 : 0),
        [rs] "s"(c.rs), [rc] "s"(mkrs((const void*)c.ctrl, CTRL_BYTES)), [fv] "s"(C_FLAG + (F_V + h) * 128), [fs] "s"(C_FLAG + (F_S + h) * 128),
        [fz] "s"(C_FLAG + (F_Z + w * 32 + h) * 128), [xp] "s"(para ^ 1), [vb] "s"(R_QX + h * 16384), [vd] "s"(C_VERDICT + c.att * 128), [z0] "n"(ZC_PRIME), [zb] "n"(R_Z), [rg] "n"(ZRING - 2), [rm] "n"(ZRING - 1), GTRACE ACC_IN(wa)
      : CL_S, CL_16_32, CL_32_100);
  if (c.budget == 0u) __hip_atomic_store(c.ctrl + C_TMO / 4, 1u, RLX_AGENT);
}

DEV void conv_role(const Params& p, Ctx& c, int cg, int far) {
  constexpr int FS = 4160, PL = 1280, UO = 4 * FS, WS = UO + 8 * PL;
  const int w = c.w, lane = c.lane = fresh_lane(), tid = w * 64 + lane, g = lane >> 4, cc = lane & 15;
  const int d0 = 8 * cg + 2 * w;
  const int pg = ((far ^ (cg >> 5)) & 1) * 8192;
  auto crow = [](int ch) { return (ch >> 1) * 16384 + (ch & 1) * 4096; };
  const int grp = cg >> 3, de = grp * 64 + (tid & 7) * 8, t0 = (cg & 7) * 256 + (tid >> 3) * 8;
  auto arrive = [&](int n) {
    VMWAIT(0);
    BAR();
    if (w == 0 && lane == 0) __hip_atomic_fetch_add(c.ctrl + (C_GRP + n * 128) / 4, 1u, RLX_AGENT);
  };
  auto await = [&](int n) {
    if (w == 0) {
#pragma unroll 1
      while (__builtin_amdgcn_readfirstlane(__hip_atomic_load(c.ctrl + (C_GRP + n * 128) / 4, RLX_AGENT)) < 8u) { if (spend(c)) break; __builtin_amdgcn_s_sleep(1); }
    }
    BAR();
  };
  v4u fr[2][4], ur[2][4], qr[8];
  unsigned up[2][4];
  const int so = cc * 64 + g * 16;
  const int fw = opq(w * WS + 48 + lane * 16), uw = opq(w * WS + UO + g * PL + cc * 16);
  auto copies = [&](int ch) {
#pragma unroll
    for (int i = 0; i < 4; ++i) {
      const v4u f = fr[ch][i];
      sts16(c.L, fw + 3 * FS + i * 1024, f);
      sts8(c.L, fw + FS + i * 1024 + 8, v2u{f[0], f[1]}); sts8(c.L, fw + FS + i * 1024 + 16, v2u{f[2], f[3]});
#pragma unroll
      for (int r = 0; r < 2; ++r) {
        const int o = fw + 2 * r * FS + i * 1024 + 12 - 8 * r;
        *(LAS unsigned*)(c.L + o) = f[0]; sts8(c.L, o + 4, v2u{f[1], f[2]}); *(LAS unsigned*)(c.L + o + 12) = f[3];
      }
    }
  };
  CST(0);
  const int turns = (grp & 3) == 2 ? 2 : (grp & 3) == 3 ? 0 : 1;
#pragma unroll 1
  for (int n = turns - 1; n >= 0; --n) {
    const rsrc_t rk = mkrs(p.bk, 2048 * 2048), rv = mkrs(p.bv, 2048 * 2048);
    const int dn = de + 64 * n;
    v4u kr[11], vr[8], sh[4];
#pragma unroll
    for (int i = 0; i < 4; ++i) sh[i] = ld16c(mkrs(p.shift, 8192), dn * 8 + i * 16, 0);
#pragma unroll
    for (int i = 0; i < 11; ++i) kr[i] = ld16c(rk, (t0 + i - 3) * 2048 + dn * 2, 0);
#pragma unroll
    for (int i = 0; i < 8; ++i) vr[i] = ld16c(rv, (t0 + i) * 2048 + dn * 2, 0);
    SCHED();
    CST(1);
#pragma unroll
    for (int ch = 0; ch < 8; ++ch) {
      const unsigned sa = sh[ch >> 1][(ch & 1) * 2], sb = sh[ch >> 1][(ch & 1) * 2 + 1];
      const float s0 = blo(sa), s1 = bhi(sa), s2 = blo(sb), s3 = bhi(sb);
      float kk[11], o[8];
#pragma unroll
      for (int i = 0; i < 11; ++i) kk[i] = (ch & 1) ? bhi(kr[i][ch >> 1]) : blo(kr[i][ch >> 1]);
#pragma unroll
      for (int i = 0; i < 8; ++i) o[i] = fmaf(kk[i + 3], s0, fmaf(kk[i + 2], s1, fmaf(kk[i + 1], s2, kk[i] * s3))) * ((ch & 1) ? bhi(vr[i][ch >> 1]) : blo(vr[i][ch >> 1]));
      st16s(c.rs, dn * 8192 + t0 * 2, R_KVT + pg + crow(ch), v4u{pk(o[0], o[1]), pk(o[2], o[3]), pk(o[4], o[5]), pk(o[6], o[7])});
    }
    CST(3);
    arrive(2 * (grp + n));
  }
  const unsigned dd = __builtin_amdgcn_raw_buffer_load_b32(mkrs(p.ssmD, 2048), d0 * 2, 0, CONV_AUX);
#pragma unroll
  for (int i = 0; i < 8; ++i) fr[i >> 2][i & 3] = ld16c(mkrs(p.ssmk, 4 * MB), lane * 16 + (i & 3) * 1024, (d0 + (i >> 2)) * 4096);
  SCHED();
  CST(4);
  await(2 * grp);
  CST(5);
#pragma unroll
  for (int i = 0; i < 8; ++i) {
    const int b = R_KVT + pg + d0 * 8192 + (i >> 2) * 4096 + (i & 3) * 1024;
    ur[i >> 2][i & 3] = ld16s(c.rs, so, b);
    up[i >> 2][i & 3] = __builtin_amdgcn_raw_buffer_load_b32(c.rs, (i & 3) == 0 && lane == 0 ? 1 << 30 : so, b - 4, AUX_SC1);
  }
  SCHED();
  if (lane < 16) sts16(c.L, w * WS + (lane >> 2) * FS + (lane & 3) * 16, v4u{0, 0, 0, 0});
#pragma unroll
  for (int i = 0; i < 2; ++i) sts16(c.L, w * WS + UO + (4 * i + g) * PL + cc * 16, v4u{0, 0, 0, 0});
  STAMP(5);
  const int fa = opq(w * WS + (cc & 3) * FS + (3 + (cc >> 2) - g) * 16);
  sfor<2>([&](auto CH) __attribute__((always_inline)) {
    constexpr int ch = CH.value;
    SCHED();
    copies(ch);
    SCHED();
#pragma unroll
    for (int i = 0; i < 4; ++i) {
      const v4u a = ur[ch][i];
      v4u r0, r1;
#pragma unroll
      for (int r = 0; r < 4; ++r) {
        r0[r] = __builtin_amdgcn_alignbyte(a[3 - r], a[3 - r], 2);
        r1[r] = __builtin_amdgcn_perm(r < 3 ? a[2 - r] : up[ch][i], a[3 - r], 0x07060100u);
      }
      sts16(c.L, uw + (16 + 16 * i) * 16, r0);
      sts16(c.L, uw + 4 * PL + (16 + 16 * i) * 16, r1);
    }
    CST(6 + 2 * ch);
    f32x4 acc[4][2];
#pragma unroll
    for (int i = 0; i < 8; ++i) acc[i >> 1][i & 1] = f32x4{0.f, 0.f, 0.f, 0.f};
    v4u A[2][4], B[2][8];
    auto frag = [&](int d, int n) {
      constexpr int order[12] = {0, 4, 5, 6, 7, 8, 9, 10, 11, 1, 2, 3};
      const int x = order[n];
      if (x < 4) A[d & 1][x] = lds16(c.L, fa + (d + 16 * x) * 64);
      else B[d & 1][x - 4] = lds16(c.L, uw + ((x - 4) & 1) * 4 * PL + (16 + 16 * ((x - 4) >> 1) - d) * 16);
    };
    asm volatile("" ::: "memory");
#pragma unroll
    for (int n = 0; n < 12; ++n) frag(0, n);
    SCHED();
    sfor<16>([&](auto D) __attribute__((always_inline)) {
      constexpr int d = D.value;
      int n = 0;
#pragma unroll
      for (int k = 0; k < 4; ++k)
#pragma unroll
        for (int m = 0; m + k < 4; ++m)
#pragma unroll
          for (int e = 0; e < 2; ++e) {
            acc[m + k][e] = mfma16(A[d & 1][k], B[d & 1][2 * m + e], acc[m + k][e]);
            if (d < 15 && n < 12) frag(d + 1, n);
            if (ch == 0 && n == 14 && (d & 1)) qr[d >> 1] = ld16c(mkrs(p.bq, 4 * MB), (t0 + (d >> 1)) * 2048 + de * 2, 0);
            ++n;
            SCHED();
          }
    });
    CST(7 + 2 * ch);
    const float dv = ch ? bhi(dd) : blo(dd);
#pragma unroll
    for (int q = 0; q < 4; ++q) {
      v4u z;
#pragma unroll
      for (int i = 0; i < 4; ++i) {
        const unsigned yy = pk(acc[q][1][i], acc[q][0][i]), ud = pk(blo(ur[ch][q][i]) * dv, bhi(ur[ch][q][i]) * dv);
        z[i] = pk(blo(yy) + blo(ud), bhi(yy) + bhi(ud));
      }
      st16s(c.rs, so + q * 1024, R_YT + pg + d0 * 8192 + ch * 4096, z);
    }
  });
  STAMP(6);
  {
    const rsrc_t ro = mkrs(p.out, 2048 * 2048 * 2);
    v4u yv[8];
    arrive(2 * grp + 1);
    await(2 * grp + 1);
    CST(10);
#pragma unroll
    for (int ch = 0; ch < 8; ++ch) yv[ch] = ld16s(c.rs, de * 8192 + t0 * 2, R_YT + pg + crow(ch));
#pragma unroll
    for (int i = 0; i < 8; ++i) {
      float o[8];
#pragma unroll
      for (int ch = 0; ch < 8; ++ch) o[ch] = ((ch & 1) ? bhi(qr[i][ch >> 1]) : blo(qr[i][ch >> 1])) * ((i & 1) ? bhi(yv[ch][i >> 1]) : blo(yv[ch][i >> 1]));
      __builtin_amdgcn_raw_buffer_store_b128(v4u{pk(o[0], o[1]), pk(o[2], o[3]), pk(o[4], o[5]), pk(o[6], o[7])}, ro, (t0 + i) * 4096 + (1024 + de) * 2, 0, OUT_AUX2);
    }
  }
  if (c.budget == 0u) __hip_atomic_store(c.ctrl + C_TMO / 4, 1u, RLX_AGENT);
}

extern "C" __global__ void __launch_bounds__(256, 1) mk_mega(Params p) {
  extern __shared__ __attribute__((aligned(16))) unsigned char lds_raw[];
  Ctx c;
  c.ctrl = (gu32*)p.ctrl;
  c.rs = mkrs(p.data, R_END);
  c.L = (lds_t)lds_raw;
  c.w = __builtin_amdgcn_readfirstlane(threadIdx.x >> 6);
  c.lane = threadIdx.x & 63;
  c.t0 = (unsigned)__builtin_amdgcn_s_memrealtime();
  c.b = (int)(blockIdx.x & 255u);
  c.att = (int)(blockIdx.x >> 8);
  c.ask = 0;
  c.slot = (c.att ? (c.b & 7) : (int)((unsigned)__builtin_amdgcn_s_getreg(20 | (3 << 11)) & 7u)) * 32 + (c.b >> 3);
  const int x = c.slot >> 5, t = c.slot & 31;
  if (c.w == 1) {
    asm volatile("s_getpc_b64 s[60:61]\n1:\n\ts_sub_u32 s60, s60, 1b-mk_mega\n\ts_subb_u32 s61, s61, 0\n\ts_mov_b32 s62, mk_tail-mk_mega\n\ts_mov_b32 s63, 0x20000\n\t"
                 "buffer_load_dword v255, %0, s[60:63], 0 offen\n\ts_waitcnt vmcnt(0)" :: "v"((c.lane * 32 + t) * 128) : "s60", "s61", "s62", "s63", "v255", "scc", "memory");
  }
  if (c.b == SERVER && c.w == 3) serve(c);
  if (x < 4) {
    if (t < 8) qk_role(p, c, x * 8 + t);
    else if (t < 16) vo_role(p, c, x * 8 + t - 8);
    else y_role(p, c, x * 16 + t - 16);
  } else {
    if (t < 24) g_role(p, c, t >> 3, (x - 4) * 8 + (t & 7), t);
    else zc_role(p, c, (x - 4) * 8 + t - 24);
    STAMP(2);
    c.budget = SPIN_CAP;
    conv_role(p, c, (x - 4) * 32 + t, vote_a(wait_verdict(c)) ^ 1);
  }
  STAMP(3);
}

}

extern "C" void kernel_launch(void* const* d_in, const int* in_sizes, int n_in, void* d_out, int out_size, void* d_ws, size_t ws_size,
                              hipStream_t stream) {
  using namespace mk;
  (void)in_sizes; (void)n_in; (void)out_size;
  Params p;
  p.x = (const u16*)d_in[0]; p.mem0 = (const u16*)d_in[1];
  p.Wq = (const u16*)d_in[2]; p.Wk = (const u16*)d_in[3]; p.Wv = (const u16*)d_in[4]; p.Wo = (const u16*)d_in[5];
  p.bq = (const u16*)d_in[7]; p.bk = (const u16*)d_in[8]; p.bv = (const u16*)d_in[9];
  p.shift = (const u16*)d_in[10]; p.ssmk = (const u16*)d_in[11]; p.ssmD = (const u16*)d_in[12];
  p.out = (u16*)d_out;
  p.ctrl = (unsigned char*)d_ws;
  p.data = data_base(d_ws, ws_size);
  (void)hipFuncSetAttribute((const void*)mk_mega, hipFuncAttributeMaxDynamicSharedMemorySize, LDS_BYTES);
  hipLaunchKernelGGL(mk_mega, dim3(256), dim3(256), LDS_BYTES, stream, p);
}
```
